```python
import torch

def _softmax(x, axis=-1):
    return torch.softmax(x, dim=axis)
def _gelu(x):
    return torch.nn.functional.gelu(x, approximate="tanh")
def _silu(x):
    return torch.nn.functional.silu(x)
def _sigmoid(x):
    return torch.sigmoid(x)
def _layernorm(x, axis=-1, eps=1e-5):
    m = x.mean(dim=axis, keepdim=True)
    v = x.var(dim=axis, keepdim=True, unbiased=False)
    return (x - m) / torch.sqrt(v + eps)
def _scatter_add(tgt, idx, src, axis=0):
    return tgt.clone().index_add_(axis, idx, src)
def _segment_sum(src, seg, n):
    out = torch.zeros((n,) + src.shape[1:], dtype=src.dtype)
    return out.index_add_(0, seg, src)
def _pad(x, pw):
    flat = tuple(v for pair in reversed(pw) for v in pair)
    return torch.nn.functional.pad(x, flat)

kernel_name = 'parallel_sum__compressed_kv_cla__moe_arch_mod__prompt'

def setup_inputs():

    _w = (lambda a: (lambda t: t.to(torch.bfloat16) if t.is_floating_point() else t)(torch.as_tensor(a)))
    def _A_setup():
        B, H, Sq, Sk, D, M = 1, 32, 2048, 2048, 128, 640
        Dc = 4 * D
        return {
            "x": _w((lambda *s: torch.randn(*s, dtype=torch.bfloat16) * 0.05)(B, Sq, M)),
            "c_kv": _w((lambda *s: torch.randn(*s, dtype=torch.bfloat16) * 1.6)(B, Sk, Dc)),
            "w_q": _w((lambda *s: torch.randn(*s, dtype=torch.bfloat16) * 0.8)(M, H, D) * (M ** -0.5)),
            "w_uk": _w((lambda *s: torch.randn(*s, dtype=torch.bfloat16) * 0.8)(Dc, H, D) * (Dc ** -0.5)),
            "w_uv": _w((lambda *s: torch.randn(*s, dtype=torch.bfloat16) * 1.6)(Dc, H, D) * (Dc ** -0.5)),
            "w_o": _w((lambda *s: torch.randn(*s, dtype=torch.bfloat16) * 1.6)(H, D, M) * ((H * D) ** -0.5)),
        }
    _A_in = _A_setup()

    def _B_setup():
        B, S, D, F, C = 1, 2048, 640, 1728, 8
        return {
            "x": _w((lambda *s: torch.randn(*s, dtype=torch.bfloat16) * 0.05)(B, S, D)),
            "w_r": _w((lambda *s: torch.randn(*s, dtype=torch.bfloat16) * 0.05)(D)),
            "w_gate": _w((lambda *s: torch.randn(*s, dtype=torch.bfloat16) * 0.05)(D, F)),
            "w_up": _w((lambda *s: torch.randn(*s, dtype=torch.bfloat16) * 0.05)(D, F)),
            "w_down": _w((lambda *s: torch.randn(*s, dtype=torch.bfloat16) * 0.05)(F, D)),
        }
    _B_in = _B_setup()

    return {
        "A_x": _A_in["x"],
        "A_c_kv": _A_in["c_kv"],
        "A_w_q": _A_in["w_q"],
        "A_w_uk": _A_in["w_uk"],
        "A_w_uv": _A_in["w_uv"],
        "A_w_o": _A_in["w_o"],
        "B_x": _B_in["x"],
        "B_w_r": _B_in["w_r"],
        "B_w_gate": _B_in["w_gate"],
        "B_w_up": _B_in["w_up"],
        "B_w_down": _B_in["w_down"],
    }

def reference(A_x, A_c_kv, A_w_q, A_w_uk, A_w_uv, A_w_o, B_x, B_w_r, B_w_gate, B_w_up, B_w_down):
    def _A_ref(x, c_kv, w_q, w_uk, w_uv, w_o):
        B, Sq, M = x.shape[0], x.shape[1], x.shape[2]
        Sk = c_kv.shape[1]
        H, D = w_q.shape[1], w_q.shape[2]
        q = torch.einsum("bqm,mhd->bhqd", x, w_q)
        k = torch.einsum("bkc,chd->bhkd", c_kv, w_uk)
        v = torch.einsum("bkc,chd->bhkd", c_kv, w_uv)
        scores = torch.einsum("bhqd,bhkd->bhqk", q, k) * (D ** -0.5)
        off = Sk - Sq
        qi = torch.arange(Sq)[:, None]
        kj = torch.arange(Sk)[None, :]
        cmask = kj <= (qi + off)
        scores = torch.where(cmask[None, None], scores, -1e9)
        p = _softmax(scores, axis=-1)
        ctx = torch.einsum("bhqk,bhkd->bhqd", p, v)
        return torch.einsum("bhqd,hdm->bqm", ctx, w_o)

    def _B_ref(x, w_r, w_gate, w_up, w_down):
        B, S, D = x.shape
        C = 8
        r = x @ w_r
        idx = torch.argsort(-r, axis=-1)[:, :C]
        idx_e = (lambda x, n, axis: torch.repeat_interleave(x, n, dim=axis))(idx.reshape(B, C, 1), D, axis=2)
        x_sel = (lambda x, i, axis: torch.take_along_dim(x, i, dim=axis))(x, idx_e, axis=1)
        g = _silu(x_sel @ w_gate)
        u = x_sel @ w_up
        y = (g * u) @ w_down
        r_sel = _sigmoid((lambda x, i, axis: torch.take_along_dim(x, i, dim=axis))(r, idx, axis=1))
        y = y * r_sel.reshape(B, C, 1)
        b_off = (torch.arange(B) * S).reshape(B, 1)
        flat_idx = (idx + b_off).reshape(B * C)
        zero = (x * 0.0).reshape(B * S, D)
        upd = _scatter_add(zero, flat_idx, y.reshape(B * C, D), axis=0)
        return x + upd.reshape(B, S, D)

    x = A_x
    xn = _layernorm(x, axis=-1)
    return x + _A_ref(xn, A_c_kv, A_w_q, A_w_uk, A_w_uv, A_w_o) + _B_ref(xn, B_w_r, B_w_gate, B_w_up, B_w_down)
```

```cpp
#include <hip/hip_runtime.h>
#include <hip/hip_bf16.h>
typedef __hip_bfloat16 bf16;

__global__ __launch_bounds__(256, 1) void mega_kernel(const bf16* x, const bf16* ckv, const bf16* wq, const bf16* wuk, const bf16* wuv, const bf16* wo, const bf16* w_r,
                                                      const bf16* wg, const bf16* wu, const bf16* wd, char* ws, bf16* out) {
  asm volatile(
"s_load_dwordx16 s[64:79], %[ka], 0\n"
"s_load_dwordx8 s[80:87], %[ka], 64\n"
"s_mov_b32 s31, %[b]\n"
"v_lshrrev_b32 v1, 6, %[t]\n"
"v_and_b32 v2, 63, %[t]\n"
"v_readfirstlane_b32 s30, v1\n"
"v_lshlrev_b32 v224, 4, v2\n"
"s_and_b32 s32, s31, 7\n"
"s_lshr_b32 s33, s31, 6\n"
"s_bfe_u32 s34, s31, 0x30003\n"
"s_lshl_b32 s35, s32, 2\n"
"s_add_u32 s35, s35, s33\n"
"s_add_u32 s46, s34, s32\n"
"s_and_b32 s46, s46, 6\n"
"s_lshl_b32 s47, s46, 19\n"
"s_lshl_b32 s46, s46, 7\n"
"s_lshl_b32 s36, s30, 12\n"
"s_mul_i32 s37, s30, 24576\n"
"s_add_u32 s37, s37, 65536\n"
"v_lshrrev_b32 v3, 3, v2\n"
"v_lshrrev_b32 v4, 4, v2\n"
"v_and_b32 v5, 7, v2\n"
"v_xor_b32 v6, v5, v4\n"
"v_lshlrev_b32 v6, 4, v6\n"
"v_lshl_add_u32 v6, v3, 10, v6\n"
"s_lshl_b32 s50, s30, 15\n"
"v_add_u32 v234, s50, v6\n"
"v_xor_b32 v235, 64, v234\n"
"v_add_u32 v235, 7168, v235\n"
"v_add_u32 v236, 14336, v234\n"
"v_add_u32 v237, 14336, v235\n"
"v_and_b32 v7, 1, v4\n"
"v_lshlrev_b32 v7, 1, v7\n"
"v_xor_b32 v7, v7, v5\n"
"v_lshlrev_b32 v7, 4, v7\n"
"v_lshl_add_u32 v238, v3, 13, v7\n"
"v_xor_b32 v239, 64, v238\n"
"v_add_u32 v239, 64512, v239\n"
"v_add_u32 v240, 129024, v238\n"
"v_add_u32 v241, 129024, v239\n"
"v_and_b32 v8, 31, v2\n"
"v_lshlrev_b32 v8, 3, v8\n"
"v_lshrrev_b32 v9, 5, v2\n"
"s_lshl_b32 s50, s31, 2\n"
"s_add_u32 s50, s50, s30\n"
"s_mul_i32 s50, s50, 2560\n"
"v_mul_u32_u24 v9, 1280, v9\n"
"v_add3_u32 v9, v9, v8, s50\n"
"s_lshl_b32 s50, s34, 17\n"
"s_lshl_b32 s51, s35, 8\n"
"s_and_b32 s52, s30, 1\n"
"s_lshl_b32 s52, s52, 7\n"
"s_add_u32 s52, s52, s51\n"
"s_mov_b32 s14, 0x120000\n"
"s_mov_b32 s15, 0x20000\n"
"s_sub_u32 s18, 4194304, s52\n"
"s_mov_b32 s19, 0x20000\n"
"s_waitcnt lgkmcnt(0)\n"
"s_load_dwordx2 s[28:29], s[84:85], 0\n"
"s_add_u32 s12, s66, s50\n"
"s_addc_u32 s13, s67, 0\n"
"s_and_b32 s13, s13, 0xffff\n"
"s_cmp_lt_u32 s30, 2\n"
"s_cselect_b64 s[16:17], s[70:71], s[72:73]\n"
"s_add_u32 s16, s16, s52\n"
"s_addc_u32 s17, s17, 0\n"
"s_and_b32 s17, s17, 0xffff\n"
"global_load_dwordx2 v[128:129], v9, s[64:65] offset:0\n"
"global_load_dwordx2 v[130:131], v9, s[64:65] offset:256\n"
"global_load_dwordx2 v[132:133], v9, s[64:65] offset:512\n"
"global_load_dwordx2 v[134:135], v9, s[64:65] offset:768\n"
"global_load_dwordx2 v[136:137], v9, s[64:65] offset:1024\n"
"global_load_dwordx2 v[138:139], v8, s[76:77] offset:0\n"
"global_load_dwordx2 v[140:141], v8, s[76:77] offset:256\n"
"global_load_dwordx2 v[142:143], v8, s[76:77] offset:512\n"
"global_load_dwordx2 v[144:145], v8, s[76:77] offset:768\n"
"global_load_dwordx2 v[146:147], v8, s[76:77] offset:1024\n"
"s_add_u32 m0, s37, 0\n"
"s_add_u32 s48, s47, 0\n"
"s_and_b32 s48, s48, 4194303\n"
"buffer_load_dwordx4 v238, s[16:19], s48 offen offset:0 lds\n"
"buffer_load_dwordx4 v239, s[16:19], s48 offen offset:1024 lds\n"
"buffer_load_dwordx4 v240, s[16:19], s48 offen offset:2048 lds\n"
"buffer_load_dwordx4 v241, s[16:19], s48 offen offset:3072 lds\n"
"s_add_u32 m0, s36, 0\n"
"s_add_u32 s48, s46, 0\n"
"s_and_b32 s48, s48, 1023\n"
"buffer_load_dwordx4 v234, s[12:15], s48 offen offset:0 lds\n"
"buffer_load_dwordx4 v235, s[12:15], s48 offen offset:1024 lds\n"
"buffer_load_dwordx4 v236, s[12:15], s48 offen offset:2048 lds\n"
"buffer_load_dwordx4 v237, s[12:15], s48 offen offset:3072 lds\n"
"s_add_u32 m0, s37, 4096\n"
"s_add_u32 s48, s47, 262144\n"
"s_and_b32 s48, s48, 4194303\n"
"buffer_load_dwordx4 v238, s[16:19], s48 offen offset:0 lds\n"
"buffer_load_dwordx4 v239, s[16:19], s48 offen offset:1024 lds\n"
"buffer_load_dwordx4 v240, s[16:19], s48 offen offset:2048 lds\n"
"buffer_load_dwordx4 v241, s[16:19], s48 offen offset:3072 lds\n"
"s_add_u32 m0, s37, 8192\n"
"s_add_u32 s48, s47, 524288\n"
"s_and_b32 s48, s48, 4194303\n"
"buffer_load_dwordx4 v238, s[16:19], s48 offen offset:0 lds\n"
"buffer_load_dwordx4 v239, s[16:19], s48 offen offset:1024 lds\n"
"buffer_load_dwordx4 v240, s[16:19], s48 offen offset:2048 lds\n"
"buffer_load_dwordx4 v241, s[16:19], s48 offen offset:3072 lds\n"
"v_lshlrev_b32 v253, 7, %[t]\n"
"s_mov_b32 s8, s84\n"
"s_and_b32 s9, s85, 0xffff\n"
"s_mov_b32 s10, 81788928\n"
"s_mov_b32 s11, 0x20000\n"
"s_add_u32 s20, s68, s51\n"
"s_addc_u32 s21, s69, 0\n"
"s_and_b32 s21, s21, 0xffff\n"
"s_sub_u32 s22, 5242880, s51\n"
"s_mov_b32 s23, 0x20000\n"
"v_and_b32 v11, 15, v2\n"
"v_lshrrev_b32 v12, 1, v11\n"
"v_xor_b32 v12, v12, v4\n"
"v_lshlrev_b32 v12, 4, v12\n"
"v_lshl_add_u32 v242, v11, 7, v12\n"
"v_xor_b32 v243, 64, v242\n"
"v_lshrrev_b32 v12, 2, v11\n"
"v_lshl_add_u32 v12, v4, 3, v12\n"
"v_lshlrev_b32 v12, 7, v12\n"
"v_and_b32 v13, 3, v2\n"
"v_lshl_add_u32 v12, v13, 3, v12\n"
"v_and_b32 v13, 3, v3\n"
"v_lshl_add_u32 v12, v13, 5, v12\n"
"v_add_u32 v246, s37, v12\n"
"v_xor_b32 v247, 32, v246\n"
"v_xor_b32 v248, 64, v246\n"
"v_xor_b32 v249, 96, v246\n"
"v_mov_b32 v13, 2130706432\n"
"v_cmp_eq_u32 vcc, 0, v2\n"
"s_cmp_eq_u32 s30, 0\n"
"s_cselect_b32 s45, 0, 2130706432\n"
"v_cndmask_b32_e64 v230, v13, 0, vcc\n"
"s_lshl_b32 s39, s30, 8\n"
"s_add_u32 s39, s39, s46\n"
"s_add_u32 s98, s39, 128\n"
"s_and_b32 s39, s39, 1023\n"
"s_and_b32 s98, s98, 1023\n"
"s_lshl_b32 s50, s30, 15\n"
"s_sub_u32 s50, 1048576, s50\n"
"s_add_u32 s39, s39, s50\n"
"s_add_u32 s98, s98, s50\n"
"s_mov_b32 s55, 0x3e0293ee\n"
"s_waitcnt lgkmcnt(0)\n"
"s_add_u32 s28, s28, 0x7f4a7c15\n"
"s_addc_u32 s29, s29, 0x9e3779b9\n"
"v_mov_b32 v232, s28\n"
"v_mov_b32 v233, s29\n"
"s_cmp_lt_u32 s30, 2\n"
"s_cbranch_scc0 LV_%=\n"
"s_waitcnt vmcnt(25)\n"
"v_lshlrev_b32 v148, 16, v128\n"
"v_and_b32 v149, 0xffff0000, v128\n"
"v_lshlrev_b32 v150, 16, v129\n"
"v_and_b32 v151, 0xffff0000, v129\n"
"s_waitcnt vmcnt(24)\n"
"v_lshlrev_b32 v152, 16, v130\n"
"v_and_b32 v153, 0xffff0000, v130\n"
"v_lshlrev_b32 v154, 16, v131\n"
"v_and_b32 v155, 0xffff0000, v131\n"
"s_waitcnt vmcnt(23)\n"
"v_lshlrev_b32 v156, 16, v132\n"
"v_and_b32 v157, 0xffff0000, v132\n"
"v_lshlrev_b32 v158, 16, v133\n"
"v_and_b32 v159, 0xffff0000, v133\n"
"s_waitcnt vmcnt(22)\n"
"v_lshlrev_b32 v160, 16, v134\n"
"v_and_b32 v161, 0xffff0000, v134\n"
"v_lshlrev_b32 v162, 16, v135\n"
"v_and_b32 v163, 0xffff0000, v135\n"
"s_waitcnt vmcnt(21)\n"
"v_lshlrev_b32 v164, 16, v136\n"
"v_and_b32 v165, 0xffff0000, v136\n"
"v_lshlrev_b32 v166, 16, v137\n"
"v_and_b32 v167, 0xffff0000, v137\n"
"v_add_f32 v188, v148, v149\n"
"v_add_f32 v189, v150, v151\n"
"v_add_f32 v188, v188, v152\n"
"v_add_f32 v189, v189, v153\n"
"v_add_f32 v188, v188, v154\n"
"v_add_f32 v189, v189, v155\n"
"v_add_f32 v188, v188, v156\n"
"v_add_f32 v189, v189, v157\n"
"v_add_f32 v188, v188, v158\n"
"v_add_f32 v189, v189, v159\n"
"v_add_f32 v188, v188, v160\n"
"v_add_f32 v189, v189, v161\n"
"v_add_f32 v188, v188, v162\n"
"v_add_f32 v189, v189, v163\n"
"v_add_f32 v188, v188, v164\n"
"v_add_f32 v189, v189, v165\n"
"v_add_f32 v188, v188, v166\n"
"v_add_f32 v189, v189, v167\n"
"v_add_f32 v188, v188, v189\n"
"s_nop 1\n"
"v_add_f32_dpp v188, v188, v188 quad_perm:[1,0,3,2] row_mask:0xf bank_mask:0xf\n"
"s_waitcnt vmcnt(20)\n"
"v_lshlrev_b32 v168, 16, v138\n"
"v_and_b32 v169, 0xffff0000, v138\n"
"v_add_f32_dpp v188, v188, v188 quad_perm:[2,3,0,1] row_mask:0xf bank_mask:0xf\n"
"v_lshlrev_b32 v170, 16, v139\n"
"v_and_b32 v171, 0xffff0000, v139\n"
"v_add_f32_dpp v188, v188, v188 row_half_mirror row_mask:0xf bank_mask:0xf\n"
"s_waitcnt vmcnt(19)\n"
"v_lshlrev_b32 v172, 16, v140\n"
"v_and_b32 v173, 0xffff0000, v140\n"
"v_add_f32_dpp v188, v188, v188 row_mirror row_mask:0xf bank_mask:0xf\n"
"v_lshlrev_b32 v174, 16, v141\n"
"v_and_b32 v175, 0xffff0000, v141\n"
"v_mov_b32 v189, v188\n"
"s_waitcnt vmcnt(18)\n"
"v_lshlrev_b32 v176, 16, v142\n"
"v_and_b32 v177, 0xffff0000, v142\n"
"v_permlane16_swap_b32 v189, v188\n"
"v_lshlrev_b32 v178, 16, v143\n"
"v_and_b32 v179, 0xffff0000, v143\n"
"v_add_f32 v188, v188, v189\n"
"s_waitcnt vmcnt(17)\n"
"v_lshlrev_b32 v180, 16, v144\n"
"v_and_b32 v181, 0xffff0000, v144\n"
"v_mul_f32 v191, 0x3acccccd, v188\n"
"v_lshlrev_b32 v182, 16, v145\n"
"v_and_b32 v183, 0xffff0000, v145\n"
"v_cvt_pk_bf16_f32 v128, v191, v191\n"
"s_waitcnt vmcnt(16)\n"
"v_lshlrev_b32 v184, 16, v146\n"
"v_and_b32 v185, 0xffff0000, v146\n"
"v_lshlrev_b32 v128, 16, v128\n"
"v_lshlrev_b32 v186, 16, v147\n"
"v_and_b32 v187, 0xffff0000, v147\n"
"s_add_u32 m0, s36, 16384\n"
"s_add_u32 s48, s46, 128\n"
"s_and_b32 s48, s48, 1023\n"
"buffer_load_dwordx4 v234, s[12:15], s48 offen offset:0 lds\n"
"buffer_load_dwordx4 v235, s[12:15], s48 offen offset:1024 lds\n"
"buffer_load_dwordx4 v236, s[12:15], s48 offen offset:2048 lds\n"
"buffer_load_dwordx4 v237, s[12:15], s48 offen offset:3072 lds\n"
"v_sub_f32 v148, v148, v128\n"
"v_sub_f32 v149, v149, v128\n"
"v_sub_f32 v150, v150, v128\n"
"v_sub_f32 v151, v151, v128\n"
"v_sub_f32 v152, v152, v128\n"
"v_sub_f32 v153, v153, v128\n"
"v_sub_f32 v154, v154, v128\n"
"v_sub_f32 v155, v155, v128\n"
"v_sub_f32 v156, v156, v128\n"
"v_sub_f32 v157, v157, v128\n"
"v_sub_f32 v158, v158, v128\n"
"v_sub_f32 v159, v159, v128\n"
"v_sub_f32 v160, v160, v128\n"
"v_sub_f32 v161, v161, v128\n"
"v_sub_f32 v162, v162, v128\n"
"v_sub_f32 v163, v163, v128\n"
"v_sub_f32 v164, v164, v128\n"
"v_sub_f32 v165, v165, v128\n"
"v_sub_f32 v166, v166, v128\n"
"v_sub_f32 v167, v167, v128\n"
"v_mul_f32 v188, v148, v148\n"
"v_mul_f32 v189, v149, v149\n"
"v_fmac_f32 v188, v150, v150\n"
"v_fmac_f32 v189, v151, v151\n"
"v_fmac_f32 v188, v152, v152\n"
"v_fmac_f32 v189, v153, v153\n"
"v_fmac_f32 v188, v154, v154\n"
"v_fmac_f32 v189, v155, v155\n"
"v_fmac_f32 v188, v156, v156\n"
"v_fmac_f32 v189, v157, v157\n"
"v_fmac_f32 v188, v158, v158\n"
"v_fmac_f32 v189, v159, v159\n"
"v_fmac_f32 v188, v160, v160\n"
"v_fmac_f32 v189, v161, v161\n"
"v_fmac_f32 v188, v162, v162\n"
"v_fmac_f32 v189, v163, v163\n"
"v_fmac_f32 v188, v164, v164\n"
"v_fmac_f32 v189, v165, v165\n"
"v_fmac_f32 v188, v166, v166\n"
"v_fmac_f32 v189, v167, v167\n"
"v_add_f32 v188, v188, v189\n"
"v_cvt_pk_bf16_f32 v149, v148, v149\n"
"v_lshlrev_b32 v148, 16, v149\n"
"v_add_f32_dpp v188, v188, v188 quad_perm:[1,0,3,2] row_mask:0xf bank_mask:0xf\n"
"v_and_b32 v149, 0xffff0000, v149\n"
"v_cvt_pk_bf16_f32 v151, v150, v151\n"
"v_add_f32_dpp v188, v188, v188 quad_perm:[2,3,0,1] row_mask:0xf bank_mask:0xf\n"
"v_lshlrev_b32 v150, 16, v151\n"
"v_and_b32 v151, 0xffff0000, v151\n"
"v_add_f32_dpp v188, v188, v188 row_half_mirror row_mask:0xf bank_mask:0xf\n"
"v_cvt_pk_bf16_f32 v153, v152, v153\n"
"v_lshlrev_b32 v152, 16, v153\n"
"v_add_f32_dpp v188, v188, v188 row_mirror row_mask:0xf bank_mask:0xf\n"
"v_and_b32 v153, 0xffff0000, v153\n"
"v_cvt_pk_bf16_f32 v155, v154, v155\n"
"v_mov_b32 v189, v188\n"
"v_lshlrev_b32 v154, 16, v155\n"
"v_and_b32 v155, 0xffff0000, v155\n"
"v_permlane16_swap_b32 v189, v188\n"
"v_cvt_pk_bf16_f32 v157, v156, v157\n"
"v_lshlrev_b32 v156, 16, v157\n"
"v_add_f32 v188, v188, v189\n"
"v_and_b32 v157, 0xffff0000, v157\n"
"v_cvt_pk_bf16_f32 v159, v158, v159\n"
"v_sub_f32 v191, v191, v128\n"
"v_lshlrev_b32 v158, 16, v159\n"
"v_and_b32 v159, 0xffff0000, v159\n"
"v_mul_f32 v188, 0x3acccccd, v188\n"
"v_cvt_pk_bf16_f32 v161, v160, v161\n"
"s_add_u32 m0, s37, 12288\n"
"s_add_u32 s48, s47, 786432\n"
"s_and_b32 s48, s48, 4194303\n"
"buffer_load_dwordx4 v238, s[16:19], s48 offen offset:0 lds\n"
"buffer_load_dwordx4 v239, s[16:19], s48 offen offset:1024 lds\n"
"buffer_load_dwordx4 v240, s[16:19], s48 offen offset:2048 lds\n"
"buffer_load_dwordx4 v241, s[16:19], s48 offen offset:3072 lds\n"
"v_lshlrev_b32 v160, 16, v161\n"
"v_fma_f32 v188, -v191, v191, v188\n"
"v_and_b32 v161, 0xffff0000, v161\n"
"v_cvt_pk_bf16_f32 v163, v162, v163\n"
"v_max_f32 v188, 0, v188\n"
"v_lshlrev_b32 v162, 16, v163\n"
"v_and_b32 v163, 0xffff0000, v163\n"
"v_cvt_pk_bf16_f32 v188, v188, v188\n"
"v_cvt_pk_bf16_f32 v165, v164, v165\n"
"v_lshlrev_b32 v164, 16, v165\n"
"v_lshlrev_b32 v188, 16, v188\n"
"v_and_b32 v165, 0xffff0000, v165\n"
"v_cvt_pk_bf16_f32 v167, v166, v167\n"
"v_add_f32 v188, 0x3727c5ac, v188\n"
"v_lshlrev_b32 v166, 16, v167\n"
"v_and_b32 v167, 0xffff0000, v167\n"
"v_cvt_pk_bf16_f32 v188, v188, v188\n"
"v_bfe_u32 v130, v224, 5, 4\n"
"v_mul_u32_u24 v130, 144, v130\n"
"v_lshlrev_b32 v188, 16, v188\n"
"v_bfe_u32 v131, v224, 4, 1\n"
"v_lshl_add_u32 v130, v131, 3, v130\n"
"v_sqrt_f32 v189, v188\n"
"v_lshrrev_b32 v131, 9, v224\n"
"v_lshl_add_u32 v130, v131, 4, v130\n"
"v_rcp_f32 v190, v189\n"
"s_lshl_b32 s52, s30, 5\n"
"v_add_u32 v130, s52, v130\n"
"v_fma_f32 v188, -v189, v189, v188\n"
"v_mul_f32 v190, 0.5, v190\n"
"v_fma_f32 v189, v188, v190, v189\n"
"v_cvt_pk_bf16_f32 v189, v189, v189\n"
"v_lshlrev_b32 v189, 16, v189\n"
"v_rcp_f32 v129, v189\n"
"s_nop 0\n"
"v_mul_f32 v148, v148, v129\n"
"v_mul_f32 v149, v149, v129\n"
"v_cvt_pk_bf16_f32 v138, v148, v149\n"
"v_mul_f32 v150, v150, v129\n"
"v_mul_f32 v151, v151, v129\n"
"v_cvt_pk_bf16_f32 v139, v150, v151\n"
"ds_write_b64 v130, v[138:139] offset:49152\n"
"v_mul_f32 v152, v152, v129\n"
"v_mul_f32 v153, v153, v129\n"
"v_cvt_pk_bf16_f32 v140, v152, v153\n"
"v_mul_f32 v154, v154, v129\n"
"v_mul_f32 v155, v155, v129\n"
"v_cvt_pk_bf16_f32 v141, v154, v155\n"
"ds_write_b64 v130, v[140:141] offset:51456\n"
"v_mul_f32 v156, v156, v129\n"
"v_mul_f32 v157, v157, v129\n"
"v_cvt_pk_bf16_f32 v142, v156, v157\n"
"v_mul_f32 v158, v158, v129\n"
"v_mul_f32 v159, v159, v129\n"
"v_cvt_pk_bf16_f32 v143, v158, v159\n"
"ds_write_b64 v130, v[142:143] offset:53760\n"
"v_mul_f32 v160, v160, v129\n"
"v_mul_f32 v161, v161, v129\n"
"v_cvt_pk_bf16_f32 v144, v160, v161\n"
"v_mul_f32 v162, v162, v129\n"
"v_mul_f32 v163, v163, v129\n"
"v_cvt_pk_bf16_f32 v145, v162, v163\n"
"ds_write_b64 v130, v[144:145] offset:56064\n"
"v_mul_f32 v164, v164, v129\n"
"v_mul_f32 v165, v165, v129\n"
"v_cvt_pk_bf16_f32 v146, v164, v165\n"
"v_mul_f32 v166, v166, v129\n"
"v_mul_f32 v167, v167, v129\n"
"v_cvt_pk_bf16_f32 v147, v166, v167\n"
"ds_write_b64 v130, v[146:147] offset:58368\n"
"s_waitcnt vmcnt(20)\n"
"ds_read_b64_tr_b16 a[0:1], v246 offset:0\n"
"ds_read_b64_tr_b16 a[2:3], v246 offset:512\n"
"ds_read_b64_tr_b16 a[4:5], v247 offset:0\n"
"ds_read_b64_tr_b16 a[6:7], v247 offset:512\n"
"ds_read_b64_tr_b16 a[8:9], v248 offset:0\n"
"ds_read_b64_tr_b16 a[10:11], v248 offset:512\n"
"ds_read_b64_tr_b16 a[12:13], v249 offset:0\n"
"ds_read_b64_tr_b16 a[14:15], v249 offset:512\n"
"s_waitcnt vmcnt(12)\n"
"ds_read_b64_tr_b16 a[16:17], v246 offset:4096\n"
"ds_read_b64_tr_b16 a[18:19], v246 offset:4608\n"
"s_waitcnt lgkmcnt(10)\n"
"s_barrier\n"
"ds_read_b128 v[192:195], v242 offset:0\n"
"ds_read_b128 v[196:199], v242 offset:2048\n"
"ds_read_b128 v[200:203], v242 offset:4096\n"
"ds_read_b128 v[204:207], v242 offset:6144\n"
"ds_read_b128 v[208:211], v242 offset:8192\n"
"v_lshrrev_b32 v131, 7, v224\n"
"s_waitcnt lgkmcnt(4)\n"
"v_mfma_f32_16x16x32_bf16 v[0:3], a[0:3], v[192:195], 0\n"
"s_add_u32 m0, s37, 16384\n"
"ds_read_b128 v[212:215], v242 offset:10240\n"
"v_lshl_add_u32 v131, v131, 4, v224\n"
"v_mfma_f32_16x16x32_bf16 v[4:7], a[4:7], v[192:195], 0\n"
"s_add_u32 s48, s47, 1048576\n"
"s_and_b32 s48, s48, 4194303\n"
"s_mul_i32 s52, s30, 1152\n"
"v_mfma_f32_16x16x32_bf16 v[8:11], a[8:11], v[192:195], 0\n"
"ds_read_b64_tr_b16 a[20:21], v247 offset:4096\n"
"v_add_u32 v128, s52, v131\n"
"v_mfma_f32_16x16x32_bf16 v[12:15], a[12:15], v[192:195], 0\n"
"s_waitcnt lgkmcnt(5)\n"
"buffer_load_dwordx4 v238, s[16:19], s48 offen offset:0 lds\n"
"v_mfma_f32_16x16x32_bf16 v[16:19], a[0:3], v[196:199], 0\n"
"s_and_b32 s52, s30, 1\n"
"ds_read_b128 v[216:219], v242 offset:12288\n"
"v_lshlrev_b32 v148, 16, v138\n"
"v_mfma_f32_16x16x32_bf16 v[20:23], a[4:7], v[196:199], 0\n"
"s_mul_i32 s51, s52, 1152\n"
"v_and_b32 v149, 0xffff0000, v138\n"
"v_add_u32 v131, s51, v131\n"
"v_mfma_f32_16x16x32_bf16 v[24:27], a[8:11], v[196:199], 0\n"
"s_lshl_b32 s52, s52, 10\n"
"ds_read_b64_tr_b16 a[22:23], v247 offset:4608\n"
"v_mul_f32 v188, v148, v168\n"
"v_mfma_f32_16x16x32_bf16 v[28:31], a[12:15], v[196:199], 0\n"
"s_waitcnt lgkmcnt(3)\n"
"buffer_load_dwordx4 v239, s[16:19], s48 offen offset:1024 lds\n"
"v_mfma_f32_16x16x32_bf16 v[32:35], a[0:3], v[200:203], 0\n"
"ds_read_b128 v[220:223], v242 offset:14336\n"
"v_and_b32 v129, 0x70, v224\n"
"v_mfma_f32_16x16x32_bf16 v[36:39], a[4:7], v[200:203], 0\n"
"s_lshr_b32 s50, s31, 1\n"
"ds_read_b128 v[132:135], v128 offset:49152\n"
"v_mul_f32 v189, v149, v169\n"
"v_mfma_f32_16x16x32_bf16 v[40:43], a[8:11], v[200:203], 0\n"
"s_mul_i32 s50, s50, 20480\n"
"ds_read_b128 a[240:243], v128 offset:53760\n"
"v_and_b32 v130, 0x380, v224\n"
"v_mfma_f32_16x16x32_bf16 v[44:47], a[12:15], v[200:203], 0\n"
"s_and_b32 s51, s31, 1\n"
"ds_read_b128 a[244:247], v131 offset:58368\n"
"v_lshlrev_b32 v150, 16, v139\n"
"v_mfma_f32_16x16x32_bf16 v[48:51], a[0:3], v[204:207], 0\n"
"s_lshl_b32 s51, s51, 7\n"
"ds_read_b128 v[192:195], v243 offset:0\n"
"v_lshl_add_u32 v129, v130, 1, v129\n"
"v_mfma_f32_16x16x32_bf16 v[52:55], a[4:7], v[204:207], 0\n"
"s_add_u32 s50, s50, s51\n"
"ds_read_b64_tr_b16 a[24:25], v248 offset:4096\n"
"v_and_b32 v151, 0xffff0000, v139\n"
"v_mfma_f32_16x16x32_bf16 v[56:59], a[8:11], v[204:207], 0\n"
"s_add_u32 s50, s50, 65536\n"
"buffer_load_dwordx4 v240, s[16:19], s48 offen offset:2048 lds\n"
"v_mfma_f32_16x16x32_bf16 v[60:63], a[12:15], v[204:207], 0\n"
"s_lshl_b32 s52, s52, 1\n"
"ds_read_b128 v[196:199], v243 offset:2048\n"
"v_fmac_f32 v188, v150, v170\n"
"v_mfma_f32_16x16x32_bf16 v[64:67], a[0:3], v[208:211], 0\n"
"s_add_u32 s52, s52, s50\n"
"ds_read_b64_tr_b16 a[26:27], v248 offset:4608\n"
"v_fmac_f32 v189, v151, v171\n"
"v_mfma_f32_16x16x32_bf16 v[68:71], a[4:7], v[208:211], 0\n"
"s_add_u32 s52, s52, 16384\n"
"buffer_load_dwordx4 v241, s[16:19], s48 offen offset:3072 lds\n"
"v_mfma_f32_16x16x32_bf16 v[72:75], a[8:11], v[208:211], 0\n"
"s_add_u32 m0, s36, 32768\n"
"ds_read_b128 v[200:203], v243 offset:4096\n"
"v_lshlrev_b32 v152, 16, v140\n"
"v_mfma_f32_16x16x32_bf16 v[76:79], a[12:15], v[208:211], 0\n"
"s_add_u32 s48, s46, 256\n"
"ds_read_b64_tr_b16 a[28:29], v249 offset:4096\n"
"v_and_b32 v153, 0xffff0000, v140\n"
"v_mfma_f32_16x16x32_bf16 v[80:83], a[0:3], v[212:215], 0\n"
"s_and_b32 s48, s48, 1023\n"
"v_fmac_f32 v188, v152, v172\n"
"v_fmac_f32 v189, v153, v173\n"
"v_mfma_f32_16x16x32_bf16 v[84:87], a[4:7], v[212:215], 0\n"
"s_lshl_b32 s51, s30, 11\n"
"buffer_load_dwordx4 v234, s[12:15], s48 offen offset:0 lds\n"
"v_mfma_f32_16x16x32_bf16 v[88:91], a[8:11], v[212:215], 0\n"
"s_add_u32 s50, s50, s51\n"
"ds_read_b128 v[204:207], v243 offset:6144\n"
"v_lshlrev_b32 v154, 16, v141\n"
"v_mfma_f32_16x16x32_bf16 v[92:95], a[12:15], v[212:215], 0\n"
"s_waitcnt lgkmcnt(3)\n"
"ds_read_b64_tr_b16 a[30:31], v249 offset:4608\n"
"v_and_b32 v155, 0xffff0000, v141\n"
"v_mfma_f32_16x16x32_bf16 v[96:99], a[0:3], v[216:219], 0\n"
"s_add_u32 s51, s50, 8192\n"
"buffer_store_dwordx4 v[132:135], v129, s[8:11], s50 offen sc1\n"
"v_mfma_f32_16x16x32_bf16 v[100:103], a[4:7], v[216:219], 0\n"
"ds_read_b128 v[208:211], v243 offset:8192\n"
"v_fmac_f32 v188, v154, v174\n"
"v_mfma_f32_16x16x32_bf16 v[104:107], a[8:11], v[216:219], 0\n"
"buffer_load_dwordx4 v235, s[12:15], s48 offen offset:1024 lds\n"
"v_mfma_f32_16x16x32_bf16 v[108:111], a[12:15], v[216:219], 0\n"
"buffer_store_dwordx4 a[240:243], v129, s[8:11], s51 offen sc1\n"
"v_mfma_f32_16x16x32_bf16 v[112:115], a[0:3], v[220:223], 0\n"
"s_waitcnt vmcnt(16)\n"
"ds_read_b64_tr_b16 a[32:33], v246 offset:8192\n"
"v_fmac_f32 v189, v155, v175\n"
"v_mfma_f32_16x16x32_bf16 v[116:119], a[4:7], v[220:223], 0\n"
"buffer_store_dwordx4 a[244:247], v129, s[8:11], s52 offen sc1\n"
"v_mfma_f32_16x16x32_bf16 v[120:123], a[8:11], v[220:223], 0\n"
"ds_read_b128 v[212:215], v243 offset:10240\n"
"v_lshlrev_b32 v156, 16, v142\n"
"v_mfma_f32_16x16x32_bf16 v[124:127], a[12:15], v[220:223], 0\n"
"buffer_load_dwordx4 v236, s[12:15], s48 offen offset:2048 lds\n"
"v_mfma_f32_16x16x32_bf16 v[0:3], a[16:19], v[192:195], v[0:3]\n"
"ds_read_b64_tr_b16 a[34:35], v246 offset:8704\n"
"v_and_b32 v157, 0xffff0000, v142\n"
"v_mfma_f32_16x16x32_bf16 v[4:7], a[20:23], v[192:195], v[4:7]\n"
"ds_read_b128 v[216:219], v243 offset:12288\n"
"v_fmac_f32 v188, v156, v176\n"
"v_mfma_f32_16x16x32_bf16 v[8:11], a[24:27], v[192:195], v[8:11]\n"
"s_waitcnt lgkmcnt(4)\n"
"buffer_load_dwordx4 v237, s[12:15], s48 offen offset:3072 lds\n"
"v_mfma_f32_16x16x32_bf16 v[12:15], a[28:31], v[192:195], v[12:15]\n"
"ds_read_b64_tr_b16 a[36:37], v247 offset:8192\n"
"v_fmac_f32 v189, v157, v177\n"
"v_mfma_f32_16x16x32_bf16 v[16:19], a[16:19], v[196:199], v[16:19]\n"
"s_add_u32 m0, s37, 20480\n"
"ds_read_b128 v[220:223], v243 offset:14336\n"
"v_lshlrev_b32 v158, 16, v143\n"
"v_mfma_f32_16x16x32_bf16 v[20:23], a[20:23], v[196:199], v[20:23]\n"
"s_add_u32 s48, s47, 1310720\n"
"v_and_b32 v159, 0xffff0000, v143\n"
"v_fmac_f32 v188, v158, v178\n"
"v_mfma_f32_16x16x32_bf16 v[24:27], a[24:27], v[196:199], v[24:27]\n"
"s_and_b32 s48, s48, 4194303\n"
"ds_read_b64_tr_b16 a[38:39], v247 offset:8704\n"
"v_fmac_f32 v189, v159, v179\n"
"v_mfma_f32_16x16x32_bf16 v[28:31], a[28:31], v[196:199], v[28:31]\n"
"buffer_load_dwordx4 v238, s[16:19], s48 offen offset:0 lds\n"
"v_mfma_f32_16x16x32_bf16 v[32:35], a[16:19], v[200:203], v[32:35]\n"
"v_lshlrev_b32 v160, 16, v144\n"
"v_and_b32 v161, 0xffff0000, v144\n"
"v_mfma_f32_16x16x32_bf16 v[36:39], a[20:23], v[200:203], v[36:39]\n"
"v_fmac_f32 v188, v160, v180\n"
"v_fmac_f32 v189, v161, v181\n"
"v_mfma_f32_16x16x32_bf16 v[40:43], a[24:27], v[200:203], v[40:43]\n"
"ds_read_b64_tr_b16 a[40:41], v248 offset:8192\n"
"v_lshlrev_b32 v162, 16, v145\n"
"v_mfma_f32_16x16x32_bf16 v[44:47], a[28:31], v[200:203], v[44:47]\n"
"buffer_load_dwordx4 v239, s[16:19], s48 offen offset:1024 lds\n"
"v_mfma_f32_16x16x32_bf16 v[48:51], a[16:19], v[204:207], v[48:51]\n"
"v_and_b32 v163, 0xffff0000, v145\n"
"v_fmac_f32 v188, v162, v182\n"
"v_mfma_f32_16x16x32_bf16 v[52:55], a[20:23], v[204:207], v[52:55]\n"
"s_waitcnt vmcnt(17) lgkmcnt(2)\n"
"s_barrier\n"
"v_mfma_f32_16x16x32_bf16 v[56:59], a[24:27], v[204:207], v[56:59]\n"
"ds_read_b128 v[192:195], v242 offset:16384\n"
"v_fmac_f32 v189, v163, v183\n"
"v_mfma_f32_16x16x32_bf16 v[60:63], a[28:31], v[204:207], v[60:63]\n"
"ds_read_b64_tr_b16 a[42:43], v248 offset:8704\n"
"v_lshlrev_b32 v164, 16, v146\n"
"v_mfma_f32_16x16x32_bf16 v[64:67], a[16:19], v[208:211], v[64:67]\n"
"buffer_load_dwordx4 v240, s[16:19], s48 offen offset:2048 lds\n"
"v_mfma_f32_16x16x32_bf16 v[68:71], a[20:23], v[208:211], v[68:71]\n"
"v_and_b32 v165, 0xffff0000, v146\n"
"v_fmac_f32 v188, v164, v184\n"
"v_mfma_f32_16x16x32_bf16 v[72:75], a[24:27], v[208:211], v[72:75]\n"
"ds_read_b128 v[196:199], v242 offset:18432\n"
"v_fmac_f32 v189, v165, v185\n"
"v_mfma_f32_16x16x32_bf16 v[76:79], a[28:31], v[208:211], v[76:79]\n"
"ds_read_b64_tr_b16 a[44:45], v249 offset:8192\n"
"v_lshlrev_b32 v166, 16, v147\n"
"v_mfma_f32_16x16x32_bf16 v[80:83], a[16:19], v[212:215], v[80:83]\n"
"s_lshl_b32 s90, s31, 2\n"
"buffer_load_dwordx4 v241, s[16:19], s48 offen offset:3072 lds\n"
"v_mfma_f32_16x16x32_bf16 v[84:87], a[20:23], v[212:215], v[84:87]\n"
"s_add_u32 s90, s90, s30\n"
"ds_read_b128 v[200:203], v242 offset:20480\n"
"v_and_b32 v167, 0xffff0000, v147\n"
"v_mfma_f32_16x16x32_bf16 v[88:91], a[24:27], v[212:215], v[88:91]\n"
"s_add_u32 m0, s37, 0\n"
"ds_read_b64_tr_b16 a[46:47], v249 offset:8704\n"
"v_fmac_f32 v188, v166, v186\n"
"v_mfma_f32_16x16x32_bf16 v[92:95], a[28:31], v[212:215], v[92:95]\n"
"s_add_u32 s48, s47, 1572864\n"
"v_fmac_f32 v189, v167, v187\n"
"v_add_f32 v188, v188, v189\n"
"v_mfma_f32_16x16x32_bf16 v[96:99], a[16:19], v[216:219], v[96:99]\n"
"s_and_b32 s48, s48, 4194303\n"
"v_add_f32_dpp v188, v188, v188 quad_perm:[1,0,3,2] row_mask:0xf bank_mask:0xf\n"
"s_lshl_b32 s90, s90, 1\n"
"v_mfma_f32_16x16x32_bf16 v[100:103], a[20:23], v[216:219], v[100:103]\n"
"s_sub_u32 s91, 2047, s90\n"
"buffer_load_dwordx4 v238, s[16:19], s48 offen offset:0 lds\n"
"v_mfma_f32_16x16x32_bf16 v[104:107], a[24:27], v[216:219], v[104:107]\n"
"ds_read_b128 v[204:207], v242 offset:22528\n"
"v_add_f32_dpp v188, v188, v188 quad_perm:[2,3,0,1] row_mask:0xf bank_mask:0xf\n"
"v_mfma_f32_16x16x32_bf16 v[108:111], a[28:31], v[216:219], v[108:111]\n"
"s_waitcnt vmcnt(16)\n"
"ds_read_b64_tr_b16 a[48:49], v246 offset:12288\n"
"v_add_f32_dpp v188, v188, v188 row_half_mirror row_mask:0xf bank_mask:0xf\n"
"v_mfma_f32_16x16x32_bf16 v[112:115], a[16:19], v[220:223], v[112:115]\n"
"ds_read_b128 v[208:211], v242 offset:24576\n"
"v_add_f32_dpp v188, v188, v188 row_mirror row_mask:0xf bank_mask:0xf\n"
"v_mfma_f32_16x16x32_bf16 v[116:119], a[20:23], v[220:223], v[116:119]\n"
"s_lshl_b32 s90, s90, 2\n"
"buffer_load_dwordx4 v239, s[16:19], s48 offen offset:1024 lds\n"
"v_mfma_f32_16x16x32_bf16 v[120:123], a[24:27], v[220:223], v[120:123]\n"
"s_add_u32 s90, s90, 2686976\n"
"ds_read_b64_tr_b16 a[50:51], v246 offset:12800\n"
"v_mov_b32 v189, v188\n"
"v_mfma_f32_16x16x32_bf16 v[124:127], a[28:31], v[220:223], v[124:127]\n"
"s_waitcnt lgkmcnt(4)\n"
"v_permlane16_swap_b32 v189, v188\n"
"v_add_f32 v188, v188, v189\n"
"v_mfma_f32_16x16x32_bf16 v[0:3], a[32:35], v[192:195], v[0:3]\n"
"ds_read_b128 v[212:215], v242 offset:26624\n"
"v_cvt_pk_bf16_f32 v188, v188, v188\n"
"v_mfma_f32_16x16x32_bf16 v[4:7], a[36:39], v[192:195], v[4:7]\n"
"buffer_load_dwordx4 v240, s[16:19], s48 offen offset:2048 lds\n"
"v_mfma_f32_16x16x32_bf16 v[8:11], a[40:43], v[192:195], v[8:11]\n"
"ds_read_b64_tr_b16 a[52:53], v247 offset:12288\n"
"v_lshlrev_b32 v188, 16, v188\n"
"v_mfma_f32_16x16x32_bf16 v[12:15], a[44:47], v[192:195], v[12:15]\n"
"ds_read_b128 v[216:219], v242 offset:28672\n"
"v_ashrrev_i32 v189, 31, v188\n"
"v_mfma_f32_16x16x32_bf16 v[16:19], a[32:35], v[196:199], v[16:19]\n"
"v_or_b32 v189, 0x80000000, v189\n"
"v_xor_b32 v188, v188, v189\n"
"v_mfma_f32_16x16x32_bf16 v[20:23], a[36:39], v[196:199], v[20:23]\n"
"buffer_load_dwordx4 v241, s[16:19], s48 offen offset:3072 lds\n"
"v_mfma_f32_16x16x32_bf16 v[24:27], a[40:43], v[196:199], v[24:27]\n"
"ds_read_b64_tr_b16 a[54:55], v247 offset:12800\n"
"v_and_b32 v188, 0xffff0000, v188\n"
"v_mfma_f32_16x16x32_bf16 v[28:31], a[44:47], v[196:199], v[28:31]\n"
"ds_read_b128 v[220:223], v242 offset:30720\n"
"v_lshrrev_b32 v189, 9, v224\n"
"v_mfma_f32_16x16x32_bf16 v[32:35], a[32:35], v[200:203], v[32:35]\n"
"s_add_u32 m0, s36, 0\n"
"v_sub_u32 v190, s91, v189\n"
"v_or_b32 v188, v188, v190\n"
"v_mfma_f32_16x16x32_bf16 v[36:39], a[36:39], v[200:203], v[36:39]\n"
"s_add_u32 s48, s46, 384\n"
"v_and_b32 v190, 0x1f0, v224\n"
"v_lshlrev_b32 v190, 23, v190\n"
"v_mfma_f32_16x16x32_bf16 v[40:43], a[40:43], v[200:203], v[40:43]\n"
"s_and_b32 s48, s48, 1023\n"
"ds_read_b128 v[192:195], v243 offset:16384\n"
"v_lshl_add_u32 v190, v189, 2, v190\n"
"v_mfma_f32_16x16x32_bf16 v[44:47], a[44:47], v[200:203], v[44:47]\n"
"s_waitcnt lgkmcnt(3)\n"
"buffer_store_dword v188, v190, s[8:11], s90 offen sc1\n"
"v_mfma_f32_16x16x32_bf16 v[48:51], a[32:35], v[204:207], v[48:51]\n"
"ds_read_b64_tr_b16 a[56:57], v248 offset:12288\n"
"v_mfma_f32_16x16x32_bf16 v[52:55], a[36:39], v[204:207], v[52:55]\n"
"buffer_load_dwordx4 v234, s[12:15], s48 offen offset:0 lds\n"
"v_mfma_f32_16x16x32_bf16 v[56:59], a[40:43], v[204:207], v[56:59]\n"
"ds_read_b128 v[196:199], v243 offset:18432\n"
"v_mfma_f32_16x16x32_bf16 v[60:63], a[44:47], v[204:207], v[60:63]\n"
"ds_read_b64_tr_b16 a[58:59], v248 offset:12800\n"
"v_mfma_f32_16x16x32_bf16 v[64:67], a[32:35], v[208:211], v[64:67]\n"
"buffer_load_dwordx4 v235, s[12:15], s48 offen offset:1024 lds\n"
"v_mfma_f32_16x16x32_bf16 v[68:71], a[36:39], v[208:211], v[68:71]\n"
"ds_read_b128 v[200:203], v243 offset:20480\n"
"v_mfma_f32_16x16x32_bf16 v[72:75], a[40:43], v[208:211], v[72:75]\n"
"ds_read_b64_tr_b16 a[60:61], v249 offset:12288\n"
"v_mfma_f32_16x16x32_bf16 v[76:79], a[44:47], v[208:211], v[76:79]\n"
"buffer_load_dwordx4 v236, s[12:15], s48 offen offset:2048 lds\n"
"v_mfma_f32_16x16x32_bf16 v[80:83], a[32:35], v[212:215], v[80:83]\n"
"v_mfma_f32_16x16x32_bf16 v[84:87], a[36:39], v[212:215], v[84:87]\n"
"ds_read_b128 v[204:207], v243 offset:22528\n"
"v_mfma_f32_16x16x32_bf16 v[88:91], a[40:43], v[212:215], v[88:91]\n"
"ds_read_b64_tr_b16 a[62:63], v249 offset:12800\n"
"v_mfma_f32_16x16x32_bf16 v[92:95], a[44:47], v[212:215], v[92:95]\n"
"buffer_load_dwordx4 v237, s[12:15], s48 offen offset:3072 lds\n"
"v_mfma_f32_16x16x32_bf16 v[96:99], a[32:35], v[216:219], v[96:99]\n"
"ds_read_b128 v[208:211], v243 offset:24576\n"
"v_mfma_f32_16x16x32_bf16 v[100:103], a[36:39], v[216:219], v[100:103]\n"
"v_mfma_f32_16x16x32_bf16 v[104:107], a[40:43], v[216:219], v[104:107]\n"
"s_add_u32 m0, s37, 4096\n"
"s_waitcnt vmcnt(20)\n"
"ds_read_b64_tr_b16 a[64:65], v246 offset:16384\n"
"v_mfma_f32_16x16x32_bf16 v[108:111], a[44:47], v[216:219], v[108:111]\n"
"s_waitcnt lgkmcnt(4)\n"
"s_add_u32 s48, s47, 1835008\n"
"s_and_b32 s48, s48, 4194303\n"
"v_mfma_f32_16x16x32_bf16 v[112:115], a[32:35], v[220:223], v[112:115]\n"
"ds_read_b128 v[212:215], v243 offset:26624\n"
"v_mfma_f32_16x16x32_bf16 v[116:119], a[36:39], v[220:223], v[116:119]\n"
"buffer_load_dwordx4 v238, s[16:19], s48 offen offset:0 lds\n"
"v_mfma_f32_16x16x32_bf16 v[120:123], a[40:43], v[220:223], v[120:123]\n"
"ds_read_b64_tr_b16 a[66:67], v246 offset:16896\n"
"v_mfma_f32_16x16x32_bf16 v[124:127], a[44:47], v[220:223], v[124:127]\n"
"ds_read_b128 v[216:219], v243 offset:28672\n"
"v_mfma_f32_16x16x32_bf16 v[0:3], a[48:51], v[192:195], v[0:3]\n"
"buffer_load_dwordx4 v239, s[16:19], s48 offen offset:1024 lds\n"
"v_mfma_f32_16x16x32_bf16 v[4:7], a[52:55], v[192:195], v[4:7]\n"
"v_mfma_f32_16x16x32_bf16 v[8:11], a[56:59], v[192:195], v[8:11]\n"
"s_waitcnt lgkmcnt(3)\n"
"ds_read_b64_tr_b16 a[68:69], v247 offset:16384\n"
"v_mfma_f32_16x16x32_bf16 v[12:15], a[60:63], v[192:195], v[12:15]\n"
"ds_read_b128 v[220:223], v243 offset:30720\n"
"v_mfma_f32_16x16x32_bf16 v[16:19], a[48:51], v[196:199], v[16:19]\n"
"v_mfma_f32_16x16x32_bf16 v[20:23], a[52:55], v[196:199], v[20:23]\n"
"buffer_load_dwordx4 v240, s[16:19], s48 offen offset:2048 lds\n"
"v_mfma_f32_16x16x32_bf16 v[24:27], a[56:59], v[196:199], v[24:27]\n"
"ds_read_b64_tr_b16 a[70:71], v247 offset:16896\n"
"v_mfma_f32_16x16x32_bf16 v[28:31], a[60:63], v[196:199], v[28:31]\n"
"v_mfma_f32_16x16x32_bf16 v[32:35], a[48:51], v[200:203], v[32:35]\n"
"v_mfma_f32_16x16x32_bf16 v[36:39], a[52:55], v[200:203], v[36:39]\n"
"buffer_load_dwordx4 v241, s[16:19], s48 offen offset:3072 lds\n"
"v_mfma_f32_16x16x32_bf16 v[40:43], a[56:59], v[200:203], v[40:43]\n"
"ds_read_b64_tr_b16 a[72:73], v248 offset:16384\n"
"v_mfma_f32_16x16x32_bf16 v[44:47], a[60:63], v[200:203], v[44:47]\n"
"v_mfma_f32_16x16x32_bf16 v[48:51], a[48:51], v[204:207], v[48:51]\n"
"s_add_u32 m0, s37, 8192\n"
"s_add_u32 s48, s47, 2097152\n"
"s_and_b32 s48, s48, 4194303\n"
"v_mfma_f32_16x16x32_bf16 v[52:55], a[52:55], v[204:207], v[52:55]\n"
"s_waitcnt vmcnt(17) lgkmcnt(2)\n"
"s_barrier\n"
"v_mfma_f32_16x16x32_bf16 v[56:59], a[56:59], v[204:207], v[56:59]\n"
"ds_read_b128 v[192:195], v242 offset:32768\n"
"v_mfma_f32_16x16x32_bf16 v[60:63], a[60:63], v[204:207], v[60:63]\n"
"ds_read_b64_tr_b16 a[74:75], v248 offset:16896\n"
"v_mfma_f32_16x16x32_bf16 v[64:67], a[48:51], v[208:211], v[64:67]\n"
"buffer_load_dwordx4 v238, s[16:19], s48 offen offset:0 lds\n"
"v_mfma_f32_16x16x32_bf16 v[68:71], a[52:55], v[208:211], v[68:71]\n"
"v_mfma_f32_16x16x32_bf16 v[72:75], a[56:59], v[208:211], v[72:75]\n"
"ds_read_b128 v[196:199], v242 offset:34816\n"
"v_mfma_f32_16x16x32_bf16 v[76:79], a[60:63], v[208:211], v[76:79]\n"
"ds_read_b64_tr_b16 a[76:77], v249 offset:16384\n"
"v_mfma_f32_16x16x32_bf16 v[80:83], a[48:51], v[212:215], v[80:83]\n"
"buffer_load_dwordx4 v239, s[16:19], s48 offen offset:1024 lds\n"
"v_mfma_f32_16x16x32_bf16 v[84:87], a[52:55], v[212:215], v[84:87]\n"
"ds_read_b128 v[200:203], v242 offset:36864\n"
"v_mfma_f32_16x16x32_bf16 v[88:91], a[56:59], v[212:215], v[88:91]\n"
"ds_read_b64_tr_b16 a[78:79], v249 offset:16896\n"
"v_mfma_f32_16x16x32_bf16 v[92:95], a[60:63], v[212:215], v[92:95]\n"
"buffer_load_dwordx4 v240, s[16:19], s48 offen offset:2048 lds\n"
"v_mfma_f32_16x16x32_bf16 v[96:99], a[48:51], v[216:219], v[96:99]\n"
"v_mfma_f32_16x16x32_bf16 v[100:103], a[52:55], v[216:219], v[100:103]\n"
"ds_read_b128 v[204:207], v242 offset:38912\n"
"v_mfma_f32_16x16x32_bf16 v[104:107], a[56:59], v[216:219], v[104:107]\n"
"s_waitcnt vmcnt(16)\n"
"ds_read_b64_tr_b16 a[80:81], v246 offset:20480\n"
"v_mfma_f32_16x16x32_bf16 v[108:111], a[60:63], v[216:219], v[108:111]\n"
"buffer_load_dwordx4 v241, s[16:19], s48 offen offset:3072 lds\n"
"v_mfma_f32_16x16x32_bf16 v[112:115], a[48:51], v[220:223], v[112:115]\n"
"ds_read_b128 v[208:211], v242 offset:40960\n"
"v_mfma_f32_16x16x32_bf16 v[116:119], a[52:55], v[220:223], v[116:119]\n"
"v_mfma_f32_16x16x32_bf16 v[120:123], a[56:59], v[220:223], v[120:123]\n"
"s_add_u32 m0, s36, 16384\n"
"ds_read_b64_tr_b16 a[82:83], v246 offset:20992\n"
"s_add_u32 s48, s46, 512\n"
"v_mfma_f32_16x16x32_bf16 v[124:127], a[60:63], v[220:223], v[124:127]\n"
"s_waitcnt lgkmcnt(4)\n"
"s_and_b32 s48, s48, 1023\n"
"v_mfma_f32_16x16x32_bf16 v[0:3], a[64:67], v[192:195], v[0:3]\n"
"ds_read_b128 v[212:215], v242 offset:43008\n"
"v_mfma_f32_16x16x32_bf16 v[4:7], a[68:71], v[192:195], v[4:7]\n"
"buffer_load_dwordx4 v234, s[12:15], s48 offen offset:0 lds\n"
"v_mfma_f32_16x16x32_bf16 v[8:11], a[72:75], v[192:195], v[8:11]\n"
"ds_read_b64_tr_b16 a[84:85], v247 offset:20480\n"
"v_mfma_f32_16x16x32_bf16 v[12:15], a[76:79], v[192:195], v[12:15]\n"
"ds_read_b128 v[216:219], v242 offset:45056\n"
"v_mfma_f32_16x16x32_bf16 v[16:19], a[64:67], v[196:199], v[16:19]\n"
"buffer_load_dwordx4 v235, s[12:15], s48 offen offset:1024 lds\n"
"v_mfma_f32_16x16x32_bf16 v[20:23], a[68:71], v[196:199], v[20:23]\n"
"v_mfma_f32_16x16x32_bf16 v[24:27], a[72:75], v[196:199], v[24:27]\n"
"ds_read_b64_tr_b16 a[86:87], v247 offset:20992\n"
"v_mfma_f32_16x16x32_bf16 v[28:31], a[76:79], v[196:199], v[28:31]\n"
"ds_read_b128 v[220:223], v242 offset:47104\n"
"v_mfma_f32_16x16x32_bf16 v[32:35], a[64:67], v[200:203], v[32:35]\n"
"v_mfma_f32_16x16x32_bf16 v[36:39], a[68:71], v[200:203], v[36:39]\n"
"buffer_load_dwordx4 v236, s[12:15], s48 offen offset:2048 lds\n"
"v_mfma_f32_16x16x32_bf16 v[40:43], a[72:75], v[200:203], v[40:43]\n"
"ds_read_b128 v[192:195], v243 offset:32768\n"
"v_mfma_f32_16x16x32_bf16 v[44:47], a[76:79], v[200:203], v[44:47]\n"
"s_waitcnt lgkmcnt(3)\n"
"ds_read_b64_tr_b16 a[88:89], v248 offset:20480\n"
"v_mfma_f32_16x16x32_bf16 v[48:51], a[64:67], v[204:207], v[48:51]\n"
"v_mfma_f32_16x16x32_bf16 v[52:55], a[68:71], v[204:207], v[52:55]\n"
"buffer_load_dwordx4 v237, s[12:15], s48 offen offset:3072 lds\n"
"v_mfma_f32_16x16x32_bf16 v[56:59], a[72:75], v[204:207], v[56:59]\n"
"ds_read_b128 v[196:199], v243 offset:34816\n"
"v_mfma_f32_16x16x32_bf16 v[60:63], a[76:79], v[204:207], v[60:63]\n"
"ds_read_b64_tr_b16 a[90:91], v248 offset:20992\n"
"v_mfma_f32_16x16x32_bf16 v[64:67], a[64:67], v[208:211], v[64:67]\n"
"s_add_u32 m0, s37, 12288\n"
"s_add_u32 s48, s47, 2359296\n"
"s_and_b32 s48, s48, 4194303\n"
"v_mfma_f32_16x16x32_bf16 v[68:71], a[68:71], v[208:211], v[68:71]\n"
"ds_read_b128 v[200:203], v243 offset:36864\n"
"v_mfma_f32_16x16x32_bf16 v[72:75], a[72:75], v[208:211], v[72:75]\n"
"ds_read_b64_tr_b16 a[92:93], v249 offset:20480\n"
"v_mfma_f32_16x16x32_bf16 v[76:79], a[76:79], v[208:211], v[76:79]\n"
"buffer_load_dwordx4 v238, s[16:19], s48 offen offset:0 lds\n"
"v_mfma_f32_16x16x32_bf16 v[80:83], a[64:67], v[212:215], v[80:83]\n"
"v_mfma_f32_16x16x32_bf16 v[84:87], a[68:71], v[212:215], v[84:87]\n"
"ds_read_b128 v[204:207], v243 offset:38912\n"
"v_mfma_f32_16x16x32_bf16 v[88:91], a[72:75], v[212:215], v[88:91]\n"
"ds_read_b64_tr_b16 a[94:95], v249 offset:20992\n"
"v_mfma_f32_16x16x32_bf16 v[92:95], a[76:79], v[212:215], v[92:95]\n"
"buffer_load_dwordx4 v239, s[16:19], s48 offen offset:1024 lds\n"
"v_mfma_f32_16x16x32_bf16 v[96:99], a[64:67], v[216:219], v[96:99]\n"
"ds_read_b128 v[208:211], v243 offset:40960\n"
"v_mfma_f32_16x16x32_bf16 v[100:103], a[68:71], v[216:219], v[100:103]\n"
"v_mfma_f32_16x16x32_bf16 v[104:107], a[72:75], v[216:219], v[104:107]\n"
"s_waitcnt vmcnt(19)\n"
"ds_read_b64_tr_b16 a[96:97], v246 offset:0\n"
"v_mfma_f32_16x16x32_bf16 v[108:111], a[76:79], v[216:219], v[108:111]\n"
"s_waitcnt lgkmcnt(4)\n"
"buffer_load_dwordx4 v240, s[16:19], s48 offen offset:2048 lds\n"
"v_mfma_f32_16x16x32_bf16 v[112:115], a[64:67], v[220:223], v[112:115]\n"
"ds_read_b128 v[212:215], v243 offset:43008\n"
"v_mfma_f32_16x16x32_bf16 v[116:119], a[68:71], v[220:223], v[116:119]\n"
"v_mfma_f32_16x16x32_bf16 v[120:123], a[72:75], v[220:223], v[120:123]\n"
"ds_read_b64_tr_b16 a[98:99], v246 offset:512\n"
"v_mfma_f32_16x16x32_bf16 v[124:127], a[76:79], v[220:223], v[124:127]\n"
"ds_read_b128 v[216:219], v243 offset:45056\n"
"v_mfma_f32_16x16x32_bf16 v[0:3], a[80:83], v[192:195], v[0:3]\n"
"buffer_load_dwordx4 v241, s[16:19], s48 offen offset:3072 lds\n"
"v_mfma_f32_16x16x32_bf16 v[4:7], a[84:87], v[192:195], v[4:7]\n"
"v_mfma_f32_16x16x32_bf16 v[8:11], a[88:91], v[192:195], v[8:11]\n"
"s_waitcnt lgkmcnt(3)\n"
"ds_read_b64_tr_b16 a[100:101], v247 offset:0\n"
"s_add_u32 m0, s37, 16384\n"
"v_mfma_f32_16x16x32_bf16 v[12:15], a[92:95], v[192:195], v[12:15]\n"
"s_add_u32 s48, s47, 2621440\n"
"ds_read_b128 v[220:223], v243 offset:47104\n"
"s_and_b32 s48, s48, 4194303\n"
"v_mfma_f32_16x16x32_bf16 v[16:19], a[80:83], v[196:199], v[16:19]\n"
"buffer_load_dwordx4 v238, s[16:19], s48 offen offset:0 lds\n"
"v_mfma_f32_16x16x32_bf16 v[20:23], a[84:87], v[196:199], v[20:23]\n"
"v_mfma_f32_16x16x32_bf16 v[24:27], a[88:91], v[196:199], v[24:27]\n"
"ds_read_b64_tr_b16 a[102:103], v247 offset:512\n"
"v_mfma_f32_16x16x32_bf16 v[28:31], a[92:95], v[196:199], v[28:31]\n"
"v_mfma_f32_16x16x32_bf16 v[32:35], a[80:83], v[200:203], v[32:35]\n"
"s_lshl_b32 s50, s31, 3\n"
"buffer_load_dwordx4 v239, s[16:19], s48 offen offset:1024 lds\n"
"v_mfma_f32_16x16x32_bf16 v[36:39], a[84:87], v[200:203], v[36:39]\n"
"s_add_u32 s50, s50, 256\n"
"s_add_u32 s50, s50, s45\n"
"v_mfma_f32_16x16x32_bf16 v[40:43], a[88:91], v[200:203], v[40:43]\n"
"ds_read_b64_tr_b16 a[104:105], v248 offset:0\n"
"v_mfma_f32_16x16x32_bf16 v[44:47], a[92:95], v[200:203], v[44:47]\n"
"v_mfma_f32_16x16x32_bf16 v[48:51], a[80:83], v[204:207], v[48:51]\n"
"v_mfma_f32_16x16x32_bf16 v[52:55], a[84:87], v[204:207], v[52:55]\n"
"s_waitcnt vmcnt(18) lgkmcnt(2)\n"
"s_barrier\n"
"v_mfma_f32_16x16x32_bf16 v[56:59], a[88:91], v[204:207], v[56:59]\n"
"buffer_load_dwordx4 v240, s[16:19], s48 offen offset:2048 lds\n"
"v_mfma_f32_16x16x32_bf16 v[60:63], a[92:95], v[204:207], v[60:63]\n"
"ds_read_b128 v[192:195], v242 offset:0\n"
"v_mfma_f32_16x16x32_bf16 v[64:67], a[80:83], v[208:211], v[64:67]\n"
"ds_read_b64_tr_b16 a[106:107], v248 offset:512\n"
"v_mfma_f32_16x16x32_bf16 v[68:71], a[84:87], v[208:211], v[68:71]\n"
"buffer_store_dwordx2 v[232:233], v230, s[8:11], s50 offen sc1\n"
"v_mfma_f32_16x16x32_bf16 v[72:75], a[88:91], v[208:211], v[72:75]\n"
"buffer_load_dwordx4 v241, s[16:19], s48 offen offset:3072 lds\n"
"v_mfma_f32_16x16x32_bf16 v[76:79], a[92:95], v[208:211], v[76:79]\n"
"ds_read_b128 v[196:199], v242 offset:2048\n"
"v_mfma_f32_16x16x32_bf16 v[80:83], a[80:83], v[212:215], v[80:83]\n"
"s_add_u32 m0, s36, 32768\n"
"ds_read_b64_tr_b16 a[108:109], v249 offset:0\n"
"s_add_u32 s48, s46, 640\n"
"v_mfma_f32_16x16x32_bf16 v[84:87], a[84:87], v[212:215], v[84:87]\n"
"s_and_b32 s48, s48, 1023\n"
"ds_read_b128 v[200:203], v242 offset:4096\n"
"v_mfma_f32_16x16x32_bf16 v[88:91], a[88:91], v[212:215], v[88:91]\n"
"ds_read_b64_tr_b16 a[110:111], v249 offset:512\n"
"v_mfma_f32_16x16x32_bf16 v[92:95], a[92:95], v[212:215], v[92:95]\n"
"buffer_load_dwordx4 v234, s[12:15], s48 offen offset:0 lds\n"
"v_mfma_f32_16x16x32_bf16 v[96:99], a[80:83], v[216:219], v[96:99]\n"
"v_mfma_f32_16x16x32_bf16 v[100:103], a[84:87], v[216:219], v[100:103]\n"
"ds_read_b128 v[204:207], v242 offset:6144\n"
"v_mfma_f32_16x16x32_bf16 v[104:107], a[88:91], v[216:219], v[104:107]\n"
"s_waitcnt vmcnt(18)\n"
"ds_read_b64_tr_b16 a[112:113], v246 offset:4096\n"
"v_mfma_f32_16x16x32_bf16 v[108:111], a[92:95], v[216:219], v[108:111]\n"
"buffer_load_dwordx4 v235, s[12:15], s48 offen offset:1024 lds\n"
"v_mfma_f32_16x16x32_bf16 v[112:115], a[80:83], v[220:223], v[112:115]\n"
"ds_read_b128 v[208:211], v242 offset:8192\n"
"v_mfma_f32_16x16x32_bf16 v[116:119], a[84:87], v[220:223], v[116:119]\n"
"v_mfma_f32_16x16x32_bf16 v[120:123], a[88:91], v[220:223], v[120:123]\n"
"ds_read_b64_tr_b16 a[114:115], v246 offset:4608\n"
"v_mfma_f32_16x16x32_bf16 v[124:127], a[92:95], v[220:223], v[124:127]\n"
"s_waitcnt lgkmcnt(4)\n"
"buffer_load_dwordx4 v236, s[12:15], s48 offen offset:2048 lds\n"
"v_mfma_f32_16x16x32_bf16 v[0:3], a[96:99], v[192:195], v[0:3]\n"
"ds_read_b128 v[212:215], v242 offset:10240\n"
"v_mfma_f32_16x16x32_bf16 v[4:7], a[100:103], v[192:195], v[4:7]\n"
"v_mfma_f32_16x16x32_bf16 v[8:11], a[104:107], v[192:195], v[8:11]\n"
"ds_read_b64_tr_b16 a[116:117], v247 offset:4096\n"
"v_mfma_f32_16x16x32_bf16 v[12:15], a[108:111], v[192:195], v[12:15]\n"
"ds_read_b128 v[216:219], v242 offset:12288\n"
"v_mfma_f32_16x16x32_bf16 v[16:19], a[96:99], v[196:199], v[16:19]\n"
"buffer_load_dwordx4 v237, s[12:15], s48 offen offset:3072 lds\n"
"v_mfma_f32_16x16x32_bf16 v[20:23], a[100:103], v[196:199], v[20:23]\n"
"v_mfma_f32_16x16x32_bf16 v[24:27], a[104:107], v[196:199], v[24:27]\n"
"s_add_u32 m0, s37, 20480\n"
"ds_read_b64_tr_b16 a[118:119], v247 offset:4608\n"
"s_add_u32 s48, s47, 2883584\n"
"v_mfma_f32_16x16x32_bf16 v[28:31], a[108:111], v[196:199], v[28:31]\n"
"s_and_b32 s48, s48, 4194303\n"
"ds_read_b128 v[220:223], v242 offset:14336\n"
"v_mfma_f32_16x16x32_bf16 v[32:35], a[96:99], v[200:203], v[32:35]\n"
"buffer_load_dwordx4 v238, s[16:19], s48 offen offset:0 lds\n"
"v_mfma_f32_16x16x32_bf16 v[36:39], a[100:103], v[200:203], v[36:39]\n"
"v_mfma_f32_16x16x32_bf16 v[40:43], a[104:107], v[200:203], v[40:43]\n"
"ds_read_b128 v[192:195], v243 offset:0\n"
"v_mfma_f32_16x16x32_bf16 v[44:47], a[108:111], v[200:203], v[44:47]\n"
"s_waitcnt lgkmcnt(3)\n"
"ds_read_b64_tr_b16 a[120:121], v248 offset:4096\n"
"v_mfma_f32_16x16x32_bf16 v[48:51], a[96:99], v[204:207], v[48:51]\n"
"buffer_load_dwordx4 v239, s[16:19], s48 offen offset:1024 lds\n"
"v_mfma_f32_16x16x32_bf16 v[52:55], a[100:103], v[204:207], v[52:55]\n"
"v_mfma_f32_16x16x32_bf16 v[56:59], a[104:107], v[204:207], v[56:59]\n"
"ds_read_b128 v[196:199], v243 offset:2048\n"
"v_mfma_f32_16x16x32_bf16 v[60:63], a[108:111], v[204:207], v[60:63]\n"
"ds_read_b64_tr_b16 a[122:123], v248 offset:4608\n"
"v_mfma_f32_16x16x32_bf16 v[64:67], a[96:99], v[208:211], v[64:67]\n"
"v_mfma_f32_16x16x32_bf16 v[68:71], a[100:103], v[208:211], v[68:71]\n"
"ds_read_b128 v[200:203], v243 offset:4096\n"
"v_mfma_f32_16x16x32_bf16 v[72:75], a[104:107], v[208:211], v[72:75]\n"
"buffer_load_dwordx4 v240, s[16:19], s48 offen offset:2048 lds\n"
"v_mfma_f32_16x16x32_bf16 v[76:79], a[108:111], v[208:211], v[76:79]\n"
"ds_read_b64_tr_b16 a[124:125], v249 offset:4096\n"
"v_mfma_f32_16x16x32_bf16 v[80:83], a[96:99], v[212:215], v[80:83]\n"
"v_mfma_f32_16x16x32_bf16 v[84:87], a[100:103], v[212:215], v[84:87]\n"
"ds_read_b128 v[204:207], v243 offset:6144\n"
"v_mfma_f32_16x16x32_bf16 v[88:91], a[104:107], v[212:215], v[88:91]\n"
"buffer_load_dwordx4 v241, s[16:19], s48 offen offset:3072 lds\n"
"v_mfma_f32_16x16x32_bf16 v[92:95], a[108:111], v[212:215], v[92:95]\n"
"ds_read_b64_tr_b16 a[126:127], v249 offset:4608\n"
"v_mfma_f32_16x16x32_bf16 v[96:99], a[96:99], v[216:219], v[96:99]\n"
"s_add_u32 m0, s37, 0\n"
"ds_read_b128 v[208:211], v243 offset:8192\n"
"s_add_u32 s48, s47, 3145728\n"
"v_mfma_f32_16x16x32_bf16 v[100:103], a[100:103], v[216:219], v[100:103]\n"
"s_and_b32 s48, s48, 4194303\n"
"v_mfma_f32_16x16x32_bf16 v[104:107], a[104:107], v[216:219], v[104:107]\n"
"s_waitcnt vmcnt(21)\n"
"ds_read_b64_tr_b16 a[128:129], v246 offset:8192\n"
"v_mfma_f32_16x16x32_bf16 v[108:111], a[108:111], v[216:219], v[108:111]\n"
"s_waitcnt lgkmcnt(4)\n"
"buffer_load_dwordx4 v238, s[16:19], s48 offen offset:0 lds\n"
"v_mfma_f32_16x16x32_bf16 v[112:115], a[96:99], v[220:223], v[112:115]\n"
"ds_read_b128 v[212:215], v243 offset:10240\n"
"v_mfma_f32_16x16x32_bf16 v[116:119], a[100:103], v[220:223], v[116:119]\n"
"v_mfma_f32_16x16x32_bf16 v[120:123], a[104:107], v[220:223], v[120:123]\n"
"ds_read_b64_tr_b16 a[130:131], v246 offset:8704\n"
"v_mfma_f32_16x16x32_bf16 v[124:127], a[108:111], v[220:223], v[124:127]\n"
"buffer_load_dwordx4 v239, s[16:19], s48 offen offset:1024 lds\n"
"v_mfma_f32_16x16x32_bf16 v[0:3], a[112:115], v[192:195], v[0:3]\n"
"ds_read_b128 v[216:219], v243 offset:12288\n"
"v_mfma_f32_16x16x32_bf16 v[4:7], a[116:119], v[192:195], v[4:7]\n"
"v_mfma_f32_16x16x32_bf16 v[8:11], a[120:123], v[192:195], v[8:11]\n"
"s_waitcnt lgkmcnt(3)\n"
"ds_read_b64_tr_b16 a[132:133], v247 offset:8192\n"
"v_mfma_f32_16x16x32_bf16 v[12:15], a[124:127], v[192:195], v[12:15]\n"
"ds_read_b128 v[220:223], v243 offset:14336\n"
"v_mfma_f32_16x16x32_bf16 v[16:19], a[112:115], v[196:199], v[16:19]\n"
"buffer_load_dwordx4 v240, s[16:19], s48 offen offset:2048 lds\n"
"v_mfma_f32_16x16x32_bf16 v[20:23], a[116:119], v[196:199], v[20:23]\n"
"v_mfma_f32_16x16x32_bf16 v[24:27], a[120:123], v[196:199], v[24:27]\n"
"ds_read_b64_tr_b16 a[134:135], v247 offset:8704\n"
"v_mfma_f32_16x16x32_bf16 v[28:31], a[124:127], v[196:199], v[28:31]\n"
"buffer_load_dwordx4 v241, s[16:19], s48 offen offset:3072 lds\n"
"v_mfma_f32_16x16x32_bf16 v[32:35], a[112:115], v[200:203], v[32:35]\n"
"v_mfma_f32_16x16x32_bf16 v[36:39], a[116:119], v[200:203], v[36:39]\n"
"v_mfma_f32_16x16x32_bf16 v[40:43], a[120:123], v[200:203], v[40:43]\n"
"ds_read_b64_tr_b16 a[136:137], v248 offset:8192\n"
"v_mfma_f32_16x16x32_bf16 v[44:47], a[124:127], v[200:203], v[44:47]\n"
"v_mfma_f32_16x16x32_bf16 v[48:51], a[112:115], v[204:207], v[48:51]\n"
"v_mfma_f32_16x16x32_bf16 v[52:55], a[116:119], v[204:207], v[52:55]\n"
"s_waitcnt vmcnt(21) lgkmcnt(2)\n"
"s_barrier\n"
"v_mfma_f32_16x16x32_bf16 v[56:59], a[120:123], v[204:207], v[56:59]\n"
"s_add_u32 m0, s36, 0\n"
"ds_read_b128 v[192:195], v242 offset:16384\n"
"s_add_u32 s48, s46, 768\n"
"v_mfma_f32_16x16x32_bf16 v[60:63], a[124:127], v[204:207], v[60:63]\n"
"s_and_b32 s48, s48, 1023\n"
"ds_read_b64_tr_b16 a[138:139], v248 offset:8704\n"
"v_mfma_f32_16x16x32_bf16 v[64:67], a[112:115], v[208:211], v[64:67]\n"
"buffer_load_dwordx4 v234, s[12:15], s48 offen offset:0 lds\n"
"v_mfma_f32_16x16x32_bf16 v[68:71], a[116:119], v[208:211], v[68:71]\n"
"v_mfma_f32_16x16x32_bf16 v[72:75], a[120:123], v[208:211], v[72:75]\n"
"ds_read_b128 v[196:199], v242 offset:18432\n"
"v_mfma_f32_16x16x32_bf16 v[76:79], a[124:127], v[208:211], v[76:79]\n"
"ds_read_b64_tr_b16 a[140:141], v249 offset:8192\n"
"v_mfma_f32_16x16x32_bf16 v[80:83], a[112:115], v[212:215], v[80:83]\n"
"buffer_load_dwordx4 v235, s[12:15], s48 offen offset:1024 lds\n"
"v_mfma_f32_16x16x32_bf16 v[84:87], a[116:119], v[212:215], v[84:87]\n"
"ds_read_b128 v[200:203], v242 offset:20480\n"
"v_mfma_f32_16x16x32_bf16 v[88:91], a[120:123], v[212:215], v[88:91]\n"
"ds_read_b64_tr_b16 a[142:143], v249 offset:8704\n"
"v_mfma_f32_16x16x32_bf16 v[92:95], a[124:127], v[212:215], v[92:95]\n"
"v_mfma_f32_16x16x32_bf16 v[96:99], a[112:115], v[216:219], v[96:99]\n"
"v_mfma_f32_16x16x32_bf16 v[100:103], a[116:119], v[216:219], v[100:103]\n"
"ds_read_b128 v[204:207], v242 offset:22528\n"
"v_mfma_f32_16x16x32_bf16 v[104:107], a[120:123], v[216:219], v[104:107]\n"
"buffer_load_dwordx4 v236, s[12:15], s48 offen offset:2048 lds\n"
"v_mfma_f32_16x16x32_bf16 v[108:111], a[124:127], v[216:219], v[108:111]\n"
"s_waitcnt vmcnt(20)\n"
"ds_read_b64_tr_b16 a[144:145], v246 offset:12288\n"
"v_mfma_f32_16x16x32_bf16 v[112:115], a[112:115], v[220:223], v[112:115]\n"
"ds_read_b128 v[208:211], v242 offset:24576\n"
"v_mfma_f32_16x16x32_bf16 v[116:119], a[116:119], v[220:223], v[116:119]\n"
"buffer_load_dwordx4 v237, s[12:15], s48 offen offset:3072 lds\n"
"v_mfma_f32_16x16x32_bf16 v[120:123], a[120:123], v[220:223], v[120:123]\n"
"ds_read_b64_tr_b16 a[146:147], v246 offset:12800\n"
"v_mfma_f32_16x16x32_bf16 v[124:127], a[124:127], v[220:223], v[124:127]\n"
"s_waitcnt lgkmcnt(4)\n"
"v_mfma_f32_16x16x32_bf16 v[0:3], a[128:131], v[192:195], v[0:3]\n"
"s_add_u32 m0, s37, 4096\n"
"ds_read_b128 v[212:215], v242 offset:26624\n"
"s_add_u32 s48, s47, 3407872\n"
"v_mfma_f32_16x16x32_bf16 v[4:7], a[132:135], v[192:195], v[4:7]\n"
"s_and_b32 s48, s48, 4194303\n"
"v_mfma_f32_16x16x32_bf16 v[8:11], a[136:139], v[192:195], v[8:11]\n"
"ds_read_b64_tr_b16 a[148:149], v247 offset:12288\n"
"v_mfma_f32_16x16x32_bf16 v[12:15], a[140:143], v[192:195], v[12:15]\n"
"buffer_load_dwordx4 v238, s[16:19], s48 offen offset:0 lds\n"
"v_mfma_f32_16x16x32_bf16 v[16:19], a[128:131], v[196:199], v[16:19]\n"
"ds_read_b128 v[216:219], v242 offset:28672\n"
"v_mfma_f32_16x16x32_bf16 v[20:23], a[132:135], v[196:199], v[20:23]\n"
"v_mfma_f32_16x16x32_bf16 v[24:27], a[136:139], v[196:199], v[24:27]\n"
"ds_read_b64_tr_b16 a[150:151], v247 offset:12800\n"
"v_mfma_f32_16x16x32_bf16 v[28:31], a[140:143], v[196:199], v[28:31]\n"
"buffer_load_dwordx4 v239, s[16:19], s48 offen offset:1024 lds\n"
"v_mfma_f32_16x16x32_bf16 v[32:35], a[128:131], v[200:203], v[32:35]\n"
"ds_read_b128 v[220:223], v242 offset:30720\n"
"v_mfma_f32_16x16x32_bf16 v[36:39], a[132:135], v[200:203], v[36:39]\n"
"v_mfma_f32_16x16x32_bf16 v[40:43], a[136:139], v[200:203], v[40:43]\n"
"ds_read_b128 v[192:195], v243 offset:16384\n"
"v_mfma_f32_16x16x32_bf16 v[44:47], a[140:143], v[200:203], v[44:47]\n"
"s_waitcnt lgkmcnt(3)\n"
"ds_read_b64_tr_b16 a[152:153], v248 offset:12288\n"
"v_mfma_f32_16x16x32_bf16 v[48:51], a[128:131], v[204:207], v[48:51]\n"
"buffer_load_dwordx4 v240, s[16:19], s48 offen offset:2048 lds\n"
"v_mfma_f32_16x16x32_bf16 v[52:55], a[132:135], v[204:207], v[52:55]\n"
"v_mfma_f32_16x16x32_bf16 v[56:59], a[136:139], v[204:207], v[56:59]\n"
"ds_read_b128 v[196:199], v243 offset:18432\n"
"v_mfma_f32_16x16x32_bf16 v[60:63], a[140:143], v[204:207], v[60:63]\n"
"ds_read_b64_tr_b16 a[154:155], v248 offset:12800\n"
"v_mfma_f32_16x16x32_bf16 v[64:67], a[128:131], v[208:211], v[64:67]\n"
"buffer_load_dwordx4 v241, s[16:19], s48 offen offset:3072 lds\n"
"v_mfma_f32_16x16x32_bf16 v[68:71], a[132:135], v[208:211], v[68:71]\n"
"ds_read_b128 v[200:203], v243 offset:20480\n"
"v_mfma_f32_16x16x32_bf16 v[72:75], a[136:139], v[208:211], v[72:75]\n"
"s_add_u32 m0, s37, 8192\n"
"ds_read_b64_tr_b16 a[156:157], v249 offset:12288\n"
"s_add_u32 s48, s47, 3670016\n"
"v_mfma_f32_16x16x32_bf16 v[76:79], a[140:143], v[208:211], v[76:79]\n"
"s_and_b32 s48, s48, 4194303\n"
"v_mfma_f32_16x16x32_bf16 v[80:83], a[128:131], v[212:215], v[80:83]\n"
"buffer_load_dwordx4 v238, s[16:19], s48 offen offset:0 lds\n"
"v_mfma_f32_16x16x32_bf16 v[84:87], a[132:135], v[212:215], v[84:87]\n"
"ds_read_b128 v[204:207], v243 offset:22528\n"
"v_mfma_f32_16x16x32_bf16 v[88:91], a[136:139], v[212:215], v[88:91]\n"
"ds_read_b64_tr_b16 a[158:159], v249 offset:12800\n"
"v_mfma_f32_16x16x32_bf16 v[92:95], a[140:143], v[212:215], v[92:95]\n"
"v_mfma_f32_16x16x32_bf16 v[96:99], a[128:131], v[216:219], v[96:99]\n"
"ds_read_b128 v[208:211], v243 offset:24576\n"
"v_mfma_f32_16x16x32_bf16 v[100:103], a[132:135], v[216:219], v[100:103]\n"
"buffer_load_dwordx4 v239, s[16:19], s48 offen offset:1024 lds\n"
"v_mfma_f32_16x16x32_bf16 v[104:107], a[136:139], v[216:219], v[104:107]\n"
"s_waitcnt vmcnt(22)\n"
"ds_read_b64_tr_b16 a[160:161], v246 offset:16384\n"
"v_mfma_f32_16x16x32_bf16 v[108:111], a[140:143], v[216:219], v[108:111]\n"
"s_waitcnt lgkmcnt(4)\n"
"v_mfma_f32_16x16x32_bf16 v[112:115], a[128:131], v[220:223], v[112:115]\n"
"ds_read_b128 v[212:215], v243 offset:26624\n"
"v_mfma_f32_16x16x32_bf16 v[116:119], a[132:135], v[220:223], v[116:119]\n"
"buffer_load_dwordx4 v240, s[16:19], s48 offen offset:2048 lds\n"
"v_mfma_f32_16x16x32_bf16 v[120:123], a[136:139], v[220:223], v[120:123]\n"
"ds_read_b64_tr_b16 a[162:163], v246 offset:16896\n"
"v_mfma_f32_16x16x32_bf16 v[124:127], a[140:143], v[220:223], v[124:127]\n"
"ds_read_b128 v[216:219], v243 offset:28672\n"
"v_mfma_f32_16x16x32_bf16 v[0:3], a[144:147], v[192:195], v[0:3]\n"
"v_mfma_f32_16x16x32_bf16 v[4:7], a[148:151], v[192:195], v[4:7]\n"
"buffer_load_dwordx4 v241, s[16:19], s48 offen offset:3072 lds\n"
"v_mfma_f32_16x16x32_bf16 v[8:11], a[152:155], v[192:195], v[8:11]\n"
"s_waitcnt lgkmcnt(3)\n"
"ds_read_b64_tr_b16 a[164:165], v247 offset:16384\n"
"v_mfma_f32_16x16x32_bf16 v[12:15], a[156:159], v[192:195], v[12:15]\n"
"ds_read_b128 v[220:223], v243 offset:30720\n"
"v_mfma_f32_16x16x32_bf16 v[16:19], a[144:147], v[196:199], v[16:19]\n"
"v_mfma_f32_16x16x32_bf16 v[20:23], a[148:151], v[196:199], v[20:23]\n"
"v_mfma_f32_16x16x32_bf16 v[24:27], a[152:155], v[196:199], v[24:27]\n"
"ds_read_b64_tr_b16 a[166:167], v247 offset:16896\n"
"v_mfma_f32_16x16x32_bf16 v[28:31], a[156:159], v[196:199], v[28:31]\n"
"v_mfma_f32_16x16x32_bf16 v[32:35], a[144:147], v[200:203], v[32:35]\n"
"v_mfma_f32_16x16x32_bf16 v[36:39], a[148:151], v[200:203], v[36:39]\n"
"v_mfma_f32_16x16x32_bf16 v[40:43], a[152:155], v[200:203], v[40:43]\n"
"ds_read_b64_tr_b16 a[168:169], v248 offset:16384\n"
"v_mfma_f32_16x16x32_bf16 v[44:47], a[156:159], v[200:203], v[44:47]\n"
"v_mfma_f32_16x16x32_bf16 v[48:51], a[144:147], v[204:207], v[48:51]\n"
"v_mfma_f32_16x16x32_bf16 v[52:55], a[148:151], v[204:207], v[52:55]\n"
"s_waitcnt vmcnt(20) lgkmcnt(2)\n"
"s_barrier\n"
"v_mfma_f32_16x16x32_bf16 v[56:59], a[152:155], v[204:207], v[56:59]\n"
"s_add_u32 m0, s36, 16384\n"
"ds_read_b128 v[192:195], v242 offset:32768\n"
"s_add_u32 s48, s46, 896\n"
"v_mfma_f32_16x16x32_bf16 v[60:63], a[156:159], v[204:207], v[60:63]\n"
"s_and_b32 s48, s48, 1023\n"
"ds_read_b64_tr_b16 a[170:171], v248 offset:16896\n"
"v_mfma_f32_16x16x32_bf16 v[64:67], a[144:147], v[208:211], v[64:67]\n"
"buffer_load_dwordx4 v234, s[12:15], s48 offen offset:0 lds\n"
"v_mfma_f32_16x16x32_bf16 v[68:71], a[148:151], v[208:211], v[68:71]\n"
"v_mfma_f32_16x16x32_bf16 v[72:75], a[152:155], v[208:211], v[72:75]\n"
"ds_read_b128 v[196:199], v242 offset:34816\n"
"v_mfma_f32_16x16x32_bf16 v[76:79], a[156:159], v[208:211], v[76:79]\n"
"ds_read_b64_tr_b16 a[172:173], v249 offset:16384\n"
"v_mfma_f32_16x16x32_bf16 v[80:83], a[144:147], v[212:215], v[80:83]\n"
"buffer_load_dwordx4 v235, s[12:15], s48 offen offset:1024 lds\n"
"v_mfma_f32_16x16x32_bf16 v[84:87], a[148:151], v[212:215], v[84:87]\n"
"ds_read_b128 v[200:203], v242 offset:36864\n"
"v_mfma_f32_16x16x32_bf16 v[88:91], a[152:155], v[212:215], v[88:91]\n"
"ds_read_b64_tr_b16 a[174:175], v249 offset:16896\n"
"v_mfma_f32_16x16x32_bf16 v[92:95], a[156:159], v[212:215], v[92:95]\n"
"v_mfma_f32_16x16x32_bf16 v[96:99], a[144:147], v[216:219], v[96:99]\n"
"v_mfma_f32_16x16x32_bf16 v[100:103], a[148:151], v[216:219], v[100:103]\n"
"ds_read_b128 v[204:207], v242 offset:38912\n"
"v_mfma_f32_16x16x32_bf16 v[104:107], a[152:155], v[216:219], v[104:107]\n"
"buffer_load_dwordx4 v236, s[12:15], s48 offen offset:2048 lds\n"
"v_mfma_f32_16x16x32_bf16 v[108:111], a[156:159], v[216:219], v[108:111]\n"
"s_waitcnt vmcnt(19)\n"
"ds_read_b64_tr_b16 a[176:177], v246 offset:20480\n"
"v_mfma_f32_16x16x32_bf16 v[112:115], a[144:147], v[220:223], v[112:115]\n"
"ds_read_b128 v[208:211], v242 offset:40960\n"
"v_mfma_f32_16x16x32_bf16 v[116:119], a[148:151], v[220:223], v[116:119]\n"
"buffer_load_dwordx4 v237, s[12:15], s48 offen offset:3072 lds\n"
"v_mfma_f32_16x16x32_bf16 v[120:123], a[152:155], v[220:223], v[120:123]\n"
"ds_read_b64_tr_b16 a[178:179], v246 offset:20992\n"
"v_mfma_f32_16x16x32_bf16 v[124:127], a[156:159], v[220:223], v[124:127]\n"
"s_waitcnt lgkmcnt(4)\n"
"v_mfma_f32_16x16x32_bf16 v[0:3], a[160:163], v[192:195], v[0:3]\n"
"s_add_u32 m0, s37, 12288\n"
"ds_read_b128 v[212:215], v242 offset:43008\n"
"s_add_u32 s48, s47, 3932160\n"
"v_mfma_f32_16x16x32_bf16 v[4:7], a[164:167], v[192:195], v[4:7]\n"
"s_and_b32 s48, s48, 4194303\n"
"v_mfma_f32_16x16x32_bf16 v[8:11], a[168:171], v[192:195], v[8:11]\n"
"ds_read_b64_tr_b16 a[180:181], v247 offset:20480\n"
"v_mfma_f32_16x16x32_bf16 v[12:15], a[172:175], v[192:195], v[12:15]\n"
"buffer_load_dwordx4 v238, s[16:19], s48 offen offset:0 lds\n"
"v_mfma_f32_16x16x32_bf16 v[16:19], a[160:163], v[196:199], v[16:19]\n"
"ds_read_b128 v[216:219], v242 offset:45056\n"
"v_mfma_f32_16x16x32_bf16 v[20:23], a[164:167], v[196:199], v[20:23]\n"
"v_mfma_f32_16x16x32_bf16 v[24:27], a[168:171], v[196:199], v[24:27]\n"
"ds_read_b64_tr_b16 a[182:183], v247 offset:20992\n"
"v_mfma_f32_16x16x32_bf16 v[28:31], a[172:175], v[196:199], v[28:31]\n"
"buffer_load_dwordx4 v239, s[16:19], s48 offen offset:1024 lds\n"
"v_mfma_f32_16x16x32_bf16 v[32:35], a[160:163], v[200:203], v[32:35]\n"
"ds_read_b128 v[220:223], v242 offset:47104\n"
"v_mfma_f32_16x16x32_bf16 v[36:39], a[164:167], v[200:203], v[36:39]\n"
"v_mfma_f32_16x16x32_bf16 v[40:43], a[168:171], v[200:203], v[40:43]\n"
"ds_read_b128 v[192:195], v243 offset:32768\n"
"v_mfma_f32_16x16x32_bf16 v[44:47], a[172:175], v[200:203], v[44:47]\n"
"s_waitcnt lgkmcnt(3)\n"
"ds_read_b64_tr_b16 a[184:185], v248 offset:20480\n"
"v_mfma_f32_16x16x32_bf16 v[48:51], a[160:163], v[204:207], v[48:51]\n"
"buffer_load_dwordx4 v240, s[16:19], s48 offen offset:2048 lds\n"
"v_mfma_f32_16x16x32_bf16 v[52:55], a[164:167], v[204:207], v[52:55]\n"
"v_mfma_f32_16x16x32_bf16 v[56:59], a[168:171], v[204:207], v[56:59]\n"
"ds_read_b128 v[196:199], v243 offset:34816\n"
"v_mfma_f32_16x16x32_bf16 v[60:63], a[172:175], v[204:207], v[60:63]\n"
"s_mul_i32 s91, s30, 5120\n"
"ds_read_b64_tr_b16 a[186:187], v248 offset:20992\n"
"v_mfma_f32_16x16x32_bf16 v[64:67], a[160:163], v[208:211], v[64:67]\n"
"s_lshl_b32 s88, s32, 3\n"
"buffer_load_dwordx4 v241, s[16:19], s48 offen offset:3072 lds\n"
"v_mfma_f32_16x16x32_bf16 v[68:71], a[164:167], v[208:211], v[68:71]\n"
"s_add_u32 s88, s88, s34\n"
"ds_read_b128 v[200:203], v243 offset:36864\n"
"v_mfma_f32_16x16x32_bf16 v[72:75], a[168:171], v[208:211], v[72:75]\n"
"s_add_u32 m0, s37, 16384\n"
"ds_read_b64_tr_b16 a[188:189], v249 offset:20480\n"
"s_add_u32 s48, s39, 0\n"
"v_mfma_f32_16x16x32_bf16 v[76:79], a[172:175], v[208:211], v[76:79]\n"
"s_mul_i32 s88, s88, 81920\n"
"s_lshl_b32 s89, s33, 1\n"
"v_mfma_f32_16x16x32_bf16 v[80:83], a[160:163], v[212:215], v[80:83]\n"
"s_add_u32 s89, s89, s30\n"
"buffer_load_dwordx4 v234, s[12:15], s48 offen offset:0 lds\n"
"v_mfma_f32_16x16x32_bf16 v[84:87], a[164:167], v[212:215], v[84:87]\n"
"s_mul_i32 s89, s89, 160\n"
"ds_read_b128 v[204:207], v243 offset:38912\n"
"v_mfma_f32_16x16x32_bf16 v[88:91], a[168:171], v[212:215], v[88:91]\n"
"s_add_u32 s88, s88, s89\n"
"ds_read_b64_tr_b16 a[190:191], v249 offset:20992\n"
"v_mfma_f32_16x16x32_bf16 v[92:95], a[172:175], v[212:215], v[92:95]\n"
"s_add_u32 s24, s74, s88\n"
"s_addc_u32 s25, s75, 0\n"
"v_mfma_f32_16x16x32_bf16 v[96:99], a[160:163], v[216:219], v[96:99]\n"
"s_and_b32 s25, s25, 0xffff\n"
"ds_read_b128 v[208:211], v243 offset:40960\n"
"v_mfma_f32_16x16x32_bf16 v[100:103], a[164:167], v[216:219], v[100:103]\n"
"s_mov_b32 s26, 39840\n"
"buffer_load_dwordx4 v235, s[12:15], s48 offen offset:1024 lds\n"
"v_mfma_f32_16x16x32_bf16 v[104:107], a[168:171], v[216:219], v[104:107]\n"
"s_cmp_lt_u32 s30, 2\n"
"s_waitcnt vmcnt(22)\n"
"ds_read_b64_tr_b16 a[192:193], v246 offset:0\n"
"v_mfma_f32_16x16x32_bf16 v[108:111], a[172:175], v[216:219], v[108:111]\n"
"s_waitcnt lgkmcnt(4)\n"
"s_cselect_b64 s[40:41], s[78:79], s[80:81]\n"
"s_mov_b32 s27, 0x20000\n"
"v_mfma_f32_16x16x32_bf16 v[112:115], a[160:163], v[220:223], v[112:115]\n"
"s_and_b32 s41, s41, 0xffff\n"
"ds_read_b128 v[212:215], v243 offset:43008\n"
"s_mov_b32 s42, 2211840\n"
"v_mfma_f32_16x16x32_bf16 v[116:119], a[164:167], v[220:223], v[116:119]\n"
"s_mov_b32 s43, 0x20000\n"
"buffer_load_dwordx4 v236, s[12:15], s48 offen offset:2048 lds\n"
"v_mfma_f32_16x16x32_bf16 v[120:123], a[168:171], v[220:223], v[120:123]\n"
"ds_read_b64_tr_b16 a[194:195], v246 offset:512\n"
"v_lshrrev_b32 v254, 6, v224\n"
"v_mfma_f32_16x16x32_bf16 v[124:127], a[172:175], v[220:223], v[124:127]\n"
"ds_read_b128 v[216:219], v243 offset:45056\n"
"v_mul_u32_u24 v254, 160, v254\n"
"v_mfma_f32_16x16x32_bf16 v[0:3], a[176:179], v[192:195], v[0:3]\n"
"v_bfe_u32 v255, v224, 4, 2\n"
"v_lshl_add_u32 v254, v255, 3, v254\n"
"v_mfma_f32_16x16x32_bf16 v[4:7], a[180:183], v[192:195], v[4:7]\n"
"buffer_load_dwordx4 v237, s[12:15], s48 offen offset:3072 lds\n"
"v_mfma_f32_16x16x32_bf16 v[8:11], a[184:187], v[192:195], v[8:11]\n"
"s_waitcnt lgkmcnt(3)\n"
"ds_read_b64_tr_b16 a[196:197], v247 offset:0\n"
"v_add_u32 v254, s91, v254\n"
"v_mfma_f32_16x16x32_bf16 v[12:15], a[188:191], v[192:195], v[12:15]\n"
"ds_read_b128 v[220:223], v243 offset:47104\n"
"v_mfma_f32_16x16x32_bf16 v[16:19], a[176:179], v[196:199], v[16:19]\n"
"s_add_u32 m0, s37, 20480\n"
"s_add_u32 s48, s98, 0\n"
"v_mfma_f32_16x16x32_bf16 v[20:23], a[180:183], v[196:199], v[20:23]\n"
"v_mfma_f32_16x16x32_bf16 v[24:27], a[184:187], v[196:199], v[24:27]\n"
"ds_read_b64_tr_b16 a[198:199], v247 offset:512\n"
"v_mfma_f32_16x16x32_bf16 v[28:31], a[188:191], v[196:199], v[28:31]\n"
"buffer_load_dwordx4 v234, s[12:15], s48 offen offset:0 lds\n"
"v_mfma_f32_16x16x32_bf16 v[32:35], a[176:179], v[200:203], v[32:35]\n"
"v_lshrrev_b32 v149, 4, v224\n"
"v_mfma_f32_16x16x32_bf16 v[36:39], a[180:183], v[200:203], v[36:39]\n"
"v_min_u32 v149, 49, v149\n"
"v_mfma_f32_16x16x32_bf16 v[40:43], a[184:187], v[200:203], v[40:43]\n"
"ds_read_b64_tr_b16 a[200:201], v248 offset:0\n"
"v_mul_u32_u24 v150, 52, v149\n"
"v_mfma_f32_16x16x32_bf16 v[44:47], a[188:191], v[200:203], v[44:47]\n"
"buffer_load_dwordx4 v235, s[12:15], s48 offen offset:1024 lds\n"
"v_mfma_f32_16x16x32_bf16 v[48:51], a[176:179], v[204:207], v[48:51]\n"
"v_lshrrev_b32 v150, 8, v150\n"
"v_mul_u32_u24 v150, 2901, v150\n"
"v_mfma_f32_16x16x32_bf16 v[52:55], a[180:183], v[204:207], v[52:55]\n"
"s_waitcnt vmcnt(22) lgkmcnt(2)\n"
"s_barrier\n"
"v_mfma_f32_16x16x32_bf16 v[56:59], a[184:187], v[204:207], v[56:59]\n"
"ds_read_b128 v[192:195], v242 offset:0\n"
"v_mul_u32_u24 v149, 111, v149\n"
"v_mfma_f32_16x16x32_bf16 v[60:63], a[188:191], v[204:207], v[60:63]\n"
"ds_read_b64_tr_b16 a[202:203], v248 offset:512\n"
"v_add_u32 v149, v149, v150\n"
"v_mfma_f32_16x16x32_bf16 v[64:67], a[176:179], v[208:211], v[64:67]\n"
"s_lshr_b32 s50, s31, 3\n"
"buffer_load_dwordx4 v236, s[12:15], s48 offen offset:2048 lds\n"
"v_mfma_f32_16x16x32_bf16 v[68:71], a[180:183], v[208:211], v[68:71]\n"
"s_lshl_b32 s50, s50, 1\n"
"v_and_b32 v149, -4, v149\n"
"v_mfma_f32_16x16x32_bf16 v[72:75], a[184:187], v[208:211], v[72:75]\n"
"s_and_b32 s51, s30, 1\n"
"ds_read_b128 v[196:199], v242 offset:2048\n"
"v_mfma_f32_16x16x32_bf16 v[76:79], a[188:191], v[208:211], v[76:79]\n"
"s_add_u32 s50, s50, s51\n"
"ds_read_b64_tr_b16 a[204:205], v249 offset:0\n"
"v_lshrrev_b32 v252, 4, v224\n"
"v_mfma_f32_16x16x32_bf16 v[80:83], a[176:179], v[212:215], v[80:83]\n"
"s_mul_i32 s50, s50, 34560\n"
"buffer_load_dwordx4 v237, s[12:15], s48 offen offset:3072 lds\n"
"v_mfma_f32_16x16x32_bf16 v[84:87], a[180:183], v[212:215], v[84:87]\n"
"s_mul_i32 s51, s32, 448\n"
"ds_read_b128 v[200:203], v242 offset:4096\n"
"v_add_u32 v252, 0, v252\n"
"v_mfma_f32_16x16x32_bf16 v[88:91], a[184:187], v[212:215], v[88:91]\n"
"s_add_u32 s50, s50, s51\n"
"ds_read_b64_tr_b16 a[206:207], v249 offset:512\n"
"v_mul_u32_u24 v252, 205, v252\n"
"v_mfma_f32_16x16x32_bf16 v[92:95], a[188:191], v[212:215], v[92:95]\n"
"s_add_u32 m0, s91, 45056\n"
"v_lshrrev_b32 v252, 11, v252\n"
"v_mul_u32_u24 v252, 1120, v252\n"
"v_mfma_f32_16x16x32_bf16 v[96:99], a[176:179], v[216:219], v[96:99]\n"
"s_mov_b32 s48, 0\n"
"v_add_u32 v252, v252, v224\n"
"v_lshrrev_b32 v251, 4, v224\n"
"v_mfma_f32_16x16x32_bf16 v[100:103], a[180:183], v[216:219], v[100:103]\n"
"ds_read_b128 v[204:207], v242 offset:6144\n"
"v_add_u32 v251, 64, v251\n"
"v_mfma_f32_16x16x32_bf16 v[104:107], a[184:187], v[216:219], v[104:107]\n"
"buffer_load_dwordx4 v252, s[24:27], s48 offen lds\n"
"v_mfma_f32_16x16x32_bf16 v[108:111], a[188:191], v[216:219], v[108:111]\n"
"s_waitcnt vmcnt(21)\n"
"ds_read_b64_tr_b16 a[208:209], v246 offset:4096\n"
"v_mul_u32_u24 v251, 205, v251\n"
"v_mfma_f32_16x16x32_bf16 v[112:115], a[176:179], v[220:223], v[112:115]\n"
"s_add_u32 m0, s91, 46080\n"
"buffer_load_dword v149, v149, s[40:43], s50 offen\n"
"v_mfma_f32_16x16x32_bf16 v[116:119], a[180:183], v[220:223], v[116:119]\n"
"s_mov_b32 s48, 1024\n"
"ds_read_b128 v[208:211], v242 offset:8192\n"
"v_lshrrev_b32 v251, 11, v251\n"
"v_mfma_f32_16x16x32_bf16 v[120:123], a[184:187], v[220:223], v[120:123]\n"
"ds_read_b64_tr_b16 a[210:211], v246 offset:4608\n"
"v_mul_u32_u24 v251, 1120, v251\n"
"v_mfma_f32_16x16x32_bf16 v[124:127], a[188:191], v[220:223], v[124:127]\n"
"s_waitcnt lgkmcnt(4)\n"
"v_add_u32 v251, v251, v224\n"
"v_lshrrev_b32 v252, 4, v224\n"
"v_mfma_f32_16x16x32_bf16 v[0:3], a[192:195], v[192:195], v[0:3]\n"
"buffer_load_dwordx4 v251, s[24:27], s48 offen lds\n"
"v_mfma_f32_16x16x32_bf16 v[4:7], a[196:199], v[192:195], v[4:7]\n"
"s_add_u32 m0, s91, 47104\n"
"ds_read_b128 v[212:215], v242 offset:10240\n"
"v_add_u32 v252, 128, v252\n"
"v_mfma_f32_16x16x32_bf16 v[8:11], a[200:203], v[192:195], v[8:11]\n"
"s_mov_b32 s48, 2048\n"
"ds_read_b64_tr_b16 a[212:213], v247 offset:4096\n"
"v_mul_u32_u24 v252, 205, v252\n"
"v_mfma_f32_16x16x32_bf16 v[12:15], a[204:207], v[192:195], v[12:15]\n"
"ds_read_b128 v[216:219], v242 offset:12288\n"
"v_lshrrev_b32 v252, 11, v252\n"
"v_mfma_f32_16x16x32_bf16 v[16:19], a[192:195], v[196:199], v[16:19]\n"
"v_mul_u32_u24 v252, 1120, v252\n"
"v_add_u32 v252, v252, v224\n"
"v_mfma_f32_16x16x32_bf16 v[20:23], a[196:199], v[196:199], v[20:23]\n"
"buffer_load_dwordx4 v252, s[24:27], s48 offen lds\n"
"v_mfma_f32_16x16x32_bf16 v[24:27], a[200:203], v[196:199], v[24:27]\n"
"s_add_u32 m0, s91, 48128\n"
"ds_read_b64_tr_b16 a[214:215], v247 offset:4608\n"
"v_lshrrev_b32 v251, 4, v224\n"
"v_mfma_f32_16x16x32_bf16 v[28:31], a[204:207], v[196:199], v[28:31]\n"
"s_mov_b32 s48, 3072\n"
"ds_read_b128 v[220:223], v242 offset:14336\n"
"v_add_u32 v251, 192, v251\n"
"v_mfma_f32_16x16x32_bf16 v[32:35], a[192:195], v[200:203], v[32:35]\n"
"v_mul_u32_u24 v251, 205, v251\n"
"v_lshrrev_b32 v251, 11, v251\n"
"v_mfma_f32_16x16x32_bf16 v[36:39], a[196:199], v[200:203], v[36:39]\n"
"v_mul_u32_u24 v251, 1120, v251\n"
"v_add_u32 v251, v251, v224\n"
"v_mfma_f32_16x16x32_bf16 v[40:43], a[200:203], v[200:203], v[40:43]\n"
"buffer_load_dwordx4 v251, s[24:27], s48 offen lds\n"
"v_mfma_f32_16x16x32_bf16 v[44:47], a[204:207], v[200:203], v[44:47]\n"
"s_waitcnt lgkmcnt(2)\n"
"ds_read_b128 v[192:195], v243 offset:0\n"
"v_lshrrev_b32 v252, 4, v224\n"
"v_mfma_f32_16x16x32_bf16 v[48:51], a[192:195], v[204:207], v[48:51]\n"
"s_add_u32 m0, s91, 49152\n"
"ds_read_b64_tr_b16 a[216:217], v248 offset:4096\n"
"v_add_u32 v252, 256, v252\n"
"v_mfma_f32_16x16x32_bf16 v[52:55], a[196:199], v[204:207], v[52:55]\n"
"s_mov_b32 s48, 4096\n"
"v_mul_u32_u24 v252, 205, v252\n"
"v_lshrrev_b32 v252, 11, v252\n"
"v_mfma_f32_16x16x32_bf16 v[56:59], a[200:203], v[204:207], v[56:59]\n"
"ds_read_b128 v[196:199], v243 offset:2048\n"
"v_mul_u32_u24 v252, 1120, v252\n"
"v_mfma_f32_16x16x32_bf16 v[60:63], a[204:207], v[204:207], v[60:63]\n"
"ds_read_b64_tr_b16 a[218:219], v248 offset:4608\n"
"v_add_u32 v252, v252, v224\n"
"v_mfma_f32_16x16x32_bf16 v[64:67], a[192:195], v[208:211], v[64:67]\n"
"buffer_load_dwordx4 v252, s[24:27], s48 offen lds\n"
"v_mfma_f32_16x16x32_bf16 v[68:71], a[196:199], v[208:211], v[68:71]\n"
"s_add_u32 s24, s24, 40960\n"
"ds_read_b128 v[200:203], v243 offset:4096\n"
"v_lshrrev_b32 v251, 4, v224\n"
"v_mfma_f32_16x16x32_bf16 v[72:75], a[200:203], v[208:211], v[72:75]\n"
"s_addc_u32 s25, s25, 0\n"
"ds_read_b64_tr_b16 a[220:221], v249 offset:4096\n"
"v_add_u32 v251, 0, v251\n"
"v_mfma_f32_16x16x32_bf16 v[76:79], a[204:207], v[208:211], v[76:79]\n"
"s_add_u32 m0, s91, 55296\n"
"v_mul_u32_u24 v251, 205, v251\n"
"v_lshrrev_b32 v251, 11, v251\n"
"v_mfma_f32_16x16x32_bf16 v[80:83], a[192:195], v[212:215], v[80:83]\n"
"s_mov_b32 s48, 0\n"
"v_mul_u32_u24 v251, 1120, v251\n"
"v_add_u32 v251, v251, v224\n"
"v_mfma_f32_16x16x32_bf16 v[84:87], a[196:199], v[212:215], v[84:87]\n"
"buffer_load_dwordx4 v251, s[24:27], s48 offen lds\n"
"v_mfma_f32_16x16x32_bf16 v[88:91], a[200:203], v[212:215], v[88:91]\n"
"s_add_u32 m0, s91, 56320\n"
"ds_read_b128 v[204:207], v243 offset:6144\n"
"v_lshrrev_b32 v252, 4, v224\n"
"v_mfma_f32_16x16x32_bf16 v[92:95], a[204:207], v[212:215], v[92:95]\n"
"s_mov_b32 s48, 1024\n"
"ds_read_b64_tr_b16 a[222:223], v249 offset:4608\n"
"v_add_u32 v252, 64, v252\n"
"v_mfma_f32_16x16x32_bf16 v[96:99], a[192:195], v[216:219], v[96:99]\n"
"ds_read_b128 v[208:211], v243 offset:8192\n"
"v_mul_u32_u24 v252, 205, v252\n"
"v_mfma_f32_16x16x32_bf16 v[100:103], a[196:199], v[216:219], v[100:103]\n"
"v_lshrrev_b32 v252, 11, v252\n"
"v_mul_u32_u24 v252, 1120, v252\n"
"v_mfma_f32_16x16x32_bf16 v[104:107], a[200:203], v[216:219], v[104:107]\n"
"s_waitcnt vmcnt(23)\n"
"ds_read_b64_tr_b16 a[224:225], v246 offset:8192\n"
"v_add_u32 v252, v252, v224\n"
"v_mfma_f32_16x16x32_bf16 v[108:111], a[204:207], v[216:219], v[108:111]\n"
"s_waitcnt lgkmcnt(4)\n"
"buffer_load_dwordx4 v252, s[24:27], s48 offen lds\n"
"v_mfma_f32_16x16x32_bf16 v[112:115], a[192:195], v[220:223], v[112:115]\n"
"s_add_u32 m0, s91, 57344\n"
"ds_read_b128 v[212:215], v243 offset:10240\n"
"v_lshrrev_b32 v251, 4, v224\n"
"v_mfma_f32_16x16x32_bf16 v[116:119], a[196:199], v[220:223], v[116:119]\n"
"s_mov_b32 s48, 2048\n"
"v_add_u32 v251, 128, v251\n"
"v_mul_u32_u24 v251, 205, v251\n"
"v_mfma_f32_16x16x32_bf16 v[120:123], a[200:203], v[220:223], v[120:123]\n"
"ds_read_b64_tr_b16 a[226:227], v246 offset:8704\n"
"v_lshrrev_b32 v251, 11, v251\n"
"v_mfma_f32_16x16x32_bf16 v[124:127], a[204:207], v[220:223], v[124:127]\n"
"ds_read_b128 v[216:219], v243 offset:12288\n"
"v_mul_u32_u24 v251, 1120, v251\n"
"v_mfma_f32_16x16x32_bf16 v[0:3], a[208:211], v[192:195], v[0:3]\n"
"v_add_u32 v251, v251, v224\n"
"v_lshrrev_b32 v252, 4, v224\n"
"v_mfma_f32_16x16x32_bf16 v[4:7], a[212:215], v[192:195], v[4:7]\n"
"buffer_load_dwordx4 v251, s[24:27], s48 offen lds\n"
"v_mfma_f32_16x16x32_bf16 v[8:11], a[216:219], v[192:195], v[8:11]\n"
"s_waitcnt lgkmcnt(3)\n"
"ds_read_b64_tr_b16 a[228:229], v247 offset:8192\n"
"v_add_u32 v252, 192, v252\n"
"v_mfma_f32_16x16x32_bf16 v[12:15], a[220:223], v[192:195], v[12:15]\n"
"s_add_u32 m0, s91, 58368\n"
"ds_read_b128 v[220:223], v243 offset:14336\n"
"v_mul_u32_u24 v252, 205, v252\n"
"v_mfma_f32_16x16x32_bf16 v[16:19], a[208:211], v[196:199], v[16:19]\n"
"s_mov_b32 s48, 3072\n"
"v_lshrrev_b32 v252, 11, v252\n"
"v_mul_u32_u24 v252, 1120, v252\n"
"v_mfma_f32_16x16x32_bf16 v[20:23], a[212:215], v[196:199], v[20:23]\n"
"v_add_u32 v252, v252, v224\n"
"v_lshrrev_b32 v251, 4, v224\n"
"v_mfma_f32_16x16x32_bf16 v[24:27], a[216:219], v[196:199], v[24:27]\n"
"buffer_load_dwordx4 v252, s[24:27], s48 offen lds\n"
"v_mfma_f32_16x16x32_bf16 v[28:31], a[220:223], v[196:199], v[28:31]\n"
"s_add_u32 m0, s91, 59392\n"
"ds_read_b64_tr_b16 a[230:231], v247 offset:8704\n"
"v_add_u32 v251, 256, v251\n"
"v_mfma_f32_16x16x32_bf16 v[32:35], a[208:211], v[200:203], v[32:35]\n"
"s_mov_b32 s48, 4096\n"
"v_mul_u32_u24 v251, 205, v251\n"
"v_lshrrev_b32 v251, 11, v251\n"
"v_mfma_f32_16x16x32_bf16 v[36:39], a[212:215], v[200:203], v[36:39]\n"
"v_mul_u32_u24 v251, 1120, v251\n"
"v_add_u32 v251, v251, v224\n"
"v_mfma_f32_16x16x32_bf16 v[40:43], a[216:219], v[200:203], v[40:43]\n"
"buffer_load_dwordx4 v251, s[24:27], s48 offen lds\n"
"v_mfma_f32_16x16x32_bf16 v[44:47], a[220:223], v[200:203], v[44:47]\n"
"s_add_u32 m0, s37, 0\n"
"ds_read_b64_tr_b16 a[232:233], v248 offset:8192\n"
"s_add_u32 s48, s39, 32768\n"
"v_mfma_f32_16x16x32_bf16 v[48:51], a[208:211], v[204:207], v[48:51]\n"
"buffer_load_dwordx4 v234, s[12:15], s48 offen offset:0 lds\n"
"v_mfma_f32_16x16x32_bf16 v[52:55], a[212:215], v[204:207], v[52:55]\n"
"s_waitcnt vmcnt(24) lgkmcnt(2)\n"
"s_barrier\n"
"v_mfma_f32_16x16x32_bf16 v[56:59], a[216:219], v[204:207], v[56:59]\n"
"buffer_load_dwordx4 v235, s[12:15], s48 offen offset:1024 lds\n"
"v_mfma_f32_16x16x32_bf16 v[60:63], a[220:223], v[204:207], v[60:63]\n"
"ds_read_b128 v[192:195], v242 offset:16384\n"
"v_mfma_f32_16x16x32_bf16 v[64:67], a[208:211], v[208:211], v[64:67]\n"
"ds_read_b64_tr_b16 a[234:235], v248 offset:8704\n"
"v_mfma_f32_16x16x32_bf16 v[68:71], a[212:215], v[208:211], v[68:71]\n"
"v_mfma_f32_16x16x32_bf16 v[72:75], a[216:219], v[208:211], v[72:75]\n"
"ds_read_b128 v[196:199], v242 offset:18432\n"
"v_mfma_f32_16x16x32_bf16 v[76:79], a[220:223], v[208:211], v[76:79]\n"
"ds_read_b64_tr_b16 a[236:237], v249 offset:8192\n"
"v_mfma_f32_16x16x32_bf16 v[80:83], a[208:211], v[212:215], v[80:83]\n"
"buffer_load_dwordx4 v236, s[12:15], s48 offen offset:2048 lds\n"
"v_mfma_f32_16x16x32_bf16 v[84:87], a[212:215], v[212:215], v[84:87]\n"
"ds_read_b128 v[200:203], v242 offset:20480\n"
"v_mfma_f32_16x16x32_bf16 v[88:91], a[216:219], v[212:215], v[88:91]\n"
"ds_read_b64_tr_b16 a[238:239], v249 offset:8704\n"
"v_mfma_f32_16x16x32_bf16 v[92:95], a[220:223], v[212:215], v[92:95]\n"
"buffer_load_dwordx4 v237, s[12:15], s48 offen offset:3072 lds\n"
"v_mfma_f32_16x16x32_bf16 v[96:99], a[208:211], v[216:219], v[96:99]\n"
"s_lshl_b32 s50, s35, 20\n"
"v_mfma_f32_16x16x32_bf16 v[100:103], a[212:215], v[216:219], v[100:103]\n"
"s_lshl_b32 s51, s34, 15\n"
"ds_read_b128 v[204:207], v242 offset:22528\n"
"s_add_u32 m0, s37, 4096\n"
"v_mfma_f32_16x16x32_bf16 v[104:107], a[216:219], v[216:219], v[104:107]\n"
"s_add_u32 s48, s98, 32768\n"
"s_waitcnt vmcnt(23)\n"
"ds_read_b64_tr_b16 a[240:241], v246 offset:12288\n"
"v_mfma_f32_16x16x32_bf16 v[108:111], a[220:223], v[216:219], v[108:111]\n"
"s_add_u32 s50, s50, s51\n"
"buffer_load_dwordx4 v234, s[12:15], s48 offen offset:0 lds\n"
"v_mfma_f32_16x16x32_bf16 v[112:115], a[208:211], v[220:223], v[112:115]\n"
"s_and_b32 s51, s30, 1\n"
"ds_read_b128 v[208:211], v242 offset:24576\n"
"s_lshl_b32 s51, s51, 11\n"
"v_mfma_f32_16x16x32_bf16 v[116:119], a[212:215], v[220:223], v[116:119]\n"
"s_add_u32 s50, s50, s51\n"
"v_mfma_f32_16x16x32_bf16 v[120:123], a[216:219], v[220:223], v[120:123]\n"
"s_add_u32 s50, s50, 10485760\n"
"ds_read_b64_tr_b16 a[242:243], v246 offset:12800\n"
"v_mfma_f32_16x16x32_bf16 v[124:127], a[220:223], v[220:223], v[124:127]\n"
"s_waitcnt lgkmcnt(4)\n"
"buffer_load_dwordx4 v235, s[12:15], s48 offen offset:1024 lds\n"
"v_mfma_f32_16x16x32_bf16 v[0:3], a[224:227], v[192:195], v[0:3]\n"
"ds_read_b128 v[212:215], v242 offset:26624\n"
"v_add_u32 v250, s50, v224\n"
"v_mfma_f32_16x16x32_bf16 v[4:7], a[228:231], v[192:195], v[4:7]\n"
"v_mfma_f32_16x16x32_bf16 v[8:11], a[232:235], v[192:195], v[8:11]\n"
"ds_read_b64_tr_b16 a[244:245], v247 offset:12288\n"
"v_mfma_f32_16x16x32_bf16 v[12:15], a[236:239], v[192:195], v[12:15]\n"
"ds_read_b128 v[216:219], v242 offset:28672\n"
"v_mfma_f32_16x16x32_bf16 v[16:19], a[224:227], v[196:199], v[16:19]\n"
"buffer_load_dwordx4 v236, s[12:15], s48 offen offset:2048 lds\n"
"v_mfma_f32_16x16x32_bf16 v[20:23], a[228:231], v[196:199], v[20:23]\n"
"v_mfma_f32_16x16x32_bf16 v[24:27], a[232:235], v[196:199], v[24:27]\n"
"ds_read_b64_tr_b16 a[246:247], v247 offset:12800\n"
"v_mfma_f32_16x16x32_bf16 v[28:31], a[236:239], v[196:199], v[28:31]\n"
"ds_read_b128 v[220:223], v242 offset:30720\n"
"v_mfma_f32_16x16x32_bf16 v[32:35], a[224:227], v[200:203], v[32:35]\n"
"buffer_load_dwordx4 v237, s[12:15], s48 offen offset:3072 lds\n"
"v_mfma_f32_16x16x32_bf16 v[36:39], a[228:231], v[200:203], v[36:39]\n"
"v_mfma_f32_16x16x32_bf16 v[40:43], a[232:235], v[200:203], v[40:43]\n"
"ds_read_b128 v[192:195], v243 offset:16384\n"
"v_mfma_f32_16x16x32_bf16 v[44:47], a[236:239], v[200:203], v[44:47]\n"
"s_waitcnt lgkmcnt(3)\n"
"ds_read_b64_tr_b16 a[248:249], v248 offset:12288\n"
"s_add_u32 m0, s37, 8192\n"
"v_mfma_f32_16x16x32_bf16 v[48:51], a[224:227], v[204:207], v[48:51]\n"
"s_add_u32 s48, s39, 65536\n"
"v_mfma_f32_16x16x32_bf16 v[52:55], a[228:231], v[204:207], v[52:55]\n"
"buffer_load_dwordx4 v234, s[12:15], s48 offen offset:0 lds\n"
"v_mfma_f32_16x16x32_bf16 v[56:59], a[232:235], v[204:207], v[56:59]\n"
"ds_read_b128 v[196:199], v243 offset:18432\n"
"v_mfma_f32_16x16x32_bf16 v[60:63], a[236:239], v[204:207], v[60:63]\n"
"ds_read_b64_tr_b16 a[250:251], v248 offset:12800\n"
"v_mfma_f32_16x16x32_bf16 v[64:67], a[224:227], v[208:211], v[64:67]\n"
"v_mfma_f32_16x16x32_bf16 v[68:71], a[228:231], v[208:211], v[68:71]\n"
"ds_read_b128 v[200:203], v243 offset:20480\n"
"v_mfma_f32_16x16x32_bf16 v[72:75], a[232:235], v[208:211], v[72:75]\n"
"buffer_load_dwordx4 v235, s[12:15], s48 offen offset:1024 lds\n"
"v_mfma_f32_16x16x32_bf16 v[76:79], a[236:239], v[208:211], v[76:79]\n"
"ds_read_b64_tr_b16 a[252:253], v249 offset:12288\n"
"v_mfma_f32_16x16x32_bf16 v[80:83], a[224:227], v[212:215], v[80:83]\n"
"v_mfma_f32_16x16x32_bf16 v[84:87], a[228:231], v[212:215], v[84:87]\n"
"ds_read_b128 v[204:207], v243 offset:22528\n"
"v_mfma_f32_16x16x32_bf16 v[88:91], a[232:235], v[212:215], v[88:91]\n"
"ds_read_b64_tr_b16 a[254:255], v249 offset:12800\n"
"v_mfma_f32_16x16x32_bf16 v[92:95], a[236:239], v[212:215], v[92:95]\n"
"buffer_load_dwordx4 v236, s[12:15], s48 offen offset:2048 lds\n"
"v_mfma_f32_16x16x32_bf16 v[96:99], a[224:227], v[216:219], v[96:99]\n"
"ds_read_b128 v[208:211], v243 offset:24576\n"
"v_mfma_f32_16x16x32_bf16 v[100:103], a[228:231], v[216:219], v[100:103]\n"
"s_lshr_b32 s88, s34, 1\n"
"v_mfma_f32_16x16x32_bf16 v[104:107], a[232:235], v[216:219], v[104:107]\n"
"s_sub_u32 s88, s88, s33\n"
"buffer_load_dwordx4 v237, s[12:15], s48 offen offset:3072 lds\n"
"v_mfma_f32_16x16x32_bf16 v[108:111], a[236:239], v[216:219], v[108:111]\n"
"s_waitcnt lgkmcnt(3)\n"
"s_and_b32 s88, s88, 3\n"
"v_mfma_f32_16x16x32_bf16 v[112:115], a[224:227], v[220:223], v[112:115]\n"
"s_lshl_b32 s88, s88, 1\n"
"ds_read_b128 v[212:215], v243 offset:26624\n"
"v_mfma_f32_16x16x32_bf16 v[116:119], a[228:231], v[220:223], v[116:119]\n"
"s_and_b32 s89, s34, 1\n"
"s_add_u32 m0, s37, 12288\n"
"s_add_u32 s48, s98, 65536\n"
"v_mfma_f32_16x16x32_bf16 v[120:123], a[232:235], v[220:223], v[120:123]\n"
"s_add_u32 s88, s88, s89\n"
"s_waitcnt vmcnt(17)\n"
"ds_read_b64_tr_b16 v[152:153], v254 offset:45056\n"
"v_mfma_f32_16x16x32_bf16 v[124:127], a[236:239], v[220:223], v[124:127]\n"
"s_mul_i32 s88, s88, 20480\n"
"ds_read_b128 v[216:219], v243 offset:28672\n"
"v_mfma_f32_16x16x32_bf16 v[0:3], a[240:243], v[192:195], v[0:3]\n"
"s_add_u32 s88, s88, s91\n"
"ds_read_b64_tr_b16 v[154:155], v254 offset:47616\n"
"v_mfma_f32_16x16x32_bf16 v[4:7], a[244:247], v[192:195], v[4:7]\n"
"s_waitcnt lgkmcnt(5)\n"
"buffer_load_dwordx4 v234, s[12:15], s48 offen offset:0 lds\n"
"v_mfma_f32_16x16x32_bf16 v[8:11], a[248:251], v[192:195], v[8:11]\n"
"s_mul_i32 s89, s35, 163840\n"
"ds_read_b64_tr_b16 v[156:157], v254 offset:45088\n"
"s_add_u32 s88, s88, s89\n"
"v_mfma_f32_16x16x32_bf16 v[12:15], a[252:255], v[192:195], v[12:15]\n"
"s_add_u32 s90, s88, 4956160\n"
"ds_read_b64_tr_b16 v[158:159], v254 offset:47648\n"
"v_mfma_f32_16x16x32_bf16 v[16:19], a[240:243], v[196:199], v[16:19]\n"
"ds_read_b64_tr_b16 v[160:161], v254 offset:45120\n"
"v_mfma_f32_16x16x32_bf16 v[20:23], a[244:247], v[196:199], v[20:23]\n"
"ds_read_b128 v[220:223], v243 offset:30720\n"
"v_mfma_f32_16x16x32_bf16 v[24:27], a[248:251], v[196:199], v[24:27]\n"
"ds_read_b64_tr_b16 v[162:163], v254 offset:47680\n"
"v_mfma_f32_16x16x32_bf16 v[28:31], a[252:255], v[196:199], v[28:31]\n"
"buffer_load_dwordx4 v235, s[12:15], s48 offen offset:1024 lds\n"
"v_mfma_f32_16x16x32_bf16 v[32:35], a[240:243], v[200:203], v[32:35]\n"
"ds_read_b64_tr_b16 v[164:165], v254 offset:45152\n"
"v_mfma_f32_16x16x32_bf16 v[36:39], a[244:247], v[200:203], v[36:39]\n"
"ds_read_b64_tr_b16 v[166:167], v254 offset:47712\n"
"v_mfma_f32_16x16x32_bf16 v[40:43], a[248:251], v[200:203], v[40:43]\n"
"s_waitcnt lgkmcnt(7)\n"
"buffer_store_dwordx4 v[152:155], v224, s[8:11], s90 offen sc1\n"
"v_mfma_f32_16x16x32_bf16 v[44:47], a[252:255], v[200:203], v[44:47]\n"
"s_add_u32 s90, s90, 1024\n"
"ds_read_b64_tr_b16 v[168:169], v254 offset:45184\n"
"v_add_u32 v244, 114688, v242\n"
"v_mfma_f32_16x16x32_bf16 v[48:51], a[240:243], v[204:207], v[48:51]\n"
"ds_read_b64_tr_b16 v[170:171], v254 offset:47744\n"
"v_add_u32 v245, 114688, v243\n"
"v_mfma_f32_16x16x32_bf16 v[52:55], a[244:247], v[204:207], v[52:55]\n"
"buffer_load_dwordx4 v236, s[12:15], s48 offen offset:2048 lds\n"
"v_mfma_f32_16x16x32_bf16 v[56:59], a[248:251], v[204:207], v[56:59]\n"
"s_waitcnt lgkmcnt(7)\n"
"buffer_store_dwordx4 v[156:159], v224, s[8:11], s90 offen sc1\n"
"v_mfma_f32_16x16x32_bf16 v[60:63], a[252:255], v[204:207], v[60:63]\n"
"buffer_load_dwordx4 v237, s[12:15], s48 offen offset:3072 lds\n"
"v_mfma_f32_16x16x32_bf16 v[64:67], a[240:243], v[208:211], v[64:67]\n"
"s_add_u32 s90, s90, 1024\n"
"s_waitcnt lgkmcnt(5)\n"
"s_barrier\n"
"v_mfma_f32_16x16x32_bf16 v[68:71], a[244:247], v[208:211], v[68:71]\n"
"s_lshl_b32 s88, s31, 1\n"
"v_add_u32 v242, 65536, v242\n"
"v_add_u32 v243, 65536, v243\n"
"v_mfma_f32_16x16x32_bf16 v[72:75], a[248:251], v[208:211], v[72:75]\n"
"s_add_u32 s88, s88, s30\n"
"ds_read_b128 v[192:195], v242 offset:16384\n"
"s_lshr_b32 s89, s88, 3\n"
"v_mfma_f32_16x16x32_bf16 v[76:79], a[252:255], v[208:211], v[76:79]\n"
"s_and_b32 s88, s88, 7\n"
"ds_read_b128 v[196:199], v242 offset:18432\n"
"v_mfma_f32_16x16x32_bf16 v[80:83], a[240:243], v[212:215], v[80:83]\n"
"s_waitcnt lgkmcnt(6)\n"
"buffer_store_dwordx4 v[160:163], v224, s[8:11], s90 offen sc1\n"
"v_mfma_f32_16x16x32_bf16 v[84:87], a[244:247], v[212:215], v[84:87]\n"
"s_mul_i32 s88, s88, 160\n"
"s_mul_i32 s89, s89, 40960\n"
"v_mfma_f32_16x16x32_bf16 v[88:91], a[248:251], v[212:215], v[88:91]\n"
"s_add_u32 s90, s90, 1024\n"
"ds_read_b128 v[200:203], v243 offset:16384\n"
"v_lshrrev_b32 v252, 4, v224\n"
"v_mfma_f32_16x16x32_bf16 v[92:95], a[252:255], v[212:215], v[92:95]\n"
"s_add_u32 s88, s88, s89\n"
"v_add_u32 v252, 0, v252\n"
"s_add_u32 s24, s82, s88\n"
"v_mfma_f32_16x16x32_bf16 v[96:99], a[240:243], v[216:219], v[96:99]\n"
"s_waitcnt lgkmcnt(5)\n"
"buffer_store_dwordx4 v[164:167], v224, s[8:11], s90 offen sc1\n"
"v_mfma_f32_16x16x32_bf16 v[100:103], a[244:247], v[216:219], v[100:103]\n"
"s_addc_u32 s25, s83, 0\n"
"v_mul_u32_u24 v252, 205, v252\n"
"v_lshrrev_b32 v252, 11, v252\n"
"v_mfma_f32_16x16x32_bf16 v[104:107], a[248:251], v[216:219], v[104:107]\n"
"s_and_b32 s25, s25, 0xffff\n"
"ds_read_b128 v[204:207], v243 offset:18432\n"
"v_mul_u32_u24 v252, 1120, v252\n"
"v_mfma_f32_16x16x32_bf16 v[108:111], a[252:255], v[216:219], v[108:111]\n"
"s_cmp_lt_u32 s31, 216\n"
"v_add_u32 v252, v252, v224\n"
"v_lshrrev_b32 v251, 4, v224\n"
"v_mfma_f32_16x16x32_bf16 v[112:115], a[240:243], v[220:223], v[112:115]\n"
"s_cselect_b32 s26, 39840, 0\n"
"s_waitcnt vmcnt(20)\n"
"ds_read_b64_tr_b16 v[172:173], v254 offset:55296\n"
"v_mfma_f32_16x16x32_bf16 v[116:119], a[244:247], v[220:223], v[116:119]\n"
"s_add_u32 m0, s91, 45056\n"
"ds_read_b128 v[208:211], v242 offset:20480\n"
"v_add_u32 v251, 64, v251\n"
"v_mfma_f32_16x16x32_bf16 v[120:123], a[248:251], v[220:223], v[120:123]\n"
"s_mov_b32 s48, 0\n"
"ds_read_b64_tr_b16 v[174:175], v254 offset:57856\n"
"v_mul_u32_u24 v251, 205, v251\n"
"v_mfma_f32_16x16x32_bf16 v[124:127], a[252:255], v[220:223], v[124:127]\n"
"s_waitcnt lgkmcnt(4)\n"
"buffer_load_dwordx4 v252, s[24:27], s48 offen lds\n"
"v_mfma_f32_16x16x32_bf16 v[128:131], a[0:3], v[192:195], 0\n"
"s_add_u32 s90, s90, 1024\n"
"ds_read_b64_tr_b16 v[176:177], v254 offset:55328\n"
"v_lshrrev_b32 v251, 11, v251\n"
"v_mfma_f32_16x16x32_bf16 v[132:135], a[4:7], v[192:195], 0\n"
"s_add_u32 m0, s91, 46080\n"
"buffer_store_dwordx4 v[168:171], v224, s[8:11], s90 offen sc1\n"
"v_mfma_f32_16x16x32_bf16 v[136:139], a[8:11], v[192:195], 0\n"
"s_mov_b32 s48, 1024\n"
"ds_read_b64_tr_b16 v[178:179], v254 offset:57888\n"
"v_mul_u32_u24 v251, 1120, v251\n"
"v_mfma_f32_16x16x32_bf16 v[140:143], a[12:15], v[192:195], 0\n"
"s_add_u32 s90, s90, 6144\n"
"ds_read_b64_tr_b16 v[180:181], v254 offset:55360\n"
"v_add_u32 v251, v251, v224\n"
"v_mfma_f32_16x16x32_bf16 v[144:147], a[0:3], v[196:199], 0\n"
"ds_read_b128 v[212:215], v242 offset:22528\n"
"v_lshrrev_b32 v252, 4, v224\n"
"v_mfma_f32_16x16x32_bf16 v[148:151], a[4:7], v[196:199], 0\n"
"ds_read_b64_tr_b16 v[182:183], v254 offset:57920\n"
"v_add_u32 v252, 128, v252\n"
"v_mfma_f32_16x16x32_bf16 v[152:155], a[8:11], v[196:199], 0\n"
"buffer_load_dwordx4 v251, s[24:27], s48 offen lds\n"
"v_mfma_f32_16x16x32_bf16 v[156:159], a[12:15], v[196:199], 0\n"
"s_add_u32 m0, s91, 47104\n"
"ds_read_b64_tr_b16 v[184:185], v254 offset:55392\n"
"v_mul_u32_u24 v252, 205, v252\n"
"v_mfma_f32_16x16x32_bf16 v[128:131], a[16:19], v[200:203], v[128:131]\n"
"s_mov_b32 s48, 2048\n"
"ds_read_b64_tr_b16 v[186:187], v254 offset:57952\n"
"v_lshrrev_b32 v252, 11, v252\n"
"v_mfma_f32_16x16x32_bf16 v[132:135], a[20:23], v[200:203], v[132:135]\n"
"s_waitcnt lgkmcnt(7)\n"
"buffer_store_dwordx4 v[172:175], v224, s[8:11], s90 offen sc1\n"
"v_mfma_f32_16x16x32_bf16 v[136:139], a[24:27], v[200:203], v[136:139]\n"
"s_add_u32 s90, s90, 1024\n"
"ds_read_b64_tr_b16 v[188:189], v254 offset:55424\n"
"v_mul_u32_u24 v252, 1120, v252\n"
"v_mfma_f32_16x16x32_bf16 v[140:143], a[28:31], v[200:203], v[140:143]\n"
"ds_read_b128 v[216:219], v243 offset:20480\n"
"v_add_u32 v252, v252, v224\n"
"v_mfma_f32_16x16x32_bf16 v[144:147], a[16:19], v[204:207], v[144:147]\n"
"ds_read_b64_tr_b16 v[190:191], v254 offset:57984\n"
"v_lshrrev_b32 v251, 4, v224\n"
"v_mfma_f32_16x16x32_bf16 v[148:151], a[20:23], v[204:207], v[148:151]\n"
"buffer_load_dwordx4 v252, s[24:27], s48 offen lds\n"
"v_mfma_f32_16x16x32_bf16 v[152:155], a[24:27], v[204:207], v[152:155]\n"
"s_add_u32 m0, s91, 48128\n"
"ds_read_b128 v[220:223], v243 offset:22528\n"
"v_add_u32 v251, 192, v251\n"
"v_mfma_f32_16x16x32_bf16 v[156:159], a[28:31], v[204:207], v[156:159]\n"
"s_waitcnt lgkmcnt(9)\n"
"buffer_store_dwordx4 v[176:179], v224, s[8:11], s90 offen sc1\n"
"v_mfma_f32_16x16x32_bf16 v[128:131], a[32:35], v[208:211], v[128:131]\n"
"s_mov_b32 s48, 3072\n"
"ds_read_b128 v[192:195], v242 offset:40960\n"
"v_mul_u32_u24 v251, 205, v251\n"
"v_mfma_f32_16x16x32_bf16 v[132:135], a[36:39], v[208:211], v[132:135]\n"
"s_add_u32 s90, s90, 1024\n"
"ds_read_b128 v[196:199], v242 offset:43008\n"
"v_lshrrev_b32 v251, 11, v251\n"
"v_mfma_f32_16x16x32_bf16 v[136:139], a[40:43], v[208:211], v[136:139]\n"
"s_waitcnt lgkmcnt(8)\n"
"buffer_store_dwordx4 v[180:183], v224, s[8:11], s90 offen sc1\n"
"v_mfma_f32_16x16x32_bf16 v[140:143], a[44:47], v[208:211], v[140:143]\n"
"s_lshr_b32 s92, s31, 3\n"
"v_mul_u32_u24 v251, 1120, v251\n"
"v_add_u32 v251, v251, v224\n"
"v_mfma_f32_16x16x32_bf16 v[144:147], a[32:35], v[212:215], v[144:147]\n"
"s_add_u32 s90, s90, 1024\n"
"buffer_load_dwordx4 v251, s[24:27], s48 offen lds\n"
"v_mfma_f32_16x16x32_bf16 v[148:151], a[36:39], v[212:215], v[148:151]\n"
"s_add_u32 m0, s91, 49152\n"
"ds_read_b128 v[200:203], v243 offset:40960\n"
"v_lshrrev_b32 v252, 4, v224\n"
"v_mfma_f32_16x16x32_bf16 v[152:155], a[40:43], v[212:215], v[152:155]\n"
"s_waitcnt lgkmcnt(7)\n"
"buffer_store_dwordx4 v[184:187], v224, s[8:11], s90 offen sc1\n"
"v_mfma_f32_16x16x32_bf16 v[156:159], a[44:47], v[212:215], v[156:159]\n"
"s_waitcnt lgkmcnt(2)\n"
"v_add_u32 v252, 256, v252\n"
"v_mul_u32_u24 v252, 205, v252\n"
"v_mfma_f32_16x16x32_bf16 v[128:131], a[48:51], v[216:219], v[128:131]\n"
"s_mov_b32 s48, 4096\n"
"ds_read_b128 v[204:207], v243 offset:43008\n"
"v_lshrrev_b32 v252, 11, v252\n"
"v_mfma_f32_16x16x32_bf16 v[132:135], a[52:55], v[216:219], v[132:135]\n"
"s_mul_i32 s93, s32, 14\n"
"v_mul_u32_u24 v252, 1120, v252\n"
"v_add_u32 v252, v252, v224\n"
"v_mfma_f32_16x16x32_bf16 v[136:139], a[56:59], v[216:219], v[136:139]\n"
"s_add_u32 s93, s93, s92\n"
"buffer_load_dwordx4 v252, s[24:27], s48 offen lds\n"
"v_mfma_f32_16x16x32_bf16 v[140:143], a[60:63], v[216:219], v[140:143]\n"
"s_mov_b32 s27, 0x20000\n"
"ds_read_b128 v[208:211], v242 offset:45056\n"
"v_mul_f32 v0, s55, v0\n"
"v_mfma_f32_16x16x32_bf16 v[144:147], a[48:51], v[220:223], v[144:147]\n"
"s_sub_u32 s93, s93, 18\n"
"v_mul_f32 v1, s55, v1\n"
"v_mul_f32 v2, s55, v2\n"
"v_mfma_f32_16x16x32_bf16 v[148:151], a[52:55], v[220:223], v[148:151]\n"
"s_cmp_lt_u32 s92, 18\n"
"v_mul_f32 v3, s55, v3\n"
"v_mul_f32 v4, s55, v4\n"
"v_mfma_f32_16x16x32_bf16 v[152:155], a[56:59], v[220:223], v[152:155]\n"
"s_cselect_b32 s93, 200, s93\n"
"v_mul_f32 v5, s55, v5\n"
"v_mul_f32 v6, s55, v6\n"
"v_mfma_f32_16x16x32_bf16 v[156:159], a[60:63], v[220:223], v[156:159]\n"
"s_cmp_lt_u32 s30, 2\n"
"ds_read_b128 v[212:215], v242 offset:47104\n"
"v_mul_f32 v7, s55, v7\n"
"v_mfma_f32_16x16x32_bf16 v[128:131], a[64:67], v[192:195], v[128:131]\n"
"s_cselect_b64 s[24:25], s[78:79], s[80:81]\n"
"v_cvt_pk_bf16_f32 v0, v0, v1\n"
"v_cvt_pk_bf16_f32 v1, v2, v3\n"
"v_mfma_f32_16x16x32_bf16 v[132:135], a[68:71], v[192:195], v[132:135]\n"
"s_and_b32 s25, s25, 0xffff\n"
"v_cvt_pk_bf16_f32 v2, v4, v5\n"
"v_cvt_pk_bf16_f32 v3, v6, v7\n"
"v_mfma_f32_16x16x32_bf16 v[136:139], a[72:75], v[192:195], v[136:139]\n"
"s_add_u32 s90, s90, 1024\n"
"v_mul_f32 v8, s55, v8\n"
"v_mul_f32 v9, s55, v9\n"
"v_mfma_f32_16x16x32_bf16 v[140:143], a[76:79], v[192:195], v[140:143]\n"
"s_waitcnt lgkmcnt(1)\n"
"buffer_store_dwordx4 v[188:191], v224, s[8:11], s90 offen sc1\n"
"v_mfma_f32_16x16x32_bf16 v[144:147], a[64:67], v[196:199], v[144:147]\n"
"s_cmp_lt_u32 s93, 108\n"
"ds_read_b128 v[216:219], v243 offset:45056\n"
"v_mul_f32 v10, s55, v10\n"
"v_mfma_f32_16x16x32_bf16 v[148:151], a[68:71], v[196:199], v[148:151]\n"
"s_cselect_b32 s26, 2211840, 0\n"
"v_mul_f32 v11, s55, v11\n"
"v_mul_f32 v12, s55, v12\n"
"v_mfma_f32_16x16x32_bf16 v[152:155], a[72:75], v[196:199], v[152:155]\n"
"s_cmp_lt_u32 s93, 108\n"
"v_mul_f32 v13, s55, v13\n"
"v_mul_f32 v14, s55, v14\n"
"v_mfma_f32_16x16x32_bf16 v[156:159], a[76:79], v[196:199], v[156:159]\n"
"s_cselect_b32 s44, 0, 2130706432\n"
"ds_read_b128 v[220:223], v243 offset:47104\n"
"v_lshrrev_b32 v251, 5, v224\n"
"v_mfma_f32_16x16x32_bf16 v[128:131], a[80:83], v[200:203], v[128:131]\n"
"s_and_b32 s92, s30, 1\n"
"v_mul_u32_u24 v251, 3456, v251\n"
"v_mul_f32 v15, s55, v15\n"
"v_mfma_f32_16x16x32_bf16 v[132:135], a[84:87], v[200:203], v[132:135]\n"
"s_mul_i32 s61, s92, 1105920\n"
"v_and_b32 v252, 16, v224\n"
"v_cvt_pk_bf16_f32 v8, v8, v9\n"
"v_mfma_f32_16x16x32_bf16 v[136:139], a[88:91], v[200:203], v[136:139]\n"
"s_lshl_b32 s92, s93, 5\n"
"ds_read_b128 v[192:195], v244 offset:16384\n"
"v_add_u32 v251, v251, v252\n"
"v_mfma_f32_16x16x32_bf16 v[140:143], a[92:95], v[200:203], v[140:143]\n"
"s_mov_b32 s49, 0\n"
"v_cvt_pk_bf16_f32 v9, v10, v11\n"
"v_cvt_pk_bf16_f32 v10, v12, v13\n"
"v_mfma_f32_16x16x32_bf16 v[144:147], a[80:83], v[204:207], v[144:147]\n"
"s_add_u32 s61, s61, s92\n"
"buffer_store_dwordx4 v[0:3], v250, s[8:11], s49 offen offset:0 sc1\n"
"v_mfma_f32_16x16x32_bf16 v[148:151], a[84:87], v[204:207], v[148:151]\n"
"s_lshr_b32 s92, s93, 1\n"
"v_cvt_pk_bf16_f32 v11, v14, v15\n"
"v_mul_f32 v16, s55, v16\n"
"v_mfma_f32_16x16x32_bf16 v[152:155], a[88:91], v[204:207], v[152:155]\n"
"s_lshl_b32 s92, s92, 9\n"
"ds_read_b128 v[196:199], v244 offset:18432\n"
"v_mul_f32 v17, s55, v17\n"
"v_mfma_f32_16x16x32_bf16 v[156:159], a[92:95], v[204:207], v[156:159]\n"
"s_and_b32 s60, s93, 1\n"
"v_mul_f32 v18, s55, v18\n"
"v_mul_f32 v19, s55, v19\n"
"v_mfma_f32_16x16x32_bf16 v[128:131], a[96:99], v[208:211], v[128:131]\n"
"s_lshl_b32 s60, s60, 3\n"
"v_mul_f32 v20, s55, v20\n"
"v_mul_f32 v21, s55, v21\n"
"v_mfma_f32_16x16x32_bf16 v[132:135], a[100:103], v[208:211], v[132:135]\n"
"s_add_u32 s60, s60, s92\n"
"buffer_store_dwordx4 v[8:11], v250, s[8:11], s49 offen offset:1024 sc1\n"
"v_mfma_f32_16x16x32_bf16 v[136:139], a[104:107], v[208:211], v[136:139]\n"
"s_add_u32 s60, s60, 2695168\n"
"ds_read_b128 v[200:203], v245 offset:16384\n"
"v_mul_f32 v22, s55, v22\n"
"v_mfma_f32_16x16x32_bf16 v[140:143], a[108:111], v[208:211], v[140:143]\n"
"s_waitcnt lgkmcnt(2)\n"
"v_mul_f32 v23, s55, v23\n"
"v_cvt_pk_bf16_f32 v16, v16, v17\n"
"v_mfma_f32_16x16x32_bf16 v[144:147], a[96:99], v[212:215], v[144:147]\n"
"s_lshl_b32 s92, s30, 8\n"
"v_cvt_pk_bf16_f32 v17, v18, v19\n"
"v_cvt_pk_bf16_f32 v18, v20, v21\n"
"v_mfma_f32_16x16x32_bf16 v[148:151], a[100:103], v[212:215], v[148:151]\n"
"s_add_u32 s93, s93, s92\n"
"v_cvt_pk_bf16_f32 v19, v22, v23\n"
"v_mul_f32 v24, s55, v24\n"
"v_mfma_f32_16x16x32_bf16 v[152:155], a[104:107], v[212:215], v[152:155]\n"
"s_cmp_lt_u32 s93, 108\n"
"ds_read_b128 v[204:207], v245 offset:18432\n"
"v_mul_f32 v25, s55, v25\n"
"v_mfma_f32_16x16x32_bf16 v[156:159], a[108:111], v[212:215], v[156:159]\n"
"s_cselect_b32 s60, s60, 2130706432\n"
"v_mul_f32 v26, s55, v26\n"
"v_mul_f32 v27, s55, v27\n"
"v_mfma_f32_16x16x32_bf16 v[128:131], a[112:115], v[216:219], v[128:131]\n"
"s_mul_i32 s92, s30, 10240\n"
"v_mul_f32 v28, s55, v28\n"
"v_mul_f32 v29, s55, v29\n"
"v_mfma_f32_16x16x32_bf16 v[132:135], a[116:119], v[216:219], v[132:135]\n"
"s_add_u32 m0, s92, 0\n"
"ds_read_b128 v[208:211], v244 offset:20480\n"
"v_mul_f32 v30, s55, v30\n"
"v_mfma_f32_16x16x32_bf16 v[136:139], a[120:123], v[216:219], v[136:139]\n"
"s_add_u32 s48, s61, 0\n"
"v_mul_f32 v31, s55, v31\n"
"v_cvt_pk_bf16_f32 v24, v24, v25\n"
"v_mfma_f32_16x16x32_bf16 v[140:143], a[124:127], v[216:219], v[140:143]\n"
"buffer_load_dwordx4 v251, s[24:27], s48 offen lds\n"
"v_mfma_f32_16x16x32_bf16 v[144:147], a[112:115], v[220:223], v[144:147]\n"
"s_add_u32 m0, s92, 1024\n"
"v_cvt_pk_bf16_f32 v25, v26, v27\n"
"v_cvt_pk_bf16_f32 v26, v28, v29\n"
"v_mfma_f32_16x16x32_bf16 v[148:151], a[116:119], v[220:223], v[148:151]\n"
"s_add_u32 s48, s61, 110592\n"
"ds_read_b128 v[212:215], v244 offset:22528\n"
"v_cvt_pk_bf16_f32 v27, v30, v31\n"
"v_mfma_f32_16x16x32_bf16 v[152:155], a[120:123], v[220:223], v[152:155]\n"
"buffer_load_dwordx4 v251, s[24:27], s48 offen lds\n"
"v_mfma_f32_16x16x32_bf16 v[156:159], a[124:127], v[220:223], v[156:159]\n"
"s_mov_b32 s49, 4096\n"
"v_mul_f32 v32, s55, v32\n"
"v_mul_f32 v33, s55, v33\n"
"v_mfma_f32_16x16x32_bf16 v[128:131], a[128:131], v[192:195], v[128:131]\n"
"v_mul_f32 v34, s55, v34\n"
"v_mul_f32 v35, s55, v35\n"
"v_mfma_f32_16x16x32_bf16 v[132:135], a[132:135], v[192:195], v[132:135]\n"
"ds_read_b128 v[216:219], v245 offset:20480\n"
"v_mul_f32 v36, s55, v36\n"
"v_mfma_f32_16x16x32_bf16 v[136:139], a[136:139], v[192:195], v[136:139]\n"
"buffer_store_dwordx4 v[16:19], v250, s[8:11], s49 offen offset:0 sc1\n"
"v_mfma_f32_16x16x32_bf16 v[140:143], a[140:143], v[192:195], v[140:143]\n"
"s_waitcnt lgkmcnt(2)\n"
"v_mul_f32 v37, s55, v37\n"
"v_mul_f32 v38, s55, v38\n"
"v_mfma_f32_16x16x32_bf16 v[144:147], a[128:131], v[196:199], v[144:147]\n"
"v_mul_f32 v39, s55, v39\n"
"v_cvt_pk_bf16_f32 v32, v32, v33\n"
"v_mfma_f32_16x16x32_bf16 v[148:151], a[132:135], v[196:199], v[148:151]\n"
"ds_read_b128 v[220:223], v245 offset:22528\n"
"v_cvt_pk_bf16_f32 v33, v34, v35\n"
"v_mfma_f32_16x16x32_bf16 v[152:155], a[136:139], v[196:199], v[152:155]\n"
"v_cvt_pk_bf16_f32 v34, v36, v37\n"
"v_cvt_pk_bf16_f32 v35, v38, v39\n"
"v_mfma_f32_16x16x32_bf16 v[156:159], a[140:143], v[196:199], v[156:159]\n"
"s_add_u32 m0, s92, 2048\n"
"v_mul_f32 v40, s55, v40\n"
"v_mul_f32 v41, s55, v41\n"
"v_mfma_f32_16x16x32_bf16 v[128:131], a[144:147], v[200:203], v[128:131]\n"
"s_add_u32 s48, s61, 221184\n"
"ds_read_b128 v[192:195], v244 offset:40960\n"
"v_mul_f32 v42, s55, v42\n"
"v_mfma_f32_16x16x32_bf16 v[132:135], a[148:151], v[200:203], v[132:135]\n"
"buffer_load_dwordx4 v251, s[24:27], s48 offen lds\n"
"v_mfma_f32_16x16x32_bf16 v[136:139], a[152:155], v[200:203], v[136:139]\n"
"v_mul_f32 v43, s55, v43\n"
"v_mul_f32 v44, s55, v44\n"
"v_mfma_f32_16x16x32_bf16 v[140:143], a[156:159], v[200:203], v[140:143]\n"
"v_mul_f32 v45, s55, v45\n"
"v_mul_f32 v46, s55, v46\n"
"v_mfma_f32_16x16x32_bf16 v[144:147], a[144:147], v[204:207], v[144:147]\n"
"ds_read_b128 v[196:199], v244 offset:43008\n"
"v_mul_f32 v47, s55, v47\n"
"v_mfma_f32_16x16x32_bf16 v[148:151], a[148:151], v[204:207], v[148:151]\n"
"v_cvt_pk_bf16_f32 v40, v40, v41\n"
"v_cvt_pk_bf16_f32 v41, v42, v43\n"
"v_mfma_f32_16x16x32_bf16 v[152:155], a[152:155], v[204:207], v[152:155]\n"
"v_cvt_pk_bf16_f32 v42, v44, v45\n"
"v_cvt_pk_bf16_f32 v43, v46, v47\n"
"v_mfma_f32_16x16x32_bf16 v[156:159], a[156:159], v[204:207], v[156:159]\n"
"v_mul_f32 v48, s55, v48\n"
"v_mul_f32 v49, s55, v49\n"
"v_mfma_f32_16x16x32_bf16 v[128:131], a[160:163], v[208:211], v[128:131]\n"
"ds_read_b128 v[200:203], v245 offset:40960\n"
"v_mul_f32 v50, s55, v50\n"
"v_mfma_f32_16x16x32_bf16 v[132:135], a[164:167], v[208:211], v[132:135]\n"
"buffer_store_dwordx4 v[24:27], v250, s[8:11], s49 offen offset:1024 sc1\n"
"v_mfma_f32_16x16x32_bf16 v[136:139], a[168:171], v[208:211], v[136:139]\n"
"v_mul_f32 v51, s55, v51\n"
"v_mul_f32 v52, s55, v52\n"
"v_mfma_f32_16x16x32_bf16 v[140:143], a[172:175], v[208:211], v[140:143]\n"
"s_waitcnt lgkmcnt(1)\n"
"v_mul_f32 v53, s55, v53\n"
"v_mul_f32 v54, s55, v54\n"
"v_mfma_f32_16x16x32_bf16 v[144:147], a[160:163], v[212:215], v[144:147]\n"
"ds_read_b128 v[204:207], v245 offset:43008\n"
"v_mul_f32 v55, s55, v55\n"
"v_mfma_f32_16x16x32_bf16 v[148:151], a[164:167], v[212:215], v[148:151]\n"
"s_add_u32 m0, s92, 3072\n"
"v_cvt_pk_bf16_f32 v48, v48, v49\n"
"v_cvt_pk_bf16_f32 v49, v50, v51\n"
"v_mfma_f32_16x16x32_bf16 v[152:155], a[168:171], v[212:215], v[152:155]\n"
"s_add_u32 s48, s61, 331776\n"
"v_cvt_pk_bf16_f32 v50, v52, v53\n"
"v_cvt_pk_bf16_f32 v51, v54, v55\n"
"v_mfma_f32_16x16x32_bf16 v[156:159], a[172:175], v[212:215], v[156:159]\n"
"ds_read_b128 v[208:211], v244 offset:45056\n"
"v_mul_f32 v56, s55, v56\n"
"v_mfma_f32_16x16x32_bf16 v[128:131], a[176:179], v[216:219], v[128:131]\n"
"buffer_load_dwordx4 v251, s[24:27], s48 offen lds\n"
"v_mfma_f32_16x16x32_bf16 v[132:135], a[180:183], v[216:219], v[132:135]\n"
"v_mul_f32 v57, s55, v57\n"
"v_mul_f32 v58, s55, v58\n"
"v_mfma_f32_16x16x32_bf16 v[136:139], a[184:187], v[216:219], v[136:139]\n"
"v_mul_f32 v59, s55, v59\n"
"v_mul_f32 v60, s55, v60\n"
"v_mfma_f32_16x16x32_bf16 v[140:143], a[188:191], v[216:219], v[140:143]\n"
"ds_read_b128 v[212:215], v244 offset:47104\n"
"v_mul_f32 v61, s55, v61\n"
"v_mfma_f32_16x16x32_bf16 v[144:147], a[176:179], v[220:223], v[144:147]\n"
"v_mul_f32 v62, s55, v62\n"
"v_mul_f32 v63, s55, v63\n"
"v_mfma_f32_16x16x32_bf16 v[148:151], a[180:183], v[220:223], v[148:151]\n"
"v_cvt_pk_bf16_f32 v56, v56, v57\n"
"v_cvt_pk_bf16_f32 v57, v58, v59\n"
"v_mfma_f32_16x16x32_bf16 v[152:155], a[184:187], v[220:223], v[152:155]\n"
"v_cvt_pk_bf16_f32 v58, v60, v61\n"
"v_cvt_pk_bf16_f32 v59, v62, v63\n"
"v_mfma_f32_16x16x32_bf16 v[156:159], a[188:191], v[220:223], v[156:159]\n"
"s_mov_b32 s49, 8192\n"
"ds_read_b128 v[216:219], v245 offset:45056\n"
"v_mul_f32 v64, s55, v64\n"
"v_mfma_f32_16x16x32_bf16 v[128:131], a[192:195], v[192:195], v[128:131]\n"
"v_mul_f32 v65, s55, v65\n"
"v_mul_f32 v66, s55, v66\n"
"v_mfma_f32_16x16x32_bf16 v[132:135], a[196:199], v[192:195], v[132:135]\n"
"buffer_store_dwordx4 v[32:35], v250, s[8:11], s49 offen offset:0 sc1\n"
"v_mfma_f32_16x16x32_bf16 v[136:139], a[200:203], v[192:195], v[136:139]\n"
"v_mul_f32 v67, s55, v67\n"
"v_mul_f32 v68, s55, v68\n"
"v_mfma_f32_16x16x32_bf16 v[140:143], a[204:207], v[192:195], v[140:143]\n"
"s_add_u32 m0, s92, 4096\n"
"ds_read_b128 v[220:223], v245 offset:47104\n"
"v_mul_f32 v69, s55, v69\n"
"v_mfma_f32_16x16x32_bf16 v[144:147], a[192:195], v[196:199], v[144:147]\n"
"s_add_u32 s48, s61, 442368\n"
"v_mul_f32 v70, s55, v70\n"
"v_mul_f32 v71, s55, v71\n"
"v_mfma_f32_16x16x32_bf16 v[148:151], a[196:199], v[196:199], v[148:151]\n"
"buffer_load_dwordx4 v251, s[24:27], s48 offen lds\n"
"v_mfma_f32_16x16x32_bf16 v[152:155], a[200:203], v[196:199], v[152:155]\n"
"v_cvt_pk_bf16_f32 v64, v64, v65\n"
"v_cvt_pk_bf16_f32 v65, v66, v67\n"
"v_mfma_f32_16x16x32_bf16 v[156:159], a[204:207], v[196:199], v[156:159]\n"
"s_waitcnt lgkmcnt(1)\n"
"v_cvt_pk_bf16_f32 v66, v68, v69\n"
"v_cvt_pk_bf16_f32 v67, v70, v71\n"
"v_mfma_f32_16x16x32_bf16 v[128:131], a[208:211], v[200:203], v[128:131]\n"
"v_mul_f32 v72, s55, v72\n"
"v_mul_f32 v73, s55, v73\n"
"v_mfma_f32_16x16x32_bf16 v[132:135], a[212:215], v[200:203], v[132:135]\n"
"v_mul_f32 v74, s55, v74\n"
"v_mul_f32 v75, s55, v75\n"
"v_mfma_f32_16x16x32_bf16 v[136:139], a[216:219], v[200:203], v[136:139]\n"
"v_mul_f32 v76, s55, v76\n"
"v_mul_f32 v77, s55, v77\n"
"v_mfma_f32_16x16x32_bf16 v[140:143], a[220:223], v[200:203], v[140:143]\n"
"v_mul_f32 v78, s55, v78\n"
"v_mul_f32 v79, s55, v79\n"
"v_mfma_f32_16x16x32_bf16 v[144:147], a[208:211], v[204:207], v[144:147]\n"
"v_cvt_pk_bf16_f32 v72, v72, v73\n"
"v_cvt_pk_bf16_f32 v73, v74, v75\n"
"v_mfma_f32_16x16x32_bf16 v[148:151], a[212:215], v[204:207], v[148:151]\n"
"s_waitcnt vmcnt(33) lgkmcnt(0)\n"
"s_barrier\n"
"v_mfma_f32_16x16x32_bf16 v[152:155], a[216:219], v[204:207], v[152:155]\n"
"s_add_u32 m0, s37, 16384\n"
"ds_read_b128 v[192:195], v242 offset:0\n"
"v_cvt_pk_bf16_f32 v74, v76, v77\n"
"v_mfma_f32_16x16x32_bf16 v[156:159], a[220:223], v[204:207], v[156:159]\n"
"s_add_u32 s48, s39, 98304\n"
"v_cvt_pk_bf16_f32 v75, v78, v79\n"
"v_mfma_f32_16x16x32_bf16 v[128:131], a[224:227], v[208:211], v[128:131]\n"
"buffer_load_dwordx4 v234, s[12:15], s48 offen offset:0 lds\n"
"v_mfma_f32_16x16x32_bf16 v[132:135], a[228:231], v[208:211], v[132:135]\n"
"buffer_store_dwordx4 v[40:43], v250, s[8:11], s49 offen offset:1024 sc1\n"
"v_mfma_f32_16x16x32_bf16 v[136:139], a[232:235], v[208:211], v[136:139]\n"
"ds_read_b128 v[196:199], v242 offset:2048\n"
"v_mul_f32 v80, s55, v80\n"
"v_mfma_f32_16x16x32_bf16 v[140:143], a[236:239], v[208:211], v[140:143]\n"
"v_mul_f32 v81, s55, v81\n"
"v_mul_f32 v82, s55, v82\n"
"v_mfma_f32_16x16x32_bf16 v[144:147], a[224:227], v[212:215], v[144:147]\n"
"v_mul_f32 v83, s55, v83\n"
"v_mul_f32 v84, s55, v84\n"
"v_mfma_f32_16x16x32_bf16 v[148:151], a[228:231], v[212:215], v[148:151]\n"
"buffer_load_dwordx4 v235, s[12:15], s48 offen offset:1024 lds\n"
"v_mfma_f32_16x16x32_bf16 v[152:155], a[232:235], v[212:215], v[152:155]\n"
"ds_read_b128 v[200:203], v243 offset:0\n"
"v_mul_f32 v85, s55, v85\n"
"v_mfma_f32_16x16x32_bf16 v[156:159], a[236:239], v[212:215], v[156:159]\n"
"v_mul_f32 v86, s55, v86\n"
"v_mul_f32 v87, s55, v87\n"
"v_mfma_f32_16x16x32_bf16 v[128:131], a[240:243], v[216:219], v[128:131]\n"
"v_cvt_pk_bf16_f32 v80, v80, v81\n"
"v_cvt_pk_bf16_f32 v81, v82, v83\n"
"v_mfma_f32_16x16x32_bf16 v[132:135], a[244:247], v[216:219], v[132:135]\n"
"v_cvt_pk_bf16_f32 v82, v84, v85\n"
"v_cvt_pk_bf16_f32 v83, v86, v87\n"
"v_mfma_f32_16x16x32_bf16 v[136:139], a[248:251], v[216:219], v[136:139]\n"
"ds_read_b128 v[204:207], v243 offset:2048\n"
"v_mfma_f32_16x16x32_bf16 v[140:143], a[252:255], v[216:219], v[140:143]\n"
"buffer_load_dwordx4 v236, s[12:15], s48 offen offset:2048 lds\n"
"v_mfma_f32_16x16x32_bf16 v[144:147], a[240:243], v[220:223], v[144:147]\n"
"v_mul_f32 v88, s55, v88\n"
"v_mul_f32 v89, s55, v89\n"
"v_mfma_f32_16x16x32_bf16 v[148:151], a[244:247], v[220:223], v[148:151]\n"
"ds_read_b128 v[208:211], v242 offset:4096\n"
"v_mul_f32 v90, s55, v90\n"
"v_mfma_f32_16x16x32_bf16 v[152:155], a[248:251], v[220:223], v[152:155]\n"
"v_mul_f32 v91, s55, v91\n"
"v_mul_f32 v92, s55, v92\n"
"v_mfma_f32_16x16x32_bf16 v[156:159], a[252:255], v[220:223], v[156:159]\n"
"s_waitcnt lgkmcnt(2)\n"
"buffer_load_dwordx4 v237, s[12:15], s48 offen offset:3072 lds\n"
"v_mfma_f32_16x16x32_bf16 v[4:7], a[0:3], v[192:195], 0\n"
"s_mov_b32 s49, 12288\n"
"v_mul_f32 v93, s55, v93\n"
"v_mul_f32 v94, s55, v94\n"
"v_mfma_f32_16x16x32_bf16 v[12:15], a[4:7], v[192:195], 0\n"
"ds_read_b128 v[212:215], v242 offset:6144\n"
"v_mul_f32 v95, s55, v95\n"
"v_mfma_f32_16x16x32_bf16 v[20:23], a[8:11], v[192:195], 0\n"
"s_add_u32 m0, s37, 20480\n"
"buffer_store_dwordx4 v[48:51], v250, s[8:11], s49 offen offset:0 sc1\n"
"v_mfma_f32_16x16x32_bf16 v[28:31], a[12:15], v[192:195], 0\n"
"s_add_u32 s48, s98, 98304\n"
"v_cvt_pk_bf16_f32 v88, v88, v89\n"
"v_cvt_pk_bf16_f32 v89, v90, v91\n"
"v_mfma_f32_16x16x32_bf16 v[36:39], a[0:3], v[196:199], 0\n"
"buffer_load_dwordx4 v234, s[12:15], s48 offen offset:0 lds\n"
"v_mfma_f32_16x16x32_bf16 v[44:47], a[4:7], v[196:199], 0\n"
"ds_read_b128 v[216:219], v243 offset:4096\n"
"v_cvt_pk_bf16_f32 v90, v92, v93\n"
"v_mfma_f32_16x16x32_bf16 v[52:55], a[8:11], v[196:199], 0\n"
"v_cvt_pk_bf16_f32 v91, v94, v95\n"
"v_mul_f32 v96, s55, v96\n"
"v_mfma_f32_16x16x32_bf16 v[60:63], a[12:15], v[196:199], 0\n"
"v_mul_f32 v97, s55, v97\n"
"v_mul_f32 v98, s55, v98\n"
"v_mfma_f32_16x16x32_bf16 v[4:7], a[16:19], v[200:203], v[4:7]\n"
"v_mul_f32 v99, s55, v99\n"
"v_mul_f32 v100, s55, v100\n"
"v_mfma_f32_16x16x32_bf16 v[12:15], a[20:23], v[200:203], v[12:15]\n"
"ds_read_b128 v[220:223], v243 offset:6144\n"
"v_mul_f32 v101, s55, v101\n"
"v_mfma_f32_16x16x32_bf16 v[20:23], a[24:27], v[200:203], v[20:23]\n"
"buffer_load_dwordx4 v235, s[12:15], s48 offen offset:1024 lds\n"
"v_mfma_f32_16x16x32_bf16 v[28:31], a[28:31], v[200:203], v[28:31]\n"
"s_waitcnt lgkmcnt(2)\n"
"v_mul_f32 v102, s55, v102\n"
"v_mul_f32 v103, s55, v103\n"
"v_mfma_f32_16x16x32_bf16 v[36:39], a[16:19], v[204:207], v[36:39]\n"
"ds_read_b128 v[192:195], v242 offset:24576\n"
"v_cvt_pk_bf16_f32 v96, v96, v97\n"
"v_mfma_f32_16x16x32_bf16 v[44:47], a[20:23], v[204:207], v[44:47]\n"
"v_cvt_pk_bf16_f32 v97, v98, v99\n"
"v_cvt_pk_bf16_f32 v98, v100, v101\n"
"v_mfma_f32_16x16x32_bf16 v[52:55], a[24:27], v[204:207], v[52:55]\n"
"buffer_load_dwordx4 v236, s[12:15], s48 offen offset:2048 lds\n"
"v_mfma_f32_16x16x32_bf16 v[60:63], a[28:31], v[204:207], v[60:63]\n"
"v_cvt_pk_bf16_f32 v99, v102, v103\n"
"v_mfma_f32_16x16x32_bf16 v[4:7], a[32:35], v[208:211], v[4:7]\n"
"ds_read_b128 v[196:199], v242 offset:26624\n"
"v_mfma_f32_16x16x32_bf16 v[12:15], a[36:39], v[208:211], v[12:15]\n"
"buffer_store_dwordx4 v[56:59], v250, s[8:11], s49 offen offset:1024 sc1\n"
"v_mfma_f32_16x16x32_bf16 v[20:23], a[40:43], v[208:211], v[20:23]\n"
"v_mul_f32 v104, s55, v104\n"
"v_mul_f32 v105, s55, v105\n"
"v_mfma_f32_16x16x32_bf16 v[28:31], a[44:47], v[208:211], v[28:31]\n"
"buffer_load_dwordx4 v237, s[12:15], s48 offen offset:3072 lds\n"
"v_mfma_f32_16x16x32_bf16 v[36:39], a[32:35], v[212:215], v[36:39]\n"
"ds_read_b128 v[200:203], v243 offset:24576\n"
"v_mul_f32 v106, s55, v106\n"
"v_mfma_f32_16x16x32_bf16 v[44:47], a[36:39], v[212:215], v[44:47]\n"
"v_mul_f32 v107, s55, v107\n"
"v_mul_f32 v108, s55, v108\n"
"v_mfma_f32_16x16x32_bf16 v[52:55], a[40:43], v[212:215], v[52:55]\n"
"s_lshl_b32 s88, s31, 1\n"
"v_mul_f32 v109, s55, v109\n"
"v_mul_f32 v110, s55, v110\n"
"v_mfma_f32_16x16x32_bf16 v[60:63], a[44:47], v[212:215], v[60:63]\n"
"s_waitcnt lgkmcnt(1)\n"
"s_waitcnt vmcnt(22)\n"
"ds_read_b64_tr_b16 v[160:161], v254 offset:45056\n"
"v_mfma_f32_16x16x32_bf16 v[4:7], a[48:51], v[216:219], v[4:7]\n"
"s_add_u32 s88, s88, s30\n"
"ds_read_b128 v[204:207], v243 offset:26624\n"
"v_mul_f32 v111, s55, v111\n"
"v_mfma_f32_16x16x32_bf16 v[12:15], a[52:55], v[216:219], v[12:15]\n"
"s_add_u32 m0, s92, 5120\n"
"ds_read_b64_tr_b16 v[162:163], v254 offset:47616\n"
"v_cvt_pk_bf16_f32 v104, v104, v105\n"
"v_mfma_f32_16x16x32_bf16 v[20:23], a[56:59], v[216:219], v[20:23]\n"
"s_add_u32 s48, s61, 552960\n"
"ds_read_b64_tr_b16 v[164:165], v254 offset:45088\n"
"v_cvt_pk_bf16_f32 v105, v106, v107\n"
"v_mfma_f32_16x16x32_bf16 v[28:31], a[60:63], v[216:219], v[28:31]\n"
"s_lshr_b32 s89, s88, 3\n"
"buffer_load_dwordx4 v251, s[24:27], s48 offen lds\n"
"v_mfma_f32_16x16x32_bf16 v[36:39], a[48:51], v[220:223], v[36:39]\n"
"s_and_b32 s88, s88, 7\n"
"ds_read_b64_tr_b16 v[166:167], v254 offset:47648\n"
"v_cvt_pk_bf16_f32 v106, v108, v109\n"
"v_mfma_f32_16x16x32_bf16 v[44:47], a[52:55], v[220:223], v[44:47]\n"
"s_mul_i32 s88, s88, 270\n"
"ds_read_b128 v[208:211], v242 offset:28672\n"
"v_cvt_pk_bf16_f32 v107, v110, v111\n"
"v_mfma_f32_16x16x32_bf16 v[52:55], a[56:59], v[220:223], v[52:55]\n"
"s_add_u32 s88, s88, s89\n"
"ds_read_b64_tr_b16 v[168:169], v254 offset:45120\n"
"v_mul_f32 v112, s55, v112\n"
"v_mfma_f32_16x16x32_bf16 v[60:63], a[60:63], v[220:223], v[60:63]\n"
"s_lshl_b32 s88, s88, 10\n"
"ds_read_b64_tr_b16 v[170:171], v254 offset:47680\n"
"v_mul_f32 v113, s55, v113\n"
"v_mfma_f32_16x16x32_bf16 v[4:7], a[64:67], v[192:195], v[4:7]\n"
"s_add_u32 s88, s88, 2744320\n"
"ds_read_b64_tr_b16 v[172:173], v254 offset:45152\n"
"v_mul_f32 v114, s55, v114\n"
"v_mfma_f32_16x16x32_bf16 v[12:15], a[68:71], v[192:195], v[12:15]\n"
"s_cmp_lt_u32 s31, 216\n"
"ds_read_b64_tr_b16 v[174:175], v254 offset:47712\n"
"v_mul_f32 v115, s55, v115\n"
"v_mfma_f32_16x16x32_bf16 v[20:23], a[72:75], v[192:195], v[20:23]\n"
"s_cselect_b32 s90, s88, 2130706432\n"
"ds_read_b128 v[212:215], v242 offset:30720\n"
"v_mul_f32 v116, s55, v116\n"
"v_mfma_f32_16x16x32_bf16 v[28:31], a[76:79], v[192:195], v[28:31]\n"
"ds_read_b64_tr_b16 v[176:177], v254 offset:45184\n"
"v_mul_f32 v117, s55, v117\n"
"v_mfma_f32_16x16x32_bf16 v[36:39], a[64:67], v[196:199], v[36:39]\n"
"ds_read_b64_tr_b16 v[178:179], v254 offset:47744\n"
"v_mul_f32 v118, s55, v118\n"
"v_mfma_f32_16x16x32_bf16 v[44:47], a[68:71], v[196:199], v[44:47]\n"
"s_waitcnt lgkmcnt(10)\n"
"buffer_store_dwordx4 v[160:163], v224, s[8:11], s90 offen sc1\n"
"v_mfma_f32_16x16x32_bf16 v[52:55], a[72:75], v[196:199], v[52:55]\n"
"s_add_u32 m0, s92, 6144\n"
"ds_read_b128 v[216:219], v243 offset:28672\n"
"v_mul_f32 v119, s55, v119\n"
"v_mfma_f32_16x16x32_bf16 v[60:63], a[76:79], v[196:199], v[60:63]\n"
"s_add_u32 s48, s61, 663552\n"
"ds_read_b128 v[220:223], v243 offset:30720\n"
"v_cvt_pk_bf16_f32 v112, v112, v113\n"
"v_mfma_f32_16x16x32_bf16 v[4:7], a[80:83], v[200:203], v[4:7]\n"
"buffer_load_dwordx4 v251, s[24:27], s48 offen lds\n"
"v_mfma_f32_16x16x32_bf16 v[12:15], a[84:87], v[200:203], v[12:15]\n"
"v_cvt_pk_bf16_f32 v113, v114, v115\n"
"v_cvt_pk_bf16_f32 v114, v116, v117\n"
"v_mfma_f32_16x16x32_bf16 v[20:23], a[88:91], v[200:203], v[20:23]\n"
"ds_read_b128 v[192:195], v244 offset:0\n"
"v_cvt_pk_bf16_f32 v115, v118, v119\n"
"v_mfma_f32_16x16x32_bf16 v[28:31], a[92:95], v[200:203], v[28:31]\n"
"s_add_u32 s90, s90, 55296\n"
"v_mul_f32 v120, s55, v120\n"
"v_mul_f32 v121, s55, v121\n"
"v_mfma_f32_16x16x32_bf16 v[36:39], a[80:83], v[204:207], v[36:39]\n"
"v_mul_f32 v122, s55, v122\n"
"v_mul_f32 v123, s55, v123\n"
"v_mfma_f32_16x16x32_bf16 v[44:47], a[84:87], v[204:207], v[44:47]\n"
"s_waitcnt lgkmcnt(11)\n"
"buffer_store_dwordx4 v[164:167], v224, s[8:11], s90 offen sc1\n"
"v_mfma_f32_16x16x32_bf16 v[52:55], a[88:91], v[204:207], v[52:55]\n"
"ds_read_b128 v[196:199], v244 offset:2048\n"
"v_mul_f32 v124, s55, v124\n"
"v_mfma_f32_16x16x32_bf16 v[60:63], a[92:95], v[204:207], v[60:63]\n"
"s_waitcnt lgkmcnt(2)\n"
"v_mul_f32 v125, s55, v125\n"
"v_mul_f32 v126, s55, v126\n"
"v_mfma_f32_16x16x32_bf16 v[4:7], a[96:99], v[208:211], v[4:7]\n"
"v_mul_f32 v127, s55, v127\n"
"v_cvt_pk_bf16_f32 v120, v120, v121\n"
"v_mfma_f32_16x16x32_bf16 v[12:15], a[100:103], v[208:211], v[12:15]\n"
"v_cvt_pk_bf16_f32 v121, v122, v123\n"
"v_cvt_pk_bf16_f32 v122, v124, v125\n"
"v_mfma_f32_16x16x32_bf16 v[20:23], a[104:107], v[208:211], v[20:23]\n"
"ds_read_b128 v[200:203], v245 offset:0\n"
"v_cvt_pk_bf16_f32 v123, v126, v127\n"
"v_mfma_f32_16x16x32_bf16 v[28:31], a[108:111], v[208:211], v[28:31]\n"
"s_add_u32 m0, s92, 7168\n"
"v_mul_f32 v128, s55, v128\n"
"v_mul_f32 v129, s55, v129\n"
"v_mfma_f32_16x16x32_bf16 v[36:39], a[96:99], v[212:215], v[36:39]\n"
"s_add_u32 s48, s61, 774144\n"
"v_mul_f32 v130, s55, v130\n"
"v_mul_f32 v131, s55, v131\n"
"v_mfma_f32_16x16x32_bf16 v[44:47], a[100:103], v[212:215], v[44:47]\n"
"buffer_load_dwordx4 v251, s[24:27], s48 offen lds\n"
"v_mfma_f32_16x16x32_bf16 v[52:55], a[104:107], v[212:215], v[52:55]\n"
"ds_read_b128 v[204:207], v245 offset:2048\n"
"v_mul_f32 v132, s55, v132\n"
"v_mfma_f32_16x16x32_bf16 v[60:63], a[108:111], v[212:215], v[60:63]\n"
"s_add_u32 s90, s90, 55296\n"
"v_mul_f32 v133, s55, v133\n"
"v_mul_f32 v134, s55, v134\n"
"v_mfma_f32_16x16x32_bf16 v[4:7], a[112:115], v[216:219], v[4:7]\n"
"v_mul_f32 v135, s55, v135\n"
"v_cvt_pk_bf16_f32 v128, v128, v129\n"
"v_mfma_f32_16x16x32_bf16 v[12:15], a[116:119], v[216:219], v[12:15]\n"
"ds_read_b128 v[208:211], v244 offset:4096\n"
"v_cvt_pk_bf16_f32 v129, v130, v131\n"
"v_mfma_f32_16x16x32_bf16 v[20:23], a[120:123], v[216:219], v[20:23]\n"
"buffer_store_dwordx4 v[168:171], v224, s[8:11], s90 offen sc1\n"
"v_mfma_f32_16x16x32_bf16 v[28:31], a[124:127], v[216:219], v[28:31]\n"
"v_cvt_pk_bf16_f32 v130, v132, v133\n"
"v_cvt_pk_bf16_f32 v131, v134, v135\n"
"v_mfma_f32_16x16x32_bf16 v[36:39], a[112:115], v[220:223], v[36:39]\n"
"v_mfma_f32_16x16x32_bf16 v[44:47], a[116:119], v[220:223], v[44:47]\n"
"ds_read_b128 v[212:215], v244 offset:6144\n"
"v_mul_f32 v136, s55, v136\n"
"v_mfma_f32_16x16x32_bf16 v[52:55], a[120:123], v[220:223], v[52:55]\n"
"v_mul_f32 v137, s55, v137\n"
"v_mul_f32 v138, s55, v138\n"
"v_mfma_f32_16x16x32_bf16 v[60:63], a[124:127], v[220:223], v[60:63]\n"
"s_waitcnt lgkmcnt(2)\n"
"v_mul_f32 v139, s55, v139\n"
"v_mul_f32 v140, s55, v140\n"
"v_mfma_f32_16x16x32_bf16 v[4:7], a[128:131], v[192:195], v[4:7]\n"
"v_mul_f32 v141, s55, v141\n"
"v_mul_f32 v142, s55, v142\n"
"v_mfma_f32_16x16x32_bf16 v[12:15], a[132:135], v[192:195], v[12:15]\n"
"s_add_u32 m0, s92, 8192\n"
"ds_read_b128 v[216:219], v245 offset:4096\n"
"v_mul_f32 v143, s55, v143\n"
"v_mfma_f32_16x16x32_bf16 v[20:23], a[136:139], v[192:195], v[20:23]\n"
"s_add_u32 s48, s61, 884736\n"
"v_cvt_pk_bf16_f32 v136, v136, v137\n"
"v_cvt_pk_bf16_f32 v137, v138, v139\n"
"v_mfma_f32_16x16x32_bf16 v[28:31], a[140:143], v[192:195], v[28:31]\n"
"s_add_u32 s90, s90, 55296\n"
"buffer_load_dwordx4 v251, s[24:27], s48 offen lds\n"
"v_mfma_f32_16x16x32_bf16 v[36:39], a[128:131], v[196:199], v[36:39]\n"
"v_cvt_pk_bf16_f32 v138, v140, v141\n"
"v_cvt_pk_bf16_f32 v139, v142, v143\n"
"v_mfma_f32_16x16x32_bf16 v[44:47], a[132:135], v[196:199], v[44:47]\n"
"ds_read_b128 v[220:223], v245 offset:6144\n"
"v_mfma_f32_16x16x32_bf16 v[52:55], a[136:139], v[196:199], v[52:55]\n"
"buffer_store_dwordx4 v[172:175], v224, s[8:11], s90 offen sc1\n"
"v_mfma_f32_16x16x32_bf16 v[60:63], a[140:143], v[196:199], v[60:63]\n"
"v_mfma_f32_16x16x32_bf16 v[4:7], a[144:147], v[200:203], v[4:7]\n"
"ds_read_b128 v[192:195], v244 offset:24576\n"
"v_mul_f32 v144, s55, v144\n"
"v_mfma_f32_16x16x32_bf16 v[12:15], a[148:151], v[200:203], v[12:15]\n"
"v_mul_f32 v145, s55, v145\n"
"v_mul_f32 v146, s55, v146\n"
"v_mfma_f32_16x16x32_bf16 v[20:23], a[152:155], v[200:203], v[20:23]\n"
"v_mul_f32 v147, s55, v147\n"
"v_mul_f32 v148, s55, v148\n"
"v_mfma_f32_16x16x32_bf16 v[28:31], a[156:159], v[200:203], v[28:31]\n"
"v_mul_f32 v149, s55, v149\n"
"v_mul_f32 v150, s55, v150\n"
"v_mfma_f32_16x16x32_bf16 v[36:39], a[144:147], v[204:207], v[36:39]\n"
"ds_read_b128 v[196:199], v244 offset:26624\n"
"v_mul_f32 v151, s55, v151\n"
"v_mfma_f32_16x16x32_bf16 v[44:47], a[148:151], v[204:207], v[44:47]\n"
"v_cvt_pk_bf16_f32 v144, v144, v145\n"
"v_cvt_pk_bf16_f32 v145, v146, v147\n"
"v_mfma_f32_16x16x32_bf16 v[52:55], a[152:155], v[204:207], v[52:55]\n"
"v_cvt_pk_bf16_f32 v146, v148, v149\n"
"v_cvt_pk_bf16_f32 v147, v150, v151\n"
"v_mfma_f32_16x16x32_bf16 v[60:63], a[156:159], v[204:207], v[60:63]\n"
"s_waitcnt lgkmcnt(1)\n"
"s_add_u32 s90, s90, 55296\n"
"s_add_u32 m0, s92, 9216\n"
"v_mfma_f32_16x16x32_bf16 v[4:7], a[160:163], v[208:211], v[4:7]\n"
"s_add_u32 s48, s61, 995328\n"
"ds_read_b128 v[200:203], v245 offset:24576\n"
"v_mfma_f32_16x16x32_bf16 v[12:15], a[164:167], v[208:211], v[12:15]\n"
"buffer_store_dwordx4 v[176:179], v224, s[8:11], s90 offen sc1\n"
"v_mfma_f32_16x16x32_bf16 v[20:23], a[168:171], v[208:211], v[20:23]\n"
"buffer_load_dwordx4 v251, s[24:27], s48 offen lds\n"
"v_mfma_f32_16x16x32_bf16 v[28:31], a[172:175], v[208:211], v[28:31]\n"
"v_mul_f32 v152, s55, v152\n"
"v_mul_f32 v153, s55, v153\n"
"v_mfma_f32_16x16x32_bf16 v[36:39], a[160:163], v[212:215], v[36:39]\n"
"ds_read_b128 v[204:207], v245 offset:26624\n"
"v_mul_f32 v154, s55, v154\n"
"v_mfma_f32_16x16x32_bf16 v[44:47], a[164:167], v[212:215], v[44:47]\n"
"v_mul_f32 v155, s55, v155\n"
"v_mul_f32 v156, s55, v156\n"
"v_mfma_f32_16x16x32_bf16 v[52:55], a[168:171], v[212:215], v[52:55]\n"
"v_mul_f32 v157, s55, v157\n"
"v_mul_f32 v158, s55, v158\n"
"v_mfma_f32_16x16x32_bf16 v[60:63], a[172:175], v[212:215], v[60:63]\n"
"ds_read_b128 v[208:211], v244 offset:28672\n"
"v_mul_f32 v159, s55, v159\n"
"v_mfma_f32_16x16x32_bf16 v[4:7], a[176:179], v[216:219], v[4:7]\n"
"v_cvt_pk_bf16_f32 v152, v152, v153\n"
"v_cvt_pk_bf16_f32 v153, v154, v155\n"
"v_mfma_f32_16x16x32_bf16 v[12:15], a[180:183], v[216:219], v[12:15]\n"
"v_cvt_pk_bf16_f32 v154, v156, v157\n"
"v_cvt_pk_bf16_f32 v155, v158, v159\n"
"v_mfma_f32_16x16x32_bf16 v[20:23], a[184:187], v[216:219], v[20:23]\n"
"v_mfma_f32_16x16x32_bf16 v[28:31], a[188:191], v[216:219], v[28:31]\n"
"s_mov_b32 s49, 16384\n"
"ds_read_b128 v[212:215], v244 offset:30720\n"
"v_mfma_f32_16x16x32_bf16 v[36:39], a[176:179], v[220:223], v[36:39]\n"
"v_mfma_f32_16x16x32_bf16 v[44:47], a[180:183], v[220:223], v[44:47]\n"
"buffer_store_dwordx4 v[64:67], v250, s[8:11], s49 offen offset:0 sc1\n"
"v_mfma_f32_16x16x32_bf16 v[52:55], a[184:187], v[220:223], v[52:55]\n"
"v_mfma_f32_16x16x32_bf16 v[60:63], a[188:191], v[220:223], v[60:63]\n"
"ds_read_b128 v[216:219], v245 offset:28672\n"
"v_mfma_f32_16x16x32_bf16 v[4:7], a[192:195], v[192:195], v[4:7]\n"
"v_mfma_f32_16x16x32_bf16 v[12:15], a[196:199], v[192:195], v[12:15]\n"
"s_sub_u32 s99, s34, 5\n"
"v_mfma_f32_16x16x32_bf16 v[20:23], a[200:203], v[192:195], v[20:23]\n"
"s_cmp_lt_u32 s34, 5\n"
"s_cselect_b32 s99, s34, s99\n"
"v_mfma_f32_16x16x32_bf16 v[28:31], a[204:207], v[192:195], v[28:31]\n"
"s_waitcnt lgkmcnt(1)\n"
"ds_read_b128 v[220:223], v245 offset:30720\n"
"s_mul_i32 s99, s99, 1\n"
"v_mfma_f32_16x16x32_bf16 v[36:39], a[192:195], v[196:199], v[36:39]\n"
"s_lshl_b32 s98, s99, 20\n"
"v_mfma_f32_16x16x32_bf16 v[44:47], a[196:199], v[196:199], v[44:47]\n"
"v_mfma_f32_16x16x32_bf16 v[52:55], a[200:203], v[196:199], v[52:55]\n"
"v_mfma_f32_16x16x32_bf16 v[60:63], a[204:207], v[196:199], v[60:63]\n"
"s_lshr_b32 s50, s30, 1\n"
"v_mfma_f32_16x16x32_bf16 v[4:7], a[208:211], v[200:203], v[4:7]\n"
"s_and_b32 s51, s30, 1\n"
"v_mfma_f32_16x16x32_bf16 v[12:15], a[212:215], v[200:203], v[12:15]\n"
"s_mul_i32 s38, s50, 24576\n"
"v_mfma_f32_16x16x32_bf16 v[20:23], a[216:219], v[200:203], v[20:23]\n"
"s_lshl_b32 s39, s50, 18\n"
"v_mfma_f32_16x16x32_bf16 v[28:31], a[220:223], v[200:203], v[28:31]\n"
"s_lshl_b32 s50, s51, 12\n"
"v_mfma_f32_16x16x32_bf16 v[36:39], a[208:211], v[204:207], v[36:39]\n"
"s_add_u32 s38, s38, s50\n"
"v_mfma_f32_16x16x32_bf16 v[44:47], a[212:215], v[204:207], v[44:47]\n"
"s_lshl_b32 s50, s51, 7\n"
"s_waitcnt lgkmcnt(0)\n"
"s_barrier\n"
"v_mfma_f32_16x16x32_bf16 v[52:55], a[216:219], v[204:207], v[52:55]\n"
"s_add_u32 s39, s39, s50\n"
"buffer_store_dwordx4 v[72:75], v250, s[8:11], s49 offen offset:1024 sc1\n"
"v_mfma_f32_16x16x32_bf16 v[60:63], a[220:223], v[204:207], v[60:63]\n"
"ds_read_b128 v[192:195], v242 offset:8192\n"
"v_mfma_f32_16x16x32_bf16 v[4:7], a[224:227], v[208:211], v[4:7]\n"
"v_mfma_f32_16x16x32_bf16 v[12:15], a[228:231], v[208:211], v[12:15]\n"
"s_add_u32 m0, s38, 65536\n"
"s_add_u32 s48, s39, s98\n"
"v_mfma_f32_16x16x32_bf16 v[20:23], a[232:235], v[208:211], v[20:23]\n"
"ds_read_b128 v[196:199], v242 offset:10240\n"
"v_mfma_f32_16x16x32_bf16 v[28:31], a[236:239], v[208:211], v[28:31]\n"
"buffer_load_dwordx4 v238, s[20:23], s48 offen offset:0 lds\n"
"v_mfma_f32_16x16x32_bf16 v[36:39], a[224:227], v[212:215], v[36:39]\n"
"v_mfma_f32_16x16x32_bf16 v[44:47], a[228:231], v[212:215], v[44:47]\n"
"v_mfma_f32_16x16x32_bf16 v[52:55], a[232:235], v[212:215], v[52:55]\n"
"ds_read_b128 v[200:203], v243 offset:8192\n"
"v_mfma_f32_16x16x32_bf16 v[60:63], a[236:239], v[212:215], v[60:63]\n"
"v_mfma_f32_16x16x32_bf16 v[4:7], a[240:243], v[216:219], v[4:7]\n"
"v_mfma_f32_16x16x32_bf16 v[12:15], a[244:247], v[216:219], v[12:15]\n"
"v_mfma_f32_16x16x32_bf16 v[20:23], a[248:251], v[216:219], v[20:23]\n"
"ds_read_b128 v[204:207], v243 offset:10240\n"
"v_mfma_f32_16x16x32_bf16 v[28:31], a[252:255], v[216:219], v[28:31]\n"
"s_mov_b32 s49, 20480\n"
"buffer_load_dwordx4 v239, s[20:23], s48 offen offset:1024 lds\n"
"v_mfma_f32_16x16x32_bf16 v[36:39], a[240:243], v[220:223], v[36:39]\n"
"v_mfma_f32_16x16x32_bf16 v[44:47], a[244:247], v[220:223], v[44:47]\n"
"ds_read_b128 v[208:211], v242 offset:12288\n"
"v_mfma_f32_16x16x32_bf16 v[52:55], a[248:251], v[220:223], v[52:55]\n"
"buffer_store_dwordx4 v[80:83], v250, s[8:11], s49 offen offset:0 sc1\n"
"v_mfma_f32_16x16x32_bf16 v[60:63], a[252:255], v[220:223], v[60:63]\n"
"s_waitcnt lgkmcnt(2)\n"
"v_mfma_f32_16x16x32_bf16 v[84:87], a[0:3], v[192:195], 0\n"
"v_mfma_f32_16x16x32_bf16 v[92:95], a[4:7], v[192:195], 0\n"
"ds_read_b128 v[212:215], v242 offset:14336\n"
"v_mfma_f32_16x16x32_bf16 v[100:103], a[8:11], v[192:195], 0\n"
"buffer_load_dwordx4 v240, s[20:23], s48 offen offset:2048 lds\n"
"v_mfma_f32_16x16x32_bf16 v[108:111], a[12:15], v[192:195], 0\n"
"v_mul_f32 v4, s55, v4\n"
"v_mul_f32 v5, s55, v5\n"
"v_mfma_f32_16x16x32_bf16 v[116:119], a[0:3], v[196:199], 0\n"
"v_mul_f32 v6, s55, v6\n"
"v_mul_f32 v7, s55, v7\n"
"v_mfma_f32_16x16x32_bf16 v[124:127], a[4:7], v[196:199], 0\n"
"ds_read_b128 v[216:219], v243 offset:12288\n"
"v_mul_f32 v12, s55, v12\n"
"v_mfma_f32_16x16x32_bf16 v[132:135], a[8:11], v[196:199], 0\n"
"v_mul_f32 v13, s55, v13\n"
"v_mul_f32 v14, s55, v14\n"
"v_mfma_f32_16x16x32_bf16 v[140:143], a[12:15], v[196:199], 0\n"
"s_add_u32 s98, s98, 524288\n"
"v_mul_f32 v15, s55, v15\n"
"v_cvt_pk_bf16_f32 v4, v4, v5\n"
"v_mfma_f32_16x16x32_bf16 v[84:87], a[16:19], v[200:203], v[84:87]\n"
"buffer_load_dwordx4 v241, s[20:23], s48 offen offset:3072 lds\n"
"v_mfma_f32_16x16x32_bf16 v[92:95], a[20:23], v[200:203], v[92:95]\n"
"ds_read_b128 v[220:223], v243 offset:14336\n"
"v_cvt_pk_bf16_f32 v5, v6, v7\n"
"v_mfma_f32_16x16x32_bf16 v[100:103], a[24:27], v[200:203], v[100:103]\n"
"v_cvt_pk_bf16_f32 v6, v12, v13\n"
"v_cvt_pk_bf16_f32 v7, v14, v15\n"
"v_mfma_f32_16x16x32_bf16 v[108:111], a[28:31], v[200:203], v[108:111]\n"
"s_waitcnt lgkmcnt(2)\n"
"v_mfma_f32_16x16x32_bf16 v[116:119], a[16:19], v[204:207], v[116:119]\n"
"ds_read_b128 v[192:195], v242 offset:32768\n"
"v_mfma_f32_16x16x32_bf16 v[124:127], a[20:23], v[204:207], v[124:127]\n"
"s_add_u32 m0, s38, 114688\n"
"buffer_store_dwordx4 v[88:91], v250, s[8:11], s49 offen offset:1024 sc1\n"
"v_mfma_f32_16x16x32_bf16 v[132:135], a[24:27], v[204:207], v[132:135]\n"
"s_add_u32 s48, s39, s98\n"
"v_mul_f32 v20, s55, v20\n"
"v_mul_f32 v21, s55, v21\n"
"v_mfma_f32_16x16x32_bf16 v[140:143], a[28:31], v[204:207], v[140:143]\n"
"buffer_load_dwordx4 v238, s[20:23], s48 offen offset:0 lds\n"
"v_mfma_f32_16x16x32_bf16 v[84:87], a[32:35], v[208:211], v[84:87]\n"
"ds_read_b128 v[196:199], v242 offset:34816\n"
"v_mul_f32 v22, s55, v22\n"
"v_mfma_f32_16x16x32_bf16 v[92:95], a[36:39], v[208:211], v[92:95]\n"
"v_mul_f32 v23, s55, v23\n"
"v_mul_f32 v28, s55, v28\n"
"v_mfma_f32_16x16x32_bf16 v[100:103], a[40:43], v[208:211], v[100:103]\n"
"v_mul_f32 v29, s55, v29\n"
"v_mul_f32 v30, s55, v30\n"
"v_mfma_f32_16x16x32_bf16 v[108:111], a[44:47], v[208:211], v[108:111]\n"
"v_mul_f32 v31, s55, v31\n"
"v_cvt_pk_bf16_f32 v20, v20, v21\n"
"v_mfma_f32_16x16x32_bf16 v[116:119], a[32:35], v[212:215], v[116:119]\n"
"ds_read_b128 v[200:203], v243 offset:32768\n"
"v_cvt_pk_bf16_f32 v21, v22, v23\n"
"v_mfma_f32_16x16x32_bf16 v[124:127], a[36:39], v[212:215], v[124:127]\n"
"v_cvt_pk_bf16_f32 v22, v28, v29\n"
"v_cvt_pk_bf16_f32 v23, v30, v31\n"
"v_mfma_f32_16x16x32_bf16 v[132:135], a[40:43], v[212:215], v[132:135]\n"
"buffer_load_dwordx4 v239, s[20:23], s48 offen offset:1024 lds\n"
"v_mfma_f32_16x16x32_bf16 v[140:143], a[44:47], v[212:215], v[140:143]\n"
"s_waitcnt lgkmcnt(1)\n"
"v_mfma_f32_16x16x32_bf16 v[84:87], a[48:51], v[216:219], v[84:87]\n"
"ds_read_b128 v[204:207], v243 offset:34816\n"
"v_mfma_f32_16x16x32_bf16 v[92:95], a[52:55], v[216:219], v[92:95]\n"
"v_mul_f32 v36, s55, v36\n"
"v_mul_f32 v37, s55, v37\n"
"v_mfma_f32_16x16x32_bf16 v[100:103], a[56:59], v[216:219], v[100:103]\n"
"v_mul_f32 v38, s55, v38\n"
"v_mul_f32 v39, s55, v39\n"
"v_mfma_f32_16x16x32_bf16 v[108:111], a[60:63], v[216:219], v[108:111]\n"
"s_mov_b32 s49, 24576\n"
"ds_read_b128 v[208:211], v242 offset:36864\n"
"v_mul_f32 v44, s55, v44\n"
"v_mfma_f32_16x16x32_bf16 v[116:119], a[48:51], v[220:223], v[116:119]\n"
"v_mul_f32 v45, s55, v45\n"
"v_mul_f32 v46, s55, v46\n"
"v_mfma_f32_16x16x32_bf16 v[124:127], a[52:55], v[220:223], v[124:127]\n"
"buffer_store_dwordx4 v[96:99], v250, s[8:11], s49 offen offset:0 sc1\n"
"v_mfma_f32_16x16x32_bf16 v[132:135], a[56:59], v[220:223], v[132:135]\n"
"buffer_load_dwordx4 v240, s[20:23], s48 offen offset:2048 lds\n"
"v_mfma_f32_16x16x32_bf16 v[140:143], a[60:63], v[220:223], v[140:143]\n"
"ds_read_b128 v[212:215], v242 offset:38912\n"
"v_mul_f32 v47, s55, v47\n"
"v_mfma_f32_16x16x32_bf16 v[84:87], a[64:67], v[192:195], v[84:87]\n"
"v_cvt_pk_bf16_f32 v36, v36, v37\n"
"v_cvt_pk_bf16_f32 v37, v38, v39\n"
"v_mfma_f32_16x16x32_bf16 v[92:95], a[68:71], v[192:195], v[92:95]\n"
"v_cvt_pk_bf16_f32 v38, v44, v45\n"
"v_cvt_pk_bf16_f32 v39, v46, v47\n"
"v_mfma_f32_16x16x32_bf16 v[100:103], a[72:75], v[192:195], v[100:103]\n"
"v_mfma_f32_16x16x32_bf16 v[108:111], a[76:79], v[192:195], v[108:111]\n"
"s_add_u32 s98, s98, 524288\n"
"ds_read_b128 v[216:219], v243 offset:36864\n"
"s_lshl_b32 s50, s34, 3\n"
"v_mfma_f32_16x16x32_bf16 v[116:119], a[64:67], v[196:199], v[116:119]\n"
"s_cmp_eq_u32 s98, 5242880\n"
"buffer_load_dwordx4 v241, s[20:23], s48 offen offset:3072 lds\n"
"v_mfma_f32_16x16x32_bf16 v[124:127], a[68:71], v[196:199], v[124:127]\n"
"s_cselect_b32 s98, 0, s98\n"
"v_mul_f32 v52, s55, v52\n"
"v_mul_f32 v53, s55, v53\n"
"v_mfma_f32_16x16x32_bf16 v[132:135], a[72:75], v[196:199], v[132:135]\n"
"s_add_u32 s51, s50, s30\n"
"v_mul_f32 v54, s55, v54\n"
"v_mul_f32 v55, s55, v55\n"
"v_mfma_f32_16x16x32_bf16 v[140:143], a[76:79], v[196:199], v[140:143]\n"
"s_waitcnt lgkmcnt(1)\n"
"ds_read_b128 v[220:223], v243 offset:38912\n"
"v_mul_f32 v60, s55, v60\n"
"v_mfma_f32_16x16x32_bf16 v[84:87], a[80:83], v[200:203], v[84:87]\n"
"s_mul_i32 s51, s51, 20480\n"
"v_mul_f32 v61, s55, v61\n"
"v_mul_f32 v62, s55, v62\n"
"v_mfma_f32_16x16x32_bf16 v[92:95], a[84:87], v[200:203], v[92:95]\n"
"s_add_u32 s56, s51, 65536\n"
"v_mul_f32 v63, s55, v63\n"
"v_cvt_pk_bf16_f32 v52, v52, v53\n"
"v_mfma_f32_16x16x32_bf16 v[100:103], a[88:91], v[200:203], v[100:103]\n"
"s_add_u32 s57, s56, 81920\n"
"ds_read_b128 v[192:195], v244 offset:8192\n"
"v_cvt_pk_bf16_f32 v53, v54, v55\n"
"v_mfma_f32_16x16x32_bf16 v[108:111], a[92:95], v[200:203], v[108:111]\n"
"s_sub_u32 s51, s30, s50\n"
"v_cvt_pk_bf16_f32 v54, v60, v61\n"
"v_cvt_pk_bf16_f32 v55, v62, v63\n"
"v_mfma_f32_16x16x32_bf16 v[116:119], a[80:83], v[204:207], v[116:119]\n"
"s_add_u32 s51, s51, 120\n"
"s_mul_i32 s51, s51, 20480\n"
"s_add_u32 s58, s51, 65536\n"
"v_mfma_f32_16x16x32_bf16 v[124:127], a[84:87], v[204:207], v[124:127]\n"
"s_lshl_b32 s99, s99, 12\n"
"buffer_store_dwordx4 v[104:107], v250, s[8:11], s49 offen offset:1024 sc1\n"
"v_mfma_f32_16x16x32_bf16 v[132:135], a[88:91], v[204:207], v[132:135]\n"
"s_add_u32 s59, s58, 81920\n"
"ds_read_b128 v[196:199], v244 offset:10240\n"
"v_add_u32 v235, s99, v224\n"
"v_mfma_f32_16x16x32_bf16 v[140:143], a[92:95], v[204:207], v[140:143]\n"
"buffer_load_dwordx4 v[64:67], v235, s[8:11], s56 offen offset:0 sc1\n"
"v_mfma_f32_16x16x32_bf16 v[84:87], a[96:99], v[208:211], v[84:87]\n"
"v_mfma_f32_16x16x32_bf16 v[92:95], a[100:103], v[208:211], v[92:95]\n"
"v_mfma_f32_16x16x32_bf16 v[100:103], a[104:107], v[208:211], v[100:103]\n"
"ds_read_b128 v[200:203], v245 offset:8192\n"
"v_mfma_f32_16x16x32_bf16 v[108:111], a[108:111], v[208:211], v[108:111]\n"
"v_mfma_f32_16x16x32_bf16 v[116:119], a[96:99], v[212:215], v[116:119]\n"
"v_mfma_f32_16x16x32_bf16 v[124:127], a[100:103], v[212:215], v[124:127]\n"
"buffer_load_dwordx4 v[68:71], v235, s[8:11], s57 offen offset:0 sc1\n"
"v_mfma_f32_16x16x32_bf16 v[132:135], a[104:107], v[212:215], v[132:135]\n"
"ds_read_b128 v[204:207], v245 offset:10240\n"
"v_mfma_f32_16x16x32_bf16 v[140:143], a[108:111], v[212:215], v[140:143]\n"
"s_waitcnt lgkmcnt(2)\n"
"v_mfma_f32_16x16x32_bf16 v[84:87], a[112:115], v[216:219], v[84:87]\n"
"v_mfma_f32_16x16x32_bf16 v[92:95], a[116:119], v[216:219], v[92:95]\n"
"ds_read_b128 v[208:211], v244 offset:12288\n"
"v_mfma_f32_16x16x32_bf16 v[100:103], a[120:123], v[216:219], v[100:103]\n"
"v_mfma_f32_16x16x32_bf16 v[108:111], a[124:127], v[216:219], v[108:111]\n"
"s_mov_b32 s49, 28672\n"
"buffer_load_dwordx4 v[72:75], v235, s[8:11], s58 offen offset:0 sc1\n"
"v_mfma_f32_16x16x32_bf16 v[116:119], a[112:115], v[220:223], v[116:119]\n"
"v_mfma_f32_16x16x32_bf16 v[124:127], a[116:119], v[220:223], v[124:127]\n"
"ds_read_b128 v[212:215], v244 offset:14336\n"
"v_mfma_f32_16x16x32_bf16 v[132:135], a[120:123], v[220:223], v[132:135]\n"
"buffer_store_dwordx4 v[112:115], v250, s[8:11], s49 offen offset:0 sc1\n"
"v_mfma_f32_16x16x32_bf16 v[140:143], a[124:127], v[220:223], v[140:143]\n"
"v_mfma_f32_16x16x32_bf16 v[84:87], a[128:131], v[192:195], v[84:87]\n"
"v_mfma_f32_16x16x32_bf16 v[92:95], a[132:135], v[192:195], v[92:95]\n"
"ds_read_b128 v[216:219], v245 offset:12288\n"
"v_mfma_f32_16x16x32_bf16 v[100:103], a[136:139], v[192:195], v[100:103]\n"
"buffer_load_dwordx4 v[76:79], v235, s[8:11], s59 offen offset:0 sc1\n"
"v_mfma_f32_16x16x32_bf16 v[108:111], a[140:143], v[192:195], v[108:111]\n"
"v_mfma_f32_16x16x32_bf16 v[116:119], a[128:131], v[196:199], v[116:119]\n"
"v_mfma_f32_16x16x32_bf16 v[124:127], a[132:135], v[196:199], v[124:127]\n"
"ds_read_b128 v[220:223], v245 offset:14336\n"
"v_mfma_f32_16x16x32_bf16 v[132:135], a[136:139], v[196:199], v[132:135]\n"
"v_mfma_f32_16x16x32_bf16 v[140:143], a[140:143], v[196:199], v[140:143]\n"
"s_waitcnt lgkmcnt(2)\n"
"v_mfma_f32_16x16x32_bf16 v[84:87], a[144:147], v[200:203], v[84:87]\n"
"ds_read_b128 v[192:195], v244 offset:32768\n"
"v_mfma_f32_16x16x32_bf16 v[92:95], a[148:151], v[200:203], v[92:95]\n"
"v_mfma_f32_16x16x32_bf16 v[100:103], a[152:155], v[200:203], v[100:103]\n"
"v_mfma_f32_16x16x32_bf16 v[108:111], a[156:159], v[200:203], v[108:111]\n"
"v_mfma_f32_16x16x32_bf16 v[116:119], a[144:147], v[204:207], v[116:119]\n"
"ds_read_b128 v[196:199], v244 offset:34816\n"
"v_mfma_f32_16x16x32_bf16 v[124:127], a[148:151], v[204:207], v[124:127]\n"
"buffer_store_dwordx4 v[120:123], v250, s[8:11], s49 offen offset:1024 sc1\n"
"v_mfma_f32_16x16x32_bf16 v[132:135], a[152:155], v[204:207], v[132:135]\n"
"v_mfma_f32_16x16x32_bf16 v[140:143], a[156:159], v[204:207], v[140:143]\n"
"v_mfma_f32_16x16x32_bf16 v[84:87], a[160:163], v[208:211], v[84:87]\n"
"ds_read_b128 v[200:203], v245 offset:32768\n"
"v_mfma_f32_16x16x32_bf16 v[92:95], a[164:167], v[208:211], v[92:95]\n"
"v_mfma_f32_16x16x32_bf16 v[100:103], a[168:171], v[208:211], v[100:103]\n"
"v_mfma_f32_16x16x32_bf16 v[108:111], a[172:175], v[208:211], v[108:111]\n"
"v_mfma_f32_16x16x32_bf16 v[116:119], a[160:163], v[212:215], v[116:119]\n"
"ds_read_b128 v[204:207], v245 offset:34816\n"
"v_mfma_f32_16x16x32_bf16 v[124:127], a[164:167], v[212:215], v[124:127]\n"
"v_mfma_f32_16x16x32_bf16 v[132:135], a[168:171], v[212:215], v[132:135]\n"
"v_mfma_f32_16x16x32_bf16 v[140:143], a[172:175], v[212:215], v[140:143]\n"
"s_waitcnt lgkmcnt(1)\n"
"ds_read_b128 v[208:211], v244 offset:36864\n"
"v_mfma_f32_16x16x32_bf16 v[84:87], a[176:179], v[216:219], v[84:87]\n"
"v_mfma_f32_16x16x32_bf16 v[92:95], a[180:183], v[216:219], v[92:95]\n"
"v_mfma_f32_16x16x32_bf16 v[100:103], a[184:187], v[216:219], v[100:103]\n"
"v_mfma_f32_16x16x32_bf16 v[108:111], a[188:191], v[216:219], v[108:111]\n"
"s_mov_b32 s49, 262144\n"
"ds_read_b128 v[212:215], v244 offset:38912\n"
"v_mfma_f32_16x16x32_bf16 v[116:119], a[176:179], v[220:223], v[116:119]\n"
"v_mfma_f32_16x16x32_bf16 v[124:127], a[180:183], v[220:223], v[124:127]\n"
"buffer_store_dwordx4 v[128:131], v250, s[8:11], s49 offen offset:0 sc1\n"
"v_mfma_f32_16x16x32_bf16 v[132:135], a[184:187], v[220:223], v[132:135]\n"
"v_mfma_f32_16x16x32_bf16 v[140:143], a[188:191], v[220:223], v[140:143]\n"
"ds_read_b128 v[216:219], v245 offset:36864\n"
"v_mfma_f32_16x16x32_bf16 v[84:87], a[192:195], v[192:195], v[84:87]\n"
"v_mfma_f32_16x16x32_bf16 v[92:95], a[196:199], v[192:195], v[92:95]\n"
"v_mfma_f32_16x16x32_bf16 v[100:103], a[200:203], v[192:195], v[100:103]\n"
"v_mfma_f32_16x16x32_bf16 v[108:111], a[204:207], v[192:195], v[108:111]\n"
"ds_read_b128 v[220:223], v245 offset:38912\n"
"v_mfma_f32_16x16x32_bf16 v[116:119], a[192:195], v[196:199], v[116:119]\n"
"v_mfma_f32_16x16x32_bf16 v[124:127], a[196:199], v[196:199], v[124:127]\n"
"v_mfma_f32_16x16x32_bf16 v[132:135], a[200:203], v[196:199], v[132:135]\n"
"v_mfma_f32_16x16x32_bf16 v[140:143], a[204:207], v[196:199], v[140:143]\n"
"v_mfma_f32_16x16x32_bf16 v[84:87], a[208:211], v[200:203], v[84:87]\n"
"v_mfma_f32_16x16x32_bf16 v[92:95], a[212:215], v[200:203], v[92:95]\n"
"v_mfma_f32_16x16x32_bf16 v[100:103], a[216:219], v[200:203], v[100:103]\n"
"v_mfma_f32_16x16x32_bf16 v[108:111], a[220:223], v[200:203], v[108:111]\n"
"s_waitcnt lgkmcnt(0)\n"
"v_lshrrev_b32 v251, 2, v224\n"
"v_mfma_f32_16x16x32_bf16 v[116:119], a[208:211], v[204:207], v[116:119]\n"
"s_add_u32 s44, s44, 65536\n"
"v_and_b32 v251, 28, v251\n"
"v_mfma_f32_16x16x32_bf16 v[124:127], a[212:215], v[204:207], v[124:127]\n"
"s_lshl_b32 s92, s30, 10\n"
"s_waitcnt vmcnt(31)\n"
"s_barrier\n"
"v_mfma_f32_16x16x32_bf16 v[132:135], a[216:219], v[204:207], v[132:135]\n"
"s_and_b32 s93, s92, 1024\n"
"buffer_store_dwordx4 v[136:139], v250, s[8:11], s49 offen offset:1024 sc1\n"
"v_mfma_f32_16x16x32_bf16 v[140:143], a[220:223], v[204:207], v[140:143]\n"
"s_add_u32 m0, s38, 73728\n"
"ds_read_b128 v[192:195], v242 offset:16384\n"
"s_add_u32 s48, s39, s98\n"
"v_mfma_f32_16x16x32_bf16 v[84:87], a[224:227], v[208:211], v[84:87]\n"
"s_add_u32 s93, s93, 8192\n"
"ds_read_b32 v234, v251 offset:40960\n"
"v_mfma_f32_16x16x32_bf16 v[92:95], a[228:231], v[208:211], v[92:95]\n"
"buffer_load_dwordx4 v238, s[20:23], s48 offen offset:0 lds\n"
"v_mfma_f32_16x16x32_bf16 v[100:103], a[232:235], v[208:211], v[100:103]\n"
"ds_read_b128 v[196:199], v242 offset:18432\n"
"v_mfma_f32_16x16x32_bf16 v[108:111], a[236:239], v[208:211], v[108:111]\n"
"v_mfma_f32_16x16x32_bf16 v[116:119], a[224:227], v[212:215], v[116:119]\n"
"buffer_load_dwordx4 v239, s[20:23], s48 offen offset:1024 lds\n"
"v_mfma_f32_16x16x32_bf16 v[124:127], a[228:231], v[212:215], v[124:127]\n"
"v_mfma_f32_16x16x32_bf16 v[132:135], a[232:235], v[212:215], v[132:135]\n"
"ds_read_b128 v[200:203], v243 offset:16384\n"
"v_mfma_f32_16x16x32_bf16 v[140:143], a[236:239], v[212:215], v[140:143]\n"
"s_waitcnt lgkmcnt(2)\n"
"v_not_b32 v251, v234\n"
"v_bfe_u32 v252, v251, 4, 7\n"
"v_mfma_f32_16x16x32_bf16 v[84:87], a[240:243], v[216:219], v[84:87]\n"
"s_add_u32 s98, s98, 524288\n"
"buffer_load_dwordx4 v240, s[20:23], s48 offen offset:2048 lds\n"
"v_mfma_f32_16x16x32_bf16 v[92:95], a[244:247], v[216:219], v[92:95]\n"
"v_mul_u32_u24 v252, 20480, v252\n"
"v_and_b32 v251, 15, v251\n"
"v_mfma_f32_16x16x32_bf16 v[100:103], a[248:251], v[216:219], v[100:103]\n"
"ds_read_b128 v[204:207], v243 offset:18432\n"
"v_lshl_add_u32 v251, v251, 4, v252\n"
"v_mfma_f32_16x16x32_bf16 v[108:111], a[252:255], v[216:219], v[108:111]\n"
"s_mov_b32 s49, 266240\n"
"v_and_b32 v252, 0x380, v224\n"
"v_lshl_add_u32 v251, v252, 1, v251\n"
"v_mfma_f32_16x16x32_bf16 v[116:119], a[240:243], v[220:223], v[116:119]\n"
"buffer_load_dwordx4 v241, s[20:23], s48 offen offset:3072 lds\n"
"v_mfma_f32_16x16x32_bf16 v[124:127], a[244:247], v[220:223], v[124:127]\n"
"ds_read_b128 v[208:211], v242 offset:20480\n"
"v_mfma_f32_16x16x32_bf16 v[132:135], a[248:251], v[220:223], v[132:135]\n"
"s_add_u32 m0, s38, 122880\n"
"buffer_store_dwordx4 v[144:147], v250, s[8:11], s49 offen offset:0 sc1\n"
"v_mfma_f32_16x16x32_bf16 v[140:143], a[252:255], v[220:223], v[140:143]\n"
"s_add_u32 s48, s39, s98\n"
"v_mfma_f32_16x16x32_bf16 v[0:3], a[0:3], v[192:195], 0\n"
"buffer_load_dwordx4 v238, s[20:23], s48 offen offset:0 lds\n"
"v_mfma_f32_16x16x32_bf16 v[8:11], a[4:7], v[192:195], 0\n"
"ds_read_b128 v[212:215], v242 offset:22528\n"
"v_mfma_f32_16x16x32_bf16 v[16:19], a[8:11], v[192:195], 0\n"
"v_mfma_f32_16x16x32_bf16 v[24:27], a[12:15], v[192:195], 0\n"
"s_waitcnt lgkmcnt(2)\n"
"v_mul_f32 v84, s55, v84\n"
"v_mul_f32 v85, s55, v85\n"
"v_mfma_f32_16x16x32_bf16 v[32:35], a[0:3], v[196:199], 0\n"
"v_mul_f32 v86, s55, v86\n"
"v_mul_f32 v87, s55, v87\n"
"v_mfma_f32_16x16x32_bf16 v[40:43], a[4:7], v[196:199], 0\n"
"ds_read_b128 v[216:219], v243 offset:20480\n"
"v_mul_f32 v92, s55, v92\n"
"v_mfma_f32_16x16x32_bf16 v[48:51], a[8:11], v[196:199], 0\n"
"buffer_load_dwordx4 v239, s[20:23], s48 offen offset:1024 lds\n"
"v_mfma_f32_16x16x32_bf16 v[56:59], a[12:15], v[196:199], 0\n"
"v_mul_f32 v93, s55, v93\n"
"v_mul_f32 v94, s55, v94\n"
"v_mfma_f32_16x16x32_bf16 v[0:3], a[16:19], v[200:203], v[0:3]\n"
"buffer_load_dwordx4 a[0:3], v235, s[8:11], s56 offen offset:1024 sc1\n"
"v_mfma_f32_16x16x32_bf16 v[8:11], a[20:23], v[200:203], v[8:11]\n"
"ds_read_b128 v[220:223], v243 offset:22528\n"
"v_mul_f32 v95, s55, v95\n"
"v_mfma_f32_16x16x32_bf16 v[16:19], a[24:27], v[200:203], v[16:19]\n"
"buffer_load_dwordx4 v240, s[20:23], s48 offen offset:2048 lds\n"
"v_mfma_f32_16x16x32_bf16 v[24:27], a[28:31], v[200:203], v[24:27]\n"
"v_cvt_pk_bf16_f32 v12, v84, v85\n"
"v_cvt_pk_bf16_f32 v13, v86, v87\n"
"v_mfma_f32_16x16x32_bf16 v[32:35], a[16:19], v[204:207], v[32:35]\n"
"s_add_u32 s98, s98, 524288\n"
"ds_read_b128 v[192:195], v242 offset:40960\n"
"v_cvt_pk_bf16_f32 v14, v92, v93\n"
"v_mfma_f32_16x16x32_bf16 v[40:43], a[20:23], v[204:207], v[40:43]\n"
"s_cmp_eq_u32 s98, 5242880\n"
"buffer_store_dwordx4 v[152:155], v250, s[8:11], s49 offen offset:1024 sc1\n"
"v_mfma_f32_16x16x32_bf16 v[48:51], a[24:27], v[204:207], v[48:51]\n"
"s_cselect_b32 s98, 0, s98\n"
"buffer_load_dwordx4 a[4:7], v235, s[8:11], s57 offen offset:1024 sc1\n"
"v_mfma_f32_16x16x32_bf16 v[56:59], a[28:31], v[204:207], v[56:59]\n"
"s_waitcnt lgkmcnt(2)\n"
"buffer_load_dwordx4 v241, s[20:23], s48 offen offset:3072 lds\n"
"v_mfma_f32_16x16x32_bf16 v[0:3], a[32:35], v[208:211], v[0:3]\n"
"ds_read_b128 v[196:199], v242 offset:43008\n"
"v_cvt_pk_bf16_f32 v15, v94, v95\n"
"v_mfma_f32_16x16x32_bf16 v[8:11], a[36:39], v[208:211], v[8:11]\n"
"v_mul_f32 v100, s55, v100\n"
"v_mul_f32 v101, s55, v101\n"
"v_mfma_f32_16x16x32_bf16 v[16:19], a[40:43], v[208:211], v[16:19]\n"
"v_mul_f32 v102, s55, v102\n"
"v_mul_f32 v103, s55, v103\n"
"v_mfma_f32_16x16x32_bf16 v[24:27], a[44:47], v[208:211], v[24:27]\n"
"v_mul_f32 v108, s55, v108\n"
"v_mul_f32 v109, s55, v109\n"
"v_mfma_f32_16x16x32_bf16 v[32:35], a[32:35], v[212:215], v[32:35]\n"
"ds_read_b128 v[200:203], v243 offset:40960\n"
"v_mul_f32 v110, s55, v110\n"
"v_mfma_f32_16x16x32_bf16 v[40:43], a[36:39], v[212:215], v[40:43]\n"
"s_add_u32 m0, s92, 45056\n"
"buffer_load_dwordx4 a[8:11], v235, s[8:11], s58 offen offset:1024 sc1\n"
"v_mfma_f32_16x16x32_bf16 v[48:51], a[40:43], v[212:215], v[48:51]\n"
"s_lshl_b32 s48, s92, 1\n"
"v_mul_f32 v111, s55, v111\n"
"v_cvt_pk_bf16_f32 v28, v100, v101\n"
"v_mfma_f32_16x16x32_bf16 v[56:59], a[44:47], v[212:215], v[56:59]\n"
"s_add_u32 s48, s48, s44\n"
"v_cvt_pk_bf16_f32 v29, v102, v103\n"
"v_cvt_pk_bf16_f32 v30, v108, v109\n"
"v_mfma_f32_16x16x32_bf16 v[0:3], a[48:51], v[216:219], v[0:3]\n"
"ds_read_b128 v[204:207], v243 offset:43008\n"
"v_cvt_pk_bf16_f32 v31, v110, v111\n"
"v_mfma_f32_16x16x32_bf16 v[8:11], a[52:55], v[216:219], v[8:11]\n"
"buffer_load_dwordx4 v251, s[8:11], s48 offen sc1 lds\n"
"v_mfma_f32_16x16x32_bf16 v[16:19], a[56:59], v[216:219], v[16:19]\n"
"buffer_load_dwordx4 a[12:15], v235, s[8:11], s59 offen offset:1024 sc1\n"
"v_mfma_f32_16x16x32_bf16 v[24:27], a[60:63], v[216:219], v[24:27]\n"
"s_waitcnt lgkmcnt(1)\n"
"ds_read_b128 v[208:211], v242 offset:45056\n"
"v_mul_f32 v116, s55, v116\n"
"v_mfma_f32_16x16x32_bf16 v[32:35], a[48:51], v[220:223], v[32:35]\n"
"v_mul_f32 v117, s55, v117\n"
"v_mul_f32 v118, s55, v118\n"
"v_mfma_f32_16x16x32_bf16 v[40:43], a[52:55], v[220:223], v[40:43]\n"
"v_mul_f32 v119, s55, v119\n"
"v_mul_f32 v124, s55, v124\n"
"v_mfma_f32_16x16x32_bf16 v[48:51], a[56:59], v[220:223], v[48:51]\n"
"v_mul_f32 v125, s55, v125\n"
"v_mul_f32 v126, s55, v126\n"
"v_mfma_f32_16x16x32_bf16 v[56:59], a[60:63], v[220:223], v[56:59]\n"
"ds_read_b128 v[212:215], v242 offset:47104\n"
"v_mul_f32 v127, s55, v127\n"
"v_mfma_f32_16x16x32_bf16 v[0:3], a[64:67], v[192:195], v[0:3]\n"
"buffer_load_dwordx4 a[16:19], v235, s[8:11], s56 offen offset:2048 sc1\n"
"v_mfma_f32_16x16x32_bf16 v[8:11], a[68:71], v[192:195], v[8:11]\n"
"v_cvt_pk_bf16_f32 v44, v116, v117\n"
"v_cvt_pk_bf16_f32 v45, v118, v119\n"
"v_mfma_f32_16x16x32_bf16 v[16:19], a[72:75], v[192:195], v[16:19]\n"
"v_cvt_pk_bf16_f32 v46, v124, v125\n"
"v_cvt_pk_bf16_f32 v47, v126, v127\n"
"v_mfma_f32_16x16x32_bf16 v[24:27], a[76:79], v[192:195], v[24:27]\n"
"ds_read_b128 v[216:219], v243 offset:45056\n"
"v_mfma_f32_16x16x32_bf16 v[32:35], a[64:67], v[196:199], v[32:35]\n"
"v_mul_f32 v132, s55, v132\n"
"v_mul_f32 v133, s55, v133\n"
"v_mfma_f32_16x16x32_bf16 v[40:43], a[68:71], v[196:199], v[40:43]\n"
"s_add_u32 m0, s92, 49152\n"
"v_mul_f32 v134, s55, v134\n"
"v_mul_f32 v135, s55, v135\n"
"v_mfma_f32_16x16x32_bf16 v[48:51], a[72:75], v[196:199], v[48:51]\n"
"s_lshl_b32 s48, s92, 1\n"
"buffer_load_dwordx4 a[20:23], v235, s[8:11], s57 offen offset:2048 sc1\n"
"v_mfma_f32_16x16x32_bf16 v[56:59], a[76:79], v[196:199], v[56:59]\n"
"s_add_u32 s48, s48, s44\n"
"ds_read_b128 v[220:223], v243 offset:47104\n"
"v_mul_f32 v140, s55, v140\n"
"v_mfma_f32_16x16x32_bf16 v[0:3], a[80:83], v[200:203], v[0:3]\n"
"s_add_u32 s48, s48, 8192\n"
"v_mul_f32 v141, s55, v141\n"
"v_mul_f32 v142, s55, v142\n"
"v_mfma_f32_16x16x32_bf16 v[8:11], a[84:87], v[200:203], v[8:11]\n"
"buffer_load_dwordx4 v251, s[8:11], s48 offen sc1 lds\n"
"v_mfma_f32_16x16x32_bf16 v[16:19], a[88:91], v[200:203], v[16:19]\n"
"ds_read_b128 v[192:195], v244 offset:16384\n"
"v_mul_f32 v143, s55, v143\n"
"v_mfma_f32_16x16x32_bf16 v[24:27], a[92:95], v[200:203], v[24:27]\n"
"s_waitcnt lgkmcnt(2)\n"
"v_cvt_pk_bf16_f32 v60, v132, v133\n"
"v_cvt_pk_bf16_f32 v61, v134, v135\n"
"v_mfma_f32_16x16x32_bf16 v[32:35], a[80:83], v[204:207], v[32:35]\n"
"buffer_load_dwordx4 a[24:27], v235, s[8:11], s58 offen offset:2048 sc1\n"
"v_mfma_f32_16x16x32_bf16 v[40:43], a[84:87], v[204:207], v[40:43]\n"
"v_cvt_pk_bf16_f32 v62, v140, v141\n"
"v_cvt_pk_bf16_f32 v63, v142, v143\n"
"v_mfma_f32_16x16x32_bf16 v[48:51], a[88:91], v[204:207], v[48:51]\n"
"ds_read_b128 v[196:199], v244 offset:18432\n"
"v_mfma_f32_16x16x32_bf16 v[56:59], a[92:95], v[204:207], v[56:59]\n"
"v_mfma_f32_16x16x32_bf16 v[0:3], a[96:99], v[208:211], v[0:3]\n"
"v_mfma_f32_16x16x32_bf16 v[8:11], a[100:103], v[208:211], v[8:11]\n"
"v_mfma_f32_16x16x32_bf16 v[16:19], a[104:107], v[208:211], v[16:19]\n"
"ds_read_b128 v[200:203], v245 offset:16384\n"
"v_mfma_f32_16x16x32_bf16 v[24:27], a[108:111], v[208:211], v[24:27]\n"
"buffer_load_dwordx4 a[28:31], v235, s[8:11], s59 offen offset:2048 sc1\n"
"v_mfma_f32_16x16x32_bf16 v[32:35], a[96:99], v[212:215], v[32:35]\n"
"v_mfma_f32_16x16x32_bf16 v[40:43], a[100:103], v[212:215], v[40:43]\n"
"s_add_u32 m0, s93, 45056\n"
"s_lshl_b32 s48, s93, 1\n"
"v_mfma_f32_16x16x32_bf16 v[48:51], a[104:107], v[212:215], v[48:51]\n"
"s_add_u32 s48, s48, s44\n"
"ds_read_b128 v[204:207], v245 offset:18432\n"
"v_mfma_f32_16x16x32_bf16 v[56:59], a[108:111], v[212:215], v[56:59]\n"
"v_mfma_f32_16x16x32_bf16 v[0:3], a[112:115], v[216:219], v[0:3]\n"
"buffer_load_dwordx4 a[32:35], v235, s[8:11], s56 offen offset:3072 sc1\n"
"v_mfma_f32_16x16x32_bf16 v[8:11], a[116:119], v[216:219], v[8:11]\n"
"buffer_load_dwordx4 v251, s[8:11], s48 offen sc1 lds\n"
"v_mfma_f32_16x16x32_bf16 v[16:19], a[120:123], v[216:219], v[16:19]\n"
"ds_read_b128 v[208:211], v244 offset:20480\n"
"v_mfma_f32_16x16x32_bf16 v[24:27], a[124:127], v[216:219], v[24:27]\n"
"s_waitcnt lgkmcnt(2)\n"
"v_mfma_f32_16x16x32_bf16 v[32:35], a[112:115], v[220:223], v[32:35]\n"
"v_mfma_f32_16x16x32_bf16 v[40:43], a[116:119], v[220:223], v[40:43]\n"
"ds_read_b128 v[212:215], v244 offset:22528\n"
"v_mfma_f32_16x16x32_bf16 v[48:51], a[120:123], v[220:223], v[48:51]\n"
"buffer_load_dwordx4 a[36:39], v235, s[8:11], s57 offen offset:3072 sc1\n"
"v_mfma_f32_16x16x32_bf16 v[56:59], a[124:127], v[220:223], v[56:59]\n"
"v_mfma_f32_16x16x32_bf16 v[0:3], a[128:131], v[192:195], v[0:3]\n"
"v_mfma_f32_16x16x32_bf16 v[8:11], a[132:135], v[192:195], v[8:11]\n"
"ds_read_b128 v[216:219], v245 offset:20480\n"
"v_mfma_f32_16x16x32_bf16 v[16:19], a[136:139], v[192:195], v[16:19]\n"
"v_mfma_f32_16x16x32_bf16 v[24:27], a[140:143], v[192:195], v[24:27]\n"
"v_mfma_f32_16x16x32_bf16 v[32:35], a[128:131], v[196:199], v[32:35]\n"
"buffer_load_dwordx4 a[40:43], v235, s[8:11], s58 offen offset:3072 sc1\n"
"v_mfma_f32_16x16x32_bf16 v[40:43], a[132:135], v[196:199], v[40:43]\n"
"ds_read_b128 v[220:223], v245 offset:22528\n"
"v_mfma_f32_16x16x32_bf16 v[48:51], a[136:139], v[196:199], v[48:51]\n"
"v_mfma_f32_16x16x32_bf16 v[56:59], a[140:143], v[196:199], v[56:59]\n"
"v_mfma_f32_16x16x32_bf16 v[0:3], a[144:147], v[200:203], v[0:3]\n"
"s_add_u32 s99, s99, 4096\n"
"ds_read_b128 v[192:195], v244 offset:40960\n"
"v_mfma_f32_16x16x32_bf16 v[8:11], a[148:151], v[200:203], v[8:11]\n"
"s_cmp_eq_u32 s99, 20480\n"
"s_cselect_b32 s99, 0, s99\n"
"v_mfma_f32_16x16x32_bf16 v[16:19], a[152:155], v[200:203], v[16:19]\n"
"buffer_load_dwordx4 a[44:47], v235, s[8:11], s59 offen offset:3072 sc1\n"
"v_mfma_f32_16x16x32_bf16 v[24:27], a[156:159], v[200:203], v[24:27]\n"
"s_waitcnt lgkmcnt(2)\n"
"v_mfma_f32_16x16x32_bf16 v[32:35], a[144:147], v[204:207], v[32:35]\n"
"ds_read_b128 v[196:199], v244 offset:43008\n"
"v_mfma_f32_16x16x32_bf16 v[40:43], a[148:151], v[204:207], v[40:43]\n"
"v_mfma_f32_16x16x32_bf16 v[48:51], a[152:155], v[204:207], v[48:51]\n"
"v_mfma_f32_16x16x32_bf16 v[56:59], a[156:159], v[204:207], v[56:59]\n"
"v_add_u32 v235, s99, v224\n"
"v_mfma_f32_16x16x32_bf16 v[0:3], a[160:163], v[208:211], v[0:3]\n"
"s_sub_u32 s53, s37, 65536\n"
"ds_read_b128 v[200:203], v245 offset:40960\n"
"v_mfma_f32_16x16x32_bf16 v[8:11], a[164:167], v[208:211], v[8:11]\n"
"buffer_load_dwordx4 a[48:51], v235, s[8:11], s56 offen offset:0 sc1\n"
"v_mfma_f32_16x16x32_bf16 v[16:19], a[168:171], v[208:211], v[16:19]\n"
"v_subrev_u32 v246, s53, v246\n"
"v_subrev_u32 v247, s53, v247\n"
"v_mfma_f32_16x16x32_bf16 v[24:27], a[172:175], v[208:211], v[24:27]\n"
"v_subrev_u32 v248, s53, v248\n"
"v_subrev_u32 v249, s53, v249\n"
"v_mfma_f32_16x16x32_bf16 v[32:35], a[160:163], v[212:215], v[32:35]\n"
"ds_read_b128 v[204:207], v245 offset:43008\n"
"v_mfma_f32_16x16x32_bf16 v[40:43], a[164:167], v[212:215], v[40:43]\n"
"v_mfma_f32_16x16x32_bf16 v[48:51], a[168:171], v[212:215], v[48:51]\n"
"buffer_load_dwordx4 a[52:55], v235, s[8:11], s57 offen offset:0 sc1\n"
"v_mfma_f32_16x16x32_bf16 v[56:59], a[172:175], v[212:215], v[56:59]\n"
"ds_read_b128 v[208:211], v244 offset:45056\n"
"v_mfma_f32_16x16x32_bf16 v[0:3], a[176:179], v[216:219], v[0:3]\n"
"v_mfma_f32_16x16x32_bf16 v[8:11], a[180:183], v[216:219], v[8:11]\n"
"v_mfma_f32_16x16x32_bf16 v[16:19], a[184:187], v[216:219], v[16:19]\n"
"v_mfma_f32_16x16x32_bf16 v[24:27], a[188:191], v[216:219], v[24:27]\n"
"s_waitcnt lgkmcnt(1)\n"
"ds_read_b128 v[212:215], v244 offset:47104\n"
"v_mfma_f32_16x16x32_bf16 v[32:35], a[176:179], v[220:223], v[32:35]\n"
"buffer_load_dwordx4 a[56:59], v235, s[8:11], s58 offen offset:0 sc1\n"
"v_mfma_f32_16x16x32_bf16 v[40:43], a[180:183], v[220:223], v[40:43]\n"
"v_mfma_f32_16x16x32_bf16 v[48:51], a[184:187], v[220:223], v[48:51]\n"
"v_mfma_f32_16x16x32_bf16 v[56:59], a[188:191], v[220:223], v[56:59]\n"
"ds_read_b128 v[216:219], v245 offset:45056\n"
"v_mfma_f32_16x16x32_bf16 v[0:3], a[192:195], v[192:195], v[0:3]\n"
"v_mfma_f32_16x16x32_bf16 v[8:11], a[196:199], v[192:195], v[8:11]\n"
"v_mfma_f32_16x16x32_bf16 v[16:19], a[200:203], v[192:195], v[16:19]\n"
"buffer_load_dwordx4 a[60:63], v235, s[8:11], s59 offen offset:0 sc1\n"
"v_mfma_f32_16x16x32_bf16 v[24:27], a[204:207], v[192:195], v[24:27]\n"
"ds_read_b128 v[220:223], v245 offset:47104\n"
"v_mfma_f32_16x16x32_bf16 v[32:35], a[192:195], v[196:199], v[32:35]\n"
"v_mfma_f32_16x16x32_bf16 v[40:43], a[196:199], v[196:199], v[40:43]\n"
"v_mfma_f32_16x16x32_bf16 v[48:51], a[200:203], v[196:199], v[48:51]\n"
"s_mov_b32 s49, 270336\n"
"v_mfma_f32_16x16x32_bf16 v[56:59], a[204:207], v[196:199], v[56:59]\n"
"v_mfma_f32_16x16x32_bf16 v[0:3], a[208:211], v[200:203], v[0:3]\n"
"s_lshl_b32 s50, s35, 3\n"
"buffer_store_dwordx4 v[4:7], v250, s[8:11], s49 offen offset:0 sc1\n"
"v_mfma_f32_16x16x32_bf16 v[8:11], a[212:215], v[200:203], v[8:11]\n"
"s_add_u32 s50, s50, s34\n"
"buffer_load_dwordx4 a[64:67], v235, s[8:11], s56 offen offset:1024 sc1\n"
"v_mfma_f32_16x16x32_bf16 v[16:19], a[216:219], v[200:203], v[16:19]\n"
"s_lshl_b32 s50, s50, 3\n"
"s_add_u32 s50, s50, 2304\n"
"s_add_u32 s51, s50, s45\n"
"v_mfma_f32_16x16x32_bf16 v[24:27], a[220:223], v[200:203], v[24:27]\n"
"v_mfma_f32_16x16x32_bf16 v[32:35], a[208:211], v[204:207], v[32:35]\n"
"v_mfma_f32_16x16x32_bf16 v[40:43], a[212:215], v[204:207], v[40:43]\n"
"s_waitcnt vmcnt(33) lgkmcnt(0)\n"
"s_barrier\n"
"v_mfma_f32_16x16x32_bf16 v[48:51], a[216:219], v[204:207], v[48:51]\n"
"s_add_u32 m0, s38, 81920\n"
"ds_read_b64_tr_b16 a[128:129], v246 offset:0\n"
"s_add_u32 s48, s39, s98\n"
"v_mfma_f32_16x16x32_bf16 v[56:59], a[220:223], v[204:207], v[56:59]\n"
"buffer_load_dwordx4 a[68:71], v235, s[8:11], s57 offen offset:1024 sc1\n"
"v_mfma_f32_16x16x32_bf16 v[0:3], a[224:227], v[208:211], v[0:3]\n"
"buffer_store_dwordx2 v[232:233], v230, s[8:11], s51 offen sc1\n"
"v_mfma_f32_16x16x32_bf16 v[8:11], a[228:231], v[208:211], v[8:11]\n"
"ds_read_b64_tr_b16 a[130:131], v246 offset:512\n"
"v_mfma_f32_16x16x32_bf16 v[16:19], a[232:235], v[208:211], v[16:19]\n"
"buffer_load_dwordx4 v238, s[20:23], s48 offen offset:0 lds\n"
"v_mfma_f32_16x16x32_bf16 v[24:27], a[236:239], v[208:211], v[24:27]\n"
"ds_read_b64_tr_b16 a[132:133], v247 offset:0\n"
"v_add_u32 v242, 49152, v246\n"
"v_mfma_f32_16x16x32_bf16 v[32:35], a[224:227], v[212:215], v[32:35]\n"
"ds_read_b64_tr_b16 a[134:135], v247 offset:512\n"
"v_add_u32 v243, 49152, v247\n"
"v_mfma_f32_16x16x32_bf16 v[40:43], a[228:231], v[212:215], v[40:43]\n"
"buffer_load_dwordx4 a[72:75], v235, s[8:11], s58 offen offset:1024 sc1\n"
"v_mfma_f32_16x16x32_bf16 v[48:51], a[232:235], v[212:215], v[48:51]\n"
"ds_read_b64_tr_b16 a[136:137], v248 offset:0\n"
"v_add_u32 v244, 49152, v248\n"
"v_mfma_f32_16x16x32_bf16 v[56:59], a[236:239], v[212:215], v[56:59]\n"
"v_add_u32 v245, 49152, v249\n"
"v_mfma_f32_16x16x32_bf16 v[0:3], a[240:243], v[216:219], v[0:3]\n"
"ds_read_b64_tr_b16 a[138:139], v248 offset:512\n"
"v_mfma_f32_16x16x32_bf16 v[8:11], a[244:247], v[216:219], v[8:11]\n"
"buffer_load_dwordx4 v239, s[20:23], s48 offen offset:1024 lds\n"
"v_mfma_f32_16x16x32_bf16 v[16:19], a[248:251], v[216:219], v[16:19]\n"
"ds_read_b64_tr_b16 a[140:141], v249 offset:0\n"
"v_mfma_f32_16x16x32_bf16 v[24:27], a[252:255], v[216:219], v[24:27]\n"
"buffer_load_dwordx4 a[76:79], v235, s[8:11], s59 offen offset:1024 sc1\n"
"v_mfma_f32_16x16x32_bf16 v[32:35], a[240:243], v[220:223], v[32:35]\n"
"ds_read_b64_tr_b16 a[142:143], v249 offset:512\n"
"v_mfma_f32_16x16x32_bf16 v[40:43], a[244:247], v[220:223], v[40:43]\n"
"v_mfma_f32_16x16x32_bf16 v[48:51], a[248:251], v[220:223], v[48:51]\n"
"ds_read_b64_tr_b16 a[144:145], v246 offset:4096\n"
"v_mfma_f32_16x16x32_bf16 v[56:59], a[252:255], v[220:223], v[56:59]\n"
"s_waitcnt lgkmcnt(3)\n"
"v_mfma_f32_16x16x32_bf16 v[80:83], a[128:131], v[64:67], 0\n"
"ds_read_b64_tr_b16 a[146:147], v246 offset:4608\n"
"v_mfma_f32_16x16x32_bf16 v[88:91], a[128:131], v[68:71], 0\n"
"buffer_load_dwordx4 a[80:83], v235, s[8:11], s56 offen offset:2048 sc1\n"
"v_mfma_f32_16x16x32_bf16 v[96:99], a[128:131], v[72:75], 0\n"
"buffer_load_dwordx4 v240, s[20:23], s48 offen offset:2048 lds\n"
"v_mfma_f32_16x16x32_bf16 v[104:107], a[128:131], v[76:79], 0\n"
"ds_read_b64_tr_b16 a[148:149], v247 offset:4096\n"
"v_mul_f32 v0, s55, v0\n"
"v_mfma_f32_16x16x32_bf16 v[84:87], a[132:135], v[64:67], 0\n"
"ds_read_b64_tr_b16 a[150:151], v247 offset:4608\n"
"v_mul_f32 v1, s55, v1\n"
"v_mfma_f32_16x16x32_bf16 v[92:95], a[132:135], v[68:71], 0\n"
"ds_read_b64_tr_b16 a[152:153], v248 offset:4096\n"
"v_mul_f32 v2, s55, v2\n"
"v_mfma_f32_16x16x32_bf16 v[100:103], a[132:135], v[72:75], 0\n"
"v_mul_f32 v3, s55, v3\n"
"v_mul_f32 v8, s55, v8\n"
"v_mfma_f32_16x16x32_bf16 v[108:111], a[132:135], v[76:79], 0\n"
"ds_read_b64_tr_b16 a[154:155], v248 offset:4608\n"
"v_mul_f32 v9, s55, v9\n"
"v_mfma_f32_16x16x32_bf16 v[112:115], a[136:139], v[64:67], 0\n"
"s_add_u32 s98, s98, 524288\n"
"buffer_load_dwordx4 a[84:87], v235, s[8:11], s57 offen offset:2048 sc1\n"
"v_mfma_f32_16x16x32_bf16 v[120:123], a[136:139], v[68:71], 0\n"
"buffer_load_dwordx4 v241, s[20:23], s48 offen offset:3072 lds\n"
"v_mfma_f32_16x16x32_bf16 v[128:131], a[136:139], v[72:75], 0\n"
"ds_read_b64_tr_b16 a[156:157], v249 offset:4096\n"
"v_mul_f32 v10, s55, v10\n"
"v_mfma_f32_16x16x32_bf16 v[136:139], a[136:139], v[76:79], 0\n"
"s_waitcnt lgkmcnt(3)\n"
"ds_read_b64_tr_b16 a[158:159], v249 offset:4608\n"
"v_mul_f32 v11, s55, v11\n"
"v_mfma_f32_16x16x32_bf16 v[116:119], a[140:143], v[64:67], 0\n"
"v_cvt_pk_bf16_f32 v0, v0, v1\n"
"v_cvt_pk_bf16_f32 v1, v2, v3\n"
"v_mfma_f32_16x16x32_bf16 v[124:127], a[140:143], v[68:71], 0\n"
"ds_read_b64_tr_b16 a[160:161], v246 offset:24576\n"
"v_cvt_pk_bf16_f32 v2, v8, v9\n"
"v_mfma_f32_16x16x32_bf16 v[132:135], a[140:143], v[72:75], 0\n"
"s_add_u32 m0, s38, 131072\n"
"buffer_load_dwordx4 a[88:91], v235, s[8:11], s58 offen offset:2048 sc1\n"
"v_mfma_f32_16x16x32_bf16 v[140:143], a[140:143], v[76:79], 0\n"
"s_add_u32 s48, s39, s98\n"
"ds_read_b64_tr_b16 a[162:163], v246 offset:25088\n"
"v_cvt_pk_bf16_f32 v3, v10, v11\n"
"v_mfma_f32_16x16x32_bf16 v[144:147], a[144:147], v[64:67], 0\n"
"buffer_load_dwordx4 v238, s[20:23], s48 offen offset:0 lds\n"
"v_mfma_f32_16x16x32_bf16 v[152:155], a[144:147], v[68:71], 0\n"
"ds_read_b64_tr_b16 a[164:165], v247 offset:24576\n"
"v_mul_f32 v16, s55, v16\n"
"v_mfma_f32_16x16x32_bf16 v[160:163], a[144:147], v[72:75], 0\n"
"v_mul_f32 v17, s55, v17\n"
"v_mul_f32 v18, s55, v18\n"
"v_mfma_f32_16x16x32_bf16 v[168:171], a[144:147], v[76:79], 0\n"
"ds_read_b64_tr_b16 a[166:167], v247 offset:25088\n"
"v_mul_f32 v19, s55, v19\n"
"v_mfma_f32_16x16x32_bf16 v[148:151], a[148:151], v[64:67], 0\n"
"buffer_store_dwordx4 v[20:23], v250, s[8:11], s49 offen offset:1024 sc1\n"
"v_mfma_f32_16x16x32_bf16 v[156:159], a[148:151], v[68:71], 0\n"
"ds_read_b64_tr_b16 a[168:169], v248 offset:24576\n"
"v_mul_f32 v24, s55, v24\n"
"v_mfma_f32_16x16x32_bf16 v[164:167], a[148:151], v[72:75], 0\n"
"buffer_load_dwordx4 a[92:95], v235, s[8:11], s59 offen offset:2048 sc1\n"
"v_mfma_f32_16x16x32_bf16 v[172:175], a[148:151], v[76:79], 0\n"
"s_waitcnt lgkmcnt(3)\n"
"ds_read_b64_tr_b16 a[170:171], v248 offset:25088\n"
"v_mul_f32 v25, s55, v25\n"
"v_mfma_f32_16x16x32_bf16 v[176:179], a[152:155], v[64:67], 0\n"
"ds_read_b64_tr_b16 a[172:173], v249 offset:24576\n"
"v_mul_f32 v26, s55, v26\n"
"v_mfma_f32_16x16x32_bf16 v[184:187], a[152:155], v[68:71], 0\n"
"buffer_load_dwordx4 v239, s[20:23], s48 offen offset:1024 lds\n"
"v_mfma_f32_16x16x32_bf16 v[192:195], a[152:155], v[72:75], 0\n"
"ds_read_b64_tr_b16 a[174:175], v249 offset:25088\n"
"v_mul_f32 v27, s55, v27\n"
"v_mfma_f32_16x16x32_bf16 v[200:203], a[152:155], v[76:79], 0\n"
"v_cvt_pk_bf16_f32 v16, v16, v17\n"
"v_cvt_pk_bf16_f32 v17, v18, v19\n"
"v_mfma_f32_16x16x32_bf16 v[180:183], a[156:159], v[64:67], 0\n"
"ds_read_b64_tr_b16 a[176:177], v246 offset:28672\n"
"v_cvt_pk_bf16_f32 v18, v24, v25\n"
"v_mfma_f32_16x16x32_bf16 v[188:191], a[156:159], v[68:71], 0\n"
"buffer_load_dwordx4 a[96:99], v235, s[8:11], s56 offen offset:3072 sc1\n"
"v_mfma_f32_16x16x32_bf16 v[196:199], a[156:159], v[72:75], 0\n"
"ds_read_b64_tr_b16 a[178:179], v246 offset:29184\n"
"v_cvt_pk_bf16_f32 v19, v26, v27\n"
"v_mfma_f32_16x16x32_bf16 v[204:207], a[156:159], v[76:79], 0\n"
"s_waitcnt vmcnt(39)\n"
"v_mul_f32 v32, s55, v32\n"
"v_mul_f32 v33, s55, v33\n"
"v_mfma_f32_16x16x32_bf16 v[80:83], a[160:163], a[0:3], v[80:83]\n"
"s_waitcnt vmcnt(36)\n"
"ds_read_b64_tr_b16 a[180:181], v247 offset:28672\n"
"v_mul_f32 v34, s55, v34\n"
"v_mfma_f32_16x16x32_bf16 v[88:91], a[160:163], a[4:7], v[88:91]\n"
"s_waitcnt vmcnt(34)\n"
"buffer_load_dwordx4 v240, s[20:23], s48 offen offset:2048 lds\n"
"v_mfma_f32_16x16x32_bf16 v[96:99], a[160:163], a[8:11], v[96:99]\n"
"s_waitcnt vmcnt(33)\n"
"ds_read_b64_tr_b16 a[182:183], v247 offset:29184\n"
"v_mul_f32 v35, s55, v35\n"
"v_mfma_f32_16x16x32_bf16 v[104:107], a[160:163], a[12:15], v[104:107]\n"
"s_waitcnt lgkmcnt(3)\n"
"buffer_load_dwordx4 a[100:103], v235, s[8:11], s57 offen offset:3072 sc1\n"
"v_mfma_f32_16x16x32_bf16 v[84:87], a[164:167], a[0:3], v[84:87]\n"
"s_mov_b32 s88, 3072\n"
"ds_read_b64_tr_b16 a[184:185], v248 offset:28672\n"
"v_mul_f32 v40, s55, v40\n"
"v_mfma_f32_16x16x32_bf16 v[92:95], a[164:167], a[4:7], v[92:95]\n"
"s_mov_b32 s89, 3072\n"
"v_mul_f32 v41, s55, v41\n"
"v_mul_f32 v42, s55, v42\n"
"v_mfma_f32_16x16x32_bf16 v[100:103], a[164:167], a[8:11], v[100:103]\n"
"ds_read_b64_tr_b16 a[186:187], v248 offset:29184\n"
"v_mul_f32 v43, s55, v43\n"
"v_mfma_f32_16x16x32_bf16 v[108:111], a[164:167], a[12:15], v[108:111]\n"
"v_cvt_pk_bf16_f32 v32, v32, v33\n"
"v_cvt_pk_bf16_f32 v33, v34, v35\n"
"v_mfma_f32_16x16x32_bf16 v[112:115], a[168:171], a[0:3], v[112:115]\n"
"s_add_u32 s98, s98, 524288\n"
"ds_read_b64_tr_b16 a[188:189], v249 offset:28672\n"
"v_cvt_pk_bf16_f32 v34, v40, v41\n"
"v_mfma_f32_16x16x32_bf16 v[120:123], a[168:171], a[4:7], v[120:123]\n"
"s_cmp_eq_u32 s98, 5242880\n"
"buffer_load_dwordx4 v241, s[20:23], s48 offen offset:3072 lds\n"
"v_mfma_f32_16x16x32_bf16 v[128:131], a[168:171], a[8:11], v[128:131]\n"
"s_cselect_b32 s98, 0, s98\n"
"ds_read_b64_tr_b16 a[190:191], v249 offset:29184\n"
"v_cvt_pk_bf16_f32 v35, v42, v43\n"
"v_mfma_f32_16x16x32_bf16 v[136:139], a[168:171], a[12:15], v[136:139]\n"
"buffer_load_dwordx4 a[104:107], v235, s[8:11], s58 offen offset:3072 sc1\n"
"v_mfma_f32_16x16x32_bf16 v[116:119], a[172:175], a[0:3], v[116:119]\n"
"ds_read_b64_tr_b16 a[128:129], v242 offset:0\n"
"v_mul_f32 v48, s55, v48\n"
"v_mfma_f32_16x16x32_bf16 v[124:127], a[172:175], a[4:7], v[124:127]\n"
"ds_read_b64_tr_b16 a[130:131], v242 offset:512\n"
"v_mul_f32 v49, s55, v49\n"
"v_mfma_f32_16x16x32_bf16 v[132:135], a[172:175], a[8:11], v[132:135]\n"
"v_mul_f32 v50, s55, v50\n"
"v_mul_f32 v51, s55, v51\n"
"v_mfma_f32_16x16x32_bf16 v[140:143], a[172:175], a[12:15], v[140:143]\n"
"s_waitcnt lgkmcnt(3)\n"
"ds_read_b64_tr_b16 a[132:133], v243 offset:0\n"
"v_mul_f32 v56, s55, v56\n"
"v_mfma_f32_16x16x32_bf16 v[144:147], a[176:179], a[0:3], v[144:147]\n"
"s_add_u32 s99, s99, 4096\n"
"v_mul_f32 v57, s55, v57\n"
"v_mul_f32 v58, s55, v58\n"
"v_mfma_f32_16x16x32_bf16 v[152:155], a[176:179], a[4:7], v[152:155]\n"
"s_cmp_eq_u32 s99, 20480\n"
"ds_read_b64_tr_b16 a[134:135], v243 offset:512\n"
"v_mul_f32 v59, s55, v59\n"
"v_mfma_f32_16x16x32_bf16 v[160:163], a[176:179], a[8:11], v[160:163]\n"
"s_cselect_b32 s99, 0, s99\n"
"buffer_load_dwordx4 a[108:111], v235, s[8:11], s59 offen offset:3072 sc1\n"
"v_mfma_f32_16x16x32_bf16 v[168:171], a[176:179], a[12:15], v[168:171]\n"
"ds_read_b64_tr_b16 a[136:137], v244 offset:0\n"
"v_cvt_pk_bf16_f32 v48, v48, v49\n"
"v_mfma_f32_16x16x32_bf16 v[148:151], a[180:183], a[0:3], v[148:151]\n"
"v_cvt_pk_bf16_f32 v49, v50, v51\n"
"v_cvt_pk_bf16_f32 v50, v56, v57\n"
"v_mfma_f32_16x16x32_bf16 v[156:159], a[180:183], a[4:7], v[156:159]\n"
"ds_read_b64_tr_b16 a[138:139], v244 offset:512\n"
"v_cvt_pk_bf16_f32 v51, v58, v59\n"
"v_mfma_f32_16x16x32_bf16 v[164:167], a[180:183], a[8:11], v[164:167]\n"
"v_mfma_f32_16x16x32_bf16 v[172:175], a[180:183], a[12:15], v[172:175]\n"
"ds_read_b64_tr_b16 a[140:141], v245 offset:0\n"
"v_add_u32 v235, s99, v224\n"
"v_mfma_f32_16x16x32_bf16 v[176:179], a[184:187], a[0:3], v[176:179]\n"
"buffer_load_dwordx4 a[112:115], v235, s[8:11], s56 offen offset:0 sc1\n"
"v_mfma_f32_16x16x32_bf16 v[184:187], a[184:187], a[4:7], v[184:187]\n"
"ds_read_b64_tr_b16 a[142:143], v245 offset:512\n"
"v_mfma_f32_16x16x32_bf16 v[192:195], a[184:187], a[8:11], v[192:195]\n"
"v_mfma_f32_16x16x32_bf16 v[200:203], a[184:187], a[12:15], v[200:203]\n"
"s_waitcnt lgkmcnt(3)\n"
"ds_read_b64_tr_b16 a[144:145], v242 offset:4096\n"
"v_mfma_f32_16x16x32_bf16 v[180:183], a[188:191], a[0:3], v[180:183]\n"
"v_mfma_f32_16x16x32_bf16 v[188:191], a[188:191], a[4:7], v[188:191]\n"
"ds_read_b64_tr_b16 a[146:147], v242 offset:4608\n"
"v_mfma_f32_16x16x32_bf16 v[196:199], a[188:191], a[8:11], v[196:199]\n"
"s_mov_b32 s49, 274432\n"
"v_mfma_f32_16x16x32_bf16 v[204:207], a[188:191], a[12:15], v[204:207]\n"
"s_waitcnt vmcnt(37)\n"
"ds_read_b64_tr_b16 a[148:149], v243 offset:4096\n"
"v_mfma_f32_16x16x32_bf16 v[80:83], a[128:131], a[16:19], v[80:83]\n"
"s_waitcnt vmcnt(36)\n"
"buffer_load_dwordx4 a[116:119], v235, s[8:11], s57 offen offset:0 sc1\n"
"v_mfma_f32_16x16x32_bf16 v[88:91], a[128:131], a[20:23], v[88:91]\n"
"s_waitcnt vmcnt(35)\n"
"buffer_store_dwordx4 v[36:39], v250, s[8:11], s49 offen offset:0 sc1\n"
"v_mfma_f32_16x16x32_bf16 v[96:99], a[128:131], a[24:27], v[96:99]\n"
"s_waitcnt vmcnt(35)\n"
"ds_read_b64_tr_b16 a[150:151], v243 offset:4608\n"
"v_mfma_f32_16x16x32_bf16 v[104:107], a[128:131], a[28:31], v[104:107]\n"
"ds_read_b64_tr_b16 a[152:153], v244 offset:4096\n"
"v_mfma_f32_16x16x32_bf16 v[84:87], a[132:135], a[16:19], v[84:87]\n"
"ds_read_b64_tr_b16 a[154:155], v244 offset:4608\n"
"v_mfma_f32_16x16x32_bf16 v[92:95], a[132:135], a[20:23], v[92:95]\n"
"v_mfma_f32_16x16x32_bf16 v[100:103], a[132:135], a[24:27], v[100:103]\n"
"ds_read_b64_tr_b16 a[156:157], v245 offset:4096\n"
"v_mfma_f32_16x16x32_bf16 v[108:111], a[132:135], a[28:31], v[108:111]\n"
"s_waitcnt lgkmcnt(4)\n"
"buffer_load_dwordx4 a[120:123], v235, s[8:11], s58 offen offset:0 sc1\n"
"v_mfma_f32_16x16x32_bf16 v[112:115], a[136:139], a[16:19], v[112:115]\n"
"ds_read_b64_tr_b16 a[158:159], v245 offset:4608\n"
"v_mfma_f32_16x16x32_bf16 v[120:123], a[136:139], a[20:23], v[120:123]\n"
"v_mfma_f32_16x16x32_bf16 v[128:131], a[136:139], a[24:27], v[128:131]\n"
"ds_read_b64_tr_b16 a[160:161], v242 offset:24576\n"
"v_mfma_f32_16x16x32_bf16 v[136:139], a[136:139], a[28:31], v[136:139]\n"
"v_mfma_f32_16x16x32_bf16 v[116:119], a[140:143], a[16:19], v[116:119]\n"
"ds_read_b64_tr_b16 a[162:163], v242 offset:25088\n"
"v_mfma_f32_16x16x32_bf16 v[124:127], a[140:143], a[20:23], v[124:127]\n"
"buffer_load_dwordx4 a[124:127], v235, s[8:11], s59 offen offset:0 sc1\n"
"v_mfma_f32_16x16x32_bf16 v[132:135], a[140:143], a[24:27], v[132:135]\n"
"ds_read_b64_tr_b16 a[164:165], v243 offset:24576\n"
"v_mfma_f32_16x16x32_bf16 v[140:143], a[140:143], a[28:31], v[140:143]\n"
"v_mfma_f32_16x16x32_bf16 v[144:147], a[144:147], a[16:19], v[144:147]\n"
"ds_read_b64_tr_b16 a[166:167], v243 offset:25088\n"
"v_mfma_f32_16x16x32_bf16 v[152:155], a[144:147], a[20:23], v[152:155]\n"
"v_mfma_f32_16x16x32_bf16 v[160:163], a[144:147], a[24:27], v[160:163]\n"
"ds_read_b64_tr_b16 a[168:169], v244 offset:24576\n"
"v_mfma_f32_16x16x32_bf16 v[168:171], a[144:147], a[28:31], v[168:171]\n"
"s_waitcnt lgkmcnt(3)\n"
"v_mfma_f32_16x16x32_bf16 v[148:151], a[148:151], a[16:19], v[148:151]\n"
"ds_read_b64_tr_b16 a[170:171], v244 offset:25088\n"
"v_mfma_f32_16x16x32_bf16 v[156:159], a[148:151], a[20:23], v[156:159]\n"
"buffer_load_dwordx4 a[0:3], v235, s[8:11], s56 offen offset:1024 sc1\n"
"v_mfma_f32_16x16x32_bf16 v[164:167], a[148:151], a[24:27], v[164:167]\n"
"ds_read_b64_tr_b16 a[172:173], v245 offset:24576\n"
"v_mfma_f32_16x16x32_bf16 v[172:175], a[148:151], a[28:31], v[172:175]\n"
"ds_read_b64_tr_b16 a[174:175], v245 offset:25088\n"
"v_mfma_f32_16x16x32_bf16 v[176:179], a[152:155], a[16:19], v[176:179]\n"
"v_mfma_f32_16x16x32_bf16 v[184:187], a[152:155], a[20:23], v[184:187]\n"
"ds_read_b64_tr_b16 a[176:177], v242 offset:28672\n"
"v_mfma_f32_16x16x32_bf16 v[192:195], a[152:155], a[24:27], v[192:195]\n"
"v_mfma_f32_16x16x32_bf16 v[200:203], a[152:155], a[28:31], v[200:203]\n"
"ds_read_b64_tr_b16 a[178:179], v242 offset:29184\n"
"v_mfma_f32_16x16x32_bf16 v[180:183], a[156:159], a[16:19], v[180:183]\n"
"s_mov_b32 s49, 275456\n"
"buffer_load_dwordx4 a[4:7], v235, s[8:11], s57 offen offset:1024 sc1\n"
"v_mfma_f32_16x16x32_bf16 v[188:191], a[156:159], a[20:23], v[188:191]\n"
"ds_read_b64_tr_b16 a[180:181], v243 offset:28672\n"
"v_mfma_f32_16x16x32_bf16 v[196:199], a[156:159], a[24:27], v[196:199]\n"
"v_mfma_f32_16x16x32_bf16 v[204:207], a[156:159], a[28:31], v[204:207]\n"
"ds_read_b64_tr_b16 a[182:183], v243 offset:29184\n"
"s_branch LQ_%=\n"
"LV_%=:\n"
"s_waitcnt vmcnt(25)\n"
"v_lshlrev_b32 v148, 16, v128\n"
"v_and_b32 v149, 0xffff0000, v128\n"
"v_lshlrev_b32 v150, 16, v129\n"
"v_and_b32 v151, 0xffff0000, v129\n"
"s_waitcnt vmcnt(24)\n"
"v_lshlrev_b32 v152, 16, v130\n"
"v_and_b32 v153, 0xffff0000, v130\n"
"v_lshlrev_b32 v154, 16, v131\n"
"v_and_b32 v155, 0xffff0000, v131\n"
"s_waitcnt vmcnt(23)\n"
"v_lshlrev_b32 v156, 16, v132\n"
"v_and_b32 v157, 0xffff0000, v132\n"
"v_lshlrev_b32 v158, 16, v133\n"
"v_and_b32 v159, 0xffff0000, v133\n"
"s_waitcnt vmcnt(22)\n"
"v_lshlrev_b32 v160, 16, v134\n"
"v_and_b32 v161, 0xffff0000, v134\n"
"v_lshlrev_b32 v162, 16, v135\n"
"v_and_b32 v163, 0xffff0000, v135\n"
"s_waitcnt vmcnt(21)\n"
"v_lshlrev_b32 v164, 16, v136\n"
"v_and_b32 v165, 0xffff0000, v136\n"
"v_lshlrev_b32 v166, 16, v137\n"
"v_and_b32 v167, 0xffff0000, v137\n"
"v_add_f32 v188, v148, v149\n"
"v_add_f32 v189, v150, v151\n"
"v_add_f32 v188, v188, v152\n"
"v_add_f32 v189, v189, v153\n"
"v_add_f32 v188, v188, v154\n"
"v_add_f32 v189, v189, v155\n"
"v_add_f32 v188, v188, v156\n"
"v_add_f32 v189, v189, v157\n"
"v_add_f32 v188, v188, v158\n"
"v_add_f32 v189, v189, v159\n"
"v_add_f32 v188, v188, v160\n"
"v_add_f32 v189, v189, v161\n"
"v_add_f32 v188, v188, v162\n"
"v_add_f32 v189, v189, v163\n"
"v_add_f32 v188, v188, v164\n"
"v_add_f32 v189, v189, v165\n"
"v_add_f32 v188, v188, v166\n"
"v_add_f32 v189, v189, v167\n"
"v_add_f32 v188, v188, v189\n"
"s_nop 1\n"
"v_add_f32_dpp v188, v188, v188 quad_perm:[1,0,3,2] row_mask:0xf bank_mask:0xf\n"
"s_waitcnt vmcnt(20)\n"
"v_lshlrev_b32 v168, 16, v138\n"
"v_and_b32 v169, 0xffff0000, v138\n"
"v_add_f32_dpp v188, v188, v188 quad_perm:[2,3,0,1] row_mask:0xf bank_mask:0xf\n"
"v_lshlrev_b32 v170, 16, v139\n"
"v_and_b32 v171, 0xffff0000, v139\n"
"v_add_f32_dpp v188, v188, v188 row_half_mirror row_mask:0xf bank_mask:0xf\n"
"s_waitcnt vmcnt(19)\n"
"v_lshlrev_b32 v172, 16, v140\n"
"v_and_b32 v173, 0xffff0000, v140\n"
"v_add_f32_dpp v188, v188, v188 row_mirror row_mask:0xf bank_mask:0xf\n"
"v_lshlrev_b32 v174, 16, v141\n"
"v_and_b32 v175, 0xffff0000, v141\n"
"v_mov_b32 v189, v188\n"
"s_waitcnt vmcnt(18)\n"
"v_lshlrev_b32 v176, 16, v142\n"
"v_and_b32 v177, 0xffff0000, v142\n"
"v_permlane16_swap_b32 v189, v188\n"
"v_lshlrev_b32 v178, 16, v143\n"
"v_and_b32 v179, 0xffff0000, v143\n"
"v_add_f32 v188, v188, v189\n"
"s_waitcnt vmcnt(17)\n"
"v_lshlrev_b32 v180, 16, v144\n"
"v_and_b32 v181, 0xffff0000, v144\n"
"v_mul_f32 v191, 0x3acccccd, v188\n"
"v_lshlrev_b32 v182, 16, v145\n"
"v_and_b32 v183, 0xffff0000, v145\n"
"v_cvt_pk_bf16_f32 v128, v191, v191\n"
"s_waitcnt vmcnt(16)\n"
"v_lshlrev_b32 v184, 16, v146\n"
"v_and_b32 v185, 0xffff0000, v146\n"
"v_lshlrev_b32 v128, 16, v128\n"
"v_lshlrev_b32 v186, 16, v147\n"
"v_and_b32 v187, 0xffff0000, v147\n"
"s_add_u32 m0, s36, 16384\n"
"s_add_u32 s48, s46, 128\n"
"s_and_b32 s48, s48, 1023\n"
"buffer_load_dwordx4 v234, s[12:15], s48 offen offset:0 lds\n"
"buffer_load_dwordx4 v235, s[12:15], s48 offen offset:1024 lds\n"
"buffer_load_dwordx4 v236, s[12:15], s48 offen offset:2048 lds\n"
"buffer_load_dwordx4 v237, s[12:15], s48 offen offset:3072 lds\n"
"v_sub_f32 v148, v148, v128\n"
"v_sub_f32 v149, v149, v128\n"
"v_sub_f32 v150, v150, v128\n"
"v_sub_f32 v151, v151, v128\n"
"v_sub_f32 v152, v152, v128\n"
"v_sub_f32 v153, v153, v128\n"
"v_sub_f32 v154, v154, v128\n"
"v_sub_f32 v155, v155, v128\n"
"v_sub_f32 v156, v156, v128\n"
"v_sub_f32 v157, v157, v128\n"
"v_sub_f32 v158, v158, v128\n"
"v_sub_f32 v159, v159, v128\n"
"v_sub_f32 v160, v160, v128\n"
"v_sub_f32 v161, v161, v128\n"
"v_sub_f32 v162, v162, v128\n"
"v_sub_f32 v163, v163, v128\n"
"v_sub_f32 v164, v164, v128\n"
"v_sub_f32 v165, v165, v128\n"
"v_sub_f32 v166, v166, v128\n"
"v_sub_f32 v167, v167, v128\n"
"v_mul_f32 v188, v148, v148\n"
"v_mul_f32 v189, v149, v149\n"
"v_fmac_f32 v188, v150, v150\n"
"v_fmac_f32 v189, v151, v151\n"
"v_fmac_f32 v188, v152, v152\n"
"v_fmac_f32 v189, v153, v153\n"
"v_fmac_f32 v188, v154, v154\n"
"v_fmac_f32 v189, v155, v155\n"
"v_fmac_f32 v188, v156, v156\n"
"v_fmac_f32 v189, v157, v157\n"
"v_fmac_f32 v188, v158, v158\n"
"v_fmac_f32 v189, v159, v159\n"
"v_fmac_f32 v188, v160, v160\n"
"v_fmac_f32 v189, v161, v161\n"
"v_fmac_f32 v188, v162, v162\n"
"v_fmac_f32 v189, v163, v163\n"
"v_fmac_f32 v188, v164, v164\n"
"v_fmac_f32 v189, v165, v165\n"
"v_fmac_f32 v188, v166, v166\n"
"v_fmac_f32 v189, v167, v167\n"
"v_add_f32 v188, v188, v189\n"
"v_cvt_pk_bf16_f32 v149, v148, v149\n"
"v_lshlrev_b32 v148, 16, v149\n"
"v_add_f32_dpp v188, v188, v188 quad_perm:[1,0,3,2] row_mask:0xf bank_mask:0xf\n"
"v_and_b32 v149, 0xffff0000, v149\n"
"v_cvt_pk_bf16_f32 v151, v150, v151\n"
"v_add_f32_dpp v188, v188, v188 quad_perm:[2,3,0,1] row_mask:0xf bank_mask:0xf\n"
"v_lshlrev_b32 v150, 16, v151\n"
"v_and_b32 v151, 0xffff0000, v151\n"
"v_add_f32_dpp v188, v188, v188 row_half_mirror row_mask:0xf bank_mask:0xf\n"
"v_cvt_pk_bf16_f32 v153, v152, v153\n"
"v_lshlrev_b32 v152, 16, v153\n"
"v_add_f32_dpp v188, v188, v188 row_mirror row_mask:0xf bank_mask:0xf\n"
"v_and_b32 v153, 0xffff0000, v153\n"
"v_cvt_pk_bf16_f32 v155, v154, v155\n"
"v_mov_b32 v189, v188\n"
"v_lshlrev_b32 v154, 16, v155\n"
"v_and_b32 v155, 0xffff0000, v155\n"
"v_permlane16_swap_b32 v189, v188\n"
"v_cvt_pk_bf16_f32 v157, v156, v157\n"
"v_lshlrev_b32 v156, 16, v157\n"
"v_add_f32 v188, v188, v189\n"
"v_and_b32 v157, 0xffff0000, v157\n"
"v_cvt_pk_bf16_f32 v159, v158, v159\n"
"v_sub_f32 v191, v191, v128\n"
"v_lshlrev_b32 v158, 16, v159\n"
"v_and_b32 v159, 0xffff0000, v159\n"
"v_mul_f32 v188, 0x3acccccd, v188\n"
"v_cvt_pk_bf16_f32 v161, v160, v161\n"
"s_add_u32 m0, s37, 12288\n"
"s_add_u32 s48, s47, 786432\n"
"s_and_b32 s48, s48, 4194303\n"
"buffer_load_dwordx4 v238, s[16:19], s48 offen offset:0 lds\n"
"buffer_load_dwordx4 v239, s[16:19], s48 offen offset:1024 lds\n"
"buffer_load_dwordx4 v240, s[16:19], s48 offen offset:2048 lds\n"
"buffer_load_dwordx4 v241, s[16:19], s48 offen offset:3072 lds\n"
"v_lshlrev_b32 v160, 16, v161\n"
"v_fma_f32 v188, -v191, v191, v188\n"
"v_and_b32 v161, 0xffff0000, v161\n"
"v_cvt_pk_bf16_f32 v163, v162, v163\n"
"v_max_f32 v188, 0, v188\n"
"v_lshlrev_b32 v162, 16, v163\n"
"v_and_b32 v163, 0xffff0000, v163\n"
"v_cvt_pk_bf16_f32 v188, v188, v188\n"
"v_cvt_pk_bf16_f32 v165, v164, v165\n"
"v_lshlrev_b32 v164, 16, v165\n"
"v_lshlrev_b32 v188, 16, v188\n"
"v_and_b32 v165, 0xffff0000, v165\n"
"v_cvt_pk_bf16_f32 v167, v166, v167\n"
"v_add_f32 v188, 0x3727c5ac, v188\n"
"v_lshlrev_b32 v166, 16, v167\n"
"v_and_b32 v167, 0xffff0000, v167\n"
"v_cvt_pk_bf16_f32 v188, v188, v188\n"
"v_bfe_u32 v130, v224, 5, 4\n"
"v_mul_u32_u24 v130, 144, v130\n"
"v_lshlrev_b32 v188, 16, v188\n"
"v_bfe_u32 v131, v224, 4, 1\n"
"v_lshl_add_u32 v130, v131, 3, v130\n"
"v_sqrt_f32 v189, v188\n"
"v_lshrrev_b32 v131, 9, v224\n"
"v_lshl_add_u32 v130, v131, 4, v130\n"
"v_rcp_f32 v190, v189\n"
"s_lshl_b32 s52, s30, 5\n"
"v_add_u32 v130, s52, v130\n"
"v_fma_f32 v188, -v189, v189, v188\n"
"v_mul_f32 v190, 0.5, v190\n"
"v_fma_f32 v189, v188, v190, v189\n"
"v_cvt_pk_bf16_f32 v189, v189, v189\n"
"v_lshlrev_b32 v189, 16, v189\n"
"v_rcp_f32 v129, v189\n"
"s_nop 0\n"
"v_mul_f32 v148, v148, v129\n"
"v_mul_f32 v149, v149, v129\n"
"v_cvt_pk_bf16_f32 v138, v148, v149\n"
"v_mul_f32 v150, v150, v129\n"
"v_mul_f32 v151, v151, v129\n"
"v_cvt_pk_bf16_f32 v139, v150, v151\n"
"ds_write_b64 v130, v[138:139] offset:49152\n"
"v_mul_f32 v152, v152, v129\n"
"v_mul_f32 v153, v153, v129\n"
"v_cvt_pk_bf16_f32 v140, v152, v153\n"
"v_mul_f32 v154, v154, v129\n"
"v_mul_f32 v155, v155, v129\n"
"v_cvt_pk_bf16_f32 v141, v154, v155\n"
"ds_write_b64 v130, v[140:141] offset:51456\n"
"v_mul_f32 v156, v156, v129\n"
"v_mul_f32 v157, v157, v129\n"
"v_cvt_pk_bf16_f32 v142, v156, v157\n"
"v_mul_f32 v158, v158, v129\n"
"v_mul_f32 v159, v159, v129\n"
"v_cvt_pk_bf16_f32 v143, v158, v159\n"
"ds_write_b64 v130, v[142:143] offset:53760\n"
"v_mul_f32 v160, v160, v129\n"
"v_mul_f32 v161, v161, v129\n"
"v_cvt_pk_bf16_f32 v144, v160, v161\n"
"v_mul_f32 v162, v162, v129\n"
"v_mul_f32 v163, v163, v129\n"
"v_cvt_pk_bf16_f32 v145, v162, v163\n"
"ds_write_b64 v130, v[144:145] offset:56064\n"
"v_mul_f32 v164, v164, v129\n"
"v_mul_f32 v165, v165, v129\n"
"v_cvt_pk_bf16_f32 v146, v164, v165\n"
"v_mul_f32 v166, v166, v129\n"
"v_mul_f32 v167, v167, v129\n"
"v_cvt_pk_bf16_f32 v147, v166, v167\n"
"ds_write_b64 v130, v[146:147] offset:58368\n"
"s_waitcnt vmcnt(20)\n"
"ds_read_b64_tr_b16 a[0:1], v246 offset:0\n"
"ds_read_b64_tr_b16 a[2:3], v246 offset:512\n"
"ds_read_b64_tr_b16 a[4:5], v247 offset:0\n"
"ds_read_b64_tr_b16 a[6:7], v247 offset:512\n"
"ds_read_b64_tr_b16 a[8:9], v248 offset:0\n"
"ds_read_b64_tr_b16 a[10:11], v248 offset:512\n"
"ds_read_b64_tr_b16 a[12:13], v249 offset:0\n"
"ds_read_b64_tr_b16 a[14:15], v249 offset:512\n"
"s_waitcnt vmcnt(12)\n"
"ds_read_b64_tr_b16 a[16:17], v246 offset:4096\n"
"ds_read_b64_tr_b16 a[18:19], v246 offset:4608\n"
"s_waitcnt lgkmcnt(10)\n"
"s_barrier\n"
"ds_read_b128 v[192:195], v242 offset:0\n"
"ds_read_b128 v[196:199], v242 offset:2048\n"
"ds_read_b128 v[200:203], v242 offset:4096\n"
"ds_read_b128 v[204:207], v242 offset:6144\n"
"ds_read_b128 v[208:211], v242 offset:8192\n"
"v_lshrrev_b32 v131, 7, v224\n"
"s_waitcnt lgkmcnt(4)\n"
"v_mfma_f32_16x16x32_bf16 v[0:3], v[192:195], a[0:3], 0\n"
"s_add_u32 m0, s37, 16384\n"
"ds_read_b128 v[212:215], v242 offset:10240\n"
"v_lshl_add_u32 v131, v131, 4, v224\n"
"v_mfma_f32_16x16x32_bf16 v[8:11], v[192:195], a[4:7], 0\n"
"s_add_u32 s48, s47, 1048576\n"
"s_and_b32 s48, s48, 4194303\n"
"s_mul_i32 s52, s30, 1152\n"
"v_mfma_f32_16x16x32_bf16 v[16:19], v[192:195], a[8:11], 0\n"
"ds_read_b64_tr_b16 a[20:21], v247 offset:4096\n"
"v_add_u32 v128, s52, v131\n"
"v_mfma_f32_16x16x32_bf16 v[24:27], v[192:195], a[12:15], 0\n"
"s_waitcnt lgkmcnt(5)\n"
"buffer_load_dwordx4 v238, s[16:19], s48 offen offset:0 lds\n"
"v_mfma_f32_16x16x32_bf16 v[4:7], v[196:199], a[0:3], 0\n"
"s_and_b32 s52, s30, 1\n"
"ds_read_b128 v[216:219], v242 offset:12288\n"
"v_lshlrev_b32 v148, 16, v138\n"
"v_mfma_f32_16x16x32_bf16 v[12:15], v[196:199], a[4:7], 0\n"
"s_mul_i32 s51, s52, 1152\n"
"v_and_b32 v149, 0xffff0000, v138\n"
"v_add_u32 v131, s51, v131\n"
"v_mfma_f32_16x16x32_bf16 v[20:23], v[196:199], a[8:11], 0\n"
"s_lshl_b32 s52, s52, 10\n"
"ds_read_b64_tr_b16 a[22:23], v247 offset:4608\n"
"v_mul_f32 v188, v148, v168\n"
"v_mfma_f32_16x16x32_bf16 v[28:31], v[196:199], a[12:15], 0\n"
"s_waitcnt lgkmcnt(3)\n"
"buffer_load_dwordx4 v239, s[16:19], s48 offen offset:1024 lds\n"
"v_mfma_f32_16x16x32_bf16 v[32:35], v[200:203], a[0:3], 0\n"
"ds_read_b128 v[220:223], v242 offset:14336\n"
"v_and_b32 v129, 0x70, v224\n"
"v_mfma_f32_16x16x32_bf16 v[40:43], v[200:203], a[4:7], 0\n"
"s_lshr_b32 s50, s31, 1\n"
"ds_read_b128 v[132:135], v128 offset:49152\n"
"v_mul_f32 v189, v149, v169\n"
"v_mfma_f32_16x16x32_bf16 v[48:51], v[200:203], a[8:11], 0\n"
"s_mul_i32 s50, s50, 20480\n"
"ds_read_b128 a[240:243], v128 offset:53760\n"
"v_and_b32 v130, 0x380, v224\n"
"v_mfma_f32_16x16x32_bf16 v[56:59], v[200:203], a[12:15], 0\n"
"s_and_b32 s51, s31, 1\n"
"ds_read_b128 a[244:247], v131 offset:58368\n"
"v_lshlrev_b32 v150, 16, v139\n"
"v_mfma_f32_16x16x32_bf16 v[36:39], v[204:207], a[0:3], 0\n"
"s_lshl_b32 s51, s51, 7\n"
"ds_read_b128 v[192:195], v243 offset:0\n"
"v_lshl_add_u32 v129, v130, 1, v129\n"
"v_mfma_f32_16x16x32_bf16 v[44:47], v[204:207], a[4:7], 0\n"
"s_add_u32 s50, s50, s51\n"
"ds_read_b64_tr_b16 a[24:25], v248 offset:4096\n"
"v_and_b32 v151, 0xffff0000, v139\n"
"v_mfma_f32_16x16x32_bf16 v[52:55], v[204:207], a[8:11], 0\n"
"s_add_u32 s50, s50, 65536\n"
"buffer_load_dwordx4 v240, s[16:19], s48 offen offset:2048 lds\n"
"v_mfma_f32_16x16x32_bf16 v[60:63], v[204:207], a[12:15], 0\n"
"s_lshl_b32 s52, s52, 1\n"
"ds_read_b128 v[196:199], v243 offset:2048\n"
"v_fmac_f32 v188, v150, v170\n"
"v_mfma_f32_16x16x32_bf16 v[64:67], v[208:211], a[0:3], 0\n"
"s_add_u32 s52, s52, s50\n"
"ds_read_b64_tr_b16 a[26:27], v248 offset:4608\n"
"v_fmac_f32 v189, v151, v171\n"
"v_mfma_f32_16x16x32_bf16 v[72:75], v[208:211], a[4:7], 0\n"
"s_add_u32 s52, s52, 16384\n"
"buffer_load_dwordx4 v241, s[16:19], s48 offen offset:3072 lds\n"
"v_mfma_f32_16x16x32_bf16 v[80:83], v[208:211], a[8:11], 0\n"
"s_add_u32 m0, s36, 32768\n"
"ds_read_b128 v[200:203], v243 offset:4096\n"
"v_lshlrev_b32 v152, 16, v140\n"
"v_mfma_f32_16x16x32_bf16 v[88:91], v[208:211], a[12:15], 0\n"
"s_add_u32 s48, s46, 256\n"
"ds_read_b64_tr_b16 a[28:29], v249 offset:4096\n"
"v_and_b32 v153, 0xffff0000, v140\n"
"v_mfma_f32_16x16x32_bf16 v[68:71], v[212:215], a[0:3], 0\n"
"s_and_b32 s48, s48, 1023\n"
"v_fmac_f32 v188, v152, v172\n"
"v_fmac_f32 v189, v153, v173\n"
"v_mfma_f32_16x16x32_bf16 v[76:79], v[212:215], a[4:7], 0\n"
"s_lshl_b32 s51, s30, 11\n"
"buffer_load_dwordx4 v234, s[12:15], s48 offen offset:0 lds\n"
"v_mfma_f32_16x16x32_bf16 v[84:87], v[212:215], a[8:11], 0\n"
"s_add_u32 s50, s50, s51\n"
"ds_read_b128 v[204:207], v243 offset:6144\n"
"v_lshlrev_b32 v154, 16, v141\n"
"v_mfma_f32_16x16x32_bf16 v[92:95], v[212:215], a[12:15], 0\n"
"s_waitcnt lgkmcnt(3)\n"
"ds_read_b64_tr_b16 a[30:31], v249 offset:4608\n"
"v_and_b32 v155, 0xffff0000, v141\n"
"v_mfma_f32_16x16x32_bf16 v[96:99], v[216:219], a[0:3], 0\n"
"s_add_u32 s51, s50, 8192\n"
"buffer_store_dwordx4 v[132:135], v129, s[8:11], s50 offen sc1\n"
"v_mfma_f32_16x16x32_bf16 v[104:107], v[216:219], a[4:7], 0\n"
"ds_read_b128 v[208:211], v243 offset:8192\n"
"v_fmac_f32 v188, v154, v174\n"
"v_mfma_f32_16x16x32_bf16 v[112:115], v[216:219], a[8:11], 0\n"
"buffer_load_dwordx4 v235, s[12:15], s48 offen offset:1024 lds\n"
"v_mfma_f32_16x16x32_bf16 v[120:123], v[216:219], a[12:15], 0\n"
"buffer_store_dwordx4 a[240:243], v129, s[8:11], s51 offen sc1\n"
"v_mfma_f32_16x16x32_bf16 v[100:103], v[220:223], a[0:3], 0\n"
"s_waitcnt vmcnt(16)\n"
"ds_read_b64_tr_b16 a[32:33], v246 offset:8192\n"
"v_fmac_f32 v189, v155, v175\n"
"v_mfma_f32_16x16x32_bf16 v[108:111], v[220:223], a[4:7], 0\n"
"buffer_store_dwordx4 a[244:247], v129, s[8:11], s52 offen sc1\n"
"v_mfma_f32_16x16x32_bf16 v[116:119], v[220:223], a[8:11], 0\n"
"ds_read_b128 v[212:215], v243 offset:10240\n"
"v_lshlrev_b32 v156, 16, v142\n"
"v_mfma_f32_16x16x32_bf16 v[124:127], v[220:223], a[12:15], 0\n"
"buffer_load_dwordx4 v236, s[12:15], s48 offen offset:2048 lds\n"
"v_mfma_f32_16x16x32_bf16 v[0:3], v[192:195], a[16:19], v[0:3]\n"
"ds_read_b64_tr_b16 a[34:35], v246 offset:8704\n"
"v_and_b32 v157, 0xffff0000, v142\n"
"v_mfma_f32_16x16x32_bf16 v[8:11], v[192:195], a[20:23], v[8:11]\n"
"ds_read_b128 v[216:219], v243 offset:12288\n"
"v_fmac_f32 v188, v156, v176\n"
"v_mfma_f32_16x16x32_bf16 v[16:19], v[192:195], a[24:27], v[16:19]\n"
"s_waitcnt lgkmcnt(4)\n"
"buffer_load_dwordx4 v237, s[12:15], s48 offen offset:3072 lds\n"
"v_mfma_f32_16x16x32_bf16 v[24:27], v[192:195], a[28:31], v[24:27]\n"
"ds_read_b64_tr_b16 a[36:37], v247 offset:8192\n"
"v_fmac_f32 v189, v157, v177\n"
"v_mfma_f32_16x16x32_bf16 v[4:7], v[196:199], a[16:19], v[4:7]\n"
"s_add_u32 m0, s37, 20480\n"
"ds_read_b128 v[220:223], v243 offset:14336\n"
"v_lshlrev_b32 v158, 16, v143\n"
"v_mfma_f32_16x16x32_bf16 v[12:15], v[196:199], a[20:23], v[12:15]\n"
"s_add_u32 s48, s47, 1310720\n"
"v_and_b32 v159, 0xffff0000, v143\n"
"v_fmac_f32 v188, v158, v178\n"
"v_mfma_f32_16x16x32_bf16 v[20:23], v[196:199], a[24:27], v[20:23]\n"
"s_and_b32 s48, s48, 4194303\n"
"ds_read_b64_tr_b16 a[38:39], v247 offset:8704\n"
"v_fmac_f32 v189, v159, v179\n"
"v_mfma_f32_16x16x32_bf16 v[28:31], v[196:199], a[28:31], v[28:31]\n"
"buffer_load_dwordx4 v238, s[16:19], s48 offen offset:0 lds\n"
"v_mfma_f32_16x16x32_bf16 v[32:35], v[200:203], a[16:19], v[32:35]\n"
"v_lshlrev_b32 v160, 16, v144\n"
"v_and_b32 v161, 0xffff0000, v144\n"
"v_mfma_f32_16x16x32_bf16 v[40:43], v[200:203], a[20:23], v[40:43]\n"
"v_fmac_f32 v188, v160, v180\n"
"v_fmac_f32 v189, v161, v181\n"
"v_mfma_f32_16x16x32_bf16 v[48:51], v[200:203], a[24:27], v[48:51]\n"
"ds_read_b64_tr_b16 a[40:41], v248 offset:8192\n"
"v_lshlrev_b32 v162, 16, v145\n"
"v_mfma_f32_16x16x32_bf16 v[56:59], v[200:203], a[28:31], v[56:59]\n"
"buffer_load_dwordx4 v239, s[16:19], s48 offen offset:1024 lds\n"
"v_mfma_f32_16x16x32_bf16 v[36:39], v[204:207], a[16:19], v[36:39]\n"
"v_and_b32 v163, 0xffff0000, v145\n"
"v_fmac_f32 v188, v162, v182\n"
"v_mfma_f32_16x16x32_bf16 v[44:47], v[204:207], a[20:23], v[44:47]\n"
"s_waitcnt vmcnt(17) lgkmcnt(2)\n"
"s_barrier\n"
"v_mfma_f32_16x16x32_bf16 v[52:55], v[204:207], a[24:27], v[52:55]\n"
"ds_read_b128 v[192:195], v242 offset:16384\n"
"v_fmac_f32 v189, v163, v183\n"
"v_mfma_f32_16x16x32_bf16 v[60:63], v[204:207], a[28:31], v[60:63]\n"
"ds_read_b64_tr_b16 a[42:43], v248 offset:8704\n"
"v_lshlrev_b32 v164, 16, v146\n"
"v_mfma_f32_16x16x32_bf16 v[64:67], v[208:211], a[16:19], v[64:67]\n"
"buffer_load_dwordx4 v240, s[16:19], s48 offen offset:2048 lds\n"
"v_mfma_f32_16x16x32_bf16 v[72:75], v[208:211], a[20:23], v[72:75]\n"
"v_and_b32 v165, 0xffff0000, v146\n"
"v_fmac_f32 v188, v164, v184\n"
"v_mfma_f32_16x16x32_bf16 v[80:83], v[208:211], a[24:27], v[80:83]\n"
"ds_read_b128 v[196:199], v242 offset:18432\n"
"v_fmac_f32 v189, v165, v185\n"
"v_mfma_f32_16x16x32_bf16 v[88:91], v[208:211], a[28:31], v[88:91]\n"
"ds_read_b64_tr_b16 a[44:45], v249 offset:8192\n"
"v_lshlrev_b32 v166, 16, v147\n"
"v_mfma_f32_16x16x32_bf16 v[68:71], v[212:215], a[16:19], v[68:71]\n"
"s_lshl_b32 s90, s31, 2\n"
"buffer_load_dwordx4 v241, s[16:19], s48 offen offset:3072 lds\n"
"v_mfma_f32_16x16x32_bf16 v[76:79], v[212:215], a[20:23], v[76:79]\n"
"s_add_u32 s90, s90, s30\n"
"ds_read_b128 v[200:203], v242 offset:20480\n"
"v_and_b32 v167, 0xffff0000, v147\n"
"v_mfma_f32_16x16x32_bf16 v[84:87], v[212:215], a[24:27], v[84:87]\n"
"s_add_u32 m0, s37, 0\n"
"ds_read_b64_tr_b16 a[46:47], v249 offset:8704\n"
"v_fmac_f32 v188, v166, v186\n"
"v_mfma_f32_16x16x32_bf16 v[92:95], v[212:215], a[28:31], v[92:95]\n"
"s_add_u32 s48, s47, 1572864\n"
"v_fmac_f32 v189, v167, v187\n"
"v_add_f32 v188, v188, v189\n"
"v_mfma_f32_16x16x32_bf16 v[96:99], v[216:219], a[16:19], v[96:99]\n"
"s_and_b32 s48, s48, 4194303\n"
"v_add_f32_dpp v188, v188, v188 quad_perm:[1,0,3,2] row_mask:0xf bank_mask:0xf\n"
"s_lshl_b32 s90, s90, 1\n"
"v_mfma_f32_16x16x32_bf16 v[104:107], v[216:219], a[20:23], v[104:107]\n"
"s_sub_u32 s91, 2047, s90\n"
"buffer_load_dwordx4 v238, s[16:19], s48 offen offset:0 lds\n"
"v_mfma_f32_16x16x32_bf16 v[112:115], v[216:219], a[24:27], v[112:115]\n"
"ds_read_b128 v[204:207], v242 offset:22528\n"
"v_add_f32_dpp v188, v188, v188 quad_perm:[2,3,0,1] row_mask:0xf bank_mask:0xf\n"
"v_mfma_f32_16x16x32_bf16 v[120:123], v[216:219], a[28:31], v[120:123]\n"
"s_waitcnt vmcnt(16)\n"
"ds_read_b64_tr_b16 a[48:49], v246 offset:12288\n"
"v_add_f32_dpp v188, v188, v188 row_half_mirror row_mask:0xf bank_mask:0xf\n"
"v_mfma_f32_16x16x32_bf16 v[100:103], v[220:223], a[16:19], v[100:103]\n"
"ds_read_b128 v[208:211], v242 offset:24576\n"
"v_add_f32_dpp v188, v188, v188 row_mirror row_mask:0xf bank_mask:0xf\n"
"v_mfma_f32_16x16x32_bf16 v[108:111], v[220:223], a[20:23], v[108:111]\n"
"s_lshl_b32 s90, s90, 2\n"
"buffer_load_dwordx4 v239, s[16:19], s48 offen offset:1024 lds\n"
"v_mfma_f32_16x16x32_bf16 v[116:119], v[220:223], a[24:27], v[116:119]\n"
"s_add_u32 s90, s90, 2686976\n"
"ds_read_b64_tr_b16 a[50:51], v246 offset:12800\n"
"v_mov_b32 v189, v188\n"
"v_mfma_f32_16x16x32_bf16 v[124:127], v[220:223], a[28:31], v[124:127]\n"
"s_waitcnt lgkmcnt(4)\n"
"v_permlane16_swap_b32 v189, v188\n"
"v_add_f32 v188, v188, v189\n"
"v_mfma_f32_16x16x32_bf16 v[0:3], v[192:195], a[32:35], v[0:3]\n"
"ds_read_b128 v[212:215], v242 offset:26624\n"
"v_cvt_pk_bf16_f32 v188, v188, v188\n"
"v_mfma_f32_16x16x32_bf16 v[8:11], v[192:195], a[36:39], v[8:11]\n"
"buffer_load_dwordx4 v240, s[16:19], s48 offen offset:2048 lds\n"
"v_mfma_f32_16x16x32_bf16 v[16:19], v[192:195], a[40:43], v[16:19]\n"
"ds_read_b64_tr_b16 a[52:53], v247 offset:12288\n"
"v_lshlrev_b32 v188, 16, v188\n"
"v_mfma_f32_16x16x32_bf16 v[24:27], v[192:195], a[44:47], v[24:27]\n"
"ds_read_b128 v[216:219], v242 offset:28672\n"
"v_ashrrev_i32 v189, 31, v188\n"
"v_mfma_f32_16x16x32_bf16 v[4:7], v[196:199], a[32:35], v[4:7]\n"
"v_or_b32 v189, 0x80000000, v189\n"
"v_xor_b32 v188, v188, v189\n"
"v_mfma_f32_16x16x32_bf16 v[12:15], v[196:199], a[36:39], v[12:15]\n"
"buffer_load_dwordx4 v241, s[16:19], s48 offen offset:3072 lds\n"
"v_mfma_f32_16x16x32_bf16 v[20:23], v[196:199], a[40:43], v[20:23]\n"
"ds_read_b64_tr_b16 a[54:55], v247 offset:12800\n"
"v_and_b32 v188, 0xffff0000, v188\n"
"v_mfma_f32_16x16x32_bf16 v[28:31], v[196:199], a[44:47], v[28:31]\n"
"ds_read_b128 v[220:223], v242 offset:30720\n"
"v_lshrrev_b32 v189, 9, v224\n"
"v_mfma_f32_16x16x32_bf16 v[32:35], v[200:203], a[32:35], v[32:35]\n"
"s_add_u32 m0, s36, 0\n"
"v_sub_u32 v190, s91, v189\n"
"v_or_b32 v188, v188, v190\n"
"v_mfma_f32_16x16x32_bf16 v[40:43], v[200:203], a[36:39], v[40:43]\n"
"s_add_u32 s48, s46, 384\n"
"v_and_b32 v190, 0x1f0, v224\n"
"v_lshlrev_b32 v190, 23, v190\n"
"v_mfma_f32_16x16x32_bf16 v[48:51], v[200:203], a[40:43], v[48:51]\n"
"s_and_b32 s48, s48, 1023\n"
"ds_read_b128 v[192:195], v243 offset:16384\n"
"v_lshl_add_u32 v190, v189, 2, v190\n"
"v_mfma_f32_16x16x32_bf16 v[56:59], v[200:203], a[44:47], v[56:59]\n"
"s_waitcnt lgkmcnt(3)\n"
"buffer_store_dword v188, v190, s[8:11], s90 offen sc1\n"
"v_mfma_f32_16x16x32_bf16 v[36:39], v[204:207], a[32:35], v[36:39]\n"
"ds_read_b64_tr_b16 a[56:57], v248 offset:12288\n"
"v_mfma_f32_16x16x32_bf16 v[44:47], v[204:207], a[36:39], v[44:47]\n"
"buffer_load_dwordx4 v234, s[12:15], s48 offen offset:0 lds\n"
"v_mfma_f32_16x16x32_bf16 v[52:55], v[204:207], a[40:43], v[52:55]\n"
"ds_read_b128 v[196:199], v243 offset:18432\n"
"v_mfma_f32_16x16x32_bf16 v[60:63], v[204:207], a[44:47], v[60:63]\n"
"ds_read_b64_tr_b16 a[58:59], v248 offset:12800\n"
"v_mfma_f32_16x16x32_bf16 v[64:67], v[208:211], a[32:35], v[64:67]\n"
"buffer_load_dwordx4 v235, s[12:15], s48 offen offset:1024 lds\n"
"v_mfma_f32_16x16x32_bf16 v[72:75], v[208:211], a[36:39], v[72:75]\n"
"ds_read_b128 v[200:203], v243 offset:20480\n"
"v_mfma_f32_16x16x32_bf16 v[80:83], v[208:211], a[40:43], v[80:83]\n"
"ds_read_b64_tr_b16 a[60:61], v249 offset:12288\n"
"v_mfma_f32_16x16x32_bf16 v[88:91], v[208:211], a[44:47], v[88:91]\n"
"buffer_load_dwordx4 v236, s[12:15], s48 offen offset:2048 lds\n"
"v_mfma_f32_16x16x32_bf16 v[68:71], v[212:215], a[32:35], v[68:71]\n"
"v_mfma_f32_16x16x32_bf16 v[76:79], v[212:215], a[36:39], v[76:79]\n"
"ds_read_b128 v[204:207], v243 offset:22528\n"
"v_mfma_f32_16x16x32_bf16 v[84:87], v[212:215], a[40:43], v[84:87]\n"
"ds_read_b64_tr_b16 a[62:63], v249 offset:12800\n"
"v_mfma_f32_16x16x32_bf16 v[92:95], v[212:215], a[44:47], v[92:95]\n"
"buffer_load_dwordx4 v237, s[12:15], s48 offen offset:3072 lds\n"
"v_mfma_f32_16x16x32_bf16 v[96:99], v[216:219], a[32:35], v[96:99]\n"
"ds_read_b128 v[208:211], v243 offset:24576\n"
"v_mfma_f32_16x16x32_bf16 v[104:107], v[216:219], a[36:39], v[104:107]\n"
"v_mfma_f32_16x16x32_bf16 v[112:115], v[216:219], a[40:43], v[112:115]\n"
"s_add_u32 m0, s37, 4096\n"
"s_waitcnt vmcnt(20)\n"
"ds_read_b64_tr_b16 a[64:65], v246 offset:16384\n"
"v_mfma_f32_16x16x32_bf16 v[120:123], v[216:219], a[44:47], v[120:123]\n"
"s_waitcnt lgkmcnt(4)\n"
"s_add_u32 s48, s47, 1835008\n"
"s_and_b32 s48, s48, 4194303\n"
"v_mfma_f32_16x16x32_bf16 v[100:103], v[220:223], a[32:35], v[100:103]\n"
"ds_read_b128 v[212:215], v243 offset:26624\n"
"v_mfma_f32_16x16x32_bf16 v[108:111], v[220:223], a[36:39], v[108:111]\n"
"buffer_load_dwordx4 v238, s[16:19], s48 offen offset:0 lds\n"
"v_mfma_f32_16x16x32_bf16 v[116:119], v[220:223], a[40:43], v[116:119]\n"
"ds_read_b64_tr_b16 a[66:67], v246 offset:16896\n"
"v_mfma_f32_16x16x32_bf16 v[124:127], v[220:223], a[44:47], v[124:127]\n"
"ds_read_b128 v[216:219], v243 offset:28672\n"
"v_mfma_f32_16x16x32_bf16 v[0:3], v[192:195], a[48:51], v[0:3]\n"
"buffer_load_dwordx4 v239, s[16:19], s48 offen offset:1024 lds\n"
"v_mfma_f32_16x16x32_bf16 v[8:11], v[192:195], a[52:55], v[8:11]\n"
"v_mfma_f32_16x16x32_bf16 v[16:19], v[192:195], a[56:59], v[16:19]\n"
"s_waitcnt lgkmcnt(3)\n"
"ds_read_b64_tr_b16 a[68:69], v247 offset:16384\n"
"v_mfma_f32_16x16x32_bf16 v[24:27], v[192:195], a[60:63], v[24:27]\n"
"ds_read_b128 v[220:223], v243 offset:30720\n"
"v_mfma_f32_16x16x32_bf16 v[4:7], v[196:199], a[48:51], v[4:7]\n"
"v_mfma_f32_16x16x32_bf16 v[12:15], v[196:199], a[52:55], v[12:15]\n"
"buffer_load_dwordx4 v240, s[16:19], s48 offen offset:2048 lds\n"
"v_mfma_f32_16x16x32_bf16 v[20:23], v[196:199], a[56:59], v[20:23]\n"
"ds_read_b64_tr_b16 a[70:71], v247 offset:16896\n"
"v_mfma_f32_16x16x32_bf16 v[28:31], v[196:199], a[60:63], v[28:31]\n"
"v_mfma_f32_16x16x32_bf16 v[32:35], v[200:203], a[48:51], v[32:35]\n"
"v_mfma_f32_16x16x32_bf16 v[40:43], v[200:203], a[52:55], v[40:43]\n"
"buffer_load_dwordx4 v241, s[16:19], s48 offen offset:3072 lds\n"
"v_mfma_f32_16x16x32_bf16 v[48:51], v[200:203], a[56:59], v[48:51]\n"
"ds_read_b64_tr_b16 a[72:73], v248 offset:16384\n"
"v_mfma_f32_16x16x32_bf16 v[56:59], v[200:203], a[60:63], v[56:59]\n"
"v_mfma_f32_16x16x32_bf16 v[36:39], v[204:207], a[48:51], v[36:39]\n"
"s_add_u32 m0, s37, 8192\n"
"s_add_u32 s48, s47, 2097152\n"
"s_and_b32 s48, s48, 4194303\n"
"v_mfma_f32_16x16x32_bf16 v[44:47], v[204:207], a[52:55], v[44:47]\n"
"s_waitcnt vmcnt(17) lgkmcnt(2)\n"
"s_barrier\n"
"v_mfma_f32_16x16x32_bf16 v[52:55], v[204:207], a[56:59], v[52:55]\n"
"ds_read_b128 v[192:195], v242 offset:32768\n"
"v_mfma_f32_16x16x32_bf16 v[60:63], v[204:207], a[60:63], v[60:63]\n"
"ds_read_b64_tr_b16 a[74:75], v248 offset:16896\n"
"v_mfma_f32_16x16x32_bf16 v[64:67], v[208:211], a[48:51], v[64:67]\n"
"buffer_load_dwordx4 v238, s[16:19], s48 offen offset:0 lds\n"
"v_mfma_f32_16x16x32_bf16 v[72:75], v[208:211], a[52:55], v[72:75]\n"
"v_mfma_f32_16x16x32_bf16 v[80:83], v[208:211], a[56:59], v[80:83]\n"
"ds_read_b128 v[196:199], v242 offset:34816\n"
"v_mfma_f32_16x16x32_bf16 v[88:91], v[208:211], a[60:63], v[88:91]\n"
"ds_read_b64_tr_b16 a[76:77], v249 offset:16384\n"
"v_mfma_f32_16x16x32_bf16 v[68:71], v[212:215], a[48:51], v[68:71]\n"
"buffer_load_dwordx4 v239, s[16:19], s48 offen offset:1024 lds\n"
"v_mfma_f32_16x16x32_bf16 v[76:79], v[212:215], a[52:55], v[76:79]\n"
"ds_read_b128 v[200:203], v242 offset:36864\n"
"v_mfma_f32_16x16x32_bf16 v[84:87], v[212:215], a[56:59], v[84:87]\n"
"ds_read_b64_tr_b16 a[78:79], v249 offset:16896\n"
"v_mfma_f32_16x16x32_bf16 v[92:95], v[212:215], a[60:63], v[92:95]\n"
"buffer_load_dwordx4 v240, s[16:19], s48 offen offset:2048 lds\n"
"v_mfma_f32_16x16x32_bf16 v[96:99], v[216:219], a[48:51], v[96:99]\n"
"v_mfma_f32_16x16x32_bf16 v[104:107], v[216:219], a[52:55], v[104:107]\n"
"ds_read_b128 v[204:207], v242 offset:38912\n"
"v_mfma_f32_16x16x32_bf16 v[112:115], v[216:219], a[56:59], v[112:115]\n"
"s_waitcnt vmcnt(16)\n"
"ds_read_b64_tr_b16 a[80:81], v246 offset:20480\n"
"v_mfma_f32_16x16x32_bf16 v[120:123], v[216:219], a[60:63], v[120:123]\n"
"buffer_load_dwordx4 v241, s[16:19], s48 offen offset:3072 lds\n"
"v_mfma_f32_16x16x32_bf16 v[100:103], v[220:223], a[48:51], v[100:103]\n"
"ds_read_b128 v[208:211], v242 offset:40960\n"
"v_mfma_f32_16x16x32_bf16 v[108:111], v[220:223], a[52:55], v[108:111]\n"
"v_mfma_f32_16x16x32_bf16 v[116:119], v[220:223], a[56:59], v[116:119]\n"
"s_add_u32 m0, s36, 16384\n"
"ds_read_b64_tr_b16 a[82:83], v246 offset:20992\n"
"s_add_u32 s48, s46, 512\n"
"v_mfma_f32_16x16x32_bf16 v[124:127], v[220:223], a[60:63], v[124:127]\n"
"s_waitcnt lgkmcnt(4)\n"
"s_and_b32 s48, s48, 1023\n"
"v_mfma_f32_16x16x32_bf16 v[0:3], v[192:195], a[64:67], v[0:3]\n"
"ds_read_b128 v[212:215], v242 offset:43008\n"
"v_mfma_f32_16x16x32_bf16 v[8:11], v[192:195], a[68:71], v[8:11]\n"
"buffer_load_dwordx4 v234, s[12:15], s48 offen offset:0 lds\n"
"v_mfma_f32_16x16x32_bf16 v[16:19], v[192:195], a[72:75], v[16:19]\n"
"ds_read_b64_tr_b16 a[84:85], v247 offset:20480\n"
"v_mfma_f32_16x16x32_bf16 v[24:27], v[192:195], a[76:79], v[24:27]\n"
"ds_read_b128 v[216:219], v242 offset:45056\n"
"v_mfma_f32_16x16x32_bf16 v[4:7], v[196:199], a[64:67], v[4:7]\n"
"buffer_load_dwordx4 v235, s[12:15], s48 offen offset:1024 lds\n"
"v_mfma_f32_16x16x32_bf16 v[12:15], v[196:199], a[68:71], v[12:15]\n"
"v_mfma_f32_16x16x32_bf16 v[20:23], v[196:199], a[72:75], v[20:23]\n"
"ds_read_b64_tr_b16 a[86:87], v247 offset:20992\n"
"v_mfma_f32_16x16x32_bf16 v[28:31], v[196:199], a[76:79], v[28:31]\n"
"ds_read_b128 v[220:223], v242 offset:47104\n"
"v_mfma_f32_16x16x32_bf16 v[32:35], v[200:203], a[64:67], v[32:35]\n"
"v_mfma_f32_16x16x32_bf16 v[40:43], v[200:203], a[68:71], v[40:43]\n"
"buffer_load_dwordx4 v236, s[12:15], s48 offen offset:2048 lds\n"
"v_mfma_f32_16x16x32_bf16 v[48:51], v[200:203], a[72:75], v[48:51]\n"
"ds_read_b128 v[192:195], v243 offset:32768\n"
"v_mfma_f32_16x16x32_bf16 v[56:59], v[200:203], a[76:79], v[56:59]\n"
"s_waitcnt lgkmcnt(3)\n"
"ds_read_b64_tr_b16 a[88:89], v248 offset:20480\n"
"v_mfma_f32_16x16x32_bf16 v[36:39], v[204:207], a[64:67], v[36:39]\n"
"v_mfma_f32_16x16x32_bf16 v[44:47], v[204:207], a[68:71], v[44:47]\n"
"buffer_load_dwordx4 v237, s[12:15], s48 offen offset:3072 lds\n"
"v_mfma_f32_16x16x32_bf16 v[52:55], v[204:207], a[72:75], v[52:55]\n"
"ds_read_b128 v[196:199], v243 offset:34816\n"
"v_mfma_f32_16x16x32_bf16 v[60:63], v[204:207], a[76:79], v[60:63]\n"
"ds_read_b64_tr_b16 a[90:91], v248 offset:20992\n"
"v_mfma_f32_16x16x32_bf16 v[64:67], v[208:211], a[64:67], v[64:67]\n"
"s_add_u32 m0, s37, 12288\n"
"s_add_u32 s48, s47, 2359296\n"
"s_and_b32 s48, s48, 4194303\n"
"v_mfma_f32_16x16x32_bf16 v[72:75], v[208:211], a[68:71], v[72:75]\n"
"ds_read_b128 v[200:203], v243 offset:36864\n"
"v_mfma_f32_16x16x32_bf16 v[80:83], v[208:211], a[72:75], v[80:83]\n"
"ds_read_b64_tr_b16 a[92:93], v249 offset:20480\n"
"v_mfma_f32_16x16x32_bf16 v[88:91], v[208:211], a[76:79], v[88:91]\n"
"buffer_load_dwordx4 v238, s[16:19], s48 offen offset:0 lds\n"
"v_mfma_f32_16x16x32_bf16 v[68:71], v[212:215], a[64:67], v[68:71]\n"
"v_mfma_f32_16x16x32_bf16 v[76:79], v[212:215], a[68:71], v[76:79]\n"
"ds_read_b128 v[204:207], v243 offset:38912\n"
"v_mfma_f32_16x16x32_bf16 v[84:87], v[212:215], a[72:75], v[84:87]\n"
"ds_read_b64_tr_b16 a[94:95], v249 offset:20992\n"
"v_mfma_f32_16x16x32_bf16 v[92:95], v[212:215], a[76:79], v[92:95]\n"
"buffer_load_dwordx4 v239, s[16:19], s48 offen offset:1024 lds\n"
"v_mfma_f32_16x16x32_bf16 v[96:99], v[216:219], a[64:67], v[96:99]\n"
"ds_read_b128 v[208:211], v243 offset:40960\n"
"v_mfma_f32_16x16x32_bf16 v[104:107], v[216:219], a[68:71], v[104:107]\n"
"v_mfma_f32_16x16x32_bf16 v[112:115], v[216:219], a[72:75], v[112:115]\n"
"s_waitcnt vmcnt(19)\n"
"ds_read_b64_tr_b16 a[96:97], v246 offset:0\n"
"v_mfma_f32_16x16x32_bf16 v[120:123], v[216:219], a[76:79], v[120:123]\n"
"s_waitcnt lgkmcnt(4)\n"
"buffer_load_dwordx4 v240, s[16:19], s48 offen offset:2048 lds\n"
"v_mfma_f32_16x16x32_bf16 v[100:103], v[220:223], a[64:67], v[100:103]\n"
"ds_read_b128 v[212:215], v243 offset:43008\n"
"v_mfma_f32_16x16x32_bf16 v[108:111], v[220:223], a[68:71], v[108:111]\n"
"v_mfma_f32_16x16x32_bf16 v[116:119], v[220:223], a[72:75], v[116:119]\n"
"ds_read_b64_tr_b16 a[98:99], v246 offset:512\n"
"v_mfma_f32_16x16x32_bf16 v[124:127], v[220:223], a[76:79], v[124:127]\n"
"ds_read_b128 v[216:219], v243 offset:45056\n"
"v_mfma_f32_16x16x32_bf16 v[0:3], v[192:195], a[80:83], v[0:3]\n"
"buffer_load_dwordx4 v241, s[16:19], s48 offen offset:3072 lds\n"
"v_mfma_f32_16x16x32_bf16 v[8:11], v[192:195], a[84:87], v[8:11]\n"
"v_mfma_f32_16x16x32_bf16 v[16:19], v[192:195], a[88:91], v[16:19]\n"
"s_waitcnt lgkmcnt(3)\n"
"ds_read_b64_tr_b16 a[100:101], v247 offset:0\n"
"s_add_u32 m0, s37, 16384\n"
"v_mfma_f32_16x16x32_bf16 v[24:27], v[192:195], a[92:95], v[24:27]\n"
"s_add_u32 s48, s47, 2621440\n"
"ds_read_b128 v[220:223], v243 offset:47104\n"
"s_and_b32 s48, s48, 4194303\n"
"v_mfma_f32_16x16x32_bf16 v[4:7], v[196:199], a[80:83], v[4:7]\n"
"buffer_load_dwordx4 v238, s[16:19], s48 offen offset:0 lds\n"
"v_mfma_f32_16x16x32_bf16 v[12:15], v[196:199], a[84:87], v[12:15]\n"
"v_mfma_f32_16x16x32_bf16 v[20:23], v[196:199], a[88:91], v[20:23]\n"
"ds_read_b64_tr_b16 a[102:103], v247 offset:512\n"
"v_mfma_f32_16x16x32_bf16 v[28:31], v[196:199], a[92:95], v[28:31]\n"
"v_mfma_f32_16x16x32_bf16 v[32:35], v[200:203], a[80:83], v[32:35]\n"
"s_lshl_b32 s50, s31, 3\n"
"buffer_load_dwordx4 v239, s[16:19], s48 offen offset:1024 lds\n"
"v_mfma_f32_16x16x32_bf16 v[40:43], v[200:203], a[84:87], v[40:43]\n"
"s_add_u32 s50, s50, 256\n"
"s_add_u32 s50, s50, s45\n"
"v_mfma_f32_16x16x32_bf16 v[48:51], v[200:203], a[88:91], v[48:51]\n"
"ds_read_b64_tr_b16 a[104:105], v248 offset:0\n"
"v_mfma_f32_16x16x32_bf16 v[56:59], v[200:203], a[92:95], v[56:59]\n"
"v_mfma_f32_16x16x32_bf16 v[36:39], v[204:207], a[80:83], v[36:39]\n"
"v_mfma_f32_16x16x32_bf16 v[44:47], v[204:207], a[84:87], v[44:47]\n"
"s_waitcnt vmcnt(18) lgkmcnt(2)\n"
"s_barrier\n"
"v_mfma_f32_16x16x32_bf16 v[52:55], v[204:207], a[88:91], v[52:55]\n"
"buffer_load_dwordx4 v240, s[16:19], s48 offen offset:2048 lds\n"
"v_mfma_f32_16x16x32_bf16 v[60:63], v[204:207], a[92:95], v[60:63]\n"
"ds_read_b128 v[192:195], v242 offset:0\n"
"v_mfma_f32_16x16x32_bf16 v[64:67], v[208:211], a[80:83], v[64:67]\n"
"ds_read_b64_tr_b16 a[106:107], v248 offset:512\n"
"v_mfma_f32_16x16x32_bf16 v[72:75], v[208:211], a[84:87], v[72:75]\n"
"buffer_store_dwordx2 v[232:233], v230, s[8:11], s50 offen sc1\n"
"v_mfma_f32_16x16x32_bf16 v[80:83], v[208:211], a[88:91], v[80:83]\n"
"buffer_load_dwordx4 v241, s[16:19], s48 offen offset:3072 lds\n"
"v_mfma_f32_16x16x32_bf16 v[88:91], v[208:211], a[92:95], v[88:91]\n"
"ds_read_b128 v[196:199], v242 offset:2048\n"
"v_mfma_f32_16x16x32_bf16 v[68:71], v[212:215], a[80:83], v[68:71]\n"
"s_add_u32 m0, s36, 32768\n"
"ds_read_b64_tr_b16 a[108:109], v249 offset:0\n"
"s_add_u32 s48, s46, 640\n"
"v_mfma_f32_16x16x32_bf16 v[76:79], v[212:215], a[84:87], v[76:79]\n"
"s_and_b32 s48, s48, 1023\n"
"ds_read_b128 v[200:203], v242 offset:4096\n"
"v_mfma_f32_16x16x32_bf16 v[84:87], v[212:215], a[88:91], v[84:87]\n"
"ds_read_b64_tr_b16 a[110:111], v249 offset:512\n"
"v_mfma_f32_16x16x32_bf16 v[92:95], v[212:215], a[92:95], v[92:95]\n"
"buffer_load_dwordx4 v234, s[12:15], s48 offen offset:0 lds\n"
"v_mfma_f32_16x16x32_bf16 v[96:99], v[216:219], a[80:83], v[96:99]\n"
"v_mfma_f32_16x16x32_bf16 v[104:107], v[216:219], a[84:87], v[104:107]\n"
"ds_read_b128 v[204:207], v242 offset:6144\n"
"v_mfma_f32_16x16x32_bf16 v[112:115], v[216:219], a[88:91], v[112:115]\n"
"s_waitcnt vmcnt(18)\n"
"ds_read_b64_tr_b16 a[112:113], v246 offset:4096\n"
"v_mfma_f32_16x16x32_bf16 v[120:123], v[216:219], a[92:95], v[120:123]\n"
"buffer_load_dwordx4 v235, s[12:15], s48 offen offset:1024 lds\n"
"v_mfma_f32_16x16x32_bf16 v[100:103], v[220:223], a[80:83], v[100:103]\n"
"ds_read_b128 v[208:211], v242 offset:8192\n"
"v_mfma_f32_16x16x32_bf16 v[108:111], v[220:223], a[84:87], v[108:111]\n"
"v_mfma_f32_16x16x32_bf16 v[116:119], v[220:223], a[88:91], v[116:119]\n"
"ds_read_b64_tr_b16 a[114:115], v246 offset:4608\n"
"v_mfma_f32_16x16x32_bf16 v[124:127], v[220:223], a[92:95], v[124:127]\n"
"s_waitcnt lgkmcnt(4)\n"
"buffer_load_dwordx4 v236, s[12:15], s48 offen offset:2048 lds\n"
"v_mfma_f32_16x16x32_bf16 v[0:3], v[192:195], a[96:99], v[0:3]\n"
"ds_read_b128 v[212:215], v242 offset:10240\n"
"v_mfma_f32_16x16x32_bf16 v[8:11], v[192:195], a[100:103], v[8:11]\n"
"v_mfma_f32_16x16x32_bf16 v[16:19], v[192:195], a[104:107], v[16:19]\n"
"ds_read_b64_tr_b16 a[116:117], v247 offset:4096\n"
"v_mfma_f32_16x16x32_bf16 v[24:27], v[192:195], a[108:111], v[24:27]\n"
"ds_read_b128 v[216:219], v242 offset:12288\n"
"v_mfma_f32_16x16x32_bf16 v[4:7], v[196:199], a[96:99], v[4:7]\n"
"buffer_load_dwordx4 v237, s[12:15], s48 offen offset:3072 lds\n"
"v_mfma_f32_16x16x32_bf16 v[12:15], v[196:199], a[100:103], v[12:15]\n"
"v_mfma_f32_16x16x32_bf16 v[20:23], v[196:199], a[104:107], v[20:23]\n"
"s_add_u32 m0, s37, 20480\n"
"ds_read_b64_tr_b16 a[118:119], v247 offset:4608\n"
"s_add_u32 s48, s47, 2883584\n"
"v_mfma_f32_16x16x32_bf16 v[28:31], v[196:199], a[108:111], v[28:31]\n"
"s_and_b32 s48, s48, 4194303\n"
"ds_read_b128 v[220:223], v242 offset:14336\n"
"v_mfma_f32_16x16x32_bf16 v[32:35], v[200:203], a[96:99], v[32:35]\n"
"buffer_load_dwordx4 v238, s[16:19], s48 offen offset:0 lds\n"
"v_mfma_f32_16x16x32_bf16 v[40:43], v[200:203], a[100:103], v[40:43]\n"
"v_mfma_f32_16x16x32_bf16 v[48:51], v[200:203], a[104:107], v[48:51]\n"
"ds_read_b128 v[192:195], v243 offset:0\n"
"v_mfma_f32_16x16x32_bf16 v[56:59], v[200:203], a[108:111], v[56:59]\n"
"s_waitcnt lgkmcnt(3)\n"
"ds_read_b64_tr_b16 a[120:121], v248 offset:4096\n"
"v_mfma_f32_16x16x32_bf16 v[36:39], v[204:207], a[96:99], v[36:39]\n"
"buffer_load_dwordx4 v239, s[16:19], s48 offen offset:1024 lds\n"
"v_mfma_f32_16x16x32_bf16 v[44:47], v[204:207], a[100:103], v[44:47]\n"
"v_mfma_f32_16x16x32_bf16 v[52:55], v[204:207], a[104:107], v[52:55]\n"
"ds_read_b128 v[196:199], v243 offset:2048\n"
"v_mfma_f32_16x16x32_bf16 v[60:63], v[204:207], a[108:111], v[60:63]\n"
"ds_read_b64_tr_b16 a[122:123], v248 offset:4608\n"
"v_mfma_f32_16x16x32_bf16 v[64:67], v[208:211], a[96:99], v[64:67]\n"
"v_mfma_f32_16x16x32_bf16 v[72:75], v[208:211], a[100:103], v[72:75]\n"
"ds_read_b128 v[200:203], v243 offset:4096\n"
"v_mfma_f32_16x16x32_bf16 v[80:83], v[208:211], a[104:107], v[80:83]\n"
"buffer_load_dwordx4 v240, s[16:19], s48 offen offset:2048 lds\n"
"v_mfma_f32_16x16x32_bf16 v[88:91], v[208:211], a[108:111], v[88:91]\n"
"ds_read_b64_tr_b16 a[124:125], v249 offset:4096\n"
"v_mfma_f32_16x16x32_bf16 v[68:71], v[212:215], a[96:99], v[68:71]\n"
"v_mfma_f32_16x16x32_bf16 v[76:79], v[212:215], a[100:103], v[76:79]\n"
"ds_read_b128 v[204:207], v243 offset:6144\n"
"v_mfma_f32_16x16x32_bf16 v[84:87], v[212:215], a[104:107], v[84:87]\n"
"buffer_load_dwordx4 v241, s[16:19], s48 offen offset:3072 lds\n"
"v_mfma_f32_16x16x32_bf16 v[92:95], v[212:215], a[108:111], v[92:95]\n"
"ds_read_b64_tr_b16 a[126:127], v249 offset:4608\n"
"v_mfma_f32_16x16x32_bf16 v[96:99], v[216:219], a[96:99], v[96:99]\n"
"s_add_u32 m0, s37, 0\n"
"ds_read_b128 v[208:211], v243 offset:8192\n"
"s_add_u32 s48, s47, 3145728\n"
"v_mfma_f32_16x16x32_bf16 v[104:107], v[216:219], a[100:103], v[104:107]\n"
"s_and_b32 s48, s48, 4194303\n"
"v_mfma_f32_16x16x32_bf16 v[112:115], v[216:219], a[104:107], v[112:115]\n"
"s_waitcnt vmcnt(21)\n"
"ds_read_b64_tr_b16 a[128:129], v246 offset:8192\n"
"v_mfma_f32_16x16x32_bf16 v[120:123], v[216:219], a[108:111], v[120:123]\n"
"s_waitcnt lgkmcnt(4)\n"
"buffer_load_dwordx4 v238, s[16:19], s48 offen offset:0 lds\n"
"v_mfma_f32_16x16x32_bf16 v[100:103], v[220:223], a[96:99], v[100:103]\n"
"ds_read_b128 v[212:215], v243 offset:10240\n"
"v_mfma_f32_16x16x32_bf16 v[108:111], v[220:223], a[100:103], v[108:111]\n"
"v_mfma_f32_16x16x32_bf16 v[116:119], v[220:223], a[104:107], v[116:119]\n"
"ds_read_b64_tr_b16 a[130:131], v246 offset:8704\n"
"v_mfma_f32_16x16x32_bf16 v[124:127], v[220:223], a[108:111], v[124:127]\n"
"buffer_load_dwordx4 v239, s[16:19], s48 offen offset:1024 lds\n"
"v_mfma_f32_16x16x32_bf16 v[0:3], v[192:195], a[112:115], v[0:3]\n"
"ds_read_b128 v[216:219], v243 offset:12288\n"
"v_mfma_f32_16x16x32_bf16 v[8:11], v[192:195], a[116:119], v[8:11]\n"
"v_mfma_f32_16x16x32_bf16 v[16:19], v[192:195], a[120:123], v[16:19]\n"
"s_waitcnt lgkmcnt(3)\n"
"ds_read_b64_tr_b16 a[132:133], v247 offset:8192\n"
"v_mfma_f32_16x16x32_bf16 v[24:27], v[192:195], a[124:127], v[24:27]\n"
"ds_read_b128 v[220:223], v243 offset:14336\n"
"v_mfma_f32_16x16x32_bf16 v[4:7], v[196:199], a[112:115], v[4:7]\n"
"buffer_load_dwordx4 v240, s[16:19], s48 offen offset:2048 lds\n"
"v_mfma_f32_16x16x32_bf16 v[12:15], v[196:199], a[116:119], v[12:15]\n"
"v_mfma_f32_16x16x32_bf16 v[20:23], v[196:199], a[120:123], v[20:23]\n"
"ds_read_b64_tr_b16 a[134:135], v247 offset:8704\n"
"v_mfma_f32_16x16x32_bf16 v[28:31], v[196:199], a[124:127], v[28:31]\n"
"buffer_load_dwordx4 v241, s[16:19], s48 offen offset:3072 lds\n"
"v_mfma_f32_16x16x32_bf16 v[32:35], v[200:203], a[112:115], v[32:35]\n"
"v_mfma_f32_16x16x32_bf16 v[40:43], v[200:203], a[116:119], v[40:43]\n"
"v_mfma_f32_16x16x32_bf16 v[48:51], v[200:203], a[120:123], v[48:51]\n"
"ds_read_b64_tr_b16 a[136:137], v248 offset:8192\n"
"v_mfma_f32_16x16x32_bf16 v[56:59], v[200:203], a[124:127], v[56:59]\n"
"v_mfma_f32_16x16x32_bf16 v[36:39], v[204:207], a[112:115], v[36:39]\n"
"v_mfma_f32_16x16x32_bf16 v[44:47], v[204:207], a[116:119], v[44:47]\n"
"s_waitcnt vmcnt(21) lgkmcnt(2)\n"
"s_barrier\n"
"v_mfma_f32_16x16x32_bf16 v[52:55], v[204:207], a[120:123], v[52:55]\n"
"s_add_u32 m0, s36, 0\n"
"ds_read_b128 v[192:195], v242 offset:16384\n"
"s_add_u32 s48, s46, 768\n"
"v_mfma_f32_16x16x32_bf16 v[60:63], v[204:207], a[124:127], v[60:63]\n"
"s_and_b32 s48, s48, 1023\n"
"ds_read_b64_tr_b16 a[138:139], v248 offset:8704\n"
"v_mfma_f32_16x16x32_bf16 v[64:67], v[208:211], a[112:115], v[64:67]\n"
"buffer_load_dwordx4 v234, s[12:15], s48 offen offset:0 lds\n"
"v_mfma_f32_16x16x32_bf16 v[72:75], v[208:211], a[116:119], v[72:75]\n"
"v_mfma_f32_16x16x32_bf16 v[80:83], v[208:211], a[120:123], v[80:83]\n"
"ds_read_b128 v[196:199], v242 offset:18432\n"
"v_mfma_f32_16x16x32_bf16 v[88:91], v[208:211], a[124:127], v[88:91]\n"
"ds_read_b64_tr_b16 a[140:141], v249 offset:8192\n"
"v_mfma_f32_16x16x32_bf16 v[68:71], v[212:215], a[112:115], v[68:71]\n"
"buffer_load_dwordx4 v235, s[12:15], s48 offen offset:1024 lds\n"
"v_mfma_f32_16x16x32_bf16 v[76:79], v[212:215], a[116:119], v[76:79]\n"
"ds_read_b128 v[200:203], v242 offset:20480\n"
"v_mfma_f32_16x16x32_bf16 v[84:87], v[212:215], a[120:123], v[84:87]\n"
"ds_read_b64_tr_b16 a[142:143], v249 offset:8704\n"
"v_mfma_f32_16x16x32_bf16 v[92:95], v[212:215], a[124:127], v[92:95]\n"
"v_mfma_f32_16x16x32_bf16 v[96:99], v[216:219], a[112:115], v[96:99]\n"
"v_mfma_f32_16x16x32_bf16 v[104:107], v[216:219], a[116:119], v[104:107]\n"
"ds_read_b128 v[204:207], v242 offset:22528\n"
"v_mfma_f32_16x16x32_bf16 v[112:115], v[216:219], a[120:123], v[112:115]\n"
"buffer_load_dwordx4 v236, s[12:15], s48 offen offset:2048 lds\n"
"v_mfma_f32_16x16x32_bf16 v[120:123], v[216:219], a[124:127], v[120:123]\n"
"s_waitcnt vmcnt(20)\n"
"ds_read_b64_tr_b16 a[144:145], v246 offset:12288\n"
"v_mfma_f32_16x16x32_bf16 v[100:103], v[220:223], a[112:115], v[100:103]\n"
"ds_read_b128 v[208:211], v242 offset:24576\n"
"v_mfma_f32_16x16x32_bf16 v[108:111], v[220:223], a[116:119], v[108:111]\n"
"buffer_load_dwordx4 v237, s[12:15], s48 offen offset:3072 lds\n"
"v_mfma_f32_16x16x32_bf16 v[116:119], v[220:223], a[120:123], v[116:119]\n"
"ds_read_b64_tr_b16 a[146:147], v246 offset:12800\n"
"v_mfma_f32_16x16x32_bf16 v[124:127], v[220:223], a[124:127], v[124:127]\n"
"s_waitcnt lgkmcnt(4)\n"
"v_mfma_f32_16x16x32_bf16 v[0:3], v[192:195], a[128:131], v[0:3]\n"
"s_add_u32 m0, s37, 4096\n"
"ds_read_b128 v[212:215], v242 offset:26624\n"
"s_add_u32 s48, s47, 3407872\n"
"v_mfma_f32_16x16x32_bf16 v[8:11], v[192:195], a[132:135], v[8:11]\n"
"s_and_b32 s48, s48, 4194303\n"
"v_mfma_f32_16x16x32_bf16 v[16:19], v[192:195], a[136:139], v[16:19]\n"
"ds_read_b64_tr_b16 a[148:149], v247 offset:12288\n"
"v_mfma_f32_16x16x32_bf16 v[24:27], v[192:195], a[140:143], v[24:27]\n"
"buffer_load_dwordx4 v238, s[16:19], s48 offen offset:0 lds\n"
"v_mfma_f32_16x16x32_bf16 v[4:7], v[196:199], a[128:131], v[4:7]\n"
"ds_read_b128 v[216:219], v242 offset:28672\n"
"v_mfma_f32_16x16x32_bf16 v[12:15], v[196:199], a[132:135], v[12:15]\n"
"v_mfma_f32_16x16x32_bf16 v[20:23], v[196:199], a[136:139], v[20:23]\n"
"ds_read_b64_tr_b16 a[150:151], v247 offset:12800\n"
"v_mfma_f32_16x16x32_bf16 v[28:31], v[196:199], a[140:143], v[28:31]\n"
"buffer_load_dwordx4 v239, s[16:19], s48 offen offset:1024 lds\n"
"v_mfma_f32_16x16x32_bf16 v[32:35], v[200:203], a[128:131], v[32:35]\n"
"ds_read_b128 v[220:223], v242 offset:30720\n"
"v_mfma_f32_16x16x32_bf16 v[40:43], v[200:203], a[132:135], v[40:43]\n"
"v_mfma_f32_16x16x32_bf16 v[48:51], v[200:203], a[136:139], v[48:51]\n"
"ds_read_b128 v[192:195], v243 offset:16384\n"
"v_mfma_f32_16x16x32_bf16 v[56:59], v[200:203], a[140:143], v[56:59]\n"
"s_waitcnt lgkmcnt(3)\n"
"ds_read_b64_tr_b16 a[152:153], v248 offset:12288\n"
"v_mfma_f32_16x16x32_bf16 v[36:39], v[204:207], a[128:131], v[36:39]\n"
"buffer_load_dwordx4 v240, s[16:19], s48 offen offset:2048 lds\n"
"v_mfma_f32_16x16x32_bf16 v[44:47], v[204:207], a[132:135], v[44:47]\n"
"v_mfma_f32_16x16x32_bf16 v[52:55], v[204:207], a[136:139], v[52:55]\n"
"ds_read_b128 v[196:199], v243 offset:18432\n"
"v_mfma_f32_16x16x32_bf16 v[60:63], v[204:207], a[140:143], v[60:63]\n"
"ds_read_b64_tr_b16 a[154:155], v248 offset:12800\n"
"v_mfma_f32_16x16x32_bf16 v[64:67], v[208:211], a[128:131], v[64:67]\n"
"buffer_load_dwordx4 v241, s[16:19], s48 offen offset:3072 lds\n"
"v_mfma_f32_16x16x32_bf16 v[72:75], v[208:211], a[132:135], v[72:75]\n"
"ds_read_b128 v[200:203], v243 offset:20480\n"
"v_mfma_f32_16x16x32_bf16 v[80:83], v[208:211], a[136:139], v[80:83]\n"
"s_add_u32 m0, s37, 8192\n"
"ds_read_b64_tr_b16 a[156:157], v249 offset:12288\n"
"s_add_u32 s48, s47, 3670016\n"
"v_mfma_f32_16x16x32_bf16 v[88:91], v[208:211], a[140:143], v[88:91]\n"
"s_and_b32 s48, s48, 4194303\n"
"v_mfma_f32_16x16x32_bf16 v[68:71], v[212:215], a[128:131], v[68:71]\n"
"buffer_load_dwordx4 v238, s[16:19], s48 offen offset:0 lds\n"
"v_mfma_f32_16x16x32_bf16 v[76:79], v[212:215], a[132:135], v[76:79]\n"
"ds_read_b128 v[204:207], v243 offset:22528\n"
"v_mfma_f32_16x16x32_bf16 v[84:87], v[212:215], a[136:139], v[84:87]\n"
"ds_read_b64_tr_b16 a[158:159], v249 offset:12800\n"
"v_mfma_f32_16x16x32_bf16 v[92:95], v[212:215], a[140:143], v[92:95]\n"
"v_mfma_f32_16x16x32_bf16 v[96:99], v[216:219], a[128:131], v[96:99]\n"
"ds_read_b128 v[208:211], v243 offset:24576\n"
"v_mfma_f32_16x16x32_bf16 v[104:107], v[216:219], a[132:135], v[104:107]\n"
"buffer_load_dwordx4 v239, s[16:19], s48 offen offset:1024 lds\n"
"v_mfma_f32_16x16x32_bf16 v[112:115], v[216:219], a[136:139], v[112:115]\n"
"s_waitcnt vmcnt(22)\n"
"ds_read_b64_tr_b16 a[160:161], v246 offset:16384\n"
"v_mfma_f32_16x16x32_bf16 v[120:123], v[216:219], a[140:143], v[120:123]\n"
"s_waitcnt lgkmcnt(4)\n"
"v_mfma_f32_16x16x32_bf16 v[100:103], v[220:223], a[128:131], v[100:103]\n"
"ds_read_b128 v[212:215], v243 offset:26624\n"
"v_mfma_f32_16x16x32_bf16 v[108:111], v[220:223], a[132:135], v[108:111]\n"
"buffer_load_dwordx4 v240, s[16:19], s48 offen offset:2048 lds\n"
"v_mfma_f32_16x16x32_bf16 v[116:119], v[220:223], a[136:139], v[116:119]\n"
"ds_read_b64_tr_b16 a[162:163], v246 offset:16896\n"
"v_mfma_f32_16x16x32_bf16 v[124:127], v[220:223], a[140:143], v[124:127]\n"
"ds_read_b128 v[216:219], v243 offset:28672\n"
"v_mfma_f32_16x16x32_bf16 v[0:3], v[192:195], a[144:147], v[0:3]\n"
"v_mfma_f32_16x16x32_bf16 v[8:11], v[192:195], a[148:151], v[8:11]\n"
"buffer_load_dwordx4 v241, s[16:19], s48 offen offset:3072 lds\n"
"v_mfma_f32_16x16x32_bf16 v[16:19], v[192:195], a[152:155], v[16:19]\n"
"s_waitcnt lgkmcnt(3)\n"
"ds_read_b64_tr_b16 a[164:165], v247 offset:16384\n"
"v_mfma_f32_16x16x32_bf16 v[24:27], v[192:195], a[156:159], v[24:27]\n"
"ds_read_b128 v[220:223], v243 offset:30720\n"
"v_mfma_f32_16x16x32_bf16 v[4:7], v[196:199], a[144:147], v[4:7]\n"
"v_mfma_f32_16x16x32_bf16 v[12:15], v[196:199], a[148:151], v[12:15]\n"
"v_mfma_f32_16x16x32_bf16 v[20:23], v[196:199], a[152:155], v[20:23]\n"
"ds_read_b64_tr_b16 a[166:167], v247 offset:16896\n"
"v_mfma_f32_16x16x32_bf16 v[28:31], v[196:199], a[156:159], v[28:31]\n"
"v_mfma_f32_16x16x32_bf16 v[32:35], v[200:203], a[144:147], v[32:35]\n"
"v_mfma_f32_16x16x32_bf16 v[40:43], v[200:203], a[148:151], v[40:43]\n"
"v_mfma_f32_16x16x32_bf16 v[48:51], v[200:203], a[152:155], v[48:51]\n"
"ds_read_b64_tr_b16 a[168:169], v248 offset:16384\n"
"v_mfma_f32_16x16x32_bf16 v[56:59], v[200:203], a[156:159], v[56:59]\n"
"v_mfma_f32_16x16x32_bf16 v[36:39], v[204:207], a[144:147], v[36:39]\n"
"v_mfma_f32_16x16x32_bf16 v[44:47], v[204:207], a[148:151], v[44:47]\n"
"s_waitcnt vmcnt(20) lgkmcnt(2)\n"
"s_barrier\n"
"v_mfma_f32_16x16x32_bf16 v[52:55], v[204:207], a[152:155], v[52:55]\n"
"s_add_u32 m0, s36, 16384\n"
"ds_read_b128 v[192:195], v242 offset:32768\n"
"s_add_u32 s48, s46, 896\n"
"v_mfma_f32_16x16x32_bf16 v[60:63], v[204:207], a[156:159], v[60:63]\n"
"s_and_b32 s48, s48, 1023\n"
"ds_read_b64_tr_b16 a[170:171], v248 offset:16896\n"
"v_mfma_f32_16x16x32_bf16 v[64:67], v[208:211], a[144:147], v[64:67]\n"
"buffer_load_dwordx4 v234, s[12:15], s48 offen offset:0 lds\n"
"v_mfma_f32_16x16x32_bf16 v[72:75], v[208:211], a[148:151], v[72:75]\n"
"v_mfma_f32_16x16x32_bf16 v[80:83], v[208:211], a[152:155], v[80:83]\n"
"ds_read_b128 v[196:199], v242 offset:34816\n"
"v_mfma_f32_16x16x32_bf16 v[88:91], v[208:211], a[156:159], v[88:91]\n"
"ds_read_b64_tr_b16 a[172:173], v249 offset:16384\n"
"v_mfma_f32_16x16x32_bf16 v[68:71], v[212:215], a[144:147], v[68:71]\n"
"buffer_load_dwordx4 v235, s[12:15], s48 offen offset:1024 lds\n"
"v_mfma_f32_16x16x32_bf16 v[76:79], v[212:215], a[148:151], v[76:79]\n"
"ds_read_b128 v[200:203], v242 offset:36864\n"
"v_mfma_f32_16x16x32_bf16 v[84:87], v[212:215], a[152:155], v[84:87]\n"
"ds_read_b64_tr_b16 a[174:175], v249 offset:16896\n"
"v_mfma_f32_16x16x32_bf16 v[92:95], v[212:215], a[156:159], v[92:95]\n"
"v_mfma_f32_16x16x32_bf16 v[96:99], v[216:219], a[144:147], v[96:99]\n"
"v_mfma_f32_16x16x32_bf16 v[104:107], v[216:219], a[148:151], v[104:107]\n"
"ds_read_b128 v[204:207], v242 offset:38912\n"
"v_mfma_f32_16x16x32_bf16 v[112:115], v[216:219], a[152:155], v[112:115]\n"
"buffer_load_dwordx4 v236, s[12:15], s48 offen offset:2048 lds\n"
"v_mfma_f32_16x16x32_bf16 v[120:123], v[216:219], a[156:159], v[120:123]\n"
"s_waitcnt vmcnt(19)\n"
"ds_read_b64_tr_b16 a[176:177], v246 offset:20480\n"
"v_mfma_f32_16x16x32_bf16 v[100:103], v[220:223], a[144:147], v[100:103]\n"
"ds_read_b128 v[208:211], v242 offset:40960\n"
"v_mfma_f32_16x16x32_bf16 v[108:111], v[220:223], a[148:151], v[108:111]\n"
"buffer_load_dwordx4 v237, s[12:15], s48 offen offset:3072 lds\n"
"v_mfma_f32_16x16x32_bf16 v[116:119], v[220:223], a[152:155], v[116:119]\n"
"ds_read_b64_tr_b16 a[178:179], v246 offset:20992\n"
"v_mfma_f32_16x16x32_bf16 v[124:127], v[220:223], a[156:159], v[124:127]\n"
"s_waitcnt lgkmcnt(4)\n"
"v_mfma_f32_16x16x32_bf16 v[0:3], v[192:195], a[160:163], v[0:3]\n"
"s_add_u32 m0, s37, 12288\n"
"ds_read_b128 v[212:215], v242 offset:43008\n"
"s_add_u32 s48, s47, 3932160\n"
"v_mfma_f32_16x16x32_bf16 v[8:11], v[192:195], a[164:167], v[8:11]\n"
"s_and_b32 s48, s48, 4194303\n"
"v_mfma_f32_16x16x32_bf16 v[16:19], v[192:195], a[168:171], v[16:19]\n"
"ds_read_b64_tr_b16 a[180:181], v247 offset:20480\n"
"v_mfma_f32_16x16x32_bf16 v[24:27], v[192:195], a[172:175], v[24:27]\n"
"buffer_load_dwordx4 v238, s[16:19], s48 offen offset:0 lds\n"
"v_mfma_f32_16x16x32_bf16 v[4:7], v[196:199], a[160:163], v[4:7]\n"
"ds_read_b128 v[216:219], v242 offset:45056\n"
"v_mfma_f32_16x16x32_bf16 v[12:15], v[196:199], a[164:167], v[12:15]\n"
"v_mfma_f32_16x16x32_bf16 v[20:23], v[196:199], a[168:171], v[20:23]\n"
"ds_read_b64_tr_b16 a[182:183], v247 offset:20992\n"
"v_mfma_f32_16x16x32_bf16 v[28:31], v[196:199], a[172:175], v[28:31]\n"
"buffer_load_dwordx4 v239, s[16:19], s48 offen offset:1024 lds\n"
"v_mfma_f32_16x16x32_bf16 v[32:35], v[200:203], a[160:163], v[32:35]\n"
"ds_read_b128 v[220:223], v242 offset:47104\n"
"v_mfma_f32_16x16x32_bf16 v[40:43], v[200:203], a[164:167], v[40:43]\n"
"v_mfma_f32_16x16x32_bf16 v[48:51], v[200:203], a[168:171], v[48:51]\n"
"ds_read_b128 v[192:195], v243 offset:32768\n"
"v_mfma_f32_16x16x32_bf16 v[56:59], v[200:203], a[172:175], v[56:59]\n"
"s_waitcnt lgkmcnt(3)\n"
"ds_read_b64_tr_b16 a[184:185], v248 offset:20480\n"
"v_mfma_f32_16x16x32_bf16 v[36:39], v[204:207], a[160:163], v[36:39]\n"
"buffer_load_dwordx4 v[160:163], v224, s[8:11], 0 offen offset:256 sc1\n"
"v_mfma_f32_16x16x32_bf16 v[44:47], v[204:207], a[164:167], v[44:47]\n"
"buffer_load_dwordx4 v240, s[16:19], s48 offen offset:2048 lds\n"
"v_mfma_f32_16x16x32_bf16 v[52:55], v[204:207], a[168:171], v[52:55]\n"
"buffer_load_dwordx4 v[164:167], v224, s[8:11], 0 offen offset:1280 sc1\n"
"v_mfma_f32_16x16x32_bf16 v[60:63], v[204:207], a[172:175], v[60:63]\n"
"ds_read_b128 v[196:199], v243 offset:34816\n"
"v_mfma_f32_16x16x32_bf16 v[64:67], v[208:211], a[160:163], v[64:67]\n"
"ds_read_b64_tr_b16 a[186:187], v248 offset:20992\n"
"v_mfma_f32_16x16x32_bf16 v[72:75], v[208:211], a[164:167], v[72:75]\n"
"buffer_load_dwordx4 v241, s[16:19], s48 offen offset:3072 lds\n"
"v_mfma_f32_16x16x32_bf16 v[80:83], v[208:211], a[168:171], v[80:83]\n"
"s_add_u32 m0, s37, 16384\n"
"ds_read_b128 v[200:203], v243 offset:36864\n"
"s_add_u32 s48, s39, 0\n"
"v_mfma_f32_16x16x32_bf16 v[88:91], v[208:211], a[172:175], v[88:91]\n"
"ds_read_b64_tr_b16 a[188:189], v249 offset:20480\n"
"v_mfma_f32_16x16x32_bf16 v[68:71], v[212:215], a[160:163], v[68:71]\n"
"buffer_load_dwordx4 v234, s[12:15], s48 offen offset:0 lds\n"
"v_mfma_f32_16x16x32_bf16 v[76:79], v[212:215], a[164:167], v[76:79]\n"
"ds_read_b128 v[204:207], v243 offset:38912\n"
"v_mfma_f32_16x16x32_bf16 v[84:87], v[212:215], a[168:171], v[84:87]\n"
"ds_read_b64_tr_b16 a[190:191], v249 offset:20992\n"
"v_mfma_f32_16x16x32_bf16 v[92:95], v[212:215], a[172:175], v[92:95]\n"
"v_mfma_f32_16x16x32_bf16 v[96:99], v[216:219], a[160:163], v[96:99]\n"
"ds_read_b128 v[208:211], v243 offset:40960\n"
"v_mfma_f32_16x16x32_bf16 v[104:107], v[216:219], a[164:167], v[104:107]\n"
"buffer_load_dwordx4 v235, s[12:15], s48 offen offset:1024 lds\n"
"v_mfma_f32_16x16x32_bf16 v[112:115], v[216:219], a[168:171], v[112:115]\n"
"s_cmp_lt_u32 s30, 2\n"
"s_waitcnt vmcnt(24)\n"
"ds_read_b64_tr_b16 a[192:193], v246 offset:0\n"
"v_mfma_f32_16x16x32_bf16 v[120:123], v[216:219], a[172:175], v[120:123]\n"
"s_waitcnt lgkmcnt(4)\n"
"s_cselect_b64 s[40:41], s[78:79], s[80:81]\n"
"s_and_b32 s41, s41, 0xffff\n"
"v_mfma_f32_16x16x32_bf16 v[100:103], v[220:223], a[160:163], v[100:103]\n"
"s_mov_b32 s42, 2211840\n"
"ds_read_b128 v[212:215], v243 offset:43008\n"
"v_mfma_f32_16x16x32_bf16 v[108:111], v[220:223], a[164:167], v[108:111]\n"
"s_mov_b32 s43, 0x20000\n"
"buffer_load_dwordx4 v236, s[12:15], s48 offen offset:2048 lds\n"
"v_mfma_f32_16x16x32_bf16 v[116:119], v[220:223], a[168:171], v[116:119]\n"
"ds_read_b64_tr_b16 a[194:195], v246 offset:512\n"
"v_mfma_f32_16x16x32_bf16 v[124:127], v[220:223], a[172:175], v[124:127]\n"
"ds_read_b128 v[216:219], v243 offset:45056\n"
"v_mfma_f32_16x16x32_bf16 v[0:3], v[192:195], a[176:179], v[0:3]\n"
"v_mfma_f32_16x16x32_bf16 v[8:11], v[192:195], a[180:183], v[8:11]\n"
"buffer_load_dwordx4 v237, s[12:15], s48 offen offset:3072 lds\n"
"v_mfma_f32_16x16x32_bf16 v[16:19], v[192:195], a[184:187], v[16:19]\n"
"s_waitcnt lgkmcnt(3)\n"
"ds_read_b64_tr_b16 a[196:197], v247 offset:0\n"
"v_mfma_f32_16x16x32_bf16 v[24:27], v[192:195], a[188:191], v[24:27]\n"
"ds_read_b128 v[220:223], v243 offset:47104\n"
"v_mfma_f32_16x16x32_bf16 v[4:7], v[196:199], a[176:179], v[4:7]\n"
"s_add_u32 m0, s37, 20480\n"
"s_add_u32 s48, s98, 0\n"
"v_mfma_f32_16x16x32_bf16 v[12:15], v[196:199], a[180:183], v[12:15]\n"
"v_mfma_f32_16x16x32_bf16 v[20:23], v[196:199], a[184:187], v[20:23]\n"
"ds_read_b64_tr_b16 a[198:199], v247 offset:512\n"
"v_mfma_f32_16x16x32_bf16 v[28:31], v[196:199], a[188:191], v[28:31]\n"
"buffer_load_dwordx4 v234, s[12:15], s48 offen offset:0 lds\n"
"v_mfma_f32_16x16x32_bf16 v[32:35], v[200:203], a[176:179], v[32:35]\n"
"v_lshrrev_b32 v149, 4, v224\n"
"v_mfma_f32_16x16x32_bf16 v[40:43], v[200:203], a[180:183], v[40:43]\n"
"v_min_u32 v149, 49, v149\n"
"v_mfma_f32_16x16x32_bf16 v[48:51], v[200:203], a[184:187], v[48:51]\n"
"ds_read_b64_tr_b16 a[200:201], v248 offset:0\n"
"v_mul_u32_u24 v150, 52, v149\n"
"v_mfma_f32_16x16x32_bf16 v[56:59], v[200:203], a[188:191], v[56:59]\n"
"buffer_load_dwordx4 v235, s[12:15], s48 offen offset:1024 lds\n"
"v_mfma_f32_16x16x32_bf16 v[36:39], v[204:207], a[176:179], v[36:39]\n"
"v_lshrrev_b32 v150, 8, v150\n"
"v_mul_u32_u24 v150, 2901, v150\n"
"v_mfma_f32_16x16x32_bf16 v[44:47], v[204:207], a[180:183], v[44:47]\n"
"s_waitcnt vmcnt(24) lgkmcnt(2)\n"
"s_barrier\n"
"v_mfma_f32_16x16x32_bf16 v[52:55], v[204:207], a[184:187], v[52:55]\n"
"ds_read_b128 v[192:195], v242 offset:0\n"
"v_mul_u32_u24 v149, 111, v149\n"
"v_mfma_f32_16x16x32_bf16 v[60:63], v[204:207], a[188:191], v[60:63]\n"
"ds_read_b64_tr_b16 a[202:203], v248 offset:512\n"
"v_add_u32 v149, v149, v150\n"
"v_mfma_f32_16x16x32_bf16 v[64:67], v[208:211], a[176:179], v[64:67]\n"
"s_lshr_b32 s50, s31, 3\n"
"buffer_load_dwordx4 v236, s[12:15], s48 offen offset:2048 lds\n"
"v_mfma_f32_16x16x32_bf16 v[72:75], v[208:211], a[180:183], v[72:75]\n"
"s_lshl_b32 s50, s50, 1\n"
"v_and_b32 v149, -4, v149\n"
"v_mfma_f32_16x16x32_bf16 v[80:83], v[208:211], a[184:187], v[80:83]\n"
"s_and_b32 s51, s30, 1\n"
"ds_read_b128 v[196:199], v242 offset:2048\n"
"v_mfma_f32_16x16x32_bf16 v[88:91], v[208:211], a[188:191], v[88:91]\n"
"s_add_u32 s50, s50, s51\n"
"ds_read_b64_tr_b16 a[204:205], v249 offset:0\n"
"v_mfma_f32_16x16x32_bf16 v[68:71], v[212:215], a[176:179], v[68:71]\n"
"s_mul_i32 s50, s50, 34560\n"
"buffer_load_dwordx4 v237, s[12:15], s48 offen offset:3072 lds\n"
"v_mfma_f32_16x16x32_bf16 v[76:79], v[212:215], a[180:183], v[76:79]\n"
"s_mul_i32 s51, s32, 448\n"
"ds_read_b128 v[200:203], v242 offset:4096\n"
"v_mfma_f32_16x16x32_bf16 v[84:87], v[212:215], a[184:187], v[84:87]\n"
"s_add_u32 s50, s50, s51\n"
"ds_read_b64_tr_b16 a[206:207], v249 offset:512\n"
"v_mfma_f32_16x16x32_bf16 v[92:95], v[212:215], a[188:191], v[92:95]\n"
"v_mfma_f32_16x16x32_bf16 v[96:99], v[216:219], a[176:179], v[96:99]\n"
"v_mfma_f32_16x16x32_bf16 v[104:107], v[216:219], a[180:183], v[104:107]\n"
"ds_read_b128 v[204:207], v242 offset:6144\n"
"v_mfma_f32_16x16x32_bf16 v[112:115], v[216:219], a[184:187], v[112:115]\n"
"s_add_u32 m0, s37, 0\n"
"s_waitcnt vmcnt(22)\n"
"ds_read_b64_tr_b16 a[208:209], v246 offset:4096\n"
"v_mfma_f32_16x16x32_bf16 v[120:123], v[216:219], a[188:191], v[120:123]\n"
"s_add_u32 s48, s39, 32768\n"
"buffer_load_dword v149, v149, s[40:43], s50 offen\n"
"v_mfma_f32_16x16x32_bf16 v[100:103], v[220:223], a[176:179], v[100:103]\n"
"ds_read_b128 v[208:211], v242 offset:8192\n"
"v_mfma_f32_16x16x32_bf16 v[108:111], v[220:223], a[180:183], v[108:111]\n"
"s_waitcnt lgkmcnt(3)\n"
"buffer_load_dwordx4 v234, s[12:15], s48 offen offset:0 lds\n"
"v_mfma_f32_16x16x32_bf16 v[116:119], v[220:223], a[184:187], v[116:119]\n"
"ds_read_b64_tr_b16 a[210:211], v246 offset:4608\n"
"v_mfma_f32_16x16x32_bf16 v[124:127], v[220:223], a[188:191], v[124:127]\n"
"v_mfma_f32_16x16x32_bf16 v[0:3], v[192:195], a[192:195], v[0:3]\n"
"ds_read_b128 v[212:215], v242 offset:10240\n"
"v_mfma_f32_16x16x32_bf16 v[8:11], v[192:195], a[196:199], v[8:11]\n"
"buffer_load_dwordx4 v235, s[12:15], s48 offen offset:1024 lds\n"
"v_mfma_f32_16x16x32_bf16 v[16:19], v[192:195], a[200:203], v[16:19]\n"
"s_mov_b32 s94, 2686976\n"
"ds_read_b64_tr_b16 a[212:213], v247 offset:4096\n"
"s_mov_b32 s95, 2691072\n"
"v_mfma_f32_16x16x32_bf16 v[24:27], v[192:195], a[204:207], v[24:27]\n"
"s_waitcnt vmcnt(12)\n"
"v_cmp_eq_u64 vcc, s[28:29], v[160:161]\n"
"v_cmp_eq_u64_e64 s[62:63], s[28:29], v[162:163]\n"
"s_and_b64 vcc, vcc, s[62:63]\n"
"v_cmp_eq_u64_e64 s[62:63], s[28:29], v[164:165]\n"
"s_and_b64 vcc, vcc, s[62:63]\n"
"v_cmp_eq_u64_e64 s[62:63], s[28:29], v[166:167]\n"
"s_and_b64 vcc, vcc, s[62:63]\n"
"s_cmp_eq_u64 vcc, exec\n"
"s_cbranch_scc0 L1004_%=\n"
"L1005_%=:\n"
"v_mfma_f32_16x16x32_bf16 v[4:7], v[196:199], a[192:195], v[4:7]\n"
"ds_read_b128 v[216:219], v242 offset:12288\n"
"v_mfma_f32_16x16x32_bf16 v[12:15], v[196:199], a[196:199], v[12:15]\n"
"buffer_load_dwordx4 v[160:163], v224, s[8:11], s94 offen offset:0 sc1\n"
"v_mfma_f32_16x16x32_bf16 v[20:23], v[196:199], a[200:203], v[20:23]\n"
"buffer_load_dwordx4 v236, s[12:15], s48 offen offset:2048 lds\n"
"v_mfma_f32_16x16x32_bf16 v[28:31], v[196:199], a[204:207], v[28:31]\n"
"ds_read_b64_tr_b16 a[214:215], v247 offset:4608\n"
"v_mfma_f32_16x16x32_bf16 v[32:35], v[200:203], a[192:195], v[32:35]\n"
"buffer_load_dwordx4 v[164:167], v224, s[8:11], s94 offen offset:1024 sc1\n"
"v_mfma_f32_16x16x32_bf16 v[40:43], v[200:203], a[196:199], v[40:43]\n"
"ds_read_b128 v[220:223], v242 offset:14336\n"
"v_mfma_f32_16x16x32_bf16 v[48:51], v[200:203], a[200:203], v[48:51]\n"
"buffer_load_dwordx4 v[168:171], v224, s[8:11], s94 offen offset:2048 sc1\n"
"v_mfma_f32_16x16x32_bf16 v[56:59], v[200:203], a[204:207], v[56:59]\n"
"s_waitcnt lgkmcnt(2)\n"
"buffer_load_dwordx4 v237, s[12:15], s48 offen offset:3072 lds\n"
"v_mfma_f32_16x16x32_bf16 v[36:39], v[204:207], a[192:195], v[36:39]\n"
"ds_read_b128 v[192:195], v243 offset:0\n"
"v_mfma_f32_16x16x32_bf16 v[44:47], v[204:207], a[196:199], v[44:47]\n"
"ds_read_b64_tr_b16 a[216:217], v248 offset:4096\n"
"v_mfma_f32_16x16x32_bf16 v[52:55], v[204:207], a[200:203], v[52:55]\n"
"buffer_load_dwordx4 v[172:175], v224, s[8:11], s94 offen offset:3072 sc1\n"
"v_mfma_f32_16x16x32_bf16 v[60:63], v[204:207], a[204:207], v[60:63]\n"
"buffer_load_dwordx4 v[176:179], v224, s[8:11], s95 offen offset:0 sc1\n"
"v_mfma_f32_16x16x32_bf16 v[64:67], v[208:211], a[192:195], v[64:67]\n"
"ds_read_b128 v[196:199], v243 offset:2048\n"
"v_mfma_f32_16x16x32_bf16 v[72:75], v[208:211], a[196:199], v[72:75]\n"
"ds_read_b64_tr_b16 a[218:219], v248 offset:4608\n"
"v_mfma_f32_16x16x32_bf16 v[80:83], v[208:211], a[200:203], v[80:83]\n"
"buffer_load_dwordx4 v[180:183], v224, s[8:11], s95 offen offset:1024 sc1\n"
"v_mfma_f32_16x16x32_bf16 v[88:91], v[208:211], a[204:207], v[88:91]\n"
"buffer_load_dwordx4 v[184:187], v224, s[8:11], s95 offen offset:2048 sc1\n"
"v_mfma_f32_16x16x32_bf16 v[68:71], v[212:215], a[192:195], v[68:71]\n"
"ds_read_b128 v[200:203], v243 offset:4096\n"
"v_mfma_f32_16x16x32_bf16 v[76:79], v[212:215], a[196:199], v[76:79]\n"
"ds_read_b64_tr_b16 a[220:221], v249 offset:4096\n"
"v_mfma_f32_16x16x32_bf16 v[84:87], v[212:215], a[200:203], v[84:87]\n"
"buffer_load_dwordx4 v[188:191], v224, s[8:11], s95 offen offset:3072 sc1\n"
"v_mfma_f32_16x16x32_bf16 v[92:95], v[212:215], a[204:207], v[92:95]\n"
"ds_read_b128 v[204:207], v243 offset:6144\n"
"v_mfma_f32_16x16x32_bf16 v[96:99], v[216:219], a[192:195], v[96:99]\n"
"ds_read_b64_tr_b16 a[222:223], v249 offset:4608\n"
"v_mfma_f32_16x16x32_bf16 v[104:107], v[216:219], a[196:199], v[104:107]\n"
"ds_read_b128 v[208:211], v243 offset:8192\n"
"v_mfma_f32_16x16x32_bf16 v[112:115], v[216:219], a[200:203], v[112:115]\n"
"s_add_u32 m0, s37, 4096\n"
"ds_read_b64_tr_b16 a[224:225], v246 offset:8192\n"
"s_add_u32 s48, s98, 32768\n"
"v_mfma_f32_16x16x32_bf16 v[120:123], v[216:219], a[204:207], v[120:123]\n"
"s_waitcnt lgkmcnt(5)\n"
"v_mfma_f32_16x16x32_bf16 v[100:103], v[220:223], a[192:195], v[100:103]\n"
"ds_read_b128 v[212:215], v243 offset:10240\n"
"v_mfma_f32_16x16x32_bf16 v[108:111], v[220:223], a[196:199], v[108:111]\n"
"s_waitcnt lgkmcnt(3)\n"
"buffer_load_dwordx4 v234, s[12:15], s48 offen offset:0 lds\n"
"v_mfma_f32_16x16x32_bf16 v[116:119], v[220:223], a[200:203], v[116:119]\n"
"ds_read_b64_tr_b16 a[226:227], v246 offset:8704\n"
"v_mfma_f32_16x16x32_bf16 v[124:127], v[220:223], a[204:207], v[124:127]\n"
"ds_read_b128 v[216:219], v243 offset:12288\n"
"v_mfma_f32_16x16x32_bf16 v[0:3], v[192:195], a[208:211], v[0:3]\n"
"buffer_load_dwordx4 v235, s[12:15], s48 offen offset:1024 lds\n"
"v_mfma_f32_16x16x32_bf16 v[8:11], v[192:195], a[212:215], v[8:11]\n"
"v_mfma_f32_16x16x32_bf16 v[16:19], v[192:195], a[216:219], v[16:19]\n"
"ds_read_b64_tr_b16 a[228:229], v247 offset:8192\n"
"v_mfma_f32_16x16x32_bf16 v[24:27], v[192:195], a[220:223], v[24:27]\n"
"ds_read_b128 v[220:223], v243 offset:14336\n"
"v_mfma_f32_16x16x32_bf16 v[4:7], v[196:199], a[208:211], v[4:7]\n"
"v_mfma_f32_16x16x32_bf16 v[12:15], v[196:199], a[212:215], v[12:15]\n"
"buffer_load_dwordx4 v236, s[12:15], s48 offen offset:2048 lds\n"
"v_mfma_f32_16x16x32_bf16 v[20:23], v[196:199], a[216:219], v[20:23]\n"
"ds_read_b64_tr_b16 a[230:231], v247 offset:8704\n"
"v_mfma_f32_16x16x32_bf16 v[28:31], v[196:199], a[220:223], v[28:31]\n"
"v_mfma_f32_16x16x32_bf16 v[32:35], v[200:203], a[208:211], v[32:35]\n"
"v_mfma_f32_16x16x32_bf16 v[40:43], v[200:203], a[212:215], v[40:43]\n"
"buffer_load_dwordx4 v237, s[12:15], s48 offen offset:3072 lds\n"
"v_mfma_f32_16x16x32_bf16 v[48:51], v[200:203], a[216:219], v[48:51]\n"
"ds_read_b64_tr_b16 a[232:233], v248 offset:8192\n"
"v_mfma_f32_16x16x32_bf16 v[56:59], v[200:203], a[220:223], v[56:59]\n"
"v_mfma_f32_16x16x32_bf16 v[36:39], v[204:207], a[208:211], v[36:39]\n"
"v_mfma_f32_16x16x32_bf16 v[44:47], v[204:207], a[212:215], v[44:47]\n"
"s_waitcnt lgkmcnt(2)\n"
"s_barrier\n"
"v_mfma_f32_16x16x32_bf16 v[52:55], v[204:207], a[216:219], v[52:55]\n"
"ds_read_b128 v[192:195], v242 offset:16384\n"
"v_mfma_f32_16x16x32_bf16 v[60:63], v[204:207], a[220:223], v[60:63]\n"
"ds_read_b64_tr_b16 a[234:235], v248 offset:8704\n"
"v_mfma_f32_16x16x32_bf16 v[64:67], v[208:211], a[208:211], v[64:67]\n"
"v_mfma_f32_16x16x32_bf16 v[72:75], v[208:211], a[212:215], v[72:75]\n"
"v_mfma_f32_16x16x32_bf16 v[80:83], v[208:211], a[216:219], v[80:83]\n"
"ds_read_b128 v[196:199], v242 offset:18432\n"
"v_mfma_f32_16x16x32_bf16 v[88:91], v[208:211], a[220:223], v[88:91]\n"
"ds_read_b64_tr_b16 a[236:237], v249 offset:8192\n"
"v_mfma_f32_16x16x32_bf16 v[68:71], v[212:215], a[208:211], v[68:71]\n"
"v_mfma_f32_16x16x32_bf16 v[76:79], v[212:215], a[212:215], v[76:79]\n"
"ds_read_b128 v[200:203], v242 offset:20480\n"
"v_mfma_f32_16x16x32_bf16 v[84:87], v[212:215], a[216:219], v[84:87]\n"
"ds_read_b64_tr_b16 a[238:239], v249 offset:8704\n"
"v_mfma_f32_16x16x32_bf16 v[92:95], v[212:215], a[220:223], v[92:95]\n"
"v_mfma_f32_16x16x32_bf16 v[96:99], v[216:219], a[208:211], v[96:99]\n"
"s_lshl_b32 s50, s35, 20\n"
"v_mfma_f32_16x16x32_bf16 v[104:107], v[216:219], a[212:215], v[104:107]\n"
"s_lshl_b32 s51, s34, 15\n"
"ds_read_b128 v[204:207], v242 offset:22528\n"
"v_mfma_f32_16x16x32_bf16 v[112:115], v[216:219], a[216:219], v[112:115]\n"
"s_add_u32 s50, s50, s51\n"
"s_waitcnt vmcnt(25)\n"
"ds_read_b64_tr_b16 a[240:241], v246 offset:12288\n"
"v_mfma_f32_16x16x32_bf16 v[120:123], v[216:219], a[220:223], v[120:123]\n"
"s_add_u32 m0, s37, 8192\n"
"s_add_u32 s48, s39, 65536\n"
"s_and_b32 s51, s30, 1\n"
"v_mfma_f32_16x16x32_bf16 v[100:103], v[220:223], a[208:211], v[100:103]\n"
"s_lshl_b32 s51, s51, 12\n"
"ds_read_b128 v[208:211], v242 offset:24576\n"
"v_mfma_f32_16x16x32_bf16 v[108:111], v[220:223], a[212:215], v[108:111]\n"
"s_waitcnt lgkmcnt(3)\n"
"buffer_load_dwordx4 v234, s[12:15], s48 offen offset:0 lds\n"
"v_mfma_f32_16x16x32_bf16 v[116:119], v[220:223], a[216:219], v[116:119]\n"
"s_add_u32 s50, s50, s51\n"
"ds_read_b64_tr_b16 a[242:243], v246 offset:12800\n"
"v_mov_b32 v227, 0\n"
"v_mfma_f32_16x16x32_bf16 v[124:127], v[220:223], a[220:223], v[124:127]\n"
"s_add_u32 s50, s50, 11010048\n"
"s_waitcnt vmcnt(14)\n"
"v_mov_b32 v226, v160\n"
"v_mfma_f32_16x16x32_bf16 v[0:3], v[192:195], a[224:227], v[0:3]\n"
"s_cmp_eq_u32 s31, 1\n"
"ds_read_b128 v[212:215], v242 offset:26624\n"
"v_add_u32 v250, s50, v224\n"
"v_mfma_f32_16x16x32_bf16 v[8:11], v[192:195], a[228:231], v[8:11]\n"
"s_cselect_b32 s94, 0, 2130706432\n"
"buffer_load_dwordx4 v235, s[12:15], s48 offen offset:1024 lds\n"
"v_mfma_f32_16x16x32_bf16 v[16:19], v[192:195], a[232:235], v[16:19]\n"
"buffer_store_dwordx2 v[232:233], v230, s[8:11], s94 offen sc1\n"
"v_mfma_f32_16x16x32_bf16 v[24:27], v[192:195], a[236:239], v[24:27]\n"
"ds_read_b64_tr_b16 a[244:245], v247 offset:12288\n"
"v_mov_b32 v228, 0\n"
"v_mfma_f32_16x16x32_bf16 v[4:7], v[196:199], a[224:227], v[4:7]\n"
"ds_read_b128 v[216:219], v242 offset:28672\n"
"v_med3_u32 v228, v227, v228, v161\n"
"v_mfma_f32_16x16x32_bf16 v[12:15], v[196:199], a[228:231], v[12:15]\n"
"buffer_load_dwordx4 v236, s[12:15], s48 offen offset:2048 lds\n"
"v_mfma_f32_16x16x32_bf16 v[20:23], v[196:199], a[232:235], v[20:23]\n"
"ds_read_b64_tr_b16 a[246:247], v247 offset:12800\n"
"v_med3_u32 v227, v226, v227, v161\n"
"v_mfma_f32_16x16x32_bf16 v[28:31], v[196:199], a[236:239], v[28:31]\n"
"ds_read_b128 v[220:223], v242 offset:30720\n"
"v_max_u32 v226, v226, v161\n"
"v_mfma_f32_16x16x32_bf16 v[32:35], v[200:203], a[224:227], v[32:35]\n"
"v_med3_u32 v228, v227, v228, v162\n"
"v_med3_u32 v227, v226, v227, v162\n"
"v_mfma_f32_16x16x32_bf16 v[40:43], v[200:203], a[228:231], v[40:43]\n"
"buffer_load_dwordx4 v237, s[12:15], s48 offen offset:3072 lds\n"
"v_mfma_f32_16x16x32_bf16 v[48:51], v[200:203], a[232:235], v[48:51]\n"
"ds_read_b128 v[192:195], v243 offset:16384\n"
"v_max_u32 v226, v226, v162\n"
"v_mfma_f32_16x16x32_bf16 v[56:59], v[200:203], a[236:239], v[56:59]\n"
"s_waitcnt lgkmcnt(3)\n"
"ds_read_b64_tr_b16 a[248:249], v248 offset:12288\n"
"v_med3_u32 v228, v227, v228, v163\n"
"v_mfma_f32_16x16x32_bf16 v[36:39], v[204:207], a[224:227], v[36:39]\n"
"v_med3_u32 v227, v226, v227, v163\n"
"v_max_u32 v226, v226, v163\n"
"v_mfma_f32_16x16x32_bf16 v[44:47], v[204:207], a[228:231], v[44:47]\n"
"s_waitcnt vmcnt(16)\n"
"v_med3_u32 v228, v227, v228, v164\n"
"v_med3_u32 v227, v226, v227, v164\n"
"v_mfma_f32_16x16x32_bf16 v[52:55], v[204:207], a[232:235], v[52:55]\n"
"ds_read_b128 v[196:199], v243 offset:18432\n"
"v_max_u32 v226, v226, v164\n"
"v_mfma_f32_16x16x32_bf16 v[60:63], v[204:207], a[236:239], v[60:63]\n"
"ds_read_b64_tr_b16 a[250:251], v248 offset:12800\n"
"v_med3_u32 v228, v227, v228, v165\n"
"v_mfma_f32_16x16x32_bf16 v[64:67], v[208:211], a[224:227], v[64:67]\n"
"v_med3_u32 v227, v226, v227, v165\n"
"v_max_u32 v226, v226, v165\n"
"v_mfma_f32_16x16x32_bf16 v[72:75], v[208:211], a[228:231], v[72:75]\n"
"ds_read_b128 v[200:203], v243 offset:20480\n"
"v_med3_u32 v228, v227, v228, v166\n"
"v_mfma_f32_16x16x32_bf16 v[80:83], v[208:211], a[232:235], v[80:83]\n"
"ds_read_b64_tr_b16 a[252:253], v249 offset:12288\n"
"v_med3_u32 v227, v226, v227, v166\n"
"v_mfma_f32_16x16x32_bf16 v[88:91], v[208:211], a[236:239], v[88:91]\n"
"v_max_u32 v226, v226, v166\n"
"v_med3_u32 v228, v227, v228, v167\n"
"v_mfma_f32_16x16x32_bf16 v[68:71], v[212:215], a[224:227], v[68:71]\n"
"v_med3_u32 v227, v226, v227, v167\n"
"v_max_u32 v226, v226, v167\n"
"v_mfma_f32_16x16x32_bf16 v[76:79], v[212:215], a[228:231], v[76:79]\n"
"ds_read_b128 v[204:207], v243 offset:22528\n"
"v_mfma_f32_16x16x32_bf16 v[84:87], v[212:215], a[232:235], v[84:87]\n"
"ds_read_b64_tr_b16 a[254:255], v249 offset:12800\n"
"v_mfma_f32_16x16x32_bf16 v[92:95], v[212:215], a[236:239], v[92:95]\n"
"s_waitcnt vmcnt(15)\n"
"v_med3_u32 v228, v227, v228, v168\n"
"v_med3_u32 v227, v226, v227, v168\n"
"v_mfma_f32_16x16x32_bf16 v[96:99], v[216:219], a[224:227], v[96:99]\n"
"ds_read_b128 v[208:211], v243 offset:24576\n"
"v_max_u32 v226, v226, v168\n"
"v_mfma_f32_16x16x32_bf16 v[104:107], v[216:219], a[228:231], v[104:107]\n"
"v_med3_u32 v228, v227, v228, v169\n"
"v_med3_u32 v227, v226, v227, v169\n"
"v_mfma_f32_16x16x32_bf16 v[112:115], v[216:219], a[232:235], v[112:115]\n"
"s_add_u32 m0, s37, 12288\n"
"v_max_u32 v226, v226, v169\n"
"v_med3_u32 v228, v227, v228, v170\n"
"v_mfma_f32_16x16x32_bf16 v[120:123], v[216:219], a[236:239], v[120:123]\n"
"s_waitcnt lgkmcnt(3)\n"
"v_med3_u32 v227, v226, v227, v170\n"
"v_max_u32 v226, v226, v170\n"
"v_mfma_f32_16x16x32_bf16 v[100:103], v[220:223], a[224:227], v[100:103]\n"
"s_add_u32 s48, s98, 65536\n"
"ds_read_b128 v[212:215], v243 offset:26624\n"
"v_med3_u32 v228, v227, v228, v171\n"
"v_mfma_f32_16x16x32_bf16 v[108:111], v[220:223], a[228:231], v[108:111]\n"
"s_waitcnt lgkmcnt(2)\n"
"buffer_load_dwordx4 v234, s[12:15], s48 offen offset:0 lds\n"
"v_mfma_f32_16x16x32_bf16 v[116:119], v[220:223], a[232:235], v[116:119]\n"
"v_med3_u32 v227, v226, v227, v171\n"
"v_max_u32 v226, v226, v171\n"
"v_mfma_f32_16x16x32_bf16 v[124:127], v[220:223], a[236:239], v[124:127]\n"
"ds_read_b128 v[216:219], v243 offset:28672\n"
"v_mfma_f32_16x16x32_bf16 v[0:3], v[192:195], a[240:243], v[0:3]\n"
"buffer_load_dwordx4 v235, s[12:15], s48 offen offset:1024 lds\n"
"v_mfma_f32_16x16x32_bf16 v[8:11], v[192:195], a[244:247], v[8:11]\n"
"s_waitcnt vmcnt(15)\n"
"v_med3_u32 v228, v227, v228, v172\n"
"v_med3_u32 v227, v226, v227, v172\n"
"v_mfma_f32_16x16x32_bf16 v[16:19], v[192:195], a[248:251], v[16:19]\n"
"v_max_u32 v226, v226, v172\n"
"v_med3_u32 v228, v227, v228, v173\n"
"v_mfma_f32_16x16x32_bf16 v[24:27], v[192:195], a[252:255], v[24:27]\n"
"ds_read_b128 v[220:223], v243 offset:30720\n"
"v_med3_u32 v227, v226, v227, v173\n"
"v_mfma_f32_16x16x32_bf16 v[4:7], v[196:199], a[240:243], v[4:7]\n"
"v_max_u32 v226, v226, v173\n"
"v_med3_u32 v228, v227, v228, v174\n"
"v_mfma_f32_16x16x32_bf16 v[12:15], v[196:199], a[244:247], v[12:15]\n"
"buffer_load_dwordx4 v236, s[12:15], s48 offen offset:2048 lds\n"
"v_mfma_f32_16x16x32_bf16 v[20:23], v[196:199], a[248:251], v[20:23]\n"
"v_med3_u32 v227, v226, v227, v174\n"
"v_max_u32 v226, v226, v174\n"
"v_mfma_f32_16x16x32_bf16 v[28:31], v[196:199], a[252:255], v[28:31]\n"
"v_med3_u32 v228, v227, v228, v175\n"
"v_med3_u32 v227, v226, v227, v175\n"
"v_mfma_f32_16x16x32_bf16 v[32:35], v[200:203], a[240:243], v[32:35]\n"
"v_max_u32 v226, v226, v175\n"
"v_mfma_f32_16x16x32_bf16 v[40:43], v[200:203], a[244:247], v[40:43]\n"
"buffer_load_dwordx4 v237, s[12:15], s48 offen offset:3072 lds\n"
"v_mfma_f32_16x16x32_bf16 v[48:51], v[200:203], a[248:251], v[48:51]\n"
"s_waitcnt vmcnt(16)\n"
"v_med3_u32 v228, v227, v228, v176\n"
"v_med3_u32 v227, v226, v227, v176\n"
"v_mfma_f32_16x16x32_bf16 v[56:59], v[200:203], a[252:255], v[56:59]\n"
"v_max_u32 v226, v226, v176\n"
"v_med3_u32 v228, v227, v228, v177\n"
"v_mfma_f32_16x16x32_bf16 v[36:39], v[204:207], a[240:243], v[36:39]\n"
"v_med3_u32 v227, v226, v227, v177\n"
"v_max_u32 v226, v226, v177\n"
"v_mfma_f32_16x16x32_bf16 v[44:47], v[204:207], a[244:247], v[44:47]\n"
"s_mov_b32 s27, 0x20000\n"
"s_waitcnt lgkmcnt(0)\n"
"s_barrier\n"
"v_mfma_f32_16x16x32_bf16 v[52:55], v[204:207], a[248:251], v[52:55]\n"
"s_lshr_b32 s92, s31, 3\n"
"v_med3_u32 v228, v227, v228, v178\n"
"v_med3_u32 v227, v226, v227, v178\n"
"v_mfma_f32_16x16x32_bf16 v[60:63], v[204:207], a[252:255], v[60:63]\n"
"s_mul_i32 s93, s32, 14\n"
"v_max_u32 v226, v226, v178\n"
"v_med3_u32 v228, v227, v228, v179\n"
"v_mfma_f32_16x16x32_bf16 v[64:67], v[208:211], a[240:243], v[64:67]\n"
"s_add_u32 s93, s93, s92\n"
"v_med3_u32 v227, v226, v227, v179\n"
"v_max_u32 v226, v226, v179\n"
"v_mfma_f32_16x16x32_bf16 v[72:75], v[208:211], a[244:247], v[72:75]\n"
"s_sub_u32 s93, s93, 18\n"
"s_waitcnt vmcnt(15)\n"
"v_med3_u32 v228, v227, v228, v180\n"
"v_mfma_f32_16x16x32_bf16 v[80:83], v[208:211], a[248:251], v[80:83]\n"
"s_cmp_lt_u32 s92, 18\n"
"v_med3_u32 v227, v226, v227, v180\n"
"v_max_u32 v226, v226, v180\n"
"v_mfma_f32_16x16x32_bf16 v[88:91], v[208:211], a[252:255], v[88:91]\n"
"s_cselect_b32 s93, 200, s93\n"
"v_add_u32 v244, 114688, v242\n"
"v_add_u32 v245, 114688, v243\n"
"v_mfma_f32_16x16x32_bf16 v[68:71], v[212:215], a[240:243], v[68:71]\n"
"s_cmp_lt_u32 s30, 2\n"
"v_med3_u32 v228, v227, v228, v181\n"
"v_med3_u32 v227, v226, v227, v181\n"
"v_mfma_f32_16x16x32_bf16 v[76:79], v[212:215], a[244:247], v[76:79]\n"
"s_cselect_b64 s[24:25], s[78:79], s[80:81]\n"
"v_add_u32 v242, 65536, v242\n"
"v_add_u32 v243, 65536, v243\n"
"v_mfma_f32_16x16x32_bf16 v[84:87], v[212:215], a[248:251], v[84:87]\n"
"s_and_b32 s25, s25, 0xffff\n"
"ds_read_b128 v[192:195], v242 offset:16384\n"
"v_max_u32 v226, v226, v181\n"
"v_mfma_f32_16x16x32_bf16 v[92:95], v[212:215], a[252:255], v[92:95]\n"
"s_cmp_lt_u32 s93, 108\n"
"ds_read_b128 v[196:199], v242 offset:18432\n"
"v_med3_u32 v228, v227, v228, v182\n"
"v_mfma_f32_16x16x32_bf16 v[96:99], v[216:219], a[240:243], v[96:99]\n"
"s_cselect_b32 s26, 2211840, 0\n"
"ds_read_b128 v[200:203], v243 offset:16384\n"
"v_med3_u32 v227, v226, v227, v182\n"
"v_mfma_f32_16x16x32_bf16 v[104:107], v[216:219], a[244:247], v[104:107]\n"
"s_cmp_lt_u32 s93, 108\n"
"v_max_u32 v226, v226, v182\n"
"v_med3_u32 v228, v227, v228, v183\n"
"v_mfma_f32_16x16x32_bf16 v[112:115], v[216:219], a[248:251], v[112:115]\n"
"s_cselect_b32 s44, 0, 2130706432\n"
"ds_read_b128 v[204:207], v243 offset:18432\n"
"v_med3_u32 v227, v226, v227, v183\n"
"v_mfma_f32_16x16x32_bf16 v[120:123], v[216:219], a[252:255], v[120:123]\n"
"s_and_b32 s92, s30, 1\n"
"v_max_u32 v226, v226, v183\n"
"s_mul_i32 s61, s92, 1105920\n"
"v_mfma_f32_16x16x32_bf16 v[100:103], v[220:223], a[240:243], v[100:103]\n"
"s_lshl_b32 s92, s93, 5\n"
"s_waitcnt vmcnt(14)\n"
"v_med3_u32 v228, v227, v228, v184\n"
"v_mfma_f32_16x16x32_bf16 v[108:111], v[220:223], a[244:247], v[108:111]\n"
"s_add_u32 s61, s61, s92\n"
"ds_read_b128 v[208:211], v242 offset:20480\n"
"v_med3_u32 v227, v226, v227, v184\n"
"v_mfma_f32_16x16x32_bf16 v[116:119], v[220:223], a[248:251], v[116:119]\n"
"s_lshr_b32 s92, s93, 1\n"
"v_max_u32 v226, v226, v184\n"
"v_med3_u32 v228, v227, v228, v185\n"
"v_mfma_f32_16x16x32_bf16 v[124:127], v[220:223], a[252:255], v[124:127]\n"
"s_waitcnt lgkmcnt(2)\n"
"v_med3_u32 v227, v226, v227, v185\n"
"v_max_u32 v226, v226, v185\n"
"v_mfma_f32_16x16x32_bf16 v[128:131], v[192:195], a[0:3], 0\n"
"s_lshl_b32 s92, s92, 9\n"
"v_med3_u32 v228, v227, v228, v186\n"
"v_med3_u32 v227, v226, v227, v186\n"
"v_mfma_f32_16x16x32_bf16 v[136:139], v[192:195], a[4:7], 0\n"
"s_and_b32 s60, s93, 1\n"
"ds_read_b128 v[212:215], v242 offset:22528\n"
"v_max_u32 v226, v226, v186\n"
"v_mfma_f32_16x16x32_bf16 v[144:147], v[192:195], a[8:11], 0\n"
"s_lshl_b32 s60, s60, 3\n"
"v_lshrrev_b32 v251, 5, v224\n"
"v_med3_u32 v228, v227, v228, v187\n"
"v_mfma_f32_16x16x32_bf16 v[152:155], v[192:195], a[12:15], 0\n"
"s_add_u32 s60, s60, s92\n"
"v_mul_u32_u24 v251, 3456, v251\n"
"v_med3_u32 v227, v226, v227, v187\n"
"v_mfma_f32_16x16x32_bf16 v[132:135], v[196:199], a[0:3], 0\n"
"s_add_u32 s60, s60, 2695168\n"
"v_and_b32 v252, 16, v224\n"
"v_max_u32 v226, v226, v187\n"
"v_mfma_f32_16x16x32_bf16 v[140:143], v[196:199], a[4:7], 0\n"
"s_lshl_b32 s92, s30, 8\n"
"ds_read_b128 v[216:219], v243 offset:20480\n"
"v_add_u32 v251, v251, v252\n"
"v_mfma_f32_16x16x32_bf16 v[148:151], v[196:199], a[8:11], 0\n"
"s_add_u32 s93, s93, s92\n"
"s_waitcnt vmcnt(13)\n"
"v_med3_u32 v228, v227, v228, v188\n"
"v_mfma_f32_16x16x32_bf16 v[156:159], v[196:199], a[12:15], 0\n"
"s_cmp_lt_u32 s93, 108\n"
"v_med3_u32 v227, v226, v227, v188\n"
"v_max_u32 v226, v226, v188\n"
"v_mfma_f32_16x16x32_bf16 v[128:131], v[200:203], a[16:19], v[128:131]\n"
"s_cselect_b32 s60, s60, 2130706432\n"
"v_med3_u32 v228, v227, v228, v189\n"
"v_med3_u32 v227, v226, v227, v189\n"
"v_mfma_f32_16x16x32_bf16 v[136:139], v[200:203], a[20:23], v[136:139]\n"
"s_mul_i32 s92, s30, 10240\n"
"ds_read_b128 v[220:223], v243 offset:22528\n"
"v_max_u32 v226, v226, v189\n"
"v_mfma_f32_16x16x32_bf16 v[144:147], v[200:203], a[24:27], v[144:147]\n"
"s_add_u32 m0, s92, 0\n"
"v_med3_u32 v228, v227, v228, v190\n"
"v_med3_u32 v227, v226, v227, v190\n"
"v_mfma_f32_16x16x32_bf16 v[152:155], v[200:203], a[28:31], v[152:155]\n"
"s_waitcnt lgkmcnt(2)\n"
"v_max_u32 v226, v226, v190\n"
"v_med3_u32 v228, v227, v228, v191\n"
"v_mfma_f32_16x16x32_bf16 v[132:135], v[204:207], a[16:19], v[132:135]\n"
"s_add_u32 s48, s61, 0\n"
"ds_read_b128 v[192:195], v242 offset:40960\n"
"v_med3_u32 v227, v226, v227, v191\n"
"v_mfma_f32_16x16x32_bf16 v[140:143], v[204:207], a[20:23], v[140:143]\n"
"s_mov_b32 s49, 0\n"
"buffer_load_dwordx4 v251, s[24:27], s48 offen lds\n"
"v_mfma_f32_16x16x32_bf16 v[148:151], v[204:207], a[24:27], v[148:151]\n"
"v_max_u32 v226, v226, v191\n"
"v_mov_b32 v229, v226\n"
"v_mfma_f32_16x16x32_bf16 v[156:159], v[204:207], a[28:31], v[156:159]\n"
"v_cvt_pk_bf16_f32 v0, v0, v1\n"
"v_cvt_pk_bf16_f32 v1, v2, v3\n"
"v_mfma_f32_16x16x32_bf16 v[128:131], v[208:211], a[32:35], v[128:131]\n"
"ds_read_b128 v[196:199], v242 offset:43008\n"
"v_max_u32_dpp v229, v229, v229 row_shr:1 row_mask:0xf bank_mask:0xf\n"
"v_mfma_f32_16x16x32_bf16 v[136:139], v[208:211], a[36:39], v[136:139]\n"
"v_cvt_pk_bf16_f32 v2, v4, v5\n"
"v_cvt_pk_bf16_f32 v3, v6, v7\n"
"v_mfma_f32_16x16x32_bf16 v[144:147], v[208:211], a[40:43], v[144:147]\n"
"buffer_store_dwordx4 v[0:3], v250, s[8:11], s49 offen offset:0 sc1\n"
"v_mfma_f32_16x16x32_bf16 v[152:155], v[208:211], a[44:47], v[152:155]\n"
"v_max_u32_dpp v229, v229, v229 row_shr:2 row_mask:0xf bank_mask:0xf\n"
"v_cvt_pk_bf16_f32 v8, v8, v9\n"
"v_mfma_f32_16x16x32_bf16 v[132:135], v[212:215], a[32:35], v[132:135]\n"
"s_add_u32 m0, s92, 1024\n"
"ds_read_b128 v[200:203], v243 offset:40960\n"
"v_max_u32_dpp v229, v229, v229 row_shr:4 row_mask:0xf bank_mask:0xf\n"
"v_mfma_f32_16x16x32_bf16 v[140:143], v[212:215], a[36:39], v[140:143]\n"
"s_add_u32 s48, s61, 110592\n"
"v_max_u32_dpp v229, v229, v229 row_shr:8 row_mask:0xf bank_mask:0xf\n"
"v_cvt_pk_bf16_f32 v9, v10, v11\n"
"v_mfma_f32_16x16x32_bf16 v[148:151], v[212:215], a[40:43], v[148:151]\n"
"buffer_load_dwordx4 v251, s[24:27], s48 offen lds\n"
"v_mfma_f32_16x16x32_bf16 v[156:159], v[212:215], a[44:47], v[156:159]\n"
"s_waitcnt lgkmcnt(1)\n"
"v_max_u32_dpp v229, v229, v229 row_bcast:15 row_mask:0xa bank_mask:0xf\n"
"v_cvt_pk_bf16_f32 v10, v12, v13\n"
"v_mfma_f32_16x16x32_bf16 v[128:131], v[216:219], a[48:51], v[128:131]\n"
"ds_read_b128 v[204:207], v243 offset:43008\n"
"v_cvt_pk_bf16_f32 v11, v14, v15\n"
"v_mfma_f32_16x16x32_bf16 v[136:139], v[216:219], a[52:55], v[136:139]\n"
"v_max_u32_dpp v229, v229, v229 row_bcast:31 row_mask:0xc bank_mask:0xf\n"
"v_cvt_pk_bf16_f32 v16, v16, v17\n"
"v_mfma_f32_16x16x32_bf16 v[144:147], v[216:219], a[56:59], v[144:147]\n"
"v_readlane_b32 s62, v229, 63\n"
"v_cvt_pk_bf16_f32 v17, v18, v19\n"
"v_mfma_f32_16x16x32_bf16 v[152:155], v[216:219], a[60:63], v[152:155]\n"
"ds_read_b128 v[208:211], v242 offset:45056\n"
"v_cmp_eq_u32 vcc, s62, v226\n"
"v_mfma_f32_16x16x32_bf16 v[132:135], v[220:223], a[48:51], v[132:135]\n"
"v_writelane_b32 v225, s62, 0\n"
"v_cvt_pk_bf16_f32 v18, v20, v21\n"
"v_mfma_f32_16x16x32_bf16 v[140:143], v[220:223], a[52:55], v[140:143]\n"
"v_cvt_pk_bf16_f32 v19, v22, v23\n"
"v_cndmask_b32 v226, v226, v227, vcc\n"
"v_mfma_f32_16x16x32_bf16 v[148:151], v[220:223], a[56:59], v[148:151]\n"
"buffer_store_dwordx4 v[8:11], v250, s[8:11], s49 offen offset:1024 sc1\n"
"v_mfma_f32_16x16x32_bf16 v[156:159], v[220:223], a[60:63], v[156:159]\n"
"ds_read_b128 v[212:215], v242 offset:47104\n"
"v_cndmask_b32_e64 v227, v227, 0, vcc\n"
"v_mfma_f32_16x16x32_bf16 v[128:131], v[192:195], a[64:67], v[128:131]\n"
"v_mov_b32 v229, v226\n"
"v_cvt_pk_bf16_f32 v24, v24, v25\n"
"v_mfma_f32_16x16x32_bf16 v[136:139], v[192:195], a[68:71], v[136:139]\n"
"v_max_u32_dpp v229, v229, v229 row_shr:1 row_mask:0xf bank_mask:0xf\n"
"v_cvt_pk_bf16_f32 v25, v26, v27\n"
"v_mfma_f32_16x16x32_bf16 v[144:147], v[192:195], a[72:75], v[144:147]\n"
"s_add_u32 m0, s92, 2048\n"
"v_max_u32_dpp v229, v229, v229 row_shr:2 row_mask:0xf bank_mask:0xf\n"
"v_cvt_pk_bf16_f32 v26, v28, v29\n"
"v_mfma_f32_16x16x32_bf16 v[152:155], v[192:195], a[76:79], v[152:155]\n"
"s_add_u32 s48, s61, 221184\n"
"ds_read_b128 v[216:219], v243 offset:45056\n"
"v_cvt_pk_bf16_f32 v27, v30, v31\n"
"v_mfma_f32_16x16x32_bf16 v[132:135], v[196:199], a[64:67], v[132:135]\n"
"buffer_load_dwordx4 v251, s[24:27], s48 offen lds\n"
"v_mfma_f32_16x16x32_bf16 v[140:143], v[196:199], a[68:71], v[140:143]\n"
"v_max_u32_dpp v229, v229, v229 row_shr:4 row_mask:0xf bank_mask:0xf\n"
"v_cvt_pk_bf16_f32 v32, v32, v33\n"
"v_mfma_f32_16x16x32_bf16 v[148:151], v[196:199], a[72:75], v[148:151]\n"
"v_max_u32_dpp v229, v229, v229 row_shr:8 row_mask:0xf bank_mask:0xf\n"
"v_cvt_pk_bf16_f32 v33, v34, v35\n"
"v_mfma_f32_16x16x32_bf16 v[156:159], v[196:199], a[76:79], v[156:159]\n"
"s_waitcnt lgkmcnt(1)\n"
"ds_read_b128 v[220:223], v243 offset:47104\n"
"v_max_u32_dpp v229, v229, v229 row_bcast:15 row_mask:0xa bank_mask:0xf\n"
"v_mfma_f32_16x16x32_bf16 v[128:131], v[200:203], a[80:83], v[128:131]\n"
"v_cvt_pk_bf16_f32 v34, v36, v37\n"
"v_max_u32_dpp v229, v229, v229 row_bcast:31 row_mask:0xc bank_mask:0xf\n"
"v_mfma_f32_16x16x32_bf16 v[136:139], v[200:203], a[84:87], v[136:139]\n"
"v_cvt_pk_bf16_f32 v35, v38, v39\n"
"v_readlane_b32 s62, v229, 63\n"
"v_mfma_f32_16x16x32_bf16 v[144:147], v[200:203], a[88:91], v[144:147]\n"
"ds_read_b128 v[192:195], v244 offset:16384\n"
"v_cmp_eq_u32 vcc, s62, v226\n"
"v_mfma_f32_16x16x32_bf16 v[152:155], v[200:203], a[92:95], v[152:155]\n"
"v_writelane_b32 v225, s62, 1\n"
"v_cvt_pk_bf16_f32 v40, v40, v41\n"
"v_mfma_f32_16x16x32_bf16 v[132:135], v[204:207], a[80:83], v[132:135]\n"
"v_cvt_pk_bf16_f32 v41, v42, v43\n"
"v_cndmask_b32 v226, v226, v227, vcc\n"
"v_mfma_f32_16x16x32_bf16 v[140:143], v[204:207], a[84:87], v[140:143]\n"
"v_cvt_pk_bf16_f32 v42, v44, v45\n"
"v_cvt_pk_bf16_f32 v43, v46, v47\n"
"v_mfma_f32_16x16x32_bf16 v[148:151], v[204:207], a[88:91], v[148:151]\n"
"ds_read_b128 v[196:199], v244 offset:18432\n"
"v_cndmask_b32_e64 v227, v227, 0, vcc\n"
"v_mfma_f32_16x16x32_bf16 v[156:159], v[204:207], a[92:95], v[156:159]\n"
"v_mov_b32 v229, v226\n"
"v_cvt_pk_bf16_f32 v48, v48, v49\n"
"v_mfma_f32_16x16x32_bf16 v[128:131], v[208:211], a[96:99], v[128:131]\n"
"s_add_u32 m0, s92, 3072\n"
"buffer_store_dwordx4 v[16:19], v250, s[8:11], s49 offen offset:2048 sc1\n"
"v_mfma_f32_16x16x32_bf16 v[136:139], v[208:211], a[100:103], v[136:139]\n"
"s_add_u32 s48, s61, 331776\n"
"v_max_u32_dpp v229, v229, v229 row_shr:1 row_mask:0xf bank_mask:0xf\n"
"v_cvt_pk_bf16_f32 v49, v50, v51\n"
"v_mfma_f32_16x16x32_bf16 v[144:147], v[208:211], a[104:107], v[144:147]\n"
"ds_read_b128 v[200:203], v245 offset:16384\n"
"v_max_u32_dpp v229, v229, v229 row_shr:2 row_mask:0xf bank_mask:0xf\n"
"v_mfma_f32_16x16x32_bf16 v[152:155], v[208:211], a[108:111], v[152:155]\n"
"buffer_load_dwordx4 v251, s[24:27], s48 offen lds\n"
"v_mfma_f32_16x16x32_bf16 v[132:135], v[212:215], a[96:99], v[132:135]\n"
"v_cvt_pk_bf16_f32 v50, v52, v53\n"
"v_cvt_pk_bf16_f32 v51, v54, v55\n"
"v_mfma_f32_16x16x32_bf16 v[140:143], v[212:215], a[100:103], v[140:143]\n"
"v_max_u32_dpp v229, v229, v229 row_shr:4 row_mask:0xf bank_mask:0xf\n"
"v_cvt_pk_bf16_f32 v56, v56, v57\n"
"v_mfma_f32_16x16x32_bf16 v[148:151], v[212:215], a[104:107], v[148:151]\n"
"ds_read_b128 v[204:207], v245 offset:18432\n"
"v_max_u32_dpp v229, v229, v229 row_shr:8 row_mask:0xf bank_mask:0xf\n"
"v_mfma_f32_16x16x32_bf16 v[156:159], v[212:215], a[108:111], v[156:159]\n"
"s_waitcnt lgkmcnt(2)\n"
"v_max_u32_dpp v229, v229, v229 row_bcast:15 row_mask:0xa bank_mask:0xf\n"
"v_cvt_pk_bf16_f32 v57, v58, v59\n"
"v_mfma_f32_16x16x32_bf16 v[128:131], v[216:219], a[112:115], v[128:131]\n"
"v_max_u32_dpp v229, v229, v229 row_bcast:31 row_mask:0xc bank_mask:0xf\n"
"v_cvt_pk_bf16_f32 v58, v60, v61\n"
"v_mfma_f32_16x16x32_bf16 v[136:139], v[216:219], a[116:119], v[136:139]\n"
"ds_read_b128 v[208:211], v244 offset:20480\n"
"v_cvt_pk_bf16_f32 v59, v62, v63\n"
"v_mfma_f32_16x16x32_bf16 v[144:147], v[216:219], a[120:123], v[144:147]\n"
"v_readlane_b32 s62, v229, 63\n"
"v_cvt_pk_bf16_f32 v64, v64, v65\n"
"v_mfma_f32_16x16x32_bf16 v[152:155], v[216:219], a[124:127], v[152:155]\n"
"v_cmp_eq_u32 vcc, s62, v226\n"
"v_writelane_b32 v225, s62, 2\n"
"v_mfma_f32_16x16x32_bf16 v[132:135], v[220:223], a[112:115], v[132:135]\n"
"v_cndmask_b32 v226, v226, v227, vcc\n"
"v_cvt_pk_bf16_f32 v65, v66, v67\n"
"v_mfma_f32_16x16x32_bf16 v[140:143], v[220:223], a[116:119], v[140:143]\n"
"ds_read_b128 v[212:215], v244 offset:22528\n"
"v_cvt_pk_bf16_f32 v66, v68, v69\n"
"v_mfma_f32_16x16x32_bf16 v[148:151], v[220:223], a[120:123], v[148:151]\n"
"s_add_u32 m0, s92, 4096\n"
"v_cvt_pk_bf16_f32 v67, v70, v71\n"
"v_cndmask_b32_e64 v227, v227, 0, vcc\n"
"v_mfma_f32_16x16x32_bf16 v[156:159], v[220:223], a[124:127], v[156:159]\n"
"s_add_u32 s48, s61, 442368\n"
"v_mov_b32 v229, v226\n"
"v_cvt_pk_bf16_f32 v72, v72, v73\n"
"v_mfma_f32_16x16x32_bf16 v[128:131], v[192:195], a[128:131], v[128:131]\n"
"buffer_load_dwordx4 v251, s[24:27], s48 offen lds\n"
"v_mfma_f32_16x16x32_bf16 v[136:139], v[192:195], a[132:135], v[136:139]\n"
"ds_read_b128 v[216:219], v245 offset:20480\n"
"v_max_u32_dpp v229, v229, v229 row_shr:1 row_mask:0xf bank_mask:0xf\n"
"v_mfma_f32_16x16x32_bf16 v[144:147], v[192:195], a[136:139], v[144:147]\n"
"buffer_store_dwordx4 v[24:27], v250, s[8:11], s49 offen offset:3072 sc1\n"
"v_mfma_f32_16x16x32_bf16 v[152:155], v[192:195], a[140:143], v[152:155]\n"
"v_max_u32_dpp v229, v229, v229 row_shr:2 row_mask:0xf bank_mask:0xf\n"
"v_cvt_pk_bf16_f32 v73, v74, v75\n"
"v_mfma_f32_16x16x32_bf16 v[132:135], v[196:199], a[128:131], v[132:135]\n"
"v_max_u32_dpp v229, v229, v229 row_shr:4 row_mask:0xf bank_mask:0xf\n"
"v_cvt_pk_bf16_f32 v74, v76, v77\n"
"v_mfma_f32_16x16x32_bf16 v[140:143], v[196:199], a[132:135], v[140:143]\n"
"ds_read_b128 v[220:223], v245 offset:22528\n"
"v_cvt_pk_bf16_f32 v75, v78, v79\n"
"v_mfma_f32_16x16x32_bf16 v[148:151], v[196:199], a[136:139], v[148:151]\n"
"v_max_u32_dpp v229, v229, v229 row_shr:8 row_mask:0xf bank_mask:0xf\n"
"v_cvt_pk_bf16_f32 v80, v80, v81\n"
"v_mfma_f32_16x16x32_bf16 v[156:159], v[196:199], a[140:143], v[156:159]\n"
"s_waitcnt lgkmcnt(2)\n"
"v_max_u32_dpp v229, v229, v229 row_bcast:15 row_mask:0xa bank_mask:0xf\n"
"v_cvt_pk_bf16_f32 v81, v82, v83\n"
"v_mfma_f32_16x16x32_bf16 v[128:131], v[200:203], a[144:147], v[128:131]\n"
"ds_read_b128 v[192:195], v244 offset:40960\n"
"v_max_u32_dpp v229, v229, v229 row_bcast:31 row_mask:0xc bank_mask:0xf\n"
"v_mfma_f32_16x16x32_bf16 v[136:139], v[200:203], a[148:151], v[136:139]\n"
"v_readlane_b32 s62, v229, 63\n"
"v_cvt_pk_bf16_f32 v82, v84, v85\n"
"v_mfma_f32_16x16x32_bf16 v[144:147], v[200:203], a[152:155], v[144:147]\n"
"v_cvt_pk_bf16_f32 v83, v86, v87\n"
"v_cmp_eq_u32 vcc, s62, v226\n"
"v_mfma_f32_16x16x32_bf16 v[152:155], v[200:203], a[156:159], v[152:155]\n"
"v_writelane_b32 v225, s62, 3\n"
"v_cndmask_b32 v226, v226, v227, vcc\n"
"v_mfma_f32_16x16x32_bf16 v[132:135], v[204:207], a[144:147], v[132:135]\n"
"s_add_u32 m0, s92, 5120\n"
"ds_read_b128 v[196:199], v244 offset:43008\n"
"v_cvt_pk_bf16_f32 v88, v88, v89\n"
"v_mfma_f32_16x16x32_bf16 v[140:143], v[204:207], a[148:151], v[140:143]\n"
"s_add_u32 s48, s61, 552960\n"
"v_cvt_pk_bf16_f32 v89, v90, v91\n"
"v_cndmask_b32_e64 v227, v227, 0, vcc\n"
"v_mfma_f32_16x16x32_bf16 v[148:151], v[204:207], a[152:155], v[148:151]\n"
"buffer_load_dwordx4 v251, s[24:27], s48 offen lds\n"
"v_mfma_f32_16x16x32_bf16 v[156:159], v[204:207], a[156:159], v[156:159]\n"
"v_cvt_pk_bf16_f32 v90, v92, v93\n"
"v_cvt_pk_bf16_f32 v91, v94, v95\n"
"v_mfma_f32_16x16x32_bf16 v[128:131], v[208:211], a[160:163], v[128:131]\n"
"ds_read_b128 v[200:203], v245 offset:40960\n"
"v_mov_b32 v229, v226\n"
"v_mfma_f32_16x16x32_bf16 v[136:139], v[208:211], a[164:167], v[136:139]\n"
"v_cvt_pk_bf16_f32 v96, v96, v97\n"
"v_max_u32_dpp v229, v229, v229 row_shr:1 row_mask:0xf bank_mask:0xf\n"
"v_mfma_f32_16x16x32_bf16 v[144:147], v[208:211], a[168:171], v[144:147]\n"
"s_mov_b32 s49, 8192\n"
"v_max_u32_dpp v229, v229, v229 row_shr:2 row_mask:0xf bank_mask:0xf\n"
"v_cvt_pk_bf16_f32 v97, v98, v99\n"
"v_mfma_f32_16x16x32_bf16 v[152:155], v[208:211], a[172:175], v[152:155]\n"
"v_max_u32_dpp v229, v229, v229 row_shr:4 row_mask:0xf bank_mask:0xf\n"
"v_cvt_pk_bf16_f32 v98, v100, v101\n"
"v_mfma_f32_16x16x32_bf16 v[132:135], v[212:215], a[160:163], v[132:135]\n"
"ds_read_b128 v[204:207], v245 offset:43008\n"
"v_cvt_pk_bf16_f32 v99, v102, v103\n"
"v_mfma_f32_16x16x32_bf16 v[140:143], v[212:215], a[164:167], v[140:143]\n"
"buffer_store_dwordx4 v[32:35], v250, s[8:11], s49 offen offset:0 sc1\n"
"v_mfma_f32_16x16x32_bf16 v[148:151], v[212:215], a[168:171], v[148:151]\n"
"v_max_u32_dpp v229, v229, v229 row_shr:8 row_mask:0xf bank_mask:0xf\n"
"v_cvt_pk_bf16_f32 v104, v104, v105\n"
"v_mfma_f32_16x16x32_bf16 v[156:159], v[212:215], a[172:175], v[156:159]\n"
"s_waitcnt lgkmcnt(1)\n"
"ds_read_b128 v[208:211], v244 offset:45056\n"
"v_max_u32_dpp v229, v229, v229 row_bcast:15 row_mask:0xa bank_mask:0xf\n"
"v_mfma_f32_16x16x32_bf16 v[128:131], v[216:219], a[176:179], v[128:131]\n"
"v_cvt_pk_bf16_f32 v105, v106, v107\n"
"v_max_u32_dpp v229, v229, v229 row_bcast:31 row_mask:0xc bank_mask:0xf\n"
"v_mfma_f32_16x16x32_bf16 v[136:139], v[216:219], a[180:183], v[136:139]\n"
"v_readlane_b32 s62, v229, 63\n"
"v_cvt_pk_bf16_f32 v106, v108, v109\n"
"v_mfma_f32_16x16x32_bf16 v[144:147], v[216:219], a[184:187], v[144:147]\n"
"s_add_u32 m0, s92, 6144\n"
"v_cvt_pk_bf16_f32 v107, v110, v111\n"
"v_cmp_eq_u32 vcc, s62, v226\n"
"v_mfma_f32_16x16x32_bf16 v[152:155], v[216:219], a[188:191], v[152:155]\n"
"s_add_u32 s48, s61, 663552\n"
"ds_read_b128 v[212:215], v244 offset:47104\n"
"v_writelane_b32 v225, s62, 4\n"
"v_mfma_f32_16x16x32_bf16 v[132:135], v[220:223], a[176:179], v[132:135]\n"
"buffer_load_dwordx4 v251, s[24:27], s48 offen lds\n"
"v_mfma_f32_16x16x32_bf16 v[140:143], v[220:223], a[180:183], v[140:143]\n"
"v_cndmask_b32 v226, v226, v227, vcc\n"
"v_cndmask_b32_e64 v227, v227, 0, vcc\n"
"v_mfma_f32_16x16x32_bf16 v[148:151], v[220:223], a[184:187], v[148:151]\n"
"v_cvt_pk_bf16_f32 v112, v112, v113\n"
"v_cvt_pk_bf16_f32 v113, v114, v115\n"
"v_mfma_f32_16x16x32_bf16 v[156:159], v[220:223], a[188:191], v[156:159]\n"
"ds_read_b128 v[216:219], v245 offset:45056\n"
"v_cvt_pk_bf16_f32 v114, v116, v117\n"
"v_mfma_f32_16x16x32_bf16 v[128:131], v[192:195], a[192:195], v[128:131]\n"
"v_cvt_pk_bf16_f32 v115, v118, v119\n"
"v_mov_b32 v229, v226\n"
"v_mfma_f32_16x16x32_bf16 v[136:139], v[192:195], a[196:199], v[136:139]\n"
"v_cvt_pk_bf16_f32 v120, v120, v121\n"
"v_max_u32_dpp v229, v229, v229 row_shr:1 row_mask:0xf bank_mask:0xf\n"
"v_mfma_f32_16x16x32_bf16 v[144:147], v[192:195], a[200:203], v[144:147]\n"
"v_cvt_pk_bf16_f32 v121, v122, v123\n"
"v_max_u32_dpp v229, v229, v229 row_shr:2 row_mask:0xf bank_mask:0xf\n"
"v_mfma_f32_16x16x32_bf16 v[152:155], v[192:195], a[204:207], v[152:155]\n"
"ds_read_b128 v[220:223], v245 offset:47104\n"
"v_max_u32_dpp v229, v229, v229 row_shr:4 row_mask:0xf bank_mask:0xf\n"
"v_mfma_f32_16x16x32_bf16 v[132:135], v[196:199], a[192:195], v[132:135]\n"
"v_cvt_pk_bf16_f32 v122, v124, v125\n"
"v_max_u32_dpp v229, v229, v229 row_shr:8 row_mask:0xf bank_mask:0xf\n"
"v_mfma_f32_16x16x32_bf16 v[140:143], v[196:199], a[196:199], v[140:143]\n"
"v_cvt_pk_bf16_f32 v123, v126, v127\n"
"v_max_u32_dpp v229, v229, v229 row_bcast:15 row_mask:0xa bank_mask:0xf\n"
"v_mfma_f32_16x16x32_bf16 v[148:151], v[196:199], a[200:203], v[148:151]\n"
"buffer_store_dwordx4 v[40:43], v250, s[8:11], s49 offen offset:1024 sc1\n"
"v_mfma_f32_16x16x32_bf16 v[156:159], v[196:199], a[204:207], v[156:159]\n"
"v_max_u32_dpp v229, v229, v229 row_bcast:31 row_mask:0xc bank_mask:0xf\n"
"v_mfma_f32_16x16x32_bf16 v[128:131], v[200:203], a[208:211], v[128:131]\n"
"s_add_u32 m0, s92, 7168\n"
"v_readlane_b32 s62, v229, 63\n"
"s_add_u32 s48, s61, 774144\n"
"v_mfma_f32_16x16x32_bf16 v[136:139], v[200:203], a[212:215], v[136:139]\n"
"v_cmp_eq_u32 vcc, s62, v226\n"
"v_writelane_b32 v225, s62, 5\n"
"v_mfma_f32_16x16x32_bf16 v[144:147], v[200:203], a[216:219], v[144:147]\n"
"buffer_load_dwordx4 v251, s[24:27], s48 offen lds\n"
"v_mfma_f32_16x16x32_bf16 v[152:155], v[200:203], a[220:223], v[152:155]\n"
"s_waitcnt lgkmcnt(0)\n"
"v_cndmask_b32 v226, v226, v227, vcc\n"
"v_cndmask_b32_e64 v227, v227, 0, vcc\n"
"v_mfma_f32_16x16x32_bf16 v[132:135], v[204:207], a[208:211], v[132:135]\n"
"v_mov_b32 v229, v226\n"
"v_mfma_f32_16x16x32_bf16 v[140:143], v[204:207], a[212:215], v[140:143]\n"
"s_waitcnt vmcnt(23)\n"
"s_barrier\n"
"v_mfma_f32_16x16x32_bf16 v[148:151], v[204:207], a[216:219], v[148:151]\n"
"s_add_u32 m0, s37, 16384\n"
"ds_read_b128 v[192:195], v242 offset:0\n"
"v_max_u32_dpp v229, v229, v229 row_shr:1 row_mask:0xf bank_mask:0xf\n"
"v_mfma_f32_16x16x32_bf16 v[156:159], v[204:207], a[220:223], v[156:159]\n"
"s_add_u32 s48, s39, 98304\n"
"v_max_u32_dpp v229, v229, v229 row_shr:2 row_mask:0xf bank_mask:0xf\n"
"v_mfma_f32_16x16x32_bf16 v[128:131], v[208:211], a[224:227], v[128:131]\n"
"buffer_load_dwordx4 v234, s[12:15], s48 offen offset:0 lds\n"
"v_mfma_f32_16x16x32_bf16 v[136:139], v[208:211], a[228:231], v[136:139]\n"
"v_max_u32_dpp v229, v229, v229 row_shr:4 row_mask:0xf bank_mask:0xf\n"
"v_mfma_f32_16x16x32_bf16 v[144:147], v[208:211], a[232:235], v[144:147]\n"
"ds_read_b128 v[196:199], v242 offset:2048\n"
"v_max_u32_dpp v229, v229, v229 row_shr:8 row_mask:0xf bank_mask:0xf\n"
"v_mfma_f32_16x16x32_bf16 v[152:155], v[208:211], a[236:239], v[152:155]\n"
"v_mfma_f32_16x16x32_bf16 v[132:135], v[212:215], a[224:227], v[132:135]\n"
"v_max_u32_dpp v229, v229, v229 row_bcast:15 row_mask:0xa bank_mask:0xf\n"
"v_mfma_f32_16x16x32_bf16 v[140:143], v[212:215], a[228:231], v[140:143]\n"
"buffer_load_dwordx4 v235, s[12:15], s48 offen offset:1024 lds\n"
"v_mfma_f32_16x16x32_bf16 v[148:151], v[212:215], a[232:235], v[148:151]\n"
"ds_read_b128 v[200:203], v243 offset:0\n"
"v_max_u32_dpp v229, v229, v229 row_bcast:31 row_mask:0xc bank_mask:0xf\n"
"v_mfma_f32_16x16x32_bf16 v[156:159], v[212:215], a[236:239], v[156:159]\n"
"v_readlane_b32 s62, v229, 63\n"
"v_mfma_f32_16x16x32_bf16 v[128:131], v[216:219], a[240:243], v[128:131]\n"
"buffer_store_dwordx4 v[48:51], v250, s[8:11], s49 offen offset:2048 sc1\n"
"v_mfma_f32_16x16x32_bf16 v[136:139], v[216:219], a[244:247], v[136:139]\n"
"v_cmp_eq_u32 vcc, s62, v226\n"
"v_writelane_b32 v225, s62, 6\n"
"v_mfma_f32_16x16x32_bf16 v[144:147], v[216:219], a[248:251], v[144:147]\n"
"ds_read_b128 v[204:207], v243 offset:2048\n"
"v_cndmask_b32 v226, v226, v227, vcc\n"
"v_mfma_f32_16x16x32_bf16 v[152:155], v[216:219], a[252:255], v[152:155]\n"
"buffer_load_dwordx4 v236, s[12:15], s48 offen offset:2048 lds\n"
"v_mfma_f32_16x16x32_bf16 v[132:135], v[220:223], a[240:243], v[132:135]\n"
"v_cndmask_b32_e64 v227, v227, 0, vcc\n"
"v_mov_b32 v229, v226\n"
"v_mfma_f32_16x16x32_bf16 v[140:143], v[220:223], a[244:247], v[140:143]\n"
"ds_read_b128 v[208:211], v242 offset:4096\n"
"v_max_u32_dpp v229, v229, v229 row_shr:1 row_mask:0xf bank_mask:0xf\n"
"v_mfma_f32_16x16x32_bf16 v[148:151], v[220:223], a[248:251], v[148:151]\n"
"v_mfma_f32_16x16x32_bf16 v[156:159], v[220:223], a[252:255], v[156:159]\n"
"s_waitcnt lgkmcnt(2)\n"
"buffer_load_dwordx4 v237, s[12:15], s48 offen offset:3072 lds\n"
"v_mfma_f32_16x16x32_bf16 v[4:7], v[192:195], a[0:3], 0\n"
"v_max_u32_dpp v229, v229, v229 row_shr:2 row_mask:0xf bank_mask:0xf\n"
"v_mfma_f32_16x16x32_bf16 v[20:23], v[192:195], a[4:7], 0\n"
"ds_read_b128 v[212:215], v242 offset:6144\n"
"v_max_u32_dpp v229, v229, v229 row_shr:4 row_mask:0xf bank_mask:0xf\n"
"v_mfma_f32_16x16x32_bf16 v[36:39], v[192:195], a[8:11], 0\n"
"s_add_u32 m0, s37, 20480\n"
"v_max_u32_dpp v229, v229, v229 row_shr:8 row_mask:0xf bank_mask:0xf\n"
"s_add_u32 s48, s98, 98304\n"
"v_mfma_f32_16x16x32_bf16 v[52:55], v[192:195], a[12:15], 0\n"
"v_max_u32_dpp v229, v229, v229 row_bcast:15 row_mask:0xa bank_mask:0xf\n"
"v_cvt_pk_bf16_f32 v128, v128, v129\n"
"v_mfma_f32_16x16x32_bf16 v[12:15], v[196:199], a[0:3], 0\n"
"buffer_load_dwordx4 v234, s[12:15], s48 offen offset:0 lds\n"
"v_mfma_f32_16x16x32_bf16 v[28:31], v[196:199], a[4:7], 0\n"
"ds_read_b128 v[216:219], v243 offset:4096\n"
"v_max_u32_dpp v229, v229, v229 row_bcast:31 row_mask:0xc bank_mask:0xf\n"
"v_mfma_f32_16x16x32_bf16 v[44:47], v[196:199], a[8:11], 0\n"
"v_readlane_b32 s62, v229, 63\n"
"v_mov_b32 v229, v228\n"
"v_mfma_f32_16x16x32_bf16 v[60:63], v[196:199], a[12:15], 0\n"
"v_cmp_eq_u32 vcc, s62, v226\n"
"v_writelane_b32 v225, s62, 7\n"
"v_mfma_f32_16x16x32_bf16 v[4:7], v[200:203], a[16:19], v[4:7]\n"
"v_cndmask_b32 v226, v226, v227, vcc\n"
"v_cndmask_b32_e64 v227, v227, 0, vcc\n"
"v_mfma_f32_16x16x32_bf16 v[20:23], v[200:203], a[20:23], v[20:23]\n"
"ds_read_b128 v[220:223], v243 offset:6144\n"
"v_max_u32_dpp v229, v229, v229 row_shr:1 row_mask:0xf bank_mask:0xf\n"
"v_mfma_f32_16x16x32_bf16 v[36:39], v[200:203], a[24:27], v[36:39]\n"
"buffer_load_dwordx4 v235, s[12:15], s48 offen offset:1024 lds\n"
"v_mfma_f32_16x16x32_bf16 v[52:55], v[200:203], a[28:31], v[52:55]\n"
"s_waitcnt lgkmcnt(2)\n"
"buffer_store_dwordx4 v[56:59], v250, s[8:11], s49 offen offset:3072 sc1\n"
"v_mfma_f32_16x16x32_bf16 v[12:15], v[204:207], a[16:19], v[12:15]\n"
"ds_read_b128 v[192:195], v242 offset:24576\n"
"v_max_u32_dpp v229, v229, v229 row_shr:2 row_mask:0xf bank_mask:0xf\n"
"v_mfma_f32_16x16x32_bf16 v[28:31], v[204:207], a[20:23], v[28:31]\n"
"v_cvt_pk_bf16_f32 v129, v130, v131\n"
"v_max_u32_dpp v229, v229, v229 row_shr:4 row_mask:0xf bank_mask:0xf\n"
"v_mfma_f32_16x16x32_bf16 v[44:47], v[204:207], a[24:27], v[44:47]\n"
"buffer_load_dwordx4 v236, s[12:15], s48 offen offset:2048 lds\n"
"v_mfma_f32_16x16x32_bf16 v[60:63], v[204:207], a[28:31], v[60:63]\n"
"v_max_u32_dpp v229, v229, v229 row_shr:8 row_mask:0xf bank_mask:0xf\n"
"v_cvt_pk_bf16_f32 v130, v132, v133\n"
"v_mfma_f32_16x16x32_bf16 v[4:7], v[208:211], a[32:35], v[4:7]\n"
"ds_read_b128 v[196:199], v242 offset:26624\n"
"v_max_u32_dpp v229, v229, v229 row_bcast:15 row_mask:0xa bank_mask:0xf\n"
"v_mfma_f32_16x16x32_bf16 v[20:23], v[208:211], a[36:39], v[20:23]\n"
"v_cvt_pk_bf16_f32 v131, v134, v135\n"
"v_max_u32_dpp v229, v229, v229 row_bcast:31 row_mask:0xc bank_mask:0xf\n"
"v_mfma_f32_16x16x32_bf16 v[36:39], v[208:211], a[40:43], v[36:39]\n"
"v_readlane_b32 s63, v229, 63\n"
"v_cvt_pk_bf16_f32 v136, v136, v137\n"
"v_mfma_f32_16x16x32_bf16 v[52:55], v[208:211], a[44:47], v[52:55]\n"
"s_cmp_gt_u32 s63, s62\n"
"s_cbranch_scc1 L1007_%=\n"
"L1008_%=:\n"
"v_mfma_f32_16x16x32_bf16 v[12:15], v[212:215], a[32:35], v[12:15]\n"
"buffer_load_dwordx4 v237, s[12:15], s48 offen offset:3072 lds\n"
"v_mfma_f32_16x16x32_bf16 v[28:31], v[212:215], a[36:39], v[28:31]\n"
"ds_read_b128 v[200:203], v243 offset:24576\n"
"v_lshrrev_b32 v229, 2, v224\n"
"v_mfma_f32_16x16x32_bf16 v[44:47], v[212:215], a[40:43], v[44:47]\n"
"ds_write_b32 v229, v225 offset:40960\n"
"v_mfma_f32_16x16x32_bf16 v[60:63], v[212:215], a[44:47], v[60:63]\n"
"s_waitcnt lgkmcnt(2)\n"
"v_cvt_pk_bf16_f32 v137, v138, v139\n"
"v_cvt_pk_bf16_f32 v138, v140, v141\n"
"v_mfma_f32_16x16x32_bf16 v[4:7], v[216:219], a[48:51], v[4:7]\n"
"s_add_u32 m0, s92, 8192\n"
"ds_read_b128 v[204:207], v243 offset:26624\n"
"v_cvt_pk_bf16_f32 v139, v142, v143\n"
"v_mfma_f32_16x16x32_bf16 v[20:23], v[216:219], a[52:55], v[20:23]\n"
"s_add_u32 s48, s61, 884736\n"
"v_cvt_pk_bf16_f32 v144, v144, v145\n"
"v_cvt_pk_bf16_f32 v145, v146, v147\n"
"v_mfma_f32_16x16x32_bf16 v[36:39], v[216:219], a[56:59], v[36:39]\n"
"s_mov_b32 s49, 16384\n"
"buffer_load_dwordx4 v251, s[24:27], s48 offen lds\n"
"v_mfma_f32_16x16x32_bf16 v[52:55], v[216:219], a[60:63], v[52:55]\n"
"ds_read_b128 v[208:211], v242 offset:28672\n"
"v_cvt_pk_bf16_f32 v146, v148, v149\n"
"v_mfma_f32_16x16x32_bf16 v[12:15], v[220:223], a[48:51], v[12:15]\n"
"buffer_store_dwordx4 v[64:67], v250, s[8:11], s49 offen offset:0 sc1\n"
"v_mfma_f32_16x16x32_bf16 v[28:31], v[220:223], a[52:55], v[28:31]\n"
"v_cvt_pk_bf16_f32 v147, v150, v151\n"
"v_cvt_pk_bf16_f32 v152, v152, v153\n"
"v_mfma_f32_16x16x32_bf16 v[44:47], v[220:223], a[56:59], v[44:47]\n"
"v_cvt_pk_bf16_f32 v153, v154, v155\n"
"v_cvt_pk_bf16_f32 v154, v156, v157\n"
"v_mfma_f32_16x16x32_bf16 v[60:63], v[220:223], a[60:63], v[60:63]\n"
"ds_read_b128 v[212:215], v242 offset:30720\n"
"v_cvt_pk_bf16_f32 v155, v158, v159\n"
"v_mfma_f32_16x16x32_bf16 v[4:7], v[192:195], a[64:67], v[4:7]\n"
"v_mfma_f32_16x16x32_bf16 v[20:23], v[192:195], a[68:71], v[20:23]\n"
"v_mfma_f32_16x16x32_bf16 v[36:39], v[192:195], a[72:75], v[36:39]\n"
"v_mfma_f32_16x16x32_bf16 v[52:55], v[192:195], a[76:79], v[52:55]\n"
"ds_read_b128 v[216:219], v243 offset:28672\n"
"v_mfma_f32_16x16x32_bf16 v[12:15], v[196:199], a[64:67], v[12:15]\n"
"v_mfma_f32_16x16x32_bf16 v[28:31], v[196:199], a[68:71], v[28:31]\n"
"s_add_u32 m0, s92, 9216\n"
"s_add_u32 s48, s61, 995328\n"
"v_mfma_f32_16x16x32_bf16 v[44:47], v[196:199], a[72:75], v[44:47]\n"
"v_mfma_f32_16x16x32_bf16 v[60:63], v[196:199], a[76:79], v[60:63]\n"
"s_waitcnt lgkmcnt(1)\n"
"ds_read_b128 v[220:223], v243 offset:30720\n"
"v_mfma_f32_16x16x32_bf16 v[4:7], v[200:203], a[80:83], v[4:7]\n"
"buffer_load_dwordx4 v251, s[24:27], s48 offen lds\n"
"v_mfma_f32_16x16x32_bf16 v[20:23], v[200:203], a[84:87], v[20:23]\n"
"v_mfma_f32_16x16x32_bf16 v[36:39], v[200:203], a[88:91], v[36:39]\n"
"ds_read_b128 v[192:195], v244 offset:0\n"
"v_mfma_f32_16x16x32_bf16 v[52:55], v[200:203], a[92:95], v[52:55]\n"
"v_mfma_f32_16x16x32_bf16 v[12:15], v[204:207], a[80:83], v[12:15]\n"
"v_mfma_f32_16x16x32_bf16 v[28:31], v[204:207], a[84:87], v[28:31]\n"
"v_mfma_f32_16x16x32_bf16 v[44:47], v[204:207], a[88:91], v[44:47]\n"
"ds_read_b128 v[196:199], v244 offset:2048\n"
"v_mfma_f32_16x16x32_bf16 v[60:63], v[204:207], a[92:95], v[60:63]\n"
"buffer_store_dwordx4 v[72:75], v250, s[8:11], s49 offen offset:1024 sc1\n"
"v_mfma_f32_16x16x32_bf16 v[4:7], v[208:211], a[96:99], v[4:7]\n"
"v_mfma_f32_16x16x32_bf16 v[20:23], v[208:211], a[100:103], v[20:23]\n"
"v_mfma_f32_16x16x32_bf16 v[36:39], v[208:211], a[104:107], v[36:39]\n"
"ds_read_b128 v[200:203], v245 offset:0\n"
"v_mfma_f32_16x16x32_bf16 v[52:55], v[208:211], a[108:111], v[52:55]\n"
"v_mfma_f32_16x16x32_bf16 v[12:15], v[212:215], a[96:99], v[12:15]\n"
"v_mfma_f32_16x16x32_bf16 v[28:31], v[212:215], a[100:103], v[28:31]\n"
"v_mfma_f32_16x16x32_bf16 v[44:47], v[212:215], a[104:107], v[44:47]\n"
"ds_read_b128 v[204:207], v245 offset:2048\n"
"v_mfma_f32_16x16x32_bf16 v[60:63], v[212:215], a[108:111], v[60:63]\n"
"s_waitcnt lgkmcnt(2)\n"
"v_mfma_f32_16x16x32_bf16 v[4:7], v[216:219], a[112:115], v[4:7]\n"
"v_mfma_f32_16x16x32_bf16 v[20:23], v[216:219], a[116:119], v[20:23]\n"
"ds_read_b128 v[208:211], v244 offset:4096\n"
"v_mfma_f32_16x16x32_bf16 v[36:39], v[216:219], a[120:123], v[36:39]\n"
"v_mfma_f32_16x16x32_bf16 v[52:55], v[216:219], a[124:127], v[52:55]\n"
"v_mfma_f32_16x16x32_bf16 v[12:15], v[220:223], a[112:115], v[12:15]\n"
"v_mfma_f32_16x16x32_bf16 v[28:31], v[220:223], a[116:119], v[28:31]\n"
"ds_read_b128 v[212:215], v244 offset:6144\n"
"v_mfma_f32_16x16x32_bf16 v[44:47], v[220:223], a[120:123], v[44:47]\n"
"v_mfma_f32_16x16x32_bf16 v[60:63], v[220:223], a[124:127], v[60:63]\n"
"v_mfma_f32_16x16x32_bf16 v[4:7], v[192:195], a[128:131], v[4:7]\n"
"buffer_store_dwordx4 v[80:83], v250, s[8:11], s49 offen offset:2048 sc1\n"
"v_mfma_f32_16x16x32_bf16 v[20:23], v[192:195], a[132:135], v[20:23]\n"
"ds_read_b128 v[216:219], v245 offset:4096\n"
"v_mfma_f32_16x16x32_bf16 v[36:39], v[192:195], a[136:139], v[36:39]\n"
"v_mfma_f32_16x16x32_bf16 v[52:55], v[192:195], a[140:143], v[52:55]\n"
"v_mfma_f32_16x16x32_bf16 v[12:15], v[196:199], a[128:131], v[12:15]\n"
"v_mfma_f32_16x16x32_bf16 v[28:31], v[196:199], a[132:135], v[28:31]\n"
"ds_read_b128 v[220:223], v245 offset:6144\n"
"v_mfma_f32_16x16x32_bf16 v[44:47], v[196:199], a[136:139], v[44:47]\n"
"v_mfma_f32_16x16x32_bf16 v[60:63], v[196:199], a[140:143], v[60:63]\n"
"s_waitcnt lgkmcnt(2)\n"
"v_mfma_f32_16x16x32_bf16 v[4:7], v[200:203], a[144:147], v[4:7]\n"
"ds_read_b128 v[192:195], v244 offset:24576\n"
"v_mfma_f32_16x16x32_bf16 v[20:23], v[200:203], a[148:151], v[20:23]\n"
"v_mfma_f32_16x16x32_bf16 v[36:39], v[200:203], a[152:155], v[36:39]\n"
"v_mfma_f32_16x16x32_bf16 v[52:55], v[200:203], a[156:159], v[52:55]\n"
"v_mfma_f32_16x16x32_bf16 v[12:15], v[204:207], a[144:147], v[12:15]\n"
"ds_read_b128 v[196:199], v244 offset:26624\n"
"v_mfma_f32_16x16x32_bf16 v[28:31], v[204:207], a[148:151], v[28:31]\n"
"v_mfma_f32_16x16x32_bf16 v[44:47], v[204:207], a[152:155], v[44:47]\n"
"v_mfma_f32_16x16x32_bf16 v[60:63], v[204:207], a[156:159], v[60:63]\n"
"v_mfma_f32_16x16x32_bf16 v[4:7], v[208:211], a[160:163], v[4:7]\n"
"ds_read_b128 v[200:203], v245 offset:24576\n"
"v_mfma_f32_16x16x32_bf16 v[20:23], v[208:211], a[164:167], v[20:23]\n"
"v_mfma_f32_16x16x32_bf16 v[36:39], v[208:211], a[168:171], v[36:39]\n"
"buffer_store_dwordx4 v[88:91], v250, s[8:11], s49 offen offset:3072 sc1\n"
"v_mfma_f32_16x16x32_bf16 v[52:55], v[208:211], a[172:175], v[52:55]\n"
"v_mfma_f32_16x16x32_bf16 v[12:15], v[212:215], a[160:163], v[12:15]\n"
"ds_read_b128 v[204:207], v245 offset:26624\n"
"v_mfma_f32_16x16x32_bf16 v[28:31], v[212:215], a[164:167], v[28:31]\n"
"v_mfma_f32_16x16x32_bf16 v[44:47], v[212:215], a[168:171], v[44:47]\n"
"v_mfma_f32_16x16x32_bf16 v[60:63], v[212:215], a[172:175], v[60:63]\n"
"s_waitcnt lgkmcnt(1)\n"
"ds_read_b128 v[208:211], v244 offset:28672\n"
"v_mfma_f32_16x16x32_bf16 v[4:7], v[216:219], a[176:179], v[4:7]\n"
"v_mfma_f32_16x16x32_bf16 v[20:23], v[216:219], a[180:183], v[20:23]\n"
"v_mfma_f32_16x16x32_bf16 v[36:39], v[216:219], a[184:187], v[36:39]\n"
"v_mfma_f32_16x16x32_bf16 v[52:55], v[216:219], a[188:191], v[52:55]\n"
"ds_read_b128 v[212:215], v244 offset:30720\n"
"v_mfma_f32_16x16x32_bf16 v[12:15], v[220:223], a[176:179], v[12:15]\n"
"v_mfma_f32_16x16x32_bf16 v[28:31], v[220:223], a[180:183], v[28:31]\n"
"v_mfma_f32_16x16x32_bf16 v[44:47], v[220:223], a[184:187], v[44:47]\n"
"v_mfma_f32_16x16x32_bf16 v[60:63], v[220:223], a[188:191], v[60:63]\n"
"ds_read_b128 v[216:219], v245 offset:28672\n"
"v_mfma_f32_16x16x32_bf16 v[4:7], v[192:195], a[192:195], v[4:7]\n"
"v_mfma_f32_16x16x32_bf16 v[20:23], v[192:195], a[196:199], v[20:23]\n"
"s_sub_u32 s99, s34, 5\n"
"v_mfma_f32_16x16x32_bf16 v[36:39], v[192:195], a[200:203], v[36:39]\n"
"s_mov_b32 s49, 24576\n"
"s_cmp_lt_u32 s34, 5\n"
"s_cselect_b32 s99, s34, s99\n"
"v_mfma_f32_16x16x32_bf16 v[52:55], v[192:195], a[204:207], v[52:55]\n"
"s_mul_i32 s99, s99, 1\n"
"ds_read_b128 v[220:223], v245 offset:30720\n"
"v_mfma_f32_16x16x32_bf16 v[12:15], v[196:199], a[192:195], v[12:15]\n"
"s_lshl_b32 s98, s99, 20\n"
"buffer_store_dwordx4 v[96:99], v250, s[8:11], s49 offen offset:0 sc1\n"
"v_mfma_f32_16x16x32_bf16 v[28:31], v[196:199], a[196:199], v[28:31]\n"
"v_mfma_f32_16x16x32_bf16 v[44:47], v[196:199], a[200:203], v[44:47]\n"
"v_mfma_f32_16x16x32_bf16 v[60:63], v[196:199], a[204:207], v[60:63]\n"
"s_lshr_b32 s50, s30, 1\n"
"v_mfma_f32_16x16x32_bf16 v[4:7], v[200:203], a[208:211], v[4:7]\n"
"s_and_b32 s51, s30, 1\n"
"v_mfma_f32_16x16x32_bf16 v[20:23], v[200:203], a[212:215], v[20:23]\n"
"s_mul_i32 s38, s50, 24576\n"
"v_mfma_f32_16x16x32_bf16 v[36:39], v[200:203], a[216:219], v[36:39]\n"
"s_lshl_b32 s39, s50, 18\n"
"v_mfma_f32_16x16x32_bf16 v[52:55], v[200:203], a[220:223], v[52:55]\n"
"s_waitcnt lgkmcnt(0)\n"
"s_lshl_b32 s50, s51, 12\n"
"v_mfma_f32_16x16x32_bf16 v[12:15], v[204:207], a[208:211], v[12:15]\n"
"s_add_u32 s38, s38, s50\n"
"v_mfma_f32_16x16x32_bf16 v[28:31], v[204:207], a[212:215], v[28:31]\n"
"s_lshl_b32 s50, s51, 7\n"
"s_waitcnt vmcnt(31)\n"
"s_barrier\n"
"v_mfma_f32_16x16x32_bf16 v[44:47], v[204:207], a[216:219], v[44:47]\n"
"s_add_u32 s39, s39, s50\n"
"ds_read_b128 v[192:195], v242 offset:8192\n"
"v_mfma_f32_16x16x32_bf16 v[60:63], v[204:207], a[220:223], v[60:63]\n"
"v_mfma_f32_16x16x32_bf16 v[4:7], v[208:211], a[224:227], v[4:7]\n"
"v_mfma_f32_16x16x32_bf16 v[20:23], v[208:211], a[228:231], v[20:23]\n"
"s_add_u32 m0, s38, 65536\n"
"s_add_u32 s48, s39, s98\n"
"v_mfma_f32_16x16x32_bf16 v[36:39], v[208:211], a[232:235], v[36:39]\n"
"ds_read_b128 v[196:199], v242 offset:10240\n"
"v_mfma_f32_16x16x32_bf16 v[52:55], v[208:211], a[236:239], v[52:55]\n"
"buffer_load_dwordx4 v238, s[20:23], s48 offen offset:0 lds\n"
"v_mfma_f32_16x16x32_bf16 v[12:15], v[212:215], a[224:227], v[12:15]\n"
"v_mfma_f32_16x16x32_bf16 v[28:31], v[212:215], a[228:231], v[28:31]\n"
"v_mfma_f32_16x16x32_bf16 v[44:47], v[212:215], a[232:235], v[44:47]\n"
"ds_read_b128 v[200:203], v243 offset:8192\n"
"v_mfma_f32_16x16x32_bf16 v[60:63], v[212:215], a[236:239], v[60:63]\n"
"buffer_store_dwordx4 v[104:107], v250, s[8:11], s49 offen offset:1024 sc1\n"
"v_mfma_f32_16x16x32_bf16 v[4:7], v[216:219], a[240:243], v[4:7]\n"
"v_mfma_f32_16x16x32_bf16 v[20:23], v[216:219], a[244:247], v[20:23]\n"
"v_mfma_f32_16x16x32_bf16 v[36:39], v[216:219], a[248:251], v[36:39]\n"
"ds_read_b128 v[204:207], v243 offset:10240\n"
"v_mfma_f32_16x16x32_bf16 v[52:55], v[216:219], a[252:255], v[52:55]\n"
"buffer_load_dwordx4 v239, s[20:23], s48 offen offset:1024 lds\n"
"v_mfma_f32_16x16x32_bf16 v[12:15], v[220:223], a[240:243], v[12:15]\n"
"v_mfma_f32_16x16x32_bf16 v[28:31], v[220:223], a[244:247], v[28:31]\n"
"ds_read_b128 v[208:211], v242 offset:12288\n"
"v_mfma_f32_16x16x32_bf16 v[44:47], v[220:223], a[248:251], v[44:47]\n"
"v_mfma_f32_16x16x32_bf16 v[60:63], v[220:223], a[252:255], v[60:63]\n"
"s_waitcnt lgkmcnt(2)\n"
"v_mfma_f32_16x16x32_bf16 v[84:87], v[192:195], a[0:3], 0\n"
"v_mfma_f32_16x16x32_bf16 v[100:103], v[192:195], a[4:7], 0\n"
"ds_read_b128 v[212:215], v242 offset:14336\n"
"v_mfma_f32_16x16x32_bf16 v[116:119], v[192:195], a[8:11], 0\n"
"buffer_load_dwordx4 v240, s[20:23], s48 offen offset:2048 lds\n"
"v_mfma_f32_16x16x32_bf16 v[132:135], v[192:195], a[12:15], 0\n"
"v_cvt_pk_bf16_f32 v4, v4, v5\n"
"v_cvt_pk_bf16_f32 v5, v6, v7\n"
"v_mfma_f32_16x16x32_bf16 v[92:95], v[196:199], a[0:3], 0\n"
"v_cvt_pk_bf16_f32 v6, v12, v13\n"
"v_cvt_pk_bf16_f32 v7, v14, v15\n"
"v_mfma_f32_16x16x32_bf16 v[108:111], v[196:199], a[4:7], 0\n"
"ds_read_b128 v[216:219], v243 offset:12288\n"
"v_mfma_f32_16x16x32_bf16 v[124:127], v[196:199], a[8:11], 0\n"
"v_mfma_f32_16x16x32_bf16 v[140:143], v[196:199], a[12:15], 0\n"
"s_add_u32 s98, s98, 524288\n"
"v_mfma_f32_16x16x32_bf16 v[84:87], v[200:203], a[16:19], v[84:87]\n"
"buffer_store_dwordx4 v[112:115], v250, s[8:11], s49 offen offset:2048 sc1\n"
"v_mfma_f32_16x16x32_bf16 v[100:103], v[200:203], a[20:23], v[100:103]\n"
"buffer_load_dwordx4 v241, s[20:23], s48 offen offset:3072 lds\n"
"v_mfma_f32_16x16x32_bf16 v[116:119], v[200:203], a[24:27], v[116:119]\n"
"ds_read_b128 v[220:223], v243 offset:14336\n"
"v_cvt_pk_bf16_f32 v20, v20, v21\n"
"v_mfma_f32_16x16x32_bf16 v[132:135], v[200:203], a[28:31], v[132:135]\n"
"s_waitcnt lgkmcnt(2)\n"
"v_cvt_pk_bf16_f32 v21, v22, v23\n"
"v_cvt_pk_bf16_f32 v22, v28, v29\n"
"v_mfma_f32_16x16x32_bf16 v[92:95], v[204:207], a[16:19], v[92:95]\n"
"ds_read_b128 v[192:195], v242 offset:32768\n"
"v_cvt_pk_bf16_f32 v23, v30, v31\n"
"v_mfma_f32_16x16x32_bf16 v[108:111], v[204:207], a[20:23], v[108:111]\n"
"s_add_u32 m0, s38, 114688\n"
"v_cvt_pk_bf16_f32 v36, v36, v37\n"
"v_cvt_pk_bf16_f32 v37, v38, v39\n"
"v_mfma_f32_16x16x32_bf16 v[124:127], v[204:207], a[24:27], v[124:127]\n"
"s_add_u32 s48, s39, s98\n"
"v_cvt_pk_bf16_f32 v38, v44, v45\n"
"v_cvt_pk_bf16_f32 v39, v46, v47\n"
"v_mfma_f32_16x16x32_bf16 v[140:143], v[204:207], a[28:31], v[140:143]\n"
"buffer_load_dwordx4 v238, s[20:23], s48 offen offset:0 lds\n"
"v_mfma_f32_16x16x32_bf16 v[84:87], v[208:211], a[32:35], v[84:87]\n"
"ds_read_b128 v[196:199], v242 offset:34816\n"
"v_mfma_f32_16x16x32_bf16 v[100:103], v[208:211], a[36:39], v[100:103]\n"
"v_mfma_f32_16x16x32_bf16 v[116:119], v[208:211], a[40:43], v[116:119]\n"
"v_cvt_pk_bf16_f32 v52, v52, v53\n"
"v_cvt_pk_bf16_f32 v53, v54, v55\n"
"v_mfma_f32_16x16x32_bf16 v[132:135], v[208:211], a[44:47], v[132:135]\n"
"v_cvt_pk_bf16_f32 v54, v60, v61\n"
"v_cvt_pk_bf16_f32 v55, v62, v63\n"
"v_mfma_f32_16x16x32_bf16 v[92:95], v[212:215], a[32:35], v[92:95]\n"
"ds_read_b128 v[200:203], v243 offset:32768\n"
"v_mfma_f32_16x16x32_bf16 v[108:111], v[212:215], a[36:39], v[108:111]\n"
"v_mfma_f32_16x16x32_bf16 v[124:127], v[212:215], a[40:43], v[124:127]\n"
"buffer_load_dwordx4 v239, s[20:23], s48 offen offset:1024 lds\n"
"v_mfma_f32_16x16x32_bf16 v[140:143], v[212:215], a[44:47], v[140:143]\n"
"s_waitcnt lgkmcnt(1)\n"
"v_mfma_f32_16x16x32_bf16 v[84:87], v[216:219], a[48:51], v[84:87]\n"
"ds_read_b128 v[204:207], v243 offset:34816\n"
"v_mfma_f32_16x16x32_bf16 v[100:103], v[216:219], a[52:55], v[100:103]\n"
"v_mfma_f32_16x16x32_bf16 v[116:119], v[216:219], a[56:59], v[116:119]\n"
"buffer_store_dwordx4 v[120:123], v250, s[8:11], s49 offen offset:3072 sc1\n"
"v_mfma_f32_16x16x32_bf16 v[132:135], v[216:219], a[60:63], v[132:135]\n"
"ds_read_b128 v[208:211], v242 offset:36864\n"
"v_mfma_f32_16x16x32_bf16 v[92:95], v[220:223], a[48:51], v[92:95]\n"
"v_mfma_f32_16x16x32_bf16 v[108:111], v[220:223], a[52:55], v[108:111]\n"
"buffer_load_dwordx4 v240, s[20:23], s48 offen offset:2048 lds\n"
"v_mfma_f32_16x16x32_bf16 v[124:127], v[220:223], a[56:59], v[124:127]\n"
"v_mfma_f32_16x16x32_bf16 v[140:143], v[220:223], a[60:63], v[140:143]\n"
"ds_read_b128 v[212:215], v242 offset:38912\n"
"v_mfma_f32_16x16x32_bf16 v[84:87], v[192:195], a[64:67], v[84:87]\n"
"v_mfma_f32_16x16x32_bf16 v[100:103], v[192:195], a[68:71], v[100:103]\n"
"v_mfma_f32_16x16x32_bf16 v[116:119], v[192:195], a[72:75], v[116:119]\n"
"v_mfma_f32_16x16x32_bf16 v[132:135], v[192:195], a[76:79], v[132:135]\n"
"s_add_u32 s98, s98, 524288\n"
"ds_read_b128 v[216:219], v243 offset:36864\n"
"s_lshl_b32 s50, s34, 3\n"
"v_mfma_f32_16x16x32_bf16 v[92:95], v[196:199], a[64:67], v[92:95]\n"
"s_cmp_eq_u32 s98, 5242880\n"
"buffer_load_dwordx4 v241, s[20:23], s48 offen offset:3072 lds\n"
"v_mfma_f32_16x16x32_bf16 v[108:111], v[196:199], a[68:71], v[108:111]\n"
"s_cselect_b32 s98, 0, s98\n"
"s_add_u32 s51, s50, s30\n"
"s_mul_i32 s51, s51, 20480\n"
"v_mfma_f32_16x16x32_bf16 v[124:127], v[196:199], a[72:75], v[124:127]\n"
"s_add_u32 s56, s51, 65536\n"
"v_mfma_f32_16x16x32_bf16 v[140:143], v[196:199], a[76:79], v[140:143]\n"
"s_waitcnt lgkmcnt(1)\n"
"ds_read_b128 v[220:223], v243 offset:38912\n"
"s_add_u32 s57, s56, 81920\n"
"v_mfma_f32_16x16x32_bf16 v[84:87], v[200:203], a[80:83], v[84:87]\n"
"s_sub_u32 s51, s30, s50\n"
"v_mfma_f32_16x16x32_bf16 v[100:103], v[200:203], a[84:87], v[100:103]\n"
"s_add_u32 s51, s51, 120\n"
"v_mfma_f32_16x16x32_bf16 v[116:119], v[200:203], a[88:91], v[116:119]\n"
"s_mov_b32 s49, 262144\n"
"ds_read_b128 v[192:195], v244 offset:8192\n"
"s_mul_i32 s51, s51, 20480\n"
"v_mfma_f32_16x16x32_bf16 v[132:135], v[200:203], a[92:95], v[132:135]\n"
"s_add_u32 s58, s51, 65536\n"
"s_lshl_b32 s99, s99, 12\n"
"v_mfma_f32_16x16x32_bf16 v[92:95], v[204:207], a[80:83], v[92:95]\n"
"s_add_u32 s59, s58, 81920\n"
"buffer_store_dwordx4 v[128:131], v250, s[8:11], s49 offen offset:0 sc1\n"
"v_mfma_f32_16x16x32_bf16 v[108:111], v[204:207], a[84:87], v[108:111]\n"
"v_add_u32 v235, s99, v224\n"
"v_mfma_f32_16x16x32_bf16 v[124:127], v[204:207], a[88:91], v[124:127]\n"
"ds_read_b128 v[196:199], v244 offset:10240\n"
"v_mfma_f32_16x16x32_bf16 v[140:143], v[204:207], a[92:95], v[140:143]\n"
"buffer_load_dwordx4 v[64:67], v235, s[8:11], s56 offen offset:0 sc1\n"
"v_mfma_f32_16x16x32_bf16 v[84:87], v[208:211], a[96:99], v[84:87]\n"
"v_mfma_f32_16x16x32_bf16 v[100:103], v[208:211], a[100:103], v[100:103]\n"
"v_mfma_f32_16x16x32_bf16 v[116:119], v[208:211], a[104:107], v[116:119]\n"
"ds_read_b128 v[200:203], v245 offset:8192\n"
"v_mfma_f32_16x16x32_bf16 v[132:135], v[208:211], a[108:111], v[132:135]\n"
"v_mfma_f32_16x16x32_bf16 v[92:95], v[212:215], a[96:99], v[92:95]\n"
"v_mfma_f32_16x16x32_bf16 v[108:111], v[212:215], a[100:103], v[108:111]\n"
"buffer_load_dwordx4 v[68:71], v235, s[8:11], s57 offen offset:0 sc1\n"
"v_mfma_f32_16x16x32_bf16 v[124:127], v[212:215], a[104:107], v[124:127]\n"
"ds_read_b128 v[204:207], v245 offset:10240\n"
"v_mfma_f32_16x16x32_bf16 v[140:143], v[212:215], a[108:111], v[140:143]\n"
"s_waitcnt lgkmcnt(2)\n"
"v_mfma_f32_16x16x32_bf16 v[84:87], v[216:219], a[112:115], v[84:87]\n"
"v_mfma_f32_16x16x32_bf16 v[100:103], v[216:219], a[116:119], v[100:103]\n"
"ds_read_b128 v[208:211], v244 offset:12288\n"
"v_mfma_f32_16x16x32_bf16 v[116:119], v[216:219], a[120:123], v[116:119]\n"
"v_mfma_f32_16x16x32_bf16 v[132:135], v[216:219], a[124:127], v[132:135]\n"
"buffer_load_dwordx4 v[72:75], v235, s[8:11], s58 offen offset:0 sc1\n"
"v_mfma_f32_16x16x32_bf16 v[92:95], v[220:223], a[112:115], v[92:95]\n"
"v_mfma_f32_16x16x32_bf16 v[108:111], v[220:223], a[116:119], v[108:111]\n"
"ds_read_b128 v[212:215], v244 offset:14336\n"
"v_mfma_f32_16x16x32_bf16 v[124:127], v[220:223], a[120:123], v[124:127]\n"
"buffer_store_dwordx4 v[136:139], v250, s[8:11], s49 offen offset:1024 sc1\n"
"v_mfma_f32_16x16x32_bf16 v[140:143], v[220:223], a[124:127], v[140:143]\n"
"v_mfma_f32_16x16x32_bf16 v[84:87], v[192:195], a[128:131], v[84:87]\n"
"v_mfma_f32_16x16x32_bf16 v[100:103], v[192:195], a[132:135], v[100:103]\n"
"ds_read_b128 v[216:219], v245 offset:12288\n"
"v_mfma_f32_16x16x32_bf16 v[116:119], v[192:195], a[136:139], v[116:119]\n"
"buffer_load_dwordx4 v[76:79], v235, s[8:11], s59 offen offset:0 sc1\n"
"v_mfma_f32_16x16x32_bf16 v[132:135], v[192:195], a[140:143], v[132:135]\n"
"v_mfma_f32_16x16x32_bf16 v[92:95], v[196:199], a[128:131], v[92:95]\n"
"v_mfma_f32_16x16x32_bf16 v[108:111], v[196:199], a[132:135], v[108:111]\n"
"ds_read_b128 v[220:223], v245 offset:14336\n"
"v_mfma_f32_16x16x32_bf16 v[124:127], v[196:199], a[136:139], v[124:127]\n"
"v_mfma_f32_16x16x32_bf16 v[140:143], v[196:199], a[140:143], v[140:143]\n"
"s_waitcnt lgkmcnt(2)\n"
"v_mfma_f32_16x16x32_bf16 v[84:87], v[200:203], a[144:147], v[84:87]\n"
"ds_read_b128 v[192:195], v244 offset:32768\n"
"v_mfma_f32_16x16x32_bf16 v[100:103], v[200:203], a[148:151], v[100:103]\n"
"v_mfma_f32_16x16x32_bf16 v[116:119], v[200:203], a[152:155], v[116:119]\n"
"v_mfma_f32_16x16x32_bf16 v[132:135], v[200:203], a[156:159], v[132:135]\n"
"v_mfma_f32_16x16x32_bf16 v[92:95], v[204:207], a[144:147], v[92:95]\n"
"ds_read_b128 v[196:199], v244 offset:34816\n"
"v_mfma_f32_16x16x32_bf16 v[108:111], v[204:207], a[148:151], v[108:111]\n"
"v_mfma_f32_16x16x32_bf16 v[124:127], v[204:207], a[152:155], v[124:127]\n"
"v_mfma_f32_16x16x32_bf16 v[140:143], v[204:207], a[156:159], v[140:143]\n"
"v_mfma_f32_16x16x32_bf16 v[84:87], v[208:211], a[160:163], v[84:87]\n"
"ds_read_b128 v[200:203], v245 offset:32768\n"
"v_mfma_f32_16x16x32_bf16 v[100:103], v[208:211], a[164:167], v[100:103]\n"
"buffer_store_dwordx4 v[144:147], v250, s[8:11], s49 offen offset:2048 sc1\n"
"v_mfma_f32_16x16x32_bf16 v[116:119], v[208:211], a[168:171], v[116:119]\n"
"v_mfma_f32_16x16x32_bf16 v[132:135], v[208:211], a[172:175], v[132:135]\n"
"v_mfma_f32_16x16x32_bf16 v[92:95], v[212:215], a[160:163], v[92:95]\n"
"ds_read_b128 v[204:207], v245 offset:34816\n"
"v_mfma_f32_16x16x32_bf16 v[108:111], v[212:215], a[164:167], v[108:111]\n"
"v_mfma_f32_16x16x32_bf16 v[124:127], v[212:215], a[168:171], v[124:127]\n"
"v_mfma_f32_16x16x32_bf16 v[140:143], v[212:215], a[172:175], v[140:143]\n"
"s_waitcnt lgkmcnt(1)\n"
"ds_read_b128 v[208:211], v244 offset:36864\n"
"v_mfma_f32_16x16x32_bf16 v[84:87], v[216:219], a[176:179], v[84:87]\n"
"v_mfma_f32_16x16x32_bf16 v[100:103], v[216:219], a[180:183], v[100:103]\n"
"v_mfma_f32_16x16x32_bf16 v[116:119], v[216:219], a[184:187], v[116:119]\n"
"v_mfma_f32_16x16x32_bf16 v[132:135], v[216:219], a[188:191], v[132:135]\n"
"ds_read_b128 v[212:215], v244 offset:38912\n"
"v_mfma_f32_16x16x32_bf16 v[92:95], v[220:223], a[176:179], v[92:95]\n"
"v_mfma_f32_16x16x32_bf16 v[108:111], v[220:223], a[180:183], v[108:111]\n"
"v_mfma_f32_16x16x32_bf16 v[124:127], v[220:223], a[184:187], v[124:127]\n"
"v_mfma_f32_16x16x32_bf16 v[140:143], v[220:223], a[188:191], v[140:143]\n"
"ds_read_b128 v[216:219], v245 offset:36864\n"
"v_mfma_f32_16x16x32_bf16 v[84:87], v[192:195], a[192:195], v[84:87]\n"
"v_mfma_f32_16x16x32_bf16 v[100:103], v[192:195], a[196:199], v[100:103]\n"
"v_mfma_f32_16x16x32_bf16 v[116:119], v[192:195], a[200:203], v[116:119]\n"
"buffer_store_dwordx4 v[152:155], v250, s[8:11], s49 offen offset:3072 sc1\n"
"v_mfma_f32_16x16x32_bf16 v[132:135], v[192:195], a[204:207], v[132:135]\n"
"ds_read_b128 v[220:223], v245 offset:38912\n"
"v_mfma_f32_16x16x32_bf16 v[92:95], v[196:199], a[192:195], v[92:95]\n"
"v_mfma_f32_16x16x32_bf16 v[108:111], v[196:199], a[196:199], v[108:111]\n"
"v_mfma_f32_16x16x32_bf16 v[124:127], v[196:199], a[200:203], v[124:127]\n"
"v_mfma_f32_16x16x32_bf16 v[140:143], v[196:199], a[204:207], v[140:143]\n"
"v_mfma_f32_16x16x32_bf16 v[84:87], v[200:203], a[208:211], v[84:87]\n"
"v_mfma_f32_16x16x32_bf16 v[100:103], v[200:203], a[212:215], v[100:103]\n"
"v_mfma_f32_16x16x32_bf16 v[116:119], v[200:203], a[216:219], v[116:119]\n"
"v_mfma_f32_16x16x32_bf16 v[132:135], v[200:203], a[220:223], v[132:135]\n"
"s_waitcnt lgkmcnt(0)\n"
"v_lshrrev_b32 v251, 2, v224\n"
"v_mfma_f32_16x16x32_bf16 v[92:95], v[204:207], a[208:211], v[92:95]\n"
"s_add_u32 s44, s44, 65536\n"
"v_and_b32 v251, 28, v251\n"
"v_mfma_f32_16x16x32_bf16 v[108:111], v[204:207], a[212:215], v[108:111]\n"
"s_lshl_b32 s92, s30, 10\n"
"s_waitcnt vmcnt(26)\n"
"s_barrier\n"
"v_mfma_f32_16x16x32_bf16 v[124:127], v[204:207], a[216:219], v[124:127]\n"
"s_and_b32 s93, s92, 1024\n"
"ds_read_b128 v[192:195], v242 offset:16384\n"
"s_add_u32 m0, s38, 73728\n"
"v_mfma_f32_16x16x32_bf16 v[140:143], v[204:207], a[220:223], v[140:143]\n"
"s_add_u32 s48, s39, s98\n"
"ds_read_b32 v234, v251 offset:40960\n"
"s_add_u32 s93, s93, 8192\n"
"v_mfma_f32_16x16x32_bf16 v[84:87], v[208:211], a[224:227], v[84:87]\n"
"buffer_load_dwordx4 v238, s[20:23], s48 offen offset:0 lds\n"
"v_mfma_f32_16x16x32_bf16 v[100:103], v[208:211], a[228:231], v[100:103]\n"
"v_mfma_f32_16x16x32_bf16 v[116:119], v[208:211], a[232:235], v[116:119]\n"
"ds_read_b128 v[196:199], v242 offset:18432\n"
"v_mfma_f32_16x16x32_bf16 v[132:135], v[208:211], a[236:239], v[132:135]\n"
"v_mfma_f32_16x16x32_bf16 v[92:95], v[212:215], a[224:227], v[92:95]\n"
"buffer_load_dwordx4 v239, s[20:23], s48 offen offset:1024 lds\n"
"v_mfma_f32_16x16x32_bf16 v[108:111], v[212:215], a[228:231], v[108:111]\n"
"s_waitcnt lgkmcnt(1)\n"
"v_not_b32 v251, v234\n"
"v_bfe_u32 v252, v251, 4, 7\n"
"v_mfma_f32_16x16x32_bf16 v[124:127], v[212:215], a[232:235], v[124:127]\n"
"ds_read_b128 v[200:203], v243 offset:16384\n"
"v_mul_u32_u24 v252, 20480, v252\n"
"v_mfma_f32_16x16x32_bf16 v[140:143], v[212:215], a[236:239], v[140:143]\n"
"v_and_b32 v251, 15, v251\n"
"v_lshl_add_u32 v251, v251, 4, v252\n"
"v_mfma_f32_16x16x32_bf16 v[84:87], v[216:219], a[240:243], v[84:87]\n"
"s_add_u32 s98, s98, 524288\n"
"buffer_load_dwordx4 v240, s[20:23], s48 offen offset:2048 lds\n"
"v_mfma_f32_16x16x32_bf16 v[100:103], v[216:219], a[244:247], v[100:103]\n"
"v_and_b32 v252, 0x380, v224\n"
"v_lshl_add_u32 v251, v252, 1, v251\n"
"v_mfma_f32_16x16x32_bf16 v[116:119], v[216:219], a[248:251], v[116:119]\n"
"ds_read_b128 v[204:207], v243 offset:18432\n"
"v_mfma_f32_16x16x32_bf16 v[132:135], v[216:219], a[252:255], v[132:135]\n"
"v_mfma_f32_16x16x32_bf16 v[92:95], v[220:223], a[240:243], v[92:95]\n"
"buffer_load_dwordx4 v241, s[20:23], s48 offen offset:3072 lds\n"
"v_mfma_f32_16x16x32_bf16 v[108:111], v[220:223], a[244:247], v[108:111]\n"
"ds_read_b128 v[208:211], v242 offset:20480\n"
"v_mfma_f32_16x16x32_bf16 v[124:127], v[220:223], a[248:251], v[124:127]\n"
"s_add_u32 m0, s38, 122880\n"
"s_add_u32 s48, s39, s98\n"
"v_mfma_f32_16x16x32_bf16 v[140:143], v[220:223], a[252:255], v[140:143]\n"
"v_mfma_f32_16x16x32_bf16 v[0:3], v[192:195], a[0:3], 0\n"
"buffer_load_dwordx4 v238, s[20:23], s48 offen offset:0 lds\n"
"v_mfma_f32_16x16x32_bf16 v[16:19], v[192:195], a[4:7], 0\n"
"ds_read_b128 v[212:215], v242 offset:22528\n"
"v_mfma_f32_16x16x32_bf16 v[32:35], v[192:195], a[8:11], 0\n"
"v_mfma_f32_16x16x32_bf16 v[48:51], v[192:195], a[12:15], 0\n"
"s_waitcnt lgkmcnt(2)\n"
"v_cvt_pk_bf16_f32 v12, v84, v85\n"
"v_cvt_pk_bf16_f32 v13, v86, v87\n"
"v_mfma_f32_16x16x32_bf16 v[8:11], v[196:199], a[0:3], 0\n"
"v_cvt_pk_bf16_f32 v14, v92, v93\n"
"v_cvt_pk_bf16_f32 v15, v94, v95\n"
"v_mfma_f32_16x16x32_bf16 v[24:27], v[196:199], a[4:7], 0\n"
"ds_read_b128 v[216:219], v243 offset:20480\n"
"v_mfma_f32_16x16x32_bf16 v[40:43], v[196:199], a[8:11], 0\n"
"buffer_load_dwordx4 v239, s[20:23], s48 offen offset:1024 lds\n"
"v_mfma_f32_16x16x32_bf16 v[56:59], v[196:199], a[12:15], 0\n"
"v_mfma_f32_16x16x32_bf16 v[0:3], v[200:203], a[16:19], v[0:3]\n"
"buffer_load_dwordx4 a[0:3], v235, s[8:11], s56 offen offset:1024 sc1\n"
"v_mfma_f32_16x16x32_bf16 v[16:19], v[200:203], a[20:23], v[16:19]\n"
"ds_read_b128 v[220:223], v243 offset:22528\n"
"v_cvt_pk_bf16_f32 v28, v100, v101\n"
"v_mfma_f32_16x16x32_bf16 v[32:35], v[200:203], a[24:27], v[32:35]\n"
"buffer_load_dwordx4 v240, s[20:23], s48 offen offset:2048 lds\n"
"v_mfma_f32_16x16x32_bf16 v[48:51], v[200:203], a[28:31], v[48:51]\n"
"v_cvt_pk_bf16_f32 v29, v102, v103\n"
"v_cvt_pk_bf16_f32 v30, v108, v109\n"
"v_mfma_f32_16x16x32_bf16 v[8:11], v[204:207], a[16:19], v[8:11]\n"
"s_add_u32 s98, s98, 524288\n"
"ds_read_b128 v[192:195], v242 offset:40960\n"
"v_cvt_pk_bf16_f32 v31, v110, v111\n"
"v_mfma_f32_16x16x32_bf16 v[24:27], v[204:207], a[20:23], v[24:27]\n"
"s_cmp_eq_u32 s98, 5242880\n"
"v_cvt_pk_bf16_f32 v44, v116, v117\n"
"v_cvt_pk_bf16_f32 v45, v118, v119\n"
"v_mfma_f32_16x16x32_bf16 v[40:43], v[204:207], a[24:27], v[40:43]\n"
"s_cselect_b32 s98, 0, s98\n"
"buffer_load_dwordx4 a[4:7], v235, s[8:11], s57 offen offset:1024 sc1\n"
"v_mfma_f32_16x16x32_bf16 v[56:59], v[204:207], a[28:31], v[56:59]\n"
"s_waitcnt lgkmcnt(2)\n"
"buffer_load_dwordx4 v241, s[20:23], s48 offen offset:3072 lds\n"
"v_mfma_f32_16x16x32_bf16 v[0:3], v[208:211], a[32:35], v[0:3]\n"
"ds_read_b128 v[196:199], v242 offset:43008\n"
"v_cvt_pk_bf16_f32 v46, v124, v125\n"
"v_mfma_f32_16x16x32_bf16 v[16:19], v[208:211], a[36:39], v[16:19]\n"
"v_cvt_pk_bf16_f32 v47, v126, v127\n"
"v_mfma_f32_16x16x32_bf16 v[32:35], v[208:211], a[40:43], v[32:35]\n"
"v_cvt_pk_bf16_f32 v60, v132, v133\n"
"v_cvt_pk_bf16_f32 v61, v134, v135\n"
"v_mfma_f32_16x16x32_bf16 v[48:51], v[208:211], a[44:47], v[48:51]\n"
"v_cvt_pk_bf16_f32 v62, v140, v141\n"
"v_cvt_pk_bf16_f32 v63, v142, v143\n"
"v_mfma_f32_16x16x32_bf16 v[8:11], v[212:215], a[32:35], v[8:11]\n"
"ds_read_b128 v[200:203], v243 offset:40960\n"
"v_mfma_f32_16x16x32_bf16 v[24:27], v[212:215], a[36:39], v[24:27]\n"
"s_add_u32 m0, s92, 45056\n"
"buffer_load_dwordx4 a[8:11], v235, s[8:11], s58 offen offset:1024 sc1\n"
"v_mfma_f32_16x16x32_bf16 v[40:43], v[212:215], a[40:43], v[40:43]\n"
"s_lshl_b32 s48, s92, 1\n"
"s_add_u32 s48, s48, s44\n"
"v_mfma_f32_16x16x32_bf16 v[56:59], v[212:215], a[44:47], v[56:59]\n"
"v_mfma_f32_16x16x32_bf16 v[0:3], v[216:219], a[48:51], v[0:3]\n"
"ds_read_b128 v[204:207], v243 offset:43008\n"
"v_mfma_f32_16x16x32_bf16 v[16:19], v[216:219], a[52:55], v[16:19]\n"
"buffer_load_dwordx4 v251, s[8:11], s48 offen sc1 lds\n"
"v_mfma_f32_16x16x32_bf16 v[32:35], v[216:219], a[56:59], v[32:35]\n"
"buffer_load_dwordx4 a[12:15], v235, s[8:11], s59 offen offset:1024 sc1\n"
"v_mfma_f32_16x16x32_bf16 v[48:51], v[216:219], a[60:63], v[48:51]\n"
"s_waitcnt lgkmcnt(1)\n"
"ds_read_b128 v[208:211], v242 offset:45056\n"
"v_mfma_f32_16x16x32_bf16 v[8:11], v[220:223], a[48:51], v[8:11]\n"
"v_mfma_f32_16x16x32_bf16 v[24:27], v[220:223], a[52:55], v[24:27]\n"
"v_mfma_f32_16x16x32_bf16 v[40:43], v[220:223], a[56:59], v[40:43]\n"
"v_mfma_f32_16x16x32_bf16 v[56:59], v[220:223], a[60:63], v[56:59]\n"
"ds_read_b128 v[212:215], v242 offset:47104\n"
"v_mfma_f32_16x16x32_bf16 v[0:3], v[192:195], a[64:67], v[0:3]\n"
"buffer_load_dwordx4 a[16:19], v235, s[8:11], s56 offen offset:2048 sc1\n"
"v_mfma_f32_16x16x32_bf16 v[16:19], v[192:195], a[68:71], v[16:19]\n"
"v_mfma_f32_16x16x32_bf16 v[32:35], v[192:195], a[72:75], v[32:35]\n"
"v_mfma_f32_16x16x32_bf16 v[48:51], v[192:195], a[76:79], v[48:51]\n"
"ds_read_b128 v[216:219], v243 offset:45056\n"
"v_mfma_f32_16x16x32_bf16 v[8:11], v[196:199], a[64:67], v[8:11]\n"
"v_mfma_f32_16x16x32_bf16 v[24:27], v[196:199], a[68:71], v[24:27]\n"
"s_add_u32 m0, s92, 49152\n"
"s_lshl_b32 s48, s92, 1\n"
"v_mfma_f32_16x16x32_bf16 v[40:43], v[196:199], a[72:75], v[40:43]\n"
"s_add_u32 s48, s48, s44\n"
"buffer_load_dwordx4 a[20:23], v235, s[8:11], s57 offen offset:2048 sc1\n"
"v_mfma_f32_16x16x32_bf16 v[56:59], v[196:199], a[76:79], v[56:59]\n"
"s_add_u32 s48, s48, 8192\n"
"ds_read_b128 v[220:223], v243 offset:47104\n"
"v_mfma_f32_16x16x32_bf16 v[0:3], v[200:203], a[80:83], v[0:3]\n"
"buffer_load_dwordx4 v251, s[8:11], s48 offen sc1 lds\n"
"v_mfma_f32_16x16x32_bf16 v[16:19], v[200:203], a[84:87], v[16:19]\n"
"v_mfma_f32_16x16x32_bf16 v[32:35], v[200:203], a[88:91], v[32:35]\n"
"ds_read_b128 v[192:195], v244 offset:16384\n"
"v_mfma_f32_16x16x32_bf16 v[48:51], v[200:203], a[92:95], v[48:51]\n"
"s_waitcnt lgkmcnt(2)\n"
"v_mfma_f32_16x16x32_bf16 v[8:11], v[204:207], a[80:83], v[8:11]\n"
"buffer_load_dwordx4 a[24:27], v235, s[8:11], s58 offen offset:2048 sc1\n"
"v_mfma_f32_16x16x32_bf16 v[24:27], v[204:207], a[84:87], v[24:27]\n"
"v_mfma_f32_16x16x32_bf16 v[40:43], v[204:207], a[88:91], v[40:43]\n"
"ds_read_b128 v[196:199], v244 offset:18432\n"
"v_mfma_f32_16x16x32_bf16 v[56:59], v[204:207], a[92:95], v[56:59]\n"
"v_mfma_f32_16x16x32_bf16 v[0:3], v[208:211], a[96:99], v[0:3]\n"
"v_mfma_f32_16x16x32_bf16 v[16:19], v[208:211], a[100:103], v[16:19]\n"
"v_mfma_f32_16x16x32_bf16 v[32:35], v[208:211], a[104:107], v[32:35]\n"
"ds_read_b128 v[200:203], v245 offset:16384\n"
"v_mfma_f32_16x16x32_bf16 v[48:51], v[208:211], a[108:111], v[48:51]\n"
"buffer_load_dwordx4 a[28:31], v235, s[8:11], s59 offen offset:2048 sc1\n"
"v_mfma_f32_16x16x32_bf16 v[8:11], v[212:215], a[96:99], v[8:11]\n"
"v_mfma_f32_16x16x32_bf16 v[24:27], v[212:215], a[100:103], v[24:27]\n"
"s_add_u32 m0, s93, 45056\n"
"s_lshl_b32 s48, s93, 1\n"
"v_mfma_f32_16x16x32_bf16 v[40:43], v[212:215], a[104:107], v[40:43]\n"
"s_add_u32 s48, s48, s44\n"
"ds_read_b128 v[204:207], v245 offset:18432\n"
"v_mfma_f32_16x16x32_bf16 v[56:59], v[212:215], a[108:111], v[56:59]\n"
"v_mfma_f32_16x16x32_bf16 v[0:3], v[216:219], a[112:115], v[0:3]\n"
"buffer_load_dwordx4 a[32:35], v235, s[8:11], s56 offen offset:3072 sc1\n"
"v_mfma_f32_16x16x32_bf16 v[16:19], v[216:219], a[116:119], v[16:19]\n"
"buffer_load_dwordx4 v251, s[8:11], s48 offen sc1 lds\n"
"v_mfma_f32_16x16x32_bf16 v[32:35], v[216:219], a[120:123], v[32:35]\n"
"ds_read_b128 v[208:211], v244 offset:20480\n"
"v_mfma_f32_16x16x32_bf16 v[48:51], v[216:219], a[124:127], v[48:51]\n"
"s_waitcnt lgkmcnt(2)\n"
"v_mfma_f32_16x16x32_bf16 v[8:11], v[220:223], a[112:115], v[8:11]\n"
"v_mfma_f32_16x16x32_bf16 v[24:27], v[220:223], a[116:119], v[24:27]\n"
"ds_read_b128 v[212:215], v244 offset:22528\n"
"v_mfma_f32_16x16x32_bf16 v[40:43], v[220:223], a[120:123], v[40:43]\n"
"buffer_load_dwordx4 a[36:39], v235, s[8:11], s57 offen offset:3072 sc1\n"
"v_mfma_f32_16x16x32_bf16 v[56:59], v[220:223], a[124:127], v[56:59]\n"
"v_mfma_f32_16x16x32_bf16 v[0:3], v[192:195], a[128:131], v[0:3]\n"
"v_mfma_f32_16x16x32_bf16 v[16:19], v[192:195], a[132:135], v[16:19]\n"
"ds_read_b128 v[216:219], v245 offset:20480\n"
"v_mfma_f32_16x16x32_bf16 v[32:35], v[192:195], a[136:139], v[32:35]\n"
"v_mfma_f32_16x16x32_bf16 v[48:51], v[192:195], a[140:143], v[48:51]\n"
"v_mfma_f32_16x16x32_bf16 v[8:11], v[196:199], a[128:131], v[8:11]\n"
"buffer_load_dwordx4 a[40:43], v235, s[8:11], s58 offen offset:3072 sc1\n"
"v_mfma_f32_16x16x32_bf16 v[24:27], v[196:199], a[132:135], v[24:27]\n"
"ds_read_b128 v[220:223], v245 offset:22528\n"
"v_mfma_f32_16x16x32_bf16 v[40:43], v[196:199], a[136:139], v[40:43]\n"
"v_mfma_f32_16x16x32_bf16 v[56:59], v[196:199], a[140:143], v[56:59]\n"
"v_mfma_f32_16x16x32_bf16 v[0:3], v[200:203], a[144:147], v[0:3]\n"
"s_add_u32 s99, s99, 4096\n"
"ds_read_b128 v[192:195], v244 offset:40960\n"
"v_mfma_f32_16x16x32_bf16 v[16:19], v[200:203], a[148:151], v[16:19]\n"
"s_cmp_eq_u32 s99, 20480\n"
"s_cselect_b32 s99, 0, s99\n"
"v_mfma_f32_16x16x32_bf16 v[32:35], v[200:203], a[152:155], v[32:35]\n"
"buffer_load_dwordx4 a[44:47], v235, s[8:11], s59 offen offset:3072 sc1\n"
"v_mfma_f32_16x16x32_bf16 v[48:51], v[200:203], a[156:159], v[48:51]\n"
"s_waitcnt lgkmcnt(2)\n"
"v_mfma_f32_16x16x32_bf16 v[8:11], v[204:207], a[144:147], v[8:11]\n"
"ds_read_b128 v[196:199], v244 offset:43008\n"
"v_mfma_f32_16x16x32_bf16 v[24:27], v[204:207], a[148:151], v[24:27]\n"
"v_mfma_f32_16x16x32_bf16 v[40:43], v[204:207], a[152:155], v[40:43]\n"
"v_mfma_f32_16x16x32_bf16 v[56:59], v[204:207], a[156:159], v[56:59]\n"
"v_add_u32 v235, s99, v224\n"
"v_mfma_f32_16x16x32_bf16 v[0:3], v[208:211], a[160:163], v[0:3]\n"
"s_sub_u32 s53, s37, 65536\n"
"ds_read_b128 v[200:203], v245 offset:40960\n"
"v_mfma_f32_16x16x32_bf16 v[16:19], v[208:211], a[164:167], v[16:19]\n"
"buffer_load_dwordx4 a[48:51], v235, s[8:11], s56 offen offset:0 sc1\n"
"v_mfma_f32_16x16x32_bf16 v[32:35], v[208:211], a[168:171], v[32:35]\n"
"v_subrev_u32 v246, s53, v246\n"
"v_subrev_u32 v247, s53, v247\n"
"v_mfma_f32_16x16x32_bf16 v[48:51], v[208:211], a[172:175], v[48:51]\n"
"v_subrev_u32 v248, s53, v248\n"
"v_subrev_u32 v249, s53, v249\n"
"v_mfma_f32_16x16x32_bf16 v[8:11], v[212:215], a[160:163], v[8:11]\n"
"ds_read_b128 v[204:207], v245 offset:43008\n"
"v_mfma_f32_16x16x32_bf16 v[24:27], v[212:215], a[164:167], v[24:27]\n"
"v_mfma_f32_16x16x32_bf16 v[40:43], v[212:215], a[168:171], v[40:43]\n"
"buffer_load_dwordx4 a[52:55], v235, s[8:11], s57 offen offset:0 sc1\n"
"v_mfma_f32_16x16x32_bf16 v[56:59], v[212:215], a[172:175], v[56:59]\n"
"ds_read_b128 v[208:211], v244 offset:45056\n"
"v_mfma_f32_16x16x32_bf16 v[0:3], v[216:219], a[176:179], v[0:3]\n"
"v_mfma_f32_16x16x32_bf16 v[16:19], v[216:219], a[180:183], v[16:19]\n"
"v_mfma_f32_16x16x32_bf16 v[32:35], v[216:219], a[184:187], v[32:35]\n"
"v_mfma_f32_16x16x32_bf16 v[48:51], v[216:219], a[188:191], v[48:51]\n"
"s_waitcnt lgkmcnt(1)\n"
"ds_read_b128 v[212:215], v244 offset:47104\n"
"v_mfma_f32_16x16x32_bf16 v[8:11], v[220:223], a[176:179], v[8:11]\n"
"buffer_load_dwordx4 a[56:59], v235, s[8:11], s58 offen offset:0 sc1\n"
"v_mfma_f32_16x16x32_bf16 v[24:27], v[220:223], a[180:183], v[24:27]\n"
"v_mfma_f32_16x16x32_bf16 v[40:43], v[220:223], a[184:187], v[40:43]\n"
"v_mfma_f32_16x16x32_bf16 v[56:59], v[220:223], a[188:191], v[56:59]\n"
"ds_read_b128 v[216:219], v245 offset:45056\n"
"v_mfma_f32_16x16x32_bf16 v[0:3], v[192:195], a[192:195], v[0:3]\n"
"v_mfma_f32_16x16x32_bf16 v[16:19], v[192:195], a[196:199], v[16:19]\n"
"v_mfma_f32_16x16x32_bf16 v[32:35], v[192:195], a[200:203], v[32:35]\n"
"buffer_load_dwordx4 a[60:63], v235, s[8:11], s59 offen offset:0 sc1\n"
"v_mfma_f32_16x16x32_bf16 v[48:51], v[192:195], a[204:207], v[48:51]\n"
"ds_read_b128 v[220:223], v245 offset:47104\n"
"v_mfma_f32_16x16x32_bf16 v[8:11], v[196:199], a[192:195], v[8:11]\n"
"v_mfma_f32_16x16x32_bf16 v[24:27], v[196:199], a[196:199], v[24:27]\n"
"v_mfma_f32_16x16x32_bf16 v[40:43], v[196:199], a[200:203], v[40:43]\n"
"s_mov_b32 s49, 270336\n"
"v_mfma_f32_16x16x32_bf16 v[56:59], v[196:199], a[204:207], v[56:59]\n"
"v_mfma_f32_16x16x32_bf16 v[0:3], v[200:203], a[208:211], v[0:3]\n"
"s_lshl_b32 s50, s35, 3\n"
"buffer_store_dwordx4 v[4:7], v250, s[8:11], s49 offen offset:0 sc1\n"
"v_mfma_f32_16x16x32_bf16 v[16:19], v[200:203], a[212:215], v[16:19]\n"
"s_add_u32 s50, s50, s34\n"
"buffer_load_dwordx4 a[64:67], v235, s[8:11], s56 offen offset:1024 sc1\n"
"v_mfma_f32_16x16x32_bf16 v[32:35], v[200:203], a[216:219], v[32:35]\n"
"s_lshl_b32 s50, s50, 3\n"
"s_add_u32 s50, s50, 2304\n"
"s_add_u32 s51, s50, s45\n"
"v_mfma_f32_16x16x32_bf16 v[48:51], v[200:203], a[220:223], v[48:51]\n"
"v_mfma_f32_16x16x32_bf16 v[8:11], v[204:207], a[208:211], v[8:11]\n"
"v_mfma_f32_16x16x32_bf16 v[24:27], v[204:207], a[212:215], v[24:27]\n"
"s_waitcnt vmcnt(37) lgkmcnt(0)\n"
"s_barrier\n"
"v_mfma_f32_16x16x32_bf16 v[40:43], v[204:207], a[216:219], v[40:43]\n"
"s_add_u32 m0, s38, 81920\n"
"ds_read_b64_tr_b16 a[128:129], v246 offset:0\n"
"s_add_u32 s48, s39, s98\n"
"v_mfma_f32_16x16x32_bf16 v[56:59], v[204:207], a[220:223], v[56:59]\n"
"buffer_load_dwordx4 a[68:71], v235, s[8:11], s57 offen offset:1024 sc1\n"
"v_mfma_f32_16x16x32_bf16 v[0:3], v[208:211], a[224:227], v[0:3]\n"
"buffer_store_dwordx2 v[232:233], v230, s[8:11], s51 offen sc1\n"
"v_mfma_f32_16x16x32_bf16 v[16:19], v[208:211], a[228:231], v[16:19]\n"
"ds_read_b64_tr_b16 a[130:131], v246 offset:512\n"
"v_mfma_f32_16x16x32_bf16 v[32:35], v[208:211], a[232:235], v[32:35]\n"
"buffer_load_dwordx4 v238, s[20:23], s48 offen offset:0 lds\n"
"v_mfma_f32_16x16x32_bf16 v[48:51], v[208:211], a[236:239], v[48:51]\n"
"ds_read_b64_tr_b16 a[132:133], v247 offset:0\n"
"v_add_u32 v242, 49152, v246\n"
"v_mfma_f32_16x16x32_bf16 v[8:11], v[212:215], a[224:227], v[8:11]\n"
"ds_read_b64_tr_b16 a[134:135], v247 offset:512\n"
"v_add_u32 v243, 49152, v247\n"
"v_mfma_f32_16x16x32_bf16 v[24:27], v[212:215], a[228:231], v[24:27]\n"
"buffer_load_dwordx4 a[72:75], v235, s[8:11], s58 offen offset:1024 sc1\n"
"v_mfma_f32_16x16x32_bf16 v[40:43], v[212:215], a[232:235], v[40:43]\n"
"ds_read_b64_tr_b16 a[136:137], v248 offset:0\n"
"v_add_u32 v244, 49152, v248\n"
"v_mfma_f32_16x16x32_bf16 v[56:59], v[212:215], a[236:239], v[56:59]\n"
"v_add_u32 v245, 49152, v249\n"
"v_mfma_f32_16x16x32_bf16 v[0:3], v[216:219], a[240:243], v[0:3]\n"
"ds_read_b64_tr_b16 a[138:139], v248 offset:512\n"
"v_mfma_f32_16x16x32_bf16 v[16:19], v[216:219], a[244:247], v[16:19]\n"
"buffer_load_dwordx4 v239, s[20:23], s48 offen offset:1024 lds\n"
"v_mfma_f32_16x16x32_bf16 v[32:35], v[216:219], a[248:251], v[32:35]\n"
"ds_read_b64_tr_b16 a[140:141], v249 offset:0\n"
"v_mfma_f32_16x16x32_bf16 v[48:51], v[216:219], a[252:255], v[48:51]\n"
"buffer_load_dwordx4 a[76:79], v235, s[8:11], s59 offen offset:1024 sc1\n"
"v_mfma_f32_16x16x32_bf16 v[8:11], v[220:223], a[240:243], v[8:11]\n"
"ds_read_b64_tr_b16 a[142:143], v249 offset:512\n"
"v_mfma_f32_16x16x32_bf16 v[24:27], v[220:223], a[244:247], v[24:27]\n"
"v_mfma_f32_16x16x32_bf16 v[40:43], v[220:223], a[248:251], v[40:43]\n"
"ds_read_b64_tr_b16 a[144:145], v246 offset:4096\n"
"v_mfma_f32_16x16x32_bf16 v[56:59], v[220:223], a[252:255], v[56:59]\n"
"s_waitcnt vmcnt(41) lgkmcnt(3)\n"
"v_mfma_f32_16x16x32_bf16 v[80:83], a[128:131], v[64:67], 0\n"
"s_waitcnt vmcnt(40)\n"
"ds_read_b64_tr_b16 a[146:147], v246 offset:4608\n"
"v_mfma_f32_16x16x32_bf16 v[88:91], a[128:131], v[68:71], 0\n"
"s_waitcnt vmcnt(39)\n"
"buffer_load_dwordx4 a[80:83], v235, s[8:11], s56 offen offset:2048 sc1\n"
"v_mfma_f32_16x16x32_bf16 v[96:99], a[128:131], v[72:75], 0\n"
"s_waitcnt vmcnt(38)\n"
"buffer_load_dwordx4 v240, s[20:23], s48 offen offset:2048 lds\n"
"v_mfma_f32_16x16x32_bf16 v[104:107], a[128:131], v[76:79], 0\n"
"ds_read_b64_tr_b16 a[148:149], v247 offset:4096\n"
"v_cvt_pk_bf16_f32 v0, v0, v1\n"
"v_mfma_f32_16x16x32_bf16 v[84:87], a[132:135], v[64:67], 0\n"
"ds_read_b64_tr_b16 a[150:151], v247 offset:4608\n"
"v_cvt_pk_bf16_f32 v1, v2, v3\n"
"v_mfma_f32_16x16x32_bf16 v[92:95], a[132:135], v[68:71], 0\n"
"ds_read_b64_tr_b16 a[152:153], v248 offset:4096\n"
"v_cvt_pk_bf16_f32 v2, v8, v9\n"
"v_mfma_f32_16x16x32_bf16 v[100:103], a[132:135], v[72:75], 0\n"
"v_cvt_pk_bf16_f32 v3, v10, v11\n"
"v_mfma_f32_16x16x32_bf16 v[108:111], a[132:135], v[76:79], 0\n"
"ds_read_b64_tr_b16 a[154:155], v248 offset:4608\n"
"v_mfma_f32_16x16x32_bf16 v[112:115], a[136:139], v[64:67], 0\n"
"s_add_u32 s98, s98, 524288\n"
"buffer_load_dwordx4 a[84:87], v235, s[8:11], s57 offen offset:2048 sc1\n"
"v_mfma_f32_16x16x32_bf16 v[120:123], a[136:139], v[68:71], 0\n"
"buffer_load_dwordx4 v241, s[20:23], s48 offen offset:3072 lds\n"
"v_mfma_f32_16x16x32_bf16 v[128:131], a[136:139], v[72:75], 0\n"
"ds_read_b64_tr_b16 a[156:157], v249 offset:4096\n"
"v_cvt_pk_bf16_f32 v16, v16, v17\n"
"v_mfma_f32_16x16x32_bf16 v[136:139], a[136:139], v[76:79], 0\n"
"s_waitcnt lgkmcnt(3)\n"
"ds_read_b64_tr_b16 a[158:159], v249 offset:4608\n"
"v_cvt_pk_bf16_f32 v17, v18, v19\n"
"v_mfma_f32_16x16x32_bf16 v[116:119], a[140:143], v[64:67], 0\n"
"v_cvt_pk_bf16_f32 v18, v24, v25\n"
"v_cvt_pk_bf16_f32 v19, v26, v27\n"
"v_mfma_f32_16x16x32_bf16 v[124:127], a[140:143], v[68:71], 0\n"
"ds_read_b64_tr_b16 a[160:161], v246 offset:24576\n"
"v_cvt_pk_bf16_f32 v32, v32, v33\n"
"v_mfma_f32_16x16x32_bf16 v[132:135], a[140:143], v[72:75], 0\n"
"s_add_u32 m0, s38, 131072\n"
"buffer_load_dwordx4 a[88:91], v235, s[8:11], s58 offen offset:2048 sc1\n"
"v_mfma_f32_16x16x32_bf16 v[140:143], a[140:143], v[76:79], 0\n"
"s_add_u32 s48, s39, s98\n"
"ds_read_b64_tr_b16 a[162:163], v246 offset:25088\n"
"v_cvt_pk_bf16_f32 v33, v34, v35\n"
"v_mfma_f32_16x16x32_bf16 v[144:147], a[144:147], v[64:67], 0\n"
"buffer_load_dwordx4 v238, s[20:23], s48 offen offset:0 lds\n"
"v_mfma_f32_16x16x32_bf16 v[152:155], a[144:147], v[68:71], 0\n"
"ds_read_b64_tr_b16 a[164:165], v247 offset:24576\n"
"v_cvt_pk_bf16_f32 v34, v40, v41\n"
"v_mfma_f32_16x16x32_bf16 v[160:163], a[144:147], v[72:75], 0\n"
"v_cvt_pk_bf16_f32 v35, v42, v43\n"
"v_cvt_pk_bf16_f32 v48, v48, v49\n"
"v_mfma_f32_16x16x32_bf16 v[168:171], a[144:147], v[76:79], 0\n"
"ds_read_b64_tr_b16 a[166:167], v247 offset:25088\n"
"v_cvt_pk_bf16_f32 v49, v50, v51\n"
"v_mfma_f32_16x16x32_bf16 v[148:151], a[148:151], v[64:67], 0\n"
"buffer_store_dwordx4 v[20:23], v250, s[8:11], s49 offen offset:1024 sc1\n"
"v_mfma_f32_16x16x32_bf16 v[156:159], a[148:151], v[68:71], 0\n"
"ds_read_b64_tr_b16 a[168:169], v248 offset:24576\n"
"v_cvt_pk_bf16_f32 v50, v56, v57\n"
"v_mfma_f32_16x16x32_bf16 v[164:167], a[148:151], v[72:75], 0\n"
"buffer_load_dwordx4 a[92:95], v235, s[8:11], s59 offen offset:2048 sc1\n"
"v_mfma_f32_16x16x32_bf16 v[172:175], a[148:151], v[76:79], 0\n"
"s_waitcnt lgkmcnt(3)\n"
"ds_read_b64_tr_b16 a[170:171], v248 offset:25088\n"
"v_cvt_pk_bf16_f32 v51, v58, v59\n"
"v_mfma_f32_16x16x32_bf16 v[176:179], a[152:155], v[64:67], 0\n"
"ds_read_b64_tr_b16 a[172:173], v249 offset:24576\n"
"v_mfma_f32_16x16x32_bf16 v[184:187], a[152:155], v[68:71], 0\n"
"buffer_load_dwordx4 v239, s[20:23], s48 offen offset:1024 lds\n"
"v_mfma_f32_16x16x32_bf16 v[192:195], a[152:155], v[72:75], 0\n"
"ds_read_b64_tr_b16 a[174:175], v249 offset:25088\n"
"v_mfma_f32_16x16x32_bf16 v[200:203], a[152:155], v[76:79], 0\n"
"v_mfma_f32_16x16x32_bf16 v[180:183], a[156:159], v[64:67], 0\n"
"ds_read_b64_tr_b16 a[176:177], v246 offset:28672\n"
"v_mfma_f32_16x16x32_bf16 v[188:191], a[156:159], v[68:71], 0\n"
"buffer_load_dwordx4 a[96:99], v235, s[8:11], s56 offen offset:3072 sc1\n"
"v_mfma_f32_16x16x32_bf16 v[196:199], a[156:159], v[72:75], 0\n"
"ds_read_b64_tr_b16 a[178:179], v246 offset:29184\n"
"v_mfma_f32_16x16x32_bf16 v[204:207], a[156:159], v[76:79], 0\n"
"s_waitcnt vmcnt(38)\n"
"v_mfma_f32_16x16x32_bf16 v[80:83], a[160:163], a[0:3], v[80:83]\n"
"s_waitcnt vmcnt(36)\n"
"ds_read_b64_tr_b16 a[180:181], v247 offset:28672\n"
"v_mfma_f32_16x16x32_bf16 v[88:91], a[160:163], a[4:7], v[88:91]\n"
"s_waitcnt vmcnt(34)\n"
"buffer_load_dwordx4 v240, s[20:23], s48 offen offset:2048 lds\n"
"v_mfma_f32_16x16x32_bf16 v[96:99], a[160:163], a[8:11], v[96:99]\n"
"s_waitcnt vmcnt(33)\n"
"ds_read_b64_tr_b16 a[182:183], v247 offset:29184\n"
"v_mfma_f32_16x16x32_bf16 v[104:107], a[160:163], a[12:15], v[104:107]\n"
"s_waitcnt lgkmcnt(3)\n"
"buffer_load_dwordx4 a[100:103], v235, s[8:11], s57 offen offset:3072 sc1\n"
"v_mfma_f32_16x16x32_bf16 v[84:87], a[164:167], a[0:3], v[84:87]\n"
"s_mov_b32 s88, 5120\n"
"ds_read_b64_tr_b16 a[184:185], v248 offset:28672\n"
"v_mfma_f32_16x16x32_bf16 v[92:95], a[164:167], a[4:7], v[92:95]\n"
"s_mov_b32 s89, 1024\n"
"v_mfma_f32_16x16x32_bf16 v[100:103], a[164:167], a[8:11], v[100:103]\n"
"ds_read_b64_tr_b16 a[186:187], v248 offset:29184\n"
"v_mfma_f32_16x16x32_bf16 v[108:111], a[164:167], a[12:15], v[108:111]\n"
"v_mfma_f32_16x16x32_bf16 v[112:115], a[168:171], a[0:3], v[112:115]\n"
"s_add_u32 s98, s98, 524288\n"
"ds_read_b64_tr_b16 a[188:189], v249 offset:28672\n"
"v_mfma_f32_16x16x32_bf16 v[120:123], a[168:171], a[4:7], v[120:123]\n"
"s_cmp_eq_u32 s98, 5242880\n"
"buffer_load_dwordx4 v241, s[20:23], s48 offen offset:3072 lds\n"
"v_mfma_f32_16x16x32_bf16 v[128:131], a[168:171], a[8:11], v[128:131]\n"
"s_cselect_b32 s98, 0, s98\n"
"ds_read_b64_tr_b16 a[190:191], v249 offset:29184\n"
"v_mfma_f32_16x16x32_bf16 v[136:139], a[168:171], a[12:15], v[136:139]\n"
"buffer_load_dwordx4 a[104:107], v235, s[8:11], s58 offen offset:3072 sc1\n"
"v_mfma_f32_16x16x32_bf16 v[116:119], a[172:175], a[0:3], v[116:119]\n"
"ds_read_b64_tr_b16 a[128:129], v242 offset:0\n"
"v_mfma_f32_16x16x32_bf16 v[124:127], a[172:175], a[4:7], v[124:127]\n"
"ds_read_b64_tr_b16 a[130:131], v242 offset:512\n"
"v_mfma_f32_16x16x32_bf16 v[132:135], a[172:175], a[8:11], v[132:135]\n"
"v_mfma_f32_16x16x32_bf16 v[140:143], a[172:175], a[12:15], v[140:143]\n"
"s_waitcnt lgkmcnt(3)\n"
"ds_read_b64_tr_b16 a[132:133], v243 offset:0\n"
"v_mfma_f32_16x16x32_bf16 v[144:147], a[176:179], a[0:3], v[144:147]\n"
"s_add_u32 s99, s99, 4096\n"
"v_mfma_f32_16x16x32_bf16 v[152:155], a[176:179], a[4:7], v[152:155]\n"
"s_cmp_eq_u32 s99, 20480\n"
"ds_read_b64_tr_b16 a[134:135], v243 offset:512\n"
"s_cselect_b32 s99, 0, s99\n"
"v_mfma_f32_16x16x32_bf16 v[160:163], a[176:179], a[8:11], v[160:163]\n"
"buffer_load_dwordx4 a[108:111], v235, s[8:11], s59 offen offset:3072 sc1\n"
"v_mfma_f32_16x16x32_bf16 v[168:171], a[176:179], a[12:15], v[168:171]\n"
"ds_read_b64_tr_b16 a[136:137], v244 offset:0\n"
"v_mfma_f32_16x16x32_bf16 v[148:151], a[180:183], a[0:3], v[148:151]\n"
"v_mfma_f32_16x16x32_bf16 v[156:159], a[180:183], a[4:7], v[156:159]\n"
"ds_read_b64_tr_b16 a[138:139], v244 offset:512\n"
"v_mfma_f32_16x16x32_bf16 v[164:167], a[180:183], a[8:11], v[164:167]\n"
"v_mfma_f32_16x16x32_bf16 v[172:175], a[180:183], a[12:15], v[172:175]\n"
"ds_read_b64_tr_b16 a[140:141], v245 offset:0\n"
"v_add_u32 v235, s99, v224\n"
"v_mfma_f32_16x16x32_bf16 v[176:179], a[184:187], a[0:3], v[176:179]\n"
"buffer_load_dwordx4 a[112:115], v235, s[8:11], s56 offen offset:0 sc1\n"
"v_mfma_f32_16x16x32_bf16 v[184:187], a[184:187], a[4:7], v[184:187]\n"
"ds_read_b64_tr_b16 a[142:143], v245 offset:512\n"
"v_mfma_f32_16x16x32_bf16 v[192:195], a[184:187], a[8:11], v[192:195]\n"
"v_mfma_f32_16x16x32_bf16 v[200:203], a[184:187], a[12:15], v[200:203]\n"
"s_waitcnt lgkmcnt(3)\n"
"ds_read_b64_tr_b16 a[144:145], v242 offset:4096\n"
"v_mfma_f32_16x16x32_bf16 v[180:183], a[188:191], a[0:3], v[180:183]\n"
"v_mfma_f32_16x16x32_bf16 v[188:191], a[188:191], a[4:7], v[188:191]\n"
"ds_read_b64_tr_b16 a[146:147], v242 offset:4608\n"
"v_mfma_f32_16x16x32_bf16 v[196:199], a[188:191], a[8:11], v[196:199]\n"
"v_mfma_f32_16x16x32_bf16 v[204:207], a[188:191], a[12:15], v[204:207]\n"
"s_waitcnt vmcnt(37)\n"
"ds_read_b64_tr_b16 a[148:149], v243 offset:4096\n"
"v_mfma_f32_16x16x32_bf16 v[80:83], a[128:131], a[16:19], v[80:83]\n"
"s_waitcnt vmcnt(36)\n"
"buffer_load_dwordx4 a[116:119], v235, s[8:11], s57 offen offset:0 sc1\n"
"v_mfma_f32_16x16x32_bf16 v[88:91], a[128:131], a[20:23], v[88:91]\n"
"s_waitcnt vmcnt(35)\n"
"buffer_store_dwordx4 v[36:39], v250, s[8:11], s49 offen offset:2048 sc1\n"
"v_mfma_f32_16x16x32_bf16 v[96:99], a[128:131], a[24:27], v[96:99]\n"
"s_waitcnt vmcnt(35)\n"
"ds_read_b64_tr_b16 a[150:151], v243 offset:4608\n"
"v_mfma_f32_16x16x32_bf16 v[104:107], a[128:131], a[28:31], v[104:107]\n"
"ds_read_b64_tr_b16 a[152:153], v244 offset:4096\n"
"v_mfma_f32_16x16x32_bf16 v[84:87], a[132:135], a[16:19], v[84:87]\n"
"ds_read_b64_tr_b16 a[154:155], v244 offset:4608\n"
"v_mfma_f32_16x16x32_bf16 v[92:95], a[132:135], a[20:23], v[92:95]\n"
"v_mfma_f32_16x16x32_bf16 v[100:103], a[132:135], a[24:27], v[100:103]\n"
"ds_read_b64_tr_b16 a[156:157], v245 offset:4096\n"
"v_mfma_f32_16x16x32_bf16 v[108:111], a[132:135], a[28:31], v[108:111]\n"
"s_waitcnt lgkmcnt(4)\n"
"buffer_load_dwordx4 a[120:123], v235, s[8:11], s58 offen offset:0 sc1\n"
"v_mfma_f32_16x16x32_bf16 v[112:115], a[136:139], a[16:19], v[112:115]\n"
"ds_read_b64_tr_b16 a[158:159], v245 offset:4608\n"
"v_mfma_f32_16x16x32_bf16 v[120:123], a[136:139], a[20:23], v[120:123]\n"
"v_mfma_f32_16x16x32_bf16 v[128:131], a[136:139], a[24:27], v[128:131]\n"
"ds_read_b64_tr_b16 a[160:161], v242 offset:24576\n"
"v_mfma_f32_16x16x32_bf16 v[136:139], a[136:139], a[28:31], v[136:139]\n"
"v_mfma_f32_16x16x32_bf16 v[116:119], a[140:143], a[16:19], v[116:119]\n"
"ds_read_b64_tr_b16 a[162:163], v242 offset:25088\n"
"v_mfma_f32_16x16x32_bf16 v[124:127], a[140:143], a[20:23], v[124:127]\n"
"buffer_load_dwordx4 a[124:127], v235, s[8:11], s59 offen offset:0 sc1\n"
"v_mfma_f32_16x16x32_bf16 v[132:135], a[140:143], a[24:27], v[132:135]\n"
"ds_read_b64_tr_b16 a[164:165], v243 offset:24576\n"
"v_mfma_f32_16x16x32_bf16 v[140:143], a[140:143], a[28:31], v[140:143]\n"
"v_mfma_f32_16x16x32_bf16 v[144:147], a[144:147], a[16:19], v[144:147]\n"
"ds_read_b64_tr_b16 a[166:167], v243 offset:25088\n"
"v_mfma_f32_16x16x32_bf16 v[152:155], a[144:147], a[20:23], v[152:155]\n"
"v_mfma_f32_16x16x32_bf16 v[160:163], a[144:147], a[24:27], v[160:163]\n"
"ds_read_b64_tr_b16 a[168:169], v244 offset:24576\n"
"v_mfma_f32_16x16x32_bf16 v[168:171], a[144:147], a[28:31], v[168:171]\n"
"s_waitcnt lgkmcnt(3)\n"
"v_mfma_f32_16x16x32_bf16 v[148:151], a[148:151], a[16:19], v[148:151]\n"
"ds_read_b64_tr_b16 a[170:171], v244 offset:25088\n"
"v_mfma_f32_16x16x32_bf16 v[156:159], a[148:151], a[20:23], v[156:159]\n"
"buffer_load_dwordx4 a[0:3], v235, s[8:11], s56 offen offset:1024 sc1\n"
"v_mfma_f32_16x16x32_bf16 v[164:167], a[148:151], a[24:27], v[164:167]\n"
"ds_read_b64_tr_b16 a[172:173], v245 offset:24576\n"
"v_mfma_f32_16x16x32_bf16 v[172:175], a[148:151], a[28:31], v[172:175]\n"
"ds_read_b64_tr_b16 a[174:175], v245 offset:25088\n"
"v_mfma_f32_16x16x32_bf16 v[176:179], a[152:155], a[16:19], v[176:179]\n"
"v_mfma_f32_16x16x32_bf16 v[184:187], a[152:155], a[20:23], v[184:187]\n"
"ds_read_b64_tr_b16 a[176:177], v242 offset:28672\n"
"v_mfma_f32_16x16x32_bf16 v[192:195], a[152:155], a[24:27], v[192:195]\n"
"v_mfma_f32_16x16x32_bf16 v[200:203], a[152:155], a[28:31], v[200:203]\n"
"ds_read_b64_tr_b16 a[178:179], v242 offset:29184\n"
"v_mfma_f32_16x16x32_bf16 v[180:183], a[156:159], a[16:19], v[180:183]\n"
"s_mov_b32 s49, 273408\n"
"buffer_load_dwordx4 a[4:7], v235, s[8:11], s57 offen offset:1024 sc1\n"
"v_mfma_f32_16x16x32_bf16 v[188:191], a[156:159], a[20:23], v[188:191]\n"
"ds_read_b64_tr_b16 a[180:181], v243 offset:28672\n"
"v_mfma_f32_16x16x32_bf16 v[196:199], a[156:159], a[24:27], v[196:199]\n"
"v_mfma_f32_16x16x32_bf16 v[204:207], a[156:159], a[28:31], v[204:207]\n"
"ds_read_b64_tr_b16 a[182:183], v243 offset:29184\n"
"LQ_%=:\n"
"s_waitcnt vmcnt(38)\n"
"v_mfma_f32_16x16x32_bf16 v[80:83], a[160:163], a[32:35], v[80:83]\n"
"s_waitcnt vmcnt(36)\n"
"v_mfma_f32_16x16x32_bf16 v[88:91], a[160:163], a[36:39], v[88:91]\n"
"s_waitcnt vmcnt(35)\n"
"ds_read_b64_tr_b16 a[184:185], v244 offset:28672\n"
"v_mfma_f32_16x16x32_bf16 v[96:99], a[160:163], a[40:43], v[96:99]\n"
"s_waitcnt vmcnt(34)\n"
"buffer_load_dwordx4 a[8:11], v235, s[8:11], s58 offen offset:1024 sc1\n"
"v_mfma_f32_16x16x32_bf16 v[104:107], a[160:163], a[44:47], v[104:107]\n"
"s_waitcnt lgkmcnt(3)\n"
"ds_read_b64_tr_b16 a[186:187], v244 offset:29184\n"
"v_mfma_f32_16x16x32_bf16 v[84:87], a[164:167], a[32:35], v[84:87]\n"
"v_mfma_f32_16x16x32_bf16 v[92:95], a[164:167], a[36:39], v[92:95]\n"
"ds_read_b64_tr_b16 a[188:189], v245 offset:28672\n"
"v_mfma_f32_16x16x32_bf16 v[100:103], a[164:167], a[40:43], v[100:103]\n"
"v_mfma_f32_16x16x32_bf16 v[108:111], a[164:167], a[44:47], v[108:111]\n"
"ds_read_b64_tr_b16 a[190:191], v245 offset:29184\n"
"v_mfma_f32_16x16x32_bf16 v[112:115], a[168:171], a[32:35], v[112:115]\n"
"v_mfma_f32_16x16x32_bf16 v[120:123], a[168:171], a[36:39], v[120:123]\n"
"buffer_load_dwordx4 a[12:15], v235, s[8:11], s59 offen offset:1024 sc1\n"
"v_mfma_f32_16x16x32_bf16 v[128:131], a[168:171], a[40:43], v[128:131]\n"
"v_mfma_f32_16x16x32_bf16 v[136:139], a[168:171], a[44:47], v[136:139]\n"
"v_mfma_f32_16x16x32_bf16 v[116:119], a[172:175], a[32:35], v[116:119]\n"
"buffer_store_dwordx4 v[52:55], v250, s[8:11], s49 offen offset:0 sc1\n"
"v_mfma_f32_16x16x32_bf16 v[124:127], a[172:175], a[36:39], v[124:127]\n"
"s_waitcnt lgkmcnt(0)\n"
"s_barrier\n"
"v_mfma_f32_16x16x32_bf16 v[132:135], a[172:175], a[40:43], v[132:135]\n"
"s_add_u32 m0, s38, 65536\n"
"ds_read_b64_tr_b16 a[128:129], v246 offset:8192\n"
"s_add_u32 s48, s39, s98\n"
"v_mfma_f32_16x16x32_bf16 v[140:143], a[172:175], a[44:47], v[140:143]\n"
"v_mfma_f32_16x16x32_bf16 v[144:147], a[176:179], a[32:35], v[144:147]\n"
"ds_read_b64_tr_b16 a[130:131], v246 offset:8704\n"
"v_mfma_f32_16x16x32_bf16 v[152:155], a[176:179], a[36:39], v[152:155]\n"
"buffer_load_dwordx4 a[16:19], v235, s[8:11], s56 offen offset:2048 sc1\n"
"v_mfma_f32_16x16x32_bf16 v[160:163], a[176:179], a[40:43], v[160:163]\n"
"buffer_load_dwordx4 v238, s[20:23], s48 offen offset:0 lds\n"
"v_mfma_f32_16x16x32_bf16 v[168:171], a[176:179], a[44:47], v[168:171]\n"
"ds_read_b64_tr_b16 a[132:133], v247 offset:8192\n"
"v_mfma_f32_16x16x32_bf16 v[148:151], a[180:183], a[32:35], v[148:151]\n"
"ds_read_b64_tr_b16 a[134:135], v247 offset:8704\n"
"v_mfma_f32_16x16x32_bf16 v[156:159], a[180:183], a[36:39], v[156:159]\n"
"v_mfma_f32_16x16x32_bf16 v[164:167], a[180:183], a[40:43], v[164:167]\n"
"ds_read_b64_tr_b16 a[136:137], v248 offset:8192\n"
"v_mfma_f32_16x16x32_bf16 v[172:175], a[180:183], a[44:47], v[172:175]\n"
"buffer_load_dwordx4 a[20:23], v235, s[8:11], s57 offen offset:2048 sc1\n"
"v_mfma_f32_16x16x32_bf16 v[176:179], a[184:187], a[32:35], v[176:179]\n"
"ds_read_b64_tr_b16 a[138:139], v248 offset:8704\n"
"v_mfma_f32_16x16x32_bf16 v[184:187], a[184:187], a[36:39], v[184:187]\n"
"buffer_load_dwordx4 v239, s[20:23], s48 offen offset:1024 lds\n"
"v_mfma_f32_16x16x32_bf16 v[192:195], a[184:187], a[40:43], v[192:195]\n"
"ds_read_b64_tr_b16 a[140:141], v249 offset:8192\n"
"v_mfma_f32_16x16x32_bf16 v[200:203], a[184:187], a[44:47], v[200:203]\n"
"v_mfma_f32_16x16x32_bf16 v[180:183], a[188:191], a[32:35], v[180:183]\n"
"ds_read_b64_tr_b16 a[142:143], v249 offset:8704\n"
"v_lshrrev_b32 v254, 8, v224\n"
"v_mfma_f32_16x16x32_bf16 v[188:191], a[188:191], a[36:39], v[188:191]\n"
"v_lshlrev_b32 v251, 7, v254\n"
"v_mfma_f32_16x16x32_bf16 v[196:199], a[188:191], a[40:43], v[196:199]\n"
"ds_read_b64_tr_b16 a[144:145], v246 offset:12288\n"
"v_lshlrev_b32 v254, 8, v254\n"
"v_mfma_f32_16x16x32_bf16 v[204:207], a[188:191], a[44:47], v[204:207]\n"
"s_waitcnt vmcnt(40) lgkmcnt(3)\n"
"buffer_load_dwordx4 a[24:27], v235, s[8:11], s58 offen offset:2048 sc1\n"
"v_mfma_f32_16x16x32_bf16 v[80:83], a[128:131], a[48:51], v[80:83]\n"
"s_waitcnt vmcnt(40)\n"
"ds_read_b64_tr_b16 a[146:147], v246 offset:12800\n"
"v_lshrrev_b32 v255, 1, v224\n"
"v_mfma_f32_16x16x32_bf16 v[88:91], a[128:131], a[52:55], v[88:91]\n"
"s_waitcnt vmcnt(39)\n"
"buffer_load_dwordx4 v240, s[20:23], s48 offen offset:2048 lds\n"
"v_mfma_f32_16x16x32_bf16 v[96:99], a[128:131], a[56:59], v[96:99]\n"
"s_waitcnt vmcnt(39)\n"
"ds_read_b64_tr_b16 a[148:149], v247 offset:12288\n"
"v_and_b32 v255, 0x78, v255\n"
"v_mfma_f32_16x16x32_bf16 v[104:107], a[128:131], a[60:63], v[104:107]\n"
"s_mul_i32 s92, s30, 10240\n"
"v_add3_u32 v254, v254, v255, s92\n"
"v_and_b32 v255, 0x70, v224\n"
"v_mfma_f32_16x16x32_bf16 v[84:87], a[132:135], a[48:51], v[84:87]\n"
"s_and_b32 s93, s30, 1\n"
"ds_read_b64_tr_b16 a[150:151], v247 offset:12800\n"
"v_mfma_f32_16x16x32_bf16 v[92:95], a[132:135], a[52:55], v[92:95]\n"
"s_mul_i32 s93, s93, 5120\n"
"ds_read_b64_tr_b16 a[152:153], v248 offset:12288\n"
"v_mfma_f32_16x16x32_bf16 v[100:103], a[132:135], a[56:59], v[100:103]\n"
"buffer_load_dwordx4 a[28:31], v235, s[8:11], s59 offen offset:2048 sc1\n"
"v_mfma_f32_16x16x32_bf16 v[108:111], a[132:135], a[60:63], v[108:111]\n"
"ds_read_b64_tr_b16 a[154:155], v248 offset:12800\n"
"v_add3_u32 v251, v251, v255, s93\n"
"v_mfma_f32_16x16x32_bf16 v[112:115], a[136:139], a[48:51], v[112:115]\n"
"s_add_u32 s98, s98, 524288\n"
"buffer_load_dwordx4 v241, s[20:23], s48 offen offset:3072 lds\n"
"v_mfma_f32_16x16x32_bf16 v[120:123], a[136:139], a[52:55], v[120:123]\n"
"ds_read_b64_tr_b16 a[192:193], v254 offset:0\n"
"v_mfma_f32_16x16x32_bf16 v[128:131], a[136:139], a[56:59], v[128:131]\n"
"ds_read_b64_tr_b16 a[156:157], v249 offset:12288\n"
"v_mfma_f32_16x16x32_bf16 v[136:139], a[136:139], a[60:63], v[136:139]\n"
"s_waitcnt lgkmcnt(4)\n"
"ds_read_b64_tr_b16 a[194:195], v254 offset:128\n"
"v_mfma_f32_16x16x32_bf16 v[116:119], a[140:143], a[48:51], v[116:119]\n"
"ds_read_b64_tr_b16 a[158:159], v249 offset:12800\n"
"v_mfma_f32_16x16x32_bf16 v[124:127], a[140:143], a[52:55], v[124:127]\n"
"buffer_load_dwordx4 a[32:35], v235, s[8:11], s56 offen offset:3072 sc1\n"
"v_mfma_f32_16x16x32_bf16 v[132:135], a[140:143], a[56:59], v[132:135]\n"
"s_add_u32 m0, s38, 114688\n"
"ds_read_b128 a[196:199], v251 offset:45056\n"
"s_add_u32 s48, s39, s98\n"
"v_mfma_f32_16x16x32_bf16 v[140:143], a[140:143], a[60:63], v[140:143]\n"
"ds_read_b64_tr_b16 a[160:161], v246 offset:32768\n"
"v_mfma_f32_16x16x32_bf16 v[144:147], a[144:147], a[48:51], v[144:147]\n"
"ds_read_b64_tr_b16 a[200:201], v254 offset:1024\n"
"v_mfma_f32_16x16x32_bf16 v[152:155], a[144:147], a[52:55], v[152:155]\n"
"ds_read_b64_tr_b16 a[162:163], v246 offset:33280\n"
"v_mfma_f32_16x16x32_bf16 v[160:163], a[144:147], a[56:59], v[160:163]\n"
"buffer_load_dwordx4 v238, s[20:23], s48 offen offset:0 lds\n"
"v_mfma_f32_16x16x32_bf16 v[168:171], a[144:147], a[60:63], v[168:171]\n"
"ds_read_b64_tr_b16 a[202:203], v254 offset:1152\n"
"v_mfma_f32_16x16x32_bf16 v[148:151], a[148:151], a[48:51], v[148:151]\n"
"ds_read_b64_tr_b16 a[164:165], v247 offset:32768\n"
"v_mfma_f32_16x16x32_bf16 v[156:159], a[148:151], a[52:55], v[156:159]\n"
"ds_read_b128 a[204:207], v251 offset:45568\n"
"v_mfma_f32_16x16x32_bf16 v[164:167], a[148:151], a[56:59], v[164:167]\n"
"s_add_u32 s49, s49, s88\n"
"ds_read_b64_tr_b16 a[166:167], v247 offset:33280\n"
"v_mfma_f32_16x16x32_bf16 v[172:175], a[148:151], a[60:63], v[172:175]\n"
"s_waitcnt lgkmcnt(5)\n"
"buffer_load_dwordx4 a[36:39], v235, s[8:11], s57 offen offset:3072 sc1\n"
"v_mfma_f32_16x16x32_bf16 v[176:179], a[152:155], a[48:51], v[176:179]\n"
"ds_read_b64_tr_b16 a[208:209], v254 offset:2048\n"
"v_mfma_f32_16x16x32_bf16 v[208:211], a[192:195], a[196:199], 0\n"
"ds_read_b64_tr_b16 a[168:169], v248 offset:32768\n"
"v_mfma_f32_16x16x32_bf16 v[184:187], a[152:155], a[52:55], v[184:187]\n"
"ds_read_b64_tr_b16 a[210:211], v254 offset:2176\n"
"v_mfma_f32_16x16x32_bf16 v[192:195], a[152:155], a[56:59], v[192:195]\n"
"ds_read_b64_tr_b16 a[170:171], v248 offset:33280\n"
"v_mfma_f32_16x16x32_bf16 v[200:203], a[152:155], a[60:63], v[200:203]\n"
"ds_read_b64_tr_b16 a[172:173], v249 offset:32768\n"
"v_mfma_f32_16x16x32_bf16 v[180:183], a[156:159], a[48:51], v[180:183]\n"
"buffer_store_dwordx4 v[12:15], v250, s[8:11], s49 offen offset:0 sc1\n"
"v_mfma_f32_16x16x32_bf16 v[188:191], a[156:159], a[52:55], v[188:191]\n"
"buffer_load_dwordx4 v239, s[20:23], s48 offen offset:1024 lds\n"
"v_mfma_f32_16x16x32_bf16 v[196:199], a[156:159], a[56:59], v[196:199]\n"
"s_waitcnt lgkmcnt(4)\n"
"ds_read_b128 a[212:215], v251 offset:46080\n"
"v_mfma_f32_16x16x32_bf16 v[208:211], a[200:203], a[204:207], v[208:211]\n"
"ds_read_b64_tr_b16 a[174:175], v249 offset:33280\n"
"v_mfma_f32_16x16x32_bf16 v[204:207], a[156:159], a[60:63], v[204:207]\n"
"s_waitcnt vmcnt(44)\n"
"buffer_load_dwordx4 a[40:43], v235, s[8:11], s58 offen offset:3072 sc1\n"
"v_mfma_f32_16x16x32_bf16 v[80:83], a[160:163], a[64:67], v[80:83]\n"
"s_waitcnt vmcnt(44)\n"
"ds_read_b64_tr_b16 a[216:217], v254 offset:3072\n"
"s_add_u32 s99, s99, 4096\n"
"v_mfma_f32_16x16x32_bf16 v[88:91], a[160:163], a[68:71], v[88:91]\n"
"s_waitcnt vmcnt(41)\n"
"ds_read_b64_tr_b16 a[176:177], v246 offset:36864\n"
"s_cmp_eq_u32 s99, 20480\n"
"v_mfma_f32_16x16x32_bf16 v[96:99], a[160:163], a[72:75], v[96:99]\n"
"s_waitcnt vmcnt(39)\n"
"ds_read_b64_tr_b16 a[218:219], v254 offset:3200\n"
"s_cselect_b32 s99, 0, s99\n"
"v_mfma_f32_16x16x32_bf16 v[104:107], a[160:163], a[76:79], v[104:107]\n"
"ds_read_b64_tr_b16 a[178:179], v246 offset:37376\n"
"v_mfma_f32_16x16x32_bf16 v[84:87], a[164:167], a[64:67], v[84:87]\n"
"s_waitcnt lgkmcnt(5)\n"
"ds_read_b128 a[220:223], v251 offset:46592\n"
"v_mfma_f32_16x16x32_bf16 v[208:211], a[208:211], a[212:215], v[208:211]\n"
"ds_read_b64_tr_b16 a[180:181], v247 offset:36864\n"
"v_mfma_f32_16x16x32_bf16 v[92:95], a[164:167], a[68:71], v[92:95]\n"
"buffer_load_dwordx4 v240, s[20:23], s48 offen offset:2048 lds\n"
"v_mfma_f32_16x16x32_bf16 v[100:103], a[164:167], a[72:75], v[100:103]\n"
"ds_read_b64_tr_b16 a[224:225], v254 offset:4096\n"
"v_mfma_f32_16x16x32_bf16 v[108:111], a[164:167], a[76:79], v[108:111]\n"
"buffer_load_dwordx4 a[44:47], v235, s[8:11], s59 offen offset:3072 sc1\n"
"v_mfma_f32_16x16x32_bf16 v[112:115], a[168:171], a[64:67], v[112:115]\n"
"s_add_u32 s98, s98, 524288\n"
"ds_read_b64_tr_b16 a[182:183], v247 offset:37376\n"
"v_add_u32 v235, s99, v224\n"
"v_mfma_f32_16x16x32_bf16 v[120:123], a[168:171], a[68:71], v[120:123]\n"
"s_cmp_eq_u32 s98, 5242880\n"
"ds_read_b64_tr_b16 a[226:227], v254 offset:4224\n"
"s_cselect_b32 s98, 0, s98\n"
"v_mfma_f32_16x16x32_bf16 v[128:131], a[168:171], a[72:75], v[128:131]\n"
"s_waitcnt lgkmcnt(4)\n"
"ds_read_b64_tr_b16 a[184:185], v248 offset:36864\n"
"v_mfma_f32_16x16x32_bf16 v[208:211], a[216:219], a[220:223], v[208:211]\n"
"ds_read_b128 a[228:231], v251 offset:47104\n"
"v_mfma_f32_16x16x32_bf16 v[136:139], a[168:171], a[76:79], v[136:139]\n"
"ds_read_b64_tr_b16 a[186:187], v248 offset:37376\n"
"v_mfma_f32_16x16x32_bf16 v[116:119], a[172:175], a[64:67], v[116:119]\n"
"ds_read_b64_tr_b16 a[232:233], v254 offset:5120\n"
"v_mfma_f32_16x16x32_bf16 v[124:127], a[172:175], a[68:71], v[124:127]\n"
"ds_read_b64_tr_b16 a[188:189], v249 offset:36864\n"
"v_mfma_f32_16x16x32_bf16 v[132:135], a[172:175], a[72:75], v[132:135]\n"
"buffer_load_dwordx4 a[48:51], v235, s[8:11], s56 offen offset:0 sc1\n"
"v_mfma_f32_16x16x32_bf16 v[140:143], a[172:175], a[76:79], v[140:143]\n"
"buffer_load_dwordx4 v241, s[20:23], s48 offen offset:3072 lds\n"
"v_mfma_f32_16x16x32_bf16 v[144:147], a[176:179], a[64:67], v[144:147]\n"
"s_waitcnt lgkmcnt(3)\n"
"ds_read_b64_tr_b16 a[234:235], v254 offset:5248\n"
"v_mfma_f32_16x16x32_bf16 v[208:211], a[224:227], a[228:231], v[208:211]\n"
"ds_read_b128 a[236:239], v251 offset:47616\n"
"v_mfma_f32_16x16x32_bf16 v[152:155], a[176:179], a[68:71], v[152:155]\n"
"ds_read_b64_tr_b16 a[190:191], v249 offset:37376\n"
"v_mfma_f32_16x16x32_bf16 v[160:163], a[176:179], a[72:75], v[160:163]\n"
"ds_read_b64_tr_b16 a[128:129], v242 offset:8192\n"
"v_mfma_f32_16x16x32_bf16 v[168:171], a[176:179], a[76:79], v[168:171]\n"
"ds_read_b64_tr_b16 a[240:241], v254 offset:6144\n"
"v_mfma_f32_16x16x32_bf16 v[148:151], a[180:183], a[64:67], v[148:151]\n"
"ds_read_b64_tr_b16 a[130:131], v242 offset:8704\n"
"v_mfma_f32_16x16x32_bf16 v[156:159], a[180:183], a[68:71], v[156:159]\n"
"ds_read_b64_tr_b16 a[242:243], v254 offset:6272\n"
"v_mfma_f32_16x16x32_bf16 v[164:167], a[180:183], a[72:75], v[164:167]\n"
"s_waitcnt lgkmcnt(5)\n"
"ds_read_b64_tr_b16 a[132:133], v243 offset:8192\n"
"v_mfma_f32_16x16x32_bf16 v[208:211], a[232:235], a[236:239], v[208:211]\n"
"ds_read_b128 a[244:247], v251 offset:48128\n"
"v_mfma_f32_16x16x32_bf16 v[172:175], a[180:183], a[76:79], v[172:175]\n"
"buffer_load_dwordx4 a[52:55], v235, s[8:11], s57 offen offset:0 sc1\n"
"v_mfma_f32_16x16x32_bf16 v[176:179], a[184:187], a[64:67], v[176:179]\n"
"ds_read_b64_tr_b16 a[134:135], v243 offset:8704\n"
"v_mfma_f32_16x16x32_bf16 v[184:187], a[184:187], a[68:71], v[184:187]\n"
"ds_read_b64_tr_b16 a[248:249], v254 offset:7168\n"
"v_mfma_f32_16x16x32_bf16 v[192:195], a[184:187], a[72:75], v[192:195]\n"
"ds_read_b64_tr_b16 a[136:137], v244 offset:8192\n"
"v_mfma_f32_16x16x32_bf16 v[200:203], a[184:187], a[76:79], v[200:203]\n"
"s_waitcnt lgkmcnt(5)\n"
"ds_read_b64_tr_b16 a[250:251], v254 offset:7296\n"
"v_mfma_f32_16x16x32_bf16 v[180:183], a[188:191], a[64:67], v[180:183]\n"
"s_waitcnt lgkmcnt(4)\n"
"ds_read_b64_tr_b16 a[138:139], v244 offset:8704\n"
"v_mfma_f32_16x16x32_bf16 v[208:211], a[240:243], a[244:247], v[208:211]\n"
"ds_read_b128 a[252:255], v251 offset:48640\n"
"v_mfma_f32_16x16x32_bf16 v[188:191], a[188:191], a[68:71], v[188:191]\n"
"buffer_load_dwordx4 a[56:59], v235, s[8:11], s58 offen offset:0 sc1\n"
"v_mfma_f32_16x16x32_bf16 v[196:199], a[188:191], a[72:75], v[196:199]\n"
"ds_read_b64_tr_b16 a[140:141], v245 offset:8192\n"
"v_mfma_f32_16x16x32_bf16 v[204:207], a[188:191], a[76:79], v[204:207]\n"
"s_waitcnt vmcnt(44)\n"
"ds_read_b64_tr_b16 a[192:193], v254 offset:8192\n"
"v_mfma_f32_16x16x32_bf16 v[80:83], a[128:131], a[80:83], v[80:83]\n"
"s_waitcnt vmcnt(42)\n"
"ds_read_b64_tr_b16 a[142:143], v245 offset:8704\n"
"v_mfma_f32_16x16x32_bf16 v[88:91], a[128:131], a[84:87], v[88:91]\n"
"s_waitcnt vmcnt(40)\n"
"ds_read_b64_tr_b16 a[194:195], v254 offset:8320\n"
"v_mfma_f32_16x16x32_bf16 v[96:99], a[128:131], a[88:91], v[96:99]\n"
"s_waitcnt lgkmcnt(4)\n"
"ds_read_b64_tr_b16 a[144:145], v242 offset:12288\n"
"v_mfma_f32_16x16x32_bf16 v[208:211], a[248:251], a[252:255], v[208:211]\n"
"s_waitcnt vmcnt(37)\n"
"ds_read_b128 a[196:199], v251 offset:49152\n"
"s_add_u32 s49, s49, 1024\n"
"v_mfma_f32_16x16x32_bf16 v[104:107], a[128:131], a[92:95], v[104:107]\n"
"ds_read_b64_tr_b16 a[146:147], v242 offset:12800\n"
"v_mfma_f32_16x16x32_bf16 v[84:87], a[132:135], a[80:83], v[84:87]\n"
"buffer_load_dwordx4 a[60:63], v235, s[8:11], s59 offen offset:0 sc1\n"
"v_mfma_f32_16x16x32_bf16 v[92:95], a[132:135], a[84:87], v[92:95]\n"
"ds_read_b64_tr_b16 a[200:201], v254 offset:9216\n"
"v_mfma_f32_16x16x32_bf16 v[100:103], a[132:135], a[88:91], v[100:103]\n"
"ds_read_b64_tr_b16 a[148:149], v243 offset:12288\n"
"v_mfma_f32_16x16x32_bf16 v[108:111], a[132:135], a[92:95], v[108:111]\n"
"ds_read_b64_tr_b16 a[202:203], v254 offset:9344\n"
"v_mfma_f32_16x16x32_bf16 v[112:115], a[136:139], a[80:83], v[112:115]\n"
"s_waitcnt lgkmcnt(4)\n"
"ds_read_b64_tr_b16 a[150:151], v243 offset:12800\n"
"v_mfma_f32_16x16x32_bf16 v[208:211], a[192:195], a[196:199], v[208:211]\n"
"ds_read_b128 a[204:207], v251 offset:49664\n"
"v_mfma_f32_16x16x32_bf16 v[120:123], a[136:139], a[84:87], v[120:123]\n"
"ds_read_b64_tr_b16 a[152:153], v244 offset:12288\n"
"v_mfma_f32_16x16x32_bf16 v[128:131], a[136:139], a[88:91], v[128:131]\n"
"ds_read_b64_tr_b16 a[154:155], v244 offset:12800\n"
"v_mfma_f32_16x16x32_bf16 v[136:139], a[136:139], a[92:95], v[136:139]\n"
"buffer_store_dwordx4 v[28:31], v250, s[8:11], s49 offen offset:0 sc1\n"
"v_mfma_f32_16x16x32_bf16 v[116:119], a[140:143], a[80:83], v[116:119]\n"
"s_lshl_b32 s92, s30, 10\n"
"buffer_load_dwordx4 a[64:67], v235, s[8:11], s56 offen offset:1024 sc1\n"
"v_mfma_f32_16x16x32_bf16 v[124:127], a[140:143], a[84:87], v[124:127]\n"
"ds_read_b64_tr_b16 a[156:157], v245 offset:12288\n"
"v_mfma_f32_16x16x32_bf16 v[132:135], a[140:143], a[88:91], v[132:135]\n"
"s_waitcnt lgkmcnt(3)\n"
"ds_read_b64_tr_b16 a[158:159], v245 offset:12800\n"
"v_mfma_f32_16x16x32_bf16 v[208:211], a[200:203], a[204:207], v[208:211]\n"
"ds_read_b64_tr_b16 a[160:161], v242 offset:32768\n"
"v_add_u32 v252, s92, v224\n"
"v_mfma_f32_16x16x32_bf16 v[140:143], a[140:143], a[92:95], v[140:143]\n"
"buffer_load_dwordx4 a[68:71], v235, s[8:11], s57 offen offset:1024 sc1\n"
"v_mfma_f32_16x16x32_bf16 v[144:147], a[144:147], a[80:83], v[144:147]\n"
"ds_read_b64_tr_b16 a[162:163], v242 offset:33280\n"
"v_mfma_f32_16x16x32_bf16 v[152:155], a[144:147], a[84:87], v[152:155]\n"
"ds_read_b64_tr_b16 a[164:165], v243 offset:32768\n"
"v_mfma_f32_16x16x32_bf16 v[160:163], a[144:147], a[88:91], v[160:163]\n"
"ds_read_b64_tr_b16 a[166:167], v243 offset:33280\n"
"v_mfma_f32_16x16x32_bf16 v[168:171], a[144:147], a[92:95], v[168:171]\n"
"ds_read_b64_tr_b16 a[168:169], v244 offset:32768\n"
"v_mfma_f32_16x16x32_bf16 v[148:151], a[148:151], a[80:83], v[148:151]\n"
"buffer_load_dwordx4 a[72:75], v235, s[8:11], s58 offen offset:1024 sc1\n"
"v_mfma_f32_16x16x32_bf16 v[156:159], a[148:151], a[84:87], v[156:159]\n"
"ds_read_b64_tr_b16 a[170:171], v244 offset:33280\n"
"v_mfma_f32_16x16x32_bf16 v[164:167], a[148:151], a[88:91], v[164:167]\n"
"ds_write_b128 v252, v[208:211] offset:57344\n"
"v_mfma_f32_16x16x32_bf16 v[172:175], a[148:151], a[92:95], v[172:175]\n"
"s_waitcnt lgkmcnt(5)\n"
"ds_read_b64_tr_b16 a[172:173], v245 offset:32768\n"
"v_mfma_f32_16x16x32_bf16 v[176:179], a[152:155], a[80:83], v[176:179]\n"
"ds_read_b64_tr_b16 a[174:175], v245 offset:33280\n"
"v_mfma_f32_16x16x32_bf16 v[184:187], a[152:155], a[84:87], v[184:187]\n"
"ds_read_b64_tr_b16 a[176:177], v242 offset:36864\n"
"v_mfma_f32_16x16x32_bf16 v[192:195], a[152:155], a[88:91], v[192:195]\n"
"buffer_load_dwordx4 a[76:79], v235, s[8:11], s59 offen offset:1024 sc1\n"
"v_mfma_f32_16x16x32_bf16 v[200:203], a[152:155], a[92:95], v[200:203]\n"
"ds_read_b64_tr_b16 a[178:179], v242 offset:37376\n"
"v_mfma_f32_16x16x32_bf16 v[180:183], a[156:159], a[80:83], v[180:183]\n"
"v_mfma_f32_16x16x32_bf16 v[188:191], a[156:159], a[84:87], v[188:191]\n"
"ds_read_b64_tr_b16 a[180:181], v243 offset:36864\n"
"v_mfma_f32_16x16x32_bf16 v[196:199], a[156:159], a[88:91], v[196:199]\n"
"v_mfma_f32_16x16x32_bf16 v[204:207], a[156:159], a[92:95], v[204:207]\n"
"s_waitcnt vmcnt(41)\n"
"ds_read_b64_tr_b16 a[182:183], v243 offset:37376\n"
"v_mfma_f32_16x16x32_bf16 v[80:83], a[160:163], a[96:99], v[80:83]\n"
"s_waitcnt vmcnt(39)\n"
"buffer_load_dwordx4 a[80:83], v235, s[8:11], s56 offen offset:2048 sc1\n"
"v_mfma_f32_16x16x32_bf16 v[88:91], a[160:163], a[100:103], v[88:91]\n"
"s_waitcnt vmcnt(38)\n"
"ds_read_b64_tr_b16 a[184:185], v244 offset:36864\n"
"v_mfma_f32_16x16x32_bf16 v[96:99], a[160:163], a[104:107], v[96:99]\n"
"s_waitcnt vmcnt(37)\n"
"v_mfma_f32_16x16x32_bf16 v[104:107], a[160:163], a[108:111], v[104:107]\n"
"s_waitcnt lgkmcnt(3)\n"
"ds_read_b64_tr_b16 a[186:187], v244 offset:37376\n"
"v_mfma_f32_16x16x32_bf16 v[84:87], a[164:167], a[96:99], v[84:87]\n"
"v_mfma_f32_16x16x32_bf16 v[92:95], a[164:167], a[100:103], v[92:95]\n"
"ds_read_b64_tr_b16 a[188:189], v245 offset:36864\n"
"v_mfma_f32_16x16x32_bf16 v[100:103], a[164:167], a[104:107], v[100:103]\n"
"v_mfma_f32_16x16x32_bf16 v[108:111], a[164:167], a[108:111], v[108:111]\n"
"ds_read_b64_tr_b16 a[190:191], v245 offset:37376\n"
"v_mfma_f32_16x16x32_bf16 v[112:115], a[168:171], a[96:99], v[112:115]\n"
"buffer_load_dwordx4 a[84:87], v235, s[8:11], s57 offen offset:2048 sc1\n"
"v_mfma_f32_16x16x32_bf16 v[120:123], a[168:171], a[100:103], v[120:123]\n"
"v_mfma_f32_16x16x32_bf16 v[128:131], a[168:171], a[104:107], v[128:131]\n"
"v_mfma_f32_16x16x32_bf16 v[136:139], a[168:171], a[108:111], v[136:139]\n"
"v_mfma_f32_16x16x32_bf16 v[116:119], a[172:175], a[96:99], v[116:119]\n"
"v_mfma_f32_16x16x32_bf16 v[124:127], a[172:175], a[100:103], v[124:127]\n"
"s_waitcnt lgkmcnt(0)\n"
"s_barrier\n"
"v_mfma_f32_16x16x32_bf16 v[132:135], a[172:175], a[104:107], v[132:135]\n"
"s_add_u32 s49, s49, s89\n"
"ds_read_b64_tr_b16 a[128:129], v246 offset:16384\n"
"s_add_u32 m0, s38, 73728\n"
"v_mfma_f32_16x16x32_bf16 v[140:143], a[172:175], a[108:111], v[140:143]\n"
"s_add_u32 s48, s39, s98\n"
"buffer_load_dwordx4 a[88:91], v235, s[8:11], s58 offen offset:2048 sc1\n"
"v_mfma_f32_16x16x32_bf16 v[144:147], a[176:179], a[96:99], v[144:147]\n"
"ds_read_b128 v[208:211], v224 offset:57344\n"
"v_mfma_f32_16x16x32_bf16 v[152:155], a[176:179], a[100:103], v[152:155]\n"
"ds_read_b64_tr_b16 a[130:131], v246 offset:16896\n"
"v_mfma_f32_16x16x32_bf16 v[160:163], a[176:179], a[104:107], v[160:163]\n"
"buffer_store_dwordx4 v[44:47], v250, s[8:11], s49 offen offset:0 sc1\n"
"v_mfma_f32_16x16x32_bf16 v[168:171], a[176:179], a[108:111], v[168:171]\n"
"buffer_load_dwordx4 v238, s[20:23], s48 offen offset:0 lds\n"
"v_mfma_f32_16x16x32_bf16 v[148:151], a[180:183], a[96:99], v[148:151]\n"
"ds_read_b128 v[212:215], v224 offset:58368\n"
"v_mfma_f32_16x16x32_bf16 v[156:159], a[180:183], a[100:103], v[156:159]\n"
"ds_read_b64_tr_b16 a[132:133], v247 offset:16384\n"
"v_mfma_f32_16x16x32_bf16 v[164:167], a[180:183], a[104:107], v[164:167]\n"
"ds_read_b128 v[216:219], v224 offset:59392\n"
"v_mfma_f32_16x16x32_bf16 v[172:175], a[180:183], a[108:111], v[172:175]\n"
"ds_read_b64_tr_b16 a[134:135], v247 offset:16896\n"
"v_mfma_f32_16x16x32_bf16 v[176:179], a[184:187], a[96:99], v[176:179]\n"
"buffer_load_dwordx4 a[92:95], v235, s[8:11], s59 offen offset:2048 sc1\n"
"v_mfma_f32_16x16x32_bf16 v[184:187], a[184:187], a[100:103], v[184:187]\n"
"ds_read_b128 v[220:223], v224 offset:60416\n"
"v_mfma_f32_16x16x32_bf16 v[192:195], a[184:187], a[104:107], v[192:195]\n"
"ds_read_b64_tr_b16 a[136:137], v248 offset:16384\n"
"v_mfma_f32_16x16x32_bf16 v[200:203], a[184:187], a[108:111], v[200:203]\n"
"ds_read_b64_tr_b16 a[138:139], v248 offset:16896\n"
"v_mfma_f32_16x16x32_bf16 v[180:183], a[188:191], a[96:99], v[180:183]\n"
"buffer_load_dwordx4 v239, s[20:23], s48 offen offset:1024 lds\n"
"v_mfma_f32_16x16x32_bf16 v[188:191], a[188:191], a[100:103], v[188:191]\n"
"ds_read_b64_tr_b16 a[140:141], v249 offset:16384\n"
"v_mfma_f32_16x16x32_bf16 v[196:199], a[188:191], a[104:107], v[196:199]\n"
"ds_read_b64_tr_b16 a[142:143], v249 offset:16896\n"
"v_mfma_f32_16x16x32_bf16 v[204:207], a[188:191], a[108:111], v[204:207]\n"
"s_waitcnt vmcnt(42) lgkmcnt(5)\n"
"ds_read_b64_tr_b16 a[144:145], v246 offset:20480\n"
"v_add_f32 v208, v208, v212\n"
"v_mfma_f32_16x16x32_bf16 v[80:83], a[128:131], a[112:115], v[80:83]\n"
"s_waitcnt vmcnt(41)\n"
"ds_read_b64_tr_b16 a[146:147], v246 offset:20992\n"
"v_add_f32 v209, v209, v213\n"
"v_mfma_f32_16x16x32_bf16 v[88:91], a[128:131], a[116:119], v[88:91]\n"
"s_waitcnt vmcnt(39)\n"
"buffer_load_dwordx4 a[96:99], v235, s[8:11], s56 offen offset:3072 sc1\n"
"v_mfma_f32_16x16x32_bf16 v[96:99], a[128:131], a[120:123], v[96:99]\n"
"s_waitcnt vmcnt(39)\n"
"buffer_load_dwordx4 v240, s[20:23], s48 offen offset:2048 lds\n"
"v_mfma_f32_16x16x32_bf16 v[104:107], a[128:131], a[124:127], v[104:107]\n"
"ds_read_b64_tr_b16 a[148:149], v247 offset:20480\n"
"v_add_f32 v210, v210, v214\n"
"v_mfma_f32_16x16x32_bf16 v[84:87], a[132:135], a[112:115], v[84:87]\n"
"ds_read_b64_tr_b16 a[150:151], v247 offset:20992\n"
"v_add_f32 v211, v211, v215\n"
"v_mfma_f32_16x16x32_bf16 v[92:95], a[132:135], a[116:119], v[92:95]\n"
"ds_read_b64_tr_b16 a[152:153], v248 offset:20480\n"
"v_cvt_pk_bf16_f32 v208, v208, v208\n"
"v_mfma_f32_16x16x32_bf16 v[100:103], a[132:135], a[120:123], v[100:103]\n"
"s_waitcnt lgkmcnt(9)\n"
"v_add_f32 v216, v216, v220\n"
"v_add_f32 v217, v217, v221\n"
"v_mfma_f32_16x16x32_bf16 v[108:111], a[132:135], a[124:127], v[108:111]\n"
"s_waitcnt lgkmcnt(3)\n"
"ds_read_b64_tr_b16 a[154:155], v248 offset:20992\n"
"v_add_f32 v218, v218, v222\n"
"v_mfma_f32_16x16x32_bf16 v[112:115], a[136:139], a[112:115], v[112:115]\n"
"s_add_u32 s98, s98, 524288\n"
"buffer_load_dwordx4 a[100:103], v235, s[8:11], s57 offen offset:3072 sc1\n"
"v_mfma_f32_16x16x32_bf16 v[120:123], a[136:139], a[116:119], v[120:123]\n"
"buffer_load_dwordx4 v241, s[20:23], s48 offen offset:3072 lds\n"
"v_mfma_f32_16x16x32_bf16 v[128:131], a[136:139], a[120:123], v[128:131]\n"
"ds_read_b64_tr_b16 a[156:157], v249 offset:20480\n"
"v_add_f32 v219, v219, v223\n"
"v_mfma_f32_16x16x32_bf16 v[136:139], a[136:139], a[124:127], v[136:139]\n"
"ds_read_b64_tr_b16 a[158:159], v249 offset:20992\n"
"v_lshlrev_b32 v208, 16, v208\n"
"v_mfma_f32_16x16x32_bf16 v[116:119], a[140:143], a[112:115], v[116:119]\n"
"v_cvt_pk_bf16_f32 v209, v209, v209\n"
"v_lshlrev_b32 v209, 16, v209\n"
"v_mfma_f32_16x16x32_bf16 v[124:127], a[140:143], a[116:119], v[124:127]\n"
"ds_read_b64_tr_b16 a[160:161], v246 offset:40960\n"
"v_cvt_pk_bf16_f32 v210, v210, v210\n"
"v_mfma_f32_16x16x32_bf16 v[132:135], a[140:143], a[120:123], v[132:135]\n"
"s_add_u32 m0, s38, 122880\n"
"buffer_load_dwordx4 a[104:107], v235, s[8:11], s58 offen offset:3072 sc1\n"
"v_mfma_f32_16x16x32_bf16 v[140:143], a[140:143], a[124:127], v[140:143]\n"
"s_add_u32 s48, s39, s98\n"
"ds_read_b64_tr_b16 a[162:163], v246 offset:41472\n"
"v_lshlrev_b32 v210, 16, v210\n"
"v_mfma_f32_16x16x32_bf16 v[144:147], a[144:147], a[112:115], v[144:147]\n"
"buffer_load_dwordx4 v238, s[20:23], s48 offen offset:0 lds\n"
"v_mfma_f32_16x16x32_bf16 v[152:155], a[144:147], a[116:119], v[152:155]\n"
"ds_read_b64_tr_b16 a[164:165], v247 offset:40960\n"
"v_cvt_pk_bf16_f32 v211, v211, v211\n"
"v_mfma_f32_16x16x32_bf16 v[160:163], a[144:147], a[120:123], v[160:163]\n"
"v_lshlrev_b32 v211, 16, v211\n"
"v_mul_f32 v64, 0xbfb8aa3b, v208\n"
"v_mfma_f32_16x16x32_bf16 v[168:171], a[144:147], a[124:127], v[168:171]\n"
"s_waitcnt lgkmcnt(3)\n"
"v_exp_f32 v64, v64\n"
"v_mfma_f32_16x16x32_bf16 v[148:151], a[148:151], a[112:115], v[148:151]\n"
"s_add_u32 s99, s99, 4096\n"
"ds_read_b64_tr_b16 a[166:167], v247 offset:41472\n"
"v_mul_f32 v65, 0xbfb8aa3b, v209\n"
"v_mfma_f32_16x16x32_bf16 v[156:159], a[148:151], a[116:119], v[156:159]\n"
"s_cmp_eq_u32 s99, 20480\n"
"v_exp_f32 v65, v65\n"
"v_mfma_f32_16x16x32_bf16 v[164:167], a[148:151], a[120:123], v[164:167]\n"
"s_cselect_b32 s99, 0, s99\n"
"ds_read_b64_tr_b16 a[168:169], v248 offset:40960\n"
"v_mul_f32 v66, 0xbfb8aa3b, v210\n"
"v_mfma_f32_16x16x32_bf16 v[172:175], a[148:151], a[124:127], v[172:175]\n"
"buffer_load_dwordx4 a[108:111], v235, s[8:11], s59 offen offset:3072 sc1\n"
"v_mfma_f32_16x16x32_bf16 v[176:179], a[152:155], a[112:115], v[176:179]\n"
"ds_read_b64_tr_b16 a[170:171], v248 offset:41472\n"
"v_mul_f32 v67, 0xbfb8aa3b, v211\n"
"v_mfma_f32_16x16x32_bf16 v[184:187], a[152:155], a[116:119], v[184:187]\n"
"v_exp_f32 v66, v66\n"
"v_mfma_f32_16x16x32_bf16 v[192:195], a[152:155], a[120:123], v[192:195]\n"
"s_add_u32 s49, s49, 1024\n"
"ds_read_b64_tr_b16 a[172:173], v249 offset:40960\n"
"ds_read_b64_tr_b16 a[174:175], v249 offset:41472\n"
"v_mfma_f32_16x16x32_bf16 v[200:203], a[152:155], a[124:127], v[200:203]\n"
"buffer_load_dwordx4 v239, s[20:23], s48 offen offset:1024 lds\n"
"v_mfma_f32_16x16x32_bf16 v[180:183], a[156:159], a[112:115], v[180:183]\n"
"v_exp_f32 v67, v67\n"
"v_mfma_f32_16x16x32_bf16 v[188:191], a[156:159], a[116:119], v[188:191]\n"
"ds_read_b64_tr_b16 a[176:177], v246 offset:45056\n"
"v_add_u32 v235, s99, v224\n"
"v_mfma_f32_16x16x32_bf16 v[196:199], a[156:159], a[120:123], v[196:199]\n"
"buffer_store_dwordx4 v[60:63], v250, s[8:11], s49 offen offset:0 sc1\n"
"v_mfma_f32_16x16x32_bf16 v[204:207], a[156:159], a[124:127], v[204:207]\n"
"s_waitcnt vmcnt(46) lgkmcnt(3)\n"
"buffer_load_dwordx4 a[192:195], v235, s[8:11], s56 offen offset:0 sc1\n"
"v_mfma_f32_16x16x32_bf16 v[80:83], a[160:163], a[0:3], v[80:83]\n"
"s_waitcnt vmcnt(46)\n"
"ds_read_b64_tr_b16 a[178:179], v246 offset:45568\n"
"v_cvt_pk_bf16_f32 v216, v216, v216\n"
"v_mfma_f32_16x16x32_bf16 v[88:91], a[160:163], a[4:7], v[88:91]\n"
"s_waitcnt vmcnt(45)\n"
"ds_read_b64_tr_b16 a[180:181], v247 offset:45056\n"
"v_lshlrev_b32 v216, 16, v216\n"
"v_mfma_f32_16x16x32_bf16 v[96:99], a[160:163], a[8:11], v[96:99]\n"
"s_waitcnt vmcnt(44)\n"
"buffer_load_dwordx4 v240, s[20:23], s48 offen offset:2048 lds\n"
"v_mfma_f32_16x16x32_bf16 v[104:107], a[160:163], a[12:15], v[104:107]\n"
"ds_read_b64_tr_b16 a[182:183], v247 offset:45568\n"
"v_cvt_pk_bf16_f32 v217, v217, v217\n"
"v_mfma_f32_16x16x32_bf16 v[84:87], a[164:167], a[0:3], v[84:87]\n"
"buffer_load_dwordx4 a[196:199], v235, s[8:11], s57 offen offset:0 sc1\n"
"v_mfma_f32_16x16x32_bf16 v[92:95], a[164:167], a[4:7], v[92:95]\n"
"ds_read_b64_tr_b16 a[184:185], v248 offset:45056\n"
"v_lshlrev_b32 v217, 16, v217\n"
"v_mfma_f32_16x16x32_bf16 v[100:103], a[164:167], a[8:11], v[100:103]\n"
"ds_read_b64_tr_b16 a[186:187], v248 offset:45568\n"
"v_cvt_pk_bf16_f32 v218, v218, v218\n"
"v_mfma_f32_16x16x32_bf16 v[108:111], a[164:167], a[12:15], v[108:111]\n"
"v_lshlrev_b32 v218, 16, v218\n"
"v_cvt_pk_bf16_f32 v219, v219, v219\n"
"v_mfma_f32_16x16x32_bf16 v[112:115], a[168:171], a[0:3], v[112:115]\n"
"s_add_u32 s98, s98, 524288\n"
"ds_read_b64_tr_b16 a[188:189], v249 offset:45056\n"
"v_lshlrev_b32 v219, 16, v219\n"
"v_mfma_f32_16x16x32_bf16 v[120:123], a[168:171], a[4:7], v[120:123]\n"
"s_cmp_eq_u32 s98, 5242880\n"
"buffer_load_dwordx4 v241, s[20:23], s48 offen offset:3072 lds\n"
"v_mfma_f32_16x16x32_bf16 v[128:131], a[168:171], a[8:11], v[128:131]\n"
"s_cselect_b32 s98, 0, s98\n"
"ds_read_b64_tr_b16 a[190:191], v249 offset:45568\n"
"v_add_f32 v64, 1.0, v64\n"
"v_mfma_f32_16x16x32_bf16 v[136:139], a[168:171], a[12:15], v[136:139]\n"
"s_waitcnt lgkmcnt(4)\n"
"v_rcp_f32 v64, v64\n"
"v_mfma_f32_16x16x32_bf16 v[116:119], a[172:175], a[0:3], v[116:119]\n"
"buffer_load_dwordx4 a[200:203], v235, s[8:11], s58 offen offset:0 sc1\n"
"v_mfma_f32_16x16x32_bf16 v[124:127], a[172:175], a[4:7], v[124:127]\n"
"ds_read_b64_tr_b16 a[128:129], v242 offset:16384\n"
"v_add_f32 v65, 1.0, v65\n"
"v_mfma_f32_16x16x32_bf16 v[132:135], a[172:175], a[8:11], v[132:135]\n"
"v_rcp_f32 v65, v65\n"
"v_mfma_f32_16x16x32_bf16 v[140:143], a[172:175], a[12:15], v[140:143]\n"
"ds_read_b64_tr_b16 a[130:131], v242 offset:16896\n"
"ds_read_b64_tr_b16 a[132:133], v243 offset:16384\n"
"v_mfma_f32_16x16x32_bf16 v[144:147], a[176:179], a[0:3], v[144:147]\n"
"s_lshl_b32 s39, s35, 20\n"
"v_add_f32 v66, 1.0, v66\n"
"v_add_f32 v67, 1.0, v67\n"
"v_mfma_f32_16x16x32_bf16 v[152:155], a[176:179], a[4:7], v[152:155]\n"
"s_add_u32 s39, s39, 10485760\n"
"v_rcp_f32 v66, v66\n"
"v_mfma_f32_16x16x32_bf16 v[160:163], a[176:179], a[8:11], v[160:163]\n"
"v_rcp_f32 v67, v67\n"
"v_mfma_f32_16x16x32_bf16 v[168:171], a[176:179], a[12:15], v[168:171]\n"
"ds_read_b64_tr_b16 a[134:135], v243 offset:16896\n"
"ds_read_b64_tr_b16 a[136:137], v244 offset:16384\n"
"v_mfma_f32_16x16x32_bf16 v[148:151], a[180:183], a[0:3], v[148:151]\n"
"buffer_load_dwordx4 a[204:207], v235, s[8:11], s59 offen offset:0 sc1\n"
"v_mfma_f32_16x16x32_bf16 v[156:159], a[180:183], a[4:7], v[156:159]\n"
"ds_read_b64_tr_b16 a[138:139], v244 offset:16896\n"
"v_mul_f32 v208, v208, v64\n"
"v_mfma_f32_16x16x32_bf16 v[164:167], a[180:183], a[8:11], v[164:167]\n"
"v_mul_f32 v209, v209, v65\n"
"v_mul_f32 v210, v210, v66\n"
"v_mfma_f32_16x16x32_bf16 v[172:175], a[180:183], a[12:15], v[172:175]\n"
"s_waitcnt lgkmcnt(3)\n"
"ds_read_b64_tr_b16 a[140:141], v245 offset:16384\n"
"v_mul_f32 v211, v211, v67\n"
"v_mfma_f32_16x16x32_bf16 v[176:179], a[184:187], a[0:3], v[176:179]\n"
"buffer_load_dwordx4 a[208:211], v235, s[8:11], s56 offen offset:1024 sc1\n"
"v_mfma_f32_16x16x32_bf16 v[184:187], a[184:187], a[4:7], v[184:187]\n"
"ds_read_b64_tr_b16 a[142:143], v245 offset:16896\n"
"v_cvt_pk_bf16_f32 v208, v208, v208\n"
"v_mfma_f32_16x16x32_bf16 v[192:195], a[184:187], a[8:11], v[192:195]\n"
"v_lshlrev_b32 v208, 16, v208\n"
"v_cvt_pk_bf16_f32 v209, v209, v209\n"
"v_mfma_f32_16x16x32_bf16 v[200:203], a[184:187], a[12:15], v[200:203]\n"
"ds_read_b64_tr_b16 a[144:145], v242 offset:20480\n"
"v_lshlrev_b32 v209, 16, v209\n"
"v_mfma_f32_16x16x32_bf16 v[180:183], a[188:191], a[0:3], v[180:183]\n"
"v_cvt_pk_bf16_f32 v210, v210, v210\n"
"v_lshlrev_b32 v210, 16, v210\n"
"v_mfma_f32_16x16x32_bf16 v[188:191], a[188:191], a[4:7], v[188:191]\n"
"ds_read_b64_tr_b16 a[146:147], v242 offset:20992\n"
"v_cvt_pk_bf16_f32 v211, v211, v211\n"
"v_mfma_f32_16x16x32_bf16 v[196:199], a[188:191], a[8:11], v[196:199]\n"
"v_lshlrev_b32 v211, 16, v211\n"
"v_add_u32 v225, s36, v224\n"
"v_mfma_f32_16x16x32_bf16 v[204:207], a[188:191], a[12:15], v[204:207]\n"
"s_waitcnt vmcnt(48)\n"
"ds_read_b64_tr_b16 a[148:149], v243 offset:20480\n"
"v_mul_f32 v208, v208, v216\n"
"v_mfma_f32_16x16x32_bf16 v[80:83], a[128:131], a[16:19], v[80:83]\n"
"s_waitcnt vmcnt(46)\n"
"buffer_load_dwordx4 a[212:215], v235, s[8:11], s57 offen offset:1024 sc1\n"
"v_mfma_f32_16x16x32_bf16 v[88:91], a[128:131], a[20:23], v[88:91]\n"
"s_waitcnt vmcnt(45)\n"
"ds_read_b64_tr_b16 a[150:151], v243 offset:20992\n"
"v_mul_f32 v209, v209, v217\n"
"v_mfma_f32_16x16x32_bf16 v[96:99], a[128:131], a[24:27], v[96:99]\n"
"s_waitcnt vmcnt(43)\n"
"ds_read_b64_tr_b16 a[152:153], v244 offset:20480\n"
"v_mul_f32 v210, v210, v218\n"
"v_mfma_f32_16x16x32_bf16 v[104:107], a[128:131], a[28:31], v[104:107]\n"
"s_waitcnt lgkmcnt(4)\n"
"v_mul_f32 v211, v211, v219\n"
"v_cvt_pk_bf16_f32 v68, v208, v209\n"
"v_mfma_f32_16x16x32_bf16 v[84:87], a[132:135], a[16:19], v[84:87]\n"
"s_lshl_b32 s50, s35, 6\n"
"ds_read_b64_tr_b16 a[154:155], v244 offset:20992\n"
"v_cvt_pk_bf16_f32 v69, v210, v211\n"
"v_mfma_f32_16x16x32_bf16 v[92:95], a[132:135], a[20:23], v[92:95]\n"
"s_add_u32 s50, s50, 2304\n"
"v_lshrrev_b32 v251, 1, v224\n"
"v_and_b32 v251, 0x180, v251\n"
"v_mfma_f32_16x16x32_bf16 v[100:103], a[132:135], a[24:27], v[100:103]\n"
"s_lshl_b32 s94, s34, 1\n"
"ds_read_b64_tr_b16 a[156:157], v245 offset:20480\n"
"v_and_b32 v252, 0x70, v224\n"
"v_mfma_f32_16x16x32_bf16 v[108:111], a[132:135], a[28:31], v[108:111]\n"
"s_add_u32 s95, s94, 2\n"
"buffer_load_dwordx4 a[216:219], v235, s[8:11], s58 offen offset:1024 sc1\n"
"v_mfma_f32_16x16x32_bf16 v[112:115], a[136:139], a[16:19], v[112:115]\n"
"s_sub_u32 s94, 32, s94\n"
"ds_read_b64_tr_b16 a[158:159], v245 offset:20992\n"
"v_add_u32 v251, v251, v252\n"
"v_mfma_f32_16x16x32_bf16 v[120:123], a[136:139], a[20:23], v[120:123]\n"
"s_add_u32 s49, s49, s88\n"
"v_and_b32 v252, 0x80, v224\n"
"v_lshl_add_u32 v251, v252, 23, v251\n"
"v_mfma_f32_16x16x32_bf16 v[128:131], a[136:139], a[24:27], v[128:131]\n"
"buffer_store_dwordx2 v[68:69], v251, s[8:11], s60 offen sc1\n"
"v_mfma_f32_16x16x32_bf16 v[136:139], a[136:139], a[28:31], v[136:139]\n"
"buffer_store_dwordx4 v[0:3], v250, s[8:11], s49 offen offset:0 sc1\n"
"v_mfma_f32_16x16x32_bf16 v[116:119], a[140:143], a[16:19], v[116:119]\n"
"ds_read_b64_tr_b16 a[160:161], v242 offset:40960\n"
"v_lshrrev_b32 v252, 1, v224\n"
"v_mfma_f32_16x16x32_bf16 v[124:127], a[140:143], a[20:23], v[124:127]\n"
"ds_read_b64_tr_b16 a[162:163], v242 offset:41472\n"
"v_and_b32 v252, 56, v252\n"
"v_mfma_f32_16x16x32_bf16 v[132:135], a[140:143], a[24:27], v[132:135]\n"
"buffer_load_dwordx2 v[72:73], v252, s[8:11], s50 offen sc1\n"
"v_mfma_f32_16x16x32_bf16 v[140:143], a[140:143], a[28:31], v[140:143]\n"
"s_waitcnt lgkmcnt(2)\n"
"buffer_load_dwordx4 a[220:223], v235, s[8:11], s59 offen offset:1024 sc1\n"
"v_mfma_f32_16x16x32_bf16 v[144:147], a[144:147], a[16:19], v[144:147]\n"
"ds_read_b64_tr_b16 a[164:165], v243 offset:40960\n"
"v_lshlrev_b32 v254, 10, v224\n"
"v_mfma_f32_16x16x32_bf16 v[152:155], a[144:147], a[20:23], v[152:155]\n"
"ds_read_b64_tr_b16 a[166:167], v243 offset:41472\n"
"v_lshrrev_b32 v255, 4, v224\n"
"v_mfma_f32_16x16x32_bf16 v[160:163], a[144:147], a[24:27], v[160:163]\n"
"ds_read_b64_tr_b16 a[168:169], v244 offset:40960\n"
"v_add_u32 v228, s39, v254\n"
"v_mfma_f32_16x16x32_bf16 v[168:171], a[144:147], a[28:31], v[168:171]\n"
"v_and_b32 v254, 1, v255\n"
"v_mul_u32_u24 v254, 491520, v254\n"
"v_mfma_f32_16x16x32_bf16 v[148:151], a[148:151], a[16:19], v[148:151]\n"
"ds_read_b64_tr_b16 a[170:171], v244 offset:41472\n"
"v_cmp_gt_u32 vcc, s95, v255\n"
"v_mfma_f32_16x16x32_bf16 v[156:159], a[148:151], a[20:23], v[156:159]\n"
"s_mul_i32 s95, s35, 163840\n"
"buffer_load_dwordx4 a[224:227], v235, s[8:11], s56 offen offset:2048 sc1\n"
"v_mfma_f32_16x16x32_bf16 v[164:167], a[148:151], a[24:27], v[164:167]\n"
"s_add_u32 s95, s95, 4923392\n"
"ds_read_b64_tr_b16 a[172:173], v245 offset:40960\n"
"v_mov_b32 v229, 16384\n"
"v_mfma_f32_16x16x32_bf16 v[172:175], a[148:151], a[28:31], v[172:175]\n"
"ds_read_b64_tr_b16 a[174:175], v245 offset:41472\n"
"v_mov_b32 v253, 516096\n"
"v_mfma_f32_16x16x32_bf16 v[176:179], a[152:155], a[16:19], v[176:179]\n"
"v_cndmask_b32_e64 v254, v254, 0, vcc\n"
"v_cndmask_b32 v229, v229, v253, vcc\n"
"v_mfma_f32_16x16x32_bf16 v[184:187], a[152:155], a[20:23], v[184:187]\n"
"s_mov_b32 s47, s36\n"
"ds_read_b64_tr_b16 a[176:177], v242 offset:45056\n"
"v_cmp_eq_u32 vcc, s94, v255\n"
"v_mfma_f32_16x16x32_bf16 v[192:195], a[152:155], a[24:27], v[192:195]\n"
"s_add_u32 s46, s36, 163840\n"
"v_mov_b32 v253, 507904\n"
"v_cndmask_b32 v254, v254, v253, vcc\n"
"v_mfma_f32_16x16x32_bf16 v[200:203], a[152:155], a[28:31], v[200:203]\n"
"s_add_u32 s40, s36, 65536\n"
"ds_read_b64_tr_b16 a[178:179], v242 offset:45568\n"
"v_mov_b32 v253, 1862270976\n"
"v_mfma_f32_16x16x32_bf16 v[180:183], a[156:159], a[16:19], v[180:183]\n"
"s_mov_b32 s41, 0\n"
"buffer_load_dwordx4 a[228:231], v235, s[8:11], s57 offen offset:2048 sc1\n"
"v_mfma_f32_16x16x32_bf16 v[188:191], a[156:159], a[20:23], v[188:191]\n"
"s_mov_b32 s42, 2\n"
"ds_read_b64_tr_b16 a[180:181], v243 offset:45056\n"
"v_cndmask_b32 v229, v229, v253, vcc\n"
"v_mfma_f32_16x16x32_bf16 v[196:199], a[156:159], a[24:27], v[196:199]\n"
"v_add_u32 v228, v228, v254\n"
"v_subrev_u32 v254, s94, v255\n"
"v_mfma_f32_16x16x32_bf16 v[204:207], a[156:159], a[28:31], v[204:207]\n"
"s_waitcnt vmcnt(48) lgkmcnt(3)\n"
"ds_read_b64_tr_b16 a[182:183], v243 offset:45568\n"
"v_lshlrev_b32 v254, 15, v254\n"
"v_mfma_f32_16x16x32_bf16 v[80:83], a[160:163], a[32:35], v[80:83]\n"
"s_waitcnt vmcnt(46)\n"
"v_add_u32 v254, s95, v254\n"
"v_cmp_lt_u32 vcc, s94, v255\n"
"v_mfma_f32_16x16x32_bf16 v[88:91], a[160:163], a[36:39], v[88:91]\n"
"s_waitcnt vmcnt(43)\n"
"ds_read_b64_tr_b16 a[184:185], v244 offset:45056\n"
"v_cndmask_b32 v228, v228, v254, vcc\n"
"v_mfma_f32_16x16x32_bf16 v[96:99], a[160:163], a[40:43], v[96:99]\n"
"s_waitcnt vmcnt(41)\n"
"buffer_load_dwordx4 a[232:235], v235, s[8:11], s58 offen offset:2048 sc1\n"
"v_mfma_f32_16x16x32_bf16 v[104:107], a[160:163], a[44:47], v[104:107]\n"
"s_lshl_b32 s94, s30, 4\n"
"ds_read_b64_tr_b16 a[186:187], v244 offset:45568\n"
"v_add_u32 v229, v229, v228\n"
"v_mfma_f32_16x16x32_bf16 v[84:87], a[164:167], a[32:35], v[84:87]\n"
"v_mov_b32 v226, v224\n"
"v_bfe_u32 v254, v224, 4, 4\n"
"v_mfma_f32_16x16x32_bf16 v[92:95], a[164:167], a[36:39], v[92:95]\n"
"ds_read_b64_tr_b16 a[188:189], v245 offset:45056\n"
"v_lshrrev_b32 v255, 8, v224\n"
"v_mfma_f32_16x16x32_bf16 v[100:103], a[164:167], a[40:43], v[100:103]\n"
"v_lshlrev_b32 v255, 2, v255\n"
"v_sub_u32 v254, v254, v255\n"
"v_mfma_f32_16x16x32_bf16 v[108:111], a[164:167], a[44:47], v[108:111]\n"
"ds_read_b64_tr_b16 a[190:191], v245 offset:45568\n"
"v_add_u32 v254, s94, v254\n"
"v_mfma_f32_16x16x32_bf16 v[112:115], a[168:171], a[32:35], v[112:115]\n"
"v_mov_b32 v255, 0xff800000\n"
"v_cmp_gt_i32_e64 vcc, 0, v254\n"
"v_mfma_f32_16x16x32_bf16 v[120:123], a[168:171], a[36:39], v[120:123]\n"
"buffer_load_dwordx4 a[236:239], v235, s[8:11], s59 offen offset:2048 sc1\n"
"v_mfma_f32_16x16x32_bf16 v[128:131], a[168:171], a[40:43], v[128:131]\n"
"v_cndmask_b32_e64 v208, 0, v255, vcc\n"
"v_cmp_gt_i32_e64 s[52:53], 1, v254\n"
"v_mfma_f32_16x16x32_bf16 v[136:139], a[168:171], a[44:47], v[136:139]\n"
"v_cmp_gt_i32_e64 s[62:63], 2, v254\n"
"v_cndmask_b32_e64 v209, 0, v255, s[52:53]\n"
"v_mfma_f32_16x16x32_bf16 v[116:119], a[172:175], a[32:35], v[116:119]\n"
"v_cndmask_b32_e64 v210, 0, v255, s[62:63]\n"
"v_cmp_gt_i32_e64 vcc, 3, v254\n"
"v_mfma_f32_16x16x32_bf16 v[124:127], a[172:175], a[36:39], v[124:127]\n"
"s_waitcnt vmcnt(41)\n"
"s_barrier\n"
"v_mfma_f32_16x16x32_bf16 v[132:135], a[172:175], a[40:43], v[132:135]\n"
"ds_read_b64_tr_b16 a[128:129], v246 offset:0\n"
"v_cndmask_b32_e64 v211, 0, v255, vcc\n"
"v_mfma_f32_16x16x32_bf16 v[140:143], a[172:175], a[44:47], v[140:143]\n"
"s_waitcnt lgkmcnt(1)\n"
"s_waitcnt vmcnt(5)\n"
"v_cmp_eq_u64 vcc, s[28:29], v[72:73]\n"
"s_cmp_eq_u64 vcc, exec\n"
"s_cbranch_scc0 L1010_%=\n"
"L1011_%=:\n"
"v_mfma_f32_16x16x32_bf16 v[144:147], a[176:179], a[32:35], v[144:147]\n"
"s_add_u32 m0, s36, 0\n"
"ds_read_b64_tr_b16 a[130:131], v246 offset:512\n"
"v_cmp_gt_i32_e64 s[52:53], 16, v254\n"
"v_mfma_f32_16x16x32_bf16 v[152:155], a[176:179], a[36:39], v[152:155]\n"
"s_add_u32 s48, s39, 0\n"
"buffer_load_dwordx4 a[240:243], v235, s[8:11], s56 offen offset:3072 sc1\n"
"v_mfma_f32_16x16x32_bf16 v[160:163], a[176:179], a[40:43], v[160:163]\n"
"s_add_u32 s49, s49, 1024\n"
"ds_read_b64_tr_b16 a[132:133], v247 offset:0\n"
"v_cndmask_b32_e64 v212, 0, v255, s[52:53]\n"
"v_mfma_f32_16x16x32_bf16 v[168:171], a[176:179], a[44:47], v[168:171]\n"
"buffer_load_dwordx4 v225, s[8:11], s48 offen offset:0 sc1 lds\n"
"v_mfma_f32_16x16x32_bf16 v[148:151], a[180:183], a[32:35], v[148:151]\n"
"ds_read_b64_tr_b16 a[134:135], v247 offset:512\n"
"v_cmp_gt_i32_e64 s[62:63], 17, v254\n"
"v_mfma_f32_16x16x32_bf16 v[156:159], a[180:183], a[36:39], v[156:159]\n"
"buffer_store_dwordx4 v[16:19], v250, s[8:11], s49 offen offset:0 sc1\n"
"v_mfma_f32_16x16x32_bf16 v[164:167], a[180:183], a[40:43], v[164:167]\n"
"ds_read_b64_tr_b16 a[136:137], v248 offset:0\n"
"v_cndmask_b32_e64 v213, 0, v255, s[62:63]\n"
"v_mfma_f32_16x16x32_bf16 v[172:175], a[180:183], a[44:47], v[172:175]\n"
"buffer_load_dwordx4 a[244:247], v235, s[8:11], s57 offen offset:3072 sc1\n"
"v_mfma_f32_16x16x32_bf16 v[176:179], a[184:187], a[32:35], v[176:179]\n"
"buffer_load_dwordx4 v225, s[8:11], s48 offen offset:1024 sc1 lds\n"
"v_mfma_f32_16x16x32_bf16 v[184:187], a[184:187], a[36:39], v[184:187]\n"
"ds_read_b64_tr_b16 a[138:139], v248 offset:512\n"
"v_cmp_gt_i32_e64 vcc, 18, v254\n"
"v_mfma_f32_16x16x32_bf16 v[192:195], a[184:187], a[40:43], v[192:195]\n"
"ds_read_b64_tr_b16 a[140:141], v249 offset:0\n"
"v_cndmask_b32_e64 v214, 0, v255, vcc\n"
"v_mfma_f32_16x16x32_bf16 v[200:203], a[184:187], a[44:47], v[200:203]\n"
"v_cmp_gt_i32_e64 s[52:53], 19, v254\n"
"v_cmp_gt_i32_e64 s[62:63], 32, v254\n"
"v_mfma_f32_16x16x32_bf16 v[180:183], a[188:191], a[32:35], v[180:183]\n"
"ds_read_b64_tr_b16 a[142:143], v249 offset:512\n"
"v_cndmask_b32_e64 v215, 0, v255, s[52:53]\n"
"v_mfma_f32_16x16x32_bf16 v[188:191], a[188:191], a[36:39], v[188:191]\n"
"buffer_load_dwordx4 v225, s[8:11], s48 offen offset:2048 sc1 lds\n"
"v_mfma_f32_16x16x32_bf16 v[196:199], a[188:191], a[40:43], v[196:199]\n"
"ds_read_b64_tr_b16 a[144:145], v246 offset:4096\n"
"v_cndmask_b32_e64 v216, 0, v255, s[62:63]\n"
"v_mfma_f32_16x16x32_bf16 v[204:207], a[188:191], a[44:47], v[204:207]\n"
"s_waitcnt lgkmcnt(4)\n"
"buffer_load_dwordx4 a[248:251], v235, s[8:11], s58 offen offset:3072 sc1\n"
"v_mfma_f32_16x16x32_bf16 v[80:83], a[128:131], a[48:51], v[80:83]\n"
"ds_read_b64_tr_b16 a[146:147], v246 offset:4608\n"
"v_cmp_gt_i32_e64 vcc, 33, v254\n"
"v_mfma_f32_16x16x32_bf16 v[88:91], a[128:131], a[52:55], v[88:91]\n"
"buffer_load_dwordx4 v225, s[8:11], s48 offen offset:3072 sc1 lds\n"
"v_mfma_f32_16x16x32_bf16 v[96:99], a[128:131], a[56:59], v[96:99]\n"
"ds_read_b64_tr_b16 a[148:149], v247 offset:4096\n"
"v_cndmask_b32_e64 v217, 0, v255, vcc\n"
"v_mfma_f32_16x16x32_bf16 v[104:107], a[128:131], a[60:63], v[104:107]\n"
"v_cmp_gt_i32_e64 s[52:53], 34, v254\n"
"v_cmp_gt_i32_e64 s[62:63], 35, v254\n"
"v_mfma_f32_16x16x32_bf16 v[84:87], a[132:135], a[48:51], v[84:87]\n"
"s_add_u32 m0, s36, 16384\n"
"ds_read_b64_tr_b16 a[150:151], v247 offset:4608\n"
"v_cndmask_b32_e64 v218, 0, v255, s[52:53]\n"
"v_mfma_f32_16x16x32_bf16 v[92:95], a[132:135], a[52:55], v[92:95]\n"
"s_add_u32 s48, s39, 516096\n"
"ds_read_b64_tr_b16 a[152:153], v248 offset:4096\n"
"v_cndmask_b32_e64 v219, 0, v255, s[62:63]\n"
"v_mfma_f32_16x16x32_bf16 v[100:103], a[132:135], a[56:59], v[100:103]\n"
"buffer_load_dwordx4 a[252:255], v235, s[8:11], s59 offen offset:3072 sc1\n"
"v_mfma_f32_16x16x32_bf16 v[108:111], a[132:135], a[60:63], v[108:111]\n"
"s_waitcnt lgkmcnt(3)\n"
"buffer_load_dwordx4 v225, s[8:11], s48 offen offset:0 sc1 lds\n"
"v_mfma_f32_16x16x32_bf16 v[112:115], a[136:139], a[48:51], v[112:115]\n"
"ds_read_b64_tr_b16 a[154:155], v248 offset:4608\n"
"v_cmp_gt_i32_e64 vcc, 48, v254\n"
"v_mfma_f32_16x16x32_bf16 v[120:123], a[136:139], a[52:55], v[120:123]\n"
"ds_read_b64_tr_b16 a[156:157], v249 offset:4096\n"
"v_cndmask_b32_e64 v220, 0, v255, vcc\n"
"v_mfma_f32_16x16x32_bf16 v[128:131], a[136:139], a[56:59], v[128:131]\n"
"v_cmp_gt_i32_e64 s[52:53], 49, v254\n"
"v_cmp_gt_i32_e64 s[62:63], 50, v254\n"
"v_mfma_f32_16x16x32_bf16 v[136:139], a[136:139], a[60:63], v[136:139]\n"
"ds_read_b64_tr_b16 a[158:159], v249 offset:4608\n"
"v_cndmask_b32_e64 v221, 0, v255, s[52:53]\n"
"v_mfma_f32_16x16x32_bf16 v[116:119], a[140:143], a[48:51], v[116:119]\n"
"buffer_load_dwordx4 v225, s[8:11], s48 offen offset:1024 sc1 lds\n"
"v_mfma_f32_16x16x32_bf16 v[124:127], a[140:143], a[52:55], v[124:127]\n"
"ds_read_b64_tr_b16 a[160:161], v246 offset:24576\n"
"v_cndmask_b32_e64 v222, 0, v255, s[62:63]\n"
"v_mfma_f32_16x16x32_bf16 v[132:135], a[140:143], a[56:59], v[132:135]\n"
"v_cmp_gt_i32_e64 vcc, 51, v254\n"
"v_mfma_f32_16x16x32_bf16 v[140:143], a[140:143], a[60:63], v[140:143]\n"
"ds_read_b64_tr_b16 a[162:163], v246 offset:25088\n"
"v_cndmask_b32_e64 v223, 0, v255, vcc\n"
"v_mfma_f32_16x16x32_bf16 v[144:147], a[144:147], a[48:51], v[144:147]\n"
"buffer_load_dwordx4 v225, s[8:11], s48 offen offset:2048 sc1 lds\n"
"v_mfma_f32_16x16x32_bf16 v[152:155], a[144:147], a[52:55], v[152:155]\n"
"ds_read_b64_tr_b16 a[164:165], v247 offset:24576\n"
"v_mfma_f32_16x16x32_bf16 v[160:163], a[144:147], a[56:59], v[160:163]\n"
"v_mfma_f32_16x16x32_bf16 v[168:171], a[144:147], a[60:63], v[168:171]\n"
"s_waitcnt lgkmcnt(3)\n"
"ds_read_b64_tr_b16 a[166:167], v247 offset:25088\n"
"v_mfma_f32_16x16x32_bf16 v[148:151], a[148:151], a[48:51], v[148:151]\n"
"v_mfma_f32_16x16x32_bf16 v[156:159], a[148:151], a[52:55], v[156:159]\n"
"ds_read_b64_tr_b16 a[168:169], v248 offset:24576\n"
"v_mfma_f32_16x16x32_bf16 v[164:167], a[148:151], a[56:59], v[164:167]\n"
"buffer_load_dwordx4 v225, s[8:11], s48 offen offset:3072 sc1 lds\n"
"v_mfma_f32_16x16x32_bf16 v[172:175], a[148:151], a[60:63], v[172:175]\n"
"ds_read_b64_tr_b16 a[170:171], v248 offset:25088\n"
"v_mfma_f32_16x16x32_bf16 v[176:179], a[152:155], a[48:51], v[176:179]\n"
"s_add_u32 m0, s36, 32768\n"
"ds_read_b64_tr_b16 a[172:173], v249 offset:24576\n"
"s_add_u32 s48, s39, 16384\n"
"v_mfma_f32_16x16x32_bf16 v[184:187], a[152:155], a[52:55], v[184:187]\n"
"v_mfma_f32_16x16x32_bf16 v[192:195], a[152:155], a[56:59], v[192:195]\n"
"ds_read_b64_tr_b16 a[174:175], v249 offset:25088\n"
"v_mfma_f32_16x16x32_bf16 v[200:203], a[152:155], a[60:63], v[200:203]\n"
"buffer_load_dwordx4 v225, s[8:11], s48 offen offset:0 sc1 lds\n"
"v_mfma_f32_16x16x32_bf16 v[180:183], a[156:159], a[48:51], v[180:183]\n"
"ds_read_b64_tr_b16 a[176:177], v246 offset:28672\n"
"v_mfma_f32_16x16x32_bf16 v[188:191], a[156:159], a[52:55], v[188:191]\n"
"v_mfma_f32_16x16x32_bf16 v[196:199], a[156:159], a[56:59], v[196:199]\n"
"s_add_u32 s49, s49, s89\n"
"ds_read_b64_tr_b16 a[178:179], v246 offset:29184\n"
"v_mfma_f32_16x16x32_bf16 v[204:207], a[156:159], a[60:63], v[204:207]\n"
"s_waitcnt lgkmcnt(3)\n"
"buffer_load_dwordx4 v225, s[8:11], s48 offen offset:1024 sc1 lds\n"
"v_mfma_f32_16x16x32_bf16 v[80:83], a[160:163], a[64:67], v[80:83]\n"
"ds_read_b64_tr_b16 a[180:181], v247 offset:28672\n"
"v_mfma_f32_16x16x32_bf16 v[88:91], a[160:163], a[68:71], v[88:91]\n"
"buffer_store_dwordx4 v[32:35], v250, s[8:11], s49 offen offset:0 sc1\n"
"v_mfma_f32_16x16x32_bf16 v[96:99], a[160:163], a[72:75], v[96:99]\n"
"ds_read_b64_tr_b16 a[182:183], v247 offset:29184\n"
"v_mfma_f32_16x16x32_bf16 v[104:107], a[160:163], a[76:79], v[104:107]\n"
"v_mfma_f32_16x16x32_bf16 v[84:87], a[164:167], a[64:67], v[84:87]\n"
"ds_read_b64_tr_b16 a[184:185], v248 offset:28672\n"
"v_mfma_f32_16x16x32_bf16 v[92:95], a[164:167], a[68:71], v[92:95]\n"
"buffer_load_dwordx4 v225, s[8:11], s48 offen offset:2048 sc1 lds\n"
"v_mfma_f32_16x16x32_bf16 v[100:103], a[164:167], a[72:75], v[100:103]\n"
"ds_read_b64_tr_b16 a[186:187], v248 offset:29184\n"
"v_mfma_f32_16x16x32_bf16 v[108:111], a[164:167], a[76:79], v[108:111]\n"
"v_mfma_f32_16x16x32_bf16 v[112:115], a[168:171], a[64:67], v[112:115]\n"
"ds_read_b64_tr_b16 a[188:189], v249 offset:28672\n"
"v_mfma_f32_16x16x32_bf16 v[120:123], a[168:171], a[68:71], v[120:123]\n"
"buffer_load_dwordx4 v225, s[8:11], s48 offen offset:3072 sc1 lds\n"
"v_mfma_f32_16x16x32_bf16 v[128:131], a[168:171], a[72:75], v[128:131]\n"
"ds_read_b64_tr_b16 a[190:191], v249 offset:29184\n"
"v_mfma_f32_16x16x32_bf16 v[136:139], a[168:171], a[76:79], v[136:139]\n"
"s_waitcnt lgkmcnt(3)\n"
"ds_read_b64_tr_b16 a[128:129], v242 offset:0\n"
"v_mfma_f32_16x16x32_bf16 v[116:119], a[172:175], a[64:67], v[116:119]\n"
"s_add_u32 m0, s36, 49152\n"
"s_add_u32 s48, s39, 532480\n"
"v_mfma_f32_16x16x32_bf16 v[124:127], a[172:175], a[68:71], v[124:127]\n"
"ds_read_b64_tr_b16 a[130:131], v242 offset:512\n"
"v_mfma_f32_16x16x32_bf16 v[132:135], a[172:175], a[72:75], v[132:135]\n"
"buffer_load_dwordx4 v225, s[8:11], s48 offen offset:0 sc1 lds\n"
"v_mfma_f32_16x16x32_bf16 v[140:143], a[172:175], a[76:79], v[140:143]\n"
"ds_read_b64_tr_b16 a[132:133], v243 offset:0\n"
"v_mfma_f32_16x16x32_bf16 v[144:147], a[176:179], a[64:67], v[144:147]\n"
"v_mfma_f32_16x16x32_bf16 v[152:155], a[176:179], a[68:71], v[152:155]\n"
"ds_read_b64_tr_b16 a[134:135], v243 offset:512\n"
"v_mfma_f32_16x16x32_bf16 v[160:163], a[176:179], a[72:75], v[160:163]\n"
"v_mfma_f32_16x16x32_bf16 v[168:171], a[176:179], a[76:79], v[168:171]\n"
"ds_read_b64_tr_b16 a[136:137], v244 offset:0\n"
"v_mfma_f32_16x16x32_bf16 v[148:151], a[180:183], a[64:67], v[148:151]\n"
"buffer_load_dwordx4 v225, s[8:11], s48 offen offset:1024 sc1 lds\n"
"v_mfma_f32_16x16x32_bf16 v[156:159], a[180:183], a[68:71], v[156:159]\n"
"ds_read_b64_tr_b16 a[138:139], v244 offset:512\n"
"v_mfma_f32_16x16x32_bf16 v[164:167], a[180:183], a[72:75], v[164:167]\n"
"v_mfma_f32_16x16x32_bf16 v[172:175], a[180:183], a[76:79], v[172:175]\n"
"s_waitcnt lgkmcnt(3)\n"
"ds_read_b64_tr_b16 a[140:141], v245 offset:0\n"
"v_mfma_f32_16x16x32_bf16 v[176:179], a[184:187], a[64:67], v[176:179]\n"
"buffer_load_dwordx4 v225, s[8:11], s48 offen offset:2048 sc1 lds\n"
"v_mfma_f32_16x16x32_bf16 v[184:187], a[184:187], a[68:71], v[184:187]\n"
"ds_read_b64_tr_b16 a[142:143], v245 offset:512\n"
"v_mfma_f32_16x16x32_bf16 v[192:195], a[184:187], a[72:75], v[192:195]\n"
"v_mfma_f32_16x16x32_bf16 v[200:203], a[184:187], a[76:79], v[200:203]\n"
"ds_read_b64_tr_b16 a[144:145], v242 offset:4096\n"
"v_mfma_f32_16x16x32_bf16 v[180:183], a[188:191], a[64:67], v[180:183]\n"
"v_mfma_f32_16x16x32_bf16 v[188:191], a[188:191], a[68:71], v[188:191]\n"
"ds_read_b64_tr_b16 a[146:147], v242 offset:4608\n"
"v_mfma_f32_16x16x32_bf16 v[196:199], a[188:191], a[72:75], v[196:199]\n"
"buffer_load_dwordx4 v225, s[8:11], s48 offen offset:3072 sc1 lds\n"
"v_mfma_f32_16x16x32_bf16 v[204:207], a[188:191], a[76:79], v[204:207]\n"
"ds_read_b64_tr_b16 a[148:149], v243 offset:4096\n"
"v_mfma_f32_16x16x32_bf16 v[80:83], a[128:131], a[80:83], v[80:83]\n"
"v_mfma_f32_16x16x32_bf16 v[88:91], a[128:131], a[84:87], v[88:91]\n"
"ds_read_b64_tr_b16 a[150:151], v243 offset:4608\n"
"v_mfma_f32_16x16x32_bf16 v[96:99], a[128:131], a[88:91], v[96:99]\n"
"ds_read_b64_tr_b16 a[152:153], v244 offset:4096\n"
"v_mfma_f32_16x16x32_bf16 v[104:107], a[128:131], a[92:95], v[104:107]\n"
"s_waitcnt lgkmcnt(4)\n"
"v_mfma_f32_16x16x32_bf16 v[84:87], a[132:135], a[80:83], v[84:87]\n"
"ds_read_b64_tr_b16 a[154:155], v244 offset:4608\n"
"v_mfma_f32_16x16x32_bf16 v[92:95], a[132:135], a[84:87], v[92:95]\n"
"v_mfma_f32_16x16x32_bf16 v[100:103], a[132:135], a[88:91], v[100:103]\n"
"ds_read_b64_tr_b16 a[156:157], v245 offset:4096\n"
"v_mfma_f32_16x16x32_bf16 v[108:111], a[132:135], a[92:95], v[108:111]\n"
"v_mfma_f32_16x16x32_bf16 v[112:115], a[136:139], a[80:83], v[112:115]\n"
"ds_read_b64_tr_b16 a[158:159], v245 offset:4608\n"
"v_mfma_f32_16x16x32_bf16 v[120:123], a[136:139], a[84:87], v[120:123]\n"
"v_mfma_f32_16x16x32_bf16 v[128:131], a[136:139], a[88:91], v[128:131]\n"
"s_add_u32 s49, s49, 1024\n"
"ds_read_b64_tr_b16 a[160:161], v242 offset:24576\n"
"v_mfma_f32_16x16x32_bf16 v[136:139], a[136:139], a[92:95], v[136:139]\n"
"v_mfma_f32_16x16x32_bf16 v[116:119], a[140:143], a[80:83], v[116:119]\n"
"ds_read_b64_tr_b16 a[162:163], v242 offset:25088\n"
"v_mfma_f32_16x16x32_bf16 v[124:127], a[140:143], a[84:87], v[124:127]\n"
"buffer_store_dwordx4 v[48:51], v250, s[8:11], s49 offen offset:0 sc1\n"
"v_mfma_f32_16x16x32_bf16 v[132:135], a[140:143], a[88:91], v[132:135]\n"
"ds_read_b64_tr_b16 a[164:165], v243 offset:24576\n"
"v_mfma_f32_16x16x32_bf16 v[140:143], a[140:143], a[92:95], v[140:143]\n"
"s_waitcnt lgkmcnt(3)\n"
"v_mfma_f32_16x16x32_bf16 v[144:147], a[144:147], a[80:83], v[144:147]\n"
"ds_read_b64_tr_b16 a[166:167], v243 offset:25088\n"
"v_mfma_f32_16x16x32_bf16 v[152:155], a[144:147], a[84:87], v[152:155]\n"
"v_mfma_f32_16x16x32_bf16 v[160:163], a[144:147], a[88:91], v[160:163]\n"
"ds_read_b64_tr_b16 a[168:169], v244 offset:24576\n"
"v_mfma_f32_16x16x32_bf16 v[168:171], a[144:147], a[92:95], v[168:171]\n"
"v_mfma_f32_16x16x32_bf16 v[148:151], a[148:151], a[80:83], v[148:151]\n"
"ds_read_b64_tr_b16 a[170:171], v244 offset:25088\n"
"v_mfma_f32_16x16x32_bf16 v[156:159], a[148:151], a[84:87], v[156:159]\n"
"ds_read_b64_tr_b16 a[172:173], v245 offset:24576\n"
"v_mfma_f32_16x16x32_bf16 v[164:167], a[148:151], a[88:91], v[164:167]\n"
"v_mfma_f32_16x16x32_bf16 v[172:175], a[148:151], a[92:95], v[172:175]\n"
"ds_read_b64_tr_b16 a[174:175], v245 offset:25088\n"
"v_mfma_f32_16x16x32_bf16 v[176:179], a[152:155], a[80:83], v[176:179]\n"
"v_mfma_f32_16x16x32_bf16 v[184:187], a[152:155], a[84:87], v[184:187]\n"
"ds_read_b64_tr_b16 a[176:177], v242 offset:28672\n"
"v_mfma_f32_16x16x32_bf16 v[192:195], a[152:155], a[88:91], v[192:195]\n"
"v_mfma_f32_16x16x32_bf16 v[200:203], a[152:155], a[92:95], v[200:203]\n"
"ds_read_b64_tr_b16 a[178:179], v242 offset:29184\n"
"v_mfma_f32_16x16x32_bf16 v[180:183], a[156:159], a[80:83], v[180:183]\n"
"v_mfma_f32_16x16x32_bf16 v[188:191], a[156:159], a[84:87], v[188:191]\n"
"ds_read_b64_tr_b16 a[180:181], v243 offset:28672\n"
"v_mfma_f32_16x16x32_bf16 v[196:199], a[156:159], a[88:91], v[196:199]\n"
"v_mfma_f32_16x16x32_bf16 v[204:207], a[156:159], a[92:95], v[204:207]\n"
"s_waitcnt lgkmcnt(3)\n"
"ds_read_b64_tr_b16 a[182:183], v243 offset:29184\n"
"v_mfma_f32_16x16x32_bf16 v[80:83], a[160:163], a[96:99], v[80:83]\n"
"v_mfma_f32_16x16x32_bf16 v[88:91], a[160:163], a[100:103], v[88:91]\n"
"ds_read_b64_tr_b16 a[184:185], v244 offset:28672\n"
"v_mfma_f32_16x16x32_bf16 v[96:99], a[160:163], a[104:107], v[96:99]\n"
"v_mfma_f32_16x16x32_bf16 v[104:107], a[160:163], a[108:111], v[104:107]\n"
"ds_read_b64_tr_b16 a[186:187], v244 offset:29184\n"
"v_mfma_f32_16x16x32_bf16 v[84:87], a[164:167], a[96:99], v[84:87]\n"
"v_mfma_f32_16x16x32_bf16 v[92:95], a[164:167], a[100:103], v[92:95]\n"
"ds_read_b64_tr_b16 a[188:189], v245 offset:28672\n"
"v_mfma_f32_16x16x32_bf16 v[100:103], a[164:167], a[104:107], v[100:103]\n"
"v_mfma_f32_16x16x32_bf16 v[108:111], a[164:167], a[108:111], v[108:111]\n"
"ds_read_b64_tr_b16 a[190:191], v245 offset:29184\n"
"v_mfma_f32_16x16x32_bf16 v[112:115], a[168:171], a[96:99], v[112:115]\n"
"v_mfma_f32_16x16x32_bf16 v[120:123], a[168:171], a[100:103], v[120:123]\n"
"v_mfma_f32_16x16x32_bf16 v[128:131], a[168:171], a[104:107], v[128:131]\n"
"v_mfma_f32_16x16x32_bf16 v[136:139], a[168:171], a[108:111], v[136:139]\n"
"v_mfma_f32_16x16x32_bf16 v[116:119], a[172:175], a[96:99], v[116:119]\n"
"v_mfma_f32_16x16x32_bf16 v[124:127], a[172:175], a[100:103], v[124:127]\n"
"s_barrier\n"
"v_mfma_f32_16x16x32_bf16 v[132:135], a[172:175], a[104:107], v[132:135]\n"
"ds_read_b64_tr_b16 a[128:129], v246 offset:8192\n"
"v_mfma_f32_16x16x32_bf16 v[140:143], a[172:175], a[108:111], v[140:143]\n"
"s_waitcnt lgkmcnt(1)\n"
"v_mfma_f32_16x16x32_bf16 v[144:147], a[176:179], a[96:99], v[144:147]\n"
"ds_read_b64_tr_b16 a[130:131], v246 offset:8704\n"
"v_mfma_f32_16x16x32_bf16 v[152:155], a[176:179], a[100:103], v[152:155]\n"
"ds_read_b64_tr_b16 a[132:133], v247 offset:8192\n"
"v_mfma_f32_16x16x32_bf16 v[160:163], a[176:179], a[104:107], v[160:163]\n"
"v_mfma_f32_16x16x32_bf16 v[168:171], a[176:179], a[108:111], v[168:171]\n"
"ds_read_b64_tr_b16 a[134:135], v247 offset:8704\n"
"v_mfma_f32_16x16x32_bf16 v[148:151], a[180:183], a[96:99], v[148:151]\n"
"ds_read_b64_tr_b16 a[136:137], v246 offset:32768\n"
"v_mfma_f32_16x16x32_bf16 v[156:159], a[180:183], a[100:103], v[156:159]\n"
"v_mfma_f32_16x16x32_bf16 v[164:167], a[180:183], a[104:107], v[164:167]\n"
"ds_read_b64_tr_b16 a[138:139], v246 offset:33280\n"
"v_mfma_f32_16x16x32_bf16 v[172:175], a[180:183], a[108:111], v[172:175]\n"
"ds_read_b64_tr_b16 a[140:141], v247 offset:32768\n"
"v_mfma_f32_16x16x32_bf16 v[176:179], a[184:187], a[96:99], v[176:179]\n"
"v_mfma_f32_16x16x32_bf16 v[184:187], a[184:187], a[100:103], v[184:187]\n"
"ds_read_b64_tr_b16 a[142:143], v247 offset:33280\n"
"v_mfma_f32_16x16x32_bf16 v[192:195], a[184:187], a[104:107], v[192:195]\n"
"v_mfma_f32_16x16x32_bf16 v[200:203], a[184:187], a[108:111], v[200:203]\n"
"ds_read_b64_tr_b16 a[144:145], v242 offset:8192\n"
"v_mfma_f32_16x16x32_bf16 v[180:183], a[188:191], a[96:99], v[180:183]\n"
"v_mfma_f32_16x16x32_bf16 v[188:191], a[188:191], a[100:103], v[188:191]\n"
"ds_read_b64_tr_b16 a[146:147], v242 offset:8704\n"
"v_mfma_f32_16x16x32_bf16 v[196:199], a[188:191], a[104:107], v[196:199]\n"
"ds_read_b64_tr_b16 a[148:149], v243 offset:8192\n"
"v_mfma_f32_16x16x32_bf16 v[204:207], a[188:191], a[108:111], v[204:207]\n"
"s_waitcnt lgkmcnt(4)\n"
"v_mfma_f32_16x16x32_bf16 v[80:83], a[128:131], a[192:195], v[80:83]\n"
"ds_read_b64_tr_b16 a[150:151], v243 offset:8704\n"
"v_mfma_f32_16x16x32_bf16 v[88:91], a[128:131], a[196:199], v[88:91]\n"
"ds_read_b64_tr_b16 a[152:153], v242 offset:32768\n"
"v_mfma_f32_16x16x32_bf16 v[96:99], a[128:131], a[200:203], v[96:99]\n"
"v_mfma_f32_16x16x32_bf16 v[104:107], a[128:131], a[204:207], v[104:107]\n"
"ds_read_b64_tr_b16 a[154:155], v242 offset:33280\n"
"v_mfma_f32_16x16x32_bf16 v[84:87], a[132:135], a[192:195], v[84:87]\n"
"ds_read_b64_tr_b16 a[156:157], v243 offset:32768\n"
"v_mfma_f32_16x16x32_bf16 v[92:95], a[132:135], a[196:199], v[92:95]\n"
"v_mfma_f32_16x16x32_bf16 v[100:103], a[132:135], a[200:203], v[100:103]\n"
"ds_read_b64_tr_b16 a[158:159], v243 offset:33280\n"
"v_mfma_f32_16x16x32_bf16 v[108:111], a[132:135], a[204:207], v[108:111]\n"
"v_mfma_f32_16x16x32_bf16 v[80:83], a[136:139], a[208:211], v[80:83]\n"
"ds_read_b64_tr_b16 a[160:161], v248 offset:8192\n"
"v_mfma_f32_16x16x32_bf16 v[88:91], a[136:139], a[212:215], v[88:91]\n"
"v_mfma_f32_16x16x32_bf16 v[96:99], a[136:139], a[216:219], v[96:99]\n"
"s_waitcnt vmcnt(27)\n"
"ds_read_b64_tr_b16 a[162:163], v248 offset:8704\n"
"v_mfma_f32_16x16x32_bf16 v[104:107], a[136:139], a[220:223], v[104:107]\n"
"s_waitcnt lgkmcnt(3)\n"
"ds_read_b64_tr_b16 a[164:165], v249 offset:8192\n"
"v_mfma_f32_16x16x32_bf16 v[84:87], a[140:143], a[208:211], v[84:87]\n"
"v_mfma_f32_16x16x32_bf16 v[92:95], a[140:143], a[212:215], v[92:95]\n"
"ds_read_b64_tr_b16 a[166:167], v249 offset:8704\n"
"v_mfma_f32_16x16x32_bf16 v[100:103], a[140:143], a[216:219], v[100:103]\n"
"ds_read_b64_tr_b16 a[168:169], v248 offset:32768\n"
"v_mfma_f32_16x16x32_bf16 v[108:111], a[140:143], a[220:223], v[108:111]\n"
"s_waitcnt vmcnt(26)\n"
"v_mfma_f32_16x16x32_bf16 v[80:83], a[144:147], a[224:227], v[80:83]\n"
"s_waitcnt vmcnt(25)\n"
"ds_read_b64_tr_b16 a[170:171], v248 offset:33280\n"
"v_mfma_f32_16x16x32_bf16 v[88:91], a[144:147], a[228:231], v[88:91]\n"
"s_waitcnt vmcnt(24)\n"
"ds_read_b64_tr_b16 a[172:173], v249 offset:32768\n"
"v_mfma_f32_16x16x32_bf16 v[96:99], a[144:147], a[232:235], v[96:99]\n"
"s_waitcnt vmcnt(23)\n"
"v_mfma_f32_16x16x32_bf16 v[104:107], a[144:147], a[236:239], v[104:107]\n"
"ds_read_b64_tr_b16 a[174:175], v249 offset:33280\n"
"v_mfma_f32_16x16x32_bf16 v[84:87], a[148:151], a[224:227], v[84:87]\n"
"v_mfma_f32_16x16x32_bf16 v[92:95], a[148:151], a[228:231], v[92:95]\n"
"ds_read_b64_tr_b16 a[176:177], v244 offset:8192\n"
"v_mfma_f32_16x16x32_bf16 v[100:103], a[148:151], a[232:235], v[100:103]\n"
"v_mfma_f32_16x16x32_bf16 v[108:111], a[148:151], a[236:239], v[108:111]\n"
"s_waitcnt vmcnt(22)\n"
"ds_read_b64_tr_b16 a[178:179], v244 offset:8704\n"
"v_mfma_f32_16x16x32_bf16 v[80:83], a[152:155], a[240:243], v[80:83]\n"
"s_waitcnt vmcnt(19)\n"
"ds_read_b64_tr_b16 a[180:181], v245 offset:8192\n"
"v_mfma_f32_16x16x32_bf16 v[88:91], a[152:155], a[244:247], v[88:91]\n"
"s_waitcnt vmcnt(16)\n"
"v_mfma_f32_16x16x32_bf16 v[96:99], a[152:155], a[248:251], v[96:99]\n"
"s_waitcnt vmcnt(14)\n"
"ds_read_b64_tr_b16 a[182:183], v245 offset:8704\n"
"v_mfma_f32_16x16x32_bf16 v[104:107], a[152:155], a[252:255], v[104:107]\n"
"s_waitcnt lgkmcnt(4)\n"
"ds_read_b64_tr_b16 a[184:185], v244 offset:32768\n"
"v_mfma_f32_16x16x32_bf16 v[84:87], a[156:159], a[240:243], v[84:87]\n"
"v_mfma_f32_16x16x32_bf16 v[92:95], a[156:159], a[244:247], v[92:95]\n"
"ds_read_b64_tr_b16 a[186:187], v244 offset:33280\n"
"v_mfma_f32_16x16x32_bf16 v[100:103], a[156:159], a[248:251], v[100:103]\n"
"ds_read_b64_tr_b16 a[188:189], v245 offset:32768\n"
"v_mfma_f32_16x16x32_bf16 v[108:111], a[156:159], a[252:255], v[108:111]\n"
"v_mfma_f32_16x16x32_bf16 v[112:115], a[160:163], a[192:195], v[112:115]\n"
"ds_read_b64_tr_b16 a[190:191], v245 offset:33280\n"
"v_mfma_f32_16x16x32_bf16 v[120:123], a[160:163], a[196:199], v[120:123]\n"
"v_mfma_f32_16x16x32_bf16 v[128:131], a[160:163], a[200:203], v[128:131]\n"
"ds_read_b64_tr_b16 a[128:129], v246 offset:12288\n"
"v_cvt_pk_bf16_f32 v80, v80, v81\n"
"v_mfma_f32_16x16x32_bf16 v[136:139], a[160:163], a[204:207], v[136:139]\n"
"v_cvt_pk_bf16_f32 v81, v82, v83\n"
"v_cvt_pk_bf16_f32 v82, v84, v85\n"
"v_mfma_f32_16x16x32_bf16 v[116:119], a[164:167], a[192:195], v[116:119]\n"
"ds_read_b64_tr_b16 a[130:131], v246 offset:12800\n"
"v_cvt_pk_bf16_f32 v83, v86, v87\n"
"v_mfma_f32_16x16x32_bf16 v[124:127], a[164:167], a[196:199], v[124:127]\n"
"ds_read_b64_tr_b16 a[132:133], v247 offset:12288\n"
"v_accvgpr_write_b32 a0, v80\n"
"v_mfma_f32_16x16x32_bf16 v[132:135], a[164:167], a[200:203], v[132:135]\n"
"v_accvgpr_write_b32 a1, v81\n"
"v_accvgpr_write_b32 a2, v82\n"
"v_mfma_f32_16x16x32_bf16 v[140:143], a[164:167], a[204:207], v[140:143]\n"
"ds_read_b64_tr_b16 a[134:135], v247 offset:12800\n"
"v_accvgpr_write_b32 a3, v83\n"
"v_mfma_f32_16x16x32_bf16 v[112:115], a[168:171], a[208:211], v[112:115]\n"
"ds_read_b64_tr_b16 a[136:137], v246 offset:36864\n"
"v_cvt_pk_bf16_f32 v88, v88, v89\n"
"v_mfma_f32_16x16x32_bf16 v[120:123], a[168:171], a[212:215], v[120:123]\n"
"v_cvt_pk_bf16_f32 v89, v90, v91\n"
"v_cvt_pk_bf16_f32 v90, v92, v93\n"
"v_mfma_f32_16x16x32_bf16 v[128:131], a[168:171], a[216:219], v[128:131]\n"
"ds_read_b64_tr_b16 a[138:139], v246 offset:37376\n"
"v_cvt_pk_bf16_f32 v91, v94, v95\n"
"v_mfma_f32_16x16x32_bf16 v[136:139], a[168:171], a[220:223], v[136:139]\n"
"ds_read_b64_tr_b16 a[140:141], v247 offset:36864\n"
"v_accvgpr_write_b32 a16, v88\n"
"v_mfma_f32_16x16x32_bf16 v[116:119], a[172:175], a[208:211], v[116:119]\n"
"v_accvgpr_write_b32 a17, v89\n"
"v_accvgpr_write_b32 a18, v90\n"
"v_mfma_f32_16x16x32_bf16 v[124:127], a[172:175], a[212:215], v[124:127]\n"
"ds_read_b64_tr_b16 a[142:143], v247 offset:37376\n"
"v_accvgpr_write_b32 a19, v91\n"
"v_mfma_f32_16x16x32_bf16 v[132:135], a[172:175], a[216:219], v[132:135]\n"
"v_cvt_pk_bf16_f32 v96, v96, v97\n"
"v_cvt_pk_bf16_f32 v97, v98, v99\n"
"v_mfma_f32_16x16x32_bf16 v[140:143], a[172:175], a[220:223], v[140:143]\n"
"s_waitcnt lgkmcnt(3)\n"
"ds_read_b64_tr_b16 a[144:145], v242 offset:12288\n"
"v_cvt_pk_bf16_f32 v98, v100, v101\n"
"v_mfma_f32_16x16x32_bf16 v[112:115], a[176:179], a[224:227], v[112:115]\n"
"v_cvt_pk_bf16_f32 v99, v102, v103\n"
"v_accvgpr_write_b32 a32, v96\n"
"v_mfma_f32_16x16x32_bf16 v[120:123], a[176:179], a[228:231], v[120:123]\n"
"ds_read_b64_tr_b16 a[146:147], v242 offset:12800\n"
"v_accvgpr_write_b32 a33, v97\n"
"v_mfma_f32_16x16x32_bf16 v[128:131], a[176:179], a[232:235], v[128:131]\n"
"ds_read_b64_tr_b16 a[148:149], v243 offset:12288\n"
"v_accvgpr_write_b32 a34, v98\n"
"v_mfma_f32_16x16x32_bf16 v[136:139], a[176:179], a[236:239], v[136:139]\n"
"v_accvgpr_write_b32 a35, v99\n"
"v_mfma_f32_16x16x32_bf16 v[116:119], a[180:183], a[224:227], v[116:119]\n"
"ds_read_b64_tr_b16 a[150:151], v243 offset:12800\n"
"v_cvt_pk_bf16_f32 v104, v104, v105\n"
"v_mfma_f32_16x16x32_bf16 v[124:127], a[180:183], a[228:231], v[124:127]\n"
"ds_read_b64_tr_b16 a[152:153], v242 offset:36864\n"
"v_cvt_pk_bf16_f32 v105, v106, v107\n"
"v_mfma_f32_16x16x32_bf16 v[132:135], a[180:183], a[232:235], v[132:135]\n"
"v_cvt_pk_bf16_f32 v106, v108, v109\n"
"v_cvt_pk_bf16_f32 v107, v110, v111\n"
"v_mfma_f32_16x16x32_bf16 v[140:143], a[180:183], a[236:239], v[140:143]\n"
"ds_read_b64_tr_b16 a[154:155], v242 offset:37376\n"
"v_accvgpr_write_b32 a48, v104\n"
"v_mfma_f32_16x16x32_bf16 v[112:115], a[184:187], a[240:243], v[112:115]\n"
"ds_read_b64_tr_b16 a[156:157], v243 offset:36864\n"
"v_accvgpr_write_b32 a49, v105\n"
"v_mfma_f32_16x16x32_bf16 v[120:123], a[184:187], a[244:247], v[120:123]\n"
"v_accvgpr_write_b32 a50, v106\n"
"v_accvgpr_write_b32 a51, v107\n"
"v_mfma_f32_16x16x32_bf16 v[128:131], a[184:187], a[248:251], v[128:131]\n"
"ds_read_b64_tr_b16 a[158:159], v243 offset:37376\n"
"v_mfma_f32_16x16x32_bf16 v[136:139], a[184:187], a[252:255], v[136:139]\n"
"v_mfma_f32_16x16x32_bf16 v[116:119], a[188:191], a[240:243], v[116:119]\n"
"ds_read_b64_tr_b16 a[160:161], v248 offset:12288\n"
"v_mfma_f32_16x16x32_bf16 v[124:127], a[188:191], a[244:247], v[124:127]\n"
"v_mfma_f32_16x16x32_bf16 v[132:135], a[188:191], a[248:251], v[132:135]\n"
"ds_read_b64_tr_b16 a[162:163], v248 offset:12800\n"
"v_mfma_f32_16x16x32_bf16 v[140:143], a[188:191], a[252:255], v[140:143]\n"
"ds_read_b64_tr_b16 a[164:165], v249 offset:12288\n"
"v_mfma_f32_16x16x32_bf16 v[144:147], a[128:131], a[192:195], v[144:147]\n"
"v_mfma_f32_16x16x32_bf16 v[152:155], a[128:131], a[196:199], v[152:155]\n"
"ds_read_b64_tr_b16 a[166:167], v249 offset:12800\n"
"v_mfma_f32_16x16x32_bf16 v[160:163], a[128:131], a[200:203], v[160:163]\n"
"ds_read_b64_tr_b16 a[168:169], v248 offset:36864\n"
"v_cvt_pk_bf16_f32 v112, v112, v113\n"
"v_mfma_f32_16x16x32_bf16 v[168:171], a[128:131], a[204:207], v[168:171]\n"
"v_cvt_pk_bf16_f32 v113, v114, v115\n"
"v_cvt_pk_bf16_f32 v114, v116, v117\n"
"v_mfma_f32_16x16x32_bf16 v[148:151], a[132:135], a[192:195], v[148:151]\n"
"ds_read_b64_tr_b16 a[170:171], v248 offset:37376\n"
"v_cvt_pk_bf16_f32 v115, v118, v119\n"
"v_mfma_f32_16x16x32_bf16 v[156:159], a[132:135], a[196:199], v[156:159]\n"
"ds_read_b64_tr_b16 a[172:173], v249 offset:36864\n"
"v_accvgpr_write_b32 a4, v112\n"
"v_mfma_f32_16x16x32_bf16 v[164:167], a[132:135], a[200:203], v[164:167]\n"
"v_accvgpr_write_b32 a5, v113\n"
"v_accvgpr_write_b32 a6, v114\n"
"v_mfma_f32_16x16x32_bf16 v[172:175], a[132:135], a[204:207], v[172:175]\n"
"s_waitcnt lgkmcnt(4)\n"
"ds_read_b64_tr_b16 a[174:175], v249 offset:37376\n"
"v_accvgpr_write_b32 a7, v115\n"
"v_mfma_f32_16x16x32_bf16 v[144:147], a[136:139], a[208:211], v[144:147]\n"
"v_cvt_pk_bf16_f32 v120, v120, v121\n"
"v_cvt_pk_bf16_f32 v121, v122, v123\n"
"v_mfma_f32_16x16x32_bf16 v[152:155], a[136:139], a[212:215], v[152:155]\n"
"ds_read_b64_tr_b16 a[176:177], v244 offset:12288\n"
"v_cvt_pk_bf16_f32 v122, v124, v125\n"
"v_mfma_f32_16x16x32_bf16 v[160:163], a[136:139], a[216:219], v[160:163]\n"
"v_cvt_pk_bf16_f32 v123, v126, v127\n"
"v_accvgpr_write_b32 a20, v120\n"
"v_mfma_f32_16x16x32_bf16 v[168:171], a[136:139], a[220:223], v[168:171]\n"
"ds_read_b64_tr_b16 a[178:179], v244 offset:12800\n"
"v_accvgpr_write_b32 a21, v121\n"
"v_mfma_f32_16x16x32_bf16 v[148:151], a[140:143], a[208:211], v[148:151]\n"
"ds_read_b64_tr_b16 a[180:181], v245 offset:12288\n"
"v_accvgpr_write_b32 a22, v122\n"
"v_mfma_f32_16x16x32_bf16 v[156:159], a[140:143], a[212:215], v[156:159]\n"
"v_accvgpr_write_b32 a23, v123\n"
"v_mfma_f32_16x16x32_bf16 v[164:167], a[140:143], a[216:219], v[164:167]\n"
"ds_read_b64_tr_b16 a[182:183], v245 offset:12800\n"
"v_cvt_pk_bf16_f32 v128, v128, v129\n"
"v_mfma_f32_16x16x32_bf16 v[172:175], a[140:143], a[220:223], v[172:175]\n"
"ds_read_b64_tr_b16 a[184:185], v244 offset:36864\n"
"v_cvt_pk_bf16_f32 v129, v130, v131\n"
"v_mfma_f32_16x16x32_bf16 v[144:147], a[144:147], a[224:227], v[144:147]\n"
"v_cvt_pk_bf16_f32 v130, v132, v133\n"
"v_cvt_pk_bf16_f32 v131, v134, v135\n"
"v_mfma_f32_16x16x32_bf16 v[152:155], a[144:147], a[228:231], v[152:155]\n"
"ds_read_b64_tr_b16 a[186:187], v244 offset:37376\n"
"v_accvgpr_write_b32 a36, v128\n"
"v_mfma_f32_16x16x32_bf16 v[160:163], a[144:147], a[232:235], v[160:163]\n"
"ds_read_b64_tr_b16 a[188:189], v245 offset:36864\n"
"v_accvgpr_write_b32 a37, v129\n"
"v_mfma_f32_16x16x32_bf16 v[168:171], a[144:147], a[236:239], v[168:171]\n"
"v_accvgpr_write_b32 a38, v130\n"
"v_accvgpr_write_b32 a39, v131\n"
"v_mfma_f32_16x16x32_bf16 v[148:151], a[148:151], a[224:227], v[148:151]\n"
"ds_read_b64_tr_b16 a[190:191], v245 offset:37376\n"
"v_cvt_pk_bf16_f32 v136, v136, v137\n"
"v_mfma_f32_16x16x32_bf16 v[156:159], a[148:151], a[228:231], v[156:159]\n"
"v_cvt_pk_bf16_f32 v137, v138, v139\n"
"v_cvt_pk_bf16_f32 v138, v140, v141\n"
"v_mfma_f32_16x16x32_bf16 v[164:167], a[148:151], a[232:235], v[164:167]\n"
"v_cvt_pk_bf16_f32 v139, v142, v143\n"
"v_accvgpr_write_b32 a52, v136\n"
"v_mfma_f32_16x16x32_bf16 v[172:175], a[148:151], a[236:239], v[172:175]\n"
"v_accvgpr_write_b32 a53, v137\n"
"v_accvgpr_write_b32 a54, v138\n"
"v_mfma_f32_16x16x32_bf16 v[144:147], a[152:155], a[240:243], v[144:147]\n"
"v_accvgpr_write_b32 a55, v139\n"
"v_mfma_f32_16x16x32_bf16 v[152:155], a[152:155], a[244:247], v[152:155]\n"
"v_mfma_f32_16x16x32_bf16 v[160:163], a[152:155], a[248:251], v[160:163]\n"
"v_mfma_f32_16x16x32_bf16 v[168:171], a[152:155], a[252:255], v[168:171]\n"
"v_mfma_f32_16x16x32_bf16 v[148:151], a[156:159], a[240:243], v[148:151]\n"
"v_mfma_f32_16x16x32_bf16 v[156:159], a[156:159], a[244:247], v[156:159]\n"
"v_mfma_f32_16x16x32_bf16 v[164:167], a[156:159], a[248:251], v[164:167]\n"
"v_mfma_f32_16x16x32_bf16 v[172:175], a[156:159], a[252:255], v[172:175]\n"
"v_mfma_f32_16x16x32_bf16 v[176:179], a[160:163], a[192:195], v[176:179]\n"
"s_waitcnt lgkmcnt(0)\n"
"v_mfma_f32_16x16x32_bf16 v[180:183], a[164:167], a[192:195], v[180:183]\n"
"v_mfma_f32_16x16x32_bf16 v[176:179], a[168:171], a[208:211], v[176:179]\n"
"v_cvt_pk_bf16_f32 v144, v144, v145\n"
"v_cvt_pk_bf16_f32 v145, v146, v147\n"
"v_mfma_f32_16x16x32_bf16 v[180:183], a[172:175], a[208:211], v[180:183]\n"
"v_cvt_pk_bf16_f32 v146, v148, v149\n"
"v_cvt_pk_bf16_f32 v147, v150, v151\n"
"v_mfma_f32_16x16x32_bf16 v[176:179], a[176:179], a[224:227], v[176:179]\n"
"v_mov_b32 v255, 0x3f803f80\n"
"v_accvgpr_write_b32 a8, v144\n"
"v_mfma_f32_16x16x32_bf16 v[180:183], a[180:183], a[224:227], v[180:183]\n"
"v_accvgpr_write_b32 a9, v145\n"
"v_accvgpr_write_b32 a128, v255\n"
"v_mfma_f32_16x16x32_bf16 v[176:179], a[184:187], a[240:243], v[176:179]\n"
"v_accvgpr_write_b32 a129, v255\n"
"v_accvgpr_write_b32 a130, v255\n"
"v_mfma_f32_16x16x32_bf16 v[180:183], a[188:191], a[240:243], v[180:183]\n"
"v_accvgpr_write_b32 a131, v255\n"
"v_accvgpr_write_b32 a10, v146\n"
"v_mfma_f32_16x16x32_bf16 v[184:187], a[160:163], a[196:199], v[184:187]\n"
"v_accvgpr_write_b32 a11, v147\n"
"v_cvt_pk_bf16_f32 v152, v152, v153\n"
"v_mfma_f32_16x16x32_bf16 v[188:191], a[164:167], a[196:199], v[188:191]\n"
"v_cvt_pk_bf16_f32 v153, v154, v155\n"
"v_cvt_pk_bf16_f32 v154, v156, v157\n"
"v_mfma_f32_16x16x32_bf16 v[184:187], a[168:171], a[212:215], v[184:187]\n"
"v_cvt_pk_bf16_f32 v155, v158, v159\n"
"v_accvgpr_write_b32 a24, v152\n"
"v_mfma_f32_16x16x32_bf16 v[188:191], a[172:175], a[212:215], v[188:191]\n"
"v_accvgpr_write_b32 a25, v153\n"
"v_cvt_pk_bf16_f32 v176, v176, v177\n"
"v_mfma_f32_16x16x32_bf16 v[184:187], a[176:179], a[228:231], v[184:187]\n"
"v_cvt_pk_bf16_f32 v177, v178, v179\n"
"v_accvgpr_write_b32 a26, v154\n"
"v_mfma_f32_16x16x32_bf16 v[188:191], a[180:183], a[228:231], v[188:191]\n"
"v_accvgpr_write_b32 a27, v155\n"
"v_cvt_pk_bf16_f32 v178, v180, v181\n"
"v_mfma_f32_16x16x32_bf16 v[184:187], a[184:187], a[244:247], v[184:187]\n"
"v_cvt_pk_bf16_f32 v179, v182, v183\n"
"v_accvgpr_write_b32 a12, v176\n"
"v_mfma_f32_16x16x32_bf16 v[188:191], a[188:191], a[244:247], v[188:191]\n"
"v_accvgpr_write_b32 a13, v177\n"
"v_accvgpr_write_b32 a14, v178\n"
"v_mfma_f32_16x16x32_bf16 v[192:195], a[160:163], a[200:203], v[192:195]\n"
"v_accvgpr_write_b32 a15, v179\n"
"v_cvt_pk_bf16_f32 v160, v160, v161\n"
"v_mfma_f32_16x16x32_bf16 v[196:199], a[164:167], a[200:203], v[196:199]\n"
"s_waitcnt vmcnt(10)\n"
"s_barrier\n"
"v_mfma_f32_16x16x32_bf16 v[192:195], a[168:171], a[216:219], v[192:195]\n"
"ds_read_b128 a[64:67], v226 offset:0\n"
"v_cvt_pk_bf16_f32 v161, v162, v163\n"
"v_mfma_f32_16x16x32_bf16 v[196:199], a[172:175], a[216:219], v[196:199]\n"
"v_cvt_pk_bf16_f32 v162, v164, v165\n"
"v_cvt_pk_bf16_f32 v163, v166, v167\n"
"v_mfma_f32_16x16x32_bf16 v[192:195], a[176:179], a[232:235], v[192:195]\n"
"ds_read_b128 a[68:71], v226 offset:1024\n"
"v_accvgpr_write_b32 a40, v160\n"
"v_mfma_f32_16x16x32_bf16 v[196:199], a[180:183], a[232:235], v[196:199]\n"
"v_accvgpr_write_b32 a41, v161\n"
"v_accvgpr_write_b32 a42, v162\n"
"v_mfma_f32_16x16x32_bf16 v[192:195], a[184:187], a[248:251], v[192:195]\n"
"ds_read_b128 a[72:75], v226 offset:2048\n"
"v_accvgpr_write_b32 a43, v163\n"
"v_mfma_f32_16x16x32_bf16 v[196:199], a[188:191], a[248:251], v[196:199]\n"
"v_cvt_pk_bf16_f32 v184, v184, v185\n"
"v_cvt_pk_bf16_f32 v185, v186, v187\n"
"v_mfma_f32_16x16x32_bf16 v[200:203], a[160:163], a[204:207], v[200:203]\n"
"ds_read_b128 a[76:79], v226 offset:3072\n"
"v_cvt_pk_bf16_f32 v186, v188, v189\n"
"v_mfma_f32_16x16x32_bf16 v[204:207], a[164:167], a[204:207], v[204:207]\n"
"v_cvt_pk_bf16_f32 v187, v190, v191\n"
"v_cvt_pk_bf16_f32 v168, v168, v169\n"
"v_mfma_f32_16x16x32_bf16 v[200:203], a[168:171], a[220:223], v[200:203]\n"
"ds_read_b128 a[80:83], v226 offset:4096\n"
"v_cvt_pk_bf16_f32 v169, v170, v171\n"
"v_mfma_f32_16x16x32_bf16 v[204:207], a[172:175], a[220:223], v[204:207]\n"
"v_accvgpr_write_b32 a28, v184\n"
"v_accvgpr_write_b32 a29, v185\n"
"v_mfma_f32_16x16x32_bf16 v[200:203], a[176:179], a[236:239], v[200:203]\n"
"ds_read_b128 a[84:87], v226 offset:5120\n"
"v_cvt_pk_bf16_f32 v170, v172, v173\n"
"v_mfma_f32_16x16x32_bf16 v[204:207], a[180:183], a[236:239], v[204:207]\n"
"v_cvt_pk_bf16_f32 v171, v174, v175\n"
"v_accvgpr_write_b32 a30, v186\n"
"v_mfma_f32_16x16x32_bf16 v[200:203], a[184:187], a[252:255], v[200:203]\n"
"v_accvgpr_write_b32 a31, v187\n"
"v_accvgpr_write_b32 a56, v168\n"
"v_mfma_f32_16x16x32_bf16 v[204:207], a[188:191], a[252:255], v[204:207]\n"
"v_accvgpr_write_b32 a57, v169\n"
"v_accvgpr_write_b32 a58, v170\n"
"v_accvgpr_write_b32 a59, v171\n"
"s_cmp_eq_u32 s34, 0\n"
"s_cbranch_scc1 LFA_%=\n"
"s_waitcnt lgkmcnt(5)\n"
"v_mfma_f32_16x16x32_bf16 v[0:3], a[64:67], a[0:3], 0\n"
"s_mov_b32 m0, s40\n"
"v_cvt_pk_bf16_f32 v192, v192, v193\n"
"v_cvt_pk_bf16_f32 v193, v194, v195\n"
"v_mfma_f32_16x16x32_bf16 v[8:11], a[64:67], a[16:19], 0\n"
"v_readlane_b32 s43, v228, s42\n"
"v_cvt_pk_bf16_f32 v194, v196, v197\n"
"v_mfma_f32_16x16x32_bf16 v[16:19], a[64:67], a[32:35], 0\n"
"v_cvt_pk_bf16_f32 v195, v198, v199\n"
"v_readlane_b32 s44, v229, s42\n"
"v_mfma_f32_16x16x32_bf16 v[24:27], a[64:67], a[48:51], 0\n"
"s_waitcnt lgkmcnt(0)\n"
"buffer_load_dwordx4 v225, s[8:11], s43 offen offset:0 sc1 lds\n"
"v_mfma_f32_16x16x32_bf16 v[0:3], a[68:71], a[4:7], v[0:3]\n"
"s_add_u32 s42, s42, 1\n"
"v_accvgpr_write_b32 a44, v192\n"
"v_accvgpr_write_b32 a45, v193\n"
"v_mfma_f32_16x16x32_bf16 v[8:11], a[68:71], a[20:23], v[8:11]\n"
"v_accvgpr_write_b32 a46, v194\n"
"v_accvgpr_write_b32 a47, v195\n"
"v_mfma_f32_16x16x32_bf16 v[16:19], a[68:71], a[36:39], v[16:19]\n"
"v_cvt_pk_bf16_f32 v200, v200, v201\n"
"v_cvt_pk_bf16_f32 v201, v202, v203\n"
"v_mfma_f32_16x16x32_bf16 v[24:27], a[68:71], a[52:55], v[24:27]\n"
"v_cvt_pk_bf16_f32 v202, v204, v205\n"
"v_cvt_pk_bf16_f32 v203, v206, v207\n"
"v_mov_b32 v227, v226\n"
"v_mfma_f32_16x16x32_bf16 v[0:3], a[72:75], a[8:11], v[0:3]\n"
"buffer_load_dwordx4 v225, s[8:11], s43 offen offset:1024 sc1 lds\n"
"v_mfma_f32_16x16x32_bf16 v[8:11], a[72:75], a[24:27], v[8:11]\n"
"ds_read_b128 a[88:91], v226 offset:6144\n"
"v_accvgpr_write_b32 a60, v200\n"
"v_accvgpr_write_b32 a61, v201\n"
"v_mfma_f32_16x16x32_bf16 v[16:19], a[72:75], a[40:43], v[16:19]\n"
"s_lshl_b32 s59, s34, 5\n"
"v_accvgpr_write_b32 a62, v202\n"
"v_accvgpr_write_b32 a63, v203\n"
"v_mfma_f32_16x16x32_bf16 v[24:27], a[72:75], a[56:59], v[24:27]\n"
"s_sub_u32 s60, 480, s59\n"
"v_mfma_f32_16x16x32_bf16 v[0:3], a[76:79], a[12:15], v[0:3]\n"
"s_add_u32 s59, s59, s35\n"
"v_mfma_f32_16x16x32_bf16 v[8:11], a[76:79], a[28:31], v[8:11]\n"
"s_add_u32 s60, s60, s35\n"
"ds_read_b128 a[92:95], v226 offset:7168\n"
"v_mfma_f32_16x16x32_bf16 v[16:19], a[76:79], a[44:47], v[16:19]\n"
"s_lshl_b32 s59, s59, 15\n"
"buffer_load_dwordx4 v225, s[8:11], s43 offen offset:2048 sc1 lds\n"
"v_mfma_f32_16x16x32_bf16 v[24:27], a[76:79], a[60:63], v[24:27]\n"
"s_lshl_b32 s60, s60, 15\n"
"v_mfma_f32_16x16x32_bf16 v[4:7], a[80:83], a[0:3], 0\n"
"s_add_u32 s48, s36, 44040192\n"
"v_exp_f32 v0, v0\n"
"v_mfma_f32_16x16x32_bf16 v[12:15], a[80:83], a[16:19], 0\n"
"s_add_u32 s59, s59, s48\n"
"ds_read_b128 a[96:99], v226 offset:8192\n"
"v_mfma_f32_16x16x32_bf16 v[20:23], a[80:83], a[32:35], 0\n"
"s_add_u32 s60, s60, s48\n"
"v_exp_f32 v1, v1\n"
"v_mfma_f32_16x16x32_bf16 v[28:31], a[80:83], a[48:51], 0\n"
"s_lshl_b32 s61, s34, 3\n"
"v_exp_f32 v2, v2\n"
"v_mfma_f32_16x16x32_bf16 v[4:7], a[84:87], a[4:7], v[4:7]\n"
"s_add_u32 s61, s61, s32\n"
"buffer_load_dwordx4 v225, s[8:11], s43 offen offset:3072 sc1 lds\n"
"v_mfma_f32_16x16x32_bf16 v[12:15], a[84:87], a[20:23], v[12:15]\n"
"s_lshl_b32 s61, s61, 2\n"
"v_exp_f32 v3, v3\n"
"v_mfma_f32_16x16x32_bf16 v[20:23], a[84:87], a[36:39], v[20:23]\n"
"s_add_u32 s61, s61, s30\n"
"ds_read_b128 a[100:103], v226 offset:9216\n"
"v_cvt_pk_bf16_f32 v0, v0, v1\n"
"v_mfma_f32_16x16x32_bf16 v[28:31], a[84:87], a[52:55], v[28:31]\n"
"s_waitcnt lgkmcnt(2)\n"
"v_exp_f32 v8, v8\n"
"v_mfma_f32_16x16x32_bf16 v[4:7], a[88:91], a[8:11], v[4:7]\n"
"s_add_u32 m0, s40, 16384\n"
"v_exp_f32 v9, v9\n"
"v_cvt_pk_bf16_f32 v1, v2, v3\n"
"v_mfma_f32_16x16x32_bf16 v[12:15], a[88:91], a[24:27], v[12:15]\n"
"s_lshl_b32 s61, s61, 2\n"
"v_exp_f32 v10, v10\n"
"v_mfma_f32_16x16x32_bf16 v[20:23], a[88:91], a[40:43], v[20:23]\n"
"s_add_u32 s61, s61, s33\n"
"ds_read_b128 a[104:107], v226 offset:10240\n"
"v_cvt_pk_bf16_f32 v8, v8, v9\n"
"v_mfma_f32_16x16x32_bf16 v[28:31], a[88:91], a[56:59], v[28:31]\n"
"s_lshl_b32 s61, s61, 3\n"
"buffer_load_dwordx4 v225, s[8:11], s44 offen offset:0 sc1 lds\n"
"v_mfma_f32_16x16x32_bf16 v[4:7], a[92:95], a[12:15], v[4:7]\n"
"s_add_u32 s61, s61, 6912\n"
"v_exp_f32 v11, v11\n"
"v_mfma_f32_16x16x32_bf16 v[12:15], a[92:95], a[28:31], v[12:15]\n"
"s_lshr_b32 s48, s31, 3\n"
"v_exp_f32 v16, v16\n"
"v_mfma_f32_16x16x32_bf16 v[20:23], a[92:95], a[44:47], v[20:23]\n"
"s_add_u32 s40, s40, 32768\n"
"v_exp_f32 v17, v17\n"
"v_cvt_pk_bf16_f32 v9, v10, v11\n"
"v_mfma_f32_16x16x32_bf16 v[28:31], a[92:95], a[60:63], v[28:31]\n"
"s_waitcnt lgkmcnt(1)\n"
"ds_read_b128 a[108:111], v226 offset:11264\n"
"v_cvt_pk_bf16_f32 v16, v16, v17\n"
"v_mfma_f32_16x16x32_bf16 v[32:35], a[96:99], a[0:3], 0\n"
"s_add_u32 s58, s32, 32\n"
"v_exp_f32 v18, v18\n"
"v_mfma_f32_16x16x32_bf16 v[40:43], a[96:99], a[16:19], 0\n"
"s_cmp_eq_u32 s40, s46\n"
"v_exp_f32 v19, v19\n"
"v_mfma_f32_16x16x32_bf16 v[48:51], a[96:99], a[32:35], 0\n"
"s_cselect_b32 s40, s47, s40\n"
"buffer_load_dwordx4 v225, s[8:11], s44 offen offset:1024 sc1 lds\n"
"v_mfma_f32_16x16x32_bf16 v[56:59], a[96:99], a[48:51], 0\n"
"s_cmp_eq_u32 s48, 5\n"
"v_exp_f32 v24, v24\n"
"v_cvt_pk_bf16_f32 v17, v18, v19\n"
"v_mfma_f32_16x16x32_bf16 v[32:35], a[100:103], a[4:7], v[32:35]\n"
"s_cselect_b32 s58, s58, 99\n"
"ds_read_b128 a[112:115], v226 offset:12288\n"
"s_cmp_eq_u32 s34, 6\n"
"v_mfma_f32_16x16x32_bf16 v[40:43], a[100:103], a[20:23], v[40:43]\n"
"s_cselect_b32 s58, s35, s58\n"
"v_exp_f32 v25, v25\n"
"v_mfma_f32_16x16x32_bf16 v[48:51], a[100:103], a[36:39], v[48:51]\n"
"s_lshl_b32 s56, s34, 1\n"
"v_exp_f32 v26, v26\n"
"v_cvt_pk_bf16_f32 v24, v24, v25\n"
"v_mfma_f32_16x16x32_bf16 v[56:59], a[100:103], a[52:55], v[56:59]\n"
"s_waitcnt lgkmcnt(1)\n"
"v_exp_f32 v27, v27\n"
"v_mfma_f32_16x16x32_bf16 v[32:35], a[104:107], a[8:11], v[32:35]\n"
"s_sub_u32 s56, s56, 2\n"
"ds_read_b128 a[116:119], v226 offset:13312\n"
"v_cvt_pk_bf16_f32 v25, v26, v27\n"
"v_mfma_f32_16x16x32_bf16 v[40:43], a[104:107], a[24:27], v[40:43]\n"
"s_cmp_ge_u32 s34, 5\n"
"buffer_load_dwordx4 v225, s[8:11], s44 offen offset:2048 sc1 lds\n"
"v_mfma_f32_16x16x32_bf16 v[48:51], a[104:107], a[40:43], v[48:51]\n"
"s_cselect_b32 s56, 1, s56\n"
"v_exp_f32 v4, v4\n"
"v_mfma_f32_16x16x32_bf16 v[56:59], a[104:107], a[56:59], v[56:59]\n"
"s_cmp_ge_u32 s34, 5\n"
"v_exp_f32 v5, v5\n"
"v_mfma_f32_16x16x32_bf16 v[32:35], a[108:111], a[12:15], v[32:35]\n"
"s_cselect_b32 s57, 1, 0\n"
"v_exp_f32 v6, v6\n"
"v_cvt_pk_bf16_f32 v2, v4, v5\n"
"v_mfma_f32_16x16x32_bf16 v[40:43], a[108:111], a[28:31], v[40:43]\n"
"s_lshl_b32 s48, s35, 3\n"
"ds_read_b128 a[120:123], v226 offset:14336\n"
"ds_read_b128 a[124:127], v226 offset:15360\n"
"v_readlane_b32 s43, v228, s42\n"
"v_mfma_f32_16x16x32_bf16 v[48:51], a[108:111], a[44:47], v[48:51]\n"
"s_add_u32 s48, s48, s34\n"
"v_exp_f32 v7, v7\n"
"v_mfma_f32_16x16x32_bf16 v[56:59], a[108:111], a[60:63], v[56:59]\n"
"s_waitcnt lgkmcnt(2)\n"
"v_exp_f32 v12, v12\n"
"v_cvt_pk_bf16_f32 v3, v6, v7\n"
"v_mfma_f32_16x16x32_bf16 v[36:39], a[112:115], a[0:3], 0\n"
"s_lshl_b32 s48, s48, 3\n"
"buffer_load_dwordx4 v225, s[8:11], s44 offen offset:3072 sc1 lds\n"
"v_mfma_f32_16x16x32_bf16 v[44:47], a[112:115], a[16:19], 0\n"
"s_add_u32 s48, s48, 4352\n"
"v_exp_f32 v13, v13\n"
"v_mfma_f32_16x16x32_bf16 v[52:55], a[112:115], a[32:35], 0\n"
"s_add_u32 s48, s48, s45\n"
"v_exp_f32 v14, v14\n"
"v_cvt_pk_bf16_f32 v10, v12, v13\n"
"v_mfma_f32_16x16x32_bf16 v[60:63], a[112:115], a[48:51], 0\n"
"s_mov_b32 m0, s40\n"
"v_exp_f32 v15, v15\n"
"v_readlane_b32 s44, v229, s42\n"
"v_mfma_f32_16x16x32_bf16 v[36:39], a[116:119], a[4:7], v[36:39]\n"
"s_add_u32 s42, s42, 1\n"
"v_exp_f32 v20, v20\n"
"v_cvt_pk_bf16_f32 v11, v14, v15\n"
"v_mfma_f32_16x16x32_bf16 v[44:47], a[116:119], a[20:23], v[44:47]\n"
"ds_read_b128 a[96:99], v227 offset:24576\n"
"ds_read_b128 a[100:103], v227 offset:25600\n"
"v_mfma_f32_16x16x32_bf16 v[52:55], a[116:119], a[36:39], v[52:55]\n"
"buffer_load_dwordx4 v225, s[8:11], s43 offen offset:0 sc1 lds\n"
"v_mfma_f32_16x16x32_bf16 v[60:63], a[116:119], a[52:55], v[60:63]\n"
"s_waitcnt lgkmcnt(2)\n"
"v_exp_f32 v21, v21\n"
"v_mfma_f32_16x16x32_bf16 v[36:39], a[120:123], a[8:11], v[36:39]\n"
"v_exp_f32 v22, v22\n"
"v_cvt_pk_bf16_f32 v18, v20, v21\n"
"v_mfma_f32_16x16x32_bf16 v[44:47], a[120:123], a[24:27], v[44:47]\n"
"v_exp_f32 v23, v23\n"
"v_mfma_f32_16x16x32_bf16 v[52:55], a[120:123], a[40:43], v[52:55]\n"
"buffer_load_dwordx4 v225, s[8:11], s43 offen offset:1024 sc1 lds\n"
"v_cvt_pk_bf16_f32 v19, v22, v23\n"
"v_mfma_f32_16x16x32_bf16 v[60:63], a[120:123], a[56:59], v[60:63]\n"
"v_exp_f32 v28, v28\n"
"v_mfma_f32_16x16x32_bf16 v[36:39], a[124:127], a[12:15], v[36:39]\n"
"ds_read_b128 a[104:107], v227 offset:26624\n"
"v_mfma_f32_16x16x32_bf16 v[44:47], a[124:127], a[28:31], v[44:47]\n"
"v_exp_f32 v29, v29\n"
"v_mfma_f32_16x16x32_bf16 v[52:55], a[124:127], a[44:47], v[52:55]\n"
"v_exp_f32 v30, v30\n"
"v_cvt_pk_bf16_f32 v26, v28, v29\n"
"v_mfma_f32_16x16x32_bf16 v[60:63], a[124:127], a[60:63], v[60:63]\n"
"s_waitcnt lgkmcnt(1)\n"
"v_exp_f32 v31, v31\n"
"v_mfma_f32_16x16x32_bf16 v[80:83], a[96:99], v[0:3], 0\n"
"s_add_u32 s41, s41, 32768\n"
"v_cvt_pk_bf16_f32 v27, v30, v31\n"
"s_cmp_eq_u32 s41, 163840\n"
"v_mfma_f32_16x16x32_bf16 v[112:115], a[96:99], v[8:11], 0\n"
"s_cselect_b32 s41, 0, s41\n"
"buffer_load_dwordx4 v225, s[8:11], s43 offen offset:2048 sc1 lds\n"
"v_mfma_f32_16x16x32_bf16 v[144:147], a[96:99], v[16:19], 0\n"
"ds_read_b128 a[108:111], v227 offset:27648\n"
"v_add_u32 v226, s41, v224\n"
"v_mfma_f32_16x16x32_bf16 v[176:179], a[96:99], v[24:27], 0\n"
"v_exp_f32 v32, v32\n"
"v_mfma_f32_16x16x32_bf16 v[84:87], a[100:103], v[0:3], 0\n"
"v_exp_f32 v33, v33\n"
"v_mfma_f32_16x16x32_bf16 v[116:119], a[100:103], v[8:11], 0\n"
"v_exp_f32 v34, v34\n"
"v_cvt_pk_bf16_f32 v64, v32, v33\n"
"v_mfma_f32_16x16x32_bf16 v[148:151], a[100:103], v[16:19], 0\n"
"ds_read_b128 a[112:115], v227 offset:28672\n"
"ds_read_b128 a[116:119], v227 offset:29696\n"
"v_mfma_f32_16x16x32_bf16 v[180:183], a[100:103], v[24:27], 0\n"
"s_waitcnt lgkmcnt(3)\n"
"v_exp_f32 v35, v35\n"
"v_mfma_f32_16x16x32_bf16 v[88:91], a[104:107], v[0:3], 0\n"
"buffer_load_dwordx4 v225, s[8:11], s43 offen offset:3072 sc1 lds\n"
"v_cvt_pk_bf16_f32 v65, v34, v35\n"
"v_mfma_f32_16x16x32_bf16 v[120:123], a[104:107], v[8:11], 0\n"
"s_add_u32 m0, s40, 16384\n"
"v_exp_f32 v40, v40\n"
"v_mfma_f32_16x16x32_bf16 v[152:155], a[104:107], v[16:19], 0\n"
"s_add_u32 s40, s40, 32768\n"
"v_exp_f32 v41, v41\n"
"v_mfma_f32_16x16x32_bf16 v[184:187], a[104:107], v[24:27], 0\n"
"s_waitcnt lgkmcnt(2)\n"
"v_exp_f32 v42, v42\n"
"v_cvt_pk_bf16_f32 v68, v40, v41\n"
"v_mfma_f32_16x16x32_bf16 v[92:95], a[108:111], v[0:3], 0\n"
"s_cmp_eq_u32 s40, s46\n"
"v_exp_f32 v43, v43\n"
"v_mfma_f32_16x16x32_bf16 v[124:127], a[108:111], v[8:11], 0\n"
"s_cselect_b32 s40, s47, s40\n"
"buffer_load_dwordx4 v225, s[8:11], s44 offen offset:0 sc1 lds\n"
"v_cvt_pk_bf16_f32 v69, v42, v43\n"
"v_mfma_f32_16x16x32_bf16 v[156:159], a[108:111], v[16:19], 0\n"
"ds_read_b128 a[120:123], v227 offset:30720\n"
"v_mfma_f32_16x16x32_bf16 v[188:191], a[108:111], v[24:27], 0\n"
"s_waitcnt lgkmcnt(1)\n"
"v_exp_f32 v48, v48\n"
"v_mfma_f32_16x16x32_bf16 v[96:99], a[112:115], v[0:3], 0\n"
"v_exp_f32 v49, v49\n"
"v_mfma_f32_16x16x32_bf16 v[128:131], a[112:115], v[8:11], 0\n"
"v_exp_f32 v50, v50\n"
"v_cvt_pk_bf16_f32 v72, v48, v49\n"
"v_mfma_f32_16x16x32_bf16 v[160:163], a[112:115], v[16:19], 0\n"
"ds_read_b128 a[124:127], v227 offset:31744\n"
"v_mfma_f32_16x16x32_bf16 v[192:195], a[112:115], v[24:27], 0\n"
"v_exp_f32 v51, v51\n"
"v_mfma_f32_16x16x32_bf16 v[100:103], a[116:119], v[0:3], 0\n"
"buffer_load_dwordx4 v225, s[8:11], s44 offen offset:1024 sc1 lds\n"
"v_cvt_pk_bf16_f32 v73, v50, v51\n"
"v_mfma_f32_16x16x32_bf16 v[132:135], a[116:119], v[8:11], 0\n"
"v_exp_f32 v36, v36\n"
"v_mfma_f32_16x16x32_bf16 v[164:167], a[116:119], v[16:19], 0\n"
"v_exp_f32 v37, v37\n"
"v_mfma_f32_16x16x32_bf16 v[196:199], a[116:119], v[24:27], 0\n"
"s_waitcnt lgkmcnt(1)\n"
"v_exp_f32 v38, v38\n"
"v_cvt_pk_bf16_f32 v66, v36, v37\n"
"v_mfma_f32_16x16x32_bf16 v[104:107], a[120:123], v[0:3], 0\n"
"v_exp_f32 v39, v39\n"
"v_mfma_f32_16x16x32_bf16 v[136:139], a[120:123], v[8:11], 0\n"
"s_waitcnt vmcnt(14)\n"
"s_barrier\n"
"v_mfma_f32_16x16x32_bf16 v[168:171], a[120:123], v[16:19], 0\n"
"buffer_load_dwordx4 v225, s[8:11], s44 offen offset:2048 sc1 lds\n"
"v_cvt_pk_bf16_f32 v67, v38, v39\n"
"v_mfma_f32_16x16x32_bf16 v[200:203], a[120:123], v[24:27], 0\n"
"s_waitcnt lgkmcnt(0)\n"
"ds_read_b128 a[64:67], v226 offset:0\n"
"v_mfma_f32_16x16x32_bf16 v[108:111], a[124:127], v[0:3], 0\n"
"buffer_store_dwordx2 v[232:233], v230, s[8:11], s48 offen sc1\n"
"v_mfma_f32_16x16x32_bf16 v[140:143], a[124:127], v[8:11], 0\n"
"v_exp_f32 v44, v44\n"
"v_mfma_f32_16x16x32_bf16 v[172:175], a[124:127], v[16:19], 0\n"
"v_exp_f32 v45, v45\n"
"v_mfma_f32_16x16x32_bf16 v[204:207], a[124:127], v[24:27], 0\n"
"v_exp_f32 v46, v46\n"
"v_cvt_pk_bf16_f32 v70, v44, v45\n"
"v_mfma_f32_16x16x32_bf16 a[132:135], a[128:131], v[0:3], 0\n"
"ds_read_b128 a[68:71], v226 offset:1024\n"
"ds_read_b128 a[72:75], v226 offset:2048\n"
"v_mfma_f32_16x16x32_bf16 a[136:139], a[128:131], v[8:11], 0\n"
"v_exp_f32 v47, v47\n"
"v_mfma_f32_16x16x32_bf16 a[140:143], a[128:131], v[16:19], 0\n"
"v_exp_f32 v52, v52\n"
"v_cvt_pk_bf16_f32 v71, v46, v47\n"
"v_mfma_f32_16x16x32_bf16 a[144:147], a[128:131], v[24:27], 0\n"
"v_exp_f32 v53, v53\n"
"buffer_load_dwordx4 v225, s[8:11], s44 offen offset:3072 sc1 lds\n"
"v_cvt_pk_bf16_f32 v74, v52, v53\n"
"v_exp_f32 v54, v54\n"
"v_exp_f32 v55, v55\n"
"s_nop 0\n"
"v_cvt_pk_bf16_f32 v75, v54, v55\n"
"ds_read_b128 a[76:79], v226 offset:3072\n"
"ds_read_b128 a[80:83], v226 offset:4096\n"
"ds_read_b128 a[84:87], v226 offset:5120\n"
".p2align 6\n"
"LP1_%=:\n"
"s_waitcnt lgkmcnt(5)\n"
"v_mfma_f32_16x16x32_bf16 v[0:3], a[64:67], a[0:3], 0\n"
"v_exp_f32 v56, v56\n"
"v_mfma_f32_16x16x32_bf16 v[8:11], a[64:67], a[16:19], 0\n"
"v_readlane_b32 s43, v228, s42\n"
"v_mfma_f32_16x16x32_bf16 v[16:19], a[64:67], a[32:35], 0\n"
"v_exp_f32 v57, v57\n"
"v_mfma_f32_16x16x32_bf16 v[24:27], a[64:67], a[48:51], 0\n"
"s_waitcnt lgkmcnt(4)\n"
"v_cvt_pk_bf16_f32 v76, v56, v57\n"
"s_mov_b32 m0, s40\n"
"v_mfma_f32_16x16x32_bf16 v[0:3], a[68:71], a[4:7], v[0:3]\n"
"v_exp_f32 v58, v58\n"
"v_mfma_f32_16x16x32_bf16 v[8:11], a[68:71], a[20:23], v[8:11]\n"
"v_readlane_b32 s44, v229, s42\n"
"v_mov_b32 v227, v226\n"
"v_mfma_f32_16x16x32_bf16 v[16:19], a[68:71], a[36:39], v[16:19]\n"
"v_exp_f32 v59, v59\n"
"v_mfma_f32_16x16x32_bf16 v[24:27], a[68:71], a[52:55], v[24:27]\n"
"s_waitcnt lgkmcnt(0)\n"
"buffer_load_dwordx4 v225, s[8:11], s43 offen offset:0 sc1 lds\n"
"v_mfma_f32_16x16x32_bf16 v[0:3], a[72:75], a[8:11], v[0:3]\n"
"s_add_u32 s42, s42, 1\n"
"v_exp_f32 v60, v60\n"
"v_mfma_f32_16x16x32_bf16 v[8:11], a[72:75], a[24:27], v[8:11]\n"
"ds_read_b128 a[88:91], v226 offset:6144\n"
"v_cvt_pk_bf16_f32 v77, v58, v59\n"
"v_mfma_f32_16x16x32_bf16 v[16:19], a[72:75], a[40:43], v[16:19]\n"
"v_exp_f32 v61, v61\n"
"v_mfma_f32_16x16x32_bf16 v[24:27], a[72:75], a[56:59], v[24:27]\n"
"v_cvt_pk_bf16_f32 v78, v60, v61\n"
"v_mfma_f32_16x16x32_bf16 v[0:3], a[76:79], a[12:15], v[0:3]\n"
"v_exp_f32 v62, v62\n"
"v_mfma_f32_16x16x32_bf16 v[8:11], a[76:79], a[28:31], v[8:11]\n"
"ds_read_b128 a[92:95], v226 offset:7168\n"
"v_mfma_f32_16x16x32_bf16 v[16:19], a[76:79], a[44:47], v[16:19]\n"
"v_exp_f32 v63, v63\n"
"v_mfma_f32_16x16x32_bf16 v[24:27], a[76:79], a[60:63], v[24:27]\n"
"buffer_load_dwordx4 v225, s[8:11], s43 offen offset:1024 sc1 lds\n"
"v_mfma_f32_16x16x32_bf16 v[4:7], a[80:83], a[0:3], 0\n"
"v_exp_f32 v0, v0\n"
"v_mfma_f32_16x16x32_bf16 v[12:15], a[80:83], a[16:19], 0\n"
"ds_read_b128 a[96:99], v226 offset:8192\n"
"v_cvt_pk_bf16_f32 v79, v62, v63\n"
"v_mfma_f32_16x16x32_bf16 v[20:23], a[80:83], a[32:35], 0\n"
"v_exp_f32 v1, v1\n"
"v_mfma_f32_16x16x32_bf16 v[28:31], a[80:83], a[48:51], 0\n"
"v_cvt_pk_bf16_f32 v0, v0, v1\n"
"v_mfma_f32_16x16x32_bf16 v[4:7], a[84:87], a[4:7], v[4:7]\n"
"v_exp_f32 v2, v2\n"
"v_mfma_f32_16x16x32_bf16 v[12:15], a[84:87], a[20:23], v[12:15]\n"
"ds_read_b128 a[100:103], v226 offset:9216\n"
"v_mfma_f32_16x16x32_bf16 v[20:23], a[84:87], a[36:39], v[20:23]\n"
"v_exp_f32 v3, v3\n"
"v_mfma_f32_16x16x32_bf16 v[28:31], a[84:87], a[52:55], v[28:31]\n"
"s_waitcnt lgkmcnt(2)\n"
"buffer_load_dwordx4 v225, s[8:11], s43 offen offset:2048 sc1 lds\n"
"v_mfma_f32_16x16x32_bf16 v[4:7], a[88:91], a[8:11], v[4:7]\n"
"v_exp_f32 v8, v8\n"
"v_mfma_f32_16x16x32_bf16 v[12:15], a[88:91], a[24:27], v[12:15]\n"
"ds_read_b128 a[104:107], v226 offset:10240\n"
"v_cvt_pk_bf16_f32 v1, v2, v3\n"
"v_mfma_f32_16x16x32_bf16 v[20:23], a[88:91], a[40:43], v[20:23]\n"
"v_exp_f32 v9, v9\n"
"v_mfma_f32_16x16x32_bf16 v[28:31], a[88:91], a[56:59], v[28:31]\n"
"v_cvt_pk_bf16_f32 v8, v8, v9\n"
"v_mfma_f32_16x16x32_bf16 v[4:7], a[92:95], a[12:15], v[4:7]\n"
"v_exp_f32 v10, v10\n"
"v_mfma_f32_16x16x32_bf16 v[12:15], a[92:95], a[28:31], v[12:15]\n"
"ds_read_b128 a[108:111], v226 offset:11264\n"
"v_mfma_f32_16x16x32_bf16 v[20:23], a[92:95], a[44:47], v[20:23]\n"
"v_exp_f32 v11, v11\n"
"v_mfma_f32_16x16x32_bf16 v[28:31], a[92:95], a[60:63], v[28:31]\n"
"s_waitcnt lgkmcnt(2)\n"
"buffer_load_dwordx4 v225, s[8:11], s43 offen offset:3072 sc1 lds\n"
"v_mfma_f32_16x16x32_bf16 v[32:35], a[96:99], a[0:3], 0\n"
"v_exp_f32 v16, v16\n"
"v_mfma_f32_16x16x32_bf16 v[40:43], a[96:99], a[16:19], 0\n"
"ds_read_b128 a[112:115], v226 offset:12288\n"
"v_cvt_pk_bf16_f32 v9, v10, v11\n"
"v_mfma_f32_16x16x32_bf16 v[48:51], a[96:99], a[32:35], 0\n"
"v_exp_f32 v17, v17\n"
"v_mfma_f32_16x16x32_bf16 v[56:59], a[96:99], a[48:51], 0\n"
"s_add_u32 m0, s40, 16384\n"
"v_cvt_pk_bf16_f32 v16, v16, v17\n"
"v_mfma_f32_16x16x32_bf16 v[32:35], a[100:103], a[4:7], v[32:35]\n"
"v_exp_f32 v18, v18\n"
"v_mfma_f32_16x16x32_bf16 v[40:43], a[100:103], a[20:23], v[40:43]\n"
"ds_read_b128 a[116:119], v226 offset:13312\n"
"v_mfma_f32_16x16x32_bf16 v[48:51], a[100:103], a[36:39], v[48:51]\n"
"v_exp_f32 v19, v19\n"
"v_mfma_f32_16x16x32_bf16 v[56:59], a[100:103], a[52:55], v[56:59]\n"
"s_waitcnt lgkmcnt(2)\n"
"buffer_load_dwordx4 v225, s[8:11], s44 offen offset:0 sc1 lds\n"
"v_mfma_f32_16x16x32_bf16 v[32:35], a[104:107], a[8:11], v[32:35]\n"
"s_add_u32 s40, s40, 32768\n"
"v_exp_f32 v24, v24\n"
"v_mfma_f32_16x16x32_bf16 v[40:43], a[104:107], a[24:27], v[40:43]\n"
"s_cmp_eq_u32 s40, s46\n"
"ds_read_b128 a[120:123], v226 offset:14336\n"
"v_cvt_pk_bf16_f32 v17, v18, v19\n"
"v_mfma_f32_16x16x32_bf16 v[48:51], a[104:107], a[40:43], v[48:51]\n"
"s_cselect_b32 s40, s47, s40\n"
"v_exp_f32 v25, v25\n"
"v_mfma_f32_16x16x32_bf16 v[56:59], a[104:107], a[56:59], v[56:59]\n"
"v_cvt_pk_bf16_f32 v24, v24, v25\n"
"v_mfma_f32_16x16x32_bf16 v[32:35], a[108:111], a[12:15], v[32:35]\n"
"v_exp_f32 v26, v26\n"
"v_mfma_f32_16x16x32_bf16 v[40:43], a[108:111], a[28:31], v[40:43]\n"
"ds_read_b128 a[124:127], v226 offset:15360\n"
"v_mfma_f32_16x16x32_bf16 v[48:51], a[108:111], a[44:47], v[48:51]\n"
"v_exp_f32 v27, v27\n"
"v_mfma_f32_16x16x32_bf16 v[56:59], a[108:111], a[60:63], v[56:59]\n"
"s_waitcnt lgkmcnt(2)\n"
"buffer_load_dwordx4 v225, s[8:11], s44 offen offset:1024 sc1 lds\n"
"v_mfma_f32_16x16x32_bf16 v[36:39], a[112:115], a[0:3], 0\n"
"v_exp_f32 v4, v4\n"
"v_mfma_f32_16x16x32_bf16 v[44:47], a[112:115], a[16:19], 0\n"
"ds_read_b128 a[64:67], v227 offset:16384\n"
"v_cvt_pk_bf16_f32 v25, v26, v27\n"
"v_mfma_f32_16x16x32_bf16 v[52:55], a[112:115], a[32:35], 0\n"
"v_exp_f32 v5, v5\n"
"v_mfma_f32_16x16x32_bf16 v[60:63], a[112:115], a[48:51], 0\n"
"v_cvt_pk_bf16_f32 v2, v4, v5\n"
"v_mfma_f32_16x16x32_bf16 v[36:39], a[116:119], a[4:7], v[36:39]\n"
"v_exp_f32 v6, v6\n"
"v_mfma_f32_16x16x32_bf16 v[44:47], a[116:119], a[20:23], v[44:47]\n"
"ds_read_b128 a[68:71], v227 offset:17408\n"
"v_mfma_f32_16x16x32_bf16 v[52:55], a[116:119], a[36:39], v[52:55]\n"
"v_exp_f32 v7, v7\n"
"v_mfma_f32_16x16x32_bf16 v[60:63], a[116:119], a[52:55], v[60:63]\n"
"s_waitcnt lgkmcnt(2)\n"
"buffer_load_dwordx4 v225, s[8:11], s44 offen offset:2048 sc1 lds\n"
"v_mfma_f32_16x16x32_bf16 v[36:39], a[120:123], a[8:11], v[36:39]\n"
"v_exp_f32 v12, v12\n"
"v_mfma_f32_16x16x32_bf16 v[44:47], a[120:123], a[24:27], v[44:47]\n"
"ds_read_b128 a[72:75], v227 offset:18432\n"
"v_cvt_pk_bf16_f32 v3, v6, v7\n"
"v_mfma_f32_16x16x32_bf16 v[52:55], a[120:123], a[40:43], v[52:55]\n"
"v_exp_f32 v13, v13\n"
"v_mfma_f32_16x16x32_bf16 v[60:63], a[120:123], a[56:59], v[60:63]\n"
"v_cvt_pk_bf16_f32 v10, v12, v13\n"
"v_mfma_f32_16x16x32_bf16 v[36:39], a[124:127], a[12:15], v[36:39]\n"
"v_exp_f32 v14, v14\n"
"v_mfma_f32_16x16x32_bf16 v[44:47], a[124:127], a[28:31], v[44:47]\n"
"ds_read_b128 a[76:79], v227 offset:19456\n"
"v_mfma_f32_16x16x32_bf16 v[52:55], a[124:127], a[44:47], v[52:55]\n"
"v_exp_f32 v15, v15\n"
"v_mfma_f32_16x16x32_bf16 v[60:63], a[124:127], a[60:63], v[60:63]\n"
"s_waitcnt lgkmcnt(2)\n"
"buffer_load_dwordx4 v225, s[8:11], s44 offen offset:3072 sc1 lds\n"
"v_mfma_f32_16x16x32_bf16 v[80:83], a[64:67], v[64:67], v[80:83]\n"
"s_add_u32 s41, s41, 32768\n"
"v_exp_f32 v20, v20\n"
"v_mfma_f32_16x16x32_bf16 v[112:115], a[64:67], v[68:71], v[112:115]\n"
"s_cmp_eq_u32 s41, 163840\n"
"ds_read_b128 a[80:83], v227 offset:20480\n"
"v_cvt_pk_bf16_f32 v11, v14, v15\n"
"v_mfma_f32_16x16x32_bf16 v[144:147], a[64:67], v[72:75], v[144:147]\n"
"s_cselect_b32 s41, 0, s41\n"
"v_exp_f32 v21, v21\n"
"v_mfma_f32_16x16x32_bf16 v[176:179], a[64:67], v[76:79], v[176:179]\n"
"v_add_u32 v226, s41, v224\n"
"v_cvt_pk_bf16_f32 v18, v20, v21\n"
"v_mfma_f32_16x16x32_bf16 v[84:87], a[68:71], v[64:67], v[84:87]\n"
"v_exp_f32 v22, v22\n"
"v_mfma_f32_16x16x32_bf16 v[116:119], a[68:71], v[68:71], v[116:119]\n"
"ds_read_b128 a[84:87], v227 offset:21504\n"
"v_mfma_f32_16x16x32_bf16 v[148:151], a[68:71], v[72:75], v[148:151]\n"
"v_exp_f32 v23, v23\n"
"v_mfma_f32_16x16x32_bf16 v[180:183], a[68:71], v[76:79], v[180:183]\n"
"s_waitcnt lgkmcnt(2)\n"
"v_cvt_pk_bf16_f32 v19, v22, v23\n"
"v_mfma_f32_16x16x32_bf16 v[88:91], a[72:75], v[64:67], v[88:91]\n"
"v_exp_f32 v28, v28\n"
"v_mfma_f32_16x16x32_bf16 v[120:123], a[72:75], v[68:71], v[120:123]\n"
"ds_read_b128 a[88:91], v227 offset:22528\n"
"v_mfma_f32_16x16x32_bf16 v[152:155], a[72:75], v[72:75], v[152:155]\n"
"v_exp_f32 v29, v29\n"
"v_mfma_f32_16x16x32_bf16 v[184:187], a[72:75], v[76:79], v[184:187]\n"
"v_cvt_pk_bf16_f32 v26, v28, v29\n"
"v_mfma_f32_16x16x32_bf16 v[92:95], a[76:79], v[64:67], v[92:95]\n"
"v_exp_f32 v30, v30\n"
"v_mfma_f32_16x16x32_bf16 v[124:127], a[76:79], v[68:71], v[124:127]\n"
"ds_read_b128 a[92:95], v227 offset:23552\n"
"v_mfma_f32_16x16x32_bf16 v[156:159], a[76:79], v[72:75], v[156:159]\n"
"v_exp_f32 v31, v31\n"
"v_mfma_f32_16x16x32_bf16 v[188:191], a[76:79], v[76:79], v[188:191]\n"
"s_waitcnt lgkmcnt(2)\n"
"v_cvt_pk_bf16_f32 v27, v30, v31\n"
"v_mfma_f32_16x16x32_bf16 v[96:99], a[80:83], v[64:67], v[96:99]\n"
"v_exp_f32 v32, v32\n"
"v_mfma_f32_16x16x32_bf16 v[128:131], a[80:83], v[68:71], v[128:131]\n"
"v_mfma_f32_16x16x32_bf16 v[160:163], a[80:83], v[72:75], v[160:163]\n"
"v_exp_f32 v33, v33\n"
"v_mfma_f32_16x16x32_bf16 v[192:195], a[80:83], v[76:79], v[192:195]\n"
"v_mfma_f32_16x16x32_bf16 v[100:103], a[84:87], v[64:67], v[100:103]\n"
"v_exp_f32 v34, v34\n"
"v_mfma_f32_16x16x32_bf16 v[132:135], a[84:87], v[68:71], v[132:135]\n"
"ds_read_b128 a[96:99], v227 offset:24576\n"
"v_mfma_f32_16x16x32_bf16 v[164:167], a[84:87], v[72:75], v[164:167]\n"
"v_exp_f32 v35, v35\n"
"v_mfma_f32_16x16x32_bf16 v[196:199], a[84:87], v[76:79], v[196:199]\n"
"s_waitcnt lgkmcnt(1)\n"
"v_mfma_f32_16x16x32_bf16 v[104:107], a[88:91], v[64:67], v[104:107]\n"
"v_exp_f32 v40, v40\n"
"v_mfma_f32_16x16x32_bf16 v[136:139], a[88:91], v[68:71], v[136:139]\n"
"ds_read_b128 a[100:103], v227 offset:25600\n"
"v_mfma_f32_16x16x32_bf16 v[168:171], a[88:91], v[72:75], v[168:171]\n"
"v_exp_f32 v41, v41\n"
"v_mfma_f32_16x16x32_bf16 v[200:203], a[88:91], v[76:79], v[200:203]\n"
"v_mfma_f32_16x16x32_bf16 v[108:111], a[92:95], v[64:67], v[108:111]\n"
"v_exp_f32 v42, v42\n"
"v_mfma_f32_16x16x32_bf16 v[140:143], a[92:95], v[68:71], v[140:143]\n"
"ds_read_b128 a[104:107], v227 offset:26624\n"
"v_mfma_f32_16x16x32_bf16 v[172:175], a[92:95], v[72:75], v[172:175]\n"
"v_exp_f32 v43, v43\n"
"v_mfma_f32_16x16x32_bf16 v[204:207], a[92:95], v[76:79], v[204:207]\n"
"v_mfma_f32_16x16x32_bf16 a[132:135], a[128:131], v[64:67], a[132:135]\n"
"v_exp_f32 v48, v48\n"
"v_mfma_f32_16x16x32_bf16 a[136:139], a[128:131], v[68:71], a[136:139]\n"
"ds_read_b128 a[108:111], v227 offset:27648\n"
"v_cvt_pk_bf16_f32 v64, v32, v33\n"
"v_mfma_f32_16x16x32_bf16 a[140:143], a[128:131], v[72:75], a[140:143]\n"
"v_exp_f32 v49, v49\n"
"v_mfma_f32_16x16x32_bf16 a[144:147], a[128:131], v[76:79], a[144:147]\n"
"s_waitcnt lgkmcnt(2)\n"
"v_cvt_pk_bf16_f32 v65, v34, v35\n"
"v_cvt_pk_bf16_f32 v68, v40, v41\n"
"v_mfma_f32_16x16x32_bf16 v[80:83], a[96:99], v[0:3], v[80:83]\n"
"v_exp_f32 v50, v50\n"
"v_mfma_f32_16x16x32_bf16 v[112:115], a[96:99], v[8:11], v[112:115]\n"
"ds_read_b128 a[112:115], v227 offset:28672\n"
"v_cvt_pk_bf16_f32 v69, v42, v43\n"
"v_mfma_f32_16x16x32_bf16 v[144:147], a[96:99], v[16:19], v[144:147]\n"
"v_exp_f32 v51, v51\n"
"v_mfma_f32_16x16x32_bf16 v[176:179], a[96:99], v[24:27], v[176:179]\n"
"v_cvt_pk_bf16_f32 v72, v48, v49\n"
"v_cvt_pk_bf16_f32 v73, v50, v51\n"
"v_mfma_f32_16x16x32_bf16 v[84:87], a[100:103], v[0:3], v[84:87]\n"
"v_exp_f32 v36, v36\n"
"v_mfma_f32_16x16x32_bf16 v[116:119], a[100:103], v[8:11], v[116:119]\n"
"ds_read_b128 a[116:119], v227 offset:29696\n"
"v_mfma_f32_16x16x32_bf16 v[148:151], a[100:103], v[16:19], v[148:151]\n"
"v_exp_f32 v37, v37\n"
"v_mfma_f32_16x16x32_bf16 v[180:183], a[100:103], v[24:27], v[180:183]\n"
"s_waitcnt lgkmcnt(2)\n"
"v_cvt_pk_bf16_f32 v66, v36, v37\n"
"v_mfma_f32_16x16x32_bf16 v[88:91], a[104:107], v[0:3], v[88:91]\n"
"v_exp_f32 v38, v38\n"
"v_mfma_f32_16x16x32_bf16 v[120:123], a[104:107], v[8:11], v[120:123]\n"
"ds_read_b128 a[120:123], v227 offset:30720\n"
"v_mfma_f32_16x16x32_bf16 v[152:155], a[104:107], v[16:19], v[152:155]\n"
"v_exp_f32 v39, v39\n"
"v_mfma_f32_16x16x32_bf16 v[184:187], a[104:107], v[24:27], v[184:187]\n"
"v_cvt_pk_bf16_f32 v67, v38, v39\n"
"v_mfma_f32_16x16x32_bf16 v[92:95], a[108:111], v[0:3], v[92:95]\n"
"v_exp_f32 v44, v44\n"
"v_mfma_f32_16x16x32_bf16 v[124:127], a[108:111], v[8:11], v[124:127]\n"
"ds_read_b128 a[124:127], v227 offset:31744\n"
"v_mfma_f32_16x16x32_bf16 v[156:159], a[108:111], v[16:19], v[156:159]\n"
"v_exp_f32 v45, v45\n"
"v_mfma_f32_16x16x32_bf16 v[188:191], a[108:111], v[24:27], v[188:191]\n"
"s_waitcnt lgkmcnt(2)\n"
"v_cvt_pk_bf16_f32 v70, v44, v45\n"
"v_mfma_f32_16x16x32_bf16 v[96:99], a[112:115], v[0:3], v[96:99]\n"
"v_exp_f32 v46, v46\n"
"v_mfma_f32_16x16x32_bf16 v[128:131], a[112:115], v[8:11], v[128:131]\n"
"v_mfma_f32_16x16x32_bf16 v[160:163], a[112:115], v[16:19], v[160:163]\n"
"v_exp_f32 v47, v47\n"
"v_mfma_f32_16x16x32_bf16 v[192:195], a[112:115], v[24:27], v[192:195]\n"
"v_cvt_pk_bf16_f32 v71, v46, v47\n"
"v_mfma_f32_16x16x32_bf16 v[100:103], a[116:119], v[0:3], v[100:103]\n"
"s_waitcnt vmcnt(16)\n"
"s_barrier\n"
"v_mfma_f32_16x16x32_bf16 v[132:135], a[116:119], v[8:11], v[132:135]\n"
"ds_read_b128 a[64:67], v226 offset:0\n"
"v_mfma_f32_16x16x32_bf16 v[164:167], a[116:119], v[16:19], v[164:167]\n"
"v_exp_f32 v52, v52\n"
"v_mfma_f32_16x16x32_bf16 v[196:199], a[116:119], v[24:27], v[196:199]\n"
"s_waitcnt lgkmcnt(1)\n"
"ds_read_b128 a[68:71], v226 offset:1024\n"
"v_mfma_f32_16x16x32_bf16 v[104:107], a[120:123], v[0:3], v[104:107]\n"
"v_exp_f32 v53, v53\n"
"v_mfma_f32_16x16x32_bf16 v[136:139], a[120:123], v[8:11], v[136:139]\n"
"ds_read_b128 a[72:75], v226 offset:2048\n"
"v_cvt_pk_bf16_f32 v74, v52, v53\n"
"v_mfma_f32_16x16x32_bf16 v[168:171], a[120:123], v[16:19], v[168:171]\n"
"v_exp_f32 v54, v54\n"
"v_mfma_f32_16x16x32_bf16 v[200:203], a[120:123], v[24:27], v[200:203]\n"
"v_mfma_f32_16x16x32_bf16 v[108:111], a[124:127], v[0:3], v[108:111]\n"
"v_exp_f32 v55, v55\n"
"v_mfma_f32_16x16x32_bf16 v[140:143], a[124:127], v[8:11], v[140:143]\n"
"ds_read_b128 a[76:79], v226 offset:3072\n"
"v_cvt_pk_bf16_f32 v75, v54, v55\n"
"v_mfma_f32_16x16x32_bf16 v[172:175], a[124:127], v[16:19], v[172:175]\n"
"v_mfma_f32_16x16x32_bf16 v[204:207], a[124:127], v[24:27], v[204:207]\n"
"ds_read_b128 a[80:83], v226 offset:4096\n"
"v_mfma_f32_16x16x32_bf16 a[132:135], a[128:131], v[0:3], a[132:135]\n"
"v_mfma_f32_16x16x32_bf16 a[136:139], a[128:131], v[8:11], a[136:139]\n"
"ds_read_b128 a[84:87], v226 offset:5120\n"
"v_mfma_f32_16x16x32_bf16 a[140:143], a[128:131], v[16:19], a[140:143]\n"
"v_mfma_f32_16x16x32_bf16 a[144:147], a[128:131], v[24:27], a[144:147]\n"
"s_sub_u32 s56, s56, 1\n"
"s_cbranch_scc0 LP1_%=\n"
"s_cmp_lg_u32 s57, 0\n"
"s_cbranch_scc1 LEV_%=\n"
"s_waitcnt lgkmcnt(5)\n"
"v_mfma_f32_16x16x32_bf16 v[0:3], a[64:67], a[0:3], v[208:211]\n"
"v_exp_f32 v56, v56\n"
"v_mfma_f32_16x16x32_bf16 v[8:11], a[64:67], a[16:19], 0\n"
"v_readlane_b32 s43, v228, s42\n"
"v_mfma_f32_16x16x32_bf16 v[16:19], a[64:67], a[32:35], 0\n"
"v_exp_f32 v57, v57\n"
"v_mfma_f32_16x16x32_bf16 v[24:27], a[64:67], a[48:51], 0\n"
"s_waitcnt lgkmcnt(4)\n"
"v_cvt_pk_bf16_f32 v76, v56, v57\n"
"s_mov_b32 m0, s40\n"
"v_mfma_f32_16x16x32_bf16 v[0:3], a[68:71], a[4:7], v[0:3]\n"
"v_exp_f32 v58, v58\n"
"v_mfma_f32_16x16x32_bf16 v[8:11], a[68:71], a[20:23], v[8:11]\n"
"v_readlane_b32 s44, v229, s42\n"
"v_mov_b32 v227, v226\n"
"v_mfma_f32_16x16x32_bf16 v[16:19], a[68:71], a[36:39], v[16:19]\n"
"v_exp_f32 v59, v59\n"
"v_mfma_f32_16x16x32_bf16 v[24:27], a[68:71], a[52:55], v[24:27]\n"
"s_waitcnt lgkmcnt(0)\n"
"buffer_load_dwordx4 v225, s[8:11], s43 offen offset:0 sc1 lds\n"
"v_mfma_f32_16x16x32_bf16 v[0:3], a[72:75], a[8:11], v[0:3]\n"
"s_add_u32 s42, s42, 1\n"
"v_exp_f32 v60, v60\n"
"v_mfma_f32_16x16x32_bf16 v[8:11], a[72:75], a[24:27], v[8:11]\n"
"ds_read_b128 a[88:91], v226 offset:6144\n"
"v_cvt_pk_bf16_f32 v77, v58, v59\n"
"v_mfma_f32_16x16x32_bf16 v[16:19], a[72:75], a[40:43], v[16:19]\n"
"v_exp_f32 v61, v61\n"
"v_mfma_f32_16x16x32_bf16 v[24:27], a[72:75], a[56:59], v[24:27]\n"
"v_cvt_pk_bf16_f32 v78, v60, v61\n"
"v_mfma_f32_16x16x32_bf16 v[0:3], a[76:79], a[12:15], v[0:3]\n"
"v_exp_f32 v62, v62\n"
"v_mfma_f32_16x16x32_bf16 v[8:11], a[76:79], a[28:31], v[8:11]\n"
"ds_read_b128 a[92:95], v226 offset:7168\n"
"v_mfma_f32_16x16x32_bf16 v[16:19], a[76:79], a[44:47], v[16:19]\n"
"v_exp_f32 v63, v63\n"
"v_mfma_f32_16x16x32_bf16 v[24:27], a[76:79], a[60:63], v[24:27]\n"
"buffer_load_dwordx4 v225, s[8:11], s43 offen offset:1024 sc1 lds\n"
"v_mfma_f32_16x16x32_bf16 v[4:7], a[80:83], a[0:3], v[212:215]\n"
"v_exp_f32 v0, v0\n"
"v_mfma_f32_16x16x32_bf16 v[12:15], a[80:83], a[16:19], 0\n"
"ds_read_b128 a[96:99], v226 offset:8192\n"
"v_cvt_pk_bf16_f32 v79, v62, v63\n"
"v_mfma_f32_16x16x32_bf16 v[20:23], a[80:83], a[32:35], 0\n"
"v_exp_f32 v1, v1\n"
"v_mfma_f32_16x16x32_bf16 v[28:31], a[80:83], a[48:51], 0\n"
"v_cvt_pk_bf16_f32 v0, v0, v1\n"
"v_mfma_f32_16x16x32_bf16 v[4:7], a[84:87], a[4:7], v[4:7]\n"
"v_exp_f32 v2, v2\n"
"v_mfma_f32_16x16x32_bf16 v[12:15], a[84:87], a[20:23], v[12:15]\n"
"ds_read_b128 a[100:103], v226 offset:9216\n"
"v_mfma_f32_16x16x32_bf16 v[20:23], a[84:87], a[36:39], v[20:23]\n"
"v_exp_f32 v3, v3\n"
"v_mfma_f32_16x16x32_bf16 v[28:31], a[84:87], a[52:55], v[28:31]\n"
"s_waitcnt lgkmcnt(2)\n"
"buffer_load_dwordx4 v225, s[8:11], s43 offen offset:2048 sc1 lds\n"
"v_mfma_f32_16x16x32_bf16 v[4:7], a[88:91], a[8:11], v[4:7]\n"
"v_exp_f32 v8, v8\n"
"v_mfma_f32_16x16x32_bf16 v[12:15], a[88:91], a[24:27], v[12:15]\n"
"ds_read_b128 a[104:107], v226 offset:10240\n"
"v_cvt_pk_bf16_f32 v1, v2, v3\n"
"v_mfma_f32_16x16x32_bf16 v[20:23], a[88:91], a[40:43], v[20:23]\n"
"v_exp_f32 v9, v9\n"
"v_mfma_f32_16x16x32_bf16 v[28:31], a[88:91], a[56:59], v[28:31]\n"
"v_cvt_pk_bf16_f32 v8, v8, v9\n"
"v_mfma_f32_16x16x32_bf16 v[4:7], a[92:95], a[12:15], v[4:7]\n"
"v_exp_f32 v10, v10\n"
"v_mfma_f32_16x16x32_bf16 v[12:15], a[92:95], a[28:31], v[12:15]\n"
"ds_read_b128 a[108:111], v226 offset:11264\n"
"v_mfma_f32_16x16x32_bf16 v[20:23], a[92:95], a[44:47], v[20:23]\n"
"v_exp_f32 v11, v11\n"
"v_mfma_f32_16x16x32_bf16 v[28:31], a[92:95], a[60:63], v[28:31]\n"
"s_waitcnt lgkmcnt(2)\n"
"buffer_load_dwordx4 v225, s[8:11], s43 offen offset:3072 sc1 lds\n"
"v_mfma_f32_16x16x32_bf16 v[32:35], a[96:99], a[0:3], v[216:219]\n"
"v_exp_f32 v16, v16\n"
"v_mfma_f32_16x16x32_bf16 v[40:43], a[96:99], a[16:19], 0\n"
"ds_read_b128 a[112:115], v226 offset:12288\n"
"v_cvt_pk_bf16_f32 v9, v10, v11\n"
"v_mfma_f32_16x16x32_bf16 v[48:51], a[96:99], a[32:35], 0\n"
"v_exp_f32 v17, v17\n"
"v_mfma_f32_16x16x32_bf16 v[56:59], a[96:99], a[48:51], 0\n"
"s_add_u32 m0, s40, 16384\n"
"v_cvt_pk_bf16_f32 v16, v16, v17\n"
"v_mfma_f32_16x16x32_bf16 v[32:35], a[100:103], a[4:7], v[32:35]\n"
"v_exp_f32 v18, v18\n"
"v_mfma_f32_16x16x32_bf16 v[40:43], a[100:103], a[20:23], v[40:43]\n"
"ds_read_b128 a[116:119], v226 offset:13312\n"
"v_mfma_f32_16x16x32_bf16 v[48:51], a[100:103], a[36:39], v[48:51]\n"
"v_exp_f32 v19, v19\n"
"v_mfma_f32_16x16x32_bf16 v[56:59], a[100:103], a[52:55], v[56:59]\n"
"s_waitcnt lgkmcnt(2)\n"
"buffer_load_dwordx4 v225, s[8:11], s44 offen offset:0 sc1 lds\n"
"v_mfma_f32_16x16x32_bf16 v[32:35], a[104:107], a[8:11], v[32:35]\n"
"s_add_u32 s40, s40, 32768\n"
"v_exp_f32 v24, v24\n"
"v_mfma_f32_16x16x32_bf16 v[40:43], a[104:107], a[24:27], v[40:43]\n"
"s_cmp_eq_u32 s40, s46\n"
"ds_read_b128 a[120:123], v226 offset:14336\n"
"v_cvt_pk_bf16_f32 v17, v18, v19\n"
"v_mfma_f32_16x16x32_bf16 v[48:51], a[104:107], a[40:43], v[48:51]\n"
"s_cselect_b32 s40, s47, s40\n"
"v_exp_f32 v25, v25\n"
"v_mfma_f32_16x16x32_bf16 v[56:59], a[104:107], a[56:59], v[56:59]\n"
"v_cvt_pk_bf16_f32 v24, v24, v25\n"
"v_mfma_f32_16x16x32_bf16 v[32:35], a[108:111], a[12:15], v[32:35]\n"
"v_exp_f32 v26, v26\n"
"v_mfma_f32_16x16x32_bf16 v[40:43], a[108:111], a[28:31], v[40:43]\n"
"ds_read_b128 a[124:127], v226 offset:15360\n"
"v_mfma_f32_16x16x32_bf16 v[48:51], a[108:111], a[44:47], v[48:51]\n"
"v_exp_f32 v27, v27\n"
"v_mfma_f32_16x16x32_bf16 v[56:59], a[108:111], a[60:63], v[56:59]\n"
"s_waitcnt lgkmcnt(2)\n"
"buffer_load_dwordx4 v225, s[8:11], s44 offen offset:1024 sc1 lds\n"
"v_mfma_f32_16x16x32_bf16 v[36:39], a[112:115], a[0:3], v[220:223]\n"
"v_exp_f32 v4, v4\n"
"v_mfma_f32_16x16x32_bf16 v[44:47], a[112:115], a[16:19], 0\n"
"ds_read_b128 a[64:67], v227 offset:16384\n"
"v_cvt_pk_bf16_f32 v25, v26, v27\n"
"v_mfma_f32_16x16x32_bf16 v[52:55], a[112:115], a[32:35], 0\n"
"v_exp_f32 v5, v5\n"
"v_mfma_f32_16x16x32_bf16 v[60:63], a[112:115], a[48:51], 0\n"
"v_cvt_pk_bf16_f32 v2, v4, v5\n"
"v_mfma_f32_16x16x32_bf16 v[36:39], a[116:119], a[4:7], v[36:39]\n"
"v_exp_f32 v6, v6\n"
"v_mfma_f32_16x16x32_bf16 v[44:47], a[116:119], a[20:23], v[44:47]\n"
"ds_read_b128 a[68:71], v227 offset:17408\n"
"v_mfma_f32_16x16x32_bf16 v[52:55], a[116:119], a[36:39], v[52:55]\n"
"v_exp_f32 v7, v7\n"
"v_mfma_f32_16x16x32_bf16 v[60:63], a[116:119], a[52:55], v[60:63]\n"
"s_waitcnt lgkmcnt(2)\n"
"buffer_load_dwordx4 v225, s[8:11], s44 offen offset:2048 sc1 lds\n"
"v_mfma_f32_16x16x32_bf16 v[36:39], a[120:123], a[8:11], v[36:39]\n"
"v_exp_f32 v12, v12\n"
"v_mfma_f32_16x16x32_bf16 v[44:47], a[120:123], a[24:27], v[44:47]\n"
"ds_read_b128 a[72:75], v227 offset:18432\n"
"v_cvt_pk_bf16_f32 v3, v6, v7\n"
"v_mfma_f32_16x16x32_bf16 v[52:55], a[120:123], a[40:43], v[52:55]\n"
"v_exp_f32 v13, v13\n"
"v_mfma_f32_16x16x32_bf16 v[60:63], a[120:123], a[56:59], v[60:63]\n"
"v_cvt_pk_bf16_f32 v10, v12, v13\n"
"v_mfma_f32_16x16x32_bf16 v[36:39], a[124:127], a[12:15], v[36:39]\n"
"v_exp_f32 v14, v14\n"
"v_mfma_f32_16x16x32_bf16 v[44:47], a[124:127], a[28:31], v[44:47]\n"
"ds_read_b128 a[76:79], v227 offset:19456\n"
"v_mfma_f32_16x16x32_bf16 v[52:55], a[124:127], a[44:47], v[52:55]\n"
"v_exp_f32 v15, v15\n"
"v_mfma_f32_16x16x32_bf16 v[60:63], a[124:127], a[60:63], v[60:63]\n"
"s_waitcnt lgkmcnt(2)\n"
"buffer_load_dwordx4 v225, s[8:11], s44 offen offset:3072 sc1 lds\n"
"v_mfma_f32_16x16x32_bf16 v[80:83], a[64:67], v[64:67], v[80:83]\n"
"s_add_u32 s41, s41, 32768\n"
"v_exp_f32 v20, v20\n"
"v_mfma_f32_16x16x32_bf16 v[112:115], a[64:67], v[68:71], v[112:115]\n"
"s_cmp_eq_u32 s41, 163840\n"
"ds_read_b128 a[80:83], v227 offset:20480\n"
"v_cvt_pk_bf16_f32 v11, v14, v15\n"
"v_mfma_f32_16x16x32_bf16 v[144:147], a[64:67], v[72:75], v[144:147]\n"
"s_cselect_b32 s41, 0, s41\n"
"v_exp_f32 v21, v21\n"
"v_mfma_f32_16x16x32_bf16 v[176:179], a[64:67], v[76:79], v[176:179]\n"
"v_add_u32 v226, s41, v224\n"
"v_cvt_pk_bf16_f32 v18, v20, v21\n"
"v_mfma_f32_16x16x32_bf16 v[84:87], a[68:71], v[64:67], v[84:87]\n"
"v_exp_f32 v22, v22\n"
"v_mfma_f32_16x16x32_bf16 v[116:119], a[68:71], v[68:71], v[116:119]\n"
"ds_read_b128 a[84:87], v227 offset:21504\n"
"v_mfma_f32_16x16x32_bf16 v[148:151], a[68:71], v[72:75], v[148:151]\n"
"v_exp_f32 v23, v23\n"
"v_mfma_f32_16x16x32_bf16 v[180:183], a[68:71], v[76:79], v[180:183]\n"
"s_waitcnt lgkmcnt(2)\n"
"v_cvt_pk_bf16_f32 v19, v22, v23\n"
"v_mfma_f32_16x16x32_bf16 v[88:91], a[72:75], v[64:67], v[88:91]\n"
"v_exp_f32 v28, v28\n"
"v_mfma_f32_16x16x32_bf16 v[120:123], a[72:75], v[68:71], v[120:123]\n"
"ds_read_b128 a[88:91], v227 offset:22528\n"
"v_mfma_f32_16x16x32_bf16 v[152:155], a[72:75], v[72:75], v[152:155]\n"
"v_exp_f32 v29, v29\n"
"v_mfma_f32_16x16x32_bf16 v[184:187], a[72:75], v[76:79], v[184:187]\n"
"v_cvt_pk_bf16_f32 v26, v28, v29\n"
"v_mfma_f32_16x16x32_bf16 v[92:95], a[76:79], v[64:67], v[92:95]\n"
"v_exp_f32 v30, v30\n"
"v_mfma_f32_16x16x32_bf16 v[124:127], a[76:79], v[68:71], v[124:127]\n"
"ds_read_b128 a[92:95], v227 offset:23552\n"
"v_mfma_f32_16x16x32_bf16 v[156:159], a[76:79], v[72:75], v[156:159]\n"
"v_exp_f32 v31, v31\n"
"v_mfma_f32_16x16x32_bf16 v[188:191], a[76:79], v[76:79], v[188:191]\n"
"s_waitcnt lgkmcnt(2)\n"
"v_cvt_pk_bf16_f32 v27, v30, v31\n"
"v_mfma_f32_16x16x32_bf16 v[96:99], a[80:83], v[64:67], v[96:99]\n"
"v_exp_f32 v32, v32\n"
"v_mfma_f32_16x16x32_bf16 v[128:131], a[80:83], v[68:71], v[128:131]\n"
"v_mfma_f32_16x16x32_bf16 v[160:163], a[80:83], v[72:75], v[160:163]\n"
"v_exp_f32 v33, v33\n"
"v_mfma_f32_16x16x32_bf16 v[192:195], a[80:83], v[76:79], v[192:195]\n"
"v_mfma_f32_16x16x32_bf16 v[100:103], a[84:87], v[64:67], v[100:103]\n"
"v_exp_f32 v34, v34\n"
"v_mfma_f32_16x16x32_bf16 v[132:135], a[84:87], v[68:71], v[132:135]\n"
"ds_read_b128 a[96:99], v227 offset:24576\n"
"v_mfma_f32_16x16x32_bf16 v[164:167], a[84:87], v[72:75], v[164:167]\n"
"v_exp_f32 v35, v35\n"
"v_mfma_f32_16x16x32_bf16 v[196:199], a[84:87], v[76:79], v[196:199]\n"
"s_waitcnt lgkmcnt(1)\n"
"v_mfma_f32_16x16x32_bf16 v[104:107], a[88:91], v[64:67], v[104:107]\n"
"v_exp_f32 v40, v40\n"
"v_mfma_f32_16x16x32_bf16 v[136:139], a[88:91], v[68:71], v[136:139]\n"
"ds_read_b128 a[100:103], v227 offset:25600\n"
"v_mfma_f32_16x16x32_bf16 v[168:171], a[88:91], v[72:75], v[168:171]\n"
"v_exp_f32 v41, v41\n"
"v_mfma_f32_16x16x32_bf16 v[200:203], a[88:91], v[76:79], v[200:203]\n"
"v_mfma_f32_16x16x32_bf16 v[108:111], a[92:95], v[64:67], v[108:111]\n"
"v_exp_f32 v42, v42\n"
"v_mfma_f32_16x16x32_bf16 v[140:143], a[92:95], v[68:71], v[140:143]\n"
"ds_read_b128 a[104:107], v227 offset:26624\n"
"v_mfma_f32_16x16x32_bf16 v[172:175], a[92:95], v[72:75], v[172:175]\n"
"v_exp_f32 v43, v43\n"
"v_mfma_f32_16x16x32_bf16 v[204:207], a[92:95], v[76:79], v[204:207]\n"
"v_mfma_f32_16x16x32_bf16 a[132:135], a[128:131], v[64:67], a[132:135]\n"
"v_exp_f32 v48, v48\n"
"v_mfma_f32_16x16x32_bf16 a[136:139], a[128:131], v[68:71], a[136:139]\n"
"ds_read_b128 a[108:111], v227 offset:27648\n"
"v_cvt_pk_bf16_f32 v64, v32, v33\n"
"v_mfma_f32_16x16x32_bf16 a[140:143], a[128:131], v[72:75], a[140:143]\n"
"v_exp_f32 v49, v49\n"
"v_mfma_f32_16x16x32_bf16 a[144:147], a[128:131], v[76:79], a[144:147]\n"
"s_waitcnt lgkmcnt(2)\n"
"v_cvt_pk_bf16_f32 v65, v34, v35\n"
"v_cvt_pk_bf16_f32 v68, v40, v41\n"
"v_mfma_f32_16x16x32_bf16 v[80:83], a[96:99], v[0:3], v[80:83]\n"
"v_exp_f32 v50, v50\n"
"v_mfma_f32_16x16x32_bf16 v[112:115], a[96:99], v[8:11], v[112:115]\n"
"ds_read_b128 a[112:115], v227 offset:28672\n"
"v_cvt_pk_bf16_f32 v69, v42, v43\n"
"v_mfma_f32_16x16x32_bf16 v[144:147], a[96:99], v[16:19], v[144:147]\n"
"v_exp_f32 v51, v51\n"
"v_mfma_f32_16x16x32_bf16 v[176:179], a[96:99], v[24:27], v[176:179]\n"
"v_cvt_pk_bf16_f32 v72, v48, v49\n"
"v_cvt_pk_bf16_f32 v73, v50, v51\n"
"v_mfma_f32_16x16x32_bf16 v[84:87], a[100:103], v[0:3], v[84:87]\n"
"v_exp_f32 v36, v36\n"
"v_mfma_f32_16x16x32_bf16 v[116:119], a[100:103], v[8:11], v[116:119]\n"
"ds_read_b128 a[116:119], v227 offset:29696\n"
"v_mfma_f32_16x16x32_bf16 v[148:151], a[100:103], v[16:19], v[148:151]\n"
"v_exp_f32 v37, v37\n"
"v_mfma_f32_16x16x32_bf16 v[180:183], a[100:103], v[24:27], v[180:183]\n"
"s_waitcnt lgkmcnt(2)\n"
"v_cvt_pk_bf16_f32 v66, v36, v37\n"
"v_mfma_f32_16x16x32_bf16 v[88:91], a[104:107], v[0:3], v[88:91]\n"
"v_exp_f32 v38, v38\n"
"v_mfma_f32_16x16x32_bf16 v[120:123], a[104:107], v[8:11], v[120:123]\n"
"ds_read_b128 a[120:123], v227 offset:30720\n"
"v_mfma_f32_16x16x32_bf16 v[152:155], a[104:107], v[16:19], v[152:155]\n"
"v_exp_f32 v39, v39\n"
"v_mfma_f32_16x16x32_bf16 v[184:187], a[104:107], v[24:27], v[184:187]\n"
"v_cvt_pk_bf16_f32 v67, v38, v39\n"
"v_mfma_f32_16x16x32_bf16 v[92:95], a[108:111], v[0:3], v[92:95]\n"
"v_exp_f32 v44, v44\n"
"v_mfma_f32_16x16x32_bf16 v[124:127], a[108:111], v[8:11], v[124:127]\n"
"ds_read_b128 a[124:127], v227 offset:31744\n"
"v_mfma_f32_16x16x32_bf16 v[156:159], a[108:111], v[16:19], v[156:159]\n"
"v_exp_f32 v45, v45\n"
"v_mfma_f32_16x16x32_bf16 v[188:191], a[108:111], v[24:27], v[188:191]\n"
"s_waitcnt lgkmcnt(2)\n"
"v_cvt_pk_bf16_f32 v70, v44, v45\n"
"v_mfma_f32_16x16x32_bf16 v[96:99], a[112:115], v[0:3], v[96:99]\n"
"v_exp_f32 v46, v46\n"
"v_mfma_f32_16x16x32_bf16 v[128:131], a[112:115], v[8:11], v[128:131]\n"
"v_mfma_f32_16x16x32_bf16 v[160:163], a[112:115], v[16:19], v[160:163]\n"
"v_exp_f32 v47, v47\n"
"v_mfma_f32_16x16x32_bf16 v[192:195], a[112:115], v[24:27], v[192:195]\n"
"v_cvt_pk_bf16_f32 v71, v46, v47\n"
"v_mfma_f32_16x16x32_bf16 v[100:103], a[116:119], v[0:3], v[100:103]\n"
"s_waitcnt vmcnt(16)\n"
"s_barrier\n"
"v_mfma_f32_16x16x32_bf16 v[132:135], a[116:119], v[8:11], v[132:135]\n"
"ds_read_b128 a[64:67], v226 offset:0\n"
"v_mfma_f32_16x16x32_bf16 v[164:167], a[116:119], v[16:19], v[164:167]\n"
"v_exp_f32 v52, v52\n"
"v_mfma_f32_16x16x32_bf16 v[196:199], a[116:119], v[24:27], v[196:199]\n"
"s_waitcnt lgkmcnt(1)\n"
"ds_read_b128 a[68:71], v226 offset:1024\n"
"v_mfma_f32_16x16x32_bf16 v[104:107], a[120:123], v[0:3], v[104:107]\n"
"v_exp_f32 v53, v53\n"
"v_mfma_f32_16x16x32_bf16 v[136:139], a[120:123], v[8:11], v[136:139]\n"
"ds_read_b128 a[72:75], v226 offset:2048\n"
"v_cvt_pk_bf16_f32 v74, v52, v53\n"
"v_mfma_f32_16x16x32_bf16 v[168:171], a[120:123], v[16:19], v[168:171]\n"
"v_exp_f32 v54, v54\n"
"v_mfma_f32_16x16x32_bf16 v[200:203], a[120:123], v[24:27], v[200:203]\n"
"v_mfma_f32_16x16x32_bf16 v[108:111], a[124:127], v[0:3], v[108:111]\n"
"v_exp_f32 v55, v55\n"
"v_mfma_f32_16x16x32_bf16 v[140:143], a[124:127], v[8:11], v[140:143]\n"
"ds_read_b128 a[76:79], v226 offset:3072\n"
"v_cvt_pk_bf16_f32 v75, v54, v55\n"
"v_mfma_f32_16x16x32_bf16 v[172:175], a[124:127], v[16:19], v[172:175]\n"
"v_mfma_f32_16x16x32_bf16 v[204:207], a[124:127], v[24:27], v[204:207]\n"
"ds_read_b128 a[80:83], v226 offset:4096\n"
"v_mfma_f32_16x16x32_bf16 a[132:135], a[128:131], v[0:3], a[132:135]\n"
"v_mfma_f32_16x16x32_bf16 a[136:139], a[128:131], v[8:11], a[136:139]\n"
"ds_read_b128 a[84:87], v226 offset:5120\n"
"v_mfma_f32_16x16x32_bf16 a[140:143], a[128:131], v[16:19], a[140:143]\n"
"v_mfma_f32_16x16x32_bf16 a[144:147], a[128:131], v[24:27], a[144:147]\n"
"LDB_%=:\n"
"s_waitcnt lgkmcnt(5)\n"
"v_mfma_f32_16x16x32_bf16 v[8:11], a[64:67], a[16:19], v[208:211]\n"
"v_exp_f32 v56, v56\n"
"v_mfma_f32_16x16x32_bf16 v[16:19], a[64:67], a[32:35], 0\n"
"v_readlane_b32 s43, v228, s42\n"
"v_mfma_f32_16x16x32_bf16 v[24:27], a[64:67], a[48:51], 0\n"
"s_waitcnt lgkmcnt(4)\n"
"v_exp_f32 v57, v57\n"
"v_mfma_f32_16x16x32_bf16 v[8:11], a[68:71], a[20:23], v[8:11]\n"
"s_lshl_b32 s50, s35, 6\n"
"ds_read_b128 a[88:91], v226 offset:6144\n"
"v_cvt_pk_bf16_f32 v76, v56, v57\n"
"v_mfma_f32_16x16x32_bf16 v[16:19], a[68:71], a[36:39], v[16:19]\n"
"s_mov_b32 m0, s40\n"
"v_exp_f32 v58, v58\n"
"v_mfma_f32_16x16x32_bf16 v[24:27], a[68:71], a[52:55], v[24:27]\n"
"s_waitcnt lgkmcnt(1)\n"
"ds_read_b128 a[92:95], v226 offset:7168\n"
"v_readlane_b32 s44, v229, s42\n"
"v_mfma_f32_16x16x32_bf16 v[8:11], a[72:75], a[24:27], v[8:11]\n"
"s_add_u32 s50, s50, 4352\n"
"v_exp_f32 v59, v59\n"
"v_mfma_f32_16x16x32_bf16 v[16:19], a[72:75], a[40:43], v[16:19]\n"
"s_add_u32 s42, s42, 1\n"
"buffer_load_dwordx4 v225, s[8:11], s43 offen offset:0 sc1 lds\n"
"v_mov_b32 v227, v226\n"
"v_mfma_f32_16x16x32_bf16 v[24:27], a[72:75], a[56:59], v[24:27]\n"
"v_exp_f32 v60, v60\n"
"v_lshrrev_b32 v235, 1, v224\n"
"v_mfma_f32_16x16x32_bf16 v[8:11], a[76:79], a[28:31], v[8:11]\n"
"ds_read_b128 a[96:99], v226 offset:8192\n"
"v_and_b32 v235, 56, v235\n"
"v_mfma_f32_16x16x32_bf16 v[16:19], a[76:79], a[44:47], v[16:19]\n"
"v_exp_f32 v61, v61\n"
"v_cvt_pk_bf16_f32 v77, v58, v59\n"
"v_mfma_f32_16x16x32_bf16 v[24:27], a[76:79], a[60:63], v[24:27]\n"
"ds_read_b128 a[100:103], v226 offset:9216\n"
"v_cvt_pk_bf16_f32 v78, v60, v61\n"
"v_mfma_f32_16x16x32_bf16 v[12:15], a[80:83], a[16:19], v[212:215]\n"
"v_exp_f32 v62, v62\n"
"v_mfma_f32_16x16x32_bf16 v[20:23], a[80:83], a[32:35], 0\n"
"v_mfma_f32_16x16x32_bf16 v[28:31], a[80:83], a[48:51], 0\n"
"v_exp_f32 v63, v63\n"
"v_mfma_f32_16x16x32_bf16 v[12:15], a[84:87], a[20:23], v[12:15]\n"
"buffer_load_dwordx4 v225, s[8:11], s43 offen offset:1024 sc1 lds\n"
"v_mfma_f32_16x16x32_bf16 v[20:23], a[84:87], a[36:39], v[20:23]\n"
"v_exp_f32 v8, v8\n"
"v_mfma_f32_16x16x32_bf16 v[28:31], a[84:87], a[52:55], v[28:31]\n"
"s_waitcnt lgkmcnt(1)\n"
"ds_read_b128 a[104:107], v226 offset:10240\n"
"v_cvt_pk_bf16_f32 v79, v62, v63\n"
"v_mfma_f32_16x16x32_bf16 v[12:15], a[88:91], a[24:27], v[12:15]\n"
"v_exp_f32 v9, v9\n"
"v_mfma_f32_16x16x32_bf16 v[20:23], a[88:91], a[40:43], v[20:23]\n"
"ds_read_b128 a[108:111], v226 offset:11264\n"
"v_cvt_pk_bf16_f32 v8, v8, v9\n"
"v_mfma_f32_16x16x32_bf16 v[28:31], a[88:91], a[56:59], v[28:31]\n"
"s_lshl_b32 s56, s34, 1\n"
"v_exp_f32 v10, v10\n"
"v_mfma_f32_16x16x32_bf16 v[12:15], a[92:95], a[28:31], v[12:15]\n"
"s_sub_u32 s56, 12, s56\n"
"ds_read_b128 a[112:115], v226 offset:12288\n"
"v_mfma_f32_16x16x32_bf16 v[20:23], a[92:95], a[44:47], v[20:23]\n"
"v_exp_f32 v11, v11\n"
"v_mfma_f32_16x16x32_bf16 v[28:31], a[92:95], a[60:63], v[28:31]\n"
"buffer_load_dwordx4 v225, s[8:11], s43 offen offset:2048 sc1 lds\n"
"v_mfma_f32_16x16x32_bf16 v[40:43], a[96:99], a[16:19], v[216:219]\n"
"v_exp_f32 v16, v16\n"
"v_mfma_f32_16x16x32_bf16 v[48:51], a[96:99], a[32:35], 0\n"
"ds_read_b128 a[116:119], v226 offset:13312\n"
"v_cvt_pk_bf16_f32 v9, v10, v11\n"
"v_mfma_f32_16x16x32_bf16 v[56:59], a[96:99], a[48:51], 0\n"
"s_waitcnt lgkmcnt(2)\n"
"v_exp_f32 v17, v17\n"
"v_mfma_f32_16x16x32_bf16 v[40:43], a[100:103], a[20:23], v[40:43]\n"
"ds_read_b128 a[120:123], v226 offset:14336\n"
"v_cvt_pk_bf16_f32 v16, v16, v17\n"
"v_mfma_f32_16x16x32_bf16 v[48:51], a[100:103], a[36:39], v[48:51]\n"
"v_exp_f32 v18, v18\n"
"v_mfma_f32_16x16x32_bf16 v[56:59], a[100:103], a[52:55], v[56:59]\n"
"ds_read_b128 a[124:127], v226 offset:15360\n"
"v_mfma_f32_16x16x32_bf16 v[40:43], a[104:107], a[24:27], v[40:43]\n"
"v_exp_f32 v19, v19\n"
"v_mfma_f32_16x16x32_bf16 v[48:51], a[104:107], a[40:43], v[48:51]\n"
"buffer_load_dwordx4 v225, s[8:11], s43 offen offset:3072 sc1 lds\n"
"v_mfma_f32_16x16x32_bf16 v[56:59], a[104:107], a[56:59], v[56:59]\n"
"v_exp_f32 v24, v24\n"
"v_mfma_f32_16x16x32_bf16 v[40:43], a[108:111], a[28:31], v[40:43]\n"
"ds_read_b128 a[64:67], v227 offset:16384\n"
"v_cvt_pk_bf16_f32 v17, v18, v19\n"
"v_mfma_f32_16x16x32_bf16 v[48:51], a[108:111], a[44:47], v[48:51]\n"
"v_exp_f32 v25, v25\n"
"v_mfma_f32_16x16x32_bf16 v[56:59], a[108:111], a[60:63], v[56:59]\n"
"s_waitcnt lgkmcnt(2)\n"
"v_cvt_pk_bf16_f32 v24, v24, v25\n"
"s_add_u32 m0, s40, 16384\n"
"v_mfma_f32_16x16x32_bf16 v[44:47], a[112:115], a[16:19], v[220:223]\n"
"v_exp_f32 v26, v26\n"
"v_mfma_f32_16x16x32_bf16 v[52:55], a[112:115], a[32:35], 0\n"
"ds_read_b128 a[68:71], v227 offset:17408\n"
"v_mfma_f32_16x16x32_bf16 v[60:63], a[112:115], a[48:51], 0\n"
"v_exp_f32 v27, v27\n"
"v_mfma_f32_16x16x32_bf16 v[44:47], a[116:119], a[20:23], v[44:47]\n"
"buffer_load_dwordx4 v225, s[8:11], s44 offen offset:0 sc1 lds\n"
"v_mfma_f32_16x16x32_bf16 v[52:55], a[116:119], a[36:39], v[52:55]\n"
"s_add_u32 s40, s40, 32768\n"
"v_exp_f32 v12, v12\n"
"v_mfma_f32_16x16x32_bf16 v[60:63], a[116:119], a[52:55], v[60:63]\n"
"s_cmp_eq_u32 s40, s46\n"
"buffer_load_dwordx2 v[236:237], v235, s[8:11], s50 offen sc1\n"
"v_mfma_f32_16x16x32_bf16 v[44:47], a[120:123], a[24:27], v[44:47]\n"
"s_cselect_b32 s40, s47, s40\n"
"ds_read_b128 a[72:75], v227 offset:18432\n"
"v_cvt_pk_bf16_f32 v25, v26, v27\n"
"v_mfma_f32_16x16x32_bf16 v[52:55], a[120:123], a[40:43], v[52:55]\n"
"v_exp_f32 v13, v13\n"
"v_mfma_f32_16x16x32_bf16 v[60:63], a[120:123], a[56:59], v[60:63]\n"
"s_waitcnt lgkmcnt(1)\n"
"v_exp_f32 v14, v14\n"
"v_mfma_f32_16x16x32_bf16 v[44:47], a[124:127], a[28:31], v[44:47]\n"
"ds_read_b128 a[76:79], v227 offset:19456\n"
"v_cvt_pk_bf16_f32 v10, v12, v13\n"
"v_mfma_f32_16x16x32_bf16 v[52:55], a[124:127], a[44:47], v[52:55]\n"
"v_exp_f32 v15, v15\n"
"v_mfma_f32_16x16x32_bf16 v[60:63], a[124:127], a[60:63], v[60:63]\n"
"s_add_u32 s41, s41, 32768\n"
"buffer_load_dwordx4 v225, s[8:11], s44 offen offset:1024 sc1 lds\n"
"v_mfma_f32_16x16x32_bf16 v[80:83], a[64:67], v[64:67], v[80:83]\n"
"s_cmp_eq_u32 s41, 163840\n"
"v_exp_f32 v20, v20\n"
"v_mfma_f32_16x16x32_bf16 v[112:115], a[64:67], v[68:71], v[112:115]\n"
"s_cselect_b32 s41, 0, s41\n"
"ds_read_b128 a[80:83], v227 offset:20480\n"
"v_add_u32 v226, s41, v224\n"
"v_mfma_f32_16x16x32_bf16 v[144:147], a[64:67], v[72:75], v[144:147]\n"
"v_exp_f32 v21, v21\n"
"v_cvt_pk_bf16_f32 v11, v14, v15\n"
"v_mfma_f32_16x16x32_bf16 v[176:179], a[64:67], v[76:79], v[176:179]\n"
"v_cvt_pk_bf16_f32 v18, v20, v21\n"
"v_mfma_f32_16x16x32_bf16 v[84:87], a[68:71], v[64:67], v[84:87]\n"
"v_exp_f32 v22, v22\n"
"v_mfma_f32_16x16x32_bf16 v[116:119], a[68:71], v[68:71], v[116:119]\n"
"ds_read_b128 a[84:87], v227 offset:21504\n"
"v_mfma_f32_16x16x32_bf16 v[148:151], a[68:71], v[72:75], v[148:151]\n"
"v_exp_f32 v23, v23\n"
"v_mfma_f32_16x16x32_bf16 v[180:183], a[68:71], v[76:79], v[180:183]\n"
"s_waitcnt lgkmcnt(2)\n"
"buffer_load_dwordx4 v225, s[8:11], s44 offen offset:2048 sc1 lds\n"
"v_mfma_f32_16x16x32_bf16 v[88:91], a[72:75], v[64:67], v[88:91]\n"
"v_exp_f32 v28, v28\n"
"v_mfma_f32_16x16x32_bf16 v[120:123], a[72:75], v[68:71], v[120:123]\n"
"ds_read_b128 a[88:91], v227 offset:22528\n"
"v_cvt_pk_bf16_f32 v19, v22, v23\n"
"v_mfma_f32_16x16x32_bf16 v[152:155], a[72:75], v[72:75], v[152:155]\n"
"v_exp_f32 v29, v29\n"
"v_mfma_f32_16x16x32_bf16 v[184:187], a[72:75], v[76:79], v[184:187]\n"
"v_cvt_pk_bf16_f32 v26, v28, v29\n"
"v_mfma_f32_16x16x32_bf16 v[92:95], a[76:79], v[64:67], v[92:95]\n"
"v_exp_f32 v30, v30\n"
"v_mfma_f32_16x16x32_bf16 v[124:127], a[76:79], v[68:71], v[124:127]\n"
"ds_read_b128 a[92:95], v227 offset:23552\n"
"v_mfma_f32_16x16x32_bf16 v[156:159], a[76:79], v[72:75], v[156:159]\n"
"v_exp_f32 v31, v31\n"
"v_mfma_f32_16x16x32_bf16 v[188:191], a[76:79], v[76:79], v[188:191]\n"
"s_waitcnt lgkmcnt(2)\n"
"buffer_load_dwordx4 v225, s[8:11], s44 offen offset:3072 sc1 lds\n"
"v_mfma_f32_16x16x32_bf16 v[96:99], a[80:83], v[64:67], v[96:99]\n"
"v_exp_f32 v40, v40\n"
"v_mfma_f32_16x16x32_bf16 v[128:131], a[80:83], v[68:71], v[128:131]\n"
"v_cvt_pk_bf16_f32 v27, v30, v31\n"
"v_mfma_f32_16x16x32_bf16 v[160:163], a[80:83], v[72:75], v[160:163]\n"
"v_exp_f32 v41, v41\n"
"v_mfma_f32_16x16x32_bf16 v[192:195], a[80:83], v[76:79], v[192:195]\n"
"v_mfma_f32_16x16x32_bf16 v[100:103], a[84:87], v[64:67], v[100:103]\n"
"v_exp_f32 v42, v42\n"
"v_mfma_f32_16x16x32_bf16 v[132:135], a[84:87], v[68:71], v[132:135]\n"
"ds_read_b128 a[96:99], v227 offset:24576\n"
"v_mfma_f32_16x16x32_bf16 v[164:167], a[84:87], v[72:75], v[164:167]\n"
"v_exp_f32 v43, v43\n"
"v_mfma_f32_16x16x32_bf16 v[196:199], a[84:87], v[76:79], v[196:199]\n"
"s_waitcnt lgkmcnt(1)\n"
"v_mfma_f32_16x16x32_bf16 v[104:107], a[88:91], v[64:67], v[104:107]\n"
"v_exp_f32 v48, v48\n"
"v_mfma_f32_16x16x32_bf16 v[136:139], a[88:91], v[68:71], v[136:139]\n"
"ds_read_b128 a[100:103], v227 offset:25600\n"
"v_mfma_f32_16x16x32_bf16 v[168:171], a[88:91], v[72:75], v[168:171]\n"
"v_exp_f32 v49, v49\n"
"v_mfma_f32_16x16x32_bf16 v[200:203], a[88:91], v[76:79], v[200:203]\n"
"ds_read_b128 a[104:107], v227 offset:26624\n"
"v_mfma_f32_16x16x32_bf16 v[108:111], a[92:95], v[64:67], v[108:111]\n"
"v_exp_f32 v50, v50\n"
"v_mfma_f32_16x16x32_bf16 v[140:143], a[92:95], v[68:71], v[140:143]\n"
"ds_read_b128 a[108:111], v227 offset:27648\n"
"v_mfma_f32_16x16x32_bf16 v[172:175], a[92:95], v[72:75], v[172:175]\n"
"v_exp_f32 v51, v51\n"
"v_mfma_f32_16x16x32_bf16 v[204:207], a[92:95], v[76:79], v[204:207]\n"
"v_mfma_f32_16x16x32_bf16 a[132:135], a[128:131], v[64:67], a[132:135]\n"
"v_exp_f32 v44, v44\n"
"v_mfma_f32_16x16x32_bf16 a[136:139], a[128:131], v[68:71], a[136:139]\n"
"ds_read_b128 a[112:115], v227 offset:28672\n"
"v_cvt_pk_bf16_f32 v68, v40, v41\n"
"v_cvt_pk_bf16_f32 v69, v42, v43\n"
"v_mfma_f32_16x16x32_bf16 a[140:143], a[128:131], v[72:75], a[140:143]\n"
"v_exp_f32 v45, v45\n"
"v_cvt_pk_bf16_f32 v72, v48, v49\n"
"v_mfma_f32_16x16x32_bf16 a[144:147], a[128:131], v[76:79], a[144:147]\n"
"s_waitcnt lgkmcnt(2)\n"
"ds_read_b128 a[116:119], v227 offset:29696\n"
"v_cvt_pk_bf16_f32 v73, v50, v51\n"
"v_mfma_f32_16x16x32_bf16 v[112:115], a[96:99], v[8:11], v[112:115]\n"
"v_exp_f32 v46, v46\n"
"v_mfma_f32_16x16x32_bf16 v[144:147], a[96:99], v[16:19], v[144:147]\n"
"v_cvt_pk_bf16_f32 v70, v44, v45\n"
"v_mfma_f32_16x16x32_bf16 v[176:179], a[96:99], v[24:27], v[176:179]\n"
"v_exp_f32 v47, v47\n"
"v_mfma_f32_16x16x32_bf16 v[116:119], a[100:103], v[8:11], v[116:119]\n"
"v_accvgpr_read_b32 v251, a132\n"
"v_subrev_u32 v253, 0x2b800000, v251\n"
"v_mfma_f32_16x16x32_bf16 v[148:151], a[100:103], v[16:19], v[148:151]\n"
"v_cmp_le_u32 vcc, 0x34000000, v253\n"
"s_cmp_lg_u64 vcc, 0\n"
"s_cbranch_scc1 L1014_%=\n"
"L1013_%=:\n"
"v_mfma_f32_16x16x32_bf16 v[180:183], a[100:103], v[24:27], v[180:183]\n"
"v_exp_f32 v52, v52\n"
"v_mfma_f32_16x16x32_bf16 v[120:123], a[104:107], v[8:11], v[120:123]\n"
"v_rcp_f32 v251, v251\n"
"v_cvt_pk_bf16_f32 v71, v46, v47\n"
"v_mfma_f32_16x16x32_bf16 v[152:155], a[104:107], v[16:19], v[152:155]\n"
"ds_read_b128 a[120:123], v227 offset:30720\n"
"ds_read_b128 a[124:127], v227 offset:31744\n"
"v_mfma_f32_16x16x32_bf16 v[184:187], a[104:107], v[24:27], v[184:187]\n"
"s_waitcnt lgkmcnt(2)\n"
"v_exp_f32 v53, v53\n"
"v_mul_f32 v80, v251, v80\n"
"v_mfma_f32_16x16x32_bf16 v[124:127], a[108:111], v[8:11], v[124:127]\n"
"v_exp_f32 v54, v54\n"
"v_mul_f32 v81, v251, v81\n"
"v_mfma_f32_16x16x32_bf16 v[156:159], a[108:111], v[16:19], v[156:159]\n"
"v_exp_f32 v55, v55\n"
"v_mul_f32 v82, v251, v82\n"
"v_mfma_f32_16x16x32_bf16 v[188:191], a[108:111], v[24:27], v[188:191]\n"
"v_mul_f32 v83, v251, v83\n"
"v_cvt_pk_bf16_f32 v74, v52, v53\n"
"v_mul_f32 v84, v251, v84\n"
"v_mfma_f32_16x16x32_bf16 v[128:131], a[112:115], v[8:11], v[128:131]\n"
"s_waitcnt vmcnt(17)\n"
"s_barrier\n"
"v_mfma_f32_16x16x32_bf16 v[160:163], a[112:115], v[16:19], v[160:163]\n"
"ds_read_b128 a[64:67], v226 offset:0\n"
"v_mul_f32 v85, v251, v85\n"
"v_mul_f32 v86, v251, v86\n"
"v_mfma_f32_16x16x32_bf16 v[192:195], a[112:115], v[24:27], v[192:195]\n"
"v_mul_f32 v87, v251, v87\n"
"v_cvt_pk_bf16_f32 v80, v80, v81\n"
"v_cvt_pk_bf16_f32 v81, v82, v83\n"
"v_mfma_f32_16x16x32_bf16 v[132:135], a[116:119], v[8:11], v[132:135]\n"
"ds_read_b128 a[68:71], v226 offset:1024\n"
"v_cvt_pk_bf16_f32 v82, v84, v85\n"
"v_cvt_pk_bf16_f32 v83, v86, v87\n"
"v_mfma_f32_16x16x32_bf16 v[164:167], a[116:119], v[16:19], v[164:167]\n"
"buffer_store_dwordx4 v[80:83], v224, s[8:11], s59 offen offset:0 sc1\n"
"v_cvt_pk_bf16_f32 v75, v54, v55\n"
"v_mfma_f32_16x16x32_bf16 v[196:199], a[116:119], v[24:27], v[196:199]\n"
"s_waitcnt lgkmcnt(2)\n"
"s_waitcnt vmcnt(4)\n"
"v_cmp_eq_u64 vcc, s[28:29], v[236:237]\n"
"s_cmp_eq_u64 vcc, exec\n"
"s_cbranch_scc0 L1019_%=\n"
"L1020_%=:\n"
"v_mfma_f32_16x16x32_bf16 v[136:139], a[120:123], v[8:11], v[136:139]\n"
"ds_read_b128 a[72:75], v226 offset:2048\n"
"v_mul_f32 v88, v251, v88\n"
"v_mul_f32 v89, v251, v89\n"
"v_mfma_f32_16x16x32_bf16 v[168:171], a[120:123], v[16:19], v[168:171]\n"
"v_mul_f32 v90, v251, v90\n"
"v_mul_f32 v91, v251, v91\n"
"v_mul_f32 v92, v251, v92\n"
"v_mfma_f32_16x16x32_bf16 v[200:203], a[120:123], v[24:27], v[200:203]\n"
"v_mul_f32 v93, v251, v93\n"
"v_mul_f32 v94, v251, v94\n"
"v_mul_f32 v95, v251, v95\n"
"v_mfma_f32_16x16x32_bf16 v[140:143], a[124:127], v[8:11], v[140:143]\n"
"ds_read_b128 a[76:79], v226 offset:3072\n"
"v_cvt_pk_bf16_f32 v88, v88, v89\n"
"v_cvt_pk_bf16_f32 v89, v90, v91\n"
"v_mfma_f32_16x16x32_bf16 v[172:175], a[124:127], v[16:19], v[172:175]\n"
"v_cvt_pk_bf16_f32 v90, v92, v93\n"
"v_cvt_pk_bf16_f32 v91, v94, v95\n"
"v_mul_f32 v96, v251, v96\n"
"v_mfma_f32_16x16x32_bf16 v[204:207], a[124:127], v[24:27], v[204:207]\n"
"v_mul_f32 v97, v251, v97\n"
"v_mul_f32 v104, v251, v104\n"
"v_mul_f32 v98, v251, v98\n"
"v_mfma_f32_16x16x32_bf16 a[136:139], a[128:131], v[8:11], a[136:139]\n"
"v_mul_f32 v105, v251, v105\n"
"v_mul_f32 v99, v251, v99\n"
"v_cvt_pk_bf16_f32 v96, v96, v97\n"
"v_mfma_f32_16x16x32_bf16 a[140:143], a[128:131], v[16:19], a[140:143]\n"
"v_mul_f32 v106, v251, v106\n"
"v_mul_f32 v107, v251, v107\n"
"v_mul_f32 v100, v251, v100\n"
"v_mfma_f32_16x16x32_bf16 a[144:147], a[128:131], v[24:27], a[144:147]\n"
"v_mul_f32 v101, v251, v101\n"
"v_mul_f32 v102, v251, v102\n"
"v_mul_f32 v103, v251, v103\n"
"buffer_store_dwordx4 v[88:91], v224, s[8:11], s59 offen offset:1024 sc1\n"
"v_cvt_pk_bf16_f32 v97, v98, v99\n"
"v_cvt_pk_bf16_f32 v98, v100, v101\n"
"v_cvt_pk_bf16_f32 v99, v102, v103\n"
"buffer_store_dwordx4 v[96:99], v224, s[8:11], s59 offen offset:2048 sc1\n"
"v_mul_f32 v108, v251, v108\n"
"v_mul_f32 v109, v251, v109\n"
"v_mul_f32 v110, v251, v110\n"
"v_mul_f32 v111, v251, v111\n"
"v_cvt_pk_bf16_f32 v104, v104, v105\n"
"v_cvt_pk_bf16_f32 v105, v106, v107\n"
"v_cvt_pk_bf16_f32 v106, v108, v109\n"
"v_cvt_pk_bf16_f32 v107, v110, v111\n"
"buffer_store_dwordx4 v[104:107], v224, s[8:11], s59 offen offset:3072 sc1\n"
"ds_read_b128 a[80:83], v226 offset:4096\n"
"ds_read_b128 a[84:87], v226 offset:5120\n"
"s_cmp_eq_u32 s34, 7\n"
"s_cbranch_scc1 LD2_%=\n"
"s_waitcnt lgkmcnt(5)\n"
"v_mfma_f32_16x16x32_bf16 v[0:3], a[64:67], a[32:35], 0\n"
"s_mov_b32 m0, s40\n"
"v_exp_f32 v56, v56\n"
"v_mfma_f32_16x16x32_bf16 v[8:11], a[64:67], a[48:51], 0\n"
"s_waitcnt lgkmcnt(4)\n"
"ds_read_b128 a[88:91], v226 offset:6144\n"
"v_readlane_b32 s43, v228, s42\n"
"v_mfma_f32_16x16x32_bf16 v[0:3], a[68:71], a[36:39], v[0:3]\n"
"v_exp_f32 v57, v57\n"
"v_mfma_f32_16x16x32_bf16 v[8:11], a[68:71], a[52:55], v[8:11]\n"
"s_waitcnt lgkmcnt(4)\n"
"ds_read_b128 a[92:95], v226 offset:7168\n"
"ds_read_b128 a[96:99], v226 offset:8192\n"
"v_mfma_f32_16x16x32_bf16 v[0:3], a[72:75], a[40:43], v[0:3]\n"
"s_add_u32 s41, s41, 32768\n"
"buffer_load_dwordx4 v225, s[8:11], s43 offen offset:0 sc1 lds\n"
"v_readlane_b32 s44, v229, s42\n"
"v_mfma_f32_16x16x32_bf16 v[8:11], a[72:75], a[56:59], v[8:11]\n"
"s_waitcnt lgkmcnt(3)\n"
"v_exp_f32 v58, v58\n"
"v_mfma_f32_16x16x32_bf16 v[0:3], a[76:79], a[44:47], v[0:3]\n"
"s_add_u32 s42, s42, 1\n"
"ds_read_b128 a[100:103], v226 offset:9216\n"
"v_cvt_pk_bf16_f32 v76, v56, v57\n"
"v_mfma_f32_16x16x32_bf16 v[8:11], a[76:79], a[60:63], v[8:11]\n"
"s_cmp_eq_u32 s41, 163840\n"
"v_exp_f32 v59, v59\n"
"v_mfma_f32_16x16x32_bf16 v[4:7], a[80:83], a[32:35], 0\n"
"s_cselect_b32 s41, 0, s41\n"
"v_exp_f32 v60, v60\n"
"v_add_u32 v227, s41, v224\n"
"v_mfma_f32_16x16x32_bf16 v[12:15], a[80:83], a[48:51], 0\n"
"ds_read_b128 a[104:107], v226 offset:10240\n"
"v_cvt_pk_bf16_f32 v77, v58, v59\n"
"v_mfma_f32_16x16x32_bf16 v[4:7], a[84:87], a[36:39], v[4:7]\n"
"v_exp_f32 v61, v61\n"
"v_mfma_f32_16x16x32_bf16 v[12:15], a[84:87], a[52:55], v[12:15]\n"
"s_waitcnt lgkmcnt(2)\n"
"buffer_load_dwordx4 v225, s[8:11], s43 offen offset:1024 sc1 lds\n"
"v_mfma_f32_16x16x32_bf16 v[4:7], a[88:91], a[40:43], v[4:7]\n"
"ds_read_b128 a[108:111], v226 offset:11264\n"
"v_cvt_pk_bf16_f32 v78, v60, v61\n"
"v_mfma_f32_16x16x32_bf16 v[12:15], a[88:91], a[56:59], v[12:15]\n"
"ds_read_b128 a[112:115], v226 offset:12288\n"
"v_mfma_f32_16x16x32_bf16 v[4:7], a[92:95], a[44:47], v[4:7]\n"
"v_exp_f32 v62, v62\n"
"v_mfma_f32_16x16x32_bf16 v[12:15], a[92:95], a[60:63], v[12:15]\n"
"ds_read_b128 a[116:119], v226 offset:13312\n"
"v_mfma_f32_16x16x32_bf16 v[16:19], a[96:99], a[32:35], 0\n"
"v_exp_f32 v63, v63\n"
"v_mfma_f32_16x16x32_bf16 v[24:27], a[96:99], a[48:51], 0\n"
"s_waitcnt lgkmcnt(3)\n"
"v_exp_f32 v0, v0\n"
"v_mfma_f32_16x16x32_bf16 v[16:19], a[100:103], a[36:39], v[16:19]\n"
"ds_read_b128 a[120:123], v226 offset:14336\n"
"v_cvt_pk_bf16_f32 v79, v62, v63\n"
"v_mfma_f32_16x16x32_bf16 v[24:27], a[100:103], a[52:55], v[24:27]\n"
"buffer_load_dwordx4 v225, s[8:11], s43 offen offset:2048 sc1 lds\n"
"v_mfma_f32_16x16x32_bf16 v[16:19], a[104:107], a[40:43], v[16:19]\n"
"v_exp_f32 v1, v1\n"
"v_mfma_f32_16x16x32_bf16 v[24:27], a[104:107], a[56:59], v[24:27]\n"
"s_waitcnt lgkmcnt(2)\n"
"ds_read_b128 a[124:127], v226 offset:15360\n"
"ds_read_b128 a[64:67], v226 offset:16384\n"
"v_mfma_f32_16x16x32_bf16 v[16:19], a[108:111], a[44:47], v[16:19]\n"
"v_exp_f32 v2, v2\n"
"v_cvt_pk_bf16_f32 v0, v0, v1\n"
"v_mfma_f32_16x16x32_bf16 v[24:27], a[108:111], a[60:63], v[24:27]\n"
"ds_read_b128 a[68:71], v226 offset:17408\n"
"v_mfma_f32_16x16x32_bf16 v[20:23], a[112:115], a[32:35], 0\n"
"v_exp_f32 v3, v3\n"
"v_mfma_f32_16x16x32_bf16 v[28:31], a[112:115], a[48:51], 0\n"
"s_waitcnt lgkmcnt(3)\n"
"v_exp_f32 v8, v8\n"
"v_mfma_f32_16x16x32_bf16 v[20:23], a[116:119], a[36:39], v[20:23]\n"
"buffer_load_dwordx4 v225, s[8:11], s43 offen offset:3072 sc1 lds\n"
"v_cvt_pk_bf16_f32 v1, v2, v3\n"
"v_mfma_f32_16x16x32_bf16 v[28:31], a[116:119], a[52:55], v[28:31]\n"
"ds_read_b128 a[72:75], v226 offset:18432\n"
"v_mfma_f32_16x16x32_bf16 v[20:23], a[120:123], a[40:43], v[20:23]\n"
"v_exp_f32 v9, v9\n"
"v_mfma_f32_16x16x32_bf16 v[28:31], a[120:123], a[56:59], v[28:31]\n"
"s_waitcnt lgkmcnt(2)\n"
"ds_read_b128 a[76:79], v226 offset:19456\n"
"ds_read_b128 a[80:83], v226 offset:20480\n"
"v_mfma_f32_16x16x32_bf16 v[20:23], a[124:127], a[44:47], v[20:23]\n"
"s_add_u32 m0, s40, 16384\n"
"v_exp_f32 v10, v10\n"
"v_cvt_pk_bf16_f32 v8, v8, v9\n"
"v_mfma_f32_16x16x32_bf16 v[28:31], a[124:127], a[60:63], v[28:31]\n"
"ds_read_b128 a[84:87], v226 offset:21504\n"
"v_mfma_f32_16x16x32_bf16 v[32:35], a[64:67], a[32:35], 0\n"
"v_exp_f32 v11, v11\n"
"v_mfma_f32_16x16x32_bf16 v[40:43], a[64:67], a[48:51], 0\n"
"s_waitcnt lgkmcnt(4)\n"
"v_exp_f32 v4, v4\n"
"v_mfma_f32_16x16x32_bf16 v[32:35], a[68:71], a[36:39], v[32:35]\n"
"s_add_u32 s40, s40, 32768\n"
"buffer_load_dwordx4 v225, s[8:11], s44 offen offset:0 sc1 lds\n"
"v_cvt_pk_bf16_f32 v9, v10, v11\n"
"v_mfma_f32_16x16x32_bf16 v[40:43], a[68:71], a[52:55], v[40:43]\n"
"s_waitcnt lgkmcnt(3)\n"
"ds_read_b128 a[88:91], v226 offset:22528\n"
"s_cmp_eq_u32 s40, s46\n"
"v_mfma_f32_16x16x32_bf16 v[32:35], a[72:75], a[40:43], v[32:35]\n"
"s_cselect_b32 s40, s47, s40\n"
"v_exp_f32 v5, v5\n"
"v_mfma_f32_16x16x32_bf16 v[40:43], a[72:75], a[56:59], v[40:43]\n"
"s_waitcnt lgkmcnt(2)\n"
"ds_read_b128 a[92:95], v226 offset:23552\n"
"ds_read_b128 a[96:99], v226 offset:24576\n"
"v_mfma_f32_16x16x32_bf16 v[32:35], a[76:79], a[44:47], v[32:35]\n"
"v_exp_f32 v6, v6\n"
"v_cvt_pk_bf16_f32 v2, v4, v5\n"
"v_mfma_f32_16x16x32_bf16 v[40:43], a[76:79], a[60:63], v[40:43]\n"
"ds_read_b128 a[100:103], v226 offset:25600\n"
"v_mfma_f32_16x16x32_bf16 v[36:39], a[80:83], a[32:35], 0\n"
"v_exp_f32 v7, v7\n"
"v_mfma_f32_16x16x32_bf16 v[44:47], a[80:83], a[48:51], 0\n"
"s_waitcnt lgkmcnt(4)\n"
"buffer_load_dwordx4 v225, s[8:11], s44 offen offset:1024 sc1 lds\n"
"v_mfma_f32_16x16x32_bf16 v[36:39], a[84:87], a[36:39], v[36:39]\n"
"v_exp_f32 v12, v12\n"
"v_cvt_pk_bf16_f32 v3, v6, v7\n"
"v_mfma_f32_16x16x32_bf16 v[44:47], a[84:87], a[52:55], v[44:47]\n"
"s_waitcnt lgkmcnt(3)\n"
"ds_read_b128 a[104:107], v226 offset:26624\n"
"ds_read_b128 a[108:111], v226 offset:27648\n"
"v_mfma_f32_16x16x32_bf16 v[36:39], a[88:91], a[40:43], v[36:39]\n"
"v_exp_f32 v13, v13\n"
"v_mfma_f32_16x16x32_bf16 v[44:47], a[88:91], a[56:59], v[44:47]\n"
"s_waitcnt lgkmcnt(3)\n"
"ds_read_b128 a[112:115], v226 offset:28672\n"
"ds_read_b128 a[116:119], v226 offset:29696\n"
"v_mfma_f32_16x16x32_bf16 v[36:39], a[92:95], a[44:47], v[36:39]\n"
"v_exp_f32 v14, v14\n"
"v_cvt_pk_bf16_f32 v10, v12, v13\n"
"v_mfma_f32_16x16x32_bf16 v[44:47], a[92:95], a[60:63], v[44:47]\n"
"v_exp_f32 v15, v15\n"
"v_mfma_f32_16x16x32_bf16 v[48:51], a[96:99], a[32:35], 0\n"
"buffer_load_dwordx4 v225, s[8:11], s44 offen offset:2048 sc1 lds\n"
"v_mfma_f32_16x16x32_bf16 v[56:59], a[96:99], a[48:51], 0\n"
"s_waitcnt lgkmcnt(4)\n"
"v_exp_f32 v16, v16\n"
"v_mfma_f32_16x16x32_bf16 v[48:51], a[100:103], a[36:39], v[48:51]\n"
"ds_read_b128 a[120:123], v226 offset:30720\n"
"v_cvt_pk_bf16_f32 v11, v14, v15\n"
"v_mfma_f32_16x16x32_bf16 v[56:59], a[100:103], a[52:55], v[56:59]\n"
"s_waitcnt lgkmcnt(3)\n"
"v_exp_f32 v17, v17\n"
"v_mfma_f32_16x16x32_bf16 v[48:51], a[104:107], a[40:43], v[48:51]\n"
"ds_read_b128 a[124:127], v226 offset:31744\n"
"v_cvt_pk_bf16_f32 v16, v16, v17\n"
"v_mfma_f32_16x16x32_bf16 v[56:59], a[104:107], a[56:59], v[56:59]\n"
"s_waitcnt vmcnt(15)\n"
"s_barrier\n"
"v_mfma_f32_16x16x32_bf16 v[48:51], a[108:111], a[44:47], v[48:51]\n"
"ds_read_b128 a[96:99], v227 offset:8192\n"
"v_mfma_f32_16x16x32_bf16 v[56:59], a[108:111], a[60:63], v[56:59]\n"
"s_waitcnt lgkmcnt(3)\n"
"v_exp_f32 v18, v18\n"
"v_mfma_f32_16x16x32_bf16 v[52:55], a[112:115], a[32:35], 0\n"
"buffer_load_dwordx4 v225, s[8:11], s44 offen offset:3072 sc1 lds\n"
"v_mfma_f32_16x16x32_bf16 v[60:63], a[112:115], a[48:51], 0\n"
"v_exp_f32 v19, v19\n"
"v_mfma_f32_16x16x32_bf16 v[52:55], a[116:119], a[36:39], v[52:55]\n"
"ds_read_b128 a[100:103], v227 offset:9216\n"
"v_readlane_b32 s43, v228, s42\n"
"v_mfma_f32_16x16x32_bf16 v[60:63], a[116:119], a[52:55], v[60:63]\n"
"s_waitcnt lgkmcnt(2)\n"
"v_exp_f32 v24, v24\n"
"v_mfma_f32_16x16x32_bf16 v[52:55], a[120:123], a[40:43], v[52:55]\n"
"s_mov_b32 m0, s40\n"
"ds_read_b128 a[104:107], v227 offset:10240\n"
"ds_read_b128 a[108:111], v227 offset:11264\n"
"v_cvt_pk_bf16_f32 v17, v18, v19\n"
"v_mfma_f32_16x16x32_bf16 v[60:63], a[120:123], a[56:59], v[60:63]\n"
"v_exp_f32 v25, v25\n"
"v_mfma_f32_16x16x32_bf16 v[52:55], a[124:127], a[44:47], v[52:55]\n"
"v_exp_f32 v26, v26\n"
"v_mfma_f32_16x16x32_bf16 v[60:63], a[124:127], a[60:63], v[60:63]\n"
"s_waitcnt lgkmcnt(3)\n"
"buffer_load_dwordx4 v225, s[8:11], s43 offen offset:0 sc1 lds\n"
"v_mfma_f32_16x16x32_bf16 v[112:115], a[96:99], v[68:71], v[112:115]\n"
"s_add_u32 s41, s41, 32768\n"
"v_exp_f32 v27, v27\n"
"v_readlane_b32 s44, v229, s42\n"
"v_mfma_f32_16x16x32_bf16 v[144:147], a[96:99], v[72:75], v[144:147]\n"
"s_cmp_eq_u32 s41, 163840\n"
"ds_read_b128 a[112:115], v227 offset:12288\n"
"v_cvt_pk_bf16_f32 v24, v24, v25\n"
"v_mfma_f32_16x16x32_bf16 v[176:179], a[96:99], v[76:79], v[176:179]\n"
"s_waitcnt lgkmcnt(3)\n"
"v_exp_f32 v20, v20\n"
"v_mfma_f32_16x16x32_bf16 v[116:119], a[100:103], v[68:71], v[116:119]\n"
"s_cselect_b32 s41, 0, s41\n"
"ds_read_b128 a[116:119], v227 offset:13312\n"
"v_add_u32 v226, s41, v224\n"
"v_mfma_f32_16x16x32_bf16 v[148:151], a[100:103], v[72:75], v[148:151]\n"
"s_add_u32 s42, s42, 1\n"
"v_exp_f32 v21, v21\n"
"v_cvt_pk_bf16_f32 v25, v26, v27\n"
"v_mfma_f32_16x16x32_bf16 v[180:183], a[100:103], v[76:79], v[180:183]\n"
"s_waitcnt lgkmcnt(2)\n"
"ds_read_b128 a[120:123], v227 offset:14336\n"
"v_cvt_pk_bf16_f32 v18, v20, v21\n"
"v_mfma_f32_16x16x32_bf16 v[120:123], a[104:107], v[68:71], v[120:123]\n"
"v_exp_f32 v22, v22\n"
"v_mfma_f32_16x16x32_bf16 v[152:155], a[104:107], v[72:75], v[152:155]\n"
"buffer_load_dwordx4 v225, s[8:11], s43 offen offset:1024 sc1 lds\n"
"v_mfma_f32_16x16x32_bf16 v[184:187], a[104:107], v[76:79], v[184:187]\n"
"v_exp_f32 v23, v23\n"
"v_mfma_f32_16x16x32_bf16 v[124:127], a[108:111], v[68:71], v[124:127]\n"
"ds_read_b128 a[124:127], v227 offset:15360\n"
"v_cvt_pk_bf16_f32 v19, v22, v23\n"
"v_mfma_f32_16x16x32_bf16 v[156:159], a[108:111], v[72:75], v[156:159]\n"
"v_exp_f32 v28, v28\n"
"v_mfma_f32_16x16x32_bf16 v[188:191], a[108:111], v[76:79], v[188:191]\n"
"s_waitcnt lgkmcnt(2)\n"
"v_mfma_f32_16x16x32_bf16 v[128:131], a[112:115], v[68:71], v[128:131]\n"
"v_exp_f32 v29, v29\n"
"v_mfma_f32_16x16x32_bf16 v[160:163], a[112:115], v[72:75], v[160:163]\n"
"ds_read_b128 a[64:67], v227 offset:16384\n"
"v_cvt_pk_bf16_f32 v26, v28, v29\n"
"v_mfma_f32_16x16x32_bf16 v[192:195], a[112:115], v[76:79], v[192:195]\n"
"v_exp_f32 v30, v30\n"
"v_mfma_f32_16x16x32_bf16 v[132:135], a[116:119], v[68:71], v[132:135]\n"
"buffer_load_dwordx4 v225, s[8:11], s43 offen offset:2048 sc1 lds\n"
"v_mfma_f32_16x16x32_bf16 v[164:167], a[116:119], v[72:75], v[164:167]\n"
"v_exp_f32 v31, v31\n"
"v_mfma_f32_16x16x32_bf16 v[196:199], a[116:119], v[76:79], v[196:199]\n"
"s_waitcnt lgkmcnt(1)\n"
"ds_read_b128 a[68:71], v227 offset:17408\n"
"v_cvt_pk_bf16_f32 v27, v30, v31\n"
"v_mfma_f32_16x16x32_bf16 v[136:139], a[120:123], v[68:71], v[136:139]\n"
"v_exp_f32 v32, v32\n"
"v_mfma_f32_16x16x32_bf16 v[168:171], a[120:123], v[72:75], v[168:171]\n"
"ds_read_b128 a[72:75], v227 offset:18432\n"
"ds_read_b128 a[76:79], v227 offset:19456\n"
"v_mfma_f32_16x16x32_bf16 v[200:203], a[120:123], v[76:79], v[200:203]\n"
"v_exp_f32 v33, v33\n"
"v_mfma_f32_16x16x32_bf16 v[140:143], a[124:127], v[68:71], v[140:143]\n"
"ds_read_b128 a[80:83], v227 offset:20480\n"
"v_cvt_pk_bf16_f32 v64, v32, v33\n"
"v_mfma_f32_16x16x32_bf16 v[172:175], a[124:127], v[72:75], v[172:175]\n"
"buffer_load_dwordx4 v225, s[8:11], s43 offen offset:3072 sc1 lds\n"
"v_mfma_f32_16x16x32_bf16 v[204:207], a[124:127], v[76:79], v[204:207]\n"
"v_exp_f32 v34, v34\n"
"v_mfma_f32_16x16x32_bf16 a[136:139], a[128:131], v[68:71], a[136:139]\n"
"v_exp_f32 v35, v35\n"
"v_mfma_f32_16x16x32_bf16 a[140:143], a[128:131], v[72:75], a[140:143]\n"
"s_add_u32 m0, s40, 16384\n"
"ds_read_b128 a[84:87], v227 offset:21504\n"
"v_cvt_pk_bf16_f32 v65, v34, v35\n"
"v_mfma_f32_16x16x32_bf16 a[144:147], a[128:131], v[76:79], a[144:147]\n"
"s_waitcnt lgkmcnt(2)\n"
"v_exp_f32 v40, v40\n"
"v_mfma_f32_16x16x32_bf16 v[144:147], a[64:67], v[0:3], v[144:147]\n"
"ds_read_b128 a[88:91], v227 offset:22528\n"
"ds_read_b128 a[92:95], v227 offset:23552\n"
"v_mfma_f32_16x16x32_bf16 v[176:179], a[64:67], v[8:11], v[176:179]\n"
"v_exp_f32 v41, v41\n"
"v_mfma_f32_16x16x32_bf16 v[148:151], a[68:71], v[0:3], v[148:151]\n"
"s_add_u32 s51, s59, 16384\n"
"buffer_load_dwordx4 v225, s[8:11], s44 offen offset:0 sc1 lds\n"
"v_mfma_f32_16x16x32_bf16 v[180:183], a[68:71], v[8:11], v[180:183]\n"
"s_add_u32 s40, s40, 32768\n"
"v_exp_f32 v42, v42\n"
"v_mfma_f32_16x16x32_bf16 v[152:155], a[72:75], v[0:3], v[152:155]\n"
"s_cmp_eq_u32 s40, s46\n"
"v_accvgpr_read_b32 v252, a136\n"
"v_subrev_u32 v253, 0x2b800000, v252\n"
"v_mfma_f32_16x16x32_bf16 v[184:187], a[72:75], v[8:11], v[184:187]\n"
"s_cselect_b32 s40, s47, s40\n"
"v_exp_f32 v43, v43\n"
"v_cvt_pk_bf16_f32 v68, v40, v41\n"
"v_mfma_f32_16x16x32_bf16 v[156:159], a[76:79], v[0:3], v[156:159]\n"
"v_cmp_le_u32 vcc, 0x34000000, v253\n"
"s_cmp_lg_u64 vcc, 0\n"
"s_cbranch_scc1 L1023_%=\n"
"L1022_%=:\n"
"v_mfma_f32_16x16x32_bf16 v[188:191], a[76:79], v[8:11], v[188:191]\n"
"s_waitcnt lgkmcnt(0)\n"
"v_rcp_f32 v252, v252\n"
"v_mfma_f32_16x16x32_bf16 v[160:163], a[80:83], v[0:3], v[160:163]\n"
"ds_read_b128 a[96:99], v227 offset:24576\n"
"ds_read_b128 a[100:103], v227 offset:25600\n"
"v_mul_f32 v112, v252, v112\n"
"v_mfma_f32_16x16x32_bf16 v[192:195], a[80:83], v[8:11], v[192:195]\n"
"v_exp_f32 v36, v36\n"
"v_mul_f32 v113, v252, v113\n"
"v_mfma_f32_16x16x32_bf16 v[164:167], a[84:87], v[0:3], v[164:167]\n"
"ds_read_b128 a[104:107], v227 offset:26624\n"
"ds_read_b128 a[108:111], v227 offset:27648\n"
"v_cvt_pk_bf16_f32 v69, v42, v43\n"
"v_mfma_f32_16x16x32_bf16 v[196:199], a[84:87], v[8:11], v[196:199]\n"
"v_exp_f32 v37, v37\n"
"v_mul_f32 v114, v252, v114\n"
"v_mfma_f32_16x16x32_bf16 v[168:171], a[88:91], v[0:3], v[168:171]\n"
"buffer_load_dwordx4 v225, s[8:11], s44 offen offset:1024 sc1 lds\n"
"v_mul_f32 v115, v252, v115\n"
"v_mfma_f32_16x16x32_bf16 v[200:203], a[88:91], v[8:11], v[200:203]\n"
"v_exp_f32 v38, v38\n"
"v_mul_f32 v116, v252, v116\n"
"v_mfma_f32_16x16x32_bf16 v[172:175], a[92:95], v[0:3], v[172:175]\n"
"v_exp_f32 v39, v39\n"
"v_mul_f32 v117, v252, v117\n"
"v_mfma_f32_16x16x32_bf16 v[204:207], a[92:95], v[8:11], v[204:207]\n"
"ds_read_b128 a[112:115], v227 offset:28672\n"
"v_mul_f32 v118, v252, v118\n"
"v_mul_f32 v119, v252, v119\n"
"v_mfma_f32_16x16x32_bf16 a[140:143], a[128:131], v[0:3], a[140:143]\n"
"v_exp_f32 v44, v44\n"
"v_cvt_pk_bf16_f32 v112, v112, v113\n"
"v_mfma_f32_16x16x32_bf16 a[144:147], a[128:131], v[8:11], a[144:147]\n"
"s_waitcnt lgkmcnt(1)\n"
"ds_read_b128 a[116:119], v227 offset:29696\n"
"ds_read_b128 a[120:123], v227 offset:30720\n"
"v_cvt_pk_bf16_f32 v66, v36, v37\n"
"v_mfma_f32_16x16x32_bf16 v[144:147], a[96:99], v[16:19], v[144:147]\n"
"buffer_load_dwordx4 v225, s[8:11], s44 offen offset:2048 sc1 lds\n"
"v_cvt_pk_bf16_f32 v113, v114, v115\n"
"v_mfma_f32_16x16x32_bf16 v[176:179], a[96:99], v[24:27], v[176:179]\n"
"v_exp_f32 v45, v45\n"
"v_cvt_pk_bf16_f32 v114, v116, v117\n"
"v_mfma_f32_16x16x32_bf16 v[148:151], a[100:103], v[16:19], v[148:151]\n"
"ds_read_b128 a[124:127], v227 offset:31744\n"
"v_cvt_pk_bf16_f32 v115, v118, v119\n"
"v_mul_f32 v120, v252, v120\n"
"v_mfma_f32_16x16x32_bf16 v[180:183], a[100:103], v[24:27], v[180:183]\n"
"buffer_store_dwordx4 v[112:115], v224, s[8:11], s51 offen offset:0 sc1\n"
"v_mul_f32 v121, v252, v121\n"
"v_mfma_f32_16x16x32_bf16 v[152:155], a[104:107], v[16:19], v[152:155]\n"
"v_exp_f32 v46, v46\n"
"v_mul_f32 v122, v252, v122\n"
"v_mfma_f32_16x16x32_bf16 v[184:187], a[104:107], v[24:27], v[184:187]\n"
"s_waitcnt vmcnt(16)\n"
"s_barrier\n"
"v_mfma_f32_16x16x32_bf16 v[156:159], a[108:111], v[16:19], v[156:159]\n"
"ds_read_b128 a[64:67], v226 offset:0\n"
"v_cvt_pk_bf16_f32 v67, v38, v39\n"
"v_mul_f32 v123, v252, v123\n"
"v_mfma_f32_16x16x32_bf16 v[188:191], a[108:111], v[24:27], v[188:191]\n"
"s_waitcnt lgkmcnt(2)\n"
"v_exp_f32 v47, v47\n"
"v_mul_f32 v124, v252, v124\n"
"v_mfma_f32_16x16x32_bf16 v[160:163], a[112:115], v[16:19], v[160:163]\n"
"ds_read_b128 a[68:71], v226 offset:1024\n"
"ds_read_b128 a[72:75], v226 offset:2048\n"
"v_mul_f32 v125, v252, v125\n"
"v_mfma_f32_16x16x32_bf16 v[192:195], a[112:115], v[24:27], v[192:195]\n"
"buffer_load_dwordx4 v225, s[8:11], s44 offen offset:3072 sc1 lds\n"
"v_mul_f32 v126, v252, v126\n"
"v_mfma_f32_16x16x32_bf16 v[164:167], a[116:119], v[16:19], v[164:167]\n"
"v_exp_f32 v48, v48\n"
"v_mul_f32 v127, v252, v127\n"
"v_mfma_f32_16x16x32_bf16 v[196:199], a[116:119], v[24:27], v[196:199]\n"
"v_exp_f32 v49, v49\n"
"v_cvt_pk_bf16_f32 v120, v120, v121\n"
"v_mfma_f32_16x16x32_bf16 v[168:171], a[120:123], v[16:19], v[168:171]\n"
"v_exp_f32 v50, v50\n"
"v_cvt_pk_bf16_f32 v121, v122, v123\n"
"v_mfma_f32_16x16x32_bf16 v[200:203], a[120:123], v[24:27], v[200:203]\n"
"s_waitcnt lgkmcnt(2)\n"
"ds_read_b128 a[76:79], v226 offset:3072\n"
"v_cvt_pk_bf16_f32 v70, v44, v45\n"
"v_cvt_pk_bf16_f32 v122, v124, v125\n"
"v_mfma_f32_16x16x32_bf16 v[172:175], a[124:127], v[16:19], v[172:175]\n"
"v_mul_f32 v128, v252, v128\n"
"v_mul_f32 v129, v252, v129\n"
"v_mul_f32 v136, v252, v136\n"
"v_mfma_f32_16x16x32_bf16 v[204:207], a[124:127], v[24:27], v[204:207]\n"
"v_mul_f32 v137, v252, v137\n"
"v_mul_f32 v130, v252, v130\n"
"v_mul_f32 v131, v252, v131\n"
"v_mfma_f32_16x16x32_bf16 a[140:143], a[128:131], v[16:19], a[140:143]\n"
"v_cvt_pk_bf16_f32 v128, v128, v129\n"
"v_mul_f32 v138, v252, v138\n"
"v_mul_f32 v139, v252, v139\n"
"v_mfma_f32_16x16x32_bf16 a[144:147], a[128:131], v[24:27], a[144:147]\n"
"v_cvt_pk_bf16_f32 v136, v136, v137\n"
"v_mul_f32 v132, v252, v132\n"
"v_mul_f32 v133, v252, v133\n"
"v_cvt_pk_bf16_f32 v71, v46, v47\n"
"v_cvt_pk_bf16_f32 v72, v48, v49\n"
"v_exp_f32 v51, v51\n"
"s_nop 0\n"
"v_cvt_pk_bf16_f32 v73, v50, v51\n"
"v_exp_f32 v52, v52\n"
"v_exp_f32 v53, v53\n"
"s_nop 0\n"
"v_cvt_pk_bf16_f32 v74, v52, v53\n"
"v_exp_f32 v54, v54\n"
"v_exp_f32 v55, v55\n"
"s_nop 0\n"
"v_cvt_pk_bf16_f32 v75, v54, v55\n"
"v_cvt_pk_bf16_f32 v123, v126, v127\n"
"buffer_store_dwordx4 v[120:123], v224, s[8:11], s51 offen offset:1024 sc1\n"
"v_mul_f32 v134, v252, v134\n"
"v_mul_f32 v135, v252, v135\n"
"v_cvt_pk_bf16_f32 v129, v130, v131\n"
"v_cvt_pk_bf16_f32 v130, v132, v133\n"
"v_cvt_pk_bf16_f32 v131, v134, v135\n"
"buffer_store_dwordx4 v[128:131], v224, s[8:11], s51 offen offset:2048 sc1\n"
"v_mul_f32 v140, v252, v140\n"
"v_mul_f32 v141, v252, v141\n"
"v_mul_f32 v142, v252, v142\n"
"v_mul_f32 v143, v252, v143\n"
"v_cvt_pk_bf16_f32 v137, v138, v139\n"
"v_cvt_pk_bf16_f32 v138, v140, v141\n"
"v_cvt_pk_bf16_f32 v139, v142, v143\n"
"buffer_store_dwordx4 v[136:139], v224, s[8:11], s51 offen offset:3072 sc1\n"
"ds_read_b128 a[80:83], v226 offset:4096\n"
"ds_read_b128 a[84:87], v226 offset:5120\n"
".p2align 6\n"
"LP2_%=:\n"
"s_waitcnt lgkmcnt(5)\n"
"v_mfma_f32_16x16x32_bf16 v[0:3], a[64:67], a[32:35], 0\n"
"v_exp_f32 v56, v56\n"
"v_mfma_f32_16x16x32_bf16 v[8:11], a[64:67], a[48:51], 0\n"
"s_waitcnt lgkmcnt(4)\n"
"ds_read_b128 a[88:91], v226 offset:6144\n"
"v_readlane_b32 s43, v228, s42\n"
"v_mfma_f32_16x16x32_bf16 v[0:3], a[68:71], a[36:39], v[0:3]\n"
"v_exp_f32 v57, v57\n"
"v_mfma_f32_16x16x32_bf16 v[8:11], a[68:71], a[52:55], v[8:11]\n"
"s_waitcnt lgkmcnt(4)\n"
"ds_read_b128 a[92:95], v226 offset:7168\n"
"ds_read_b128 a[96:99], v226 offset:8192\n"
"v_mfma_f32_16x16x32_bf16 v[0:3], a[72:75], a[40:43], v[0:3]\n"
"s_mov_b32 m0, s40\n"
"v_exp_f32 v58, v58\n"
"v_mfma_f32_16x16x32_bf16 v[8:11], a[72:75], a[56:59], v[8:11]\n"
"s_waitcnt lgkmcnt(3)\n"
"ds_read_b128 a[100:103], v226 offset:9216\n"
"v_readlane_b32 s44, v229, s42\n"
"v_mfma_f32_16x16x32_bf16 v[0:3], a[76:79], a[44:47], v[0:3]\n"
"s_add_u32 s41, s41, 32768\n"
"v_exp_f32 v59, v59\n"
"v_mfma_f32_16x16x32_bf16 v[8:11], a[76:79], a[60:63], v[8:11]\n"
"s_cmp_eq_u32 s41, 163840\n"
"buffer_load_dwordx4 v225, s[8:11], s43 offen offset:0 sc1 lds\n"
"v_mfma_f32_16x16x32_bf16 v[4:7], a[80:83], a[32:35], 0\n"
"s_cselect_b32 s41, 0, s41\n"
"v_exp_f32 v60, v60\n"
"v_mfma_f32_16x16x32_bf16 v[12:15], a[80:83], a[48:51], 0\n"
"s_add_u32 s42, s42, 1\n"
"ds_read_b128 a[104:107], v226 offset:10240\n"
"v_add_u32 v227, s41, v224\n"
"v_mfma_f32_16x16x32_bf16 v[4:7], a[84:87], a[36:39], v[4:7]\n"
"v_exp_f32 v61, v61\n"
"v_mfma_f32_16x16x32_bf16 v[12:15], a[84:87], a[52:55], v[12:15]\n"
"s_waitcnt lgkmcnt(2)\n"
"ds_read_b128 a[108:111], v226 offset:11264\n"
"ds_read_b128 a[112:115], v226 offset:12288\n"
"v_mfma_f32_16x16x32_bf16 v[4:7], a[88:91], a[40:43], v[4:7]\n"
"v_exp_f32 v62, v62\n"
"v_mfma_f32_16x16x32_bf16 v[12:15], a[88:91], a[56:59], v[12:15]\n"
"ds_read_b128 a[116:119], v226 offset:13312\n"
"v_cvt_pk_bf16_f32 v76, v56, v57\n"
"v_mfma_f32_16x16x32_bf16 v[4:7], a[92:95], a[44:47], v[4:7]\n"
"v_exp_f32 v63, v63\n"
"v_mfma_f32_16x16x32_bf16 v[12:15], a[92:95], a[60:63], v[12:15]\n"
"buffer_load_dwordx4 v225, s[8:11], s43 offen offset:1024 sc1 lds\n"
"v_mfma_f32_16x16x32_bf16 v[16:19], a[96:99], a[32:35], 0\n"
"v_exp_f32 v0, v0\n"
"v_mfma_f32_16x16x32_bf16 v[24:27], a[96:99], a[48:51], 0\n"
"s_waitcnt lgkmcnt(3)\n"
"ds_read_b128 a[120:123], v226 offset:14336\n"
"v_cvt_pk_bf16_f32 v77, v58, v59\n"
"v_mfma_f32_16x16x32_bf16 v[16:19], a[100:103], a[36:39], v[16:19]\n"
"v_exp_f32 v1, v1\n"
"v_mfma_f32_16x16x32_bf16 v[24:27], a[100:103], a[52:55], v[24:27]\n"
"ds_read_b128 a[124:127], v226 offset:15360\n"
"ds_read_b128 a[64:67], v226 offset:16384\n"
"v_mfma_f32_16x16x32_bf16 v[16:19], a[104:107], a[40:43], v[16:19]\n"
"v_exp_f32 v2, v2\n"
"v_mfma_f32_16x16x32_bf16 v[24:27], a[104:107], a[56:59], v[24:27]\n"
"s_waitcnt lgkmcnt(3)\n"
"ds_read_b128 a[68:71], v226 offset:17408\n"
"v_cvt_pk_bf16_f32 v78, v60, v61\n"
"v_mfma_f32_16x16x32_bf16 v[16:19], a[108:111], a[44:47], v[16:19]\n"
"v_exp_f32 v3, v3\n"
"v_mfma_f32_16x16x32_bf16 v[24:27], a[108:111], a[60:63], v[24:27]\n"
"buffer_load_dwordx4 v225, s[8:11], s43 offen offset:2048 sc1 lds\n"
"v_mfma_f32_16x16x32_bf16 v[20:23], a[112:115], a[32:35], 0\n"
"v_exp_f32 v8, v8\n"
"v_mfma_f32_16x16x32_bf16 v[28:31], a[112:115], a[48:51], 0\n"
"ds_read_b128 a[72:75], v226 offset:18432\n"
"v_cvt_pk_bf16_f32 v79, v62, v63\n"
"v_mfma_f32_16x16x32_bf16 v[20:23], a[116:119], a[36:39], v[20:23]\n"
"v_exp_f32 v9, v9\n"
"v_mfma_f32_16x16x32_bf16 v[28:31], a[116:119], a[52:55], v[28:31]\n"
"s_waitcnt lgkmcnt(2)\n"
"ds_read_b128 a[76:79], v226 offset:19456\n"
"ds_read_b128 a[80:83], v226 offset:20480\n"
"v_mfma_f32_16x16x32_bf16 v[20:23], a[120:123], a[40:43], v[20:23]\n"
"v_exp_f32 v10, v10\n"
"v_mfma_f32_16x16x32_bf16 v[28:31], a[120:123], a[56:59], v[28:31]\n"
"ds_read_b128 a[84:87], v226 offset:21504\n"
"v_cvt_pk_bf16_f32 v0, v0, v1\n"
"v_mfma_f32_16x16x32_bf16 v[20:23], a[124:127], a[44:47], v[20:23]\n"
"v_exp_f32 v11, v11\n"
"v_mfma_f32_16x16x32_bf16 v[28:31], a[124:127], a[60:63], v[28:31]\n"
"buffer_load_dwordx4 v225, s[8:11], s43 offen offset:3072 sc1 lds\n"
"v_mfma_f32_16x16x32_bf16 v[32:35], a[64:67], a[32:35], 0\n"
"v_exp_f32 v4, v4\n"
"v_mfma_f32_16x16x32_bf16 v[40:43], a[64:67], a[48:51], 0\n"
"s_waitcnt lgkmcnt(3)\n"
"ds_read_b128 a[88:91], v226 offset:22528\n"
"v_cvt_pk_bf16_f32 v1, v2, v3\n"
"v_mfma_f32_16x16x32_bf16 v[32:35], a[68:71], a[36:39], v[32:35]\n"
"v_exp_f32 v5, v5\n"
"v_mfma_f32_16x16x32_bf16 v[40:43], a[68:71], a[52:55], v[40:43]\n"
"s_add_u32 m0, s40, 16384\n"
"ds_read_b128 a[92:95], v226 offset:23552\n"
"ds_read_b128 a[96:99], v226 offset:24576\n"
"v_mfma_f32_16x16x32_bf16 v[32:35], a[72:75], a[40:43], v[32:35]\n"
"v_exp_f32 v6, v6\n"
"v_mfma_f32_16x16x32_bf16 v[40:43], a[72:75], a[56:59], v[40:43]\n"
"s_waitcnt lgkmcnt(3)\n"
"ds_read_b128 a[100:103], v226 offset:25600\n"
"v_cvt_pk_bf16_f32 v8, v8, v9\n"
"v_mfma_f32_16x16x32_bf16 v[32:35], a[76:79], a[44:47], v[32:35]\n"
"v_exp_f32 v7, v7\n"
"v_mfma_f32_16x16x32_bf16 v[40:43], a[76:79], a[60:63], v[40:43]\n"
"buffer_load_dwordx4 v225, s[8:11], s44 offen offset:0 sc1 lds\n"
"v_mfma_f32_16x16x32_bf16 v[36:39], a[80:83], a[32:35], 0\n"
"s_add_u32 s40, s40, 32768\n"
"v_exp_f32 v12, v12\n"
"v_mfma_f32_16x16x32_bf16 v[44:47], a[80:83], a[48:51], 0\n"
"s_cmp_eq_u32 s40, s46\n"
"ds_read_b128 a[104:107], v226 offset:26624\n"
"v_cvt_pk_bf16_f32 v9, v10, v11\n"
"v_mfma_f32_16x16x32_bf16 v[36:39], a[84:87], a[36:39], v[36:39]\n"
"s_cselect_b32 s40, s47, s40\n"
"v_exp_f32 v13, v13\n"
"v_mfma_f32_16x16x32_bf16 v[44:47], a[84:87], a[52:55], v[44:47]\n"
"s_waitcnt lgkmcnt(2)\n"
"ds_read_b128 a[108:111], v226 offset:27648\n"
"ds_read_b128 a[112:115], v226 offset:28672\n"
"v_mfma_f32_16x16x32_bf16 v[36:39], a[88:91], a[40:43], v[36:39]\n"
"v_exp_f32 v14, v14\n"
"v_mfma_f32_16x16x32_bf16 v[44:47], a[88:91], a[56:59], v[44:47]\n"
"ds_read_b128 a[116:119], v226 offset:29696\n"
"v_cvt_pk_bf16_f32 v2, v4, v5\n"
"v_mfma_f32_16x16x32_bf16 v[36:39], a[92:95], a[44:47], v[36:39]\n"
"v_exp_f32 v15, v15\n"
"v_mfma_f32_16x16x32_bf16 v[44:47], a[92:95], a[60:63], v[44:47]\n"
"buffer_load_dwordx4 v225, s[8:11], s44 offen offset:1024 sc1 lds\n"
"v_mfma_f32_16x16x32_bf16 v[48:51], a[96:99], a[32:35], 0\n"
"v_exp_f32 v16, v16\n"
"v_mfma_f32_16x16x32_bf16 v[56:59], a[96:99], a[48:51], 0\n"
"s_waitcnt lgkmcnt(3)\n"
"ds_read_b128 a[120:123], v226 offset:30720\n"
"v_cvt_pk_bf16_f32 v3, v6, v7\n"
"v_mfma_f32_16x16x32_bf16 v[48:51], a[100:103], a[36:39], v[48:51]\n"
"v_exp_f32 v17, v17\n"
"v_mfma_f32_16x16x32_bf16 v[56:59], a[100:103], a[52:55], v[56:59]\n"
"ds_read_b128 a[124:127], v226 offset:31744\n"
"v_cvt_pk_bf16_f32 v10, v12, v13\n"
"v_mfma_f32_16x16x32_bf16 v[48:51], a[104:107], a[40:43], v[48:51]\n"
"s_waitcnt vmcnt(14)\n"
"s_barrier\n"
"v_mfma_f32_16x16x32_bf16 v[56:59], a[104:107], a[56:59], v[56:59]\n"
"s_waitcnt lgkmcnt(2)\n"
"ds_read_b128 a[64:67], v227 offset:0\n"
"v_cvt_pk_bf16_f32 v11, v14, v15\n"
"v_mfma_f32_16x16x32_bf16 v[48:51], a[108:111], a[44:47], v[48:51]\n"
"v_exp_f32 v18, v18\n"
"v_mfma_f32_16x16x32_bf16 v[56:59], a[108:111], a[60:63], v[56:59]\n"
"buffer_load_dwordx4 v225, s[8:11], s44 offen offset:2048 sc1 lds\n"
"v_mfma_f32_16x16x32_bf16 v[52:55], a[112:115], a[32:35], 0\n"
"v_exp_f32 v19, v19\n"
"v_mfma_f32_16x16x32_bf16 v[60:63], a[112:115], a[48:51], 0\n"
"ds_read_b128 a[68:71], v227 offset:1024\n"
"v_cvt_pk_bf16_f32 v16, v16, v17\n"
"v_mfma_f32_16x16x32_bf16 v[52:55], a[116:119], a[36:39], v[52:55]\n"
"v_exp_f32 v24, v24\n"
"v_mfma_f32_16x16x32_bf16 v[60:63], a[116:119], a[52:55], v[60:63]\n"
"s_waitcnt lgkmcnt(2)\n"
"ds_read_b128 a[72:75], v227 offset:2048\n"
"ds_read_b128 a[76:79], v227 offset:3072\n"
"v_mfma_f32_16x16x32_bf16 v[52:55], a[120:123], a[40:43], v[52:55]\n"
"v_exp_f32 v25, v25\n"
"v_mfma_f32_16x16x32_bf16 v[60:63], a[120:123], a[56:59], v[60:63]\n"
"ds_read_b128 a[80:83], v227 offset:4096\n"
"v_cvt_pk_bf16_f32 v17, v18, v19\n"
"v_mfma_f32_16x16x32_bf16 v[52:55], a[124:127], a[44:47], v[52:55]\n"
"v_exp_f32 v26, v26\n"
"v_mfma_f32_16x16x32_bf16 v[60:63], a[124:127], a[60:63], v[60:63]\n"
"s_waitcnt lgkmcnt(4)\n"
"buffer_load_dwordx4 v225, s[8:11], s44 offen offset:3072 sc1 lds\n"
"v_mfma_f32_16x16x32_bf16 v[144:147], a[64:67], v[64:67], v[144:147]\n"
"s_add_u32 s41, s41, 32768\n"
"v_exp_f32 v27, v27\n"
"v_mfma_f32_16x16x32_bf16 v[176:179], a[64:67], v[68:71], v[176:179]\n"
"s_waitcnt lgkmcnt(3)\n"
"ds_read_b128 a[84:87], v227 offset:5120\n"
"v_readlane_b32 s43, v228, s42\n"
"v_mfma_f32_16x16x32_bf16 v[148:151], a[68:71], v[64:67], v[148:151]\n"
"s_cmp_eq_u32 s41, 163840\n"
"v_exp_f32 v20, v20\n"
"v_mfma_f32_16x16x32_bf16 v[180:183], a[68:71], v[68:71], v[180:183]\n"
"s_waitcnt lgkmcnt(2)\n"
"ds_read_b128 a[88:91], v227 offset:6144\n"
"ds_read_b128 a[92:95], v227 offset:7168\n"
"v_mfma_f32_16x16x32_bf16 v[152:155], a[72:75], v[64:67], v[152:155]\n"
"s_cselect_b32 s41, 0, s41\n"
"v_exp_f32 v21, v21\n"
"v_mfma_f32_16x16x32_bf16 v[184:187], a[72:75], v[68:71], v[184:187]\n"
"s_mov_b32 m0, s40\n"
"ds_read_b128 a[96:99], v227 offset:8192\n"
"v_add_u32 v226, s41, v224\n"
"v_mfma_f32_16x16x32_bf16 v[156:159], a[76:79], v[64:67], v[156:159]\n"
"v_exp_f32 v22, v22\n"
"v_mfma_f32_16x16x32_bf16 v[188:191], a[76:79], v[68:71], v[188:191]\n"
"s_waitcnt lgkmcnt(4)\n"
"buffer_load_dwordx4 v225, s[8:11], s43 offen offset:0 sc1 lds\n"
"v_mfma_f32_16x16x32_bf16 v[160:163], a[80:83], v[64:67], v[160:163]\n"
"v_exp_f32 v23, v23\n"
"v_mfma_f32_16x16x32_bf16 v[192:195], a[80:83], v[68:71], v[192:195]\n"
"s_waitcnt lgkmcnt(3)\n"
"ds_read_b128 a[100:103], v227 offset:9216\n"
"v_readlane_b32 s44, v229, s42\n"
"v_mfma_f32_16x16x32_bf16 v[164:167], a[84:87], v[64:67], v[164:167]\n"
"s_add_u32 s42, s42, 1\n"
"v_exp_f32 v28, v28\n"
"v_mfma_f32_16x16x32_bf16 v[196:199], a[84:87], v[68:71], v[196:199]\n"
"s_waitcnt lgkmcnt(2)\n"
"ds_read_b128 a[104:107], v227 offset:10240\n"
"ds_read_b128 a[108:111], v227 offset:11264\n"
"v_mfma_f32_16x16x32_bf16 v[168:171], a[88:91], v[64:67], v[168:171]\n"
"v_exp_f32 v29, v29\n"
"v_mfma_f32_16x16x32_bf16 v[200:203], a[88:91], v[68:71], v[200:203]\n"
"ds_read_b128 a[112:115], v227 offset:12288\n"
"v_cvt_pk_bf16_f32 v24, v24, v25\n"
"v_mfma_f32_16x16x32_bf16 v[172:175], a[92:95], v[64:67], v[172:175]\n"
"v_exp_f32 v30, v30\n"
"v_mfma_f32_16x16x32_bf16 v[204:207], a[92:95], v[68:71], v[204:207]\n"
"buffer_load_dwordx4 v225, s[8:11], s43 offen offset:1024 sc1 lds\n"
"v_mfma_f32_16x16x32_bf16 a[140:143], a[128:131], v[64:67], a[140:143]\n"
"v_exp_f32 v31, v31\n"
"v_mfma_f32_16x16x32_bf16 a[144:147], a[128:131], v[68:71], a[144:147]\n"
"s_waitcnt lgkmcnt(3)\n"
"ds_read_b128 a[116:119], v227 offset:13312\n"
"v_cvt_pk_bf16_f32 v25, v26, v27\n"
"v_mfma_f32_16x16x32_bf16 v[144:147], a[96:99], v[72:75], v[144:147]\n"
"v_exp_f32 v32, v32\n"
"v_mfma_f32_16x16x32_bf16 v[176:179], a[96:99], v[76:79], v[176:179]\n"
"ds_read_b128 a[120:123], v227 offset:14336\n"
"ds_read_b128 a[124:127], v227 offset:15360\n"
"v_mfma_f32_16x16x32_bf16 v[148:151], a[100:103], v[72:75], v[148:151]\n"
"v_exp_f32 v33, v33\n"
"v_mfma_f32_16x16x32_bf16 v[180:183], a[100:103], v[76:79], v[180:183]\n"
"s_waitcnt lgkmcnt(3)\n"
"v_cvt_pk_bf16_f32 v18, v20, v21\n"
"v_cvt_pk_bf16_f32 v19, v22, v23\n"
"v_mfma_f32_16x16x32_bf16 v[152:155], a[104:107], v[72:75], v[152:155]\n"
"v_exp_f32 v34, v34\n"
"v_mfma_f32_16x16x32_bf16 v[184:187], a[104:107], v[76:79], v[184:187]\n"
"buffer_load_dwordx4 v225, s[8:11], s43 offen offset:2048 sc1 lds\n"
"v_mfma_f32_16x16x32_bf16 v[156:159], a[108:111], v[72:75], v[156:159]\n"
"v_exp_f32 v35, v35\n"
"v_mfma_f32_16x16x32_bf16 v[188:191], a[108:111], v[76:79], v[188:191]\n"
"ds_read_b128 a[64:67], v227 offset:16384\n"
"v_cvt_pk_bf16_f32 v26, v28, v29\n"
"v_mfma_f32_16x16x32_bf16 v[160:163], a[112:115], v[72:75], v[160:163]\n"
"v_exp_f32 v40, v40\n"
"v_mfma_f32_16x16x32_bf16 v[192:195], a[112:115], v[76:79], v[192:195]\n"
"s_waitcnt lgkmcnt(1)\n"
"ds_read_b128 a[68:71], v227 offset:17408\n"
"ds_read_b128 a[72:75], v227 offset:18432\n"
"v_mfma_f32_16x16x32_bf16 v[164:167], a[116:119], v[72:75], v[164:167]\n"
"v_exp_f32 v41, v41\n"
"v_mfma_f32_16x16x32_bf16 v[196:199], a[116:119], v[76:79], v[196:199]\n"
"ds_read_b128 a[76:79], v227 offset:19456\n"
"v_cvt_pk_bf16_f32 v27, v30, v31\n"
"v_mfma_f32_16x16x32_bf16 v[168:171], a[120:123], v[72:75], v[168:171]\n"
"v_exp_f32 v42, v42\n"
"v_mfma_f32_16x16x32_bf16 v[200:203], a[120:123], v[76:79], v[200:203]\n"
"buffer_load_dwordx4 v225, s[8:11], s43 offen offset:3072 sc1 lds\n"
"v_mfma_f32_16x16x32_bf16 v[172:175], a[124:127], v[72:75], v[172:175]\n"
"v_exp_f32 v43, v43\n"
"v_mfma_f32_16x16x32_bf16 v[204:207], a[124:127], v[76:79], v[204:207]\n"
"ds_read_b128 a[80:83], v227 offset:20480\n"
"v_cvt_pk_bf16_f32 v64, v32, v33\n"
"v_mfma_f32_16x16x32_bf16 a[140:143], a[128:131], v[72:75], a[140:143]\n"
"v_exp_f32 v36, v36\n"
"v_mfma_f32_16x16x32_bf16 a[144:147], a[128:131], v[76:79], a[144:147]\n"
"s_waitcnt lgkmcnt(2)\n"
"ds_read_b128 a[84:87], v227 offset:21504\n"
"ds_read_b128 a[88:91], v227 offset:22528\n"
"v_mfma_f32_16x16x32_bf16 v[144:147], a[64:67], v[0:3], v[144:147]\n"
"s_add_u32 m0, s40, 16384\n"
"v_exp_f32 v37, v37\n"
"v_mfma_f32_16x16x32_bf16 v[176:179], a[64:67], v[8:11], v[176:179]\n"
"ds_read_b128 a[92:95], v227 offset:23552\n"
"v_cvt_pk_bf16_f32 v65, v34, v35\n"
"v_mfma_f32_16x16x32_bf16 v[148:151], a[68:71], v[0:3], v[148:151]\n"
"v_exp_f32 v38, v38\n"
"v_mfma_f32_16x16x32_bf16 v[180:183], a[68:71], v[8:11], v[180:183]\n"
"buffer_load_dwordx4 v225, s[8:11], s44 offen offset:0 sc1 lds\n"
"v_mfma_f32_16x16x32_bf16 v[152:155], a[72:75], v[0:3], v[152:155]\n"
"s_add_u32 s40, s40, 32768\n"
"v_exp_f32 v39, v39\n"
"v_mfma_f32_16x16x32_bf16 v[184:187], a[72:75], v[8:11], v[184:187]\n"
"s_waitcnt lgkmcnt(3)\n"
"ds_read_b128 a[96:99], v227 offset:24576\n"
"v_cvt_pk_bf16_f32 v68, v40, v41\n"
"v_mfma_f32_16x16x32_bf16 v[156:159], a[76:79], v[0:3], v[156:159]\n"
"s_cmp_eq_u32 s40, s46\n"
"v_exp_f32 v44, v44\n"
"v_mfma_f32_16x16x32_bf16 v[188:191], a[76:79], v[8:11], v[188:191]\n"
"s_cselect_b32 s40, s47, s40\n"
"ds_read_b128 a[100:103], v227 offset:25600\n"
"v_cvt_pk_bf16_f32 v69, v42, v43\n"
"v_mfma_f32_16x16x32_bf16 v[160:163], a[80:83], v[0:3], v[160:163]\n"
"v_exp_f32 v45, v45\n"
"v_mfma_f32_16x16x32_bf16 v[192:195], a[80:83], v[8:11], v[192:195]\n"
"s_waitcnt lgkmcnt(2)\n"
"ds_read_b128 a[104:107], v227 offset:26624\n"
"v_cvt_pk_bf16_f32 v66, v36, v37\n"
"v_mfma_f32_16x16x32_bf16 v[164:167], a[84:87], v[0:3], v[164:167]\n"
"v_exp_f32 v46, v46\n"
"v_mfma_f32_16x16x32_bf16 v[196:199], a[84:87], v[8:11], v[196:199]\n"
"buffer_load_dwordx4 v225, s[8:11], s44 offen offset:1024 sc1 lds\n"
"v_mfma_f32_16x16x32_bf16 v[168:171], a[88:91], v[0:3], v[168:171]\n"
"v_exp_f32 v47, v47\n"
"v_mfma_f32_16x16x32_bf16 v[200:203], a[88:91], v[8:11], v[200:203]\n"
"ds_read_b128 a[108:111], v227 offset:27648\n"
"v_cvt_pk_bf16_f32 v67, v38, v39\n"
"v_mfma_f32_16x16x32_bf16 v[172:175], a[92:95], v[0:3], v[172:175]\n"
"v_exp_f32 v48, v48\n"
"v_mfma_f32_16x16x32_bf16 v[204:207], a[92:95], v[8:11], v[204:207]\n"
"ds_read_b128 a[112:115], v227 offset:28672\n"
"ds_read_b128 a[116:119], v227 offset:29696\n"
"v_mfma_f32_16x16x32_bf16 a[140:143], a[128:131], v[0:3], a[140:143]\n"
"v_exp_f32 v49, v49\n"
"v_mfma_f32_16x16x32_bf16 a[144:147], a[128:131], v[8:11], a[144:147]\n"
"s_waitcnt lgkmcnt(3)\n"
"ds_read_b128 a[120:123], v227 offset:30720\n"
"v_cvt_pk_bf16_f32 v70, v44, v45\n"
"v_mfma_f32_16x16x32_bf16 v[144:147], a[96:99], v[16:19], v[144:147]\n"
"v_exp_f32 v50, v50\n"
"v_mfma_f32_16x16x32_bf16 v[176:179], a[96:99], v[24:27], v[176:179]\n"
"buffer_load_dwordx4 v225, s[8:11], s44 offen offset:2048 sc1 lds\n"
"v_mfma_f32_16x16x32_bf16 v[148:151], a[100:103], v[16:19], v[148:151]\n"
"v_exp_f32 v51, v51\n"
"v_mfma_f32_16x16x32_bf16 v[180:183], a[100:103], v[24:27], v[180:183]\n"
"ds_read_b128 a[124:127], v227 offset:31744\n"
"v_cvt_pk_bf16_f32 v71, v46, v47\n"
"v_mfma_f32_16x16x32_bf16 v[152:155], a[104:107], v[16:19], v[152:155]\n"
"s_waitcnt vmcnt(15)\n"
"s_barrier\n"
"v_mfma_f32_16x16x32_bf16 v[184:187], a[104:107], v[24:27], v[184:187]\n"
"s_waitcnt lgkmcnt(2)\n"
"ds_read_b128 a[64:67], v226 offset:0\n"
"v_cvt_pk_bf16_f32 v72, v48, v49\n"
"v_mfma_f32_16x16x32_bf16 v[156:159], a[108:111], v[16:19], v[156:159]\n"
"v_exp_f32 v52, v52\n"
"v_mfma_f32_16x16x32_bf16 v[188:191], a[108:111], v[24:27], v[188:191]\n"
"ds_read_b128 a[68:71], v226 offset:1024\n"
"v_cvt_pk_bf16_f32 v73, v50, v51\n"
"v_mfma_f32_16x16x32_bf16 v[160:163], a[112:115], v[16:19], v[160:163]\n"
"v_exp_f32 v53, v53\n"
"v_mfma_f32_16x16x32_bf16 v[192:195], a[112:115], v[24:27], v[192:195]\n"
"buffer_load_dwordx4 v225, s[8:11], s44 offen offset:3072 sc1 lds\n"
"v_mfma_f32_16x16x32_bf16 v[164:167], a[116:119], v[16:19], v[164:167]\n"
"v_exp_f32 v54, v54\n"
"v_mfma_f32_16x16x32_bf16 v[196:199], a[116:119], v[24:27], v[196:199]\n"
"s_waitcnt lgkmcnt(2)\n"
"ds_read_b128 a[72:75], v226 offset:2048\n"
"v_cvt_pk_bf16_f32 v74, v52, v53\n"
"v_mfma_f32_16x16x32_bf16 v[168:171], a[120:123], v[16:19], v[168:171]\n"
"v_exp_f32 v55, v55\n"
"v_mfma_f32_16x16x32_bf16 v[200:203], a[120:123], v[24:27], v[200:203]\n"
"ds_read_b128 a[76:79], v226 offset:3072\n"
"ds_read_b128 a[80:83], v226 offset:4096\n"
"v_mfma_f32_16x16x32_bf16 v[172:175], a[124:127], v[16:19], v[172:175]\n"
"v_cvt_pk_bf16_f32 v75, v54, v55\n"
"v_mfma_f32_16x16x32_bf16 v[204:207], a[124:127], v[24:27], v[204:207]\n"
"ds_read_b128 a[84:87], v226 offset:5120\n"
"v_mfma_f32_16x16x32_bf16 a[140:143], a[128:131], v[16:19], a[140:143]\n"
"v_mfma_f32_16x16x32_bf16 a[144:147], a[128:131], v[24:27], a[144:147]\n"
"s_sub_u32 s56, s56, 1\n"
"s_cbranch_scc0 LP2_%=\n"
"s_waitcnt lgkmcnt(5)\n"
"v_mfma_f32_16x16x32_bf16 v[0:3], a[64:67], a[32:35], v[208:211]\n"
"s_mov_b32 m0, s40\n"
"v_exp_f32 v56, v56\n"
"v_mfma_f32_16x16x32_bf16 v[8:11], a[64:67], a[48:51], 0\n"
"s_waitcnt lgkmcnt(4)\n"
"ds_read_b128 a[88:91], v226 offset:6144\n"
"v_readlane_b32 s43, v228, s42\n"
"v_mfma_f32_16x16x32_bf16 v[0:3], a[68:71], a[36:39], v[0:3]\n"
"s_waitcnt vmcnt(16)\n"
"buffer_store_dwordx2 v[232:233], v230, s[8:11], s61 offen sc1\n"
"v_mfma_f32_16x16x32_bf16 v[8:11], a[68:71], a[52:55], v[8:11]\n"
"s_waitcnt lgkmcnt(4)\n"
"v_exp_f32 v57, v57\n"
"v_mfma_f32_16x16x32_bf16 v[0:3], a[72:75], a[40:43], v[0:3]\n"
"s_add_u32 s41, s41, 32768\n"
"buffer_load_dwordx4 v225, s[8:11], s43 offen offset:0 sc1 lds\n"
"v_readlane_b32 s44, v229, s42\n"
"v_mfma_f32_16x16x32_bf16 v[8:11], a[72:75], a[56:59], v[8:11]\n"
"s_waitcnt lgkmcnt(1)\n"
"ds_read_b128 a[92:95], v226 offset:7168\n"
"v_cvt_pk_bf16_f32 v76, v56, v57\n"
"v_mfma_f32_16x16x32_bf16 v[0:3], a[76:79], a[44:47], v[0:3]\n"
"s_add_u32 s42, s42, 1\n"
"ds_read_b128 a[96:99], v226 offset:8192\n"
"s_cmp_eq_u32 s41, 163840\n"
"v_mfma_f32_16x16x32_bf16 v[8:11], a[76:79], a[60:63], v[8:11]\n"
"s_cselect_b32 s41, 0, s41\n"
"v_exp_f32 v58, v58\n"
"v_add_u32 v227, s41, v224\n"
"v_mfma_f32_16x16x32_bf16 v[4:7], a[80:83], a[32:35], v[212:215]\n"
"ds_read_b128 a[100:103], v226 offset:9216\n"
"v_mfma_f32_16x16x32_bf16 v[12:15], a[80:83], a[48:51], 0\n"
"v_exp_f32 v59, v59\n"
"v_mfma_f32_16x16x32_bf16 v[4:7], a[84:87], a[36:39], v[4:7]\n"
"v_exp_f32 v60, v60\n"
"v_cvt_pk_bf16_f32 v77, v58, v59\n"
"v_mfma_f32_16x16x32_bf16 v[12:15], a[84:87], a[52:55], v[12:15]\n"
"s_waitcnt lgkmcnt(3)\n"
"buffer_load_dwordx4 v225, s[8:11], s43 offen offset:1024 sc1 lds\n"
"v_mfma_f32_16x16x32_bf16 v[4:7], a[88:91], a[40:43], v[4:7]\n"
"ds_read_b128 a[104:107], v226 offset:10240\n"
"v_mfma_f32_16x16x32_bf16 v[12:15], a[88:91], a[56:59], v[12:15]\n"
"s_waitcnt lgkmcnt(2)\n"
"v_exp_f32 v61, v61\n"
"v_mfma_f32_16x16x32_bf16 v[4:7], a[92:95], a[44:47], v[4:7]\n"
"ds_read_b128 a[108:111], v226 offset:11264\n"
"ds_read_b128 a[112:115], v226 offset:12288\n"
"v_cvt_pk_bf16_f32 v78, v60, v61\n"
"v_mfma_f32_16x16x32_bf16 v[12:15], a[92:95], a[60:63], v[12:15]\n"
"v_exp_f32 v62, v62\n"
"v_mfma_f32_16x16x32_bf16 v[16:19], a[96:99], a[32:35], v[216:219]\n"
"ds_read_b128 a[116:119], v226 offset:13312\n"
"ds_read_b128 a[120:123], v226 offset:14336\n"
"v_mfma_f32_16x16x32_bf16 v[24:27], a[96:99], a[48:51], 0\n"
"s_waitcnt lgkmcnt(5)\n"
"buffer_load_dwordx4 v225, s[8:11], s43 offen offset:2048 sc1 lds\n"
"v_mfma_f32_16x16x32_bf16 v[16:19], a[100:103], a[36:39], v[16:19]\n"
"v_exp_f32 v63, v63\n"
"v_mfma_f32_16x16x32_bf16 v[24:27], a[100:103], a[52:55], v[24:27]\n"
"s_waitcnt lgkmcnt(4)\n"
"ds_read_b128 a[124:127], v226 offset:15360\n"
"ds_read_b128 a[64:67], v226 offset:16384\n"
"v_cvt_pk_bf16_f32 v79, v62, v63\n"
"v_mfma_f32_16x16x32_bf16 v[16:19], a[104:107], a[40:43], v[16:19]\n"
"buffer_load_dwordx4 v225, s[8:11], s43 offen offset:3072 sc1 lds\n"
"v_mfma_f32_16x16x32_bf16 v[24:27], a[104:107], a[56:59], v[24:27]\n"
"s_waitcnt lgkmcnt(4)\n"
"ds_read_b128 a[68:71], v226 offset:17408\n"
"s_add_u32 m0, s40, 16384\n"
"v_mfma_f32_16x16x32_bf16 v[16:19], a[108:111], a[44:47], v[16:19]\n"
"ds_read_b128 a[72:75], v226 offset:18432\n"
"v_mfma_f32_16x16x32_bf16 v[24:27], a[108:111], a[60:63], v[24:27]\n"
"ds_read_b128 a[76:79], v226 offset:19456\n"
"v_mfma_f32_16x16x32_bf16 v[20:23], a[112:115], a[32:35], v[220:223]\n"
"ds_read_b128 a[80:83], v226 offset:20480\n"
"v_mfma_f32_16x16x32_bf16 v[28:31], a[112:115], a[48:51], 0\n"
"s_waitcnt lgkmcnt(6)\n"
"v_exp_f32 v0, v0\n"
"v_mfma_f32_16x16x32_bf16 v[20:23], a[116:119], a[36:39], v[20:23]\n"
"s_add_u32 s40, s40, 32768\n"
"ds_read_b128 a[84:87], v226 offset:21504\n"
"ds_read_b128 a[88:91], v226 offset:22528\n"
"v_mfma_f32_16x16x32_bf16 v[28:31], a[116:119], a[52:55], v[28:31]\n"
"s_cmp_eq_u32 s40, s46\n"
"buffer_load_dwordx4 v225, s[8:11], s44 offen offset:0 sc1 lds\n"
"v_mfma_f32_16x16x32_bf16 v[20:23], a[120:123], a[40:43], v[20:23]\n"
"s_cselect_b32 s40, s47, s40\n"
"ds_read_b128 a[92:95], v226 offset:23552\n"
"v_mfma_f32_16x16x32_bf16 v[28:31], a[120:123], a[56:59], v[28:31]\n"
"s_waitcnt lgkmcnt(5)\n"
"ds_read_b128 a[96:99], v226 offset:24576\n"
"v_mfma_f32_16x16x32_bf16 v[20:23], a[124:127], a[44:47], v[20:23]\n"
"ds_read_b128 a[100:103], v226 offset:25600\n"
"v_mfma_f32_16x16x32_bf16 v[28:31], a[124:127], a[60:63], v[28:31]\n"
"ds_read_b128 a[104:107], v226 offset:26624\n"
"v_mfma_f32_16x16x32_bf16 v[40:43], a[64:67], a[48:51], v[208:211]\n"
"ds_read_b128 a[108:111], v226 offset:27648\n"
"v_mfma_f32_16x16x32_bf16 v[40:43], a[68:71], a[52:55], v[40:43]\n"
"buffer_load_dwordx4 v225, s[8:11], s44 offen offset:1024 sc1 lds\n"
"v_mfma_f32_16x16x32_bf16 v[40:43], a[72:75], a[56:59], v[40:43]\n"
"s_waitcnt lgkmcnt(5)\n"
"ds_read_b128 a[112:115], v226 offset:28672\n"
"v_mfma_f32_16x16x32_bf16 v[40:43], a[76:79], a[60:63], v[40:43]\n"
"v_exp_f32 v1, v1\n"
"v_mfma_f32_16x16x32_bf16 v[44:47], a[80:83], a[48:51], v[212:215]\n"
"ds_read_b128 a[116:119], v226 offset:29696\n"
"ds_read_b128 a[120:123], v226 offset:30720\n"
"v_cvt_pk_bf16_f32 v0, v0, v1\n"
"v_mfma_f32_16x16x32_bf16 v[44:47], a[84:87], a[52:55], v[44:47]\n"
"ds_read_b128 a[124:127], v226 offset:31744\n"
"v_mfma_f32_16x16x32_bf16 v[44:47], a[88:91], a[56:59], v[44:47]\n"
"s_waitcnt lgkmcnt(5)\n"
"v_exp_f32 v2, v2\n"
"v_mfma_f32_16x16x32_bf16 v[44:47], a[92:95], a[60:63], v[44:47]\n"
"buffer_load_dwordx4 v225, s[8:11], s44 offen offset:2048 sc1 lds\n"
"v_mfma_f32_16x16x32_bf16 v[56:59], a[96:99], a[48:51], v[216:219]\n"
"s_waitcnt vmcnt(16)\n"
"s_barrier\n"
"v_mfma_f32_16x16x32_bf16 v[56:59], a[100:103], a[52:55], v[56:59]\n"
"ds_read_b128 a[64:67], v227 offset:0\n"
"v_readlane_b32 s43, v228, s42\n"
"v_mfma_f32_16x16x32_bf16 v[56:59], a[104:107], a[56:59], v[56:59]\n"
"s_waitcnt lgkmcnt(4)\n"
"v_exp_f32 v3, v3\n"
"v_mfma_f32_16x16x32_bf16 v[56:59], a[108:111], a[60:63], v[56:59]\n"
"ds_read_b128 a[68:71], v227 offset:1024\n"
"ds_read_b128 a[72:75], v227 offset:2048\n"
"v_cvt_pk_bf16_f32 v1, v2, v3\n"
"v_mfma_f32_16x16x32_bf16 v[60:63], a[112:115], a[48:51], v[220:223]\n"
"s_waitcnt lgkmcnt(3)\n"
"v_exp_f32 v8, v8\n"
"v_mfma_f32_16x16x32_bf16 v[60:63], a[116:119], a[52:55], v[60:63]\n"
"v_exp_f32 v9, v9\n"
"v_mfma_f32_16x16x32_bf16 v[60:63], a[120:123], a[56:59], v[60:63]\n"
"buffer_load_dwordx4 v225, s[8:11], s44 offen offset:3072 sc1 lds\n"
"v_cvt_pk_bf16_f32 v8, v8, v9\n"
"v_mfma_f32_16x16x32_bf16 v[60:63], a[124:127], a[60:63], v[60:63]\n"
"s_waitcnt lgkmcnt(2)\n"
"ds_read_b128 a[76:79], v227 offset:3072\n"
"v_readlane_b32 s44, v229, s42\n"
"v_mfma_f32_16x16x32_bf16 v[144:147], a[64:67], v[64:67], v[144:147]\n"
"s_mov_b32 m0, s40\n"
"v_exp_f32 v10, v10\n"
"v_mfma_f32_16x16x32_bf16 v[176:179], a[64:67], v[68:71], v[176:179]\n"
"s_waitcnt lgkmcnt(2)\n"
"ds_read_b128 a[80:83], v227 offset:4096\n"
"ds_read_b128 a[84:87], v227 offset:5120\n"
"v_mfma_f32_16x16x32_bf16 v[148:151], a[68:71], v[64:67], v[148:151]\n"
"s_add_u32 s41, s41, 32768\n"
"v_exp_f32 v11, v11\n"
"v_mfma_f32_16x16x32_bf16 v[180:183], a[68:71], v[68:71], v[180:183]\n"
"s_waitcnt lgkmcnt(3)\n"
"v_exp_f32 v4, v4\n"
"v_cvt_pk_bf16_f32 v9, v10, v11\n"
"v_mfma_f32_16x16x32_bf16 v[152:155], a[72:75], v[64:67], v[152:155]\n"
"s_cmp_eq_u32 s41, 163840\n"
"buffer_load_dwordx4 v225, s[8:11], s43 offen offset:0 sc1 lds\n"
"v_mfma_f32_16x16x32_bf16 v[184:187], a[72:75], v[68:71], v[184:187]\n"
"s_waitcnt lgkmcnt(2)\n"
"ds_read_b128 a[88:91], v227 offset:6144\n"
"s_cselect_b32 s41, 0, s41\n"
"v_add_u32 v226, s41, v224\n"
"v_mfma_f32_16x16x32_bf16 v[156:159], a[76:79], v[64:67], v[156:159]\n"
"s_add_u32 s42, s42, 1\n"
"v_exp_f32 v5, v5\n"
"v_mfma_f32_16x16x32_bf16 v[188:191], a[76:79], v[68:71], v[188:191]\n"
"s_waitcnt lgkmcnt(2)\n"
"ds_read_b128 a[92:95], v227 offset:7168\n"
"ds_read_b128 a[96:99], v227 offset:8192\n"
"v_cvt_pk_bf16_f32 v2, v4, v5\n"
"v_mfma_f32_16x16x32_bf16 v[160:163], a[80:83], v[64:67], v[160:163]\n"
"v_exp_f32 v6, v6\n"
"v_mfma_f32_16x16x32_bf16 v[192:195], a[80:83], v[68:71], v[192:195]\n"
"s_waitcnt lgkmcnt(3)\n"
"buffer_load_dwordx4 v225, s[8:11], s43 offen offset:1024 sc1 lds\n"
"v_mfma_f32_16x16x32_bf16 v[164:167], a[84:87], v[64:67], v[164:167]\n"
"v_exp_f32 v7, v7\n"
"v_mfma_f32_16x16x32_bf16 v[196:199], a[84:87], v[68:71], v[196:199]\n"
"s_waitcnt lgkmcnt(2)\n"
"v_exp_f32 v12, v12\n"
"v_cvt_pk_bf16_f32 v3, v6, v7\n"
"v_mfma_f32_16x16x32_bf16 v[168:171], a[88:91], v[64:67], v[168:171]\n"
"v_exp_f32 v13, v13\n"
"v_mfma_f32_16x16x32_bf16 v[200:203], a[88:91], v[68:71], v[200:203]\n"
"s_waitcnt lgkmcnt(1)\n"
"ds_read_b128 a[100:103], v227 offset:9216\n"
"ds_read_b128 a[104:107], v227 offset:10240\n"
"v_cvt_pk_bf16_f32 v10, v12, v13\n"
"v_mfma_f32_16x16x32_bf16 v[172:175], a[92:95], v[64:67], v[172:175]\n"
"buffer_load_dwordx4 v225, s[8:11], s43 offen offset:2048 sc1 lds\n"
"v_mfma_f32_16x16x32_bf16 v[204:207], a[92:95], v[68:71], v[204:207]\n"
"ds_read_b128 a[108:111], v227 offset:11264\n"
"v_mfma_f32_16x16x32_bf16 a[140:143], a[128:131], v[64:67], a[140:143]\n"
"v_exp_f32 v14, v14\n"
"v_mfma_f32_16x16x32_bf16 a[144:147], a[128:131], v[68:71], a[144:147]\n"
"s_waitcnt lgkmcnt(3)\n"
"ds_read_b128 a[112:115], v227 offset:12288\n"
"ds_read_b128 a[116:119], v227 offset:13312\n"
"v_mfma_f32_16x16x32_bf16 v[144:147], a[96:99], v[72:75], v[144:147]\n"
"v_exp_f32 v15, v15\n"
"v_mfma_f32_16x16x32_bf16 v[176:179], a[96:99], v[76:79], v[176:179]\n"
"s_waitcnt lgkmcnt(4)\n"
"buffer_load_dwordx4 v225, s[8:11], s43 offen offset:3072 sc1 lds\n"
"v_cvt_pk_bf16_f32 v11, v14, v15\n"
"v_mfma_f32_16x16x32_bf16 v[148:151], a[100:103], v[72:75], v[148:151]\n"
"s_add_u32 m0, s40, 16384\n"
"v_exp_f32 v16, v16\n"
"v_mfma_f32_16x16x32_bf16 v[180:183], a[100:103], v[76:79], v[180:183]\n"
"s_waitcnt lgkmcnt(3)\n"
"v_exp_f32 v17, v17\n"
"v_mfma_f32_16x16x32_bf16 v[152:155], a[104:107], v[72:75], v[152:155]\n"
"s_add_u32 s40, s40, 32768\n"
"ds_read_b128 a[120:123], v227 offset:14336\n"
"ds_read_b128 a[124:127], v227 offset:15360\n"
"v_cvt_pk_bf16_f32 v16, v16, v17\n"
"v_mfma_f32_16x16x32_bf16 v[184:187], a[104:107], v[76:79], v[184:187]\n"
"s_waitcnt lgkmcnt(4)\n"
"v_exp_f32 v18, v18\n"
"v_mfma_f32_16x16x32_bf16 v[156:159], a[108:111], v[72:75], v[156:159]\n"
"s_cmp_eq_u32 s40, s46\n"
"buffer_load_dwordx4 v225, s[8:11], s44 offen offset:0 sc1 lds\n"
"v_mfma_f32_16x16x32_bf16 v[188:191], a[108:111], v[76:79], v[188:191]\n"
"s_waitcnt lgkmcnt(2)\n"
"v_exp_f32 v19, v19\n"
"v_mfma_f32_16x16x32_bf16 v[160:163], a[112:115], v[72:75], v[160:163]\n"
"s_cselect_b32 s40, s47, s40\n"
"v_exp_f32 v24, v24\n"
"v_cvt_pk_bf16_f32 v17, v18, v19\n"
"v_mfma_f32_16x16x32_bf16 v[192:195], a[112:115], v[76:79], v[192:195]\n"
"v_exp_f32 v25, v25\n"
"v_mfma_f32_16x16x32_bf16 v[164:167], a[116:119], v[72:75], v[164:167]\n"
"ds_read_b128 a[64:67], v227 offset:16384\n"
"ds_read_b128 a[68:71], v227 offset:17408\n"
"v_cvt_pk_bf16_f32 v24, v24, v25\n"
"v_mfma_f32_16x16x32_bf16 v[196:199], a[116:119], v[76:79], v[196:199]\n"
"s_waitcnt lgkmcnt(2)\n"
"buffer_load_dwordx4 v225, s[8:11], s44 offen offset:1024 sc1 lds\n"
"v_mfma_f32_16x16x32_bf16 v[168:171], a[120:123], v[72:75], v[168:171]\n"
"ds_read_b128 a[72:75], v227 offset:18432\n"
"v_mfma_f32_16x16x32_bf16 v[200:203], a[120:123], v[76:79], v[200:203]\n"
"v_exp_f32 v26, v26\n"
"v_mfma_f32_16x16x32_bf16 v[172:175], a[124:127], v[72:75], v[172:175]\n"
"ds_read_b128 a[76:79], v227 offset:19456\n"
"v_mfma_f32_16x16x32_bf16 v[204:207], a[124:127], v[76:79], v[204:207]\n"
"v_exp_f32 v27, v27\n"
"v_mfma_f32_16x16x32_bf16 a[140:143], a[128:131], v[72:75], a[140:143]\n"
"v_exp_f32 v20, v20\n"
"v_cvt_pk_bf16_f32 v25, v26, v27\n"
"v_mfma_f32_16x16x32_bf16 a[144:147], a[128:131], v[76:79], a[144:147]\n"
"s_waitcnt lgkmcnt(2)\n"
"buffer_load_dwordx4 v225, s[8:11], s44 offen offset:2048 sc1 lds\n"
"v_mfma_f32_16x16x32_bf16 v[144:147], a[64:67], v[0:3], v[144:147]\n"
"ds_read_b128 a[80:83], v227 offset:20480\n"
"v_readlane_b32 s43, v228, s42\n"
"v_mfma_f32_16x16x32_bf16 v[176:179], a[64:67], v[8:11], v[176:179]\n"
"v_exp_f32 v21, v21\n"
"v_mfma_f32_16x16x32_bf16 v[148:151], a[68:71], v[0:3], v[148:151]\n"
"ds_read_b128 a[84:87], v227 offset:21504\n"
"ds_read_b128 a[88:91], v227 offset:22528\n"
"v_cvt_pk_bf16_f32 v18, v20, v21\n"
"v_mfma_f32_16x16x32_bf16 v[180:183], a[68:71], v[8:11], v[180:183]\n"
"s_waitcnt lgkmcnt(3)\n"
"v_exp_f32 v22, v22\n"
"v_mfma_f32_16x16x32_bf16 v[152:155], a[72:75], v[0:3], v[152:155]\n"
"buffer_load_dwordx4 v225, s[8:11], s44 offen offset:3072 sc1 lds\n"
"v_mfma_f32_16x16x32_bf16 v[184:187], a[72:75], v[8:11], v[184:187]\n"
"s_mov_b32 m0, s40\n"
"ds_read_b128 a[92:95], v227 offset:23552\n"
"v_readlane_b32 s44, v229, s42\n"
"v_mfma_f32_16x16x32_bf16 v[156:159], a[76:79], v[0:3], v[156:159]\n"
"v_exp_f32 v23, v23\n"
"v_mfma_f32_16x16x32_bf16 v[188:191], a[76:79], v[8:11], v[188:191]\n"
"s_waitcnt lgkmcnt(3)\n"
"v_exp_f32 v28, v28\n"
"v_cvt_pk_bf16_f32 v19, v22, v23\n"
"v_mfma_f32_16x16x32_bf16 v[160:163], a[80:83], v[0:3], v[160:163]\n"
"s_add_u32 s42, s42, 1\n"
"v_exp_f32 v29, v29\n"
"v_mfma_f32_16x16x32_bf16 v[192:195], a[80:83], v[8:11], v[192:195]\n"
"s_waitcnt lgkmcnt(1)\n"
"ds_read_b128 a[96:99], v227 offset:24576\n"
"ds_read_b128 a[100:103], v227 offset:25600\n"
"v_cvt_pk_bf16_f32 v26, v28, v29\n"
"v_mfma_f32_16x16x32_bf16 v[164:167], a[84:87], v[0:3], v[164:167]\n"
"v_exp_f32 v30, v30\n"
"v_mfma_f32_16x16x32_bf16 v[196:199], a[84:87], v[8:11], v[196:199]\n"
"v_exp_f32 v31, v31\n"
"v_mfma_f32_16x16x32_bf16 v[168:171], a[88:91], v[0:3], v[168:171]\n"
"ds_read_b128 a[104:107], v227 offset:26624\n"
"v_cvt_pk_bf16_f32 v27, v30, v31\n"
"v_mfma_f32_16x16x32_bf16 v[200:203], a[88:91], v[8:11], v[200:203]\n"
"s_waitcnt lgkmcnt(3)\n"
"v_exp_f32 v40, v40\n"
"v_mfma_f32_16x16x32_bf16 v[172:175], a[92:95], v[0:3], v[172:175]\n"
"v_exp_f32 v41, v41\n"
"v_mfma_f32_16x16x32_bf16 v[204:207], a[92:95], v[8:11], v[204:207]\n"
"ds_read_b128 a[108:111], v227 offset:27648\n"
"ds_read_b128 a[112:115], v227 offset:28672\n"
"v_cvt_pk_bf16_f32 v68, v40, v41\n"
"v_mfma_f32_16x16x32_bf16 a[140:143], a[128:131], v[0:3], a[140:143]\n"
"v_exp_f32 v42, v42\n"
"v_mfma_f32_16x16x32_bf16 a[144:147], a[128:131], v[8:11], a[144:147]\n"
"s_waitcnt lgkmcnt(3)\n"
"ds_read_b128 a[116:119], v227 offset:29696\n"
"ds_read_b128 a[120:123], v227 offset:30720\n"
"v_mfma_f32_16x16x32_bf16 v[144:147], a[96:99], v[16:19], v[144:147]\n"
"v_exp_f32 v43, v43\n"
"v_mfma_f32_16x16x32_bf16 v[176:179], a[96:99], v[24:27], v[176:179]\n"
"v_exp_f32 v44, v44\n"
"v_mfma_f32_16x16x32_bf16 v[148:151], a[100:103], v[16:19], v[148:151]\n"
"ds_read_b128 a[124:127], v227 offset:31744\n"
"v_cvt_pk_bf16_f32 v69, v42, v43\n"
"v_mfma_f32_16x16x32_bf16 v[180:183], a[100:103], v[24:27], v[180:183]\n"
"s_waitcnt lgkmcnt(5)\n"
"s_waitcnt vmcnt(17)\n"
"s_barrier\n"
"v_mfma_f32_16x16x32_bf16 v[152:155], a[104:107], v[16:19], v[152:155]\n"
"s_add_u32 s41, s41, 32768\n"
"buffer_load_dwordx4 v225, s[8:11], s43 offen offset:0 sc1 lds\n"
"v_mfma_f32_16x16x32_bf16 v[184:187], a[104:107], v[24:27], v[184:187]\n"
"s_waitcnt lgkmcnt(3)\n"
"buffer_load_dwordx4 v225, s[8:11], s43 offen offset:1024 sc1 lds\n"
"v_mfma_f32_16x16x32_bf16 v[156:159], a[108:111], v[16:19], v[156:159]\n"
"s_cmp_eq_u32 s41, 163840\n"
"buffer_load_dwordx4 v225, s[8:11], s43 offen offset:2048 sc1 lds\n"
"v_mfma_f32_16x16x32_bf16 v[188:191], a[108:111], v[24:27], v[188:191]\n"
"s_cselect_b32 s41, 0, s41\n"
"ds_read_b128 a[64:67], v226 offset:0\n"
"v_add_u32 v227, s41, v224\n"
"v_mfma_f32_16x16x32_bf16 v[160:163], a[112:115], v[16:19], v[160:163]\n"
"buffer_load_dwordx4 v225, s[8:11], s43 offen offset:3072 sc1 lds\n"
"v_mfma_f32_16x16x32_bf16 v[192:195], a[112:115], v[24:27], v[192:195]\n"
"s_waitcnt lgkmcnt(1)\n"
"ds_read_b128 a[68:71], v226 offset:1024\n"
"s_add_u32 m0, s40, 16384\n"
"v_mfma_f32_16x16x32_bf16 v[164:167], a[116:119], v[16:19], v[164:167]\n"
"ds_read_b128 a[72:75], v226 offset:2048\n"
"v_mfma_f32_16x16x32_bf16 v[196:199], a[116:119], v[24:27], v[196:199]\n"
"ds_read_b128 a[76:79], v226 offset:3072\n"
"v_mfma_f32_16x16x32_bf16 v[168:171], a[120:123], v[16:19], v[168:171]\n"
"s_add_u32 s40, s40, 32768\n"
"ds_read_b128 a[80:83], v226 offset:4096\n"
"v_mfma_f32_16x16x32_bf16 v[200:203], a[120:123], v[24:27], v[200:203]\n"
"s_cmp_eq_u32 s40, s46\n"
"v_exp_f32 v45, v45\n"
"v_mfma_f32_16x16x32_bf16 v[172:175], a[124:127], v[16:19], v[172:175]\n"
"s_cselect_b32 s40, s47, s40\n"
"ds_read_b128 a[84:87], v226 offset:5120\n"
"ds_read_b128 a[88:91], v226 offset:6144\n"
"v_cvt_pk_bf16_f32 v70, v44, v45\n"
"v_mfma_f32_16x16x32_bf16 v[204:207], a[124:127], v[24:27], v[204:207]\n"
"buffer_load_dwordx4 v225, s[8:11], s44 offen offset:0 sc1 lds\n"
"v_mfma_f32_16x16x32_bf16 a[140:143], a[128:131], v[16:19], a[140:143]\n"
"ds_read_b128 a[92:95], v226 offset:7168\n"
"v_mfma_f32_16x16x32_bf16 a[144:147], a[128:131], v[24:27], a[144:147]\n"
"v_exp_f32 v46, v46\n"
"v_exp_f32 v47, v47\n"
"s_nop 0\n"
"v_cvt_pk_bf16_f32 v71, v46, v47\n"
"s_waitcnt lgkmcnt(7)\n"
"s_nop 0\n"
"v_mfma_f32_16x16x32_bf16 v[176:179], a[64:67], v[68:71], v[176:179]\n"
"s_waitcnt lgkmcnt(4)\n"
"ds_read_b128 a[96:99], v226 offset:8192\n"
"ds_read_b128 a[100:103], v226 offset:9216\n"
"v_mfma_f32_16x16x32_bf16 v[180:183], a[68:71], v[68:71], v[180:183]\n"
"ds_read_b128 a[104:107], v226 offset:10240\n"
"v_mfma_f32_16x16x32_bf16 v[184:187], a[72:75], v[68:71], v[184:187]\n"
"buffer_load_dwordx4 v225, s[8:11], s44 offen offset:1024 sc1 lds\n"
"v_mfma_f32_16x16x32_bf16 v[188:191], a[76:79], v[68:71], v[188:191]\n"
"s_waitcnt lgkmcnt(4)\n"
"ds_read_b128 a[108:111], v226 offset:11264\n"
"v_accvgpr_read_b32 v251, a140\n"
"v_mfma_f32_16x16x32_bf16 v[192:195], a[80:83], v[68:71], v[192:195]\n"
"ds_read_b128 a[112:115], v226 offset:12288\n"
"v_subrev_u32 v253, 0x2b800000, v251\n"
"v_mfma_f32_16x16x32_bf16 v[196:199], a[84:87], v[68:71], v[196:199]\n"
"ds_read_b128 a[116:119], v226 offset:13312\n"
"v_mfma_f32_16x16x32_bf16 v[200:203], a[88:91], v[68:71], v[200:203]\n"
"s_waitcnt lgkmcnt(6)\n"
"ds_read_b128 a[120:123], v226 offset:14336\n"
"v_mfma_f32_16x16x32_bf16 v[204:207], a[92:95], v[68:71], v[204:207]\n"
"v_exp_f32 v56, v56\n"
"v_mfma_f32_16x16x32_bf16 a[144:147], a[128:131], v[68:71], a[144:147]\n"
"v_exp_f32 v57, v57\n"
"s_nop 0\n"
"v_cvt_pk_bf16_f32 v76, v56, v57\n"
"v_exp_f32 v59, v59\n"
"v_exp_f32 v58, v58\n"
"s_nop 0\n"
"v_cvt_pk_bf16_f32 v77, v58, v59\n"
"v_exp_f32 v62, v62\n"
"v_exp_f32 v63, v63\n"
"s_nop 0\n"
"v_cvt_pk_bf16_f32 v79, v62, v63\n"
"v_exp_f32 v60, v60\n"
"v_exp_f32 v61, v61\n"
"s_nop 0\n"
"v_cvt_pk_bf16_f32 v78, v60, v61\n"
"s_waitcnt lgkmcnt(6)\n"
"s_nop 0\n"
"v_mfma_f32_16x16x32_bf16 v[176:179], a[96:99], v[76:79], v[176:179]\n"
"s_waitcnt lgkmcnt(4)\n"
"ds_read_b128 a[124:127], v226 offset:15360\n"
"v_mfma_f32_16x16x32_bf16 v[180:183], a[100:103], v[76:79], v[180:183]\n"
"v_cmp_le_u32 vcc, 0x34000000, v253\n"
"s_cmp_lg_u64 vcc, 0\n"
"s_cbranch_scc1 L1029_%=\n"
"L1028_%=:\n"
"v_mfma_f32_16x16x32_bf16 v[184:187], a[104:107], v[76:79], v[184:187]\n"
"s_waitcnt lgkmcnt(3)\n"
"v_rcp_f32 v251, v251\n"
"v_mfma_f32_16x16x32_bf16 v[188:191], a[108:111], v[76:79], v[188:191]\n"
"buffer_load_dwordx4 v225, s[8:11], s44 offen offset:2048 sc1 lds\n"
"v_mul_f32 v144, v251, v144\n"
"v_mfma_f32_16x16x32_bf16 v[192:195], a[112:115], v[76:79], v[192:195]\n"
"s_waitcnt lgkmcnt(1)\n"
"s_waitcnt vmcnt(15)\n"
"s_barrier\n"
"v_mfma_f32_16x16x32_bf16 v[196:199], a[116:119], v[76:79], v[196:199]\n"
"v_mul_f32 v145, v251, v145\n"
"v_mul_f32 v152, v251, v152\n"
"v_mul_f32 v153, v251, v153\n"
"v_mfma_f32_16x16x32_bf16 v[200:203], a[120:123], v[76:79], v[200:203]\n"
"s_waitcnt lgkmcnt(0)\n"
"v_mul_f32 v160, v251, v160\n"
"v_mul_f32 v161, v251, v161\n"
"v_mul_f32 v146, v251, v146\n"
"v_mfma_f32_16x16x32_bf16 v[204:207], a[124:127], v[76:79], v[204:207]\n"
"v_mul_f32 v147, v251, v147\n"
"v_cvt_pk_bf16_f32 v144, v144, v145\n"
"v_mul_f32 v154, v251, v154\n"
"v_mfma_f32_16x16x32_bf16 a[144:147], a[128:131], v[76:79], a[144:147]\n"
"v_mul_f32 v155, v251, v155\n"
"v_cvt_pk_bf16_f32 v152, v152, v153\n"
"v_mul_f32 v148, v251, v148\n"
"v_mul_f32 v149, v251, v149\n"
"v_mul_f32 v150, v251, v150\n"
"v_mul_f32 v151, v251, v151\n"
"v_cvt_pk_bf16_f32 v145, v146, v147\n"
"v_cvt_pk_bf16_f32 v146, v148, v149\n"
"v_cvt_pk_bf16_f32 v147, v150, v151\n"
"buffer_store_dwordx4 v[144:147], v224, s[8:11], s60 offen offset:0 sc1\n"
"v_mul_f32 v156, v251, v156\n"
"v_mul_f32 v157, v251, v157\n"
"v_mul_f32 v158, v251, v158\n"
"v_mul_f32 v159, v251, v159\n"
"v_cvt_pk_bf16_f32 v153, v154, v155\n"
"v_cvt_pk_bf16_f32 v154, v156, v157\n"
"v_cvt_pk_bf16_f32 v155, v158, v159\n"
"buffer_store_dwordx4 v[152:155], v224, s[8:11], s60 offen offset:1024 sc1\n"
"v_mul_f32 v162, v251, v162\n"
"v_mul_f32 v163, v251, v163\n"
"v_mul_f32 v164, v251, v164\n"
"v_mul_f32 v165, v251, v165\n"
"v_mul_f32 v166, v251, v166\n"
"v_mul_f32 v167, v251, v167\n"
"v_cvt_pk_bf16_f32 v160, v160, v161\n"
"v_cvt_pk_bf16_f32 v161, v162, v163\n"
"v_cvt_pk_bf16_f32 v162, v164, v165\n"
"v_cvt_pk_bf16_f32 v163, v166, v167\n"
"buffer_store_dwordx4 v[160:163], v224, s[8:11], s60 offen offset:2048 sc1\n"
"v_mul_f32 v168, v251, v168\n"
"v_mul_f32 v169, v251, v169\n"
"v_mul_f32 v170, v251, v170\n"
"v_mul_f32 v171, v251, v171\n"
"v_mul_f32 v172, v251, v172\n"
"v_mul_f32 v173, v251, v173\n"
"v_mul_f32 v174, v251, v174\n"
"v_mul_f32 v175, v251, v175\n"
"v_cvt_pk_bf16_f32 v168, v168, v169\n"
"v_cvt_pk_bf16_f32 v169, v170, v171\n"
"v_cvt_pk_bf16_f32 v170, v172, v173\n"
"v_cvt_pk_bf16_f32 v171, v174, v175\n"
"buffer_store_dwordx4 v[168:171], v224, s[8:11], s60 offen offset:3072 sc1\n"
"buffer_load_dwordx4 v225, s[8:11], s44 offen offset:3072 sc1 lds\n"
"ds_read_b128 a[64:67], v227 offset:0\n"
"ds_read_b128 a[68:71], v227 offset:1024\n"
"ds_read_b128 a[72:75], v227 offset:2048\n"
"ds_read_b128 a[76:79], v227 offset:3072\n"
"ds_read_b128 a[80:83], v227 offset:4096\n"
"ds_read_b128 a[84:87], v227 offset:5120\n"
"LTL_%=:\n"
"ds_read_b128 a[88:91], v227 offset:6144\n"
"s_waitcnt lgkmcnt(6)\n"
"v_mfma_f32_16x16x32_bf16 v[0:3], a[64:67], v[80:83], 0\n"
"v_mfma_f32_16x16x32_bf16 v[40:43], a[64:67], v[112:115], 0\n"
"s_waitcnt lgkmcnt(5)\n"
"ds_read_b128 a[92:95], v227 offset:7168\n"
"v_mfma_f32_16x16x32_bf16 v[4:7], a[68:71], v[80:83], 0\n"
"v_readlane_b32 s43, v228, s42\n"
"v_mfma_f32_16x16x32_bf16 v[44:47], a[68:71], v[112:115], 0\n"
"s_waitcnt lgkmcnt(5)\n"
"ds_read_b128 a[96:99], v227 offset:8192\n"
"v_accvgpr_read_b32 v252, a144\n"
"v_mfma_f32_16x16x32_bf16 v[8:11], a[72:75], v[80:83], 0\n"
"s_add_u32 s51, s60, 16384\n"
"v_subrev_u32 v253, 0x2b800000, v252\n"
"s_add_u32 s41, s41, 32768\n"
"v_mfma_f32_16x16x32_bf16 v[48:51], a[72:75], v[112:115], 0\n"
"s_waitcnt lgkmcnt(5)\n"
"v_cmp_le_u32 vcc, 0x34000000, v253\n"
"s_cmp_lg_u64 vcc, 0\n"
"s_cbranch_scc1 L1047_%=\n"
"L1046_%=:\n"
"v_mfma_f32_16x16x32_bf16 v[12:15], a[76:79], v[80:83], 0\n"
"s_cmp_eq_u32 s41, 163840\n"
"ds_read_b128 a[100:103], v227 offset:9216\n"
"s_cselect_b32 s41, 0, s41\n"
"v_mfma_f32_16x16x32_bf16 v[52:55], a[76:79], v[112:115], 0\n"
"s_waitcnt lgkmcnt(3)\n"
"v_rcp_f32 v252, v252\n"
"v_add_u32 v226, s41, v224\n"
"v_mfma_f32_16x16x32_bf16 v[16:19], a[80:83], v[80:83], 0\n"
"s_mov_b32 m0, s40\n"
"ds_read_b128 a[104:107], v227 offset:10240\n"
"v_readlane_b32 s44, v229, s42\n"
"v_mfma_f32_16x16x32_bf16 v[56:59], a[80:83], v[112:115], 0\n"
"s_add_u32 s41, s41, 32768\n"
"ds_read_b128 a[108:111], v227 offset:11264\n"
"v_mul_f32 v176, v252, v176\n"
"v_mul_f32 v177, v252, v177\n"
"v_mfma_f32_16x16x32_bf16 v[20:23], a[84:87], v[80:83], 0\n"
"s_cmp_eq_u32 s41, 163840\n"
"buffer_load_dwordx4 v225, s[8:11], s43 offen offset:0 sc1 lds\n"
"v_mul_f32 v178, v252, v178\n"
"v_mfma_f32_16x16x32_bf16 v[60:63], a[84:87], v[112:115], 0\n"
"s_cselect_b32 s41, 0, s41\n"
"ds_read_b128 a[112:115], v227 offset:12288\n"
"v_add_u32 v235, s41, v224\n"
"v_mul_f32 v179, v252, v179\n"
"v_mfma_f32_16x16x32_bf16 v[24:27], a[88:91], v[80:83], 0\n"
"s_add_u32 s42, s42, 1\n"
"v_mul_f32 v180, v252, v180\n"
"v_mul_f32 v181, v252, v181\n"
"v_mfma_f32_16x16x32_bf16 v[64:67], a[88:91], v[112:115], 0\n"
"s_waitcnt lgkmcnt(3)\n"
"ds_read_b128 a[116:119], v227 offset:13312\n"
"v_mul_f32 v182, v252, v182\n"
"v_mul_f32 v183, v252, v183\n"
"v_mfma_f32_16x16x32_bf16 v[28:31], a[92:95], v[80:83], 0\n"
"s_add_u32 s41, s41, 32768\n"
"buffer_load_dwordx4 v225, s[8:11], s43 offen offset:1024 sc1 lds\n"
"v_cvt_pk_bf16_f32 v176, v176, v177\n"
"v_mfma_f32_16x16x32_bf16 v[68:71], a[92:95], v[112:115], 0\n"
"s_cmp_eq_u32 s41, 163840\n"
"ds_read_b128 a[120:123], v227 offset:14336\n"
"v_cvt_pk_bf16_f32 v177, v178, v179\n"
"v_mfma_f32_16x16x32_bf16 v[32:35], a[96:99], v[80:83], 0\n"
"s_cselect_b32 s41, 0, s41\n"
"v_cvt_pk_bf16_f32 v178, v180, v181\n"
"v_add_u32 v236, s41, v224\n"
"v_cvt_pk_bf16_f32 v179, v182, v183\n"
"v_mfma_f32_16x16x32_bf16 v[72:75], a[96:99], v[112:115], 0\n"
"s_add_u32 s41, s41, 32768\n"
"buffer_store_dwordx4 v[176:179], v224, s[8:11], s51 offen offset:0 sc1\n"
"v_mul_f32 v184, v252, v184\n"
"v_mfma_f32_16x16x32_bf16 v[36:39], a[100:103], v[80:83], 0\n"
"s_cmp_eq_u32 s41, 163840\n"
"ds_read_b128 a[124:127], v227 offset:15360\n"
"v_mul_f32 v185, v252, v185\n"
"v_mul_f32 v186, v252, v186\n"
"v_mfma_f32_16x16x32_bf16 v[76:79], a[100:103], v[112:115], 0\n"
"s_waitcnt lgkmcnt(3)\n"
"buffer_load_dwordx4 v225, s[8:11], s43 offen offset:2048 sc1 lds\n"
"v_mul_f32 v187, v252, v187\n"
"v_mfma_f32_16x16x32_bf16 v[0:3], a[104:107], v[88:91], v[0:3]\n"
"s_cselect_b32 s41, 0, s41\n"
"ds_read_b128 a[64:67], v227 offset:16384\n"
"v_add_u32 v237, s41, v224\n"
"v_mul_f32 v188, v252, v188\n"
"v_mfma_f32_16x16x32_bf16 v[40:43], a[104:107], v[120:123], v[40:43]\n"
"s_waitcnt vmcnt(10)\n"
"buffer_store_dwordx2 v[232:233], v230, s[8:11], s61 offen sc1\n"
"v_mul_f32 v189, v252, v189\n"
"v_mfma_f32_16x16x32_bf16 v[4:7], a[108:111], v[88:91], v[4:7]\n"
"ds_read_b128 a[68:71], v227 offset:17408\n"
"v_mul_f32 v190, v252, v190\n"
"v_mul_f32 v191, v252, v191\n"
"v_mfma_f32_16x16x32_bf16 v[44:47], a[108:111], v[120:123], v[44:47]\n"
"buffer_load_dwordx4 v225, s[8:11], s43 offen offset:3072 sc1 lds\n"
"v_cvt_pk_bf16_f32 v184, v184, v185\n"
"v_mfma_f32_16x16x32_bf16 v[8:11], a[112:115], v[88:91], v[8:11]\n"
"s_add_u32 m0, s40, 16384\n"
"ds_read_b128 a[72:75], v227 offset:18432\n"
"v_cvt_pk_bf16_f32 v185, v186, v187\n"
"v_cvt_pk_bf16_f32 v186, v188, v189\n"
"v_mfma_f32_16x16x32_bf16 v[48:51], a[112:115], v[120:123], v[48:51]\n"
"s_waitcnt lgkmcnt(3)\n"
"ds_read_b128 a[76:79], v227 offset:19456\n"
"v_cvt_pk_bf16_f32 v187, v190, v191\n"
"v_mul_f32 v192, v252, v192\n"
"v_mfma_f32_16x16x32_bf16 v[12:15], a[116:119], v[88:91], v[12:15]\n"
"s_lshl_b32 s62, s34, 11\n"
"buffer_store_dwordx4 v[184:187], v224, s[8:11], s51 offen offset:1024 sc1\n"
"v_mul_f32 v193, v252, v193\n"
"v_mfma_f32_16x16x32_bf16 v[52:55], a[116:119], v[120:123], v[52:55]\n"
"s_sub_u32 s62, s61, s62\n"
"buffer_load_dwordx4 v225, s[8:11], s44 offen offset:0 sc1 lds\n"
"v_mul_f32 v194, v252, v194\n"
"v_mfma_f32_16x16x32_bf16 v[16:19], a[120:123], v[88:91], v[16:19]\n"
"s_add_u32 s62, s62, 15360\n"
"ds_read_b128 a[80:83], v227 offset:20480\n"
"v_mul_f32 v195, v252, v195\n"
"v_mul_f32 v196, v252, v196\n"
"v_mfma_f32_16x16x32_bf16 v[56:59], a[120:123], v[120:123], v[56:59]\n"
"s_add_u32 s40, s40, 32768\n"
"ds_read_b128 a[84:87], v227 offset:21504\n"
"v_mul_f32 v197, v252, v197\n"
"v_mfma_f32_16x16x32_bf16 v[20:23], a[124:127], v[88:91], v[20:23]\n"
"s_cmp_eq_u32 s40, s46\n"
"buffer_load_dwordx4 v225, s[8:11], s44 offen offset:1024 sc1 lds\n"
"v_mul_f32 v198, v252, v198\n"
"v_mfma_f32_16x16x32_bf16 v[60:63], a[124:127], v[120:123], v[60:63]\n"
"s_waitcnt lgkmcnt(3)\n"
"ds_read_b128 a[88:91], v227 offset:22528\n"
"v_mul_f32 v199, v252, v199\n"
"v_lshrrev_b32 v238, 1, v224\n"
"v_mfma_f32_16x16x32_bf16 v[24:27], a[64:67], v[88:91], v[24:27]\n"
"s_cselect_b32 s40, s47, s40\n"
"v_cvt_pk_bf16_f32 v192, v192, v193\n"
"v_and_b32 v238, 24, v238\n"
"v_cvt_pk_bf16_f32 v193, v194, v195\n"
"v_mfma_f32_16x16x32_bf16 v[64:67], a[64:67], v[120:123], v[64:67]\n"
"ds_read_b128 a[92:95], v227 offset:23552\n"
"v_cvt_pk_bf16_f32 v194, v196, v197\n"
"v_cvt_pk_bf16_f32 v195, v198, v199\n"
"v_mfma_f32_16x16x32_bf16 v[28:31], a[68:71], v[88:91], v[28:31]\n"
"buffer_store_dwordx4 v[192:195], v224, s[8:11], s51 offen offset:2048 sc1\n"
"v_mul_f32 v200, v252, v200\n"
"v_mfma_f32_16x16x32_bf16 v[68:71], a[68:71], v[120:123], v[68:71]\n"
"buffer_load_dwordx4 v225, s[8:11], s44 offen offset:2048 sc1 lds\n"
"v_mul_f32 v201, v252, v201\n"
"v_mfma_f32_16x16x32_bf16 v[32:35], a[72:75], v[88:91], v[32:35]\n"
"ds_read_b128 a[96:99], v227 offset:24576\n"
"v_mul_f32 v202, v252, v202\n"
"v_mul_f32 v203, v252, v203\n"
"v_mfma_f32_16x16x32_bf16 v[72:75], a[72:75], v[120:123], v[72:75]\n"
"s_waitcnt lgkmcnt(3)\n"
"ds_read_b128 a[100:103], v227 offset:25600\n"
"v_mul_f32 v204, v252, v204\n"
"v_mul_f32 v205, v252, v205\n"
"v_mfma_f32_16x16x32_bf16 v[36:39], a[76:79], v[88:91], v[36:39]\n"
"buffer_load_dwordx4 v225, s[8:11], s44 offen offset:3072 sc1 lds\n"
"v_mul_f32 v206, v252, v206\n"
"v_mfma_f32_16x16x32_bf16 v[76:79], a[76:79], v[120:123], v[76:79]\n"
"ds_read_b128 a[104:107], v227 offset:26624\n"
"v_mul_f32 v207, v252, v207\n"
"v_cvt_pk_bf16_f32 v200, v200, v201\n"
"v_mfma_f32_16x16x32_bf16 v[0:3], a[80:83], v[96:99], v[0:3]\n"
"v_cvt_pk_bf16_f32 v201, v202, v203\n"
"v_cvt_pk_bf16_f32 v202, v204, v205\n"
"v_mfma_f32_16x16x32_bf16 v[40:43], a[80:83], v[128:131], v[40:43]\n"
"ds_read_b128 a[108:111], v227 offset:27648\n"
"v_cvt_pk_bf16_f32 v203, v206, v207\n"
"v_mfma_f32_16x16x32_bf16 v[4:7], a[84:87], v[96:99], v[4:7]\n"
"buffer_store_dwordx4 v[200:203], v224, s[8:11], s51 offen offset:3072 sc1\n"
"v_mfma_f32_16x16x32_bf16 v[44:47], a[84:87], v[128:131], v[44:47]\n"
"s_waitcnt lgkmcnt(3)\n"
"ds_read_b128 a[112:115], v227 offset:28672\n"
"v_mfma_f32_16x16x32_bf16 v[8:11], a[88:91], v[96:99], v[8:11]\n"
"v_readlane_b32 s43, v228, s42\n"
"v_mfma_f32_16x16x32_bf16 v[48:51], a[88:91], v[128:131], v[48:51]\n"
"ds_read_b128 a[116:119], v227 offset:29696\n"
"v_mfma_f32_16x16x32_bf16 v[12:15], a[92:95], v[96:99], v[12:15]\n"
"v_mfma_f32_16x16x32_bf16 v[52:55], a[92:95], v[128:131], v[52:55]\n"
"ds_read_b128 a[120:123], v227 offset:30720\n"
"v_mfma_f32_16x16x32_bf16 v[16:19], a[96:99], v[96:99], v[16:19]\n"
"s_mov_b32 m0, s40\n"
"s_andn2_b32 s63, s61, 31\n"
"v_mfma_f32_16x16x32_bf16 v[56:59], a[96:99], v[128:131], v[56:59]\n"
"s_waitcnt lgkmcnt(3)\n"
"ds_read_b128 a[124:127], v227 offset:31744\n"
"v_mfma_f32_16x16x32_bf16 v[20:23], a[100:103], v[96:99], v[20:23]\n"
"s_barrier\n"
"v_mfma_f32_16x16x32_bf16 v[60:63], a[100:103], v[128:131], v[60:63]\n"
"buffer_load_dwordx2 v[240:241], v238, s[8:11], s63 offen sc1\n"
"v_mfma_f32_16x16x32_bf16 v[24:27], a[104:107], v[96:99], v[24:27]\n"
"ds_read_b128 a[64:67], v226 offset:0\n"
"v_readlane_b32 s44, v229, s42\n"
"v_mfma_f32_16x16x32_bf16 v[64:67], a[104:107], v[128:131], v[64:67]\n"
"s_add_u32 s42, s42, 1\n"
"buffer_load_dwordx4 v225, s[8:11], s43 offen offset:0 sc1 lds\n"
"v_mfma_f32_16x16x32_bf16 v[28:31], a[108:111], v[96:99], v[28:31]\n"
"ds_read_b128 a[68:71], v226 offset:1024\n"
"v_mfma_f32_16x16x32_bf16 v[68:71], a[108:111], v[128:131], v[68:71]\n"
"s_waitcnt lgkmcnt(3)\n"
"ds_read_b128 a[72:75], v226 offset:2048\n"
"v_mfma_f32_16x16x32_bf16 v[32:35], a[112:115], v[96:99], v[32:35]\n"
"buffer_load_dwordx4 v225, s[8:11], s43 offen offset:1024 sc1 lds\n"
"v_mfma_f32_16x16x32_bf16 v[72:75], a[112:115], v[128:131], v[72:75]\n"
"ds_read_b128 a[76:79], v226 offset:3072\n"
"v_mfma_f32_16x16x32_bf16 v[36:39], a[116:119], v[96:99], v[36:39]\n"
"v_mfma_f32_16x16x32_bf16 v[76:79], a[116:119], v[128:131], v[76:79]\n"
"ds_read_b128 a[80:83], v226 offset:4096\n"
"v_mfma_f32_16x16x32_bf16 v[0:3], a[120:123], v[104:107], v[0:3]\n"
"buffer_load_dwordx4 v225, s[8:11], s43 offen offset:2048 sc1 lds\n"
"v_mfma_f32_16x16x32_bf16 v[40:43], a[120:123], v[136:139], v[40:43]\n"
"s_waitcnt lgkmcnt(3)\n"
"ds_read_b128 a[84:87], v226 offset:5120\n"
"v_mfma_f32_16x16x32_bf16 v[4:7], a[124:127], v[104:107], v[4:7]\n"
"v_mfma_f32_16x16x32_bf16 v[44:47], a[124:127], v[136:139], v[44:47]\n"
"ds_read_b128 a[88:91], v226 offset:6144\n"
"v_mfma_f32_16x16x32_bf16 v[8:11], a[64:67], v[104:107], v[8:11]\n"
"buffer_load_dwordx4 v225, s[8:11], s43 offen offset:3072 sc1 lds\n"
"v_mfma_f32_16x16x32_bf16 v[48:51], a[64:67], v[136:139], v[48:51]\n"
"s_add_u32 m0, s40, 16384\n"
"ds_read_b128 a[92:95], v226 offset:7168\n"
"v_mfma_f32_16x16x32_bf16 v[12:15], a[68:71], v[104:107], v[12:15]\n"
"v_mfma_f32_16x16x32_bf16 v[52:55], a[68:71], v[136:139], v[52:55]\n"
"s_waitcnt lgkmcnt(3)\n"
"ds_read_b128 a[96:99], v227 offset:0\n"
"v_mfma_f32_16x16x32_bf16 v[16:19], a[72:75], v[104:107], v[16:19]\n"
"buffer_load_dwordx4 v225, s[8:11], s44 offen offset:0 sc1 lds\n"
"v_mfma_f32_16x16x32_bf16 v[56:59], a[72:75], v[136:139], v[56:59]\n"
"s_add_u32 s40, s40, 32768\n"
"ds_read_b128 a[100:103], v227 offset:1024\n"
"v_mfma_f32_16x16x32_bf16 v[20:23], a[76:79], v[104:107], v[20:23]\n"
"s_cmp_eq_u32 s40, s46\n"
"s_cselect_b32 s40, s47, s40\n"
"v_mfma_f32_16x16x32_bf16 v[60:63], a[76:79], v[136:139], v[60:63]\n"
"ds_read_b128 a[104:107], v227 offset:2048\n"
"v_mfma_f32_16x16x32_bf16 v[24:27], a[80:83], v[104:107], v[24:27]\n"
"buffer_load_dwordx4 v225, s[8:11], s44 offen offset:1024 sc1 lds\n"
"v_mfma_f32_16x16x32_bf16 v[64:67], a[80:83], v[136:139], v[64:67]\n"
"s_waitcnt lgkmcnt(3)\n"
"ds_read_b128 a[108:111], v227 offset:3072\n"
"v_mfma_f32_16x16x32_bf16 v[28:31], a[84:87], v[104:107], v[28:31]\n"
"v_mfma_f32_16x16x32_bf16 v[68:71], a[84:87], v[136:139], v[68:71]\n"
"ds_read_b128 a[112:115], v227 offset:4096\n"
"v_mfma_f32_16x16x32_bf16 v[32:35], a[88:91], v[104:107], v[32:35]\n"
"buffer_load_dwordx4 v225, s[8:11], s44 offen offset:2048 sc1 lds\n"
"v_mfma_f32_16x16x32_bf16 v[72:75], a[88:91], v[136:139], v[72:75]\n"
"ds_read_b128 a[116:119], v227 offset:5120\n"
"v_mfma_f32_16x16x32_bf16 v[36:39], a[92:95], v[104:107], v[36:39]\n"
"v_mfma_f32_16x16x32_bf16 v[76:79], a[92:95], v[136:139], v[76:79]\n"
"s_waitcnt lgkmcnt(3)\n"
"ds_read_b128 a[120:123], v227 offset:6144\n"
"v_mfma_f32_16x16x32_bf16 v[84:87], a[96:99], v[144:147], 0\n"
"buffer_load_dwordx4 v225, s[8:11], s44 offen offset:3072 sc1 lds\n"
"v_mfma_f32_16x16x32_bf16 v[164:167], a[96:99], v[176:179], 0\n"
"ds_read_b128 a[124:127], v227 offset:7168\n"
"v_mfma_f32_16x16x32_bf16 v[92:95], a[100:103], v[144:147], 0\n"
"v_mfma_f32_16x16x32_bf16 v[172:175], a[100:103], v[176:179], 0\n"
"ds_read_b128 a[64:67], v227 offset:8192\n"
"v_mfma_f32_16x16x32_bf16 v[100:103], a[104:107], v[144:147], 0\n"
"v_mfma_f32_16x16x32_bf16 v[180:183], a[104:107], v[176:179], 0\n"
"s_waitcnt lgkmcnt(3)\n"
"ds_read_b128 a[68:71], v227 offset:9216\n"
"v_mfma_f32_16x16x32_bf16 v[108:111], a[108:111], v[144:147], 0\n"
"v_mfma_f32_16x16x32_bf16 v[188:191], a[108:111], v[176:179], 0\n"
"ds_read_b128 a[72:75], v227 offset:10240\n"
"v_mfma_f32_16x16x32_bf16 v[116:119], a[112:115], v[144:147], 0\n"
"v_mfma_f32_16x16x32_bf16 v[196:199], a[112:115], v[176:179], 0\n"
"s_add_u32 s48, s59, 32768\n"
"ds_read_b128 a[76:79], v227 offset:11264\n"
"v_mfma_f32_16x16x32_bf16 v[124:127], a[116:119], v[144:147], 0\n"
"s_and_b32 s48, s48, 0x18000\n"
"v_mfma_f32_16x16x32_bf16 v[204:207], a[116:119], v[176:179], 0\n"
"s_waitcnt lgkmcnt(3)\n"
"ds_read_b128 a[80:83], v227 offset:12288\n"
"s_andn2_b32 s49, s59, 0x18000\n"
"v_mfma_f32_16x16x32_bf16 v[132:135], a[120:123], v[144:147], 0\n"
"s_or_b32 s52, s48, s49\n"
"s_waitcnt vmcnt(8)\n"
"v_cmp_eq_u64 vcc, s[28:29], v[240:241]\n"
"s_cmp_eq_u64 vcc, exec\n"
"s_cbranch_scc0 L1052_%=\n"
"L1053_%=:\n"
"v_mfma_f32_16x16x32_bf16 v[208:211], a[120:123], v[176:179], 0\n"
"s_add_u32 s53, s52, 16384\n"
"ds_read_b128 a[84:87], v227 offset:13312\n"
"v_mfma_f32_16x16x32_bf16 v[140:143], a[124:127], v[144:147], 0\n"
"buffer_load_dwordx4 a[0:3], v224, s[8:11], s52 offen offset:0 sc1\n"
"v_mfma_f32_16x16x32_bf16 v[212:215], a[124:127], v[176:179], 0\n"
"ds_read_b128 a[88:91], v227 offset:14336\n"
"v_mfma_f32_16x16x32_bf16 v[148:151], a[64:67], v[144:147], 0\n"
"v_mfma_f32_16x16x32_bf16 v[216:219], a[64:67], v[176:179], 0\n"
"s_waitcnt lgkmcnt(3)\n"
"ds_read_b128 a[92:95], v227 offset:15360\n"
"v_mfma_f32_16x16x32_bf16 v[156:159], a[68:71], v[144:147], 0\n"
"v_mfma_f32_16x16x32_bf16 v[220:223], a[68:71], v[176:179], 0\n"
"ds_read_b128 a[96:99], v227 offset:16384\n"
"v_mfma_f32_16x16x32_bf16 v[84:87], a[72:75], v[152:155], v[84:87]\n"
"buffer_load_dwordx4 a[4:7], v224, s[8:11], s53 offen offset:0 sc1\n"
"v_mfma_f32_16x16x32_bf16 v[164:167], a[72:75], v[184:187], v[164:167]\n"
"ds_read_b128 a[100:103], v227 offset:17408\n"
"v_mfma_f32_16x16x32_bf16 v[92:95], a[76:79], v[152:155], v[92:95]\n"
"v_mfma_f32_16x16x32_bf16 v[172:175], a[76:79], v[184:187], v[172:175]\n"
"s_waitcnt lgkmcnt(3)\n"
"ds_read_b128 a[104:107], v227 offset:18432\n"
"v_mfma_f32_16x16x32_bf16 v[100:103], a[80:83], v[152:155], v[100:103]\n"
"buffer_load_dwordx4 a[8:11], v224, s[8:11], s52 offen offset:1024 sc1\n"
"v_mfma_f32_16x16x32_bf16 v[180:183], a[80:83], v[184:187], v[180:183]\n"
"ds_read_b128 a[108:111], v227 offset:19456\n"
"v_mfma_f32_16x16x32_bf16 v[108:111], a[84:87], v[152:155], v[108:111]\n"
"v_mfma_f32_16x16x32_bf16 v[188:191], a[84:87], v[184:187], v[188:191]\n"
"ds_read_b128 a[112:115], v227 offset:20480\n"
"v_mfma_f32_16x16x32_bf16 v[116:119], a[88:91], v[152:155], v[116:119]\n"
"v_mfma_f32_16x16x32_bf16 v[196:199], a[88:91], v[184:187], v[196:199]\n"
"s_waitcnt lgkmcnt(3)\n"
"ds_read_b128 a[116:119], v227 offset:21504\n"
"v_mfma_f32_16x16x32_bf16 v[124:127], a[92:95], v[152:155], v[124:127]\n"
"buffer_load_dwordx4 a[12:15], v224, s[8:11], s53 offen offset:1024 sc1\n"
"v_mfma_f32_16x16x32_bf16 v[204:207], a[92:95], v[184:187], v[204:207]\n"
"buffer_store_dwordx2 v[232:233], v230, s[8:11], s62 offen sc1\n"
"v_mfma_f32_16x16x32_bf16 v[132:135], a[96:99], v[152:155], v[132:135]\n"
"ds_read_b128 a[120:123], v227 offset:22528\n"
"v_mfma_f32_16x16x32_bf16 v[208:211], a[96:99], v[184:187], v[208:211]\n"
"ds_read_b128 a[124:127], v227 offset:23552\n"
"v_mfma_f32_16x16x32_bf16 v[140:143], a[100:103], v[152:155], v[140:143]\n"
"buffer_load_dwordx4 a[16:19], v224, s[8:11], s52 offen offset:2048 sc1\n"
"v_mfma_f32_16x16x32_bf16 v[212:215], a[100:103], v[184:187], v[212:215]\n"
"s_waitcnt lgkmcnt(3)\n"
"ds_read_b128 a[64:67], v227 offset:24576\n"
"v_mfma_f32_16x16x32_bf16 v[148:151], a[104:107], v[152:155], v[148:151]\n"
"v_mfma_f32_16x16x32_bf16 v[216:219], a[104:107], v[184:187], v[216:219]\n"
"ds_read_b128 a[68:71], v227 offset:25600\n"
"v_mfma_f32_16x16x32_bf16 v[156:159], a[108:111], v[152:155], v[156:159]\n"
"v_mfma_f32_16x16x32_bf16 v[220:223], a[108:111], v[184:187], v[220:223]\n"
"ds_read_b128 a[72:75], v227 offset:26624\n"
"v_mfma_f32_16x16x32_bf16 v[84:87], a[112:115], v[160:163], v[84:87]\n"
"s_lshr_b32 s89, s31, 3\n"
"buffer_load_dwordx4 a[20:23], v224, s[8:11], s53 offen offset:2048 sc1\n"
"v_mfma_f32_16x16x32_bf16 v[164:167], a[112:115], v[192:195], v[164:167]\n"
"s_waitcnt lgkmcnt(3)\n"
"ds_read_b128 a[76:79], v227 offset:27648\n"
"s_lshl_b32 s89, s89, 4\n"
"v_mfma_f32_16x16x32_bf16 v[92:95], a[116:119], v[160:163], v[92:95]\n"
"s_add_u32 s89, s89, s30\n"
"v_mfma_f32_16x16x32_bf16 v[172:175], a[116:119], v[192:195], v[172:175]\n"
"s_lshl_b32 s89, s89, 3\n"
"ds_read_b128 a[80:83], v227 offset:28672\n"
"v_mfma_f32_16x16x32_bf16 v[100:103], a[120:123], v[160:163], v[100:103]\n"
"s_add_u32 s89, s89, s32\n"
"buffer_load_dwordx4 a[24:27], v224, s[8:11], s52 offen offset:3072 sc1\n"
"v_mfma_f32_16x16x32_bf16 v[180:183], a[120:123], v[192:195], v[180:183]\n"
"s_mul_i32 s88, s89, 5120\n"
"ds_read_b128 a[84:87], v227 offset:29696\n"
"v_mfma_f32_16x16x32_bf16 v[108:111], a[124:127], v[160:163], v[108:111]\n"
"s_add_u32 s88, s88, 60817408\n"
"v_mfma_f32_16x16x32_bf16 v[188:191], a[124:127], v[192:195], v[188:191]\n"
"s_waitcnt lgkmcnt(3)\n"
"ds_read_b128 a[88:91], v227 offset:30720\n"
"s_lshl_b32 s89, s89, 3\n"
"v_mfma_f32_16x16x32_bf16 v[116:119], a[64:67], v[160:163], v[116:119]\n"
"s_add_u32 s89, s89, 23296\n"
"v_mfma_f32_16x16x32_bf16 v[196:199], a[64:67], v[192:195], v[196:199]\n"
"s_add_u32 s48, s59, 65536\n"
"ds_read_b128 a[92:95], v227 offset:31744\n"
"v_lshlrev_b32 v239, 4, v224\n"
"v_mfma_f32_16x16x32_bf16 v[124:127], a[68:71], v[160:163], v[124:127]\n"
"s_and_b32 s48, s48, 0x18000\n"
"buffer_load_dwordx4 a[28:31], v224, s[8:11], s53 offen offset:3072 sc1\n"
"v_mfma_f32_16x16x32_bf16 v[204:207], a[68:71], v[192:195], v[204:207]\n"
"s_andn2_b32 s49, s59, 0x18000\n"
"ds_read_b128 a[96:99], v226 offset:0\n"
"v_and_b32 v242, 0x3e0, v224\n"
"v_mfma_f32_16x16x32_bf16 v[132:135], a[72:75], v[160:163], v[132:135]\n"
"s_or_b32 s52, s48, s49\n"
"v_lshl_add_u32 v239, v242, 22, v239\n"
"s_add_u32 s53, s52, 16384\n"
"v_mfma_f32_16x16x32_bf16 v[208:211], a[72:75], v[192:195], v[208:211]\n"
"s_waitcnt lgkmcnt(3)\n"
"ds_read_b128 a[100:103], v226 offset:1024\n"
"v_mfma_f32_16x16x32_bf16 v[140:143], a[76:79], v[160:163], v[140:143]\n"
"buffer_load_dwordx4 a[32:35], v224, s[8:11], s52 offen offset:0 sc1\n"
"v_mfma_f32_16x16x32_bf16 v[212:215], a[76:79], v[192:195], v[212:215]\n"
"ds_read_b128 a[104:107], v226 offset:2048\n"
"v_mfma_f32_16x16x32_bf16 v[148:151], a[80:83], v[160:163], v[148:151]\n"
"v_mfma_f32_16x16x32_bf16 v[216:219], a[80:83], v[192:195], v[216:219]\n"
"ds_read_b128 a[108:111], v226 offset:3072\n"
"v_mfma_f32_16x16x32_bf16 v[156:159], a[84:87], v[160:163], v[156:159]\n"
"v_mfma_f32_16x16x32_bf16 v[220:223], a[84:87], v[192:195], v[220:223]\n"
"s_waitcnt lgkmcnt(3)\n"
"ds_read_b128 a[112:115], v226 offset:4096\n"
"v_mfma_f32_16x16x32_bf16 v[84:87], a[88:91], v[168:171], v[84:87]\n"
"buffer_load_dwordx4 a[36:39], v224, s[8:11], s53 offen offset:0 sc1\n"
"v_mfma_f32_16x16x32_bf16 v[164:167], a[88:91], v[200:203], v[164:167]\n"
"ds_read_b128 a[116:119], v226 offset:5120\n"
"v_mfma_f32_16x16x32_bf16 v[92:95], a[92:95], v[168:171], v[92:95]\n"
"v_mfma_f32_16x16x32_bf16 v[172:175], a[92:95], v[200:203], v[172:175]\n"
"ds_read_b128 a[120:123], v226 offset:6144\n"
"v_mfma_f32_16x16x32_bf16 v[100:103], a[96:99], v[168:171], v[100:103]\n"
"buffer_load_dwordx4 a[40:43], v224, s[8:11], s52 offen offset:1024 sc1\n"
"v_mfma_f32_16x16x32_bf16 v[180:183], a[96:99], v[200:203], v[180:183]\n"
"s_waitcnt lgkmcnt(3)\n"
"ds_read_b128 a[124:127], v226 offset:7168\n"
"v_mfma_f32_16x16x32_bf16 v[108:111], a[100:103], v[168:171], v[108:111]\n"
"v_mfma_f32_16x16x32_bf16 v[188:191], a[100:103], v[200:203], v[188:191]\n"
"ds_read_b128 a[64:67], v226 offset:8192\n"
"v_mfma_f32_16x16x32_bf16 v[116:119], a[104:107], v[168:171], v[116:119]\n"
"v_mfma_f32_16x16x32_bf16 v[196:199], a[104:107], v[200:203], v[196:199]\n"
"ds_read_b128 a[68:71], v226 offset:9216\n"
"v_mfma_f32_16x16x32_bf16 v[124:127], a[108:111], v[168:171], v[124:127]\n"
"buffer_load_dwordx4 a[44:47], v224, s[8:11], s53 offen offset:1024 sc1\n"
"v_mfma_f32_16x16x32_bf16 v[204:207], a[108:111], v[200:203], v[204:207]\n"
"s_waitcnt lgkmcnt(3)\n"
"ds_read_b128 a[72:75], v226 offset:10240\n"
"v_mfma_f32_16x16x32_bf16 v[132:135], a[112:115], v[168:171], v[132:135]\n"
"v_mfma_f32_16x16x32_bf16 v[208:211], a[112:115], v[200:203], v[208:211]\n"
"ds_read_b128 a[76:79], v226 offset:11264\n"
"v_mfma_f32_16x16x32_bf16 v[140:143], a[116:119], v[168:171], v[140:143]\n"
"buffer_load_dwordx4 a[48:51], v224, s[8:11], s52 offen offset:2048 sc1\n"
"v_mfma_f32_16x16x32_bf16 v[212:215], a[116:119], v[200:203], v[212:215]\n"
"ds_read_b128 a[80:83], v226 offset:12288\n"
"v_mfma_f32_16x16x32_bf16 v[148:151], a[120:123], v[168:171], v[148:151]\n"
"v_mfma_f32_16x16x32_bf16 v[216:219], a[120:123], v[200:203], v[216:219]\n"
"s_waitcnt lgkmcnt(3)\n"
"ds_read_b128 a[84:87], v226 offset:13312\n"
"v_mfma_f32_16x16x32_bf16 v[156:159], a[124:127], v[168:171], v[156:159]\n"
"v_mfma_f32_16x16x32_bf16 v[220:223], a[124:127], v[200:203], v[220:223]\n"
"s_waitcnt vmcnt(13)\n"
"ds_read_b128 a[88:91], v226 offset:14336\n"
"v_mfma_f32_16x16x32_bf16 v[0:3], a[64:67], a[0:3], v[0:3]\n"
"s_waitcnt vmcnt(12)\n"
"buffer_load_dwordx4 a[52:55], v224, s[8:11], s53 offen offset:2048 sc1\n"
"v_mfma_f32_16x16x32_bf16 v[40:43], a[64:67], a[4:7], v[40:43]\n"
"ds_read_b128 a[92:95], v226 offset:15360\n"
"v_mfma_f32_16x16x32_bf16 v[4:7], a[68:71], a[0:3], v[4:7]\n"
"v_mfma_f32_16x16x32_bf16 v[44:47], a[68:71], a[4:7], v[44:47]\n"
"s_waitcnt lgkmcnt(3)\n"
"ds_read_b128 a[96:99], v226 offset:16384\n"
"v_mfma_f32_16x16x32_bf16 v[8:11], a[72:75], a[0:3], v[8:11]\n"
"buffer_load_dwordx4 a[56:59], v224, s[8:11], s52 offen offset:3072 sc1\n"
"v_mfma_f32_16x16x32_bf16 v[48:51], a[72:75], a[4:7], v[48:51]\n"
"ds_read_b128 a[100:103], v226 offset:17408\n"
"v_mfma_f32_16x16x32_bf16 v[12:15], a[76:79], a[0:3], v[12:15]\n"
"v_mfma_f32_16x16x32_bf16 v[52:55], a[76:79], a[4:7], v[52:55]\n"
"ds_read_b128 a[104:107], v226 offset:18432\n"
"v_mfma_f32_16x16x32_bf16 v[16:19], a[80:83], a[0:3], v[16:19]\n"
"v_mfma_f32_16x16x32_bf16 v[56:59], a[80:83], a[4:7], v[56:59]\n"
"s_waitcnt lgkmcnt(3)\n"
"ds_read_b128 a[108:111], v226 offset:19456\n"
"s_add_u32 s48, s59, 98304\n"
"v_mfma_f32_16x16x32_bf16 v[20:23], a[84:87], a[0:3], v[20:23]\n"
"s_and_b32 s48, s48, 0x18000\n"
"buffer_load_dwordx4 a[60:63], v224, s[8:11], s53 offen offset:3072 sc1\n"
"v_mfma_f32_16x16x32_bf16 v[60:63], a[84:87], a[4:7], v[60:63]\n"
"s_andn2_b32 s49, s59, 0x18000\n"
"ds_read_b128 a[112:115], v226 offset:20480\n"
"v_mfma_f32_16x16x32_bf16 v[24:27], a[88:91], a[0:3], v[24:27]\n"
"s_or_b32 s52, s48, s49\n"
"s_add_u32 s53, s52, 16384\n"
"v_mfma_f32_16x16x32_bf16 v[64:67], a[88:91], a[4:7], v[64:67]\n"
"ds_read_b128 a[116:119], v226 offset:21504\n"
"v_mfma_f32_16x16x32_bf16 v[28:31], a[92:95], a[0:3], v[28:31]\n"
"buffer_load_dwordx4 a[180:183], v224, s[8:11], s52 offen offset:0 sc1\n"
"v_mfma_f32_16x16x32_bf16 v[68:71], a[92:95], a[4:7], v[68:71]\n"
"s_waitcnt lgkmcnt(3)\n"
"ds_read_b128 a[120:123], v226 offset:22528\n"
"v_mfma_f32_16x16x32_bf16 v[32:35], a[96:99], a[0:3], v[32:35]\n"
"v_mfma_f32_16x16x32_bf16 v[72:75], a[96:99], a[4:7], v[72:75]\n"
"ds_read_b128 a[124:127], v226 offset:23552\n"
"v_mfma_f32_16x16x32_bf16 v[36:39], a[100:103], a[0:3], v[36:39]\n"
"v_mfma_f32_16x16x32_bf16 v[76:79], a[100:103], a[4:7], v[76:79]\n"
"s_waitcnt vmcnt(15)\n"
"ds_read_b128 a[64:67], v226 offset:24576\n"
"v_mfma_f32_16x16x32_bf16 v[0:3], a[104:107], a[8:11], v[0:3]\n"
"s_waitcnt vmcnt(14)\n"
"buffer_load_dwordx4 a[184:187], v224, s[8:11], s53 offen offset:0 sc1\n"
"v_mfma_f32_16x16x32_bf16 v[40:43], a[104:107], a[12:15], v[40:43]\n"
"s_waitcnt lgkmcnt(3)\n"
"ds_read_b128 a[68:71], v226 offset:25600\n"
"v_mfma_f32_16x16x32_bf16 v[4:7], a[108:111], a[8:11], v[4:7]\n"
"v_mfma_f32_16x16x32_bf16 v[44:47], a[108:111], a[12:15], v[44:47]\n"
"ds_read_b128 a[72:75], v226 offset:26624\n"
"v_mfma_f32_16x16x32_bf16 v[8:11], a[112:115], a[8:11], v[8:11]\n"
"buffer_load_dwordx4 a[188:191], v224, s[8:11], s52 offen offset:1024 sc1\n"
"v_mfma_f32_16x16x32_bf16 v[48:51], a[112:115], a[12:15], v[48:51]\n"
"ds_read_b128 a[76:79], v226 offset:27648\n"
"v_mfma_f32_16x16x32_bf16 v[12:15], a[116:119], a[8:11], v[12:15]\n"
"v_mfma_f32_16x16x32_bf16 v[52:55], a[116:119], a[12:15], v[52:55]\n"
"s_waitcnt lgkmcnt(3)\n"
"ds_read_b128 a[80:83], v226 offset:28672\n"
"v_mfma_f32_16x16x32_bf16 v[16:19], a[120:123], a[8:11], v[16:19]\n"
"v_mfma_f32_16x16x32_bf16 v[56:59], a[120:123], a[12:15], v[56:59]\n"
"ds_read_b128 a[84:87], v226 offset:29696\n"
"v_mfma_f32_16x16x32_bf16 v[20:23], a[124:127], a[8:11], v[20:23]\n"
"buffer_load_dwordx4 a[192:195], v224, s[8:11], s53 offen offset:1024 sc1\n"
"v_mfma_f32_16x16x32_bf16 v[60:63], a[124:127], a[12:15], v[60:63]\n"
"ds_read_b128 a[88:91], v226 offset:30720\n"
"v_mfma_f32_16x16x32_bf16 v[24:27], a[64:67], a[8:11], v[24:27]\n"
"v_mfma_f32_16x16x32_bf16 v[64:67], a[64:67], a[12:15], v[64:67]\n"
"s_waitcnt lgkmcnt(3)\n"
"ds_read_b128 a[92:95], v226 offset:31744\n"
"v_mfma_f32_16x16x32_bf16 v[28:31], a[68:71], a[8:11], v[28:31]\n"
"s_barrier\n"
"v_mfma_f32_16x16x32_bf16 v[68:71], a[68:71], a[12:15], v[68:71]\n"
"buffer_load_dwordx4 a[196:199], v224, s[8:11], s52 offen offset:2048 sc1\n"
"v_mfma_f32_16x16x32_bf16 v[32:35], a[72:75], a[8:11], v[32:35]\n"
"ds_read_b128 a[96:99], v235 offset:0\n"
"v_mfma_f32_16x16x32_bf16 v[72:75], a[72:75], a[12:15], v[72:75]\n"
"ds_read_b128 a[100:103], v235 offset:1024\n"
"v_mfma_f32_16x16x32_bf16 v[36:39], a[76:79], a[8:11], v[36:39]\n"
"s_andn2_b32 s63, s62, 31\n"
"v_mfma_f32_16x16x32_bf16 v[76:79], a[76:79], a[12:15], v[76:79]\n"
"s_waitcnt vmcnt(16) lgkmcnt(3)\n"
"ds_read_b128 a[104:107], v235 offset:2048\n"
"v_mfma_f32_16x16x32_bf16 v[0:3], a[80:83], a[16:19], v[0:3]\n"
"s_waitcnt vmcnt(15)\n"
"buffer_load_dwordx4 a[200:203], v224, s[8:11], s53 offen offset:2048 sc1\n"
"v_mfma_f32_16x16x32_bf16 v[40:43], a[80:83], a[20:23], v[40:43]\n"
"buffer_load_dwordx2 v[240:241], v238, s[8:11], s63 offen sc1\n"
"v_mfma_f32_16x16x32_bf16 v[4:7], a[84:87], a[16:19], v[4:7]\n"
"ds_read_b128 a[108:111], v235 offset:3072\n"
"v_mfma_f32_16x16x32_bf16 v[44:47], a[84:87], a[20:23], v[44:47]\n"
"ds_read_b128 a[112:115], v235 offset:4096\n"
"v_mfma_f32_16x16x32_bf16 v[8:11], a[88:91], a[16:19], v[8:11]\n"
"buffer_load_dwordx4 a[204:207], v224, s[8:11], s52 offen offset:3072 sc1\n"
"v_mfma_f32_16x16x32_bf16 v[48:51], a[88:91], a[20:23], v[48:51]\n"
"s_waitcnt lgkmcnt(3)\n"
"ds_read_b128 a[116:119], v235 offset:5120\n"
"v_mfma_f32_16x16x32_bf16 v[12:15], a[92:95], a[16:19], v[12:15]\n"
"v_mfma_f32_16x16x32_bf16 v[52:55], a[92:95], a[20:23], v[52:55]\n"
"ds_read_b128 a[120:123], v235 offset:6144\n"
"v_mfma_f32_16x16x32_bf16 v[16:19], a[96:99], a[16:19], v[16:19]\n"
"v_mfma_f32_16x16x32_bf16 v[56:59], a[96:99], a[20:23], v[56:59]\n"
"ds_read_b128 a[124:127], v235 offset:7168\n"
"v_mfma_f32_16x16x32_bf16 v[20:23], a[100:103], a[16:19], v[20:23]\n"
"buffer_load_dwordx4 a[208:211], v224, s[8:11], s53 offen offset:3072 sc1\n"
"v_mfma_f32_16x16x32_bf16 v[60:63], a[100:103], a[20:23], v[60:63]\n"
"s_waitcnt lgkmcnt(3)\n"
"ds_read_b128 a[64:67], v235 offset:8192\n"
"v_mfma_f32_16x16x32_bf16 v[24:27], a[104:107], a[16:19], v[24:27]\n"
"v_mfma_f32_16x16x32_bf16 v[64:67], a[104:107], a[20:23], v[64:67]\n"
"ds_read_b128 a[68:71], v235 offset:9216\n"
"v_mfma_f32_16x16x32_bf16 v[28:31], a[108:111], a[16:19], v[28:31]\n"
"v_mfma_f32_16x16x32_bf16 v[68:71], a[108:111], a[20:23], v[68:71]\n"
"ds_read_b128 a[72:75], v235 offset:10240\n"
"v_mfma_f32_16x16x32_bf16 v[32:35], a[112:115], a[16:19], v[32:35]\n"
"v_mfma_f32_16x16x32_bf16 v[72:75], a[112:115], a[20:23], v[72:75]\n"
"s_waitcnt lgkmcnt(3)\n"
"ds_read_b128 a[76:79], v235 offset:11264\n"
"v_mfma_f32_16x16x32_bf16 v[36:39], a[116:119], a[16:19], v[36:39]\n"
"v_mfma_f32_16x16x32_bf16 v[76:79], a[116:119], a[20:23], v[76:79]\n"
"s_waitcnt vmcnt(18)\n"
"ds_read_b128 a[80:83], v235 offset:12288\n"
"v_mfma_f32_16x16x32_bf16 v[0:3], a[120:123], a[24:27], v[0:3]\n"
"s_waitcnt vmcnt(17)\n"
"v_mfma_f32_16x16x32_bf16 v[40:43], a[120:123], a[28:31], v[40:43]\n"
"ds_read_b128 a[84:87], v235 offset:13312\n"
"v_mfma_f32_16x16x32_bf16 v[4:7], a[124:127], a[24:27], v[4:7]\n"
"v_mfma_f32_16x16x32_bf16 v[44:47], a[124:127], a[28:31], v[44:47]\n"
"s_waitcnt lgkmcnt(3)\n"
"ds_read_b128 a[88:91], v235 offset:14336\n"
"v_mfma_f32_16x16x32_bf16 v[8:11], a[64:67], a[24:27], v[8:11]\n"
"v_mfma_f32_16x16x32_bf16 v[48:51], a[64:67], a[28:31], v[48:51]\n"
"ds_read_b128 a[92:95], v235 offset:15360\n"
"v_mfma_f32_16x16x32_bf16 v[12:15], a[68:71], a[24:27], v[12:15]\n"
"v_mfma_f32_16x16x32_bf16 v[52:55], a[68:71], a[28:31], v[52:55]\n"
"ds_read_b128 a[96:99], v235 offset:16384\n"
"v_mfma_f32_16x16x32_bf16 v[16:19], a[72:75], a[24:27], v[16:19]\n"
"v_mfma_f32_16x16x32_bf16 v[56:59], a[72:75], a[28:31], v[56:59]\n"
"s_waitcnt lgkmcnt(3)\n"
"ds_read_b128 a[100:103], v235 offset:17408\n"
"v_mfma_f32_16x16x32_bf16 v[20:23], a[76:79], a[24:27], v[20:23]\n"
"v_mfma_f32_16x16x32_bf16 v[60:63], a[76:79], a[28:31], v[60:63]\n"
"ds_read_b128 a[104:107], v235 offset:26624\n"
"v_mfma_f32_16x16x32_bf16 v[24:27], a[80:83], a[24:27], v[24:27]\n"
"v_mfma_f32_16x16x32_bf16 v[64:67], a[80:83], a[28:31], v[64:67]\n"
"ds_read_b128 a[108:111], v235 offset:27648\n"
"v_mfma_f32_16x16x32_bf16 v[28:31], a[84:87], a[24:27], v[28:31]\n"
"v_mfma_f32_16x16x32_bf16 v[68:71], a[84:87], a[28:31], v[68:71]\n"
"s_waitcnt lgkmcnt(3)\n"
"ds_read_b128 a[112:115], v236 offset:4096\n"
"v_mfma_f32_16x16x32_bf16 v[32:35], a[88:91], a[24:27], v[32:35]\n"
"v_mfma_f32_16x16x32_bf16 v[72:75], a[88:91], a[28:31], v[72:75]\n"
"ds_read_b128 a[116:119], v236 offset:5120\n"
"v_mfma_f32_16x16x32_bf16 v[36:39], a[92:95], a[24:27], v[36:39]\n"
"v_mfma_f32_16x16x32_bf16 v[76:79], a[92:95], a[28:31], v[76:79]\n"
"s_waitcnt vmcnt(16)\n"
"ds_read_b128 a[120:123], v236 offset:14336\n"
"v_mfma_f32_16x16x32_bf16 v[0:3], a[96:99], a[32:35], v[0:3]\n"
"s_waitcnt vmcnt(15)\n"
"v_mfma_f32_16x16x32_bf16 v[40:43], a[96:99], a[36:39], v[40:43]\n"
"s_waitcnt lgkmcnt(3)\n"
"ds_read_b128 a[124:127], v236 offset:15360\n"
"v_mfma_f32_16x16x32_bf16 v[4:7], a[100:103], a[32:35], v[4:7]\n"
"v_mfma_f32_16x16x32_bf16 v[44:47], a[100:103], a[36:39], v[44:47]\n"
"s_waitcnt vmcnt(14)\n"
"ds_read_b128 a[64:67], v236 offset:24576\n"
"v_mfma_f32_16x16x32_bf16 v[0:3], a[104:107], a[40:43], v[0:3]\n"
"s_waitcnt vmcnt(13)\n"
"v_mfma_f32_16x16x32_bf16 v[40:43], a[104:107], a[44:47], v[40:43]\n"
"ds_read_b128 a[68:71], v236 offset:25600\n"
"v_mfma_f32_16x16x32_bf16 v[4:7], a[108:111], a[40:43], v[4:7]\n"
"v_mfma_f32_16x16x32_bf16 v[44:47], a[108:111], a[44:47], v[44:47]\n"
"s_waitcnt vmcnt(12) lgkmcnt(3)\n"
"ds_read_b128 a[72:75], v237 offset:2048\n"
"s_add_u32 s48, s60, 32768\n"
"v_mfma_f32_16x16x32_bf16 v[0:3], a[112:115], a[48:51], v[0:3]\n"
"s_waitcnt vmcnt(11)\n"
"s_and_b32 s48, s48, 0x18000\n"
"v_mfma_f32_16x16x32_bf16 v[40:43], a[112:115], a[52:55], v[40:43]\n"
"s_andn2_b32 s49, s60, 0x18000\n"
"ds_read_b128 a[76:79], v237 offset:3072\n"
"v_mfma_f32_16x16x32_bf16 v[4:7], a[116:119], a[48:51], v[4:7]\n"
"s_or_b32 s52, s48, s49\n"
"s_waitcnt vmcnt(2)\n"
"v_cmp_eq_u64 vcc, s[28:29], v[240:241]\n"
"s_cmp_eq_u64 vcc, exec\n"
"s_cbranch_scc0 L1055_%=\n"
"L1056_%=:\n"
"v_mfma_f32_16x16x32_bf16 v[44:47], a[116:119], a[52:55], v[44:47]\n"
"s_add_u32 s53, s52, 16384\n"
"ds_read_b128 a[80:83], v237 offset:12288\n"
"v_mfma_f32_16x16x32_bf16 v[0:3], a[120:123], a[56:59], v[0:3]\n"
"buffer_load_dwordx4 a[212:215], v224, s[8:11], s52 offen offset:0 sc1\n"
"v_mfma_f32_16x16x32_bf16 v[40:43], a[120:123], a[60:63], v[40:43]\n"
"s_waitcnt lgkmcnt(3)\n"
"ds_read_b128 a[84:87], v237 offset:13312\n"
"v_mfma_f32_16x16x32_bf16 v[4:7], a[124:127], a[56:59], v[4:7]\n"
"v_mfma_f32_16x16x32_bf16 v[44:47], a[124:127], a[60:63], v[44:47]\n"
"ds_read_b128 a[88:91], v237 offset:22528\n"
"v_mfma_f32_16x16x32_bf16 v[0:3], a[64:67], a[180:183], v[0:3]\n"
"v_mfma_f32_16x16x32_bf16 v[40:43], a[64:67], a[184:187], v[40:43]\n"
"ds_read_b128 a[92:95], v237 offset:23552\n"
"v_mfma_f32_16x16x32_bf16 v[4:7], a[68:71], a[180:183], v[4:7]\n"
"buffer_load_dwordx4 a[216:219], v224, s[8:11], s53 offen offset:0 sc1\n"
"v_mfma_f32_16x16x32_bf16 v[44:47], a[68:71], a[184:187], v[44:47]\n"
"s_waitcnt lgkmcnt(3)\n"
"ds_read_b128 a[96:99], v235 offset:18432\n"
"v_mfma_f32_16x16x32_bf16 v[0:3], a[72:75], a[188:191], v[0:3]\n"
"v_mfma_f32_16x16x32_bf16 v[40:43], a[72:75], a[192:195], v[40:43]\n"
"ds_read_b128 a[100:103], v235 offset:19456\n"
"v_mfma_f32_16x16x32_bf16 v[4:7], a[76:79], a[188:191], v[4:7]\n"
"buffer_load_dwordx4 a[220:223], v224, s[8:11], s52 offen offset:1024 sc1\n"
"v_mfma_f32_16x16x32_bf16 v[44:47], a[76:79], a[192:195], v[44:47]\n"
"ds_read_b128 a[104:107], v235 offset:28672\n"
"v_bfe_u32 v243, v224, 6, 2\n"
"v_mfma_f32_16x16x32_bf16 v[0:3], a[80:83], a[196:199], v[0:3]\n"
"v_bfe_u32 v244, v224, 4, 2\n"
"v_lshrrev_b32 v245, 8, v224\n"
"v_mfma_f32_16x16x32_bf16 v[40:43], a[80:83], a[200:203], v[40:43]\n"
"s_waitcnt lgkmcnt(3)\n"
"ds_read_b128 a[108:111], v235 offset:29696\n"
"v_lshlrev_b32 v245, 3, v245\n"
"v_mfma_f32_16x16x32_bf16 v[4:7], a[84:87], a[196:199], v[4:7]\n"
"v_sub_u32 v244, v244, v245\n"
"v_mfma_f32_16x16x32_bf16 v[44:47], a[84:87], a[200:203], v[44:47]\n"
"s_waitcnt vmcnt(4)\n"
"ds_read_b128 a[112:115], v236 offset:6144\n"
"v_and_b32 v245, 16, v224\n"
"v_mfma_f32_16x16x32_bf16 v[0:3], a[88:91], a[204:207], v[0:3]\n"
"s_waitcnt vmcnt(3)\n"
"buffer_load_dwordx4 a[224:227], v224, s[8:11], s53 offen offset:1024 sc1\n"
"v_mfma_f32_16x16x32_bf16 v[40:43], a[88:91], a[208:211], v[40:43]\n"
"ds_read_b128 a[116:119], v236 offset:7168\n"
"v_mov_b32 v246, 0x3f80\n"
"v_mfma_f32_16x16x32_bf16 v[4:7], a[92:95], a[204:207], v[4:7]\n"
"v_lshlrev_b32 v245, v245, v246\n"
"v_lshlrev_b32 v246, 4, v243\n"
"v_mfma_f32_16x16x32_bf16 v[44:47], a[92:95], a[208:211], v[44:47]\n"
"s_waitcnt lgkmcnt(3)\n"
"ds_read_b128 a[120:123], v236 offset:16384\n"
"v_and_b32 v246, 16, v246\n"
"v_mfma_f32_16x16x32_bf16 v[8:11], a[96:99], a[32:35], v[8:11]\n"
"buffer_load_dwordx4 a[228:231], v224, s[8:11], s52 offen offset:2048 sc1\n"
"v_mfma_f32_16x16x32_bf16 v[48:51], a[96:99], a[36:39], v[48:51]\n"
"ds_read_b128 a[124:127], v236 offset:17408\n"
"v_and_b32 v247, 2, v243\n"
"v_mfma_f32_16x16x32_bf16 v[12:15], a[100:103], a[32:35], v[12:15]\n"
"s_add_u32 s90, s88, 0\n"
"v_lshl_add_u32 v246, v247, 1, v246\n"
"v_cvt_pk_bf16_f32 v0, v0, v1\n"
"v_mfma_f32_16x16x32_bf16 v[52:55], a[100:103], a[36:39], v[52:55]\n"
"ds_read_b128 a[64:67], v236 offset:26624\n"
"v_cvt_pk_bf16_f32 v1, v2, v3\n"
"v_mfma_f32_16x16x32_bf16 v[8:11], a[104:107], a[40:43], v[8:11]\n"
"v_add3_u32 v246, v246, v244, 0\n"
"v_cvt_pk_bf16_f32 v2, v4, v5\n"
"v_mfma_f32_16x16x32_bf16 v[48:51], a[104:107], a[44:47], v[48:51]\n"
"s_waitcnt lgkmcnt(3)\n"
"ds_read_b128 a[68:71], v236 offset:27648\n"
"v_cvt_pk_bf16_f32 v3, v6, v7\n"
"v_mfma_f32_16x16x32_bf16 v[12:15], a[108:111], a[40:43], v[12:15]\n"
"buffer_load_dwordx4 a[232:235], v224, s[8:11], s53 offen offset:2048 sc1\n"
"v_ashrrev_i32 v246, 1, v246\n"
"v_mfma_f32_16x16x32_bf16 v[52:55], a[108:111], a[44:47], v[52:55]\n"
"buffer_store_dwordx4 v[0:3], v224, s[8:11], s90 offen sc1\n"
"v_cmp_eq_u32_e64 vcc, 0, v246\n"
"v_mfma_f32_16x16x32_bf16 v[8:11], a[112:115], a[48:51], v[8:11]\n"
"ds_read_b128 a[72:75], v237 offset:4096\n"
"v_cndmask_b32_e64 v80, 0, v245, vcc\n"
"v_mfma_f32_16x16x32_bf16 v[48:51], a[112:115], a[52:55], v[48:51]\n"
"s_add_u32 s91, s88, 163840\n"
"ds_read_b128 a[76:79], v237 offset:5120\n"
"v_cmp_eq_u32_e64 s[92:93], 1, v246\n"
"v_mfma_f32_16x16x32_bf16 v[12:15], a[116:119], a[48:51], v[12:15]\n"
"buffer_load_dwordx4 a[236:239], v224, s[8:11], s52 offen offset:3072 sc1\n"
"v_cndmask_b32_e64 v81, 0, v245, s[92:93]\n"
"v_mfma_f32_16x16x32_bf16 v[52:55], a[116:119], a[52:55], v[52:55]\n"
"s_waitcnt lgkmcnt(3)\n"
"ds_read_b128 a[80:83], v237 offset:14336\n"
"v_cmp_eq_u32_e64 s[94:95], 2, v246\n"
"v_mfma_f32_16x16x32_bf16 v[8:11], a[120:123], a[56:59], v[8:11]\n"
"v_cvt_pk_bf16_f32 v40, v40, v41\n"
"v_cvt_pk_bf16_f32 v41, v42, v43\n"
"v_cndmask_b32_e64 v82, 0, v245, s[94:95]\n"
"v_mfma_f32_16x16x32_bf16 v[48:51], a[120:123], a[60:63], v[48:51]\n"
"ds_read_b128 a[84:87], v237 offset:15360\n"
"v_cvt_pk_bf16_f32 v42, v44, v45\n"
"v_cvt_pk_bf16_f32 v43, v46, v47\n"
"v_mfma_f32_16x16x32_bf16 v[12:15], a[124:127], a[56:59], v[12:15]\n"
"buffer_store_dwordx4 v[40:43], v224, s[8:11], s91 offen sc1\n"
"v_cmp_eq_u32_e64 vcc, 3, v246\n"
"v_mfma_f32_16x16x32_bf16 v[52:55], a[124:127], a[60:63], v[52:55]\n"
"s_add_u32 s48, s60, 65536\n"
"ds_read_b128 a[88:91], v237 offset:24576\n"
"v_cndmask_b32_e64 v83, 0, v245, vcc\n"
"v_lshlrev_b32 v246, 4, v243\n"
"v_mfma_f32_16x16x32_bf16 v[8:11], a[64:67], a[180:183], v[8:11]\n"
"s_and_b32 s48, s48, 0x18000\n"
"buffer_load_dwordx4 a[240:243], v224, s[8:11], s53 offen offset:3072 sc1\n"
"v_and_b32 v246, 16, v246\n"
"v_mfma_f32_16x16x32_bf16 v[48:51], a[64:67], a[184:187], v[48:51]\n"
"s_waitcnt lgkmcnt(3)\n"
"ds_read_b128 a[92:95], v237 offset:25600\n"
"v_and_b32 v247, 2, v243\n"
"v_mfma_f32_16x16x32_bf16 v[12:15], a[68:71], a[180:183], v[12:15]\n"
"s_andn2_b32 s49, s60, 0x18000\n"
"v_lshl_add_u32 v246, v247, 1, v246\n"
"v_add3_u32 v246, v246, v244, 8\n"
"v_mfma_f32_16x16x32_bf16 v[52:55], a[68:71], a[184:187], v[52:55]\n"
"s_or_b32 s52, s48, s49\n"
"ds_read_b128 a[96:99], v235 offset:20480\n"
"v_ashrrev_i32 v246, 1, v246\n"
"v_mfma_f32_16x16x32_bf16 v[8:11], a[72:75], a[188:191], v[8:11]\n"
"s_add_u32 s53, s52, 16384\n"
"buffer_load_dwordx4 a[244:247], v224, s[8:11], s52 offen offset:0 sc1\n"
"v_mfma_f32_16x16x32_bf16 v[48:51], a[72:75], a[192:195], v[48:51]\n"
"ds_read_b128 a[100:103], v235 offset:21504\n"
"v_cmp_eq_u32_e64 vcc, 0, v246\n"
"v_mfma_f32_16x16x32_bf16 v[12:15], a[76:79], a[188:191], v[12:15]\n"
"v_cmp_eq_u32_e64 s[92:93], 1, v246\n"
"v_cndmask_b32_e64 v88, 0, v245, vcc\n"
"v_mfma_f32_16x16x32_bf16 v[52:55], a[76:79], a[192:195], v[52:55]\n"
"s_waitcnt lgkmcnt(3)\n"
"ds_read_b128 a[104:107], v235 offset:30720\n"
"v_cndmask_b32_e64 v89, 0, v245, s[92:93]\n"
"v_mfma_f32_16x16x32_bf16 v[8:11], a[80:83], a[196:199], v[8:11]\n"
"v_cmp_eq_u32_e64 s[94:95], 2, v246\n"
"v_cmp_eq_u32_e64 vcc, 3, v246\n"
"v_mfma_f32_16x16x32_bf16 v[48:51], a[80:83], a[200:203], v[48:51]\n"
"ds_read_b128 a[108:111], v235 offset:31744\n"
"v_cndmask_b32_e64 v90, 0, v245, s[94:95]\n"
"v_mfma_f32_16x16x32_bf16 v[12:15], a[84:87], a[196:199], v[12:15]\n"
"buffer_load_dwordx4 a[248:251], v224, s[8:11], s53 offen offset:0 sc1\n"
"v_mfma_f32_16x16x32_bf16 v[52:55], a[84:87], a[200:203], v[52:55]\n"
"ds_read_b128 a[112:115], v236 offset:8192\n"
"v_cndmask_b32_e64 v91, 0, v245, vcc\n"
"v_mfma_f32_16x16x32_bf16 v[8:11], a[88:91], a[204:207], v[8:11]\n"
"v_lshl_add_u32 v246, v243, 3, v244\n"
"v_add_u32 v246, 0, v246\n"
"v_mfma_f32_16x16x32_bf16 v[48:51], a[88:91], a[208:211], v[48:51]\n"
"s_waitcnt lgkmcnt(3)\n"
"ds_read_b128 a[116:119], v236 offset:9216\n"
"v_ashrrev_i32 v246, 1, v246\n"
"v_mfma_f32_16x16x32_bf16 v[12:15], a[92:95], a[204:207], v[12:15]\n"
"buffer_load_dwordx4 a[252:255], v224, s[8:11], s52 offen offset:1024 sc1\n"
"v_mfma_f32_16x16x32_bf16 v[52:55], a[92:95], a[208:211], v[52:55]\n"
"ds_read_b128 a[120:123], v236 offset:18432\n"
"v_cmp_eq_u32_e64 vcc, 0, v246\n"
"v_mfma_f32_16x16x32_bf16 v[16:19], a[96:99], a[32:35], v[16:19]\n"
"v_cmp_eq_u32_e64 s[92:93], 1, v246\n"
"v_cndmask_b32_e64 v96, 0, v245, vcc\n"
"v_mfma_f32_16x16x32_bf16 v[56:59], a[96:99], a[36:39], v[56:59]\n"
"ds_read_b128 a[124:127], v236 offset:19456\n"
"v_cndmask_b32_e64 v97, 0, v245, s[92:93]\n"
"v_mfma_f32_16x16x32_bf16 v[20:23], a[100:103], a[32:35], v[20:23]\n"
"s_add_u32 s90, s88, 1024\n"
"v_cmp_eq_u32_e64 s[94:95], 2, v246\n"
"v_cvt_pk_bf16_f32 v8, v8, v9\n"
"v_mfma_f32_16x16x32_bf16 v[60:63], a[100:103], a[36:39], v[60:63]\n"
"s_waitcnt lgkmcnt(3)\n"
"ds_read_b128 a[64:67], v236 offset:28672\n"
"v_cvt_pk_bf16_f32 v9, v10, v11\n"
"v_mfma_f32_16x16x32_bf16 v[16:19], a[104:107], a[40:43], v[16:19]\n"
"buffer_load_dwordx4 a[128:131], v224, s[8:11], s53 offen offset:1024 sc1\n"
"v_mfma_f32_16x16x32_bf16 v[56:59], a[104:107], a[44:47], v[56:59]\n"
"ds_read_b128 a[68:71], v236 offset:29696\n"
"v_cndmask_b32_e64 v98, 0, v245, s[94:95]\n"
"v_mfma_f32_16x16x32_bf16 v[20:23], a[108:111], a[40:43], v[20:23]\n"
"v_cvt_pk_bf16_f32 v10, v12, v13\n"
"v_cvt_pk_bf16_f32 v11, v14, v15\n"
"v_cmp_eq_u32_e64 vcc, 3, v246\n"
"v_mfma_f32_16x16x32_bf16 v[60:63], a[108:111], a[44:47], v[60:63]\n"
"buffer_store_dwordx4 v[8:11], v224, s[8:11], s90 offen sc1\n"
"v_cndmask_b32_e64 v99, 0, v245, vcc\n"
"v_mfma_f32_16x16x32_bf16 v[16:19], a[112:115], a[48:51], v[16:19]\n"
"ds_read_b128 a[72:75], v237 offset:6144\n"
"v_lshl_add_u32 v246, v243, 3, v244\n"
"v_mfma_f32_16x16x32_bf16 v[56:59], a[112:115], a[52:55], v[56:59]\n"
"s_waitcnt lgkmcnt(3)\n"
"buffer_load_dwordx4 a[132:135], v224, s[8:11], s52 offen offset:2048 sc1\n"
"v_add_u32 v246, 4, v246\n"
"v_mfma_f32_16x16x32_bf16 v[20:23], a[116:119], a[48:51], v[20:23]\n"
"s_add_u32 s91, s88, 164864\n"
"ds_read_b128 a[76:79], v237 offset:7168\n"
"v_ashrrev_i32 v246, 1, v246\n"
"v_mfma_f32_16x16x32_bf16 v[60:63], a[116:119], a[52:55], v[60:63]\n"
"ds_read_b128 a[80:83], v237 offset:16384\n"
"v_cmp_eq_u32_e64 vcc, 0, v246\n"
"v_mfma_f32_16x16x32_bf16 v[16:19], a[120:123], a[56:59], v[16:19]\n"
"v_cvt_pk_bf16_f32 v48, v48, v49\n"
"v_cvt_pk_bf16_f32 v49, v50, v51\n"
"v_cndmask_b32_e64 v104, 0, v245, vcc\n"
"v_mfma_f32_16x16x32_bf16 v[56:59], a[120:123], a[60:63], v[56:59]\n"
"ds_read_b128 a[84:87], v237 offset:17408\n"
"v_cvt_pk_bf16_f32 v50, v52, v53\n"
"v_cvt_pk_bf16_f32 v51, v54, v55\n"
"v_mfma_f32_16x16x32_bf16 v[20:23], a[124:127], a[56:59], v[20:23]\n"
"buffer_store_dwordx4 v[48:51], v224, s[8:11], s91 offen sc1\n"
"v_cmp_eq_u32_e64 s[92:93], 1, v246\n"
"v_mfma_f32_16x16x32_bf16 v[60:63], a[124:127], a[60:63], v[60:63]\n"
"s_waitcnt lgkmcnt(3)\n"
"buffer_load_dwordx4 a[136:139], v224, s[8:11], s53 offen offset:2048 sc1\n"
"v_cndmask_b32_e64 v105, 0, v245, s[92:93]\n"
"v_mfma_f32_16x16x32_bf16 v[16:19], a[64:67], a[180:183], v[16:19]\n"
"s_mov_b32 s12, s64\n"
"ds_read_b128 a[88:91], v237 offset:26624\n"
"v_cmp_eq_u32_e64 s[94:95], 2, v246\n"
"v_mfma_f32_16x16x32_bf16 v[56:59], a[64:67], a[184:187], v[56:59]\n"
"s_and_b32 s13, s65, 0xffff\n"
"ds_read_b128 a[92:95], v237 offset:27648\n"
"v_cndmask_b32_e64 v106, 0, v245, s[94:95]\n"
"v_cmp_eq_u32_e64 vcc, 3, v246\n"
"v_mfma_f32_16x16x32_bf16 v[20:23], a[68:71], a[180:183], v[20:23]\n"
"s_mov_b32 s14, 2621440\n"
"buffer_load_dwordx4 a[140:143], v224, s[8:11], s52 offen offset:3072 sc1\n"
"v_cndmask_b32_e64 v107, 0, v245, vcc\n"
"v_mfma_f32_16x16x32_bf16 v[60:63], a[68:71], a[184:187], v[60:63]\n"
"s_mov_b32 s15, 0x20000\n"
"ds_read_b128 a[96:99], v235 offset:22528\n"
"v_mfma_f32_16x16x32_bf16 v[16:19], a[72:75], a[188:191], v[16:19]\n"
"s_mov_b32 s16, s86\n"
"v_mfma_f32_16x16x32_bf16 v[56:59], a[72:75], a[192:195], v[56:59]\n"
"s_waitcnt lgkmcnt(3)\n"
"ds_read_b128 a[100:103], v235 offset:23552\n"
"s_and_b32 s17, s87, 0xffff\n"
"v_mfma_f32_16x16x32_bf16 v[20:23], a[76:79], a[188:191], v[20:23]\n"
"s_mov_b32 s18, 2621440\n"
"v_mfma_f32_16x16x32_bf16 v[60:63], a[76:79], a[192:195], v[60:63]\n"
"s_add_u32 s48, s60, 98304\n"
"ds_read_b128 a[104:107], v236 offset:0\n"
"s_mov_b32 s19, 0x20000\n"
"v_mfma_f32_16x16x32_bf16 v[16:19], a[80:83], a[196:199], v[16:19]\n"
"s_and_b32 s48, s48, 0x18000\n"
"buffer_load_dwordx4 a[144:147], v224, s[8:11], s53 offen offset:3072 sc1\n"
"v_mfma_f32_16x16x32_bf16 v[56:59], a[80:83], a[200:203], v[56:59]\n"
"s_andn2_b32 s49, s60, 0x18000\n"
"ds_read_b128 a[108:111], v236 offset:1024\n"
"v_bfe_u32 v243, v224, 4, 4\n"
"v_mfma_f32_16x16x32_bf16 v[20:23], a[84:87], a[196:199], v[20:23]\n"
"s_lshl_b32 s92, s34, 3\n"
"s_or_b32 s52, s48, s49\n"
"s_add_u32 s53, s52, 16384\n"
"v_mfma_f32_16x16x32_bf16 v[60:63], a[84:87], a[200:203], v[60:63]\n"
"s_waitcnt lgkmcnt(3)\n"
"ds_read_b128 a[112:115], v236 offset:10240\n"
"s_add_u32 s93, s92, s32\n"
"v_mfma_f32_16x16x32_bf16 v[16:19], a[88:91], a[204:207], v[16:19]\n"
"s_sub_u32 s92, s32, s92\n"
"buffer_load_dwordx4 a[148:151], v224, s[8:11], s52 offen offset:0 sc1\n"
"v_mfma_f32_16x16x32_bf16 v[56:59], a[88:91], a[208:211], v[56:59]\n"
"s_add_u32 s92, s92, 120\n"
"ds_read_b128 a[116:119], v236 offset:11264\n"
"v_lshl_add_u32 v244, s93, 4, v243\n"
"v_mfma_f32_16x16x32_bf16 v[20:23], a[92:95], a[204:207], v[20:23]\n"
"v_lshl_add_u32 v245, s92, 4, v243\n"
"v_mfma_f32_16x16x32_bf16 v[60:63], a[92:95], a[208:211], v[60:63]\n"
"s_mul_i32 s93, s93, 20480\n"
"ds_read_b128 a[120:123], v236 offset:20480\n"
"v_mfma_f32_16x16x32_bf16 v[24:27], a[96:99], a[32:35], v[24:27]\n"
"s_mul_i32 s92, s92, 20480\n"
"v_mfma_f32_16x16x32_bf16 v[64:67], a[96:99], a[36:39], v[64:67]\n"
"s_waitcnt lgkmcnt(3)\n"
"ds_read_b128 a[124:127], v236 offset:21504\n"
"v_add_u32 v178, s93, v224\n"
"v_mfma_f32_16x16x32_bf16 v[28:31], a[100:103], a[32:35], v[28:31]\n"
"s_add_u32 s90, s88, 2048\n"
"buffer_load_dwordx4 a[152:155], v224, s[8:11], s53 offen offset:0 sc1\n"
"v_mfma_f32_16x16x32_bf16 v[68:71], a[100:103], a[36:39], v[68:71]\n"
"ds_read_b128 a[64:67], v236 offset:30720\n"
"v_cvt_pk_bf16_f32 v16, v16, v17\n"
"v_mfma_f32_16x16x32_bf16 v[24:27], a[104:107], a[40:43], v[24:27]\n"
"v_cvt_pk_bf16_f32 v17, v18, v19\n"
"v_add_u32 v179, s92, v224\n"
"v_mfma_f32_16x16x32_bf16 v[64:67], a[104:107], a[44:47], v[64:67]\n"
"ds_read_b128 a[68:71], v236 offset:31744\n"
"v_cvt_pk_bf16_f32 v18, v20, v21\n"
"v_mfma_f32_16x16x32_bf16 v[28:31], a[108:111], a[40:43], v[28:31]\n"
"buffer_load_dwordx4 a[156:159], v224, s[8:11], s52 offen offset:1024 sc1\n"
"v_cvt_pk_bf16_f32 v19, v22, v23\n"
"v_mfma_f32_16x16x32_bf16 v[68:71], a[108:111], a[44:47], v[68:71]\n"
"s_waitcnt lgkmcnt(3)\n"
"buffer_store_dwordx4 v[16:19], v224, s[8:11], s90 offen sc1\n"
"v_lshrrev_b32 v243, 4, v224\n"
"v_mfma_f32_16x16x32_bf16 v[24:27], a[112:115], a[48:51], v[24:27]\n"
"ds_read_b128 a[72:75], v237 offset:8192\n"
"v_and_b32 v243, 48, v243\n"
"v_mul_u32_u24 v176, 1280, v244\n"
"v_mfma_f32_16x16x32_bf16 v[64:67], a[112:115], a[52:55], v[64:67]\n"
"s_add_u32 s91, s88, 165888\n"
"ds_read_b128 a[76:79], v237 offset:9216\n"
"v_mul_u32_u24 v177, 1280, v245\n"
"v_mfma_f32_16x16x32_bf16 v[28:31], a[116:119], a[48:51], v[28:31]\n"
"v_add_u32 v176, v176, v243\n"
"v_add_u32 v177, v177, v243\n"
"v_mfma_f32_16x16x32_bf16 v[68:71], a[116:119], a[52:55], v[68:71]\n"
"ds_read_b128 a[80:83], v237 offset:18432\n"
"v_cvt_pk_bf16_f32 v56, v56, v57\n"
"v_mfma_f32_16x16x32_bf16 v[24:27], a[120:123], a[56:59], v[24:27]\n"
"buffer_load_dwordx4 a[160:163], v224, s[8:11], s53 offen offset:1024 sc1\n"
"v_cvt_pk_bf16_f32 v57, v58, v59\n"
"v_mfma_f32_16x16x32_bf16 v[64:67], a[120:123], a[60:63], v[64:67]\n"
"s_waitcnt lgkmcnt(3)\n"
"ds_read_b128 a[84:87], v237 offset:19456\n"
"v_and_b32 v243, 0x300, v224\n"
"v_cvt_pk_bf16_f32 v58, v60, v61\n"
"v_mfma_f32_16x16x32_bf16 v[28:31], a[124:127], a[56:59], v[28:31]\n"
"v_cvt_pk_bf16_f32 v59, v62, v63\n"
"v_add_u32 v243, 2657280, v243\n"
"v_sub_u32 v244, 2047, v244\n"
"v_mfma_f32_16x16x32_bf16 v[68:71], a[124:127], a[60:63], v[68:71]\n"
"buffer_store_dwordx4 v[56:59], v224, s[8:11], s91 offen sc1\n"
"v_sub_u32 v245, 2047, v245\n"
"v_mfma_f32_16x16x32_bf16 v[24:27], a[64:67], a[180:183], v[24:27]\n"
"ds_read_b128 a[88:91], v237 offset:28672\n"
"v_readlane_b32 s92, v234, 0\n"
"v_mfma_f32_16x16x32_bf16 v[64:67], a[64:67], a[184:187], v[64:67]\n"
"s_and_b32 s92, s92, 0xffff\n"
"buffer_load_dwordx4 a[164:167], v224, s[8:11], s52 offen offset:2048 sc1\n"
"v_mfma_f32_16x16x32_bf16 v[28:31], a[68:71], a[180:183], v[28:31]\n"
"ds_read_b128 a[92:95], v237 offset:29696\n"
"v_cmp_eq_u32 vcc, s92, v244\n"
"v_mfma_f32_16x16x32_bf16 v[68:71], a[68:71], a[184:187], v[68:71]\n"
"s_waitcnt lgkmcnt(3)\n"
"ds_read_b128 a[96:99], v235 offset:24576\n"
"v_cmp_eq_u32_e64 s[94:95], s92, v245\n"
"v_mfma_f32_16x16x32_bf16 v[24:27], a[72:75], a[188:191], v[24:27]\n"
"v_cndmask_b32 v178, v178, v243, vcc\n"
"v_cndmask_b32_e64 v179, v179, v243, s[94:95]\n"
"v_mfma_f32_16x16x32_bf16 v[64:67], a[72:75], a[192:195], v[64:67]\n"
"ds_read_b128 a[100:103], v235 offset:25600\n"
"v_add_u32 v243, 16, v243\n"
"v_mfma_f32_16x16x32_bf16 v[28:31], a[76:79], a[188:191], v[28:31]\n"
"buffer_load_dwordx4 a[168:171], v224, s[8:11], s53 offen offset:2048 sc1\n"
"v_mfma_f32_16x16x32_bf16 v[68:71], a[76:79], a[192:195], v[68:71]\n"
"ds_read_b128 a[104:107], v236 offset:2048\n"
"v_readlane_b32 s92, v234, 1\n"
"v_mfma_f32_16x16x32_bf16 v[24:27], a[80:83], a[196:199], v[24:27]\n"
"s_and_b32 s92, s92, 0xffff\n"
"v_cmp_eq_u32 vcc, s92, v244\n"
"v_cmp_eq_u32_e64 s[94:95], s92, v245\n"
"v_mfma_f32_16x16x32_bf16 v[64:67], a[80:83], a[200:203], v[64:67]\n"
"s_waitcnt lgkmcnt(3)\n"
"ds_read_b128 a[108:111], v236 offset:3072\n"
"v_cndmask_b32 v178, v178, v243, vcc\n"
"v_mfma_f32_16x16x32_bf16 v[28:31], a[84:87], a[196:199], v[28:31]\n"
"buffer_load_dwordx4 a[172:175], v224, s[8:11], s52 offen offset:3072 sc1\n"
"v_mfma_f32_16x16x32_bf16 v[68:71], a[84:87], a[200:203], v[68:71]\n"
"ds_read_b128 a[112:115], v236 offset:12288\n"
"v_cndmask_b32_e64 v179, v179, v243, s[94:95]\n"
"v_mfma_f32_16x16x32_bf16 v[24:27], a[88:91], a[204:207], v[24:27]\n"
"v_add_u32 v243, 16, v243\n"
"v_readlane_b32 s92, v234, 2\n"
"v_mfma_f32_16x16x32_bf16 v[64:67], a[88:91], a[208:211], v[64:67]\n"
"s_and_b32 s92, s92, 0xffff\n"
"ds_read_b128 a[116:119], v236 offset:13312\n"
"v_cmp_eq_u32 vcc, s92, v244\n"
"v_mfma_f32_16x16x32_bf16 v[28:31], a[92:95], a[204:207], v[28:31]\n"
"v_cmp_eq_u32_e64 s[94:95], s92, v245\n"
"v_mfma_f32_16x16x32_bf16 v[68:71], a[92:95], a[208:211], v[68:71]\n"
"s_waitcnt lgkmcnt(3)\n"
"ds_read_b128 a[120:123], v236 offset:22528\n"
"v_cndmask_b32 v178, v178, v243, vcc\n"
"v_mfma_f32_16x16x32_bf16 v[32:35], a[96:99], a[32:35], v[32:35]\n"
"buffer_load_dwordx4 a[176:179], v224, s[8:11], s53 offen offset:3072 sc1\n"
"v_mfma_f32_16x16x32_bf16 v[72:75], a[96:99], a[36:39], v[72:75]\n"
"ds_read_b128 a[124:127], v236 offset:23552\n"
"v_cndmask_b32_e64 v179, v179, v243, s[94:95]\n"
"v_mfma_f32_16x16x32_bf16 v[36:39], a[100:103], a[32:35], v[36:39]\n"
"s_add_u32 s90, s88, 3072\n"
"v_add_u32 v243, 16, v243\n"
"v_cvt_pk_bf16_f32 v24, v24, v25\n"
"v_mfma_f32_16x16x32_bf16 v[76:79], a[100:103], a[36:39], v[76:79]\n"
"ds_read_b128 a[64:67], v237 offset:0\n"
"v_cvt_pk_bf16_f32 v25, v26, v27\n"
"v_mfma_f32_16x16x32_bf16 v[32:35], a[104:107], a[40:43], v[32:35]\n"
"v_readlane_b32 s92, v234, 3\n"
"v_cvt_pk_bf16_f32 v26, v28, v29\n"
"v_mfma_f32_16x16x32_bf16 v[72:75], a[104:107], a[44:47], v[72:75]\n"
"s_waitcnt lgkmcnt(3)\n"
"ds_read_b128 a[68:71], v237 offset:1024\n"
"v_cvt_pk_bf16_f32 v27, v30, v31\n"
"v_mfma_f32_16x16x32_bf16 v[36:39], a[108:111], a[40:43], v[36:39]\n"
"s_and_b32 s92, s92, 0xffff\n"
"buffer_store_dwordx4 v[24:27], v224, s[8:11], s90 offen sc1\n"
"v_mfma_f32_16x16x32_bf16 v[76:79], a[108:111], a[44:47], v[76:79]\n"
"ds_read_b128 a[72:75], v237 offset:10240\n"
"v_cmp_eq_u32 vcc, s92, v244\n"
"v_mfma_f32_16x16x32_bf16 v[32:35], a[112:115], a[48:51], v[32:35]\n"
"v_cmp_eq_u32_e64 s[94:95], s92, v245\n"
"v_cndmask_b32 v178, v178, v243, vcc\n"
"v_mfma_f32_16x16x32_bf16 v[72:75], a[112:115], a[52:55], v[72:75]\n"
"s_add_u32 s91, s88, 166912\n"
"ds_read_b128 a[76:79], v237 offset:11264\n"
"v_cndmask_b32_e64 v179, v179, v243, s[94:95]\n"
"v_mfma_f32_16x16x32_bf16 v[36:39], a[116:119], a[48:51], v[36:39]\n"
"v_add_u32 v243, 16, v243\n"
"v_cvt_pk_bf16_f32 v64, v64, v65\n"
"v_mfma_f32_16x16x32_bf16 v[76:79], a[116:119], a[52:55], v[76:79]\n"
"s_waitcnt lgkmcnt(3)\n"
"ds_read_b128 a[80:83], v237 offset:20480\n"
"v_cvt_pk_bf16_f32 v65, v66, v67\n"
"v_mfma_f32_16x16x32_bf16 v[32:35], a[120:123], a[56:59], v[32:35]\n"
"v_readlane_b32 s92, v234, 4\n"
"v_cvt_pk_bf16_f32 v66, v68, v69\n"
"v_mfma_f32_16x16x32_bf16 v[72:75], a[120:123], a[60:63], v[72:75]\n"
"s_and_b32 s92, s92, 0xffff\n"
"ds_read_b128 a[84:87], v237 offset:21504\n"
"v_cvt_pk_bf16_f32 v67, v70, v71\n"
"v_mfma_f32_16x16x32_bf16 v[36:39], a[124:127], a[56:59], v[36:39]\n"
"buffer_store_dwordx4 v[64:67], v224, s[8:11], s91 offen sc1\n"
"v_cmp_eq_u32 vcc, s92, v244\n"
"v_mfma_f32_16x16x32_bf16 v[76:79], a[124:127], a[60:63], v[76:79]\n"
"ds_read_b128 a[88:91], v237 offset:30720\n"
"v_cmp_eq_u32_e64 s[94:95], s92, v245\n"
"v_mfma_f32_16x16x32_bf16 v[32:35], a[64:67], a[180:183], v[32:35]\n"
"v_cndmask_b32 v178, v178, v243, vcc\n"
"v_cndmask_b32_e64 v179, v179, v243, s[94:95]\n"
"v_mfma_f32_16x16x32_bf16 v[72:75], a[64:67], a[184:187], v[72:75]\n"
"s_waitcnt lgkmcnt(3)\n"
"ds_read_b128 a[92:95], v237 offset:31744\n"
"v_add_u32 v243, 16, v243\n"
"v_mfma_f32_16x16x32_bf16 v[36:39], a[68:71], a[180:183], v[36:39]\n"
"v_readlane_b32 s92, v234, 5\n"
"s_and_b32 s92, s92, 0xffff\n"
"v_mfma_f32_16x16x32_bf16 v[76:79], a[68:71], a[184:187], v[76:79]\n"
"ds_read_b128 a[96:99], v226 offset:8192\n"
"v_cmp_eq_u32 vcc, s92, v244\n"
"v_mfma_f32_16x16x32_bf16 v[32:35], a[72:75], a[188:191], v[32:35]\n"
"v_cmp_eq_u32_e64 s[94:95], s92, v245\n"
"v_cndmask_b32 v178, v178, v243, vcc\n"
"v_mfma_f32_16x16x32_bf16 v[72:75], a[72:75], a[192:195], v[72:75]\n"
"ds_read_b128 a[100:103], v226 offset:9216\n"
"v_cndmask_b32_e64 v179, v179, v243, s[94:95]\n"
"v_mfma_f32_16x16x32_bf16 v[36:39], a[76:79], a[188:191], v[36:39]\n"
"v_add_u32 v243, 16, v243\n"
"v_mfma_f32_16x16x32_bf16 v[76:79], a[76:79], a[192:195], v[76:79]\n"
"s_waitcnt lgkmcnt(3)\n"
"ds_read_b128 a[104:107], v226 offset:18432\n"
"v_readlane_b32 s92, v234, 6\n"
"v_mfma_f32_16x16x32_bf16 v[32:35], a[80:83], a[196:199], v[32:35]\n"
"s_and_b32 s92, s92, 0xffff\n"
"v_mfma_f32_16x16x32_bf16 v[72:75], a[80:83], a[200:203], v[72:75]\n"
"ds_read_b128 a[108:111], v226 offset:19456\n"
"v_cmp_eq_u32 vcc, s92, v244\n"
"v_mfma_f32_16x16x32_bf16 v[36:39], a[84:87], a[196:199], v[36:39]\n"
"v_cmp_eq_u32_e64 s[94:95], s92, v245\n"
"v_mfma_f32_16x16x32_bf16 v[76:79], a[84:87], a[200:203], v[76:79]\n"
"ds_read_b128 a[112:115], v226 offset:28672\n"
"v_cndmask_b32 v178, v178, v243, vcc\n"
"v_mfma_f32_16x16x32_bf16 v[32:35], a[88:91], a[204:207], v[32:35]\n"
"v_cndmask_b32_e64 v179, v179, v243, s[94:95]\n"
"v_mfma_f32_16x16x32_bf16 v[72:75], a[88:91], a[208:211], v[72:75]\n"
"s_waitcnt lgkmcnt(3)\n"
"ds_read_b128 a[116:119], v226 offset:29696\n"
"v_add_u32 v243, 16, v243\n"
"v_mfma_f32_16x16x32_bf16 v[36:39], a[92:95], a[204:207], v[36:39]\n"
"v_readlane_b32 s92, v234, 7\n"
"v_mfma_f32_16x16x32_bf16 v[76:79], a[92:95], a[208:211], v[76:79]\n"
"s_waitcnt vmcnt(31)\n"
"ds_read_b128 a[120:123], v235 offset:6144\n"
"s_and_b32 s92, s92, 0xffff\n"
"v_mfma_f32_16x16x32_bf16 v[84:87], a[96:99], a[212:215], v[84:87]\n"
"s_waitcnt vmcnt(30)\n"
"v_cmp_eq_u32 vcc, s92, v244\n"
"v_mfma_f32_16x16x32_bf16 v[164:167], a[96:99], a[216:219], v[164:167]\n"
"ds_read_b128 a[124:127], v235 offset:7168\n"
"v_cmp_eq_u32_e64 s[94:95], s92, v245\n"
"v_mfma_f32_16x16x32_bf16 v[92:95], a[100:103], a[212:215], v[92:95]\n"
"s_add_u32 s90, s88, 4096\n"
"v_cvt_pk_bf16_f32 v32, v32, v33\n"
"v_cvt_pk_bf16_f32 v33, v34, v35\n"
"v_mfma_f32_16x16x32_bf16 v[172:175], a[100:103], a[216:219], v[172:175]\n"
"s_waitcnt vmcnt(29) lgkmcnt(3)\n"
"ds_read_b128 a[64:67], v235 offset:16384\n"
"v_cndmask_b32 v178, v178, v243, vcc\n"
"v_mfma_f32_16x16x32_bf16 v[84:87], a[104:107], a[220:223], v[84:87]\n"
"s_waitcnt vmcnt(28)\n"
"v_cvt_pk_bf16_f32 v34, v36, v37\n"
"v_cvt_pk_bf16_f32 v35, v38, v39\n"
"v_mfma_f32_16x16x32_bf16 v[164:167], a[104:107], a[224:227], v[164:167]\n"
"s_mul_i32 s92, s33, 5\n"
"ds_read_b128 a[68:71], v235 offset:17408\n"
"v_cndmask_b32_e64 v179, v179, v243, s[94:95]\n"
"v_mfma_f32_16x16x32_bf16 v[92:95], a[108:111], a[220:223], v[92:95]\n"
"s_add_u32 s93, s92, s30\n"
"buffer_store_dwordx4 v[32:35], v224, s[8:11], s90 offen sc1\n"
"v_mfma_f32_16x16x32_bf16 v[172:175], a[108:111], a[224:227], v[172:175]\n"
"s_waitcnt vmcnt(28)\n"
"ds_read_b128 a[72:75], v235 offset:26624\n"
"s_add_u32 s92, s92, 4\n"
"v_mfma_f32_16x16x32_bf16 v[84:87], a[112:115], a[228:231], v[84:87]\n"
"s_waitcnt vmcnt(27)\n"
"s_lshl_b32 s38, s93, 6\n"
"s_lshl_b32 s55, s92, 6\n"
"v_mfma_f32_16x16x32_bf16 v[164:167], a[112:115], a[232:235], v[164:167]\n"
"s_waitcnt lgkmcnt(3)\n"
"ds_read_b128 a[76:79], v235 offset:27648\n"
"v_cvt_pk_bf16_f32 v72, v72, v73\n"
"v_mfma_f32_16x16x32_bf16 v[92:95], a[116:119], a[228:231], v[92:95]\n"
"s_add_u32 s91, s88, 167936\n"
"v_cvt_pk_bf16_f32 v73, v74, v75\n"
"v_cvt_pk_bf16_f32 v74, v76, v77\n"
"v_mfma_f32_16x16x32_bf16 v[172:175], a[116:119], a[232:235], v[172:175]\n"
"s_waitcnt vmcnt(25)\n"
"ds_read_b128 a[80:83], v236 offset:4096\n"
"v_cvt_pk_bf16_f32 v75, v78, v79\n"
"v_mfma_f32_16x16x32_bf16 v[84:87], a[120:123], a[236:239], v[84:87]\n"
"s_waitcnt vmcnt(23)\n"
"buffer_store_dwordx4 v[72:75], v224, s[8:11], s91 offen sc1\n"
"v_mfma_f32_16x16x32_bf16 v[164:167], a[120:123], a[240:243], v[164:167]\n"
"s_add_u32 s55, s55, s45\n"
"ds_read_b128 a[84:87], v236 offset:5120\n"
"s_lshl_b32 s36, s93, 10\n"
"v_mfma_f32_16x16x32_bf16 v[92:95], a[124:127], a[236:239], v[92:95]\n"
"s_lshl_b32 s37, s92, 10\n"
"s_add_u32 s36, s36, 65536\n"
"s_add_u32 s37, s37, 65536\n"
"v_mfma_f32_16x16x32_bf16 v[172:175], a[124:127], a[240:243], v[172:175]\n"
"s_waitcnt vmcnt(23) lgkmcnt(3)\n"
"ds_read_b128 a[88:91], v236 offset:14336\n"
"v_lshrrev_b32 v243, 1, v224\n"
"v_mfma_f32_16x16x32_bf16 v[84:87], a[64:67], a[244:247], v[84:87]\n"
"s_waitcnt vmcnt(22)\n"
"v_min_u32 v244, 312, v243\n"
"s_add_u32 s37, s37, s45\n"
"v_mfma_f32_16x16x32_bf16 v[164:167], a[64:67], a[248:251], v[164:167]\n"
"ds_read_b128 a[92:95], v236 offset:15360\n"
"v_and_b32 v243, 56, v243\n"
"v_mfma_f32_16x16x32_bf16 v[92:95], a[68:71], a[244:247], v[92:95]\n"
"s_mov_b32 s92, 6400\n"
"v_mfma_f32_16x16x32_bf16 v[172:175], a[68:71], a[248:251], v[172:175]\n"
"s_waitcnt vmcnt(21)\n"
"ds_read_b128 a[96:99], v236 offset:24576\n"
"v_mfma_f32_16x16x32_bf16 v[84:87], a[72:75], a[252:255], v[84:87]\n"
"s_waitcnt vmcnt(20)\n"
"v_mfma_f32_16x16x32_bf16 v[164:167], a[72:75], a[128:131], v[164:167]\n"
"s_waitcnt lgkmcnt(3)\n"
"ds_read_b128 a[100:103], v236 offset:25600\n"
"v_mfma_f32_16x16x32_bf16 v[92:95], a[76:79], a[252:255], v[92:95]\n"
"v_mfma_f32_16x16x32_bf16 v[172:175], a[76:79], a[128:131], v[172:175]\n"
"s_waitcnt vmcnt(18)\n"
"ds_read_b128 a[104:107], v237 offset:2048\n"
"v_mfma_f32_16x16x32_bf16 v[84:87], a[80:83], a[132:135], v[84:87]\n"
"s_waitcnt vmcnt(16)\n"
"v_mfma_f32_16x16x32_bf16 v[164:167], a[80:83], a[136:139], v[164:167]\n"
"ds_read_b128 a[108:111], v237 offset:3072\n"
"v_mfma_f32_16x16x32_bf16 v[92:95], a[84:87], a[132:135], v[92:95]\n"
"v_mfma_f32_16x16x32_bf16 v[172:175], a[84:87], a[136:139], v[172:175]\n"
"s_waitcnt vmcnt(15) lgkmcnt(3)\n"
"ds_read_b128 a[112:115], v237 offset:12288\n"
"v_mfma_f32_16x16x32_bf16 v[84:87], a[88:91], a[140:143], v[84:87]\n"
"s_waitcnt vmcnt(14)\n"
"v_mfma_f32_16x16x32_bf16 v[164:167], a[88:91], a[144:147], v[164:167]\n"
"ds_read_b128 a[116:119], v237 offset:13312\n"
"v_mfma_f32_16x16x32_bf16 v[92:95], a[92:95], a[140:143], v[92:95]\n"
"v_mfma_f32_16x16x32_bf16 v[172:175], a[92:95], a[144:147], v[172:175]\n"
"s_waitcnt vmcnt(13)\n"
"ds_read_b128 a[120:123], v237 offset:22528\n"
"v_mfma_f32_16x16x32_bf16 v[84:87], a[96:99], a[148:151], v[84:87]\n"
"s_waitcnt vmcnt(12)\n"
"v_mfma_f32_16x16x32_bf16 v[164:167], a[96:99], a[152:155], v[164:167]\n"
"s_waitcnt lgkmcnt(3)\n"
"ds_read_b128 a[124:127], v237 offset:23552\n"
"v_mfma_f32_16x16x32_bf16 v[92:95], a[100:103], a[148:151], v[92:95]\n"
"v_mfma_f32_16x16x32_bf16 v[172:175], a[100:103], a[152:155], v[172:175]\n"
"s_waitcnt vmcnt(11)\n"
"ds_read_b128 a[64:67], v226 offset:10240\n"
"v_mfma_f32_16x16x32_bf16 v[84:87], a[104:107], a[156:159], v[84:87]\n"
"s_waitcnt vmcnt(9)\n"
"v_mfma_f32_16x16x32_bf16 v[164:167], a[104:107], a[160:163], v[164:167]\n"
"ds_read_b128 a[68:71], v226 offset:11264\n"
"v_mfma_f32_16x16x32_bf16 v[92:95], a[108:111], a[156:159], v[92:95]\n"
"v_mfma_f32_16x16x32_bf16 v[172:175], a[108:111], a[160:163], v[172:175]\n"
"s_waitcnt vmcnt(7) lgkmcnt(3)\n"
"ds_read_b128 a[72:75], v226 offset:20480\n"
"v_mfma_f32_16x16x32_bf16 v[84:87], a[112:115], a[164:167], v[84:87]\n"
"s_waitcnt vmcnt(6)\n"
"buffer_load_dwordx2 v[240:241], v244, s[8:11], s92 offen sc1\n"
"v_mfma_f32_16x16x32_bf16 v[164:167], a[112:115], a[168:171], v[164:167]\n"
"ds_read_b128 a[76:79], v226 offset:21504\n"
"v_mfma_f32_16x16x32_bf16 v[92:95], a[116:119], a[164:167], v[92:95]\n"
"v_mfma_f32_16x16x32_bf16 v[172:175], a[116:119], a[168:171], v[172:175]\n"
"s_waitcnt vmcnt(6)\n"
"ds_read_b128 a[80:83], v226 offset:30720\n"
"v_mfma_f32_16x16x32_bf16 v[84:87], a[120:123], a[172:175], v[84:87]\n"
"s_waitcnt vmcnt(5)\n"
"v_mfma_f32_16x16x32_bf16 v[164:167], a[120:123], a[176:179], v[164:167]\n"
"s_waitcnt lgkmcnt(3)\n"
"ds_read_b128 a[84:87], v226 offset:31744\n"
"v_mfma_f32_16x16x32_bf16 v[92:95], a[124:127], a[172:175], v[92:95]\n"
"v_mfma_f32_16x16x32_bf16 v[172:175], a[124:127], a[176:179], v[172:175]\n"
"ds_read_b128 a[88:91], v235 offset:8192\n"
"v_mfma_f32_16x16x32_bf16 v[100:103], a[64:67], a[212:215], v[100:103]\n"
"v_mfma_f32_16x16x32_bf16 v[180:183], a[64:67], a[216:219], v[180:183]\n"
"ds_read_b128 a[92:95], v235 offset:9216\n"
"v_mfma_f32_16x16x32_bf16 v[108:111], a[68:71], a[212:215], v[108:111]\n"
"s_add_u32 s90, s88, 327680\n"
"v_cvt_pk_bf16_f32 v84, v84, v85\n"
"v_cvt_pk_bf16_f32 v85, v86, v87\n"
"v_mfma_f32_16x16x32_bf16 v[188:191], a[68:71], a[216:219], v[188:191]\n"
"s_waitcnt lgkmcnt(3)\n"
"ds_read_b128 a[96:99], v235 offset:18432\n"
"v_cvt_pk_bf16_f32 v86, v92, v93\n"
"v_mfma_f32_16x16x32_bf16 v[100:103], a[72:75], a[220:223], v[100:103]\n"
"v_cvt_pk_bf16_f32 v87, v94, v95\n"
"v_mfma_f32_16x16x32_bf16 v[180:183], a[72:75], a[224:227], v[180:183]\n"
"ds_read_b128 a[100:103], v235 offset:19456\n"
"v_mfma_f32_16x16x32_bf16 v[108:111], a[76:79], a[220:223], v[108:111]\n"
"buffer_store_dwordx4 v[84:87], v224, s[8:11], s90 offen sc1\n"
"v_mfma_f32_16x16x32_bf16 v[188:191], a[76:79], a[224:227], v[188:191]\n"
"ds_read_b128 a[104:107], v235 offset:28672\n"
"v_mfma_f32_16x16x32_bf16 v[100:103], a[80:83], a[228:231], v[100:103]\n"
"v_mfma_f32_16x16x32_bf16 v[180:183], a[80:83], a[232:235], v[180:183]\n"
"s_waitcnt lgkmcnt(3)\n"
"ds_read_b128 a[108:111], v235 offset:29696\n"
"v_cvt_pk_bf16_f32 v164, v164, v165\n"
"v_mfma_f32_16x16x32_bf16 v[108:111], a[84:87], a[228:231], v[108:111]\n"
"s_add_u32 s91, s88, 491520\n"
"v_cvt_pk_bf16_f32 v165, v166, v167\n"
"v_cvt_pk_bf16_f32 v166, v172, v173\n"
"v_mfma_f32_16x16x32_bf16 v[188:191], a[84:87], a[232:235], v[188:191]\n"
"ds_read_b128 a[112:115], v236 offset:6144\n"
"v_cvt_pk_bf16_f32 v167, v174, v175\n"
"v_mfma_f32_16x16x32_bf16 v[100:103], a[88:91], a[236:239], v[100:103]\n"
"buffer_store_dwordx4 v[164:167], v224, s[8:11], s91 offen sc1\n"
"v_mfma_f32_16x16x32_bf16 v[180:183], a[88:91], a[240:243], v[180:183]\n"
"ds_read_b128 a[116:119], v236 offset:7168\n"
"v_mfma_f32_16x16x32_bf16 v[108:111], a[92:95], a[236:239], v[108:111]\n"
"v_mfma_f32_16x16x32_bf16 v[188:191], a[92:95], a[240:243], v[188:191]\n"
"s_waitcnt lgkmcnt(3)\n"
"ds_read_b128 a[120:123], v236 offset:16384\n"
"v_mfma_f32_16x16x32_bf16 v[100:103], a[96:99], a[244:247], v[100:103]\n"
"v_mfma_f32_16x16x32_bf16 v[180:183], a[96:99], a[248:251], v[180:183]\n"
"ds_read_b128 a[124:127], v236 offset:17408\n"
"v_mfma_f32_16x16x32_bf16 v[108:111], a[100:103], a[244:247], v[108:111]\n"
"v_mfma_f32_16x16x32_bf16 v[188:191], a[100:103], a[248:251], v[188:191]\n"
"ds_read_b128 a[64:67], v236 offset:26624\n"
"v_mfma_f32_16x16x32_bf16 v[100:103], a[104:107], a[252:255], v[100:103]\n"
"v_mfma_f32_16x16x32_bf16 v[180:183], a[104:107], a[128:131], v[180:183]\n"
"s_waitcnt lgkmcnt(3)\n"
"ds_read_b128 a[68:71], v236 offset:27648\n"
"v_mfma_f32_16x16x32_bf16 v[108:111], a[108:111], a[252:255], v[108:111]\n"
"v_mfma_f32_16x16x32_bf16 v[188:191], a[108:111], a[128:131], v[188:191]\n"
"ds_read_b128 a[72:75], v237 offset:4096\n"
"v_mfma_f32_16x16x32_bf16 v[100:103], a[112:115], a[132:135], v[100:103]\n"
"v_mfma_f32_16x16x32_bf16 v[180:183], a[112:115], a[136:139], v[180:183]\n"
"ds_read_b128 a[76:79], v237 offset:5120\n"
"v_mfma_f32_16x16x32_bf16 v[108:111], a[116:119], a[132:135], v[108:111]\n"
"v_mfma_f32_16x16x32_bf16 v[188:191], a[116:119], a[136:139], v[188:191]\n"
"s_waitcnt lgkmcnt(3)\n"
"ds_read_b128 a[80:83], v237 offset:14336\n"
"v_mfma_f32_16x16x32_bf16 v[100:103], a[120:123], a[140:143], v[100:103]\n"
"v_mfma_f32_16x16x32_bf16 v[180:183], a[120:123], a[144:147], v[180:183]\n"
"ds_read_b128 a[84:87], v237 offset:15360\n"
"v_mfma_f32_16x16x32_bf16 v[108:111], a[124:127], a[140:143], v[108:111]\n"
"v_mfma_f32_16x16x32_bf16 v[188:191], a[124:127], a[144:147], v[188:191]\n"
"ds_read_b128 a[88:91], v237 offset:24576\n"
"v_mfma_f32_16x16x32_bf16 v[100:103], a[64:67], a[148:151], v[100:103]\n"
"s_waitcnt vmcnt(3)\n"
"buffer_store_dwordx2 v[232:233], v239, s[8:11], s89 offen sc1\n"
"v_mfma_f32_16x16x32_bf16 v[180:183], a[64:67], a[152:155], v[180:183]\n"
"s_waitcnt lgkmcnt(3)\n"
"ds_read_b128 a[92:95], v237 offset:25600\n"
"v_mfma_f32_16x16x32_bf16 v[108:111], a[68:71], a[148:151], v[108:111]\n"
"v_mfma_f32_16x16x32_bf16 v[188:191], a[68:71], a[152:155], v[188:191]\n"
"ds_read_b128 a[96:99], v226 offset:12288\n"
"v_mfma_f32_16x16x32_bf16 v[100:103], a[72:75], a[156:159], v[100:103]\n"
"v_mfma_f32_16x16x32_bf16 v[180:183], a[72:75], a[160:163], v[180:183]\n"
"ds_read_b128 a[100:103], v226 offset:13312\n"
"v_mfma_f32_16x16x32_bf16 v[108:111], a[76:79], a[156:159], v[108:111]\n"
"v_mfma_f32_16x16x32_bf16 v[188:191], a[76:79], a[160:163], v[188:191]\n"
"s_waitcnt lgkmcnt(3)\n"
"ds_read_b128 a[104:107], v226 offset:22528\n"
"v_mfma_f32_16x16x32_bf16 v[100:103], a[80:83], a[164:167], v[100:103]\n"
"v_mfma_f32_16x16x32_bf16 v[180:183], a[80:83], a[168:171], v[180:183]\n"
"ds_read_b128 a[108:111], v226 offset:23552\n"
"v_mfma_f32_16x16x32_bf16 v[108:111], a[84:87], a[164:167], v[108:111]\n"
"v_mfma_f32_16x16x32_bf16 v[188:191], a[84:87], a[168:171], v[188:191]\n"
"ds_read_b128 a[112:115], v235 offset:0\n"
"v_mfma_f32_16x16x32_bf16 v[100:103], a[88:91], a[172:175], v[100:103]\n"
"v_mfma_f32_16x16x32_bf16 v[180:183], a[88:91], a[176:179], v[180:183]\n"
"s_waitcnt lgkmcnt(3)\n"
"ds_read_b128 a[116:119], v235 offset:1024\n"
"v_mfma_f32_16x16x32_bf16 v[108:111], a[92:95], a[172:175], v[108:111]\n"
"v_mfma_f32_16x16x32_bf16 v[188:191], a[92:95], a[176:179], v[188:191]\n"
"ds_read_b128 a[120:123], v235 offset:10240\n"
"v_mfma_f32_16x16x32_bf16 v[116:119], a[96:99], a[212:215], v[116:119]\n"
"v_mfma_f32_16x16x32_bf16 v[196:199], a[96:99], a[216:219], v[196:199]\n"
"ds_read_b128 a[124:127], v235 offset:11264\n"
"v_mfma_f32_16x16x32_bf16 v[124:127], a[100:103], a[212:215], v[124:127]\n"
"s_add_u32 s90, s88, 328704\n"
"v_cvt_pk_bf16_f32 v100, v100, v101\n"
"v_cvt_pk_bf16_f32 v101, v102, v103\n"
"v_mfma_f32_16x16x32_bf16 v[204:207], a[100:103], a[216:219], v[204:207]\n"
"s_waitcnt lgkmcnt(3)\n"
"ds_read_b128 a[64:67], v235 offset:20480\n"
"v_cvt_pk_bf16_f32 v102, v108, v109\n"
"v_mfma_f32_16x16x32_bf16 v[116:119], a[104:107], a[220:223], v[116:119]\n"
"s_waitcnt vmcnt(3)\n"
"v_cmp_eq_u64 vcc, s[28:29], v[240:241]\n"
"s_cmp_eq_u64 vcc, exec\n"
"s_cbranch_scc0 L1058_%=\n"
"L1059_%=:\n"
"v_mfma_f32_16x16x32_bf16 v[196:199], a[104:107], a[224:227], v[196:199]\n"
"ds_read_b128 a[68:71], v235 offset:21504\n"
"v_cvt_pk_bf16_f32 v103, v110, v111\n"
"v_mfma_f32_16x16x32_bf16 v[124:127], a[108:111], a[220:223], v[124:127]\n"
"buffer_store_dwordx4 v[100:103], v224, s[8:11], s90 offen sc1\n"
"v_mfma_f32_16x16x32_bf16 v[204:207], a[108:111], a[224:227], v[204:207]\n"
"ds_read_b128 a[72:75], v235 offset:30720\n"
"v_mfma_f32_16x16x32_bf16 v[116:119], a[112:115], a[228:231], v[116:119]\n"
"buffer_load_dwordx4 v[112:115], v176, s[12:15], s38 offen\n"
"v_mfma_f32_16x16x32_bf16 v[196:199], a[112:115], a[232:235], v[196:199]\n"
"s_waitcnt lgkmcnt(3)\n"
"ds_read_b128 a[76:79], v235 offset:31744\n"
"v_cvt_pk_bf16_f32 v180, v180, v181\n"
"v_mfma_f32_16x16x32_bf16 v[124:127], a[116:119], a[228:231], v[124:127]\n"
"s_add_u32 s91, s88, 492544\n"
"buffer_load_dwordx4 v[144:147], v178, s[8:11], s36 offen sc1\n"
"v_mfma_f32_16x16x32_bf16 v[204:207], a[116:119], a[232:235], v[204:207]\n"
"ds_read_b128 a[80:83], v236 offset:8192\n"
"v_cvt_pk_bf16_f32 v181, v182, v183\n"
"v_mfma_f32_16x16x32_bf16 v[116:119], a[120:123], a[236:239], v[116:119]\n"
"v_cvt_pk_bf16_f32 v182, v188, v189\n"
"v_cvt_pk_bf16_f32 v183, v190, v191\n"
"v_mfma_f32_16x16x32_bf16 v[196:199], a[120:123], a[240:243], v[196:199]\n"
"buffer_store_dwordx4 v[180:183], v224, s[8:11], s91 offen sc1\n"
"v_mfma_f32_16x16x32_bf16 v[124:127], a[124:127], a[236:239], v[124:127]\n"
"ds_read_b128 a[84:87], v236 offset:9216\n"
"v_mfma_f32_16x16x32_bf16 v[204:207], a[124:127], a[240:243], v[204:207]\n"
"s_waitcnt lgkmcnt(3)\n"
"buffer_load_dwordx4 v[120:123], v176, s[12:15], s55 offen\n"
"v_mfma_f32_16x16x32_bf16 v[116:119], a[64:67], a[244:247], v[116:119]\n"
"ds_read_b128 a[88:91], v236 offset:18432\n"
"v_mfma_f32_16x16x32_bf16 v[196:199], a[64:67], a[248:251], v[196:199]\n"
"buffer_load_dwordx4 v[152:155], v178, s[8:11], s37 offen sc1\n"
"v_mfma_f32_16x16x32_bf16 v[124:127], a[68:71], a[244:247], v[124:127]\n"
"ds_read_b128 a[92:95], v236 offset:19456\n"
"v_mfma_f32_16x16x32_bf16 v[204:207], a[68:71], a[248:251], v[204:207]\n"
"ds_read_b128 a[96:99], v236 offset:28672\n"
"v_mfma_f32_16x16x32_bf16 v[116:119], a[72:75], a[252:255], v[116:119]\n"
"buffer_load_dwordx4 v[128:131], v177, s[12:15], s38 offen\n"
"v_mfma_f32_16x16x32_bf16 v[196:199], a[72:75], a[128:131], v[196:199]\n"
"s_waitcnt lgkmcnt(3)\n"
"ds_read_b128 a[100:103], v236 offset:29696\n"
"v_mfma_f32_16x16x32_bf16 v[124:127], a[76:79], a[252:255], v[124:127]\n"
"buffer_load_dwordx4 v[160:163], v179, s[8:11], s36 offen sc1\n"
"v_mfma_f32_16x16x32_bf16 v[204:207], a[76:79], a[128:131], v[204:207]\n"
"ds_read_b128 a[104:107], v237 offset:6144\n"
"v_mfma_f32_16x16x32_bf16 v[116:119], a[80:83], a[132:135], v[116:119]\n"
"v_mfma_f32_16x16x32_bf16 v[196:199], a[80:83], a[136:139], v[196:199]\n"
"ds_read_b128 a[108:111], v237 offset:7168\n"
"v_mfma_f32_16x16x32_bf16 v[124:127], a[84:87], a[132:135], v[124:127]\n"
"buffer_load_dwordx4 v[136:139], v177, s[12:15], s55 offen\n"
"v_mfma_f32_16x16x32_bf16 v[204:207], a[84:87], a[136:139], v[204:207]\n"
"s_waitcnt lgkmcnt(3)\n"
"ds_read_b128 a[112:115], v237 offset:16384\n"
"v_mfma_f32_16x16x32_bf16 v[116:119], a[88:91], a[140:143], v[116:119]\n"
"buffer_load_dwordx4 v[168:171], v179, s[8:11], s37 offen sc1\n"
"v_mfma_f32_16x16x32_bf16 v[196:199], a[88:91], a[144:147], v[196:199]\n"
"ds_read_b128 a[116:119], v237 offset:17408\n"
"v_mfma_f32_16x16x32_bf16 v[124:127], a[92:95], a[140:143], v[124:127]\n"
"v_mfma_f32_16x16x32_bf16 v[204:207], a[92:95], a[144:147], v[204:207]\n"
"ds_read_b128 a[120:123], v237 offset:26624\n"
"v_mfma_f32_16x16x32_bf16 v[116:119], a[96:99], a[148:151], v[116:119]\n"
"v_mfma_f32_16x16x32_bf16 v[196:199], a[96:99], a[152:155], v[196:199]\n"
"s_waitcnt lgkmcnt(3)\n"
"ds_read_b128 a[124:127], v237 offset:27648\n"
"v_mfma_f32_16x16x32_bf16 v[124:127], a[100:103], a[148:151], v[124:127]\n"
"s_lshr_b32 s93, s31, 3\n"
"v_mfma_f32_16x16x32_bf16 v[204:207], a[100:103], a[152:155], v[204:207]\n"
"s_lshl_b32 s93, s93, 4\n"
"ds_read_b128 a[64:67], v226 offset:14336\n"
"v_mfma_f32_16x16x32_bf16 v[116:119], a[104:107], a[156:159], v[116:119]\n"
"s_add_u32 s93, s93, s32\n"
"v_mfma_f32_16x16x32_bf16 v[196:199], a[104:107], a[160:163], v[196:199]\n"
"s_mul_i32 s94, s93, 40960\n"
"ds_read_b128 a[68:71], v226 offset:15360\n"
"v_mfma_f32_16x16x32_bf16 v[124:127], a[108:111], a[156:159], v[124:127]\n"
"s_lshl_b32 s93, s93, 6\n"
"v_mfma_f32_16x16x32_bf16 v[204:207], a[108:111], a[160:163], v[204:207]\n"
"s_waitcnt lgkmcnt(3)\n"
"ds_read_b128 a[72:75], v226 offset:24576\n"
"s_add_u32 s93, s93, 23296\n"
"v_mfma_f32_16x16x32_bf16 v[116:119], a[112:115], a[164:167], v[116:119]\n"
"s_add_u32 s94, s94, 60817408\n"
"v_mfma_f32_16x16x32_bf16 v[196:199], a[112:115], a[168:171], v[196:199]\n"
"s_lshl_b32 s92, s30, 10\n"
"ds_read_b128 a[76:79], v226 offset:25600\n"
"v_mfma_f32_16x16x32_bf16 v[124:127], a[116:119], a[164:167], v[124:127]\n"
"v_add_u32 v244, s92, v224\n"
"v_mfma_f32_16x16x32_bf16 v[204:207], a[116:119], a[168:171], v[204:207]\n"
"s_add_u32 s92, s45, 4096\n"
"ds_read_b128 a[80:83], v235 offset:2048\n"
"v_mfma_f32_16x16x32_bf16 v[116:119], a[120:123], a[172:175], v[116:119]\n"
"v_add_u32 v245, s92, v224\n"
"v_mfma_f32_16x16x32_bf16 v[196:199], a[120:123], a[176:179], v[196:199]\n"
"s_waitcnt lgkmcnt(3)\n"
"ds_read_b128 a[84:87], v235 offset:3072\n"
"v_mfma_f32_16x16x32_bf16 v[124:127], a[124:127], a[172:175], v[124:127]\n"
"v_mfma_f32_16x16x32_bf16 v[204:207], a[124:127], a[176:179], v[204:207]\n"
"ds_read_b128 a[88:91], v235 offset:12288\n"
"v_mfma_f32_16x16x32_bf16 v[132:135], a[64:67], a[212:215], v[132:135]\n"
"v_mfma_f32_16x16x32_bf16 v[208:211], a[64:67], a[216:219], v[208:211]\n"
"ds_read_b128 a[92:95], v235 offset:13312\n"
"v_mfma_f32_16x16x32_bf16 v[140:143], a[68:71], a[212:215], v[140:143]\n"
"s_add_u32 s90, s88, 329728\n"
"v_cvt_pk_bf16_f32 v116, v116, v117\n"
"v_cvt_pk_bf16_f32 v117, v118, v119\n"
"v_mfma_f32_16x16x32_bf16 v[212:215], a[68:71], a[216:219], v[212:215]\n"
"s_waitcnt lgkmcnt(3)\n"
"ds_read_b128 a[96:99], v235 offset:22528\n"
"v_cvt_pk_bf16_f32 v118, v124, v125\n"
"v_mfma_f32_16x16x32_bf16 v[132:135], a[72:75], a[220:223], v[132:135]\n"
"v_cvt_pk_bf16_f32 v119, v126, v127\n"
"v_mfma_f32_16x16x32_bf16 v[208:211], a[72:75], a[224:227], v[208:211]\n"
"ds_read_b128 a[100:103], v235 offset:23552\n"
"v_mfma_f32_16x16x32_bf16 v[140:143], a[76:79], a[220:223], v[140:143]\n"
"buffer_store_dwordx4 v[116:119], v224, s[8:11], s90 offen sc1\n"
"v_mfma_f32_16x16x32_bf16 v[212:215], a[76:79], a[224:227], v[212:215]\n"
"ds_read_b128 a[104:107], v236 offset:0\n"
"v_mfma_f32_16x16x32_bf16 v[132:135], a[80:83], a[228:231], v[132:135]\n"
"v_mfma_f32_16x16x32_bf16 v[208:211], a[80:83], a[232:235], v[208:211]\n"
"s_waitcnt lgkmcnt(3)\n"
"ds_read_b128 a[108:111], v236 offset:1024\n"
"v_cvt_pk_bf16_f32 v196, v196, v197\n"
"v_mfma_f32_16x16x32_bf16 v[140:143], a[84:87], a[228:231], v[140:143]\n"
"s_add_u32 s91, s88, 493568\n"
"v_cvt_pk_bf16_f32 v197, v198, v199\n"
"v_cvt_pk_bf16_f32 v198, v204, v205\n"
"v_mfma_f32_16x16x32_bf16 v[212:215], a[84:87], a[232:235], v[212:215]\n"
"ds_read_b128 a[112:115], v236 offset:10240\n"
"v_cvt_pk_bf16_f32 v199, v206, v207\n"
"v_mfma_f32_16x16x32_bf16 v[132:135], a[88:91], a[236:239], v[132:135]\n"
"buffer_store_dwordx4 v[196:199], v224, s[8:11], s91 offen sc1\n"
"v_mfma_f32_16x16x32_bf16 v[208:211], a[88:91], a[240:243], v[208:211]\n"
"ds_read_b128 a[116:119], v236 offset:11264\n"
"v_mfma_f32_16x16x32_bf16 v[140:143], a[92:95], a[236:239], v[140:143]\n"
"v_mfma_f32_16x16x32_bf16 v[212:215], a[92:95], a[240:243], v[212:215]\n"
"s_waitcnt lgkmcnt(3)\n"
"ds_read_b128 a[120:123], v236 offset:20480\n"
"v_mfma_f32_16x16x32_bf16 v[132:135], a[96:99], a[244:247], v[132:135]\n"
"v_mfma_f32_16x16x32_bf16 v[208:211], a[96:99], a[248:251], v[208:211]\n"
"ds_read_b128 a[124:127], v236 offset:21504\n"
"v_mfma_f32_16x16x32_bf16 v[140:143], a[100:103], a[244:247], v[140:143]\n"
"v_mfma_f32_16x16x32_bf16 v[212:215], a[100:103], a[248:251], v[212:215]\n"
"ds_read_b128 a[64:67], v236 offset:30720\n"
"v_mfma_f32_16x16x32_bf16 v[132:135], a[104:107], a[252:255], v[132:135]\n"
"v_mfma_f32_16x16x32_bf16 v[208:211], a[104:107], a[128:131], v[208:211]\n"
"s_waitcnt lgkmcnt(3)\n"
"ds_read_b128 a[68:71], v236 offset:31744\n"
"v_mfma_f32_16x16x32_bf16 v[140:143], a[108:111], a[252:255], v[140:143]\n"
"v_mfma_f32_16x16x32_bf16 v[212:215], a[108:111], a[128:131], v[212:215]\n"
"ds_read_b128 a[72:75], v237 offset:8192\n"
"v_mfma_f32_16x16x32_bf16 v[132:135], a[112:115], a[132:135], v[132:135]\n"
"v_mfma_f32_16x16x32_bf16 v[208:211], a[112:115], a[136:139], v[208:211]\n"
"ds_read_b128 a[76:79], v237 offset:9216\n"
"v_mfma_f32_16x16x32_bf16 v[140:143], a[116:119], a[132:135], v[140:143]\n"
"v_mfma_f32_16x16x32_bf16 v[212:215], a[116:119], a[136:139], v[212:215]\n"
"s_waitcnt lgkmcnt(3)\n"
"ds_read_b128 a[80:83], v237 offset:18432\n"
"v_mfma_f32_16x16x32_bf16 v[132:135], a[120:123], a[140:143], v[132:135]\n"
"v_mfma_f32_16x16x32_bf16 v[208:211], a[120:123], a[144:147], v[208:211]\n"
"ds_read_b128 a[84:87], v237 offset:19456\n"
"v_mfma_f32_16x16x32_bf16 v[140:143], a[124:127], a[140:143], v[140:143]\n"
"v_mfma_f32_16x16x32_bf16 v[212:215], a[124:127], a[144:147], v[212:215]\n"
"ds_read_b128 a[88:91], v237 offset:28672\n"
"v_mfma_f32_16x16x32_bf16 v[132:135], a[64:67], a[148:151], v[132:135]\n"
"v_mfma_f32_16x16x32_bf16 v[208:211], a[64:67], a[152:155], v[208:211]\n"
"s_waitcnt lgkmcnt(3)\n"
"ds_read_b128 a[92:95], v237 offset:29696\n"
"v_mfma_f32_16x16x32_bf16 v[140:143], a[68:71], a[148:151], v[140:143]\n"
"v_mfma_f32_16x16x32_bf16 v[212:215], a[68:71], a[152:155], v[212:215]\n"
"ds_read_b128 a[96:99], v226 offset:16384\n"
"v_mfma_f32_16x16x32_bf16 v[132:135], a[72:75], a[156:159], v[132:135]\n"
"v_mfma_f32_16x16x32_bf16 v[208:211], a[72:75], a[160:163], v[208:211]\n"
"ds_read_b128 a[100:103], v226 offset:17408\n"
"v_mfma_f32_16x16x32_bf16 v[140:143], a[76:79], a[156:159], v[140:143]\n"
"v_mfma_f32_16x16x32_bf16 v[212:215], a[76:79], a[160:163], v[212:215]\n"
"s_waitcnt lgkmcnt(3)\n"
"ds_read_b128 a[104:107], v226 offset:26624\n"
"v_mfma_f32_16x16x32_bf16 v[132:135], a[80:83], a[164:167], v[132:135]\n"
"v_mfma_f32_16x16x32_bf16 v[208:211], a[80:83], a[168:171], v[208:211]\n"
"ds_read_b128 a[108:111], v226 offset:27648\n"
"v_mfma_f32_16x16x32_bf16 v[140:143], a[84:87], a[164:167], v[140:143]\n"
"v_mfma_f32_16x16x32_bf16 v[212:215], a[84:87], a[168:171], v[212:215]\n"
"ds_read_b128 a[112:115], v235 offset:4096\n"
"v_mfma_f32_16x16x32_bf16 v[132:135], a[88:91], a[172:175], v[132:135]\n"
"v_mfma_f32_16x16x32_bf16 v[208:211], a[88:91], a[176:179], v[208:211]\n"
"s_waitcnt lgkmcnt(3)\n"
"ds_read_b128 a[116:119], v235 offset:5120\n"
"v_mfma_f32_16x16x32_bf16 v[140:143], a[92:95], a[172:175], v[140:143]\n"
"buffer_load_dwordx2 v[240:241], v243, s[8:11], s93 offen sc1\n"
"v_mfma_f32_16x16x32_bf16 v[212:215], a[92:95], a[176:179], v[212:215]\n"
"ds_read_b128 a[120:123], v235 offset:14336\n"
"v_mfma_f32_16x16x32_bf16 v[148:151], a[96:99], a[212:215], v[148:151]\n"
"v_mfma_f32_16x16x32_bf16 v[216:219], a[96:99], a[216:219], v[216:219]\n"
"ds_read_b128 a[124:127], v235 offset:15360\n"
"v_mfma_f32_16x16x32_bf16 v[156:159], a[100:103], a[212:215], v[156:159]\n"
"s_add_u32 s90, s88, 330752\n"
"v_cvt_pk_bf16_f32 v132, v132, v133\n"
"v_cvt_pk_bf16_f32 v133, v134, v135\n"
"v_mfma_f32_16x16x32_bf16 v[220:223], a[100:103], a[216:219], v[220:223]\n"
"s_waitcnt lgkmcnt(3)\n"
"ds_read_b128 a[64:67], v235 offset:24576\n"
"v_cvt_pk_bf16_f32 v134, v140, v141\n"
"v_mfma_f32_16x16x32_bf16 v[148:151], a[104:107], a[220:223], v[148:151]\n"
"v_cvt_pk_bf16_f32 v135, v142, v143\n"
"v_mfma_f32_16x16x32_bf16 v[216:219], a[104:107], a[224:227], v[216:219]\n"
"ds_read_b128 a[68:71], v235 offset:25600\n"
"v_mfma_f32_16x16x32_bf16 v[156:159], a[108:111], a[220:223], v[156:159]\n"
"buffer_store_dwordx4 v[132:135], v224, s[8:11], s90 offen sc1\n"
"v_mfma_f32_16x16x32_bf16 v[220:223], a[108:111], a[224:227], v[220:223]\n"
"ds_read_b128 a[72:75], v236 offset:2048\n"
"v_mfma_f32_16x16x32_bf16 v[148:151], a[112:115], a[228:231], v[148:151]\n"
"v_mfma_f32_16x16x32_bf16 v[216:219], a[112:115], a[232:235], v[216:219]\n"
"s_waitcnt lgkmcnt(3)\n"
"ds_read_b128 a[76:79], v236 offset:3072\n"
"v_cvt_pk_bf16_f32 v208, v208, v209\n"
"v_mfma_f32_16x16x32_bf16 v[156:159], a[116:119], a[228:231], v[156:159]\n"
"s_add_u32 s91, s88, 494592\n"
"v_cvt_pk_bf16_f32 v209, v210, v211\n"
"v_cvt_pk_bf16_f32 v210, v212, v213\n"
"v_mfma_f32_16x16x32_bf16 v[220:223], a[116:119], a[232:235], v[220:223]\n"
"ds_read_b128 a[80:83], v236 offset:12288\n"
"v_cvt_pk_bf16_f32 v211, v214, v215\n"
"v_mfma_f32_16x16x32_bf16 v[148:151], a[120:123], a[236:239], v[148:151]\n"
"buffer_store_dwordx4 v[208:211], v224, s[8:11], s91 offen sc1\n"
"v_mfma_f32_16x16x32_bf16 v[216:219], a[120:123], a[240:243], v[216:219]\n"
"ds_read_b128 a[84:87], v236 offset:13312\n"
"v_mfma_f32_16x16x32_bf16 v[156:159], a[124:127], a[236:239], v[156:159]\n"
"v_mfma_f32_16x16x32_bf16 v[220:223], a[124:127], a[240:243], v[220:223]\n"
"s_waitcnt lgkmcnt(3)\n"
"ds_read_b128 a[88:91], v236 offset:22528\n"
"v_mfma_f32_16x16x32_bf16 v[148:151], a[64:67], a[244:247], v[148:151]\n"
"v_mfma_f32_16x16x32_bf16 v[216:219], a[64:67], a[248:251], v[216:219]\n"
"ds_read_b128 a[92:95], v236 offset:23552\n"
"v_mfma_f32_16x16x32_bf16 v[156:159], a[68:71], a[244:247], v[156:159]\n"
"v_mfma_f32_16x16x32_bf16 v[220:223], a[68:71], a[248:251], v[220:223]\n"
"ds_read_b128 a[96:99], v237 offset:0\n"
"v_mfma_f32_16x16x32_bf16 v[148:151], a[72:75], a[252:255], v[148:151]\n"
"v_mfma_f32_16x16x32_bf16 v[216:219], a[72:75], a[128:131], v[216:219]\n"
"s_waitcnt lgkmcnt(3)\n"
"ds_read_b128 a[100:103], v237 offset:1024\n"
"v_mfma_f32_16x16x32_bf16 v[156:159], a[76:79], a[252:255], v[156:159]\n"
"v_mfma_f32_16x16x32_bf16 v[220:223], a[76:79], a[128:131], v[220:223]\n"
"ds_read_b128 a[104:107], v237 offset:10240\n"
"v_mfma_f32_16x16x32_bf16 v[148:151], a[80:83], a[132:135], v[148:151]\n"
"v_mfma_f32_16x16x32_bf16 v[216:219], a[80:83], a[136:139], v[216:219]\n"
"ds_read_b128 a[108:111], v237 offset:11264\n"
"v_mfma_f32_16x16x32_bf16 v[156:159], a[84:87], a[132:135], v[156:159]\n"
"v_mfma_f32_16x16x32_bf16 v[220:223], a[84:87], a[136:139], v[220:223]\n"
"s_waitcnt lgkmcnt(3)\n"
"ds_read_b128 a[112:115], v237 offset:20480\n"
"v_mfma_f32_16x16x32_bf16 v[148:151], a[88:91], a[140:143], v[148:151]\n"
"v_mfma_f32_16x16x32_bf16 v[216:219], a[88:91], a[144:147], v[216:219]\n"
"ds_read_b128 a[116:119], v237 offset:21504\n"
"v_mfma_f32_16x16x32_bf16 v[156:159], a[92:95], a[140:143], v[156:159]\n"
"v_mfma_f32_16x16x32_bf16 v[220:223], a[92:95], a[144:147], v[220:223]\n"
"ds_read_b128 a[120:123], v237 offset:30720\n"
"v_mfma_f32_16x16x32_bf16 v[148:151], a[96:99], a[148:151], v[148:151]\n"
"v_mfma_f32_16x16x32_bf16 v[216:219], a[96:99], a[152:155], v[216:219]\n"
"s_waitcnt lgkmcnt(3)\n"
"ds_read_b128 a[124:127], v237 offset:31744\n"
"v_mfma_f32_16x16x32_bf16 v[156:159], a[100:103], a[148:151], v[156:159]\n"
"v_mfma_f32_16x16x32_bf16 v[220:223], a[100:103], a[152:155], v[220:223]\n"
"v_mfma_f32_16x16x32_bf16 v[148:151], a[104:107], a[156:159], v[148:151]\n"
"v_mfma_f32_16x16x32_bf16 v[216:219], a[104:107], a[160:163], v[216:219]\n"
"v_mfma_f32_16x16x32_bf16 v[156:159], a[108:111], a[156:159], v[156:159]\n"
"v_mfma_f32_16x16x32_bf16 v[220:223], a[108:111], a[160:163], v[220:223]\n"
"s_waitcnt lgkmcnt(1)\n"
"v_mfma_f32_16x16x32_bf16 v[148:151], a[112:115], a[164:167], v[148:151]\n"
"v_mfma_f32_16x16x32_bf16 v[216:219], a[112:115], a[168:171], v[216:219]\n"
"v_mfma_f32_16x16x32_bf16 v[156:159], a[116:119], a[164:167], v[156:159]\n"
"v_mfma_f32_16x16x32_bf16 v[220:223], a[116:119], a[168:171], v[220:223]\n"
"v_mfma_f32_16x16x32_bf16 v[148:151], a[120:123], a[172:175], v[148:151]\n"
"v_mfma_f32_16x16x32_bf16 v[216:219], a[120:123], a[176:179], v[216:219]\n"
"s_waitcnt lgkmcnt(0)\n"
"v_mfma_f32_16x16x32_bf16 v[156:159], a[124:127], a[172:175], v[156:159]\n"
"v_mfma_f32_16x16x32_bf16 v[220:223], a[124:127], a[176:179], v[220:223]\n"
"s_nop 5\n"
"v_cvt_pk_bf16_f32 v148, v148, v149\n"
"v_cvt_pk_bf16_f32 v149, v150, v151\n"
"v_cvt_pk_bf16_f32 v150, v156, v157\n"
"v_cvt_pk_bf16_f32 v151, v158, v159\n"
"s_add_u32 s90, s88, 331776\n"
"buffer_store_dwordx4 v[148:151], v224, s[8:11], s90 offen sc1\n"
"v_cvt_pk_bf16_f32 v216, v216, v217\n"
"v_cvt_pk_bf16_f32 v217, v218, v219\n"
"v_cvt_pk_bf16_f32 v218, v220, v221\n"
"v_cvt_pk_bf16_f32 v219, v222, v223\n"
"s_add_u32 s91, s88, 495616\n"
"buffer_store_dwordx4 v[216:219], v224, s[8:11], s91 offen sc1\n"
"s_waitcnt vmcnt(4)\n"
"v_cmp_eq_u64 vcc, s[28:29], v[240:241]\n"
"s_cmp_eq_u64 vcc, exec\n"
"s_cbranch_scc0 L1061_%=\n"
"L1062_%=:\n"
"s_add_u32 s92, s94, 0\n"
"buffer_load_dwordx4 a[0:3], v244, s[8:11], s92 offen sc1\n"
"buffer_load_dwordx4 a[32:35], v245, s[8:11], s92 offen sc1\n"
"s_add_u32 s92, s94, 5120\n"
"buffer_load_dwordx4 a[4:7], v244, s[8:11], s92 offen sc1\n"
"buffer_load_dwordx4 a[36:39], v245, s[8:11], s92 offen sc1\n"
"s_add_u32 s92, s94, 10240\n"
"buffer_load_dwordx4 a[8:11], v244, s[8:11], s92 offen sc1\n"
"buffer_load_dwordx4 a[40:43], v245, s[8:11], s92 offen sc1\n"
"s_add_u32 s92, s94, 15360\n"
"buffer_load_dwordx4 a[12:15], v244, s[8:11], s92 offen sc1\n"
"buffer_load_dwordx4 a[44:47], v245, s[8:11], s92 offen sc1\n"
"s_add_u32 s92, s94, 20480\n"
"buffer_load_dwordx4 a[16:19], v244, s[8:11], s92 offen sc1\n"
"buffer_load_dwordx4 a[48:51], v245, s[8:11], s92 offen sc1\n"
"s_add_u32 s92, s94, 25600\n"
"buffer_load_dwordx4 a[20:23], v244, s[8:11], s92 offen sc1\n"
"buffer_load_dwordx4 a[52:55], v245, s[8:11], s92 offen sc1\n"
"s_add_u32 s92, s94, 30720\n"
"buffer_load_dwordx4 a[24:27], v244, s[8:11], s92 offen sc1\n"
"buffer_load_dwordx4 a[56:59], v245, s[8:11], s92 offen sc1\n"
"s_add_u32 s92, s94, 35840\n"
"buffer_load_dwordx4 a[28:31], v244, s[8:11], s92 offen sc1\n"
"buffer_load_dwordx4 a[60:63], v245, s[8:11], s92 offen sc1\n"
"s_add_u32 s89, s89, 512\n"
"s_waitcnt vmcnt(16)\n"
"buffer_store_dwordx2 v[232:233], v239, s[8:11], s89 offen sc1\n"
"buffer_load_dwordx2 v[240:241], v243, s[8:11], s93 offen offset:512 sc1\n"
"v_mfma_f32_16x16x32_bf16 v[0:3], v[96:99], v[112:115], 0\n"
"v_mfma_f32_16x16x32_bf16 v[4:7], v[104:107], v[112:115], 0\n"
"v_mfma_f32_16x16x32_bf16 v[8:11], v[96:99], v[120:123], 0\n"
"v_mfma_f32_16x16x32_bf16 v[12:15], v[104:107], v[120:123], 0\n"
"v_mfma_f32_16x16x32_bf16 v[0:3], v[96:99], v[144:147], v[0:3]\n"
"v_mfma_f32_16x16x32_bf16 v[4:7], v[104:107], v[144:147], v[4:7]\n"
"v_mfma_f32_16x16x32_bf16 v[8:11], v[96:99], v[152:155], v[8:11]\n"
"v_mfma_f32_16x16x32_bf16 v[12:15], v[104:107], v[152:155], v[12:15]\n"
"v_mfma_f32_16x16x32_bf16 v[16:19], v[96:99], v[128:131], 0\n"
"v_mfma_f32_16x16x32_bf16 v[20:23], v[104:107], v[128:131], 0\n"
"v_mfma_f32_16x16x32_bf16 v[24:27], v[96:99], v[136:139], 0\n"
"v_mfma_f32_16x16x32_bf16 v[28:31], v[104:107], v[136:139], 0\n"
"v_mfma_f32_16x16x32_bf16 v[16:19], v[96:99], v[160:163], v[16:19]\n"
"v_mfma_f32_16x16x32_bf16 v[20:23], v[104:107], v[160:163], v[20:23]\n"
"v_mfma_f32_16x16x32_bf16 v[24:27], v[96:99], v[168:171], v[24:27]\n"
"v_mfma_f32_16x16x32_bf16 v[28:31], v[104:107], v[168:171], v[28:31]\n"
"s_waitcnt vmcnt(16)\n"
"v_mfma_f32_16x16x32_bf16 v[0:3], v[80:83], a[0:3], v[0:3]\n"
"v_mfma_f32_16x16x32_bf16 v[4:7], v[88:91], a[0:3], v[4:7]\n"
"v_mfma_f32_16x16x32_bf16 v[8:11], v[80:83], a[32:35], v[8:11]\n"
"v_mfma_f32_16x16x32_bf16 v[12:15], v[88:91], a[32:35], v[12:15]\n"
"s_waitcnt vmcnt(14)\n"
"v_mfma_f32_16x16x32_bf16 v[0:3], v[80:83], a[4:7], v[0:3]\n"
"v_mfma_f32_16x16x32_bf16 v[4:7], v[88:91], a[4:7], v[4:7]\n"
"v_mfma_f32_16x16x32_bf16 v[8:11], v[80:83], a[36:39], v[8:11]\n"
"v_mfma_f32_16x16x32_bf16 v[12:15], v[88:91], a[36:39], v[12:15]\n"
"s_waitcnt vmcnt(12)\n"
"v_mfma_f32_16x16x32_bf16 v[0:3], v[80:83], a[8:11], v[0:3]\n"
"v_mfma_f32_16x16x32_bf16 v[4:7], v[88:91], a[8:11], v[4:7]\n"
"v_mfma_f32_16x16x32_bf16 v[8:11], v[80:83], a[40:43], v[8:11]\n"
"v_mfma_f32_16x16x32_bf16 v[12:15], v[88:91], a[40:43], v[12:15]\n"
"s_waitcnt vmcnt(10)\n"
"v_mfma_f32_16x16x32_bf16 v[0:3], v[80:83], a[12:15], v[0:3]\n"
"v_mfma_f32_16x16x32_bf16 v[4:7], v[88:91], a[12:15], v[4:7]\n"
"v_mfma_f32_16x16x32_bf16 v[8:11], v[80:83], a[44:47], v[8:11]\n"
"v_mfma_f32_16x16x32_bf16 v[12:15], v[88:91], a[44:47], v[12:15]\n"
"s_waitcnt vmcnt(8)\n"
"v_mfma_f32_16x16x32_bf16 v[0:3], v[80:83], a[16:19], v[0:3]\n"
"v_mfma_f32_16x16x32_bf16 v[4:7], v[88:91], a[16:19], v[4:7]\n"
"v_mfma_f32_16x16x32_bf16 v[8:11], v[80:83], a[48:51], v[8:11]\n"
"v_mfma_f32_16x16x32_bf16 v[12:15], v[88:91], a[48:51], v[12:15]\n"
"s_waitcnt vmcnt(6)\n"
"v_mfma_f32_16x16x32_bf16 v[0:3], v[80:83], a[20:23], v[0:3]\n"
"v_mfma_f32_16x16x32_bf16 v[4:7], v[88:91], a[20:23], v[4:7]\n"
"v_mfma_f32_16x16x32_bf16 v[8:11], v[80:83], a[52:55], v[8:11]\n"
"v_mfma_f32_16x16x32_bf16 v[12:15], v[88:91], a[52:55], v[12:15]\n"
"s_waitcnt vmcnt(4)\n"
"v_mfma_f32_16x16x32_bf16 v[0:3], v[80:83], a[24:27], v[0:3]\n"
"v_mfma_f32_16x16x32_bf16 v[4:7], v[88:91], a[24:27], v[4:7]\n"
"v_mfma_f32_16x16x32_bf16 v[8:11], v[80:83], a[56:59], v[8:11]\n"
"v_mfma_f32_16x16x32_bf16 v[12:15], v[88:91], a[56:59], v[12:15]\n"
"s_waitcnt vmcnt(2)\n"
"v_mfma_f32_16x16x32_bf16 v[0:3], v[80:83], a[28:31], v[0:3]\n"
"v_mfma_f32_16x16x32_bf16 v[4:7], v[88:91], a[28:31], v[4:7]\n"
"v_mfma_f32_16x16x32_bf16 v[8:11], v[80:83], a[60:63], v[8:11]\n"
"v_mfma_f32_16x16x32_bf16 v[12:15], v[88:91], a[60:63], v[12:15]\n"
"s_nop 5\n"
"v_cvt_pk_bf16_f32 v0, v0, v1\n"
"v_cvt_pk_bf16_f32 v1, v2, v3\n"
"v_cvt_pk_bf16_f32 v2, v4, v5\n"
"v_cvt_pk_bf16_f32 v3, v6, v7\n"
"buffer_store_dwordx4 v[0:3], v176, s[16:19], s38 offen\n"
"v_cvt_pk_bf16_f32 v8, v8, v9\n"
"v_cvt_pk_bf16_f32 v9, v10, v11\n"
"v_cvt_pk_bf16_f32 v10, v12, v13\n"
"v_cvt_pk_bf16_f32 v11, v14, v15\n"
"buffer_store_dwordx4 v[8:11], v176, s[16:19], s55 offen\n"
"s_waitcnt vmcnt(2)\n"
"v_cmp_eq_u64 vcc, s[28:29], v[240:241]\n"
"s_cmp_eq_u64 vcc, exec\n"
"s_cbranch_scc0 L1064_%=\n"
"L1065_%=:\n"
"s_add_u32 s92, s94, 327680\n"
"buffer_load_dwordx4 a[64:67], v244, s[8:11], s92 offen sc1\n"
"buffer_load_dwordx4 a[96:99], v245, s[8:11], s92 offen sc1\n"
"s_add_u32 s92, s94, 332800\n"
"buffer_load_dwordx4 a[68:71], v244, s[8:11], s92 offen sc1\n"
"buffer_load_dwordx4 a[100:103], v245, s[8:11], s92 offen sc1\n"
"s_add_u32 s92, s94, 337920\n"
"buffer_load_dwordx4 a[72:75], v244, s[8:11], s92 offen sc1\n"
"buffer_load_dwordx4 a[104:107], v245, s[8:11], s92 offen sc1\n"
"s_add_u32 s92, s94, 343040\n"
"buffer_load_dwordx4 a[76:79], v244, s[8:11], s92 offen sc1\n"
"buffer_load_dwordx4 a[108:111], v245, s[8:11], s92 offen sc1\n"
"s_add_u32 s92, s94, 348160\n"
"buffer_load_dwordx4 a[80:83], v244, s[8:11], s92 offen sc1\n"
"buffer_load_dwordx4 a[112:115], v245, s[8:11], s92 offen sc1\n"
"s_add_u32 s92, s94, 353280\n"
"buffer_load_dwordx4 a[84:87], v244, s[8:11], s92 offen sc1\n"
"buffer_load_dwordx4 a[116:119], v245, s[8:11], s92 offen sc1\n"
"s_add_u32 s92, s94, 358400\n"
"buffer_load_dwordx4 a[88:91], v244, s[8:11], s92 offen sc1\n"
"buffer_load_dwordx4 a[120:123], v245, s[8:11], s92 offen sc1\n"
"s_add_u32 s92, s94, 363520\n"
"buffer_load_dwordx4 a[92:95], v244, s[8:11], s92 offen sc1\n"
"buffer_load_dwordx4 a[124:127], v245, s[8:11], s92 offen sc1\n"
"s_waitcnt vmcnt(14)\n"
"v_mfma_f32_16x16x32_bf16 v[16:19], v[80:83], a[64:67], v[16:19]\n"
"v_mfma_f32_16x16x32_bf16 v[20:23], v[88:91], a[64:67], v[20:23]\n"
"v_mfma_f32_16x16x32_bf16 v[24:27], v[80:83], a[96:99], v[24:27]\n"
"v_mfma_f32_16x16x32_bf16 v[28:31], v[88:91], a[96:99], v[28:31]\n"
"s_waitcnt vmcnt(12)\n"
"v_mfma_f32_16x16x32_bf16 v[16:19], v[80:83], a[68:71], v[16:19]\n"
"v_mfma_f32_16x16x32_bf16 v[20:23], v[88:91], a[68:71], v[20:23]\n"
"v_mfma_f32_16x16x32_bf16 v[24:27], v[80:83], a[100:103], v[24:27]\n"
"v_mfma_f32_16x16x32_bf16 v[28:31], v[88:91], a[100:103], v[28:31]\n"
"s_waitcnt vmcnt(10)\n"
"v_mfma_f32_16x16x32_bf16 v[16:19], v[80:83], a[72:75], v[16:19]\n"
"v_mfma_f32_16x16x32_bf16 v[20:23], v[88:91], a[72:75], v[20:23]\n"
"v_mfma_f32_16x16x32_bf16 v[24:27], v[80:83], a[104:107], v[24:27]\n"
"v_mfma_f32_16x16x32_bf16 v[28:31], v[88:91], a[104:107], v[28:31]\n"
"s_waitcnt vmcnt(8)\n"
"v_mfma_f32_16x16x32_bf16 v[16:19], v[80:83], a[76:79], v[16:19]\n"
"v_mfma_f32_16x16x32_bf16 v[20:23], v[88:91], a[76:79], v[20:23]\n"
"v_mfma_f32_16x16x32_bf16 v[24:27], v[80:83], a[108:111], v[24:27]\n"
"v_mfma_f32_16x16x32_bf16 v[28:31], v[88:91], a[108:111], v[28:31]\n"
"s_waitcnt vmcnt(6)\n"
"v_mfma_f32_16x16x32_bf16 v[16:19], v[80:83], a[80:83], v[16:19]\n"
"v_mfma_f32_16x16x32_bf16 v[20:23], v[88:91], a[80:83], v[20:23]\n"
"v_mfma_f32_16x16x32_bf16 v[24:27], v[80:83], a[112:115], v[24:27]\n"
"v_mfma_f32_16x16x32_bf16 v[28:31], v[88:91], a[112:115], v[28:31]\n"
"s_waitcnt vmcnt(4)\n"
"v_mfma_f32_16x16x32_bf16 v[16:19], v[80:83], a[84:87], v[16:19]\n"
"v_mfma_f32_16x16x32_bf16 v[20:23], v[88:91], a[84:87], v[20:23]\n"
"v_mfma_f32_16x16x32_bf16 v[24:27], v[80:83], a[116:119], v[24:27]\n"
"v_mfma_f32_16x16x32_bf16 v[28:31], v[88:91], a[116:119], v[28:31]\n"
"s_waitcnt vmcnt(2)\n"
"v_mfma_f32_16x16x32_bf16 v[16:19], v[80:83], a[88:91], v[16:19]\n"
"v_mfma_f32_16x16x32_bf16 v[20:23], v[88:91], a[88:91], v[20:23]\n"
"v_mfma_f32_16x16x32_bf16 v[24:27], v[80:83], a[120:123], v[24:27]\n"
"v_mfma_f32_16x16x32_bf16 v[28:31], v[88:91], a[120:123], v[28:31]\n"
"s_waitcnt vmcnt(0)\n"
"v_mfma_f32_16x16x32_bf16 v[16:19], v[80:83], a[92:95], v[16:19]\n"
"v_mfma_f32_16x16x32_bf16 v[20:23], v[88:91], a[92:95], v[20:23]\n"
"v_mfma_f32_16x16x32_bf16 v[24:27], v[80:83], a[124:127], v[24:27]\n"
"v_mfma_f32_16x16x32_bf16 v[28:31], v[88:91], a[124:127], v[28:31]\n"
"s_nop 5\n"
"v_cvt_pk_bf16_f32 v16, v16, v17\n"
"v_cvt_pk_bf16_f32 v17, v18, v19\n"
"v_cvt_pk_bf16_f32 v18, v20, v21\n"
"v_cvt_pk_bf16_f32 v19, v22, v23\n"
"buffer_store_dwordx4 v[16:19], v177, s[16:19], s38 offen\n"
"v_cvt_pk_bf16_f32 v24, v24, v25\n"
"v_cvt_pk_bf16_f32 v25, v26, v27\n"
"v_cvt_pk_bf16_f32 v26, v28, v29\n"
"v_cvt_pk_bf16_f32 v27, v30, v31\n"
"buffer_store_dwordx4 v[24:27], v177, s[16:19], s55 offen\n"
"s_waitcnt vmcnt(0) lgkmcnt(0)\n"
"s_endpgm\n"
"L1004_%=:\n"
"s_mov_b32 s54, 0\n"
"L1006_%=:\n"
"s_sleep 2\n"
"buffer_load_dwordx4 v[160:163], v224, s[8:11], 0 offen offset:256 sc1\n"
"buffer_load_dwordx4 v[164:167], v224, s[8:11], 0 offen offset:1280 sc1\n"
"s_waitcnt vmcnt(0)\n"
"s_add_u32 s54, s54, 1\n"
"v_cmp_eq_u64 vcc, s[28:29], v[160:161]\n"
"v_cmp_eq_u64_e64 s[62:63], s[28:29], v[162:163]\n"
"s_and_b64 vcc, vcc, s[62:63]\n"
"v_cmp_eq_u64_e64 s[62:63], s[28:29], v[164:165]\n"
"s_and_b64 vcc, vcc, s[62:63]\n"
"v_cmp_eq_u64_e64 s[62:63], s[28:29], v[166:167]\n"
"s_and_b64 vcc, vcc, s[62:63]\n"
"s_cmp_eq_u64 vcc, exec\n"
"s_cbranch_scc1 L1005_%=\n"
"s_cmp_lt_u32 s54, 1048576\n"
"s_cbranch_scc1 L1006_%=\n"
"s_branch L1005_%=\n"
"L1007_%=:\n"
"s_mov_b32 s54, 0\n"
"L1009_%=:\n"
"v_max3_u32 v226, v160, v161, v162\n"
"v_max3_u32 v226, v226, v163, v164\n"
"v_max3_u32 v226, v226, v165, v166\n"
"v_max3_u32 v226, v226, v167, v168\n"
"v_max3_u32 v226, v226, v169, v170\n"
"v_max3_u32 v226, v226, v171, v172\n"
"v_max3_u32 v226, v226, v173, v174\n"
"v_max3_u32 v226, v226, v175, v176\n"
"v_max3_u32 v226, v226, v177, v178\n"
"v_max3_u32 v226, v226, v179, v180\n"
"v_max3_u32 v226, v226, v181, v182\n"
"v_max3_u32 v226, v226, v183, v184\n"
"v_max3_u32 v226, v226, v185, v186\n"
"v_max3_u32 v226, v226, v187, v188\n"
"v_max3_u32 v226, v226, v189, v190\n"
"v_max_u32 v226, v226, v191\n"
"v_mov_b32 v229, v226\n"
"s_nop 1\n"
"v_max_u32_dpp v229, v229, v229 row_shr:1 row_mask:0xf bank_mask:0xf\n"
"s_nop 1\n"
"v_max_u32_dpp v229, v229, v229 row_shr:2 row_mask:0xf bank_mask:0xf\n"
"s_nop 1\n"
"v_max_u32_dpp v229, v229, v229 row_shr:4 row_mask:0xf bank_mask:0xf\n"
"s_nop 1\n"
"v_max_u32_dpp v229, v229, v229 row_shr:8 row_mask:0xf bank_mask:0xf\n"
"s_nop 1\n"
"v_max_u32_dpp v229, v229, v229 row_bcast:15 row_mask:0xa bank_mask:0xf\n"
"s_nop 1\n"
"v_max_u32_dpp v229, v229, v229 row_bcast:31 row_mask:0xc bank_mask:0xf\n"
"s_nop 1\n"
"v_readlane_b32 s62, v229, 63\n"
"s_nop 1\n"
"v_cmp_eq_u32 vcc, s62, v160\n"
"s_nop 1\n"
"v_cndmask_b32_e64 v160, v160, 0, vcc\n"
"v_cmp_eq_u32 vcc, s62, v161\n"
"s_nop 1\n"
"v_cndmask_b32_e64 v161, v161, 0, vcc\n"
"v_cmp_eq_u32 vcc, s62, v162\n"
"s_nop 1\n"
"v_cndmask_b32_e64 v162, v162, 0, vcc\n"
"v_cmp_eq_u32 vcc, s62, v163\n"
"s_nop 1\n"
"v_cndmask_b32_e64 v163, v163, 0, vcc\n"
"v_cmp_eq_u32 vcc, s62, v164\n"
"s_nop 1\n"
"v_cndmask_b32_e64 v164, v164, 0, vcc\n"
"v_cmp_eq_u32 vcc, s62, v165\n"
"s_nop 1\n"
"v_cndmask_b32_e64 v165, v165, 0, vcc\n"
"v_cmp_eq_u32 vcc, s62, v166\n"
"s_nop 1\n"
"v_cndmask_b32_e64 v166, v166, 0, vcc\n"
"v_cmp_eq_u32 vcc, s62, v167\n"
"s_nop 1\n"
"v_cndmask_b32_e64 v167, v167, 0, vcc\n"
"v_cmp_eq_u32 vcc, s62, v168\n"
"s_nop 1\n"
"v_cndmask_b32_e64 v168, v168, 0, vcc\n"
"v_cmp_eq_u32 vcc, s62, v169\n"
"s_nop 1\n"
"v_cndmask_b32_e64 v169, v169, 0, vcc\n"
"v_cmp_eq_u32 vcc, s62, v170\n"
"s_nop 1\n"
"v_cndmask_b32_e64 v170, v170, 0, vcc\n"
"v_cmp_eq_u32 vcc, s62, v171\n"
"s_nop 1\n"
"v_cndmask_b32_e64 v171, v171, 0, vcc\n"
"v_cmp_eq_u32 vcc, s62, v172\n"
"s_nop 1\n"
"v_cndmask_b32_e64 v172, v172, 0, vcc\n"
"v_cmp_eq_u32 vcc, s62, v173\n"
"s_nop 1\n"
"v_cndmask_b32_e64 v173, v173, 0, vcc\n"
"v_cmp_eq_u32 vcc, s62, v174\n"
"s_nop 1\n"
"v_cndmask_b32_e64 v174, v174, 0, vcc\n"
"v_cmp_eq_u32 vcc, s62, v175\n"
"s_nop 1\n"
"v_cndmask_b32_e64 v175, v175, 0, vcc\n"
"v_cmp_eq_u32 vcc, s62, v176\n"
"s_nop 1\n"
"v_cndmask_b32_e64 v176, v176, 0, vcc\n"
"v_cmp_eq_u32 vcc, s62, v177\n"
"s_nop 1\n"
"v_cndmask_b32_e64 v177, v177, 0, vcc\n"
"v_cmp_eq_u32 vcc, s62, v178\n"
"s_nop 1\n"
"v_cndmask_b32_e64 v178, v178, 0, vcc\n"
"v_cmp_eq_u32 vcc, s62, v179\n"
"s_nop 1\n"
"v_cndmask_b32_e64 v179, v179, 0, vcc\n"
"v_cmp_eq_u32 vcc, s62, v180\n"
"s_nop 1\n"
"v_cndmask_b32_e64 v180, v180, 0, vcc\n"
"v_cmp_eq_u32 vcc, s62, v181\n"
"s_nop 1\n"
"v_cndmask_b32_e64 v181, v181, 0, vcc\n"
"v_cmp_eq_u32 vcc, s62, v182\n"
"s_nop 1\n"
"v_cndmask_b32_e64 v182, v182, 0, vcc\n"
"v_cmp_eq_u32 vcc, s62, v183\n"
"s_nop 1\n"
"v_cndmask_b32_e64 v183, v183, 0, vcc\n"
"v_cmp_eq_u32 vcc, s62, v184\n"
"s_nop 1\n"
"v_cndmask_b32_e64 v184, v184, 0, vcc\n"
"v_cmp_eq_u32 vcc, s62, v185\n"
"s_nop 1\n"
"v_cndmask_b32_e64 v185, v185, 0, vcc\n"
"v_cmp_eq_u32 vcc, s62, v186\n"
"s_nop 1\n"
"v_cndmask_b32_e64 v186, v186, 0, vcc\n"
"v_cmp_eq_u32 vcc, s62, v187\n"
"s_nop 1\n"
"v_cndmask_b32_e64 v187, v187, 0, vcc\n"
"v_cmp_eq_u32 vcc, s62, v188\n"
"s_nop 1\n"
"v_cndmask_b32_e64 v188, v188, 0, vcc\n"
"v_cmp_eq_u32 vcc, s62, v189\n"
"s_nop 1\n"
"v_cndmask_b32_e64 v189, v189, 0, vcc\n"
"v_cmp_eq_u32 vcc, s62, v190\n"
"s_nop 1\n"
"v_cndmask_b32_e64 v190, v190, 0, vcc\n"
"v_cmp_eq_u32 vcc, s62, v191\n"
"s_nop 1\n"
"v_cndmask_b32_e64 v191, v191, 0, vcc\n"
"v_mov_b32 v229, s62\n"
"s_lshl_b64 exec, 1, s54\n"
"v_mov_b32 v225, v229\n"
"s_mov_b64 exec, -1\n"
"s_add_u32 s54, s54, 1\n"
"s_cmp_lt_u32 s54, 8\n"
"s_cbranch_scc1 L1009_%=\n"
"s_nop 4\n"
"s_branch L1008_%=\n"
"L1010_%=:\n"
"s_mov_b32 s54, 0\n"
"L1012_%=:\n"
"s_sleep 2\n"
"buffer_load_dwordx2 v[72:73], v252, s[8:11], s50 offen sc1\n"
"s_waitcnt vmcnt(0)\n"
"s_add_u32 s54, s54, 1\n"
"v_cmp_eq_u64 vcc, s[28:29], v[72:73]\n"
"s_cmp_eq_u64 vcc, exec\n"
"s_cbranch_scc1 L1011_%=\n"
"s_cmp_lt_u32 s54, 1048576\n"
"s_cbranch_scc1 L1012_%=\n"
"s_branch L1011_%=\n"
"L1014_%=:\n"
"s_nop 15\n"
"s_lshl_b32 s89, s34, 3\n"
"s_add_u32 s89, s89, s30\n"
"s_add_u32 s89, s89, 0\n"
"s_lshl_b32 s91, s89, 4\n"
"s_lshr_b32 s89, s89, 1\n"
"v_bfe_u32 v249, v224, 4, 4\n"
"v_lshrrev_b32 v254, 8, v224\n"
"v_lshlrev_b32 v254, 2, v254\n"
"v_sub_u32 v249, v249, v254\n"
"v_add_u32 v249, s91, v249\n"
"v_mov_b32 v255, 0xff800000\n"
"v_mov_b32 v248, v255\n"
"s_mov_b32 s90, 0\n"
"s_mov_b32 s88, 0\n"
"v_mov_b32 v80, 0\n"
"v_mov_b32 v81, 0\n"
"v_mov_b32 v82, 0\n"
"v_mov_b32 v83, 0\n"
"v_mov_b32 v84, 0\n"
"v_mov_b32 v85, 0\n"
"v_mov_b32 v86, 0\n"
"v_mov_b32 v87, 0\n"
"v_mov_b32 v88, 0\n"
"v_mov_b32 v89, 0\n"
"v_mov_b32 v90, 0\n"
"v_mov_b32 v91, 0\n"
"v_mov_b32 v92, 0\n"
"v_mov_b32 v93, 0\n"
"v_mov_b32 v94, 0\n"
"v_mov_b32 v95, 0\n"
"v_mov_b32 v96, 0\n"
"v_mov_b32 v97, 0\n"
"v_mov_b32 v98, 0\n"
"v_mov_b32 v99, 0\n"
"v_mov_b32 v100, 0\n"
"v_mov_b32 v101, 0\n"
"v_mov_b32 v102, 0\n"
"v_mov_b32 v103, 0\n"
"v_mov_b32 v104, 0\n"
"v_mov_b32 v105, 0\n"
"v_mov_b32 v106, 0\n"
"v_mov_b32 v107, 0\n"
"v_mov_b32 v108, 0\n"
"v_mov_b32 v109, 0\n"
"v_mov_b32 v110, 0\n"
"v_mov_b32 v111, 0\n"
"v_accvgpr_write_b32 a132, 0\n"
"v_accvgpr_write_b32 a133, 0\n"
"v_accvgpr_write_b32 a134, 0\n"
"v_accvgpr_write_b32 a135, 0\n"
"L1015_%=:\n"
"s_lshl_b32 s91, s88, 13\n"
"s_add_u32 s91, s91, s39\n"
"s_add_u32 s91, s91, 0\n"
"buffer_load_dwordx4 a[148:151], v224, s[8:11], s91 offen offset:0 sc1\n"
"buffer_load_dwordx4 a[152:155], v224, s[8:11], s91 offen offset:1024 sc1\n"
"buffer_load_dwordx4 a[156:159], v224, s[8:11], s91 offen offset:2048 sc1\n"
"buffer_load_dwordx4 a[160:163], v224, s[8:11], s91 offen offset:3072 sc1\n"
"s_add_u32 s91, s91, 4096\n"
"buffer_load_dwordx4 a[164:167], v224, s[8:11], s91 offen offset:0 sc1\n"
"buffer_load_dwordx4 a[168:171], v224, s[8:11], s91 offen offset:1024 sc1\n"
"buffer_load_dwordx4 a[172:175], v224, s[8:11], s91 offen offset:2048 sc1\n"
"buffer_load_dwordx4 a[176:179], v224, s[8:11], s91 offen offset:3072 sc1\n"
"s_waitcnt vmcnt(0)\n"
"v_mfma_f32_16x16x32_bf16 v[240:243], a[148:151], a[0:3], 0\n"
"v_mfma_f32_16x16x32_bf16 v[240:243], a[152:155], a[4:7], v[240:243]\n"
"v_mfma_f32_16x16x32_bf16 v[240:243], a[156:159], a[8:11], v[240:243]\n"
"v_mfma_f32_16x16x32_bf16 v[240:243], a[160:163], a[12:15], v[240:243]\n"
"v_mfma_f32_16x16x32_bf16 v[244:247], a[164:167], a[0:3], 0\n"
"v_mfma_f32_16x16x32_bf16 v[244:247], a[168:171], a[4:7], v[244:247]\n"
"v_mfma_f32_16x16x32_bf16 v[244:247], a[172:175], a[8:11], v[244:247]\n"
"v_mfma_f32_16x16x32_bf16 v[244:247], a[176:179], a[12:15], v[244:247]\n"
"s_lshl_b32 s91, s88, 5\n"
"v_subrev_u32 v250, s91, v249\n"
"s_nop 7\n"
"v_cmp_gt_i32 vcc, 0, v250\n"
"s_nop 1\n"
"v_cndmask_b32 v240, v240, v255, vcc\n"
"v_cmp_gt_i32 vcc, 1, v250\n"
"s_nop 1\n"
"v_cndmask_b32 v241, v241, v255, vcc\n"
"v_cmp_gt_i32 vcc, 2, v250\n"
"s_nop 1\n"
"v_cndmask_b32 v242, v242, v255, vcc\n"
"v_cmp_gt_i32 vcc, 3, v250\n"
"s_nop 1\n"
"v_cndmask_b32 v243, v243, v255, vcc\n"
"v_cmp_gt_i32 vcc, 16, v250\n"
"s_nop 1\n"
"v_cndmask_b32 v244, v244, v255, vcc\n"
"v_cmp_gt_i32 vcc, 17, v250\n"
"s_nop 1\n"
"v_cndmask_b32 v245, v245, v255, vcc\n"
"v_cmp_gt_i32 vcc, 18, v250\n"
"s_nop 1\n"
"v_cndmask_b32 v246, v246, v255, vcc\n"
"v_cmp_gt_i32 vcc, 19, v250\n"
"s_nop 1\n"
"v_cndmask_b32 v247, v247, v255, vcc\n"
"s_cmp_lg_u32 s90, 0\n"
"s_cbranch_scc1 L1016_%=\n"
"v_max3_f32 v248, v248, v240, v241\n"
"v_max3_f32 v248, v248, v242, v243\n"
"v_max3_f32 v248, v248, v244, v245\n"
"v_max3_f32 v248, v248, v246, v247\n"
"s_branch L1017_%=\n"
"L1016_%=:\n"
"s_lshl_b32 s91, s88, 13\n"
"s_add_u32 s91, s91, s39\n"
"s_add_u32 s91, s91, 524288\n"
"buffer_load_dwordx4 a[148:151], v224, s[8:11], s91 offen offset:0 sc1\n"
"buffer_load_dwordx4 a[152:155], v224, s[8:11], s91 offen offset:1024 sc1\n"
"buffer_load_dwordx4 a[156:159], v224, s[8:11], s91 offen offset:2048 sc1\n"
"buffer_load_dwordx4 a[160:163], v224, s[8:11], s91 offen offset:3072 sc1\n"
"s_add_u32 s91, s91, 4096\n"
"buffer_load_dwordx4 a[164:167], v224, s[8:11], s91 offen offset:0 sc1\n"
"buffer_load_dwordx4 a[168:171], v224, s[8:11], s91 offen offset:1024 sc1\n"
"buffer_load_dwordx4 a[172:175], v224, s[8:11], s91 offen offset:2048 sc1\n"
"buffer_load_dwordx4 a[176:179], v224, s[8:11], s91 offen offset:3072 sc1\n"
"s_waitcnt vmcnt(0)\n"
"v_sub_f32 v240, v240, v248\n"
"v_sub_f32 v241, v241, v248\n"
"v_sub_f32 v242, v242, v248\n"
"v_sub_f32 v243, v243, v248\n"
"v_sub_f32 v244, v244, v248\n"
"v_sub_f32 v245, v245, v248\n"
"v_sub_f32 v246, v246, v248\n"
"v_sub_f32 v247, v247, v248\n"
"v_exp_f32 v240, v240\n"
"v_exp_f32 v241, v241\n"
"v_exp_f32 v242, v242\n"
"v_exp_f32 v243, v243\n"
"v_exp_f32 v244, v244\n"
"v_exp_f32 v245, v245\n"
"v_exp_f32 v246, v246\n"
"v_exp_f32 v247, v247\n"
"s_nop 0\n"
"v_cvt_pk_bf16_f32 v240, v240, v241\n"
"v_cvt_pk_bf16_f32 v241, v242, v243\n"
"v_cvt_pk_bf16_f32 v242, v244, v245\n"
"v_cvt_pk_bf16_f32 v243, v246, v247\n"
"s_nop 1\n"
"v_mfma_f32_16x16x32_bf16 v[80:83], a[148:151], v[240:243], v[80:83]\n"
"v_mfma_f32_16x16x32_bf16 v[84:87], a[152:155], v[240:243], v[84:87]\n"
"v_mfma_f32_16x16x32_bf16 v[88:91], a[156:159], v[240:243], v[88:91]\n"
"v_mfma_f32_16x16x32_bf16 v[92:95], a[160:163], v[240:243], v[92:95]\n"
"v_mfma_f32_16x16x32_bf16 v[96:99], a[164:167], v[240:243], v[96:99]\n"
"v_mfma_f32_16x16x32_bf16 v[100:103], a[168:171], v[240:243], v[100:103]\n"
"v_mfma_f32_16x16x32_bf16 v[104:107], a[172:175], v[240:243], v[104:107]\n"
"v_mfma_f32_16x16x32_bf16 v[108:111], a[176:179], v[240:243], v[108:111]\n"
"v_mfma_f32_16x16x32_bf16 a[132:135], a[128:131], v[240:243], a[132:135]\n"
"s_nop 7\n"
"L1017_%=:\n"
"s_add_u32 s88, s88, 1\n"
"s_cmp_le_u32 s88, s89\n"
"s_cbranch_scc1 L1015_%=\n"
"s_cmp_lg_u32 s90, 0\n"
"s_cbranch_scc1 L1018_%=\n"
"v_mov_b32 v254, v248\n"
"s_nop 1\n"
"v_permlane16_swap_b32 v254, v248\n"
"s_nop 1\n"
"v_max_f32 v248, v248, v254\n"
"v_mov_b32 v254, v248\n"
"s_nop 1\n"
"v_permlane32_swap_b32 v254, v248\n"
"s_nop 1\n"
"v_max_f32 v248, v248, v254\n"
"s_mov_b32 s90, 1\n"
"s_mov_b32 s88, 0\n"
"s_branch L1015_%=\n"
"L1018_%=:\n"
"s_nop 7\n"
"v_accvgpr_read_b32 v251, a132\n"
"s_nop 1\n"
"s_branch L1013_%=\n"
"L1019_%=:\n"
"s_mov_b32 s54, 0\n"
"L1021_%=:\n"
"s_sleep 2\n"
"buffer_load_dwordx2 v[236:237], v235, s[8:11], s50 offen sc1\n"
"s_waitcnt vmcnt(0)\n"
"s_add_u32 s54, s54, 1\n"
"v_cmp_eq_u64 vcc, s[28:29], v[236:237]\n"
"s_cmp_eq_u64 vcc, exec\n"
"s_cbranch_scc1 L1020_%=\n"
"s_cmp_lt_u32 s54, 1048576\n"
"s_cbranch_scc1 L1021_%=\n"
"s_branch L1020_%=\n"
"L1023_%=:\n"
"s_nop 15\n"
"s_lshl_b32 s89, s34, 3\n"
"s_add_u32 s89, s89, s30\n"
"s_add_u32 s89, s89, 4\n"
"s_lshl_b32 s91, s89, 4\n"
"s_lshr_b32 s89, s89, 1\n"
"v_bfe_u32 v249, v224, 4, 4\n"
"v_lshrrev_b32 v254, 8, v224\n"
"v_lshlrev_b32 v254, 2, v254\n"
"v_sub_u32 v249, v249, v254\n"
"v_add_u32 v249, s91, v249\n"
"v_mov_b32 v255, 0xff800000\n"
"v_mov_b32 v248, v255\n"
"s_mov_b32 s90, 0\n"
"s_mov_b32 s88, 0\n"
"v_mov_b32 v112, 0\n"
"v_mov_b32 v113, 0\n"
"v_mov_b32 v114, 0\n"
"v_mov_b32 v115, 0\n"
"v_mov_b32 v116, 0\n"
"v_mov_b32 v117, 0\n"
"v_mov_b32 v118, 0\n"
"v_mov_b32 v119, 0\n"
"v_mov_b32 v120, 0\n"
"v_mov_b32 v121, 0\n"
"v_mov_b32 v122, 0\n"
"v_mov_b32 v123, 0\n"
"v_mov_b32 v124, 0\n"
"v_mov_b32 v125, 0\n"
"v_mov_b32 v126, 0\n"
"v_mov_b32 v127, 0\n"
"v_mov_b32 v128, 0\n"
"v_mov_b32 v129, 0\n"
"v_mov_b32 v130, 0\n"
"v_mov_b32 v131, 0\n"
"v_mov_b32 v132, 0\n"
"v_mov_b32 v133, 0\n"
"v_mov_b32 v134, 0\n"
"v_mov_b32 v135, 0\n"
"v_mov_b32 v136, 0\n"
"v_mov_b32 v137, 0\n"
"v_mov_b32 v138, 0\n"
"v_mov_b32 v139, 0\n"
"v_mov_b32 v140, 0\n"
"v_mov_b32 v141, 0\n"
"v_mov_b32 v142, 0\n"
"v_mov_b32 v143, 0\n"
"v_accvgpr_write_b32 a136, 0\n"
"v_accvgpr_write_b32 a137, 0\n"
"v_accvgpr_write_b32 a138, 0\n"
"v_accvgpr_write_b32 a139, 0\n"
"L1024_%=:\n"
"s_lshl_b32 s91, s88, 13\n"
"s_add_u32 s91, s91, s39\n"
"s_add_u32 s91, s91, 0\n"
"buffer_load_dwordx4 a[148:151], v224, s[8:11], s91 offen offset:0 sc1\n"
"buffer_load_dwordx4 a[152:155], v224, s[8:11], s91 offen offset:1024 sc1\n"
"buffer_load_dwordx4 a[156:159], v224, s[8:11], s91 offen offset:2048 sc1\n"
"buffer_load_dwordx4 a[160:163], v224, s[8:11], s91 offen offset:3072 sc1\n"
"s_add_u32 s91, s91, 4096\n"
"buffer_load_dwordx4 a[164:167], v224, s[8:11], s91 offen offset:0 sc1\n"
"buffer_load_dwordx4 a[168:171], v224, s[8:11], s91 offen offset:1024 sc1\n"
"buffer_load_dwordx4 a[172:175], v224, s[8:11], s91 offen offset:2048 sc1\n"
"buffer_load_dwordx4 a[176:179], v224, s[8:11], s91 offen offset:3072 sc1\n"
"s_waitcnt vmcnt(0)\n"
"v_mfma_f32_16x16x32_bf16 v[240:243], a[148:151], a[16:19], 0\n"
"v_mfma_f32_16x16x32_bf16 v[240:243], a[152:155], a[20:23], v[240:243]\n"
"v_mfma_f32_16x16x32_bf16 v[240:243], a[156:159], a[24:27], v[240:243]\n"
"v_mfma_f32_16x16x32_bf16 v[240:243], a[160:163], a[28:31], v[240:243]\n"
"v_mfma_f32_16x16x32_bf16 v[244:247], a[164:167], a[16:19], 0\n"
"v_mfma_f32_16x16x32_bf16 v[244:247], a[168:171], a[20:23], v[244:247]\n"
"v_mfma_f32_16x16x32_bf16 v[244:247], a[172:175], a[24:27], v[244:247]\n"
"v_mfma_f32_16x16x32_bf16 v[244:247], a[176:179], a[28:31], v[244:247]\n"
"s_lshl_b32 s91, s88, 5\n"
"v_subrev_u32 v250, s91, v249\n"
"s_nop 7\n"
"v_cmp_gt_i32 vcc, 0, v250\n"
"s_nop 1\n"
"v_cndmask_b32 v240, v240, v255, vcc\n"
"v_cmp_gt_i32 vcc, 1, v250\n"
"s_nop 1\n"
"v_cndmask_b32 v241, v241, v255, vcc\n"
"v_cmp_gt_i32 vcc, 2, v250\n"
"s_nop 1\n"
"v_cndmask_b32 v242, v242, v255, vcc\n"
"v_cmp_gt_i32 vcc, 3, v250\n"
"s_nop 1\n"
"v_cndmask_b32 v243, v243, v255, vcc\n"
"v_cmp_gt_i32 vcc, 16, v250\n"
"s_nop 1\n"
"v_cndmask_b32 v244, v244, v255, vcc\n"
"v_cmp_gt_i32 vcc, 17, v250\n"
"s_nop 1\n"
"v_cndmask_b32 v245, v245, v255, vcc\n"
"v_cmp_gt_i32 vcc, 18, v250\n"
"s_nop 1\n"
"v_cndmask_b32 v246, v246, v255, vcc\n"
"v_cmp_gt_i32 vcc, 19, v250\n"
"s_nop 1\n"
"v_cndmask_b32 v247, v247, v255, vcc\n"
"s_cmp_lg_u32 s90, 0\n"
"s_cbranch_scc1 L1025_%=\n"
"v_max3_f32 v248, v248, v240, v241\n"
"v_max3_f32 v248, v248, v242, v243\n"
"v_max3_f32 v248, v248, v244, v245\n"
"v_max3_f32 v248, v248, v246, v247\n"
"s_branch L1026_%=\n"
"L1025_%=:\n"
"s_lshl_b32 s91, s88, 13\n"
"s_add_u32 s91, s91, s39\n"
"s_add_u32 s91, s91, 524288\n"
"buffer_load_dwordx4 a[148:151], v224, s[8:11], s91 offen offset:0 sc1\n"
"buffer_load_dwordx4 a[152:155], v224, s[8:11], s91 offen offset:1024 sc1\n"
"buffer_load_dwordx4 a[156:159], v224, s[8:11], s91 offen offset:2048 sc1\n"
"buffer_load_dwordx4 a[160:163], v224, s[8:11], s91 offen offset:3072 sc1\n"
"s_add_u32 s91, s91, 4096\n"
"buffer_load_dwordx4 a[164:167], v224, s[8:11], s91 offen offset:0 sc1\n"
"buffer_load_dwordx4 a[168:171], v224, s[8:11], s91 offen offset:1024 sc1\n"
"buffer_load_dwordx4 a[172:175], v224, s[8:11], s91 offen offset:2048 sc1\n"
"buffer_load_dwordx4 a[176:179], v224, s[8:11], s91 offen offset:3072 sc1\n"
"s_waitcnt vmcnt(0)\n"
"v_sub_f32 v240, v240, v248\n"
"v_sub_f32 v241, v241, v248\n"
"v_sub_f32 v242, v242, v248\n"
"v_sub_f32 v243, v243, v248\n"
"v_sub_f32 v244, v244, v248\n"
"v_sub_f32 v245, v245, v248\n"
"v_sub_f32 v246, v246, v248\n"
"v_sub_f32 v247, v247, v248\n"
"v_exp_f32 v240, v240\n"
"v_exp_f32 v241, v241\n"
"v_exp_f32 v242, v242\n"
"v_exp_f32 v243, v243\n"
"v_exp_f32 v244, v244\n"
"v_exp_f32 v245, v245\n"
"v_exp_f32 v246, v246\n"
"v_exp_f32 v247, v247\n"
"s_nop 0\n"
"v_cvt_pk_bf16_f32 v240, v240, v241\n"
"v_cvt_pk_bf16_f32 v241, v242, v243\n"
"v_cvt_pk_bf16_f32 v242, v244, v245\n"
"v_cvt_pk_bf16_f32 v243, v246, v247\n"
"s_nop 1\n"
"v_mfma_f32_16x16x32_bf16 v[112:115], a[148:151], v[240:243], v[112:115]\n"
"v_mfma_f32_16x16x32_bf16 v[116:119], a[152:155], v[240:243], v[116:119]\n"
"v_mfma_f32_16x16x32_bf16 v[120:123], a[156:159], v[240:243], v[120:123]\n"
"v_mfma_f32_16x16x32_bf16 v[124:127], a[160:163], v[240:243], v[124:127]\n"
"v_mfma_f32_16x16x32_bf16 v[128:131], a[164:167], v[240:243], v[128:131]\n"
"v_mfma_f32_16x16x32_bf16 v[132:135], a[168:171], v[240:243], v[132:135]\n"
"v_mfma_f32_16x16x32_bf16 v[136:139], a[172:175], v[240:243], v[136:139]\n"
"v_mfma_f32_16x16x32_bf16 v[140:143], a[176:179], v[240:243], v[140:143]\n"
"v_mfma_f32_16x16x32_bf16 a[136:139], a[128:131], v[240:243], a[136:139]\n"
"s_nop 7\n"
"L1026_%=:\n"
"s_add_u32 s88, s88, 1\n"
"s_cmp_le_u32 s88, s89\n"
"s_cbranch_scc1 L1024_%=\n"
"s_cmp_lg_u32 s90, 0\n"
"s_cbranch_scc1 L1027_%=\n"
"v_mov_b32 v254, v248\n"
"s_nop 1\n"
"v_permlane16_swap_b32 v254, v248\n"
"s_nop 1\n"
"v_max_f32 v248, v248, v254\n"
"v_mov_b32 v254, v248\n"
"s_nop 1\n"
"v_permlane32_swap_b32 v254, v248\n"
"s_nop 1\n"
"v_max_f32 v248, v248, v254\n"
"s_mov_b32 s90, 1\n"
"s_mov_b32 s88, 0\n"
"s_branch L1024_%=\n"
"L1027_%=:\n"
"s_nop 7\n"
"v_accvgpr_read_b32 v252, a136\n"
"s_nop 1\n"
"s_branch L1022_%=\n"
"L1029_%=:\n"
"s_nop 15\n"
"s_lshl_b32 s89, s34, 3\n"
"s_sub_u32 s89, 120, s89\n"
"s_add_u32 s89, s89, s30\n"
"s_add_u32 s89, s89, 0\n"
"s_lshl_b32 s91, s89, 4\n"
"s_lshr_b32 s89, s89, 1\n"
"v_bfe_u32 v249, v224, 4, 4\n"
"v_lshrrev_b32 v254, 8, v224\n"
"v_lshlrev_b32 v254, 2, v254\n"
"v_sub_u32 v249, v249, v254\n"
"v_add_u32 v249, s91, v249\n"
"v_mov_b32 v255, 0xff800000\n"
"v_mov_b32 v248, v255\n"
"s_mov_b32 s90, 0\n"
"s_mov_b32 s88, 0\n"
"v_mov_b32 v144, 0\n"
"v_mov_b32 v145, 0\n"
"v_mov_b32 v146, 0\n"
"v_mov_b32 v147, 0\n"
"v_mov_b32 v148, 0\n"
"v_mov_b32 v149, 0\n"
"v_mov_b32 v150, 0\n"
"v_mov_b32 v151, 0\n"
"v_mov_b32 v152, 0\n"
"v_mov_b32 v153, 0\n"
"v_mov_b32 v154, 0\n"
"v_mov_b32 v155, 0\n"
"v_mov_b32 v156, 0\n"
"v_mov_b32 v157, 0\n"
"v_mov_b32 v158, 0\n"
"v_mov_b32 v159, 0\n"
"v_mov_b32 v160, 0\n"
"v_mov_b32 v161, 0\n"
"v_mov_b32 v162, 0\n"
"v_mov_b32 v163, 0\n"
"v_mov_b32 v164, 0\n"
"v_mov_b32 v165, 0\n"
"v_mov_b32 v166, 0\n"
"v_mov_b32 v167, 0\n"
"v_mov_b32 v168, 0\n"
"v_mov_b32 v169, 0\n"
"v_mov_b32 v170, 0\n"
"v_mov_b32 v171, 0\n"
"v_mov_b32 v172, 0\n"
"v_mov_b32 v173, 0\n"
"v_mov_b32 v174, 0\n"
"v_mov_b32 v175, 0\n"
"v_accvgpr_write_b32 a140, 0\n"
"v_accvgpr_write_b32 a141, 0\n"
"v_accvgpr_write_b32 a142, 0\n"
"v_accvgpr_write_b32 a143, 0\n"
"L1030_%=:\n"
"s_lshl_b32 s91, s88, 13\n"
"s_add_u32 s91, s91, s39\n"
"s_add_u32 s91, s91, 0\n"
"buffer_load_dwordx4 a[148:151], v224, s[8:11], s91 offen offset:0 sc1\n"
"buffer_load_dwordx4 a[152:155], v224, s[8:11], s91 offen offset:1024 sc1\n"
"buffer_load_dwordx4 a[156:159], v224, s[8:11], s91 offen offset:2048 sc1\n"
"buffer_load_dwordx4 a[160:163], v224, s[8:11], s91 offen offset:3072 sc1\n"
"s_add_u32 s91, s91, 4096\n"
"buffer_load_dwordx4 a[164:167], v224, s[8:11], s91 offen offset:0 sc1\n"
"buffer_load_dwordx4 a[168:171], v224, s[8:11], s91 offen offset:1024 sc1\n"
"buffer_load_dwordx4 a[172:175], v224, s[8:11], s91 offen offset:2048 sc1\n"
"buffer_load_dwordx4 a[176:179], v224, s[8:11], s91 offen offset:3072 sc1\n"
"s_waitcnt vmcnt(0)\n"
"v_mfma_f32_16x16x32_bf16 v[240:243], a[148:151], a[32:35], 0\n"
"v_mfma_f32_16x16x32_bf16 v[240:243], a[152:155], a[36:39], v[240:243]\n"
"v_mfma_f32_16x16x32_bf16 v[240:243], a[156:159], a[40:43], v[240:243]\n"
"v_mfma_f32_16x16x32_bf16 v[240:243], a[160:163], a[44:47], v[240:243]\n"
"v_mfma_f32_16x16x32_bf16 v[244:247], a[164:167], a[32:35], 0\n"
"v_mfma_f32_16x16x32_bf16 v[244:247], a[168:171], a[36:39], v[244:247]\n"
"v_mfma_f32_16x16x32_bf16 v[244:247], a[172:175], a[40:43], v[244:247]\n"
"v_mfma_f32_16x16x32_bf16 v[244:247], a[176:179], a[44:47], v[244:247]\n"
"s_lshl_b32 s91, s88, 5\n"
"v_subrev_u32 v250, s91, v249\n"
"s_nop 7\n"
"v_cmp_gt_i32 vcc, 0, v250\n"
"s_nop 1\n"
"v_cndmask_b32 v240, v240, v255, vcc\n"
"v_cmp_gt_i32 vcc, 1, v250\n"
"s_nop 1\n"
"v_cndmask_b32 v241, v241, v255, vcc\n"
"v_cmp_gt_i32 vcc, 2, v250\n"
"s_nop 1\n"
"v_cndmask_b32 v242, v242, v255, vcc\n"
"v_cmp_gt_i32 vcc, 3, v250\n"
"s_nop 1\n"
"v_cndmask_b32 v243, v243, v255, vcc\n"
"v_cmp_gt_i32 vcc, 16, v250\n"
"s_nop 1\n"
"v_cndmask_b32 v244, v244, v255, vcc\n"
"v_cmp_gt_i32 vcc, 17, v250\n"
"s_nop 1\n"
"v_cndmask_b32 v245, v245, v255, vcc\n"
"v_cmp_gt_i32 vcc, 18, v250\n"
"s_nop 1\n"
"v_cndmask_b32 v246, v246, v255, vcc\n"
"v_cmp_gt_i32 vcc, 19, v250\n"
"s_nop 1\n"
"v_cndmask_b32 v247, v247, v255, vcc\n"
"s_cmp_lg_u32 s90, 0\n"
"s_cbranch_scc1 L1031_%=\n"
"v_max3_f32 v248, v248, v240, v241\n"
"v_max3_f32 v248, v248, v242, v243\n"
"v_max3_f32 v248, v248, v244, v245\n"
"v_max3_f32 v248, v248, v246, v247\n"
"s_branch L1032_%=\n"
"L1031_%=:\n"
"s_lshl_b32 s91, s88, 13\n"
"s_add_u32 s91, s91, s39\n"
"s_add_u32 s91, s91, 524288\n"
"buffer_load_dwordx4 a[148:151], v224, s[8:11], s91 offen offset:0 sc1\n"
"buffer_load_dwordx4 a[152:155], v224, s[8:11], s91 offen offset:1024 sc1\n"
"buffer_load_dwordx4 a[156:159], v224, s[8:11], s91 offen offset:2048 sc1\n"
"buffer_load_dwordx4 a[160:163], v224, s[8:11], s91 offen offset:3072 sc1\n"
"s_add_u32 s91, s91, 4096\n"
"buffer_load_dwordx4 a[164:167], v224, s[8:11], s91 offen offset:0 sc1\n"
"buffer_load_dwordx4 a[168:171], v224, s[8:11], s91 offen offset:1024 sc1\n"
"buffer_load_dwordx4 a[172:175], v224, s[8:11], s91 offen offset:2048 sc1\n"
"buffer_load_dwordx4 a[176:179], v224, s[8:11], s91 offen offset:3072 sc1\n"
"s_waitcnt vmcnt(0)\n"
"v_sub_f32 v240, v240, v248\n"
"v_sub_f32 v241, v241, v248\n"
"v_sub_f32 v242, v242, v248\n"
"v_sub_f32 v243, v243, v248\n"
"v_sub_f32 v244, v244, v248\n"
"v_sub_f32 v245, v245, v248\n"
"v_sub_f32 v246, v246, v248\n"
"v_sub_f32 v247, v247, v248\n"
"v_exp_f32 v240, v240\n"
"v_exp_f32 v241, v241\n"
"v_exp_f32 v242, v242\n"
"v_exp_f32 v243, v243\n"
"v_exp_f32 v244, v244\n"
"v_exp_f32 v245, v245\n"
"v_exp_f32 v246, v246\n"
"v_exp_f32 v247, v247\n"
"s_nop 0\n"
"v_cvt_pk_bf16_f32 v240, v240, v241\n"
"v_cvt_pk_bf16_f32 v241, v242, v243\n"
"v_cvt_pk_bf16_f32 v242, v244, v245\n"
"v_cvt_pk_bf16_f32 v243, v246, v247\n"
"s_nop 1\n"
"v_mfma_f32_16x16x32_bf16 v[144:147], a[148:151], v[240:243], v[144:147]\n"
"v_mfma_f32_16x16x32_bf16 v[148:151], a[152:155], v[240:243], v[148:151]\n"
"v_mfma_f32_16x16x32_bf16 v[152:155], a[156:159], v[240:243], v[152:155]\n"
"v_mfma_f32_16x16x32_bf16 v[156:159], a[160:163], v[240:243], v[156:159]\n"
"v_mfma_f32_16x16x32_bf16 v[160:163], a[164:167], v[240:243], v[160:163]\n"
"v_mfma_f32_16x16x32_bf16 v[164:167], a[168:171], v[240:243], v[164:167]\n"
"v_mfma_f32_16x16x32_bf16 v[168:171], a[172:175], v[240:243], v[168:171]\n"
"v_mfma_f32_16x16x32_bf16 v[172:175], a[176:179], v[240:243], v[172:175]\n"
"v_mfma_f32_16x16x32_bf16 a[140:143], a[128:131], v[240:243], a[140:143]\n"
"s_nop 7\n"
"L1032_%=:\n"
"s_add_u32 s88, s88, 1\n"
"s_cmp_le_u32 s88, s89\n"
"s_cbranch_scc1 L1030_%=\n"
"s_cmp_lg_u32 s90, 0\n"
"s_cbranch_scc1 L1033_%=\n"
"v_mov_b32 v254, v248\n"
"s_nop 1\n"
"v_permlane16_swap_b32 v254, v248\n"
"s_nop 1\n"
"v_max_f32 v248, v248, v254\n"
"v_mov_b32 v254, v248\n"
"s_nop 1\n"
"v_permlane32_swap_b32 v254, v248\n"
"s_nop 1\n"
"v_max_f32 v248, v248, v254\n"
"s_mov_b32 s90, 1\n"
"s_mov_b32 s88, 0\n"
"s_branch L1030_%=\n"
"L1033_%=:\n"
"s_nop 7\n"
"v_accvgpr_read_b32 v251, a140\n"
"s_nop 1\n"
"s_branch L1028_%=\n"
"L1035_%=:\n"
"s_nop 15\n"
"s_lshl_b32 s89, s34, 3\n"
"s_add_u32 s89, s89, s30\n"
"s_add_u32 s89, s89, 4\n"
"s_lshl_b32 s91, s89, 4\n"
"s_lshr_b32 s89, s89, 1\n"
"v_bfe_u32 v249, v224, 4, 4\n"
"v_lshrrev_b32 v254, 8, v224\n"
"v_lshlrev_b32 v254, 2, v254\n"
"v_sub_u32 v249, v249, v254\n"
"v_add_u32 v249, s91, v249\n"
"v_mov_b32 v255, 0xff800000\n"
"v_mov_b32 v248, v255\n"
"s_mov_b32 s90, 0\n"
"s_mov_b32 s88, 0\n"
"v_mov_b32 v112, 0\n"
"v_mov_b32 v113, 0\n"
"v_mov_b32 v114, 0\n"
"v_mov_b32 v115, 0\n"
"v_mov_b32 v116, 0\n"
"v_mov_b32 v117, 0\n"
"v_mov_b32 v118, 0\n"
"v_mov_b32 v119, 0\n"
"v_mov_b32 v120, 0\n"
"v_mov_b32 v121, 0\n"
"v_mov_b32 v122, 0\n"
"v_mov_b32 v123, 0\n"
"v_mov_b32 v124, 0\n"
"v_mov_b32 v125, 0\n"
"v_mov_b32 v126, 0\n"
"v_mov_b32 v127, 0\n"
"v_mov_b32 v128, 0\n"
"v_mov_b32 v129, 0\n"
"v_mov_b32 v130, 0\n"
"v_mov_b32 v131, 0\n"
"v_mov_b32 v132, 0\n"
"v_mov_b32 v133, 0\n"
"v_mov_b32 v134, 0\n"
"v_mov_b32 v135, 0\n"
"v_mov_b32 v136, 0\n"
"v_mov_b32 v137, 0\n"
"v_mov_b32 v138, 0\n"
"v_mov_b32 v139, 0\n"
"v_mov_b32 v140, 0\n"
"v_mov_b32 v141, 0\n"
"v_mov_b32 v142, 0\n"
"v_mov_b32 v143, 0\n"
"v_accvgpr_write_b32 a136, 0\n"
"v_accvgpr_write_b32 a137, 0\n"
"v_accvgpr_write_b32 a138, 0\n"
"v_accvgpr_write_b32 a139, 0\n"
"L1036_%=:\n"
"s_lshl_b32 s91, s88, 13\n"
"s_add_u32 s91, s91, s39\n"
"s_add_u32 s91, s91, 0\n"
"buffer_load_dwordx4 a[148:151], v224, s[8:11], s91 offen offset:0 sc1\n"
"buffer_load_dwordx4 a[152:155], v224, s[8:11], s91 offen offset:1024 sc1\n"
"buffer_load_dwordx4 a[156:159], v224, s[8:11], s91 offen offset:2048 sc1\n"
"buffer_load_dwordx4 a[160:163], v224, s[8:11], s91 offen offset:3072 sc1\n"
"s_add_u32 s91, s91, 4096\n"
"buffer_load_dwordx4 a[164:167], v224, s[8:11], s91 offen offset:0 sc1\n"
"buffer_load_dwordx4 a[168:171], v224, s[8:11], s91 offen offset:1024 sc1\n"
"buffer_load_dwordx4 a[172:175], v224, s[8:11], s91 offen offset:2048 sc1\n"
"buffer_load_dwordx4 a[176:179], v224, s[8:11], s91 offen offset:3072 sc1\n"
"s_waitcnt vmcnt(0)\n"
"v_mfma_f32_16x16x32_bf16 v[240:243], a[148:151], a[16:19], 0\n"
"v_mfma_f32_16x16x32_bf16 v[240:243], a[152:155], a[20:23], v[240:243]\n"
"v_mfma_f32_16x16x32_bf16 v[240:243], a[156:159], a[24:27], v[240:243]\n"
"v_mfma_f32_16x16x32_bf16 v[240:243], a[160:163], a[28:31], v[240:243]\n"
"v_mfma_f32_16x16x32_bf16 v[244:247], a[164:167], a[16:19], 0\n"
"v_mfma_f32_16x16x32_bf16 v[244:247], a[168:171], a[20:23], v[244:247]\n"
"v_mfma_f32_16x16x32_bf16 v[244:247], a[172:175], a[24:27], v[244:247]\n"
"v_mfma_f32_16x16x32_bf16 v[244:247], a[176:179], a[28:31], v[244:247]\n"
"s_lshl_b32 s91, s88, 5\n"
"v_subrev_u32 v250, s91, v249\n"
"s_nop 7\n"
"v_cmp_gt_i32 vcc, 0, v250\n"
"s_nop 1\n"
"v_cndmask_b32 v240, v240, v255, vcc\n"
"v_cmp_gt_i32 vcc, 1, v250\n"
"s_nop 1\n"
"v_cndmask_b32 v241, v241, v255, vcc\n"
"v_cmp_gt_i32 vcc, 2, v250\n"
"s_nop 1\n"
"v_cndmask_b32 v242, v242, v255, vcc\n"
"v_cmp_gt_i32 vcc, 3, v250\n"
"s_nop 1\n"
"v_cndmask_b32 v243, v243, v255, vcc\n"
"v_cmp_gt_i32 vcc, 16, v250\n"
"s_nop 1\n"
"v_cndmask_b32 v244, v244, v255, vcc\n"
"v_cmp_gt_i32 vcc, 17, v250\n"
"s_nop 1\n"
"v_cndmask_b32 v245, v245, v255, vcc\n"
"v_cmp_gt_i32 vcc, 18, v250\n"
"s_nop 1\n"
"v_cndmask_b32 v246, v246, v255, vcc\n"
"v_cmp_gt_i32 vcc, 19, v250\n"
"s_nop 1\n"
"v_cndmask_b32 v247, v247, v255, vcc\n"
"s_cmp_lg_u32 s90, 0\n"
"s_cbranch_scc1 L1037_%=\n"
"v_max3_f32 v248, v248, v240, v241\n"
"v_max3_f32 v248, v248, v242, v243\n"
"v_max3_f32 v248, v248, v244, v245\n"
"v_max3_f32 v248, v248, v246, v247\n"
"s_branch L1038_%=\n"
"L1037_%=:\n"
"s_lshl_b32 s91, s88, 13\n"
"s_add_u32 s91, s91, s39\n"
"s_add_u32 s91, s91, 524288\n"
"buffer_load_dwordx4 a[148:151], v224, s[8:11], s91 offen offset:0 sc1\n"
"buffer_load_dwordx4 a[152:155], v224, s[8:11], s91 offen offset:1024 sc1\n"
"buffer_load_dwordx4 a[156:159], v224, s[8:11], s91 offen offset:2048 sc1\n"
"buffer_load_dwordx4 a[160:163], v224, s[8:11], s91 offen offset:3072 sc1\n"
"s_add_u32 s91, s91, 4096\n"
"buffer_load_dwordx4 a[164:167], v224, s[8:11], s91 offen offset:0 sc1\n"
"buffer_load_dwordx4 a[168:171], v224, s[8:11], s91 offen offset:1024 sc1\n"
"buffer_load_dwordx4 a[172:175], v224, s[8:11], s91 offen offset:2048 sc1\n"
"buffer_load_dwordx4 a[176:179], v224, s[8:11], s91 offen offset:3072 sc1\n"
"s_waitcnt vmcnt(0)\n"
"v_sub_f32 v240, v240, v248\n"
"v_sub_f32 v241, v241, v248\n"
"v_sub_f32 v242, v242, v248\n"
"v_sub_f32 v243, v243, v248\n"
"v_sub_f32 v244, v244, v248\n"
"v_sub_f32 v245, v245, v248\n"
"v_sub_f32 v246, v246, v248\n"
"v_sub_f32 v247, v247, v248\n"
"v_exp_f32 v240, v240\n"
"v_exp_f32 v241, v241\n"
"v_exp_f32 v242, v242\n"
"v_exp_f32 v243, v243\n"
"v_exp_f32 v244, v244\n"
"v_exp_f32 v245, v245\n"
"v_exp_f32 v246, v246\n"
"v_exp_f32 v247, v247\n"
"s_nop 0\n"
"v_cvt_pk_bf16_f32 v240, v240, v241\n"
"v_cvt_pk_bf16_f32 v241, v242, v243\n"
"v_cvt_pk_bf16_f32 v242, v244, v245\n"
"v_cvt_pk_bf16_f32 v243, v246, v247\n"
"s_nop 1\n"
"v_mfma_f32_16x16x32_bf16 v[112:115], a[148:151], v[240:243], v[112:115]\n"
"v_mfma_f32_16x16x32_bf16 v[116:119], a[152:155], v[240:243], v[116:119]\n"
"v_mfma_f32_16x16x32_bf16 v[120:123], a[156:159], v[240:243], v[120:123]\n"
"v_mfma_f32_16x16x32_bf16 v[124:127], a[160:163], v[240:243], v[124:127]\n"
"v_mfma_f32_16x16x32_bf16 v[128:131], a[164:167], v[240:243], v[128:131]\n"
"v_mfma_f32_16x16x32_bf16 v[132:135], a[168:171], v[240:243], v[132:135]\n"
"v_mfma_f32_16x16x32_bf16 v[136:139], a[172:175], v[240:243], v[136:139]\n"
"v_mfma_f32_16x16x32_bf16 v[140:143], a[176:179], v[240:243], v[140:143]\n"
"v_mfma_f32_16x16x32_bf16 a[136:139], a[128:131], v[240:243], a[136:139]\n"
"s_nop 7\n"
"L1038_%=:\n"
"s_add_u32 s88, s88, 1\n"
"s_cmp_le_u32 s88, s89\n"
"s_cbranch_scc1 L1036_%=\n"
"s_cmp_lg_u32 s90, 0\n"
"s_cbranch_scc1 L1039_%=\n"
"v_mov_b32 v254, v248\n"
"s_nop 1\n"
"v_permlane16_swap_b32 v254, v248\n"
"s_nop 1\n"
"v_max_f32 v248, v248, v254\n"
"v_mov_b32 v254, v248\n"
"s_nop 1\n"
"v_permlane32_swap_b32 v254, v248\n"
"s_nop 1\n"
"v_max_f32 v248, v248, v254\n"
"s_mov_b32 s90, 1\n"
"s_mov_b32 s88, 0\n"
"s_branch L1036_%=\n"
"L1039_%=:\n"
"s_nop 7\n"
"v_accvgpr_read_b32 v252, a136\n"
"s_nop 1\n"
"s_branch L1034_%=\n"
"L1041_%=:\n"
"s_nop 15\n"
"s_lshl_b32 s89, s34, 3\n"
"s_sub_u32 s89, 120, s89\n"
"s_add_u32 s89, s89, s30\n"
"s_add_u32 s89, s89, 0\n"
"s_lshl_b32 s91, s89, 4\n"
"s_lshr_b32 s89, s89, 1\n"
"v_bfe_u32 v249, v224, 4, 4\n"
"v_lshrrev_b32 v254, 8, v224\n"
"v_lshlrev_b32 v254, 2, v254\n"
"v_sub_u32 v249, v249, v254\n"
"v_add_u32 v249, s91, v249\n"
"v_mov_b32 v255, 0xff800000\n"
"v_mov_b32 v248, v255\n"
"s_mov_b32 s90, 0\n"
"s_mov_b32 s88, 0\n"
"v_mov_b32 v144, 0\n"
"v_mov_b32 v145, 0\n"
"v_mov_b32 v146, 0\n"
"v_mov_b32 v147, 0\n"
"v_mov_b32 v148, 0\n"
"v_mov_b32 v149, 0\n"
"v_mov_b32 v150, 0\n"
"v_mov_b32 v151, 0\n"
"v_mov_b32 v152, 0\n"
"v_mov_b32 v153, 0\n"
"v_mov_b32 v154, 0\n"
"v_mov_b32 v155, 0\n"
"v_mov_b32 v156, 0\n"
"v_mov_b32 v157, 0\n"
"v_mov_b32 v158, 0\n"
"v_mov_b32 v159, 0\n"
"v_mov_b32 v160, 0\n"
"v_mov_b32 v161, 0\n"
"v_mov_b32 v162, 0\n"
"v_mov_b32 v163, 0\n"
"v_mov_b32 v164, 0\n"
"v_mov_b32 v165, 0\n"
"v_mov_b32 v166, 0\n"
"v_mov_b32 v167, 0\n"
"v_mov_b32 v168, 0\n"
"v_mov_b32 v169, 0\n"
"v_mov_b32 v170, 0\n"
"v_mov_b32 v171, 0\n"
"v_mov_b32 v172, 0\n"
"v_mov_b32 v173, 0\n"
"v_mov_b32 v174, 0\n"
"v_mov_b32 v175, 0\n"
"v_accvgpr_write_b32 a140, 0\n"
"v_accvgpr_write_b32 a141, 0\n"
"v_accvgpr_write_b32 a142, 0\n"
"v_accvgpr_write_b32 a143, 0\n"
"L1042_%=:\n"
"s_lshl_b32 s91, s88, 13\n"
"s_add_u32 s91, s91, s39\n"
"s_add_u32 s91, s91, 0\n"
"buffer_load_dwordx4 a[148:151], v224, s[8:11], s91 offen offset:0 sc1\n"
"buffer_load_dwordx4 a[152:155], v224, s[8:11], s91 offen offset:1024 sc1\n"
"buffer_load_dwordx4 a[156:159], v224, s[8:11], s91 offen offset:2048 sc1\n"
"buffer_load_dwordx4 a[160:163], v224, s[8:11], s91 offen offset:3072 sc1\n"
"s_add_u32 s91, s91, 4096\n"
"buffer_load_dwordx4 a[164:167], v224, s[8:11], s91 offen offset:0 sc1\n"
"buffer_load_dwordx4 a[168:171], v224, s[8:11], s91 offen offset:1024 sc1\n"
"buffer_load_dwordx4 a[172:175], v224, s[8:11], s91 offen offset:2048 sc1\n"
"buffer_load_dwordx4 a[176:179], v224, s[8:11], s91 offen offset:3072 sc1\n"
"s_waitcnt vmcnt(0)\n"
"v_mfma_f32_16x16x32_bf16 v[240:243], a[148:151], a[32:35], 0\n"
"v_mfma_f32_16x16x32_bf16 v[240:243], a[152:155], a[36:39], v[240:243]\n"
"v_mfma_f32_16x16x32_bf16 v[240:243], a[156:159], a[40:43], v[240:243]\n"
"v_mfma_f32_16x16x32_bf16 v[240:243], a[160:163], a[44:47], v[240:243]\n"
"v_mfma_f32_16x16x32_bf16 v[244:247], a[164:167], a[32:35], 0\n"
"v_mfma_f32_16x16x32_bf16 v[244:247], a[168:171], a[36:39], v[244:247]\n"
"v_mfma_f32_16x16x32_bf16 v[244:247], a[172:175], a[40:43], v[244:247]\n"
"v_mfma_f32_16x16x32_bf16 v[244:247], a[176:179], a[44:47], v[244:247]\n"
"s_lshl_b32 s91, s88, 5\n"
"v_subrev_u32 v250, s91, v249\n"
"s_nop 7\n"
"v_cmp_gt_i32 vcc, 0, v250\n"
"s_nop 1\n"
"v_cndmask_b32 v240, v240, v255, vcc\n"
"v_cmp_gt_i32 vcc, 1, v250\n"
"s_nop 1\n"
"v_cndmask_b32 v241, v241, v255, vcc\n"
"v_cmp_gt_i32 vcc, 2, v250\n"
"s_nop 1\n"
"v_cndmask_b32 v242, v242, v255, vcc\n"
"v_cmp_gt_i32 vcc, 3, v250\n"
"s_nop 1\n"
"v_cndmask_b32 v243, v243, v255, vcc\n"
"v_cmp_gt_i32 vcc, 16, v250\n"
"s_nop 1\n"
"v_cndmask_b32 v244, v244, v255, vcc\n"
"v_cmp_gt_i32 vcc, 17, v250\n"
"s_nop 1\n"
"v_cndmask_b32 v245, v245, v255, vcc\n"
"v_cmp_gt_i32 vcc, 18, v250\n"
"s_nop 1\n"
"v_cndmask_b32 v246, v246, v255, vcc\n"
"v_cmp_gt_i32 vcc, 19, v250\n"
"s_nop 1\n"
"v_cndmask_b32 v247, v247, v255, vcc\n"
"s_cmp_lg_u32 s90, 0\n"
"s_cbranch_scc1 L1043_%=\n"
"v_max3_f32 v248, v248, v240, v241\n"
"v_max3_f32 v248, v248, v242, v243\n"
"v_max3_f32 v248, v248, v244, v245\n"
"v_max3_f32 v248, v248, v246, v247\n"
"s_branch L1044_%=\n"
"L1043_%=:\n"
"s_lshl_b32 s91, s88, 13\n"
"s_add_u32 s91, s91, s39\n"
"s_add_u32 s91, s91, 524288\n"
"buffer_load_dwordx4 a[148:151], v224, s[8:11], s91 offen offset:0 sc1\n"
"buffer_load_dwordx4 a[152:155], v224, s[8:11], s91 offen offset:1024 sc1\n"
"buffer_load_dwordx4 a[156:159], v224, s[8:11], s91 offen offset:2048 sc1\n"
"buffer_load_dwordx4 a[160:163], v224, s[8:11], s91 offen offset:3072 sc1\n"
"s_add_u32 s91, s91, 4096\n"
"buffer_load_dwordx4 a[164:167], v224, s[8:11], s91 offen offset:0 sc1\n"
"buffer_load_dwordx4 a[168:171], v224, s[8:11], s91 offen offset:1024 sc1\n"
"buffer_load_dwordx4 a[172:175], v224, s[8:11], s91 offen offset:2048 sc1\n"
"buffer_load_dwordx4 a[176:179], v224, s[8:11], s91 offen offset:3072 sc1\n"
"s_waitcnt vmcnt(0)\n"
"v_sub_f32 v240, v240, v248\n"
"v_sub_f32 v241, v241, v248\n"
"v_sub_f32 v242, v242, v248\n"
"v_sub_f32 v243, v243, v248\n"
"v_sub_f32 v244, v244, v248\n"
"v_sub_f32 v245, v245, v248\n"
"v_sub_f32 v246, v246, v248\n"
"v_sub_f32 v247, v247, v248\n"
"v_exp_f32 v240, v240\n"
"v_exp_f32 v241, v241\n"
"v_exp_f32 v242, v242\n"
"v_exp_f32 v243, v243\n"
"v_exp_f32 v244, v244\n"
"v_exp_f32 v245, v245\n"
"v_exp_f32 v246, v246\n"
"v_exp_f32 v247, v247\n"
"s_nop 0\n"
"v_cvt_pk_bf16_f32 v240, v240, v241\n"
"v_cvt_pk_bf16_f32 v241, v242, v243\n"
"v_cvt_pk_bf16_f32 v242, v244, v245\n"
"v_cvt_pk_bf16_f32 v243, v246, v247\n"
"s_nop 1\n"
"v_mfma_f32_16x16x32_bf16 v[144:147], a[148:151], v[240:243], v[144:147]\n"
"v_mfma_f32_16x16x32_bf16 v[148:151], a[152:155], v[240:243], v[148:151]\n"
"v_mfma_f32_16x16x32_bf16 v[152:155], a[156:159], v[240:243], v[152:155]\n"
"v_mfma_f32_16x16x32_bf16 v[156:159], a[160:163], v[240:243], v[156:159]\n"
"v_mfma_f32_16x16x32_bf16 v[160:163], a[164:167], v[240:243], v[160:163]\n"
"v_mfma_f32_16x16x32_bf16 v[164:167], a[168:171], v[240:243], v[164:167]\n"
"v_mfma_f32_16x16x32_bf16 v[168:171], a[172:175], v[240:243], v[168:171]\n"
"v_mfma_f32_16x16x32_bf16 v[172:175], a[176:179], v[240:243], v[172:175]\n"
"v_mfma_f32_16x16x32_bf16 a[140:143], a[128:131], v[240:243], a[140:143]\n"
"s_nop 7\n"
"L1044_%=:\n"
"s_add_u32 s88, s88, 1\n"
"s_cmp_le_u32 s88, s89\n"
"s_cbranch_scc1 L1042_%=\n"
"s_cmp_lg_u32 s90, 0\n"
"s_cbranch_scc1 L1045_%=\n"
"v_mov_b32 v254, v248\n"
"s_nop 1\n"
"v_permlane16_swap_b32 v254, v248\n"
"s_nop 1\n"
"v_max_f32 v248, v248, v254\n"
"v_mov_b32 v254, v248\n"
"s_nop 1\n"
"v_permlane32_swap_b32 v254, v248\n"
"s_nop 1\n"
"v_max_f32 v248, v248, v254\n"
"s_mov_b32 s90, 1\n"
"s_mov_b32 s88, 0\n"
"s_branch L1042_%=\n"
"L1045_%=:\n"
"s_nop 7\n"
"v_accvgpr_read_b32 v251, a140\n"
"s_nop 1\n"
"s_branch L1040_%=\n"
"L1047_%=:\n"
"s_nop 15\n"
"s_lshl_b32 s89, s34, 3\n"
"s_sub_u32 s89, 120, s89\n"
"s_add_u32 s89, s89, s30\n"
"s_add_u32 s89, s89, 4\n"
"s_lshl_b32 s91, s89, 4\n"
"s_lshr_b32 s89, s89, 1\n"
"v_bfe_u32 v249, v224, 4, 4\n"
"v_lshrrev_b32 v254, 8, v224\n"
"v_lshlrev_b32 v254, 2, v254\n"
"v_sub_u32 v249, v249, v254\n"
"v_add_u32 v249, s91, v249\n"
"v_mov_b32 v255, 0xff800000\n"
"v_mov_b32 v248, v255\n"
"s_mov_b32 s90, 0\n"
"s_mov_b32 s88, 0\n"
"v_mov_b32 v176, 0\n"
"v_mov_b32 v177, 0\n"
"v_mov_b32 v178, 0\n"
"v_mov_b32 v179, 0\n"
"v_mov_b32 v180, 0\n"
"v_mov_b32 v181, 0\n"
"v_mov_b32 v182, 0\n"
"v_mov_b32 v183, 0\n"
"v_mov_b32 v184, 0\n"
"v_mov_b32 v185, 0\n"
"v_mov_b32 v186, 0\n"
"v_mov_b32 v187, 0\n"
"v_mov_b32 v188, 0\n"
"v_mov_b32 v189, 0\n"
"v_mov_b32 v190, 0\n"
"v_mov_b32 v191, 0\n"
"v_mov_b32 v192, 0\n"
"v_mov_b32 v193, 0\n"
"v_mov_b32 v194, 0\n"
"v_mov_b32 v195, 0\n"
"v_mov_b32 v196, 0\n"
"v_mov_b32 v197, 0\n"
"v_mov_b32 v198, 0\n"
"v_mov_b32 v199, 0\n"
"v_mov_b32 v200, 0\n"
"v_mov_b32 v201, 0\n"
"v_mov_b32 v202, 0\n"
"v_mov_b32 v203, 0\n"
"v_mov_b32 v204, 0\n"
"v_mov_b32 v205, 0\n"
"v_mov_b32 v206, 0\n"
"v_mov_b32 v207, 0\n"
"v_accvgpr_write_b32 a144, 0\n"
"v_accvgpr_write_b32 a145, 0\n"
"v_accvgpr_write_b32 a146, 0\n"
"v_accvgpr_write_b32 a147, 0\n"
"L1048_%=:\n"
"s_lshl_b32 s91, s88, 13\n"
"s_add_u32 s91, s91, s39\n"
"s_add_u32 s91, s91, 0\n"
"buffer_load_dwordx4 a[148:151], v224, s[8:11], s91 offen offset:0 sc1\n"
"buffer_load_dwordx4 a[152:155], v224, s[8:11], s91 offen offset:1024 sc1\n"
"buffer_load_dwordx4 a[156:159], v224, s[8:11], s91 offen offset:2048 sc1\n"
"buffer_load_dwordx4 a[160:163], v224, s[8:11], s91 offen offset:3072 sc1\n"
"s_add_u32 s91, s91, 4096\n"
"buffer_load_dwordx4 a[164:167], v224, s[8:11], s91 offen offset:0 sc1\n"
"buffer_load_dwordx4 a[168:171], v224, s[8:11], s91 offen offset:1024 sc1\n"
"buffer_load_dwordx4 a[172:175], v224, s[8:11], s91 offen offset:2048 sc1\n"
"buffer_load_dwordx4 a[176:179], v224, s[8:11], s91 offen offset:3072 sc1\n"
"s_waitcnt vmcnt(0)\n"
"v_mfma_f32_16x16x32_bf16 v[240:243], a[148:151], a[48:51], 0\n"
"v_mfma_f32_16x16x32_bf16 v[240:243], a[152:155], a[52:55], v[240:243]\n"
"v_mfma_f32_16x16x32_bf16 v[240:243], a[156:159], a[56:59], v[240:243]\n"
"v_mfma_f32_16x16x32_bf16 v[240:243], a[160:163], a[60:63], v[240:243]\n"
"v_mfma_f32_16x16x32_bf16 v[244:247], a[164:167], a[48:51], 0\n"
"v_mfma_f32_16x16x32_bf16 v[244:247], a[168:171], a[52:55], v[244:247]\n"
"v_mfma_f32_16x16x32_bf16 v[244:247], a[172:175], a[56:59], v[244:247]\n"
"v_mfma_f32_16x16x32_bf16 v[244:247], a[176:179], a[60:63], v[244:247]\n"
"s_lshl_b32 s91, s88, 5\n"
"v_subrev_u32 v250, s91, v249\n"
"s_nop 7\n"
"v_cmp_gt_i32 vcc, 0, v250\n"
"s_nop 1\n"
"v_cndmask_b32 v240, v240, v255, vcc\n"
"v_cmp_gt_i32 vcc, 1, v250\n"
"s_nop 1\n"
"v_cndmask_b32 v241, v241, v255, vcc\n"
"v_cmp_gt_i32 vcc, 2, v250\n"
"s_nop 1\n"
"v_cndmask_b32 v242, v242, v255, vcc\n"
"v_cmp_gt_i32 vcc, 3, v250\n"
"s_nop 1\n"
"v_cndmask_b32 v243, v243, v255, vcc\n"
"v_cmp_gt_i32 vcc, 16, v250\n"
"s_nop 1\n"
"v_cndmask_b32 v244, v244, v255, vcc\n"
"v_cmp_gt_i32 vcc, 17, v250\n"
"s_nop 1\n"
"v_cndmask_b32 v245, v245, v255, vcc\n"
"v_cmp_gt_i32 vcc, 18, v250\n"
"s_nop 1\n"
"v_cndmask_b32 v246, v246, v255, vcc\n"
"v_cmp_gt_i32 vcc, 19, v250\n"
"s_nop 1\n"
"v_cndmask_b32 v247, v247, v255, vcc\n"
"s_cmp_lg_u32 s90, 0\n"
"s_cbranch_scc1 L1049_%=\n"
"v_max3_f32 v248, v248, v240, v241\n"
"v_max3_f32 v248, v248, v242, v243\n"
"v_max3_f32 v248, v248, v244, v245\n"
"v_max3_f32 v248, v248, v246, v247\n"
"s_branch L1050_%=\n"
"L1049_%=:\n"
"s_lshl_b32 s91, s88, 13\n"
"s_add_u32 s91, s91, s39\n"
"s_add_u32 s91, s91, 524288\n"
"buffer_load_dwordx4 a[148:151], v224, s[8:11], s91 offen offset:0 sc1\n"
"buffer_load_dwordx4 a[152:155], v224, s[8:11], s91 offen offset:1024 sc1\n"
"buffer_load_dwordx4 a[156:159], v224, s[8:11], s91 offen offset:2048 sc1\n"
"buffer_load_dwordx4 a[160:163], v224, s[8:11], s91 offen offset:3072 sc1\n"
"s_add_u32 s91, s91, 4096\n"
"buffer_load_dwordx4 a[164:167], v224, s[8:11], s91 offen offset:0 sc1\n"
"buffer_load_dwordx4 a[168:171], v224, s[8:11], s91 offen offset:1024 sc1\n"
"buffer_load_dwordx4 a[172:175], v224, s[8:11], s91 offen offset:2048 sc1\n"
"buffer_load_dwordx4 a[176:179], v224, s[8:11], s91 offen offset:3072 sc1\n"
"s_waitcnt vmcnt(0)\n"
"v_sub_f32 v240, v240, v248\n"
"v_sub_f32 v241, v241, v248\n"
"v_sub_f32 v242, v242, v248\n"
"v_sub_f32 v243, v243, v248\n"
"v_sub_f32 v244, v244, v248\n"
"v_sub_f32 v245, v245, v248\n"
"v_sub_f32 v246, v246, v248\n"
"v_sub_f32 v247, v247, v248\n"
"v_exp_f32 v240, v240\n"
"v_exp_f32 v241, v241\n"
"v_exp_f32 v242, v242\n"
"v_exp_f32 v243, v243\n"
"v_exp_f32 v244, v244\n"
"v_exp_f32 v245, v245\n"
"v_exp_f32 v246, v246\n"
"v_exp_f32 v247, v247\n"
"s_nop 0\n"
"v_cvt_pk_bf16_f32 v240, v240, v241\n"
"v_cvt_pk_bf16_f32 v241, v242, v243\n"
"v_cvt_pk_bf16_f32 v242, v244, v245\n"
"v_cvt_pk_bf16_f32 v243, v246, v247\n"
"s_nop 1\n"
"v_mfma_f32_16x16x32_bf16 v[176:179], a[148:151], v[240:243], v[176:179]\n"
"v_mfma_f32_16x16x32_bf16 v[180:183], a[152:155], v[240:243], v[180:183]\n"
"v_mfma_f32_16x16x32_bf16 v[184:187], a[156:159], v[240:243], v[184:187]\n"
"v_mfma_f32_16x16x32_bf16 v[188:191], a[160:163], v[240:243], v[188:191]\n"
"v_mfma_f32_16x16x32_bf16 v[192:195], a[164:167], v[240:243], v[192:195]\n"
"v_mfma_f32_16x16x32_bf16 v[196:199], a[168:171], v[240:243], v[196:199]\n"
"v_mfma_f32_16x16x32_bf16 v[200:203], a[172:175], v[240:243], v[200:203]\n"
"v_mfma_f32_16x16x32_bf16 v[204:207], a[176:179], v[240:243], v[204:207]\n"
"v_mfma_f32_16x16x32_bf16 a[144:147], a[128:131], v[240:243], a[144:147]\n"
"s_nop 7\n"
"L1050_%=:\n"
"s_add_u32 s88, s88, 1\n"
"s_cmp_le_u32 s88, s89\n"
"s_cbranch_scc1 L1048_%=\n"
"s_cmp_lg_u32 s90, 0\n"
"s_cbranch_scc1 L1051_%=\n"
"v_mov_b32 v254, v248\n"
"s_nop 1\n"
"v_permlane16_swap_b32 v254, v248\n"
"s_nop 1\n"
"v_max_f32 v248, v248, v254\n"
"v_mov_b32 v254, v248\n"
"s_nop 1\n"
"v_permlane32_swap_b32 v254, v248\n"
"s_nop 1\n"
"v_max_f32 v248, v248, v254\n"
"s_mov_b32 s90, 1\n"
"s_mov_b32 s88, 0\n"
"s_branch L1048_%=\n"
"L1051_%=:\n"
"s_nop 7\n"
"v_accvgpr_read_b32 v252, a144\n"
"s_nop 1\n"
"s_branch L1046_%=\n"
"L1052_%=:\n"
"s_mov_b32 s54, 0\n"
"L1054_%=:\n"
"s_sleep 2\n"
"buffer_load_dwordx2 v[240:241], v238, s[8:11], s63 offen sc1\n"
"s_waitcnt vmcnt(0)\n"
"s_add_u32 s54, s54, 1\n"
"v_cmp_eq_u64 vcc, s[28:29], v[240:241]\n"
"s_cmp_eq_u64 vcc, exec\n"
"s_cbranch_scc1 L1053_%=\n"
"s_cmp_lt_u32 s54, 1048576\n"
"s_cbranch_scc1 L1054_%=\n"
"s_branch L1053_%=\n"
"L1055_%=:\n"
"s_mov_b32 s54, 0\n"
"L1057_%=:\n"
"s_sleep 2\n"
"buffer_load_dwordx2 v[240:241], v238, s[8:11], s63 offen sc1\n"
"s_waitcnt vmcnt(0)\n"
"s_add_u32 s54, s54, 1\n"
"v_cmp_eq_u64 vcc, s[28:29], v[240:241]\n"
"s_cmp_eq_u64 vcc, exec\n"
"s_cbranch_scc1 L1056_%=\n"
"s_cmp_lt_u32 s54, 1048576\n"
"s_cbranch_scc1 L1057_%=\n"
"s_branch L1056_%=\n"
"L1058_%=:\n"
"s_mov_b32 s54, 0\n"
"L1060_%=:\n"
"s_sleep 2\n"
"buffer_load_dwordx2 v[240:241], v244, s[8:11], s92 offen sc1\n"
"s_waitcnt vmcnt(0)\n"
"s_add_u32 s54, s54, 1\n"
"v_cmp_eq_u64 vcc, s[28:29], v[240:241]\n"
"s_cmp_eq_u64 vcc, exec\n"
"s_cbranch_scc1 L1059_%=\n"
"s_cmp_lt_u32 s54, 1048576\n"
"s_cbranch_scc1 L1060_%=\n"
"s_branch L1059_%=\n"
"L1061_%=:\n"
"s_mov_b32 s54, 0\n"
"L1063_%=:\n"
"s_sleep 2\n"
"buffer_load_dwordx2 v[240:241], v243, s[8:11], s93 offen sc1\n"
"s_waitcnt vmcnt(0)\n"
"s_add_u32 s54, s54, 1\n"
"v_cmp_eq_u64 vcc, s[28:29], v[240:241]\n"
"s_cmp_eq_u64 vcc, exec\n"
"s_cbranch_scc1 L1062_%=\n"
"s_cmp_lt_u32 s54, 1048576\n"
"s_cbranch_scc1 L1063_%=\n"
"s_branch L1062_%=\n"
"L1064_%=:\n"
"s_mov_b32 s54, 0\n"
"L1066_%=:\n"
"s_sleep 2\n"
"buffer_load_dwordx2 v[240:241], v243, s[8:11], s93 offen offset:512 sc1\n"
"s_waitcnt vmcnt(0)\n"
"s_add_u32 s54, s54, 1\n"
"v_cmp_eq_u64 vcc, s[28:29], v[240:241]\n"
"s_cmp_eq_u64 vcc, exec\n"
"s_cbranch_scc1 L1065_%=\n"
"s_cmp_lt_u32 s54, 1048576\n"
"s_cbranch_scc1 L1066_%=\n"
"s_branch L1065_%=\n"
"L1072_%=:\n"
"s_mov_b32 s54, 0\n"
"L1074_%=:\n"
"s_sleep 2\n"
"buffer_load_dwordx4 v[240:243], v224, s[8:11], s48 offen offset:0 sc1\n"
"buffer_load_dwordx4 v[244:247], v224, s[8:11], s48 offen offset:1024 sc1\n"
"s_waitcnt vmcnt(0)\n"
"s_add_u32 s54, s54, 1\n"
"v_cmp_eq_u64 vcc, s[28:29], v[240:241]\n"
"v_cmp_eq_u64_e64 s[52:53], s[28:29], v[242:243]\n"
"s_and_b64 vcc, vcc, s[52:53]\n"
"v_cmp_eq_u64_e64 s[52:53], s[28:29], v[244:245]\n"
"s_and_b64 vcc, vcc, s[52:53]\n"
"v_cmp_eq_u64_e64 s[52:53], s[28:29], v[246:247]\n"
"s_and_b64 vcc, vcc, s[52:53]\n"
"s_cmp_eq_u64 vcc, exec\n"
"s_cbranch_scc1 L1073_%=\n"
"s_cmp_lt_u32 s54, 1048576\n"
"s_cbranch_scc1 L1074_%=\n"
"s_branch L1073_%=\n"
"LFA_%=:\n"
"s_waitcnt lgkmcnt(5)\n"
"v_mfma_f32_16x16x32_bf16 v[0:3], a[64:67], a[0:3], v[208:211]\n"
"s_mov_b32 m0, s40\n"
"v_cvt_pk_bf16_f32 v192, v192, v193\n"
"v_cvt_pk_bf16_f32 v193, v194, v195\n"
"v_mfma_f32_16x16x32_bf16 v[8:11], a[64:67], a[16:19], 0\n"
"v_readlane_b32 s43, v228, s42\n"
"v_cvt_pk_bf16_f32 v194, v196, v197\n"
"v_mfma_f32_16x16x32_bf16 v[16:19], a[64:67], a[32:35], 0\n"
"v_cvt_pk_bf16_f32 v195, v198, v199\n"
"v_readlane_b32 s44, v229, s42\n"
"v_mfma_f32_16x16x32_bf16 v[24:27], a[64:67], a[48:51], 0\n"
"s_waitcnt lgkmcnt(0)\n"
"buffer_load_dwordx4 v225, s[8:11], s43 offen offset:0 sc1 lds\n"
"v_mfma_f32_16x16x32_bf16 v[0:3], a[68:71], a[4:7], v[0:3]\n"
"s_add_u32 s42, s42, 1\n"
"v_accvgpr_write_b32 a44, v192\n"
"v_accvgpr_write_b32 a45, v193\n"
"v_mfma_f32_16x16x32_bf16 v[8:11], a[68:71], a[20:23], v[8:11]\n"
"v_accvgpr_write_b32 a46, v194\n"
"v_accvgpr_write_b32 a47, v195\n"
"v_mfma_f32_16x16x32_bf16 v[16:19], a[68:71], a[36:39], v[16:19]\n"
"v_cvt_pk_bf16_f32 v200, v200, v201\n"
"v_cvt_pk_bf16_f32 v201, v202, v203\n"
"v_mfma_f32_16x16x32_bf16 v[24:27], a[68:71], a[52:55], v[24:27]\n"
"v_cvt_pk_bf16_f32 v202, v204, v205\n"
"v_cvt_pk_bf16_f32 v203, v206, v207\n"
"v_mov_b32 v227, v226\n"
"v_mfma_f32_16x16x32_bf16 v[0:3], a[72:75], a[8:11], v[0:3]\n"
"buffer_load_dwordx4 v225, s[8:11], s43 offen offset:1024 sc1 lds\n"
"v_mfma_f32_16x16x32_bf16 v[8:11], a[72:75], a[24:27], v[8:11]\n"
"ds_read_b128 a[88:91], v226 offset:6144\n"
"v_accvgpr_write_b32 a60, v200\n"
"v_accvgpr_write_b32 a61, v201\n"
"v_mfma_f32_16x16x32_bf16 v[16:19], a[72:75], a[40:43], v[16:19]\n"
"s_lshl_b32 s59, s34, 5\n"
"v_accvgpr_write_b32 a62, v202\n"
"v_accvgpr_write_b32 a63, v203\n"
"v_mfma_f32_16x16x32_bf16 v[24:27], a[72:75], a[56:59], v[24:27]\n"
"s_sub_u32 s60, 480, s59\n"
"v_mfma_f32_16x16x32_bf16 v[0:3], a[76:79], a[12:15], v[0:3]\n"
"s_add_u32 s59, s59, s35\n"
"v_mfma_f32_16x16x32_bf16 v[8:11], a[76:79], a[28:31], v[8:11]\n"
"s_add_u32 s60, s60, s35\n"
"ds_read_b128 a[92:95], v226 offset:7168\n"
"v_mfma_f32_16x16x32_bf16 v[16:19], a[76:79], a[44:47], v[16:19]\n"
"s_lshl_b32 s59, s59, 15\n"
"buffer_load_dwordx4 v225, s[8:11], s43 offen offset:2048 sc1 lds\n"
"v_mfma_f32_16x16x32_bf16 v[24:27], a[76:79], a[60:63], v[24:27]\n"
"s_lshl_b32 s60, s60, 15\n"
"v_mfma_f32_16x16x32_bf16 v[4:7], a[80:83], a[0:3], v[212:215]\n"
"s_add_u32 s48, s36, 44040192\n"
"v_exp_f32 v0, v0\n"
"v_mfma_f32_16x16x32_bf16 v[12:15], a[80:83], a[16:19], 0\n"
"s_add_u32 s59, s59, s48\n"
"ds_read_b128 a[96:99], v226 offset:8192\n"
"v_mfma_f32_16x16x32_bf16 v[20:23], a[80:83], a[32:35], 0\n"
"s_add_u32 s60, s60, s48\n"
"v_exp_f32 v1, v1\n"
"v_mfma_f32_16x16x32_bf16 v[28:31], a[80:83], a[48:51], 0\n"
"s_lshl_b32 s61, s34, 3\n"
"v_exp_f32 v2, v2\n"
"v_mfma_f32_16x16x32_bf16 v[4:7], a[84:87], a[4:7], v[4:7]\n"
"s_add_u32 s61, s61, s32\n"
"buffer_load_dwordx4 v225, s[8:11], s43 offen offset:3072 sc1 lds\n"
"v_mfma_f32_16x16x32_bf16 v[12:15], a[84:87], a[20:23], v[12:15]\n"
"s_lshl_b32 s61, s61, 2\n"
"v_exp_f32 v3, v3\n"
"v_mfma_f32_16x16x32_bf16 v[20:23], a[84:87], a[36:39], v[20:23]\n"
"s_add_u32 s61, s61, s30\n"
"ds_read_b128 a[100:103], v226 offset:9216\n"
"v_cvt_pk_bf16_f32 v0, v0, v1\n"
"v_mfma_f32_16x16x32_bf16 v[28:31], a[84:87], a[52:55], v[28:31]\n"
"s_waitcnt lgkmcnt(2)\n"
"v_exp_f32 v8, v8\n"
"v_mfma_f32_16x16x32_bf16 v[4:7], a[88:91], a[8:11], v[4:7]\n"
"s_add_u32 m0, s40, 16384\n"
"v_exp_f32 v9, v9\n"
"v_cvt_pk_bf16_f32 v1, v2, v3\n"
"v_mfma_f32_16x16x32_bf16 v[12:15], a[88:91], a[24:27], v[12:15]\n"
"s_lshl_b32 s61, s61, 2\n"
"v_exp_f32 v10, v10\n"
"v_mfma_f32_16x16x32_bf16 v[20:23], a[88:91], a[40:43], v[20:23]\n"
"s_add_u32 s61, s61, s33\n"
"ds_read_b128 a[104:107], v226 offset:10240\n"
"v_cvt_pk_bf16_f32 v8, v8, v9\n"
"v_mfma_f32_16x16x32_bf16 v[28:31], a[88:91], a[56:59], v[28:31]\n"
"s_lshl_b32 s61, s61, 3\n"
"buffer_load_dwordx4 v225, s[8:11], s44 offen offset:0 sc1 lds\n"
"v_mfma_f32_16x16x32_bf16 v[4:7], a[92:95], a[12:15], v[4:7]\n"
"s_add_u32 s61, s61, 6912\n"
"v_exp_f32 v11, v11\n"
"v_mfma_f32_16x16x32_bf16 v[12:15], a[92:95], a[28:31], v[12:15]\n"
"s_lshr_b32 s48, s31, 3\n"
"v_exp_f32 v16, v16\n"
"v_mfma_f32_16x16x32_bf16 v[20:23], a[92:95], a[44:47], v[20:23]\n"
"s_add_u32 s40, s40, 32768\n"
"v_exp_f32 v17, v17\n"
"v_cvt_pk_bf16_f32 v9, v10, v11\n"
"v_mfma_f32_16x16x32_bf16 v[28:31], a[92:95], a[60:63], v[28:31]\n"
"s_waitcnt lgkmcnt(1)\n"
"ds_read_b128 a[108:111], v226 offset:11264\n"
"v_cvt_pk_bf16_f32 v16, v16, v17\n"
"v_mfma_f32_16x16x32_bf16 v[32:35], a[96:99], a[0:3], v[216:219]\n"
"s_add_u32 s58, s32, 32\n"
"v_exp_f32 v18, v18\n"
"v_mfma_f32_16x16x32_bf16 v[40:43], a[96:99], a[16:19], 0\n"
"s_cmp_eq_u32 s40, s46\n"
"v_exp_f32 v19, v19\n"
"v_mfma_f32_16x16x32_bf16 v[48:51], a[96:99], a[32:35], 0\n"
"s_cselect_b32 s40, s47, s40\n"
"buffer_load_dwordx4 v225, s[8:11], s44 offen offset:1024 sc1 lds\n"
"v_mfma_f32_16x16x32_bf16 v[56:59], a[96:99], a[48:51], 0\n"
"s_cmp_eq_u32 s48, 5\n"
"v_exp_f32 v24, v24\n"
"v_cvt_pk_bf16_f32 v17, v18, v19\n"
"v_mfma_f32_16x16x32_bf16 v[32:35], a[100:103], a[4:7], v[32:35]\n"
"s_cselect_b32 s58, s58, 99\n"
"ds_read_b128 a[112:115], v226 offset:12288\n"
"s_cmp_eq_u32 s34, 6\n"
"v_mfma_f32_16x16x32_bf16 v[40:43], a[100:103], a[20:23], v[40:43]\n"
"s_cselect_b32 s58, s35, s58\n"
"v_exp_f32 v25, v25\n"
"v_mfma_f32_16x16x32_bf16 v[48:51], a[100:103], a[36:39], v[48:51]\n"
"s_lshl_b32 s56, s34, 1\n"
"v_exp_f32 v26, v26\n"
"v_cvt_pk_bf16_f32 v24, v24, v25\n"
"v_mfma_f32_16x16x32_bf16 v[56:59], a[100:103], a[52:55], v[56:59]\n"
"s_waitcnt lgkmcnt(1)\n"
"v_exp_f32 v27, v27\n"
"v_mfma_f32_16x16x32_bf16 v[32:35], a[104:107], a[8:11], v[32:35]\n"
"s_sub_u32 s56, s56, 2\n"
"ds_read_b128 a[116:119], v226 offset:13312\n"
"v_cvt_pk_bf16_f32 v25, v26, v27\n"
"v_mfma_f32_16x16x32_bf16 v[40:43], a[104:107], a[24:27], v[40:43]\n"
"s_cmp_ge_u32 s34, 5\n"
"buffer_load_dwordx4 v225, s[8:11], s44 offen offset:2048 sc1 lds\n"
"v_mfma_f32_16x16x32_bf16 v[48:51], a[104:107], a[40:43], v[48:51]\n"
"s_cselect_b32 s56, 1, s56\n"
"v_exp_f32 v4, v4\n"
"v_mfma_f32_16x16x32_bf16 v[56:59], a[104:107], a[56:59], v[56:59]\n"
"s_cmp_ge_u32 s34, 5\n"
"v_exp_f32 v5, v5\n"
"v_mfma_f32_16x16x32_bf16 v[32:35], a[108:111], a[12:15], v[32:35]\n"
"s_cselect_b32 s57, 1, 0\n"
"v_exp_f32 v6, v6\n"
"v_cvt_pk_bf16_f32 v2, v4, v5\n"
"v_mfma_f32_16x16x32_bf16 v[40:43], a[108:111], a[28:31], v[40:43]\n"
"s_lshl_b32 s48, s35, 3\n"
"ds_read_b128 a[120:123], v226 offset:14336\n"
"ds_read_b128 a[124:127], v226 offset:15360\n"
"v_readlane_b32 s43, v228, s42\n"
"v_mfma_f32_16x16x32_bf16 v[48:51], a[108:111], a[44:47], v[48:51]\n"
"s_add_u32 s48, s48, s34\n"
"v_exp_f32 v7, v7\n"
"v_mfma_f32_16x16x32_bf16 v[56:59], a[108:111], a[60:63], v[56:59]\n"
"s_waitcnt lgkmcnt(2)\n"
"v_exp_f32 v12, v12\n"
"v_cvt_pk_bf16_f32 v3, v6, v7\n"
"v_mfma_f32_16x16x32_bf16 v[36:39], a[112:115], a[0:3], v[220:223]\n"
"s_lshl_b32 s48, s48, 3\n"
"buffer_load_dwordx4 v225, s[8:11], s44 offen offset:3072 sc1 lds\n"
"v_mfma_f32_16x16x32_bf16 v[44:47], a[112:115], a[16:19], 0\n"
"s_add_u32 s48, s48, 4352\n"
"v_exp_f32 v13, v13\n"
"v_mfma_f32_16x16x32_bf16 v[52:55], a[112:115], a[32:35], 0\n"
"s_add_u32 s48, s48, s45\n"
"v_exp_f32 v14, v14\n"
"v_cvt_pk_bf16_f32 v10, v12, v13\n"
"v_mfma_f32_16x16x32_bf16 v[60:63], a[112:115], a[48:51], 0\n"
"s_mov_b32 m0, s40\n"
"v_exp_f32 v15, v15\n"
"v_readlane_b32 s44, v229, s42\n"
"v_mfma_f32_16x16x32_bf16 v[36:39], a[116:119], a[4:7], v[36:39]\n"
"s_add_u32 s42, s42, 1\n"
"v_exp_f32 v20, v20\n"
"v_cvt_pk_bf16_f32 v11, v14, v15\n"
"v_mfma_f32_16x16x32_bf16 v[44:47], a[116:119], a[20:23], v[44:47]\n"
"ds_read_b128 a[96:99], v227 offset:24576\n"
"ds_read_b128 a[100:103], v227 offset:25600\n"
"v_mfma_f32_16x16x32_bf16 v[52:55], a[116:119], a[36:39], v[52:55]\n"
"buffer_load_dwordx4 v225, s[8:11], s43 offen offset:0 sc1 lds\n"
"v_mfma_f32_16x16x32_bf16 v[60:63], a[116:119], a[52:55], v[60:63]\n"
"s_waitcnt lgkmcnt(2)\n"
"v_exp_f32 v21, v21\n"
"v_mfma_f32_16x16x32_bf16 v[36:39], a[120:123], a[8:11], v[36:39]\n"
"v_exp_f32 v22, v22\n"
"v_cvt_pk_bf16_f32 v18, v20, v21\n"
"v_mfma_f32_16x16x32_bf16 v[44:47], a[120:123], a[24:27], v[44:47]\n"
"v_exp_f32 v23, v23\n"
"v_mfma_f32_16x16x32_bf16 v[52:55], a[120:123], a[40:43], v[52:55]\n"
"buffer_load_dwordx4 v225, s[8:11], s43 offen offset:1024 sc1 lds\n"
"v_cvt_pk_bf16_f32 v19, v22, v23\n"
"v_mfma_f32_16x16x32_bf16 v[60:63], a[120:123], a[56:59], v[60:63]\n"
"v_exp_f32 v28, v28\n"
"v_mfma_f32_16x16x32_bf16 v[36:39], a[124:127], a[12:15], v[36:39]\n"
"ds_read_b128 a[104:107], v227 offset:26624\n"
"v_mfma_f32_16x16x32_bf16 v[44:47], a[124:127], a[28:31], v[44:47]\n"
"v_exp_f32 v29, v29\n"
"v_mfma_f32_16x16x32_bf16 v[52:55], a[124:127], a[44:47], v[52:55]\n"
"v_exp_f32 v30, v30\n"
"v_cvt_pk_bf16_f32 v26, v28, v29\n"
"v_mfma_f32_16x16x32_bf16 v[60:63], a[124:127], a[60:63], v[60:63]\n"
"s_waitcnt lgkmcnt(1)\n"
"v_exp_f32 v31, v31\n"
"v_mfma_f32_16x16x32_bf16 v[80:83], a[96:99], v[0:3], 0\n"
"s_add_u32 s41, s41, 32768\n"
"v_cvt_pk_bf16_f32 v27, v30, v31\n"
"s_cmp_eq_u32 s41, 163840\n"
"v_mfma_f32_16x16x32_bf16 v[112:115], a[96:99], v[8:11], 0\n"
"s_cselect_b32 s41, 0, s41\n"
"buffer_load_dwordx4 v225, s[8:11], s43 offen offset:2048 sc1 lds\n"
"v_mfma_f32_16x16x32_bf16 v[144:147], a[96:99], v[16:19], 0\n"
"ds_read_b128 a[108:111], v227 offset:27648\n"
"v_add_u32 v226, s41, v224\n"
"v_mfma_f32_16x16x32_bf16 v[176:179], a[96:99], v[24:27], 0\n"
"v_exp_f32 v32, v32\n"
"v_mfma_f32_16x16x32_bf16 v[84:87], a[100:103], v[0:3], 0\n"
"v_exp_f32 v33, v33\n"
"v_mfma_f32_16x16x32_bf16 v[116:119], a[100:103], v[8:11], 0\n"
"v_exp_f32 v34, v34\n"
"v_cvt_pk_bf16_f32 v64, v32, v33\n"
"v_mfma_f32_16x16x32_bf16 v[148:151], a[100:103], v[16:19], 0\n"
"ds_read_b128 a[112:115], v227 offset:28672\n"
"ds_read_b128 a[116:119], v227 offset:29696\n"
"v_mfma_f32_16x16x32_bf16 v[180:183], a[100:103], v[24:27], 0\n"
"s_waitcnt lgkmcnt(3)\n"
"v_exp_f32 v35, v35\n"
"v_mfma_f32_16x16x32_bf16 v[88:91], a[104:107], v[0:3], 0\n"
"buffer_load_dwordx4 v225, s[8:11], s43 offen offset:3072 sc1 lds\n"
"v_cvt_pk_bf16_f32 v65, v34, v35\n"
"v_mfma_f32_16x16x32_bf16 v[120:123], a[104:107], v[8:11], 0\n"
"s_add_u32 m0, s40, 16384\n"
"v_exp_f32 v40, v40\n"
"v_mfma_f32_16x16x32_bf16 v[152:155], a[104:107], v[16:19], 0\n"
"s_add_u32 s40, s40, 32768\n"
"v_exp_f32 v41, v41\n"
"v_mfma_f32_16x16x32_bf16 v[184:187], a[104:107], v[24:27], 0\n"
"s_waitcnt lgkmcnt(2)\n"
"v_exp_f32 v42, v42\n"
"v_cvt_pk_bf16_f32 v68, v40, v41\n"
"v_mfma_f32_16x16x32_bf16 v[92:95], a[108:111], v[0:3], 0\n"
"s_cmp_eq_u32 s40, s46\n"
"v_exp_f32 v43, v43\n"
"v_mfma_f32_16x16x32_bf16 v[124:127], a[108:111], v[8:11], 0\n"
"s_cselect_b32 s40, s47, s40\n"
"buffer_load_dwordx4 v225, s[8:11], s44 offen offset:0 sc1 lds\n"
"v_cvt_pk_bf16_f32 v69, v42, v43\n"
"v_mfma_f32_16x16x32_bf16 v[156:159], a[108:111], v[16:19], 0\n"
"ds_read_b128 a[120:123], v227 offset:30720\n"
"v_mfma_f32_16x16x32_bf16 v[188:191], a[108:111], v[24:27], 0\n"
"s_waitcnt lgkmcnt(1)\n"
"v_exp_f32 v48, v48\n"
"v_mfma_f32_16x16x32_bf16 v[96:99], a[112:115], v[0:3], 0\n"
"v_exp_f32 v49, v49\n"
"v_mfma_f32_16x16x32_bf16 v[128:131], a[112:115], v[8:11], 0\n"
"v_exp_f32 v50, v50\n"
"v_cvt_pk_bf16_f32 v72, v48, v49\n"
"v_mfma_f32_16x16x32_bf16 v[160:163], a[112:115], v[16:19], 0\n"
"ds_read_b128 a[124:127], v227 offset:31744\n"
"v_mfma_f32_16x16x32_bf16 v[192:195], a[112:115], v[24:27], 0\n"
"v_exp_f32 v51, v51\n"
"v_mfma_f32_16x16x32_bf16 v[100:103], a[116:119], v[0:3], 0\n"
"buffer_load_dwordx4 v225, s[8:11], s44 offen offset:1024 sc1 lds\n"
"v_cvt_pk_bf16_f32 v73, v50, v51\n"
"v_mfma_f32_16x16x32_bf16 v[132:135], a[116:119], v[8:11], 0\n"
"v_exp_f32 v36, v36\n"
"v_mfma_f32_16x16x32_bf16 v[164:167], a[116:119], v[16:19], 0\n"
"v_exp_f32 v37, v37\n"
"v_mfma_f32_16x16x32_bf16 v[196:199], a[116:119], v[24:27], 0\n"
"s_waitcnt lgkmcnt(1)\n"
"v_exp_f32 v38, v38\n"
"v_cvt_pk_bf16_f32 v66, v36, v37\n"
"v_mfma_f32_16x16x32_bf16 v[104:107], a[120:123], v[0:3], 0\n"
"v_exp_f32 v39, v39\n"
"v_mfma_f32_16x16x32_bf16 v[136:139], a[120:123], v[8:11], 0\n"
"s_waitcnt vmcnt(14)\n"
"s_barrier\n"
"v_mfma_f32_16x16x32_bf16 v[168:171], a[120:123], v[16:19], 0\n"
"buffer_load_dwordx4 v225, s[8:11], s44 offen offset:2048 sc1 lds\n"
"v_cvt_pk_bf16_f32 v67, v38, v39\n"
"v_mfma_f32_16x16x32_bf16 v[200:203], a[120:123], v[24:27], 0\n"
"s_waitcnt lgkmcnt(0)\n"
"ds_read_b128 a[64:67], v226 offset:0\n"
"v_mfma_f32_16x16x32_bf16 v[108:111], a[124:127], v[0:3], 0\n"
"buffer_store_dwordx2 v[232:233], v230, s[8:11], s48 offen sc1\n"
"v_mfma_f32_16x16x32_bf16 v[140:143], a[124:127], v[8:11], 0\n"
"v_exp_f32 v44, v44\n"
"v_mfma_f32_16x16x32_bf16 v[172:175], a[124:127], v[16:19], 0\n"
"v_exp_f32 v45, v45\n"
"v_mfma_f32_16x16x32_bf16 v[204:207], a[124:127], v[24:27], 0\n"
"v_exp_f32 v46, v46\n"
"v_cvt_pk_bf16_f32 v70, v44, v45\n"
"v_mfma_f32_16x16x32_bf16 a[132:135], a[128:131], v[0:3], 0\n"
"ds_read_b128 a[68:71], v226 offset:1024\n"
"ds_read_b128 a[72:75], v226 offset:2048\n"
"v_mfma_f32_16x16x32_bf16 a[136:139], a[128:131], v[8:11], 0\n"
"v_exp_f32 v47, v47\n"
"v_mfma_f32_16x16x32_bf16 a[140:143], a[128:131], v[16:19], 0\n"
"v_exp_f32 v52, v52\n"
"v_cvt_pk_bf16_f32 v71, v46, v47\n"
"v_mfma_f32_16x16x32_bf16 a[144:147], a[128:131], v[24:27], 0\n"
"v_exp_f32 v53, v53\n"
"buffer_load_dwordx4 v225, s[8:11], s44 offen offset:3072 sc1 lds\n"
"v_cvt_pk_bf16_f32 v74, v52, v53\n"
"v_exp_f32 v54, v54\n"
"v_exp_f32 v55, v55\n"
"s_nop 0\n"
"v_cvt_pk_bf16_f32 v75, v54, v55\n"
"ds_read_b128 a[76:79], v226 offset:3072\n"
"ds_read_b128 a[80:83], v226 offset:4096\n"
"ds_read_b128 a[84:87], v226 offset:5120\n"
"s_branch LDB_%=\n"
"LD2_%=:\n"
"s_waitcnt lgkmcnt(5)\n"
"v_mfma_f32_16x16x32_bf16 v[0:3], a[64:67], a[32:35], v[208:211]\n"
"s_mov_b32 m0, s40\n"
"v_exp_f32 v56, v56\n"
"v_mfma_f32_16x16x32_bf16 v[8:11], a[64:67], a[48:51], 0\n"
"s_waitcnt lgkmcnt(2)\n"
"ds_read_b128 a[88:91], v226 offset:6144\n"
"v_readlane_b32 s43, v228, s42\n"
"v_mfma_f32_16x16x32_bf16 v[0:3], a[68:71], a[36:39], v[0:3]\n"
"v_exp_f32 v57, v57\n"
"v_mfma_f32_16x16x32_bf16 v[8:11], a[68:71], a[52:55], v[8:11]\n"
"ds_read_b128 a[92:95], v226 offset:7168\n"
"ds_read_b128 a[96:99], v226 offset:8192\n"
"v_mfma_f32_16x16x32_bf16 v[0:3], a[72:75], a[40:43], v[0:3]\n"
"s_add_u32 s41, s41, 32768\n"
"buffer_load_dwordx4 v225, s[8:11], s43 offen offset:0 sc1 lds\n"
"v_readlane_b32 s44, v229, s42\n"
"v_mfma_f32_16x16x32_bf16 v[8:11], a[72:75], a[56:59], v[8:11]\n"
"s_add_u32 s42, s42, 1\n"
"v_exp_f32 v58, v58\n"
"v_mfma_f32_16x16x32_bf16 v[0:3], a[76:79], a[44:47], v[0:3]\n"
"s_cmp_eq_u32 s41, 163840\n"
"ds_read_b128 a[100:103], v226 offset:9216\n"
"v_cvt_pk_bf16_f32 v76, v56, v57\n"
"v_mfma_f32_16x16x32_bf16 v[8:11], a[76:79], a[60:63], v[8:11]\n"
"s_waitcnt lgkmcnt(4)\n"
"v_exp_f32 v59, v59\n"
"v_mfma_f32_16x16x32_bf16 v[4:7], a[80:83], a[32:35], v[212:215]\n"
"s_cselect_b32 s41, 0, s41\n"
"v_exp_f32 v60, v60\n"
"v_add_u32 v227, s41, v224\n"
"v_mfma_f32_16x16x32_bf16 v[12:15], a[80:83], a[48:51], 0\n"
"buffer_load_dwordx4 v225, s[8:11], s43 offen offset:1024 sc1 lds\n"
"v_mfma_f32_16x16x32_bf16 v[4:7], a[84:87], a[36:39], v[4:7]\n"
"ds_read_b128 a[104:107], v226 offset:10240\n"
"v_cvt_pk_bf16_f32 v77, v58, v59\n"
"v_mfma_f32_16x16x32_bf16 v[12:15], a[84:87], a[52:55], v[12:15]\n"
"s_waitcnt lgkmcnt(2)\n"
"v_exp_f32 v61, v61\n"
"v_mfma_f32_16x16x32_bf16 v[4:7], a[88:91], a[40:43], v[4:7]\n"
"ds_read_b128 a[108:111], v226 offset:11264\n"
"ds_read_b128 a[112:115], v226 offset:12288\n"
"v_mfma_f32_16x16x32_bf16 v[12:15], a[88:91], a[56:59], v[12:15]\n"
"v_exp_f32 v62, v62\n"
"v_mfma_f32_16x16x32_bf16 v[4:7], a[92:95], a[44:47], v[4:7]\n"
"buffer_load_dwordx4 v225, s[8:11], s43 offen offset:2048 sc1 lds\n"
"v_cvt_pk_bf16_f32 v78, v60, v61\n"
"v_mfma_f32_16x16x32_bf16 v[12:15], a[92:95], a[60:63], v[12:15]\n"
"ds_read_b128 a[116:119], v226 offset:13312\n"
"v_mfma_f32_16x16x32_bf16 v[16:19], a[96:99], a[32:35], v[216:219]\n"
"v_exp_f32 v63, v63\n"
"v_mfma_f32_16x16x32_bf16 v[24:27], a[96:99], a[48:51], 0\n"
"s_waitcnt lgkmcnt(3)\n"
"v_exp_f32 v0, v0\n"
"v_mfma_f32_16x16x32_bf16 v[16:19], a[100:103], a[36:39], v[16:19]\n"
"ds_read_b128 a[120:123], v226 offset:14336\n"
"ds_read_b128 a[124:127], v226 offset:15360\n"
"v_cvt_pk_bf16_f32 v79, v62, v63\n"
"v_mfma_f32_16x16x32_bf16 v[24:27], a[100:103], a[52:55], v[24:27]\n"
"buffer_load_dwordx4 v225, s[8:11], s43 offen offset:3072 sc1 lds\n"
"v_mfma_f32_16x16x32_bf16 v[16:19], a[104:107], a[40:43], v[16:19]\n"
"s_add_u32 m0, s40, 16384\n"
"ds_read_b128 a[64:67], v226 offset:16384\n"
"v_mfma_f32_16x16x32_bf16 v[24:27], a[104:107], a[56:59], v[24:27]\n"
"s_waitcnt lgkmcnt(4)\n"
"ds_read_b128 a[68:71], v226 offset:17408\n"
"v_mfma_f32_16x16x32_bf16 v[16:19], a[108:111], a[44:47], v[16:19]\n"
"ds_read_b128 a[72:75], v226 offset:18432\n"
"v_mfma_f32_16x16x32_bf16 v[24:27], a[108:111], a[60:63], v[24:27]\n"
"ds_read_b128 a[76:79], v226 offset:19456\n"
"v_mfma_f32_16x16x32_bf16 v[20:23], a[112:115], a[32:35], v[220:223]\n"
"s_add_u32 s40, s40, 32768\n"
"ds_read_b128 a[80:83], v226 offset:20480\n"
"v_mfma_f32_16x16x32_bf16 v[28:31], a[112:115], a[48:51], 0\n"
"s_waitcnt lgkmcnt(5)\n"
"buffer_load_dwordx4 v225, s[8:11], s44 offen offset:0 sc1 lds\n"
"v_mfma_f32_16x16x32_bf16 v[20:23], a[116:119], a[36:39], v[20:23]\n"
"s_cmp_eq_u32 s40, s46\n"
"ds_read_b128 a[84:87], v226 offset:21504\n"
"s_cselect_b32 s40, s47, s40\n"
"v_mfma_f32_16x16x32_bf16 v[28:31], a[116:119], a[52:55], v[28:31]\n"
"ds_read_b128 a[88:91], v226 offset:22528\n"
"v_mfma_f32_16x16x32_bf16 v[20:23], a[120:123], a[40:43], v[20:23]\n"
"ds_read_b128 a[92:95], v226 offset:23552\n"
"v_mfma_f32_16x16x32_bf16 v[28:31], a[120:123], a[56:59], v[28:31]\n"
"ds_read_b128 a[96:99], v226 offset:24576\n"
"v_mfma_f32_16x16x32_bf16 v[20:23], a[124:127], a[44:47], v[20:23]\n"
"ds_read_b128 a[100:103], v226 offset:25600\n"
"v_mfma_f32_16x16x32_bf16 v[28:31], a[124:127], a[60:63], v[28:31]\n"
"s_waitcnt lgkmcnt(5)\n"
"buffer_load_dwordx4 v225, s[8:11], s44 offen offset:1024 sc1 lds\n"
"v_mfma_f32_16x16x32_bf16 v[40:43], a[64:67], a[48:51], v[208:211]\n"
"ds_read_b128 a[104:107], v226 offset:26624\n"
"v_mfma_f32_16x16x32_bf16 v[40:43], a[68:71], a[52:55], v[40:43]\n"
"ds_read_b128 a[108:111], v226 offset:27648\n"
"v_mfma_f32_16x16x32_bf16 v[40:43], a[72:75], a[56:59], v[40:43]\n"
"ds_read_b128 a[112:115], v226 offset:28672\n"
"v_mfma_f32_16x16x32_bf16 v[40:43], a[76:79], a[60:63], v[40:43]\n"
"v_exp_f32 v1, v1\n"
"v_mfma_f32_16x16x32_bf16 v[44:47], a[80:83], a[48:51], v[212:215]\n"
"s_waitcnt lgkmcnt(4)\n"
"ds_read_b128 a[116:119], v226 offset:29696\n"
"ds_read_b128 a[120:123], v226 offset:30720\n"
"v_cvt_pk_bf16_f32 v0, v0, v1\n"
"v_mfma_f32_16x16x32_bf16 v[44:47], a[84:87], a[52:55], v[44:47]\n"
"buffer_load_dwordx4 v225, s[8:11], s44 offen offset:2048 sc1 lds\n"
"v_mfma_f32_16x16x32_bf16 v[44:47], a[88:91], a[56:59], v[44:47]\n"
"ds_read_b128 a[124:127], v226 offset:31744\n"
"v_readlane_b32 s43, v228, s42\n"
"v_mfma_f32_16x16x32_bf16 v[44:47], a[92:95], a[60:63], v[44:47]\n"
"v_exp_f32 v2, v2\n"
"v_mfma_f32_16x16x32_bf16 v[56:59], a[96:99], a[48:51], v[216:219]\n"
"s_waitcnt lgkmcnt(4)\n"
"s_barrier\n"
"v_mfma_f32_16x16x32_bf16 v[56:59], a[100:103], a[52:55], v[56:59]\n"
"ds_read_b128 a[96:99], v227 offset:8192\n"
"v_mfma_f32_16x16x32_bf16 v[56:59], a[104:107], a[56:59], v[56:59]\n"
"buffer_load_dwordx4 v225, s[8:11], s44 offen offset:3072 sc1 lds\n"
"v_mfma_f32_16x16x32_bf16 v[56:59], a[108:111], a[60:63], v[56:59]\n"
"s_waitcnt lgkmcnt(2)\n"
"v_exp_f32 v3, v3\n"
"v_mfma_f32_16x16x32_bf16 v[60:63], a[112:115], a[48:51], v[220:223]\n"
"s_mov_b32 m0, s40\n"
"ds_read_b128 a[100:103], v227 offset:9216\n"
"ds_read_b128 a[104:107], v227 offset:10240\n"
"v_cvt_pk_bf16_f32 v1, v2, v3\n"
"v_mfma_f32_16x16x32_bf16 v[60:63], a[116:119], a[52:55], v[60:63]\n"
"v_exp_f32 v8, v8\n"
"v_readlane_b32 s44, v229, s42\n"
"v_mfma_f32_16x16x32_bf16 v[60:63], a[120:123], a[56:59], v[60:63]\n"
"s_waitcnt lgkmcnt(3)\n"
"v_exp_f32 v9, v9\n"
"v_mfma_f32_16x16x32_bf16 v[60:63], a[124:127], a[60:63], v[60:63]\n"
"s_waitcnt lgkmcnt(2)\n"
"ds_read_b128 a[108:111], v227 offset:11264\n"
"v_cvt_pk_bf16_f32 v8, v8, v9\n"
"v_mfma_f32_16x16x32_bf16 v[112:115], a[96:99], v[68:71], v[112:115]\n"
"s_add_u32 s42, s42, 1\n"
"buffer_load_dwordx4 v225, s[8:11], s43 offen offset:0 sc1 lds\n"
"v_mfma_f32_16x16x32_bf16 v[144:147], a[96:99], v[72:75], v[144:147]\n"
"s_add_u32 s41, s41, 32768\n"
"v_exp_f32 v10, v10\n"
"v_mfma_f32_16x16x32_bf16 v[176:179], a[96:99], v[76:79], v[176:179]\n"
"s_waitcnt lgkmcnt(2)\n"
"v_exp_f32 v11, v11\n"
"v_mfma_f32_16x16x32_bf16 v[116:119], a[100:103], v[68:71], v[116:119]\n"
"s_cmp_eq_u32 s41, 163840\n"
"v_exp_f32 v4, v4\n"
"v_cvt_pk_bf16_f32 v9, v10, v11\n"
"v_mfma_f32_16x16x32_bf16 v[148:151], a[100:103], v[72:75], v[148:151]\n"
"s_cselect_b32 s41, 0, s41\n"
"v_exp_f32 v5, v5\n"
"v_add_u32 v226, s41, v224\n"
"v_mfma_f32_16x16x32_bf16 v[180:183], a[100:103], v[76:79], v[180:183]\n"
"s_waitcnt lgkmcnt(1)\n"
"ds_read_b128 a[112:115], v227 offset:12288\n"
"ds_read_b128 a[116:119], v227 offset:13312\n"
"v_cvt_pk_bf16_f32 v2, v4, v5\n"
"v_mfma_f32_16x16x32_bf16 v[120:123], a[104:107], v[68:71], v[120:123]\n"
"buffer_load_dwordx4 v225, s[8:11], s43 offen offset:1024 sc1 lds\n"
"v_mfma_f32_16x16x32_bf16 v[152:155], a[104:107], v[72:75], v[152:155]\n"
"v_exp_f32 v6, v6\n"
"v_mfma_f32_16x16x32_bf16 v[184:187], a[104:107], v[76:79], v[184:187]\n"
"s_waitcnt lgkmcnt(2)\n"
"v_exp_f32 v7, v7\n"
"v_mfma_f32_16x16x32_bf16 v[124:127], a[108:111], v[68:71], v[124:127]\n"
"ds_read_b128 a[120:123], v227 offset:14336\n"
"ds_read_b128 a[124:127], v227 offset:15360\n"
"v_cvt_pk_bf16_f32 v3, v6, v7\n"
"v_mfma_f32_16x16x32_bf16 v[156:159], a[108:111], v[72:75], v[156:159]\n"
"buffer_load_dwordx4 v225, s[8:11], s43 offen offset:2048 sc1 lds\n"
"v_mfma_f32_16x16x32_bf16 v[188:191], a[108:111], v[76:79], v[188:191]\n"
"s_waitcnt lgkmcnt(3)\n"
"v_exp_f32 v12, v12\n"
"v_mfma_f32_16x16x32_bf16 v[128:131], a[112:115], v[68:71], v[128:131]\n"
"v_exp_f32 v13, v13\n"
"v_mfma_f32_16x16x32_bf16 v[160:163], a[112:115], v[72:75], v[160:163]\n"
"v_exp_f32 v14, v14\n"
"v_cvt_pk_bf16_f32 v10, v12, v13\n"
"v_mfma_f32_16x16x32_bf16 v[192:195], a[112:115], v[76:79], v[192:195]\n"
"s_waitcnt lgkmcnt(2)\n"
"buffer_load_dwordx4 v225, s[8:11], s43 offen offset:3072 sc1 lds\n"
"v_mfma_f32_16x16x32_bf16 v[132:135], a[116:119], v[68:71], v[132:135]\n"
"s_add_u32 m0, s40, 16384\n"
"v_exp_f32 v15, v15\n"
"v_mfma_f32_16x16x32_bf16 v[164:167], a[116:119], v[72:75], v[164:167]\n"
"ds_read_b128 a[64:67], v227 offset:16384\n"
"v_cvt_pk_bf16_f32 v11, v14, v15\n"
"v_mfma_f32_16x16x32_bf16 v[196:199], a[116:119], v[76:79], v[196:199]\n"
"s_waitcnt lgkmcnt(1)\n"
"v_exp_f32 v16, v16\n"
"v_mfma_f32_16x16x32_bf16 v[136:139], a[120:123], v[68:71], v[136:139]\n"
"s_add_u32 s40, s40, 32768\n"
"v_exp_f32 v17, v17\n"
"v_mfma_f32_16x16x32_bf16 v[168:171], a[120:123], v[72:75], v[168:171]\n"
"s_cmp_eq_u32 s40, s46\n"
"buffer_load_dwordx4 v225, s[8:11], s44 offen offset:0 sc1 lds\n"
"v_cvt_pk_bf16_f32 v16, v16, v17\n"
"v_mfma_f32_16x16x32_bf16 v[200:203], a[120:123], v[76:79], v[200:203]\n"
"s_cselect_b32 s40, s47, s40\n"
"ds_read_b128 a[68:71], v227 offset:17408\n"
"v_mfma_f32_16x16x32_bf16 v[140:143], a[124:127], v[68:71], v[140:143]\n"
"v_exp_f32 v18, v18\n"
"v_mfma_f32_16x16x32_bf16 v[172:175], a[124:127], v[72:75], v[172:175]\n"
"ds_read_b128 a[72:75], v227 offset:18432\n"
"ds_read_b128 a[76:79], v227 offset:19456\n"
"v_mfma_f32_16x16x32_bf16 v[204:207], a[124:127], v[76:79], v[204:207]\n"
"v_exp_f32 v19, v19\n"
"v_mfma_f32_16x16x32_bf16 a[136:139], a[128:131], v[68:71], a[136:139]\n"
"buffer_load_dwordx4 v225, s[8:11], s44 offen offset:1024 sc1 lds\n"
"v_cvt_pk_bf16_f32 v17, v18, v19\n"
"v_mfma_f32_16x16x32_bf16 a[140:143], a[128:131], v[72:75], a[140:143]\n"
"ds_read_b128 a[80:83], v227 offset:20480\n"
"v_mfma_f32_16x16x32_bf16 a[144:147], a[128:131], v[76:79], a[144:147]\n"
"s_waitcnt lgkmcnt(4)\n"
"v_exp_f32 v24, v24\n"
"v_mfma_f32_16x16x32_bf16 v[144:147], a[64:67], v[0:3], v[144:147]\n"
"v_exp_f32 v25, v25\n"
"v_mfma_f32_16x16x32_bf16 v[176:179], a[64:67], v[8:11], v[176:179]\n"
"s_waitcnt lgkmcnt(3)\n"
"v_exp_f32 v26, v26\n"
"v_cvt_pk_bf16_f32 v24, v24, v25\n"
"v_mfma_f32_16x16x32_bf16 v[148:151], a[68:71], v[0:3], v[148:151]\n"
"s_add_u32 s51, s59, 16384\n"
"ds_read_b128 a[84:87], v227 offset:21504\n"
"ds_read_b128 a[88:91], v227 offset:22528\n"
"v_mfma_f32_16x16x32_bf16 v[180:183], a[68:71], v[8:11], v[180:183]\n"
"s_waitcnt lgkmcnt(3)\n"
"buffer_load_dwordx4 v225, s[8:11], s44 offen offset:2048 sc1 lds\n"
"v_mfma_f32_16x16x32_bf16 v[152:155], a[72:75], v[0:3], v[152:155]\n"
"ds_read_b128 a[92:95], v227 offset:23552\n"
"v_accvgpr_read_b32 v252, a136\n"
"v_mfma_f32_16x16x32_bf16 v[184:187], a[72:75], v[8:11], v[184:187]\n"
"v_exp_f32 v27, v27\n"
"v_subrev_u32 v253, 0x2b800000, v252\n"
"v_mfma_f32_16x16x32_bf16 v[156:159], a[76:79], v[0:3], v[156:159]\n"
"v_exp_f32 v20, v20\n"
"v_readlane_b32 s43, v228, s42\n"
"v_mfma_f32_16x16x32_bf16 v[188:191], a[76:79], v[8:11], v[188:191]\n"
"s_waitcnt lgkmcnt(3)\n"
"v_exp_f32 v21, v21\n"
"v_cvt_pk_bf16_f32 v25, v26, v27\n"
"v_mfma_f32_16x16x32_bf16 v[160:163], a[80:83], v[0:3], v[160:163]\n"
"v_cmp_le_u32 vcc, 0x34000000, v253\n"
"s_cmp_lg_u64 vcc, 0\n"
"s_cbranch_scc1 L1035_%=\n"
"L1034_%=:\n"
"v_mfma_f32_16x16x32_bf16 v[192:195], a[80:83], v[8:11], v[192:195]\n"
"s_waitcnt lgkmcnt(1)\n"
"buffer_load_dwordx4 v225, s[8:11], s44 offen offset:3072 sc1 lds\n"
"v_cvt_pk_bf16_f32 v18, v20, v21\n"
"v_mfma_f32_16x16x32_bf16 v[164:167], a[84:87], v[0:3], v[164:167]\n"
"s_mov_b32 m0, s40\n"
"v_rcp_f32 v252, v252\n"
"v_mfma_f32_16x16x32_bf16 v[196:199], a[84:87], v[8:11], v[196:199]\n"
"ds_read_b128 a[96:99], v227 offset:24576\n"
"ds_read_b128 a[100:103], v227 offset:25600\n"
"v_readlane_b32 s44, v229, s42\n"
"v_mfma_f32_16x16x32_bf16 v[168:171], a[88:91], v[0:3], v[168:171]\n"
"s_add_u32 s42, s42, 1\n"
"v_exp_f32 v22, v22\n"
"v_mul_f32 v112, v252, v112\n"
"v_mfma_f32_16x16x32_bf16 v[200:203], a[88:91], v[8:11], v[200:203]\n"
"s_waitcnt lgkmcnt(2)\n"
"v_exp_f32 v23, v23\n"
"v_mul_f32 v113, v252, v113\n"
"v_mfma_f32_16x16x32_bf16 v[172:175], a[92:95], v[0:3], v[172:175]\n"
"ds_read_b128 a[104:107], v227 offset:26624\n"
"ds_read_b128 a[108:111], v227 offset:27648\n"
"v_mul_f32 v114, v252, v114\n"
"v_mfma_f32_16x16x32_bf16 v[204:207], a[92:95], v[8:11], v[204:207]\n"
"v_cvt_pk_bf16_f32 v19, v22, v23\n"
"v_mul_f32 v115, v252, v115\n"
"v_mul_f32 v116, v252, v116\n"
"v_mfma_f32_16x16x32_bf16 a[140:143], a[128:131], v[0:3], a[140:143]\n"
"v_exp_f32 v28, v28\n"
"v_mul_f32 v117, v252, v117\n"
"v_mfma_f32_16x16x32_bf16 a[144:147], a[128:131], v[8:11], a[144:147]\n"
"s_waitcnt lgkmcnt(3)\n"
"v_exp_f32 v29, v29\n"
"v_mul_f32 v118, v252, v118\n"
"v_mfma_f32_16x16x32_bf16 v[144:147], a[96:99], v[16:19], v[144:147]\n"
"v_exp_f32 v30, v30\n"
"v_cvt_pk_bf16_f32 v26, v28, v29\n"
"v_exp_f32 v31, v31\n"
"s_nop 0\n"
"v_cvt_pk_bf16_f32 v27, v30, v31\n"
"s_nop 1\n"
"v_mfma_f32_16x16x32_bf16 v[176:179], a[96:99], v[24:27], v[176:179]\n"
"s_waitcnt lgkmcnt(2)\n"
"ds_read_b128 a[112:115], v227 offset:28672\n"
"ds_read_b128 a[116:119], v227 offset:29696\n"
"v_mul_f32 v119, v252, v119\n"
"v_mfma_f32_16x16x32_bf16 v[148:151], a[100:103], v[16:19], v[148:151]\n"
"ds_read_b128 a[120:123], v227 offset:30720\n"
"v_cvt_pk_bf16_f32 v112, v112, v113\n"
"v_cvt_pk_bf16_f32 v113, v114, v115\n"
"v_mfma_f32_16x16x32_bf16 v[180:183], a[100:103], v[24:27], v[180:183]\n"
"s_waitcnt lgkmcnt(3)\n"
"v_exp_f32 v40, v40\n"
"v_cvt_pk_bf16_f32 v114, v116, v117\n"
"v_mfma_f32_16x16x32_bf16 v[152:155], a[104:107], v[16:19], v[152:155]\n"
"s_add_u32 s41, s41, 32768\n"
"ds_read_b128 a[124:127], v227 offset:31744\n"
"v_cvt_pk_bf16_f32 v115, v118, v119\n"
"v_mul_f32 v120, v252, v120\n"
"v_mfma_f32_16x16x32_bf16 v[184:187], a[104:107], v[24:27], v[184:187]\n"
"s_cmp_eq_u32 s41, 163840\n"
"buffer_store_dwordx4 v[112:115], v224, s[8:11], s51 offen offset:0 sc1\n"
"v_mul_f32 v121, v252, v121\n"
"v_mfma_f32_16x16x32_bf16 v[156:159], a[108:111], v[16:19], v[156:159]\n"
"s_cselect_b32 s41, 0, s41\n"
"v_exp_f32 v41, v41\n"
"v_mul_f32 v122, v252, v122\n"
"v_mfma_f32_16x16x32_bf16 v[188:191], a[108:111], v[24:27], v[188:191]\n"
"s_waitcnt lgkmcnt(3)\n"
"s_waitcnt vmcnt(21)\n"
"s_barrier\n"
"v_mfma_f32_16x16x32_bf16 v[160:163], a[112:115], v[16:19], v[160:163]\n"
"buffer_load_dwordx4 v225, s[8:11], s43 offen offset:0 sc1 lds\n"
"v_add_u32 v227, s41, v224\n"
"v_mfma_f32_16x16x32_bf16 v[192:195], a[112:115], v[24:27], v[192:195]\n"
"s_waitcnt lgkmcnt(1)\n"
"buffer_load_dwordx4 v225, s[8:11], s43 offen offset:1024 sc1 lds\n"
"v_mul_f32 v123, v252, v123\n"
"v_mfma_f32_16x16x32_bf16 v[164:167], a[116:119], v[16:19], v[164:167]\n"
"buffer_load_dwordx4 v225, s[8:11], s43 offen offset:2048 sc1 lds\n"
"v_mul_f32 v124, v252, v124\n"
"v_mfma_f32_16x16x32_bf16 v[196:199], a[116:119], v[24:27], v[196:199]\n"
"ds_read_b128 a[64:67], v226 offset:0\n"
"v_mul_f32 v125, v252, v125\n"
"v_mul_f32 v126, v252, v126\n"
"v_mfma_f32_16x16x32_bf16 v[168:171], a[120:123], v[16:19], v[168:171]\n"
"ds_read_b128 a[68:71], v226 offset:1024\n"
"v_mul_f32 v127, v252, v127\n"
"v_cvt_pk_bf16_f32 v120, v120, v121\n"
"v_mfma_f32_16x16x32_bf16 v[200:203], a[120:123], v[24:27], v[200:203]\n"
"s_waitcnt lgkmcnt(2)\n"
"ds_read_b128 a[72:75], v226 offset:2048\n"
"v_cvt_pk_bf16_f32 v121, v122, v123\n"
"v_cvt_pk_bf16_f32 v122, v124, v125\n"
"v_mfma_f32_16x16x32_bf16 v[172:175], a[124:127], v[16:19], v[172:175]\n"
"ds_read_b128 a[76:79], v226 offset:3072\n"
"v_cvt_pk_bf16_f32 v123, v126, v127\n"
"v_mul_f32 v128, v252, v128\n"
"v_mfma_f32_16x16x32_bf16 v[204:207], a[124:127], v[24:27], v[204:207]\n"
"v_exp_f32 v42, v42\n"
"v_cvt_pk_bf16_f32 v68, v40, v41\n"
"v_mfma_f32_16x16x32_bf16 a[140:143], a[128:131], v[16:19], a[140:143]\n"
"ds_read_b128 a[80:83], v226 offset:4096\n"
"ds_read_b128 a[84:87], v226 offset:5120\n"
"v_mul_f32 v129, v252, v129\n"
"v_mfma_f32_16x16x32_bf16 a[144:147], a[128:131], v[24:27], a[144:147]\n"
"ds_read_b128 a[88:91], v226 offset:6144\n"
"v_mul_f32 v130, v252, v130\n"
"v_mul_f32 v131, v252, v131\n"
"v_exp_f32 v44, v44\n"
"v_exp_f32 v45, v45\n"
"s_nop 0\n"
"v_cvt_pk_bf16_f32 v70, v44, v45\n"
"v_exp_f32 v43, v43\n"
"s_nop 0\n"
"v_cvt_pk_bf16_f32 v69, v42, v43\n"
"v_exp_f32 v46, v46\n"
"v_exp_f32 v47, v47\n"
"s_nop 0\n"
"v_cvt_pk_bf16_f32 v71, v46, v47\n"
"s_waitcnt lgkmcnt(6)\n"
"s_nop 0\n"
"v_mfma_f32_16x16x32_bf16 v[176:179], a[64:67], v[68:71], v[176:179]\n"
"s_waitcnt lgkmcnt(5)\n"
"ds_read_b128 a[92:95], v226 offset:7168\n"
"ds_read_b128 a[96:99], v226 offset:8192\n"
"v_mul_f32 v132, v252, v132\n"
"v_mfma_f32_16x16x32_bf16 v[180:183], a[68:71], v[68:71], v[180:183]\n"
"s_waitcnt lgkmcnt(6)\n"
"buffer_store_dwordx4 v[120:123], v224, s[8:11], s51 offen offset:1024 sc1\n"
"v_mul_f32 v133, v252, v133\n"
"v_mfma_f32_16x16x32_bf16 v[184:187], a[72:75], v[68:71], v[184:187]\n"
"s_waitcnt lgkmcnt(5)\n"
"buffer_load_dwordx4 v225, s[8:11], s43 offen offset:3072 sc1 lds\n"
"v_mul_f32 v134, v252, v134\n"
"v_mfma_f32_16x16x32_bf16 v[188:191], a[76:79], v[68:71], v[188:191]\n"
"s_waitcnt lgkmcnt(4)\n"
"ds_read_b128 a[100:103], v226 offset:9216\n"
"v_accvgpr_read_b32 v251, a140\n"
"v_mul_f32 v135, v252, v135\n"
"v_mfma_f32_16x16x32_bf16 v[192:195], a[80:83], v[68:71], v[192:195]\n"
"s_waitcnt lgkmcnt(4)\n"
"ds_read_b128 a[104:107], v226 offset:10240\n"
"v_subrev_u32 v253, 0x2b800000, v251\n"
"v_cvt_pk_bf16_f32 v128, v128, v129\n"
"v_mfma_f32_16x16x32_bf16 v[196:199], a[84:87], v[68:71], v[196:199]\n"
"s_waitcnt lgkmcnt(4)\n"
"ds_read_b128 a[108:111], v226 offset:11264\n"
"v_cvt_pk_bf16_f32 v129, v130, v131\n"
"v_cvt_pk_bf16_f32 v130, v132, v133\n"
"v_mfma_f32_16x16x32_bf16 v[200:203], a[88:91], v[68:71], v[200:203]\n"
"s_waitcnt lgkmcnt(4)\n"
"ds_read_b128 a[112:115], v226 offset:12288\n"
"v_cvt_pk_bf16_f32 v131, v134, v135\n"
"v_mul_f32 v136, v252, v136\n"
"v_mfma_f32_16x16x32_bf16 v[204:207], a[92:95], v[68:71], v[204:207]\n"
"s_add_u32 m0, s40, 16384\n"
"ds_read_b128 a[116:119], v226 offset:13312\n"
"v_mul_f32 v137, v252, v137\n"
"v_mul_f32 v138, v252, v138\n"
"v_mfma_f32_16x16x32_bf16 a[144:147], a[128:131], v[68:71], a[144:147]\n"
"s_add_u32 s40, s40, 32768\n"
"ds_read_b128 a[120:123], v226 offset:14336\n"
"v_mul_f32 v139, v252, v139\n"
"v_mul_f32 v140, v252, v140\n"
"v_exp_f32 v57, v57\n"
"v_exp_f32 v56, v56\n"
"s_nop 0\n"
"v_cvt_pk_bf16_f32 v76, v56, v57\n"
"v_exp_f32 v59, v59\n"
"v_exp_f32 v58, v58\n"
"s_nop 0\n"
"v_cvt_pk_bf16_f32 v77, v58, v59\n"
"v_exp_f32 v62, v62\n"
"v_exp_f32 v63, v63\n"
"s_nop 0\n"
"v_cvt_pk_bf16_f32 v79, v62, v63\n"
"v_exp_f32 v60, v60\n"
"v_exp_f32 v61, v61\n"
"s_nop 0\n"
"v_cvt_pk_bf16_f32 v78, v60, v61\n"
"s_waitcnt lgkmcnt(6)\n"
"s_nop 0\n"
"v_mfma_f32_16x16x32_bf16 v[176:179], a[96:99], v[76:79], v[176:179]\n"
"s_waitcnt lgkmcnt(5)\n"
"ds_read_b128 a[124:127], v226 offset:15360\n"
"v_mul_f32 v141, v252, v141\n"
"v_mul_f32 v142, v252, v142\n"
"v_mfma_f32_16x16x32_bf16 v[180:183], a[100:103], v[76:79], v[180:183]\n"
"s_waitcnt lgkmcnt(5)\n"
"buffer_store_dwordx4 v[128:131], v224, s[8:11], s51 offen offset:2048 sc1\n"
"v_mul_f32 v143, v252, v143\n"
"v_mfma_f32_16x16x32_bf16 v[184:187], a[104:107], v[76:79], v[184:187]\n"
"s_waitcnt lgkmcnt(4)\n"
"buffer_load_dwordx4 v225, s[8:11], s44 offen offset:0 sc1 lds\n"
"v_cvt_pk_bf16_f32 v136, v136, v137\n"
"v_mfma_f32_16x16x32_bf16 v[188:191], a[108:111], v[76:79], v[188:191]\n"
"s_waitcnt lgkmcnt(3)\n"
"buffer_load_dwordx4 v225, s[8:11], s44 offen offset:1024 sc1 lds\n"
"v_cvt_pk_bf16_f32 v137, v138, v139\n"
"v_mfma_f32_16x16x32_bf16 v[192:195], a[112:115], v[76:79], v[192:195]\n"
"s_waitcnt lgkmcnt(2)\n"
"v_cmp_le_u32 vcc, 0x34000000, v253\n"
"s_cmp_lg_u64 vcc, 0\n"
"s_cbranch_scc1 L1041_%=\n"
"L1040_%=:\n"
"v_mfma_f32_16x16x32_bf16 v[196:199], a[116:119], v[76:79], v[196:199]\n"
"s_waitcnt lgkmcnt(1)\n"
"v_rcp_f32 v251, v251\n"
"v_cvt_pk_bf16_f32 v138, v140, v141\n"
"v_mfma_f32_16x16x32_bf16 v[200:203], a[120:123], v[76:79], v[200:203]\n"
"s_waitcnt lgkmcnt(0)\n"
"v_mul_f32 v144, v251, v144\n"
"v_mul_f32 v145, v251, v145\n"
"v_mul_f32 v152, v251, v152\n"
"v_mfma_f32_16x16x32_bf16 v[204:207], a[124:127], v[76:79], v[204:207]\n"
"s_cmp_eq_u32 s40, s46\n"
"v_mul_f32 v153, v251, v153\n"
"v_mul_f32 v160, v251, v160\n"
"v_mul_f32 v146, v251, v146\n"
"v_mfma_f32_16x16x32_bf16 a[144:147], a[128:131], v[76:79], a[144:147]\n"
"s_cselect_b32 s40, s47, s40\n"
"v_mul_f32 v161, v251, v161\n"
"v_mul_f32 v147, v251, v147\n"
"v_cvt_pk_bf16_f32 v144, v144, v145\n"
"v_cvt_pk_bf16_f32 v139, v142, v143\n"
"buffer_store_dwordx4 v[136:139], v224, s[8:11], s51 offen offset:3072 sc1\n"
"v_mul_f32 v148, v251, v148\n"
"v_mul_f32 v149, v251, v149\n"
"v_mul_f32 v150, v251, v150\n"
"v_mul_f32 v151, v251, v151\n"
"v_cvt_pk_bf16_f32 v145, v146, v147\n"
"v_cvt_pk_bf16_f32 v146, v148, v149\n"
"v_cvt_pk_bf16_f32 v147, v150, v151\n"
"buffer_store_dwordx4 v[144:147], v224, s[8:11], s60 offen offset:0 sc1\n"
"v_mul_f32 v154, v251, v154\n"
"v_mul_f32 v155, v251, v155\n"
"v_mul_f32 v156, v251, v156\n"
"v_mul_f32 v157, v251, v157\n"
"v_mul_f32 v158, v251, v158\n"
"v_mul_f32 v159, v251, v159\n"
"v_cvt_pk_bf16_f32 v152, v152, v153\n"
"v_cvt_pk_bf16_f32 v153, v154, v155\n"
"v_cvt_pk_bf16_f32 v154, v156, v157\n"
"v_cvt_pk_bf16_f32 v155, v158, v159\n"
"buffer_store_dwordx4 v[152:155], v224, s[8:11], s60 offen offset:1024 sc1\n"
"v_mul_f32 v162, v251, v162\n"
"v_mul_f32 v163, v251, v163\n"
"v_mul_f32 v164, v251, v164\n"
"v_mul_f32 v165, v251, v165\n"
"v_mul_f32 v166, v251, v166\n"
"v_mul_f32 v167, v251, v167\n"
"v_cvt_pk_bf16_f32 v160, v160, v161\n"
"v_cvt_pk_bf16_f32 v161, v162, v163\n"
"v_cvt_pk_bf16_f32 v162, v164, v165\n"
"v_cvt_pk_bf16_f32 v163, v166, v167\n"
"buffer_store_dwordx4 v[160:163], v224, s[8:11], s60 offen offset:2048 sc1\n"
"v_mul_f32 v168, v251, v168\n"
"v_mul_f32 v169, v251, v169\n"
"v_mul_f32 v170, v251, v170\n"
"v_mul_f32 v171, v251, v171\n"
"v_mul_f32 v172, v251, v172\n"
"v_mul_f32 v173, v251, v173\n"
"v_mul_f32 v174, v251, v174\n"
"v_mul_f32 v175, v251, v175\n"
"v_cvt_pk_bf16_f32 v168, v168, v169\n"
"v_cvt_pk_bf16_f32 v169, v170, v171\n"
"v_cvt_pk_bf16_f32 v170, v172, v173\n"
"v_cvt_pk_bf16_f32 v171, v174, v175\n"
"buffer_store_dwordx4 v[168:171], v224, s[8:11], s60 offen offset:3072 sc1\n"
"buffer_load_dwordx4 v225, s[8:11], s44 offen offset:2048 sc1 lds\n"
"buffer_load_dwordx4 v225, s[8:11], s44 offen offset:3072 sc1 lds\n"
"s_waitcnt vmcnt(24)\n"
"s_barrier\n"
"ds_read_b128 a[64:67], v227 offset:0\n"
"ds_read_b128 a[68:71], v227 offset:1024\n"
"ds_read_b128 a[72:75], v227 offset:2048\n"
"ds_read_b128 a[76:79], v227 offset:3072\n"
"ds_read_b128 a[80:83], v227 offset:4096\n"
"ds_read_b128 a[84:87], v227 offset:5120\n"
"s_branch LTL_%=\n"
"LEV_%=:\n"
"s_mov_b32 s56, 0\n"
"s_cmp_eq_u32 s57, 1\n"
"s_cbranch_scc0 L1067_%=\n"
"s_mov_b32 s48, 4352\n"
"buffer_load_dwordx4 v[240:243], v224, s[8:11], s48 offen offset:0 sc1\n"
"buffer_load_dwordx4 v[244:247], v224, s[8:11], s48 offen offset:1024 sc1\n"
"s_branch L1070_%=\n"
"L1067_%=:\n"
"s_cmp_eq_u32 s57, 2\n"
"s_cbranch_scc0 L1068_%=\n"
"s_mov_b32 s48, 4352\n"
"s_waitcnt vmcnt(8)\n"
"v_cmp_eq_u64 vcc, s[28:29], v[240:241]\n"
"v_cmp_eq_u64_e64 s[52:53], s[28:29], v[242:243]\n"
"s_and_b64 vcc, vcc, s[52:53]\n"
"v_cmp_eq_u64_e64 s[52:53], s[28:29], v[244:245]\n"
"s_and_b64 vcc, vcc, s[52:53]\n"
"v_cmp_eq_u64_e64 s[52:53], s[28:29], v[246:247]\n"
"s_and_b64 vcc, vcc, s[52:53]\n"
"s_cmp_eq_u64 vcc, exec\n"
"s_cbranch_scc0 L1072_%=\n"
"L1073_%=:\n"
"s_cmp_lt_u32 s58, 40\n"
"s_cbranch_scc0 L1071_%=\n"
"s_mul_i32 s48, s30, 14\n"
"s_cmp_eq_u32 s30, 3\n"
"s_subb_u32 s48, s48, 0\n"
"s_mul_i32 s49, s58, 54\n"
"s_add_u32 s49, s49, s48\n"
"s_lshl_b32 s49, s49, 10\n"
"s_add_u32 s49, s49, 2744320\n"
"s_lshl_b32 s48, s48, 9\n"
"s_add_u32 s48, s48, 2695168\n"
"s_cmp_lt_u32 s30, 2\n"
"s_cselect_b32 s50, 0, 2130706432\n"
"v_lshrrev_b32 v235, 1, v224\n"
"v_and_b32 v235, 0x180, v235\n"
"v_and_b32 v239, 0x70, v224\n"
"v_add_u32 v235, v235, v239\n"
"buffer_load_dwordx4 a[148:151], v224, s[8:11], s49 offen offset:0 sc1\n"
"buffer_load_dwordx4 a[204:207], v235, s[8:11], s48 offen offset:0 sc1\n"
"buffer_load_dwordx4 a[152:155], v224, s[8:11], s49 offen offset:1024 sc1\n"
"buffer_load_dwordx4 a[208:211], v235, s[8:11], s48 offen offset:512 sc1\n"
"buffer_load_dwordx4 a[156:159], v224, s[8:11], s49 offen offset:2048 sc1\n"
"buffer_load_dwordx4 a[212:215], v235, s[8:11], s48 offen offset:1024 sc1\n"
"buffer_load_dwordx4 a[160:163], v224, s[8:11], s49 offen offset:3072 sc1\n"
"buffer_load_dwordx4 a[216:219], v235, s[8:11], s48 offen offset:1536 sc1\n"
"s_add_u32 s49, s49, 4096\n"
"buffer_load_dwordx4 a[164:167], v224, s[8:11], s49 offen offset:0 sc1\n"
"buffer_load_dwordx4 a[220:223], v235, s[8:11], s48 offen offset:2048 sc1\n"
"buffer_load_dwordx4 a[168:171], v224, s[8:11], s49 offen offset:1024 sc1\n"
"buffer_load_dwordx4 a[224:227], v235, s[8:11], s48 offen offset:2560 sc1\n"
"buffer_load_dwordx4 a[172:175], v224, s[8:11], s49 offen offset:2048 sc1\n"
"buffer_load_dwordx4 a[228:231], v235, s[8:11], s48 offen offset:3072 sc1\n"
"buffer_load_dwordx4 a[176:179], v224, s[8:11], s49 offen offset:3072 sc1\n"
"buffer_load_dwordx4 a[232:235], v235, s[8:11], s48 offen offset:3584 sc1\n"
"s_add_u32 s49, s49, 4096\n"
"s_add_u32 s48, s48, 4096\n"
"buffer_load_dwordx4 a[180:183], v224, s[8:11], s49 offen offset:0 sc1\n"
"buffer_load_dwordx4 a[236:239], v235, s[8:11], s48 offen offset:0 sc1\n"
"buffer_load_dwordx4 a[184:187], v224, s[8:11], s49 offen offset:1024 sc1\n"
"buffer_load_dwordx4 a[240:243], v235, s[8:11], s48 offen offset:512 sc1\n"
"buffer_load_dwordx4 a[188:191], v224, s[8:11], s49 offen offset:2048 sc1\n"
"buffer_load_dwordx4 a[244:247], v235, s[8:11], s48 offen offset:1024 sc1\n"
"buffer_load_dwordx4 a[192:195], v224, s[8:11], s49 offen offset:3072 sc1\n"
"buffer_load_dwordx4 a[248:251], v235, s[8:11], s48 offen offset:1536 sc1\n"
"s_add_u32 s49, s49, 4096\n"
"buffer_load_dwordx4 a[196:199], v224, s[8:11], s49 offen offset:0 sc1\n"
"buffer_load_dwordx4 a[252:255], v235, s[8:11], s48 offen offset:2048 sc1\n"
"s_add_u32 s48, s48, s50\n"
"s_add_u32 s49, s49, s50\n"
"buffer_load_dwordx4 a[200:203], v224, s[8:11], s49 offen offset:1024 sc1\n"
"buffer_load_dwordx4 v[244:247], v235, s[8:11], s48 offen offset:2560 sc1\n"
"v_lshrrev_b32 v235, 9, v224\n"
"v_lshlrev_b32 v235, 8, v235\n"
"v_bfe_u32 v238, v224, 8, 1\n"
"v_lshl_add_u32 v235, v238, 3, v235\n"
"s_lshl_b32 s48, s58, 9\n"
"v_add_u32 v235, s48, v235\n"
"v_add_u32 v239, v239, v235\n"
"v_and_b32 v238, 0x80, v224\n"
"v_lshl_add_u32 v239, v238, 23, v239\n"
"v_not_b32 v238, v234\n"
"v_and_b32 v240, 15, v238\n"
"v_lshl_add_u32 v235, v240, 4, v235\n"
"v_bfe_u32 v238, v238, 4, 7\n"
"v_mul_u32_u24 v238, 20480, v238\n"
"v_add_u32 v238, v238, v235\n"
"s_mov_b32 s48, 65536\n"
"buffer_load_dwordx2 v[236:237], v238, s[8:11], s48 offen sc1\n"
"s_branch L1070_%=\n"
"L1068_%=:\n"
"s_cmp_eq_u32 s57, 3\n"
"s_cbranch_scc0 L1069_%=\n"
"s_waitcnt vmcnt(8)\n"
"v_mfma_f32_16x16x32_bf16 v[240:243], a[148:151], a[204:207], 0\n"
"v_mfma_f32_16x16x32_bf16 v[240:243], a[152:155], a[208:211], v[240:243]\n"
"v_mfma_f32_16x16x32_bf16 v[240:243], a[156:159], a[212:215], v[240:243]\n"
"v_mfma_f32_16x16x32_bf16 v[240:243], a[160:163], a[216:219], v[240:243]\n"
"v_mfma_f32_16x16x32_bf16 v[240:243], a[164:167], a[220:223], v[240:243]\n"
"v_mfma_f32_16x16x32_bf16 v[240:243], a[168:171], a[224:227], v[240:243]\n"
"v_mfma_f32_16x16x32_bf16 v[240:243], a[172:175], a[228:231], v[240:243]\n"
"v_mfma_f32_16x16x32_bf16 v[240:243], a[176:179], a[232:235], v[240:243]\n"
"v_mfma_f32_16x16x32_bf16 v[240:243], a[180:183], a[236:239], v[240:243]\n"
"v_mfma_f32_16x16x32_bf16 v[240:243], a[184:187], a[240:243], v[240:243]\n"
"v_mfma_f32_16x16x32_bf16 v[240:243], a[188:191], a[244:247], v[240:243]\n"
"v_mfma_f32_16x16x32_bf16 v[240:243], a[192:195], a[248:251], v[240:243]\n"
"v_mfma_f32_16x16x32_bf16 v[240:243], a[196:199], a[252:255], v[240:243]\n"
"v_mfma_f32_16x16x32_bf16 v[240:243], a[200:203], v[244:247], v[240:243]\n"
"s_sub_u32 s48, s41, 32768\n"
"s_cselect_b32 s48, 131072, s48\n"
"v_add_u32 v235, s48, v224\n"
"s_lshl_b32 s49, s30, 10\n"
"v_add_u32 v238, s49, v235\n"
"s_barrier\n"
"s_nop 3\n"
"ds_write_b128 v238, v[240:243]\n"
"s_waitcnt lgkmcnt(0)\n"
"s_barrier\n"
"ds_read_b128 v[240:243], v235 offset:0\n"
"ds_read_b128 v[244:247], v235 offset:1024\n"
"ds_read_b128 v[248:251], v235 offset:2048\n"
"ds_read_b128 v[252:255], v235 offset:3072\n"
"v_ashrrev_i32 v235, 31, v234\n"
"v_not_b32 v235, v235\n"
"v_or_b32 v235, 0x80000000, v235\n"
"v_xor_b32 v235, v235, v234\n"
"v_and_b32 v235, 0xffff0000, v235\n"
"v_mul_f32 v235, 0xbfb8aa3b, v235\n"
"v_exp_f32 v235, v235\n"
"s_nop 0\n"
"v_add_f32 v235, 1.0, v235\n"
"v_rcp_f32 v235, v235\n"
"s_nop 0\n"
"v_cvt_pk_bf16_f32 v235, v235, v235\n"
"v_lshlrev_b32 v235, 16, v235\n"
"s_waitcnt lgkmcnt(0)\n"
"v_add_f32 v240, v240, v244\n"
"v_add_f32 v248, v248, v252\n"
"v_add_f32 v241, v241, v245\n"
"v_add_f32 v249, v249, v253\n"
"v_add_f32 v242, v242, v246\n"
"v_add_f32 v250, v250, v254\n"
"v_add_f32 v243, v243, v247\n"
"v_add_f32 v251, v251, v255\n"
"v_add_f32 v240, v240, v248\n"
"v_add_f32 v241, v241, v249\n"
"v_add_f32 v242, v242, v250\n"
"v_add_f32 v243, v243, v251\n"
"v_cvt_pk_bf16_f32 v240, v240, v240\n"
"v_lshlrev_b32 v240, 16, v240\n"
"v_mul_f32 v240, v240, v235\n"
"v_cvt_pk_bf16_f32 v240, v240, v240\n"
"v_lshlrev_b32 v240, 16, v240\n"
"v_cvt_pk_bf16_f32 v241, v241, v241\n"
"v_lshlrev_b32 v241, 16, v241\n"
"v_mul_f32 v241, v241, v235\n"
"v_cvt_pk_bf16_f32 v241, v241, v241\n"
"v_lshlrev_b32 v241, 16, v241\n"
"v_cvt_pk_bf16_f32 v242, v242, v242\n"
"v_lshlrev_b32 v242, 16, v242\n"
"v_mul_f32 v242, v242, v235\n"
"v_cvt_pk_bf16_f32 v242, v242, v242\n"
"v_lshlrev_b32 v242, 16, v242\n"
"v_cvt_pk_bf16_f32 v243, v243, v243\n"
"v_lshlrev_b32 v243, 16, v243\n"
"v_mul_f32 v243, v243, v235\n"
"v_cvt_pk_bf16_f32 v243, v243, v243\n"
"v_lshlrev_b32 v243, 16, v243\n"
"v_lshlrev_b32 v244, 16, v236\n"
"v_and_b32 v245, 0xffff0000, v236\n"
"v_lshlrev_b32 v246, 16, v237\n"
"v_and_b32 v247, 0xffff0000, v237\n"
"v_add_f32 v240, v240, v244\n"
"v_add_f32 v241, v241, v245\n"
"v_add_f32 v242, v242, v246\n"
"v_add_f32 v243, v243, v247\n"
"v_cvt_pk_bf16_f32 v240, v240, v241\n"
"v_cvt_pk_bf16_f32 v241, v242, v243\n"
"s_add_u32 s48, s45, 2722816\n"
"buffer_store_dwordx2 v[240:241], v239, s[8:11], s48 offen sc1\n"
"s_branch L1070_%=\n"
"L1069_%=:\n"
"s_lshl_b32 s48, s58, 3\n"
"s_add_u32 s48, s48, s45\n"
"s_add_u32 s48, s48, 6400\n"
"s_waitcnt vmcnt(8)\n"
"buffer_store_dwordx2 v[232:233], v230, s[8:11], s48 offen sc1\n"
"L1071_%=:\n"
"s_lshl_b32 s56, s34, 1\n"
"s_sub_u32 s56, s56, s57\n"
"s_sub_u32 s56, s56, 3\n"
"s_mov_b32 s57, -1\n"
"L1070_%=:\n"
"s_add_u32 s57, s57, 1\n"
"s_branch LP1_%=\n"
      : : [ka] "s"(__builtin_amdgcn_kernarg_segment_ptr()), [b] "s"(blockIdx.x), [t] "v"(threadIdx.x)
      : "v1", "v2", "v3", "v4", "v5", "v6", "v7", "v8", "v9", "v10", "v11", "v12", "v13", "v14", "v15", "v16", "v17", "v18", "v19", "v20", "v21", "v22", "v23", "v24", "v25", "v26", "v27", "v28", "v29", "v30", "v31", "v32", "v33", "v34", "v35", "v36", "v37", "v38", "v39", "v40", "v41", "v42", "v43", "v44", "v45", "v46", "v47", "v48", "v49", "v50", "v51", "v52", "v53", "v54", "v55", "v56", "v57", "v58", "v59", "v60", "v61", "v62", "v63", "v64", "v65", "v66", "v67", "v68", "v69", "v70", "v71", "v72", "v73", "v74", "v75", "v76", "v77", "v78", "v79", "v80", "v81", "v82", "v83", "v84", "v85", "v86", "v87", "v88", "v89", "v90", "v91", "v92", "v93", "v94", "v95", "v96", "v97", "v98", "v99", "v100", "v101", "v102", "v103", "v104", "v105", "v106", "v107", "v108", "v109", "v110", "v111", "v112", "v113", "v114", "v115", "v116", "v117", "v118", "v119", "v120", "v121", "v122", "v123", "v124", "v125", "v126", "v127", "v128", "v129", "v130", "v131", "v132", "v133", "v134", "v135", "v136", "v137", "v138", "v139", "v140", "v141", "v142", "v143", "v144", "v145", "v146", "v147", "v148", "v149", "v150", "v151", "v152", "v153", "v154", "v155", "v156", "v157", "v158", "v159", "v160", "v161", "v162", "v163", "v164", "v165", "v166", "v167", "v168", "v169", "v170", "v171", "v172", "v173", "v174", "v175", "v176", "v177", "v178", "v179", "v180", "v181", "v182", "v183", "v184", "v185", "v186", "v187", "v188", "v189", "v190", "v191", "v192", "v193", "v194", "v195", "v196", "v197", "v198", "v199", "v200", "v201", "v202", "v203", "v204", "v205", "v206", "v207", "v208", "v209", "v210", "v211", "v212", "v213", "v214", "v215", "v216", "v217", "v218", "v219", "v220", "v221", "v222", "v223", "v224", "v225", "v226", "v227", "v228", "v229", "v230", "v231", "v232", "v233", "v234", "v235", "v236", "v237", "v238", "v239", "v240", "v241", "v242", "v243", "v244", "v245", "v246", "v247", "v248", "v249", "v250", "v251", "v252", "v253", "v254", "v255", "a0", "a1", "a2", "a3", "a4", "a5", "a6", "a7", "a8", "a9", "a10", "a11", "a12", "a13", "a14", "a15", "a16", "a17", "a18", "a19", "a20", "a21", "a22", "a23", "a24", "a25", "a26", "a27", "a28", "a29", "a30", "a31", "a32", "a33", "a34", "a35", "a36", "a37", "a38", "a39", "a40", "a41", "a42", "a43", "a44", "a45", "a46", "a47", "a48", "a49", "a50", "a51", "a52", "a53", "a54", "a55", "a56", "a57", "a58", "a59", "a60", "a61", "a62", "a63", "a64", "a65", "a66", "a67", "a68", "a69", "a70", "a71", "a72", "a73", "a74", "a75", "a76", "a77", "a78", "a79", "a80", "a81", "a82", "a83", "a84", "a85", "a86", "a87", "a88", "a89", "a90", "a91", "a92", "a93", "a94", "a95", "a96", "a97", "a98", "a99", "a100", "a101", "a102", "a103", "a104", "a105", "a106", "a107", "a108", "a109", "a110", "a111", "a112", "a113", "a114", "a115", "a116", "a117", "a118", "a119", "a120", "a121", "a122", "a123", "a124", "a125", "a126", "a127", "a128", "a129", "a130", "a131", "a132", "a133", "a134", "a135", "a136", "a137", "a138", "a139", "a140", "a141", "a142", "a143", "a144", "a145", "a146", "a147", "a148", "a149", "a150", "a151", "a152", "a153", "a154", "a155", "a156", "a157", "a158", "a159", "a160", "a161", "a162", "a163", "a164", "a165", "a166", "a167", "a168", "a169", "a170", "a171", "a172", "a173", "a174", "a175", "a176", "a177", "a178", "a179", "a180", "a181", "a182", "a183", "a184", "a185", "a186", "a187", "a188", "a189", "a190", "a191", "a192", "a193", "a194", "a195", "a196", "a197", "a198", "a199", "a200", "a201", "a202", "a203", "a204", "a205", "a206", "a207", "a208", "a209", "a210", "a211", "a212", "a213", "a214", "a215", "a216", "a217", "a218", "a219", "a220", "a221", "a222", "a223", "a224", "a225", "a226", "a227", "a228", "a229", "a230", "a231", "a232", "a233", "a234", "a235", "a236", "a237", "a238", "a239", "a240", "a241", "a242", "a243", "a244", "a245", "a246", "a247", "a248", "a249", "a250", "a251", "a252", "a253", "a254", "a255", "s8", "s9", "s10", "s11", "s12", "s13", "s14", "s15", "s16", "s17", "s18", "s19", "s20", "s21", "s22", "s23", "s24", "s25", "s26", "s27", "s28", "s29", "s30", "s31", "s32", "s33", "s34", "s35", "s36", "s37", "s38", "s39", "s40", "s41", "s42", "s43", "s44", "s45", "s46", "s47", "s48", "s49", "s50", "s51", "s52", "s53", "s54", "s55", "s56", "s57", "s58", "s59", "s60", "s61", "s62", "s63", "s64", "s65", "s66", "s67", "s68", "s69", "s70", "s71", "s72", "s73", "s74", "s75", "s76", "s77", "s78", "s79", "s80", "s81", "s82", "s83", "s84", "s85", "s86", "s87", "s88", "s89", "s90", "s91", "s92", "s93", "s94", "s95", "s96", "s97", "s98", "s99", "scc", "vcc", "memory");
}

extern "C" void kernel_launch(void* const* d_in, const int* in_sizes, int n_in, void* d_out, int out_size, void* d_ws, size_t ws_size, hipStream_t stream) {
  (void)hipFuncSetAttribute((const void*)mega_kernel, hipFuncAttributeMaxDynamicSharedMemorySize, 163840);
  mega_kernel<<<256, 256, 163840, stream>>>((const bf16*)d_in[0], (const bf16*)d_in[1], (const bf16*)d_in[2], (const bf16*)d_in[3], (const bf16*)d_in[4], (const bf16*)d_in[5],
      (const bf16*)d_in[7], (const bf16*)d_in[8], (const bf16*)d_in[9], (const bf16*)d_in[10], (char*)d_ws, (bf16*)d_out);
}
```

```cpp
#include <hip/hip_runtime.h>
#include <hip/hip_bf16.h>
typedef __hip_bfloat16 bf16;

__global__ __launch_bounds__(256, 1) void mega_kernel(const bf16* x, const bf16* ckv, const bf16* wq, const bf16* wuk, const bf16* wuv, const bf16* wo, const bf16* w_r,
                                                      const bf16* wg, const bf16* wu, const bf16* wd, char* ws, bf16* out) {
  asm volatile(
"s_load_dwordx16 s[64:79], %[ka], 0\n"
"s_load_dwordx8 s[80:87], %[ka], 64\n"
"s_mov_b32 s31, %[b]\n"
"v_lshrrev_b32 v1, 6, %[t]\n"
"v_and_b32 v2, 63, %[t]\n"
"v_readfirstlane_b32 s30, v1\n"
"v_lshlrev_b32 v224, 4, v2\n"
"s_and_b32 s32, s31, 7\n"
"s_lshr_b32 s33, s31, 6\n"
"s_bfe_u32 s34, s31, 0x30003\n"
"s_lshl_b32 s35, s32, 2\n"
"s_add_u32 s35, s35, s33\n"
"s_add_u32 s46, s34, s32\n"
"s_and_b32 s46, s46, 6\n"
"s_lshl_b32 s47, s46, 19\n"
"s_lshl_b32 s46, s46, 7\n"
"s_lshl_b32 s36, s30, 12\n"
"s_mul_i32 s37, s30, 24576\n"
"s_add_u32 s37, s37, 65536\n"
"v_lshrrev_b32 v3, 3, v2\n"
"v_lshrrev_b32 v4, 4, v2\n"
"v_and_b32 v5, 7, v2\n"
"v_xor_b32 v6, v5, v4\n"
"v_lshlrev_b32 v6, 4, v6\n"
"v_lshl_add_u32 v6, v3, 10, v6\n"
"s_lshl_b32 s50, s30, 15\n"
"v_add_u32 v234, s50, v6\n"
"v_xor_b32 v235, 64, v234\n"
"v_add_u32 v235, 7168, v235\n"
"v_add_u32 v236, 14336, v234\n"
"v_add_u32 v237, 14336, v235\n"
"v_and_b32 v7, 1, v4\n"
"v_lshlrev_b32 v7, 1, v7\n"
"v_xor_b32 v7, v7, v5\n"
"v_lshlrev_b32 v7, 4, v7\n"
"v_lshl_add_u32 v238, v3, 13, v7\n"
"v_xor_b32 v239, 64, v238\n"
"v_add_u32 v239, 64512, v239\n"
"v_add_u32 v240, 129024, v238\n"
"v_add_u32 v241, 129024, v239\n"
"v_and_b32 v8, 31, v2\n"
"v_lshlrev_b32 v8, 3, v8\n"
"v_lshrrev_b32 v9, 5, v2\n"
"s_lshl_b32 s50, s31, 2\n"
"s_add_u32 s50, s50, s30\n"
"s_mul_i32 s50, s50, 2560\n"
"v_mul_u32_u24 v9, 1280, v9\n"
"v_add3_u32 v9, v9, v8, s50\n"
"s_lshl_b32 s50, s34, 17\n"
"s_lshl_b32 s51, s35, 8\n"
"s_and_b32 s52, s30, 1\n"
"s_lshl_b32 s52, s52, 7\n"
"s_add_u32 s52, s52, s51\n"
"s_mov_b32 s14, 0x120000\n"
"s_mov_b32 s15, 0x20000\n"
"s_sub_u32 s18, 4194304, s52\n"
"s_mov_b32 s19, 0x20000\n"
"s_waitcnt lgkmcnt(0)\n"
"s_load_dwordx2 s[28:29], s[84:85], 0\n"
"s_add_u32 s12, s66, s50\n"
"s_addc_u32 s13, s67, 0\n"
"s_and_b32 s13, s13, 0xffff\n"
"s_cmp_lt_u32 s30, 2\n"
"s_cselect_b64 s[16:17], s[70:71], s[72:73]\n"
"s_add_u32 s16, s16, s52\n"
"s_addc_u32 s17, s17, 0\n"
"s_and_b32 s17, s17, 0xffff\n"
"global_load_dwordx2 v[128:129], v9, s[64:65] offset:0\n"
"global_load_dwordx2 v[130:131], v9, s[64:65] offset:256\n"
"global_load_dwordx2 v[132:133], v9, s[64:65] offset:512\n"
"global_load_dwordx2 v[134:135], v9, s[64:65] offset:768\n"
"global_load_dwordx2 v[136:137], v9, s[64:65] offset:1024\n"
"global_load_dwordx2 v[138:139], v8, s[76:77] offset:0\n"
"global_load_dwordx2 v[140:141], v8, s[76:77] offset:256\n"
"global_load_dwordx2 v[142:143], v8, s[76:77] offset:512\n"
"global_load_dwordx2 v[144:145], v8, s[76:77] offset:768\n"
"global_load_dwordx2 v[146:147], v8, s[76:77] offset:1024\n"
"s_add_u32 m0, s37, 0\n"
"s_add_u32 s48, s47, 0\n"
"s_and_b32 s48, s48, 4194303\n"
"buffer_load_dwordx4 v238, s[16:19], s48 offen offset:0 lds\n"
"buffer_load_dwordx4 v239, s[16:19], s48 offen offset:1024 lds\n"
"buffer_load_dwordx4 v240, s[16:19], s48 offen offset:2048 lds\n"
"buffer_load_dwordx4 v241, s[16:19], s48 offen offset:3072 lds\n"
"s_getpc_b64 s[24:25]\n"
"LPC_%=:\n"
"s_and_b32 s25, s25, 0xffff\n"
"s_mov_b32 s26, LEND_%=-LPC_%=\n"
"s_mov_b32 s27, 0x20000\n"
"s_lshr_b32 s49, s31, 3\n"
"s_lshl_b32 s49, s49, 2\n"
"s_add_u32 s49, s49, s30\n"
"s_lshl_b32 s49, s49, 7\n"
"v_lshlrev_b32 v10, 14, v2\n"
"buffer_load_dword v255, v10, s[24:27], s49 offen\n"
"s_add_u32 m0, s36, 0\n"
"s_add_u32 s48, s46, 0\n"
"s_and_b32 s48, s48, 1023\n"
"buffer_load_dwordx4 v234, s[12:15], s48 offen offset:0 lds\n"
"buffer_load_dwordx4 v235, s[12:15], s48 offen offset:1024 lds\n"
"buffer_load_dwordx4 v236, s[12:15], s48 offen offset:2048 lds\n"
"buffer_load_dwordx4 v237, s[12:15], s48 offen offset:3072 lds\n"
"s_add_u32 m0, s37, 4096\n"
"s_add_u32 s48, s47, 262144\n"
"s_and_b32 s48, s48, 4194303\n"
"buffer_load_dwordx4 v238, s[16:19], s48 offen offset:0 lds\n"
"buffer_load_dwordx4 v239, s[16:19], s48 offen offset:1024 lds\n"
"buffer_load_dwordx4 v240, s[16:19], s48 offen offset:2048 lds\n"
"buffer_load_dwordx4 v241, s[16:19], s48 offen offset:3072 lds\n"
"s_add_u32 m0, s37, 8192\n"
"s_add_u32 s48, s47, 524288\n"
"s_and_b32 s48, s48, 4194303\n"
"buffer_load_dwordx4 v238, s[16:19], s48 offen offset:0 lds\n"
"buffer_load_dwordx4 v239, s[16:19], s48 offen offset:1024 lds\n"
"buffer_load_dwordx4 v240, s[16:19], s48 offen offset:2048 lds\n"
"buffer_load_dwordx4 v241, s[16:19], s48 offen offset:3072 lds\n"
"v_lshlrev_b32 v253, 7, %[t]\n"
"s_mov_b32 s8, s84\n"
"s_and_b32 s9, s85, 0xffff\n"
"s_mov_b32 s10, 81788928\n"
"s_mov_b32 s11, 0x20000\n"
"s_add_u32 s20, s68, s51\n"
"s_addc_u32 s21, s69, 0\n"
"s_and_b32 s21, s21, 0xffff\n"
"s_sub_u32 s22, 5242880, s51\n"
"s_mov_b32 s23, 0x20000\n"
"v_and_b32 v11, 15, v2\n"
"v_lshrrev_b32 v12, 1, v11\n"
"v_xor_b32 v12, v12, v4\n"
"v_lshlrev_b32 v12, 4, v12\n"
"v_lshl_add_u32 v242, v11, 7, v12\n"
"v_xor_b32 v243, 64, v242\n"
"v_lshrrev_b32 v12, 2, v11\n"
"v_lshl_add_u32 v12, v4, 3, v12\n"
"v_lshlrev_b32 v12, 7, v12\n"
"v_and_b32 v13, 3, v2\n"
"v_lshl_add_u32 v12, v13, 3, v12\n"
"v_and_b32 v13, 3, v3\n"
"v_lshl_add_u32 v12, v13, 5, v12\n"
"v_add_u32 v246, s37, v12\n"
"v_xor_b32 v247, 32, v246\n"
"v_xor_b32 v248, 64, v246\n"
"v_xor_b32 v249, 96, v246\n"
"v_mov_b32 v13, 2130706432\n"
"v_cmp_eq_u32 vcc, 0, v2\n"
"s_cmp_eq_u32 s30, 0\n"
"s_cselect_b32 s45, 0, 2130706432\n"
"v_cndmask_b32_e64 v230, v13, 0, vcc\n"
"s_lshl_b32 s39, s30, 8\n"
"s_add_u32 s39, s39, s46\n"
"s_add_u32 s98, s39, 128\n"
"s_and_b32 s39, s39, 1023\n"
"s_and_b32 s98, s98, 1023\n"
"s_lshl_b32 s50, s30, 15\n"
"s_sub_u32 s50, 1048576, s50\n"
"s_add_u32 s39, s39, s50\n"
"s_add_u32 s98, s98, s50\n"
"s_mov_b32 s55, 0x3e0293ee\n"
"s_waitcnt lgkmcnt(0)\n"
"s_add_u32 s28, s28, 0x7f4a7c15\n"
"s_addc_u32 s29, s29, 0x9e3779b9\n"
"v_mov_b32 v232, s28\n"
"v_mov_b32 v233, s29\n"
"s_cmp_lt_u32 s30, 2\n"
"s_cbranch_scc0 LV_%=\n"
"s_waitcnt vmcnt(26)\n"
"v_lshlrev_b32 v148, 16, v128\n"
"v_and_b32 v149, 0xffff0000, v128\n"
"v_lshlrev_b32 v150, 16, v129\n"
"v_and_b32 v151, 0xffff0000, v129\n"
"s_waitcnt vmcnt(25)\n"
"v_lshlrev_b32 v152, 16, v130\n"
"v_and_b32 v153, 0xffff0000, v130\n"
"v_lshlrev_b32 v154, 16, v131\n"
"v_and_b32 v155, 0xffff0000, v131\n"
"s_waitcnt vmcnt(24)\n"
"v_lshlrev_b32 v156, 16, v132\n"
"v_and_b32 v157, 0xffff0000, v132\n"
"v_lshlrev_b32 v158, 16, v133\n"
"v_and_b32 v159, 0xffff0000, v133\n"
"s_waitcnt vmcnt(23)\n"
"v_lshlrev_b32 v160, 16, v134\n"
"v_and_b32 v161, 0xffff0000, v134\n"
"v_lshlrev_b32 v162, 16, v135\n"
"v_and_b32 v163, 0xffff0000, v135\n"
"s_waitcnt vmcnt(22)\n"
"v_lshlrev_b32 v164, 16, v136\n"
"v_and_b32 v165, 0xffff0000, v136\n"
"v_lshlrev_b32 v166, 16, v137\n"
"v_and_b32 v167, 0xffff0000, v137\n"
"v_add_f32 v188, v148, v149\n"
"v_add_f32 v189, v150, v151\n"
"v_add_f32 v188, v188, v152\n"
"v_add_f32 v189, v189, v153\n"
"v_add_f32 v188, v188, v154\n"
"v_add_f32 v189, v189, v155\n"
"v_add_f32 v188, v188, v156\n"
"v_add_f32 v189, v189, v157\n"
"v_add_f32 v188, v188, v158\n"
"v_add_f32 v189, v189, v159\n"
"v_add_f32 v188, v188, v160\n"
"v_add_f32 v189, v189, v161\n"
"v_add_f32 v188, v188, v162\n"
"v_add_f32 v189, v189, v163\n"
"v_add_f32 v188, v188, v164\n"
"v_add_f32 v189, v189, v165\n"
"v_add_f32 v188, v188, v166\n"
"v_add_f32 v189, v189, v167\n"
"v_add_f32 v188, v188, v189\n"
"s_nop 1\n"
"v_add_f32_dpp v188, v188, v188 quad_perm:[1,0,3,2] row_mask:0xf bank_mask:0xf\n"
"s_waitcnt vmcnt(21)\n"
"v_lshlrev_b32 v168, 16, v138\n"
"v_and_b32 v169, 0xffff0000, v138\n"
"v_add_f32_dpp v188, v188, v188 quad_perm:[2,3,0,1] row_mask:0xf bank_mask:0xf\n"
"v_lshlrev_b32 v170, 16, v139\n"
"v_and_b32 v171, 0xffff0000, v139\n"
"v_add_f32_dpp v188, v188, v188 row_half_mirror row_mask:0xf bank_mask:0xf\n"
"s_waitcnt vmcnt(20)\n"
"v_lshlrev_b32 v172, 16, v140\n"
"v_and_b32 v173, 0xffff0000, v140\n"
"v_add_f32_dpp v188, v188, v188 row_mirror row_mask:0xf bank_mask:0xf\n"
"v_lshlrev_b32 v174, 16, v141\n"
"v_and_b32 v175, 0xffff0000, v141\n"
"v_mov_b32 v189, v188\n"
"s_waitcnt vmcnt(19)\n"
"v_lshlrev_b32 v176, 16, v142\n"
"v_and_b32 v177, 0xffff0000, v142\n"
"v_permlane16_swap_b32 v189, v188\n"
"v_lshlrev_b32 v178, 16, v143\n"
"v_and_b32 v179, 0xffff0000, v143\n"
"v_add_f32 v188, v188, v189\n"
"s_waitcnt vmcnt(18)\n"
"v_lshlrev_b32 v180, 16, v144\n"
"v_and_b32 v181, 0xffff0000, v144\n"
"v_mul_f32 v191, 0x3acccccd, v188\n"
"v_lshlrev_b32 v182, 16, v145\n"
"v_and_b32 v183, 0xffff0000, v145\n"
"v_cvt_pk_bf16_f32 v128, v191, v191\n"
"s_waitcnt vmcnt(17)\n"
"v_lshlrev_b32 v184, 16, v146\n"
"v_and_b32 v185, 0xffff0000, v146\n"
"v_lshlrev_b32 v128, 16, v128\n"
"v_lshlrev_b32 v186, 16, v147\n"
"v_and_b32 v187, 0xffff0000, v147\n"
"s_add_u32 m0, s36, 16384\n"
"s_add_u32 s48, s46, 128\n"
"s_and_b32 s48, s48, 1023\n"
"buffer_load_dwordx4 v234, s[12:15], s48 offen offset:0 lds\n"
"buffer_load_dwordx4 v235, s[12:15], s48 offen offset:1024 lds\n"
"buffer_load_dwordx4 v236, s[12:15], s48 offen offset:2048 lds\n"
"buffer_load_dwordx4 v237, s[12:15], s48 offen offset:3072 lds\n"
"v_sub_f32 v148, v148, v128\n"
"v_sub_f32 v149, v149, v128\n"
"v_sub_f32 v150, v150, v128\n"
"v_sub_f32 v151, v151, v128\n"
"v_sub_f32 v152, v152, v128\n"
"v_sub_f32 v153, v153, v128\n"
"v_sub_f32 v154, v154, v128\n"
"v_sub_f32 v155, v155, v128\n"
"v_sub_f32 v156, v156, v128\n"
"v_sub_f32 v157, v157, v128\n"
"v_sub_f32 v158, v158, v128\n"
"v_sub_f32 v159, v159, v128\n"
"v_sub_f32 v160, v160, v128\n"
"v_sub_f32 v161, v161, v128\n"
"v_sub_f32 v162, v162, v128\n"
"v_sub_f32 v163, v163, v128\n"
"v_sub_f32 v164, v164, v128\n"
"v_sub_f32 v165, v165, v128\n"
"v_sub_f32 v166, v166, v128\n"
"v_sub_f32 v167, v167, v128\n"
"v_mul_f32 v188, v148, v148\n"
"v_mul_f32 v189, v149, v149\n"
"v_fmac_f32 v188, v150, v150\n"
"v_fmac_f32 v189, v151, v151\n"
"v_fmac_f32 v188, v152, v152\n"
"v_fmac_f32 v189, v153, v153\n"
"v_fmac_f32 v188, v154, v154\n"
"v_fmac_f32 v189, v155, v155\n"
"v_fmac_f32 v188, v156, v156\n"
"v_fmac_f32 v189, v157, v157\n"
"v_fmac_f32 v188, v158, v158\n"
"v_fmac_f32 v189, v159, v159\n"
"v_fmac_f32 v188, v160, v160\n"
"v_fmac_f32 v189, v161, v161\n"
"v_fmac_f32 v188, v162, v162\n"
"v_fmac_f32 v189, v163, v163\n"
"v_fmac_f32 v188, v164, v164\n"
"v_fmac_f32 v189, v165, v165\n"
"v_fmac_f32 v188, v166, v166\n"
"v_fmac_f32 v189, v167, v167\n"
"v_add_f32 v188, v188, v189\n"
"v_cvt_pk_bf16_f32 v149, v148, v149\n"
"v_lshlrev_b32 v148, 16, v149\n"
"v_add_f32_dpp v188, v188, v188 quad_perm:[1,0,3,2] row_mask:0xf bank_mask:0xf\n"
"v_and_b32 v149, 0xffff0000, v149\n"
"v_cvt_pk_bf16_f32 v151, v150, v151\n"
"v_add_f32_dpp v188, v188, v188 quad_perm:[2,3,0,1] row_mask:0xf bank_mask:0xf\n"
"v_lshlrev_b32 v150, 16, v151\n"
"v_and_b32 v151, 0xffff0000, v151\n"
"v_add_f32_dpp v188, v188, v188 row_half_mirror row_mask:0xf bank_mask:0xf\n"
"v_cvt_pk_bf16_f32 v153, v152, v153\n"
"v_lshlrev_b32 v152, 16, v153\n"
"v_add_f32_dpp v188, v188, v188 row_mirror row_mask:0xf bank_mask:0xf\n"
"v_and_b32 v153, 0xffff0000, v153\n"
"v_cvt_pk_bf16_f32 v155, v154, v155\n"
"v_mov_b32 v189, v188\n"
"v_lshlrev_b32 v154, 16, v155\n"
"v_and_b32 v155, 0xffff0000, v155\n"
"v_permlane16_swap_b32 v189, v188\n"
"v_cvt_pk_bf16_f32 v157, v156, v157\n"
"v_lshlrev_b32 v156, 16, v157\n"
"v_add_f32 v188, v188, v189\n"
"v_and_b32 v157, 0xffff0000, v157\n"
"v_cvt_pk_bf16_f32 v159, v158, v159\n"
"v_sub_f32 v191, v191, v128\n"
"v_lshlrev_b32 v158, 16, v159\n"
"v_and_b32 v159, 0xffff0000, v159\n"
"v_mul_f32 v188, 0x3acccccd, v188\n"
"v_cvt_pk_bf16_f32 v161, v160, v161\n"
"s_add_u32 m0, s37, 12288\n"
"s_add_u32 s48, s47, 786432\n"
"s_and_b32 s48, s48, 4194303\n"
"buffer_load_dwordx4 v238, s[16:19], s48 offen offset:0 lds\n"
"buffer_load_dwordx4 v239, s[16:19], s48 offen offset:1024 lds\n"
"buffer_load_dwordx4 v240, s[16:19], s48 offen offset:2048 lds\n"
"buffer_load_dwordx4 v241, s[16:19], s48 offen offset:3072 lds\n"
"v_lshlrev_b32 v160, 16, v161\n"
"v_fma_f32 v188, -v191, v191, v188\n"
"v_and_b32 v161, 0xffff0000, v161\n"
"v_cvt_pk_bf16_f32 v163, v162, v163\n"
"v_max_f32 v188, 0, v188\n"
"v_lshlrev_b32 v162, 16, v163\n"
"v_and_b32 v163, 0xffff0000, v163\n"
"v_cvt_pk_bf16_f32 v188, v188, v188\n"
"v_cvt_pk_bf16_f32 v165, v164, v165\n"
"v_lshlrev_b32 v164, 16, v165\n"
"v_lshlrev_b32 v188, 16, v188\n"
"v_and_b32 v165, 0xffff0000, v165\n"
"v_cvt_pk_bf16_f32 v167, v166, v167\n"
"v_add_f32 v188, 0x3727c5ac, v188\n"
"v_lshlrev_b32 v166, 16, v167\n"
"v_and_b32 v167, 0xffff0000, v167\n"
"v_cvt_pk_bf16_f32 v188, v188, v188\n"
"v_bfe_u32 v130, v224, 5, 4\n"
"v_mul_u32_u24 v130, 144, v130\n"
"v_lshlrev_b32 v188, 16, v188\n"
"v_bfe_u32 v131, v224, 4, 1\n"
"v_lshl_add_u32 v130, v131, 3, v130\n"
"v_sqrt_f32 v189, v188\n"
"v_lshrrev_b32 v131, 9, v224\n"
"v_lshl_add_u32 v130, v131, 4, v130\n"
"v_rcp_f32 v190, v189\n"
"s_lshl_b32 s52, s30, 5\n"
"v_add_u32 v130, s52, v130\n"
"v_fma_f32 v188, -v189, v189, v188\n"
"v_mul_f32 v190, 0.5, v190\n"
"v_fma_f32 v189, v188, v190, v189\n"
"v_cvt_pk_bf16_f32 v189, v189, v189\n"
"v_lshlrev_b32 v189, 16, v189\n"
"v_rcp_f32 v129, v189\n"
"s_nop 0\n"
"v_mul_f32 v148, v148, v129\n"
"v_mul_f32 v149, v149, v129\n"
"v_cvt_pk_bf16_f32 v138, v148, v149\n"
"v_mul_f32 v150, v150, v129\n"
"v_mul_f32 v151, v151, v129\n"
"v_cvt_pk_bf16_f32 v139, v150, v151\n"
"ds_write_b64 v130, v[138:139] offset:49152\n"
"v_mul_f32 v152, v152, v129\n"
"v_mul_f32 v153, v153, v129\n"
"v_cvt_pk_bf16_f32 v140, v152, v153\n"
"v_mul_f32 v154, v154, v129\n"
"v_mul_f32 v155, v155, v129\n"
"v_cvt_pk_bf16_f32 v141, v154, v155\n"
"ds_write_b64 v130, v[140:141] offset:51456\n"
"v_mul_f32 v156, v156, v129\n"
"v_mul_f32 v157, v157, v129\n"
"v_cvt_pk_bf16_f32 v142, v156, v157\n"
"v_mul_f32 v158, v158, v129\n"
"v_mul_f32 v159, v159, v129\n"
"v_cvt_pk_bf16_f32 v143, v158, v159\n"
"ds_write_b64 v130, v[142:143] offset:53760\n"
"v_mul_f32 v160, v160, v129\n"
"v_mul_f32 v161, v161, v129\n"
"v_cvt_pk_bf16_f32 v144, v160, v161\n"
"v_mul_f32 v162, v162, v129\n"
"v_mul_f32 v163, v163, v129\n"
"v_cvt_pk_bf16_f32 v145, v162, v163\n"
"ds_write_b64 v130, v[144:145] offset:56064\n"
"v_mul_f32 v164, v164, v129\n"
"v_mul_f32 v165, v165, v129\n"
"v_cvt_pk_bf16_f32 v146, v164, v165\n"
"v_mul_f32 v166, v166, v129\n"
"v_mul_f32 v167, v167, v129\n"
"v_cvt_pk_bf16_f32 v147, v166, v167\n"
"ds_write_b64 v130, v[146:147] offset:58368\n"
"s_waitcnt vmcnt(21)\n"
"ds_read_b64_tr_b16 a[0:1], v246 offset:0\n"
"ds_read_b64_tr_b16 a[2:3], v246 offset:512\n"
"ds_read_b64_tr_b16 a[4:5], v247 offset:0\n"
"ds_read_b64_tr_b16 a[6:7], v247 offset:512\n"
"ds_read_b64_tr_b16 a[8:9], v248 offset:0\n"
"ds_read_b64_tr_b16 a[10:11], v248 offset:512\n"
"ds_read_b64_tr_b16 a[12:13], v249 offset:0\n"
"ds_read_b64_tr_b16 a[14:15], v249 offset:512\n"
"s_waitcnt vmcnt(12)\n"
"ds_read_b64_tr_b16 a[16:17], v246 offset:4096\n"
"ds_read_b64_tr_b16 a[18:19], v246 offset:4608\n"
"s_waitcnt lgkmcnt(10)\n"
"s_barrier\n"
"ds_read_b128 v[192:195], v242 offset:0\n"
"ds_read_b128 v[196:199], v242 offset:2048\n"
"ds_read_b128 v[200:203], v242 offset:4096\n"
"ds_read_b128 v[204:207], v242 offset:6144\n"
"ds_read_b128 v[208:211], v242 offset:8192\n"
"v_lshrrev_b32 v131, 7, v224\n"
"s_waitcnt lgkmcnt(4)\n"
"v_mfma_f32_16x16x32_bf16 v[0:3], a[0:3], v[192:195], 0\n"
"s_add_u32 m0, s37, 16384\n"
"ds_read_b128 v[212:215], v242 offset:10240\n"
"v_lshl_add_u32 v131, v131, 4, v224\n"
"v_mfma_f32_16x16x32_bf16 v[4:7], a[4:7], v[192:195], 0\n"
"s_add_u32 s48, s47, 1048576\n"
"s_and_b32 s48, s48, 4194303\n"
"s_mul_i32 s52, s30, 1152\n"
"v_mfma_f32_16x16x32_bf16 v[8:11], a[8:11], v[192:195], 0\n"
"ds_read_b64_tr_b16 a[20:21], v247 offset:4096\n"
"v_add_u32 v128, s52, v131\n"
"v_mfma_f32_16x16x32_bf16 v[12:15], a[12:15], v[192:195], 0\n"
"s_waitcnt lgkmcnt(5)\n"
"buffer_load_dwordx4 v238, s[16:19], s48 offen offset:0 lds\n"
"v_mfma_f32_16x16x32_bf16 v[16:19], a[0:3], v[196:199], 0\n"
"s_and_b32 s52, s30, 1\n"
"ds_read_b128 v[216:219], v242 offset:12288\n"
"v_lshlrev_b32 v148, 16, v138\n"
"v_mfma_f32_16x16x32_bf16 v[20:23], a[4:7], v[196:199], 0\n"
"s_mul_i32 s51, s52, 1152\n"
"v_and_b32 v149, 0xffff0000, v138\n"
"v_add_u32 v131, s51, v131\n"
"v_mfma_f32_16x16x32_bf16 v[24:27], a[8:11], v[196:199], 0\n"
"s_lshl_b32 s52, s52, 10\n"
"ds_read_b64_tr_b16 a[22:23], v247 offset:4608\n"
"v_mul_f32 v188, v148, v168\n"
"v_mfma_f32_16x16x32_bf16 v[28:31], a[12:15], v[196:199], 0\n"
"s_waitcnt lgkmcnt(3)\n"
"buffer_load_dwordx4 v239, s[16:19], s48 offen offset:1024 lds\n"
"v_mfma_f32_16x16x32_bf16 v[32:35], a[0:3], v[200:203], 0\n"
"ds_read_b128 v[220:223], v242 offset:14336\n"
"v_and_b32 v129, 0x70, v224\n"
"v_mfma_f32_16x16x32_bf16 v[36:39], a[4:7], v[200:203], 0\n"
"s_lshr_b32 s50, s31, 1\n"
"ds_read_b128 v[132:135], v128 offset:49152\n"
"v_mul_f32 v189, v149, v169\n"
"v_mfma_f32_16x16x32_bf16 v[40:43], a[8:11], v[200:203], 0\n"
"s_mul_i32 s50, s50, 20480\n"
"ds_read_b128 a[240:243], v128 offset:53760\n"
"v_and_b32 v130, 0x380, v224\n"
"v_mfma_f32_16x16x32_bf16 v[44:47], a[12:15], v[200:203], 0\n"
"s_and_b32 s51, s31, 1\n"
"ds_read_b128 a[244:247], v131 offset:58368\n"
"v_lshlrev_b32 v150, 16, v139\n"
"v_mfma_f32_16x16x32_bf16 v[48:51], a[0:3], v[204:207], 0\n"
"s_lshl_b32 s51, s51, 7\n"
"ds_read_b128 v[192:195], v243 offset:0\n"
"v_lshl_add_u32 v129, v130, 1, v129\n"
"v_mfma_f32_16x16x32_bf16 v[52:55], a[4:7], v[204:207], 0\n"
"s_add_u32 s50, s50, s51\n"
"ds_read_b64_tr_b16 a[24:25], v248 offset:4096\n"
"v_and_b32 v151, 0xffff0000, v139\n"
"v_mfma_f32_16x16x32_bf16 v[56:59], a[8:11], v[204:207], 0\n"
"s_add_u32 s50, s50, 65536\n"
"buffer_load_dwordx4 v240, s[16:19], s48 offen offset:2048 lds\n"
"v_mfma_f32_16x16x32_bf16 v[60:63], a[12:15], v[204:207], 0\n"
"s_lshl_b32 s52, s52, 1\n"
"ds_read_b128 v[196:199], v243 offset:2048\n"
"v_fmac_f32 v188, v150, v170\n"
"v_mfma_f32_16x16x32_bf16 v[64:67], a[0:3], v[208:211], 0\n"
"s_add_u32 s52, s52, s50\n"
"ds_read_b64_tr_b16 a[26:27], v248 offset:4608\n"
"v_fmac_f32 v189, v151, v171\n"
"v_mfma_f32_16x16x32_bf16 v[68:71], a[4:7], v[208:211], 0\n"
"s_add_u32 s52, s52, 16384\n"
"buffer_load_dwordx4 v241, s[16:19], s48 offen offset:3072 lds\n"
"v_mfma_f32_16x16x32_bf16 v[72:75], a[8:11], v[208:211], 0\n"
"s_add_u32 m0, s36, 32768\n"
"ds_read_b128 v[200:203], v243 offset:4096\n"
"v_lshlrev_b32 v152, 16, v140\n"
"v_mfma_f32_16x16x32_bf16 v[76:79], a[12:15], v[208:211], 0\n"
"s_add_u32 s48, s46, 256\n"
"ds_read_b64_tr_b16 a[28:29], v249 offset:4096\n"
"v_and_b32 v153, 0xffff0000, v140\n"
"v_mfma_f32_16x16x32_bf16 v[80:83], a[0:3], v[212:215], 0\n"
"s_and_b32 s48, s48, 1023\n"
"v_fmac_f32 v188, v152, v172\n"
"v_fmac_f32 v189, v153, v173\n"
"v_mfma_f32_16x16x32_bf16 v[84:87], a[4:7], v[212:215], 0\n"
"s_lshl_b32 s51, s30, 11\n"
"buffer_load_dwordx4 v234, s[12:15], s48 offen offset:0 lds\n"
"v_mfma_f32_16x16x32_bf16 v[88:91], a[8:11], v[212:215], 0\n"
"s_add_u32 s50, s50, s51\n"
"ds_read_b128 v[204:207], v243 offset:6144\n"
"v_lshlrev_b32 v154, 16, v141\n"
"v_mfma_f32_16x16x32_bf16 v[92:95], a[12:15], v[212:215], 0\n"
"s_waitcnt lgkmcnt(3)\n"
"ds_read_b64_tr_b16 a[30:31], v249 offset:4608\n"
"v_and_b32 v155, 0xffff0000, v141\n"
"v_mfma_f32_16x16x32_bf16 v[96:99], a[0:3], v[216:219], 0\n"
"s_add_u32 s51, s50, 8192\n"
"buffer_store_dwordx4 v[132:135], v129, s[8:11], s50 offen sc1\n"
"v_mfma_f32_16x16x32_bf16 v[100:103], a[4:7], v[216:219], 0\n"
"ds_read_b128 v[208:211], v243 offset:8192\n"
"v_fmac_f32 v188, v154, v174\n"
"v_mfma_f32_16x16x32_bf16 v[104:107], a[8:11], v[216:219], 0\n"
"buffer_load_dwordx4 v235, s[12:15], s48 offen offset:1024 lds\n"
"v_mfma_f32_16x16x32_bf16 v[108:111], a[12:15], v[216:219], 0\n"
"buffer_store_dwordx4 a[240:243], v129, s[8:11], s51 offen sc1\n"
"v_mfma_f32_16x16x32_bf16 v[112:115], a[0:3], v[220:223], 0\n"
"s_waitcnt vmcnt(16)\n"
"ds_read_b64_tr_b16 a[32:33], v246 offset:8192\n"
"v_fmac_f32 v189, v155, v175\n"
"v_mfma_f32_16x16x32_bf16 v[116:119], a[4:7], v[220:223], 0\n"
"buffer_store_dwordx4 a[244:247], v129, s[8:11], s52 offen sc1\n"
"v_mfma_f32_16x16x32_bf16 v[120:123], a[8:11], v[220:223], 0\n"
"ds_read_b128 v[212:215], v243 offset:10240\n"
"v_lshlrev_b32 v156, 16, v142\n"
"v_mfma_f32_16x16x32_bf16 v[124:127], a[12:15], v[220:223], 0\n"
"buffer_load_dwordx4 v236, s[12:15], s48 offen offset:2048 lds\n"
"v_mfma_f32_16x16x32_bf16 v[0:3], a[16:19], v[192:195], v[0:3]\n"
"ds_read_b64_tr_b16 a[34:35], v246 offset:8704\n"
"v_and_b32 v157, 0xffff0000, v142\n"
"v_mfma_f32_16x16x32_bf16 v[4:7], a[20:23], v[192:195], v[4:7]\n"
"ds_read_b128 v[216:219], v243 offset:12288\n"
"v_fmac_f32 v188, v156, v176\n"
"v_mfma_f32_16x16x32_bf16 v[8:11], a[24:27], v[192:195], v[8:11]\n"
"s_waitcnt lgkmcnt(4)\n"
"buffer_load_dwordx4 v237, s[12:15], s48 offen offset:3072 lds\n"
"v_mfma_f32_16x16x32_bf16 v[12:15], a[28:31], v[192:195], v[12:15]\n"
"ds_read_b64_tr_b16 a[36:37], v247 offset:8192\n"
"v_fmac_f32 v189, v157, v177\n"
"v_mfma_f32_16x16x32_bf16 v[16:19], a[16:19], v[196:199], v[16:19]\n"
"s_add_u32 m0, s37, 20480\n"
"ds_read_b128 v[220:223], v243 offset:14336\n"
"v_lshlrev_b32 v158, 16, v143\n"
"v_mfma_f32_16x16x32_bf16 v[20:23], a[20:23], v[196:199], v[20:23]\n"
"s_add_u32 s48, s47, 1310720\n"
"v_and_b32 v159, 0xffff0000, v143\n"
"v_fmac_f32 v188, v158, v178\n"
"v_mfma_f32_16x16x32_bf16 v[24:27], a[24:27], v[196:199], v[24:27]\n"
"s_and_b32 s48, s48, 4194303\n"
"ds_read_b64_tr_b16 a[38:39], v247 offset:8704\n"
"v_fmac_f32 v189, v159, v179\n"
"v_mfma_f32_16x16x32_bf16 v[28:31], a[28:31], v[196:199], v[28:31]\n"
"buffer_load_dwordx4 v238, s[16:19], s48 offen offset:0 lds\n"
"v_mfma_f32_16x16x32_bf16 v[32:35], a[16:19], v[200:203], v[32:35]\n"
"v_lshlrev_b32 v160, 16, v144\n"
"v_and_b32 v161, 0xffff0000, v144\n"
"v_mfma_f32_16x16x32_bf16 v[36:39], a[20:23], v[200:203], v[36:39]\n"
"v_fmac_f32 v188, v160, v180\n"
"v_fmac_f32 v189, v161, v181\n"
"v_mfma_f32_16x16x32_bf16 v[40:43], a[24:27], v[200:203], v[40:43]\n"
"ds_read_b64_tr_b16 a[40:41], v248 offset:8192\n"
"v_lshlrev_b32 v162, 16, v145\n"
"v_mfma_f32_16x16x32_bf16 v[44:47], a[28:31], v[200:203], v[44:47]\n"
"buffer_load_dwordx4 v239, s[16:19], s48 offen offset:1024 lds\n"
"v_mfma_f32_16x16x32_bf16 v[48:51], a[16:19], v[204:207], v[48:51]\n"
"v_and_b32 v163, 0xffff0000, v145\n"
"v_fmac_f32 v188, v162, v182\n"
"v_mfma_f32_16x16x32_bf16 v[52:55], a[20:23], v[204:207], v[52:55]\n"
"s_waitcnt vmcnt(17) lgkmcnt(2)\n"
"s_barrier\n"
"v_mfma_f32_16x16x32_bf16 v[56:59], a[24:27], v[204:207], v[56:59]\n"
"ds_read_b128 v[192:195], v242 offset:16384\n"
"v_fmac_f32 v189, v163, v183\n"
"v_mfma_f32_16x16x32_bf16 v[60:63], a[28:31], v[204:207], v[60:63]\n"
"ds_read_b64_tr_b16 a[42:43], v248 offset:8704\n"
"v_lshlrev_b32 v164, 16, v146\n"
"v_mfma_f32_16x16x32_bf16 v[64:67], a[16:19], v[208:211], v[64:67]\n"
"buffer_load_dwordx4 v240, s[16:19], s48 offen offset:2048 lds\n"
"v_mfma_f32_16x16x32_bf16 v[68:71], a[20:23], v[208:211], v[68:71]\n"
"v_and_b32 v165, 0xffff0000, v146\n"
"v_fmac_f32 v188, v164, v184\n"
"v_mfma_f32_16x16x32_bf16 v[72:75], a[24:27], v[208:211], v[72:75]\n"
"ds_read_b128 v[196:199], v242 offset:18432\n"
"v_fmac_f32 v189, v165, v185\n"
"v_mfma_f32_16x16x32_bf16 v[76:79], a[28:31], v[208:211], v[76:79]\n"
"ds_read_b64_tr_b16 a[44:45], v249 offset:8192\n"
"v_lshlrev_b32 v166, 16, v147\n"
"v_mfma_f32_16x16x32_bf16 v[80:83], a[16:19], v[212:215], v[80:83]\n"
"s_lshl_b32 s90, s31, 2\n"
"buffer_load_dwordx4 v241, s[16:19], s48 offen offset:3072 lds\n"
"v_mfma_f32_16x16x32_bf16 v[84:87], a[20:23], v[212:215], v[84:87]\n"
"s_add_u32 s90, s90, s30\n"
"ds_read_b128 v[200:203], v242 offset:20480\n"
"v_and_b32 v167, 0xffff0000, v147\n"
"v_mfma_f32_16x16x32_bf16 v[88:91], a[24:27], v[212:215], v[88:91]\n"
"s_add_u32 m0, s37, 0\n"
"ds_read_b64_tr_b16 a[46:47], v249 offset:8704\n"
"v_fmac_f32 v188, v166, v186\n"
"v_mfma_f32_16x16x32_bf16 v[92:95], a[28:31], v[212:215], v[92:95]\n"
"s_add_u32 s48, s47, 1572864\n"
"v_fmac_f32 v189, v167, v187\n"
"v_add_f32 v188, v188, v189\n"
"v_mfma_f32_16x16x32_bf16 v[96:99], a[16:19], v[216:219], v[96:99]\n"
"s_and_b32 s48, s48, 4194303\n"
"v_add_f32_dpp v188, v188, v188 quad_perm:[1,0,3,2] row_mask:0xf bank_mask:0xf\n"
"s_lshl_b32 s90, s90, 1\n"
"v_mfma_f32_16x16x32_bf16 v[100:103], a[20:23], v[216:219], v[100:103]\n"
"s_sub_u32 s91, 2047, s90\n"
"buffer_load_dwordx4 v238, s[16:19], s48 offen offset:0 lds\n"
"v_mfma_f32_16x16x32_bf16 v[104:107], a[24:27], v[216:219], v[104:107]\n"
"ds_read_b128 v[204:207], v242 offset:22528\n"
"v_add_f32_dpp v188, v188, v188 quad_perm:[2,3,0,1] row_mask:0xf bank_mask:0xf\n"
"v_mfma_f32_16x16x32_bf16 v[108:111], a[28:31], v[216:219], v[108:111]\n"
"s_waitcnt vmcnt(16)\n"
"ds_read_b64_tr_b16 a[48:49], v246 offset:12288\n"
"v_add_f32_dpp v188, v188, v188 row_half_mirror row_mask:0xf bank_mask:0xf\n"
"v_mfma_f32_16x16x32_bf16 v[112:115], a[16:19], v[220:223], v[112:115]\n"
"ds_read_b128 v[208:211], v242 offset:24576\n"
"v_add_f32_dpp v188, v188, v188 row_mirror row_mask:0xf bank_mask:0xf\n"
"v_mfma_f32_16x16x32_bf16 v[116:119], a[20:23], v[220:223], v[116:119]\n"
"s_lshl_b32 s90, s90, 2\n"
"buffer_load_dwordx4 v239, s[16:19], s48 offen offset:1024 lds\n"
"v_mfma_f32_16x16x32_bf16 v[120:123], a[24:27], v[220:223], v[120:123]\n"
"s_add_u32 s90, s90, 2686976\n"
"ds_read_b64_tr_b16 a[50:51], v246 offset:12800\n"
"v_mov_b32 v189, v188\n"
"v_mfma_f32_16x16x32_bf16 v[124:127], a[28:31], v[220:223], v[124:127]\n"
"s_waitcnt lgkmcnt(4)\n"
"v_permlane16_swap_b32 v189, v188\n"
"v_add_f32 v188, v188, v189\n"
"v_mfma_f32_16x16x32_bf16 v[0:3], a[32:35], v[192:195], v[0:3]\n"
"ds_read_b128 v[212:215], v242 offset:26624\n"
"v_cvt_pk_bf16_f32 v188, v188, v188\n"
"v_mfma_f32_16x16x32_bf16 v[4:7], a[36:39], v[192:195], v[4:7]\n"
"buffer_load_dwordx4 v240, s[16:19], s48 offen offset:2048 lds\n"
"v_mfma_f32_16x16x32_bf16 v[8:11], a[40:43], v[192:195], v[8:11]\n"
"ds_read_b64_tr_b16 a[52:53], v247 offset:12288\n"
"v_lshlrev_b32 v188, 16, v188\n"
"v_mfma_f32_16x16x32_bf16 v[12:15], a[44:47], v[192:195], v[12:15]\n"
"ds_read_b128 v[216:219], v242 offset:28672\n"
"v_ashrrev_i32 v189, 31, v188\n"
"v_mfma_f32_16x16x32_bf16 v[16:19], a[32:35], v[196:199], v[16:19]\n"
"v_or_b32 v189, 0x80000000, v189\n"
"v_xor_b32 v188, v188, v189\n"
"v_mfma_f32_16x16x32_bf16 v[20:23], a[36:39], v[196:199], v[20:23]\n"
"buffer_load_dwordx4 v241, s[16:19], s48 offen offset:3072 lds\n"
"v_mfma_f32_16x16x32_bf16 v[24:27], a[40:43], v[196:199], v[24:27]\n"
"ds_read_b64_tr_b16 a[54:55], v247 offset:12800\n"
"v_and_b32 v188, 0xffff0000, v188\n"
"v_mfma_f32_16x16x32_bf16 v[28:31], a[44:47], v[196:199], v[28:31]\n"
"ds_read_b128 v[220:223], v242 offset:30720\n"
"v_lshrrev_b32 v189, 9, v224\n"
"v_mfma_f32_16x16x32_bf16 v[32:35], a[32:35], v[200:203], v[32:35]\n"
"s_add_u32 m0, s36, 0\n"
"v_sub_u32 v190, s91, v189\n"
"v_or_b32 v188, v188, v190\n"
"v_mfma_f32_16x16x32_bf16 v[36:39], a[36:39], v[200:203], v[36:39]\n"
"s_add_u32 s48, s46, 384\n"
"v_and_b32 v190, 0x1f0, v224\n"
"v_lshlrev_b32 v190, 23, v190\n"
"v_mfma_f32_16x16x32_bf16 v[40:43], a[40:43], v[200:203], v[40:43]\n"
"s_and_b32 s48, s48, 1023\n"
"ds_read_b128 v[192:195], v243 offset:16384\n"
"v_lshl_add_u32 v190, v189, 2, v190\n"
"v_mfma_f32_16x16x32_bf16 v[44:47], a[44:47], v[200:203], v[44:47]\n"
"s_waitcnt lgkmcnt(3)\n"
"buffer_store_dword v188, v190, s[8:11], s90 offen sc1\n"
"v_mfma_f32_16x16x32_bf16 v[48:51], a[32:35], v[204:207], v[48:51]\n"
"ds_read_b64_tr_b16 a[56:57], v248 offset:12288\n"
"v_mfma_f32_16x16x32_bf16 v[52:55], a[36:39], v[204:207], v[52:55]\n"
"buffer_load_dwordx4 v234, s[12:15], s48 offen offset:0 lds\n"
"v_mfma_f32_16x16x32_bf16 v[56:59], a[40:43], v[204:207], v[56:59]\n"
"ds_read_b128 v[196:199], v243 offset:18432\n"
"v_mfma_f32_16x16x32_bf16 v[60:63], a[44:47], v[204:207], v[60:63]\n"
"ds_read_b64_tr_b16 a[58:59], v248 offset:12800\n"
"v_mfma_f32_16x16x32_bf16 v[64:67], a[32:35], v[208:211], v[64:67]\n"
"buffer_load_dwordx4 v235, s[12:15], s48 offen offset:1024 lds\n"
"v_mfma_f32_16x16x32_bf16 v[68:71], a[36:39], v[208:211], v[68:71]\n"
"ds_read_b128 v[200:203], v243 offset:20480\n"
"v_mfma_f32_16x16x32_bf16 v[72:75], a[40:43], v[208:211], v[72:75]\n"
"ds_read_b64_tr_b16 a[60:61], v249 offset:12288\n"
"v_mfma_f32_16x16x32_bf16 v[76:79], a[44:47], v[208:211], v[76:79]\n"
"buffer_load_dwordx4 v236, s[12:15], s48 offen offset:2048 lds\n"
"v_mfma_f32_16x16x32_bf16 v[80:83], a[32:35], v[212:215], v[80:83]\n"
"v_mfma_f32_16x16x32_bf16 v[84:87], a[36:39], v[212:215], v[84:87]\n"
"ds_read_b128 v[204:207], v243 offset:22528\n"
"v_mfma_f32_16x16x32_bf16 v[88:91], a[40:43], v[212:215], v[88:91]\n"
"ds_read_b64_tr_b16 a[62:63], v249 offset:12800\n"
"v_mfma_f32_16x16x32_bf16 v[92:95], a[44:47], v[212:215], v[92:95]\n"
"buffer_load_dwordx4 v237, s[12:15], s48 offen offset:3072 lds\n"
"v_mfma_f32_16x16x32_bf16 v[96:99], a[32:35], v[216:219], v[96:99]\n"
"ds_read_b128 v[208:211], v243 offset:24576\n"
"v_mfma_f32_16x16x32_bf16 v[100:103], a[36:39], v[216:219], v[100:103]\n"
"v_mfma_f32_16x16x32_bf16 v[104:107], a[40:43], v[216:219], v[104:107]\n"
"s_add_u32 m0, s37, 4096\n"
"s_waitcnt vmcnt(20)\n"
"ds_read_b64_tr_b16 a[64:65], v246 offset:16384\n"
"v_mfma_f32_16x16x32_bf16 v[108:111], a[44:47], v[216:219], v[108:111]\n"
"s_waitcnt lgkmcnt(4)\n"
"s_add_u32 s48, s47, 1835008\n"
"s_and_b32 s48, s48, 4194303\n"
"v_mfma_f32_16x16x32_bf16 v[112:115], a[32:35], v[220:223], v[112:115]\n"
"ds_read_b128 v[212:215], v243 offset:26624\n"
"v_mfma_f32_16x16x32_bf16 v[116:119], a[36:39], v[220:223], v[116:119]\n"
"buffer_load_dwordx4 v238, s[16:19], s48 offen offset:0 lds\n"
"v_mfma_f32_16x16x32_bf16 v[120:123], a[40:43], v[220:223], v[120:123]\n"
"ds_read_b64_tr_b16 a[66:67], v246 offset:16896\n"
"v_mfma_f32_16x16x32_bf16 v[124:127], a[44:47], v[220:223], v[124:127]\n"
"ds_read_b128 v[216:219], v243 offset:28672\n"
"v_mfma_f32_16x16x32_bf16 v[0:3], a[48:51], v[192:195], v[0:3]\n"
"buffer_load_dwordx4 v239, s[16:19], s48 offen offset:1024 lds\n"
"v_mfma_f32_16x16x32_bf16 v[4:7], a[52:55], v[192:195], v[4:7]\n"
"v_mfma_f32_16x16x32_bf16 v[8:11], a[56:59], v[192:195], v[8:11]\n"
"s_waitcnt lgkmcnt(3)\n"
"ds_read_b64_tr_b16 a[68:69], v247 offset:16384\n"
"v_mfma_f32_16x16x32_bf16 v[12:15], a[60:63], v[192:195], v[12:15]\n"
"ds_read_b128 v[220:223], v243 offset:30720\n"
"v_mfma_f32_16x16x32_bf16 v[16:19], a[48:51], v[196:199], v[16:19]\n"
"v_mfma_f32_16x16x32_bf16 v[20:23], a[52:55], v[196:199], v[20:23]\n"
"buffer_load_dwordx4 v240, s[16:19], s48 offen offset:2048 lds\n"
"v_mfma_f32_16x16x32_bf16 v[24:27], a[56:59], v[196:199], v[24:27]\n"
"ds_read_b64_tr_b16 a[70:71], v247 offset:16896\n"
"v_mfma_f32_16x16x32_bf16 v[28:31], a[60:63], v[196:199], v[28:31]\n"
"v_mfma_f32_16x16x32_bf16 v[32:35], a[48:51], v[200:203], v[32:35]\n"
"v_mfma_f32_16x16x32_bf16 v[36:39], a[52:55], v[200:203], v[36:39]\n"
"buffer_load_dwordx4 v241, s[16:19], s48 offen offset:3072 lds\n"
"v_mfma_f32_16x16x32_bf16 v[40:43], a[56:59], v[200:203], v[40:43]\n"
"ds_read_b64_tr_b16 a[72:73], v248 offset:16384\n"
"v_mfma_f32_16x16x32_bf16 v[44:47], a[60:63], v[200:203], v[44:47]\n"
"v_mfma_f32_16x16x32_bf16 v[48:51], a[48:51], v[204:207], v[48:51]\n"
"s_add_u32 m0, s37, 8192\n"
"s_add_u32 s48, s47, 2097152\n"
"s_and_b32 s48, s48, 4194303\n"
"v_mfma_f32_16x16x32_bf16 v[52:55], a[52:55], v[204:207], v[52:55]\n"
"s_waitcnt vmcnt(17) lgkmcnt(2)\n"
"s_barrier\n"
"v_mfma_f32_16x16x32_bf16 v[56:59], a[56:59], v[204:207], v[56:59]\n"
"ds_read_b128 v[192:195], v242 offset:32768\n"
"v_mfma_f32_16x16x32_bf16 v[60:63], a[60:63], v[204:207], v[60:63]\n"
"ds_read_b64_tr_b16 a[74:75], v248 offset:16896\n"
"v_mfma_f32_16x16x32_bf16 v[64:67], a[48:51], v[208:211], v[64:67]\n"
"buffer_load_dwordx4 v238, s[16:19], s48 offen offset:0 lds\n"
"v_mfma_f32_16x16x32_bf16 v[68:71], a[52:55], v[208:211], v[68:71]\n"
"v_mfma_f32_16x16x32_bf16 v[72:75], a[56:59], v[208:211], v[72:75]\n"
"ds_read_b128 v[196:199], v242 offset:34816\n"
"v_mfma_f32_16x16x32_bf16 v[76:79], a[60:63], v[208:211], v[76:79]\n"
"ds_read_b64_tr_b16 a[76:77], v249 offset:16384\n"
"v_mfma_f32_16x16x32_bf16 v[80:83], a[48:51], v[212:215], v[80:83]\n"
"buffer_load_dwordx4 v239, s[16:19], s48 offen offset:1024 lds\n"
"v_mfma_f32_16x16x32_bf16 v[84:87], a[52:55], v[212:215], v[84:87]\n"
"ds_read_b128 v[200:203], v242 offset:36864\n"
"v_mfma_f32_16x16x32_bf16 v[88:91], a[56:59], v[212:215], v[88:91]\n"
"ds_read_b64_tr_b16 a[78:79], v249 offset:16896\n"
"v_mfma_f32_16x16x32_bf16 v[92:95], a[60:63], v[212:215], v[92:95]\n"
"buffer_load_dwordx4 v240, s[16:19], s48 offen offset:2048 lds\n"
"v_mfma_f32_16x16x32_bf16 v[96:99], a[48:51], v[216:219], v[96:99]\n"
"v_mfma_f32_16x16x32_bf16 v[100:103], a[52:55], v[216:219], v[100:103]\n"
"ds_read_b128 v[204:207], v242 offset:38912\n"
"v_mfma_f32_16x16x32_bf16 v[104:107], a[56:59], v[216:219], v[104:107]\n"
"s_waitcnt vmcnt(16)\n"
"ds_read_b64_tr_b16 a[80:81], v246 offset:20480\n"
"v_mfma_f32_16x16x32_bf16 v[108:111], a[60:63], v[216:219], v[108:111]\n"
"buffer_load_dwordx4 v241, s[16:19], s48 offen offset:3072 lds\n"
"v_mfma_f32_16x16x32_bf16 v[112:115], a[48:51], v[220:223], v[112:115]\n"
"ds_read_b128 v[208:211], v242 offset:40960\n"
"v_mfma_f32_16x16x32_bf16 v[116:119], a[52:55], v[220:223], v[116:119]\n"
"v_mfma_f32_16x16x32_bf16 v[120:123], a[56:59], v[220:223], v[120:123]\n"
"s_add_u32 m0, s36, 16384\n"
"ds_read_b64_tr_b16 a[82:83], v246 offset:20992\n"
"s_add_u32 s48, s46, 512\n"
"v_mfma_f32_16x16x32_bf16 v[124:127], a[60:63], v[220:223], v[124:127]\n"
"s_waitcnt lgkmcnt(4)\n"
"s_and_b32 s48, s48, 1023\n"
"v_mfma_f32_16x16x32_bf16 v[0:3], a[64:67], v[192:195], v[0:3]\n"
"ds_read_b128 v[212:215], v242 offset:43008\n"
"v_mfma_f32_16x16x32_bf16 v[4:7], a[68:71], v[192:195], v[4:7]\n"
"buffer_load_dwordx4 v234, s[12:15], s48 offen offset:0 lds\n"
"v_mfma_f32_16x16x32_bf16 v[8:11], a[72:75], v[192:195], v[8:11]\n"
"ds_read_b64_tr_b16 a[84:85], v247 offset:20480\n"
"v_mfma_f32_16x16x32_bf16 v[12:15], a[76:79], v[192:195], v[12:15]\n"
"ds_read_b128 v[216:219], v242 offset:45056\n"
"v_mfma_f32_16x16x32_bf16 v[16:19], a[64:67], v[196:199], v[16:19]\n"
"buffer_load_dwordx4 v235, s[12:15], s48 offen offset:1024 lds\n"
"v_mfma_f32_16x16x32_bf16 v[20:23], a[68:71], v[196:199], v[20:23]\n"
"v_mfma_f32_16x16x32_bf16 v[24:27], a[72:75], v[196:199], v[24:27]\n"
"ds_read_b64_tr_b16 a[86:87], v247 offset:20992\n"
"v_mfma_f32_16x16x32_bf16 v[28:31], a[76:79], v[196:199], v[28:31]\n"
"ds_read_b128 v[220:223], v242 offset:47104\n"
"v_mfma_f32_16x16x32_bf16 v[32:35], a[64:67], v[200:203], v[32:35]\n"
"v_mfma_f32_16x16x32_bf16 v[36:39], a[68:71], v[200:203], v[36:39]\n"
"buffer_load_dwordx4 v236, s[12:15], s48 offen offset:2048 lds\n"
"v_mfma_f32_16x16x32_bf16 v[40:43], a[72:75], v[200:203], v[40:43]\n"
"ds_read_b128 v[192:195], v243 offset:32768\n"
"v_mfma_f32_16x16x32_bf16 v[44:47], a[76:79], v[200:203], v[44:47]\n"
"s_waitcnt lgkmcnt(3)\n"
"ds_read_b64_tr_b16 a[88:89], v248 offset:20480\n"
"v_mfma_f32_16x16x32_bf16 v[48:51], a[64:67], v[204:207], v[48:51]\n"
"v_mfma_f32_16x16x32_bf16 v[52:55], a[68:71], v[204:207], v[52:55]\n"
"buffer_load_dwordx4 v237, s[12:15], s48 offen offset:3072 lds\n"
"v_mfma_f32_16x16x32_bf16 v[56:59], a[72:75], v[204:207], v[56:59]\n"
"ds_read_b128 v[196:199], v243 offset:34816\n"
"v_mfma_f32_16x16x32_bf16 v[60:63], a[76:79], v[204:207], v[60:63]\n"
"ds_read_b64_tr_b16 a[90:91], v248 offset:20992\n"
"v_mfma_f32_16x16x32_bf16 v[64:67], a[64:67], v[208:211], v[64:67]\n"
"s_add_u32 m0, s37, 12288\n"
"s_add_u32 s48, s47, 2359296\n"
"s_and_b32 s48, s48, 4194303\n"
"v_mfma_f32_16x16x32_bf16 v[68:71], a[68:71], v[208:211], v[68:71]\n"
"ds_read_b128 v[200:203], v243 offset:36864\n"
"v_mfma_f32_16x16x32_bf16 v[72:75], a[72:75], v[208:211], v[72:75]\n"
"ds_read_b64_tr_b16 a[92:93], v249 offset:20480\n"
"v_mfma_f32_16x16x32_bf16 v[76:79], a[76:79], v[208:211], v[76:79]\n"
"buffer_load_dwordx4 v238, s[16:19], s48 offen offset:0 lds\n"
"v_mfma_f32_16x16x32_bf16 v[80:83], a[64:67], v[212:215], v[80:83]\n"
"v_mfma_f32_16x16x32_bf16 v[84:87], a[68:71], v[212:215], v[84:87]\n"
"ds_read_b128 v[204:207], v243 offset:38912\n"
"v_mfma_f32_16x16x32_bf16 v[88:91], a[72:75], v[212:215], v[88:91]\n"
"ds_read_b64_tr_b16 a[94:95], v249 offset:20992\n"
"v_mfma_f32_16x16x32_bf16 v[92:95], a[76:79], v[212:215], v[92:95]\n"
"buffer_load_dwordx4 v239, s[16:19], s48 offen offset:1024 lds\n"
"v_mfma_f32_16x16x32_bf16 v[96:99], a[64:67], v[216:219], v[96:99]\n"
"ds_read_b128 v[208:211], v243 offset:40960\n"
"v_mfma_f32_16x16x32_bf16 v[100:103], a[68:71], v[216:219], v[100:103]\n"
"v_mfma_f32_16x16x32_bf16 v[104:107], a[72:75], v[216:219], v[104:107]\n"
"s_waitcnt vmcnt(19)\n"
"ds_read_b64_tr_b16 a[96:97], v246 offset:0\n"
"v_mfma_f32_16x16x32_bf16 v[108:111], a[76:79], v[216:219], v[108:111]\n"
"s_waitcnt lgkmcnt(4)\n"
"buffer_load_dwordx4 v240, s[16:19], s48 offen offset:2048 lds\n"
"v_mfma_f32_16x16x32_bf16 v[112:115], a[64:67], v[220:223], v[112:115]\n"
"ds_read_b128 v[212:215], v243 offset:43008\n"
"v_mfma_f32_16x16x32_bf16 v[116:119], a[68:71], v[220:223], v[116:119]\n"
"v_mfma_f32_16x16x32_bf16 v[120:123], a[72:75], v[220:223], v[120:123]\n"
"ds_read_b64_tr_b16 a[98:99], v246 offset:512\n"
"v_mfma_f32_16x16x32_bf16 v[124:127], a[76:79], v[220:223], v[124:127]\n"
"ds_read_b128 v[216:219], v243 offset:45056\n"
"v_mfma_f32_16x16x32_bf16 v[0:3], a[80:83], v[192:195], v[0:3]\n"
"buffer_load_dwordx4 v241, s[16:19], s48 offen offset:3072 lds\n"
"v_mfma_f32_16x16x32_bf16 v[4:7], a[84:87], v[192:195], v[4:7]\n"
"v_mfma_f32_16x16x32_bf16 v[8:11], a[88:91], v[192:195], v[8:11]\n"
"s_waitcnt lgkmcnt(3)\n"
"ds_read_b64_tr_b16 a[100:101], v247 offset:0\n"
"s_add_u32 m0, s37, 16384\n"
"v_mfma_f32_16x16x32_bf16 v[12:15], a[92:95], v[192:195], v[12:15]\n"
"s_add_u32 s48, s47, 2621440\n"
"ds_read_b128 v[220:223], v243 offset:47104\n"
"s_and_b32 s48, s48, 4194303\n"
"v_mfma_f32_16x16x32_bf16 v[16:19], a[80:83], v[196:199], v[16:19]\n"
"buffer_load_dwordx4 v238, s[16:19], s48 offen offset:0 lds\n"
"v_mfma_f32_16x16x32_bf16 v[20:23], a[84:87], v[196:199], v[20:23]\n"
"v_mfma_f32_16x16x32_bf16 v[24:27], a[88:91], v[196:199], v[24:27]\n"
"ds_read_b64_tr_b16 a[102:103], v247 offset:512\n"
"v_mfma_f32_16x16x32_bf16 v[28:31], a[92:95], v[196:199], v[28:31]\n"
"v_mfma_f32_16x16x32_bf16 v[32:35], a[80:83], v[200:203], v[32:35]\n"
"s_lshl_b32 s50, s31, 3\n"
"buffer_load_dwordx4 v239, s[16:19], s48 offen offset:1024 lds\n"
"v_mfma_f32_16x16x32_bf16 v[36:39], a[84:87], v[200:203], v[36:39]\n"
"s_add_u32 s50, s50, 256\n"
"s_add_u32 s50, s50, s45\n"
"v_mfma_f32_16x16x32_bf16 v[40:43], a[88:91], v[200:203], v[40:43]\n"
"ds_read_b64_tr_b16 a[104:105], v248 offset:0\n"
"v_mfma_f32_16x16x32_bf16 v[44:47], a[92:95], v[200:203], v[44:47]\n"
"v_mfma_f32_16x16x32_bf16 v[48:51], a[80:83], v[204:207], v[48:51]\n"
"v_mfma_f32_16x16x32_bf16 v[52:55], a[84:87], v[204:207], v[52:55]\n"
"s_waitcnt vmcnt(18) lgkmcnt(2)\n"
"s_barrier\n"
"v_mfma_f32_16x16x32_bf16 v[56:59], a[88:91], v[204:207], v[56:59]\n"
"buffer_load_dwordx4 v240, s[16:19], s48 offen offset:2048 lds\n"
"v_mfma_f32_16x16x32_bf16 v[60:63], a[92:95], v[204:207], v[60:63]\n"
"ds_read_b128 v[192:195], v242 offset:0\n"
"v_mfma_f32_16x16x32_bf16 v[64:67], a[80:83], v[208:211], v[64:67]\n"
"ds_read_b64_tr_b16 a[106:107], v248 offset:512\n"
"v_mfma_f32_16x16x32_bf16 v[68:71], a[84:87], v[208:211], v[68:71]\n"
"buffer_store_dwordx2 v[232:233], v230, s[8:11], s50 offen sc1\n"
"v_mfma_f32_16x16x32_bf16 v[72:75], a[88:91], v[208:211], v[72:75]\n"
"buffer_load_dwordx4 v241, s[16:19], s48 offen offset:3072 lds\n"
"v_mfma_f32_16x16x32_bf16 v[76:79], a[92:95], v[208:211], v[76:79]\n"
"ds_read_b128 v[196:199], v242 offset:2048\n"
"v_mfma_f32_16x16x32_bf16 v[80:83], a[80:83], v[212:215], v[80:83]\n"
"s_add_u32 m0, s36, 32768\n"
"ds_read_b64_tr_b16 a[108:109], v249 offset:0\n"
"s_add_u32 s48, s46, 640\n"
"v_mfma_f32_16x16x32_bf16 v[84:87], a[84:87], v[212:215], v[84:87]\n"
"s_and_b32 s48, s48, 1023\n"
"ds_read_b128 v[200:203], v242 offset:4096\n"
"v_mfma_f32_16x16x32_bf16 v[88:91], a[88:91], v[212:215], v[88:91]\n"
"ds_read_b64_tr_b16 a[110:111], v249 offset:512\n"
"v_mfma_f32_16x16x32_bf16 v[92:95], a[92:95], v[212:215], v[92:95]\n"
"buffer_load_dwordx4 v234, s[12:15], s48 offen offset:0 lds\n"
"v_mfma_f32_16x16x32_bf16 v[96:99], a[80:83], v[216:219], v[96:99]\n"
"v_mfma_f32_16x16x32_bf16 v[100:103], a[84:87], v[216:219], v[100:103]\n"
"ds_read_b128 v[204:207], v242 offset:6144\n"
"v_mfma_f32_16x16x32_bf16 v[104:107], a[88:91], v[216:219], v[104:107]\n"
"s_waitcnt vmcnt(18)\n"
"ds_read_b64_tr_b16 a[112:113], v246 offset:4096\n"
"v_mfma_f32_16x16x32_bf16 v[108:111], a[92:95], v[216:219], v[108:111]\n"
"buffer_load_dwordx4 v235, s[12:15], s48 offen offset:1024 lds\n"
"v_mfma_f32_16x16x32_bf16 v[112:115], a[80:83], v[220:223], v[112:115]\n"
"ds_read_b128 v[208:211], v242 offset:8192\n"
"v_mfma_f32_16x16x32_bf16 v[116:119], a[84:87], v[220:223], v[116:119]\n"
"v_mfma_f32_16x16x32_bf16 v[120:123], a[88:91], v[220:223], v[120:123]\n"
"ds_read_b64_tr_b16 a[114:115], v246 offset:4608\n"
"v_mfma_f32_16x16x32_bf16 v[124:127], a[92:95], v[220:223], v[124:127]\n"
"s_waitcnt lgkmcnt(4)\n"
"buffer_load_dwordx4 v236, s[12:15], s48 offen offset:2048 lds\n"
"v_mfma_f32_16x16x32_bf16 v[0:3], a[96:99], v[192:195], v[0:3]\n"
"ds_read_b128 v[212:215], v242 offset:10240\n"
"v_mfma_f32_16x16x32_bf16 v[4:7], a[100:103], v[192:195], v[4:7]\n"
"v_mfma_f32_16x16x32_bf16 v[8:11], a[104:107], v[192:195], v[8:11]\n"
"ds_read_b64_tr_b16 a[116:117], v247 offset:4096\n"
"v_mfma_f32_16x16x32_bf16 v[12:15], a[108:111], v[192:195], v[12:15]\n"
"ds_read_b128 v[216:219], v242 offset:12288\n"
"v_mfma_f32_16x16x32_bf16 v[16:19], a[96:99], v[196:199], v[16:19]\n"
"buffer_load_dwordx4 v237, s[12:15], s48 offen offset:3072 lds\n"
"v_mfma_f32_16x16x32_bf16 v[20:23], a[100:103], v[196:199], v[20:23]\n"
"v_mfma_f32_16x16x32_bf16 v[24:27], a[104:107], v[196:199], v[24:27]\n"
"s_add_u32 m0, s37, 20480\n"
"ds_read_b64_tr_b16 a[118:119], v247 offset:4608\n"
"s_add_u32 s48, s47, 2883584\n"
"v_mfma_f32_16x16x32_bf16 v[28:31], a[108:111], v[196:199], v[28:31]\n"
"s_and_b32 s48, s48, 4194303\n"
"ds_read_b128 v[220:223], v242 offset:14336\n"
"v_mfma_f32_16x16x32_bf16 v[32:35], a[96:99], v[200:203], v[32:35]\n"
"buffer_load_dwordx4 v238, s[16:19], s48 offen offset:0 lds\n"
"v_mfma_f32_16x16x32_bf16 v[36:39], a[100:103], v[200:203], v[36:39]\n"
"v_mfma_f32_16x16x32_bf16 v[40:43], a[104:107], v[200:203], v[40:43]\n"
"ds_read_b128 v[192:195], v243 offset:0\n"
"v_mfma_f32_16x16x32_bf16 v[44:47], a[108:111], v[200:203], v[44:47]\n"
"s_waitcnt lgkmcnt(3)\n"
"ds_read_b64_tr_b16 a[120:121], v248 offset:4096\n"
"v_mfma_f32_16x16x32_bf16 v[48:51], a[96:99], v[204:207], v[48:51]\n"
"buffer_load_dwordx4 v239, s[16:19], s48 offen offset:1024 lds\n"
"v_mfma_f32_16x16x32_bf16 v[52:55], a[100:103], v[204:207], v[52:55]\n"
"v_mfma_f32_16x16x32_bf16 v[56:59], a[104:107], v[204:207], v[56:59]\n"
"ds_read_b128 v[196:199], v243 offset:2048\n"
"v_mfma_f32_16x16x32_bf16 v[60:63], a[108:111], v[204:207], v[60:63]\n"
"ds_read_b64_tr_b16 a[122:123], v248 offset:4608\n"
"v_mfma_f32_16x16x32_bf16 v[64:67], a[96:99], v[208:211], v[64:67]\n"
"v_mfma_f32_16x16x32_bf16 v[68:71], a[100:103], v[208:211], v[68:71]\n"
"ds_read_b128 v[200:203], v243 offset:4096\n"
"v_mfma_f32_16x16x32_bf16 v[72:75], a[104:107], v[208:211], v[72:75]\n"
"buffer_load_dwordx4 v240, s[16:19], s48 offen offset:2048 lds\n"
"v_mfma_f32_16x16x32_bf16 v[76:79], a[108:111], v[208:211], v[76:79]\n"
"ds_read_b64_tr_b16 a[124:125], v249 offset:4096\n"
"v_mfma_f32_16x16x32_bf16 v[80:83], a[96:99], v[212:215], v[80:83]\n"
"v_mfma_f32_16x16x32_bf16 v[84:87], a[100:103], v[212:215], v[84:87]\n"
"ds_read_b128 v[204:207], v243 offset:6144\n"
"v_mfma_f32_16x16x32_bf16 v[88:91], a[104:107], v[212:215], v[88:91]\n"
"buffer_load_dwordx4 v241, s[16:19], s48 offen offset:3072 lds\n"
"v_mfma_f32_16x16x32_bf16 v[92:95], a[108:111], v[212:215], v[92:95]\n"
"ds_read_b64_tr_b16 a[126:127], v249 offset:4608\n"
"v_mfma_f32_16x16x32_bf16 v[96:99], a[96:99], v[216:219], v[96:99]\n"
"s_add_u32 m0, s37, 0\n"
"ds_read_b128 v[208:211], v243 offset:8192\n"
"s_add_u32 s48, s47, 3145728\n"
"v_mfma_f32_16x16x32_bf16 v[100:103], a[100:103], v[216:219], v[100:103]\n"
"s_and_b32 s48, s48, 4194303\n"
"v_mfma_f32_16x16x32_bf16 v[104:107], a[104:107], v[216:219], v[104:107]\n"
"s_waitcnt vmcnt(21)\n"
"ds_read_b64_tr_b16 a[128:129], v246 offset:8192\n"
"v_mfma_f32_16x16x32_bf16 v[108:111], a[108:111], v[216:219], v[108:111]\n"
"s_waitcnt lgkmcnt(4)\n"
"buffer_load_dwordx4 v238, s[16:19], s48 offen offset:0 lds\n"
"v_mfma_f32_16x16x32_bf16 v[112:115], a[96:99], v[220:223], v[112:115]\n"
"ds_read_b128 v[212:215], v243 offset:10240\n"
"v_mfma_f32_16x16x32_bf16 v[116:119], a[100:103], v[220:223], v[116:119]\n"
"v_mfma_f32_16x16x32_bf16 v[120:123], a[104:107], v[220:223], v[120:123]\n"
"ds_read_b64_tr_b16 a[130:131], v246 offset:8704\n"
"v_mfma_f32_16x16x32_bf16 v[124:127], a[108:111], v[220:223], v[124:127]\n"
"buffer_load_dwordx4 v239, s[16:19], s48 offen offset:1024 lds\n"
"v_mfma_f32_16x16x32_bf16 v[0:3], a[112:115], v[192:195], v[0:3]\n"
"ds_read_b128 v[216:219], v243 offset:12288\n"
"v_mfma_f32_16x16x32_bf16 v[4:7], a[116:119], v[192:195], v[4:7]\n"
"v_mfma_f32_16x16x32_bf16 v[8:11], a[120:123], v[192:195], v[8:11]\n"
"s_waitcnt lgkmcnt(3)\n"
"ds_read_b64_tr_b16 a[132:133], v247 offset:8192\n"
"v_mfma_f32_16x16x32_bf16 v[12:15], a[124:127], v[192:195], v[12:15]\n"
"ds_read_b128 v[220:223], v243 offset:14336\n"
"v_mfma_f32_16x16x32_bf16 v[16:19], a[112:115], v[196:199], v[16:19]\n"
"buffer_load_dwordx4 v240, s[16:19], s48 offen offset:2048 lds\n"
"v_mfma_f32_16x16x32_bf16 v[20:23], a[116:119], v[196:199], v[20:23]\n"
"v_mfma_f32_16x16x32_bf16 v[24:27], a[120:123], v[196:199], v[24:27]\n"
"ds_read_b64_tr_b16 a[134:135], v247 offset:8704\n"
"v_mfma_f32_16x16x32_bf16 v[28:31], a[124:127], v[196:199], v[28:31]\n"
"buffer_load_dwordx4 v241, s[16:19], s48 offen offset:3072 lds\n"
"v_mfma_f32_16x16x32_bf16 v[32:35], a[112:115], v[200:203], v[32:35]\n"
"v_mfma_f32_16x16x32_bf16 v[36:39], a[116:119], v[200:203], v[36:39]\n"
"v_mfma_f32_16x16x32_bf16 v[40:43], a[120:123], v[200:203], v[40:43]\n"
"ds_read_b64_tr_b16 a[136:137], v248 offset:8192\n"
"v_mfma_f32_16x16x32_bf16 v[44:47], a[124:127], v[200:203], v[44:47]\n"
"v_mfma_f32_16x16x32_bf16 v[48:51], a[112:115], v[204:207], v[48:51]\n"
"v_mfma_f32_16x16x32_bf16 v[52:55], a[116:119], v[204:207], v[52:55]\n"
"s_waitcnt vmcnt(21) lgkmcnt(2)\n"
"s_barrier\n"
"v_mfma_f32_16x16x32_bf16 v[56:59], a[120:123], v[204:207], v[56:59]\n"
"s_add_u32 m0, s36, 0\n"
"ds_read_b128 v[192:195], v242 offset:16384\n"
"s_add_u32 s48, s46, 768\n"
"v_mfma_f32_16x16x32_bf16 v[60:63], a[124:127], v[204:207], v[60:63]\n"
"s_and_b32 s48, s48, 1023\n"
"ds_read_b64_tr_b16 a[138:139], v248 offset:8704\n"
"v_mfma_f32_16x16x32_bf16 v[64:67], a[112:115], v[208:211], v[64:67]\n"
"buffer_load_dwordx4 v234, s[12:15], s48 offen offset:0 lds\n"
"v_mfma_f32_16x16x32_bf16 v[68:71], a[116:119], v[208:211], v[68:71]\n"
"v_mfma_f32_16x16x32_bf16 v[72:75], a[120:123], v[208:211], v[72:75]\n"
"ds_read_b128 v[196:199], v242 offset:18432\n"
"v_mfma_f32_16x16x32_bf16 v[76:79], a[124:127], v[208:211], v[76:79]\n"
"ds_read_b64_tr_b16 a[140:141], v249 offset:8192\n"
"v_mfma_f32_16x16x32_bf16 v[80:83], a[112:115], v[212:215], v[80:83]\n"
"buffer_load_dwordx4 v235, s[12:15], s48 offen offset:1024 lds\n"
"v_mfma_f32_16x16x32_bf16 v[84:87], a[116:119], v[212:215], v[84:87]\n"
"ds_read_b128 v[200:203], v242 offset:20480\n"
"v_mfma_f32_16x16x32_bf16 v[88:91], a[120:123], v[212:215], v[88:91]\n"
"ds_read_b64_tr_b16 a[142:143], v249 offset:8704\n"
"v_mfma_f32_16x16x32_bf16 v[92:95], a[124:127], v[212:215], v[92:95]\n"
"v_mfma_f32_16x16x32_bf16 v[96:99], a[112:115], v[216:219], v[96:99]\n"
"v_mfma_f32_16x16x32_bf16 v[100:103], a[116:119], v[216:219], v[100:103]\n"
"ds_read_b128 v[204:207], v242 offset:22528\n"
"v_mfma_f32_16x16x32_bf16 v[104:107], a[120:123], v[216:219], v[104:107]\n"
"buffer_load_dwordx4 v236, s[12:15], s48 offen offset:2048 lds\n"
"v_mfma_f32_16x16x32_bf16 v[108:111], a[124:127], v[216:219], v[108:111]\n"
"s_waitcnt vmcnt(20)\n"
"ds_read_b64_tr_b16 a[144:145], v246 offset:12288\n"
"v_mfma_f32_16x16x32_bf16 v[112:115], a[112:115], v[220:223], v[112:115]\n"
"ds_read_b128 v[208:211], v242 offset:24576\n"
"v_mfma_f32_16x16x32_bf16 v[116:119], a[116:119], v[220:223], v[116:119]\n"
"buffer_load_dwordx4 v237, s[12:15], s48 offen offset:3072 lds\n"
"v_mfma_f32_16x16x32_bf16 v[120:123], a[120:123], v[220:223], v[120:123]\n"
"ds_read_b64_tr_b16 a[146:147], v246 offset:12800\n"
"v_mfma_f32_16x16x32_bf16 v[124:127], a[124:127], v[220:223], v[124:127]\n"
"s_waitcnt lgkmcnt(4)\n"
"v_mfma_f32_16x16x32_bf16 v[0:3], a[128:131], v[192:195], v[0:3]\n"
"s_add_u32 m0, s37, 4096\n"
"ds_read_b128 v[212:215], v242 offset:26624\n"
"s_add_u32 s48, s47, 3407872\n"
"v_mfma_f32_16x16x32_bf16 v[4:7], a[132:135], v[192:195], v[4:7]\n"
"s_and_b32 s48, s48, 4194303\n"
"v_mfma_f32_16x16x32_bf16 v[8:11], a[136:139], v[192:195], v[8:11]\n"
"ds_read_b64_tr_b16 a[148:149], v247 offset:12288\n"
"v_mfma_f32_16x16x32_bf16 v[12:15], a[140:143], v[192:195], v[12:15]\n"
"buffer_load_dwordx4 v238, s[16:19], s48 offen offset:0 lds\n"
"v_mfma_f32_16x16x32_bf16 v[16:19], a[128:131], v[196:199], v[16:19]\n"
"ds_read_b128 v[216:219], v242 offset:28672\n"
"v_mfma_f32_16x16x32_bf16 v[20:23], a[132:135], v[196:199], v[20:23]\n"
"v_mfma_f32_16x16x32_bf16 v[24:27], a[136:139], v[196:199], v[24:27]\n"
"ds_read_b64_tr_b16 a[150:151], v247 offset:12800\n"
"v_mfma_f32_16x16x32_bf16 v[28:31], a[140:143], v[196:199], v[28:31]\n"
"buffer_load_dwordx4 v239, s[16:19], s48 offen offset:1024 lds\n"
"v_mfma_f32_16x16x32_bf16 v[32:35], a[128:131], v[200:203], v[32:35]\n"
"ds_read_b128 v[220:223], v242 offset:30720\n"
"v_mfma_f32_16x16x32_bf16 v[36:39], a[132:135], v[200:203], v[36:39]\n"
"v_mfma_f32_16x16x32_bf16 v[40:43], a[136:139], v[200:203], v[40:43]\n"
"ds_read_b128 v[192:195], v243 offset:16384\n"
"v_mfma_f32_16x16x32_bf16 v[44:47], a[140:143], v[200:203], v[44:47]\n"
"s_waitcnt lgkmcnt(3)\n"
"ds_read_b64_tr_b16 a[152:153], v248 offset:12288\n"
"v_mfma_f32_16x16x32_bf16 v[48:51], a[128:131], v[204:207], v[48:51]\n"
"buffer_load_dwordx4 v240, s[16:19], s48 offen offset:2048 lds\n"
"v_mfma_f32_16x16x32_bf16 v[52:55], a[132:135], v[204:207], v[52:55]\n"
"v_mfma_f32_16x16x32_bf16 v[56:59], a[136:139], v[204:207], v[56:59]\n"
"ds_read_b128 v[196:199], v243 offset:18432\n"
"v_mfma_f32_16x16x32_bf16 v[60:63], a[140:143], v[204:207], v[60:63]\n"
"ds_read_b64_tr_b16 a[154:155], v248 offset:12800\n"
"v_mfma_f32_16x16x32_bf16 v[64:67], a[128:131], v[208:211], v[64:67]\n"
"buffer_load_dwordx4 v241, s[16:19], s48 offen offset:3072 lds\n"
"v_mfma_f32_16x16x32_bf16 v[68:71], a[132:135], v[208:211], v[68:71]\n"
"ds_read_b128 v[200:203], v243 offset:20480\n"
"v_mfma_f32_16x16x32_bf16 v[72:75], a[136:139], v[208:211], v[72:75]\n"
"s_add_u32 m0, s37, 8192\n"
"ds_read_b64_tr_b16 a[156:157], v249 offset:12288\n"
"s_add_u32 s48, s47, 3670016\n"
"v_mfma_f32_16x16x32_bf16 v[76:79], a[140:143], v[208:211], v[76:79]\n"
"s_and_b32 s48, s48, 4194303\n"
"v_mfma_f32_16x16x32_bf16 v[80:83], a[128:131], v[212:215], v[80:83]\n"
"buffer_load_dwordx4 v238, s[16:19], s48 offen offset:0 lds\n"
"v_mfma_f32_16x16x32_bf16 v[84:87], a[132:135], v[212:215], v[84:87]\n"
"ds_read_b128 v[204:207], v243 offset:22528\n"
"v_mfma_f32_16x16x32_bf16 v[88:91], a[136:139], v[212:215], v[88:91]\n"
"ds_read_b64_tr_b16 a[158:159], v249 offset:12800\n"
"v_mfma_f32_16x16x32_bf16 v[92:95], a[140:143], v[212:215], v[92:95]\n"
"v_mfma_f32_16x16x32_bf16 v[96:99], a[128:131], v[216:219], v[96:99]\n"
"ds_read_b128 v[208:211], v243 offset:24576\n"
"v_mfma_f32_16x16x32_bf16 v[100:103], a[132:135], v[216:219], v[100:103]\n"
"buffer_load_dwordx4 v239, s[16:19], s48 offen offset:1024 lds\n"
"v_mfma_f32_16x16x32_bf16 v[104:107], a[136:139], v[216:219], v[104:107]\n"
"s_waitcnt vmcnt(22)\n"
"ds_read_b64_tr_b16 a[160:161], v246 offset:16384\n"
"v_mfma_f32_16x16x32_bf16 v[108:111], a[140:143], v[216:219], v[108:111]\n"
"s_waitcnt lgkmcnt(4)\n"
"v_mfma_f32_16x16x32_bf16 v[112:115], a[128:131], v[220:223], v[112:115]\n"
"ds_read_b128 v[212:215], v243 offset:26624\n"
"v_mfma_f32_16x16x32_bf16 v[116:119], a[132:135], v[220:223], v[116:119]\n"
"buffer_load_dwordx4 v240, s[16:19], s48 offen offset:2048 lds\n"
"v_mfma_f32_16x16x32_bf16 v[120:123], a[136:139], v[220:223], v[120:123]\n"
"ds_read_b64_tr_b16 a[162:163], v246 offset:16896\n"
"v_mfma_f32_16x16x32_bf16 v[124:127], a[140:143], v[220:223], v[124:127]\n"
"ds_read_b128 v[216:219], v243 offset:28672\n"
"v_mfma_f32_16x16x32_bf16 v[0:3], a[144:147], v[192:195], v[0:3]\n"
"v_mfma_f32_16x16x32_bf16 v[4:7], a[148:151], v[192:195], v[4:7]\n"
"buffer_load_dwordx4 v241, s[16:19], s48 offen offset:3072 lds\n"
"v_mfma_f32_16x16x32_bf16 v[8:11], a[152:155], v[192:195], v[8:11]\n"
"s_waitcnt lgkmcnt(3)\n"
"ds_read_b64_tr_b16 a[164:165], v247 offset:16384\n"
"v_mfma_f32_16x16x32_bf16 v[12:15], a[156:159], v[192:195], v[12:15]\n"
"ds_read_b128 v[220:223], v243 offset:30720\n"
"v_mfma_f32_16x16x32_bf16 v[16:19], a[144:147], v[196:199], v[16:19]\n"
"v_mfma_f32_16x16x32_bf16 v[20:23], a[148:151], v[196:199], v[20:23]\n"
"v_mfma_f32_16x16x32_bf16 v[24:27], a[152:155], v[196:199], v[24:27]\n"
"ds_read_b64_tr_b16 a[166:167], v247 offset:16896\n"
"v_mfma_f32_16x16x32_bf16 v[28:31], a[156:159], v[196:199], v[28:31]\n"
"v_mfma_f32_16x16x32_bf16 v[32:35], a[144:147], v[200:203], v[32:35]\n"
"v_mfma_f32_16x16x32_bf16 v[36:39], a[148:151], v[200:203], v[36:39]\n"
"v_mfma_f32_16x16x32_bf16 v[40:43], a[152:155], v[200:203], v[40:43]\n"
"ds_read_b64_tr_b16 a[168:169], v248 offset:16384\n"
"v_mfma_f32_16x16x32_bf16 v[44:47], a[156:159], v[200:203], v[44:47]\n"
"v_mfma_f32_16x16x32_bf16 v[48:51], a[144:147], v[204:207], v[48:51]\n"
"v_mfma_f32_16x16x32_bf16 v[52:55], a[148:151], v[204:207], v[52:55]\n"
"s_waitcnt vmcnt(20) lgkmcnt(2)\n"
"s_barrier\n"
"v_mfma_f32_16x16x32_bf16 v[56:59], a[152:155], v[204:207], v[56:59]\n"
"s_add_u32 m0, s36, 16384\n"
"ds_read_b128 v[192:195], v242 offset:32768\n"
"s_add_u32 s48, s46, 896\n"
"v_mfma_f32_16x16x32_bf16 v[60:63], a[156:159], v[204:207], v[60:63]\n"
"s_and_b32 s48, s48, 1023\n"
"ds_read_b64_tr_b16 a[170:171], v248 offset:16896\n"
"v_mfma_f32_16x16x32_bf16 v[64:67], a[144:147], v[208:211], v[64:67]\n"
"buffer_load_dwordx4 v234, s[12:15], s48 offen offset:0 lds\n"
"v_mfma_f32_16x16x32_bf16 v[68:71], a[148:151], v[208:211], v[68:71]\n"
"v_mfma_f32_16x16x32_bf16 v[72:75], a[152:155], v[208:211], v[72:75]\n"
"ds_read_b128 v[196:199], v242 offset:34816\n"
"v_mfma_f32_16x16x32_bf16 v[76:79], a[156:159], v[208:211], v[76:79]\n"
"ds_read_b64_tr_b16 a[172:173], v249 offset:16384\n"
"v_mfma_f32_16x16x32_bf16 v[80:83], a[144:147], v[212:215], v[80:83]\n"
"buffer_load_dwordx4 v235, s[12:15], s48 offen offset:1024 lds\n"
"v_mfma_f32_16x16x32_bf16 v[84:87], a[148:151], v[212:215], v[84:87]\n"
"ds_read_b128 v[200:203], v242 offset:36864\n"
"v_mfma_f32_16x16x32_bf16 v[88:91], a[152:155], v[212:215], v[88:91]\n"
"ds_read_b64_tr_b16 a[174:175], v249 offset:16896\n"
"v_mfma_f32_16x16x32_bf16 v[92:95], a[156:159], v[212:215], v[92:95]\n"
"v_mfma_f32_16x16x32_bf16 v[96:99], a[144:147], v[216:219], v[96:99]\n"
"v_mfma_f32_16x16x32_bf16 v[100:103], a[148:151], v[216:219], v[100:103]\n"
"ds_read_b128 v[204:207], v242 offset:38912\n"
"v_mfma_f32_16x16x32_bf16 v[104:107], a[152:155], v[216:219], v[104:107]\n"
"buffer_load_dwordx4 v236, s[12:15], s48 offen offset:2048 lds\n"
"v_mfma_f32_16x16x32_bf16 v[108:111], a[156:159], v[216:219], v[108:111]\n"
"s_waitcnt vmcnt(19)\n"
"ds_read_b64_tr_b16 a[176:177], v246 offset:20480\n"
"v_mfma_f32_16x16x32_bf16 v[112:115], a[144:147], v[220:223], v[112:115]\n"
"ds_read_b128 v[208:211], v242 offset:40960\n"
"v_mfma_f32_16x16x32_bf16 v[116:119], a[148:151], v[220:223], v[116:119]\n"
"buffer_load_dwordx4 v237, s[12:15], s48 offen offset:3072 lds\n"
"v_mfma_f32_16x16x32_bf16 v[120:123], a[152:155], v[220:223], v[120:123]\n"
"ds_read_b64_tr_b16 a[178:179], v246 offset:20992\n"
"v_mfma_f32_16x16x32_bf16 v[124:127], a[156:159], v[220:223], v[124:127]\n"
"s_waitcnt lgkmcnt(4)\n"
"v_mfma_f32_16x16x32_bf16 v[0:3], a[160:163], v[192:195], v[0:3]\n"
"s_add_u32 m0, s37, 12288\n"
"ds_read_b128 v[212:215], v242 offset:43008\n"
"s_add_u32 s48, s47, 3932160\n"
"v_mfma_f32_16x16x32_bf16 v[4:7], a[164:167], v[192:195], v[4:7]\n"
"s_and_b32 s48, s48, 4194303\n"
"v_mfma_f32_16x16x32_bf16 v[8:11], a[168:171], v[192:195], v[8:11]\n"
"ds_read_b64_tr_b16 a[180:181], v247 offset:20480\n"
"v_mfma_f32_16x16x32_bf16 v[12:15], a[172:175], v[192:195], v[12:15]\n"
"buffer_load_dwordx4 v238, s[16:19], s48 offen offset:0 lds\n"
"v_mfma_f32_16x16x32_bf16 v[16:19], a[160:163], v[196:199], v[16:19]\n"
"ds_read_b128 v[216:219], v242 offset:45056\n"
"v_mfma_f32_16x16x32_bf16 v[20:23], a[164:167], v[196:199], v[20:23]\n"
"v_mfma_f32_16x16x32_bf16 v[24:27], a[168:171], v[196:199], v[24:27]\n"
"ds_read_b64_tr_b16 a[182:183], v247 offset:20992\n"
"v_mfma_f32_16x16x32_bf16 v[28:31], a[172:175], v[196:199], v[28:31]\n"
"buffer_load_dwordx4 v239, s[16:19], s48 offen offset:1024 lds\n"
"v_mfma_f32_16x16x32_bf16 v[32:35], a[160:163], v[200:203], v[32:35]\n"
"ds_read_b128 v[220:223], v242 offset:47104\n"
"v_mfma_f32_16x16x32_bf16 v[36:39], a[164:167], v[200:203], v[36:39]\n"
"v_mfma_f32_16x16x32_bf16 v[40:43], a[168:171], v[200:203], v[40:43]\n"
"ds_read_b128 v[192:195], v243 offset:32768\n"
"v_mfma_f32_16x16x32_bf16 v[44:47], a[172:175], v[200:203], v[44:47]\n"
"s_waitcnt lgkmcnt(3)\n"
"ds_read_b64_tr_b16 a[184:185], v248 offset:20480\n"
"v_mfma_f32_16x16x32_bf16 v[48:51], a[160:163], v[204:207], v[48:51]\n"
"buffer_load_dwordx4 v240, s[16:19], s48 offen offset:2048 lds\n"
"v_mfma_f32_16x16x32_bf16 v[52:55], a[164:167], v[204:207], v[52:55]\n"
"v_mfma_f32_16x16x32_bf16 v[56:59], a[168:171], v[204:207], v[56:59]\n"
"ds_read_b128 v[196:199], v243 offset:34816\n"
"v_mfma_f32_16x16x32_bf16 v[60:63], a[172:175], v[204:207], v[60:63]\n"
"s_mul_i32 s91, s30, 5120\n"
"ds_read_b64_tr_b16 a[186:187], v248 offset:20992\n"
"v_mfma_f32_16x16x32_bf16 v[64:67], a[160:163], v[208:211], v[64:67]\n"
"s_lshl_b32 s88, s32, 3\n"
"buffer_load_dwordx4 v241, s[16:19], s48 offen offset:3072 lds\n"
"v_mfma_f32_16x16x32_bf16 v[68:71], a[164:167], v[208:211], v[68:71]\n"
"s_add_u32 s88, s88, s34\n"
"ds_read_b128 v[200:203], v243 offset:36864\n"
"v_mfma_f32_16x16x32_bf16 v[72:75], a[168:171], v[208:211], v[72:75]\n"
"s_add_u32 m0, s37, 16384\n"
"ds_read_b64_tr_b16 a[188:189], v249 offset:20480\n"
"s_add_u32 s48, s39, 0\n"
"v_mfma_f32_16x16x32_bf16 v[76:79], a[172:175], v[208:211], v[76:79]\n"
"s_mul_i32 s88, s88, 81920\n"
"s_lshl_b32 s89, s33, 1\n"
"v_mfma_f32_16x16x32_bf16 v[80:83], a[160:163], v[212:215], v[80:83]\n"
"s_add_u32 s89, s89, s30\n"
"buffer_load_dwordx4 v234, s[12:15], s48 offen offset:0 lds\n"
"v_mfma_f32_16x16x32_bf16 v[84:87], a[164:167], v[212:215], v[84:87]\n"
"s_mul_i32 s89, s89, 160\n"
"ds_read_b128 v[204:207], v243 offset:38912\n"
"v_mfma_f32_16x16x32_bf16 v[88:91], a[168:171], v[212:215], v[88:91]\n"
"s_add_u32 s88, s88, s89\n"
"ds_read_b64_tr_b16 a[190:191], v249 offset:20992\n"
"v_mfma_f32_16x16x32_bf16 v[92:95], a[172:175], v[212:215], v[92:95]\n"
"s_add_u32 s24, s74, s88\n"
"s_addc_u32 s25, s75, 0\n"
"v_mfma_f32_16x16x32_bf16 v[96:99], a[160:163], v[216:219], v[96:99]\n"
"s_and_b32 s25, s25, 0xffff\n"
"ds_read_b128 v[208:211], v243 offset:40960\n"
"v_mfma_f32_16x16x32_bf16 v[100:103], a[164:167], v[216:219], v[100:103]\n"
"s_mov_b32 s26, 39840\n"
"buffer_load_dwordx4 v235, s[12:15], s48 offen offset:1024 lds\n"
"v_mfma_f32_16x16x32_bf16 v[104:107], a[168:171], v[216:219], v[104:107]\n"
"s_cmp_lt_u32 s30, 2\n"
"s_waitcnt vmcnt(22)\n"
"ds_read_b64_tr_b16 a[192:193], v246 offset:0\n"
"v_mfma_f32_16x16x32_bf16 v[108:111], a[172:175], v[216:219], v[108:111]\n"
"s_waitcnt lgkmcnt(4)\n"
"s_cselect_b64 s[40:41], s[78:79], s[80:81]\n"
"s_mov_b32 s27, 0x20000\n"
"v_mfma_f32_16x16x32_bf16 v[112:115], a[160:163], v[220:223], v[112:115]\n"
"s_and_b32 s41, s41, 0xffff\n"
"ds_read_b128 v[212:215], v243 offset:43008\n"
"s_mov_b32 s42, 2211840\n"
"v_mfma_f32_16x16x32_bf16 v[116:119], a[164:167], v[220:223], v[116:119]\n"
"s_mov_b32 s43, 0x20000\n"
"buffer_load_dwordx4 v236, s[12:15], s48 offen offset:2048 lds\n"
"v_mfma_f32_16x16x32_bf16 v[120:123], a[168:171], v[220:223], v[120:123]\n"
"ds_read_b64_tr_b16 a[194:195], v246 offset:512\n"
"v_lshrrev_b32 v254, 6, v224\n"
"v_mfma_f32_16x16x32_bf16 v[124:127], a[172:175], v[220:223], v[124:127]\n"
"ds_read_b128 v[216:219], v243 offset:45056\n"
"v_mul_u32_u24 v254, 160, v254\n"
"v_mfma_f32_16x16x32_bf16 v[0:3], a[176:179], v[192:195], v[0:3]\n"
"v_bfe_u32 v255, v224, 4, 2\n"
"v_lshl_add_u32 v254, v255, 3, v254\n"
"v_mfma_f32_16x16x32_bf16 v[4:7], a[180:183], v[192:195], v[4:7]\n"
"buffer_load_dwordx4 v237, s[12:15], s48 offen offset:3072 lds\n"
"v_mfma_f32_16x16x32_bf16 v[8:11], a[184:187], v[192:195], v[8:11]\n"
"s_waitcnt lgkmcnt(3)\n"
"ds_read_b64_tr_b16 a[196:197], v247 offset:0\n"
"v_add_u32 v254, s91, v254\n"
"v_mfma_f32_16x16x32_bf16 v[12:15], a[188:191], v[192:195], v[12:15]\n"
"ds_read_b128 v[220:223], v243 offset:47104\n"
"v_mfma_f32_16x16x32_bf16 v[16:19], a[176:179], v[196:199], v[16:19]\n"
"s_add_u32 m0, s37, 20480\n"
"s_add_u32 s48, s98, 0\n"
"v_mfma_f32_16x16x32_bf16 v[20:23], a[180:183], v[196:199], v[20:23]\n"
"v_mfma_f32_16x16x32_bf16 v[24:27], a[184:187], v[196:199], v[24:27]\n"
"ds_read_b64_tr_b16 a[198:199], v247 offset:512\n"
"v_mfma_f32_16x16x32_bf16 v[28:31], a[188:191], v[196:199], v[28:31]\n"
"buffer_load_dwordx4 v234, s[12:15], s48 offen offset:0 lds\n"
"v_mfma_f32_16x16x32_bf16 v[32:35], a[176:179], v[200:203], v[32:35]\n"
"v_lshrrev_b32 v149, 4, v224\n"
"v_mfma_f32_16x16x32_bf16 v[36:39], a[180:183], v[200:203], v[36:39]\n"
"v_min_u32 v149, 49, v149\n"
"v_mfma_f32_16x16x32_bf16 v[40:43], a[184:187], v[200:203], v[40:43]\n"
"ds_read_b64_tr_b16 a[200:201], v248 offset:0\n"
"v_mul_u32_u24 v150, 52, v149\n"
"v_mfma_f32_16x16x32_bf16 v[44:47], a[188:191], v[200:203], v[44:47]\n"
"buffer_load_dwordx4 v235, s[12:15], s48 offen offset:1024 lds\n"
"v_mfma_f32_16x16x32_bf16 v[48:51], a[176:179], v[204:207], v[48:51]\n"
"v_lshrrev_b32 v150, 8, v150\n"
"v_mul_u32_u24 v150, 2901, v150\n"
"v_mfma_f32_16x16x32_bf16 v[52:55], a[180:183], v[204:207], v[52:55]\n"
"s_waitcnt vmcnt(22) lgkmcnt(2)\n"
"s_barrier\n"
"v_mfma_f32_16x16x32_bf16 v[56:59], a[184:187], v[204:207], v[56:59]\n"
"ds_read_b128 v[192:195], v242 offset:0\n"
"v_mul_u32_u24 v149, 111, v149\n"
"v_mfma_f32_16x16x32_bf16 v[60:63], a[188:191], v[204:207], v[60:63]\n"
"ds_read_b64_tr_b16 a[202:203], v248 offset:512\n"
"v_add_u32 v149, v149, v150\n"
"v_mfma_f32_16x16x32_bf16 v[64:67], a[176:179], v[208:211], v[64:67]\n"
"s_lshr_b32 s50, s31, 3\n"
"buffer_load_dwordx4 v236, s[12:15], s48 offen offset:2048 lds\n"
"v_mfma_f32_16x16x32_bf16 v[68:71], a[180:183], v[208:211], v[68:71]\n"
"s_lshl_b32 s50, s50, 1\n"
"v_and_b32 v149, -4, v149\n"
"v_mfma_f32_16x16x32_bf16 v[72:75], a[184:187], v[208:211], v[72:75]\n"
"s_and_b32 s51, s30, 1\n"
"ds_read_b128 v[196:199], v242 offset:2048\n"
"v_mfma_f32_16x16x32_bf16 v[76:79], a[188:191], v[208:211], v[76:79]\n"
"s_add_u32 s50, s50, s51\n"
"ds_read_b64_tr_b16 a[204:205], v249 offset:0\n"
"v_lshrrev_b32 v252, 4, v224\n"
"v_mfma_f32_16x16x32_bf16 v[80:83], a[176:179], v[212:215], v[80:83]\n"
"s_mul_i32 s50, s50, 34560\n"
"buffer_load_dwordx4 v237, s[12:15], s48 offen offset:3072 lds\n"
"v_mfma_f32_16x16x32_bf16 v[84:87], a[180:183], v[212:215], v[84:87]\n"
"s_mul_i32 s51, s32, 448\n"
"ds_read_b128 v[200:203], v242 offset:4096\n"
"v_add_u32 v252, 0, v252\n"
"v_mfma_f32_16x16x32_bf16 v[88:91], a[184:187], v[212:215], v[88:91]\n"
"s_add_u32 s50, s50, s51\n"
"ds_read_b64_tr_b16 a[206:207], v249 offset:512\n"
"v_mul_u32_u24 v252, 205, v252\n"
"v_mfma_f32_16x16x32_bf16 v[92:95], a[188:191], v[212:215], v[92:95]\n"
"s_add_u32 m0, s91, 45056\n"
"v_lshrrev_b32 v252, 11, v252\n"
"v_mul_u32_u24 v252, 1120, v252\n"
"v_mfma_f32_16x16x32_bf16 v[96:99], a[176:179], v[216:219], v[96:99]\n"
"s_mov_b32 s48, 0\n"
"v_add_u32 v252, v252, v224\n"
"v_lshrrev_b32 v251, 4, v224\n"
"v_mfma_f32_16x16x32_bf16 v[100:103], a[180:183], v[216:219], v[100:103]\n"
"ds_read_b128 v[204:207], v242 offset:6144\n"
"v_add_u32 v251, 64, v251\n"
"v_mfma_f32_16x16x32_bf16 v[104:107], a[184:187], v[216:219], v[104:107]\n"
"buffer_load_dwordx4 v252, s[24:27], s48 offen lds\n"
"v_mfma_f32_16x16x32_bf16 v[108:111], a[188:191], v[216:219], v[108:111]\n"
"s_waitcnt vmcnt(21)\n"
"ds_read_b64_tr_b16 a[208:209], v246 offset:4096\n"
"v_mul_u32_u24 v251, 205, v251\n"
"v_mfma_f32_16x16x32_bf16 v[112:115], a[176:179], v[220:223], v[112:115]\n"
"s_add_u32 m0, s91, 46080\n"
"buffer_load_dword v149, v149, s[40:43], s50 offen\n"
"v_mfma_f32_16x16x32_bf16 v[116:119], a[180:183], v[220:223], v[116:119]\n"
"s_mov_b32 s48, 1024\n"
"ds_read_b128 v[208:211], v242 offset:8192\n"
"v_lshrrev_b32 v251, 11, v251\n"
"v_mfma_f32_16x16x32_bf16 v[120:123], a[184:187], v[220:223], v[120:123]\n"
"ds_read_b64_tr_b16 a[210:211], v246 offset:4608\n"
"v_mul_u32_u24 v251, 1120, v251\n"
"v_mfma_f32_16x16x32_bf16 v[124:127], a[188:191], v[220:223], v[124:127]\n"
"s_waitcnt lgkmcnt(4)\n"
"v_add_u32 v251, v251, v224\n"
"v_lshrrev_b32 v252, 4, v224\n"
"v_mfma_f32_16x16x32_bf16 v[0:3], a[192:195], v[192:195], v[0:3]\n"
"buffer_load_dwordx4 v251, s[24:27], s48 offen lds\n"
"v_mfma_f32_16x16x32_bf16 v[4:7], a[196:199], v[192:195], v[4:7]\n"
"s_add_u32 m0, s91, 47104\n"
"ds_read_b128 v[212:215], v242 offset:10240\n"
"v_add_u32 v252, 128, v252\n"
"v_mfma_f32_16x16x32_bf16 v[8:11], a[200:203], v[192:195], v[8:11]\n"
"s_mov_b32 s48, 2048\n"
"ds_read_b64_tr_b16 a[212:213], v247 offset:4096\n"
"v_mul_u32_u24 v252, 205, v252\n"
"v_mfma_f32_16x16x32_bf16 v[12:15], a[204:207], v[192:195], v[12:15]\n"
"ds_read_b128 v[216:219], v242 offset:12288\n"
"v_lshrrev_b32 v252, 11, v252\n"
"v_mfma_f32_16x16x32_bf16 v[16:19], a[192:195], v[196:199], v[16:19]\n"
"v_mul_u32_u24 v252, 1120, v252\n"
"v_add_u32 v252, v252, v224\n"
"v_mfma_f32_16x16x32_bf16 v[20:23], a[196:199], v[196:199], v[20:23]\n"
"buffer_load_dwordx4 v252, s[24:27], s48 offen lds\n"
"v_mfma_f32_16x16x32_bf16 v[24:27], a[200:203], v[196:199], v[24:27]\n"
"s_add_u32 m0, s91, 48128\n"
"ds_read_b64_tr_b16 a[214:215], v247 offset:4608\n"
"v_lshrrev_b32 v251, 4, v224\n"
"v_mfma_f32_16x16x32_bf16 v[28:31], a[204:207], v[196:199], v[28:31]\n"
"s_mov_b32 s48, 3072\n"
"ds_read_b128 v[220:223], v242 offset:14336\n"
"v_add_u32 v251, 192, v251\n"
"v_mfma_f32_16x16x32_bf16 v[32:35], a[192:195], v[200:203], v[32:35]\n"
"v_mul_u32_u24 v251, 205, v251\n"
"v_lshrrev_b32 v251, 11, v251\n"
"v_mfma_f32_16x16x32_bf16 v[36:39], a[196:199], v[200:203], v[36:39]\n"
"v_mul_u32_u24 v251, 1120, v251\n"
"v_add_u32 v251, v251, v224\n"
"v_mfma_f32_16x16x32_bf16 v[40:43], a[200:203], v[200:203], v[40:43]\n"
"buffer_load_dwordx4 v251, s[24:27], s48 offen lds\n"
"v_mfma_f32_16x16x32_bf16 v[44:47], a[204:207], v[200:203], v[44:47]\n"
"s_waitcnt lgkmcnt(2)\n"
"ds_read_b128 v[192:195], v243 offset:0\n"
"v_lshrrev_b32 v252, 4, v224\n"
"v_mfma_f32_16x16x32_bf16 v[48:51], a[192:195], v[204:207], v[48:51]\n"
"s_add_u32 m0, s91, 49152\n"
"ds_read_b64_tr_b16 a[216:217], v248 offset:4096\n"
"v_add_u32 v252, 256, v252\n"
"v_mfma_f32_16x16x32_bf16 v[52:55], a[196:199], v[204:207], v[52:55]\n"
"s_mov_b32 s48, 4096\n"
"v_mul_u32_u24 v252, 205, v252\n"
"v_lshrrev_b32 v252, 11, v252\n"
"v_mfma_f32_16x16x32_bf16 v[56:59], a[200:203], v[204:207], v[56:59]\n"
"ds_read_b128 v[196:199], v243 offset:2048\n"
"v_mul_u32_u24 v252, 1120, v252\n"
"v_mfma_f32_16x16x32_bf16 v[60:63], a[204:207], v[204:207], v[60:63]\n"
"ds_read_b64_tr_b16 a[218:219], v248 offset:4608\n"
"v_add_u32 v252, v252, v224\n"
"v_mfma_f32_16x16x32_bf16 v[64:67], a[192:195], v[208:211], v[64:67]\n"
"buffer_load_dwordx4 v252, s[24:27], s48 offen lds\n"
"v_mfma_f32_16x16x32_bf16 v[68:71], a[196:199], v[208:211], v[68:71]\n"
"s_add_u32 s24, s24, 40960\n"
"ds_read_b128 v[200:203], v243 offset:4096\n"
"v_lshrrev_b32 v251, 4, v224\n"
"v_mfma_f32_16x16x32_bf16 v[72:75], a[200:203], v[208:211], v[72:75]\n"
"s_addc_u32 s25, s25, 0\n"
"ds_read_b64_tr_b16 a[220:221], v249 offset:4096\n"
"v_add_u32 v251, 0, v251\n"
"v_mfma_f32_16x16x32_bf16 v[76:79], a[204:207], v[208:211], v[76:79]\n"
"s_add_u32 m0, s91, 55296\n"
"v_mul_u32_u24 v251, 205, v251\n"
"v_lshrrev_b32 v251, 11, v251\n"
"v_mfma_f32_16x16x32_bf16 v[80:83], a[192:195], v[212:215], v[80:83]\n"
"s_mov_b32 s48, 0\n"
"v_mul_u32_u24 v251, 1120, v251\n"
"v_add_u32 v251, v251, v224\n"
"v_mfma_f32_16x16x32_bf16 v[84:87], a[196:199], v[212:215], v[84:87]\n"
"buffer_load_dwordx4 v251, s[24:27], s48 offen lds\n"
"v_mfma_f32_16x16x32_bf16 v[88:91], a[200:203], v[212:215], v[88:91]\n"
"s_add_u32 m0, s91, 56320\n"
"ds_read_b128 v[204:207], v243 offset:6144\n"
"v_lshrrev_b32 v252, 4, v224\n"
"v_mfma_f32_16x16x32_bf16 v[92:95], a[204:207], v[212:215], v[92:95]\n"
"s_mov_b32 s48, 1024\n"
"ds_read_b64_tr_b16 a[222:223], v249 offset:4608\n"
"v_add_u32 v252, 64, v252\n"
"v_mfma_f32_16x16x32_bf16 v[96:99], a[192:195], v[216:219], v[96:99]\n"
"ds_read_b128 v[208:211], v243 offset:8192\n"
"v_mul_u32_u24 v252, 205, v252\n"
"v_mfma_f32_16x16x32_bf16 v[100:103], a[196:199], v[216:219], v[100:103]\n"
"v_lshrrev_b32 v252, 11, v252\n"
"v_mul_u32_u24 v252, 1120, v252\n"
"v_mfma_f32_16x16x32_bf16 v[104:107], a[200:203], v[216:219], v[104:107]\n"
"s_waitcnt vmcnt(23)\n"
"ds_read_b64_tr_b16 a[224:225], v246 offset:8192\n"
"v_add_u32 v252, v252, v224\n"
"v_mfma_f32_16x16x32_bf16 v[108:111], a[204:207], v[216:219], v[108:111]\n"
"s_waitcnt lgkmcnt(4)\n"
"buffer_load_dwordx4 v252, s[24:27], s48 offen lds\n"
"v_mfma_f32_16x16x32_bf16 v[112:115], a[192:195], v[220:223], v[112:115]\n"
"s_add_u32 m0, s91, 57344\n"
"ds_read_b128 v[212:215], v243 offset:10240\n"
"v_lshrrev_b32 v251, 4, v224\n"
"v_mfma_f32_16x16x32_bf16 v[116:119], a[196:199], v[220:223], v[116:119]\n"
"s_mov_b32 s48, 2048\n"
"v_add_u32 v251, 128, v251\n"
"v_mul_u32_u24 v251, 205, v251\n"
"v_mfma_f32_16x16x32_bf16 v[120:123], a[200:203], v[220:223], v[120:123]\n"
"ds_read_b64_tr_b16 a[226:227], v246 offset:8704\n"
"v_lshrrev_b32 v251, 11, v251\n"
"v_mfma_f32_16x16x32_bf16 v[124:127], a[204:207], v[220:223], v[124:127]\n"
"ds_read_b128 v[216:219], v243 offset:12288\n"
"v_mul_u32_u24 v251, 1120, v251\n"
"v_mfma_f32_16x16x32_bf16 v[0:3], a[208:211], v[192:195], v[0:3]\n"
"v_add_u32 v251, v251, v224\n"
"v_lshrrev_b32 v252, 4, v224\n"
"v_mfma_f32_16x16x32_bf16 v[4:7], a[212:215], v[192:195], v[4:7]\n"
"buffer_load_dwordx4 v251, s[24:27], s48 offen lds\n"
"v_mfma_f32_16x16x32_bf16 v[8:11], a[216:219], v[192:195], v[8:11]\n"
"s_waitcnt lgkmcnt(3)\n"
"ds_read_b64_tr_b16 a[228:229], v247 offset:8192\n"
"v_add_u32 v252, 192, v252\n"
"v_mfma_f32_16x16x32_bf16 v[12:15], a[220:223], v[192:195], v[12:15]\n"
"s_add_u32 m0, s91, 58368\n"
"ds_read_b128 v[220:223], v243 offset:14336\n"
"v_mul_u32_u24 v252, 205, v252\n"
"v_mfma_f32_16x16x32_bf16 v[16:19], a[208:211], v[196:199], v[16:19]\n"
"s_mov_b32 s48, 3072\n"
"v_lshrrev_b32 v252, 11, v252\n"
"v_mul_u32_u24 v252, 1120, v252\n"
"v_mfma_f32_16x16x32_bf16 v[20:23], a[212:215], v[196:199], v[20:23]\n"
"v_add_u32 v252, v252, v224\n"
"v_lshrrev_b32 v251, 4, v224\n"
"v_mfma_f32_16x16x32_bf16 v[24:27], a[216:219], v[196:199], v[24:27]\n"
"buffer_load_dwordx4 v252, s[24:27], s48 offen lds\n"
"v_mfma_f32_16x16x32_bf16 v[28:31], a[220:223], v[196:199], v[28:31]\n"
"s_add_u32 m0, s91, 59392\n"
"ds_read_b64_tr_b16 a[230:231], v247 offset:8704\n"
"v_add_u32 v251, 256, v251\n"
"v_mfma_f32_16x16x32_bf16 v[32:35], a[208:211], v[200:203], v[32:35]\n"
"s_mov_b32 s48, 4096\n"
"v_mul_u32_u24 v251, 205, v251\n"
"v_lshrrev_b32 v251, 11, v251\n"
"v_mfma_f32_16x16x32_bf16 v[36:39], a[212:215], v[200:203], v[36:39]\n"
"v_mul_u32_u24 v251, 1120, v251\n"
"v_add_u32 v251, v251, v224\n"
"v_mfma_f32_16x16x32_bf16 v[40:43], a[216:219], v[200:203], v[40:43]\n"
"buffer_load_dwordx4 v251, s[24:27], s48 offen lds\n"
"v_mfma_f32_16x16x32_bf16 v[44:47], a[220:223], v[200:203], v[44:47]\n"
"s_add_u32 m0, s37, 0\n"
"ds_read_b64_tr_b16 a[232:233], v248 offset:8192\n"
"s_add_u32 s48, s39, 32768\n"
"v_mfma_f32_16x16x32_bf16 v[48:51], a[208:211], v[204:207], v[48:51]\n"
"buffer_load_dwordx4 v234, s[12:15], s48 offen offset:0 lds\n"
"v_mfma_f32_16x16x32_bf16 v[52:55], a[212:215], v[204:207], v[52:55]\n"
"s_waitcnt vmcnt(24) lgkmcnt(2)\n"
"s_barrier\n"
"v_mfma_f32_16x16x32_bf16 v[56:59], a[216:219], v[204:207], v[56:59]\n"
"buffer_load_dwordx4 v235, s[12:15], s48 offen offset:1024 lds\n"
"v_mfma_f32_16x16x32_bf16 v[60:63], a[220:223], v[204:207], v[60:63]\n"
"ds_read_b128 v[192:195], v242 offset:16384\n"
"v_mfma_f32_16x16x32_bf16 v[64:67], a[208:211], v[208:211], v[64:67]\n"
"ds_read_b64_tr_b16 a[234:235], v248 offset:8704\n"
"v_mfma_f32_16x16x32_bf16 v[68:71], a[212:215], v[208:211], v[68:71]\n"
"v_mfma_f32_16x16x32_bf16 v[72:75], a[216:219], v[208:211], v[72:75]\n"
"ds_read_b128 v[196:199], v242 offset:18432\n"
"v_mfma_f32_16x16x32_bf16 v[76:79], a[220:223], v[208:211], v[76:79]\n"
"ds_read_b64_tr_b16 a[236:237], v249 offset:8192\n"
"v_mfma_f32_16x16x32_bf16 v[80:83], a[208:211], v[212:215], v[80:83]\n"
"buffer_load_dwordx4 v236, s[12:15], s48 offen offset:2048 lds\n"
"v_mfma_f32_16x16x32_bf16 v[84:87], a[212:215], v[212:215], v[84:87]\n"
"ds_read_b128 v[200:203], v242 offset:20480\n"
"v_mfma_f32_16x16x32_bf16 v[88:91], a[216:219], v[212:215], v[88:91]\n"
"ds_read_b64_tr_b16 a[238:239], v249 offset:8704\n"
"v_mfma_f32_16x16x32_bf16 v[92:95], a[220:223], v[212:215], v[92:95]\n"
"buffer_load_dwordx4 v237, s[12:15], s48 offen offset:3072 lds\n"
"v_mfma_f32_16x16x32_bf16 v[96:99], a[208:211], v[216:219], v[96:99]\n"
"s_lshl_b32 s50, s35, 20\n"
"v_mfma_f32_16x16x32_bf16 v[100:103], a[212:215], v[216:219], v[100:103]\n"
"s_lshl_b32 s51, s34, 15\n"
"ds_read_b128 v[204:207], v242 offset:22528\n"
"s_add_u32 m0, s37, 4096\n"
"v_mfma_f32_16x16x32_bf16 v[104:107], a[216:219], v[216:219], v[104:107]\n"
"s_add_u32 s48, s98, 32768\n"
"s_waitcnt vmcnt(23)\n"
"ds_read_b64_tr_b16 a[240:241], v246 offset:12288\n"
"v_mfma_f32_16x16x32_bf16 v[108:111], a[220:223], v[216:219], v[108:111]\n"
"s_add_u32 s50, s50, s51\n"
"buffer_load_dwordx4 v234, s[12:15], s48 offen offset:0 lds\n"
"v_mfma_f32_16x16x32_bf16 v[112:115], a[208:211], v[220:223], v[112:115]\n"
"s_and_b32 s51, s30, 1\n"
"ds_read_b128 v[208:211], v242 offset:24576\n"
"s_lshl_b32 s51, s51, 11\n"
"v_mfma_f32_16x16x32_bf16 v[116:119], a[212:215], v[220:223], v[116:119]\n"
"s_add_u32 s50, s50, s51\n"
"v_mfma_f32_16x16x32_bf16 v[120:123], a[216:219], v[220:223], v[120:123]\n"
"s_add_u32 s50, s50, 10485760\n"
"ds_read_b64_tr_b16 a[242:243], v246 offset:12800\n"
"v_mfma_f32_16x16x32_bf16 v[124:127], a[220:223], v[220:223], v[124:127]\n"
"s_waitcnt lgkmcnt(4)\n"
"buffer_load_dwordx4 v235, s[12:15], s48 offen offset:1024 lds\n"
"v_mfma_f32_16x16x32_bf16 v[0:3], a[224:227], v[192:195], v[0:3]\n"
"ds_read_b128 v[212:215], v242 offset:26624\n"
"v_add_u32 v250, s50, v224\n"
"v_mfma_f32_16x16x32_bf16 v[4:7], a[228:231], v[192:195], v[4:7]\n"
"v_mfma_f32_16x16x32_bf16 v[8:11], a[232:235], v[192:195], v[8:11]\n"
"ds_read_b64_tr_b16 a[244:245], v247 offset:12288\n"
"v_mfma_f32_16x16x32_bf16 v[12:15], a[236:239], v[192:195], v[12:15]\n"
"ds_read_b128 v[216:219], v242 offset:28672\n"
"v_mfma_f32_16x16x32_bf16 v[16:19], a[224:227], v[196:199], v[16:19]\n"
"buffer_load_dwordx4 v236, s[12:15], s48 offen offset:2048 lds\n"
"v_mfma_f32_16x16x32_bf16 v[20:23], a[228:231], v[196:199], v[20:23]\n"
"v_mfma_f32_16x16x32_bf16 v[24:27], a[232:235], v[196:199], v[24:27]\n"
"ds_read_b64_tr_b16 a[246:247], v247 offset:12800\n"
"v_mfma_f32_16x16x32_bf16 v[28:31], a[236:239], v[196:199], v[28:31]\n"
"ds_read_b128 v[220:223], v242 offset:30720\n"
"v_mfma_f32_16x16x32_bf16 v[32:35], a[224:227], v[200:203], v[32:35]\n"
"buffer_load_dwordx4 v237, s[12:15], s48 offen offset:3072 lds\n"
"v_mfma_f32_16x16x32_bf16 v[36:39], a[228:231], v[200:203], v[36:39]\n"
"v_mfma_f32_16x16x32_bf16 v[40:43], a[232:235], v[200:203], v[40:43]\n"
"ds_read_b128 v[192:195], v243 offset:16384\n"
"v_mfma_f32_16x16x32_bf16 v[44:47], a[236:239], v[200:203], v[44:47]\n"
"s_waitcnt lgkmcnt(3)\n"
"ds_read_b64_tr_b16 a[248:249], v248 offset:12288\n"
"s_add_u32 m0, s37, 8192\n"
"v_mfma_f32_16x16x32_bf16 v[48:51], a[224:227], v[204:207], v[48:51]\n"
"s_add_u32 s48, s39, 65536\n"
"v_mfma_f32_16x16x32_bf16 v[52:55], a[228:231], v[204:207], v[52:55]\n"
"buffer_load_dwordx4 v234, s[12:15], s48 offen offset:0 lds\n"
"v_mfma_f32_16x16x32_bf16 v[56:59], a[232:235], v[204:207], v[56:59]\n"
"ds_read_b128 v[196:199], v243 offset:18432\n"
"v_mfma_f32_16x16x32_bf16 v[60:63], a[236:239], v[204:207], v[60:63]\n"
"ds_read_b64_tr_b16 a[250:251], v248 offset:12800\n"
"v_mfma_f32_16x16x32_bf16 v[64:67], a[224:227], v[208:211], v[64:67]\n"
"v_mfma_f32_16x16x32_bf16 v[68:71], a[228:231], v[208:211], v[68:71]\n"
"ds_read_b128 v[200:203], v243 offset:20480\n"
"v_mfma_f32_16x16x32_bf16 v[72:75], a[232:235], v[208:211], v[72:75]\n"
"buffer_load_dwordx4 v235, s[12:15], s48 offen offset:1024 lds\n"
"v_mfma_f32_16x16x32_bf16 v[76:79], a[236:239], v[208:211], v[76:79]\n"
"ds_read_b64_tr_b16 a[252:253], v249 offset:12288\n"
"v_mfma_f32_16x16x32_bf16 v[80:83], a[224:227], v[212:215], v[80:83]\n"
"v_mfma_f32_16x16x32_bf16 v[84:87], a[228:231], v[212:215], v[84:87]\n"
"ds_read_b128 v[204:207], v243 offset:22528\n"
"v_mfma_f32_16x16x32_bf16 v[88:91], a[232:235], v[212:215], v[88:91]\n"
"ds_read_b64_tr_b16 a[254:255], v249 offset:12800\n"
"v_mfma_f32_16x16x32_bf16 v[92:95], a[236:239], v[212:215], v[92:95]\n"
"buffer_load_dwordx4 v236, s[12:15], s48 offen offset:2048 lds\n"
"v_mfma_f32_16x16x32_bf16 v[96:99], a[224:227], v[216:219], v[96:99]\n"
"ds_read_b128 v[208:211], v243 offset:24576\n"
"v_mfma_f32_16x16x32_bf16 v[100:103], a[228:231], v[216:219], v[100:103]\n"
"s_lshr_b32 s88, s34, 1\n"
"v_mfma_f32_16x16x32_bf16 v[104:107], a[232:235], v[216:219], v[104:107]\n"
"s_sub_u32 s88, s88, s33\n"
"buffer_load_dwordx4 v237, s[12:15], s48 offen offset:3072 lds\n"
"v_mfma_f32_16x16x32_bf16 v[108:111], a[236:239], v[216:219], v[108:111]\n"
"s_waitcnt lgkmcnt(3)\n"
"s_and_b32 s88, s88, 3\n"
"v_mfma_f32_16x16x32_bf16 v[112:115], a[224:227], v[220:223], v[112:115]\n"
"s_lshl_b32 s88, s88, 1\n"
"ds_read_b128 v[212:215], v243 offset:26624\n"
"v_mfma_f32_16x16x32_bf16 v[116:119], a[228:231], v[220:223], v[116:119]\n"
"s_and_b32 s89, s34, 1\n"
"s_add_u32 m0, s37, 12288\n"
"s_add_u32 s48, s98, 65536\n"
"v_mfma_f32_16x16x32_bf16 v[120:123], a[232:235], v[220:223], v[120:123]\n"
"s_add_u32 s88, s88, s89\n"
"s_waitcnt vmcnt(17)\n"
"ds_read_b64_tr_b16 v[152:153], v254 offset:45056\n"
"v_mfma_f32_16x16x32_bf16 v[124:127], a[236:239], v[220:223], v[124:127]\n"
"s_mul_i32 s88, s88, 20480\n"
"ds_read_b128 v[216:219], v243 offset:28672\n"
"v_mfma_f32_16x16x32_bf16 v[0:3], a[240:243], v[192:195], v[0:3]\n"
"s_add_u32 s88, s88, s91\n"
"ds_read_b64_tr_b16 v[154:155], v254 offset:47616\n"
"v_mfma_f32_16x16x32_bf16 v[4:7], a[244:247], v[192:195], v[4:7]\n"
"s_waitcnt lgkmcnt(5)\n"
"buffer_load_dwordx4 v234, s[12:15], s48 offen offset:0 lds\n"
"v_mfma_f32_16x16x32_bf16 v[8:11], a[248:251], v[192:195], v[8:11]\n"
"s_mul_i32 s89, s35, 163840\n"
"ds_read_b64_tr_b16 v[156:157], v254 offset:45088\n"
"s_add_u32 s88, s88, s89\n"
"v_mfma_f32_16x16x32_bf16 v[12:15], a[252:255], v[192:195], v[12:15]\n"
"s_add_u32 s90, s88, 4956160\n"
"ds_read_b64_tr_b16 v[158:159], v254 offset:47648\n"
"v_mfma_f32_16x16x32_bf16 v[16:19], a[240:243], v[196:199], v[16:19]\n"
"ds_read_b64_tr_b16 v[160:161], v254 offset:45120\n"
"v_mfma_f32_16x16x32_bf16 v[20:23], a[244:247], v[196:199], v[20:23]\n"
"ds_read_b128 v[220:223], v243 offset:30720\n"
"v_mfma_f32_16x16x32_bf16 v[24:27], a[248:251], v[196:199], v[24:27]\n"
"ds_read_b64_tr_b16 v[162:163], v254 offset:47680\n"
"v_mfma_f32_16x16x32_bf16 v[28:31], a[252:255], v[196:199], v[28:31]\n"
"buffer_load_dwordx4 v235, s[12:15], s48 offen offset:1024 lds\n"
"v_mfma_f32_16x16x32_bf16 v[32:35], a[240:243], v[200:203], v[32:35]\n"
"ds_read_b64_tr_b16 v[164:165], v254 offset:45152\n"
"v_mfma_f32_16x16x32_bf16 v[36:39], a[244:247], v[200:203], v[36:39]\n"
"ds_read_b64_tr_b16 v[166:167], v254 offset:47712\n"
"v_mfma_f32_16x16x32_bf16 v[40:43], a[248:251], v[200:203], v[40:43]\n"
"s_waitcnt lgkmcnt(7)\n"
"buffer_store_dwordx4 v[152:155], v224, s[8:11], s90 offen sc1\n"
"v_mfma_f32_16x16x32_bf16 v[44:47], a[252:255], v[200:203], v[44:47]\n"
"s_add_u32 s90, s90, 1024\n"
"ds_read_b64_tr_b16 v[168:169], v254 offset:45184\n"
"v_add_u32 v244, 114688, v242\n"
"v_mfma_f32_16x16x32_bf16 v[48:51], a[240:243], v[204:207], v[48:51]\n"
"ds_read_b64_tr_b16 v[170:171], v254 offset:47744\n"
"v_add_u32 v245, 114688, v243\n"
"v_mfma_f32_16x16x32_bf16 v[52:55], a[244:247], v[204:207], v[52:55]\n"
"buffer_load_dwordx4 v236, s[12:15], s48 offen offset:2048 lds\n"
"v_mfma_f32_16x16x32_bf16 v[56:59], a[248:251], v[204:207], v[56:59]\n"
"s_waitcnt lgkmcnt(7)\n"
"buffer_store_dwordx4 v[156:159], v224, s[8:11], s90 offen sc1\n"
"v_mfma_f32_16x16x32_bf16 v[60:63], a[252:255], v[204:207], v[60:63]\n"
"buffer_load_dwordx4 v237, s[12:15], s48 offen offset:3072 lds\n"
"v_mfma_f32_16x16x32_bf16 v[64:67], a[240:243], v[208:211], v[64:67]\n"
"s_add_u32 s90, s90, 1024\n"
"s_waitcnt lgkmcnt(5)\n"
"s_barrier\n"
"v_mfma_f32_16x16x32_bf16 v[68:71], a[244:247], v[208:211], v[68:71]\n"
"s_lshl_b32 s88, s31, 1\n"
"v_add_u32 v242, 65536, v242\n"
"v_add_u32 v243, 65536, v243\n"
"v_mfma_f32_16x16x32_bf16 v[72:75], a[248:251], v[208:211], v[72:75]\n"
"s_add_u32 s88, s88, s30\n"
"ds_read_b128 v[192:195], v242 offset:16384\n"
"s_lshr_b32 s89, s88, 3\n"
"v_mfma_f32_16x16x32_bf16 v[76:79], a[252:255], v[208:211], v[76:79]\n"
"s_and_b32 s88, s88, 7\n"
"ds_read_b128 v[196:199], v242 offset:18432\n"
"v_mfma_f32_16x16x32_bf16 v[80:83], a[240:243], v[212:215], v[80:83]\n"
"s_waitcnt lgkmcnt(6)\n"
"buffer_store_dwordx4 v[160:163], v224, s[8:11], s90 offen sc1\n"
"v_mfma_f32_16x16x32_bf16 v[84:87], a[244:247], v[212:215], v[84:87]\n"
"s_mul_i32 s88, s88, 160\n"
"s_mul_i32 s89, s89, 40960\n"
"v_mfma_f32_16x16x32_bf16 v[88:91], a[248:251], v[212:215], v[88:91]\n"
"s_add_u32 s90, s90, 1024\n"
"ds_read_b128 v[200:203], v243 offset:16384\n"
"v_lshrrev_b32 v252, 4, v224\n"
"v_mfma_f32_16x16x32_bf16 v[92:95], a[252:255], v[212:215], v[92:95]\n"
"s_add_u32 s88, s88, s89\n"
"v_add_u32 v252, 0, v252\n"
"s_add_u32 s24, s82, s88\n"
"v_mfma_f32_16x16x32_bf16 v[96:99], a[240:243], v[216:219], v[96:99]\n"
"s_waitcnt lgkmcnt(5)\n"
"buffer_store_dwordx4 v[164:167], v224, s[8:11], s90 offen sc1\n"
"v_mfma_f32_16x16x32_bf16 v[100:103], a[244:247], v[216:219], v[100:103]\n"
"s_addc_u32 s25, s83, 0\n"
"v_mul_u32_u24 v252, 205, v252\n"
"v_lshrrev_b32 v252, 11, v252\n"
"v_mfma_f32_16x16x32_bf16 v[104:107], a[248:251], v[216:219], v[104:107]\n"
"s_and_b32 s25, s25, 0xffff\n"
"ds_read_b128 v[204:207], v243 offset:18432\n"
"v_mul_u32_u24 v252, 1120, v252\n"
"v_mfma_f32_16x16x32_bf16 v[108:111], a[252:255], v[216:219], v[108:111]\n"
"s_cmp_lt_u32 s31, 216\n"
"v_add_u32 v252, v252, v224\n"
"v_lshrrev_b32 v251, 4, v224\n"
"v_mfma_f32_16x16x32_bf16 v[112:115], a[240:243], v[220:223], v[112:115]\n"
"s_cselect_b32 s26, 39840, 0\n"
"s_waitcnt vmcnt(20)\n"
"ds_read_b64_tr_b16 v[172:173], v254 offset:55296\n"
"v_mfma_f32_16x16x32_bf16 v[116:119], a[244:247], v[220:223], v[116:119]\n"
"s_add_u32 m0, s91, 45056\n"
"ds_read_b128 v[208:211], v242 offset:20480\n"
"v_add_u32 v251, 64, v251\n"
"v_mfma_f32_16x16x32_bf16 v[120:123], a[248:251], v[220:223], v[120:123]\n"
"s_mov_b32 s48, 0\n"
"ds_read_b64_tr_b16 v[174:175], v254 offset:57856\n"
"v_mul_u32_u24 v251, 205, v251\n"
"v_mfma_f32_16x16x32_bf16 v[124:127], a[252:255], v[220:223], v[124:127]\n"
"s_waitcnt lgkmcnt(4)\n"
"buffer_load_dwordx4 v252, s[24:27], s48 offen lds\n"
"v_mfma_f32_16x16x32_bf16 v[128:131], a[0:3], v[192:195], 0\n"
"s_add_u32 s90, s90, 1024\n"
"ds_read_b64_tr_b16 v[176:177], v254 offset:55328\n"
"v_lshrrev_b32 v251, 11, v251\n"
"v_mfma_f32_16x16x32_bf16 v[132:135], a[4:7], v[192:195], 0\n"
"s_add_u32 m0, s91, 46080\n"
"buffer_store_dwordx4 v[168:171], v224, s[8:11], s90 offen sc1\n"
"v_mfma_f32_16x16x32_bf16 v[136:139], a[8:11], v[192:195], 0\n"
"s_mov_b32 s48, 1024\n"
"ds_read_b64_tr_b16 v[178:179], v254 offset:57888\n"
"v_mul_u32_u24 v251, 1120, v251\n"
"v_mfma_f32_16x16x32_bf16 v[140:143], a[12:15], v[192:195], 0\n"
"s_add_u32 s90, s90, 6144\n"
"ds_read_b64_tr_b16 v[180:181], v254 offset:55360\n"
"v_add_u32 v251, v251, v224\n"
"v_mfma_f32_16x16x32_bf16 v[144:147], a[0:3], v[196:199], 0\n"
"ds_read_b128 v[212:215], v242 offset:22528\n"
"v_lshrrev_b32 v252, 4, v224\n"
"v_mfma_f32_16x16x32_bf16 v[148:151], a[4:7], v[196:199], 0\n"
"ds_read_b64_tr_b16 v[182:183], v254 offset:57920\n"
"v_add_u32 v252, 128, v252\n"
"v_mfma_f32_16x16x32_bf16 v[152:155], a[8:11], v[196:199], 0\n"
"buffer_load_dwordx4 v251, s[24:27], s48 offen lds\n"
"v_mfma_f32_16x16x32_bf16 v[156:159], a[12:15], v[196:199], 0\n"
"s_add_u32 m0, s91, 47104\n"
"ds_read_b64_tr_b16 v[184:185], v254 offset:55392\n"
"v_mul_u32_u24 v252, 205, v252\n"
"v_mfma_f32_16x16x32_bf16 v[128:131], a[16:19], v[200:203], v[128:131]\n"
"s_mov_b32 s48, 2048\n"
"ds_read_b64_tr_b16 v[186:187], v254 offset:57952\n"
"v_lshrrev_b32 v252, 11, v252\n"
"v_mfma_f32_16x16x32_bf16 v[132:135], a[20:23], v[200:203], v[132:135]\n"
"s_waitcnt lgkmcnt(7)\n"
"buffer_store_dwordx4 v[172:175], v224, s[8:11], s90 offen sc1\n"
"v_mfma_f32_16x16x32_bf16 v[136:139], a[24:27], v[200:203], v[136:139]\n"
"s_add_u32 s90, s90, 1024\n"
"ds_read_b64_tr_b16 v[188:189], v254 offset:55424\n"
"v_mul_u32_u24 v252, 1120, v252\n"
"v_mfma_f32_16x16x32_bf16 v[140:143], a[28:31], v[200:203], v[140:143]\n"
"ds_read_b128 v[216:219], v243 offset:20480\n"
"v_add_u32 v252, v252, v224\n"
"v_mfma_f32_16x16x32_bf16 v[144:147], a[16:19], v[204:207], v[144:147]\n"
"ds_read_b64_tr_b16 v[190:191], v254 offset:57984\n"
"v_lshrrev_b32 v251, 4, v224\n"
"v_mfma_f32_16x16x32_bf16 v[148:151], a[20:23], v[204:207], v[148:151]\n"
"buffer_load_dwordx4 v252, s[24:27], s48 offen lds\n"
"v_mfma_f32_16x16x32_bf16 v[152:155], a[24:27], v[204:207], v[152:155]\n"
"s_add_u32 m0, s91, 48128\n"
"ds_read_b128 v[220:223], v243 offset:22528\n"
"v_add_u32 v251, 192, v251\n"
"v_mfma_f32_16x16x32_bf16 v[156:159], a[28:31], v[204:207], v[156:159]\n"
"s_waitcnt lgkmcnt(9)\n"
"buffer_store_dwordx4 v[176:179], v224, s[8:11], s90 offen sc1\n"
"v_mfma_f32_16x16x32_bf16 v[128:131], a[32:35], v[208:211], v[128:131]\n"
"s_mov_b32 s48, 3072\n"
"ds_read_b128 v[192:195], v242 offset:40960\n"
"v_mul_u32_u24 v251, 205, v251\n"
"v_mfma_f32_16x16x32_bf16 v[132:135], a[36:39], v[208:211], v[132:135]\n"
"s_add_u32 s90, s90, 1024\n"
"ds_read_b128 v[196:199], v242 offset:43008\n"
"v_lshrrev_b32 v251, 11, v251\n"
"v_mfma_f32_16x16x32_bf16 v[136:139], a[40:43], v[208:211], v[136:139]\n"
"s_waitcnt lgkmcnt(8)\n"
"buffer_store_dwordx4 v[180:183], v224, s[8:11], s90 offen sc1\n"
"v_mfma_f32_16x16x32_bf16 v[140:143], a[44:47], v[208:211], v[140:143]\n"
"s_lshr_b32 s92, s31, 3\n"
"v_mul_u32_u24 v251, 1120, v251\n"
"v_add_u32 v251, v251, v224\n"
"v_mfma_f32_16x16x32_bf16 v[144:147], a[32:35], v[212:215], v[144:147]\n"
"s_add_u32 s90, s90, 1024\n"
"buffer_load_dwordx4 v251, s[24:27], s48 offen lds\n"
"v_mfma_f32_16x16x32_bf16 v[148:151], a[36:39], v[212:215], v[148:151]\n"
"s_add_u32 m0, s91, 49152\n"
"ds_read_b128 v[200:203], v243 offset:40960\n"
"v_lshrrev_b32 v252, 4, v224\n"
"v_mfma_f32_16x16x32_bf16 v[152:155], a[40:43], v[212:215], v[152:155]\n"
"s_waitcnt lgkmcnt(7)\n"
"buffer_store_dwordx4 v[184:187], v224, s[8:11], s90 offen sc1\n"
"v_mfma_f32_16x16x32_bf16 v[156:159], a[44:47], v[212:215], v[156:159]\n"
"s_waitcnt lgkmcnt(2)\n"
"v_add_u32 v252, 256, v252\n"
"v_mul_u32_u24 v252, 205, v252\n"
"v_mfma_f32_16x16x32_bf16 v[128:131], a[48:51], v[216:219], v[128:131]\n"
"s_mov_b32 s48, 4096\n"
"ds_read_b128 v[204:207], v243 offset:43008\n"
"v_lshrrev_b32 v252, 11, v252\n"
"v_mfma_f32_16x16x32_bf16 v[132:135], a[52:55], v[216:219], v[132:135]\n"
"s_mul_i32 s93, s32, 14\n"
"v_mul_u32_u24 v252, 1120, v252\n"
"v_add_u32 v252, v252, v224\n"
"v_mfma_f32_16x16x32_bf16 v[136:139], a[56:59], v[216:219], v[136:139]\n"
"s_add_u32 s93, s93, s92\n"
"buffer_load_dwordx4 v252, s[24:27], s48 offen lds\n"
"v_mfma_f32_16x16x32_bf16 v[140:143], a[60:63], v[216:219], v[140:143]\n"
"s_mov_b32 s27, 0x20000\n"
"ds_read_b128 v[208:211], v242 offset:45056\n"
"v_mul_f32 v0, s55, v0\n"
"v_mfma_f32_16x16x32_bf16 v[144:147], a[48:51], v[220:223], v[144:147]\n"
"s_sub_u32 s93, s93, 18\n"
"v_mul_f32 v1, s55, v1\n"
"v_mul_f32 v2, s55, v2\n"
"v_mfma_f32_16x16x32_bf16 v[148:151], a[52:55], v[220:223], v[148:151]\n"
"s_cmp_lt_u32 s92, 18\n"
"v_mul_f32 v3, s55, v3\n"
"v_mul_f32 v4, s55, v4\n"
"v_mfma_f32_16x16x32_bf16 v[152:155], a[56:59], v[220:223], v[152:155]\n"
"s_cselect_b32 s93, 200, s93\n"
"v_mul_f32 v5, s55, v5\n"
"v_mul_f32 v6, s55, v6\n"
"v_mfma_f32_16x16x32_bf16 v[156:159], a[60:63], v[220:223], v[156:159]\n"
"s_cmp_lt_u32 s30, 2\n"
"ds_read_b128 v[212:215], v242 offset:47104\n"
"v_mul_f32 v7, s55, v7\n"
"v_mfma_f32_16x16x32_bf16 v[128:131], a[64:67], v[192:195], v[128:131]\n"
"s_cselect_b64 s[24:25], s[78:79], s[80:81]\n"
"v_cvt_pk_bf16_f32 v0, v0, v1\n"
"v_cvt_pk_bf16_f32 v1, v2, v3\n"
"v_mfma_f32_16x16x32_bf16 v[132:135], a[68:71], v[192:195], v[132:135]\n"
"s_and_b32 s25, s25, 0xffff\n"
"v_cvt_pk_bf16_f32 v2, v4, v5\n"
"v_cvt_pk_bf16_f32 v3, v6, v7\n"
"v_mfma_f32_16x16x32_bf16 v[136:139], a[72:75], v[192:195], v[136:139]\n"
"s_add_u32 s90, s90, 1024\n"
"v_mul_f32 v8, s55, v8\n"
"v_mul_f32 v9, s55, v9\n"
"v_mfma_f32_16x16x32_bf16 v[140:143], a[76:79], v[192:195], v[140:143]\n"
"s_waitcnt lgkmcnt(1)\n"
"buffer_store_dwordx4 v[188:191], v224, s[8:11], s90 offen sc1\n"
"v_mfma_f32_16x16x32_bf16 v[144:147], a[64:67], v[196:199], v[144:147]\n"
"s_cmp_lt_u32 s93, 108\n"
"ds_read_b128 v[216:219], v243 offset:45056\n"
"v_mul_f32 v10, s55, v10\n"
"v_mfma_f32_16x16x32_bf16 v[148:151], a[68:71], v[196:199], v[148:151]\n"
"s_cselect_b32 s26, 2211840, 0\n"
"v_mul_f32 v11, s55, v11\n"
"v_mul_f32 v12, s55, v12\n"
"v_mfma_f32_16x16x32_bf16 v[152:155], a[72:75], v[196:199], v[152:155]\n"
"s_cmp_lt_u32 s93, 108\n"
"v_mul_f32 v13, s55, v13\n"
"v_mul_f32 v14, s55, v14\n"
"v_mfma_f32_16x16x32_bf16 v[156:159], a[76:79], v[196:199], v[156:159]\n"
"s_cselect_b32 s44, 0, 2130706432\n"
"ds_read_b128 v[220:223], v243 offset:47104\n"
"v_lshrrev_b32 v251, 5, v224\n"
"v_mfma_f32_16x16x32_bf16 v[128:131], a[80:83], v[200:203], v[128:131]\n"
"s_and_b32 s92, s30, 1\n"
"v_mul_u32_u24 v251, 3456, v251\n"
"v_mul_f32 v15, s55, v15\n"
"v_mfma_f32_16x16x32_bf16 v[132:135], a[84:87], v[200:203], v[132:135]\n"
"s_mul_i32 s61, s92, 1105920\n"
"v_and_b32 v252, 16, v224\n"
"v_cvt_pk_bf16_f32 v8, v8, v9\n"
"v_mfma_f32_16x16x32_bf16 v[136:139], a[88:91], v[200:203], v[136:139]\n"
"s_lshl_b32 s92, s93, 5\n"
"ds_read_b128 v[192:195], v244 offset:16384\n"
"v_add_u32 v251, v251, v252\n"
"v_mfma_f32_16x16x32_bf16 v[140:143], a[92:95], v[200:203], v[140:143]\n"
"s_mov_b32 s49, 0\n"
"v_cvt_pk_bf16_f32 v9, v10, v11\n"
"v_cvt_pk_bf16_f32 v10, v12, v13\n"
"v_mfma_f32_16x16x32_bf16 v[144:147], a[80:83], v[204:207], v[144:147]\n"
"s_add_u32 s61, s61, s92\n"
"buffer_store_dwordx4 v[0:3], v250, s[8:11], s49 offen offset:0 sc1\n"
"v_mfma_f32_16x16x32_bf16 v[148:151], a[84:87], v[204:207], v[148:151]\n"
"s_lshr_b32 s92, s93, 1\n"
"v_cvt_pk_bf16_f32 v11, v14, v15\n"
"v_mul_f32 v16, s55, v16\n"
"v_mfma_f32_16x16x32_bf16 v[152:155], a[88:91], v[204:207], v[152:155]\n"
"s_lshl_b32 s92, s92, 9\n"
"ds_read_b128 v[196:199], v244 offset:18432\n"
"v_mul_f32 v17, s55, v17\n"
"v_mfma_f32_16x16x32_bf16 v[156:159], a[92:95], v[204:207], v[156:159]\n"
"s_and_b32 s60, s93, 1\n"
"v_mul_f32 v18, s55, v18\n"
"v_mul_f32 v19, s55, v19\n"
"v_mfma_f32_16x16x32_bf16 v[128:131], a[96:99], v[208:211], v[128:131]\n"
"s_lshl_b32 s60, s60, 3\n"
"v_mul_f32 v20, s55, v20\n"
"v_mul_f32 v21, s55, v21\n"
"v_mfma_f32_16x16x32_bf16 v[132:135], a[100:103], v[208:211], v[132:135]\n"
"s_add_u32 s60, s60, s92\n"
"buffer_store_dwordx4 v[8:11], v250, s[8:11], s49 offen offset:1024 sc1\n"
"v_mfma_f32_16x16x32_bf16 v[136:139], a[104:107], v[208:211], v[136:139]\n"
"s_add_u32 s60, s60, 2695168\n"
"ds_read_b128 v[200:203], v245 offset:16384\n"
"v_mul_f32 v22, s55, v22\n"
"v_mfma_f32_16x16x32_bf16 v[140:143], a[108:111], v[208:211], v[140:143]\n"
"s_waitcnt lgkmcnt(2)\n"
"v_mul_f32 v23, s55, v23\n"
"v_cvt_pk_bf16_f32 v16, v16, v17\n"
"v_mfma_f32_16x16x32_bf16 v[144:147], a[96:99], v[212:215], v[144:147]\n"
"s_lshl_b32 s92, s30, 8\n"
"v_cvt_pk_bf16_f32 v17, v18, v19\n"
"v_cvt_pk_bf16_f32 v18, v20, v21\n"
"v_mfma_f32_16x16x32_bf16 v[148:151], a[100:103], v[212:215], v[148:151]\n"
"s_add_u32 s93, s93, s92\n"
"v_cvt_pk_bf16_f32 v19, v22, v23\n"
"v_mul_f32 v24, s55, v24\n"
"v_mfma_f32_16x16x32_bf16 v[152:155], a[104:107], v[212:215], v[152:155]\n"
"s_cmp_lt_u32 s93, 108\n"
"ds_read_b128 v[204:207], v245 offset:18432\n"
"v_mul_f32 v25, s55, v25\n"
"v_mfma_f32_16x16x32_bf16 v[156:159], a[108:111], v[212:215], v[156:159]\n"
"s_cselect_b32 s60, s60, 2130706432\n"
"v_mul_f32 v26, s55, v26\n"
"v_mul_f32 v27, s55, v27\n"
"v_mfma_f32_16x16x32_bf16 v[128:131], a[112:115], v[216:219], v[128:131]\n"
"s_mul_i32 s92, s30, 10240\n"
"v_mul_f32 v28, s55, v28\n"
"v_mul_f32 v29, s55, v29\n"
"v_mfma_f32_16x16x32_bf16 v[132:135], a[116:119], v[216:219], v[132:135]\n"
"s_add_u32 m0, s92, 0\n"
"ds_read_b128 v[208:211], v244 offset:20480\n"
"v_mul_f32 v30, s55, v30\n"
"v_mfma_f32_16x16x32_bf16 v[136:139], a[120:123], v[216:219], v[136:139]\n"
"s_add_u32 s48, s61, 0\n"
"v_mul_f32 v31, s55, v31\n"
"v_cvt_pk_bf16_f32 v24, v24, v25\n"
"v_mfma_f32_16x16x32_bf16 v[140:143], a[124:127], v[216:219], v[140:143]\n"
"buffer_load_dwordx4 v251, s[24:27], s48 offen lds\n"
"v_mfma_f32_16x16x32_bf16 v[144:147], a[112:115], v[220:223], v[144:147]\n"
"s_add_u32 m0, s92, 1024\n"
"v_cvt_pk_bf16_f32 v25, v26, v27\n"
"v_cvt_pk_bf16_f32 v26, v28, v29\n"
"v_mfma_f32_16x16x32_bf16 v[148:151], a[116:119], v[220:223], v[148:151]\n"
"s_add_u32 s48, s61, 110592\n"
"ds_read_b128 v[212:215], v244 offset:22528\n"
"v_cvt_pk_bf16_f32 v27, v30, v31\n"
"v_mfma_f32_16x16x32_bf16 v[152:155], a[120:123], v[220:223], v[152:155]\n"
"buffer_load_dwordx4 v251, s[24:27], s48 offen lds\n"
"v_mfma_f32_16x16x32_bf16 v[156:159], a[124:127], v[220:223], v[156:159]\n"
"s_mov_b32 s49, 4096\n"
"v_mul_f32 v32, s55, v32\n"
"v_mul_f32 v33, s55, v33\n"
"v_mfma_f32_16x16x32_bf16 v[128:131], a[128:131], v[192:195], v[128:131]\n"
"v_mul_f32 v34, s55, v34\n"
"v_mul_f32 v35, s55, v35\n"
"v_mfma_f32_16x16x32_bf16 v[132:135], a[132:135], v[192:195], v[132:135]\n"
"ds_read_b128 v[216:219], v245 offset:20480\n"
"v_mul_f32 v36, s55, v36\n"
"v_mfma_f32_16x16x32_bf16 v[136:139], a[136:139], v[192:195], v[136:139]\n"
"buffer_store_dwordx4 v[16:19], v250, s[8:11], s49 offen offset:0 sc1\n"
"v_mfma_f32_16x16x32_bf16 v[140:143], a[140:143], v[192:195], v[140:143]\n"
"s_waitcnt lgkmcnt(2)\n"
"v_mul_f32 v37, s55, v37\n"
"v_mul_f32 v38, s55, v38\n"
"v_mfma_f32_16x16x32_bf16 v[144:147], a[128:131], v[196:199], v[144:147]\n"
"v_mul_f32 v39, s55, v39\n"
"v_cvt_pk_bf16_f32 v32, v32, v33\n"
"v_mfma_f32_16x16x32_bf16 v[148:151], a[132:135], v[196:199], v[148:151]\n"
"ds_read_b128 v[220:223], v245 offset:22528\n"
"v_cvt_pk_bf16_f32 v33, v34, v35\n"
"v_mfma_f32_16x16x32_bf16 v[152:155], a[136:139], v[196:199], v[152:155]\n"
"v_cvt_pk_bf16_f32 v34, v36, v37\n"
"v_cvt_pk_bf16_f32 v35, v38, v39\n"
"v_mfma_f32_16x16x32_bf16 v[156:159], a[140:143], v[196:199], v[156:159]\n"
"s_add_u32 m0, s92, 2048\n"
"v_mul_f32 v40, s55, v40\n"
"v_mul_f32 v41, s55, v41\n"
"v_mfma_f32_16x16x32_bf16 v[128:131], a[144:147], v[200:203], v[128:131]\n"
"s_add_u32 s48, s61, 221184\n"
"ds_read_b128 v[192:195], v244 offset:40960\n"
"v_mul_f32 v42, s55, v42\n"
"v_mfma_f32_16x16x32_bf16 v[132:135], a[148:151], v[200:203], v[132:135]\n"
"buffer_load_dwordx4 v251, s[24:27], s48 offen lds\n"
"v_mfma_f32_16x16x32_bf16 v[136:139], a[152:155], v[200:203], v[136:139]\n"
"v_mul_f32 v43, s55, v43\n"
"v_mul_f32 v44, s55, v44\n"
"v_mfma_f32_16x16x32_bf16 v[140:143], a[156:159], v[200:203], v[140:143]\n"
"v_mul_f32 v45, s55, v45\n"
"v_mul_f32 v46, s55, v46\n"
"v_mfma_f32_16x16x32_bf16 v[144:147], a[144:147], v[204:207], v[144:147]\n"
"ds_read_b128 v[196:199], v244 offset:43008\n"
"v_mul_f32 v47, s55, v47\n"
"v_mfma_f32_16x16x32_bf16 v[148:151], a[148:151], v[204:207], v[148:151]\n"
"v_cvt_pk_bf16_f32 v40, v40, v41\n"
"v_cvt_pk_bf16_f32 v41, v42, v43\n"
"v_mfma_f32_16x16x32_bf16 v[152:155], a[152:155], v[204:207], v[152:155]\n"
"v_cvt_pk_bf16_f32 v42, v44, v45\n"
"v_cvt_pk_bf16_f32 v43, v46, v47\n"
"v_mfma_f32_16x16x32_bf16 v[156:159], a[156:159], v[204:207], v[156:159]\n"
"v_mul_f32 v48, s55, v48\n"
"v_mul_f32 v49, s55, v49\n"
"v_mfma_f32_16x16x32_bf16 v[128:131], a[160:163], v[208:211], v[128:131]\n"
"ds_read_b128 v[200:203], v245 offset:40960\n"
"v_mul_f32 v50, s55, v50\n"
"v_mfma_f32_16x16x32_bf16 v[132:135], a[164:167], v[208:211], v[132:135]\n"
"buffer_store_dwordx4 v[24:27], v250, s[8:11], s49 offen offset:1024 sc1\n"
"v_mfma_f32_16x16x32_bf16 v[136:139], a[168:171], v[208:211], v[136:139]\n"
"v_mul_f32 v51, s55, v51\n"
"v_mul_f32 v52, s55, v52\n"
"v_mfma_f32_16x16x32_bf16 v[140:143], a[172:175], v[208:211], v[140:143]\n"
"s_waitcnt lgkmcnt(1)\n"
"v_mul_f32 v53, s55, v53\n"
"v_mul_f32 v54, s55, v54\n"
"v_mfma_f32_16x16x32_bf16 v[144:147], a[160:163], v[212:215], v[144:147]\n"
"ds_read_b128 v[204:207], v245 offset:43008\n"
"v_mul_f32 v55, s55, v55\n"
"v_mfma_f32_16x16x32_bf16 v[148:151], a[164:167], v[212:215], v[148:151]\n"
"s_add_u32 m0, s92, 3072\n"
"v_cvt_pk_bf16_f32 v48, v48, v49\n"
"v_cvt_pk_bf16_f32 v49, v50, v51\n"
"v_mfma_f32_16x16x32_bf16 v[152:155], a[168:171], v[212:215], v[152:155]\n"
"s_add_u32 s48, s61, 331776\n"
"v_cvt_pk_bf16_f32 v50, v52, v53\n"
"v_cvt_pk_bf16_f32 v51, v54, v55\n"
"v_mfma_f32_16x16x32_bf16 v[156:159], a[172:175], v[212:215], v[156:159]\n"
"ds_read_b128 v[208:211], v244 offset:45056\n"
"v_mul_f32 v56, s55, v56\n"
"v_mfma_f32_16x16x32_bf16 v[128:131], a[176:179], v[216:219], v[128:131]\n"
"buffer_load_dwordx4 v251, s[24:27], s48 offen lds\n"
"v_mfma_f32_16x16x32_bf16 v[132:135], a[180:183], v[216:219], v[132:135]\n"
"v_mul_f32 v57, s55, v57\n"
"v_mul_f32 v58, s55, v58\n"
"v_mfma_f32_16x16x32_bf16 v[136:139], a[184:187], v[216:219], v[136:139]\n"
"v_mul_f32 v59, s55, v59\n"
"v_mul_f32 v60, s55, v60\n"
"v_mfma_f32_16x16x32_bf16 v[140:143], a[188:191], v[216:219], v[140:143]\n"
"ds_read_b128 v[212:215], v244 offset:47104\n"
"v_mul_f32 v61, s55, v61\n"
"v_mfma_f32_16x16x32_bf16 v[144:147], a[176:179], v[220:223], v[144:147]\n"
"v_mul_f32 v62, s55, v62\n"
"v_mul_f32 v63, s55, v63\n"
"v_mfma_f32_16x16x32_bf16 v[148:151], a[180:183], v[220:223], v[148:151]\n"
"v_cvt_pk_bf16_f32 v56, v56, v57\n"
"v_cvt_pk_bf16_f32 v57, v58, v59\n"
"v_mfma_f32_16x16x32_bf16 v[152:155], a[184:187], v[220:223], v[152:155]\n"
"v_cvt_pk_bf16_f32 v58, v60, v61\n"
"v_cvt_pk_bf16_f32 v59, v62, v63\n"
"v_mfma_f32_16x16x32_bf16 v[156:159], a[188:191], v[220:223], v[156:159]\n"
"s_mov_b32 s49, 8192\n"
"ds_read_b128 v[216:219], v245 offset:45056\n"
"v_mul_f32 v64, s55, v64\n"
"v_mfma_f32_16x16x32_bf16 v[128:131], a[192:195], v[192:195], v[128:131]\n"
"v_mul_f32 v65, s55, v65\n"
"v_mul_f32 v66, s55, v66\n"
"v_mfma_f32_16x16x32_bf16 v[132:135], a[196:199], v[192:195], v[132:135]\n"
"buffer_store_dwordx4 v[32:35], v250, s[8:11], s49 offen offset:0 sc1\n"
"v_mfma_f32_16x16x32_bf16 v[136:139], a[200:203], v[192:195], v[136:139]\n"
"v_mul_f32 v67, s55, v67\n"
"v_mul_f32 v68, s55, v68\n"
"v_mfma_f32_16x16x32_bf16 v[140:143], a[204:207], v[192:195], v[140:143]\n"
"s_add_u32 m0, s92, 4096\n"
"ds_read_b128 v[220:223], v245 offset:47104\n"
"v_mul_f32 v69, s55, v69\n"
"v_mfma_f32_16x16x32_bf16 v[144:147], a[192:195], v[196:199], v[144:147]\n"
"s_add_u32 s48, s61, 442368\n"
"v_mul_f32 v70, s55, v70\n"
"v_mul_f32 v71, s55, v71\n"
"v_mfma_f32_16x16x32_bf16 v[148:151], a[196:199], v[196:199], v[148:151]\n"
"buffer_load_dwordx4 v251, s[24:27], s48 offen lds\n"
"v_mfma_f32_16x16x32_bf16 v[152:155], a[200:203], v[196:199], v[152:155]\n"
"v_cvt_pk_bf16_f32 v64, v64, v65\n"
"v_cvt_pk_bf16_f32 v65, v66, v67\n"
"v_mfma_f32_16x16x32_bf16 v[156:159], a[204:207], v[196:199], v[156:159]\n"
"s_waitcnt lgkmcnt(1)\n"
"v_cvt_pk_bf16_f32 v66, v68, v69\n"
"v_cvt_pk_bf16_f32 v67, v70, v71\n"
"v_mfma_f32_16x16x32_bf16 v[128:131], a[208:211], v[200:203], v[128:131]\n"
"v_mul_f32 v72, s55, v72\n"
"v_mul_f32 v73, s55, v73\n"
"v_mfma_f32_16x16x32_bf16 v[132:135], a[212:215], v[200:203], v[132:135]\n"
"v_mul_f32 v74, s55, v74\n"
"v_mul_f32 v75, s55, v75\n"
"v_mfma_f32_16x16x32_bf16 v[136:139], a[216:219], v[200:203], v[136:139]\n"
"v_mul_f32 v76, s55, v76\n"
"v_mul_f32 v77, s55, v77\n"
"v_mfma_f32_16x16x32_bf16 v[140:143], a[220:223], v[200:203], v[140:143]\n"
"v_mul_f32 v78, s55, v78\n"
"v_mul_f32 v79, s55, v79\n"
"v_mfma_f32_16x16x32_bf16 v[144:147], a[208:211], v[204:207], v[144:147]\n"
"v_cvt_pk_bf16_f32 v72, v72, v73\n"
"v_cvt_pk_bf16_f32 v73, v74, v75\n"
"v_mfma_f32_16x16x32_bf16 v[148:151], a[212:215], v[204:207], v[148:151]\n"
"s_waitcnt vmcnt(33) lgkmcnt(0)\n"
"s_barrier\n"
"v_mfma_f32_16x16x32_bf16 v[152:155], a[216:219], v[204:207], v[152:155]\n"
"s_add_u32 m0, s37, 16384\n"
"ds_read_b128 v[192:195], v242 offset:0\n"
"v_cvt_pk_bf16_f32 v74, v76, v77\n"
"v_mfma_f32_16x16x32_bf16 v[156:159], a[220:223], v[204:207], v[156:159]\n"
"s_add_u32 s48, s39, 98304\n"
"v_cvt_pk_bf16_f32 v75, v78, v79\n"
"v_mfma_f32_16x16x32_bf16 v[128:131], a[224:227], v[208:211], v[128:131]\n"
"buffer_load_dwordx4 v234, s[12:15], s48 offen offset:0 lds\n"
"v_mfma_f32_16x16x32_bf16 v[132:135], a[228:231], v[208:211], v[132:135]\n"
"buffer_store_dwordx4 v[40:43], v250, s[8:11], s49 offen offset:1024 sc1\n"
"v_mfma_f32_16x16x32_bf16 v[136:139], a[232:235], v[208:211], v[136:139]\n"
"ds_read_b128 v[196:199], v242 offset:2048\n"
"v_mul_f32 v80, s55, v80\n"
"v_mfma_f32_16x16x32_bf16 v[140:143], a[236:239], v[208:211], v[140:143]\n"
"v_mul_f32 v81, s55, v81\n"
"v_mul_f32 v82, s55, v82\n"
"v_mfma_f32_16x16x32_bf16 v[144:147], a[224:227], v[212:215], v[144:147]\n"
"v_mul_f32 v83, s55, v83\n"
"v_mul_f32 v84, s55, v84\n"
"v_mfma_f32_16x16x32_bf16 v[148:151], a[228:231], v[212:215], v[148:151]\n"
"buffer_load_dwordx4 v235, s[12:15], s48 offen offset:1024 lds\n"
"v_mfma_f32_16x16x32_bf16 v[152:155], a[232:235], v[212:215], v[152:155]\n"
"ds_read_b128 v[200:203], v243 offset:0\n"
"v_mul_f32 v85, s55, v85\n"
"v_mfma_f32_16x16x32_bf16 v[156:159], a[236:239], v[212:215], v[156:159]\n"
"v_mul_f32 v86, s55, v86\n"
"v_mul_f32 v87, s55, v87\n"
"v_mfma_f32_16x16x32_bf16 v[128:131], a[240:243], v[216:219], v[128:131]\n"
"v_cvt_pk_bf16_f32 v80, v80, v81\n"
"v_cvt_pk_bf16_f32 v81, v82, v83\n"
"v_mfma_f32_16x16x32_bf16 v[132:135], a[244:247], v[216:219], v[132:135]\n"
"v_cvt_pk_bf16_f32 v82, v84, v85\n"
"v_cvt_pk_bf16_f32 v83, v86, v87\n"
"v_mfma_f32_16x16x32_bf16 v[136:139], a[248:251], v[216:219], v[136:139]\n"
"ds_read_b128 v[204:207], v243 offset:2048\n"
"v_mfma_f32_16x16x32_bf16 v[140:143], a[252:255], v[216:219], v[140:143]\n"
"buffer_load_dwordx4 v236, s[12:15], s48 offen offset:2048 lds\n"
"v_mfma_f32_16x16x32_bf16 v[144:147], a[240:243], v[220:223], v[144:147]\n"
"v_mul_f32 v88, s55, v88\n"
"v_mul_f32 v89, s55, v89\n"
"v_mfma_f32_16x16x32_bf16 v[148:151], a[244:247], v[220:223], v[148:151]\n"
"ds_read_b128 v[208:211], v242 offset:4096\n"
"v_mul_f32 v90, s55, v90\n"
"v_mfma_f32_16x16x32_bf16 v[152:155], a[248:251], v[220:223], v[152:155]\n"
"v_mul_f32 v91, s55, v91\n"
"v_mul_f32 v92, s55, v92\n"
"v_mfma_f32_16x16x32_bf16 v[156:159], a[252:255], v[220:223], v[156:159]\n"
"s_waitcnt lgkmcnt(2)\n"
"buffer_load_dwordx4 v237, s[12:15], s48 offen offset:3072 lds\n"
"v_mfma_f32_16x16x32_bf16 v[4:7], a[0:3], v[192:195], 0\n"
"s_mov_b32 s49, 12288\n"
"v_mul_f32 v93, s55, v93\n"
"v_mul_f32 v94, s55, v94\n"
"v_mfma_f32_16x16x32_bf16 v[12:15], a[4:7], v[192:195], 0\n"
"ds_read_b128 v[212:215], v242 offset:6144\n"
"v_mul_f32 v95, s55, v95\n"
"v_mfma_f32_16x16x32_bf16 v[20:23], a[8:11], v[192:195], 0\n"
"s_add_u32 m0, s37, 20480\n"
"buffer_store_dwordx4 v[48:51], v250, s[8:11], s49 offen offset:0 sc1\n"
"v_mfma_f32_16x16x32_bf16 v[28:31], a[12:15], v[192:195], 0\n"
"s_add_u32 s48, s98, 98304\n"
"v_cvt_pk_bf16_f32 v88, v88, v89\n"
"v_cvt_pk_bf16_f32 v89, v90, v91\n"
"v_mfma_f32_16x16x32_bf16 v[36:39], a[0:3], v[196:199], 0\n"
"buffer_load_dwordx4 v234, s[12:15], s48 offen offset:0 lds\n"
"v_mfma_f32_16x16x32_bf16 v[44:47], a[4:7], v[196:199], 0\n"
"ds_read_b128 v[216:219], v243 offset:4096\n"
"v_cvt_pk_bf16_f32 v90, v92, v93\n"
"v_mfma_f32_16x16x32_bf16 v[52:55], a[8:11], v[196:199], 0\n"
"v_cvt_pk_bf16_f32 v91, v94, v95\n"
"v_mul_f32 v96, s55, v96\n"
"v_mfma_f32_16x16x32_bf16 v[60:63], a[12:15], v[196:199], 0\n"
"v_mul_f32 v97, s55, v97\n"
"v_mul_f32 v98, s55, v98\n"
"v_mfma_f32_16x16x32_bf16 v[4:7], a[16:19], v[200:203], v[4:7]\n"
"v_mul_f32 v99, s55, v99\n"
"v_mul_f32 v100, s55, v100\n"
"v_mfma_f32_16x16x32_bf16 v[12:15], a[20:23], v[200:203], v[12:15]\n"
"ds_read_b128 v[220:223], v243 offset:6144\n"
"v_mul_f32 v101, s55, v101\n"
"v_mfma_f32_16x16x32_bf16 v[20:23], a[24:27], v[200:203], v[20:23]\n"
"buffer_load_dwordx4 v235, s[12:15], s48 offen offset:1024 lds\n"
"v_mfma_f32_16x16x32_bf16 v[28:31], a[28:31], v[200:203], v[28:31]\n"
"s_waitcnt lgkmcnt(2)\n"
"v_mul_f32 v102, s55, v102\n"
"v_mul_f32 v103, s55, v103\n"
"v_mfma_f32_16x16x32_bf16 v[36:39], a[16:19], v[204:207], v[36:39]\n"
"ds_read_b128 v[192:195], v242 offset:24576\n"
"v_cvt_pk_bf16_f32 v96, v96, v97\n"
"v_mfma_f32_16x16x32_bf16 v[44:47], a[20:23], v[204:207], v[44:47]\n"
"v_cvt_pk_bf16_f32 v97, v98, v99\n"
"v_cvt_pk_bf16_f32 v98, v100, v101\n"
"v_mfma_f32_16x16x32_bf16 v[52:55], a[24:27], v[204:207], v[52:55]\n"
"buffer_load_dwordx4 v236, s[12:15], s48 offen offset:2048 lds\n"
"v_mfma_f32_16x16x32_bf16 v[60:63], a[28:31], v[204:207], v[60:63]\n"
"v_cvt_pk_bf16_f32 v99, v102, v103\n"
"v_mfma_f32_16x16x32_bf16 v[4:7], a[32:35], v[208:211], v[4:7]\n"
"ds_read_b128 v[196:199], v242 offset:26624\n"
"v_mfma_f32_16x16x32_bf16 v[12:15], a[36:39], v[208:211], v[12:15]\n"
"buffer_store_dwordx4 v[56:59], v250, s[8:11], s49 offen offset:1024 sc1\n"
"v_mfma_f32_16x16x32_bf16 v[20:23], a[40:43], v[208:211], v[20:23]\n"
"v_mul_f32 v104, s55, v104\n"
"v_mul_f32 v105, s55, v105\n"
"v_mfma_f32_16x16x32_bf16 v[28:31], a[44:47], v[208:211], v[28:31]\n"
"buffer_load_dwordx4 v237, s[12:15], s48 offen offset:3072 lds\n"
"v_mfma_f32_16x16x32_bf16 v[36:39], a[32:35], v[212:215], v[36:39]\n"
"ds_read_b128 v[200:203], v243 offset:24576\n"
"v_mul_f32 v106, s55, v106\n"
"v_mfma_f32_16x16x32_bf16 v[44:47], a[36:39], v[212:215], v[44:47]\n"
"v_mul_f32 v107, s55, v107\n"
"v_mul_f32 v108, s55, v108\n"
"v_mfma_f32_16x16x32_bf16 v[52:55], a[40:43], v[212:215], v[52:55]\n"
"s_lshl_b32 s88, s31, 1\n"
"v_mul_f32 v109, s55, v109\n"
"v_mul_f32 v110, s55, v110\n"
"v_mfma_f32_16x16x32_bf16 v[60:63], a[44:47], v[212:215], v[60:63]\n"
"s_waitcnt lgkmcnt(1)\n"
"s_waitcnt vmcnt(22)\n"
"ds_read_b64_tr_b16 v[160:161], v254 offset:45056\n"
"v_mfma_f32_16x16x32_bf16 v[4:7], a[48:51], v[216:219], v[4:7]\n"
"s_add_u32 s88, s88, s30\n"
"ds_read_b128 v[204:207], v243 offset:26624\n"
"v_mul_f32 v111, s55, v111\n"
"v_mfma_f32_16x16x32_bf16 v[12:15], a[52:55], v[216:219], v[12:15]\n"
"s_add_u32 m0, s92, 5120\n"
"ds_read_b64_tr_b16 v[162:163], v254 offset:47616\n"
"v_cvt_pk_bf16_f32 v104, v104, v105\n"
"v_mfma_f32_16x16x32_bf16 v[20:23], a[56:59], v[216:219], v[20:23]\n"
"s_add_u32 s48, s61, 552960\n"
"ds_read_b64_tr_b16 v[164:165], v254 offset:45088\n"
"v_cvt_pk_bf16_f32 v105, v106, v107\n"
"v_mfma_f32_16x16x32_bf16 v[28:31], a[60:63], v[216:219], v[28:31]\n"
"s_lshr_b32 s89, s88, 3\n"
"buffer_load_dwordx4 v251, s[24:27], s48 offen lds\n"
"v_mfma_f32_16x16x32_bf16 v[36:39], a[48:51], v[220:223], v[36:39]\n"
"s_and_b32 s88, s88, 7\n"
"ds_read_b64_tr_b16 v[166:167], v254 offset:47648\n"
"v_cvt_pk_bf16_f32 v106, v108, v109\n"
"v_mfma_f32_16x16x32_bf16 v[44:47], a[52:55], v[220:223], v[44:47]\n"
"s_mul_i32 s88, s88, 270\n"
"ds_read_b128 v[208:211], v242 offset:28672\n"
"v_cvt_pk_bf16_f32 v107, v110, v111\n"
"v_mfma_f32_16x16x32_bf16 v[52:55], a[56:59], v[220:223], v[52:55]\n"
"s_add_u32 s88, s88, s89\n"
"ds_read_b64_tr_b16 v[168:169], v254 offset:45120\n"
"v_mul_f32 v112, s55, v112\n"
"v_mfma_f32_16x16x32_bf16 v[60:63], a[60:63], v[220:223], v[60:63]\n"
"s_lshl_b32 s88, s88, 10\n"
"ds_read_b64_tr_b16 v[170:171], v254 offset:47680\n"
"v_mul_f32 v113, s55, v113\n"
"v_mfma_f32_16x16x32_bf16 v[4:7], a[64:67], v[192:195], v[4:7]\n"
"s_add_u32 s88, s88, 2744320\n"
"ds_read_b64_tr_b16 v[172:173], v254 offset:45152\n"
"v_mul_f32 v114, s55, v114\n"
"v_mfma_f32_16x16x32_bf16 v[12:15], a[68:71], v[192:195], v[12:15]\n"
"s_cmp_lt_u32 s31, 216\n"
"ds_read_b64_tr_b16 v[174:175], v254 offset:47712\n"
"v_mul_f32 v115, s55, v115\n"
"v_mfma_f32_16x16x32_bf16 v[20:23], a[72:75], v[192:195], v[20:23]\n"
"s_cselect_b32 s90, s88, 2130706432\n"
"ds_read_b128 v[212:215], v242 offset:30720\n"
"v_mul_f32 v116, s55, v116\n"
"v_mfma_f32_16x16x32_bf16 v[28:31], a[76:79], v[192:195], v[28:31]\n"
"ds_read_b64_tr_b16 v[176:177], v254 offset:45184\n"
"v_mul_f32 v117, s55, v117\n"
"v_mfma_f32_16x16x32_bf16 v[36:39], a[64:67], v[196:199], v[36:39]\n"
"ds_read_b64_tr_b16 v[178:179], v254 offset:47744\n"
"v_mul_f32 v118, s55, v118\n"
"v_mfma_f32_16x16x32_bf16 v[44:47], a[68:71], v[196:199], v[44:47]\n"
"s_waitcnt lgkmcnt(10)\n"
"buffer_store_dwordx4 v[160:163], v224, s[8:11], s90 offen sc1\n"
"v_mfma_f32_16x16x32_bf16 v[52:55], a[72:75], v[196:199], v[52:55]\n"
"s_add_u32 m0, s92, 6144\n"
"ds_read_b128 v[216:219], v243 offset:28672\n"
"v_mul_f32 v119, s55, v119\n"
"v_mfma_f32_16x16x32_bf16 v[60:63], a[76:79], v[196:199], v[60:63]\n"
"s_add_u32 s48, s61, 663552\n"
"ds_read_b128 v[220:223], v243 offset:30720\n"
"v_cvt_pk_bf16_f32 v112, v112, v113\n"
"v_mfma_f32_16x16x32_bf16 v[4:7], a[80:83], v[200:203], v[4:7]\n"
"buffer_load_dwordx4 v251, s[24:27], s48 offen lds\n"
"v_mfma_f32_16x16x32_bf16 v[12:15], a[84:87], v[200:203], v[12:15]\n"
"v_cvt_pk_bf16_f32 v113, v114, v115\n"
"v_cvt_pk_bf16_f32 v114, v116, v117\n"
"v_mfma_f32_16x16x32_bf16 v[20:23], a[88:91], v[200:203], v[20:23]\n"
"ds_read_b128 v[192:195], v244 offset:0\n"
"v_cvt_pk_bf16_f32 v115, v118, v119\n"
"v_mfma_f32_16x16x32_bf16 v[28:31], a[92:95], v[200:203], v[28:31]\n"
"s_add_u32 s90, s90, 55296\n"
"v_mul_f32 v120, s55, v120\n"
"v_mul_f32 v121, s55, v121\n"
"v_mfma_f32_16x16x32_bf16 v[36:39], a[80:83], v[204:207], v[36:39]\n"
"v_mul_f32 v122, s55, v122\n"
"v_mul_f32 v123, s55, v123\n"
"v_mfma_f32_16x16x32_bf16 v[44:47], a[84:87], v[204:207], v[44:47]\n"
"s_waitcnt lgkmcnt(11)\n"
"buffer_store_dwordx4 v[164:167], v224, s[8:11], s90 offen sc1\n"
"v_mfma_f32_16x16x32_bf16 v[52:55], a[88:91], v[204:207], v[52:55]\n"
"ds_read_b128 v[196:199], v244 offset:2048\n"
"v_mul_f32 v124, s55, v124\n"
"v_mfma_f32_16x16x32_bf16 v[60:63], a[92:95], v[204:207], v[60:63]\n"
"s_waitcnt lgkmcnt(2)\n"
"v_mul_f32 v125, s55, v125\n"
"v_mul_f32 v126, s55, v126\n"
"v_mfma_f32_16x16x32_bf16 v[4:7], a[96:99], v[208:211], v[4:7]\n"
"v_mul_f32 v127, s55, v127\n"
"v_cvt_pk_bf16_f32 v120, v120, v121\n"
"v_mfma_f32_16x16x32_bf16 v[12:15], a[100:103], v[208:211], v[12:15]\n"
"v_cvt_pk_bf16_f32 v121, v122, v123\n"
"v_cvt_pk_bf16_f32 v122, v124, v125\n"
"v_mfma_f32_16x16x32_bf16 v[20:23], a[104:107], v[208:211], v[20:23]\n"
"ds_read_b128 v[200:203], v245 offset:0\n"
"v_cvt_pk_bf16_f32 v123, v126, v127\n"
"v_mfma_f32_16x16x32_bf16 v[28:31], a[108:111], v[208:211], v[28:31]\n"
"s_add_u32 m0, s92, 7168\n"
"v_mul_f32 v128, s55, v128\n"
"v_mul_f32 v129, s55, v129\n"
"v_mfma_f32_16x16x32_bf16 v[36:39], a[96:99], v[212:215], v[36:39]\n"
"s_add_u32 s48, s61, 774144\n"
"v_mul_f32 v130, s55, v130\n"
"v_mul_f32 v131, s55, v131\n"
"v_mfma_f32_16x16x32_bf16 v[44:47], a[100:103], v[212:215], v[44:47]\n"
"buffer_load_dwordx4 v251, s[24:27], s48 offen lds\n"
"v_mfma_f32_16x16x32_bf16 v[52:55], a[104:107], v[212:215], v[52:55]\n"
"ds_read_b128 v[204:207], v245 offset:2048\n"
"v_mul_f32 v132, s55, v132\n"
"v_mfma_f32_16x16x32_bf16 v[60:63], a[108:111], v[212:215], v[60:63]\n"
"s_add_u32 s90, s90, 55296\n"
"v_mul_f32 v133, s55, v133\n"
"v_mul_f32 v134, s55, v134\n"
"v_mfma_f32_16x16x32_bf16 v[4:7], a[112:115], v[216:219], v[4:7]\n"
"v_mul_f32 v135, s55, v135\n"
"v_cvt_pk_bf16_f32 v128, v128, v129\n"
"v_mfma_f32_16x16x32_bf16 v[12:15], a[116:119], v[216:219], v[12:15]\n"
"ds_read_b128 v[208:211], v244 offset:4096\n"
"v_cvt_pk_bf16_f32 v129, v130, v131\n"
"v_mfma_f32_16x16x32_bf16 v[20:23], a[120:123], v[216:219], v[20:23]\n"
"buffer_store_dwordx4 v[168:171], v224, s[8:11], s90 offen sc1\n"
"v_mfma_f32_16x16x32_bf16 v[28:31], a[124:127], v[216:219], v[28:31]\n"
"v_cvt_pk_bf16_f32 v130, v132, v133\n"
"v_cvt_pk_bf16_f32 v131, v134, v135\n"
"v_mfma_f32_16x16x32_bf16 v[36:39], a[112:115], v[220:223], v[36:39]\n"
"v_mfma_f32_16x16x32_bf16 v[44:47], a[116:119], v[220:223], v[44:47]\n"
"ds_read_b128 v[212:215], v244 offset:6144\n"
"v_mul_f32 v136, s55, v136\n"
"v_mfma_f32_16x16x32_bf16 v[52:55], a[120:123], v[220:223], v[52:55]\n"
"v_mul_f32 v137, s55, v137\n"
"v_mul_f32 v138, s55, v138\n"
"v_mfma_f32_16x16x32_bf16 v[60:63], a[124:127], v[220:223], v[60:63]\n"
"s_waitcnt lgkmcnt(2)\n"
"v_mul_f32 v139, s55, v139\n"
"v_mul_f32 v140, s55, v140\n"
"v_mfma_f32_16x16x32_bf16 v[4:7], a[128:131], v[192:195], v[4:7]\n"
"v_mul_f32 v141, s55, v141\n"
"v_mul_f32 v142, s55, v142\n"
"v_mfma_f32_16x16x32_bf16 v[12:15], a[132:135], v[192:195], v[12:15]\n"
"s_add_u32 m0, s92, 8192\n"
"ds_read_b128 v[216:219], v245 offset:4096\n"
"v_mul_f32 v143, s55, v143\n"
"v_mfma_f32_16x16x32_bf16 v[20:23], a[136:139], v[192:195], v[20:23]\n"
"s_add_u32 s48, s61, 884736\n"
"v_cvt_pk_bf16_f32 v136, v136, v137\n"
"v_cvt_pk_bf16_f32 v137, v138, v139\n"
"v_mfma_f32_16x16x32_bf16 v[28:31], a[140:143], v[192:195], v[28:31]\n"
"s_add_u32 s90, s90, 55296\n"
"buffer_load_dwordx4 v251, s[24:27], s48 offen lds\n"
"v_mfma_f32_16x16x32_bf16 v[36:39], a[128:131], v[196:199], v[36:39]\n"
"v_cvt_pk_bf16_f32 v138, v140, v141\n"
"v_cvt_pk_bf16_f32 v139, v142, v143\n"
"v_mfma_f32_16x16x32_bf16 v[44:47], a[132:135], v[196:199], v[44:47]\n"
"ds_read_b128 v[220:223], v245 offset:6144\n"
"v_mfma_f32_16x16x32_bf16 v[52:55], a[136:139], v[196:199], v[52:55]\n"
"buffer_store_dwordx4 v[172:175], v224, s[8:11], s90 offen sc1\n"
"v_mfma_f32_16x16x32_bf16 v[60:63], a[140:143], v[196:199], v[60:63]\n"
"v_mfma_f32_16x16x32_bf16 v[4:7], a[144:147], v[200:203], v[4:7]\n"
"ds_read_b128 v[192:195], v244 offset:24576\n"
"v_mul_f32 v144, s55, v144\n"
"v_mfma_f32_16x16x32_bf16 v[12:15], a[148:151], v[200:203], v[12:15]\n"
"v_mul_f32 v145, s55, v145\n"
"v_mul_f32 v146, s55, v146\n"
"v_mfma_f32_16x16x32_bf16 v[20:23], a[152:155], v[200:203], v[20:23]\n"
"v_mul_f32 v147, s55, v147\n"
"v_mul_f32 v148, s55, v148\n"
"v_mfma_f32_16x16x32_bf16 v[28:31], a[156:159], v[200:203], v[28:31]\n"
"v_mul_f32 v149, s55, v149\n"
"v_mul_f32 v150, s55, v150\n"
"v_mfma_f32_16x16x32_bf16 v[36:39], a[144:147], v[204:207], v[36:39]\n"
"ds_read_b128 v[196:199], v244 offset:26624\n"
"v_mul_f32 v151, s55, v151\n"
"v_mfma_f32_16x16x32_bf16 v[44:47], a[148:151], v[204:207], v[44:47]\n"
"v_cvt_pk_bf16_f32 v144, v144, v145\n"
"v_cvt_pk_bf16_f32 v145, v146, v147\n"
"v_mfma_f32_16x16x32_bf16 v[52:55], a[152:155], v[204:207], v[52:55]\n"
"v_cvt_pk_bf16_f32 v146, v148, v149\n"
"v_cvt_pk_bf16_f32 v147, v150, v151\n"
"v_mfma_f32_16x16x32_bf16 v[60:63], a[156:159], v[204:207], v[60:63]\n"
"s_waitcnt lgkmcnt(1)\n"
"s_add_u32 s90, s90, 55296\n"
"s_add_u32 m0, s92, 9216\n"
"v_mfma_f32_16x16x32_bf16 v[4:7], a[160:163], v[208:211], v[4:7]\n"
"s_add_u32 s48, s61, 995328\n"
"ds_read_b128 v[200:203], v245 offset:24576\n"
"v_mfma_f32_16x16x32_bf16 v[12:15], a[164:167], v[208:211], v[12:15]\n"
"buffer_store_dwordx4 v[176:179], v224, s[8:11], s90 offen sc1\n"
"v_mfma_f32_16x16x32_bf16 v[20:23], a[168:171], v[208:211], v[20:23]\n"
"buffer_load_dwordx4 v251, s[24:27], s48 offen lds\n"
"v_mfma_f32_16x16x32_bf16 v[28:31], a[172:175], v[208:211], v[28:31]\n"
"v_mul_f32 v152, s55, v152\n"
"v_mul_f32 v153, s55, v153\n"
"v_mfma_f32_16x16x32_bf16 v[36:39], a[160:163], v[212:215], v[36:39]\n"
"ds_read_b128 v[204:207], v245 offset:26624\n"
"v_mul_f32 v154, s55, v154\n"
"v_mfma_f32_16x16x32_bf16 v[44:47], a[164:167], v[212:215], v[44:47]\n"
"v_mul_f32 v155, s55, v155\n"
"v_mul_f32 v156, s55, v156\n"
"v_mfma_f32_16x16x32_bf16 v[52:55], a[168:171], v[212:215], v[52:55]\n"
"v_mul_f32 v157, s55, v157\n"
"v_mul_f32 v158, s55, v158\n"
"v_mfma_f32_16x16x32_bf16 v[60:63], a[172:175], v[212:215], v[60:63]\n"
"ds_read_b128 v[208:211], v244 offset:28672\n"
"v_mul_f32 v159, s55, v159\n"
"v_mfma_f32_16x16x32_bf16 v[4:7], a[176:179], v[216:219], v[4:7]\n"
"v_cvt_pk_bf16_f32 v152, v152, v153\n"
"v_cvt_pk_bf16_f32 v153, v154, v155\n"
"v_mfma_f32_16x16x32_bf16 v[12:15], a[180:183], v[216:219], v[12:15]\n"
"v_cvt_pk_bf16_f32 v154, v156, v157\n"
"v_cvt_pk_bf16_f32 v155, v158, v159\n"
"v_mfma_f32_16x16x32_bf16 v[20:23], a[184:187], v[216:219], v[20:23]\n"
"v_mfma_f32_16x16x32_bf16 v[28:31], a[188:191], v[216:219], v[28:31]\n"
"s_mov_b32 s49, 16384\n"
"ds_read_b128 v[212:215], v244 offset:30720\n"
"v_mfma_f32_16x16x32_bf16 v[36:39], a[176:179], v[220:223], v[36:39]\n"
"v_mfma_f32_16x16x32_bf16 v[44:47], a[180:183], v[220:223], v[44:47]\n"
"buffer_store_dwordx4 v[64:67], v250, s[8:11], s49 offen offset:0 sc1\n"
"v_mfma_f32_16x16x32_bf16 v[52:55], a[184:187], v[220:223], v[52:55]\n"
"v_mfma_f32_16x16x32_bf16 v[60:63], a[188:191], v[220:223], v[60:63]\n"
"ds_read_b128 v[216:219], v245 offset:28672\n"
"v_mfma_f32_16x16x32_bf16 v[4:7], a[192:195], v[192:195], v[4:7]\n"
"v_mfma_f32_16x16x32_bf16 v[12:15], a[196:199], v[192:195], v[12:15]\n"
"s_sub_u32 s99, s34, 5\n"
"v_mfma_f32_16x16x32_bf16 v[20:23], a[200:203], v[192:195], v[20:23]\n"
"s_cmp_lt_u32 s34, 5\n"
"s_cselect_b32 s99, s34, s99\n"
"v_mfma_f32_16x16x32_bf16 v[28:31], a[204:207], v[192:195], v[28:31]\n"
"s_waitcnt lgkmcnt(1)\n"
"ds_read_b128 v[220:223], v245 offset:30720\n"
"s_mul_i32 s99, s99, 1\n"
"v_mfma_f32_16x16x32_bf16 v[36:39], a[192:195], v[196:199], v[36:39]\n"
"s_lshl_b32 s98, s99, 20\n"
"v_mfma_f32_16x16x32_bf16 v[44:47], a[196:199], v[196:199], v[44:47]\n"
"v_mfma_f32_16x16x32_bf16 v[52:55], a[200:203], v[196:199], v[52:55]\n"
"v_mfma_f32_16x16x32_bf16 v[60:63], a[204:207], v[196:199], v[60:63]\n"
"s_lshr_b32 s50, s30, 1\n"
"v_mfma_f32_16x16x32_bf16 v[4:7], a[208:211], v[200:203], v[4:7]\n"
"s_and_b32 s51, s30, 1\n"
"v_mfma_f32_16x16x32_bf16 v[12:15], a[212:215], v[200:203], v[12:15]\n"
"s_mul_i32 s38, s50, 24576\n"
"v_mfma_f32_16x16x32_bf16 v[20:23], a[216:219], v[200:203], v[20:23]\n"
"s_lshl_b32 s39, s50, 18\n"
"v_mfma_f32_16x16x32_bf16 v[28:31], a[220:223], v[200:203], v[28:31]\n"
"s_lshl_b32 s50, s51, 12\n"
"v_mfma_f32_16x16x32_bf16 v[36:39], a[208:211], v[204:207], v[36:39]\n"
"s_add_u32 s38, s38, s50\n"
"v_mfma_f32_16x16x32_bf16 v[44:47], a[212:215], v[204:207], v[44:47]\n"
"s_lshl_b32 s50, s51, 7\n"
"s_waitcnt lgkmcnt(0)\n"
"s_barrier\n"
"v_mfma_f32_16x16x32_bf16 v[52:55], a[216:219], v[204:207], v[52:55]\n"
"s_add_u32 s39, s39, s50\n"
"buffer_store_dwordx4 v[72:75], v250, s[8:11], s49 offen offset:1024 sc1\n"
"v_mfma_f32_16x16x32_bf16 v[60:63], a[220:223], v[204:207], v[60:63]\n"
"ds_read_b128 v[192:195], v242 offset:8192\n"
"v_mfma_f32_16x16x32_bf16 v[4:7], a[224:227], v[208:211], v[4:7]\n"
"v_mfma_f32_16x16x32_bf16 v[12:15], a[228:231], v[208:211], v[12:15]\n"
"s_add_u32 m0, s38, 65536\n"
"s_add_u32 s48, s39, s98\n"
"v_mfma_f32_16x16x32_bf16 v[20:23], a[232:235], v[208:211], v[20:23]\n"
"ds_read_b128 v[196:199], v242 offset:10240\n"
"v_mfma_f32_16x16x32_bf16 v[28:31], a[236:239], v[208:211], v[28:31]\n"
"buffer_load_dwordx4 v238, s[20:23], s48 offen offset:0 lds\n"
"v_mfma_f32_16x16x32_bf16 v[36:39], a[224:227], v[212:215], v[36:39]\n"
"v_mfma_f32_16x16x32_bf16 v[44:47], a[228:231], v[212:215], v[44:47]\n"
"v_mfma_f32_16x16x32_bf16 v[52:55], a[232:235], v[212:215], v[52:55]\n"
"ds_read_b128 v[200:203], v243 offset:8192\n"
"v_mfma_f32_16x16x32_bf16 v[60:63], a[236:239], v[212:215], v[60:63]\n"
"v_mfma_f32_16x16x32_bf16 v[4:7], a[240:243], v[216:219], v[4:7]\n"
"v_mfma_f32_16x16x32_bf16 v[12:15], a[244:247], v[216:219], v[12:15]\n"
"v_mfma_f32_16x16x32_bf16 v[20:23], a[248:251], v[216:219], v[20:23]\n"
"ds_read_b128 v[204:207], v243 offset:10240\n"
"v_mfma_f32_16x16x32_bf16 v[28:31], a[252:255], v[216:219], v[28:31]\n"
"s_mov_b32 s49, 20480\n"
"buffer_load_dwordx4 v239, s[20:23], s48 offen offset:1024 lds\n"
"v_mfma_f32_16x16x32_bf16 v[36:39], a[240:243], v[220:223], v[36:39]\n"
"v_mfma_f32_16x16x32_bf16 v[44:47], a[244:247], v[220:223], v[44:47]\n"
"ds_read_b128 v[208:211], v242 offset:12288\n"
"v_mfma_f32_16x16x32_bf16 v[52:55], a[248:251], v[220:223], v[52:55]\n"
"buffer_store_dwordx4 v[80:83], v250, s[8:11], s49 offen offset:0 sc1\n"
"v_mfma_f32_16x16x32_bf16 v[60:63], a[252:255], v[220:223], v[60:63]\n"
"s_waitcnt lgkmcnt(2)\n"
"v_mfma_f32_16x16x32_bf16 v[84:87], a[0:3], v[192:195], 0\n"
"v_mfma_f32_16x16x32_bf16 v[92:95], a[4:7], v[192:195], 0\n"
"ds_read_b128 v[212:215], v242 offset:14336\n"
"v_mfma_f32_16x16x32_bf16 v[100:103], a[8:11], v[192:195], 0\n"
"buffer_load_dwordx4 v240, s[20:23], s48 offen offset:2048 lds\n"
"v_mfma_f32_16x16x32_bf16 v[108:111], a[12:15], v[192:195], 0\n"
"v_mul_f32 v4, s55, v4\n"
"v_mul_f32 v5, s55, v5\n"
"v_mfma_f32_16x16x32_bf16 v[116:119], a[0:3], v[196:199], 0\n"
"v_mul_f32 v6, s55, v6\n"
"v_mul_f32 v7, s55, v7\n"
"v_mfma_f32_16x16x32_bf16 v[124:127], a[4:7], v[196:199], 0\n"
"ds_read_b128 v[216:219], v243 offset:12288\n"
"v_mul_f32 v12, s55, v12\n"
"v_mfma_f32_16x16x32_bf16 v[132:135], a[8:11], v[196:199], 0\n"
"v_mul_f32 v13, s55, v13\n"
"v_mul_f32 v14, s55, v14\n"
"v_mfma_f32_16x16x32_bf16 v[140:143], a[12:15], v[196:199], 0\n"
"s_add_u32 s98, s98, 524288\n"
"v_mul_f32 v15, s55, v15\n"
"v_cvt_pk_bf16_f32 v4, v4, v5\n"
"v_mfma_f32_16x16x32_bf16 v[84:87], a[16:19], v[200:203], v[84:87]\n"
"buffer_load_dwordx4 v241, s[20:23], s48 offen offset:3072 lds\n"
"v_mfma_f32_16x16x32_bf16 v[92:95], a[20:23], v[200:203], v[92:95]\n"
"ds_read_b128 v[220:223], v243 offset:14336\n"
"v_cvt_pk_bf16_f32 v5, v6, v7\n"
"v_mfma_f32_16x16x32_bf16 v[100:103], a[24:27], v[200:203], v[100:103]\n"
"v_cvt_pk_bf16_f32 v6, v12, v13\n"
"v_cvt_pk_bf16_f32 v7, v14, v15\n"
"v_mfma_f32_16x16x32_bf16 v[108:111], a[28:31], v[200:203], v[108:111]\n"
"s_waitcnt lgkmcnt(2)\n"
"v_mfma_f32_16x16x32_bf16 v[116:119], a[16:19], v[204:207], v[116:119]\n"
"ds_read_b128 v[192:195], v242 offset:32768\n"
"v_mfma_f32_16x16x32_bf16 v[124:127], a[20:23], v[204:207], v[124:127]\n"
"s_add_u32 m0, s38, 114688\n"
"buffer_store_dwordx4 v[88:91], v250, s[8:11], s49 offen offset:1024 sc1\n"
"v_mfma_f32_16x16x32_bf16 v[132:135], a[24:27], v[204:207], v[132:135]\n"
"s_add_u32 s48, s39, s98\n"
"v_mul_f32 v20, s55, v20\n"
"v_mul_f32 v21, s55, v21\n"
"v_mfma_f32_16x16x32_bf16 v[140:143], a[28:31], v[204:207], v[140:143]\n"
"buffer_load_dwordx4 v238, s[20:23], s48 offen offset:0 lds\n"
"v_mfma_f32_16x16x32_bf16 v[84:87], a[32:35], v[208:211], v[84:87]\n"
"ds_read_b128 v[196:199], v242 offset:34816\n"
"v_mul_f32 v22, s55, v22\n"
"v_mfma_f32_16x16x32_bf16 v[92:95], a[36:39], v[208:211], v[92:95]\n"
"v_mul_f32 v23, s55, v23\n"
"v_mul_f32 v28, s55, v28\n"
"v_mfma_f32_16x16x32_bf16 v[100:103], a[40:43], v[208:211], v[100:103]\n"
"v_mul_f32 v29, s55, v29\n"
"v_mul_f32 v30, s55, v30\n"
"v_mfma_f32_16x16x32_bf16 v[108:111], a[44:47], v[208:211], v[108:111]\n"
"v_mul_f32 v31, s55, v31\n"
"v_cvt_pk_bf16_f32 v20, v20, v21\n"
"v_mfma_f32_16x16x32_bf16 v[116:119], a[32:35], v[212:215], v[116:119]\n"
"ds_read_b128 v[200:203], v243 offset:32768\n"
"v_cvt_pk_bf16_f32 v21, v22, v23\n"
"v_mfma_f32_16x16x32_bf16 v[124:127], a[36:39], v[212:215], v[124:127]\n"
"v_cvt_pk_bf16_f32 v22, v28, v29\n"
"v_cvt_pk_bf16_f32 v23, v30, v31\n"
"v_mfma_f32_16x16x32_bf16 v[132:135], a[40:43], v[212:215], v[132:135]\n"
"buffer_load_dwordx4 v239, s[20:23], s48 offen offset:1024 lds\n"
"v_mfma_f32_16x16x32_bf16 v[140:143], a[44:47], v[212:215], v[140:143]\n"
"s_waitcnt lgkmcnt(1)\n"
"v_mfma_f32_16x16x32_bf16 v[84:87], a[48:51], v[216:219], v[84:87]\n"
"ds_read_b128 v[204:207], v243 offset:34816\n"
"v_mfma_f32_16x16x32_bf16 v[92:95], a[52:55], v[216:219], v[92:95]\n"
"v_mul_f32 v36, s55, v36\n"
"v_mul_f32 v37, s55, v37\n"
"v_mfma_f32_16x16x32_bf16 v[100:103], a[56:59], v[216:219], v[100:103]\n"
"v_mul_f32 v38, s55, v38\n"
"v_mul_f32 v39, s55, v39\n"
"v_mfma_f32_16x16x32_bf16 v[108:111], a[60:63], v[216:219], v[108:111]\n"
"s_mov_b32 s49, 24576\n"
"ds_read_b128 v[208:211], v242 offset:36864\n"
"v_mul_f32 v44, s55, v44\n"
"v_mfma_f32_16x16x32_bf16 v[116:119], a[48:51], v[220:223], v[116:119]\n"
"v_mul_f32 v45, s55, v45\n"
"v_mul_f32 v46, s55, v46\n"
"v_mfma_f32_16x16x32_bf16 v[124:127], a[52:55], v[220:223], v[124:127]\n"
"buffer_store_dwordx4 v[96:99], v250, s[8:11], s49 offen offset:0 sc1\n"
"v_mfma_f32_16x16x32_bf16 v[132:135], a[56:59], v[220:223], v[132:135]\n"
"buffer_load_dwordx4 v240, s[20:23], s48 offen offset:2048 lds\n"
"v_mfma_f32_16x16x32_bf16 v[140:143], a[60:63], v[220:223], v[140:143]\n"
"ds_read_b128 v[212:215], v242 offset:38912\n"
"v_mul_f32 v47, s55, v47\n"
"v_mfma_f32_16x16x32_bf16 v[84:87], a[64:67], v[192:195], v[84:87]\n"
"v_cvt_pk_bf16_f32 v36, v36, v37\n"
"v_cvt_pk_bf16_f32 v37, v38, v39\n"
"v_mfma_f32_16x16x32_bf16 v[92:95], a[68:71], v[192:195], v[92:95]\n"
"v_cvt_pk_bf16_f32 v38, v44, v45\n"
"v_cvt_pk_bf16_f32 v39, v46, v47\n"
"v_mfma_f32_16x16x32_bf16 v[100:103], a[72:75], v[192:195], v[100:103]\n"
"v_mfma_f32_16x16x32_bf16 v[108:111], a[76:79], v[192:195], v[108:111]\n"
"s_add_u32 s98, s98, 524288\n"
"ds_read_b128 v[216:219], v243 offset:36864\n"
"s_lshl_b32 s50, s34, 3\n"
"v_mfma_f32_16x16x32_bf16 v[116:119], a[64:67], v[196:199], v[116:119]\n"
"s_cmp_eq_u32 s98, 5242880\n"
"buffer_load_dwordx4 v241, s[20:23], s48 offen offset:3072 lds\n"
"v_mfma_f32_16x16x32_bf16 v[124:127], a[68:71], v[196:199], v[124:127]\n"
"s_cselect_b32 s98, 0, s98\n"
"v_mul_f32 v52, s55, v52\n"
"v_mul_f32 v53, s55, v53\n"
"v_mfma_f32_16x16x32_bf16 v[132:135], a[72:75], v[196:199], v[132:135]\n"
"s_add_u32 s51, s50, s30\n"
"v_mul_f32 v54, s55, v54\n"
"v_mul_f32 v55, s55, v55\n"
"v_mfma_f32_16x16x32_bf16 v[140:143], a[76:79], v[196:199], v[140:143]\n"
"s_waitcnt lgkmcnt(1)\n"
"ds_read_b128 v[220:223], v243 offset:38912\n"
"v_mul_f32 v60, s55, v60\n"
"v_mfma_f32_16x16x32_bf16 v[84:87], a[80:83], v[200:203], v[84:87]\n"
"s_mul_i32 s51, s51, 20480\n"
"v_mul_f32 v61, s55, v61\n"
"v_mul_f32 v62, s55, v62\n"
"v_mfma_f32_16x16x32_bf16 v[92:95], a[84:87], v[200:203], v[92:95]\n"
"s_add_u32 s56, s51, 65536\n"
"v_mul_f32 v63, s55, v63\n"
"v_cvt_pk_bf16_f32 v52, v52, v53\n"
"v_mfma_f32_16x16x32_bf16 v[100:103], a[88:91], v[200:203], v[100:103]\n"
"s_add_u32 s57, s56, 81920\n"
"ds_read_b128 v[192:195], v244 offset:8192\n"
"v_cvt_pk_bf16_f32 v53, v54, v55\n"
"v_mfma_f32_16x16x32_bf16 v[108:111], a[92:95], v[200:203], v[108:111]\n"
"s_sub_u32 s51, s30, s50\n"
"v_cvt_pk_bf16_f32 v54, v60, v61\n"
"v_cvt_pk_bf16_f32 v55, v62, v63\n"
"v_mfma_f32_16x16x32_bf16 v[116:119], a[80:83], v[204:207], v[116:119]\n"
"s_add_u32 s51, s51, 120\n"
"s_mul_i32 s51, s51, 20480\n"
"s_add_u32 s58, s51, 65536\n"
"v_mfma_f32_16x16x32_bf16 v[124:127], a[84:87], v[204:207], v[124:127]\n"
"s_lshl_b32 s99, s99, 12\n"
"buffer_store_dwordx4 v[104:107], v250, s[8:11], s49 offen offset:1024 sc1\n"
"v_mfma_f32_16x16x32_bf16 v[132:135], a[88:91], v[204:207], v[132:135]\n"
"s_add_u32 s59, s58, 81920\n"
"ds_read_b128 v[196:199], v244 offset:10240\n"
"v_add_u32 v235, s99, v224\n"
"v_mfma_f32_16x16x32_bf16 v[140:143], a[92:95], v[204:207], v[140:143]\n"
"buffer_load_dwordx4 v[64:67], v235, s[8:11], s56 offen offset:0 sc1\n"
"v_mfma_f32_16x16x32_bf16 v[84:87], a[96:99], v[208:211], v[84:87]\n"
"v_mfma_f32_16x16x32_bf16 v[92:95], a[100:103], v[208:211], v[92:95]\n"
"v_mfma_f32_16x16x32_bf16 v[100:103], a[104:107], v[208:211], v[100:103]\n"
"ds_read_b128 v[200:203], v245 offset:8192\n"
"v_mfma_f32_16x16x32_bf16 v[108:111], a[108:111], v[208:211], v[108:111]\n"
"v_mfma_f32_16x16x32_bf16 v[116:119], a[96:99], v[212:215], v[116:119]\n"
"v_mfma_f32_16x16x32_bf16 v[124:127], a[100:103], v[212:215], v[124:127]\n"
"buffer_load_dwordx4 v[68:71], v235, s[8:11], s57 offen offset:0 sc1\n"
"v_mfma_f32_16x16x32_bf16 v[132:135], a[104:107], v[212:215], v[132:135]\n"
"ds_read_b128 v[204:207], v245 offset:10240\n"
"v_mfma_f32_16x16x32_bf16 v[140:143], a[108:111], v[212:215], v[140:143]\n"
"s_waitcnt lgkmcnt(2)\n"
"v_mfma_f32_16x16x32_bf16 v[84:87], a[112:115], v[216:219], v[84:87]\n"
"v_mfma_f32_16x16x32_bf16 v[92:95], a[116:119], v[216:219], v[92:95]\n"
"ds_read_b128 v[208:211], v244 offset:12288\n"
"v_mfma_f32_16x16x32_bf16 v[100:103], a[120:123], v[216:219], v[100:103]\n"
"v_mfma_f32_16x16x32_bf16 v[108:111], a[124:127], v[216:219], v[108:111]\n"
"s_mov_b32 s49, 28672\n"
"buffer_load_dwordx4 v[72:75], v235, s[8:11], s58 offen offset:0 sc1\n"
"v_mfma_f32_16x16x32_bf16 v[116:119], a[112:115], v[220:223], v[116:119]\n"
"v_mfma_f32_16x16x32_bf16 v[124:127], a[116:119], v[220:223], v[124:127]\n"
"ds_read_b128 v[212:215], v244 offset:14336\n"
"v_mfma_f32_16x16x32_bf16 v[132:135], a[120:123], v[220:223], v[132:135]\n"
"buffer_store_dwordx4 v[112:115], v250, s[8:11], s49 offen offset:0 sc1\n"
"v_mfma_f32_16x16x32_bf16 v[140:143], a[124:127], v[220:223], v[140:143]\n"
"v_mfma_f32_16x16x32_bf16 v[84:87], a[128:131], v[192:195], v[84:87]\n"
"v_mfma_f32_16x16x32_bf16 v[92:95], a[132:135], v[192:195], v[92:95]\n"
"ds_read_b128 v[216:219], v245 offset:12288\n"
"v_mfma_f32_16x16x32_bf16 v[100:103], a[136:139], v[192:195], v[100:103]\n"
"buffer_load_dwordx4 v[76:79], v235, s[8:11], s59 offen offset:0 sc1\n"
"v_mfma_f32_16x16x32_bf16 v[108:111], a[140:143], v[192:195], v[108:111]\n"
"v_mfma_f32_16x16x32_bf16 v[116:119], a[128:131], v[196:199], v[116:119]\n"
"v_mfma_f32_16x16x32_bf16 v[124:127], a[132:135], v[196:199], v[124:127]\n"
"ds_read_b128 v[220:223], v245 offset:14336\n"
"v_mfma_f32_16x16x32_bf16 v[132:135], a[136:139], v[196:199], v[132:135]\n"
"v_mfma_f32_16x16x32_bf16 v[140:143], a[140:143], v[196:199], v[140:143]\n"
"s_waitcnt lgkmcnt(2)\n"
"v_mfma_f32_16x16x32_bf16 v[84:87], a[144:147], v[200:203], v[84:87]\n"
"ds_read_b128 v[192:195], v244 offset:32768\n"
"v_mfma_f32_16x16x32_bf16 v[92:95], a[148:151], v[200:203], v[92:95]\n"
"v_mfma_f32_16x16x32_bf16 v[100:103], a[152:155], v[200:203], v[100:103]\n"
"v_mfma_f32_16x16x32_bf16 v[108:111], a[156:159], v[200:203], v[108:111]\n"
"v_mfma_f32_16x16x32_bf16 v[116:119], a[144:147], v[204:207], v[116:119]\n"
"ds_read_b128 v[196:199], v244 offset:34816\n"
"v_mfma_f32_16x16x32_bf16 v[124:127], a[148:151], v[204:207], v[124:127]\n"
"buffer_store_dwordx4 v[120:123], v250, s[8:11], s49 offen offset:1024 sc1\n"
"v_mfma_f32_16x16x32_bf16 v[132:135], a[152:155], v[204:207], v[132:135]\n"
"v_mfma_f32_16x16x32_bf16 v[140:143], a[156:159], v[204:207], v[140:143]\n"
"v_mfma_f32_16x16x32_bf16 v[84:87], a[160:163], v[208:211], v[84:87]\n"
"ds_read_b128 v[200:203], v245 offset:32768\n"
"v_mfma_f32_16x16x32_bf16 v[92:95], a[164:167], v[208:211], v[92:95]\n"
"v_mfma_f32_16x16x32_bf16 v[100:103], a[168:171], v[208:211], v[100:103]\n"
"v_mfma_f32_16x16x32_bf16 v[108:111], a[172:175], v[208:211], v[108:111]\n"
"v_mfma_f32_16x16x32_bf16 v[116:119], a[160:163], v[212:215], v[116:119]\n"
"ds_read_b128 v[204:207], v245 offset:34816\n"
"v_mfma_f32_16x16x32_bf16 v[124:127], a[164:167], v[212:215], v[124:127]\n"
"v_mfma_f32_16x16x32_bf16 v[132:135], a[168:171], v[212:215], v[132:135]\n"
"v_mfma_f32_16x16x32_bf16 v[140:143], a[172:175], v[212:215], v[140:143]\n"
"s_waitcnt lgkmcnt(1)\n"
"ds_read_b128 v[208:211], v244 offset:36864\n"
"v_mfma_f32_16x16x32_bf16 v[84:87], a[176:179], v[216:219], v[84:87]\n"
"v_mfma_f32_16x16x32_bf16 v[92:95], a[180:183], v[216:219], v[92:95]\n"
"v_mfma_f32_16x16x32_bf16 v[100:103], a[184:187], v[216:219], v[100:103]\n"
"v_mfma_f32_16x16x32_bf16 v[108:111], a[188:191], v[216:219], v[108:111]\n"
"s_mov_b32 s49, 262144\n"
"ds_read_b128 v[212:215], v244 offset:38912\n"
"v_mfma_f32_16x16x32_bf16 v[116:119], a[176:179], v[220:223], v[116:119]\n"
"v_mfma_f32_16x16x32_bf16 v[124:127], a[180:183], v[220:223], v[124:127]\n"
"buffer_store_dwordx4 v[128:131], v250, s[8:11], s49 offen offset:0 sc1\n"
"v_mfma_f32_16x16x32_bf16 v[132:135], a[184:187], v[220:223], v[132:135]\n"
"v_mfma_f32_16x16x32_bf16 v[140:143], a[188:191], v[220:223], v[140:143]\n"
"ds_read_b128 v[216:219], v245 offset:36864\n"
"v_mfma_f32_16x16x32_bf16 v[84:87], a[192:195], v[192:195], v[84:87]\n"
"v_mfma_f32_16x16x32_bf16 v[92:95], a[196:199], v[192:195], v[92:95]\n"
"v_mfma_f32_16x16x32_bf16 v[100:103], a[200:203], v[192:195], v[100:103]\n"
"v_mfma_f32_16x16x32_bf16 v[108:111], a[204:207], v[192:195], v[108:111]\n"
"ds_read_b128 v[220:223], v245 offset:38912\n"
"v_mfma_f32_16x16x32_bf16 v[116:119], a[192:195], v[196:199], v[116:119]\n"
"v_mfma_f32_16x16x32_bf16 v[124:127], a[196:199], v[196:199], v[124:127]\n"
"v_mfma_f32_16x16x32_bf16 v[132:135], a[200:203], v[196:199], v[132:135]\n"
"v_mfma_f32_16x16x32_bf16 v[140:143], a[204:207], v[196:199], v[140:143]\n"
"v_mfma_f32_16x16x32_bf16 v[84:87], a[208:211], v[200:203], v[84:87]\n"
"v_mfma_f32_16x16x32_bf16 v[92:95], a[212:215], v[200:203], v[92:95]\n"
"v_mfma_f32_16x16x32_bf16 v[100:103], a[216:219], v[200:203], v[100:103]\n"
"v_mfma_f32_16x16x32_bf16 v[108:111], a[220:223], v[200:203], v[108:111]\n"
"s_waitcnt lgkmcnt(0)\n"
"v_lshrrev_b32 v251, 2, v224\n"
"v_mfma_f32_16x16x32_bf16 v[116:119], a[208:211], v[204:207], v[116:119]\n"
"s_add_u32 s44, s44, 65536\n"
"v_and_b32 v251, 28, v251\n"
"v_mfma_f32_16x16x32_bf16 v[124:127], a[212:215], v[204:207], v[124:127]\n"
"s_lshl_b32 s92, s30, 10\n"
"s_waitcnt vmcnt(31)\n"
"s_barrier\n"
"v_mfma_f32_16x16x32_bf16 v[132:135], a[216:219], v[204:207], v[132:135]\n"
"s_and_b32 s93, s92, 1024\n"
"buffer_store_dwordx4 v[136:139], v250, s[8:11], s49 offen offset:1024 sc1\n"
"v_mfma_f32_16x16x32_bf16 v[140:143], a[220:223], v[204:207], v[140:143]\n"
"s_add_u32 m0, s38, 73728\n"
"ds_read_b128 v[192:195], v242 offset:16384\n"
"s_add_u32 s48, s39, s98\n"
"v_mfma_f32_16x16x32_bf16 v[84:87], a[224:227], v[208:211], v[84:87]\n"
"s_add_u32 s93, s93, 8192\n"
"ds_read_b32 v234, v251 offset:40960\n"
"v_mfma_f32_16x16x32_bf16 v[92:95], a[228:231], v[208:211], v[92:95]\n"
"buffer_load_dwordx4 v238, s[20:23], s48 offen offset:0 lds\n"
"v_mfma_f32_16x16x32_bf16 v[100:103], a[232:235], v[208:211], v[100:103]\n"
"ds_read_b128 v[196:199], v242 offset:18432\n"
"v_mfma_f32_16x16x32_bf16 v[108:111], a[236:239], v[208:211], v[108:111]\n"
"v_mfma_f32_16x16x32_bf16 v[116:119], a[224:227], v[212:215], v[116:119]\n"
"buffer_load_dwordx4 v239, s[20:23], s48 offen offset:1024 lds\n"
"v_mfma_f32_16x16x32_bf16 v[124:127], a[228:231], v[212:215], v[124:127]\n"
"v_mfma_f32_16x16x32_bf16 v[132:135], a[232:235], v[212:215], v[132:135]\n"
"ds_read_b128 v[200:203], v243 offset:16384\n"
"v_mfma_f32_16x16x32_bf16 v[140:143], a[236:239], v[212:215], v[140:143]\n"
"s_waitcnt lgkmcnt(2)\n"
"v_not_b32 v251, v234\n"
"v_bfe_u32 v252, v251, 4, 7\n"
"v_mfma_f32_16x16x32_bf16 v[84:87], a[240:243], v[216:219], v[84:87]\n"
"s_add_u32 s98, s98, 524288\n"
"buffer_load_dwordx4 v240, s[20:23], s48 offen offset:2048 lds\n"
"v_mfma_f32_16x16x32_bf16 v[92:95], a[244:247], v[216:219], v[92:95]\n"
"v_mul_u32_u24 v252, 20480, v252\n"
"v_and_b32 v251, 15, v251\n"
"v_mfma_f32_16x16x32_bf16 v[100:103], a[248:251], v[216:219], v[100:103]\n"
"ds_read_b128 v[204:207], v243 offset:18432\n"
"v_lshl_add_u32 v251, v251, 4, v252\n"
"v_mfma_f32_16x16x32_bf16 v[108:111], a[252:255], v[216:219], v[108:111]\n"
"s_mov_b32 s49, 266240\n"
"v_and_b32 v252, 0x380, v224\n"
"v_lshl_add_u32 v251, v252, 1, v251\n"
"v_mfma_f32_16x16x32_bf16 v[116:119], a[240:243], v[220:223], v[116:119]\n"
"buffer_load_dwordx4 v241, s[20:23], s48 offen offset:3072 lds\n"
"v_mfma_f32_16x16x32_bf16 v[124:127], a[244:247], v[220:223], v[124:127]\n"
"ds_read_b128 v[208:211], v242 offset:20480\n"
"v_mfma_f32_16x16x32_bf16 v[132:135], a[248:251], v[220:223], v[132:135]\n"
"s_add_u32 m0, s38, 122880\n"
"buffer_store_dwordx4 v[144:147], v250, s[8:11], s49 offen offset:0 sc1\n"
"v_mfma_f32_16x16x32_bf16 v[140:143], a[252:255], v[220:223], v[140:143]\n"
"s_add_u32 s48, s39, s98\n"
"v_mfma_f32_16x16x32_bf16 v[0:3], a[0:3], v[192:195], 0\n"
"buffer_load_dwordx4 v238, s[20:23], s48 offen offset:0 lds\n"
"v_mfma_f32_16x16x32_bf16 v[8:11], a[4:7], v[192:195], 0\n"
"ds_read_b128 v[212:215], v242 offset:22528\n"
"v_mfma_f32_16x16x32_bf16 v[16:19], a[8:11], v[192:195], 0\n"
"v_mfma_f32_16x16x32_bf16 v[24:27], a[12:15], v[192:195], 0\n"
"s_waitcnt lgkmcnt(2)\n"
"v_mul_f32 v84, s55, v84\n"
"v_mul_f32 v85, s55, v85\n"
"v_mfma_f32_16x16x32_bf16 v[32:35], a[0:3], v[196:199], 0\n"
"v_mul_f32 v86, s55, v86\n"
"v_mul_f32 v87, s55, v87\n"
"v_mfma_f32_16x16x32_bf16 v[40:43], a[4:7], v[196:199], 0\n"
"ds_read_b128 v[216:219], v243 offset:20480\n"
"v_mul_f32 v92, s55, v92\n"
"v_mfma_f32_16x16x32_bf16 v[48:51], a[8:11], v[196:199], 0\n"
"buffer_load_dwordx4 v239, s[20:23], s48 offen offset:1024 lds\n"
"v_mfma_f32_16x16x32_bf16 v[56:59], a[12:15], v[196:199], 0\n"
"v_mul_f32 v93, s55, v93\n"
"v_mul_f32 v94, s55, v94\n"
"v_mfma_f32_16x16x32_bf16 v[0:3], a[16:19], v[200:203], v[0:3]\n"
"buffer_load_dwordx4 a[0:3], v235, s[8:11], s56 offen offset:1024 sc1\n"
"v_mfma_f32_16x16x32_bf16 v[8:11], a[20:23], v[200:203], v[8:11]\n"
"ds_read_b128 v[220:223], v243 offset:22528\n"
"v_mul_f32 v95, s55, v95\n"
"v_mfma_f32_16x16x32_bf16 v[16:19], a[24:27], v[200:203], v[16:19]\n"
"buffer_load_dwordx4 v240, s[20:23], s48 offen offset:2048 lds\n"
"v_mfma_f32_16x16x32_bf16 v[24:27], a[28:31], v[200:203], v[24:27]\n"
"v_cvt_pk_bf16_f32 v12, v84, v85\n"
"v_cvt_pk_bf16_f32 v13, v86, v87\n"
"v_mfma_f32_16x16x32_bf16 v[32:35], a[16:19], v[204:207], v[32:35]\n"
"s_add_u32 s98, s98, 524288\n"
"ds_read_b128 v[192:195], v242 offset:40960\n"
"v_cvt_pk_bf16_f32 v14, v92, v93\n"
"v_mfma_f32_16x16x32_bf16 v[40:43], a[20:23], v[204:207], v[40:43]\n"
"s_cmp_eq_u32 s98, 5242880\n"
"buffer_store_dwordx4 v[152:155], v250, s[8:11], s49 offen offset:1024 sc1\n"
"v_mfma_f32_16x16x32_bf16 v[48:51], a[24:27], v[204:207], v[48:51]\n"
"s_cselect_b32 s98, 0, s98\n"
"buffer_load_dwordx4 a[4:7], v235, s[8:11], s57 offen offset:1024 sc1\n"
"v_mfma_f32_16x16x32_bf16 v[56:59], a[28:31], v[204:207], v[56:59]\n"
"s_waitcnt lgkmcnt(2)\n"
"buffer_load_dwordx4 v241, s[20:23], s48 offen offset:3072 lds\n"
"v_mfma_f32_16x16x32_bf16 v[0:3], a[32:35], v[208:211], v[0:3]\n"
"ds_read_b128 v[196:199], v242 offset:43008\n"
"v_cvt_pk_bf16_f32 v15, v94, v95\n"
"v_mfma_f32_16x16x32_bf16 v[8:11], a[36:39], v[208:211], v[8:11]\n"
"v_mul_f32 v100, s55, v100\n"
"v_mul_f32 v101, s55, v101\n"
"v_mfma_f32_16x16x32_bf16 v[16:19], a[40:43], v[208:211], v[16:19]\n"
"v_mul_f32 v102, s55, v102\n"
"v_mul_f32 v103, s55, v103\n"
"v_mfma_f32_16x16x32_bf16 v[24:27], a[44:47], v[208:211], v[24:27]\n"
"v_mul_f32 v108, s55, v108\n"
"v_mul_f32 v109, s55, v109\n"
"v_mfma_f32_16x16x32_bf16 v[32:35], a[32:35], v[212:215], v[32:35]\n"
"ds_read_b128 v[200:203], v243 offset:40960\n"
"v_mul_f32 v110, s55, v110\n"
"v_mfma_f32_16x16x32_bf16 v[40:43], a[36:39], v[212:215], v[40:43]\n"
"s_add_u32 m0, s92, 45056\n"
"buffer_load_dwordx4 a[8:11], v235, s[8:11], s58 offen offset:1024 sc1\n"
"v_mfma_f32_16x16x32_bf16 v[48:51], a[40:43], v[212:215], v[48:51]\n"
"s_lshl_b32 s48, s92, 1\n"
"v_mul_f32 v111, s55, v111\n"
"v_cvt_pk_bf16_f32 v28, v100, v101\n"
"v_mfma_f32_16x16x32_bf16 v[56:59], a[44:47], v[212:215], v[56:59]\n"
"s_add_u32 s48, s48, s44\n"
"v_cvt_pk_bf16_f32 v29, v102, v103\n"
"v_cvt_pk_bf16_f32 v30, v108, v109\n"
"v_mfma_f32_16x16x32_bf16 v[0:3], a[48:51], v[216:219], v[0:3]\n"
"ds_read_b128 v[204:207], v243 offset:43008\n"
"v_cvt_pk_bf16_f32 v31, v110, v111\n"
"v_mfma_f32_16x16x32_bf16 v[8:11], a[52:55], v[216:219], v[8:11]\n"
"buffer_load_dwordx4 v251, s[8:11], s48 offen sc1 lds\n"
"v_mfma_f32_16x16x32_bf16 v[16:19], a[56:59], v[216:219], v[16:19]\n"
"buffer_load_dwordx4 a[12:15], v235, s[8:11], s59 offen offset:1024 sc1\n"
"v_mfma_f32_16x16x32_bf16 v[24:27], a[60:63], v[216:219], v[24:27]\n"
"s_waitcnt lgkmcnt(1)\n"
"ds_read_b128 v[208:211], v242 offset:45056\n"
"v_mul_f32 v116, s55, v116\n"
"v_mfma_f32_16x16x32_bf16 v[32:35], a[48:51], v[220:223], v[32:35]\n"
"v_mul_f32 v117, s55, v117\n"
"v_mul_f32 v118, s55, v118\n"
"v_mfma_f32_16x16x32_bf16 v[40:43], a[52:55], v[220:223], v[40:43]\n"
"v_mul_f32 v119, s55, v119\n"
"v_mul_f32 v124, s55, v124\n"
"v_mfma_f32_16x16x32_bf16 v[48:51], a[56:59], v[220:223], v[48:51]\n"
"v_mul_f32 v125, s55, v125\n"
"v_mul_f32 v126, s55, v126\n"
"v_mfma_f32_16x16x32_bf16 v[56:59], a[60:63], v[220:223], v[56:59]\n"
"ds_read_b128 v[212:215], v242 offset:47104\n"
"v_mul_f32 v127, s55, v127\n"
"v_mfma_f32_16x16x32_bf16 v[0:3], a[64:67], v[192:195], v[0:3]\n"
"buffer_load_dwordx4 a[16:19], v235, s[8:11], s56 offen offset:2048 sc1\n"
"v_mfma_f32_16x16x32_bf16 v[8:11], a[68:71], v[192:195], v[8:11]\n"
"v_cvt_pk_bf16_f32 v44, v116, v117\n"
"v_cvt_pk_bf16_f32 v45, v118, v119\n"
"v_mfma_f32_16x16x32_bf16 v[16:19], a[72:75], v[192:195], v[16:19]\n"
"v_cvt_pk_bf16_f32 v46, v124, v125\n"
"v_cvt_pk_bf16_f32 v47, v126, v127\n"
"v_mfma_f32_16x16x32_bf16 v[24:27], a[76:79], v[192:195], v[24:27]\n"
"ds_read_b128 v[216:219], v243 offset:45056\n"
"v_mfma_f32_16x16x32_bf16 v[32:35], a[64:67], v[196:199], v[32:35]\n"
"v_mul_f32 v132, s55, v132\n"
"v_mul_f32 v133, s55, v133\n"
"v_mfma_f32_16x16x32_bf16 v[40:43], a[68:71], v[196:199], v[40:43]\n"
"s_add_u32 m0, s92, 49152\n"
"v_mul_f32 v134, s55, v134\n"
"v_mul_f32 v135, s55, v135\n"
"v_mfma_f32_16x16x32_bf16 v[48:51], a[72:75], v[196:199], v[48:51]\n"
"s_lshl_b32 s48, s92, 1\n"
"buffer_load_dwordx4 a[20:23], v235, s[8:11], s57 offen offset:2048 sc1\n"
"v_mfma_f32_16x16x32_bf16 v[56:59], a[76:79], v[196:199], v[56:59]\n"
"s_add_u32 s48, s48, s44\n"
"ds_read_b128 v[220:223], v243 offset:47104\n"
"v_mul_f32 v140, s55, v140\n"
"v_mfma_f32_16x16x32_bf16 v[0:3], a[80:83], v[200:203], v[0:3]\n"
"s_add_u32 s48, s48, 8192\n"
"v_mul_f32 v141, s55, v141\n"
"v_mul_f32 v142, s55, v142\n"
"v_mfma_f32_16x16x32_bf16 v[8:11], a[84:87], v[200:203], v[8:11]\n"
"buffer_load_dwordx4 v251, s[8:11], s48 offen sc1 lds\n"
"v_mfma_f32_16x16x32_bf16 v[16:19], a[88:91], v[200:203], v[16:19]\n"
"ds_read_b128 v[192:195], v244 offset:16384\n"
"v_mul_f32 v143, s55, v143\n"
"v_mfma_f32_16x16x32_bf16 v[24:27], a[92:95], v[200:203], v[24:27]\n"
"s_waitcnt lgkmcnt(2)\n"
"v_cvt_pk_bf16_f32 v60, v132, v133\n"
"v_cvt_pk_bf16_f32 v61, v134, v135\n"
"v_mfma_f32_16x16x32_bf16 v[32:35], a[80:83], v[204:207], v[32:35]\n"
"buffer_load_dwordx4 a[24:27], v235, s[8:11], s58 offen offset:2048 sc1\n"
"v_mfma_f32_16x16x32_bf16 v[40:43], a[84:87], v[204:207], v[40:43]\n"
"v_cvt_pk_bf16_f32 v62, v140, v141\n"
"v_cvt_pk_bf16_f32 v63, v142, v143\n"
"v_mfma_f32_16x16x32_bf16 v[48:51], a[88:91], v[204:207], v[48:51]\n"
"ds_read_b128 v[196:199], v244 offset:18432\n"
"v_mfma_f32_16x16x32_bf16 v[56:59], a[92:95], v[204:207], v[56:59]\n"
"v_mfma_f32_16x16x32_bf16 v[0:3], a[96:99], v[208:211], v[0:3]\n"
"v_mfma_f32_16x16x32_bf16 v[8:11], a[100:103], v[208:211], v[8:11]\n"
"v_mfma_f32_16x16x32_bf16 v[16:19], a[104:107], v[208:211], v[16:19]\n"
"ds_read_b128 v[200:203], v245 offset:16384\n"
"v_mfma_f32_16x16x32_bf16 v[24:27], a[108:111], v[208:211], v[24:27]\n"
"buffer_load_dwordx4 a[28:31], v235, s[8:11], s59 offen offset:2048 sc1\n"
"v_mfma_f32_16x16x32_bf16 v[32:35], a[96:99], v[212:215], v[32:35]\n"
"v_mfma_f32_16x16x32_bf16 v[40:43], a[100:103], v[212:215], v[40:43]\n"
"s_add_u32 m0, s93, 45056\n"
"s_lshl_b32 s48, s93, 1\n"
"v_mfma_f32_16x16x32_bf16 v[48:51], a[104:107], v[212:215], v[48:51]\n"
"s_add_u32 s48, s48, s44\n"
"ds_read_b128 v[204:207], v245 offset:18432\n"
"v_mfma_f32_16x16x32_bf16 v[56:59], a[108:111], v[212:215], v[56:59]\n"
"v_mfma_f32_16x16x32_bf16 v[0:3], a[112:115], v[216:219], v[0:3]\n"
"buffer_load_dwordx4 a[32:35], v235, s[8:11], s56 offen offset:3072 sc1\n"
"v_mfma_f32_16x16x32_bf16 v[8:11], a[116:119], v[216:219], v[8:11]\n"
"buffer_load_dwordx4 v251, s[8:11], s48 offen sc1 lds\n"
"v_mfma_f32_16x16x32_bf16 v[16:19], a[120:123], v[216:219], v[16:19]\n"
"ds_read_b128 v[208:211], v244 offset:20480\n"
"v_mfma_f32_16x16x32_bf16 v[24:27], a[124:127], v[216:219], v[24:27]\n"
"s_waitcnt lgkmcnt(2)\n"
"v_mfma_f32_16x16x32_bf16 v[32:35], a[112:115], v[220:223], v[32:35]\n"
"v_mfma_f32_16x16x32_bf16 v[40:43], a[116:119], v[220:223], v[40:43]\n"
"ds_read_b128 v[212:215], v244 offset:22528\n"
"v_mfma_f32_16x16x32_bf16 v[48:51], a[120:123], v[220:223], v[48:51]\n"
"buffer_load_dwordx4 a[36:39], v235, s[8:11], s57 offen offset:3072 sc1\n"
"v_mfma_f32_16x16x32_bf16 v[56:59], a[124:127], v[220:223], v[56:59]\n"
"v_mfma_f32_16x16x32_bf16 v[0:3], a[128:131], v[192:195], v[0:3]\n"
"v_mfma_f32_16x16x32_bf16 v[8:11], a[132:135], v[192:195], v[8:11]\n"
"ds_read_b128 v[216:219], v245 offset:20480\n"
"v_mfma_f32_16x16x32_bf16 v[16:19], a[136:139], v[192:195], v[16:19]\n"
"v_mfma_f32_16x16x32_bf16 v[24:27], a[140:143], v[192:195], v[24:27]\n"
"v_mfma_f32_16x16x32_bf16 v[32:35], a[128:131], v[196:199], v[32:35]\n"
"buffer_load_dwordx4 a[40:43], v235, s[8:11], s58 offen offset:3072 sc1\n"
"v_mfma_f32_16x16x32_bf16 v[40:43], a[132:135], v[196:199], v[40:43]\n"
"ds_read_b128 v[220:223], v245 offset:22528\n"
"v_mfma_f32_16x16x32_bf16 v[48:51], a[136:139], v[196:199], v[48:51]\n"
"v_mfma_f32_16x16x32_bf16 v[56:59], a[140:143], v[196:199], v[56:59]\n"
"v_mfma_f32_16x16x32_bf16 v[0:3], a[144:147], v[200:203], v[0:3]\n"
"s_add_u32 s99, s99, 4096\n"
"ds_read_b128 v[192:195], v244 offset:40960\n"
"v_mfma_f32_16x16x32_bf16 v[8:11], a[148:151], v[200:203], v[8:11]\n"
"s_cmp_eq_u32 s99, 20480\n"
"s_cselect_b32 s99, 0, s99\n"
"v_mfma_f32_16x16x32_bf16 v[16:19], a[152:155], v[200:203], v[16:19]\n"
"buffer_load_dwordx4 a[44:47], v235, s[8:11], s59 offen offset:3072 sc1\n"
"v_mfma_f32_16x16x32_bf16 v[24:27], a[156:159], v[200:203], v[24:27]\n"
"s_waitcnt lgkmcnt(2)\n"
"v_mfma_f32_16x16x32_bf16 v[32:35], a[144:147], v[204:207], v[32:35]\n"
"ds_read_b128 v[196:199], v244 offset:43008\n"
"v_mfma_f32_16x16x32_bf16 v[40:43], a[148:151], v[204:207], v[40:43]\n"
"v_mfma_f32_16x16x32_bf16 v[48:51], a[152:155], v[204:207], v[48:51]\n"
"v_mfma_f32_16x16x32_bf16 v[56:59], a[156:159], v[204:207], v[56:59]\n"
"v_add_u32 v235, s99, v224\n"
"v_mfma_f32_16x16x32_bf16 v[0:3], a[160:163], v[208:211], v[0:3]\n"
"s_sub_u32 s53, s37, 65536\n"
"ds_read_b128 v[200:203], v245 offset:40960\n"
"v_mfma_f32_16x16x32_bf16 v[8:11], a[164:167], v[208:211], v[8:11]\n"
"buffer_load_dwordx4 a[48:51], v235, s[8:11], s56 offen offset:0 sc1\n"
"v_mfma_f32_16x16x32_bf16 v[16:19], a[168:171], v[208:211], v[16:19]\n"
"v_subrev_u32 v246, s53, v246\n"
"v_subrev_u32 v247, s53, v247\n"
"v_mfma_f32_16x16x32_bf16 v[24:27], a[172:175], v[208:211], v[24:27]\n"
"v_subrev_u32 v248, s53, v248\n"
"v_subrev_u32 v249, s53, v249\n"
"v_mfma_f32_16x16x32_bf16 v[32:35], a[160:163], v[212:215], v[32:35]\n"
"ds_read_b128 v[204:207], v245 offset:43008\n"
"v_mfma_f32_16x16x32_bf16 v[40:43], a[164:167], v[212:215], v[40:43]\n"
"v_mfma_f32_16x16x32_bf16 v[48:51], a[168:171], v[212:215], v[48:51]\n"
"buffer_load_dwordx4 a[52:55], v235, s[8:11], s57 offen offset:0 sc1\n"
"v_mfma_f32_16x16x32_bf16 v[56:59], a[172:175], v[212:215], v[56:59]\n"
"ds_read_b128 v[208:211], v244 offset:45056\n"
"v_mfma_f32_16x16x32_bf16 v[0:3], a[176:179], v[216:219], v[0:3]\n"
"v_mfma_f32_16x16x32_bf16 v[8:11], a[180:183], v[216:219], v[8:11]\n"
"v_mfma_f32_16x16x32_bf16 v[16:19], a[184:187], v[216:219], v[16:19]\n"
"v_mfma_f32_16x16x32_bf16 v[24:27], a[188:191], v[216:219], v[24:27]\n"
"s_waitcnt lgkmcnt(1)\n"
"ds_read_b128 v[212:215], v244 offset:47104\n"
"v_mfma_f32_16x16x32_bf16 v[32:35], a[176:179], v[220:223], v[32:35]\n"
"buffer_load_dwordx4 a[56:59], v235, s[8:11], s58 offen offset:0 sc1\n"
"v_mfma_f32_16x16x32_bf16 v[40:43], a[180:183], v[220:223], v[40:43]\n"
"v_mfma_f32_16x16x32_bf16 v[48:51], a[184:187], v[220:223], v[48:51]\n"
"v_mfma_f32_16x16x32_bf16 v[56:59], a[188:191], v[220:223], v[56:59]\n"
"ds_read_b128 v[216:219], v245 offset:45056\n"
"v_mfma_f32_16x16x32_bf16 v[0:3], a[192:195], v[192:195], v[0:3]\n"
"v_mfma_f32_16x16x32_bf16 v[8:11], a[196:199], v[192:195], v[8:11]\n"
"v_mfma_f32_16x16x32_bf16 v[16:19], a[200:203], v[192:195], v[16:19]\n"
"buffer_load_dwordx4 a[60:63], v235, s[8:11], s59 offen offset:0 sc1\n"
"v_mfma_f32_16x16x32_bf16 v[24:27], a[204:207], v[192:195], v[24:27]\n"
"ds_read_b128 v[220:223], v245 offset:47104\n"
"v_mfma_f32_16x16x32_bf16 v[32:35], a[192:195], v[196:199], v[32:35]\n"
"v_mfma_f32_16x16x32_bf16 v[40:43], a[196:199], v[196:199], v[40:43]\n"
"v_mfma_f32_16x16x32_bf16 v[48:51], a[200:203], v[196:199], v[48:51]\n"
"v_mfma_f32_16x16x32_bf16 v[56:59], a[204:207], v[196:199], v[56:59]\n"
"v_mfma_f32_16x16x32_bf16 v[0:3], a[208:211], v[200:203], v[0:3]\n"
"s_lshl_b32 s50, s35, 3\n"
"buffer_load_dwordx4 a[64:67], v235, s[8:11], s56 offen offset:1024 sc1\n"
"v_mfma_f32_16x16x32_bf16 v[8:11], a[212:215], v[200:203], v[8:11]\n"
"s_add_u32 s50, s50, s34\n"
"s_lshl_b32 s50, s50, 3\n"
"s_add_u32 s50, s50, 2304\n"
"v_mfma_f32_16x16x32_bf16 v[16:19], a[216:219], v[200:203], v[16:19]\n"
"s_add_u32 s51, s50, s45\n"
"v_mfma_f32_16x16x32_bf16 v[24:27], a[220:223], v[200:203], v[24:27]\n"
"v_mfma_f32_16x16x32_bf16 v[32:35], a[208:211], v[204:207], v[32:35]\n"
"v_mfma_f32_16x16x32_bf16 v[40:43], a[212:215], v[204:207], v[40:43]\n"
"s_waitcnt vmcnt(32) lgkmcnt(0)\n"
"s_barrier\n"
"v_mfma_f32_16x16x32_bf16 v[48:51], a[216:219], v[204:207], v[48:51]\n"
"s_add_u32 m0, s38, 81920\n"
"ds_read_b64_tr_b16 a[128:129], v246 offset:0\n"
"s_add_u32 s48, s39, s98\n"
"v_mfma_f32_16x16x32_bf16 v[56:59], a[220:223], v[204:207], v[56:59]\n"
"buffer_load_dwordx4 a[68:71], v235, s[8:11], s57 offen offset:1024 sc1\n"
"v_mfma_f32_16x16x32_bf16 v[0:3], a[224:227], v[208:211], v[0:3]\n"
"buffer_store_dwordx2 v[232:233], v230, s[8:11], s51 offen sc1\n"
"v_mfma_f32_16x16x32_bf16 v[8:11], a[228:231], v[208:211], v[8:11]\n"
"ds_read_b64_tr_b16 a[130:131], v246 offset:512\n"
"v_mfma_f32_16x16x32_bf16 v[16:19], a[232:235], v[208:211], v[16:19]\n"
"buffer_load_dwordx4 v238, s[20:23], s48 offen offset:0 lds\n"
"v_mfma_f32_16x16x32_bf16 v[24:27], a[236:239], v[208:211], v[24:27]\n"
"ds_read_b64_tr_b16 a[132:133], v247 offset:0\n"
"v_add_u32 v242, 49152, v246\n"
"v_mfma_f32_16x16x32_bf16 v[32:35], a[224:227], v[212:215], v[32:35]\n"
"ds_read_b64_tr_b16 a[134:135], v247 offset:512\n"
"v_add_u32 v243, 49152, v247\n"
"v_mfma_f32_16x16x32_bf16 v[40:43], a[228:231], v[212:215], v[40:43]\n"
"buffer_load_dwordx4 a[72:75], v235, s[8:11], s58 offen offset:1024 sc1\n"
"v_mfma_f32_16x16x32_bf16 v[48:51], a[232:235], v[212:215], v[48:51]\n"
"ds_read_b64_tr_b16 a[136:137], v248 offset:0\n"
"v_add_u32 v244, 49152, v248\n"
"v_mfma_f32_16x16x32_bf16 v[56:59], a[236:239], v[212:215], v[56:59]\n"
"v_add_u32 v245, 49152, v249\n"
"v_mfma_f32_16x16x32_bf16 v[0:3], a[240:243], v[216:219], v[0:3]\n"
"ds_read_b64_tr_b16 a[138:139], v248 offset:512\n"
"v_mfma_f32_16x16x32_bf16 v[8:11], a[244:247], v[216:219], v[8:11]\n"
"buffer_load_dwordx4 v239, s[20:23], s48 offen offset:1024 lds\n"
"v_mfma_f32_16x16x32_bf16 v[16:19], a[248:251], v[216:219], v[16:19]\n"
"ds_read_b64_tr_b16 a[140:141], v249 offset:0\n"
"v_mfma_f32_16x16x32_bf16 v[24:27], a[252:255], v[216:219], v[24:27]\n"
"buffer_load_dwordx4 a[76:79], v235, s[8:11], s59 offen offset:1024 sc1\n"
"v_mfma_f32_16x16x32_bf16 v[32:35], a[240:243], v[220:223], v[32:35]\n"
"ds_read_b64_tr_b16 a[142:143], v249 offset:512\n"
"v_mfma_f32_16x16x32_bf16 v[40:43], a[244:247], v[220:223], v[40:43]\n"
"v_mfma_f32_16x16x32_bf16 v[48:51], a[248:251], v[220:223], v[48:51]\n"
"ds_read_b64_tr_b16 a[144:145], v246 offset:4096\n"
"v_mfma_f32_16x16x32_bf16 v[56:59], a[252:255], v[220:223], v[56:59]\n"
"s_waitcnt lgkmcnt(3)\n"
"v_mfma_f32_16x16x32_bf16 v[80:83], a[128:131], v[64:67], 0\n"
"ds_read_b64_tr_b16 a[146:147], v246 offset:4608\n"
"v_mfma_f32_16x16x32_bf16 v[88:91], a[128:131], v[68:71], 0\n"
"buffer_load_dwordx4 a[80:83], v235, s[8:11], s56 offen offset:2048 sc1\n"
"v_mfma_f32_16x16x32_bf16 v[96:99], a[128:131], v[72:75], 0\n"
"buffer_load_dwordx4 v240, s[20:23], s48 offen offset:2048 lds\n"
"v_mfma_f32_16x16x32_bf16 v[104:107], a[128:131], v[76:79], 0\n"
"ds_read_b64_tr_b16 a[148:149], v247 offset:4096\n"
"v_mul_f32 v0, s55, v0\n"
"v_mfma_f32_16x16x32_bf16 v[84:87], a[132:135], v[64:67], 0\n"
"ds_read_b64_tr_b16 a[150:151], v247 offset:4608\n"
"v_mul_f32 v1, s55, v1\n"
"v_mfma_f32_16x16x32_bf16 v[92:95], a[132:135], v[68:71], 0\n"
"ds_read_b64_tr_b16 a[152:153], v248 offset:4096\n"
"v_mul_f32 v2, s55, v2\n"
"v_mfma_f32_16x16x32_bf16 v[100:103], a[132:135], v[72:75], 0\n"
"v_mul_f32 v3, s55, v3\n"
"v_mul_f32 v8, s55, v8\n"
"v_mfma_f32_16x16x32_bf16 v[108:111], a[132:135], v[76:79], 0\n"
"ds_read_b64_tr_b16 a[154:155], v248 offset:4608\n"
"v_mul_f32 v9, s55, v9\n"
"v_mfma_f32_16x16x32_bf16 v[112:115], a[136:139], v[64:67], 0\n"
"s_add_u32 s98, s98, 524288\n"
"buffer_load_dwordx4 a[84:87], v235, s[8:11], s57 offen offset:2048 sc1\n"
"v_mfma_f32_16x16x32_bf16 v[120:123], a[136:139], v[68:71], 0\n"
"buffer_load_dwordx4 v241, s[20:23], s48 offen offset:3072 lds\n"
"v_mfma_f32_16x16x32_bf16 v[128:131], a[136:139], v[72:75], 0\n"
"ds_read_b64_tr_b16 a[156:157], v249 offset:4096\n"
"v_mul_f32 v10, s55, v10\n"
"v_mfma_f32_16x16x32_bf16 v[136:139], a[136:139], v[76:79], 0\n"
"s_waitcnt lgkmcnt(3)\n"
"ds_read_b64_tr_b16 a[158:159], v249 offset:4608\n"
"v_mul_f32 v11, s55, v11\n"
"v_mfma_f32_16x16x32_bf16 v[116:119], a[140:143], v[64:67], 0\n"
"v_cvt_pk_bf16_f32 v0, v0, v1\n"
"v_cvt_pk_bf16_f32 v1, v2, v3\n"
"v_mfma_f32_16x16x32_bf16 v[124:127], a[140:143], v[68:71], 0\n"
"ds_read_b64_tr_b16 a[160:161], v246 offset:24576\n"
"v_cvt_pk_bf16_f32 v2, v8, v9\n"
"v_mfma_f32_16x16x32_bf16 v[132:135], a[140:143], v[72:75], 0\n"
"s_add_u32 m0, s38, 131072\n"
"buffer_load_dwordx4 a[88:91], v235, s[8:11], s58 offen offset:2048 sc1\n"
"v_mfma_f32_16x16x32_bf16 v[140:143], a[140:143], v[76:79], 0\n"
"s_add_u32 s48, s39, s98\n"
"ds_read_b64_tr_b16 a[162:163], v246 offset:25088\n"
"v_cvt_pk_bf16_f32 v3, v10, v11\n"
"v_mfma_f32_16x16x32_bf16 v[144:147], a[144:147], v[64:67], 0\n"
"buffer_load_dwordx4 v238, s[20:23], s48 offen offset:0 lds\n"
"v_mfma_f32_16x16x32_bf16 v[152:155], a[144:147], v[68:71], 0\n"
"ds_read_b64_tr_b16 a[164:165], v247 offset:24576\n"
"v_mul_f32 v16, s55, v16\n"
"v_mfma_f32_16x16x32_bf16 v[160:163], a[144:147], v[72:75], 0\n"
"v_mul_f32 v17, s55, v17\n"
"v_mul_f32 v18, s55, v18\n"
"v_mfma_f32_16x16x32_bf16 v[168:171], a[144:147], v[76:79], 0\n"
"ds_read_b64_tr_b16 a[166:167], v247 offset:25088\n"
"v_mul_f32 v19, s55, v19\n"
"v_mfma_f32_16x16x32_bf16 v[148:151], a[148:151], v[64:67], 0\n"
"v_mul_f32 v24, s55, v24\n"
"v_mul_f32 v25, s55, v25\n"
"v_mfma_f32_16x16x32_bf16 v[156:159], a[148:151], v[68:71], 0\n"
"ds_read_b64_tr_b16 a[168:169], v248 offset:24576\n"
"v_mul_f32 v26, s55, v26\n"
"v_mfma_f32_16x16x32_bf16 v[164:167], a[148:151], v[72:75], 0\n"
"buffer_load_dwordx4 a[92:95], v235, s[8:11], s59 offen offset:2048 sc1\n"
"v_mfma_f32_16x16x32_bf16 v[172:175], a[148:151], v[76:79], 0\n"
"s_waitcnt lgkmcnt(3)\n"
"ds_read_b64_tr_b16 a[170:171], v248 offset:25088\n"
"v_mul_f32 v27, s55, v27\n"
"v_mfma_f32_16x16x32_bf16 v[176:179], a[152:155], v[64:67], 0\n"
"ds_read_b64_tr_b16 a[172:173], v249 offset:24576\n"
"v_cvt_pk_bf16_f32 v16, v16, v17\n"
"v_mfma_f32_16x16x32_bf16 v[184:187], a[152:155], v[68:71], 0\n"
"buffer_load_dwordx4 v239, s[20:23], s48 offen offset:1024 lds\n"
"v_mfma_f32_16x16x32_bf16 v[192:195], a[152:155], v[72:75], 0\n"
"ds_read_b64_tr_b16 a[174:175], v249 offset:25088\n"
"v_cvt_pk_bf16_f32 v17, v18, v19\n"
"v_mfma_f32_16x16x32_bf16 v[200:203], a[152:155], v[76:79], 0\n"
"v_cvt_pk_bf16_f32 v18, v24, v25\n"
"v_cvt_pk_bf16_f32 v19, v26, v27\n"
"v_mfma_f32_16x16x32_bf16 v[180:183], a[156:159], v[64:67], 0\n"
"ds_read_b64_tr_b16 a[176:177], v246 offset:28672\n"
"v_mul_f32 v32, s55, v32\n"
"v_mfma_f32_16x16x32_bf16 v[188:191], a[156:159], v[68:71], 0\n"
"buffer_load_dwordx4 a[96:99], v235, s[8:11], s56 offen offset:3072 sc1\n"
"v_mfma_f32_16x16x32_bf16 v[196:199], a[156:159], v[72:75], 0\n"
"ds_read_b64_tr_b16 a[178:179], v246 offset:29184\n"
"v_mul_f32 v33, s55, v33\n"
"v_mfma_f32_16x16x32_bf16 v[204:207], a[156:159], v[76:79], 0\n"
"s_waitcnt vmcnt(37)\n"
"v_mul_f32 v34, s55, v34\n"
"v_mul_f32 v35, s55, v35\n"
"v_mfma_f32_16x16x32_bf16 v[80:83], a[160:163], a[0:3], v[80:83]\n"
"s_waitcnt vmcnt(34)\n"
"ds_read_b64_tr_b16 a[180:181], v247 offset:28672\n"
"v_mul_f32 v40, s55, v40\n"
"v_mfma_f32_16x16x32_bf16 v[88:91], a[160:163], a[4:7], v[88:91]\n"
"s_waitcnt vmcnt(32)\n"
"buffer_load_dwordx4 v240, s[20:23], s48 offen offset:2048 lds\n"
"v_mfma_f32_16x16x32_bf16 v[96:99], a[160:163], a[8:11], v[96:99]\n"
"s_waitcnt vmcnt(31)\n"
"ds_read_b64_tr_b16 a[182:183], v247 offset:29184\n"
"v_mul_f32 v41, s55, v41\n"
"v_mfma_f32_16x16x32_bf16 v[104:107], a[160:163], a[12:15], v[104:107]\n"
"s_waitcnt lgkmcnt(3)\n"
"buffer_load_dwordx4 a[100:103], v235, s[8:11], s57 offen offset:3072 sc1\n"
"v_mfma_f32_16x16x32_bf16 v[84:87], a[164:167], a[0:3], v[84:87]\n"
"s_mov_b32 s88, 3072\n"
"ds_read_b64_tr_b16 a[184:185], v248 offset:28672\n"
"v_mul_f32 v42, s55, v42\n"
"v_mfma_f32_16x16x32_bf16 v[92:95], a[164:167], a[4:7], v[92:95]\n"
"s_mov_b32 s89, 3072\n"
"v_mul_f32 v43, s55, v43\n"
"v_cvt_pk_bf16_f32 v32, v32, v33\n"
"v_mfma_f32_16x16x32_bf16 v[100:103], a[164:167], a[8:11], v[100:103]\n"
"ds_read_b64_tr_b16 a[186:187], v248 offset:29184\n"
"v_cvt_pk_bf16_f32 v33, v34, v35\n"
"v_mfma_f32_16x16x32_bf16 v[108:111], a[164:167], a[12:15], v[108:111]\n"
"v_cvt_pk_bf16_f32 v34, v40, v41\n"
"v_cvt_pk_bf16_f32 v35, v42, v43\n"
"v_mfma_f32_16x16x32_bf16 v[112:115], a[168:171], a[0:3], v[112:115]\n"
"s_add_u32 s98, s98, 524288\n"
"ds_read_b64_tr_b16 a[188:189], v249 offset:28672\n"
"v_mul_f32 v48, s55, v48\n"
"v_mfma_f32_16x16x32_bf16 v[120:123], a[168:171], a[4:7], v[120:123]\n"
"s_cmp_eq_u32 s98, 5242880\n"
"buffer_load_dwordx4 v241, s[20:23], s48 offen offset:3072 lds\n"
"v_mfma_f32_16x16x32_bf16 v[128:131], a[168:171], a[8:11], v[128:131]\n"
"s_cselect_b32 s98, 0, s98\n"
"ds_read_b64_tr_b16 a[190:191], v249 offset:29184\n"
"v_mul_f32 v49, s55, v49\n"
"v_mfma_f32_16x16x32_bf16 v[136:139], a[168:171], a[12:15], v[136:139]\n"
"buffer_load_dwordx4 a[104:107], v235, s[8:11], s58 offen offset:3072 sc1\n"
"v_mfma_f32_16x16x32_bf16 v[116:119], a[172:175], a[0:3], v[116:119]\n"
"ds_read_b64_tr_b16 a[128:129], v242 offset:0\n"
"v_mul_f32 v50, s55, v50\n"
"v_mfma_f32_16x16x32_bf16 v[124:127], a[172:175], a[4:7], v[124:127]\n"
"ds_read_b64_tr_b16 a[130:131], v242 offset:512\n"
"v_mul_f32 v51, s55, v51\n"
"v_mfma_f32_16x16x32_bf16 v[132:135], a[172:175], a[8:11], v[132:135]\n"
"v_mul_f32 v56, s55, v56\n"
"v_mul_f32 v57, s55, v57\n"
"v_mfma_f32_16x16x32_bf16 v[140:143], a[172:175], a[12:15], v[140:143]\n"
"s_waitcnt lgkmcnt(3)\n"
"ds_read_b64_tr_b16 a[132:133], v243 offset:0\n"
"v_mul_f32 v58, s55, v58\n"
"v_mfma_f32_16x16x32_bf16 v[144:147], a[176:179], a[0:3], v[144:147]\n"
"s_add_u32 s99, s99, 4096\n"
"v_mul_f32 v59, s55, v59\n"
"v_cvt_pk_bf16_f32 v48, v48, v49\n"
"v_mfma_f32_16x16x32_bf16 v[152:155], a[176:179], a[4:7], v[152:155]\n"
"s_cmp_eq_u32 s99, 20480\n"
"ds_read_b64_tr_b16 a[134:135], v243 offset:512\n"
"v_cvt_pk_bf16_f32 v49, v50, v51\n"
"v_mfma_f32_16x16x32_bf16 v[160:163], a[176:179], a[8:11], v[160:163]\n"
"s_cselect_b32 s99, 0, s99\n"
"buffer_load_dwordx4 a[108:111], v235, s[8:11], s59 offen offset:3072 sc1\n"
"v_mfma_f32_16x16x32_bf16 v[168:171], a[176:179], a[12:15], v[168:171]\n"
"ds_read_b64_tr_b16 a[136:137], v244 offset:0\n"
"v_cvt_pk_bf16_f32 v50, v56, v57\n"
"v_mfma_f32_16x16x32_bf16 v[148:151], a[180:183], a[0:3], v[148:151]\n"
"v_cvt_pk_bf16_f32 v51, v58, v59\n"
"v_mfma_f32_16x16x32_bf16 v[156:159], a[180:183], a[4:7], v[156:159]\n"
"ds_read_b64_tr_b16 a[138:139], v244 offset:512\n"
"v_mfma_f32_16x16x32_bf16 v[164:167], a[180:183], a[8:11], v[164:167]\n"
"v_mfma_f32_16x16x32_bf16 v[172:175], a[180:183], a[12:15], v[172:175]\n"
"ds_read_b64_tr_b16 a[140:141], v245 offset:0\n"
"v_add_u32 v235, s99, v224\n"
"v_mfma_f32_16x16x32_bf16 v[176:179], a[184:187], a[0:3], v[176:179]\n"
"buffer_load_dwordx4 a[112:115], v235, s[8:11], s56 offen offset:0 sc1\n"
"v_mfma_f32_16x16x32_bf16 v[184:187], a[184:187], a[4:7], v[184:187]\n"
"ds_read_b64_tr_b16 a[142:143], v245 offset:512\n"
"v_mfma_f32_16x16x32_bf16 v[192:195], a[184:187], a[8:11], v[192:195]\n"
"v_mfma_f32_16x16x32_bf16 v[200:203], a[184:187], a[12:15], v[200:203]\n"
"s_waitcnt lgkmcnt(3)\n"
"ds_read_b64_tr_b16 a[144:145], v242 offset:4096\n"
"v_mfma_f32_16x16x32_bf16 v[180:183], a[188:191], a[0:3], v[180:183]\n"
"v_mfma_f32_16x16x32_bf16 v[188:191], a[188:191], a[4:7], v[188:191]\n"
"ds_read_b64_tr_b16 a[146:147], v242 offset:4608\n"
"v_mfma_f32_16x16x32_bf16 v[196:199], a[188:191], a[8:11], v[196:199]\n"
"v_mfma_f32_16x16x32_bf16 v[204:207], a[188:191], a[12:15], v[204:207]\n"
"s_waitcnt vmcnt(35)\n"
"ds_read_b64_tr_b16 a[148:149], v243 offset:4096\n"
"v_mfma_f32_16x16x32_bf16 v[80:83], a[128:131], a[16:19], v[80:83]\n"
"s_waitcnt vmcnt(34)\n"
"buffer_load_dwordx4 a[116:119], v235, s[8:11], s57 offen offset:0 sc1\n"
"v_mfma_f32_16x16x32_bf16 v[88:91], a[128:131], a[20:23], v[88:91]\n"
"s_waitcnt vmcnt(33)\n"
"ds_read_b64_tr_b16 a[150:151], v243 offset:4608\n"
"v_mfma_f32_16x16x32_bf16 v[96:99], a[128:131], a[24:27], v[96:99]\n"
"s_waitcnt vmcnt(32)\n"
"ds_read_b64_tr_b16 a[152:153], v244 offset:4096\n"
"v_mfma_f32_16x16x32_bf16 v[104:107], a[128:131], a[28:31], v[104:107]\n"
"v_mfma_f32_16x16x32_bf16 v[84:87], a[132:135], a[16:19], v[84:87]\n"
"ds_read_b64_tr_b16 a[154:155], v244 offset:4608\n"
"v_mfma_f32_16x16x32_bf16 v[92:95], a[132:135], a[20:23], v[92:95]\n"
"v_mfma_f32_16x16x32_bf16 v[100:103], a[132:135], a[24:27], v[100:103]\n"
"ds_read_b64_tr_b16 a[156:157], v245 offset:4096\n"
"v_mfma_f32_16x16x32_bf16 v[108:111], a[132:135], a[28:31], v[108:111]\n"
"s_waitcnt lgkmcnt(4)\n"
"buffer_load_dwordx4 a[120:123], v235, s[8:11], s58 offen offset:0 sc1\n"
"v_mfma_f32_16x16x32_bf16 v[112:115], a[136:139], a[16:19], v[112:115]\n"
"ds_read_b64_tr_b16 a[158:159], v245 offset:4608\n"
"v_mfma_f32_16x16x32_bf16 v[120:123], a[136:139], a[20:23], v[120:123]\n"
"v_mfma_f32_16x16x32_bf16 v[128:131], a[136:139], a[24:27], v[128:131]\n"
"ds_read_b64_tr_b16 a[160:161], v242 offset:24576\n"
"v_mfma_f32_16x16x32_bf16 v[136:139], a[136:139], a[28:31], v[136:139]\n"
"v_mfma_f32_16x16x32_bf16 v[116:119], a[140:143], a[16:19], v[116:119]\n"
"ds_read_b64_tr_b16 a[162:163], v242 offset:25088\n"
"v_mfma_f32_16x16x32_bf16 v[124:127], a[140:143], a[20:23], v[124:127]\n"
"buffer_load_dwordx4 a[124:127], v235, s[8:11], s59 offen offset:0 sc1\n"
"v_mfma_f32_16x16x32_bf16 v[132:135], a[140:143], a[24:27], v[132:135]\n"
"ds_read_b64_tr_b16 a[164:165], v243 offset:24576\n"
"v_mfma_f32_16x16x32_bf16 v[140:143], a[140:143], a[28:31], v[140:143]\n"
"v_mfma_f32_16x16x32_bf16 v[144:147], a[144:147], a[16:19], v[144:147]\n"
"ds_read_b64_tr_b16 a[166:167], v243 offset:25088\n"
"v_mfma_f32_16x16x32_bf16 v[152:155], a[144:147], a[20:23], v[152:155]\n"
"v_mfma_f32_16x16x32_bf16 v[160:163], a[144:147], a[24:27], v[160:163]\n"
"ds_read_b64_tr_b16 a[168:169], v244 offset:24576\n"
"v_mfma_f32_16x16x32_bf16 v[168:171], a[144:147], a[28:31], v[168:171]\n"
"s_waitcnt lgkmcnt(3)\n"
"v_mfma_f32_16x16x32_bf16 v[148:151], a[148:151], a[16:19], v[148:151]\n"
"ds_read_b64_tr_b16 a[170:171], v244 offset:25088\n"
"v_mfma_f32_16x16x32_bf16 v[156:159], a[148:151], a[20:23], v[156:159]\n"
"buffer_load_dwordx4 a[0:3], v235, s[8:11], s56 offen offset:1024 sc1\n"
"v_mfma_f32_16x16x32_bf16 v[164:167], a[148:151], a[24:27], v[164:167]\n"
"ds_read_b64_tr_b16 a[172:173], v245 offset:24576\n"
"v_mfma_f32_16x16x32_bf16 v[172:175], a[148:151], a[28:31], v[172:175]\n"
"ds_read_b64_tr_b16 a[174:175], v245 offset:25088\n"
"v_mfma_f32_16x16x32_bf16 v[176:179], a[152:155], a[16:19], v[176:179]\n"
"v_mfma_f32_16x16x32_bf16 v[184:187], a[152:155], a[20:23], v[184:187]\n"
"ds_read_b64_tr_b16 a[176:177], v242 offset:28672\n"
"v_mfma_f32_16x16x32_bf16 v[192:195], a[152:155], a[24:27], v[192:195]\n"
"v_mfma_f32_16x16x32_bf16 v[200:203], a[152:155], a[28:31], v[200:203]\n"
"ds_read_b64_tr_b16 a[178:179], v242 offset:29184\n"
"v_mfma_f32_16x16x32_bf16 v[180:183], a[156:159], a[16:19], v[180:183]\n"
"s_mov_b32 s49, 270336\n"
"buffer_load_dwordx4 a[4:7], v235, s[8:11], s57 offen offset:1024 sc1\n"
"v_mfma_f32_16x16x32_bf16 v[188:191], a[156:159], a[20:23], v[188:191]\n"
"ds_read_b64_tr_b16 a[180:181], v243 offset:28672\n"
"v_mfma_f32_16x16x32_bf16 v[196:199], a[156:159], a[24:27], v[196:199]\n"
"v_mfma_f32_16x16x32_bf16 v[204:207], a[156:159], a[28:31], v[204:207]\n"
"ds_read_b64_tr_b16 a[182:183], v243 offset:29184\n"
"s_branch LQ_%=\n"
"LV_%=:\n"
"s_waitcnt vmcnt(26)\n"
"v_lshlrev_b32 v148, 16, v128\n"
"v_and_b32 v149, 0xffff0000, v128\n"
"v_lshlrev_b32 v150, 16, v129\n"
"v_and_b32 v151, 0xffff0000, v129\n"
"s_waitcnt vmcnt(25)\n"
"v_lshlrev_b32 v152, 16, v130\n"
"v_and_b32 v153, 0xffff0000, v130\n"
"v_lshlrev_b32 v154, 16, v131\n"
"v_and_b32 v155, 0xffff0000, v131\n"
"s_waitcnt vmcnt(24)\n"
"v_lshlrev_b32 v156, 16, v132\n"
"v_and_b32 v157, 0xffff0000, v132\n"
"v_lshlrev_b32 v158, 16, v133\n"
"v_and_b32 v159, 0xffff0000, v133\n"
"s_waitcnt vmcnt(23)\n"
"v_lshlrev_b32 v160, 16, v134\n"
"v_and_b32 v161, 0xffff0000, v134\n"
"v_lshlrev_b32 v162, 16, v135\n"
"v_and_b32 v163, 0xffff0000, v135\n"
"s_waitcnt vmcnt(22)\n"
"v_lshlrev_b32 v164, 16, v136\n"
"v_and_b32 v165, 0xffff0000, v136\n"
"v_lshlrev_b32 v166, 16, v137\n"
"v_and_b32 v167, 0xffff0000, v137\n"
"v_add_f32 v188, v148, v149\n"
"v_add_f32 v189, v150, v151\n"
"v_add_f32 v188, v188, v152\n"
"v_add_f32 v189, v189, v153\n"
"v_add_f32 v188, v188, v154\n"
"v_add_f32 v189, v189, v155\n"
"v_add_f32 v188, v188, v156\n"
"v_add_f32 v189, v189, v157\n"
"v_add_f32 v188, v188, v158\n"
"v_add_f32 v189, v189, v159\n"
"v_add_f32 v188, v188, v160\n"
"v_add_f32 v189, v189, v161\n"
"v_add_f32 v188, v188, v162\n"
"v_add_f32 v189, v189, v163\n"
"v_add_f32 v188, v188, v164\n"
"v_add_f32 v189, v189, v165\n"
"v_add_f32 v188, v188, v166\n"
"v_add_f32 v189, v189, v167\n"
"v_add_f32 v188, v188, v189\n"
"s_nop 1\n"
"v_add_f32_dpp v188, v188, v188 quad_perm:[1,0,3,2] row_mask:0xf bank_mask:0xf\n"
"s_waitcnt vmcnt(21)\n"
"v_lshlrev_b32 v168, 16, v138\n"
"v_and_b32 v169, 0xffff0000, v138\n"
"v_add_f32_dpp v188, v188, v188 quad_perm:[2,3,0,1] row_mask:0xf bank_mask:0xf\n"
"v_lshlrev_b32 v170, 16, v139\n"
"v_and_b32 v171, 0xffff0000, v139\n"
"v_add_f32_dpp v188, v188, v188 row_half_mirror row_mask:0xf bank_mask:0xf\n"
"s_waitcnt vmcnt(20)\n"
"v_lshlrev_b32 v172, 16, v140\n"
"v_and_b32 v173, 0xffff0000, v140\n"
"v_add_f32_dpp v188, v188, v188 row_mirror row_mask:0xf bank_mask:0xf\n"
"v_lshlrev_b32 v174, 16, v141\n"
"v_and_b32 v175, 0xffff0000, v141\n"
"v_mov_b32 v189, v188\n"
"s_waitcnt vmcnt(19)\n"
"v_lshlrev_b32 v176, 16, v142\n"
"v_and_b32 v177, 0xffff0000, v142\n"
"v_permlane16_swap_b32 v189, v188\n"
"v_lshlrev_b32 v178, 16, v143\n"
"v_and_b32 v179, 0xffff0000, v143\n"
"v_add_f32 v188, v188, v189\n"
"s_waitcnt vmcnt(18)\n"
"v_lshlrev_b32 v180, 16, v144\n"
"v_and_b32 v181, 0xffff0000, v144\n"
"v_mul_f32 v191, 0x3acccccd, v188\n"
"v_lshlrev_b32 v182, 16, v145\n"
"v_and_b32 v183, 0xffff0000, v145\n"
"v_cvt_pk_bf16_f32 v128, v191, v191\n"
"s_waitcnt vmcnt(17)\n"
"v_lshlrev_b32 v184, 16, v146\n"
"v_and_b32 v185, 0xffff0000, v146\n"
"v_lshlrev_b32 v128, 16, v128\n"
"v_lshlrev_b32 v186, 16, v147\n"
"v_and_b32 v187, 0xffff0000, v147\n"
"s_add_u32 m0, s36, 16384\n"
"s_add_u32 s48, s46, 128\n"
"s_and_b32 s48, s48, 1023\n"
"buffer_load_dwordx4 v234, s[12:15], s48 offen offset:0 lds\n"
"buffer_load_dwordx4 v235, s[12:15], s48 offen offset:1024 lds\n"
"buffer_load_dwordx4 v236, s[12:15], s48 offen offset:2048 lds\n"
"buffer_load_dwordx4 v237, s[12:15], s48 offen offset:3072 lds\n"
"v_sub_f32 v148, v148, v128\n"
"v_sub_f32 v149, v149, v128\n"
"v_sub_f32 v150, v150, v128\n"
"v_sub_f32 v151, v151, v128\n"
"v_sub_f32 v152, v152, v128\n"
"v_sub_f32 v153, v153, v128\n"
"v_sub_f32 v154, v154, v128\n"
"v_sub_f32 v155, v155, v128\n"
"v_sub_f32 v156, v156, v128\n"
"v_sub_f32 v157, v157, v128\n"
"v_sub_f32 v158, v158, v128\n"
"v_sub_f32 v159, v159, v128\n"
"v_sub_f32 v160, v160, v128\n"
"v_sub_f32 v161, v161, v128\n"
"v_sub_f32 v162, v162, v128\n"
"v_sub_f32 v163, v163, v128\n"
"v_sub_f32 v164, v164, v128\n"
"v_sub_f32 v165, v165, v128\n"
"v_sub_f32 v166, v166, v128\n"
"v_sub_f32 v167, v167, v128\n"
"v_mul_f32 v188, v148, v148\n"
"v_mul_f32 v189, v149, v149\n"
"v_fmac_f32 v188, v150, v150\n"
"v_fmac_f32 v189, v151, v151\n"
"v_fmac_f32 v188, v152, v152\n"
"v_fmac_f32 v189, v153, v153\n"
"v_fmac_f32 v188, v154, v154\n"
"v_fmac_f32 v189, v155, v155\n"
"v_fmac_f32 v188, v156, v156\n"
"v_fmac_f32 v189, v157, v157\n"
"v_fmac_f32 v188, v158, v158\n"
"v_fmac_f32 v189, v159, v159\n"
"v_fmac_f32 v188, v160, v160\n"
"v_fmac_f32 v189, v161, v161\n"
"v_fmac_f32 v188, v162, v162\n"
"v_fmac_f32 v189, v163, v163\n"
"v_fmac_f32 v188, v164, v164\n"
"v_fmac_f32 v189, v165, v165\n"
"v_fmac_f32 v188, v166, v166\n"
"v_fmac_f32 v189, v167, v167\n"
"v_add_f32 v188, v188, v189\n"
"v_cvt_pk_bf16_f32 v149, v148, v149\n"
"v_lshlrev_b32 v148, 16, v149\n"
"v_add_f32_dpp v188, v188, v188 quad_perm:[1,0,3,2] row_mask:0xf bank_mask:0xf\n"
"v_and_b32 v149, 0xffff0000, v149\n"
"v_cvt_pk_bf16_f32 v151, v150, v151\n"
"v_add_f32_dpp v188, v188, v188 quad_perm:[2,3,0,1] row_mask:0xf bank_mask:0xf\n"
"v_lshlrev_b32 v150, 16, v151\n"
"v_and_b32 v151, 0xffff0000, v151\n"
"v_add_f32_dpp v188, v188, v188 row_half_mirror row_mask:0xf bank_mask:0xf\n"
"v_cvt_pk_bf16_f32 v153, v152, v153\n"
"v_lshlrev_b32 v152, 16, v153\n"
"v_add_f32_dpp v188, v188, v188 row_mirror row_mask:0xf bank_mask:0xf\n"
"v_and_b32 v153, 0xffff0000, v153\n"
"v_cvt_pk_bf16_f32 v155, v154, v155\n"
"v_mov_b32 v189, v188\n"
"v_lshlrev_b32 v154, 16, v155\n"
"v_and_b32 v155, 0xffff0000, v155\n"
"v_permlane16_swap_b32 v189, v188\n"
"v_cvt_pk_bf16_f32 v157, v156, v157\n"
"v_lshlrev_b32 v156, 16, v157\n"
"v_add_f32 v188, v188, v189\n"
"v_and_b32 v157, 0xffff0000, v157\n"
"v_cvt_pk_bf16_f32 v159, v158, v159\n"
"v_sub_f32 v191, v191, v128\n"
"v_lshlrev_b32 v158, 16, v159\n"
"v_and_b32 v159, 0xffff0000, v159\n"
"v_mul_f32 v188, 0x3acccccd, v188\n"
"v_cvt_pk_bf16_f32 v161, v160, v161\n"
"s_add_u32 m0, s37, 12288\n"
"s_add_u32 s48, s47, 786432\n"
"s_and_b32 s48, s48, 4194303\n"
"buffer_load_dwordx4 v238, s[16:19], s48 offen offset:0 lds\n"
"buffer_load_dwordx4 v239, s[16:19], s48 offen offset:1024 lds\n"
"buffer_load_dwordx4 v240, s[16:19], s48 offen offset:2048 lds\n"
"buffer_load_dwordx4 v241, s[16:19], s48 offen offset:3072 lds\n"
"v_lshlrev_b32 v160, 16, v161\n"
"v_fma_f32 v188, -v191, v191, v188\n"
"v_and_b32 v161, 0xffff0000, v161\n"
"v_cvt_pk_bf16_f32 v163, v162, v163\n"
"v_max_f32 v188, 0, v188\n"
"v_lshlrev_b32 v162, 16, v163\n"
"v_and_b32 v163, 0xffff0000, v163\n"
"v_cvt_pk_bf16_f32 v188, v188, v188\n"
"v_cvt_pk_bf16_f32 v165, v164, v165\n"
"v_lshlrev_b32 v164, 16, v165\n"
"v_lshlrev_b32 v188, 16, v188\n"
"v_and_b32 v165, 0xffff0000, v165\n"
"v_cvt_pk_bf16_f32 v167, v166, v167\n"
"v_add_f32 v188, 0x3727c5ac, v188\n"
"v_lshlrev_b32 v166, 16, v167\n"
"v_and_b32 v167, 0xffff0000, v167\n"
"v_cvt_pk_bf16_f32 v188, v188, v188\n"
"v_bfe_u32 v130, v224, 5, 4\n"
"v_mul_u32_u24 v130, 144, v130\n"
"v_lshlrev_b32 v188, 16, v188\n"
"v_bfe_u32 v131, v224, 4, 1\n"
"v_lshl_add_u32 v130, v131, 3, v130\n"
"v_sqrt_f32 v189, v188\n"
"v_lshrrev_b32 v131, 9, v224\n"
"v_lshl_add_u32 v130, v131, 4, v130\n"
"v_rcp_f32 v190, v189\n"
"s_lshl_b32 s52, s30, 5\n"
"v_add_u32 v130, s52, v130\n"
"v_fma_f32 v188, -v189, v189, v188\n"
"v_mul_f32 v190, 0.5, v190\n"
"v_fma_f32 v189, v188, v190, v189\n"
"v_cvt_pk_bf16_f32 v189, v189, v189\n"
"v_lshlrev_b32 v189, 16, v189\n"
"v_rcp_f32 v129, v189\n"
"s_nop 0\n"
"v_mul_f32 v148, v148, v129\n"
"v_mul_f32 v149, v149, v129\n"
"v_cvt_pk_bf16_f32 v138, v148, v149\n"
"v_mul_f32 v150, v150, v129\n"
"v_mul_f32 v151, v151, v129\n"
"v_cvt_pk_bf16_f32 v139, v150, v151\n"
"ds_write_b64 v130, v[138:139] offset:49152\n"
"v_mul_f32 v152, v152, v129\n"
"v_mul_f32 v153, v153, v129\n"
"v_cvt_pk_bf16_f32 v140, v152, v153\n"
"v_mul_f32 v154, v154, v129\n"
"v_mul_f32 v155, v155, v129\n"
"v_cvt_pk_bf16_f32 v141, v154, v155\n"
"ds_write_b64 v130, v[140:141] offset:51456\n"
"v_mul_f32 v156, v156, v129\n"
"v_mul_f32 v157, v157, v129\n"
"v_cvt_pk_bf16_f32 v142, v156, v157\n"
"v_mul_f32 v158, v158, v129\n"
"v_mul_f32 v159, v159, v129\n"
"v_cvt_pk_bf16_f32 v143, v158, v159\n"
"ds_write_b64 v130, v[142:143] offset:53760\n"
"v_mul_f32 v160, v160, v129\n"
"v_mul_f32 v161, v161, v129\n"
"v_cvt_pk_bf16_f32 v144, v160, v161\n"
"v_mul_f32 v162, v162, v129\n"
"v_mul_f32 v163, v163, v129\n"
"v_cvt_pk_bf16_f32 v145, v162, v163\n"
"ds_write_b64 v130, v[144:145] offset:56064\n"
"v_mul_f32 v164, v164, v129\n"
"v_mul_f32 v165, v165, v129\n"
"v_cvt_pk_bf16_f32 v146, v164, v165\n"
"v_mul_f32 v166, v166, v129\n"
"v_mul_f32 v167, v167, v129\n"
"v_cvt_pk_bf16_f32 v147, v166, v167\n"
"ds_write_b64 v130, v[146:147] offset:58368\n"
"s_waitcnt vmcnt(21)\n"
"ds_read_b64_tr_b16 a[0:1], v246 offset:0\n"
"ds_read_b64_tr_b16 a[2:3], v246 offset:512\n"
"ds_read_b64_tr_b16 a[4:5], v247 offset:0\n"
"ds_read_b64_tr_b16 a[6:7], v247 offset:512\n"
"ds_read_b64_tr_b16 a[8:9], v248 offset:0\n"
"ds_read_b64_tr_b16 a[10:11], v248 offset:512\n"
"ds_read_b64_tr_b16 a[12:13], v249 offset:0\n"
"ds_read_b64_tr_b16 a[14:15], v249 offset:512\n"
"s_waitcnt vmcnt(12)\n"
"ds_read_b64_tr_b16 a[16:17], v246 offset:4096\n"
"ds_read_b64_tr_b16 a[18:19], v246 offset:4608\n"
"s_waitcnt lgkmcnt(10)\n"
"s_barrier\n"
"ds_read_b128 v[192:195], v242 offset:0\n"
"ds_read_b128 v[196:199], v242 offset:2048\n"
"ds_read_b128 v[200:203], v242 offset:4096\n"
"ds_read_b128 v[204:207], v242 offset:6144\n"
"ds_read_b128 v[208:211], v242 offset:8192\n"
"v_lshrrev_b32 v131, 7, v224\n"
"s_waitcnt lgkmcnt(4)\n"
"v_mfma_f32_16x16x32_bf16 v[0:3], v[192:195], a[0:3], 0\n"
"s_add_u32 m0, s37, 16384\n"
"ds_read_b128 v[212:215], v242 offset:10240\n"
"v_lshl_add_u32 v131, v131, 4, v224\n"
"v_mfma_f32_16x16x32_bf16 v[8:11], v[192:195], a[4:7], 0\n"
"s_add_u32 s48, s47, 1048576\n"
"s_and_b32 s48, s48, 4194303\n"
"s_mul_i32 s52, s30, 1152\n"
"v_mfma_f32_16x16x32_bf16 v[16:19], v[192:195], a[8:11], 0\n"
"ds_read_b64_tr_b16 a[20:21], v247 offset:4096\n"
"v_add_u32 v128, s52, v131\n"
"v_mfma_f32_16x16x32_bf16 v[24:27], v[192:195], a[12:15], 0\n"
"s_waitcnt lgkmcnt(5)\n"
"buffer_load_dwordx4 v238, s[16:19], s48 offen offset:0 lds\n"
"v_mfma_f32_16x16x32_bf16 v[4:7], v[196:199], a[0:3], 0\n"
"s_and_b32 s52, s30, 1\n"
"ds_read_b128 v[216:219], v242 offset:12288\n"
"v_lshlrev_b32 v148, 16, v138\n"
"v_mfma_f32_16x16x32_bf16 v[12:15], v[196:199], a[4:7], 0\n"
"s_mul_i32 s51, s52, 1152\n"
"v_and_b32 v149, 0xffff0000, v138\n"
"v_add_u32 v131, s51, v131\n"
"v_mfma_f32_16x16x32_bf16 v[20:23], v[196:199], a[8:11], 0\n"
"s_lshl_b32 s52, s52, 10\n"
"ds_read_b64_tr_b16 a[22:23], v247 offset:4608\n"
"v_mul_f32 v188, v148, v168\n"
"v_mfma_f32_16x16x32_bf16 v[28:31], v[196:199], a[12:15], 0\n"
"s_waitcnt lgkmcnt(3)\n"
"buffer_load_dwordx4 v239, s[16:19], s48 offen offset:1024 lds\n"
"v_mfma_f32_16x16x32_bf16 v[32:35], v[200:203], a[0:3], 0\n"
"ds_read_b128 v[220:223], v242 offset:14336\n"
"v_and_b32 v129, 0x70, v224\n"
"v_mfma_f32_16x16x32_bf16 v[40:43], v[200:203], a[4:7], 0\n"
"s_lshr_b32 s50, s31, 1\n"
"ds_read_b128 v[132:135], v128 offset:49152\n"
"v_mul_f32 v189, v149, v169\n"
"v_mfma_f32_16x16x32_bf16 v[48:51], v[200:203], a[8:11], 0\n"
"s_mul_i32 s50, s50, 20480\n"
"ds_read_b128 a[240:243], v128 offset:53760\n"
"v_and_b32 v130, 0x380, v224\n"
"v_mfma_f32_16x16x32_bf16 v[56:59], v[200:203], a[12:15], 0\n"
"s_and_b32 s51, s31, 1\n"
"ds_read_b128 a[244:247], v131 offset:58368\n"
"v_lshlrev_b32 v150, 16, v139\n"
"v_mfma_f32_16x16x32_bf16 v[36:39], v[204:207], a[0:3], 0\n"
"s_lshl_b32 s51, s51, 7\n"
"ds_read_b128 v[192:195], v243 offset:0\n"
"v_lshl_add_u32 v129, v130, 1, v129\n"
"v_mfma_f32_16x16x32_bf16 v[44:47], v[204:207], a[4:7], 0\n"
"s_add_u32 s50, s50, s51\n"
"ds_read_b64_tr_b16 a[24:25], v248 offset:4096\n"
"v_and_b32 v151, 0xffff0000, v139\n"
"v_mfma_f32_16x16x32_bf16 v[52:55], v[204:207], a[8:11], 0\n"
"s_add_u32 s50, s50, 65536\n"
"buffer_load_dwordx4 v240, s[16:19], s48 offen offset:2048 lds\n"
"v_mfma_f32_16x16x32_bf16 v[60:63], v[204:207], a[12:15], 0\n"
"s_lshl_b32 s52, s52, 1\n"
"ds_read_b128 v[196:199], v243 offset:2048\n"
"v_fmac_f32 v188, v150, v170\n"
"v_mfma_f32_16x16x32_bf16 v[64:67], v[208:211], a[0:3], 0\n"
"s_add_u32 s52, s52, s50\n"
"ds_read_b64_tr_b16 a[26:27], v248 offset:4608\n"
"v_fmac_f32 v189, v151, v171\n"
"v_mfma_f32_16x16x32_bf16 v[72:75], v[208:211], a[4:7], 0\n"
"s_add_u32 s52, s52, 16384\n"
"buffer_load_dwordx4 v241, s[16:19], s48 offen offset:3072 lds\n"
"v_mfma_f32_16x16x32_bf16 v[80:83], v[208:211], a[8:11], 0\n"
"s_add_u32 m0, s36, 32768\n"
"ds_read_b128 v[200:203], v243 offset:4096\n"
"v_lshlrev_b32 v152, 16, v140\n"
"v_mfma_f32_16x16x32_bf16 v[88:91], v[208:211], a[12:15], 0\n"
"s_add_u32 s48, s46, 256\n"
"ds_read_b64_tr_b16 a[28:29], v249 offset:4096\n"
"v_and_b32 v153, 0xffff0000, v140\n"
"v_mfma_f32_16x16x32_bf16 v[68:71], v[212:215], a[0:3], 0\n"
"s_and_b32 s48, s48, 1023\n"
"v_fmac_f32 v188, v152, v172\n"
"v_fmac_f32 v189, v153, v173\n"
"v_mfma_f32_16x16x32_bf16 v[76:79], v[212:215], a[4:7], 0\n"
"s_lshl_b32 s51, s30, 11\n"
"buffer_load_dwordx4 v234, s[12:15], s48 offen offset:0 lds\n"
"v_mfma_f32_16x16x32_bf16 v[84:87], v[212:215], a[8:11], 0\n"
"s_add_u32 s50, s50, s51\n"
"ds_read_b128 v[204:207], v243 offset:6144\n"
"v_lshlrev_b32 v154, 16, v141\n"
"v_mfma_f32_16x16x32_bf16 v[92:95], v[212:215], a[12:15], 0\n"
"s_waitcnt lgkmcnt(3)\n"
"ds_read_b64_tr_b16 a[30:31], v249 offset:4608\n"
"v_and_b32 v155, 0xffff0000, v141\n"
"v_mfma_f32_16x16x32_bf16 v[96:99], v[216:219], a[0:3], 0\n"
"s_add_u32 s51, s50, 8192\n"
"buffer_store_dwordx4 v[132:135], v129, s[8:11], s50 offen sc1\n"
"v_mfma_f32_16x16x32_bf16 v[104:107], v[216:219], a[4:7], 0\n"
"ds_read_b128 v[208:211], v243 offset:8192\n"
"v_fmac_f32 v188, v154, v174\n"
"v_mfma_f32_16x16x32_bf16 v[112:115], v[216:219], a[8:11], 0\n"
"buffer_load_dwordx4 v235, s[12:15], s48 offen offset:1024 lds\n"
"v_mfma_f32_16x16x32_bf16 v[120:123], v[216:219], a[12:15], 0\n"
"buffer_store_dwordx4 a[240:243], v129, s[8:11], s51 offen sc1\n"
"v_mfma_f32_16x16x32_bf16 v[100:103], v[220:223], a[0:3], 0\n"
"s_waitcnt vmcnt(16)\n"
"ds_read_b64_tr_b16 a[32:33], v246 offset:8192\n"
"v_fmac_f32 v189, v155, v175\n"
"v_mfma_f32_16x16x32_bf16 v[108:111], v[220:223], a[4:7], 0\n"
"buffer_store_dwordx4 a[244:247], v129, s[8:11], s52 offen sc1\n"
"v_mfma_f32_16x16x32_bf16 v[116:119], v[220:223], a[8:11], 0\n"
"ds_read_b128 v[212:215], v243 offset:10240\n"
"v_lshlrev_b32 v156, 16, v142\n"
"v_mfma_f32_16x16x32_bf16 v[124:127], v[220:223], a[12:15], 0\n"
"buffer_load_dwordx4 v236, s[12:15], s48 offen offset:2048 lds\n"
"v_mfma_f32_16x16x32_bf16 v[0:3], v[192:195], a[16:19], v[0:3]\n"
"ds_read_b64_tr_b16 a[34:35], v246 offset:8704\n"
"v_and_b32 v157, 0xffff0000, v142\n"
"v_mfma_f32_16x16x32_bf16 v[8:11], v[192:195], a[20:23], v[8:11]\n"
"ds_read_b128 v[216:219], v243 offset:12288\n"
"v_fmac_f32 v188, v156, v176\n"
"v_mfma_f32_16x16x32_bf16 v[16:19], v[192:195], a[24:27], v[16:19]\n"
"s_waitcnt lgkmcnt(4)\n"
"buffer_load_dwordx4 v237, s[12:15], s48 offen offset:3072 lds\n"
"v_mfma_f32_16x16x32_bf16 v[24:27], v[192:195], a[28:31], v[24:27]\n"
"ds_read_b64_tr_b16 a[36:37], v247 offset:8192\n"
"v_fmac_f32 v189, v157, v177\n"
"v_mfma_f32_16x16x32_bf16 v[4:7], v[196:199], a[16:19], v[4:7]\n"
"s_add_u32 m0, s37, 20480\n"
"ds_read_b128 v[220:223], v243 offset:14336\n"
"v_lshlrev_b32 v158, 16, v143\n"
"v_mfma_f32_16x16x32_bf16 v[12:15], v[196:199], a[20:23], v[12:15]\n"
"s_add_u32 s48, s47, 1310720\n"
"v_and_b32 v159, 0xffff0000, v143\n"
"v_fmac_f32 v188, v158, v178\n"
"v_mfma_f32_16x16x32_bf16 v[20:23], v[196:199], a[24:27], v[20:23]\n"
"s_and_b32 s48, s48, 4194303\n"
"ds_read_b64_tr_b16 a[38:39], v247 offset:8704\n"
"v_fmac_f32 v189, v159, v179\n"
"v_mfma_f32_16x16x32_bf16 v[28:31], v[196:199], a[28:31], v[28:31]\n"
"buffer_load_dwordx4 v238, s[16:19], s48 offen offset:0 lds\n"
"v_mfma_f32_16x16x32_bf16 v[32:35], v[200:203], a[16:19], v[32:35]\n"
"v_lshlrev_b32 v160, 16, v144\n"
"v_and_b32 v161, 0xffff0000, v144\n"
"v_mfma_f32_16x16x32_bf16 v[40:43], v[200:203], a[20:23], v[40:43]\n"
"v_fmac_f32 v188, v160, v180\n"
"v_fmac_f32 v189, v161, v181\n"
"v_mfma_f32_16x16x32_bf16 v[48:51], v[200:203], a[24:27], v[48:51]\n"
"ds_read_b64_tr_b16 a[40:41], v248 offset:8192\n"
"v_lshlrev_b32 v162, 16, v145\n"
"v_mfma_f32_16x16x32_bf16 v[56:59], v[200:203], a[28:31], v[56:59]\n"
"buffer_load_dwordx4 v239, s[16:19], s48 offen offset:1024 lds\n"
"v_mfma_f32_16x16x32_bf16 v[36:39], v[204:207], a[16:19], v[36:39]\n"
"v_and_b32 v163, 0xffff0000, v145\n"
"v_fmac_f32 v188, v162, v182\n"
"v_mfma_f32_16x16x32_bf16 v[44:47], v[204:207], a[20:23], v[44:47]\n"
"s_waitcnt vmcnt(17) lgkmcnt(2)\n"
"s_barrier\n"
"v_mfma_f32_16x16x32_bf16 v[52:55], v[204:207], a[24:27], v[52:55]\n"
"ds_read_b128 v[192:195], v242 offset:16384\n"
"v_fmac_f32 v189, v163, v183\n"
"v_mfma_f32_16x16x32_bf16 v[60:63], v[204:207], a[28:31], v[60:63]\n"
"ds_read_b64_tr_b16 a[42:43], v248 offset:8704\n"
"v_lshlrev_b32 v164, 16, v146\n"
"v_mfma_f32_16x16x32_bf16 v[64:67], v[208:211], a[16:19], v[64:67]\n"
"buffer_load_dwordx4 v240, s[16:19], s48 offen offset:2048 lds\n"
"v_mfma_f32_16x16x32_bf16 v[72:75], v[208:211], a[20:23], v[72:75]\n"
"v_and_b32 v165, 0xffff0000, v146\n"
"v_fmac_f32 v188, v164, v184\n"
"v_mfma_f32_16x16x32_bf16 v[80:83], v[208:211], a[24:27], v[80:83]\n"
"ds_read_b128 v[196:199], v242 offset:18432\n"
"v_fmac_f32 v189, v165, v185\n"
"v_mfma_f32_16x16x32_bf16 v[88:91], v[208:211], a[28:31], v[88:91]\n"
"ds_read_b64_tr_b16 a[44:45], v249 offset:8192\n"
"v_lshlrev_b32 v166, 16, v147\n"
"v_mfma_f32_16x16x32_bf16 v[68:71], v[212:215], a[16:19], v[68:71]\n"
"s_lshl_b32 s90, s31, 2\n"
"buffer_load_dwordx4 v241, s[16:19], s48 offen offset:3072 lds\n"
"v_mfma_f32_16x16x32_bf16 v[76:79], v[212:215], a[20:23], v[76:79]\n"
"s_add_u32 s90, s90, s30\n"
"ds_read_b128 v[200:203], v242 offset:20480\n"
"v_and_b32 v167, 0xffff0000, v147\n"
"v_mfma_f32_16x16x32_bf16 v[84:87], v[212:215], a[24:27], v[84:87]\n"
"s_add_u32 m0, s37, 0\n"
"ds_read_b64_tr_b16 a[46:47], v249 offset:8704\n"
"v_fmac_f32 v188, v166, v186\n"
"v_mfma_f32_16x16x32_bf16 v[92:95], v[212:215], a[28:31], v[92:95]\n"
"s_add_u32 s48, s47, 1572864\n"
"v_fmac_f32 v189, v167, v187\n"
"v_add_f32 v188, v188, v189\n"
"v_mfma_f32_16x16x32_bf16 v[96:99], v[216:219], a[16:19], v[96:99]\n"
"s_and_b32 s48, s48, 4194303\n"
"v_add_f32_dpp v188, v188, v188 quad_perm:[1,0,3,2] row_mask:0xf bank_mask:0xf\n"
"s_lshl_b32 s90, s90, 1\n"
"v_mfma_f32_16x16x32_bf16 v[104:107], v[216:219], a[20:23], v[104:107]\n"
"s_sub_u32 s91, 2047, s90\n"
"buffer_load_dwordx4 v238, s[16:19], s48 offen offset:0 lds\n"
"v_mfma_f32_16x16x32_bf16 v[112:115], v[216:219], a[24:27], v[112:115]\n"
"ds_read_b128 v[204:207], v242 offset:22528\n"
"v_add_f32_dpp v188, v188, v188 quad_perm:[2,3,0,1] row_mask:0xf bank_mask:0xf\n"
"v_mfma_f32_16x16x32_bf16 v[120:123], v[216:219], a[28:31], v[120:123]\n"
"s_waitcnt vmcnt(16)\n"
"ds_read_b64_tr_b16 a[48:49], v246 offset:12288\n"
"v_add_f32_dpp v188, v188, v188 row_half_mirror row_mask:0xf bank_mask:0xf\n"
"v_mfma_f32_16x16x32_bf16 v[100:103], v[220:223], a[16:19], v[100:103]\n"
"ds_read_b128 v[208:211], v242 offset:24576\n"
"v_add_f32_dpp v188, v188, v188 row_mirror row_mask:0xf bank_mask:0xf\n"
"v_mfma_f32_16x16x32_bf16 v[108:111], v[220:223], a[20:23], v[108:111]\n"
"s_lshl_b32 s90, s90, 2\n"
"buffer_load_dwordx4 v239, s[16:19], s48 offen offset:1024 lds\n"
"v_mfma_f32_16x16x32_bf16 v[116:119], v[220:223], a[24:27], v[116:119]\n"
"s_add_u32 s90, s90, 2686976\n"
"ds_read_b64_tr_b16 a[50:51], v246 offset:12800\n"
"v_mov_b32 v189, v188\n"
"v_mfma_f32_16x16x32_bf16 v[124:127], v[220:223], a[28:31], v[124:127]\n"
"s_waitcnt lgkmcnt(4)\n"
"v_permlane16_swap_b32 v189, v188\n"
"v_add_f32 v188, v188, v189\n"
"v_mfma_f32_16x16x32_bf16 v[0:3], v[192:195], a[32:35], v[0:3]\n"
"ds_read_b128 v[212:215], v242 offset:26624\n"
"v_cvt_pk_bf16_f32 v188, v188, v188\n"
"v_mfma_f32_16x16x32_bf16 v[8:11], v[192:195], a[36:39], v[8:11]\n"
"buffer_load_dwordx4 v240, s[16:19], s48 offen offset:2048 lds\n"
"v_mfma_f32_16x16x32_bf16 v[16:19], v[192:195], a[40:43], v[16:19]\n"
"ds_read_b64_tr_b16 a[52:53], v247 offset:12288\n"
"v_lshlrev_b32 v188, 16, v188\n"
"v_mfma_f32_16x16x32_bf16 v[24:27], v[192:195], a[44:47], v[24:27]\n"
"ds_read_b128 v[216:219], v242 offset:28672\n"
"v_ashrrev_i32 v189, 31, v188\n"
"v_mfma_f32_16x16x32_bf16 v[4:7], v[196:199], a[32:35], v[4:7]\n"
"v_or_b32 v189, 0x80000000, v189\n"
"v_xor_b32 v188, v188, v189\n"
"v_mfma_f32_16x16x32_bf16 v[12:15], v[196:199], a[36:39], v[12:15]\n"
"buffer_load_dwordx4 v241, s[16:19], s48 offen offset:3072 lds\n"
"v_mfma_f32_16x16x32_bf16 v[20:23], v[196:199], a[40:43], v[20:23]\n"
"ds_read_b64_tr_b16 a[54:55], v247 offset:12800\n"
"v_and_b32 v188, 0xffff0000, v188\n"
"v_mfma_f32_16x16x32_bf16 v[28:31], v[196:199], a[44:47], v[28:31]\n"
"ds_read_b128 v[220:223], v242 offset:30720\n"
"v_lshrrev_b32 v189, 9, v224\n"
"v_mfma_f32_16x16x32_bf16 v[32:35], v[200:203], a[32:35], v[32:35]\n"
"s_add_u32 m0, s36, 0\n"
"v_sub_u32 v190, s91, v189\n"
"v_or_b32 v188, v188, v190\n"
"v_mfma_f32_16x16x32_bf16 v[40:43], v[200:203], a[36:39], v[40:43]\n"
"s_add_u32 s48, s46, 384\n"
"v_and_b32 v190, 0x1f0, v224\n"
"v_lshlrev_b32 v190, 23, v190\n"
"v_mfma_f32_16x16x32_bf16 v[48:51], v[200:203], a[40:43], v[48:51]\n"
"s_and_b32 s48, s48, 1023\n"
"ds_read_b128 v[192:195], v243 offset:16384\n"
"v_lshl_add_u32 v190, v189, 2, v190\n"
"v_mfma_f32_16x16x32_bf16 v[56:59], v[200:203], a[44:47], v[56:59]\n"
"s_waitcnt lgkmcnt(3)\n"
"buffer_store_dword v188, v190, s[8:11], s90 offen sc1\n"
"v_mfma_f32_16x16x32_bf16 v[36:39], v[204:207], a[32:35], v[36:39]\n"
"ds_read_b64_tr_b16 a[56:57], v248 offset:12288\n"
"v_mfma_f32_16x16x32_bf16 v[44:47], v[204:207], a[36:39], v[44:47]\n"
"buffer_load_dwordx4 v234, s[12:15], s48 offen offset:0 lds\n"
"v_mfma_f32_16x16x32_bf16 v[52:55], v[204:207], a[40:43], v[52:55]\n"
"ds_read_b128 v[196:199], v243 offset:18432\n"
"v_mfma_f32_16x16x32_bf16 v[60:63], v[204:207], a[44:47], v[60:63]\n"
"ds_read_b64_tr_b16 a[58:59], v248 offset:12800\n"
"v_mfma_f32_16x16x32_bf16 v[64:67], v[208:211], a[32:35], v[64:67]\n"
"buffer_load_dwordx4 v235, s[12:15], s48 offen offset:1024 lds\n"
"v_mfma_f32_16x16x32_bf16 v[72:75], v[208:211], a[36:39], v[72:75]\n"
"ds_read_b128 v[200:203], v243 offset:20480\n"
"v_mfma_f32_16x16x32_bf16 v[80:83], v[208:211], a[40:43], v[80:83]\n"
"ds_read_b64_tr_b16 a[60:61], v249 offset:12288\n"
"v_mfma_f32_16x16x32_bf16 v[88:91], v[208:211], a[44:47], v[88:91]\n"
"buffer_load_dwordx4 v236, s[12:15], s48 offen offset:2048 lds\n"
"v_mfma_f32_16x16x32_bf16 v[68:71], v[212:215], a[32:35], v[68:71]\n"
"v_mfma_f32_16x16x32_bf16 v[76:79], v[212:215], a[36:39], v[76:79]\n"
"ds_read_b128 v[204:207], v243 offset:22528\n"
"v_mfma_f32_16x16x32_bf16 v[84:87], v[212:215], a[40:43], v[84:87]\n"
"ds_read_b64_tr_b16 a[62:63], v249 offset:12800\n"
"v_mfma_f32_16x16x32_bf16 v[92:95], v[212:215], a[44:47], v[92:95]\n"
"buffer_load_dwordx4 v237, s[12:15], s48 offen offset:3072 lds\n"
"v_mfma_f32_16x16x32_bf16 v[96:99], v[216:219], a[32:35], v[96:99]\n"
"ds_read_b128 v[208:211], v243 offset:24576\n"
"v_mfma_f32_16x16x32_bf16 v[104:107], v[216:219], a[36:39], v[104:107]\n"
"v_mfma_f32_16x16x32_bf16 v[112:115], v[216:219], a[40:43], v[112:115]\n"
"s_add_u32 m0, s37, 4096\n"
"s_waitcnt vmcnt(20)\n"
"ds_read_b64_tr_b16 a[64:65], v246 offset:16384\n"
"v_mfma_f32_16x16x32_bf16 v[120:123], v[216:219], a[44:47], v[120:123]\n"
"s_waitcnt lgkmcnt(4)\n"
"s_add_u32 s48, s47, 1835008\n"
"s_and_b32 s48, s48, 4194303\n"
"v_mfma_f32_16x16x32_bf16 v[100:103], v[220:223], a[32:35], v[100:103]\n"
"ds_read_b128 v[212:215], v243 offset:26624\n"
"v_mfma_f32_16x16x32_bf16 v[108:111], v[220:223], a[36:39], v[108:111]\n"
"buffer_load_dwordx4 v238, s[16:19], s48 offen offset:0 lds\n"
"v_mfma_f32_16x16x32_bf16 v[116:119], v[220:223], a[40:43], v[116:119]\n"
"ds_read_b64_tr_b16 a[66:67], v246 offset:16896\n"
"v_mfma_f32_16x16x32_bf16 v[124:127], v[220:223], a[44:47], v[124:127]\n"
"ds_read_b128 v[216:219], v243 offset:28672\n"
"v_mfma_f32_16x16x32_bf16 v[0:3], v[192:195], a[48:51], v[0:3]\n"
"buffer_load_dwordx4 v239, s[16:19], s48 offen offset:1024 lds\n"
"v_mfma_f32_16x16x32_bf16 v[8:11], v[192:195], a[52:55], v[8:11]\n"
"v_mfma_f32_16x16x32_bf16 v[16:19], v[192:195], a[56:59], v[16:19]\n"
"s_waitcnt lgkmcnt(3)\n"
"ds_read_b64_tr_b16 a[68:69], v247 offset:16384\n"
"v_mfma_f32_16x16x32_bf16 v[24:27], v[192:195], a[60:63], v[24:27]\n"
"ds_read_b128 v[220:223], v243 offset:30720\n"
"v_mfma_f32_16x16x32_bf16 v[4:7], v[196:199], a[48:51], v[4:7]\n"
"v_mfma_f32_16x16x32_bf16 v[12:15], v[196:199], a[52:55], v[12:15]\n"
"buffer_load_dwordx4 v240, s[16:19], s48 offen offset:2048 lds\n"
"v_mfma_f32_16x16x32_bf16 v[20:23], v[196:199], a[56:59], v[20:23]\n"
"ds_read_b64_tr_b16 a[70:71], v247 offset:16896\n"
"v_mfma_f32_16x16x32_bf16 v[28:31], v[196:199], a[60:63], v[28:31]\n"
"v_mfma_f32_16x16x32_bf16 v[32:35], v[200:203], a[48:51], v[32:35]\n"
"v_mfma_f32_16x16x32_bf16 v[40:43], v[200:203], a[52:55], v[40:43]\n"
"buffer_load_dwordx4 v241, s[16:19], s48 offen offset:3072 lds\n"
"v_mfma_f32_16x16x32_bf16 v[48:51], v[200:203], a[56:59], v[48:51]\n"
"ds_read_b64_tr_b16 a[72:73], v248 offset:16384\n"
"v_mfma_f32_16x16x32_bf16 v[56:59], v[200:203], a[60:63], v[56:59]\n"
"v_mfma_f32_16x16x32_bf16 v[36:39], v[204:207], a[48:51], v[36:39]\n"
"s_add_u32 m0, s37, 8192\n"
"s_add_u32 s48, s47, 2097152\n"
"s_and_b32 s48, s48, 4194303\n"
"v_mfma_f32_16x16x32_bf16 v[44:47], v[204:207], a[52:55], v[44:47]\n"
"s_waitcnt vmcnt(17) lgkmcnt(2)\n"
"s_barrier\n"
"v_mfma_f32_16x16x32_bf16 v[52:55], v[204:207], a[56:59], v[52:55]\n"
"ds_read_b128 v[192:195], v242 offset:32768\n"
"v_mfma_f32_16x16x32_bf16 v[60:63], v[204:207], a[60:63], v[60:63]\n"
"ds_read_b64_tr_b16 a[74:75], v248 offset:16896\n"
"v_mfma_f32_16x16x32_bf16 v[64:67], v[208:211], a[48:51], v[64:67]\n"
"buffer_load_dwordx4 v238, s[16:19], s48 offen offset:0 lds\n"
"v_mfma_f32_16x16x32_bf16 v[72:75], v[208:211], a[52:55], v[72:75]\n"
"v_mfma_f32_16x16x32_bf16 v[80:83], v[208:211], a[56:59], v[80:83]\n"
"ds_read_b128 v[196:199], v242 offset:34816\n"
"v_mfma_f32_16x16x32_bf16 v[88:91], v[208:211], a[60:63], v[88:91]\n"
"ds_read_b64_tr_b16 a[76:77], v249 offset:16384\n"
"v_mfma_f32_16x16x32_bf16 v[68:71], v[212:215], a[48:51], v[68:71]\n"
"buffer_load_dwordx4 v239, s[16:19], s48 offen offset:1024 lds\n"
"v_mfma_f32_16x16x32_bf16 v[76:79], v[212:215], a[52:55], v[76:79]\n"
"ds_read_b128 v[200:203], v242 offset:36864\n"
"v_mfma_f32_16x16x32_bf16 v[84:87], v[212:215], a[56:59], v[84:87]\n"
"ds_read_b64_tr_b16 a[78:79], v249 offset:16896\n"
"v_mfma_f32_16x16x32_bf16 v[92:95], v[212:215], a[60:63], v[92:95]\n"
"buffer_load_dwordx4 v240, s[16:19], s48 offen offset:2048 lds\n"
"v_mfma_f32_16x16x32_bf16 v[96:99], v[216:219], a[48:51], v[96:99]\n"
"v_mfma_f32_16x16x32_bf16 v[104:107], v[216:219], a[52:55], v[104:107]\n"
"ds_read_b128 v[204:207], v242 offset:38912\n"
"v_mfma_f32_16x16x32_bf16 v[112:115], v[216:219], a[56:59], v[112:115]\n"
"s_waitcnt vmcnt(16)\n"
"ds_read_b64_tr_b16 a[80:81], v246 offset:20480\n"
"v_mfma_f32_16x16x32_bf16 v[120:123], v[216:219], a[60:63], v[120:123]\n"
"buffer_load_dwordx4 v241, s[16:19], s48 offen offset:3072 lds\n"
"v_mfma_f32_16x16x32_bf16 v[100:103], v[220:223], a[48:51], v[100:103]\n"
"ds_read_b128 v[208:211], v242 offset:40960\n"
"v_mfma_f32_16x16x32_bf16 v[108:111], v[220:223], a[52:55], v[108:111]\n"
"v_mfma_f32_16x16x32_bf16 v[116:119], v[220:223], a[56:59], v[116:119]\n"
"s_add_u32 m0, s36, 16384\n"
"ds_read_b64_tr_b16 a[82:83], v246 offset:20992\n"
"s_add_u32 s48, s46, 512\n"
"v_mfma_f32_16x16x32_bf16 v[124:127], v[220:223], a[60:63], v[124:127]\n"
"s_waitcnt lgkmcnt(4)\n"
"s_and_b32 s48, s48, 1023\n"
"v_mfma_f32_16x16x32_bf16 v[0:3], v[192:195], a[64:67], v[0:3]\n"
"ds_read_b128 v[212:215], v242 offset:43008\n"
"v_mfma_f32_16x16x32_bf16 v[8:11], v[192:195], a[68:71], v[8:11]\n"
"buffer_load_dwordx4 v234, s[12:15], s48 offen offset:0 lds\n"
"v_mfma_f32_16x16x32_bf16 v[16:19], v[192:195], a[72:75], v[16:19]\n"
"ds_read_b64_tr_b16 a[84:85], v247 offset:20480\n"
"v_mfma_f32_16x16x32_bf16 v[24:27], v[192:195], a[76:79], v[24:27]\n"
"ds_read_b128 v[216:219], v242 offset:45056\n"
"v_mfma_f32_16x16x32_bf16 v[4:7], v[196:199], a[64:67], v[4:7]\n"
"buffer_load_dwordx4 v235, s[12:15], s48 offen offset:1024 lds\n"
"v_mfma_f32_16x16x32_bf16 v[12:15], v[196:199], a[68:71], v[12:15]\n"
"v_mfma_f32_16x16x32_bf16 v[20:23], v[196:199], a[72:75], v[20:23]\n"
"ds_read_b64_tr_b16 a[86:87], v247 offset:20992\n"
"v_mfma_f32_16x16x32_bf16 v[28:31], v[196:199], a[76:79], v[28:31]\n"
"ds_read_b128 v[220:223], v242 offset:47104\n"
"v_mfma_f32_16x16x32_bf16 v[32:35], v[200:203], a[64:67], v[32:35]\n"
"v_mfma_f32_16x16x32_bf16 v[40:43], v[200:203], a[68:71], v[40:43]\n"
"buffer_load_dwordx4 v236, s[12:15], s48 offen offset:2048 lds\n"
"v_mfma_f32_16x16x32_bf16 v[48:51], v[200:203], a[72:75], v[48:51]\n"
"ds_read_b128 v[192:195], v243 offset:32768\n"
"v_mfma_f32_16x16x32_bf16 v[56:59], v[200:203], a[76:79], v[56:59]\n"
"s_waitcnt lgkmcnt(3)\n"
"ds_read_b64_tr_b16 a[88:89], v248 offset:20480\n"
"v_mfma_f32_16x16x32_bf16 v[36:39], v[204:207], a[64:67], v[36:39]\n"
"v_mfma_f32_16x16x32_bf16 v[44:47], v[204:207], a[68:71], v[44:47]\n"
"buffer_load_dwordx4 v237, s[12:15], s48 offen offset:3072 lds\n"
"v_mfma_f32_16x16x32_bf16 v[52:55], v[204:207], a[72:75], v[52:55]\n"
"ds_read_b128 v[196:199], v243 offset:34816\n"
"v_mfma_f32_16x16x32_bf16 v[60:63], v[204:207], a[76:79], v[60:63]\n"
"ds_read_b64_tr_b16 a[90:91], v248 offset:20992\n"
"v_mfma_f32_16x16x32_bf16 v[64:67], v[208:211], a[64:67], v[64:67]\n"
"s_add_u32 m0, s37, 12288\n"
"s_add_u32 s48, s47, 2359296\n"
"s_and_b32 s48, s48, 4194303\n"
"v_mfma_f32_16x16x32_bf16 v[72:75], v[208:211], a[68:71], v[72:75]\n"
"ds_read_b128 v[200:203], v243 offset:36864\n"
"v_mfma_f32_16x16x32_bf16 v[80:83], v[208:211], a[72:75], v[80:83]\n"
"ds_read_b64_tr_b16 a[92:93], v249 offset:20480\n"
"v_mfma_f32_16x16x32_bf16 v[88:91], v[208:211], a[76:79], v[88:91]\n"
"buffer_load_dwordx4 v238, s[16:19], s48 offen offset:0 lds\n"
"v_mfma_f32_16x16x32_bf16 v[68:71], v[212:215], a[64:67], v[68:71]\n"
"v_mfma_f32_16x16x32_bf16 v[76:79], v[212:215], a[68:71], v[76:79]\n"
"ds_read_b128 v[204:207], v243 offset:38912\n"
"v_mfma_f32_16x16x32_bf16 v[84:87], v[212:215], a[72:75], v[84:87]\n"
"ds_read_b64_tr_b16 a[94:95], v249 offset:20992\n"
"v_mfma_f32_16x16x32_bf16 v[92:95], v[212:215], a[76:79], v[92:95]\n"
"buffer_load_dwordx4 v239, s[16:19], s48 offen offset:1024 lds\n"
"v_mfma_f32_16x16x32_bf16 v[96:99], v[216:219], a[64:67], v[96:99]\n"
"ds_read_b128 v[208:211], v243 offset:40960\n"
"v_mfma_f32_16x16x32_bf16 v[104:107], v[216:219], a[68:71], v[104:107]\n"
"v_mfma_f32_16x16x32_bf16 v[112:115], v[216:219], a[72:75], v[112:115]\n"
"s_waitcnt vmcnt(19)\n"
"ds_read_b64_tr_b16 a[96:97], v246 offset:0\n"
"v_mfma_f32_16x16x32_bf16 v[120:123], v[216:219], a[76:79], v[120:123]\n"
"s_waitcnt lgkmcnt(4)\n"
"buffer_load_dwordx4 v240, s[16:19], s48 offen offset:2048 lds\n"
"v_mfma_f32_16x16x32_bf16 v[100:103], v[220:223], a[64:67], v[100:103]\n"
"ds_read_b128 v[212:215], v243 offset:43008\n"
"v_mfma_f32_16x16x32_bf16 v[108:111], v[220:223], a[68:71], v[108:111]\n"
"v_mfma_f32_16x16x32_bf16 v[116:119], v[220:223], a[72:75], v[116:119]\n"
"ds_read_b64_tr_b16 a[98:99], v246 offset:512\n"
"v_mfma_f32_16x16x32_bf16 v[124:127], v[220:223], a[76:79], v[124:127]\n"
"ds_read_b128 v[216:219], v243 offset:45056\n"
"v_mfma_f32_16x16x32_bf16 v[0:3], v[192:195], a[80:83], v[0:3]\n"
"buffer_load_dwordx4 v241, s[16:19], s48 offen offset:3072 lds\n"
"v_mfma_f32_16x16x32_bf16 v[8:11], v[192:195], a[84:87], v[8:11]\n"
"v_mfma_f32_16x16x32_bf16 v[16:19], v[192:195], a[88:91], v[16:19]\n"
"s_waitcnt lgkmcnt(3)\n"
"ds_read_b64_tr_b16 a[100:101], v247 offset:0\n"
"s_add_u32 m0, s37, 16384\n"
"v_mfma_f32_16x16x32_bf16 v[24:27], v[192:195], a[92:95], v[24:27]\n"
"s_add_u32 s48, s47, 2621440\n"
"ds_read_b128 v[220:223], v243 offset:47104\n"
"s_and_b32 s48, s48, 4194303\n"
"v_mfma_f32_16x16x32_bf16 v[4:7], v[196:199], a[80:83], v[4:7]\n"
"buffer_load_dwordx4 v238, s[16:19], s48 offen offset:0 lds\n"
"v_mfma_f32_16x16x32_bf16 v[12:15], v[196:199], a[84:87], v[12:15]\n"
"v_mfma_f32_16x16x32_bf16 v[20:23], v[196:199], a[88:91], v[20:23]\n"
"ds_read_b64_tr_b16 a[102:103], v247 offset:512\n"
"v_mfma_f32_16x16x32_bf16 v[28:31], v[196:199], a[92:95], v[28:31]\n"
"v_mfma_f32_16x16x32_bf16 v[32:35], v[200:203], a[80:83], v[32:35]\n"
"s_lshl_b32 s50, s31, 3\n"
"buffer_load_dwordx4 v239, s[16:19], s48 offen offset:1024 lds\n"
"v_mfma_f32_16x16x32_bf16 v[40:43], v[200:203], a[84:87], v[40:43]\n"
"s_add_u32 s50, s50, 256\n"
"s_add_u32 s50, s50, s45\n"
"v_mfma_f32_16x16x32_bf16 v[48:51], v[200:203], a[88:91], v[48:51]\n"
"ds_read_b64_tr_b16 a[104:105], v248 offset:0\n"
"v_mfma_f32_16x16x32_bf16 v[56:59], v[200:203], a[92:95], v[56:59]\n"
"v_mfma_f32_16x16x32_bf16 v[36:39], v[204:207], a[80:83], v[36:39]\n"
"v_mfma_f32_16x16x32_bf16 v[44:47], v[204:207], a[84:87], v[44:47]\n"
"s_waitcnt vmcnt(18) lgkmcnt(2)\n"
"s_barrier\n"
"v_mfma_f32_16x16x32_bf16 v[52:55], v[204:207], a[88:91], v[52:55]\n"
"buffer_load_dwordx4 v240, s[16:19], s48 offen offset:2048 lds\n"
"v_mfma_f32_16x16x32_bf16 v[60:63], v[204:207], a[92:95], v[60:63]\n"
"ds_read_b128 v[192:195], v242 offset:0\n"
"v_mfma_f32_16x16x32_bf16 v[64:67], v[208:211], a[80:83], v[64:67]\n"
"ds_read_b64_tr_b16 a[106:107], v248 offset:512\n"
"v_mfma_f32_16x16x32_bf16 v[72:75], v[208:211], a[84:87], v[72:75]\n"
"buffer_store_dwordx2 v[232:233], v230, s[8:11], s50 offen sc1\n"
"v_mfma_f32_16x16x32_bf16 v[80:83], v[208:211], a[88:91], v[80:83]\n"
"buffer_load_dwordx4 v241, s[16:19], s48 offen offset:3072 lds\n"
"v_mfma_f32_16x16x32_bf16 v[88:91], v[208:211], a[92:95], v[88:91]\n"
"ds_read_b128 v[196:199], v242 offset:2048\n"
"v_mfma_f32_16x16x32_bf16 v[68:71], v[212:215], a[80:83], v[68:71]\n"
"s_add_u32 m0, s36, 32768\n"
"ds_read_b64_tr_b16 a[108:109], v249 offset:0\n"
"s_add_u32 s48, s46, 640\n"
"v_mfma_f32_16x16x32_bf16 v[76:79], v[212:215], a[84:87], v[76:79]\n"
"s_and_b32 s48, s48, 1023\n"
"ds_read_b128 v[200:203], v242 offset:4096\n"
"v_mfma_f32_16x16x32_bf16 v[84:87], v[212:215], a[88:91], v[84:87]\n"
"ds_read_b64_tr_b16 a[110:111], v249 offset:512\n"
"v_mfma_f32_16x16x32_bf16 v[92:95], v[212:215], a[92:95], v[92:95]\n"
"buffer_load_dwordx4 v234, s[12:15], s48 offen offset:0 lds\n"
"v_mfma_f32_16x16x32_bf16 v[96:99], v[216:219], a[80:83], v[96:99]\n"
"v_mfma_f32_16x16x32_bf16 v[104:107], v[216:219], a[84:87], v[104:107]\n"
"ds_read_b128 v[204:207], v242 offset:6144\n"
"v_mfma_f32_16x16x32_bf16 v[112:115], v[216:219], a[88:91], v[112:115]\n"
"s_waitcnt vmcnt(18)\n"
"ds_read_b64_tr_b16 a[112:113], v246 offset:4096\n"
"v_mfma_f32_16x16x32_bf16 v[120:123], v[216:219], a[92:95], v[120:123]\n"
"buffer_load_dwordx4 v235, s[12:15], s48 offen offset:1024 lds\n"
"v_mfma_f32_16x16x32_bf16 v[100:103], v[220:223], a[80:83], v[100:103]\n"
"ds_read_b128 v[208:211], v242 offset:8192\n"
"v_mfma_f32_16x16x32_bf16 v[108:111], v[220:223], a[84:87], v[108:111]\n"
"v_mfma_f32_16x16x32_bf16 v[116:119], v[220:223], a[88:91], v[116:119]\n"
"ds_read_b64_tr_b16 a[114:115], v246 offset:4608\n"
"v_mfma_f32_16x16x32_bf16 v[124:127], v[220:223], a[92:95], v[124:127]\n"
"s_waitcnt lgkmcnt(4)\n"
"buffer_load_dwordx4 v236, s[12:15], s48 offen offset:2048 lds\n"
"v_mfma_f32_16x16x32_bf16 v[0:3], v[192:195], a[96:99], v[0:3]\n"
"ds_read_b128 v[212:215], v242 offset:10240\n"
"v_mfma_f32_16x16x32_bf16 v[8:11], v[192:195], a[100:103], v[8:11]\n"
"v_mfma_f32_16x16x32_bf16 v[16:19], v[192:195], a[104:107], v[16:19]\n"
"ds_read_b64_tr_b16 a[116:117], v247 offset:4096\n"
"v_mfma_f32_16x16x32_bf16 v[24:27], v[192:195], a[108:111], v[24:27]\n"
"ds_read_b128 v[216:219], v242 offset:12288\n"
"v_mfma_f32_16x16x32_bf16 v[4:7], v[196:199], a[96:99], v[4:7]\n"
"buffer_load_dwordx4 v237, s[12:15], s48 offen offset:3072 lds\n"
"v_mfma_f32_16x16x32_bf16 v[12:15], v[196:199], a[100:103], v[12:15]\n"
"v_mfma_f32_16x16x32_bf16 v[20:23], v[196:199], a[104:107], v[20:23]\n"
"s_add_u32 m0, s37, 20480\n"
"ds_read_b64_tr_b16 a[118:119], v247 offset:4608\n"
"s_add_u32 s48, s47, 2883584\n"
"v_mfma_f32_16x16x32_bf16 v[28:31], v[196:199], a[108:111], v[28:31]\n"
"s_and_b32 s48, s48, 4194303\n"
"ds_read_b128 v[220:223], v242 offset:14336\n"
"v_mfma_f32_16x16x32_bf16 v[32:35], v[200:203], a[96:99], v[32:35]\n"
"buffer_load_dwordx4 v238, s[16:19], s48 offen offset:0 lds\n"
"v_mfma_f32_16x16x32_bf16 v[40:43], v[200:203], a[100:103], v[40:43]\n"
"v_mfma_f32_16x16x32_bf16 v[48:51], v[200:203], a[104:107], v[48:51]\n"
"ds_read_b128 v[192:195], v243 offset:0\n"
"v_mfma_f32_16x16x32_bf16 v[56:59], v[200:203], a[108:111], v[56:59]\n"
"s_waitcnt lgkmcnt(3)\n"
"ds_read_b64_tr_b16 a[120:121], v248 offset:4096\n"
"v_mfma_f32_16x16x32_bf16 v[36:39], v[204:207], a[96:99], v[36:39]\n"
"buffer_load_dwordx4 v239, s[16:19], s48 offen offset:1024 lds\n"
"v_mfma_f32_16x16x32_bf16 v[44:47], v[204:207], a[100:103], v[44:47]\n"
"v_mfma_f32_16x16x32_bf16 v[52:55], v[204:207], a[104:107], v[52:55]\n"
"ds_read_b128 v[196:199], v243 offset:2048\n"
"v_mfma_f32_16x16x32_bf16 v[60:63], v[204:207], a[108:111], v[60:63]\n"
"ds_read_b64_tr_b16 a[122:123], v248 offset:4608\n"
"v_mfma_f32_16x16x32_bf16 v[64:67], v[208:211], a[96:99], v[64:67]\n"
"v_mfma_f32_16x16x32_bf16 v[72:75], v[208:211], a[100:103], v[72:75]\n"
"ds_read_b128 v[200:203], v243 offset:4096\n"
"v_mfma_f32_16x16x32_bf16 v[80:83], v[208:211], a[104:107], v[80:83]\n"
"buffer_load_dwordx4 v240, s[16:19], s48 offen offset:2048 lds\n"
"v_mfma_f32_16x16x32_bf16 v[88:91], v[208:211], a[108:111], v[88:91]\n"
"ds_read_b64_tr_b16 a[124:125], v249 offset:4096\n"
"v_mfma_f32_16x16x32_bf16 v[68:71], v[212:215], a[96:99], v[68:71]\n"
"v_mfma_f32_16x16x32_bf16 v[76:79], v[212:215], a[100:103], v[76:79]\n"
"ds_read_b128 v[204:207], v243 offset:6144\n"
"v_mfma_f32_16x16x32_bf16 v[84:87], v[212:215], a[104:107], v[84:87]\n"
"buffer_load_dwordx4 v241, s[16:19], s48 offen offset:3072 lds\n"
"v_mfma_f32_16x16x32_bf16 v[92:95], v[212:215], a[108:111], v[92:95]\n"
"ds_read_b64_tr_b16 a[126:127], v249 offset:4608\n"
"v_mfma_f32_16x16x32_bf16 v[96:99], v[216:219], a[96:99], v[96:99]\n"
"s_add_u32 m0, s37, 0\n"
"ds_read_b128 v[208:211], v243 offset:8192\n"
"s_add_u32 s48, s47, 3145728\n"
"v_mfma_f32_16x16x32_bf16 v[104:107], v[216:219], a[100:103], v[104:107]\n"
"s_and_b32 s48, s48, 4194303\n"
"v_mfma_f32_16x16x32_bf16 v[112:115], v[216:219], a[104:107], v[112:115]\n"
"s_waitcnt vmcnt(21)\n"
"ds_read_b64_tr_b16 a[128:129], v246 offset:8192\n"
"v_mfma_f32_16x16x32_bf16 v[120:123], v[216:219], a[108:111], v[120:123]\n"
"s_waitcnt lgkmcnt(4)\n"
"buffer_load_dwordx4 v238, s[16:19], s48 offen offset:0 lds\n"
"v_mfma_f32_16x16x32_bf16 v[100:103], v[220:223], a[96:99], v[100:103]\n"
"ds_read_b128 v[212:215], v243 offset:10240\n"
"v_mfma_f32_16x16x32_bf16 v[108:111], v[220:223], a[100:103], v[108:111]\n"
"v_mfma_f32_16x16x32_bf16 v[116:119], v[220:223], a[104:107], v[116:119]\n"
"ds_read_b64_tr_b16 a[130:131], v246 offset:8704\n"
"v_mfma_f32_16x16x32_bf16 v[124:127], v[220:223], a[108:111], v[124:127]\n"
"buffer_load_dwordx4 v239, s[16:19], s48 offen offset:1024 lds\n"
"v_mfma_f32_16x16x32_bf16 v[0:3], v[192:195], a[112:115], v[0:3]\n"
"ds_read_b128 v[216:219], v243 offset:12288\n"
"v_mfma_f32_16x16x32_bf16 v[8:11], v[192:195], a[116:119], v[8:11]\n"
"v_mfma_f32_16x16x32_bf16 v[16:19], v[192:195], a[120:123], v[16:19]\n"
"s_waitcnt lgkmcnt(3)\n"
"ds_read_b64_tr_b16 a[132:133], v247 offset:8192\n"
"v_mfma_f32_16x16x32_bf16 v[24:27], v[192:195], a[124:127], v[24:27]\n"
"ds_read_b128 v[220:223], v243 offset:14336\n"
"v_mfma_f32_16x16x32_bf16 v[4:7], v[196:199], a[112:115], v[4:7]\n"
"buffer_load_dwordx4 v240, s[16:19], s48 offen offset:2048 lds\n"
"v_mfma_f32_16x16x32_bf16 v[12:15], v[196:199], a[116:119], v[12:15]\n"
"v_mfma_f32_16x16x32_bf16 v[20:23], v[196:199], a[120:123], v[20:23]\n"
"ds_read_b64_tr_b16 a[134:135], v247 offset:8704\n"
"v_mfma_f32_16x16x32_bf16 v[28:31], v[196:199], a[124:127], v[28:31]\n"
"buffer_load_dwordx4 v241, s[16:19], s48 offen offset:3072 lds\n"
"v_mfma_f32_16x16x32_bf16 v[32:35], v[200:203], a[112:115], v[32:35]\n"
"v_mfma_f32_16x16x32_bf16 v[40:43], v[200:203], a[116:119], v[40:43]\n"
"v_mfma_f32_16x16x32_bf16 v[48:51], v[200:203], a[120:123], v[48:51]\n"
"ds_read_b64_tr_b16 a[136:137], v248 offset:8192\n"
"v_mfma_f32_16x16x32_bf16 v[56:59], v[200:203], a[124:127], v[56:59]\n"
"v_mfma_f32_16x16x32_bf16 v[36:39], v[204:207], a[112:115], v[36:39]\n"
"v_mfma_f32_16x16x32_bf16 v[44:47], v[204:207], a[116:119], v[44:47]\n"
"s_waitcnt vmcnt(21) lgkmcnt(2)\n"
"s_barrier\n"
"v_mfma_f32_16x16x32_bf16 v[52:55], v[204:207], a[120:123], v[52:55]\n"
"s_add_u32 m0, s36, 0\n"
"ds_read_b128 v[192:195], v242 offset:16384\n"
"s_add_u32 s48, s46, 768\n"
"v_mfma_f32_16x16x32_bf16 v[60:63], v[204:207], a[124:127], v[60:63]\n"
"s_and_b32 s48, s48, 1023\n"
"ds_read_b64_tr_b16 a[138:139], v248 offset:8704\n"
"v_mfma_f32_16x16x32_bf16 v[64:67], v[208:211], a[112:115], v[64:67]\n"
"buffer_load_dwordx4 v234, s[12:15], s48 offen offset:0 lds\n"
"v_mfma_f32_16x16x32_bf16 v[72:75], v[208:211], a[116:119], v[72:75]\n"
"v_mfma_f32_16x16x32_bf16 v[80:83], v[208:211], a[120:123], v[80:83]\n"
"ds_read_b128 v[196:199], v242 offset:18432\n"
"v_mfma_f32_16x16x32_bf16 v[88:91], v[208:211], a[124:127], v[88:91]\n"
"ds_read_b64_tr_b16 a[140:141], v249 offset:8192\n"
"v_mfma_f32_16x16x32_bf16 v[68:71], v[212:215], a[112:115], v[68:71]\n"
"buffer_load_dwordx4 v235, s[12:15], s48 offen offset:1024 lds\n"
"v_mfma_f32_16x16x32_bf16 v[76:79], v[212:215], a[116:119], v[76:79]\n"
"ds_read_b128 v[200:203], v242 offset:20480\n"
"v_mfma_f32_16x16x32_bf16 v[84:87], v[212:215], a[120:123], v[84:87]\n"
"ds_read_b64_tr_b16 a[142:143], v249 offset:8704\n"
"v_mfma_f32_16x16x32_bf16 v[92:95], v[212:215], a[124:127], v[92:95]\n"
"v_mfma_f32_16x16x32_bf16 v[96:99], v[216:219], a[112:115], v[96:99]\n"
"v_mfma_f32_16x16x32_bf16 v[104:107], v[216:219], a[116:119], v[104:107]\n"
"ds_read_b128 v[204:207], v242 offset:22528\n"
"v_mfma_f32_16x16x32_bf16 v[112:115], v[216:219], a[120:123], v[112:115]\n"
"buffer_load_dwordx4 v236, s[12:15], s48 offen offset:2048 lds\n"
"v_mfma_f32_16x16x32_bf16 v[120:123], v[216:219], a[124:127], v[120:123]\n"
"s_waitcnt vmcnt(20)\n"
"ds_read_b64_tr_b16 a[144:145], v246 offset:12288\n"
"v_mfma_f32_16x16x32_bf16 v[100:103], v[220:223], a[112:115], v[100:103]\n"
"ds_read_b128 v[208:211], v242 offset:24576\n"
"v_mfma_f32_16x16x32_bf16 v[108:111], v[220:223], a[116:119], v[108:111]\n"
"buffer_load_dwordx4 v237, s[12:15], s48 offen offset:3072 lds\n"
"v_mfma_f32_16x16x32_bf16 v[116:119], v[220:223], a[120:123], v[116:119]\n"
"ds_read_b64_tr_b16 a[146:147], v246 offset:12800\n"
"v_mfma_f32_16x16x32_bf16 v[124:127], v[220:223], a[124:127], v[124:127]\n"
"s_waitcnt lgkmcnt(4)\n"
"v_mfma_f32_16x16x32_bf16 v[0:3], v[192:195], a[128:131], v[0:3]\n"
"s_add_u32 m0, s37, 4096\n"
"ds_read_b128 v[212:215], v242 offset:26624\n"
"s_add_u32 s48, s47, 3407872\n"
"v_mfma_f32_16x16x32_bf16 v[8:11], v[192:195], a[132:135], v[8:11]\n"
"s_and_b32 s48, s48, 4194303\n"
"v_mfma_f32_16x16x32_bf16 v[16:19], v[192:195], a[136:139], v[16:19]\n"
"ds_read_b64_tr_b16 a[148:149], v247 offset:12288\n"
"v_mfma_f32_16x16x32_bf16 v[24:27], v[192:195], a[140:143], v[24:27]\n"
"buffer_load_dwordx4 v238, s[16:19], s48 offen offset:0 lds\n"
"v_mfma_f32_16x16x32_bf16 v[4:7], v[196:199], a[128:131], v[4:7]\n"
"ds_read_b128 v[216:219], v242 offset:28672\n"
"v_mfma_f32_16x16x32_bf16 v[12:15], v[196:199], a[132:135], v[12:15]\n"
"v_mfma_f32_16x16x32_bf16 v[20:23], v[196:199], a[136:139], v[20:23]\n"
"ds_read_b64_tr_b16 a[150:151], v247 offset:12800\n"
"v_mfma_f32_16x16x32_bf16 v[28:31], v[196:199], a[140:143], v[28:31]\n"
"buffer_load_dwordx4 v239, s[16:19], s48 offen offset:1024 lds\n"
"v_mfma_f32_16x16x32_bf16 v[32:35], v[200:203], a[128:131], v[32:35]\n"
"ds_read_b128 v[220:223], v242 offset:30720\n"
"v_mfma_f32_16x16x32_bf16 v[40:43], v[200:203], a[132:135], v[40:43]\n"
"v_mfma_f32_16x16x32_bf16 v[48:51], v[200:203], a[136:139], v[48:51]\n"
"ds_read_b128 v[192:195], v243 offset:16384\n"
"v_mfma_f32_16x16x32_bf16 v[56:59], v[200:203], a[140:143], v[56:59]\n"
"s_waitcnt lgkmcnt(3)\n"
"ds_read_b64_tr_b16 a[152:153], v248 offset:12288\n"
"v_mfma_f32_16x16x32_bf16 v[36:39], v[204:207], a[128:131], v[36:39]\n"
"buffer_load_dwordx4 v240, s[16:19], s48 offen offset:2048 lds\n"
"v_mfma_f32_16x16x32_bf16 v[44:47], v[204:207], a[132:135], v[44:47]\n"
"v_mfma_f32_16x16x32_bf16 v[52:55], v[204:207], a[136:139], v[52:55]\n"
"ds_read_b128 v[196:199], v243 offset:18432\n"
"v_mfma_f32_16x16x32_bf16 v[60:63], v[204:207], a[140:143], v[60:63]\n"
"ds_read_b64_tr_b16 a[154:155], v248 offset:12800\n"
"v_mfma_f32_16x16x32_bf16 v[64:67], v[208:211], a[128:131], v[64:67]\n"
"buffer_load_dwordx4 v241, s[16:19], s48 offen offset:3072 lds\n"
"v_mfma_f32_16x16x32_bf16 v[72:75], v[208:211], a[132:135], v[72:75]\n"
"ds_read_b128 v[200:203], v243 offset:20480\n"
"v_mfma_f32_16x16x32_bf16 v[80:83], v[208:211], a[136:139], v[80:83]\n"
"s_add_u32 m0, s37, 8192\n"
"ds_read_b64_tr_b16 a[156:157], v249 offset:12288\n"
"s_add_u32 s48, s47, 3670016\n"
"v_mfma_f32_16x16x32_bf16 v[88:91], v[208:211], a[140:143], v[88:91]\n"
"s_and_b32 s48, s48, 4194303\n"
"v_mfma_f32_16x16x32_bf16 v[68:71], v[212:215], a[128:131], v[68:71]\n"
"buffer_load_dwordx4 v238, s[16:19], s48 offen offset:0 lds\n"
"v_mfma_f32_16x16x32_bf16 v[76:79], v[212:215], a[132:135], v[76:79]\n"
"ds_read_b128 v[204:207], v243 offset:22528\n"
"v_mfma_f32_16x16x32_bf16 v[84:87], v[212:215], a[136:139], v[84:87]\n"
"ds_read_b64_tr_b16 a[158:159], v249 offset:12800\n"
"v_mfma_f32_16x16x32_bf16 v[92:95], v[212:215], a[140:143], v[92:95]\n"
"v_mfma_f32_16x16x32_bf16 v[96:99], v[216:219], a[128:131], v[96:99]\n"
"ds_read_b128 v[208:211], v243 offset:24576\n"
"v_mfma_f32_16x16x32_bf16 v[104:107], v[216:219], a[132:135], v[104:107]\n"
"buffer_load_dwordx4 v239, s[16:19], s48 offen offset:1024 lds\n"
"v_mfma_f32_16x16x32_bf16 v[112:115], v[216:219], a[136:139], v[112:115]\n"
"s_waitcnt vmcnt(22)\n"
"ds_read_b64_tr_b16 a[160:161], v246 offset:16384\n"
"v_mfma_f32_16x16x32_bf16 v[120:123], v[216:219], a[140:143], v[120:123]\n"
"s_waitcnt lgkmcnt(4)\n"
"v_mfma_f32_16x16x32_bf16 v[100:103], v[220:223], a[128:131], v[100:103]\n"
"ds_read_b128 v[212:215], v243 offset:26624\n"
"v_mfma_f32_16x16x32_bf16 v[108:111], v[220:223], a[132:135], v[108:111]\n"
"buffer_load_dwordx4 v240, s[16:19], s48 offen offset:2048 lds\n"
"v_mfma_f32_16x16x32_bf16 v[116:119], v[220:223], a[136:139], v[116:119]\n"
"ds_read_b64_tr_b16 a[162:163], v246 offset:16896\n"
"v_mfma_f32_16x16x32_bf16 v[124:127], v[220:223], a[140:143], v[124:127]\n"
"ds_read_b128 v[216:219], v243 offset:28672\n"
"v_mfma_f32_16x16x32_bf16 v[0:3], v[192:195], a[144:147], v[0:3]\n"
"v_mfma_f32_16x16x32_bf16 v[8:11], v[192:195], a[148:151], v[8:11]\n"
"buffer_load_dwordx4 v241, s[16:19], s48 offen offset:3072 lds\n"
"v_mfma_f32_16x16x32_bf16 v[16:19], v[192:195], a[152:155], v[16:19]\n"
"s_waitcnt lgkmcnt(3)\n"
"ds_read_b64_tr_b16 a[164:165], v247 offset:16384\n"
"v_mfma_f32_16x16x32_bf16 v[24:27], v[192:195], a[156:159], v[24:27]\n"
"ds_read_b128 v[220:223], v243 offset:30720\n"
"v_mfma_f32_16x16x32_bf16 v[4:7], v[196:199], a[144:147], v[4:7]\n"
"v_mfma_f32_16x16x32_bf16 v[12:15], v[196:199], a[148:151], v[12:15]\n"
"v_mfma_f32_16x16x32_bf16 v[20:23], v[196:199], a[152:155], v[20:23]\n"
"ds_read_b64_tr_b16 a[166:167], v247 offset:16896\n"
"v_mfma_f32_16x16x32_bf16 v[28:31], v[196:199], a[156:159], v[28:31]\n"
"v_mfma_f32_16x16x32_bf16 v[32:35], v[200:203], a[144:147], v[32:35]\n"
"v_mfma_f32_16x16x32_bf16 v[40:43], v[200:203], a[148:151], v[40:43]\n"
"v_mfma_f32_16x16x32_bf16 v[48:51], v[200:203], a[152:155], v[48:51]\n"
"ds_read_b64_tr_b16 a[168:169], v248 offset:16384\n"
"v_mfma_f32_16x16x32_bf16 v[56:59], v[200:203], a[156:159], v[56:59]\n"
"v_mfma_f32_16x16x32_bf16 v[36:39], v[204:207], a[144:147], v[36:39]\n"
"v_mfma_f32_16x16x32_bf16 v[44:47], v[204:207], a[148:151], v[44:47]\n"
"s_waitcnt vmcnt(20) lgkmcnt(2)\n"
"s_barrier\n"
"v_mfma_f32_16x16x32_bf16 v[52:55], v[204:207], a[152:155], v[52:55]\n"
"s_add_u32 m0, s36, 16384\n"
"ds_read_b128 v[192:195], v242 offset:32768\n"
"s_add_u32 s48, s46, 896\n"
"v_mfma_f32_16x16x32_bf16 v[60:63], v[204:207], a[156:159], v[60:63]\n"
"s_and_b32 s48, s48, 1023\n"
"ds_read_b64_tr_b16 a[170:171], v248 offset:16896\n"
"v_mfma_f32_16x16x32_bf16 v[64:67], v[208:211], a[144:147], v[64:67]\n"
"buffer_load_dwordx4 v234, s[12:15], s48 offen offset:0 lds\n"
"v_mfma_f32_16x16x32_bf16 v[72:75], v[208:211], a[148:151], v[72:75]\n"
"v_mfma_f32_16x16x32_bf16 v[80:83], v[208:211], a[152:155], v[80:83]\n"
"ds_read_b128 v[196:199], v242 offset:34816\n"
"v_mfma_f32_16x16x32_bf16 v[88:91], v[208:211], a[156:159], v[88:91]\n"
"ds_read_b64_tr_b16 a[172:173], v249 offset:16384\n"
"v_mfma_f32_16x16x32_bf16 v[68:71], v[212:215], a[144:147], v[68:71]\n"
"buffer_load_dwordx4 v235, s[12:15], s48 offen offset:1024 lds\n"
"v_mfma_f32_16x16x32_bf16 v[76:79], v[212:215], a[148:151], v[76:79]\n"
"ds_read_b128 v[200:203], v242 offset:36864\n"
"v_mfma_f32_16x16x32_bf16 v[84:87], v[212:215], a[152:155], v[84:87]\n"
"ds_read_b64_tr_b16 a[174:175], v249 offset:16896\n"
"v_mfma_f32_16x16x32_bf16 v[92:95], v[212:215], a[156:159], v[92:95]\n"
"v_mfma_f32_16x16x32_bf16 v[96:99], v[216:219], a[144:147], v[96:99]\n"
"v_mfma_f32_16x16x32_bf16 v[104:107], v[216:219], a[148:151], v[104:107]\n"
"ds_read_b128 v[204:207], v242 offset:38912\n"
"v_mfma_f32_16x16x32_bf16 v[112:115], v[216:219], a[152:155], v[112:115]\n"
"buffer_load_dwordx4 v236, s[12:15], s48 offen offset:2048 lds\n"
"v_mfma_f32_16x16x32_bf16 v[120:123], v[216:219], a[156:159], v[120:123]\n"
"s_waitcnt vmcnt(19)\n"
"ds_read_b64_tr_b16 a[176:177], v246 offset:20480\n"
"v_mfma_f32_16x16x32_bf16 v[100:103], v[220:223], a[144:147], v[100:103]\n"
"ds_read_b128 v[208:211], v242 offset:40960\n"
"v_mfma_f32_16x16x32_bf16 v[108:111], v[220:223], a[148:151], v[108:111]\n"
"buffer_load_dwordx4 v237, s[12:15], s48 offen offset:3072 lds\n"
"v_mfma_f32_16x16x32_bf16 v[116:119], v[220:223], a[152:155], v[116:119]\n"
"ds_read_b64_tr_b16 a[178:179], v246 offset:20992\n"
"v_mfma_f32_16x16x32_bf16 v[124:127], v[220:223], a[156:159], v[124:127]\n"
"s_waitcnt lgkmcnt(4)\n"
"v_mfma_f32_16x16x32_bf16 v[0:3], v[192:195], a[160:163], v[0:3]\n"
"s_add_u32 m0, s37, 12288\n"
"ds_read_b128 v[212:215], v242 offset:43008\n"
"s_add_u32 s48, s47, 3932160\n"
"v_mfma_f32_16x16x32_bf16 v[8:11], v[192:195], a[164:167], v[8:11]\n"
"s_and_b32 s48, s48, 4194303\n"
"v_mfma_f32_16x16x32_bf16 v[16:19], v[192:195], a[168:171], v[16:19]\n"
"ds_read_b64_tr_b16 a[180:181], v247 offset:20480\n"
"v_mfma_f32_16x16x32_bf16 v[24:27], v[192:195], a[172:175], v[24:27]\n"
"buffer_load_dwordx4 v238, s[16:19], s48 offen offset:0 lds\n"
"v_mfma_f32_16x16x32_bf16 v[4:7], v[196:199], a[160:163], v[4:7]\n"
"ds_read_b128 v[216:219], v242 offset:45056\n"
"v_mfma_f32_16x16x32_bf16 v[12:15], v[196:199], a[164:167], v[12:15]\n"
"v_mfma_f32_16x16x32_bf16 v[20:23], v[196:199], a[168:171], v[20:23]\n"
"ds_read_b64_tr_b16 a[182:183], v247 offset:20992\n"
"v_mfma_f32_16x16x32_bf16 v[28:31], v[196:199], a[172:175], v[28:31]\n"
"buffer_load_dwordx4 v239, s[16:19], s48 offen offset:1024 lds\n"
"v_mfma_f32_16x16x32_bf16 v[32:35], v[200:203], a[160:163], v[32:35]\n"
"ds_read_b128 v[220:223], v242 offset:47104\n"
"v_mfma_f32_16x16x32_bf16 v[40:43], v[200:203], a[164:167], v[40:43]\n"
"v_mfma_f32_16x16x32_bf16 v[48:51], v[200:203], a[168:171], v[48:51]\n"
"ds_read_b128 v[192:195], v243 offset:32768\n"
"v_mfma_f32_16x16x32_bf16 v[56:59], v[200:203], a[172:175], v[56:59]\n"
"s_waitcnt lgkmcnt(3)\n"
"ds_read_b64_tr_b16 a[184:185], v248 offset:20480\n"
"v_mfma_f32_16x16x32_bf16 v[36:39], v[204:207], a[160:163], v[36:39]\n"
"buffer_load_dwordx4 v[160:163], v224, s[8:11], 0 offen offset:256 sc1\n"
"v_mfma_f32_16x16x32_bf16 v[44:47], v[204:207], a[164:167], v[44:47]\n"
"buffer_load_dwordx4 v240, s[16:19], s48 offen offset:2048 lds\n"
"v_mfma_f32_16x16x32_bf16 v[52:55], v[204:207], a[168:171], v[52:55]\n"
"buffer_load_dwordx4 v[164:167], v224, s[8:11], 0 offen offset:1280 sc1\n"
"v_mfma_f32_16x16x32_bf16 v[60:63], v[204:207], a[172:175], v[60:63]\n"
"ds_read_b128 v[196:199], v243 offset:34816\n"
"v_mfma_f32_16x16x32_bf16 v[64:67], v[208:211], a[160:163], v[64:67]\n"
"ds_read_b64_tr_b16 a[186:187], v248 offset:20992\n"
"v_mfma_f32_16x16x32_bf16 v[72:75], v[208:211], a[164:167], v[72:75]\n"
"buffer_load_dwordx4 v241, s[16:19], s48 offen offset:3072 lds\n"
"v_mfma_f32_16x16x32_bf16 v[80:83], v[208:211], a[168:171], v[80:83]\n"
"s_add_u32 m0, s37, 16384\n"
"ds_read_b128 v[200:203], v243 offset:36864\n"
"s_add_u32 s48, s39, 0\n"
"v_mfma_f32_16x16x32_bf16 v[88:91], v[208:211], a[172:175], v[88:91]\n"
"ds_read_b64_tr_b16 a[188:189], v249 offset:20480\n"
"v_mfma_f32_16x16x32_bf16 v[68:71], v[212:215], a[160:163], v[68:71]\n"
"buffer_load_dwordx4 v234, s[12:15], s48 offen offset:0 lds\n"
"v_mfma_f32_16x16x32_bf16 v[76:79], v[212:215], a[164:167], v[76:79]\n"
"ds_read_b128 v[204:207], v243 offset:38912\n"
"v_mfma_f32_16x16x32_bf16 v[84:87], v[212:215], a[168:171], v[84:87]\n"
"ds_read_b64_tr_b16 a[190:191], v249 offset:20992\n"
"v_mfma_f32_16x16x32_bf16 v[92:95], v[212:215], a[172:175], v[92:95]\n"
"v_mfma_f32_16x16x32_bf16 v[96:99], v[216:219], a[160:163], v[96:99]\n"
"ds_read_b128 v[208:211], v243 offset:40960\n"
"v_mfma_f32_16x16x32_bf16 v[104:107], v[216:219], a[164:167], v[104:107]\n"
"buffer_load_dwordx4 v235, s[12:15], s48 offen offset:1024 lds\n"
"v_mfma_f32_16x16x32_bf16 v[112:115], v[216:219], a[168:171], v[112:115]\n"
"s_cmp_lt_u32 s30, 2\n"
"s_waitcnt vmcnt(24)\n"
"ds_read_b64_tr_b16 a[192:193], v246 offset:0\n"
"v_mfma_f32_16x16x32_bf16 v[120:123], v[216:219], a[172:175], v[120:123]\n"
"s_waitcnt lgkmcnt(4)\n"
"s_cselect_b64 s[40:41], s[78:79], s[80:81]\n"
"s_and_b32 s41, s41, 0xffff\n"
"v_mfma_f32_16x16x32_bf16 v[100:103], v[220:223], a[160:163], v[100:103]\n"
"s_mov_b32 s42, 2211840\n"
"ds_read_b128 v[212:215], v243 offset:43008\n"
"v_mfma_f32_16x16x32_bf16 v[108:111], v[220:223], a[164:167], v[108:111]\n"
"s_mov_b32 s43, 0x20000\n"
"buffer_load_dwordx4 v236, s[12:15], s48 offen offset:2048 lds\n"
"v_mfma_f32_16x16x32_bf16 v[116:119], v[220:223], a[168:171], v[116:119]\n"
"ds_read_b64_tr_b16 a[194:195], v246 offset:512\n"
"v_mfma_f32_16x16x32_bf16 v[124:127], v[220:223], a[172:175], v[124:127]\n"
"ds_read_b128 v[216:219], v243 offset:45056\n"
"v_mfma_f32_16x16x32_bf16 v[0:3], v[192:195], a[176:179], v[0:3]\n"
"v_mfma_f32_16x16x32_bf16 v[8:11], v[192:195], a[180:183], v[8:11]\n"
"buffer_load_dwordx4 v237, s[12:15], s48 offen offset:3072 lds\n"
"v_mfma_f32_16x16x32_bf16 v[16:19], v[192:195], a[184:187], v[16:19]\n"
"s_waitcnt lgkmcnt(3)\n"
"ds_read_b64_tr_b16 a[196:197], v247 offset:0\n"
"v_mfma_f32_16x16x32_bf16 v[24:27], v[192:195], a[188:191], v[24:27]\n"
"ds_read_b128 v[220:223], v243 offset:47104\n"
"v_mfma_f32_16x16x32_bf16 v[4:7], v[196:199], a[176:179], v[4:7]\n"
"s_add_u32 m0, s37, 20480\n"
"s_add_u32 s48, s98, 0\n"
"v_mfma_f32_16x16x32_bf16 v[12:15], v[196:199], a[180:183], v[12:15]\n"
"v_mfma_f32_16x16x32_bf16 v[20:23], v[196:199], a[184:187], v[20:23]\n"
"ds_read_b64_tr_b16 a[198:199], v247 offset:512\n"
"v_mfma_f32_16x16x32_bf16 v[28:31], v[196:199], a[188:191], v[28:31]\n"
"buffer_load_dwordx4 v234, s[12:15], s48 offen offset:0 lds\n"
"v_mfma_f32_16x16x32_bf16 v[32:35], v[200:203], a[176:179], v[32:35]\n"
"v_lshrrev_b32 v149, 4, v224\n"
"v_mfma_f32_16x16x32_bf16 v[40:43], v[200:203], a[180:183], v[40:43]\n"
"v_min_u32 v149, 49, v149\n"
"v_mfma_f32_16x16x32_bf16 v[48:51], v[200:203], a[184:187], v[48:51]\n"
"ds_read_b64_tr_b16 a[200:201], v248 offset:0\n"
"v_mul_u32_u24 v150, 52, v149\n"
"v_mfma_f32_16x16x32_bf16 v[56:59], v[200:203], a[188:191], v[56:59]\n"
"buffer_load_dwordx4 v235, s[12:15], s48 offen offset:1024 lds\n"
"v_mfma_f32_16x16x32_bf16 v[36:39], v[204:207], a[176:179], v[36:39]\n"
"v_lshrrev_b32 v150, 8, v150\n"
"v_mul_u32_u24 v150, 2901, v150\n"
"v_mfma_f32_16x16x32_bf16 v[44:47], v[204:207], a[180:183], v[44:47]\n"
"s_waitcnt vmcnt(24) lgkmcnt(2)\n"
"s_barrier\n"
"v_mfma_f32_16x16x32_bf16 v[52:55], v[204:207], a[184:187], v[52:55]\n"
"ds_read_b128 v[192:195], v242 offset:0\n"
"v_mul_u32_u24 v149, 111, v149\n"
"v_mfma_f32_16x16x32_bf16 v[60:63], v[204:207], a[188:191], v[60:63]\n"
"ds_read_b64_tr_b16 a[202:203], v248 offset:512\n"
"v_add_u32 v149, v149, v150\n"
"v_mfma_f32_16x16x32_bf16 v[64:67], v[208:211], a[176:179], v[64:67]\n"
"s_lshr_b32 s50, s31, 3\n"
"buffer_load_dwordx4 v236, s[12:15], s48 offen offset:2048 lds\n"
"v_mfma_f32_16x16x32_bf16 v[72:75], v[208:211], a[180:183], v[72:75]\n"
"s_lshl_b32 s50, s50, 1\n"
"v_and_b32 v149, -4, v149\n"
"v_mfma_f32_16x16x32_bf16 v[80:83], v[208:211], a[184:187], v[80:83]\n"
"s_and_b32 s51, s30, 1\n"
"ds_read_b128 v[196:199], v242 offset:2048\n"
"v_mfma_f32_16x16x32_bf16 v[88:91], v[208:211], a[188:191], v[88:91]\n"
"s_add_u32 s50, s50, s51\n"
"ds_read_b64_tr_b16 a[204:205], v249 offset:0\n"
"v_mfma_f32_16x16x32_bf16 v[68:71], v[212:215], a[176:179], v[68:71]\n"
"s_mul_i32 s50, s50, 34560\n"
"buffer_load_dwordx4 v237, s[12:15], s48 offen offset:3072 lds\n"
"v_mfma_f32_16x16x32_bf16 v[76:79], v[212:215], a[180:183], v[76:79]\n"
"s_mul_i32 s51, s32, 448\n"
"ds_read_b128 v[200:203], v242 offset:4096\n"
"v_mfma_f32_16x16x32_bf16 v[84:87], v[212:215], a[184:187], v[84:87]\n"
"s_add_u32 s50, s50, s51\n"
"ds_read_b64_tr_b16 a[206:207], v249 offset:512\n"
"v_mfma_f32_16x16x32_bf16 v[92:95], v[212:215], a[188:191], v[92:95]\n"
"v_mfma_f32_16x16x32_bf16 v[96:99], v[216:219], a[176:179], v[96:99]\n"
"v_mfma_f32_16x16x32_bf16 v[104:107], v[216:219], a[180:183], v[104:107]\n"
"ds_read_b128 v[204:207], v242 offset:6144\n"
"v_mfma_f32_16x16x32_bf16 v[112:115], v[216:219], a[184:187], v[112:115]\n"
"s_add_u32 m0, s37, 0\n"
"s_waitcnt vmcnt(22)\n"
"ds_read_b64_tr_b16 a[208:209], v246 offset:4096\n"
"v_mfma_f32_16x16x32_bf16 v[120:123], v[216:219], a[188:191], v[120:123]\n"
"s_add_u32 s48, s39, 32768\n"
"buffer_load_dword v149, v149, s[40:43], s50 offen\n"
"v_mfma_f32_16x16x32_bf16 v[100:103], v[220:223], a[176:179], v[100:103]\n"
"ds_read_b128 v[208:211], v242 offset:8192\n"
"v_mfma_f32_16x16x32_bf16 v[108:111], v[220:223], a[180:183], v[108:111]\n"
"s_waitcnt lgkmcnt(3)\n"
"buffer_load_dwordx4 v234, s[12:15], s48 offen offset:0 lds\n"
"v_mfma_f32_16x16x32_bf16 v[116:119], v[220:223], a[184:187], v[116:119]\n"
"ds_read_b64_tr_b16 a[210:211], v246 offset:4608\n"
"v_mfma_f32_16x16x32_bf16 v[124:127], v[220:223], a[188:191], v[124:127]\n"
"v_mfma_f32_16x16x32_bf16 v[0:3], v[192:195], a[192:195], v[0:3]\n"
"ds_read_b128 v[212:215], v242 offset:10240\n"
"v_mfma_f32_16x16x32_bf16 v[8:11], v[192:195], a[196:199], v[8:11]\n"
"buffer_load_dwordx4 v235, s[12:15], s48 offen offset:1024 lds\n"
"v_mfma_f32_16x16x32_bf16 v[16:19], v[192:195], a[200:203], v[16:19]\n"
"s_mov_b32 s94, 2686976\n"
"ds_read_b64_tr_b16 a[212:213], v247 offset:4096\n"
"s_mov_b32 s95, 2691072\n"
"v_mfma_f32_16x16x32_bf16 v[24:27], v[192:195], a[204:207], v[24:27]\n"
"s_waitcnt vmcnt(12)\n"
"v_cmp_eq_u64 vcc, s[28:29], v[160:161]\n"
"v_cmp_eq_u64_e64 s[62:63], s[28:29], v[162:163]\n"
"s_and_b64 vcc, vcc, s[62:63]\n"
"v_cmp_eq_u64_e64 s[62:63], s[28:29], v[164:165]\n"
"s_and_b64 vcc, vcc, s[62:63]\n"
"v_cmp_eq_u64_e64 s[62:63], s[28:29], v[166:167]\n"
"s_and_b64 vcc, vcc, s[62:63]\n"
"s_cmp_eq_u64 vcc, exec\n"
"s_cbranch_scc0 L1004_%=\n"
"L1005_%=:\n"
"v_mfma_f32_16x16x32_bf16 v[4:7], v[196:199], a[192:195], v[4:7]\n"
"ds_read_b128 v[216:219], v242 offset:12288\n"
"v_mfma_f32_16x16x32_bf16 v[12:15], v[196:199], a[196:199], v[12:15]\n"
"buffer_load_dwordx4 v[160:163], v224, s[8:11], s94 offen offset:0 sc1\n"
"v_mfma_f32_16x16x32_bf16 v[20:23], v[196:199], a[200:203], v[20:23]\n"
"buffer_load_dwordx4 v236, s[12:15], s48 offen offset:2048 lds\n"
"v_mfma_f32_16x16x32_bf16 v[28:31], v[196:199], a[204:207], v[28:31]\n"
"ds_read_b64_tr_b16 a[214:215], v247 offset:4608\n"
"v_mfma_f32_16x16x32_bf16 v[32:35], v[200:203], a[192:195], v[32:35]\n"
"buffer_load_dwordx4 v[164:167], v224, s[8:11], s94 offen offset:1024 sc1\n"
"v_mfma_f32_16x16x32_bf16 v[40:43], v[200:203], a[196:199], v[40:43]\n"
"ds_read_b128 v[220:223], v242 offset:14336\n"
"v_mfma_f32_16x16x32_bf16 v[48:51], v[200:203], a[200:203], v[48:51]\n"
"buffer_load_dwordx4 v[168:171], v224, s[8:11], s94 offen offset:2048 sc1\n"
"v_mfma_f32_16x16x32_bf16 v[56:59], v[200:203], a[204:207], v[56:59]\n"
"s_waitcnt lgkmcnt(2)\n"
"buffer_load_dwordx4 v237, s[12:15], s48 offen offset:3072 lds\n"
"v_mfma_f32_16x16x32_bf16 v[36:39], v[204:207], a[192:195], v[36:39]\n"
"ds_read_b128 v[192:195], v243 offset:0\n"
"v_mfma_f32_16x16x32_bf16 v[44:47], v[204:207], a[196:199], v[44:47]\n"
"ds_read_b64_tr_b16 a[216:217], v248 offset:4096\n"
"v_mfma_f32_16x16x32_bf16 v[52:55], v[204:207], a[200:203], v[52:55]\n"
"buffer_load_dwordx4 v[172:175], v224, s[8:11], s94 offen offset:3072 sc1\n"
"v_mfma_f32_16x16x32_bf16 v[60:63], v[204:207], a[204:207], v[60:63]\n"
"buffer_load_dwordx4 v[176:179], v224, s[8:11], s95 offen offset:0 sc1\n"
"v_mfma_f32_16x16x32_bf16 v[64:67], v[208:211], a[192:195], v[64:67]\n"
"ds_read_b128 v[196:199], v243 offset:2048\n"
"v_mfma_f32_16x16x32_bf16 v[72:75], v[208:211], a[196:199], v[72:75]\n"
"ds_read_b64_tr_b16 a[218:219], v248 offset:4608\n"
"v_mfma_f32_16x16x32_bf16 v[80:83], v[208:211], a[200:203], v[80:83]\n"
"buffer_load_dwordx4 v[180:183], v224, s[8:11], s95 offen offset:1024 sc1\n"
"v_mfma_f32_16x16x32_bf16 v[88:91], v[208:211], a[204:207], v[88:91]\n"
"buffer_load_dwordx4 v[184:187], v224, s[8:11], s95 offen offset:2048 sc1\n"
"v_mfma_f32_16x16x32_bf16 v[68:71], v[212:215], a[192:195], v[68:71]\n"
"ds_read_b128 v[200:203], v243 offset:4096\n"
"v_mfma_f32_16x16x32_bf16 v[76:79], v[212:215], a[196:199], v[76:79]\n"
"ds_read_b64_tr_b16 a[220:221], v249 offset:4096\n"
"v_mfma_f32_16x16x32_bf16 v[84:87], v[212:215], a[200:203], v[84:87]\n"
"buffer_load_dwordx4 v[188:191], v224, s[8:11], s95 offen offset:3072 sc1\n"
"v_mfma_f32_16x16x32_bf16 v[92:95], v[212:215], a[204:207], v[92:95]\n"
"ds_read_b128 v[204:207], v243 offset:6144\n"
"v_mfma_f32_16x16x32_bf16 v[96:99], v[216:219], a[192:195], v[96:99]\n"
"ds_read_b64_tr_b16 a[222:223], v249 offset:4608\n"
"v_mfma_f32_16x16x32_bf16 v[104:107], v[216:219], a[196:199], v[104:107]\n"
"ds_read_b128 v[208:211], v243 offset:8192\n"
"v_mfma_f32_16x16x32_bf16 v[112:115], v[216:219], a[200:203], v[112:115]\n"
"s_add_u32 m0, s37, 4096\n"
"ds_read_b64_tr_b16 a[224:225], v246 offset:8192\n"
"s_add_u32 s48, s98, 32768\n"
"v_mfma_f32_16x16x32_bf16 v[120:123], v[216:219], a[204:207], v[120:123]\n"
"s_waitcnt lgkmcnt(5)\n"
"v_mfma_f32_16x16x32_bf16 v[100:103], v[220:223], a[192:195], v[100:103]\n"
"ds_read_b128 v[212:215], v243 offset:10240\n"
"v_mfma_f32_16x16x32_bf16 v[108:111], v[220:223], a[196:199], v[108:111]\n"
"s_waitcnt lgkmcnt(3)\n"
"buffer_load_dwordx4 v234, s[12:15], s48 offen offset:0 lds\n"
"v_mfma_f32_16x16x32_bf16 v[116:119], v[220:223], a[200:203], v[116:119]\n"
"ds_read_b64_tr_b16 a[226:227], v246 offset:8704\n"
"v_mfma_f32_16x16x32_bf16 v[124:127], v[220:223], a[204:207], v[124:127]\n"
"ds_read_b128 v[216:219], v243 offset:12288\n"
"v_mfma_f32_16x16x32_bf16 v[0:3], v[192:195], a[208:211], v[0:3]\n"
"buffer_load_dwordx4 v235, s[12:15], s48 offen offset:1024 lds\n"
"v_mfma_f32_16x16x32_bf16 v[8:11], v[192:195], a[212:215], v[8:11]\n"
"v_mfma_f32_16x16x32_bf16 v[16:19], v[192:195], a[216:219], v[16:19]\n"
"ds_read_b64_tr_b16 a[228:229], v247 offset:8192\n"
"v_mfma_f32_16x16x32_bf16 v[24:27], v[192:195], a[220:223], v[24:27]\n"
"ds_read_b128 v[220:223], v243 offset:14336\n"
"v_mfma_f32_16x16x32_bf16 v[4:7], v[196:199], a[208:211], v[4:7]\n"
"v_mfma_f32_16x16x32_bf16 v[12:15], v[196:199], a[212:215], v[12:15]\n"
"buffer_load_dwordx4 v236, s[12:15], s48 offen offset:2048 lds\n"
"v_mfma_f32_16x16x32_bf16 v[20:23], v[196:199], a[216:219], v[20:23]\n"
"ds_read_b64_tr_b16 a[230:231], v247 offset:8704\n"
"v_mfma_f32_16x16x32_bf16 v[28:31], v[196:199], a[220:223], v[28:31]\n"
"v_mfma_f32_16x16x32_bf16 v[32:35], v[200:203], a[208:211], v[32:35]\n"
"v_mfma_f32_16x16x32_bf16 v[40:43], v[200:203], a[212:215], v[40:43]\n"
"buffer_load_dwordx4 v237, s[12:15], s48 offen offset:3072 lds\n"
"v_mfma_f32_16x16x32_bf16 v[48:51], v[200:203], a[216:219], v[48:51]\n"
"ds_read_b64_tr_b16 a[232:233], v248 offset:8192\n"
"v_mfma_f32_16x16x32_bf16 v[56:59], v[200:203], a[220:223], v[56:59]\n"
"v_mfma_f32_16x16x32_bf16 v[36:39], v[204:207], a[208:211], v[36:39]\n"
"v_mfma_f32_16x16x32_bf16 v[44:47], v[204:207], a[212:215], v[44:47]\n"
"s_waitcnt lgkmcnt(2)\n"
"s_barrier\n"
"v_mfma_f32_16x16x32_bf16 v[52:55], v[204:207], a[216:219], v[52:55]\n"
"ds_read_b128 v[192:195], v242 offset:16384\n"
"v_mfma_f32_16x16x32_bf16 v[60:63], v[204:207], a[220:223], v[60:63]\n"
"ds_read_b64_tr_b16 a[234:235], v248 offset:8704\n"
"v_mfma_f32_16x16x32_bf16 v[64:67], v[208:211], a[208:211], v[64:67]\n"
"v_mfma_f32_16x16x32_bf16 v[72:75], v[208:211], a[212:215], v[72:75]\n"
"v_mfma_f32_16x16x32_bf16 v[80:83], v[208:211], a[216:219], v[80:83]\n"
"ds_read_b128 v[196:199], v242 offset:18432\n"
"v_mfma_f32_16x16x32_bf16 v[88:91], v[208:211], a[220:223], v[88:91]\n"
"ds_read_b64_tr_b16 a[236:237], v249 offset:8192\n"
"v_mfma_f32_16x16x32_bf16 v[68:71], v[212:215], a[208:211], v[68:71]\n"
"v_mfma_f32_16x16x32_bf16 v[76:79], v[212:215], a[212:215], v[76:79]\n"
"ds_read_b128 v[200:203], v242 offset:20480\n"
"v_mfma_f32_16x16x32_bf16 v[84:87], v[212:215], a[216:219], v[84:87]\n"
"ds_read_b64_tr_b16 a[238:239], v249 offset:8704\n"
"v_mfma_f32_16x16x32_bf16 v[92:95], v[212:215], a[220:223], v[92:95]\n"
"v_mfma_f32_16x16x32_bf16 v[96:99], v[216:219], a[208:211], v[96:99]\n"
"s_lshl_b32 s50, s35, 20\n"
"v_mfma_f32_16x16x32_bf16 v[104:107], v[216:219], a[212:215], v[104:107]\n"
"s_lshl_b32 s51, s34, 15\n"
"ds_read_b128 v[204:207], v242 offset:22528\n"
"v_mfma_f32_16x16x32_bf16 v[112:115], v[216:219], a[216:219], v[112:115]\n"
"s_add_u32 s50, s50, s51\n"
"s_waitcnt vmcnt(25)\n"
"ds_read_b64_tr_b16 a[240:241], v246 offset:12288\n"
"v_mfma_f32_16x16x32_bf16 v[120:123], v[216:219], a[220:223], v[120:123]\n"
"s_add_u32 m0, s37, 8192\n"
"s_add_u32 s48, s39, 65536\n"
"s_and_b32 s51, s30, 1\n"
"v_mfma_f32_16x16x32_bf16 v[100:103], v[220:223], a[208:211], v[100:103]\n"
"s_lshl_b32 s51, s51, 12\n"
"ds_read_b128 v[208:211], v242 offset:24576\n"
"v_mfma_f32_16x16x32_bf16 v[108:111], v[220:223], a[212:215], v[108:111]\n"
"s_waitcnt lgkmcnt(3)\n"
"buffer_load_dwordx4 v234, s[12:15], s48 offen offset:0 lds\n"
"v_mfma_f32_16x16x32_bf16 v[116:119], v[220:223], a[216:219], v[116:119]\n"
"s_add_u32 s50, s50, s51\n"
"ds_read_b64_tr_b16 a[242:243], v246 offset:12800\n"
"v_mov_b32 v227, 0\n"
"v_mfma_f32_16x16x32_bf16 v[124:127], v[220:223], a[220:223], v[124:127]\n"
"s_add_u32 s50, s50, 11010048\n"
"s_waitcnt vmcnt(14)\n"
"v_mov_b32 v226, v160\n"
"v_mfma_f32_16x16x32_bf16 v[0:3], v[192:195], a[224:227], v[0:3]\n"
"s_cmp_eq_u32 s31, 1\n"
"ds_read_b128 v[212:215], v242 offset:26624\n"
"v_add_u32 v250, s50, v224\n"
"v_mfma_f32_16x16x32_bf16 v[8:11], v[192:195], a[228:231], v[8:11]\n"
"s_cselect_b32 s94, 0, 2130706432\n"
"buffer_load_dwordx4 v235, s[12:15], s48 offen offset:1024 lds\n"
"v_mfma_f32_16x16x32_bf16 v[16:19], v[192:195], a[232:235], v[16:19]\n"
"buffer_store_dwordx2 v[232:233], v230, s[8:11], s94 offen sc1\n"
"v_mfma_f32_16x16x32_bf16 v[24:27], v[192:195], a[236:239], v[24:27]\n"
"ds_read_b64_tr_b16 a[244:245], v247 offset:12288\n"
"v_mov_b32 v228, 0\n"
"v_mfma_f32_16x16x32_bf16 v[4:7], v[196:199], a[224:227], v[4:7]\n"
"ds_read_b128 v[216:219], v242 offset:28672\n"
"v_med3_u32 v228, v227, v228, v161\n"
"v_mfma_f32_16x16x32_bf16 v[12:15], v[196:199], a[228:231], v[12:15]\n"
"buffer_load_dwordx4 v236, s[12:15], s48 offen offset:2048 lds\n"
"v_mfma_f32_16x16x32_bf16 v[20:23], v[196:199], a[232:235], v[20:23]\n"
"ds_read_b64_tr_b16 a[246:247], v247 offset:12800\n"
"v_med3_u32 v227, v226, v227, v161\n"
"v_mfma_f32_16x16x32_bf16 v[28:31], v[196:199], a[236:239], v[28:31]\n"
"ds_read_b128 v[220:223], v242 offset:30720\n"
"v_max_u32 v226, v226, v161\n"
"v_mfma_f32_16x16x32_bf16 v[32:35], v[200:203], a[224:227], v[32:35]\n"
"v_med3_u32 v228, v227, v228, v162\n"
"v_med3_u32 v227, v226, v227, v162\n"
"v_mfma_f32_16x16x32_bf16 v[40:43], v[200:203], a[228:231], v[40:43]\n"
"buffer_load_dwordx4 v237, s[12:15], s48 offen offset:3072 lds\n"
"v_mfma_f32_16x16x32_bf16 v[48:51], v[200:203], a[232:235], v[48:51]\n"
"ds_read_b128 v[192:195], v243 offset:16384\n"
"v_max_u32 v226, v226, v162\n"
"v_mfma_f32_16x16x32_bf16 v[56:59], v[200:203], a[236:239], v[56:59]\n"
"s_waitcnt lgkmcnt(3)\n"
"ds_read_b64_tr_b16 a[248:249], v248 offset:12288\n"
"v_med3_u32 v228, v227, v228, v163\n"
"v_mfma_f32_16x16x32_bf16 v[36:39], v[204:207], a[224:227], v[36:39]\n"
"v_med3_u32 v227, v226, v227, v163\n"
"v_max_u32 v226, v226, v163\n"
"v_mfma_f32_16x16x32_bf16 v[44:47], v[204:207], a[228:231], v[44:47]\n"
"s_waitcnt vmcnt(16)\n"
"v_med3_u32 v228, v227, v228, v164\n"
"v_med3_u32 v227, v226, v227, v164\n"
"v_mfma_f32_16x16x32_bf16 v[52:55], v[204:207], a[232:235], v[52:55]\n"
"ds_read_b128 v[196:199], v243 offset:18432\n"
"v_max_u32 v226, v226, v164\n"
"v_mfma_f32_16x16x32_bf16 v[60:63], v[204:207], a[236:239], v[60:63]\n"
"ds_read_b64_tr_b16 a[250:251], v248 offset:12800\n"
"v_med3_u32 v228, v227, v228, v165\n"
"v_mfma_f32_16x16x32_bf16 v[64:67], v[208:211], a[224:227], v[64:67]\n"
"v_med3_u32 v227, v226, v227, v165\n"
"v_max_u32 v226, v226, v165\n"
"v_mfma_f32_16x16x32_bf16 v[72:75], v[208:211], a[228:231], v[72:75]\n"
"ds_read_b128 v[200:203], v243 offset:20480\n"
"v_med3_u32 v228, v227, v228, v166\n"
"v_mfma_f32_16x16x32_bf16 v[80:83], v[208:211], a[232:235], v[80:83]\n"
"ds_read_b64_tr_b16 a[252:253], v249 offset:12288\n"
"v_med3_u32 v227, v226, v227, v166\n"
"v_mfma_f32_16x16x32_bf16 v[88:91], v[208:211], a[236:239], v[88:91]\n"
"v_max_u32 v226, v226, v166\n"
"v_med3_u32 v228, v227, v228, v167\n"
"v_mfma_f32_16x16x32_bf16 v[68:71], v[212:215], a[224:227], v[68:71]\n"
"v_med3_u32 v227, v226, v227, v167\n"
"v_max_u32 v226, v226, v167\n"
"v_mfma_f32_16x16x32_bf16 v[76:79], v[212:215], a[228:231], v[76:79]\n"
"ds_read_b128 v[204:207], v243 offset:22528\n"
"v_mfma_f32_16x16x32_bf16 v[84:87], v[212:215], a[232:235], v[84:87]\n"
"ds_read_b64_tr_b16 a[254:255], v249 offset:12800\n"
"v_mfma_f32_16x16x32_bf16 v[92:95], v[212:215], a[236:239], v[92:95]\n"
"s_waitcnt vmcnt(15)\n"
"v_med3_u32 v228, v227, v228, v168\n"
"v_med3_u32 v227, v226, v227, v168\n"
"v_mfma_f32_16x16x32_bf16 v[96:99], v[216:219], a[224:227], v[96:99]\n"
"ds_read_b128 v[208:211], v243 offset:24576\n"
"v_max_u32 v226, v226, v168\n"
"v_mfma_f32_16x16x32_bf16 v[104:107], v[216:219], a[228:231], v[104:107]\n"
"v_med3_u32 v228, v227, v228, v169\n"
"v_med3_u32 v227, v226, v227, v169\n"
"v_mfma_f32_16x16x32_bf16 v[112:115], v[216:219], a[232:235], v[112:115]\n"
"s_add_u32 m0, s37, 12288\n"
"v_max_u32 v226, v226, v169\n"
"v_med3_u32 v228, v227, v228, v170\n"
"v_mfma_f32_16x16x32_bf16 v[120:123], v[216:219], a[236:239], v[120:123]\n"
"s_waitcnt lgkmcnt(3)\n"
"v_med3_u32 v227, v226, v227, v170\n"
"v_max_u32 v226, v226, v170\n"
"v_mfma_f32_16x16x32_bf16 v[100:103], v[220:223], a[224:227], v[100:103]\n"
"s_add_u32 s48, s98, 65536\n"
"ds_read_b128 v[212:215], v243 offset:26624\n"
"v_med3_u32 v228, v227, v228, v171\n"
"v_mfma_f32_16x16x32_bf16 v[108:111], v[220:223], a[228:231], v[108:111]\n"
"s_waitcnt lgkmcnt(2)\n"
"buffer_load_dwordx4 v234, s[12:15], s48 offen offset:0 lds\n"
"v_mfma_f32_16x16x32_bf16 v[116:119], v[220:223], a[232:235], v[116:119]\n"
"v_med3_u32 v227, v226, v227, v171\n"
"v_max_u32 v226, v226, v171\n"
"v_mfma_f32_16x16x32_bf16 v[124:127], v[220:223], a[236:239], v[124:127]\n"
"ds_read_b128 v[216:219], v243 offset:28672\n"
"v_mfma_f32_16x16x32_bf16 v[0:3], v[192:195], a[240:243], v[0:3]\n"
"buffer_load_dwordx4 v235, s[12:15], s48 offen offset:1024 lds\n"
"v_mfma_f32_16x16x32_bf16 v[8:11], v[192:195], a[244:247], v[8:11]\n"
"s_waitcnt vmcnt(15)\n"
"v_med3_u32 v228, v227, v228, v172\n"
"v_med3_u32 v227, v226, v227, v172\n"
"v_mfma_f32_16x16x32_bf16 v[16:19], v[192:195], a[248:251], v[16:19]\n"
"v_max_u32 v226, v226, v172\n"
"v_med3_u32 v228, v227, v228, v173\n"
"v_mfma_f32_16x16x32_bf16 v[24:27], v[192:195], a[252:255], v[24:27]\n"
"ds_read_b128 v[220:223], v243 offset:30720\n"
"v_med3_u32 v227, v226, v227, v173\n"
"v_mfma_f32_16x16x32_bf16 v[4:7], v[196:199], a[240:243], v[4:7]\n"
"v_max_u32 v226, v226, v173\n"
"v_med3_u32 v228, v227, v228, v174\n"
"v_mfma_f32_16x16x32_bf16 v[12:15], v[196:199], a[244:247], v[12:15]\n"
"buffer_load_dwordx4 v236, s[12:15], s48 offen offset:2048 lds\n"
"v_mfma_f32_16x16x32_bf16 v[20:23], v[196:199], a[248:251], v[20:23]\n"
"v_med3_u32 v227, v226, v227, v174\n"
"v_max_u32 v226, v226, v174\n"
"v_mfma_f32_16x16x32_bf16 v[28:31], v[196:199], a[252:255], v[28:31]\n"
"v_med3_u32 v228, v227, v228, v175\n"
"v_med3_u32 v227, v226, v227, v175\n"
"v_mfma_f32_16x16x32_bf16 v[32:35], v[200:203], a[240:243], v[32:35]\n"
"v_max_u32 v226, v226, v175\n"
"v_mfma_f32_16x16x32_bf16 v[40:43], v[200:203], a[244:247], v[40:43]\n"
"buffer_load_dwordx4 v237, s[12:15], s48 offen offset:3072 lds\n"
"v_mfma_f32_16x16x32_bf16 v[48:51], v[200:203], a[248:251], v[48:51]\n"
"s_waitcnt vmcnt(16)\n"
"v_med3_u32 v228, v227, v228, v176\n"
"v_med3_u32 v227, v226, v227, v176\n"
"v_mfma_f32_16x16x32_bf16 v[56:59], v[200:203], a[252:255], v[56:59]\n"
"v_max_u32 v226, v226, v176\n"
"v_med3_u32 v228, v227, v228, v177\n"
"v_mfma_f32_16x16x32_bf16 v[36:39], v[204:207], a[240:243], v[36:39]\n"
"v_med3_u32 v227, v226, v227, v177\n"
"v_max_u32 v226, v226, v177\n"
"v_mfma_f32_16x16x32_bf16 v[44:47], v[204:207], a[244:247], v[44:47]\n"
"s_mov_b32 s27, 0x20000\n"
"s_waitcnt lgkmcnt(0)\n"
"s_barrier\n"
"v_mfma_f32_16x16x32_bf16 v[52:55], v[204:207], a[248:251], v[52:55]\n"
"s_lshr_b32 s92, s31, 3\n"
"v_med3_u32 v228, v227, v228, v178\n"
"v_med3_u32 v227, v226, v227, v178\n"
"v_mfma_f32_16x16x32_bf16 v[60:63], v[204:207], a[252:255], v[60:63]\n"
"s_mul_i32 s93, s32, 14\n"
"v_max_u32 v226, v226, v178\n"
"v_med3_u32 v228, v227, v228, v179\n"
"v_mfma_f32_16x16x32_bf16 v[64:67], v[208:211], a[240:243], v[64:67]\n"
"s_add_u32 s93, s93, s92\n"
"v_med3_u32 v227, v226, v227, v179\n"
"v_max_u32 v226, v226, v179\n"
"v_mfma_f32_16x16x32_bf16 v[72:75], v[208:211], a[244:247], v[72:75]\n"
"s_sub_u32 s93, s93, 18\n"
"s_waitcnt vmcnt(15)\n"
"v_med3_u32 v228, v227, v228, v180\n"
"v_mfma_f32_16x16x32_bf16 v[80:83], v[208:211], a[248:251], v[80:83]\n"
"s_cmp_lt_u32 s92, 18\n"
"v_med3_u32 v227, v226, v227, v180\n"
"v_max_u32 v226, v226, v180\n"
"v_mfma_f32_16x16x32_bf16 v[88:91], v[208:211], a[252:255], v[88:91]\n"
"s_cselect_b32 s93, 200, s93\n"
"v_add_u32 v244, 114688, v242\n"
"v_add_u32 v245, 114688, v243\n"
"v_mfma_f32_16x16x32_bf16 v[68:71], v[212:215], a[240:243], v[68:71]\n"
"s_cmp_lt_u32 s30, 2\n"
"v_med3_u32 v228, v227, v228, v181\n"
"v_med3_u32 v227, v226, v227, v181\n"
"v_mfma_f32_16x16x32_bf16 v[76:79], v[212:215], a[244:247], v[76:79]\n"
"s_cselect_b64 s[24:25], s[78:79], s[80:81]\n"
"v_add_u32 v242, 65536, v242\n"
"v_add_u32 v243, 65536, v243\n"
"v_mfma_f32_16x16x32_bf16 v[84:87], v[212:215], a[248:251], v[84:87]\n"
"s_and_b32 s25, s25, 0xffff\n"
"ds_read_b128 v[192:195], v242 offset:16384\n"
"v_max_u32 v226, v226, v181\n"
"v_mfma_f32_16x16x32_bf16 v[92:95], v[212:215], a[252:255], v[92:95]\n"
"s_cmp_lt_u32 s93, 108\n"
"ds_read_b128 v[196:199], v242 offset:18432\n"
"v_med3_u32 v228, v227, v228, v182\n"
"v_mfma_f32_16x16x32_bf16 v[96:99], v[216:219], a[240:243], v[96:99]\n"
"s_cselect_b32 s26, 2211840, 0\n"
"ds_read_b128 v[200:203], v243 offset:16384\n"
"v_med3_u32 v227, v226, v227, v182\n"
"v_mfma_f32_16x16x32_bf16 v[104:107], v[216:219], a[244:247], v[104:107]\n"
"s_cmp_lt_u32 s93, 108\n"
"v_max_u32 v226, v226, v182\n"
"v_med3_u32 v228, v227, v228, v183\n"
"v_mfma_f32_16x16x32_bf16 v[112:115], v[216:219], a[248:251], v[112:115]\n"
"s_cselect_b32 s44, 0, 2130706432\n"
"ds_read_b128 v[204:207], v243 offset:18432\n"
"v_med3_u32 v227, v226, v227, v183\n"
"v_mfma_f32_16x16x32_bf16 v[120:123], v[216:219], a[252:255], v[120:123]\n"
"s_and_b32 s92, s30, 1\n"
"v_max_u32 v226, v226, v183\n"
"s_mul_i32 s61, s92, 1105920\n"
"v_mfma_f32_16x16x32_bf16 v[100:103], v[220:223], a[240:243], v[100:103]\n"
"s_lshl_b32 s92, s93, 5\n"
"s_waitcnt vmcnt(14)\n"
"v_med3_u32 v228, v227, v228, v184\n"
"v_mfma_f32_16x16x32_bf16 v[108:111], v[220:223], a[244:247], v[108:111]\n"
"s_add_u32 s61, s61, s92\n"
"ds_read_b128 v[208:211], v242 offset:20480\n"
"v_med3_u32 v227, v226, v227, v184\n"
"v_mfma_f32_16x16x32_bf16 v[116:119], v[220:223], a[248:251], v[116:119]\n"
"s_lshr_b32 s92, s93, 1\n"
"v_max_u32 v226, v226, v184\n"
"v_med3_u32 v228, v227, v228, v185\n"
"v_mfma_f32_16x16x32_bf16 v[124:127], v[220:223], a[252:255], v[124:127]\n"
"s_waitcnt lgkmcnt(2)\n"
"v_med3_u32 v227, v226, v227, v185\n"
"v_max_u32 v226, v226, v185\n"
"v_mfma_f32_16x16x32_bf16 v[128:131], v[192:195], a[0:3], 0\n"
"s_lshl_b32 s92, s92, 9\n"
"v_med3_u32 v228, v227, v228, v186\n"
"v_med3_u32 v227, v226, v227, v186\n"
"v_mfma_f32_16x16x32_bf16 v[136:139], v[192:195], a[4:7], 0\n"
"s_and_b32 s60, s93, 1\n"
"ds_read_b128 v[212:215], v242 offset:22528\n"
"v_max_u32 v226, v226, v186\n"
"v_mfma_f32_16x16x32_bf16 v[144:147], v[192:195], a[8:11], 0\n"
"s_lshl_b32 s60, s60, 3\n"
"v_lshrrev_b32 v251, 5, v224\n"
"v_med3_u32 v228, v227, v228, v187\n"
"v_mfma_f32_16x16x32_bf16 v[152:155], v[192:195], a[12:15], 0\n"
"s_add_u32 s60, s60, s92\n"
"v_mul_u32_u24 v251, 3456, v251\n"
"v_med3_u32 v227, v226, v227, v187\n"
"v_mfma_f32_16x16x32_bf16 v[132:135], v[196:199], a[0:3], 0\n"
"s_add_u32 s60, s60, 2695168\n"
"v_and_b32 v252, 16, v224\n"
"v_max_u32 v226, v226, v187\n"
"v_mfma_f32_16x16x32_bf16 v[140:143], v[196:199], a[4:7], 0\n"
"s_lshl_b32 s92, s30, 8\n"
"ds_read_b128 v[216:219], v243 offset:20480\n"
"v_add_u32 v251, v251, v252\n"
"v_mfma_f32_16x16x32_bf16 v[148:151], v[196:199], a[8:11], 0\n"
"s_add_u32 s93, s93, s92\n"
"s_waitcnt vmcnt(13)\n"
"v_med3_u32 v228, v227, v228, v188\n"
"v_mfma_f32_16x16x32_bf16 v[156:159], v[196:199], a[12:15], 0\n"
"s_cmp_lt_u32 s93, 108\n"
"v_med3_u32 v227, v226, v227, v188\n"
"v_max_u32 v226, v226, v188\n"
"v_mfma_f32_16x16x32_bf16 v[128:131], v[200:203], a[16:19], v[128:131]\n"
"s_cselect_b32 s60, s60, 2130706432\n"
"v_med3_u32 v228, v227, v228, v189\n"
"v_med3_u32 v227, v226, v227, v189\n"
"v_mfma_f32_16x16x32_bf16 v[136:139], v[200:203], a[20:23], v[136:139]\n"
"s_mul_i32 s92, s30, 10240\n"
"ds_read_b128 v[220:223], v243 offset:22528\n"
"v_max_u32 v226, v226, v189\n"
"v_mfma_f32_16x16x32_bf16 v[144:147], v[200:203], a[24:27], v[144:147]\n"
"s_add_u32 m0, s92, 0\n"
"v_med3_u32 v228, v227, v228, v190\n"
"v_med3_u32 v227, v226, v227, v190\n"
"v_mfma_f32_16x16x32_bf16 v[152:155], v[200:203], a[28:31], v[152:155]\n"
"s_waitcnt lgkmcnt(2)\n"
"v_max_u32 v226, v226, v190\n"
"v_med3_u32 v228, v227, v228, v191\n"
"v_mfma_f32_16x16x32_bf16 v[132:135], v[204:207], a[16:19], v[132:135]\n"
"s_add_u32 s48, s61, 0\n"
"ds_read_b128 v[192:195], v242 offset:40960\n"
"v_med3_u32 v227, v226, v227, v191\n"
"v_mfma_f32_16x16x32_bf16 v[140:143], v[204:207], a[20:23], v[140:143]\n"
"s_mov_b32 s49, 0\n"
"buffer_load_dwordx4 v251, s[24:27], s48 offen lds\n"
"v_mfma_f32_16x16x32_bf16 v[148:151], v[204:207], a[24:27], v[148:151]\n"
"v_max_u32 v226, v226, v191\n"
"v_mov_b32 v229, v226\n"
"v_mfma_f32_16x16x32_bf16 v[156:159], v[204:207], a[28:31], v[156:159]\n"
"v_cvt_pk_bf16_f32 v0, v0, v1\n"
"v_cvt_pk_bf16_f32 v1, v2, v3\n"
"v_mfma_f32_16x16x32_bf16 v[128:131], v[208:211], a[32:35], v[128:131]\n"
"ds_read_b128 v[196:199], v242 offset:43008\n"
"v_max_u32_dpp v229, v229, v229 row_shr:1 row_mask:0xf bank_mask:0xf\n"
"v_mfma_f32_16x16x32_bf16 v[136:139], v[208:211], a[36:39], v[136:139]\n"
"v_cvt_pk_bf16_f32 v2, v4, v5\n"
"v_cvt_pk_bf16_f32 v3, v6, v7\n"
"v_mfma_f32_16x16x32_bf16 v[144:147], v[208:211], a[40:43], v[144:147]\n"
"buffer_store_dwordx4 v[0:3], v250, s[8:11], s49 offen offset:0 sc1\n"
"v_mfma_f32_16x16x32_bf16 v[152:155], v[208:211], a[44:47], v[152:155]\n"
"v_max_u32_dpp v229, v229, v229 row_shr:2 row_mask:0xf bank_mask:0xf\n"
"v_cvt_pk_bf16_f32 v8, v8, v9\n"
"v_mfma_f32_16x16x32_bf16 v[132:135], v[212:215], a[32:35], v[132:135]\n"
"s_add_u32 m0, s92, 1024\n"
"ds_read_b128 v[200:203], v243 offset:40960\n"
"v_max_u32_dpp v229, v229, v229 row_shr:4 row_mask:0xf bank_mask:0xf\n"
"v_mfma_f32_16x16x32_bf16 v[140:143], v[212:215], a[36:39], v[140:143]\n"
"s_add_u32 s48, s61, 110592\n"
"v_max_u32_dpp v229, v229, v229 row_shr:8 row_mask:0xf bank_mask:0xf\n"
"v_cvt_pk_bf16_f32 v9, v10, v11\n"
"v_mfma_f32_16x16x32_bf16 v[148:151], v[212:215], a[40:43], v[148:151]\n"
"buffer_load_dwordx4 v251, s[24:27], s48 offen lds\n"
"v_mfma_f32_16x16x32_bf16 v[156:159], v[212:215], a[44:47], v[156:159]\n"
"s_waitcnt lgkmcnt(1)\n"
"v_max_u32_dpp v229, v229, v229 row_bcast:15 row_mask:0xa bank_mask:0xf\n"
"v_cvt_pk_bf16_f32 v10, v12, v13\n"
"v_mfma_f32_16x16x32_bf16 v[128:131], v[216:219], a[48:51], v[128:131]\n"
"ds_read_b128 v[204:207], v243 offset:43008\n"
"v_cvt_pk_bf16_f32 v11, v14, v15\n"
"v_mfma_f32_16x16x32_bf16 v[136:139], v[216:219], a[52:55], v[136:139]\n"
"v_max_u32_dpp v229, v229, v229 row_bcast:31 row_mask:0xc bank_mask:0xf\n"
"v_cvt_pk_bf16_f32 v16, v16, v17\n"
"v_mfma_f32_16x16x32_bf16 v[144:147], v[216:219], a[56:59], v[144:147]\n"
"v_readlane_b32 s62, v229, 63\n"
"v_cvt_pk_bf16_f32 v17, v18, v19\n"
"v_mfma_f32_16x16x32_bf16 v[152:155], v[216:219], a[60:63], v[152:155]\n"
"ds_read_b128 v[208:211], v242 offset:45056\n"
"v_cmp_eq_u32 vcc, s62, v226\n"
"v_mfma_f32_16x16x32_bf16 v[132:135], v[220:223], a[48:51], v[132:135]\n"
"v_writelane_b32 v225, s62, 0\n"
"v_cvt_pk_bf16_f32 v18, v20, v21\n"
"v_mfma_f32_16x16x32_bf16 v[140:143], v[220:223], a[52:55], v[140:143]\n"
"v_cvt_pk_bf16_f32 v19, v22, v23\n"
"v_cndmask_b32 v226, v226, v227, vcc\n"
"v_mfma_f32_16x16x32_bf16 v[148:151], v[220:223], a[56:59], v[148:151]\n"
"buffer_store_dwordx4 v[8:11], v250, s[8:11], s49 offen offset:1024 sc1\n"
"v_mfma_f32_16x16x32_bf16 v[156:159], v[220:223], a[60:63], v[156:159]\n"
"ds_read_b128 v[212:215], v242 offset:47104\n"
"v_cndmask_b32_e64 v227, v227, 0, vcc\n"
"v_mfma_f32_16x16x32_bf16 v[128:131], v[192:195], a[64:67], v[128:131]\n"
"v_mov_b32 v229, v226\n"
"v_cvt_pk_bf16_f32 v24, v24, v25\n"
"v_mfma_f32_16x16x32_bf16 v[136:139], v[192:195], a[68:71], v[136:139]\n"
"v_max_u32_dpp v229, v229, v229 row_shr:1 row_mask:0xf bank_mask:0xf\n"
"v_cvt_pk_bf16_f32 v25, v26, v27\n"
"v_mfma_f32_16x16x32_bf16 v[144:147], v[192:195], a[72:75], v[144:147]\n"
"s_add_u32 m0, s92, 2048\n"
"v_max_u32_dpp v229, v229, v229 row_shr:2 row_mask:0xf bank_mask:0xf\n"
"v_cvt_pk_bf16_f32 v26, v28, v29\n"
"v_mfma_f32_16x16x32_bf16 v[152:155], v[192:195], a[76:79], v[152:155]\n"
"s_add_u32 s48, s61, 221184\n"
"ds_read_b128 v[216:219], v243 offset:45056\n"
"v_cvt_pk_bf16_f32 v27, v30, v31\n"
"v_mfma_f32_16x16x32_bf16 v[132:135], v[196:199], a[64:67], v[132:135]\n"
"buffer_load_dwordx4 v251, s[24:27], s48 offen lds\n"
"v_mfma_f32_16x16x32_bf16 v[140:143], v[196:199], a[68:71], v[140:143]\n"
"v_max_u32_dpp v229, v229, v229 row_shr:4 row_mask:0xf bank_mask:0xf\n"
"v_cvt_pk_bf16_f32 v32, v32, v33\n"
"v_mfma_f32_16x16x32_bf16 v[148:151], v[196:199], a[72:75], v[148:151]\n"
"v_max_u32_dpp v229, v229, v229 row_shr:8 row_mask:0xf bank_mask:0xf\n"
"v_cvt_pk_bf16_f32 v33, v34, v35\n"
"v_mfma_f32_16x16x32_bf16 v[156:159], v[196:199], a[76:79], v[156:159]\n"
"s_waitcnt lgkmcnt(1)\n"
"ds_read_b128 v[220:223], v243 offset:47104\n"
"v_max_u32_dpp v229, v229, v229 row_bcast:15 row_mask:0xa bank_mask:0xf\n"
"v_mfma_f32_16x16x32_bf16 v[128:131], v[200:203], a[80:83], v[128:131]\n"
"v_cvt_pk_bf16_f32 v34, v36, v37\n"
"v_max_u32_dpp v229, v229, v229 row_bcast:31 row_mask:0xc bank_mask:0xf\n"
"v_mfma_f32_16x16x32_bf16 v[136:139], v[200:203], a[84:87], v[136:139]\n"
"v_cvt_pk_bf16_f32 v35, v38, v39\n"
"v_readlane_b32 s62, v229, 63\n"
"v_mfma_f32_16x16x32_bf16 v[144:147], v[200:203], a[88:91], v[144:147]\n"
"ds_read_b128 v[192:195], v244 offset:16384\n"
"v_cmp_eq_u32 vcc, s62, v226\n"
"v_mfma_f32_16x16x32_bf16 v[152:155], v[200:203], a[92:95], v[152:155]\n"
"v_writelane_b32 v225, s62, 1\n"
"v_cvt_pk_bf16_f32 v40, v40, v41\n"
"v_mfma_f32_16x16x32_bf16 v[132:135], v[204:207], a[80:83], v[132:135]\n"
"v_cvt_pk_bf16_f32 v41, v42, v43\n"
"v_cndmask_b32 v226, v226, v227, vcc\n"
"v_mfma_f32_16x16x32_bf16 v[140:143], v[204:207], a[84:87], v[140:143]\n"
"v_cvt_pk_bf16_f32 v42, v44, v45\n"
"v_cvt_pk_bf16_f32 v43, v46, v47\n"
"v_mfma_f32_16x16x32_bf16 v[148:151], v[204:207], a[88:91], v[148:151]\n"
"ds_read_b128 v[196:199], v244 offset:18432\n"
"v_cndmask_b32_e64 v227, v227, 0, vcc\n"
"v_mfma_f32_16x16x32_bf16 v[156:159], v[204:207], a[92:95], v[156:159]\n"
"v_mov_b32 v229, v226\n"
"v_cvt_pk_bf16_f32 v48, v48, v49\n"
"v_mfma_f32_16x16x32_bf16 v[128:131], v[208:211], a[96:99], v[128:131]\n"
"s_add_u32 m0, s92, 3072\n"
"buffer_store_dwordx4 v[16:19], v250, s[8:11], s49 offen offset:2048 sc1\n"
"v_mfma_f32_16x16x32_bf16 v[136:139], v[208:211], a[100:103], v[136:139]\n"
"s_add_u32 s48, s61, 331776\n"
"v_max_u32_dpp v229, v229, v229 row_shr:1 row_mask:0xf bank_mask:0xf\n"
"v_cvt_pk_bf16_f32 v49, v50, v51\n"
"v_mfma_f32_16x16x32_bf16 v[144:147], v[208:211], a[104:107], v[144:147]\n"
"ds_read_b128 v[200:203], v245 offset:16384\n"
"v_max_u32_dpp v229, v229, v229 row_shr:2 row_mask:0xf bank_mask:0xf\n"
"v_mfma_f32_16x16x32_bf16 v[152:155], v[208:211], a[108:111], v[152:155]\n"
"buffer_load_dwordx4 v251, s[24:27], s48 offen lds\n"
"v_mfma_f32_16x16x32_bf16 v[132:135], v[212:215], a[96:99], v[132:135]\n"
"v_cvt_pk_bf16_f32 v50, v52, v53\n"
"v_cvt_pk_bf16_f32 v51, v54, v55\n"
"v_mfma_f32_16x16x32_bf16 v[140:143], v[212:215], a[100:103], v[140:143]\n"
"v_max_u32_dpp v229, v229, v229 row_shr:4 row_mask:0xf bank_mask:0xf\n"
"v_cvt_pk_bf16_f32 v56, v56, v57\n"
"v_mfma_f32_16x16x32_bf16 v[148:151], v[212:215], a[104:107], v[148:151]\n"
"ds_read_b128 v[204:207], v245 offset:18432\n"
"v_max_u32_dpp v229, v229, v229 row_shr:8 row_mask:0xf bank_mask:0xf\n"
"v_mfma_f32_16x16x32_bf16 v[156:159], v[212:215], a[108:111], v[156:159]\n"
"s_waitcnt lgkmcnt(2)\n"
"v_max_u32_dpp v229, v229, v229 row_bcast:15 row_mask:0xa bank_mask:0xf\n"
"v_cvt_pk_bf16_f32 v57, v58, v59\n"
"v_mfma_f32_16x16x32_bf16 v[128:131], v[216:219], a[112:115], v[128:131]\n"
"v_max_u32_dpp v229, v229, v229 row_bcast:31 row_mask:0xc bank_mask:0xf\n"
"v_cvt_pk_bf16_f32 v58, v60, v61\n"
"v_mfma_f32_16x16x32_bf16 v[136:139], v[216:219], a[116:119], v[136:139]\n"
"ds_read_b128 v[208:211], v244 offset:20480\n"
"v_cvt_pk_bf16_f32 v59, v62, v63\n"
"v_mfma_f32_16x16x32_bf16 v[144:147], v[216:219], a[120:123], v[144:147]\n"
"v_readlane_b32 s62, v229, 63\n"
"v_cvt_pk_bf16_f32 v64, v64, v65\n"
"v_mfma_f32_16x16x32_bf16 v[152:155], v[216:219], a[124:127], v[152:155]\n"
"v_cmp_eq_u32 vcc, s62, v226\n"
"v_writelane_b32 v225, s62, 2\n"
"v_mfma_f32_16x16x32_bf16 v[132:135], v[220:223], a[112:115], v[132:135]\n"
"v_cndmask_b32 v226, v226, v227, vcc\n"
"v_cvt_pk_bf16_f32 v65, v66, v67\n"
"v_mfma_f32_16x16x32_bf16 v[140:143], v[220:223], a[116:119], v[140:143]\n"
"ds_read_b128 v[212:215], v244 offset:22528\n"
"v_cvt_pk_bf16_f32 v66, v68, v69\n"
"v_mfma_f32_16x16x32_bf16 v[148:151], v[220:223], a[120:123], v[148:151]\n"
"s_add_u32 m0, s92, 4096\n"
"v_cvt_pk_bf16_f32 v67, v70, v71\n"
"v_cndmask_b32_e64 v227, v227, 0, vcc\n"
"v_mfma_f32_16x16x32_bf16 v[156:159], v[220:223], a[124:127], v[156:159]\n"
"s_add_u32 s48, s61, 442368\n"
"v_mov_b32 v229, v226\n"
"v_cvt_pk_bf16_f32 v72, v72, v73\n"
"v_mfma_f32_16x16x32_bf16 v[128:131], v[192:195], a[128:131], v[128:131]\n"
"buffer_load_dwordx4 v251, s[24:27], s48 offen lds\n"
"v_mfma_f32_16x16x32_bf16 v[136:139], v[192:195], a[132:135], v[136:139]\n"
"ds_read_b128 v[216:219], v245 offset:20480\n"
"v_max_u32_dpp v229, v229, v229 row_shr:1 row_mask:0xf bank_mask:0xf\n"
"v_mfma_f32_16x16x32_bf16 v[144:147], v[192:195], a[136:139], v[144:147]\n"
"buffer_store_dwordx4 v[24:27], v250, s[8:11], s49 offen offset:3072 sc1\n"
"v_mfma_f32_16x16x32_bf16 v[152:155], v[192:195], a[140:143], v[152:155]\n"
"v_max_u32_dpp v229, v229, v229 row_shr:2 row_mask:0xf bank_mask:0xf\n"
"v_cvt_pk_bf16_f32 v73, v74, v75\n"
"v_mfma_f32_16x16x32_bf16 v[132:135], v[196:199], a[128:131], v[132:135]\n"
"v_max_u32_dpp v229, v229, v229 row_shr:4 row_mask:0xf bank_mask:0xf\n"
"v_cvt_pk_bf16_f32 v74, v76, v77\n"
"v_mfma_f32_16x16x32_bf16 v[140:143], v[196:199], a[132:135], v[140:143]\n"
"ds_read_b128 v[220:223], v245 offset:22528\n"
"v_cvt_pk_bf16_f32 v75, v78, v79\n"
"v_mfma_f32_16x16x32_bf16 v[148:151], v[196:199], a[136:139], v[148:151]\n"
"v_max_u32_dpp v229, v229, v229 row_shr:8 row_mask:0xf bank_mask:0xf\n"
"v_cvt_pk_bf16_f32 v80, v80, v81\n"
"v_mfma_f32_16x16x32_bf16 v[156:159], v[196:199], a[140:143], v[156:159]\n"
"s_waitcnt lgkmcnt(2)\n"
"v_max_u32_dpp v229, v229, v229 row_bcast:15 row_mask:0xa bank_mask:0xf\n"
"v_cvt_pk_bf16_f32 v81, v82, v83\n"
"v_mfma_f32_16x16x32_bf16 v[128:131], v[200:203], a[144:147], v[128:131]\n"
"ds_read_b128 v[192:195], v244 offset:40960\n"
"v_max_u32_dpp v229, v229, v229 row_bcast:31 row_mask:0xc bank_mask:0xf\n"
"v_mfma_f32_16x16x32_bf16 v[136:139], v[200:203], a[148:151], v[136:139]\n"
"v_readlane_b32 s62, v229, 63\n"
"v_cvt_pk_bf16_f32 v82, v84, v85\n"
"v_mfma_f32_16x16x32_bf16 v[144:147], v[200:203], a[152:155], v[144:147]\n"
"v_cvt_pk_bf16_f32 v83, v86, v87\n"
"v_cmp_eq_u32 vcc, s62, v226\n"
"v_mfma_f32_16x16x32_bf16 v[152:155], v[200:203], a[156:159], v[152:155]\n"
"v_writelane_b32 v225, s62, 3\n"
"v_cndmask_b32 v226, v226, v227, vcc\n"
"v_mfma_f32_16x16x32_bf16 v[132:135], v[204:207], a[144:147], v[132:135]\n"
"s_add_u32 m0, s92, 5120\n"
"ds_read_b128 v[196:199], v244 offset:43008\n"
"v_cvt_pk_bf16_f32 v88, v88, v89\n"
"v_mfma_f32_16x16x32_bf16 v[140:143], v[204:207], a[148:151], v[140:143]\n"
"s_add_u32 s48, s61, 552960\n"
"v_cvt_pk_bf16_f32 v89, v90, v91\n"
"v_cndmask_b32_e64 v227, v227, 0, vcc\n"
"v_mfma_f32_16x16x32_bf16 v[148:151], v[204:207], a[152:155], v[148:151]\n"
"buffer_load_dwordx4 v251, s[24:27], s48 offen lds\n"
"v_mfma_f32_16x16x32_bf16 v[156:159], v[204:207], a[156:159], v[156:159]\n"
"v_cvt_pk_bf16_f32 v90, v92, v93\n"
"v_cvt_pk_bf16_f32 v91, v94, v95\n"
"v_mfma_f32_16x16x32_bf16 v[128:131], v[208:211], a[160:163], v[128:131]\n"
"ds_read_b128 v[200:203], v245 offset:40960\n"
"v_mov_b32 v229, v226\n"
"v_mfma_f32_16x16x32_bf16 v[136:139], v[208:211], a[164:167], v[136:139]\n"
"v_cvt_pk_bf16_f32 v96, v96, v97\n"
"v_max_u32_dpp v229, v229, v229 row_shr:1 row_mask:0xf bank_mask:0xf\n"
"v_mfma_f32_16x16x32_bf16 v[144:147], v[208:211], a[168:171], v[144:147]\n"
"s_mov_b32 s49, 8192\n"
"v_max_u32_dpp v229, v229, v229 row_shr:2 row_mask:0xf bank_mask:0xf\n"
"v_cvt_pk_bf16_f32 v97, v98, v99\n"
"v_mfma_f32_16x16x32_bf16 v[152:155], v[208:211], a[172:175], v[152:155]\n"
"v_max_u32_dpp v229, v229, v229 row_shr:4 row_mask:0xf bank_mask:0xf\n"
"v_cvt_pk_bf16_f32 v98, v100, v101\n"
"v_mfma_f32_16x16x32_bf16 v[132:135], v[212:215], a[160:163], v[132:135]\n"
"ds_read_b128 v[204:207], v245 offset:43008\n"
"v_cvt_pk_bf16_f32 v99, v102, v103\n"
"v_mfma_f32_16x16x32_bf16 v[140:143], v[212:215], a[164:167], v[140:143]\n"
"buffer_store_dwordx4 v[32:35], v250, s[8:11], s49 offen offset:0 sc1\n"
"v_mfma_f32_16x16x32_bf16 v[148:151], v[212:215], a[168:171], v[148:151]\n"
"v_max_u32_dpp v229, v229, v229 row_shr:8 row_mask:0xf bank_mask:0xf\n"
"v_cvt_pk_bf16_f32 v104, v104, v105\n"
"v_mfma_f32_16x16x32_bf16 v[156:159], v[212:215], a[172:175], v[156:159]\n"
"s_waitcnt lgkmcnt(1)\n"
"ds_read_b128 v[208:211], v244 offset:45056\n"
"v_max_u32_dpp v229, v229, v229 row_bcast:15 row_mask:0xa bank_mask:0xf\n"
"v_mfma_f32_16x16x32_bf16 v[128:131], v[216:219], a[176:179], v[128:131]\n"
"v_cvt_pk_bf16_f32 v105, v106, v107\n"
"v_max_u32_dpp v229, v229, v229 row_bcast:31 row_mask:0xc bank_mask:0xf\n"
"v_mfma_f32_16x16x32_bf16 v[136:139], v[216:219], a[180:183], v[136:139]\n"
"v_readlane_b32 s62, v229, 63\n"
"v_cvt_pk_bf16_f32 v106, v108, v109\n"
"v_mfma_f32_16x16x32_bf16 v[144:147], v[216:219], a[184:187], v[144:147]\n"
"s_add_u32 m0, s92, 6144\n"
"v_cvt_pk_bf16_f32 v107, v110, v111\n"
"v_cmp_eq_u32 vcc, s62, v226\n"
"v_mfma_f32_16x16x32_bf16 v[152:155], v[216:219], a[188:191], v[152:155]\n"
"s_add_u32 s48, s61, 663552\n"
"ds_read_b128 v[212:215], v244 offset:47104\n"
"v_writelane_b32 v225, s62, 4\n"
"v_mfma_f32_16x16x32_bf16 v[132:135], v[220:223], a[176:179], v[132:135]\n"
"buffer_load_dwordx4 v251, s[24:27], s48 offen lds\n"
"v_mfma_f32_16x16x32_bf16 v[140:143], v[220:223], a[180:183], v[140:143]\n"
"v_cndmask_b32 v226, v226, v227, vcc\n"
"v_cndmask_b32_e64 v227, v227, 0, vcc\n"
"v_mfma_f32_16x16x32_bf16 v[148:151], v[220:223], a[184:187], v[148:151]\n"
"v_cvt_pk_bf16_f32 v112, v112, v113\n"
"v_cvt_pk_bf16_f32 v113, v114, v115\n"
"v_mfma_f32_16x16x32_bf16 v[156:159], v[220:223], a[188:191], v[156:159]\n"
"ds_read_b128 v[216:219], v245 offset:45056\n"
"v_cvt_pk_bf16_f32 v114, v116, v117\n"
"v_mfma_f32_16x16x32_bf16 v[128:131], v[192:195], a[192:195], v[128:131]\n"
"v_cvt_pk_bf16_f32 v115, v118, v119\n"
"v_mov_b32 v229, v226\n"
"v_mfma_f32_16x16x32_bf16 v[136:139], v[192:195], a[196:199], v[136:139]\n"
"v_cvt_pk_bf16_f32 v120, v120, v121\n"
"v_max_u32_dpp v229, v229, v229 row_shr:1 row_mask:0xf bank_mask:0xf\n"
"v_mfma_f32_16x16x32_bf16 v[144:147], v[192:195], a[200:203], v[144:147]\n"
"v_cvt_pk_bf16_f32 v121, v122, v123\n"
"v_max_u32_dpp v229, v229, v229 row_shr:2 row_mask:0xf bank_mask:0xf\n"
"v_mfma_f32_16x16x32_bf16 v[152:155], v[192:195], a[204:207], v[152:155]\n"
"ds_read_b128 v[220:223], v245 offset:47104\n"
"v_max_u32_dpp v229, v229, v229 row_shr:4 row_mask:0xf bank_mask:0xf\n"
"v_mfma_f32_16x16x32_bf16 v[132:135], v[196:199], a[192:195], v[132:135]\n"
"v_cvt_pk_bf16_f32 v122, v124, v125\n"
"v_max_u32_dpp v229, v229, v229 row_shr:8 row_mask:0xf bank_mask:0xf\n"
"v_mfma_f32_16x16x32_bf16 v[140:143], v[196:199], a[196:199], v[140:143]\n"
"v_cvt_pk_bf16_f32 v123, v126, v127\n"
"v_max_u32_dpp v229, v229, v229 row_bcast:15 row_mask:0xa bank_mask:0xf\n"
"v_mfma_f32_16x16x32_bf16 v[148:151], v[196:199], a[200:203], v[148:151]\n"
"buffer_store_dwordx4 v[40:43], v250, s[8:11], s49 offen offset:1024 sc1\n"
"v_mfma_f32_16x16x32_bf16 v[156:159], v[196:199], a[204:207], v[156:159]\n"
"v_max_u32_dpp v229, v229, v229 row_bcast:31 row_mask:0xc bank_mask:0xf\n"
"v_mfma_f32_16x16x32_bf16 v[128:131], v[200:203], a[208:211], v[128:131]\n"
"s_add_u32 m0, s92, 7168\n"
"v_readlane_b32 s62, v229, 63\n"
"s_add_u32 s48, s61, 774144\n"
"v_mfma_f32_16x16x32_bf16 v[136:139], v[200:203], a[212:215], v[136:139]\n"
"v_cmp_eq_u32 vcc, s62, v226\n"
"v_writelane_b32 v225, s62, 5\n"
"v_mfma_f32_16x16x32_bf16 v[144:147], v[200:203], a[216:219], v[144:147]\n"
"buffer_load_dwordx4 v251, s[24:27], s48 offen lds\n"
"v_mfma_f32_16x16x32_bf16 v[152:155], v[200:203], a[220:223], v[152:155]\n"
"s_waitcnt lgkmcnt(0)\n"
"v_cndmask_b32 v226, v226, v227, vcc\n"
"v_cndmask_b32_e64 v227, v227, 0, vcc\n"
"v_mfma_f32_16x16x32_bf16 v[132:135], v[204:207], a[208:211], v[132:135]\n"
"v_mov_b32 v229, v226\n"
"v_mfma_f32_16x16x32_bf16 v[140:143], v[204:207], a[212:215], v[140:143]\n"
"s_waitcnt vmcnt(23)\n"
"s_barrier\n"
"v_mfma_f32_16x16x32_bf16 v[148:151], v[204:207], a[216:219], v[148:151]\n"
"s_add_u32 m0, s37, 16384\n"
"ds_read_b128 v[192:195], v242 offset:0\n"
"v_max_u32_dpp v229, v229, v229 row_shr:1 row_mask:0xf bank_mask:0xf\n"
"v_mfma_f32_16x16x32_bf16 v[156:159], v[204:207], a[220:223], v[156:159]\n"
"s_add_u32 s48, s39, 98304\n"
"v_max_u32_dpp v229, v229, v229 row_shr:2 row_mask:0xf bank_mask:0xf\n"
"v_mfma_f32_16x16x32_bf16 v[128:131], v[208:211], a[224:227], v[128:131]\n"
"buffer_load_dwordx4 v234, s[12:15], s48 offen offset:0 lds\n"
"v_mfma_f32_16x16x32_bf16 v[136:139], v[208:211], a[228:231], v[136:139]\n"
"v_max_u32_dpp v229, v229, v229 row_shr:4 row_mask:0xf bank_mask:0xf\n"
"v_mfma_f32_16x16x32_bf16 v[144:147], v[208:211], a[232:235], v[144:147]\n"
"ds_read_b128 v[196:199], v242 offset:2048\n"
"v_max_u32_dpp v229, v229, v229 row_shr:8 row_mask:0xf bank_mask:0xf\n"
"v_mfma_f32_16x16x32_bf16 v[152:155], v[208:211], a[236:239], v[152:155]\n"
"v_mfma_f32_16x16x32_bf16 v[132:135], v[212:215], a[224:227], v[132:135]\n"
"v_max_u32_dpp v229, v229, v229 row_bcast:15 row_mask:0xa bank_mask:0xf\n"
"v_mfma_f32_16x16x32_bf16 v[140:143], v[212:215], a[228:231], v[140:143]\n"
"buffer_load_dwordx4 v235, s[12:15], s48 offen offset:1024 lds\n"
"v_mfma_f32_16x16x32_bf16 v[148:151], v[212:215], a[232:235], v[148:151]\n"
"ds_read_b128 v[200:203], v243 offset:0\n"
"v_max_u32_dpp v229, v229, v229 row_bcast:31 row_mask:0xc bank_mask:0xf\n"
"v_mfma_f32_16x16x32_bf16 v[156:159], v[212:215], a[236:239], v[156:159]\n"
"v_readlane_b32 s62, v229, 63\n"
"v_mfma_f32_16x16x32_bf16 v[128:131], v[216:219], a[240:243], v[128:131]\n"
"buffer_store_dwordx4 v[48:51], v250, s[8:11], s49 offen offset:2048 sc1\n"
"v_mfma_f32_16x16x32_bf16 v[136:139], v[216:219], a[244:247], v[136:139]\n"
"v_cmp_eq_u32 vcc, s62, v226\n"
"v_writelane_b32 v225, s62, 6\n"
"v_mfma_f32_16x16x32_bf16 v[144:147], v[216:219], a[248:251], v[144:147]\n"
"ds_read_b128 v[204:207], v243 offset:2048\n"
"v_cndmask_b32 v226, v226, v227, vcc\n"
"v_mfma_f32_16x16x32_bf16 v[152:155], v[216:219], a[252:255], v[152:155]\n"
"buffer_load_dwordx4 v236, s[12:15], s48 offen offset:2048 lds\n"
"v_mfma_f32_16x16x32_bf16 v[132:135], v[220:223], a[240:243], v[132:135]\n"
"v_cndmask_b32_e64 v227, v227, 0, vcc\n"
"v_mov_b32 v229, v226\n"
"v_mfma_f32_16x16x32_bf16 v[140:143], v[220:223], a[244:247], v[140:143]\n"
"ds_read_b128 v[208:211], v242 offset:4096\n"
"v_max_u32_dpp v229, v229, v229 row_shr:1 row_mask:0xf bank_mask:0xf\n"
"v_mfma_f32_16x16x32_bf16 v[148:151], v[220:223], a[248:251], v[148:151]\n"
"v_mfma_f32_16x16x32_bf16 v[156:159], v[220:223], a[252:255], v[156:159]\n"
"s_waitcnt lgkmcnt(2)\n"
"buffer_load_dwordx4 v237, s[12:15], s48 offen offset:3072 lds\n"
"v_mfma_f32_16x16x32_bf16 v[4:7], v[192:195], a[0:3], 0\n"
"v_max_u32_dpp v229, v229, v229 row_shr:2 row_mask:0xf bank_mask:0xf\n"
"v_mfma_f32_16x16x32_bf16 v[20:23], v[192:195], a[4:7], 0\n"
"ds_read_b128 v[212:215], v242 offset:6144\n"
"v_max_u32_dpp v229, v229, v229 row_shr:4 row_mask:0xf bank_mask:0xf\n"
"v_mfma_f32_16x16x32_bf16 v[36:39], v[192:195], a[8:11], 0\n"
"s_add_u32 m0, s37, 20480\n"
"v_max_u32_dpp v229, v229, v229 row_shr:8 row_mask:0xf bank_mask:0xf\n"
"s_add_u32 s48, s98, 98304\n"
"v_mfma_f32_16x16x32_bf16 v[52:55], v[192:195], a[12:15], 0\n"
"v_max_u32_dpp v229, v229, v229 row_bcast:15 row_mask:0xa bank_mask:0xf\n"
"v_cvt_pk_bf16_f32 v128, v128, v129\n"
"v_mfma_f32_16x16x32_bf16 v[12:15], v[196:199], a[0:3], 0\n"
"buffer_load_dwordx4 v234, s[12:15], s48 offen offset:0 lds\n"
"v_mfma_f32_16x16x32_bf16 v[28:31], v[196:199], a[4:7], 0\n"
"ds_read_b128 v[216:219], v243 offset:4096\n"
"v_max_u32_dpp v229, v229, v229 row_bcast:31 row_mask:0xc bank_mask:0xf\n"
"v_mfma_f32_16x16x32_bf16 v[44:47], v[196:199], a[8:11], 0\n"
"v_readlane_b32 s62, v229, 63\n"
"v_mov_b32 v229, v228\n"
"v_mfma_f32_16x16x32_bf16 v[60:63], v[196:199], a[12:15], 0\n"
"v_cmp_eq_u32 vcc, s62, v226\n"
"v_writelane_b32 v225, s62, 7\n"
"v_mfma_f32_16x16x32_bf16 v[4:7], v[200:203], a[16:19], v[4:7]\n"
"v_cndmask_b32 v226, v226, v227, vcc\n"
"v_cndmask_b32_e64 v227, v227, 0, vcc\n"
"v_mfma_f32_16x16x32_bf16 v[20:23], v[200:203], a[20:23], v[20:23]\n"
"ds_read_b128 v[220:223], v243 offset:6144\n"
"v_max_u32_dpp v229, v229, v229 row_shr:1 row_mask:0xf bank_mask:0xf\n"
"v_mfma_f32_16x16x32_bf16 v[36:39], v[200:203], a[24:27], v[36:39]\n"
"buffer_load_dwordx4 v235, s[12:15], s48 offen offset:1024 lds\n"
"v_mfma_f32_16x16x32_bf16 v[52:55], v[200:203], a[28:31], v[52:55]\n"
"s_waitcnt lgkmcnt(2)\n"
"buffer_store_dwordx4 v[56:59], v250, s[8:11], s49 offen offset:3072 sc1\n"
"v_mfma_f32_16x16x32_bf16 v[12:15], v[204:207], a[16:19], v[12:15]\n"
"ds_read_b128 v[192:195], v242 offset:24576\n"
"v_max_u32_dpp v229, v229, v229 row_shr:2 row_mask:0xf bank_mask:0xf\n"
"v_mfma_f32_16x16x32_bf16 v[28:31], v[204:207], a[20:23], v[28:31]\n"
"v_cvt_pk_bf16_f32 v129, v130, v131\n"
"v_max_u32_dpp v229, v229, v229 row_shr:4 row_mask:0xf bank_mask:0xf\n"
"v_mfma_f32_16x16x32_bf16 v[44:47], v[204:207], a[24:27], v[44:47]\n"
"buffer_load_dwordx4 v236, s[12:15], s48 offen offset:2048 lds\n"
"v_mfma_f32_16x16x32_bf16 v[60:63], v[204:207], a[28:31], v[60:63]\n"
"v_max_u32_dpp v229, v229, v229 row_shr:8 row_mask:0xf bank_mask:0xf\n"
"v_cvt_pk_bf16_f32 v130, v132, v133\n"
"v_mfma_f32_16x16x32_bf16 v[4:7], v[208:211], a[32:35], v[4:7]\n"
"ds_read_b128 v[196:199], v242 offset:26624\n"
"v_max_u32_dpp v229, v229, v229 row_bcast:15 row_mask:0xa bank_mask:0xf\n"
"v_mfma_f32_16x16x32_bf16 v[20:23], v[208:211], a[36:39], v[20:23]\n"
"v_cvt_pk_bf16_f32 v131, v134, v135\n"
"v_max_u32_dpp v229, v229, v229 row_bcast:31 row_mask:0xc bank_mask:0xf\n"
"v_mfma_f32_16x16x32_bf16 v[36:39], v[208:211], a[40:43], v[36:39]\n"
"v_readlane_b32 s63, v229, 63\n"
"v_cvt_pk_bf16_f32 v136, v136, v137\n"
"v_mfma_f32_16x16x32_bf16 v[52:55], v[208:211], a[44:47], v[52:55]\n"
"s_cmp_gt_u32 s63, s62\n"
"s_cbranch_scc1 L1007_%=\n"
"L1008_%=:\n"
"v_mfma_f32_16x16x32_bf16 v[12:15], v[212:215], a[32:35], v[12:15]\n"
"buffer_load_dwordx4 v237, s[12:15], s48 offen offset:3072 lds\n"
"v_mfma_f32_16x16x32_bf16 v[28:31], v[212:215], a[36:39], v[28:31]\n"
"ds_read_b128 v[200:203], v243 offset:24576\n"
"v_lshrrev_b32 v229, 2, v224\n"
"v_mfma_f32_16x16x32_bf16 v[44:47], v[212:215], a[40:43], v[44:47]\n"
"ds_write_b32 v229, v225 offset:40960\n"
"v_mfma_f32_16x16x32_bf16 v[60:63], v[212:215], a[44:47], v[60:63]\n"
"s_waitcnt lgkmcnt(2)\n"
"v_cvt_pk_bf16_f32 v137, v138, v139\n"
"v_cvt_pk_bf16_f32 v138, v140, v141\n"
"v_mfma_f32_16x16x32_bf16 v[4:7], v[216:219], a[48:51], v[4:7]\n"
"s_add_u32 m0, s92, 8192\n"
"ds_read_b128 v[204:207], v243 offset:26624\n"
"v_cvt_pk_bf16_f32 v139, v142, v143\n"
"v_mfma_f32_16x16x32_bf16 v[20:23], v[216:219], a[52:55], v[20:23]\n"
"s_add_u32 s48, s61, 884736\n"
"v_cvt_pk_bf16_f32 v144, v144, v145\n"
"v_cvt_pk_bf16_f32 v145, v146, v147\n"
"v_mfma_f32_16x16x32_bf16 v[36:39], v[216:219], a[56:59], v[36:39]\n"
"s_mov_b32 s49, 16384\n"
"buffer_load_dwordx4 v251, s[24:27], s48 offen lds\n"
"v_mfma_f32_16x16x32_bf16 v[52:55], v[216:219], a[60:63], v[52:55]\n"
"ds_read_b128 v[208:211], v242 offset:28672\n"
"v_cvt_pk_bf16_f32 v146, v148, v149\n"
"v_mfma_f32_16x16x32_bf16 v[12:15], v[220:223], a[48:51], v[12:15]\n"
"buffer_store_dwordx4 v[64:67], v250, s[8:11], s49 offen offset:0 sc1\n"
"v_mfma_f32_16x16x32_bf16 v[28:31], v[220:223], a[52:55], v[28:31]\n"
"v_cvt_pk_bf16_f32 v147, v150, v151\n"
"v_cvt_pk_bf16_f32 v152, v152, v153\n"
"v_mfma_f32_16x16x32_bf16 v[44:47], v[220:223], a[56:59], v[44:47]\n"
"v_cvt_pk_bf16_f32 v153, v154, v155\n"
"v_cvt_pk_bf16_f32 v154, v156, v157\n"
"v_mfma_f32_16x16x32_bf16 v[60:63], v[220:223], a[60:63], v[60:63]\n"
"ds_read_b128 v[212:215], v242 offset:30720\n"
"v_cvt_pk_bf16_f32 v155, v158, v159\n"
"v_mfma_f32_16x16x32_bf16 v[4:7], v[192:195], a[64:67], v[4:7]\n"
"v_mfma_f32_16x16x32_bf16 v[20:23], v[192:195], a[68:71], v[20:23]\n"
"v_mfma_f32_16x16x32_bf16 v[36:39], v[192:195], a[72:75], v[36:39]\n"
"v_mfma_f32_16x16x32_bf16 v[52:55], v[192:195], a[76:79], v[52:55]\n"
"ds_read_b128 v[216:219], v243 offset:28672\n"
"v_mfma_f32_16x16x32_bf16 v[12:15], v[196:199], a[64:67], v[12:15]\n"
"v_mfma_f32_16x16x32_bf16 v[28:31], v[196:199], a[68:71], v[28:31]\n"
"s_add_u32 m0, s92, 9216\n"
"s_add_u32 s48, s61, 995328\n"
"v_mfma_f32_16x16x32_bf16 v[44:47], v[196:199], a[72:75], v[44:47]\n"
"v_mfma_f32_16x16x32_bf16 v[60:63], v[196:199], a[76:79], v[60:63]\n"
"s_waitcnt lgkmcnt(1)\n"
"ds_read_b128 v[220:223], v243 offset:30720\n"
"v_mfma_f32_16x16x32_bf16 v[4:7], v[200:203], a[80:83], v[4:7]\n"
"buffer_load_dwordx4 v251, s[24:27], s48 offen lds\n"
"v_mfma_f32_16x16x32_bf16 v[20:23], v[200:203], a[84:87], v[20:23]\n"
"v_mfma_f32_16x16x32_bf16 v[36:39], v[200:203], a[88:91], v[36:39]\n"
"ds_read_b128 v[192:195], v244 offset:0\n"
"v_mfma_f32_16x16x32_bf16 v[52:55], v[200:203], a[92:95], v[52:55]\n"
"v_mfma_f32_16x16x32_bf16 v[12:15], v[204:207], a[80:83], v[12:15]\n"
"v_mfma_f32_16x16x32_bf16 v[28:31], v[204:207], a[84:87], v[28:31]\n"
"v_mfma_f32_16x16x32_bf16 v[44:47], v[204:207], a[88:91], v[44:47]\n"
"ds_read_b128 v[196:199], v244 offset:2048\n"
"v_mfma_f32_16x16x32_bf16 v[60:63], v[204:207], a[92:95], v[60:63]\n"
"buffer_store_dwordx4 v[72:75], v250, s[8:11], s49 offen offset:1024 sc1\n"
"v_mfma_f32_16x16x32_bf16 v[4:7], v[208:211], a[96:99], v[4:7]\n"
"v_mfma_f32_16x16x32_bf16 v[20:23], v[208:211], a[100:103], v[20:23]\n"
"v_mfma_f32_16x16x32_bf16 v[36:39], v[208:211], a[104:107], v[36:39]\n"
"ds_read_b128 v[200:203], v245 offset:0\n"
"v_mfma_f32_16x16x32_bf16 v[52:55], v[208:211], a[108:111], v[52:55]\n"
"v_mfma_f32_16x16x32_bf16 v[12:15], v[212:215], a[96:99], v[12:15]\n"
"v_mfma_f32_16x16x32_bf16 v[28:31], v[212:215], a[100:103], v[28:31]\n"
"v_mfma_f32_16x16x32_bf16 v[44:47], v[212:215], a[104:107], v[44:47]\n"
"ds_read_b128 v[204:207], v245 offset:2048\n"
"v_mfma_f32_16x16x32_bf16 v[60:63], v[212:215], a[108:111], v[60:63]\n"
"s_waitcnt lgkmcnt(2)\n"
"v_mfma_f32_16x16x32_bf16 v[4:7], v[216:219], a[112:115], v[4:7]\n"
"v_mfma_f32_16x16x32_bf16 v[20:23], v[216:219], a[116:119], v[20:23]\n"
"ds_read_b128 v[208:211], v244 offset:4096\n"
"v_mfma_f32_16x16x32_bf16 v[36:39], v[216:219], a[120:123], v[36:39]\n"
"v_mfma_f32_16x16x32_bf16 v[52:55], v[216:219], a[124:127], v[52:55]\n"
"v_mfma_f32_16x16x32_bf16 v[12:15], v[220:223], a[112:115], v[12:15]\n"
"v_mfma_f32_16x16x32_bf16 v[28:31], v[220:223], a[116:119], v[28:31]\n"
"ds_read_b128 v[212:215], v244 offset:6144\n"
"v_mfma_f32_16x16x32_bf16 v[44:47], v[220:223], a[120:123], v[44:47]\n"
"v_mfma_f32_16x16x32_bf16 v[60:63], v[220:223], a[124:127], v[60:63]\n"
"v_mfma_f32_16x16x32_bf16 v[4:7], v[192:195], a[128:131], v[4:7]\n"
"buffer_store_dwordx4 v[80:83], v250, s[8:11], s49 offen offset:2048 sc1\n"
"v_mfma_f32_16x16x32_bf16 v[20:23], v[192:195], a[132:135], v[20:23]\n"
"ds_read_b128 v[216:219], v245 offset:4096\n"
"v_mfma_f32_16x16x32_bf16 v[36:39], v[192:195], a[136:139], v[36:39]\n"
"v_mfma_f32_16x16x32_bf16 v[52:55], v[192:195], a[140:143], v[52:55]\n"
"v_mfma_f32_16x16x32_bf16 v[12:15], v[196:199], a[128:131], v[12:15]\n"
"v_mfma_f32_16x16x32_bf16 v[28:31], v[196:199], a[132:135], v[28:31]\n"
"ds_read_b128 v[220:223], v245 offset:6144\n"
"v_mfma_f32_16x16x32_bf16 v[44:47], v[196:199], a[136:139], v[44:47]\n"
"v_mfma_f32_16x16x32_bf16 v[60:63], v[196:199], a[140:143], v[60:63]\n"
"s_waitcnt lgkmcnt(2)\n"
"v_mfma_f32_16x16x32_bf16 v[4:7], v[200:203], a[144:147], v[4:7]\n"
"ds_read_b128 v[192:195], v244 offset:24576\n"
"v_mfma_f32_16x16x32_bf16 v[20:23], v[200:203], a[148:151], v[20:23]\n"
"v_mfma_f32_16x16x32_bf16 v[36:39], v[200:203], a[152:155], v[36:39]\n"
"v_mfma_f32_16x16x32_bf16 v[52:55], v[200:203], a[156:159], v[52:55]\n"
"v_mfma_f32_16x16x32_bf16 v[12:15], v[204:207], a[144:147], v[12:15]\n"
"ds_read_b128 v[196:199], v244 offset:26624\n"
"v_mfma_f32_16x16x32_bf16 v[28:31], v[204:207], a[148:151], v[28:31]\n"
"v_mfma_f32_16x16x32_bf16 v[44:47], v[204:207], a[152:155], v[44:47]\n"
"v_mfma_f32_16x16x32_bf16 v[60:63], v[204:207], a[156:159], v[60:63]\n"
"v_mfma_f32_16x16x32_bf16 v[4:7], v[208:211], a[160:163], v[4:7]\n"
"ds_read_b128 v[200:203], v245 offset:24576\n"
"v_mfma_f32_16x16x32_bf16 v[20:23], v[208:211], a[164:167], v[20:23]\n"
"v_mfma_f32_16x16x32_bf16 v[36:39], v[208:211], a[168:171], v[36:39]\n"
"buffer_store_dwordx4 v[88:91], v250, s[8:11], s49 offen offset:3072 sc1\n"
"v_mfma_f32_16x16x32_bf16 v[52:55], v[208:211], a[172:175], v[52:55]\n"
"v_mfma_f32_16x16x32_bf16 v[12:15], v[212:215], a[160:163], v[12:15]\n"
"ds_read_b128 v[204:207], v245 offset:26624\n"
"v_mfma_f32_16x16x32_bf16 v[28:31], v[212:215], a[164:167], v[28:31]\n"
"v_mfma_f32_16x16x32_bf16 v[44:47], v[212:215], a[168:171], v[44:47]\n"
"v_mfma_f32_16x16x32_bf16 v[60:63], v[212:215], a[172:175], v[60:63]\n"
"s_waitcnt lgkmcnt(1)\n"
"ds_read_b128 v[208:211], v244 offset:28672\n"
"v_mfma_f32_16x16x32_bf16 v[4:7], v[216:219], a[176:179], v[4:7]\n"
"v_mfma_f32_16x16x32_bf16 v[20:23], v[216:219], a[180:183], v[20:23]\n"
"v_mfma_f32_16x16x32_bf16 v[36:39], v[216:219], a[184:187], v[36:39]\n"
"v_mfma_f32_16x16x32_bf16 v[52:55], v[216:219], a[188:191], v[52:55]\n"
"ds_read_b128 v[212:215], v244 offset:30720\n"
"v_mfma_f32_16x16x32_bf16 v[12:15], v[220:223], a[176:179], v[12:15]\n"
"v_mfma_f32_16x16x32_bf16 v[28:31], v[220:223], a[180:183], v[28:31]\n"
"v_mfma_f32_16x16x32_bf16 v[44:47], v[220:223], a[184:187], v[44:47]\n"
"v_mfma_f32_16x16x32_bf16 v[60:63], v[220:223], a[188:191], v[60:63]\n"
"ds_read_b128 v[216:219], v245 offset:28672\n"
"v_mfma_f32_16x16x32_bf16 v[4:7], v[192:195], a[192:195], v[4:7]\n"
"v_mfma_f32_16x16x32_bf16 v[20:23], v[192:195], a[196:199], v[20:23]\n"
"s_sub_u32 s99, s34, 5\n"
"v_mfma_f32_16x16x32_bf16 v[36:39], v[192:195], a[200:203], v[36:39]\n"
"s_mov_b32 s49, 24576\n"
"s_cmp_lt_u32 s34, 5\n"
"s_cselect_b32 s99, s34, s99\n"
"v_mfma_f32_16x16x32_bf16 v[52:55], v[192:195], a[204:207], v[52:55]\n"
"s_mul_i32 s99, s99, 1\n"
"ds_read_b128 v[220:223], v245 offset:30720\n"
"v_mfma_f32_16x16x32_bf16 v[12:15], v[196:199], a[192:195], v[12:15]\n"
"s_lshl_b32 s98, s99, 20\n"
"buffer_store_dwordx4 v[96:99], v250, s[8:11], s49 offen offset:0 sc1\n"
"v_mfma_f32_16x16x32_bf16 v[28:31], v[196:199], a[196:199], v[28:31]\n"
"v_mfma_f32_16x16x32_bf16 v[44:47], v[196:199], a[200:203], v[44:47]\n"
"v_mfma_f32_16x16x32_bf16 v[60:63], v[196:199], a[204:207], v[60:63]\n"
"s_lshr_b32 s50, s30, 1\n"
"v_mfma_f32_16x16x32_bf16 v[4:7], v[200:203], a[208:211], v[4:7]\n"
"s_and_b32 s51, s30, 1\n"
"v_mfma_f32_16x16x32_bf16 v[20:23], v[200:203], a[212:215], v[20:23]\n"
"s_mul_i32 s38, s50, 24576\n"
"v_mfma_f32_16x16x32_bf16 v[36:39], v[200:203], a[216:219], v[36:39]\n"
"s_lshl_b32 s39, s50, 18\n"
"v_mfma_f32_16x16x32_bf16 v[52:55], v[200:203], a[220:223], v[52:55]\n"
"s_waitcnt lgkmcnt(0)\n"
"s_lshl_b32 s50, s51, 12\n"
"v_mfma_f32_16x16x32_bf16 v[12:15], v[204:207], a[208:211], v[12:15]\n"
"s_add_u32 s38, s38, s50\n"
"v_mfma_f32_16x16x32_bf16 v[28:31], v[204:207], a[212:215], v[28:31]\n"
"s_lshl_b32 s50, s51, 7\n"
"s_waitcnt vmcnt(31)\n"
"s_barrier\n"
"v_mfma_f32_16x16x32_bf16 v[44:47], v[204:207], a[216:219], v[44:47]\n"
"s_add_u32 s39, s39, s50\n"
"ds_read_b128 v[192:195], v242 offset:8192\n"
"v_mfma_f32_16x16x32_bf16 v[60:63], v[204:207], a[220:223], v[60:63]\n"
"v_mfma_f32_16x16x32_bf16 v[4:7], v[208:211], a[224:227], v[4:7]\n"
"v_mfma_f32_16x16x32_bf16 v[20:23], v[208:211], a[228:231], v[20:23]\n"
"s_add_u32 m0, s38, 65536\n"
"s_add_u32 s48, s39, s98\n"
"v_mfma_f32_16x16x32_bf16 v[36:39], v[208:211], a[232:235], v[36:39]\n"
"ds_read_b128 v[196:199], v242 offset:10240\n"
"v_mfma_f32_16x16x32_bf16 v[52:55], v[208:211], a[236:239], v[52:55]\n"
"buffer_load_dwordx4 v238, s[20:23], s48 offen offset:0 lds\n"
"v_mfma_f32_16x16x32_bf16 v[12:15], v[212:215], a[224:227], v[12:15]\n"
"v_mfma_f32_16x16x32_bf16 v[28:31], v[212:215], a[228:231], v[28:31]\n"
"v_mfma_f32_16x16x32_bf16 v[44:47], v[212:215], a[232:235], v[44:47]\n"
"ds_read_b128 v[200:203], v243 offset:8192\n"
"v_mfma_f32_16x16x32_bf16 v[60:63], v[212:215], a[236:239], v[60:63]\n"
"buffer_store_dwordx4 v[104:107], v250, s[8:11], s49 offen offset:1024 sc1\n"
"v_mfma_f32_16x16x32_bf16 v[4:7], v[216:219], a[240:243], v[4:7]\n"
"v_mfma_f32_16x16x32_bf16 v[20:23], v[216:219], a[244:247], v[20:23]\n"
"v_mfma_f32_16x16x32_bf16 v[36:39], v[216:219], a[248:251], v[36:39]\n"
"ds_read_b128 v[204:207], v243 offset:10240\n"
"v_mfma_f32_16x16x32_bf16 v[52:55], v[216:219], a[252:255], v[52:55]\n"
"buffer_load_dwordx4 v239, s[20:23], s48 offen offset:1024 lds\n"
"v_mfma_f32_16x16x32_bf16 v[12:15], v[220:223], a[240:243], v[12:15]\n"
"v_mfma_f32_16x16x32_bf16 v[28:31], v[220:223], a[244:247], v[28:31]\n"
"ds_read_b128 v[208:211], v242 offset:12288\n"
"v_mfma_f32_16x16x32_bf16 v[44:47], v[220:223], a[248:251], v[44:47]\n"
"v_mfma_f32_16x16x32_bf16 v[60:63], v[220:223], a[252:255], v[60:63]\n"
"s_waitcnt lgkmcnt(2)\n"
"v_mfma_f32_16x16x32_bf16 v[84:87], v[192:195], a[0:3], 0\n"
"v_mfma_f32_16x16x32_bf16 v[100:103], v[192:195], a[4:7], 0\n"
"ds_read_b128 v[212:215], v242 offset:14336\n"
"v_mfma_f32_16x16x32_bf16 v[116:119], v[192:195], a[8:11], 0\n"
"buffer_load_dwordx4 v240, s[20:23], s48 offen offset:2048 lds\n"
"v_mfma_f32_16x16x32_bf16 v[132:135], v[192:195], a[12:15], 0\n"
"v_cvt_pk_bf16_f32 v4, v4, v5\n"
"v_cvt_pk_bf16_f32 v5, v6, v7\n"
"v_mfma_f32_16x16x32_bf16 v[92:95], v[196:199], a[0:3], 0\n"
"v_cvt_pk_bf16_f32 v6, v12, v13\n"
"v_cvt_pk_bf16_f32 v7, v14, v15\n"
"v_mfma_f32_16x16x32_bf16 v[108:111], v[196:199], a[4:7], 0\n"
"ds_read_b128 v[216:219], v243 offset:12288\n"
"v_mfma_f32_16x16x32_bf16 v[124:127], v[196:199], a[8:11], 0\n"
"v_mfma_f32_16x16x32_bf16 v[140:143], v[196:199], a[12:15], 0\n"
"s_add_u32 s98, s98, 524288\n"
"v_mfma_f32_16x16x32_bf16 v[84:87], v[200:203], a[16:19], v[84:87]\n"
"buffer_store_dwordx4 v[112:115], v250, s[8:11], s49 offen offset:2048 sc1\n"
"v_mfma_f32_16x16x32_bf16 v[100:103], v[200:203], a[20:23], v[100:103]\n"
"buffer_load_dwordx4 v241, s[20:23], s48 offen offset:3072 lds\n"
"v_mfma_f32_16x16x32_bf16 v[116:119], v[200:203], a[24:27], v[116:119]\n"
"ds_read_b128 v[220:223], v243 offset:14336\n"
"v_cvt_pk_bf16_f32 v20, v20, v21\n"
"v_mfma_f32_16x16x32_bf16 v[132:135], v[200:203], a[28:31], v[132:135]\n"
"s_waitcnt lgkmcnt(2)\n"
"v_cvt_pk_bf16_f32 v21, v22, v23\n"
"v_cvt_pk_bf16_f32 v22, v28, v29\n"
"v_mfma_f32_16x16x32_bf16 v[92:95], v[204:207], a[16:19], v[92:95]\n"
"ds_read_b128 v[192:195], v242 offset:32768\n"
"v_cvt_pk_bf16_f32 v23, v30, v31\n"
"v_mfma_f32_16x16x32_bf16 v[108:111], v[204:207], a[20:23], v[108:111]\n"
"s_add_u32 m0, s38, 114688\n"
"v_cvt_pk_bf16_f32 v36, v36, v37\n"
"v_cvt_pk_bf16_f32 v37, v38, v39\n"
"v_mfma_f32_16x16x32_bf16 v[124:127], v[204:207], a[24:27], v[124:127]\n"
"s_add_u32 s48, s39, s98\n"
"v_cvt_pk_bf16_f32 v38, v44, v45\n"
"v_cvt_pk_bf16_f32 v39, v46, v47\n"
"v_mfma_f32_16x16x32_bf16 v[140:143], v[204:207], a[28:31], v[140:143]\n"
"buffer_load_dwordx4 v238, s[20:23], s48 offen offset:0 lds\n"
"v_mfma_f32_16x16x32_bf16 v[84:87], v[208:211], a[32:35], v[84:87]\n"
"ds_read_b128 v[196:199], v242 offset:34816\n"
"v_mfma_f32_16x16x32_bf16 v[100:103], v[208:211], a[36:39], v[100:103]\n"
"v_mfma_f32_16x16x32_bf16 v[116:119], v[208:211], a[40:43], v[116:119]\n"
"v_cvt_pk_bf16_f32 v52, v52, v53\n"
"v_cvt_pk_bf16_f32 v53, v54, v55\n"
"v_mfma_f32_16x16x32_bf16 v[132:135], v[208:211], a[44:47], v[132:135]\n"
"v_cvt_pk_bf16_f32 v54, v60, v61\n"
"v_cvt_pk_bf16_f32 v55, v62, v63\n"
"v_mfma_f32_16x16x32_bf16 v[92:95], v[212:215], a[32:35], v[92:95]\n"
"ds_read_b128 v[200:203], v243 offset:32768\n"
"v_mfma_f32_16x16x32_bf16 v[108:111], v[212:215], a[36:39], v[108:111]\n"
"v_mfma_f32_16x16x32_bf16 v[124:127], v[212:215], a[40:43], v[124:127]\n"
"buffer_load_dwordx4 v239, s[20:23], s48 offen offset:1024 lds\n"
"v_mfma_f32_16x16x32_bf16 v[140:143], v[212:215], a[44:47], v[140:143]\n"
"s_waitcnt lgkmcnt(1)\n"
"v_mfma_f32_16x16x32_bf16 v[84:87], v[216:219], a[48:51], v[84:87]\n"
"ds_read_b128 v[204:207], v243 offset:34816\n"
"v_mfma_f32_16x16x32_bf16 v[100:103], v[216:219], a[52:55], v[100:103]\n"
"v_mfma_f32_16x16x32_bf16 v[116:119], v[216:219], a[56:59], v[116:119]\n"
"buffer_store_dwordx4 v[120:123], v250, s[8:11], s49 offen offset:3072 sc1\n"
"v_mfma_f32_16x16x32_bf16 v[132:135], v[216:219], a[60:63], v[132:135]\n"
"ds_read_b128 v[208:211], v242 offset:36864\n"
"v_mfma_f32_16x16x32_bf16 v[92:95], v[220:223], a[48:51], v[92:95]\n"
"v_mfma_f32_16x16x32_bf16 v[108:111], v[220:223], a[52:55], v[108:111]\n"
"buffer_load_dwordx4 v240, s[20:23], s48 offen offset:2048 lds\n"
"v_mfma_f32_16x16x32_bf16 v[124:127], v[220:223], a[56:59], v[124:127]\n"
"v_mfma_f32_16x16x32_bf16 v[140:143], v[220:223], a[60:63], v[140:143]\n"
"ds_read_b128 v[212:215], v242 offset:38912\n"
"v_mfma_f32_16x16x32_bf16 v[84:87], v[192:195], a[64:67], v[84:87]\n"
"v_mfma_f32_16x16x32_bf16 v[100:103], v[192:195], a[68:71], v[100:103]\n"
"v_mfma_f32_16x16x32_bf16 v[116:119], v[192:195], a[72:75], v[116:119]\n"
"v_mfma_f32_16x16x32_bf16 v[132:135], v[192:195], a[76:79], v[132:135]\n"
"s_add_u32 s98, s98, 524288\n"
"ds_read_b128 v[216:219], v243 offset:36864\n"
"s_lshl_b32 s50, s34, 3\n"
"v_mfma_f32_16x16x32_bf16 v[92:95], v[196:199], a[64:67], v[92:95]\n"
"s_cmp_eq_u32 s98, 5242880\n"
"buffer_load_dwordx4 v241, s[20:23], s48 offen offset:3072 lds\n"
"v_mfma_f32_16x16x32_bf16 v[108:111], v[196:199], a[68:71], v[108:111]\n"
"s_cselect_b32 s98, 0, s98\n"
"s_add_u32 s51, s50, s30\n"
"s_mul_i32 s51, s51, 20480\n"
"v_mfma_f32_16x16x32_bf16 v[124:127], v[196:199], a[72:75], v[124:127]\n"
"s_add_u32 s56, s51, 65536\n"
"v_mfma_f32_16x16x32_bf16 v[140:143], v[196:199], a[76:79], v[140:143]\n"
"s_waitcnt lgkmcnt(1)\n"
"ds_read_b128 v[220:223], v243 offset:38912\n"
"s_add_u32 s57, s56, 81920\n"
"v_mfma_f32_16x16x32_bf16 v[84:87], v[200:203], a[80:83], v[84:87]\n"
"s_sub_u32 s51, s30, s50\n"
"v_mfma_f32_16x16x32_bf16 v[100:103], v[200:203], a[84:87], v[100:103]\n"
"s_add_u32 s51, s51, 120\n"
"v_mfma_f32_16x16x32_bf16 v[116:119], v[200:203], a[88:91], v[116:119]\n"
"s_mov_b32 s49, 262144\n"
"ds_read_b128 v[192:195], v244 offset:8192\n"
"s_mul_i32 s51, s51, 20480\n"
"v_mfma_f32_16x16x32_bf16 v[132:135], v[200:203], a[92:95], v[132:135]\n"
"s_add_u32 s58, s51, 65536\n"
"s_lshl_b32 s99, s99, 12\n"
"v_mfma_f32_16x16x32_bf16 v[92:95], v[204:207], a[80:83], v[92:95]\n"
"s_add_u32 s59, s58, 81920\n"
"buffer_store_dwordx4 v[128:131], v250, s[8:11], s49 offen offset:0 sc1\n"
"v_mfma_f32_16x16x32_bf16 v[108:111], v[204:207], a[84:87], v[108:111]\n"
"v_add_u32 v235, s99, v224\n"
"v_mfma_f32_16x16x32_bf16 v[124:127], v[204:207], a[88:91], v[124:127]\n"
"ds_read_b128 v[196:199], v244 offset:10240\n"
"v_mfma_f32_16x16x32_bf16 v[140:143], v[204:207], a[92:95], v[140:143]\n"
"buffer_load_dwordx4 v[64:67], v235, s[8:11], s56 offen offset:0 sc1\n"
"v_mfma_f32_16x16x32_bf16 v[84:87], v[208:211], a[96:99], v[84:87]\n"
"v_mfma_f32_16x16x32_bf16 v[100:103], v[208:211], a[100:103], v[100:103]\n"
"v_mfma_f32_16x16x32_bf16 v[116:119], v[208:211], a[104:107], v[116:119]\n"
"ds_read_b128 v[200:203], v245 offset:8192\n"
"v_mfma_f32_16x16x32_bf16 v[132:135], v[208:211], a[108:111], v[132:135]\n"
"v_mfma_f32_16x16x32_bf16 v[92:95], v[212:215], a[96:99], v[92:95]\n"
"v_mfma_f32_16x16x32_bf16 v[108:111], v[212:215], a[100:103], v[108:111]\n"
"buffer_load_dwordx4 v[68:71], v235, s[8:11], s57 offen offset:0 sc1\n"
"v_mfma_f32_16x16x32_bf16 v[124:127], v[212:215], a[104:107], v[124:127]\n"
"ds_read_b128 v[204:207], v245 offset:10240\n"
"v_mfma_f32_16x16x32_bf16 v[140:143], v[212:215], a[108:111], v[140:143]\n"
"s_waitcnt lgkmcnt(2)\n"
"v_mfma_f32_16x16x32_bf16 v[84:87], v[216:219], a[112:115], v[84:87]\n"
"v_mfma_f32_16x16x32_bf16 v[100:103], v[216:219], a[116:119], v[100:103]\n"
"ds_read_b128 v[208:211], v244 offset:12288\n"
"v_mfma_f32_16x16x32_bf16 v[116:119], v[216:219], a[120:123], v[116:119]\n"
"v_mfma_f32_16x16x32_bf16 v[132:135], v[216:219], a[124:127], v[132:135]\n"
"buffer_load_dwordx4 v[72:75], v235, s[8:11], s58 offen offset:0 sc1\n"
"v_mfma_f32_16x16x32_bf16 v[92:95], v[220:223], a[112:115], v[92:95]\n"
"v_mfma_f32_16x16x32_bf16 v[108:111], v[220:223], a[116:119], v[108:111]\n"
"ds_read_b128 v[212:215], v244 offset:14336\n"
"v_mfma_f32_16x16x32_bf16 v[124:127], v[220:223], a[120:123], v[124:127]\n"
"buffer_store_dwordx4 v[136:139], v250, s[8:11], s49 offen offset:1024 sc1\n"
"v_mfma_f32_16x16x32_bf16 v[140:143], v[220:223], a[124:127], v[140:143]\n"
"v_mfma_f32_16x16x32_bf16 v[84:87], v[192:195], a[128:131], v[84:87]\n"
"v_mfma_f32_16x16x32_bf16 v[100:103], v[192:195], a[132:135], v[100:103]\n"
"ds_read_b128 v[216:219], v245 offset:12288\n"
"v_mfma_f32_16x16x32_bf16 v[116:119], v[192:195], a[136:139], v[116:119]\n"
"buffer_load_dwordx4 v[76:79], v235, s[8:11], s59 offen offset:0 sc1\n"
"v_mfma_f32_16x16x32_bf16 v[132:135], v[192:195], a[140:143], v[132:135]\n"
"v_mfma_f32_16x16x32_bf16 v[92:95], v[196:199], a[128:131], v[92:95]\n"
"v_mfma_f32_16x16x32_bf16 v[108:111], v[196:199], a[132:135], v[108:111]\n"
"ds_read_b128 v[220:223], v245 offset:14336\n"
"v_mfma_f32_16x16x32_bf16 v[124:127], v[196:199], a[136:139], v[124:127]\n"
"v_mfma_f32_16x16x32_bf16 v[140:143], v[196:199], a[140:143], v[140:143]\n"
"s_waitcnt lgkmcnt(2)\n"
"v_mfma_f32_16x16x32_bf16 v[84:87], v[200:203], a[144:147], v[84:87]\n"
"ds_read_b128 v[192:195], v244 offset:32768\n"
"v_mfma_f32_16x16x32_bf16 v[100:103], v[200:203], a[148:151], v[100:103]\n"
"v_mfma_f32_16x16x32_bf16 v[116:119], v[200:203], a[152:155], v[116:119]\n"
"v_mfma_f32_16x16x32_bf16 v[132:135], v[200:203], a[156:159], v[132:135]\n"
"v_mfma_f32_16x16x32_bf16 v[92:95], v[204:207], a[144:147], v[92:95]\n"
"ds_read_b128 v[196:199], v244 offset:34816\n"
"v_mfma_f32_16x16x32_bf16 v[108:111], v[204:207], a[148:151], v[108:111]\n"
"v_mfma_f32_16x16x32_bf16 v[124:127], v[204:207], a[152:155], v[124:127]\n"
"v_mfma_f32_16x16x32_bf16 v[140:143], v[204:207], a[156:159], v[140:143]\n"
"v_mfma_f32_16x16x32_bf16 v[84:87], v[208:211], a[160:163], v[84:87]\n"
"ds_read_b128 v[200:203], v245 offset:32768\n"
"v_mfma_f32_16x16x32_bf16 v[100:103], v[208:211], a[164:167], v[100:103]\n"
"buffer_store_dwordx4 v[144:147], v250, s[8:11], s49 offen offset:2048 sc1\n"
"v_mfma_f32_16x16x32_bf16 v[116:119], v[208:211], a[168:171], v[116:119]\n"
"v_mfma_f32_16x16x32_bf16 v[132:135], v[208:211], a[172:175], v[132:135]\n"
"v_mfma_f32_16x16x32_bf16 v[92:95], v[212:215], a[160:163], v[92:95]\n"
"ds_read_b128 v[204:207], v245 offset:34816\n"
"v_mfma_f32_16x16x32_bf16 v[108:111], v[212:215], a[164:167], v[108:111]\n"
"v_mfma_f32_16x16x32_bf16 v[124:127], v[212:215], a[168:171], v[124:127]\n"
"v_mfma_f32_16x16x32_bf16 v[140:143], v[212:215], a[172:175], v[140:143]\n"
"s_waitcnt lgkmcnt(1)\n"
"ds_read_b128 v[208:211], v244 offset:36864\n"
"v_mfma_f32_16x16x32_bf16 v[84:87], v[216:219], a[176:179], v[84:87]\n"
"v_mfma_f32_16x16x32_bf16 v[100:103], v[216:219], a[180:183], v[100:103]\n"
"v_mfma_f32_16x16x32_bf16 v[116:119], v[216:219], a[184:187], v[116:119]\n"
"v_mfma_f32_16x16x32_bf16 v[132:135], v[216:219], a[188:191], v[132:135]\n"
"ds_read_b128 v[212:215], v244 offset:38912\n"
"v_mfma_f32_16x16x32_bf16 v[92:95], v[220:223], a[176:179], v[92:95]\n"
"v_mfma_f32_16x16x32_bf16 v[108:111], v[220:223], a[180:183], v[108:111]\n"
"v_mfma_f32_16x16x32_bf16 v[124:127], v[220:223], a[184:187], v[124:127]\n"
"v_mfma_f32_16x16x32_bf16 v[140:143], v[220:223], a[188:191], v[140:143]\n"
"ds_read_b128 v[216:219], v245 offset:36864\n"
"v_mfma_f32_16x16x32_bf16 v[84:87], v[192:195], a[192:195], v[84:87]\n"
"v_mfma_f32_16x16x32_bf16 v[100:103], v[192:195], a[196:199], v[100:103]\n"
"v_mfma_f32_16x16x32_bf16 v[116:119], v[192:195], a[200:203], v[116:119]\n"
"buffer_store_dwordx4 v[152:155], v250, s[8:11], s49 offen offset:3072 sc1\n"
"v_mfma_f32_16x16x32_bf16 v[132:135], v[192:195], a[204:207], v[132:135]\n"
"ds_read_b128 v[220:223], v245 offset:38912\n"
"v_mfma_f32_16x16x32_bf16 v[92:95], v[196:199], a[192:195], v[92:95]\n"
"v_mfma_f32_16x16x32_bf16 v[108:111], v[196:199], a[196:199], v[108:111]\n"
"v_mfma_f32_16x16x32_bf16 v[124:127], v[196:199], a[200:203], v[124:127]\n"
"v_mfma_f32_16x16x32_bf16 v[140:143], v[196:199], a[204:207], v[140:143]\n"
"v_mfma_f32_16x16x32_bf16 v[84:87], v[200:203], a[208:211], v[84:87]\n"
"v_mfma_f32_16x16x32_bf16 v[100:103], v[200:203], a[212:215], v[100:103]\n"
"v_mfma_f32_16x16x32_bf16 v[116:119], v[200:203], a[216:219], v[116:119]\n"
"v_mfma_f32_16x16x32_bf16 v[132:135], v[200:203], a[220:223], v[132:135]\n"
"s_waitcnt lgkmcnt(0)\n"
"v_lshrrev_b32 v251, 2, v224\n"
"v_mfma_f32_16x16x32_bf16 v[92:95], v[204:207], a[208:211], v[92:95]\n"
"s_add_u32 s44, s44, 65536\n"
"v_and_b32 v251, 28, v251\n"
"v_mfma_f32_16x16x32_bf16 v[108:111], v[204:207], a[212:215], v[108:111]\n"
"s_lshl_b32 s92, s30, 10\n"
"s_waitcnt vmcnt(26)\n"
"s_barrier\n"
"v_mfma_f32_16x16x32_bf16 v[124:127], v[204:207], a[216:219], v[124:127]\n"
"s_and_b32 s93, s92, 1024\n"
"ds_read_b128 v[192:195], v242 offset:16384\n"
"s_add_u32 m0, s38, 73728\n"
"v_mfma_f32_16x16x32_bf16 v[140:143], v[204:207], a[220:223], v[140:143]\n"
"s_add_u32 s48, s39, s98\n"
"ds_read_b32 v234, v251 offset:40960\n"
"s_add_u32 s93, s93, 8192\n"
"v_mfma_f32_16x16x32_bf16 v[84:87], v[208:211], a[224:227], v[84:87]\n"
"buffer_load_dwordx4 v238, s[20:23], s48 offen offset:0 lds\n"
"v_mfma_f32_16x16x32_bf16 v[100:103], v[208:211], a[228:231], v[100:103]\n"
"v_mfma_f32_16x16x32_bf16 v[116:119], v[208:211], a[232:235], v[116:119]\n"
"ds_read_b128 v[196:199], v242 offset:18432\n"
"v_mfma_f32_16x16x32_bf16 v[132:135], v[208:211], a[236:239], v[132:135]\n"
"v_mfma_f32_16x16x32_bf16 v[92:95], v[212:215], a[224:227], v[92:95]\n"
"buffer_load_dwordx4 v239, s[20:23], s48 offen offset:1024 lds\n"
"v_mfma_f32_16x16x32_bf16 v[108:111], v[212:215], a[228:231], v[108:111]\n"
"s_waitcnt lgkmcnt(1)\n"
"v_not_b32 v251, v234\n"
"v_bfe_u32 v252, v251, 4, 7\n"
"v_mfma_f32_16x16x32_bf16 v[124:127], v[212:215], a[232:235], v[124:127]\n"
"ds_read_b128 v[200:203], v243 offset:16384\n"
"v_mul_u32_u24 v252, 20480, v252\n"
"v_mfma_f32_16x16x32_bf16 v[140:143], v[212:215], a[236:239], v[140:143]\n"
"v_and_b32 v251, 15, v251\n"
"v_lshl_add_u32 v251, v251, 4, v252\n"
"v_mfma_f32_16x16x32_bf16 v[84:87], v[216:219], a[240:243], v[84:87]\n"
"s_add_u32 s98, s98, 524288\n"
"buffer_load_dwordx4 v240, s[20:23], s48 offen offset:2048 lds\n"
"v_mfma_f32_16x16x32_bf16 v[100:103], v[216:219], a[244:247], v[100:103]\n"
"v_and_b32 v252, 0x380, v224\n"
"v_lshl_add_u32 v251, v252, 1, v251\n"
"v_mfma_f32_16x16x32_bf16 v[116:119], v[216:219], a[248:251], v[116:119]\n"
"ds_read_b128 v[204:207], v243 offset:18432\n"
"v_mfma_f32_16x16x32_bf16 v[132:135], v[216:219], a[252:255], v[132:135]\n"
"v_mfma_f32_16x16x32_bf16 v[92:95], v[220:223], a[240:243], v[92:95]\n"
"buffer_load_dwordx4 v241, s[20:23], s48 offen offset:3072 lds\n"
"v_mfma_f32_16x16x32_bf16 v[108:111], v[220:223], a[244:247], v[108:111]\n"
"ds_read_b128 v[208:211], v242 offset:20480\n"
"v_mfma_f32_16x16x32_bf16 v[124:127], v[220:223], a[248:251], v[124:127]\n"
"s_add_u32 m0, s38, 122880\n"
"s_add_u32 s48, s39, s98\n"
"v_mfma_f32_16x16x32_bf16 v[140:143], v[220:223], a[252:255], v[140:143]\n"
"v_mfma_f32_16x16x32_bf16 v[0:3], v[192:195], a[0:3], 0\n"
"buffer_load_dwordx4 v238, s[20:23], s48 offen offset:0 lds\n"
"v_mfma_f32_16x16x32_bf16 v[16:19], v[192:195], a[4:7], 0\n"
"ds_read_b128 v[212:215], v242 offset:22528\n"
"v_mfma_f32_16x16x32_bf16 v[32:35], v[192:195], a[8:11], 0\n"
"v_mfma_f32_16x16x32_bf16 v[48:51], v[192:195], a[12:15], 0\n"
"s_waitcnt lgkmcnt(2)\n"
"v_cvt_pk_bf16_f32 v12, v84, v85\n"
"v_cvt_pk_bf16_f32 v13, v86, v87\n"
"v_mfma_f32_16x16x32_bf16 v[8:11], v[196:199], a[0:3], 0\n"
"v_cvt_pk_bf16_f32 v14, v92, v93\n"
"v_cvt_pk_bf16_f32 v15, v94, v95\n"
"v_mfma_f32_16x16x32_bf16 v[24:27], v[196:199], a[4:7], 0\n"
"ds_read_b128 v[216:219], v243 offset:20480\n"
"v_mfma_f32_16x16x32_bf16 v[40:43], v[196:199], a[8:11], 0\n"
"buffer_load_dwordx4 v239, s[20:23], s48 offen offset:1024 lds\n"
"v_mfma_f32_16x16x32_bf16 v[56:59], v[196:199], a[12:15], 0\n"
"v_mfma_f32_16x16x32_bf16 v[0:3], v[200:203], a[16:19], v[0:3]\n"
"buffer_load_dwordx4 a[0:3], v235, s[8:11], s56 offen offset:1024 sc1\n"
"v_mfma_f32_16x16x32_bf16 v[16:19], v[200:203], a[20:23], v[16:19]\n"
"ds_read_b128 v[220:223], v243 offset:22528\n"
"v_cvt_pk_bf16_f32 v28, v100, v101\n"
"v_mfma_f32_16x16x32_bf16 v[32:35], v[200:203], a[24:27], v[32:35]\n"
"buffer_load_dwordx4 v240, s[20:23], s48 offen offset:2048 lds\n"
"v_mfma_f32_16x16x32_bf16 v[48:51], v[200:203], a[28:31], v[48:51]\n"
"v_cvt_pk_bf16_f32 v29, v102, v103\n"
"v_cvt_pk_bf16_f32 v30, v108, v109\n"
"v_mfma_f32_16x16x32_bf16 v[8:11], v[204:207], a[16:19], v[8:11]\n"
"s_add_u32 s98, s98, 524288\n"
"ds_read_b128 v[192:195], v242 offset:40960\n"
"v_cvt_pk_bf16_f32 v31, v110, v111\n"
"v_mfma_f32_16x16x32_bf16 v[24:27], v[204:207], a[20:23], v[24:27]\n"
"s_cmp_eq_u32 s98, 5242880\n"
"v_cvt_pk_bf16_f32 v44, v116, v117\n"
"v_cvt_pk_bf16_f32 v45, v118, v119\n"
"v_mfma_f32_16x16x32_bf16 v[40:43], v[204:207], a[24:27], v[40:43]\n"
"s_cselect_b32 s98, 0, s98\n"
"buffer_load_dwordx4 a[4:7], v235, s[8:11], s57 offen offset:1024 sc1\n"
"v_mfma_f32_16x16x32_bf16 v[56:59], v[204:207], a[28:31], v[56:59]\n"
"s_waitcnt lgkmcnt(2)\n"
"buffer_load_dwordx4 v241, s[20:23], s48 offen offset:3072 lds\n"
"v_mfma_f32_16x16x32_bf16 v[0:3], v[208:211], a[32:35], v[0:3]\n"
"ds_read_b128 v[196:199], v242 offset:43008\n"
"v_cvt_pk_bf16_f32 v46, v124, v125\n"
"v_mfma_f32_16x16x32_bf16 v[16:19], v[208:211], a[36:39], v[16:19]\n"
"v_cvt_pk_bf16_f32 v47, v126, v127\n"
"v_mfma_f32_16x16x32_bf16 v[32:35], v[208:211], a[40:43], v[32:35]\n"
"v_cvt_pk_bf16_f32 v60, v132, v133\n"
"v_cvt_pk_bf16_f32 v61, v134, v135\n"
"v_mfma_f32_16x16x32_bf16 v[48:51], v[208:211], a[44:47], v[48:51]\n"
"v_cvt_pk_bf16_f32 v62, v140, v141\n"
"v_cvt_pk_bf16_f32 v63, v142, v143\n"
"v_mfma_f32_16x16x32_bf16 v[8:11], v[212:215], a[32:35], v[8:11]\n"
"ds_read_b128 v[200:203], v243 offset:40960\n"
"v_mfma_f32_16x16x32_bf16 v[24:27], v[212:215], a[36:39], v[24:27]\n"
"s_add_u32 m0, s92, 45056\n"
"buffer_load_dwordx4 a[8:11], v235, s[8:11], s58 offen offset:1024 sc1\n"
"v_mfma_f32_16x16x32_bf16 v[40:43], v[212:215], a[40:43], v[40:43]\n"
"s_lshl_b32 s48, s92, 1\n"
"s_add_u32 s48, s48, s44\n"
"v_mfma_f32_16x16x32_bf16 v[56:59], v[212:215], a[44:47], v[56:59]\n"
"v_mfma_f32_16x16x32_bf16 v[0:3], v[216:219], a[48:51], v[0:3]\n"
"ds_read_b128 v[204:207], v243 offset:43008\n"
"v_mfma_f32_16x16x32_bf16 v[16:19], v[216:219], a[52:55], v[16:19]\n"
"buffer_load_dwordx4 v251, s[8:11], s48 offen sc1 lds\n"
"v_mfma_f32_16x16x32_bf16 v[32:35], v[216:219], a[56:59], v[32:35]\n"
"buffer_load_dwordx4 a[12:15], v235, s[8:11], s59 offen offset:1024 sc1\n"
"v_mfma_f32_16x16x32_bf16 v[48:51], v[216:219], a[60:63], v[48:51]\n"
"s_waitcnt lgkmcnt(1)\n"
"ds_read_b128 v[208:211], v242 offset:45056\n"
"v_mfma_f32_16x16x32_bf16 v[8:11], v[220:223], a[48:51], v[8:11]\n"
"v_mfma_f32_16x16x32_bf16 v[24:27], v[220:223], a[52:55], v[24:27]\n"
"v_mfma_f32_16x16x32_bf16 v[40:43], v[220:223], a[56:59], v[40:43]\n"
"v_mfma_f32_16x16x32_bf16 v[56:59], v[220:223], a[60:63], v[56:59]\n"
"ds_read_b128 v[212:215], v242 offset:47104\n"
"v_mfma_f32_16x16x32_bf16 v[0:3], v[192:195], a[64:67], v[0:3]\n"
"buffer_load_dwordx4 a[16:19], v235, s[8:11], s56 offen offset:2048 sc1\n"
"v_mfma_f32_16x16x32_bf16 v[16:19], v[192:195], a[68:71], v[16:19]\n"
"v_mfma_f32_16x16x32_bf16 v[32:35], v[192:195], a[72:75], v[32:35]\n"
"v_mfma_f32_16x16x32_bf16 v[48:51], v[192:195], a[76:79], v[48:51]\n"
"ds_read_b128 v[216:219], v243 offset:45056\n"
"v_mfma_f32_16x16x32_bf16 v[8:11], v[196:199], a[64:67], v[8:11]\n"
"v_mfma_f32_16x16x32_bf16 v[24:27], v[196:199], a[68:71], v[24:27]\n"
"s_add_u32 m0, s92, 49152\n"
"s_lshl_b32 s48, s92, 1\n"
"v_mfma_f32_16x16x32_bf16 v[40:43], v[196:199], a[72:75], v[40:43]\n"
"s_add_u32 s48, s48, s44\n"
"buffer_load_dwordx4 a[20:23], v235, s[8:11], s57 offen offset:2048 sc1\n"
"v_mfma_f32_16x16x32_bf16 v[56:59], v[196:199], a[76:79], v[56:59]\n"
"s_add_u32 s48, s48, 8192\n"
"ds_read_b128 v[220:223], v243 offset:47104\n"
"v_mfma_f32_16x16x32_bf16 v[0:3], v[200:203], a[80:83], v[0:3]\n"
"buffer_load_dwordx4 v251, s[8:11], s48 offen sc1 lds\n"
"v_mfma_f32_16x16x32_bf16 v[16:19], v[200:203], a[84:87], v[16:19]\n"
"v_mfma_f32_16x16x32_bf16 v[32:35], v[200:203], a[88:91], v[32:35]\n"
"ds_read_b128 v[192:195], v244 offset:16384\n"
"v_mfma_f32_16x16x32_bf16 v[48:51], v[200:203], a[92:95], v[48:51]\n"
"s_waitcnt lgkmcnt(2)\n"
"v_mfma_f32_16x16x32_bf16 v[8:11], v[204:207], a[80:83], v[8:11]\n"
"buffer_load_dwordx4 a[24:27], v235, s[8:11], s58 offen offset:2048 sc1\n"
"v_mfma_f32_16x16x32_bf16 v[24:27], v[204:207], a[84:87], v[24:27]\n"
"v_mfma_f32_16x16x32_bf16 v[40:43], v[204:207], a[88:91], v[40:43]\n"
"ds_read_b128 v[196:199], v244 offset:18432\n"
"v_mfma_f32_16x16x32_bf16 v[56:59], v[204:207], a[92:95], v[56:59]\n"
"v_mfma_f32_16x16x32_bf16 v[0:3], v[208:211], a[96:99], v[0:3]\n"
"v_mfma_f32_16x16x32_bf16 v[16:19], v[208:211], a[100:103], v[16:19]\n"
"v_mfma_f32_16x16x32_bf16 v[32:35], v[208:211], a[104:107], v[32:35]\n"
"ds_read_b128 v[200:203], v245 offset:16384\n"
"v_mfma_f32_16x16x32_bf16 v[48:51], v[208:211], a[108:111], v[48:51]\n"
"buffer_load_dwordx4 a[28:31], v235, s[8:11], s59 offen offset:2048 sc1\n"
"v_mfma_f32_16x16x32_bf16 v[8:11], v[212:215], a[96:99], v[8:11]\n"
"v_mfma_f32_16x16x32_bf16 v[24:27], v[212:215], a[100:103], v[24:27]\n"
"s_add_u32 m0, s93, 45056\n"
"s_lshl_b32 s48, s93, 1\n"
"v_mfma_f32_16x16x32_bf16 v[40:43], v[212:215], a[104:107], v[40:43]\n"
"s_add_u32 s48, s48, s44\n"
"ds_read_b128 v[204:207], v245 offset:18432\n"
"v_mfma_f32_16x16x32_bf16 v[56:59], v[212:215], a[108:111], v[56:59]\n"
"v_mfma_f32_16x16x32_bf16 v[0:3], v[216:219], a[112:115], v[0:3]\n"
"buffer_load_dwordx4 a[32:35], v235, s[8:11], s56 offen offset:3072 sc1\n"
"v_mfma_f32_16x16x32_bf16 v[16:19], v[216:219], a[116:119], v[16:19]\n"
"buffer_load_dwordx4 v251, s[8:11], s48 offen sc1 lds\n"
"v_mfma_f32_16x16x32_bf16 v[32:35], v[216:219], a[120:123], v[32:35]\n"
"ds_read_b128 v[208:211], v244 offset:20480\n"
"v_mfma_f32_16x16x32_bf16 v[48:51], v[216:219], a[124:127], v[48:51]\n"
"s_waitcnt lgkmcnt(2)\n"
"v_mfma_f32_16x16x32_bf16 v[8:11], v[220:223], a[112:115], v[8:11]\n"
"v_mfma_f32_16x16x32_bf16 v[24:27], v[220:223], a[116:119], v[24:27]\n"
"ds_read_b128 v[212:215], v244 offset:22528\n"
"v_mfma_f32_16x16x32_bf16 v[40:43], v[220:223], a[120:123], v[40:43]\n"
"buffer_load_dwordx4 a[36:39], v235, s[8:11], s57 offen offset:3072 sc1\n"
"v_mfma_f32_16x16x32_bf16 v[56:59], v[220:223], a[124:127], v[56:59]\n"
"v_mfma_f32_16x16x32_bf16 v[0:3], v[192:195], a[128:131], v[0:3]\n"
"v_mfma_f32_16x16x32_bf16 v[16:19], v[192:195], a[132:135], v[16:19]\n"
"ds_read_b128 v[216:219], v245 offset:20480\n"
"v_mfma_f32_16x16x32_bf16 v[32:35], v[192:195], a[136:139], v[32:35]\n"
"v_mfma_f32_16x16x32_bf16 v[48:51], v[192:195], a[140:143], v[48:51]\n"
"v_mfma_f32_16x16x32_bf16 v[8:11], v[196:199], a[128:131], v[8:11]\n"
"buffer_load_dwordx4 a[40:43], v235, s[8:11], s58 offen offset:3072 sc1\n"
"v_mfma_f32_16x16x32_bf16 v[24:27], v[196:199], a[132:135], v[24:27]\n"
"ds_read_b128 v[220:223], v245 offset:22528\n"
"v_mfma_f32_16x16x32_bf16 v[40:43], v[196:199], a[136:139], v[40:43]\n"
"v_mfma_f32_16x16x32_bf16 v[56:59], v[196:199], a[140:143], v[56:59]\n"
"v_mfma_f32_16x16x32_bf16 v[0:3], v[200:203], a[144:147], v[0:3]\n"
"s_add_u32 s99, s99, 4096\n"
"ds_read_b128 v[192:195], v244 offset:40960\n"
"v_mfma_f32_16x16x32_bf16 v[16:19], v[200:203], a[148:151], v[16:19]\n"
"s_cmp_eq_u32 s99, 20480\n"
"s_cselect_b32 s99, 0, s99\n"
"v_mfma_f32_16x16x32_bf16 v[32:35], v[200:203], a[152:155], v[32:35]\n"
"buffer_load_dwordx4 a[44:47], v235, s[8:11], s59 offen offset:3072 sc1\n"
"v_mfma_f32_16x16x32_bf16 v[48:51], v[200:203], a[156:159], v[48:51]\n"
"s_waitcnt lgkmcnt(2)\n"
"v_mfma_f32_16x16x32_bf16 v[8:11], v[204:207], a[144:147], v[8:11]\n"
"ds_read_b128 v[196:199], v244 offset:43008\n"
"v_mfma_f32_16x16x32_bf16 v[24:27], v[204:207], a[148:151], v[24:27]\n"
"v_mfma_f32_16x16x32_bf16 v[40:43], v[204:207], a[152:155], v[40:43]\n"
"v_mfma_f32_16x16x32_bf16 v[56:59], v[204:207], a[156:159], v[56:59]\n"
"v_add_u32 v235, s99, v224\n"
"v_mfma_f32_16x16x32_bf16 v[0:3], v[208:211], a[160:163], v[0:3]\n"
"s_sub_u32 s53, s37, 65536\n"
"ds_read_b128 v[200:203], v245 offset:40960\n"
"v_mfma_f32_16x16x32_bf16 v[16:19], v[208:211], a[164:167], v[16:19]\n"
"buffer_load_dwordx4 a[48:51], v235, s[8:11], s56 offen offset:0 sc1\n"
"v_mfma_f32_16x16x32_bf16 v[32:35], v[208:211], a[168:171], v[32:35]\n"
"v_subrev_u32 v246, s53, v246\n"
"v_subrev_u32 v247, s53, v247\n"
"v_mfma_f32_16x16x32_bf16 v[48:51], v[208:211], a[172:175], v[48:51]\n"
"v_subrev_u32 v248, s53, v248\n"
"v_subrev_u32 v249, s53, v249\n"
"v_mfma_f32_16x16x32_bf16 v[8:11], v[212:215], a[160:163], v[8:11]\n"
"ds_read_b128 v[204:207], v245 offset:43008\n"
"v_mfma_f32_16x16x32_bf16 v[24:27], v[212:215], a[164:167], v[24:27]\n"
"v_mfma_f32_16x16x32_bf16 v[40:43], v[212:215], a[168:171], v[40:43]\n"
"buffer_load_dwordx4 a[52:55], v235, s[8:11], s57 offen offset:0 sc1\n"
"v_mfma_f32_16x16x32_bf16 v[56:59], v[212:215], a[172:175], v[56:59]\n"
"ds_read_b128 v[208:211], v244 offset:45056\n"
"v_mfma_f32_16x16x32_bf16 v[0:3], v[216:219], a[176:179], v[0:3]\n"
"v_mfma_f32_16x16x32_bf16 v[16:19], v[216:219], a[180:183], v[16:19]\n"
"v_mfma_f32_16x16x32_bf16 v[32:35], v[216:219], a[184:187], v[32:35]\n"
"v_mfma_f32_16x16x32_bf16 v[48:51], v[216:219], a[188:191], v[48:51]\n"
"s_waitcnt lgkmcnt(1)\n"
"ds_read_b128 v[212:215], v244 offset:47104\n"
"v_mfma_f32_16x16x32_bf16 v[8:11], v[220:223], a[176:179], v[8:11]\n"
"buffer_load_dwordx4 a[56:59], v235, s[8:11], s58 offen offset:0 sc1\n"
"v_mfma_f32_16x16x32_bf16 v[24:27], v[220:223], a[180:183], v[24:27]\n"
"v_mfma_f32_16x16x32_bf16 v[40:43], v[220:223], a[184:187], v[40:43]\n"
"v_mfma_f32_16x16x32_bf16 v[56:59], v[220:223], a[188:191], v[56:59]\n"
"ds_read_b128 v[216:219], v245 offset:45056\n"
"v_mfma_f32_16x16x32_bf16 v[0:3], v[192:195], a[192:195], v[0:3]\n"
"v_mfma_f32_16x16x32_bf16 v[16:19], v[192:195], a[196:199], v[16:19]\n"
"v_mfma_f32_16x16x32_bf16 v[32:35], v[192:195], a[200:203], v[32:35]\n"
"buffer_load_dwordx4 a[60:63], v235, s[8:11], s59 offen offset:0 sc1\n"
"v_mfma_f32_16x16x32_bf16 v[48:51], v[192:195], a[204:207], v[48:51]\n"
"ds_read_b128 v[220:223], v245 offset:47104\n"
"v_mfma_f32_16x16x32_bf16 v[8:11], v[196:199], a[192:195], v[8:11]\n"
"v_mfma_f32_16x16x32_bf16 v[24:27], v[196:199], a[196:199], v[24:27]\n"
"v_mfma_f32_16x16x32_bf16 v[40:43], v[196:199], a[200:203], v[40:43]\n"
"v_mfma_f32_16x16x32_bf16 v[56:59], v[196:199], a[204:207], v[56:59]\n"
"v_mfma_f32_16x16x32_bf16 v[0:3], v[200:203], a[208:211], v[0:3]\n"
"s_lshl_b32 s50, s35, 3\n"
"buffer_load_dwordx4 a[64:67], v235, s[8:11], s56 offen offset:1024 sc1\n"
"v_mfma_f32_16x16x32_bf16 v[16:19], v[200:203], a[212:215], v[16:19]\n"
"s_add_u32 s50, s50, s34\n"
"s_lshl_b32 s50, s50, 3\n"
"s_add_u32 s50, s50, 2304\n"
"v_mfma_f32_16x16x32_bf16 v[32:35], v[200:203], a[216:219], v[32:35]\n"
"s_add_u32 s51, s50, s45\n"
"v_mfma_f32_16x16x32_bf16 v[48:51], v[200:203], a[220:223], v[48:51]\n"
"v_mfma_f32_16x16x32_bf16 v[8:11], v[204:207], a[208:211], v[8:11]\n"
"v_mfma_f32_16x16x32_bf16 v[24:27], v[204:207], a[212:215], v[24:27]\n"
"s_waitcnt vmcnt(36) lgkmcnt(0)\n"
"s_barrier\n"
"v_mfma_f32_16x16x32_bf16 v[40:43], v[204:207], a[216:219], v[40:43]\n"
"s_add_u32 m0, s38, 81920\n"
"ds_read_b64_tr_b16 a[128:129], v246 offset:0\n"
"s_add_u32 s48, s39, s98\n"
"v_mfma_f32_16x16x32_bf16 v[56:59], v[204:207], a[220:223], v[56:59]\n"
"buffer_load_dwordx4 a[68:71], v235, s[8:11], s57 offen offset:1024 sc1\n"
"v_mfma_f32_16x16x32_bf16 v[0:3], v[208:211], a[224:227], v[0:3]\n"
"buffer_store_dwordx2 v[232:233], v230, s[8:11], s51 offen sc1\n"
"v_mfma_f32_16x16x32_bf16 v[16:19], v[208:211], a[228:231], v[16:19]\n"
"ds_read_b64_tr_b16 a[130:131], v246 offset:512\n"
"v_mfma_f32_16x16x32_bf16 v[32:35], v[208:211], a[232:235], v[32:35]\n"
"buffer_load_dwordx4 v238, s[20:23], s48 offen offset:0 lds\n"
"v_mfma_f32_16x16x32_bf16 v[48:51], v[208:211], a[236:239], v[48:51]\n"
"ds_read_b64_tr_b16 a[132:133], v247 offset:0\n"
"v_add_u32 v242, 49152, v246\n"
"v_mfma_f32_16x16x32_bf16 v[8:11], v[212:215], a[224:227], v[8:11]\n"
"ds_read_b64_tr_b16 a[134:135], v247 offset:512\n"
"v_add_u32 v243, 49152, v247\n"
"v_mfma_f32_16x16x32_bf16 v[24:27], v[212:215], a[228:231], v[24:27]\n"
"buffer_load_dwordx4 a[72:75], v235, s[8:11], s58 offen offset:1024 sc1\n"
"v_mfma_f32_16x16x32_bf16 v[40:43], v[212:215], a[232:235], v[40:43]\n"
"ds_read_b64_tr_b16 a[136:137], v248 offset:0\n"
"v_add_u32 v244, 49152, v248\n"
"v_mfma_f32_16x16x32_bf16 v[56:59], v[212:215], a[236:239], v[56:59]\n"
"v_add_u32 v245, 49152, v249\n"
"v_mfma_f32_16x16x32_bf16 v[0:3], v[216:219], a[240:243], v[0:3]\n"
"ds_read_b64_tr_b16 a[138:139], v248 offset:512\n"
"v_mfma_f32_16x16x32_bf16 v[16:19], v[216:219], a[244:247], v[16:19]\n"
"buffer_load_dwordx4 v239, s[20:23], s48 offen offset:1024 lds\n"
"v_mfma_f32_16x16x32_bf16 v[32:35], v[216:219], a[248:251], v[32:35]\n"
"ds_read_b64_tr_b16 a[140:141], v249 offset:0\n"
"v_mfma_f32_16x16x32_bf16 v[48:51], v[216:219], a[252:255], v[48:51]\n"
"buffer_load_dwordx4 a[76:79], v235, s[8:11], s59 offen offset:1024 sc1\n"
"v_mfma_f32_16x16x32_bf16 v[8:11], v[220:223], a[240:243], v[8:11]\n"
"ds_read_b64_tr_b16 a[142:143], v249 offset:512\n"
"v_mfma_f32_16x16x32_bf16 v[24:27], v[220:223], a[244:247], v[24:27]\n"
"v_mfma_f32_16x16x32_bf16 v[40:43], v[220:223], a[248:251], v[40:43]\n"
"ds_read_b64_tr_b16 a[144:145], v246 offset:4096\n"
"v_mfma_f32_16x16x32_bf16 v[56:59], v[220:223], a[252:255], v[56:59]\n"
"s_waitcnt vmcnt(40) lgkmcnt(3)\n"
"v_mfma_f32_16x16x32_bf16 v[80:83], a[128:131], v[64:67], 0\n"
"s_waitcnt vmcnt(39)\n"
"ds_read_b64_tr_b16 a[146:147], v246 offset:4608\n"
"v_mfma_f32_16x16x32_bf16 v[88:91], a[128:131], v[68:71], 0\n"
"s_waitcnt vmcnt(38)\n"
"buffer_load_dwordx4 a[80:83], v235, s[8:11], s56 offen offset:2048 sc1\n"
"v_mfma_f32_16x16x32_bf16 v[96:99], a[128:131], v[72:75], 0\n"
"s_waitcnt vmcnt(37)\n"
"buffer_load_dwordx4 v240, s[20:23], s48 offen offset:2048 lds\n"
"v_mfma_f32_16x16x32_bf16 v[104:107], a[128:131], v[76:79], 0\n"
"ds_read_b64_tr_b16 a[148:149], v247 offset:4096\n"
"v_cvt_pk_bf16_f32 v0, v0, v1\n"
"v_mfma_f32_16x16x32_bf16 v[84:87], a[132:135], v[64:67], 0\n"
"ds_read_b64_tr_b16 a[150:151], v247 offset:4608\n"
"v_cvt_pk_bf16_f32 v1, v2, v3\n"
"v_mfma_f32_16x16x32_bf16 v[92:95], a[132:135], v[68:71], 0\n"
"ds_read_b64_tr_b16 a[152:153], v248 offset:4096\n"
"v_cvt_pk_bf16_f32 v2, v8, v9\n"
"v_mfma_f32_16x16x32_bf16 v[100:103], a[132:135], v[72:75], 0\n"
"v_cvt_pk_bf16_f32 v3, v10, v11\n"
"v_mfma_f32_16x16x32_bf16 v[108:111], a[132:135], v[76:79], 0\n"
"ds_read_b64_tr_b16 a[154:155], v248 offset:4608\n"
"v_mfma_f32_16x16x32_bf16 v[112:115], a[136:139], v[64:67], 0\n"
"s_add_u32 s98, s98, 524288\n"
"buffer_load_dwordx4 a[84:87], v235, s[8:11], s57 offen offset:2048 sc1\n"
"v_mfma_f32_16x16x32_bf16 v[120:123], a[136:139], v[68:71], 0\n"
"buffer_load_dwordx4 v241, s[20:23], s48 offen offset:3072 lds\n"
"v_mfma_f32_16x16x32_bf16 v[128:131], a[136:139], v[72:75], 0\n"
"ds_read_b64_tr_b16 a[156:157], v249 offset:4096\n"
"v_cvt_pk_bf16_f32 v16, v16, v17\n"
"v_mfma_f32_16x16x32_bf16 v[136:139], a[136:139], v[76:79], 0\n"
"s_waitcnt lgkmcnt(3)\n"
"ds_read_b64_tr_b16 a[158:159], v249 offset:4608\n"
"v_cvt_pk_bf16_f32 v17, v18, v19\n"
"v_mfma_f32_16x16x32_bf16 v[116:119], a[140:143], v[64:67], 0\n"
"v_cvt_pk_bf16_f32 v18, v24, v25\n"
"v_cvt_pk_bf16_f32 v19, v26, v27\n"
"v_mfma_f32_16x16x32_bf16 v[124:127], a[140:143], v[68:71], 0\n"
"ds_read_b64_tr_b16 a[160:161], v246 offset:24576\n"
"v_cvt_pk_bf16_f32 v32, v32, v33\n"
"v_mfma_f32_16x16x32_bf16 v[132:135], a[140:143], v[72:75], 0\n"
"s_add_u32 m0, s38, 131072\n"
"buffer_load_dwordx4 a[88:91], v235, s[8:11], s58 offen offset:2048 sc1\n"
"v_mfma_f32_16x16x32_bf16 v[140:143], a[140:143], v[76:79], 0\n"
"s_add_u32 s48, s39, s98\n"
"ds_read_b64_tr_b16 a[162:163], v246 offset:25088\n"
"v_cvt_pk_bf16_f32 v33, v34, v35\n"
"v_mfma_f32_16x16x32_bf16 v[144:147], a[144:147], v[64:67], 0\n"
"buffer_load_dwordx4 v238, s[20:23], s48 offen offset:0 lds\n"
"v_mfma_f32_16x16x32_bf16 v[152:155], a[144:147], v[68:71], 0\n"
"ds_read_b64_tr_b16 a[164:165], v247 offset:24576\n"
"v_cvt_pk_bf16_f32 v34, v40, v41\n"
"v_mfma_f32_16x16x32_bf16 v[160:163], a[144:147], v[72:75], 0\n"
"v_cvt_pk_bf16_f32 v35, v42, v43\n"
"v_cvt_pk_bf16_f32 v48, v48, v49\n"
"v_mfma_f32_16x16x32_bf16 v[168:171], a[144:147], v[76:79], 0\n"
"ds_read_b64_tr_b16 a[166:167], v247 offset:25088\n"
"v_cvt_pk_bf16_f32 v49, v50, v51\n"
"v_mfma_f32_16x16x32_bf16 v[148:151], a[148:151], v[64:67], 0\n"
"v_cvt_pk_bf16_f32 v50, v56, v57\n"
"v_cvt_pk_bf16_f32 v51, v58, v59\n"
"v_mfma_f32_16x16x32_bf16 v[156:159], a[148:151], v[68:71], 0\n"
"ds_read_b64_tr_b16 a[168:169], v248 offset:24576\n"
"v_mfma_f32_16x16x32_bf16 v[164:167], a[148:151], v[72:75], 0\n"
"buffer_load_dwordx4 a[92:95], v235, s[8:11], s59 offen offset:2048 sc1\n"
"v_mfma_f32_16x16x32_bf16 v[172:175], a[148:151], v[76:79], 0\n"
"s_waitcnt lgkmcnt(3)\n"
"ds_read_b64_tr_b16 a[170:171], v248 offset:25088\n"
"v_mfma_f32_16x16x32_bf16 v[176:179], a[152:155], v[64:67], 0\n"
"ds_read_b64_tr_b16 a[172:173], v249 offset:24576\n"
"v_mfma_f32_16x16x32_bf16 v[184:187], a[152:155], v[68:71], 0\n"
"buffer_load_dwordx4 v239, s[20:23], s48 offen offset:1024 lds\n"
"v_mfma_f32_16x16x32_bf16 v[192:195], a[152:155], v[72:75], 0\n"
"ds_read_b64_tr_b16 a[174:175], v249 offset:25088\n"
"v_mfma_f32_16x16x32_bf16 v[200:203], a[152:155], v[76:79], 0\n"
"v_mfma_f32_16x16x32_bf16 v[180:183], a[156:159], v[64:67], 0\n"
"ds_read_b64_tr_b16 a[176:177], v246 offset:28672\n"
"v_mfma_f32_16x16x32_bf16 v[188:191], a[156:159], v[68:71], 0\n"
"buffer_load_dwordx4 a[96:99], v235, s[8:11], s56 offen offset:3072 sc1\n"
"v_mfma_f32_16x16x32_bf16 v[196:199], a[156:159], v[72:75], 0\n"
"ds_read_b64_tr_b16 a[178:179], v246 offset:29184\n"
"v_mfma_f32_16x16x32_bf16 v[204:207], a[156:159], v[76:79], 0\n"
"s_waitcnt vmcnt(36)\n"
"v_mfma_f32_16x16x32_bf16 v[80:83], a[160:163], a[0:3], v[80:83]\n"
"s_waitcnt vmcnt(34)\n"
"ds_read_b64_tr_b16 a[180:181], v247 offset:28672\n"
"v_mfma_f32_16x16x32_bf16 v[88:91], a[160:163], a[4:7], v[88:91]\n"
"s_waitcnt vmcnt(32)\n"
"buffer_load_dwordx4 v240, s[20:23], s48 offen offset:2048 lds\n"
"v_mfma_f32_16x16x32_bf16 v[96:99], a[160:163], a[8:11], v[96:99]\n"
"s_waitcnt vmcnt(31)\n"
"ds_read_b64_tr_b16 a[182:183], v247 offset:29184\n"
"v_mfma_f32_16x16x32_bf16 v[104:107], a[160:163], a[12:15], v[104:107]\n"
"s_waitcnt lgkmcnt(3)\n"
"buffer_load_dwordx4 a[100:103], v235, s[8:11], s57 offen offset:3072 sc1\n"
"v_mfma_f32_16x16x32_bf16 v[84:87], a[164:167], a[0:3], v[84:87]\n"
"s_mov_b32 s88, 5120\n"
"ds_read_b64_tr_b16 a[184:185], v248 offset:28672\n"
"v_mfma_f32_16x16x32_bf16 v[92:95], a[164:167], a[4:7], v[92:95]\n"
"s_mov_b32 s89, 1024\n"
"v_mfma_f32_16x16x32_bf16 v[100:103], a[164:167], a[8:11], v[100:103]\n"
"ds_read_b64_tr_b16 a[186:187], v248 offset:29184\n"
"v_mfma_f32_16x16x32_bf16 v[108:111], a[164:167], a[12:15], v[108:111]\n"
"v_mfma_f32_16x16x32_bf16 v[112:115], a[168:171], a[0:3], v[112:115]\n"
"s_add_u32 s98, s98, 524288\n"
"ds_read_b64_tr_b16 a[188:189], v249 offset:28672\n"
"v_mfma_f32_16x16x32_bf16 v[120:123], a[168:171], a[4:7], v[120:123]\n"
"s_cmp_eq_u32 s98, 5242880\n"
"buffer_load_dwordx4 v241, s[20:23], s48 offen offset:3072 lds\n"
"v_mfma_f32_16x16x32_bf16 v[128:131], a[168:171], a[8:11], v[128:131]\n"
"s_cselect_b32 s98, 0, s98\n"
"ds_read_b64_tr_b16 a[190:191], v249 offset:29184\n"
"v_mfma_f32_16x16x32_bf16 v[136:139], a[168:171], a[12:15], v[136:139]\n"
"buffer_load_dwordx4 a[104:107], v235, s[8:11], s58 offen offset:3072 sc1\n"
"v_mfma_f32_16x16x32_bf16 v[116:119], a[172:175], a[0:3], v[116:119]\n"
"ds_read_b64_tr_b16 a[128:129], v242 offset:0\n"
"v_mfma_f32_16x16x32_bf16 v[124:127], a[172:175], a[4:7], v[124:127]\n"
"ds_read_b64_tr_b16 a[130:131], v242 offset:512\n"
"v_mfma_f32_16x16x32_bf16 v[132:135], a[172:175], a[8:11], v[132:135]\n"
"v_mfma_f32_16x16x32_bf16 v[140:143], a[172:175], a[12:15], v[140:143]\n"
"s_waitcnt lgkmcnt(3)\n"
"ds_read_b64_tr_b16 a[132:133], v243 offset:0\n"
"v_mfma_f32_16x16x32_bf16 v[144:147], a[176:179], a[0:3], v[144:147]\n"
"s_add_u32 s99, s99, 4096\n"
"v_mfma_f32_16x16x32_bf16 v[152:155], a[176:179], a[4:7], v[152:155]\n"
"s_cmp_eq_u32 s99, 20480\n"
"ds_read_b64_tr_b16 a[134:135], v243 offset:512\n"
"s_cselect_b32 s99, 0, s99\n"
"v_mfma_f32_16x16x32_bf16 v[160:163], a[176:179], a[8:11], v[160:163]\n"
"buffer_load_dwordx4 a[108:111], v235, s[8:11], s59 offen offset:3072 sc1\n"
"v_mfma_f32_16x16x32_bf16 v[168:171], a[176:179], a[12:15], v[168:171]\n"
"ds_read_b64_tr_b16 a[136:137], v244 offset:0\n"
"v_mfma_f32_16x16x32_bf16 v[148:151], a[180:183], a[0:3], v[148:151]\n"
"v_mfma_f32_16x16x32_bf16 v[156:159], a[180:183], a[4:7], v[156:159]\n"
"ds_read_b64_tr_b16 a[138:139], v244 offset:512\n"
"v_mfma_f32_16x16x32_bf16 v[164:167], a[180:183], a[8:11], v[164:167]\n"
"v_mfma_f32_16x16x32_bf16 v[172:175], a[180:183], a[12:15], v[172:175]\n"
"ds_read_b64_tr_b16 a[140:141], v245 offset:0\n"
"v_add_u32 v235, s99, v224\n"
"v_mfma_f32_16x16x32_bf16 v[176:179], a[184:187], a[0:3], v[176:179]\n"
"buffer_load_dwordx4 a[112:115], v235, s[8:11], s56 offen offset:0 sc1\n"
"v_mfma_f32_16x16x32_bf16 v[184:187], a[184:187], a[4:7], v[184:187]\n"
"ds_read_b64_tr_b16 a[142:143], v245 offset:512\n"
"v_mfma_f32_16x16x32_bf16 v[192:195], a[184:187], a[8:11], v[192:195]\n"
"v_mfma_f32_16x16x32_bf16 v[200:203], a[184:187], a[12:15], v[200:203]\n"
"s_waitcnt lgkmcnt(3)\n"
"ds_read_b64_tr_b16 a[144:145], v242 offset:4096\n"
"v_mfma_f32_16x16x32_bf16 v[180:183], a[188:191], a[0:3], v[180:183]\n"
"v_mfma_f32_16x16x32_bf16 v[188:191], a[188:191], a[4:7], v[188:191]\n"
"ds_read_b64_tr_b16 a[146:147], v242 offset:4608\n"
"v_mfma_f32_16x16x32_bf16 v[196:199], a[188:191], a[8:11], v[196:199]\n"
"v_mfma_f32_16x16x32_bf16 v[204:207], a[188:191], a[12:15], v[204:207]\n"
"s_waitcnt vmcnt(35)\n"
"ds_read_b64_tr_b16 a[148:149], v243 offset:4096\n"
"v_mfma_f32_16x16x32_bf16 v[80:83], a[128:131], a[16:19], v[80:83]\n"
"s_waitcnt vmcnt(34)\n"
"buffer_load_dwordx4 a[116:119], v235, s[8:11], s57 offen offset:0 sc1\n"
"v_mfma_f32_16x16x32_bf16 v[88:91], a[128:131], a[20:23], v[88:91]\n"
"s_waitcnt vmcnt(33)\n"
"ds_read_b64_tr_b16 a[150:151], v243 offset:4608\n"
"v_mfma_f32_16x16x32_bf16 v[96:99], a[128:131], a[24:27], v[96:99]\n"
"s_waitcnt vmcnt(32)\n"
"ds_read_b64_tr_b16 a[152:153], v244 offset:4096\n"
"v_mfma_f32_16x16x32_bf16 v[104:107], a[128:131], a[28:31], v[104:107]\n"
"v_mfma_f32_16x16x32_bf16 v[84:87], a[132:135], a[16:19], v[84:87]\n"
"ds_read_b64_tr_b16 a[154:155], v244 offset:4608\n"
"v_mfma_f32_16x16x32_bf16 v[92:95], a[132:135], a[20:23], v[92:95]\n"
"v_mfma_f32_16x16x32_bf16 v[100:103], a[132:135], a[24:27], v[100:103]\n"
"ds_read_b64_tr_b16 a[156:157], v245 offset:4096\n"
"v_mfma_f32_16x16x32_bf16 v[108:111], a[132:135], a[28:31], v[108:111]\n"
"s_waitcnt lgkmcnt(4)\n"
"buffer_load_dwordx4 a[120:123], v235, s[8:11], s58 offen offset:0 sc1\n"
"v_mfma_f32_16x16x32_bf16 v[112:115], a[136:139], a[16:19], v[112:115]\n"
"ds_read_b64_tr_b16 a[158:159], v245 offset:4608\n"
"v_mfma_f32_16x16x32_bf16 v[120:123], a[136:139], a[20:23], v[120:123]\n"
"v_mfma_f32_16x16x32_bf16 v[128:131], a[136:139], a[24:27], v[128:131]\n"
"ds_read_b64_tr_b16 a[160:161], v242 offset:24576\n"
"v_mfma_f32_16x16x32_bf16 v[136:139], a[136:139], a[28:31], v[136:139]\n"
"v_mfma_f32_16x16x32_bf16 v[116:119], a[140:143], a[16:19], v[116:119]\n"
"ds_read_b64_tr_b16 a[162:163], v242 offset:25088\n"
"v_mfma_f32_16x16x32_bf16 v[124:127], a[140:143], a[20:23], v[124:127]\n"
"buffer_load_dwordx4 a[124:127], v235, s[8:11], s59 offen offset:0 sc1\n"
"v_mfma_f32_16x16x32_bf16 v[132:135], a[140:143], a[24:27], v[132:135]\n"
"ds_read_b64_tr_b16 a[164:165], v243 offset:24576\n"
"v_mfma_f32_16x16x32_bf16 v[140:143], a[140:143], a[28:31], v[140:143]\n"
"v_mfma_f32_16x16x32_bf16 v[144:147], a[144:147], a[16:19], v[144:147]\n"
"ds_read_b64_tr_b16 a[166:167], v243 offset:25088\n"
"v_mfma_f32_16x16x32_bf16 v[152:155], a[144:147], a[20:23], v[152:155]\n"
"v_mfma_f32_16x16x32_bf16 v[160:163], a[144:147], a[24:27], v[160:163]\n"
"ds_read_b64_tr_b16 a[168:169], v244 offset:24576\n"
"v_mfma_f32_16x16x32_bf16 v[168:171], a[144:147], a[28:31], v[168:171]\n"
"s_waitcnt lgkmcnt(3)\n"
"v_mfma_f32_16x16x32_bf16 v[148:151], a[148:151], a[16:19], v[148:151]\n"
"ds_read_b64_tr_b16 a[170:171], v244 offset:25088\n"
"v_mfma_f32_16x16x32_bf16 v[156:159], a[148:151], a[20:23], v[156:159]\n"
"buffer_load_dwordx4 a[0:3], v235, s[8:11], s56 offen offset:1024 sc1\n"
"v_mfma_f32_16x16x32_bf16 v[164:167], a[148:151], a[24:27], v[164:167]\n"
"ds_read_b64_tr_b16 a[172:173], v245 offset:24576\n"
"v_mfma_f32_16x16x32_bf16 v[172:175], a[148:151], a[28:31], v[172:175]\n"
"ds_read_b64_tr_b16 a[174:175], v245 offset:25088\n"
"v_mfma_f32_16x16x32_bf16 v[176:179], a[152:155], a[16:19], v[176:179]\n"
"v_mfma_f32_16x16x32_bf16 v[184:187], a[152:155], a[20:23], v[184:187]\n"
"ds_read_b64_tr_b16 a[176:177], v242 offset:28672\n"
"v_mfma_f32_16x16x32_bf16 v[192:195], a[152:155], a[24:27], v[192:195]\n"
"v_mfma_f32_16x16x32_bf16 v[200:203], a[152:155], a[28:31], v[200:203]\n"
"ds_read_b64_tr_b16 a[178:179], v242 offset:29184\n"
"v_mfma_f32_16x16x32_bf16 v[180:183], a[156:159], a[16:19], v[180:183]\n"
"s_mov_b32 s49, 270336\n"
"buffer_load_dwordx4 a[4:7], v235, s[8:11], s57 offen offset:1024 sc1\n"
"v_mfma_f32_16x16x32_bf16 v[188:191], a[156:159], a[20:23], v[188:191]\n"
"ds_read_b64_tr_b16 a[180:181], v243 offset:28672\n"
"v_mfma_f32_16x16x32_bf16 v[196:199], a[156:159], a[24:27], v[196:199]\n"
"v_mfma_f32_16x16x32_bf16 v[204:207], a[156:159], a[28:31], v[204:207]\n"
"ds_read_b64_tr_b16 a[182:183], v243 offset:29184\n"
"LQ_%=:\n"
"s_waitcnt vmcnt(35)\n"
"v_mfma_f32_16x16x32_bf16 v[80:83], a[160:163], a[32:35], v[80:83]\n"
"s_waitcnt vmcnt(33)\n"
"v_mfma_f32_16x16x32_bf16 v[88:91], a[160:163], a[36:39], v[88:91]\n"
"s_waitcnt vmcnt(32)\n"
"ds_read_b64_tr_b16 a[184:185], v244 offset:28672\n"
"v_mfma_f32_16x16x32_bf16 v[96:99], a[160:163], a[40:43], v[96:99]\n"
"s_waitcnt vmcnt(31)\n"
"buffer_load_dwordx4 a[8:11], v235, s[8:11], s58 offen offset:1024 sc1\n"
"v_mfma_f32_16x16x32_bf16 v[104:107], a[160:163], a[44:47], v[104:107]\n"
"s_waitcnt lgkmcnt(3)\n"
"ds_read_b64_tr_b16 a[186:187], v244 offset:29184\n"
"v_mfma_f32_16x16x32_bf16 v[84:87], a[164:167], a[32:35], v[84:87]\n"
"v_mfma_f32_16x16x32_bf16 v[92:95], a[164:167], a[36:39], v[92:95]\n"
"ds_read_b64_tr_b16 a[188:189], v245 offset:28672\n"
"v_mfma_f32_16x16x32_bf16 v[100:103], a[164:167], a[40:43], v[100:103]\n"
"v_mfma_f32_16x16x32_bf16 v[108:111], a[164:167], a[44:47], v[108:111]\n"
"ds_read_b64_tr_b16 a[190:191], v245 offset:29184\n"
"v_mfma_f32_16x16x32_bf16 v[112:115], a[168:171], a[32:35], v[112:115]\n"
"v_mfma_f32_16x16x32_bf16 v[120:123], a[168:171], a[36:39], v[120:123]\n"
"buffer_load_dwordx4 a[12:15], v235, s[8:11], s59 offen offset:1024 sc1\n"
"v_mfma_f32_16x16x32_bf16 v[128:131], a[168:171], a[40:43], v[128:131]\n"
"v_mfma_f32_16x16x32_bf16 v[136:139], a[168:171], a[44:47], v[136:139]\n"
"v_mfma_f32_16x16x32_bf16 v[116:119], a[172:175], a[32:35], v[116:119]\n"
"v_mfma_f32_16x16x32_bf16 v[124:127], a[172:175], a[36:39], v[124:127]\n"
"s_waitcnt lgkmcnt(0)\n"
"s_barrier\n"
"v_mfma_f32_16x16x32_bf16 v[132:135], a[172:175], a[40:43], v[132:135]\n"
"s_add_u32 m0, s38, 65536\n"
"ds_read_b64_tr_b16 a[128:129], v246 offset:8192\n"
"s_add_u32 s48, s39, s98\n"
"v_mfma_f32_16x16x32_bf16 v[140:143], a[172:175], a[44:47], v[140:143]\n"
"v_mfma_f32_16x16x32_bf16 v[144:147], a[176:179], a[32:35], v[144:147]\n"
"ds_read_b64_tr_b16 a[130:131], v246 offset:8704\n"
"v_mfma_f32_16x16x32_bf16 v[152:155], a[176:179], a[36:39], v[152:155]\n"
"buffer_load_dwordx4 a[16:19], v235, s[8:11], s56 offen offset:2048 sc1\n"
"v_mfma_f32_16x16x32_bf16 v[160:163], a[176:179], a[40:43], v[160:163]\n"
"buffer_load_dwordx4 v238, s[20:23], s48 offen offset:0 lds\n"
"v_mfma_f32_16x16x32_bf16 v[168:171], a[176:179], a[44:47], v[168:171]\n"
"ds_read_b64_tr_b16 a[132:133], v247 offset:8192\n"
"v_mfma_f32_16x16x32_bf16 v[148:151], a[180:183], a[32:35], v[148:151]\n"
"ds_read_b64_tr_b16 a[134:135], v247 offset:8704\n"
"v_mfma_f32_16x16x32_bf16 v[156:159], a[180:183], a[36:39], v[156:159]\n"
"v_mfma_f32_16x16x32_bf16 v[164:167], a[180:183], a[40:43], v[164:167]\n"
"ds_read_b64_tr_b16 a[136:137], v248 offset:8192\n"
"v_mfma_f32_16x16x32_bf16 v[172:175], a[180:183], a[44:47], v[172:175]\n"
"buffer_load_dwordx4 a[20:23], v235, s[8:11], s57 offen offset:2048 sc1\n"
"v_mfma_f32_16x16x32_bf16 v[176:179], a[184:187], a[32:35], v[176:179]\n"
"ds_read_b64_tr_b16 a[138:139], v248 offset:8704\n"
"v_mfma_f32_16x16x32_bf16 v[184:187], a[184:187], a[36:39], v[184:187]\n"
"buffer_load_dwordx4 v239, s[20:23], s48 offen offset:1024 lds\n"
"v_mfma_f32_16x16x32_bf16 v[192:195], a[184:187], a[40:43], v[192:195]\n"
"ds_read_b64_tr_b16 a[140:141], v249 offset:8192\n"
"v_mfma_f32_16x16x32_bf16 v[200:203], a[184:187], a[44:47], v[200:203]\n"
"v_mfma_f32_16x16x32_bf16 v[180:183], a[188:191], a[32:35], v[180:183]\n"
"ds_read_b64_tr_b16 a[142:143], v249 offset:8704\n"
"v_lshrrev_b32 v254, 8, v224\n"
"v_mfma_f32_16x16x32_bf16 v[188:191], a[188:191], a[36:39], v[188:191]\n"
"v_lshlrev_b32 v251, 7, v254\n"
"v_mfma_f32_16x16x32_bf16 v[196:199], a[188:191], a[40:43], v[196:199]\n"
"ds_read_b64_tr_b16 a[144:145], v246 offset:12288\n"
"v_lshlrev_b32 v254, 8, v254\n"
"v_mfma_f32_16x16x32_bf16 v[204:207], a[188:191], a[44:47], v[204:207]\n"
"s_waitcnt vmcnt(36) lgkmcnt(3)\n"
"buffer_load_dwordx4 a[24:27], v235, s[8:11], s58 offen offset:2048 sc1\n"
"v_mfma_f32_16x16x32_bf16 v[80:83], a[128:131], a[48:51], v[80:83]\n"
"s_waitcnt vmcnt(36)\n"
"ds_read_b64_tr_b16 a[146:147], v246 offset:12800\n"
"v_lshrrev_b32 v255, 1, v224\n"
"v_mfma_f32_16x16x32_bf16 v[88:91], a[128:131], a[52:55], v[88:91]\n"
"s_waitcnt vmcnt(35)\n"
"buffer_load_dwordx4 v240, s[20:23], s48 offen offset:2048 lds\n"
"v_mfma_f32_16x16x32_bf16 v[96:99], a[128:131], a[56:59], v[96:99]\n"
"s_waitcnt vmcnt(35)\n"
"ds_read_b64_tr_b16 a[148:149], v247 offset:12288\n"
"v_and_b32 v255, 0x78, v255\n"
"v_mfma_f32_16x16x32_bf16 v[104:107], a[128:131], a[60:63], v[104:107]\n"
"s_mul_i32 s92, s30, 10240\n"
"v_add3_u32 v254, v254, v255, s92\n"
"v_and_b32 v255, 0x70, v224\n"
"v_mfma_f32_16x16x32_bf16 v[84:87], a[132:135], a[48:51], v[84:87]\n"
"s_and_b32 s93, s30, 1\n"
"ds_read_b64_tr_b16 a[150:151], v247 offset:12800\n"
"v_mfma_f32_16x16x32_bf16 v[92:95], a[132:135], a[52:55], v[92:95]\n"
"s_mul_i32 s93, s93, 5120\n"
"ds_read_b64_tr_b16 a[152:153], v248 offset:12288\n"
"v_mfma_f32_16x16x32_bf16 v[100:103], a[132:135], a[56:59], v[100:103]\n"
"buffer_load_dwordx4 a[28:31], v235, s[8:11], s59 offen offset:2048 sc1\n"
"v_mfma_f32_16x16x32_bf16 v[108:111], a[132:135], a[60:63], v[108:111]\n"
"ds_read_b64_tr_b16 a[154:155], v248 offset:12800\n"
"v_add3_u32 v251, v251, v255, s93\n"
"v_mfma_f32_16x16x32_bf16 v[112:115], a[136:139], a[48:51], v[112:115]\n"
"s_add_u32 s98, s98, 524288\n"
"buffer_load_dwordx4 v241, s[20:23], s48 offen offset:3072 lds\n"
"v_mfma_f32_16x16x32_bf16 v[120:123], a[136:139], a[52:55], v[120:123]\n"
"ds_read_b64_tr_b16 a[192:193], v254 offset:0\n"
"v_mfma_f32_16x16x32_bf16 v[128:131], a[136:139], a[56:59], v[128:131]\n"
"ds_read_b64_tr_b16 a[156:157], v249 offset:12288\n"
"v_mfma_f32_16x16x32_bf16 v[136:139], a[136:139], a[60:63], v[136:139]\n"
"s_waitcnt lgkmcnt(4)\n"
"ds_read_b64_tr_b16 a[194:195], v254 offset:128\n"
"v_mfma_f32_16x16x32_bf16 v[116:119], a[140:143], a[48:51], v[116:119]\n"
"ds_read_b64_tr_b16 a[158:159], v249 offset:12800\n"
"v_mfma_f32_16x16x32_bf16 v[124:127], a[140:143], a[52:55], v[124:127]\n"
"buffer_load_dwordx4 a[32:35], v235, s[8:11], s56 offen offset:3072 sc1\n"
"v_mfma_f32_16x16x32_bf16 v[132:135], a[140:143], a[56:59], v[132:135]\n"
"s_add_u32 m0, s38, 114688\n"
"ds_read_b128 a[196:199], v251 offset:45056\n"
"s_add_u32 s48, s39, s98\n"
"v_mfma_f32_16x16x32_bf16 v[140:143], a[140:143], a[60:63], v[140:143]\n"
"ds_read_b64_tr_b16 a[160:161], v246 offset:32768\n"
"v_mfma_f32_16x16x32_bf16 v[144:147], a[144:147], a[48:51], v[144:147]\n"
"ds_read_b64_tr_b16 a[200:201], v254 offset:1024\n"
"v_mfma_f32_16x16x32_bf16 v[152:155], a[144:147], a[52:55], v[152:155]\n"
"ds_read_b64_tr_b16 a[162:163], v246 offset:33280\n"
"v_mfma_f32_16x16x32_bf16 v[160:163], a[144:147], a[56:59], v[160:163]\n"
"buffer_load_dwordx4 v238, s[20:23], s48 offen offset:0 lds\n"
"v_mfma_f32_16x16x32_bf16 v[168:171], a[144:147], a[60:63], v[168:171]\n"
"ds_read_b64_tr_b16 a[202:203], v254 offset:1152\n"
"v_mfma_f32_16x16x32_bf16 v[148:151], a[148:151], a[48:51], v[148:151]\n"
"ds_read_b64_tr_b16 a[164:165], v247 offset:32768\n"
"v_mfma_f32_16x16x32_bf16 v[156:159], a[148:151], a[52:55], v[156:159]\n"
"ds_read_b128 a[204:207], v251 offset:45568\n"
"v_mfma_f32_16x16x32_bf16 v[164:167], a[148:151], a[56:59], v[164:167]\n"
"ds_read_b64_tr_b16 a[166:167], v247 offset:33280\n"
"v_mfma_f32_16x16x32_bf16 v[172:175], a[148:151], a[60:63], v[172:175]\n"
"s_waitcnt lgkmcnt(5)\n"
"buffer_load_dwordx4 a[36:39], v235, s[8:11], s57 offen offset:3072 sc1\n"
"v_mfma_f32_16x16x32_bf16 v[176:179], a[152:155], a[48:51], v[176:179]\n"
"ds_read_b64_tr_b16 a[208:209], v254 offset:2048\n"
"v_mfma_f32_16x16x32_bf16 v[208:211], a[192:195], a[196:199], 0\n"
"ds_read_b64_tr_b16 a[168:169], v248 offset:32768\n"
"v_mfma_f32_16x16x32_bf16 v[184:187], a[152:155], a[52:55], v[184:187]\n"
"ds_read_b64_tr_b16 a[210:211], v254 offset:2176\n"
"v_mfma_f32_16x16x32_bf16 v[192:195], a[152:155], a[56:59], v[192:195]\n"
"ds_read_b64_tr_b16 a[170:171], v248 offset:33280\n"
"v_mfma_f32_16x16x32_bf16 v[200:203], a[152:155], a[60:63], v[200:203]\n"
"ds_read_b64_tr_b16 a[172:173], v249 offset:32768\n"
"v_mfma_f32_16x16x32_bf16 v[180:183], a[156:159], a[48:51], v[180:183]\n"
"buffer_load_dwordx4 v239, s[20:23], s48 offen offset:1024 lds\n"
"v_mfma_f32_16x16x32_bf16 v[188:191], a[156:159], a[52:55], v[188:191]\n"
"ds_read_b128 a[212:215], v251 offset:46080\n"
"v_mfma_f32_16x16x32_bf16 v[196:199], a[156:159], a[56:59], v[196:199]\n"
"s_waitcnt lgkmcnt(5)\n"
"ds_read_b64_tr_b16 a[174:175], v249 offset:33280\n"
"v_mfma_f32_16x16x32_bf16 v[208:211], a[200:203], a[204:207], v[208:211]\n"
"buffer_load_dwordx4 a[40:43], v235, s[8:11], s58 offen offset:3072 sc1\n"
"v_mfma_f32_16x16x32_bf16 v[204:207], a[156:159], a[60:63], v[204:207]\n"
"s_waitcnt vmcnt(41)\n"
"ds_read_b64_tr_b16 a[216:217], v254 offset:3072\n"
"v_mfma_f32_16x16x32_bf16 v[80:83], a[160:163], a[64:67], v[80:83]\n"
"s_waitcnt vmcnt(40)\n"
"ds_read_b64_tr_b16 a[176:177], v246 offset:36864\n"
"s_add_u32 s99, s99, 4096\n"
"v_mfma_f32_16x16x32_bf16 v[88:91], a[160:163], a[68:71], v[88:91]\n"
"s_waitcnt vmcnt(37)\n"
"ds_read_b64_tr_b16 a[218:219], v254 offset:3200\n"
"s_cmp_eq_u32 s99, 20480\n"
"v_mfma_f32_16x16x32_bf16 v[96:99], a[160:163], a[72:75], v[96:99]\n"
"s_waitcnt vmcnt(35)\n"
"ds_read_b64_tr_b16 a[178:179], v246 offset:37376\n"
"s_cselect_b32 s99, 0, s99\n"
"v_mfma_f32_16x16x32_bf16 v[104:107], a[160:163], a[76:79], v[104:107]\n"
"ds_read_b128 a[220:223], v251 offset:46592\n"
"v_mfma_f32_16x16x32_bf16 v[84:87], a[164:167], a[64:67], v[84:87]\n"
"s_waitcnt lgkmcnt(5)\n"
"ds_read_b64_tr_b16 a[180:181], v247 offset:36864\n"
"v_mfma_f32_16x16x32_bf16 v[208:211], a[208:211], a[212:215], v[208:211]\n"
"buffer_load_dwordx4 v240, s[20:23], s48 offen offset:2048 lds\n"
"v_mfma_f32_16x16x32_bf16 v[92:95], a[164:167], a[68:71], v[92:95]\n"
"ds_read_b64_tr_b16 a[224:225], v254 offset:4096\n"
"v_mfma_f32_16x16x32_bf16 v[100:103], a[164:167], a[72:75], v[100:103]\n"
"buffer_load_dwordx4 a[44:47], v235, s[8:11], s59 offen offset:3072 sc1\n"
"v_mfma_f32_16x16x32_bf16 v[108:111], a[164:167], a[76:79], v[108:111]\n"
"ds_read_b64_tr_b16 a[182:183], v247 offset:37376\n"
"v_add_u32 v235, s99, v224\n"
"v_mfma_f32_16x16x32_bf16 v[112:115], a[168:171], a[64:67], v[112:115]\n"
"s_add_u32 s98, s98, 524288\n"
"ds_read_b64_tr_b16 a[226:227], v254 offset:4224\n"
"v_mfma_f32_16x16x32_bf16 v[120:123], a[168:171], a[68:71], v[120:123]\n"
"s_cmp_eq_u32 s98, 5242880\n"
"ds_read_b64_tr_b16 a[184:185], v248 offset:36864\n"
"s_cselect_b32 s98, 0, s98\n"
"v_mfma_f32_16x16x32_bf16 v[128:131], a[168:171], a[72:75], v[128:131]\n"
"s_waitcnt lgkmcnt(4)\n"
"ds_read_b128 a[228:231], v251 offset:47104\n"
"v_mfma_f32_16x16x32_bf16 v[208:211], a[216:219], a[220:223], v[208:211]\n"
"ds_read_b64_tr_b16 a[186:187], v248 offset:37376\n"
"v_mfma_f32_16x16x32_bf16 v[136:139], a[168:171], a[76:79], v[136:139]\n"
"ds_read_b64_tr_b16 a[232:233], v254 offset:5120\n"
"v_mfma_f32_16x16x32_bf16 v[116:119], a[172:175], a[64:67], v[116:119]\n"
"ds_read_b64_tr_b16 a[188:189], v249 offset:36864\n"
"v_mfma_f32_16x16x32_bf16 v[124:127], a[172:175], a[68:71], v[124:127]\n"
"buffer_load_dwordx4 a[48:51], v235, s[8:11], s56 offen offset:0 sc1\n"
"v_mfma_f32_16x16x32_bf16 v[132:135], a[172:175], a[72:75], v[132:135]\n"
"buffer_load_dwordx4 v241, s[20:23], s48 offen offset:3072 lds\n"
"v_mfma_f32_16x16x32_bf16 v[140:143], a[172:175], a[76:79], v[140:143]\n"
"ds_read_b64_tr_b16 a[234:235], v254 offset:5248\n"
"v_mfma_f32_16x16x32_bf16 v[144:147], a[176:179], a[64:67], v[144:147]\n"
"s_waitcnt lgkmcnt(4)\n"
"ds_read_b64_tr_b16 a[190:191], v249 offset:37376\n"
"v_mfma_f32_16x16x32_bf16 v[208:211], a[224:227], a[228:231], v[208:211]\n"
"ds_read_b128 a[236:239], v251 offset:47616\n"
"v_mfma_f32_16x16x32_bf16 v[152:155], a[176:179], a[68:71], v[152:155]\n"
"ds_read_b64_tr_b16 a[128:129], v242 offset:8192\n"
"v_mfma_f32_16x16x32_bf16 v[160:163], a[176:179], a[72:75], v[160:163]\n"
"ds_read_b64_tr_b16 a[240:241], v254 offset:6144\n"
"v_mfma_f32_16x16x32_bf16 v[168:171], a[176:179], a[76:79], v[168:171]\n"
"ds_read_b64_tr_b16 a[130:131], v242 offset:8704\n"
"v_mfma_f32_16x16x32_bf16 v[148:151], a[180:183], a[64:67], v[148:151]\n"
"ds_read_b64_tr_b16 a[242:243], v254 offset:6272\n"
"v_mfma_f32_16x16x32_bf16 v[156:159], a[180:183], a[68:71], v[156:159]\n"
"ds_read_b64_tr_b16 a[132:133], v243 offset:8192\n"
"v_mfma_f32_16x16x32_bf16 v[164:167], a[180:183], a[72:75], v[164:167]\n"
"s_waitcnt lgkmcnt(5)\n"
"buffer_load_dwordx4 a[52:55], v235, s[8:11], s57 offen offset:0 sc1\n"
"v_mfma_f32_16x16x32_bf16 v[208:211], a[232:235], a[236:239], v[208:211]\n"
"ds_read_b128 a[244:247], v251 offset:48128\n"
"v_mfma_f32_16x16x32_bf16 v[172:175], a[180:183], a[76:79], v[172:175]\n"
"ds_read_b64_tr_b16 a[134:135], v243 offset:8704\n"
"v_mfma_f32_16x16x32_bf16 v[176:179], a[184:187], a[64:67], v[176:179]\n"
"ds_read_b64_tr_b16 a[248:249], v254 offset:7168\n"
"v_mfma_f32_16x16x32_bf16 v[184:187], a[184:187], a[68:71], v[184:187]\n"
"ds_read_b64_tr_b16 a[136:137], v244 offset:8192\n"
"v_mfma_f32_16x16x32_bf16 v[192:195], a[184:187], a[72:75], v[192:195]\n"
"ds_read_b64_tr_b16 a[250:251], v254 offset:7296\n"
"v_mfma_f32_16x16x32_bf16 v[200:203], a[184:187], a[76:79], v[200:203]\n"
"ds_read_b64_tr_b16 a[138:139], v244 offset:8704\n"
"v_mfma_f32_16x16x32_bf16 v[180:183], a[188:191], a[64:67], v[180:183]\n"
"s_waitcnt lgkmcnt(5)\n"
"buffer_load_dwordx4 a[56:59], v235, s[8:11], s58 offen offset:0 sc1\n"
"v_mfma_f32_16x16x32_bf16 v[208:211], a[240:243], a[244:247], v[208:211]\n"
"ds_read_b128 a[252:255], v251 offset:48640\n"
"v_mfma_f32_16x16x32_bf16 v[188:191], a[188:191], a[68:71], v[188:191]\n"
"ds_read_b64_tr_b16 a[140:141], v245 offset:8192\n"
"v_mfma_f32_16x16x32_bf16 v[196:199], a[188:191], a[72:75], v[196:199]\n"
"ds_read_b64_tr_b16 a[192:193], v254 offset:8192\n"
"v_mfma_f32_16x16x32_bf16 v[204:207], a[188:191], a[76:79], v[204:207]\n"
"s_waitcnt vmcnt(40)\n"
"ds_read_b64_tr_b16 a[142:143], v245 offset:8704\n"
"v_mfma_f32_16x16x32_bf16 v[80:83], a[128:131], a[80:83], v[80:83]\n"
"s_waitcnt vmcnt(38)\n"
"ds_read_b64_tr_b16 a[194:195], v254 offset:8320\n"
"v_mfma_f32_16x16x32_bf16 v[88:91], a[128:131], a[84:87], v[88:91]\n"
"s_waitcnt vmcnt(36)\n"
"ds_read_b64_tr_b16 a[144:145], v242 offset:12288\n"
"v_mfma_f32_16x16x32_bf16 v[96:99], a[128:131], a[88:91], v[96:99]\n"
"s_waitcnt lgkmcnt(5)\n"
"ds_read_b128 a[196:199], v251 offset:49152\n"
"v_mfma_f32_16x16x32_bf16 v[208:211], a[248:251], a[252:255], v[208:211]\n"
"s_waitcnt vmcnt(34)\n"
"ds_read_b64_tr_b16 a[146:147], v242 offset:12800\n"
"v_mfma_f32_16x16x32_bf16 v[104:107], a[128:131], a[92:95], v[104:107]\n"
"buffer_load_dwordx4 a[60:63], v235, s[8:11], s59 offen offset:0 sc1\n"
"v_mfma_f32_16x16x32_bf16 v[84:87], a[132:135], a[80:83], v[84:87]\n"
"ds_read_b64_tr_b16 a[200:201], v254 offset:9216\n"
"v_mfma_f32_16x16x32_bf16 v[92:95], a[132:135], a[84:87], v[92:95]\n"
"ds_read_b64_tr_b16 a[148:149], v243 offset:12288\n"
"v_mfma_f32_16x16x32_bf16 v[100:103], a[132:135], a[88:91], v[100:103]\n"
"ds_read_b64_tr_b16 a[202:203], v254 offset:9344\n"
"v_mfma_f32_16x16x32_bf16 v[108:111], a[132:135], a[92:95], v[108:111]\n"
"ds_read_b64_tr_b16 a[150:151], v243 offset:12800\n"
"v_mfma_f32_16x16x32_bf16 v[112:115], a[136:139], a[80:83], v[112:115]\n"
"s_waitcnt lgkmcnt(5)\n"
"ds_read_b64_tr_b16 a[152:153], v244 offset:12288\n"
"v_mfma_f32_16x16x32_bf16 v[208:211], a[192:195], a[196:199], v[208:211]\n"
"ds_read_b128 a[204:207], v251 offset:49664\n"
"v_mfma_f32_16x16x32_bf16 v[120:123], a[136:139], a[84:87], v[120:123]\n"
"ds_read_b64_tr_b16 a[154:155], v244 offset:12800\n"
"v_mfma_f32_16x16x32_bf16 v[128:131], a[136:139], a[88:91], v[128:131]\n"
"buffer_load_dwordx4 a[64:67], v235, s[8:11], s56 offen offset:1024 sc1\n"
"v_mfma_f32_16x16x32_bf16 v[136:139], a[136:139], a[92:95], v[136:139]\n"
"ds_read_b64_tr_b16 a[156:157], v245 offset:12288\n"
"v_mfma_f32_16x16x32_bf16 v[116:119], a[140:143], a[80:83], v[116:119]\n"
"s_lshl_b32 s92, s30, 10\n"
"ds_read_b64_tr_b16 a[158:159], v245 offset:12800\n"
"v_mfma_f32_16x16x32_bf16 v[124:127], a[140:143], a[84:87], v[124:127]\n"
"ds_read_b64_tr_b16 a[160:161], v242 offset:32768\n"
"v_mfma_f32_16x16x32_bf16 v[132:135], a[140:143], a[88:91], v[132:135]\n"
"s_waitcnt lgkmcnt(4)\n"
"buffer_load_dwordx4 a[68:71], v235, s[8:11], s57 offen offset:1024 sc1\n"
"v_mfma_f32_16x16x32_bf16 v[208:211], a[200:203], a[204:207], v[208:211]\n"
"ds_read_b64_tr_b16 a[162:163], v242 offset:33280\n"
"v_add_u32 v252, s92, v224\n"
"v_mfma_f32_16x16x32_bf16 v[140:143], a[140:143], a[92:95], v[140:143]\n"
"ds_read_b64_tr_b16 a[164:165], v243 offset:32768\n"
"v_mfma_f32_16x16x32_bf16 v[144:147], a[144:147], a[80:83], v[144:147]\n"
"ds_read_b64_tr_b16 a[166:167], v243 offset:33280\n"
"v_mfma_f32_16x16x32_bf16 v[152:155], a[144:147], a[84:87], v[152:155]\n"
"v_mfma_f32_16x16x32_bf16 v[160:163], a[144:147], a[88:91], v[160:163]\n"
"ds_read_b64_tr_b16 a[168:169], v244 offset:32768\n"
"v_mfma_f32_16x16x32_bf16 v[168:171], a[144:147], a[92:95], v[168:171]\n"
"buffer_load_dwordx4 a[72:75], v235, s[8:11], s58 offen offset:1024 sc1\n"
"v_mfma_f32_16x16x32_bf16 v[148:151], a[148:151], a[80:83], v[148:151]\n"
"ds_read_b64_tr_b16 a[170:171], v244 offset:33280\n"
"v_mfma_f32_16x16x32_bf16 v[156:159], a[148:151], a[84:87], v[156:159]\n"
"ds_write_b128 v252, v[208:211] offset:57344\n"
"v_mfma_f32_16x16x32_bf16 v[164:167], a[148:151], a[88:91], v[164:167]\n"
"ds_read_b64_tr_b16 a[172:173], v245 offset:32768\n"
"v_mfma_f32_16x16x32_bf16 v[172:175], a[148:151], a[92:95], v[172:175]\n"
"s_waitcnt lgkmcnt(4)\n"
"ds_read_b64_tr_b16 a[174:175], v245 offset:33280\n"
"v_mfma_f32_16x16x32_bf16 v[176:179], a[152:155], a[80:83], v[176:179]\n"
"v_mfma_f32_16x16x32_bf16 v[184:187], a[152:155], a[84:87], v[184:187]\n"
"ds_read_b64_tr_b16 a[176:177], v242 offset:36864\n"
"v_mfma_f32_16x16x32_bf16 v[192:195], a[152:155], a[88:91], v[192:195]\n"
"buffer_load_dwordx4 a[76:79], v235, s[8:11], s59 offen offset:1024 sc1\n"
"v_mfma_f32_16x16x32_bf16 v[200:203], a[152:155], a[92:95], v[200:203]\n"
"ds_read_b64_tr_b16 a[178:179], v242 offset:37376\n"
"v_mfma_f32_16x16x32_bf16 v[180:183], a[156:159], a[80:83], v[180:183]\n"
"v_mfma_f32_16x16x32_bf16 v[188:191], a[156:159], a[84:87], v[188:191]\n"
"ds_read_b64_tr_b16 a[180:181], v243 offset:36864\n"
"v_mfma_f32_16x16x32_bf16 v[196:199], a[156:159], a[88:91], v[196:199]\n"
"v_mfma_f32_16x16x32_bf16 v[204:207], a[156:159], a[92:95], v[204:207]\n"
"s_waitcnt vmcnt(37)\n"
"ds_read_b64_tr_b16 a[182:183], v243 offset:37376\n"
"v_mfma_f32_16x16x32_bf16 v[80:83], a[160:163], a[96:99], v[80:83]\n"
"s_waitcnt vmcnt(35)\n"
"buffer_load_dwordx4 a[80:83], v235, s[8:11], s56 offen offset:2048 sc1\n"
"v_mfma_f32_16x16x32_bf16 v[88:91], a[160:163], a[100:103], v[88:91]\n"
"s_waitcnt vmcnt(34)\n"
"ds_read_b64_tr_b16 a[184:185], v244 offset:36864\n"
"v_mfma_f32_16x16x32_bf16 v[96:99], a[160:163], a[104:107], v[96:99]\n"
"s_waitcnt vmcnt(33)\n"
"v_mfma_f32_16x16x32_bf16 v[104:107], a[160:163], a[108:111], v[104:107]\n"
"ds_read_b64_tr_b16 a[186:187], v244 offset:37376\n"
"v_mfma_f32_16x16x32_bf16 v[84:87], a[164:167], a[96:99], v[84:87]\n"
"v_mfma_f32_16x16x32_bf16 v[92:95], a[164:167], a[100:103], v[92:95]\n"
"ds_read_b64_tr_b16 a[188:189], v245 offset:36864\n"
"v_mfma_f32_16x16x32_bf16 v[100:103], a[164:167], a[104:107], v[100:103]\n"
"v_mfma_f32_16x16x32_bf16 v[108:111], a[164:167], a[108:111], v[108:111]\n"
"s_waitcnt lgkmcnt(3)\n"
"ds_read_b64_tr_b16 a[190:191], v245 offset:37376\n"
"v_mfma_f32_16x16x32_bf16 v[112:115], a[168:171], a[96:99], v[112:115]\n"
"buffer_load_dwordx4 a[84:87], v235, s[8:11], s57 offen offset:2048 sc1\n"
"v_mfma_f32_16x16x32_bf16 v[120:123], a[168:171], a[100:103], v[120:123]\n"
"v_mfma_f32_16x16x32_bf16 v[128:131], a[168:171], a[104:107], v[128:131]\n"
"v_mfma_f32_16x16x32_bf16 v[136:139], a[168:171], a[108:111], v[136:139]\n"
"v_mfma_f32_16x16x32_bf16 v[116:119], a[172:175], a[96:99], v[116:119]\n"
"v_mfma_f32_16x16x32_bf16 v[124:127], a[172:175], a[100:103], v[124:127]\n"
"s_waitcnt lgkmcnt(0)\n"
"s_barrier\n"
"v_mfma_f32_16x16x32_bf16 v[132:135], a[172:175], a[104:107], v[132:135]\n"
"s_add_u32 m0, s38, 73728\n"
"ds_read_b64_tr_b16 a[128:129], v246 offset:16384\n"
"s_add_u32 s48, s39, s98\n"
"v_mfma_f32_16x16x32_bf16 v[140:143], a[172:175], a[108:111], v[140:143]\n"
"buffer_load_dwordx4 a[88:91], v235, s[8:11], s58 offen offset:2048 sc1\n"
"v_mfma_f32_16x16x32_bf16 v[144:147], a[176:179], a[96:99], v[144:147]\n"
"ds_read_b128 v[208:211], v224 offset:57344\n"
"v_mfma_f32_16x16x32_bf16 v[152:155], a[176:179], a[100:103], v[152:155]\n"
"ds_read_b64_tr_b16 a[130:131], v246 offset:16896\n"
"v_mfma_f32_16x16x32_bf16 v[160:163], a[176:179], a[104:107], v[160:163]\n"
"buffer_load_dwordx4 v238, s[20:23], s48 offen offset:0 lds\n"
"v_mfma_f32_16x16x32_bf16 v[168:171], a[176:179], a[108:111], v[168:171]\n"
"ds_read_b128 v[212:215], v224 offset:58368\n"
"v_mfma_f32_16x16x32_bf16 v[148:151], a[180:183], a[96:99], v[148:151]\n"
"ds_read_b64_tr_b16 a[132:133], v247 offset:16384\n"
"v_mfma_f32_16x16x32_bf16 v[156:159], a[180:183], a[100:103], v[156:159]\n"
"ds_read_b128 v[216:219], v224 offset:59392\n"
"v_mfma_f32_16x16x32_bf16 v[164:167], a[180:183], a[104:107], v[164:167]\n"
"ds_read_b64_tr_b16 a[134:135], v247 offset:16896\n"
"v_mfma_f32_16x16x32_bf16 v[172:175], a[180:183], a[108:111], v[172:175]\n"
"buffer_load_dwordx4 a[92:95], v235, s[8:11], s59 offen offset:2048 sc1\n"
"v_mfma_f32_16x16x32_bf16 v[176:179], a[184:187], a[96:99], v[176:179]\n"
"ds_read_b128 v[220:223], v224 offset:60416\n"
"v_mfma_f32_16x16x32_bf16 v[184:187], a[184:187], a[100:103], v[184:187]\n"
"ds_read_b64_tr_b16 a[136:137], v248 offset:16384\n"
"v_mfma_f32_16x16x32_bf16 v[192:195], a[184:187], a[104:107], v[192:195]\n"
"ds_read_b64_tr_b16 a[138:139], v248 offset:16896\n"
"v_mfma_f32_16x16x32_bf16 v[200:203], a[184:187], a[108:111], v[200:203]\n"
"buffer_load_dwordx4 v239, s[20:23], s48 offen offset:1024 lds\n"
"v_mfma_f32_16x16x32_bf16 v[180:183], a[188:191], a[96:99], v[180:183]\n"
"ds_read_b64_tr_b16 a[140:141], v249 offset:16384\n"
"v_mfma_f32_16x16x32_bf16 v[188:191], a[188:191], a[100:103], v[188:191]\n"
"ds_read_b64_tr_b16 a[142:143], v249 offset:16896\n"
"v_mfma_f32_16x16x32_bf16 v[196:199], a[188:191], a[104:107], v[196:199]\n"
"ds_read_b64_tr_b16 a[144:145], v246 offset:20480\n"
"v_mfma_f32_16x16x32_bf16 v[204:207], a[188:191], a[108:111], v[204:207]\n"
"s_waitcnt vmcnt(37) lgkmcnt(5)\n"
"v_add_f32 v208, v208, v212\n"
"v_add_f32 v216, v216, v220\n"
"v_mfma_f32_16x16x32_bf16 v[80:83], a[128:131], a[112:115], v[80:83]\n"
"s_waitcnt vmcnt(36)\n"
"ds_read_b64_tr_b16 a[146:147], v246 offset:20992\n"
"v_add_f32 v209, v209, v213\n"
"v_mfma_f32_16x16x32_bf16 v[88:91], a[128:131], a[116:119], v[88:91]\n"
"s_waitcnt vmcnt(35)\n"
"buffer_load_dwordx4 a[96:99], v235, s[8:11], s56 offen offset:3072 sc1\n"
"v_mfma_f32_16x16x32_bf16 v[96:99], a[128:131], a[120:123], v[96:99]\n"
"s_waitcnt vmcnt(35)\n"
"buffer_load_dwordx4 v240, s[20:23], s48 offen offset:2048 lds\n"
"v_mfma_f32_16x16x32_bf16 v[104:107], a[128:131], a[124:127], v[104:107]\n"
"ds_read_b64_tr_b16 a[148:149], v247 offset:20480\n"
"v_add_f32 v217, v217, v221\n"
"v_mfma_f32_16x16x32_bf16 v[84:87], a[132:135], a[112:115], v[84:87]\n"
"ds_read_b64_tr_b16 a[150:151], v247 offset:20992\n"
"v_add_f32 v210, v210, v214\n"
"v_mfma_f32_16x16x32_bf16 v[92:95], a[132:135], a[116:119], v[92:95]\n"
"ds_read_b64_tr_b16 a[152:153], v248 offset:20480\n"
"v_add_f32 v218, v218, v222\n"
"v_mfma_f32_16x16x32_bf16 v[100:103], a[132:135], a[120:123], v[100:103]\n"
"v_add_f32 v211, v211, v215\n"
"v_add_f32 v219, v219, v223\n"
"v_mfma_f32_16x16x32_bf16 v[108:111], a[132:135], a[124:127], v[108:111]\n"
"s_waitcnt lgkmcnt(3)\n"
"ds_read_b64_tr_b16 a[154:155], v248 offset:20992\n"
"v_cvt_pk_bf16_f32 v208, v208, v208\n"
"v_mfma_f32_16x16x32_bf16 v[112:115], a[136:139], a[112:115], v[112:115]\n"
"s_add_u32 s98, s98, 524288\n"
"buffer_load_dwordx4 a[100:103], v235, s[8:11], s57 offen offset:3072 sc1\n"
"v_mfma_f32_16x16x32_bf16 v[120:123], a[136:139], a[116:119], v[120:123]\n"
"buffer_load_dwordx4 v241, s[20:23], s48 offen offset:3072 lds\n"
"v_mfma_f32_16x16x32_bf16 v[128:131], a[136:139], a[120:123], v[128:131]\n"
"ds_read_b64_tr_b16 a[156:157], v249 offset:20480\n"
"v_lshlrev_b32 v208, 16, v208\n"
"v_mfma_f32_16x16x32_bf16 v[136:139], a[136:139], a[124:127], v[136:139]\n"
"ds_read_b64_tr_b16 a[158:159], v249 offset:20992\n"
"v_cvt_pk_bf16_f32 v209, v209, v209\n"
"v_mfma_f32_16x16x32_bf16 v[116:119], a[140:143], a[112:115], v[116:119]\n"
"v_lshlrev_b32 v209, 16, v209\n"
"v_cvt_pk_bf16_f32 v210, v210, v210\n"
"v_mfma_f32_16x16x32_bf16 v[124:127], a[140:143], a[116:119], v[124:127]\n"
"ds_read_b64_tr_b16 a[160:161], v246 offset:40960\n"
"v_lshlrev_b32 v210, 16, v210\n"
"v_mfma_f32_16x16x32_bf16 v[132:135], a[140:143], a[120:123], v[132:135]\n"
"s_add_u32 m0, s38, 122880\n"
"buffer_load_dwordx4 a[104:107], v235, s[8:11], s58 offen offset:3072 sc1\n"
"v_mfma_f32_16x16x32_bf16 v[140:143], a[140:143], a[124:127], v[140:143]\n"
"s_add_u32 s48, s39, s98\n"
"ds_read_b64_tr_b16 a[162:163], v246 offset:41472\n"
"v_cvt_pk_bf16_f32 v211, v211, v211\n"
"v_mfma_f32_16x16x32_bf16 v[144:147], a[144:147], a[112:115], v[144:147]\n"
"buffer_load_dwordx4 v238, s[20:23], s48 offen offset:0 lds\n"
"v_mfma_f32_16x16x32_bf16 v[152:155], a[144:147], a[116:119], v[152:155]\n"
"ds_read_b64_tr_b16 a[164:165], v247 offset:40960\n"
"v_lshlrev_b32 v211, 16, v211\n"
"v_mfma_f32_16x16x32_bf16 v[160:163], a[144:147], a[120:123], v[160:163]\n"
"v_mul_f32 v64, 0xbfb8aa3b, v208\n"
"v_mul_f32 v65, 0xbfb8aa3b, v209\n"
"v_mfma_f32_16x16x32_bf16 v[168:171], a[144:147], a[124:127], v[168:171]\n"
"s_waitcnt lgkmcnt(3)\n"
"v_exp_f32 v64, v64\n"
"v_mfma_f32_16x16x32_bf16 v[148:151], a[148:151], a[112:115], v[148:151]\n"
"s_add_u32 s99, s99, 4096\n"
"ds_read_b64_tr_b16 a[166:167], v247 offset:41472\n"
"v_mul_f32 v66, 0xbfb8aa3b, v210\n"
"v_mfma_f32_16x16x32_bf16 v[156:159], a[148:151], a[116:119], v[156:159]\n"
"s_cmp_eq_u32 s99, 20480\n"
"buffer_store_dwordx4 v[4:7], v250, s[8:11], s49 offen offset:0 sc1\n"
"v_mfma_f32_16x16x32_bf16 v[164:167], a[148:151], a[120:123], v[164:167]\n"
"s_cselect_b32 s99, 0, s99\n"
"v_exp_f32 v65, v65\n"
"v_mfma_f32_16x16x32_bf16 v[172:175], a[148:151], a[124:127], v[172:175]\n"
"ds_read_b64_tr_b16 a[168:169], v248 offset:40960\n"
"ds_read_b64_tr_b16 a[170:171], v248 offset:41472\n"
"v_mfma_f32_16x16x32_bf16 v[176:179], a[152:155], a[112:115], v[176:179]\n"
"buffer_load_dwordx4 a[108:111], v235, s[8:11], s59 offen offset:3072 sc1\n"
"v_mfma_f32_16x16x32_bf16 v[184:187], a[152:155], a[116:119], v[184:187]\n"
"v_exp_f32 v66, v66\n"
"v_mfma_f32_16x16x32_bf16 v[192:195], a[152:155], a[120:123], v[192:195]\n"
"ds_read_b64_tr_b16 a[172:173], v249 offset:40960\n"
"ds_read_b64_tr_b16 a[174:175], v249 offset:41472\n"
"v_mfma_f32_16x16x32_bf16 v[200:203], a[152:155], a[124:127], v[200:203]\n"
"buffer_load_dwordx4 v239, s[20:23], s48 offen offset:1024 lds\n"
"v_mfma_f32_16x16x32_bf16 v[180:183], a[156:159], a[112:115], v[180:183]\n"
"ds_read_b64_tr_b16 a[176:177], v246 offset:45056\n"
"v_add_u32 v235, s99, v224\n"
"v_mfma_f32_16x16x32_bf16 v[188:191], a[156:159], a[116:119], v[188:191]\n"
"buffer_load_dwordx4 a[192:195], v235, s[8:11], s56 offen offset:0 sc1\n"
"v_mfma_f32_16x16x32_bf16 v[196:199], a[156:159], a[120:123], v[196:199]\n"
"ds_read_b64_tr_b16 a[178:179], v246 offset:45568\n"
"v_mul_f32 v67, 0xbfb8aa3b, v211\n"
"v_mfma_f32_16x16x32_bf16 v[204:207], a[156:159], a[124:127], v[204:207]\n"
"s_waitcnt vmcnt(43) lgkmcnt(4)\n"
"v_exp_f32 v67, v67\n"
"v_mfma_f32_16x16x32_bf16 v[80:83], a[160:163], a[0:3], v[80:83]\n"
"s_waitcnt vmcnt(42)\n"
"ds_read_b64_tr_b16 a[180:181], v247 offset:45056\n"
"v_cvt_pk_bf16_f32 v216, v216, v216\n"
"v_mfma_f32_16x16x32_bf16 v[88:91], a[160:163], a[4:7], v[88:91]\n"
"s_waitcnt vmcnt(41)\n"
"buffer_load_dwordx4 v240, s[20:23], s48 offen offset:2048 lds\n"
"v_mfma_f32_16x16x32_bf16 v[96:99], a[160:163], a[8:11], v[96:99]\n"
"s_waitcnt vmcnt(41)\n"
"ds_read_b64_tr_b16 a[182:183], v247 offset:45568\n"
"v_lshlrev_b32 v216, 16, v216\n"
"v_mfma_f32_16x16x32_bf16 v[104:107], a[160:163], a[12:15], v[104:107]\n"
"buffer_load_dwordx4 a[196:199], v235, s[8:11], s57 offen offset:0 sc1\n"
"v_mfma_f32_16x16x32_bf16 v[84:87], a[164:167], a[0:3], v[84:87]\n"
"ds_read_b64_tr_b16 a[184:185], v248 offset:45056\n"
"v_cvt_pk_bf16_f32 v217, v217, v217\n"
"v_mfma_f32_16x16x32_bf16 v[92:95], a[164:167], a[4:7], v[92:95]\n"
"v_lshlrev_b32 v217, 16, v217\n"
"v_cvt_pk_bf16_f32 v218, v218, v218\n"
"v_mfma_f32_16x16x32_bf16 v[100:103], a[164:167], a[8:11], v[100:103]\n"
"ds_read_b64_tr_b16 a[186:187], v248 offset:45568\n"
"v_lshlrev_b32 v218, 16, v218\n"
"v_mfma_f32_16x16x32_bf16 v[108:111], a[164:167], a[12:15], v[108:111]\n"
"v_cvt_pk_bf16_f32 v219, v219, v219\n"
"v_lshlrev_b32 v219, 16, v219\n"
"v_mfma_f32_16x16x32_bf16 v[112:115], a[168:171], a[0:3], v[112:115]\n"
"s_add_u32 s98, s98, 524288\n"
"ds_read_b64_tr_b16 a[188:189], v249 offset:45056\n"
"v_add_f32 v64, 1.0, v64\n"
"v_mfma_f32_16x16x32_bf16 v[120:123], a[168:171], a[4:7], v[120:123]\n"
"s_cmp_eq_u32 s98, 5242880\n"
"v_rcp_f32 v64, v64\n"
"v_mfma_f32_16x16x32_bf16 v[128:131], a[168:171], a[8:11], v[128:131]\n"
"s_cselect_b32 s98, 0, s98\n"
"buffer_load_dwordx4 v241, s[20:23], s48 offen offset:3072 lds\n"
"v_mfma_f32_16x16x32_bf16 v[136:139], a[168:171], a[12:15], v[136:139]\n"
"s_waitcnt lgkmcnt(2)\n"
"ds_read_b64_tr_b16 a[190:191], v249 offset:45568\n"
"v_add_f32 v65, 1.0, v65\n"
"v_mfma_f32_16x16x32_bf16 v[116:119], a[172:175], a[0:3], v[116:119]\n"
"v_rcp_f32 v65, v65\n"
"v_mfma_f32_16x16x32_bf16 v[124:127], a[172:175], a[4:7], v[124:127]\n"
"buffer_load_dwordx4 a[200:203], v235, s[8:11], s58 offen offset:0 sc1\n"
"v_mfma_f32_16x16x32_bf16 v[132:135], a[172:175], a[8:11], v[132:135]\n"
"ds_read_b64_tr_b16 a[128:129], v242 offset:16384\n"
"v_add_f32 v66, 1.0, v66\n"
"v_mfma_f32_16x16x32_bf16 v[140:143], a[172:175], a[12:15], v[140:143]\n"
"v_rcp_f32 v66, v66\n"
"v_mfma_f32_16x16x32_bf16 v[144:147], a[176:179], a[0:3], v[144:147]\n"
"s_lshl_b32 s39, s35, 20\n"
"ds_read_b64_tr_b16 a[130:131], v242 offset:16896\n"
"ds_read_b64_tr_b16 a[132:133], v243 offset:16384\n"
"v_mfma_f32_16x16x32_bf16 v[152:155], a[176:179], a[4:7], v[152:155]\n"
"s_add_u32 s49, s49, 1024\n"
"ds_read_b64_tr_b16 a[134:135], v243 offset:16896\n"
"v_add_f32 v67, 1.0, v67\n"
"v_mfma_f32_16x16x32_bf16 v[160:163], a[176:179], a[8:11], v[160:163]\n"
"s_add_u32 s39, s39, 10485760\n"
"v_rcp_f32 v67, v67\n"
"v_mfma_f32_16x16x32_bf16 v[168:171], a[176:179], a[12:15], v[168:171]\n"
"buffer_load_dwordx4 a[204:207], v235, s[8:11], s59 offen offset:0 sc1\n"
"v_mfma_f32_16x16x32_bf16 v[148:151], a[180:183], a[0:3], v[148:151]\n"
"ds_read_b64_tr_b16 a[136:137], v244 offset:16384\n"
"v_mul_f32 v208, v208, v64\n"
"v_mfma_f32_16x16x32_bf16 v[156:159], a[180:183], a[4:7], v[156:159]\n"
"buffer_store_dwordx4 v[20:23], v250, s[8:11], s49 offen offset:0 sc1\n"
"v_mfma_f32_16x16x32_bf16 v[164:167], a[180:183], a[8:11], v[164:167]\n"
"ds_read_b64_tr_b16 a[138:139], v244 offset:16896\n"
"v_mul_f32 v209, v209, v65\n"
"v_mfma_f32_16x16x32_bf16 v[172:175], a[180:183], a[12:15], v[172:175]\n"
"s_waitcnt lgkmcnt(3)\n"
"ds_read_b64_tr_b16 a[140:141], v245 offset:16384\n"
"v_mul_f32 v210, v210, v66\n"
"v_mfma_f32_16x16x32_bf16 v[176:179], a[184:187], a[0:3], v[176:179]\n"
"buffer_load_dwordx4 a[208:211], v235, s[8:11], s56 offen offset:1024 sc1\n"
"v_mfma_f32_16x16x32_bf16 v[184:187], a[184:187], a[4:7], v[184:187]\n"
"ds_read_b64_tr_b16 a[142:143], v245 offset:16896\n"
"v_mul_f32 v211, v211, v67\n"
"v_mfma_f32_16x16x32_bf16 v[192:195], a[184:187], a[8:11], v[192:195]\n"
"v_cvt_pk_bf16_f32 v208, v208, v208\n"
"v_lshlrev_b32 v208, 16, v208\n"
"v_mfma_f32_16x16x32_bf16 v[200:203], a[184:187], a[12:15], v[200:203]\n"
"ds_read_b64_tr_b16 a[144:145], v242 offset:20480\n"
"v_cvt_pk_bf16_f32 v209, v209, v209\n"
"v_mfma_f32_16x16x32_bf16 v[180:183], a[188:191], a[0:3], v[180:183]\n"
"v_lshlrev_b32 v209, 16, v209\n"
"v_cvt_pk_bf16_f32 v210, v210, v210\n"
"v_mfma_f32_16x16x32_bf16 v[188:191], a[188:191], a[4:7], v[188:191]\n"
"ds_read_b64_tr_b16 a[146:147], v242 offset:20992\n"
"v_lshlrev_b32 v210, 16, v210\n"
"v_mfma_f32_16x16x32_bf16 v[196:199], a[188:191], a[8:11], v[196:199]\n"
"v_cvt_pk_bf16_f32 v211, v211, v211\n"
"v_lshlrev_b32 v211, 16, v211\n"
"v_mfma_f32_16x16x32_bf16 v[204:207], a[188:191], a[12:15], v[204:207]\n"
"s_waitcnt vmcnt(46)\n"
"ds_read_b64_tr_b16 a[148:149], v243 offset:20480\n"
"v_add_u32 v225, s36, v224\n"
"v_mfma_f32_16x16x32_bf16 v[80:83], a[128:131], a[16:19], v[80:83]\n"
"s_waitcnt vmcnt(44)\n"
"buffer_load_dwordx4 a[212:215], v235, s[8:11], s57 offen offset:1024 sc1\n"
"v_mfma_f32_16x16x32_bf16 v[88:91], a[128:131], a[20:23], v[88:91]\n"
"s_waitcnt vmcnt(43)\n"
"ds_read_b64_tr_b16 a[150:151], v243 offset:20992\n"
"v_mul_f32 v208, v208, v216\n"
"v_mfma_f32_16x16x32_bf16 v[96:99], a[128:131], a[24:27], v[96:99]\n"
"s_waitcnt vmcnt(41)\n"
"ds_read_b64_tr_b16 a[152:153], v244 offset:20480\n"
"v_mul_f32 v209, v209, v217\n"
"v_mfma_f32_16x16x32_bf16 v[104:107], a[128:131], a[28:31], v[104:107]\n"
"s_waitcnt lgkmcnt(4)\n"
"v_mul_f32 v210, v210, v218\n"
"v_mul_f32 v211, v211, v219\n"
"v_mfma_f32_16x16x32_bf16 v[84:87], a[132:135], a[16:19], v[84:87]\n"
"s_lshl_b32 s50, s35, 6\n"
"ds_read_b64_tr_b16 a[154:155], v244 offset:20992\n"
"v_cvt_pk_bf16_f32 v68, v208, v209\n"
"v_mfma_f32_16x16x32_bf16 v[92:95], a[132:135], a[20:23], v[92:95]\n"
"s_add_u32 s50, s50, 2304\n"
"v_cvt_pk_bf16_f32 v69, v210, v211\n"
"v_lshrrev_b32 v251, 1, v224\n"
"v_mfma_f32_16x16x32_bf16 v[100:103], a[132:135], a[24:27], v[100:103]\n"
"s_lshl_b32 s94, s34, 1\n"
"ds_read_b64_tr_b16 a[156:157], v245 offset:20480\n"
"v_and_b32 v251, 0x180, v251\n"
"v_mfma_f32_16x16x32_bf16 v[108:111], a[132:135], a[28:31], v[108:111]\n"
"s_add_u32 s95, s94, 2\n"
"buffer_load_dwordx4 a[216:219], v235, s[8:11], s58 offen offset:1024 sc1\n"
"v_mfma_f32_16x16x32_bf16 v[112:115], a[136:139], a[16:19], v[112:115]\n"
"s_sub_u32 s94, 32, s94\n"
"ds_read_b64_tr_b16 a[158:159], v245 offset:20992\n"
"v_and_b32 v252, 0x70, v224\n"
"v_mfma_f32_16x16x32_bf16 v[120:123], a[136:139], a[20:23], v[120:123]\n"
"v_add_u32 v251, v251, v252\n"
"v_and_b32 v252, 0x80, v224\n"
"v_mfma_f32_16x16x32_bf16 v[128:131], a[136:139], a[24:27], v[128:131]\n"
"ds_read_b64_tr_b16 a[160:161], v242 offset:40960\n"
"v_lshl_add_u32 v251, v252, 23, v251\n"
"v_mfma_f32_16x16x32_bf16 v[136:139], a[136:139], a[28:31], v[136:139]\n"
"buffer_store_dwordx2 v[68:69], v251, s[8:11], s60 offen sc1\n"
"v_mfma_f32_16x16x32_bf16 v[116:119], a[140:143], a[16:19], v[116:119]\n"
"ds_read_b64_tr_b16 a[162:163], v242 offset:41472\n"
"v_lshrrev_b32 v252, 1, v224\n"
"v_mfma_f32_16x16x32_bf16 v[124:127], a[140:143], a[20:23], v[124:127]\n"
"buffer_load_dwordx4 a[220:223], v235, s[8:11], s59 offen offset:1024 sc1\n"
"v_mfma_f32_16x16x32_bf16 v[132:135], a[140:143], a[24:27], v[132:135]\n"
"ds_read_b64_tr_b16 a[164:165], v243 offset:40960\n"
"v_and_b32 v252, 56, v252\n"
"v_mfma_f32_16x16x32_bf16 v[140:143], a[140:143], a[28:31], v[140:143]\n"
"s_waitcnt lgkmcnt(3)\n"
"buffer_load_dwordx2 v[72:73], v252, s[8:11], s50 offen sc1\n"
"v_mfma_f32_16x16x32_bf16 v[144:147], a[144:147], a[16:19], v[144:147]\n"
"s_add_u32 s49, s49, s89\n"
"ds_read_b64_tr_b16 a[166:167], v243 offset:41472\n"
"v_lshlrev_b32 v254, 10, v224\n"
"v_mfma_f32_16x16x32_bf16 v[152:155], a[144:147], a[20:23], v[152:155]\n"
"v_lshrrev_b32 v255, 4, v224\n"
"v_add_u32 v228, s39, v254\n"
"v_mfma_f32_16x16x32_bf16 v[160:163], a[144:147], a[24:27], v[160:163]\n"
"ds_read_b64_tr_b16 a[168:169], v244 offset:40960\n"
"v_and_b32 v254, 1, v255\n"
"v_mfma_f32_16x16x32_bf16 v[168:171], a[144:147], a[28:31], v[168:171]\n"
"buffer_store_dwordx4 v[36:39], v250, s[8:11], s49 offen offset:0 sc1\n"
"v_mfma_f32_16x16x32_bf16 v[148:151], a[148:151], a[16:19], v[148:151]\n"
"ds_read_b64_tr_b16 a[170:171], v244 offset:41472\n"
"v_mul_u32_u24 v254, 491520, v254\n"
"v_mfma_f32_16x16x32_bf16 v[156:159], a[148:151], a[20:23], v[156:159]\n"
"buffer_load_dwordx4 a[224:227], v235, s[8:11], s56 offen offset:2048 sc1\n"
"v_mfma_f32_16x16x32_bf16 v[164:167], a[148:151], a[24:27], v[164:167]\n"
"ds_read_b64_tr_b16 a[172:173], v245 offset:40960\n"
"v_cmp_gt_u32 vcc, s95, v255\n"
"v_mfma_f32_16x16x32_bf16 v[172:175], a[148:151], a[28:31], v[172:175]\n"
"s_mul_i32 s95, s35, 163840\n"
"ds_read_b64_tr_b16 a[174:175], v245 offset:41472\n"
"v_mov_b32 v229, 16384\n"
"v_mfma_f32_16x16x32_bf16 v[176:179], a[152:155], a[16:19], v[176:179]\n"
"s_add_u32 s95, s95, 4923392\n"
"v_mov_b32 v253, 516096\n"
"v_cndmask_b32_e64 v254, v254, 0, vcc\n"
"v_mfma_f32_16x16x32_bf16 v[184:187], a[152:155], a[20:23], v[184:187]\n"
"s_mov_b32 s47, s36\n"
"ds_read_b64_tr_b16 a[176:177], v242 offset:45056\n"
"v_cndmask_b32 v229, v229, v253, vcc\n"
"v_mfma_f32_16x16x32_bf16 v[192:195], a[152:155], a[24:27], v[192:195]\n"
"s_add_u32 s46, s36, 163840\n"
"v_cmp_eq_u32 vcc, s94, v255\n"
"v_mov_b32 v253, 507904\n"
"v_mfma_f32_16x16x32_bf16 v[200:203], a[152:155], a[28:31], v[200:203]\n"
"s_add_u32 s40, s36, 65536\n"
"ds_read_b64_tr_b16 a[178:179], v242 offset:45568\n"
"v_cndmask_b32 v254, v254, v253, vcc\n"
"v_mfma_f32_16x16x32_bf16 v[180:183], a[156:159], a[16:19], v[180:183]\n"
"s_mov_b32 s41, 0\n"
"buffer_load_dwordx4 a[228:231], v235, s[8:11], s57 offen offset:2048 sc1\n"
"v_mfma_f32_16x16x32_bf16 v[188:191], a[156:159], a[20:23], v[188:191]\n"
"s_mov_b32 s42, 2\n"
"ds_read_b64_tr_b16 a[180:181], v243 offset:45056\n"
"v_mov_b32 v253, 1862270976\n"
"v_mfma_f32_16x16x32_bf16 v[196:199], a[156:159], a[24:27], v[196:199]\n"
"v_cndmask_b32 v229, v229, v253, vcc\n"
"v_add_u32 v228, v228, v254\n"
"v_mfma_f32_16x16x32_bf16 v[204:207], a[156:159], a[28:31], v[204:207]\n"
"s_waitcnt vmcnt(46) lgkmcnt(3)\n"
"ds_read_b64_tr_b16 a[182:183], v243 offset:45568\n"
"v_subrev_u32 v254, s94, v255\n"
"v_mfma_f32_16x16x32_bf16 v[80:83], a[160:163], a[32:35], v[80:83]\n"
"s_waitcnt vmcnt(44)\n"
"v_lshlrev_b32 v254, 15, v254\n"
"v_add_u32 v254, s95, v254\n"
"v_mfma_f32_16x16x32_bf16 v[88:91], a[160:163], a[36:39], v[88:91]\n"
"s_waitcnt vmcnt(42)\n"
"ds_read_b64_tr_b16 a[184:185], v244 offset:45056\n"
"v_cmp_lt_u32 vcc, s94, v255\n"
"v_mfma_f32_16x16x32_bf16 v[96:99], a[160:163], a[40:43], v[96:99]\n"
"s_waitcnt vmcnt(40)\n"
"buffer_load_dwordx4 a[232:235], v235, s[8:11], s58 offen offset:2048 sc1\n"
"v_mfma_f32_16x16x32_bf16 v[104:107], a[160:163], a[44:47], v[104:107]\n"
"s_lshl_b32 s94, s30, 4\n"
"ds_read_b64_tr_b16 a[186:187], v244 offset:45568\n"
"v_cndmask_b32 v228, v228, v254, vcc\n"
"v_mfma_f32_16x16x32_bf16 v[84:87], a[164:167], a[32:35], v[84:87]\n"
"v_add_u32 v229, v229, v228\n"
"v_mov_b32 v226, v224\n"
"v_mfma_f32_16x16x32_bf16 v[92:95], a[164:167], a[36:39], v[92:95]\n"
"ds_read_b64_tr_b16 a[188:189], v245 offset:45056\n"
"v_bfe_u32 v254, v224, 4, 4\n"
"v_mfma_f32_16x16x32_bf16 v[100:103], a[164:167], a[40:43], v[100:103]\n"
"v_lshrrev_b32 v255, 8, v224\n"
"v_lshlrev_b32 v255, 2, v255\n"
"v_mfma_f32_16x16x32_bf16 v[108:111], a[164:167], a[44:47], v[108:111]\n"
"ds_read_b64_tr_b16 a[190:191], v245 offset:45568\n"
"v_sub_u32 v254, v254, v255\n"
"v_mfma_f32_16x16x32_bf16 v[112:115], a[168:171], a[32:35], v[112:115]\n"
"v_add_u32 v254, s94, v254\n"
"v_mov_b32 v255, 0xff800000\n"
"v_mfma_f32_16x16x32_bf16 v[120:123], a[168:171], a[36:39], v[120:123]\n"
"buffer_load_dwordx4 a[236:239], v235, s[8:11], s59 offen offset:2048 sc1\n"
"v_mfma_f32_16x16x32_bf16 v[128:131], a[168:171], a[40:43], v[128:131]\n"
"v_cmp_gt_i32_e64 vcc, 0, v254\n"
"v_cmp_gt_i32_e64 s[52:53], 1, v254\n"
"v_mfma_f32_16x16x32_bf16 v[136:139], a[168:171], a[44:47], v[136:139]\n"
"v_cndmask_b32_e64 v208, 0, v255, vcc\n"
"v_cndmask_b32_e64 v209, 0, v255, s[52:53]\n"
"v_mfma_f32_16x16x32_bf16 v[116:119], a[172:175], a[32:35], v[116:119]\n"
"v_cmp_gt_i32_e64 s[62:63], 2, v254\n"
"v_cmp_gt_i32_e64 vcc, 3, v254\n"
"v_mfma_f32_16x16x32_bf16 v[124:127], a[172:175], a[36:39], v[124:127]\n"
"s_waitcnt vmcnt(40)\n"
"s_barrier\n"
"v_mfma_f32_16x16x32_bf16 v[132:135], a[172:175], a[40:43], v[132:135]\n"
"ds_read_b64_tr_b16 a[128:129], v246 offset:0\n"
"v_cndmask_b32_e64 v210, 0, v255, s[62:63]\n"
"v_mfma_f32_16x16x32_bf16 v[140:143], a[172:175], a[44:47], v[140:143]\n"
"s_waitcnt lgkmcnt(1)\n"
"v_cndmask_b32_e64 v211, 0, v255, vcc\n"
"v_cmp_gt_i32_e64 s[52:53], 16, v254\n"
"v_mfma_f32_16x16x32_bf16 v[144:147], a[176:179], a[32:35], v[144:147]\n"
"s_add_u32 s49, s49, 1024\n"
"s_waitcnt vmcnt(5)\n"
"v_cmp_eq_u64 vcc, s[28:29], v[72:73]\n"
"s_cmp_eq_u64 vcc, exec\n"
"s_cbranch_scc0 L1010_%=\n"
"L1011_%=:\n"
"v_mfma_f32_16x16x32_bf16 v[152:155], a[176:179], a[36:39], v[152:155]\n"
"s_add_u32 m0, s36, 0\n"
"ds_read_b64_tr_b16 a[130:131], v246 offset:512\n"
"v_cndmask_b32_e64 v212, 0, v255, s[52:53]\n"
"v_mfma_f32_16x16x32_bf16 v[160:163], a[176:179], a[40:43], v[160:163]\n"
"s_add_u32 s48, s39, 0\n"
"buffer_load_dwordx4 a[240:243], v235, s[8:11], s56 offen offset:3072 sc1\n"
"v_mfma_f32_16x16x32_bf16 v[168:171], a[176:179], a[44:47], v[168:171]\n"
"buffer_store_dwordx4 v[52:55], v250, s[8:11], s49 offen offset:0 sc1\n"
"v_mfma_f32_16x16x32_bf16 v[148:151], a[180:183], a[32:35], v[148:151]\n"
"ds_read_b64_tr_b16 a[132:133], v247 offset:0\n"
"v_cmp_gt_i32_e64 s[62:63], 17, v254\n"
"v_mfma_f32_16x16x32_bf16 v[156:159], a[180:183], a[36:39], v[156:159]\n"
"buffer_load_dwordx4 v225, s[8:11], s48 offen offset:0 sc1 lds\n"
"v_mfma_f32_16x16x32_bf16 v[164:167], a[180:183], a[40:43], v[164:167]\n"
"ds_read_b64_tr_b16 a[134:135], v247 offset:512\n"
"v_cndmask_b32_e64 v213, 0, v255, s[62:63]\n"
"v_mfma_f32_16x16x32_bf16 v[172:175], a[180:183], a[44:47], v[172:175]\n"
"ds_read_b64_tr_b16 a[136:137], v248 offset:0\n"
"v_cmp_gt_i32_e64 vcc, 18, v254\n"
"v_mfma_f32_16x16x32_bf16 v[176:179], a[184:187], a[32:35], v[176:179]\n"
"buffer_load_dwordx4 a[244:247], v235, s[8:11], s57 offen offset:3072 sc1\n"
"v_mfma_f32_16x16x32_bf16 v[184:187], a[184:187], a[36:39], v[184:187]\n"
"buffer_load_dwordx4 v225, s[8:11], s48 offen offset:1024 sc1 lds\n"
"v_mfma_f32_16x16x32_bf16 v[192:195], a[184:187], a[40:43], v[192:195]\n"
"ds_read_b64_tr_b16 a[138:139], v248 offset:512\n"
"v_cndmask_b32_e64 v214, 0, v255, vcc\n"
"v_mfma_f32_16x16x32_bf16 v[200:203], a[184:187], a[44:47], v[200:203]\n"
"ds_read_b64_tr_b16 a[140:141], v249 offset:0\n"
"v_cmp_gt_i32_e64 s[52:53], 19, v254\n"
"v_mfma_f32_16x16x32_bf16 v[180:183], a[188:191], a[32:35], v[180:183]\n"
"ds_read_b64_tr_b16 a[142:143], v249 offset:512\n"
"v_cndmask_b32_e64 v215, 0, v255, s[52:53]\n"
"v_mfma_f32_16x16x32_bf16 v[188:191], a[188:191], a[36:39], v[188:191]\n"
"buffer_load_dwordx4 v225, s[8:11], s48 offen offset:2048 sc1 lds\n"
"v_mfma_f32_16x16x32_bf16 v[196:199], a[188:191], a[40:43], v[196:199]\n"
"ds_read_b64_tr_b16 a[144:145], v246 offset:4096\n"
"v_cmp_gt_i32_e64 s[62:63], 32, v254\n"
"v_mfma_f32_16x16x32_bf16 v[204:207], a[188:191], a[44:47], v[204:207]\n"
"s_waitcnt lgkmcnt(4)\n"
"buffer_load_dwordx4 a[248:251], v235, s[8:11], s58 offen offset:3072 sc1\n"
"v_mfma_f32_16x16x32_bf16 v[80:83], a[128:131], a[48:51], v[80:83]\n"
"ds_read_b64_tr_b16 a[146:147], v246 offset:4608\n"
"v_cndmask_b32_e64 v216, 0, v255, s[62:63]\n"
"v_mfma_f32_16x16x32_bf16 v[88:91], a[128:131], a[52:55], v[88:91]\n"
"buffer_load_dwordx4 v225, s[8:11], s48 offen offset:3072 sc1 lds\n"
"v_mfma_f32_16x16x32_bf16 v[96:99], a[128:131], a[56:59], v[96:99]\n"
"ds_read_b64_tr_b16 a[148:149], v247 offset:4096\n"
"v_cmp_gt_i32_e64 vcc, 33, v254\n"
"v_mfma_f32_16x16x32_bf16 v[104:107], a[128:131], a[60:63], v[104:107]\n"
"v_cmp_gt_i32_e64 s[52:53], 34, v254\n"
"v_cndmask_b32_e64 v217, 0, v255, vcc\n"
"v_mfma_f32_16x16x32_bf16 v[84:87], a[132:135], a[48:51], v[84:87]\n"
"s_add_u32 m0, s36, 16384\n"
"ds_read_b64_tr_b16 a[150:151], v247 offset:4608\n"
"v_cndmask_b32_e64 v218, 0, v255, s[52:53]\n"
"v_mfma_f32_16x16x32_bf16 v[92:95], a[132:135], a[52:55], v[92:95]\n"
"s_add_u32 s48, s39, 516096\n"
"ds_read_b64_tr_b16 a[152:153], v248 offset:4096\n"
"v_cmp_gt_i32_e64 s[62:63], 35, v254\n"
"v_mfma_f32_16x16x32_bf16 v[100:103], a[132:135], a[56:59], v[100:103]\n"
"buffer_load_dwordx4 a[252:255], v235, s[8:11], s59 offen offset:3072 sc1\n"
"v_mfma_f32_16x16x32_bf16 v[108:111], a[132:135], a[60:63], v[108:111]\n"
"s_waitcnt lgkmcnt(3)\n"
"buffer_load_dwordx4 v225, s[8:11], s48 offen offset:0 sc1 lds\n"
"v_mfma_f32_16x16x32_bf16 v[112:115], a[136:139], a[48:51], v[112:115]\n"
"ds_read_b64_tr_b16 a[154:155], v248 offset:4608\n"
"v_cndmask_b32_e64 v219, 0, v255, s[62:63]\n"
"v_mfma_f32_16x16x32_bf16 v[120:123], a[136:139], a[52:55], v[120:123]\n"
"ds_read_b64_tr_b16 a[156:157], v249 offset:4096\n"
"v_cmp_gt_i32_e64 vcc, 48, v254\n"
"v_mfma_f32_16x16x32_bf16 v[128:131], a[136:139], a[56:59], v[128:131]\n"
"v_cmp_gt_i32_e64 s[52:53], 49, v254\n"
"v_cndmask_b32_e64 v220, 0, v255, vcc\n"
"v_mfma_f32_16x16x32_bf16 v[136:139], a[136:139], a[60:63], v[136:139]\n"
"ds_read_b64_tr_b16 a[158:159], v249 offset:4608\n"
"v_cndmask_b32_e64 v221, 0, v255, s[52:53]\n"
"v_mfma_f32_16x16x32_bf16 v[116:119], a[140:143], a[48:51], v[116:119]\n"
"buffer_load_dwordx4 v225, s[8:11], s48 offen offset:1024 sc1 lds\n"
"v_mfma_f32_16x16x32_bf16 v[124:127], a[140:143], a[52:55], v[124:127]\n"
"ds_read_b64_tr_b16 a[160:161], v246 offset:24576\n"
"v_cmp_gt_i32_e64 s[62:63], 50, v254\n"
"v_mfma_f32_16x16x32_bf16 v[132:135], a[140:143], a[56:59], v[132:135]\n"
"s_add_u32 s49, s49, s88\n"
"v_cndmask_b32_e64 v222, 0, v255, s[62:63]\n"
"v_cmp_gt_i32_e64 vcc, 51, v254\n"
"v_mfma_f32_16x16x32_bf16 v[140:143], a[140:143], a[60:63], v[140:143]\n"
"ds_read_b64_tr_b16 a[162:163], v246 offset:25088\n"
"v_cndmask_b32_e64 v223, 0, v255, vcc\n"
"v_mfma_f32_16x16x32_bf16 v[144:147], a[144:147], a[48:51], v[144:147]\n"
"buffer_store_dwordx4 v[12:15], v250, s[8:11], s49 offen offset:0 sc1\n"
"v_mfma_f32_16x16x32_bf16 v[152:155], a[144:147], a[52:55], v[152:155]\n"
"buffer_load_dwordx4 v225, s[8:11], s48 offen offset:2048 sc1 lds\n"
"v_mfma_f32_16x16x32_bf16 v[160:163], a[144:147], a[56:59], v[160:163]\n"
"ds_read_b64_tr_b16 a[164:165], v247 offset:24576\n"
"v_mfma_f32_16x16x32_bf16 v[168:171], a[144:147], a[60:63], v[168:171]\n"
"s_waitcnt lgkmcnt(3)\n"
"ds_read_b64_tr_b16 a[166:167], v247 offset:25088\n"
"v_mfma_f32_16x16x32_bf16 v[148:151], a[148:151], a[48:51], v[148:151]\n"
"v_mfma_f32_16x16x32_bf16 v[156:159], a[148:151], a[52:55], v[156:159]\n"
"ds_read_b64_tr_b16 a[168:169], v248 offset:24576\n"
"v_mfma_f32_16x16x32_bf16 v[164:167], a[148:151], a[56:59], v[164:167]\n"
"buffer_load_dwordx4 v225, s[8:11], s48 offen offset:3072 sc1 lds\n"
"v_mfma_f32_16x16x32_bf16 v[172:175], a[148:151], a[60:63], v[172:175]\n"
"ds_read_b64_tr_b16 a[170:171], v248 offset:25088\n"
"v_mfma_f32_16x16x32_bf16 v[176:179], a[152:155], a[48:51], v[176:179]\n"
"s_add_u32 m0, s36, 32768\n"
"ds_read_b64_tr_b16 a[172:173], v249 offset:24576\n"
"s_add_u32 s48, s39, 16384\n"
"v_mfma_f32_16x16x32_bf16 v[184:187], a[152:155], a[52:55], v[184:187]\n"
"v_mfma_f32_16x16x32_bf16 v[192:195], a[152:155], a[56:59], v[192:195]\n"
"ds_read_b64_tr_b16 a[174:175], v249 offset:25088\n"
"v_mfma_f32_16x16x32_bf16 v[200:203], a[152:155], a[60:63], v[200:203]\n"
"buffer_load_dwordx4 v225, s[8:11], s48 offen offset:0 sc1 lds\n"
"v_mfma_f32_16x16x32_bf16 v[180:183], a[156:159], a[48:51], v[180:183]\n"
"ds_read_b64_tr_b16 a[176:177], v246 offset:28672\n"
"v_mfma_f32_16x16x32_bf16 v[188:191], a[156:159], a[52:55], v[188:191]\n"
"v_mfma_f32_16x16x32_bf16 v[196:199], a[156:159], a[56:59], v[196:199]\n"
"ds_read_b64_tr_b16 a[178:179], v246 offset:29184\n"
"v_mfma_f32_16x16x32_bf16 v[204:207], a[156:159], a[60:63], v[204:207]\n"
"s_waitcnt lgkmcnt(3)\n"
"buffer_load_dwordx4 v225, s[8:11], s48 offen offset:1024 sc1 lds\n"
"v_mfma_f32_16x16x32_bf16 v[80:83], a[160:163], a[64:67], v[80:83]\n"
"ds_read_b64_tr_b16 a[180:181], v247 offset:28672\n"
"v_mfma_f32_16x16x32_bf16 v[88:91], a[160:163], a[68:71], v[88:91]\n"
"v_mfma_f32_16x16x32_bf16 v[96:99], a[160:163], a[72:75], v[96:99]\n"
"ds_read_b64_tr_b16 a[182:183], v247 offset:29184\n"
"v_mfma_f32_16x16x32_bf16 v[104:107], a[160:163], a[76:79], v[104:107]\n"
"v_mfma_f32_16x16x32_bf16 v[84:87], a[164:167], a[64:67], v[84:87]\n"
"ds_read_b64_tr_b16 a[184:185], v248 offset:28672\n"
"v_mfma_f32_16x16x32_bf16 v[92:95], a[164:167], a[68:71], v[92:95]\n"
"buffer_load_dwordx4 v225, s[8:11], s48 offen offset:2048 sc1 lds\n"
"v_mfma_f32_16x16x32_bf16 v[100:103], a[164:167], a[72:75], v[100:103]\n"
"ds_read_b64_tr_b16 a[186:187], v248 offset:29184\n"
"v_mfma_f32_16x16x32_bf16 v[108:111], a[164:167], a[76:79], v[108:111]\n"
"v_mfma_f32_16x16x32_bf16 v[112:115], a[168:171], a[64:67], v[112:115]\n"
"ds_read_b64_tr_b16 a[188:189], v249 offset:28672\n"
"v_mfma_f32_16x16x32_bf16 v[120:123], a[168:171], a[68:71], v[120:123]\n"
"buffer_load_dwordx4 v225, s[8:11], s48 offen offset:3072 sc1 lds\n"
"v_mfma_f32_16x16x32_bf16 v[128:131], a[168:171], a[72:75], v[128:131]\n"
"ds_read_b64_tr_b16 a[190:191], v249 offset:29184\n"
"v_mfma_f32_16x16x32_bf16 v[136:139], a[168:171], a[76:79], v[136:139]\n"
"s_waitcnt lgkmcnt(3)\n"
"ds_read_b64_tr_b16 a[128:129], v242 offset:0\n"
"v_mfma_f32_16x16x32_bf16 v[116:119], a[172:175], a[64:67], v[116:119]\n"
"s_add_u32 m0, s36, 49152\n"
"s_add_u32 s48, s39, 532480\n"
"v_mfma_f32_16x16x32_bf16 v[124:127], a[172:175], a[68:71], v[124:127]\n"
"s_add_u32 s49, s49, 1024\n"
"ds_read_b64_tr_b16 a[130:131], v242 offset:512\n"
"v_mfma_f32_16x16x32_bf16 v[132:135], a[172:175], a[72:75], v[132:135]\n"
"buffer_load_dwordx4 v225, s[8:11], s48 offen offset:0 sc1 lds\n"
"v_mfma_f32_16x16x32_bf16 v[140:143], a[172:175], a[76:79], v[140:143]\n"
"ds_read_b64_tr_b16 a[132:133], v243 offset:0\n"
"v_mfma_f32_16x16x32_bf16 v[144:147], a[176:179], a[64:67], v[144:147]\n"
"buffer_store_dwordx4 v[28:31], v250, s[8:11], s49 offen offset:0 sc1\n"
"v_mfma_f32_16x16x32_bf16 v[152:155], a[176:179], a[68:71], v[152:155]\n"
"ds_read_b64_tr_b16 a[134:135], v243 offset:512\n"
"v_mfma_f32_16x16x32_bf16 v[160:163], a[176:179], a[72:75], v[160:163]\n"
"v_mfma_f32_16x16x32_bf16 v[168:171], a[176:179], a[76:79], v[168:171]\n"
"ds_read_b64_tr_b16 a[136:137], v244 offset:0\n"
"v_mfma_f32_16x16x32_bf16 v[148:151], a[180:183], a[64:67], v[148:151]\n"
"buffer_load_dwordx4 v225, s[8:11], s48 offen offset:1024 sc1 lds\n"
"v_mfma_f32_16x16x32_bf16 v[156:159], a[180:183], a[68:71], v[156:159]\n"
"ds_read_b64_tr_b16 a[138:139], v244 offset:512\n"
"v_mfma_f32_16x16x32_bf16 v[164:167], a[180:183], a[72:75], v[164:167]\n"
"v_mfma_f32_16x16x32_bf16 v[172:175], a[180:183], a[76:79], v[172:175]\n"
"s_waitcnt lgkmcnt(3)\n"
"ds_read_b64_tr_b16 a[140:141], v245 offset:0\n"
"v_mfma_f32_16x16x32_bf16 v[176:179], a[184:187], a[64:67], v[176:179]\n"
"buffer_load_dwordx4 v225, s[8:11], s48 offen offset:2048 sc1 lds\n"
"v_mfma_f32_16x16x32_bf16 v[184:187], a[184:187], a[68:71], v[184:187]\n"
"ds_read_b64_tr_b16 a[142:143], v245 offset:512\n"
"v_mfma_f32_16x16x32_bf16 v[192:195], a[184:187], a[72:75], v[192:195]\n"
"v_mfma_f32_16x16x32_bf16 v[200:203], a[184:187], a[76:79], v[200:203]\n"
"ds_read_b64_tr_b16 a[144:145], v242 offset:4096\n"
"v_mfma_f32_16x16x32_bf16 v[180:183], a[188:191], a[64:67], v[180:183]\n"
"v_mfma_f32_16x16x32_bf16 v[188:191], a[188:191], a[68:71], v[188:191]\n"
"ds_read_b64_tr_b16 a[146:147], v242 offset:4608\n"
"v_mfma_f32_16x16x32_bf16 v[196:199], a[188:191], a[72:75], v[196:199]\n"
"buffer_load_dwordx4 v225, s[8:11], s48 offen offset:3072 sc1 lds\n"
"v_mfma_f32_16x16x32_bf16 v[204:207], a[188:191], a[76:79], v[204:207]\n"
"ds_read_b64_tr_b16 a[148:149], v243 offset:4096\n"
"v_mfma_f32_16x16x32_bf16 v[80:83], a[128:131], a[80:83], v[80:83]\n"
"v_mfma_f32_16x16x32_bf16 v[88:91], a[128:131], a[84:87], v[88:91]\n"
"ds_read_b64_tr_b16 a[150:151], v243 offset:4608\n"
"v_mfma_f32_16x16x32_bf16 v[96:99], a[128:131], a[88:91], v[96:99]\n"
"ds_read_b64_tr_b16 a[152:153], v244 offset:4096\n"
"v_mfma_f32_16x16x32_bf16 v[104:107], a[128:131], a[92:95], v[104:107]\n"
"s_waitcnt lgkmcnt(4)\n"
"v_mfma_f32_16x16x32_bf16 v[84:87], a[132:135], a[80:83], v[84:87]\n"
"ds_read_b64_tr_b16 a[154:155], v244 offset:4608\n"
"v_mfma_f32_16x16x32_bf16 v[92:95], a[132:135], a[84:87], v[92:95]\n"
"v_mfma_f32_16x16x32_bf16 v[100:103], a[132:135], a[88:91], v[100:103]\n"
"ds_read_b64_tr_b16 a[156:157], v245 offset:4096\n"
"v_mfma_f32_16x16x32_bf16 v[108:111], a[132:135], a[92:95], v[108:111]\n"
"v_mfma_f32_16x16x32_bf16 v[112:115], a[136:139], a[80:83], v[112:115]\n"
"ds_read_b64_tr_b16 a[158:159], v245 offset:4608\n"
"v_mfma_f32_16x16x32_bf16 v[120:123], a[136:139], a[84:87], v[120:123]\n"
"v_mfma_f32_16x16x32_bf16 v[128:131], a[136:139], a[88:91], v[128:131]\n"
"ds_read_b64_tr_b16 a[160:161], v242 offset:24576\n"
"v_mfma_f32_16x16x32_bf16 v[136:139], a[136:139], a[92:95], v[136:139]\n"
"v_mfma_f32_16x16x32_bf16 v[116:119], a[140:143], a[80:83], v[116:119]\n"
"s_add_u32 s49, s49, s89\n"
"ds_read_b64_tr_b16 a[162:163], v242 offset:25088\n"
"v_mfma_f32_16x16x32_bf16 v[124:127], a[140:143], a[84:87], v[124:127]\n"
"v_mfma_f32_16x16x32_bf16 v[132:135], a[140:143], a[88:91], v[132:135]\n"
"ds_read_b64_tr_b16 a[164:165], v243 offset:24576\n"
"v_mfma_f32_16x16x32_bf16 v[140:143], a[140:143], a[92:95], v[140:143]\n"
"s_waitcnt lgkmcnt(3)\n"
"buffer_store_dwordx4 v[44:47], v250, s[8:11], s49 offen offset:0 sc1\n"
"v_mfma_f32_16x16x32_bf16 v[144:147], a[144:147], a[80:83], v[144:147]\n"
"ds_read_b64_tr_b16 a[166:167], v243 offset:25088\n"
"v_mfma_f32_16x16x32_bf16 v[152:155], a[144:147], a[84:87], v[152:155]\n"
"v_mfma_f32_16x16x32_bf16 v[160:163], a[144:147], a[88:91], v[160:163]\n"
"ds_read_b64_tr_b16 a[168:169], v244 offset:24576\n"
"v_mfma_f32_16x16x32_bf16 v[168:171], a[144:147], a[92:95], v[168:171]\n"
"v_mfma_f32_16x16x32_bf16 v[148:151], a[148:151], a[80:83], v[148:151]\n"
"ds_read_b64_tr_b16 a[170:171], v244 offset:25088\n"
"v_mfma_f32_16x16x32_bf16 v[156:159], a[148:151], a[84:87], v[156:159]\n"
"ds_read_b64_tr_b16 a[172:173], v245 offset:24576\n"
"v_mfma_f32_16x16x32_bf16 v[164:167], a[148:151], a[88:91], v[164:167]\n"
"v_mfma_f32_16x16x32_bf16 v[172:175], a[148:151], a[92:95], v[172:175]\n"
"ds_read_b64_tr_b16 a[174:175], v245 offset:25088\n"
"v_mfma_f32_16x16x32_bf16 v[176:179], a[152:155], a[80:83], v[176:179]\n"
"v_mfma_f32_16x16x32_bf16 v[184:187], a[152:155], a[84:87], v[184:187]\n"
"ds_read_b64_tr_b16 a[176:177], v242 offset:28672\n"
"v_mfma_f32_16x16x32_bf16 v[192:195], a[152:155], a[88:91], v[192:195]\n"
"v_mfma_f32_16x16x32_bf16 v[200:203], a[152:155], a[92:95], v[200:203]\n"
"ds_read_b64_tr_b16 a[178:179], v242 offset:29184\n"
"v_mfma_f32_16x16x32_bf16 v[180:183], a[156:159], a[80:83], v[180:183]\n"
"v_mfma_f32_16x16x32_bf16 v[188:191], a[156:159], a[84:87], v[188:191]\n"
"ds_read_b64_tr_b16 a[180:181], v243 offset:28672\n"
"v_mfma_f32_16x16x32_bf16 v[196:199], a[156:159], a[88:91], v[196:199]\n"
"v_mfma_f32_16x16x32_bf16 v[204:207], a[156:159], a[92:95], v[204:207]\n"
"s_waitcnt lgkmcnt(3)\n"
"ds_read_b64_tr_b16 a[182:183], v243 offset:29184\n"
"v_mfma_f32_16x16x32_bf16 v[80:83], a[160:163], a[96:99], v[80:83]\n"
"v_mfma_f32_16x16x32_bf16 v[88:91], a[160:163], a[100:103], v[88:91]\n"
"ds_read_b64_tr_b16 a[184:185], v244 offset:28672\n"
"v_mfma_f32_16x16x32_bf16 v[96:99], a[160:163], a[104:107], v[96:99]\n"
"v_mfma_f32_16x16x32_bf16 v[104:107], a[160:163], a[108:111], v[104:107]\n"
"ds_read_b64_tr_b16 a[186:187], v244 offset:29184\n"
"v_mfma_f32_16x16x32_bf16 v[84:87], a[164:167], a[96:99], v[84:87]\n"
"v_mfma_f32_16x16x32_bf16 v[92:95], a[164:167], a[100:103], v[92:95]\n"
"ds_read_b64_tr_b16 a[188:189], v245 offset:28672\n"
"v_mfma_f32_16x16x32_bf16 v[100:103], a[164:167], a[104:107], v[100:103]\n"
"v_mfma_f32_16x16x32_bf16 v[108:111], a[164:167], a[108:111], v[108:111]\n"
"ds_read_b64_tr_b16 a[190:191], v245 offset:29184\n"
"v_mfma_f32_16x16x32_bf16 v[112:115], a[168:171], a[96:99], v[112:115]\n"
"v_mfma_f32_16x16x32_bf16 v[120:123], a[168:171], a[100:103], v[120:123]\n"
"v_mfma_f32_16x16x32_bf16 v[128:131], a[168:171], a[104:107], v[128:131]\n"
"v_mfma_f32_16x16x32_bf16 v[136:139], a[168:171], a[108:111], v[136:139]\n"
"s_add_u32 s49, s49, 1024\n"
"v_mfma_f32_16x16x32_bf16 v[116:119], a[172:175], a[96:99], v[116:119]\n"
"v_mfma_f32_16x16x32_bf16 v[124:127], a[172:175], a[100:103], v[124:127]\n"
"s_barrier\n"
"v_mfma_f32_16x16x32_bf16 v[132:135], a[172:175], a[104:107], v[132:135]\n"
"buffer_store_dwordx4 v[60:63], v250, s[8:11], s49 offen offset:0 sc1\n"
"v_mfma_f32_16x16x32_bf16 v[140:143], a[172:175], a[108:111], v[140:143]\n"
"s_waitcnt lgkmcnt(0)\n"
"ds_read_b64_tr_b16 a[128:129], v246 offset:8192\n"
"v_mfma_f32_16x16x32_bf16 v[144:147], a[176:179], a[96:99], v[144:147]\n"
"ds_read_b64_tr_b16 a[130:131], v246 offset:8704\n"
"v_mfma_f32_16x16x32_bf16 v[152:155], a[176:179], a[100:103], v[152:155]\n"
"ds_read_b64_tr_b16 a[132:133], v247 offset:8192\n"
"v_mfma_f32_16x16x32_bf16 v[160:163], a[176:179], a[104:107], v[160:163]\n"
"v_mfma_f32_16x16x32_bf16 v[168:171], a[176:179], a[108:111], v[168:171]\n"
"ds_read_b64_tr_b16 a[134:135], v247 offset:8704\n"
"v_mfma_f32_16x16x32_bf16 v[148:151], a[180:183], a[96:99], v[148:151]\n"
"ds_read_b64_tr_b16 a[136:137], v246 offset:32768\n"
"v_mfma_f32_16x16x32_bf16 v[156:159], a[180:183], a[100:103], v[156:159]\n"
"v_mfma_f32_16x16x32_bf16 v[164:167], a[180:183], a[104:107], v[164:167]\n"
"ds_read_b64_tr_b16 a[138:139], v246 offset:33280\n"
"v_mfma_f32_16x16x32_bf16 v[172:175], a[180:183], a[108:111], v[172:175]\n"
"ds_read_b64_tr_b16 a[140:141], v247 offset:32768\n"
"v_mfma_f32_16x16x32_bf16 v[176:179], a[184:187], a[96:99], v[176:179]\n"
"v_mfma_f32_16x16x32_bf16 v[184:187], a[184:187], a[100:103], v[184:187]\n"
"ds_read_b64_tr_b16 a[142:143], v247 offset:33280\n"
"v_mfma_f32_16x16x32_bf16 v[192:195], a[184:187], a[104:107], v[192:195]\n"
"v_mfma_f32_16x16x32_bf16 v[200:203], a[184:187], a[108:111], v[200:203]\n"
"ds_read_b64_tr_b16 a[144:145], v242 offset:8192\n"
"v_mfma_f32_16x16x32_bf16 v[180:183], a[188:191], a[96:99], v[180:183]\n"
"v_mfma_f32_16x16x32_bf16 v[188:191], a[188:191], a[100:103], v[188:191]\n"
"ds_read_b64_tr_b16 a[146:147], v242 offset:8704\n"
"v_mfma_f32_16x16x32_bf16 v[196:199], a[188:191], a[104:107], v[196:199]\n"
"ds_read_b64_tr_b16 a[148:149], v243 offset:8192\n"
"v_mfma_f32_16x16x32_bf16 v[204:207], a[188:191], a[108:111], v[204:207]\n"
"s_waitcnt lgkmcnt(4)\n"
"v_mfma_f32_16x16x32_bf16 v[80:83], a[128:131], a[192:195], v[80:83]\n"
"ds_read_b64_tr_b16 a[150:151], v243 offset:8704\n"
"v_mfma_f32_16x16x32_bf16 v[88:91], a[128:131], a[196:199], v[88:91]\n"
"ds_read_b64_tr_b16 a[152:153], v242 offset:32768\n"
"v_mfma_f32_16x16x32_bf16 v[96:99], a[128:131], a[200:203], v[96:99]\n"
"v_mfma_f32_16x16x32_bf16 v[104:107], a[128:131], a[204:207], v[104:107]\n"
"ds_read_b64_tr_b16 a[154:155], v242 offset:33280\n"
"v_mfma_f32_16x16x32_bf16 v[84:87], a[132:135], a[192:195], v[84:87]\n"
"ds_read_b64_tr_b16 a[156:157], v243 offset:32768\n"
"v_mfma_f32_16x16x32_bf16 v[92:95], a[132:135], a[196:199], v[92:95]\n"
"v_mfma_f32_16x16x32_bf16 v[100:103], a[132:135], a[200:203], v[100:103]\n"
"ds_read_b64_tr_b16 a[158:159], v243 offset:33280\n"
"v_mfma_f32_16x16x32_bf16 v[108:111], a[132:135], a[204:207], v[108:111]\n"
"v_mfma_f32_16x16x32_bf16 v[80:83], a[136:139], a[208:211], v[80:83]\n"
"ds_read_b64_tr_b16 a[160:161], v248 offset:8192\n"
"v_mfma_f32_16x16x32_bf16 v[88:91], a[136:139], a[212:215], v[88:91]\n"
"v_mfma_f32_16x16x32_bf16 v[96:99], a[136:139], a[216:219], v[96:99]\n"
"s_add_u32 s49, s49, s88\n"
"ds_read_b64_tr_b16 a[162:163], v248 offset:8704\n"
"v_mfma_f32_16x16x32_bf16 v[104:107], a[136:139], a[220:223], v[104:107]\n"
"s_waitcnt lgkmcnt(3)\n"
"ds_read_b64_tr_b16 a[164:165], v249 offset:8192\n"
"v_mfma_f32_16x16x32_bf16 v[84:87], a[140:143], a[208:211], v[84:87]\n"
"buffer_store_dwordx4 v[0:3], v250, s[8:11], s49 offen offset:0 sc1\n"
"v_mfma_f32_16x16x32_bf16 v[92:95], a[140:143], a[212:215], v[92:95]\n"
"ds_read_b64_tr_b16 a[166:167], v249 offset:8704\n"
"v_mfma_f32_16x16x32_bf16 v[100:103], a[140:143], a[216:219], v[100:103]\n"
"ds_read_b64_tr_b16 a[168:169], v248 offset:32768\n"
"v_mfma_f32_16x16x32_bf16 v[108:111], a[140:143], a[220:223], v[108:111]\n"
"s_waitcnt vmcnt(29)\n"
"v_mfma_f32_16x16x32_bf16 v[80:83], a[144:147], a[224:227], v[80:83]\n"
"s_waitcnt vmcnt(28)\n"
"ds_read_b64_tr_b16 a[170:171], v248 offset:33280\n"
"v_mfma_f32_16x16x32_bf16 v[88:91], a[144:147], a[228:231], v[88:91]\n"
"s_waitcnt vmcnt(27)\n"
"ds_read_b64_tr_b16 a[172:173], v249 offset:32768\n"
"v_mfma_f32_16x16x32_bf16 v[96:99], a[144:147], a[232:235], v[96:99]\n"
"s_waitcnt vmcnt(26)\n"
"v_mfma_f32_16x16x32_bf16 v[104:107], a[144:147], a[236:239], v[104:107]\n"
"ds_read_b64_tr_b16 a[174:175], v249 offset:33280\n"
"v_mfma_f32_16x16x32_bf16 v[84:87], a[148:151], a[224:227], v[84:87]\n"
"v_mfma_f32_16x16x32_bf16 v[92:95], a[148:151], a[228:231], v[92:95]\n"
"ds_read_b64_tr_b16 a[176:177], v244 offset:8192\n"
"v_mfma_f32_16x16x32_bf16 v[100:103], a[148:151], a[232:235], v[100:103]\n"
"v_mfma_f32_16x16x32_bf16 v[108:111], a[148:151], a[236:239], v[108:111]\n"
"s_waitcnt vmcnt(25)\n"
"ds_read_b64_tr_b16 a[178:179], v244 offset:8704\n"
"v_mfma_f32_16x16x32_bf16 v[80:83], a[152:155], a[240:243], v[80:83]\n"
"s_waitcnt vmcnt(22)\n"
"ds_read_b64_tr_b16 a[180:181], v245 offset:8192\n"
"v_mfma_f32_16x16x32_bf16 v[88:91], a[152:155], a[244:247], v[88:91]\n"
"s_waitcnt vmcnt(19)\n"
"v_mfma_f32_16x16x32_bf16 v[96:99], a[152:155], a[248:251], v[96:99]\n"
"s_waitcnt vmcnt(17)\n"
"ds_read_b64_tr_b16 a[182:183], v245 offset:8704\n"
"v_mfma_f32_16x16x32_bf16 v[104:107], a[152:155], a[252:255], v[104:107]\n"
"s_waitcnt lgkmcnt(4)\n"
"ds_read_b64_tr_b16 a[184:185], v244 offset:32768\n"
"v_mfma_f32_16x16x32_bf16 v[84:87], a[156:159], a[240:243], v[84:87]\n"
"v_mfma_f32_16x16x32_bf16 v[92:95], a[156:159], a[244:247], v[92:95]\n"
"ds_read_b64_tr_b16 a[186:187], v244 offset:33280\n"
"v_mfma_f32_16x16x32_bf16 v[100:103], a[156:159], a[248:251], v[100:103]\n"
"ds_read_b64_tr_b16 a[188:189], v245 offset:32768\n"
"v_mfma_f32_16x16x32_bf16 v[108:111], a[156:159], a[252:255], v[108:111]\n"
"v_mfma_f32_16x16x32_bf16 v[112:115], a[160:163], a[192:195], v[112:115]\n"
"ds_read_b64_tr_b16 a[190:191], v245 offset:33280\n"
"v_mfma_f32_16x16x32_bf16 v[120:123], a[160:163], a[196:199], v[120:123]\n"
"v_mfma_f32_16x16x32_bf16 v[128:131], a[160:163], a[200:203], v[128:131]\n"
"ds_read_b64_tr_b16 a[128:129], v246 offset:12288\n"
"v_cvt_pk_bf16_f32 v80, v80, v81\n"
"v_mfma_f32_16x16x32_bf16 v[136:139], a[160:163], a[204:207], v[136:139]\n"
"v_cvt_pk_bf16_f32 v81, v82, v83\n"
"v_cvt_pk_bf16_f32 v82, v84, v85\n"
"v_mfma_f32_16x16x32_bf16 v[116:119], a[164:167], a[192:195], v[116:119]\n"
"ds_read_b64_tr_b16 a[130:131], v246 offset:12800\n"
"v_cvt_pk_bf16_f32 v83, v86, v87\n"
"v_mfma_f32_16x16x32_bf16 v[124:127], a[164:167], a[196:199], v[124:127]\n"
"ds_read_b64_tr_b16 a[132:133], v247 offset:12288\n"
"v_accvgpr_write_b32 a0, v80\n"
"v_mfma_f32_16x16x32_bf16 v[132:135], a[164:167], a[200:203], v[132:135]\n"
"v_accvgpr_write_b32 a1, v81\n"
"v_accvgpr_write_b32 a2, v82\n"
"v_mfma_f32_16x16x32_bf16 v[140:143], a[164:167], a[204:207], v[140:143]\n"
"ds_read_b64_tr_b16 a[134:135], v247 offset:12800\n"
"v_accvgpr_write_b32 a3, v83\n"
"v_mfma_f32_16x16x32_bf16 v[112:115], a[168:171], a[208:211], v[112:115]\n"
"ds_read_b64_tr_b16 a[136:137], v246 offset:36864\n"
"v_cvt_pk_bf16_f32 v88, v88, v89\n"
"v_mfma_f32_16x16x32_bf16 v[120:123], a[168:171], a[212:215], v[120:123]\n"
"s_add_u32 s49, s49, 1024\n"
"v_cvt_pk_bf16_f32 v89, v90, v91\n"
"v_cvt_pk_bf16_f32 v90, v92, v93\n"
"v_mfma_f32_16x16x32_bf16 v[128:131], a[168:171], a[216:219], v[128:131]\n"
"ds_read_b64_tr_b16 a[138:139], v246 offset:37376\n"
"v_cvt_pk_bf16_f32 v91, v94, v95\n"
"v_mfma_f32_16x16x32_bf16 v[136:139], a[168:171], a[220:223], v[136:139]\n"
"ds_read_b64_tr_b16 a[140:141], v247 offset:36864\n"
"v_accvgpr_write_b32 a16, v88\n"
"v_mfma_f32_16x16x32_bf16 v[116:119], a[172:175], a[208:211], v[116:119]\n"
"buffer_store_dwordx4 v[16:19], v250, s[8:11], s49 offen offset:0 sc1\n"
"v_mfma_f32_16x16x32_bf16 v[124:127], a[172:175], a[212:215], v[124:127]\n"
"ds_read_b64_tr_b16 a[142:143], v247 offset:37376\n"
"v_accvgpr_write_b32 a17, v89\n"
"v_mfma_f32_16x16x32_bf16 v[132:135], a[172:175], a[216:219], v[132:135]\n"
"v_accvgpr_write_b32 a18, v90\n"
"v_accvgpr_write_b32 a19, v91\n"
"v_mfma_f32_16x16x32_bf16 v[140:143], a[172:175], a[220:223], v[140:143]\n"
"s_waitcnt lgkmcnt(3)\n"
"ds_read_b64_tr_b16 a[144:145], v242 offset:12288\n"
"v_cvt_pk_bf16_f32 v96, v96, v97\n"
"v_mfma_f32_16x16x32_bf16 v[112:115], a[176:179], a[224:227], v[112:115]\n"
"v_cvt_pk_bf16_f32 v97, v98, v99\n"
"v_cvt_pk_bf16_f32 v98, v100, v101\n"
"v_mfma_f32_16x16x32_bf16 v[120:123], a[176:179], a[228:231], v[120:123]\n"
"ds_read_b64_tr_b16 a[146:147], v242 offset:12800\n"
"v_cvt_pk_bf16_f32 v99, v102, v103\n"
"v_mfma_f32_16x16x32_bf16 v[128:131], a[176:179], a[232:235], v[128:131]\n"
"ds_read_b64_tr_b16 a[148:149], v243 offset:12288\n"
"v_accvgpr_write_b32 a32, v96\n"
"v_mfma_f32_16x16x32_bf16 v[136:139], a[176:179], a[236:239], v[136:139]\n"
"v_accvgpr_write_b32 a33, v97\n"
"v_accvgpr_write_b32 a34, v98\n"
"v_mfma_f32_16x16x32_bf16 v[116:119], a[180:183], a[224:227], v[116:119]\n"
"ds_read_b64_tr_b16 a[150:151], v243 offset:12800\n"
"v_accvgpr_write_b32 a35, v99\n"
"v_mfma_f32_16x16x32_bf16 v[124:127], a[180:183], a[228:231], v[124:127]\n"
"ds_read_b64_tr_b16 a[152:153], v242 offset:36864\n"
"v_cvt_pk_bf16_f32 v104, v104, v105\n"
"v_mfma_f32_16x16x32_bf16 v[132:135], a[180:183], a[232:235], v[132:135]\n"
"v_cvt_pk_bf16_f32 v105, v106, v107\n"
"v_cvt_pk_bf16_f32 v106, v108, v109\n"
"v_mfma_f32_16x16x32_bf16 v[140:143], a[180:183], a[236:239], v[140:143]\n"
"ds_read_b64_tr_b16 a[154:155], v242 offset:37376\n"
"v_cvt_pk_bf16_f32 v107, v110, v111\n"
"v_mfma_f32_16x16x32_bf16 v[112:115], a[184:187], a[240:243], v[112:115]\n"
"ds_read_b64_tr_b16 a[156:157], v243 offset:36864\n"
"v_accvgpr_write_b32 a48, v104\n"
"v_mfma_f32_16x16x32_bf16 v[120:123], a[184:187], a[244:247], v[120:123]\n"
"v_accvgpr_write_b32 a49, v105\n"
"v_accvgpr_write_b32 a50, v106\n"
"v_mfma_f32_16x16x32_bf16 v[128:131], a[184:187], a[248:251], v[128:131]\n"
"ds_read_b64_tr_b16 a[158:159], v243 offset:37376\n"
"v_accvgpr_write_b32 a51, v107\n"
"v_mfma_f32_16x16x32_bf16 v[136:139], a[184:187], a[252:255], v[136:139]\n"
"v_mfma_f32_16x16x32_bf16 v[116:119], a[188:191], a[240:243], v[116:119]\n"
"ds_read_b64_tr_b16 a[160:161], v248 offset:12288\n"
"v_mfma_f32_16x16x32_bf16 v[124:127], a[188:191], a[244:247], v[124:127]\n"
"v_mfma_f32_16x16x32_bf16 v[132:135], a[188:191], a[248:251], v[132:135]\n"
"ds_read_b64_tr_b16 a[162:163], v248 offset:12800\n"
"v_mfma_f32_16x16x32_bf16 v[140:143], a[188:191], a[252:255], v[140:143]\n"
"ds_read_b64_tr_b16 a[164:165], v249 offset:12288\n"
"v_mfma_f32_16x16x32_bf16 v[144:147], a[128:131], a[192:195], v[144:147]\n"
"v_mfma_f32_16x16x32_bf16 v[152:155], a[128:131], a[196:199], v[152:155]\n"
"ds_read_b64_tr_b16 a[166:167], v249 offset:12800\n"
"v_mfma_f32_16x16x32_bf16 v[160:163], a[128:131], a[200:203], v[160:163]\n"
"ds_read_b64_tr_b16 a[168:169], v248 offset:36864\n"
"v_cvt_pk_bf16_f32 v112, v112, v113\n"
"v_mfma_f32_16x16x32_bf16 v[168:171], a[128:131], a[204:207], v[168:171]\n"
"v_cvt_pk_bf16_f32 v113, v114, v115\n"
"v_cvt_pk_bf16_f32 v114, v116, v117\n"
"v_mfma_f32_16x16x32_bf16 v[148:151], a[132:135], a[192:195], v[148:151]\n"
"ds_read_b64_tr_b16 a[170:171], v248 offset:37376\n"
"v_cvt_pk_bf16_f32 v115, v118, v119\n"
"v_mfma_f32_16x16x32_bf16 v[156:159], a[132:135], a[196:199], v[156:159]\n"
"ds_read_b64_tr_b16 a[172:173], v249 offset:36864\n"
"v_accvgpr_write_b32 a4, v112\n"
"v_mfma_f32_16x16x32_bf16 v[164:167], a[132:135], a[200:203], v[164:167]\n"
"v_accvgpr_write_b32 a5, v113\n"
"v_accvgpr_write_b32 a6, v114\n"
"v_mfma_f32_16x16x32_bf16 v[172:175], a[132:135], a[204:207], v[172:175]\n"
"s_waitcnt lgkmcnt(4)\n"
"ds_read_b64_tr_b16 a[174:175], v249 offset:37376\n"
"v_accvgpr_write_b32 a7, v115\n"
"v_mfma_f32_16x16x32_bf16 v[144:147], a[136:139], a[208:211], v[144:147]\n"
"s_add_u32 s49, s49, s89\n"
"v_cvt_pk_bf16_f32 v120, v120, v121\n"
"v_cvt_pk_bf16_f32 v121, v122, v123\n"
"v_mfma_f32_16x16x32_bf16 v[152:155], a[136:139], a[212:215], v[152:155]\n"
"ds_read_b64_tr_b16 a[176:177], v244 offset:12288\n"
"v_cvt_pk_bf16_f32 v122, v124, v125\n"
"v_mfma_f32_16x16x32_bf16 v[160:163], a[136:139], a[216:219], v[160:163]\n"
"buffer_store_dwordx4 v[32:35], v250, s[8:11], s49 offen offset:0 sc1\n"
"v_mfma_f32_16x16x32_bf16 v[168:171], a[136:139], a[220:223], v[168:171]\n"
"ds_read_b64_tr_b16 a[178:179], v244 offset:12800\n"
"v_cvt_pk_bf16_f32 v123, v126, v127\n"
"v_mfma_f32_16x16x32_bf16 v[148:151], a[140:143], a[208:211], v[148:151]\n"
"ds_read_b64_tr_b16 a[180:181], v245 offset:12288\n"
"v_accvgpr_write_b32 a20, v120\n"
"v_mfma_f32_16x16x32_bf16 v[156:159], a[140:143], a[212:215], v[156:159]\n"
"v_accvgpr_write_b32 a21, v121\n"
"v_accvgpr_write_b32 a22, v122\n"
"v_mfma_f32_16x16x32_bf16 v[164:167], a[140:143], a[216:219], v[164:167]\n"
"ds_read_b64_tr_b16 a[182:183], v245 offset:12800\n"
"v_accvgpr_write_b32 a23, v123\n"
"v_mfma_f32_16x16x32_bf16 v[172:175], a[140:143], a[220:223], v[172:175]\n"
"ds_read_b64_tr_b16 a[184:185], v244 offset:36864\n"
"v_cvt_pk_bf16_f32 v128, v128, v129\n"
"v_mfma_f32_16x16x32_bf16 v[144:147], a[144:147], a[224:227], v[144:147]\n"
"v_cvt_pk_bf16_f32 v129, v130, v131\n"
"v_cvt_pk_bf16_f32 v130, v132, v133\n"
"v_mfma_f32_16x16x32_bf16 v[152:155], a[144:147], a[228:231], v[152:155]\n"
"ds_read_b64_tr_b16 a[186:187], v244 offset:37376\n"
"v_cvt_pk_bf16_f32 v131, v134, v135\n"
"v_mfma_f32_16x16x32_bf16 v[160:163], a[144:147], a[232:235], v[160:163]\n"
"ds_read_b64_tr_b16 a[188:189], v245 offset:36864\n"
"v_accvgpr_write_b32 a36, v128\n"
"v_mfma_f32_16x16x32_bf16 v[168:171], a[144:147], a[236:239], v[168:171]\n"
"v_accvgpr_write_b32 a37, v129\n"
"v_accvgpr_write_b32 a38, v130\n"
"v_mfma_f32_16x16x32_bf16 v[148:151], a[148:151], a[224:227], v[148:151]\n"
"ds_read_b64_tr_b16 a[190:191], v245 offset:37376\n"
"v_accvgpr_write_b32 a39, v131\n"
"v_mfma_f32_16x16x32_bf16 v[156:159], a[148:151], a[228:231], v[156:159]\n"
"v_cvt_pk_bf16_f32 v136, v136, v137\n"
"v_cvt_pk_bf16_f32 v137, v138, v139\n"
"v_mfma_f32_16x16x32_bf16 v[164:167], a[148:151], a[232:235], v[164:167]\n"
"v_cvt_pk_bf16_f32 v138, v140, v141\n"
"v_cvt_pk_bf16_f32 v139, v142, v143\n"
"v_mfma_f32_16x16x32_bf16 v[172:175], a[148:151], a[236:239], v[172:175]\n"
"v_accvgpr_write_b32 a52, v136\n"
"v_accvgpr_write_b32 a53, v137\n"
"v_mfma_f32_16x16x32_bf16 v[144:147], a[152:155], a[240:243], v[144:147]\n"
"v_accvgpr_write_b32 a54, v138\n"
"v_accvgpr_write_b32 a55, v139\n"
"v_mfma_f32_16x16x32_bf16 v[152:155], a[152:155], a[244:247], v[152:155]\n"
"v_mfma_f32_16x16x32_bf16 v[160:163], a[152:155], a[248:251], v[160:163]\n"
"v_mfma_f32_16x16x32_bf16 v[168:171], a[152:155], a[252:255], v[168:171]\n"
"v_mfma_f32_16x16x32_bf16 v[148:151], a[156:159], a[240:243], v[148:151]\n"
"v_mfma_f32_16x16x32_bf16 v[156:159], a[156:159], a[244:247], v[156:159]\n"
"v_mfma_f32_16x16x32_bf16 v[164:167], a[156:159], a[248:251], v[164:167]\n"
"v_mfma_f32_16x16x32_bf16 v[172:175], a[156:159], a[252:255], v[172:175]\n"
"v_mfma_f32_16x16x32_bf16 v[176:179], a[160:163], a[192:195], v[176:179]\n"
"s_waitcnt lgkmcnt(0)\n"
"v_mfma_f32_16x16x32_bf16 v[180:183], a[164:167], a[192:195], v[180:183]\n"
"v_mfma_f32_16x16x32_bf16 v[176:179], a[168:171], a[208:211], v[176:179]\n"
"v_cvt_pk_bf16_f32 v144, v144, v145\n"
"v_cvt_pk_bf16_f32 v145, v146, v147\n"
"v_mfma_f32_16x16x32_bf16 v[180:183], a[172:175], a[208:211], v[180:183]\n"
"v_cvt_pk_bf16_f32 v146, v148, v149\n"
"v_cvt_pk_bf16_f32 v147, v150, v151\n"
"v_mfma_f32_16x16x32_bf16 v[176:179], a[176:179], a[224:227], v[176:179]\n"
"v_mov_b32 v255, 0x3f803f80\n"
"v_accvgpr_write_b32 a8, v144\n"
"v_mfma_f32_16x16x32_bf16 v[180:183], a[180:183], a[224:227], v[180:183]\n"
"v_accvgpr_write_b32 a9, v145\n"
"v_accvgpr_write_b32 a128, v255\n"
"v_mfma_f32_16x16x32_bf16 v[176:179], a[184:187], a[240:243], v[176:179]\n"
"v_accvgpr_write_b32 a129, v255\n"
"v_accvgpr_write_b32 a130, v255\n"
"v_mfma_f32_16x16x32_bf16 v[180:183], a[188:191], a[240:243], v[180:183]\n"
"s_add_u32 s49, s49, 1024\n"
"v_accvgpr_write_b32 a131, v255\n"
"v_accvgpr_write_b32 a10, v146\n"
"v_mfma_f32_16x16x32_bf16 v[184:187], a[160:163], a[196:199], v[184:187]\n"
"v_accvgpr_write_b32 a11, v147\n"
"v_cvt_pk_bf16_f32 v152, v152, v153\n"
"v_mfma_f32_16x16x32_bf16 v[188:191], a[164:167], a[196:199], v[188:191]\n"
"buffer_store_dwordx4 v[48:51], v250, s[8:11], s49 offen offset:0 sc1\n"
"v_mfma_f32_16x16x32_bf16 v[184:187], a[168:171], a[212:215], v[184:187]\n"
"v_cvt_pk_bf16_f32 v153, v154, v155\n"
"v_cvt_pk_bf16_f32 v154, v156, v157\n"
"v_mfma_f32_16x16x32_bf16 v[188:191], a[172:175], a[212:215], v[188:191]\n"
"v_cvt_pk_bf16_f32 v155, v158, v159\n"
"v_accvgpr_write_b32 a24, v152\n"
"v_mfma_f32_16x16x32_bf16 v[184:187], a[176:179], a[228:231], v[184:187]\n"
"v_accvgpr_write_b32 a25, v153\n"
"v_cvt_pk_bf16_f32 v176, v176, v177\n"
"v_mfma_f32_16x16x32_bf16 v[188:191], a[180:183], a[228:231], v[188:191]\n"
"v_cvt_pk_bf16_f32 v177, v178, v179\n"
"v_accvgpr_write_b32 a26, v154\n"
"v_mfma_f32_16x16x32_bf16 v[184:187], a[184:187], a[244:247], v[184:187]\n"
"v_accvgpr_write_b32 a27, v155\n"
"v_cvt_pk_bf16_f32 v178, v180, v181\n"
"v_mfma_f32_16x16x32_bf16 v[188:191], a[188:191], a[244:247], v[188:191]\n"
"v_cvt_pk_bf16_f32 v179, v182, v183\n"
"v_accvgpr_write_b32 a12, v176\n"
"v_mfma_f32_16x16x32_bf16 v[192:195], a[160:163], a[200:203], v[192:195]\n"
"v_accvgpr_write_b32 a13, v177\n"
"v_accvgpr_write_b32 a14, v178\n"
"v_mfma_f32_16x16x32_bf16 v[196:199], a[164:167], a[200:203], v[196:199]\n"
"s_waitcnt vmcnt(15)\n"
"s_barrier\n"
"v_mfma_f32_16x16x32_bf16 v[192:195], a[168:171], a[216:219], v[192:195]\n"
"ds_read_b128 a[64:67], v226 offset:0\n"
"v_accvgpr_write_b32 a15, v179\n"
"v_mfma_f32_16x16x32_bf16 v[196:199], a[172:175], a[216:219], v[196:199]\n"
"v_cvt_pk_bf16_f32 v160, v160, v161\n"
"v_cvt_pk_bf16_f32 v161, v162, v163\n"
"v_mfma_f32_16x16x32_bf16 v[192:195], a[176:179], a[232:235], v[192:195]\n"
"ds_read_b128 a[68:71], v226 offset:1024\n"
"v_cvt_pk_bf16_f32 v162, v164, v165\n"
"v_mfma_f32_16x16x32_bf16 v[196:199], a[180:183], a[232:235], v[196:199]\n"
"v_cvt_pk_bf16_f32 v163, v166, v167\n"
"v_accvgpr_write_b32 a40, v160\n"
"v_mfma_f32_16x16x32_bf16 v[192:195], a[184:187], a[248:251], v[192:195]\n"
"ds_read_b128 a[72:75], v226 offset:2048\n"
"v_accvgpr_write_b32 a41, v161\n"
"v_mfma_f32_16x16x32_bf16 v[196:199], a[188:191], a[248:251], v[196:199]\n"
"v_accvgpr_write_b32 a42, v162\n"
"v_accvgpr_write_b32 a43, v163\n"
"v_mfma_f32_16x16x32_bf16 v[200:203], a[160:163], a[204:207], v[200:203]\n"
"ds_read_b128 a[76:79], v226 offset:3072\n"
"v_cvt_pk_bf16_f32 v184, v184, v185\n"
"v_mfma_f32_16x16x32_bf16 v[204:207], a[164:167], a[204:207], v[204:207]\n"
"v_cvt_pk_bf16_f32 v185, v186, v187\n"
"v_cvt_pk_bf16_f32 v186, v188, v189\n"
"v_mfma_f32_16x16x32_bf16 v[200:203], a[168:171], a[220:223], v[200:203]\n"
"ds_read_b128 a[80:83], v226 offset:4096\n"
"v_cvt_pk_bf16_f32 v187, v190, v191\n"
"v_mfma_f32_16x16x32_bf16 v[204:207], a[172:175], a[220:223], v[204:207]\n"
"v_cvt_pk_bf16_f32 v168, v168, v169\n"
"v_cvt_pk_bf16_f32 v169, v170, v171\n"
"v_mfma_f32_16x16x32_bf16 v[200:203], a[176:179], a[236:239], v[200:203]\n"
"ds_read_b128 a[84:87], v226 offset:5120\n"
"v_accvgpr_write_b32 a28, v184\n"
"v_mfma_f32_16x16x32_bf16 v[204:207], a[180:183], a[236:239], v[204:207]\n"
"v_accvgpr_write_b32 a29, v185\n"
"v_cvt_pk_bf16_f32 v170, v172, v173\n"
"v_mfma_f32_16x16x32_bf16 v[200:203], a[184:187], a[252:255], v[200:203]\n"
"v_cvt_pk_bf16_f32 v171, v174, v175\n"
"v_accvgpr_write_b32 a30, v186\n"
"v_mfma_f32_16x16x32_bf16 v[204:207], a[188:191], a[252:255], v[204:207]\n"
"v_accvgpr_write_b32 a31, v187\n"
"v_accvgpr_write_b32 a56, v168\n"
"v_accvgpr_write_b32 a57, v169\n"
"v_accvgpr_write_b32 a58, v170\n"
"v_accvgpr_write_b32 a59, v171\n"
"s_cmp_eq_u32 s34, 0\n"
"s_cbranch_scc1 LFA_%=\n"
"s_waitcnt lgkmcnt(5)\n"
"v_mfma_f32_16x16x32_bf16 v[0:3], a[64:67], a[0:3], 0\n"
"s_mov_b32 m0, s40\n"
"v_cvt_pk_bf16_f32 v192, v192, v193\n"
"v_cvt_pk_bf16_f32 v193, v194, v195\n"
"v_mfma_f32_16x16x32_bf16 v[8:11], a[64:67], a[16:19], 0\n"
"v_readlane_b32 s43, v228, s42\n"
"v_cvt_pk_bf16_f32 v194, v196, v197\n"
"v_mfma_f32_16x16x32_bf16 v[16:19], a[64:67], a[32:35], 0\n"
"v_cvt_pk_bf16_f32 v195, v198, v199\n"
"v_readlane_b32 s44, v229, s42\n"
"v_mfma_f32_16x16x32_bf16 v[24:27], a[64:67], a[48:51], 0\n"
"s_waitcnt lgkmcnt(0)\n"
"buffer_load_dwordx4 v225, s[8:11], s43 offen offset:0 sc1 lds\n"
"v_mfma_f32_16x16x32_bf16 v[0:3], a[68:71], a[4:7], v[0:3]\n"
"s_add_u32 s42, s42, 1\n"
"v_accvgpr_write_b32 a44, v192\n"
"v_accvgpr_write_b32 a45, v193\n"
"v_mfma_f32_16x16x32_bf16 v[8:11], a[68:71], a[20:23], v[8:11]\n"
"v_accvgpr_write_b32 a46, v194\n"
"v_accvgpr_write_b32 a47, v195\n"
"v_mfma_f32_16x16x32_bf16 v[16:19], a[68:71], a[36:39], v[16:19]\n"
"v_cvt_pk_bf16_f32 v200, v200, v201\n"
"v_cvt_pk_bf16_f32 v201, v202, v203\n"
"v_mfma_f32_16x16x32_bf16 v[24:27], a[68:71], a[52:55], v[24:27]\n"
"v_cvt_pk_bf16_f32 v202, v204, v205\n"
"v_cvt_pk_bf16_f32 v203, v206, v207\n"
"v_mov_b32 v227, v226\n"
"v_mfma_f32_16x16x32_bf16 v[0:3], a[72:75], a[8:11], v[0:3]\n"
"buffer_load_dwordx4 v225, s[8:11], s43 offen offset:1024 sc1 lds\n"
"v_mfma_f32_16x16x32_bf16 v[8:11], a[72:75], a[24:27], v[8:11]\n"
"ds_read_b128 a[88:91], v226 offset:6144\n"
"v_accvgpr_write_b32 a60, v200\n"
"v_accvgpr_write_b32 a61, v201\n"
"v_mfma_f32_16x16x32_bf16 v[16:19], a[72:75], a[40:43], v[16:19]\n"
"s_lshl_b32 s59, s34, 5\n"
"v_accvgpr_write_b32 a62, v202\n"
"v_accvgpr_write_b32 a63, v203\n"
"v_mfma_f32_16x16x32_bf16 v[24:27], a[72:75], a[56:59], v[24:27]\n"
"s_sub_u32 s60, 480, s59\n"
"v_mfma_f32_16x16x32_bf16 v[0:3], a[76:79], a[12:15], v[0:3]\n"
"s_add_u32 s59, s59, s35\n"
"v_mfma_f32_16x16x32_bf16 v[8:11], a[76:79], a[28:31], v[8:11]\n"
"s_add_u32 s60, s60, s35\n"
"ds_read_b128 a[92:95], v226 offset:7168\n"
"v_mfma_f32_16x16x32_bf16 v[16:19], a[76:79], a[44:47], v[16:19]\n"
"s_lshl_b32 s59, s59, 15\n"
"buffer_load_dwordx4 v225, s[8:11], s43 offen offset:2048 sc1 lds\n"
"v_mfma_f32_16x16x32_bf16 v[24:27], a[76:79], a[60:63], v[24:27]\n"
"s_lshl_b32 s60, s60, 15\n"
"v_mfma_f32_16x16x32_bf16 v[4:7], a[80:83], a[0:3], 0\n"
"s_add_u32 s48, s36, 44040192\n"
"v_exp_f32 v0, v0\n"
"v_mfma_f32_16x16x32_bf16 v[12:15], a[80:83], a[16:19], 0\n"
"s_add_u32 s59, s59, s48\n"
"ds_read_b128 a[96:99], v226 offset:8192\n"
"v_mfma_f32_16x16x32_bf16 v[20:23], a[80:83], a[32:35], 0\n"
"s_add_u32 s60, s60, s48\n"
"v_exp_f32 v1, v1\n"
"v_mfma_f32_16x16x32_bf16 v[28:31], a[80:83], a[48:51], 0\n"
"s_lshl_b32 s61, s34, 3\n"
"v_exp_f32 v2, v2\n"
"v_mfma_f32_16x16x32_bf16 v[4:7], a[84:87], a[4:7], v[4:7]\n"
"s_add_u32 s61, s61, s32\n"
"buffer_load_dwordx4 v225, s[8:11], s43 offen offset:3072 sc1 lds\n"
"v_mfma_f32_16x16x32_bf16 v[12:15], a[84:87], a[20:23], v[12:15]\n"
"s_lshl_b32 s61, s61, 2\n"
"v_exp_f32 v3, v3\n"
"v_mfma_f32_16x16x32_bf16 v[20:23], a[84:87], a[36:39], v[20:23]\n"
"s_add_u32 s61, s61, s30\n"
"ds_read_b128 a[100:103], v226 offset:9216\n"
"v_cvt_pk_bf16_f32 v0, v0, v1\n"
"v_mfma_f32_16x16x32_bf16 v[28:31], a[84:87], a[52:55], v[28:31]\n"
"s_waitcnt lgkmcnt(2)\n"
"v_exp_f32 v8, v8\n"
"v_mfma_f32_16x16x32_bf16 v[4:7], a[88:91], a[8:11], v[4:7]\n"
"s_add_u32 m0, s40, 16384\n"
"v_exp_f32 v9, v9\n"
"v_cvt_pk_bf16_f32 v1, v2, v3\n"
"v_mfma_f32_16x16x32_bf16 v[12:15], a[88:91], a[24:27], v[12:15]\n"
"s_lshl_b32 s61, s61, 2\n"
"v_exp_f32 v10, v10\n"
"v_mfma_f32_16x16x32_bf16 v[20:23], a[88:91], a[40:43], v[20:23]\n"
"s_add_u32 s61, s61, s33\n"
"ds_read_b128 a[104:107], v226 offset:10240\n"
"v_cvt_pk_bf16_f32 v8, v8, v9\n"
"v_mfma_f32_16x16x32_bf16 v[28:31], a[88:91], a[56:59], v[28:31]\n"
"s_lshl_b32 s61, s61, 3\n"
"buffer_load_dwordx4 v225, s[8:11], s44 offen offset:0 sc1 lds\n"
"v_mfma_f32_16x16x32_bf16 v[4:7], a[92:95], a[12:15], v[4:7]\n"
"s_add_u32 s61, s61, 6912\n"
"v_exp_f32 v11, v11\n"
"v_mfma_f32_16x16x32_bf16 v[12:15], a[92:95], a[28:31], v[12:15]\n"
"s_lshr_b32 s48, s31, 3\n"
"v_exp_f32 v16, v16\n"
"v_mfma_f32_16x16x32_bf16 v[20:23], a[92:95], a[44:47], v[20:23]\n"
"s_add_u32 s40, s40, 32768\n"
"v_exp_f32 v17, v17\n"
"v_cvt_pk_bf16_f32 v9, v10, v11\n"
"v_mfma_f32_16x16x32_bf16 v[28:31], a[92:95], a[60:63], v[28:31]\n"
"s_waitcnt lgkmcnt(1)\n"
"ds_read_b128 a[108:111], v226 offset:11264\n"
"v_cvt_pk_bf16_f32 v16, v16, v17\n"
"v_mfma_f32_16x16x32_bf16 v[32:35], a[96:99], a[0:3], 0\n"
"s_add_u32 s58, s32, 32\n"
"v_exp_f32 v18, v18\n"
"v_mfma_f32_16x16x32_bf16 v[40:43], a[96:99], a[16:19], 0\n"
"s_cmp_eq_u32 s40, s46\n"
"v_exp_f32 v19, v19\n"
"v_mfma_f32_16x16x32_bf16 v[48:51], a[96:99], a[32:35], 0\n"
"s_cselect_b32 s40, s47, s40\n"
"buffer_load_dwordx4 v225, s[8:11], s44 offen offset:1024 sc1 lds\n"
"v_mfma_f32_16x16x32_bf16 v[56:59], a[96:99], a[48:51], 0\n"
"s_cmp_eq_u32 s48, 5\n"
"v_exp_f32 v24, v24\n"
"v_cvt_pk_bf16_f32 v17, v18, v19\n"
"v_mfma_f32_16x16x32_bf16 v[32:35], a[100:103], a[4:7], v[32:35]\n"
"s_cselect_b32 s58, s58, 99\n"
"ds_read_b128 a[112:115], v226 offset:12288\n"
"s_cmp_eq_u32 s34, 6\n"
"v_mfma_f32_16x16x32_bf16 v[40:43], a[100:103], a[20:23], v[40:43]\n"
"s_cselect_b32 s58, s35, s58\n"
"v_exp_f32 v25, v25\n"
"v_mfma_f32_16x16x32_bf16 v[48:51], a[100:103], a[36:39], v[48:51]\n"
"s_lshl_b32 s56, s34, 1\n"
"v_exp_f32 v26, v26\n"
"v_cvt_pk_bf16_f32 v24, v24, v25\n"
"v_mfma_f32_16x16x32_bf16 v[56:59], a[100:103], a[52:55], v[56:59]\n"
"s_waitcnt lgkmcnt(1)\n"
"v_exp_f32 v27, v27\n"
"v_mfma_f32_16x16x32_bf16 v[32:35], a[104:107], a[8:11], v[32:35]\n"
"s_sub_u32 s56, s56, 2\n"
"ds_read_b128 a[116:119], v226 offset:13312\n"
"v_cvt_pk_bf16_f32 v25, v26, v27\n"
"v_mfma_f32_16x16x32_bf16 v[40:43], a[104:107], a[24:27], v[40:43]\n"
"s_cmp_ge_u32 s34, 5\n"
"buffer_load_dwordx4 v225, s[8:11], s44 offen offset:2048 sc1 lds\n"
"v_mfma_f32_16x16x32_bf16 v[48:51], a[104:107], a[40:43], v[48:51]\n"
"s_cselect_b32 s56, 1, s56\n"
"v_exp_f32 v4, v4\n"
"v_mfma_f32_16x16x32_bf16 v[56:59], a[104:107], a[56:59], v[56:59]\n"
"s_cmp_ge_u32 s34, 5\n"
"v_exp_f32 v5, v5\n"
"v_mfma_f32_16x16x32_bf16 v[32:35], a[108:111], a[12:15], v[32:35]\n"
"s_cselect_b32 s57, 1, 0\n"
"v_exp_f32 v6, v6\n"
"v_cvt_pk_bf16_f32 v2, v4, v5\n"
"v_mfma_f32_16x16x32_bf16 v[40:43], a[108:111], a[28:31], v[40:43]\n"
"s_lshl_b32 s48, s35, 3\n"
"ds_read_b128 a[120:123], v226 offset:14336\n"
"ds_read_b128 a[124:127], v226 offset:15360\n"
"v_readlane_b32 s43, v228, s42\n"
"v_mfma_f32_16x16x32_bf16 v[48:51], a[108:111], a[44:47], v[48:51]\n"
"s_add_u32 s48, s48, s34\n"
"v_exp_f32 v7, v7\n"
"v_mfma_f32_16x16x32_bf16 v[56:59], a[108:111], a[60:63], v[56:59]\n"
"s_waitcnt lgkmcnt(2)\n"
"v_exp_f32 v12, v12\n"
"v_cvt_pk_bf16_f32 v3, v6, v7\n"
"v_mfma_f32_16x16x32_bf16 v[36:39], a[112:115], a[0:3], 0\n"
"s_lshl_b32 s48, s48, 3\n"
"buffer_load_dwordx4 v225, s[8:11], s44 offen offset:3072 sc1 lds\n"
"v_mfma_f32_16x16x32_bf16 v[44:47], a[112:115], a[16:19], 0\n"
"s_add_u32 s48, s48, 4352\n"
"v_exp_f32 v13, v13\n"
"v_mfma_f32_16x16x32_bf16 v[52:55], a[112:115], a[32:35], 0\n"
"s_add_u32 s48, s48, s45\n"
"v_exp_f32 v14, v14\n"
"v_cvt_pk_bf16_f32 v10, v12, v13\n"
"v_mfma_f32_16x16x32_bf16 v[60:63], a[112:115], a[48:51], 0\n"
"s_mov_b32 m0, s40\n"
"v_exp_f32 v15, v15\n"
"v_readlane_b32 s44, v229, s42\n"
"v_mfma_f32_16x16x32_bf16 v[36:39], a[116:119], a[4:7], v[36:39]\n"
"s_add_u32 s42, s42, 1\n"
"v_exp_f32 v20, v20\n"
"v_cvt_pk_bf16_f32 v11, v14, v15\n"
"v_mfma_f32_16x16x32_bf16 v[44:47], a[116:119], a[20:23], v[44:47]\n"
"ds_read_b128 a[96:99], v227 offset:24576\n"
"ds_read_b128 a[100:103], v227 offset:25600\n"
"v_mfma_f32_16x16x32_bf16 v[52:55], a[116:119], a[36:39], v[52:55]\n"
"buffer_load_dwordx4 v225, s[8:11], s43 offen offset:0 sc1 lds\n"
"v_mfma_f32_16x16x32_bf16 v[60:63], a[116:119], a[52:55], v[60:63]\n"
"s_waitcnt lgkmcnt(2)\n"
"v_exp_f32 v21, v21\n"
"v_mfma_f32_16x16x32_bf16 v[36:39], a[120:123], a[8:11], v[36:39]\n"
"v_exp_f32 v22, v22\n"
"v_cvt_pk_bf16_f32 v18, v20, v21\n"
"v_mfma_f32_16x16x32_bf16 v[44:47], a[120:123], a[24:27], v[44:47]\n"
"v_exp_f32 v23, v23\n"
"v_mfma_f32_16x16x32_bf16 v[52:55], a[120:123], a[40:43], v[52:55]\n"
"buffer_load_dwordx4 v225, s[8:11], s43 offen offset:1024 sc1 lds\n"
"v_cvt_pk_bf16_f32 v19, v22, v23\n"
"v_mfma_f32_16x16x32_bf16 v[60:63], a[120:123], a[56:59], v[60:63]\n"
"v_exp_f32 v28, v28\n"
"v_mfma_f32_16x16x32_bf16 v[36:39], a[124:127], a[12:15], v[36:39]\n"
"ds_read_b128 a[104:107], v227 offset:26624\n"
"v_mfma_f32_16x16x32_bf16 v[44:47], a[124:127], a[28:31], v[44:47]\n"
"v_exp_f32 v29, v29\n"
"v_mfma_f32_16x16x32_bf16 v[52:55], a[124:127], a[44:47], v[52:55]\n"
"v_exp_f32 v30, v30\n"
"v_cvt_pk_bf16_f32 v26, v28, v29\n"
"v_mfma_f32_16x16x32_bf16 v[60:63], a[124:127], a[60:63], v[60:63]\n"
"s_waitcnt lgkmcnt(1)\n"
"v_exp_f32 v31, v31\n"
"v_mfma_f32_16x16x32_bf16 v[80:83], a[96:99], v[0:3], 0\n"
"s_add_u32 s41, s41, 32768\n"
"v_cvt_pk_bf16_f32 v27, v30, v31\n"
"s_cmp_eq_u32 s41, 163840\n"
"v_mfma_f32_16x16x32_bf16 v[112:115], a[96:99], v[8:11], 0\n"
"s_cselect_b32 s41, 0, s41\n"
"buffer_load_dwordx4 v225, s[8:11], s43 offen offset:2048 sc1 lds\n"
"v_mfma_f32_16x16x32_bf16 v[144:147], a[96:99], v[16:19], 0\n"
"ds_read_b128 a[108:111], v227 offset:27648\n"
"v_add_u32 v226, s41, v224\n"
"v_mfma_f32_16x16x32_bf16 v[176:179], a[96:99], v[24:27], 0\n"
"v_exp_f32 v32, v32\n"
"v_mfma_f32_16x16x32_bf16 v[84:87], a[100:103], v[0:3], 0\n"
"v_exp_f32 v33, v33\n"
"v_mfma_f32_16x16x32_bf16 v[116:119], a[100:103], v[8:11], 0\n"
"v_exp_f32 v34, v34\n"
"v_cvt_pk_bf16_f32 v64, v32, v33\n"
"v_mfma_f32_16x16x32_bf16 v[148:151], a[100:103], v[16:19], 0\n"
"ds_read_b128 a[112:115], v227 offset:28672\n"
"ds_read_b128 a[116:119], v227 offset:29696\n"
"v_mfma_f32_16x16x32_bf16 v[180:183], a[100:103], v[24:27], 0\n"
"s_waitcnt lgkmcnt(3)\n"
"v_exp_f32 v35, v35\n"
"v_mfma_f32_16x16x32_bf16 v[88:91], a[104:107], v[0:3], 0\n"
"buffer_load_dwordx4 v225, s[8:11], s43 offen offset:3072 sc1 lds\n"
"v_cvt_pk_bf16_f32 v65, v34, v35\n"
"v_mfma_f32_16x16x32_bf16 v[120:123], a[104:107], v[8:11], 0\n"
"s_add_u32 m0, s40, 16384\n"
"v_exp_f32 v40, v40\n"
"v_mfma_f32_16x16x32_bf16 v[152:155], a[104:107], v[16:19], 0\n"
"s_add_u32 s40, s40, 32768\n"
"v_exp_f32 v41, v41\n"
"v_mfma_f32_16x16x32_bf16 v[184:187], a[104:107], v[24:27], 0\n"
"s_waitcnt lgkmcnt(2)\n"
"v_exp_f32 v42, v42\n"
"v_cvt_pk_bf16_f32 v68, v40, v41\n"
"v_mfma_f32_16x16x32_bf16 v[92:95], a[108:111], v[0:3], 0\n"
"s_cmp_eq_u32 s40, s46\n"
"v_exp_f32 v43, v43\n"
"v_mfma_f32_16x16x32_bf16 v[124:127], a[108:111], v[8:11], 0\n"
"s_cselect_b32 s40, s47, s40\n"
"buffer_load_dwordx4 v225, s[8:11], s44 offen offset:0 sc1 lds\n"
"v_cvt_pk_bf16_f32 v69, v42, v43\n"
"v_mfma_f32_16x16x32_bf16 v[156:159], a[108:111], v[16:19], 0\n"
"ds_read_b128 a[120:123], v227 offset:30720\n"
"v_mfma_f32_16x16x32_bf16 v[188:191], a[108:111], v[24:27], 0\n"
"s_waitcnt lgkmcnt(1)\n"
"v_exp_f32 v48, v48\n"
"v_mfma_f32_16x16x32_bf16 v[96:99], a[112:115], v[0:3], 0\n"
"v_exp_f32 v49, v49\n"
"v_mfma_f32_16x16x32_bf16 v[128:131], a[112:115], v[8:11], 0\n"
"v_exp_f32 v50, v50\n"
"v_cvt_pk_bf16_f32 v72, v48, v49\n"
"v_mfma_f32_16x16x32_bf16 v[160:163], a[112:115], v[16:19], 0\n"
"ds_read_b128 a[124:127], v227 offset:31744\n"
"v_mfma_f32_16x16x32_bf16 v[192:195], a[112:115], v[24:27], 0\n"
"v_exp_f32 v51, v51\n"
"v_mfma_f32_16x16x32_bf16 v[100:103], a[116:119], v[0:3], 0\n"
"buffer_load_dwordx4 v225, s[8:11], s44 offen offset:1024 sc1 lds\n"
"v_cvt_pk_bf16_f32 v73, v50, v51\n"
"v_mfma_f32_16x16x32_bf16 v[132:135], a[116:119], v[8:11], 0\n"
"v_exp_f32 v36, v36\n"
"v_mfma_f32_16x16x32_bf16 v[164:167], a[116:119], v[16:19], 0\n"
"v_exp_f32 v37, v37\n"
"v_mfma_f32_16x16x32_bf16 v[196:199], a[116:119], v[24:27], 0\n"
"s_waitcnt lgkmcnt(1)\n"
"v_exp_f32 v38, v38\n"
"v_cvt_pk_bf16_f32 v66, v36, v37\n"
"v_mfma_f32_16x16x32_bf16 v[104:107], a[120:123], v[0:3], 0\n"
"v_exp_f32 v39, v39\n"
"v_mfma_f32_16x16x32_bf16 v[136:139], a[120:123], v[8:11], 0\n"
"s_waitcnt vmcnt(14)\n"
"s_barrier\n"
"v_mfma_f32_16x16x32_bf16 v[168:171], a[120:123], v[16:19], 0\n"
"buffer_load_dwordx4 v225, s[8:11], s44 offen offset:2048 sc1 lds\n"
"v_cvt_pk_bf16_f32 v67, v38, v39\n"
"v_mfma_f32_16x16x32_bf16 v[200:203], a[120:123], v[24:27], 0\n"
"s_waitcnt lgkmcnt(0)\n"
"ds_read_b128 a[64:67], v226 offset:0\n"
"v_mfma_f32_16x16x32_bf16 v[108:111], a[124:127], v[0:3], 0\n"
"buffer_store_dwordx2 v[232:233], v230, s[8:11], s48 offen sc1\n"
"v_mfma_f32_16x16x32_bf16 v[140:143], a[124:127], v[8:11], 0\n"
"v_exp_f32 v44, v44\n"
"v_mfma_f32_16x16x32_bf16 v[172:175], a[124:127], v[16:19], 0\n"
"v_exp_f32 v45, v45\n"
"v_mfma_f32_16x16x32_bf16 v[204:207], a[124:127], v[24:27], 0\n"
"v_exp_f32 v46, v46\n"
"v_cvt_pk_bf16_f32 v70, v44, v45\n"
"v_mfma_f32_16x16x32_bf16 a[132:135], a[128:131], v[0:3], 0\n"
"ds_read_b128 a[68:71], v226 offset:1024\n"
"ds_read_b128 a[72:75], v226 offset:2048\n"
"v_mfma_f32_16x16x32_bf16 a[136:139], a[128:131], v[8:11], 0\n"
"v_exp_f32 v47, v47\n"
"v_mfma_f32_16x16x32_bf16 a[140:143], a[128:131], v[16:19], 0\n"
"v_exp_f32 v52, v52\n"
"v_cvt_pk_bf16_f32 v71, v46, v47\n"
"v_mfma_f32_16x16x32_bf16 a[144:147], a[128:131], v[24:27], 0\n"
"v_exp_f32 v53, v53\n"
"buffer_load_dwordx4 v225, s[8:11], s44 offen offset:3072 sc1 lds\n"
"v_cvt_pk_bf16_f32 v74, v52, v53\n"
"v_exp_f32 v54, v54\n"
"v_exp_f32 v55, v55\n"
"s_nop 0\n"
"v_cvt_pk_bf16_f32 v75, v54, v55\n"
"ds_read_b128 a[76:79], v226 offset:3072\n"
"ds_read_b128 a[80:83], v226 offset:4096\n"
"ds_read_b128 a[84:87], v226 offset:5120\n"
".p2align 6\n"
"LP1_%=:\n"
"s_waitcnt lgkmcnt(5)\n"
"v_mfma_f32_16x16x32_bf16 v[0:3], a[64:67], a[0:3], 0\n"
"v_exp_f32 v56, v56\n"
"v_mfma_f32_16x16x32_bf16 v[8:11], a[64:67], a[16:19], 0\n"
"v_readlane_b32 s43, v228, s42\n"
"v_mfma_f32_16x16x32_bf16 v[16:19], a[64:67], a[32:35], 0\n"
"v_exp_f32 v57, v57\n"
"v_mfma_f32_16x16x32_bf16 v[24:27], a[64:67], a[48:51], 0\n"
"s_waitcnt lgkmcnt(4)\n"
"v_cvt_pk_bf16_f32 v76, v56, v57\n"
"s_mov_b32 m0, s40\n"
"v_mfma_f32_16x16x32_bf16 v[0:3], a[68:71], a[4:7], v[0:3]\n"
"v_exp_f32 v58, v58\n"
"v_mfma_f32_16x16x32_bf16 v[8:11], a[68:71], a[20:23], v[8:11]\n"
"v_readlane_b32 s44, v229, s42\n"
"v_mov_b32 v227, v226\n"
"v_mfma_f32_16x16x32_bf16 v[16:19], a[68:71], a[36:39], v[16:19]\n"
"v_exp_f32 v59, v59\n"
"v_mfma_f32_16x16x32_bf16 v[24:27], a[68:71], a[52:55], v[24:27]\n"
"s_waitcnt lgkmcnt(0)\n"
"buffer_load_dwordx4 v225, s[8:11], s43 offen offset:0 sc1 lds\n"
"v_mfma_f32_16x16x32_bf16 v[0:3], a[72:75], a[8:11], v[0:3]\n"
"s_add_u32 s42, s42, 1\n"
"v_exp_f32 v60, v60\n"
"v_mfma_f32_16x16x32_bf16 v[8:11], a[72:75], a[24:27], v[8:11]\n"
"ds_read_b128 a[88:91], v226 offset:6144\n"
"v_cvt_pk_bf16_f32 v77, v58, v59\n"
"v_mfma_f32_16x16x32_bf16 v[16:19], a[72:75], a[40:43], v[16:19]\n"
"v_exp_f32 v61, v61\n"
"v_mfma_f32_16x16x32_bf16 v[24:27], a[72:75], a[56:59], v[24:27]\n"
"v_cvt_pk_bf16_f32 v78, v60, v61\n"
"v_mfma_f32_16x16x32_bf16 v[0:3], a[76:79], a[12:15], v[0:3]\n"
"v_exp_f32 v62, v62\n"
"v_mfma_f32_16x16x32_bf16 v[8:11], a[76:79], a[28:31], v[8:11]\n"
"ds_read_b128 a[92:95], v226 offset:7168\n"
"v_mfma_f32_16x16x32_bf16 v[16:19], a[76:79], a[44:47], v[16:19]\n"
"v_exp_f32 v63, v63\n"
"v_mfma_f32_16x16x32_bf16 v[24:27], a[76:79], a[60:63], v[24:27]\n"
"buffer_load_dwordx4 v225, s[8:11], s43 offen offset:1024 sc1 lds\n"
"v_mfma_f32_16x16x32_bf16 v[4:7], a[80:83], a[0:3], 0\n"
"v_exp_f32 v0, v0\n"
"v_mfma_f32_16x16x32_bf16 v[12:15], a[80:83], a[16:19], 0\n"
"ds_read_b128 a[96:99], v226 offset:8192\n"
"v_cvt_pk_bf16_f32 v79, v62, v63\n"
"v_mfma_f32_16x16x32_bf16 v[20:23], a[80:83], a[32:35], 0\n"
"v_exp_f32 v1, v1\n"
"v_mfma_f32_16x16x32_bf16 v[28:31], a[80:83], a[48:51], 0\n"
"v_cvt_pk_bf16_f32 v0, v0, v1\n"
"v_mfma_f32_16x16x32_bf16 v[4:7], a[84:87], a[4:7], v[4:7]\n"
"v_exp_f32 v2, v2\n"
"v_mfma_f32_16x16x32_bf16 v[12:15], a[84:87], a[20:23], v[12:15]\n"
"ds_read_b128 a[100:103], v226 offset:9216\n"
"v_mfma_f32_16x16x32_bf16 v[20:23], a[84:87], a[36:39], v[20:23]\n"
"v_exp_f32 v3, v3\n"
"v_mfma_f32_16x16x32_bf16 v[28:31], a[84:87], a[52:55], v[28:31]\n"
"s_waitcnt lgkmcnt(2)\n"
"buffer_load_dwordx4 v225, s[8:11], s43 offen offset:2048 sc1 lds\n"
"v_mfma_f32_16x16x32_bf16 v[4:7], a[88:91], a[8:11], v[4:7]\n"
"v_exp_f32 v8, v8\n"
"v_mfma_f32_16x16x32_bf16 v[12:15], a[88:91], a[24:27], v[12:15]\n"
"ds_read_b128 a[104:107], v226 offset:10240\n"
"v_cvt_pk_bf16_f32 v1, v2, v3\n"
"v_mfma_f32_16x16x32_bf16 v[20:23], a[88:91], a[40:43], v[20:23]\n"
"v_exp_f32 v9, v9\n"
"v_mfma_f32_16x16x32_bf16 v[28:31], a[88:91], a[56:59], v[28:31]\n"
"v_cvt_pk_bf16_f32 v8, v8, v9\n"
"v_mfma_f32_16x16x32_bf16 v[4:7], a[92:95], a[12:15], v[4:7]\n"
"v_exp_f32 v10, v10\n"
"v_mfma_f32_16x16x32_bf16 v[12:15], a[92:95], a[28:31], v[12:15]\n"
"ds_read_b128 a[108:111], v226 offset:11264\n"
"v_mfma_f32_16x16x32_bf16 v[20:23], a[92:95], a[44:47], v[20:23]\n"
"v_exp_f32 v11, v11\n"
"v_mfma_f32_16x16x32_bf16 v[28:31], a[92:95], a[60:63], v[28:31]\n"
"s_waitcnt lgkmcnt(2)\n"
"buffer_load_dwordx4 v225, s[8:11], s43 offen offset:3072 sc1 lds\n"
"v_mfma_f32_16x16x32_bf16 v[32:35], a[96:99], a[0:3], 0\n"
"v_exp_f32 v16, v16\n"
"v_mfma_f32_16x16x32_bf16 v[40:43], a[96:99], a[16:19], 0\n"
"ds_read_b128 a[112:115], v226 offset:12288\n"
"v_cvt_pk_bf16_f32 v9, v10, v11\n"
"v_mfma_f32_16x16x32_bf16 v[48:51], a[96:99], a[32:35], 0\n"
"v_exp_f32 v17, v17\n"
"v_mfma_f32_16x16x32_bf16 v[56:59], a[96:99], a[48:51], 0\n"
"s_add_u32 m0, s40, 16384\n"
"v_cvt_pk_bf16_f32 v16, v16, v17\n"
"v_mfma_f32_16x16x32_bf16 v[32:35], a[100:103], a[4:7], v[32:35]\n"
"v_exp_f32 v18, v18\n"
"v_mfma_f32_16x16x32_bf16 v[40:43], a[100:103], a[20:23], v[40:43]\n"
"ds_read_b128 a[116:119], v226 offset:13312\n"
"v_mfma_f32_16x16x32_bf16 v[48:51], a[100:103], a[36:39], v[48:51]\n"
"v_exp_f32 v19, v19\n"
"v_mfma_f32_16x16x32_bf16 v[56:59], a[100:103], a[52:55], v[56:59]\n"
"s_waitcnt lgkmcnt(2)\n"
"buffer_load_dwordx4 v225, s[8:11], s44 offen offset:0 sc1 lds\n"
"v_mfma_f32_16x16x32_bf16 v[32:35], a[104:107], a[8:11], v[32:35]\n"
"s_add_u32 s40, s40, 32768\n"
"v_exp_f32 v24, v24\n"
"v_mfma_f32_16x16x32_bf16 v[40:43], a[104:107], a[24:27], v[40:43]\n"
"s_cmp_eq_u32 s40, s46\n"
"ds_read_b128 a[120:123], v226 offset:14336\n"
"v_cvt_pk_bf16_f32 v17, v18, v19\n"
"v_mfma_f32_16x16x32_bf16 v[48:51], a[104:107], a[40:43], v[48:51]\n"
"s_cselect_b32 s40, s47, s40\n"
"v_exp_f32 v25, v25\n"
"v_mfma_f32_16x16x32_bf16 v[56:59], a[104:107], a[56:59], v[56:59]\n"
"v_cvt_pk_bf16_f32 v24, v24, v25\n"
"v_mfma_f32_16x16x32_bf16 v[32:35], a[108:111], a[12:15], v[32:35]\n"
"v_exp_f32 v26, v26\n"
"v_mfma_f32_16x16x32_bf16 v[40:43], a[108:111], a[28:31], v[40:43]\n"
"ds_read_b128 a[124:127], v226 offset:15360\n"
"v_mfma_f32_16x16x32_bf16 v[48:51], a[108:111], a[44:47], v[48:51]\n"
"v_exp_f32 v27, v27\n"
"v_mfma_f32_16x16x32_bf16 v[56:59], a[108:111], a[60:63], v[56:59]\n"
"s_waitcnt lgkmcnt(2)\n"
"buffer_load_dwordx4 v225, s[8:11], s44 offen offset:1024 sc1 lds\n"
"v_mfma_f32_16x16x32_bf16 v[36:39], a[112:115], a[0:3], 0\n"
"v_exp_f32 v4, v4\n"
"v_mfma_f32_16x16x32_bf16 v[44:47], a[112:115], a[16:19], 0\n"
"ds_read_b128 a[64:67], v227 offset:16384\n"
"v_cvt_pk_bf16_f32 v25, v26, v27\n"
"v_mfma_f32_16x16x32_bf16 v[52:55], a[112:115], a[32:35], 0\n"
"v_exp_f32 v5, v5\n"
"v_mfma_f32_16x16x32_bf16 v[60:63], a[112:115], a[48:51], 0\n"
"v_cvt_pk_bf16_f32 v2, v4, v5\n"
"v_mfma_f32_16x16x32_bf16 v[36:39], a[116:119], a[4:7], v[36:39]\n"
"v_exp_f32 v6, v6\n"
"v_mfma_f32_16x16x32_bf16 v[44:47], a[116:119], a[20:23], v[44:47]\n"
"ds_read_b128 a[68:71], v227 offset:17408\n"
"v_mfma_f32_16x16x32_bf16 v[52:55], a[116:119], a[36:39], v[52:55]\n"
"v_exp_f32 v7, v7\n"
"v_mfma_f32_16x16x32_bf16 v[60:63], a[116:119], a[52:55], v[60:63]\n"
"s_waitcnt lgkmcnt(2)\n"
"buffer_load_dwordx4 v225, s[8:11], s44 offen offset:2048 sc1 lds\n"
"v_mfma_f32_16x16x32_bf16 v[36:39], a[120:123], a[8:11], v[36:39]\n"
"v_exp_f32 v12, v12\n"
"v_mfma_f32_16x16x32_bf16 v[44:47], a[120:123], a[24:27], v[44:47]\n"
"ds_read_b128 a[72:75], v227 offset:18432\n"
"v_cvt_pk_bf16_f32 v3, v6, v7\n"
"v_mfma_f32_16x16x32_bf16 v[52:55], a[120:123], a[40:43], v[52:55]\n"
"v_exp_f32 v13, v13\n"
"v_mfma_f32_16x16x32_bf16 v[60:63], a[120:123], a[56:59], v[60:63]\n"
"v_cvt_pk_bf16_f32 v10, v12, v13\n"
"v_mfma_f32_16x16x32_bf16 v[36:39], a[124:127], a[12:15], v[36:39]\n"
"v_exp_f32 v14, v14\n"
"v_mfma_f32_16x16x32_bf16 v[44:47], a[124:127], a[28:31], v[44:47]\n"
"ds_read_b128 a[76:79], v227 offset:19456\n"
"v_mfma_f32_16x16x32_bf16 v[52:55], a[124:127], a[44:47], v[52:55]\n"
"v_exp_f32 v15, v15\n"
"v_mfma_f32_16x16x32_bf16 v[60:63], a[124:127], a[60:63], v[60:63]\n"
"s_waitcnt lgkmcnt(2)\n"
"buffer_load_dwordx4 v225, s[8:11], s44 offen offset:3072 sc1 lds\n"
"v_mfma_f32_16x16x32_bf16 v[80:83], a[64:67], v[64:67], v[80:83]\n"
"s_add_u32 s41, s41, 32768\n"
"v_exp_f32 v20, v20\n"
"v_mfma_f32_16x16x32_bf16 v[112:115], a[64:67], v[68:71], v[112:115]\n"
"s_cmp_eq_u32 s41, 163840\n"
"ds_read_b128 a[80:83], v227 offset:20480\n"
"v_cvt_pk_bf16_f32 v11, v14, v15\n"
"v_mfma_f32_16x16x32_bf16 v[144:147], a[64:67], v[72:75], v[144:147]\n"
"s_cselect_b32 s41, 0, s41\n"
"v_exp_f32 v21, v21\n"
"v_mfma_f32_16x16x32_bf16 v[176:179], a[64:67], v[76:79], v[176:179]\n"
"v_add_u32 v226, s41, v224\n"
"v_cvt_pk_bf16_f32 v18, v20, v21\n"
"v_mfma_f32_16x16x32_bf16 v[84:87], a[68:71], v[64:67], v[84:87]\n"
"v_exp_f32 v22, v22\n"
"v_mfma_f32_16x16x32_bf16 v[116:119], a[68:71], v[68:71], v[116:119]\n"
"ds_read_b128 a[84:87], v227 offset:21504\n"
"v_mfma_f32_16x16x32_bf16 v[148:151], a[68:71], v[72:75], v[148:151]\n"
"v_exp_f32 v23, v23\n"
"v_mfma_f32_16x16x32_bf16 v[180:183], a[68:71], v[76:79], v[180:183]\n"
"s_waitcnt lgkmcnt(2)\n"
"v_cvt_pk_bf16_f32 v19, v22, v23\n"
"v_mfma_f32_16x16x32_bf16 v[88:91], a[72:75], v[64:67], v[88:91]\n"
"v_exp_f32 v28, v28\n"
"v_mfma_f32_16x16x32_bf16 v[120:123], a[72:75], v[68:71], v[120:123]\n"
"ds_read_b128 a[88:91], v227 offset:22528\n"
"v_mfma_f32_16x16x32_bf16 v[152:155], a[72:75], v[72:75], v[152:155]\n"
"v_exp_f32 v29, v29\n"
"v_mfma_f32_16x16x32_bf16 v[184:187], a[72:75], v[76:79], v[184:187]\n"
"v_cvt_pk_bf16_f32 v26, v28, v29\n"
"v_mfma_f32_16x16x32_bf16 v[92:95], a[76:79], v[64:67], v[92:95]\n"
"v_exp_f32 v30, v30\n"
"v_mfma_f32_16x16x32_bf16 v[124:127], a[76:79], v[68:71], v[124:127]\n"
"ds_read_b128 a[92:95], v227 offset:23552\n"
"v_mfma_f32_16x16x32_bf16 v[156:159], a[76:79], v[72:75], v[156:159]\n"
"v_exp_f32 v31, v31\n"
"v_mfma_f32_16x16x32_bf16 v[188:191], a[76:79], v[76:79], v[188:191]\n"
"s_waitcnt lgkmcnt(2)\n"
"v_cvt_pk_bf16_f32 v27, v30, v31\n"
"v_mfma_f32_16x16x32_bf16 v[96:99], a[80:83], v[64:67], v[96:99]\n"
"v_exp_f32 v32, v32\n"
"v_mfma_f32_16x16x32_bf16 v[128:131], a[80:83], v[68:71], v[128:131]\n"
"v_mfma_f32_16x16x32_bf16 v[160:163], a[80:83], v[72:75], v[160:163]\n"
"v_exp_f32 v33, v33\n"
"v_mfma_f32_16x16x32_bf16 v[192:195], a[80:83], v[76:79], v[192:195]\n"
"v_mfma_f32_16x16x32_bf16 v[100:103], a[84:87], v[64:67], v[100:103]\n"
"v_exp_f32 v34, v34\n"
"v_mfma_f32_16x16x32_bf16 v[132:135], a[84:87], v[68:71], v[132:135]\n"
"ds_read_b128 a[96:99], v227 offset:24576\n"
"v_mfma_f32_16x16x32_bf16 v[164:167], a[84:87], v[72:75], v[164:167]\n"
"v_exp_f32 v35, v35\n"
"v_mfma_f32_16x16x32_bf16 v[196:199], a[84:87], v[76:79], v[196:199]\n"
"s_waitcnt lgkmcnt(1)\n"
"v_mfma_f32_16x16x32_bf16 v[104:107], a[88:91], v[64:67], v[104:107]\n"
"v_exp_f32 v40, v40\n"
"v_mfma_f32_16x16x32_bf16 v[136:139], a[88:91], v[68:71], v[136:139]\n"
"ds_read_b128 a[100:103], v227 offset:25600\n"
"v_mfma_f32_16x16x32_bf16 v[168:171], a[88:91], v[72:75], v[168:171]\n"
"v_exp_f32 v41, v41\n"
"v_mfma_f32_16x16x32_bf16 v[200:203], a[88:91], v[76:79], v[200:203]\n"
"v_mfma_f32_16x16x32_bf16 v[108:111], a[92:95], v[64:67], v[108:111]\n"
"v_exp_f32 v42, v42\n"
"v_mfma_f32_16x16x32_bf16 v[140:143], a[92:95], v[68:71], v[140:143]\n"
"ds_read_b128 a[104:107], v227 offset:26624\n"
"v_mfma_f32_16x16x32_bf16 v[172:175], a[92:95], v[72:75], v[172:175]\n"
"v_exp_f32 v43, v43\n"
"v_mfma_f32_16x16x32_bf16 v[204:207], a[92:95], v[76:79], v[204:207]\n"
"v_mfma_f32_16x16x32_bf16 a[132:135], a[128:131], v[64:67], a[132:135]\n"
"v_exp_f32 v48, v48\n"
"v_mfma_f32_16x16x32_bf16 a[136:139], a[128:131], v[68:71], a[136:139]\n"
"ds_read_b128 a[108:111], v227 offset:27648\n"
"v_cvt_pk_bf16_f32 v64, v32, v33\n"
"v_mfma_f32_16x16x32_bf16 a[140:143], a[128:131], v[72:75], a[140:143]\n"
"v_exp_f32 v49, v49\n"
"v_mfma_f32_16x16x32_bf16 a[144:147], a[128:131], v[76:79], a[144:147]\n"
"s_waitcnt lgkmcnt(2)\n"
"v_cvt_pk_bf16_f32 v65, v34, v35\n"
"v_cvt_pk_bf16_f32 v68, v40, v41\n"
"v_mfma_f32_16x16x32_bf16 v[80:83], a[96:99], v[0:3], v[80:83]\n"
"v_exp_f32 v50, v50\n"
"v_mfma_f32_16x16x32_bf16 v[112:115], a[96:99], v[8:11], v[112:115]\n"
"ds_read_b128 a[112:115], v227 offset:28672\n"
"v_cvt_pk_bf16_f32 v69, v42, v43\n"
"v_mfma_f32_16x16x32_bf16 v[144:147], a[96:99], v[16:19], v[144:147]\n"
"v_exp_f32 v51, v51\n"
"v_mfma_f32_16x16x32_bf16 v[176:179], a[96:99], v[24:27], v[176:179]\n"
"v_cvt_pk_bf16_f32 v72, v48, v49\n"
"v_cvt_pk_bf16_f32 v73, v50, v51\n"
"v_mfma_f32_16x16x32_bf16 v[84:87], a[100:103], v[0:3], v[84:87]\n"
"v_exp_f32 v36, v36\n"
"v_mfma_f32_16x16x32_bf16 v[116:119], a[100:103], v[8:11], v[116:119]\n"
"ds_read_b128 a[116:119], v227 offset:29696\n"
"v_mfma_f32_16x16x32_bf16 v[148:151], a[100:103], v[16:19], v[148:151]\n"
"v_exp_f32 v37, v37\n"
"v_mfma_f32_16x16x32_bf16 v[180:183], a[100:103], v[24:27], v[180:183]\n"
"s_waitcnt lgkmcnt(2)\n"
"v_cvt_pk_bf16_f32 v66, v36, v37\n"
"v_mfma_f32_16x16x32_bf16 v[88:91], a[104:107], v[0:3], v[88:91]\n"
"v_exp_f32 v38, v38\n"
"v_mfma_f32_16x16x32_bf16 v[120:123], a[104:107], v[8:11], v[120:123]\n"
"ds_read_b128 a[120:123], v227 offset:30720\n"
"v_mfma_f32_16x16x32_bf16 v[152:155], a[104:107], v[16:19], v[152:155]\n"
"v_exp_f32 v39, v39\n"
"v_mfma_f32_16x16x32_bf16 v[184:187], a[104:107], v[24:27], v[184:187]\n"
"v_cvt_pk_bf16_f32 v67, v38, v39\n"
"v_mfma_f32_16x16x32_bf16 v[92:95], a[108:111], v[0:3], v[92:95]\n"
"v_exp_f32 v44, v44\n"
"v_mfma_f32_16x16x32_bf16 v[124:127], a[108:111], v[8:11], v[124:127]\n"
"ds_read_b128 a[124:127], v227 offset:31744\n"
"v_mfma_f32_16x16x32_bf16 v[156:159], a[108:111], v[16:19], v[156:159]\n"
"v_exp_f32 v45, v45\n"
"v_mfma_f32_16x16x32_bf16 v[188:191], a[108:111], v[24:27], v[188:191]\n"
"s_waitcnt lgkmcnt(2)\n"
"v_cvt_pk_bf16_f32 v70, v44, v45\n"
"v_mfma_f32_16x16x32_bf16 v[96:99], a[112:115], v[0:3], v[96:99]\n"
"v_exp_f32 v46, v46\n"
"v_mfma_f32_16x16x32_bf16 v[128:131], a[112:115], v[8:11], v[128:131]\n"
"v_mfma_f32_16x16x32_bf16 v[160:163], a[112:115], v[16:19], v[160:163]\n"
"v_exp_f32 v47, v47\n"
"v_mfma_f32_16x16x32_bf16 v[192:195], a[112:115], v[24:27], v[192:195]\n"
"v_cvt_pk_bf16_f32 v71, v46, v47\n"
"v_mfma_f32_16x16x32_bf16 v[100:103], a[116:119], v[0:3], v[100:103]\n"
"s_waitcnt vmcnt(16)\n"
"s_barrier\n"
"v_mfma_f32_16x16x32_bf16 v[132:135], a[116:119], v[8:11], v[132:135]\n"
"ds_read_b128 a[64:67], v226 offset:0\n"
"v_mfma_f32_16x16x32_bf16 v[164:167], a[116:119], v[16:19], v[164:167]\n"
"v_exp_f32 v52, v52\n"
"v_mfma_f32_16x16x32_bf16 v[196:199], a[116:119], v[24:27], v[196:199]\n"
"s_waitcnt lgkmcnt(1)\n"
"ds_read_b128 a[68:71], v226 offset:1024\n"
"v_mfma_f32_16x16x32_bf16 v[104:107], a[120:123], v[0:3], v[104:107]\n"
"v_exp_f32 v53, v53\n"
"v_mfma_f32_16x16x32_bf16 v[136:139], a[120:123], v[8:11], v[136:139]\n"
"ds_read_b128 a[72:75], v226 offset:2048\n"
"v_cvt_pk_bf16_f32 v74, v52, v53\n"
"v_mfma_f32_16x16x32_bf16 v[168:171], a[120:123], v[16:19], v[168:171]\n"
"v_exp_f32 v54, v54\n"
"v_mfma_f32_16x16x32_bf16 v[200:203], a[120:123], v[24:27], v[200:203]\n"
"v_mfma_f32_16x16x32_bf16 v[108:111], a[124:127], v[0:3], v[108:111]\n"
"v_exp_f32 v55, v55\n"
"v_mfma_f32_16x16x32_bf16 v[140:143], a[124:127], v[8:11], v[140:143]\n"
"ds_read_b128 a[76:79], v226 offset:3072\n"
"v_cvt_pk_bf16_f32 v75, v54, v55\n"
"v_mfma_f32_16x16x32_bf16 v[172:175], a[124:127], v[16:19], v[172:175]\n"
"v_mfma_f32_16x16x32_bf16 v[204:207], a[124:127], v[24:27], v[204:207]\n"
"ds_read_b128 a[80:83], v226 offset:4096\n"
"v_mfma_f32_16x16x32_bf16 a[132:135], a[128:131], v[0:3], a[132:135]\n"
"v_mfma_f32_16x16x32_bf16 a[136:139], a[128:131], v[8:11], a[136:139]\n"
"ds_read_b128 a[84:87], v226 offset:5120\n"
"v_mfma_f32_16x16x32_bf16 a[140:143], a[128:131], v[16:19], a[140:143]\n"
"v_mfma_f32_16x16x32_bf16 a[144:147], a[128:131], v[24:27], a[144:147]\n"
"s_sub_u32 s56, s56, 1\n"
"s_cbranch_scc0 LP1_%=\n"
"s_cmp_lg_u32 s57, 0\n"
"s_cbranch_scc1 LEV_%=\n"
"s_waitcnt lgkmcnt(5)\n"
"v_mfma_f32_16x16x32_bf16 v[0:3], a[64:67], a[0:3], v[208:211]\n"
"v_exp_f32 v56, v56\n"
"v_mfma_f32_16x16x32_bf16 v[8:11], a[64:67], a[16:19], 0\n"
"v_readlane_b32 s43, v228, s42\n"
"v_mfma_f32_16x16x32_bf16 v[16:19], a[64:67], a[32:35], 0\n"
"v_exp_f32 v57, v57\n"
"v_mfma_f32_16x16x32_bf16 v[24:27], a[64:67], a[48:51], 0\n"
"s_waitcnt lgkmcnt(4)\n"
"v_cvt_pk_bf16_f32 v76, v56, v57\n"
"s_mov_b32 m0, s40\n"
"v_mfma_f32_16x16x32_bf16 v[0:3], a[68:71], a[4:7], v[0:3]\n"
"v_exp_f32 v58, v58\n"
"v_mfma_f32_16x16x32_bf16 v[8:11], a[68:71], a[20:23], v[8:11]\n"
"v_readlane_b32 s44, v229, s42\n"
"v_mov_b32 v227, v226\n"
"v_mfma_f32_16x16x32_bf16 v[16:19], a[68:71], a[36:39], v[16:19]\n"
"v_exp_f32 v59, v59\n"
"v_mfma_f32_16x16x32_bf16 v[24:27], a[68:71], a[52:55], v[24:27]\n"
"s_waitcnt lgkmcnt(0)\n"
"buffer_load_dwordx4 v225, s[8:11], s43 offen offset:0 sc1 lds\n"
"v_mfma_f32_16x16x32_bf16 v[0:3], a[72:75], a[8:11], v[0:3]\n"
"s_add_u32 s42, s42, 1\n"
"v_exp_f32 v60, v60\n"
"v_mfma_f32_16x16x32_bf16 v[8:11], a[72:75], a[24:27], v[8:11]\n"
"ds_read_b128 a[88:91], v226 offset:6144\n"
"v_cvt_pk_bf16_f32 v77, v58, v59\n"
"v_mfma_f32_16x16x32_bf16 v[16:19], a[72:75], a[40:43], v[16:19]\n"
"v_exp_f32 v61, v61\n"
"v_mfma_f32_16x16x32_bf16 v[24:27], a[72:75], a[56:59], v[24:27]\n"
"v_cvt_pk_bf16_f32 v78, v60, v61\n"
"v_mfma_f32_16x16x32_bf16 v[0:3], a[76:79], a[12:15], v[0:3]\n"
"v_exp_f32 v62, v62\n"
"v_mfma_f32_16x16x32_bf16 v[8:11], a[76:79], a[28:31], v[8:11]\n"
"ds_read_b128 a[92:95], v226 offset:7168\n"
"v_mfma_f32_16x16x32_bf16 v[16:19], a[76:79], a[44:47], v[16:19]\n"
"v_exp_f32 v63, v63\n"
"v_mfma_f32_16x16x32_bf16 v[24:27], a[76:79], a[60:63], v[24:27]\n"
"buffer_load_dwordx4 v225, s[8:11], s43 offen offset:1024 sc1 lds\n"
"v_mfma_f32_16x16x32_bf16 v[4:7], a[80:83], a[0:3], v[212:215]\n"
"v_exp_f32 v0, v0\n"
"v_mfma_f32_16x16x32_bf16 v[12:15], a[80:83], a[16:19], 0\n"
"ds_read_b128 a[96:99], v226 offset:8192\n"
"v_cvt_pk_bf16_f32 v79, v62, v63\n"
"v_mfma_f32_16x16x32_bf16 v[20:23], a[80:83], a[32:35], 0\n"
"v_exp_f32 v1, v1\n"
"v_mfma_f32_16x16x32_bf16 v[28:31], a[80:83], a[48:51], 0\n"
"v_cvt_pk_bf16_f32 v0, v0, v1\n"
"v_mfma_f32_16x16x32_bf16 v[4:7], a[84:87], a[4:7], v[4:7]\n"
"v_exp_f32 v2, v2\n"
"v_mfma_f32_16x16x32_bf16 v[12:15], a[84:87], a[20:23], v[12:15]\n"
"ds_read_b128 a[100:103], v226 offset:9216\n"
"v_mfma_f32_16x16x32_bf16 v[20:23], a[84:87], a[36:39], v[20:23]\n"
"v_exp_f32 v3, v3\n"
"v_mfma_f32_16x16x32_bf16 v[28:31], a[84:87], a[52:55], v[28:31]\n"
"s_waitcnt lgkmcnt(2)\n"
"buffer_load_dwordx4 v225, s[8:11], s43 offen offset:2048 sc1 lds\n"
"v_mfma_f32_16x16x32_bf16 v[4:7], a[88:91], a[8:11], v[4:7]\n"
"v_exp_f32 v8, v8\n"
"v_mfma_f32_16x16x32_bf16 v[12:15], a[88:91], a[24:27], v[12:15]\n"
"ds_read_b128 a[104:107], v226 offset:10240\n"
"v_cvt_pk_bf16_f32 v1, v2, v3\n"
"v_mfma_f32_16x16x32_bf16 v[20:23], a[88:91], a[40:43], v[20:23]\n"
"v_exp_f32 v9, v9\n"
"v_mfma_f32_16x16x32_bf16 v[28:31], a[88:91], a[56:59], v[28:31]\n"
"v_cvt_pk_bf16_f32 v8, v8, v9\n"
"v_mfma_f32_16x16x32_bf16 v[4:7], a[92:95], a[12:15], v[4:7]\n"
"v_exp_f32 v10, v10\n"
"v_mfma_f32_16x16x32_bf16 v[12:15], a[92:95], a[28:31], v[12:15]\n"
"ds_read_b128 a[108:111], v226 offset:11264\n"
"v_mfma_f32_16x16x32_bf16 v[20:23], a[92:95], a[44:47], v[20:23]\n"
"v_exp_f32 v11, v11\n"
"v_mfma_f32_16x16x32_bf16 v[28:31], a[92:95], a[60:63], v[28:31]\n"
"s_waitcnt lgkmcnt(2)\n"
"buffer_load_dwordx4 v225, s[8:11], s43 offen offset:3072 sc1 lds\n"
"v_mfma_f32_16x16x32_bf16 v[32:35], a[96:99], a[0:3], v[216:219]\n"
"v_exp_f32 v16, v16\n"
"v_mfma_f32_16x16x32_bf16 v[40:43], a[96:99], a[16:19], 0\n"
"ds_read_b128 a[112:115], v226 offset:12288\n"
"v_cvt_pk_bf16_f32 v9, v10, v11\n"
"v_mfma_f32_16x16x32_bf16 v[48:51], a[96:99], a[32:35], 0\n"
"v_exp_f32 v17, v17\n"
"v_mfma_f32_16x16x32_bf16 v[56:59], a[96:99], a[48:51], 0\n"
"s_add_u32 m0, s40, 16384\n"
"v_cvt_pk_bf16_f32 v16, v16, v17\n"
"v_mfma_f32_16x16x32_bf16 v[32:35], a[100:103], a[4:7], v[32:35]\n"
"v_exp_f32 v18, v18\n"
"v_mfma_f32_16x16x32_bf16 v[40:43], a[100:103], a[20:23], v[40:43]\n"
"ds_read_b128 a[116:119], v226 offset:13312\n"
"v_mfma_f32_16x16x32_bf16 v[48:51], a[100:103], a[36:39], v[48:51]\n"
"v_exp_f32 v19, v19\n"
"v_mfma_f32_16x16x32_bf16 v[56:59], a[100:103], a[52:55], v[56:59]\n"
"s_waitcnt lgkmcnt(2)\n"
"buffer_load_dwordx4 v225, s[8:11], s44 offen offset:0 sc1 lds\n"
"v_mfma_f32_16x16x32_bf16 v[32:35], a[104:107], a[8:11], v[32:35]\n"
"s_add_u32 s40, s40, 32768\n"
"v_exp_f32 v24, v24\n"
"v_mfma_f32_16x16x32_bf16 v[40:43], a[104:107], a[24:27], v[40:43]\n"
"s_cmp_eq_u32 s40, s46\n"
"ds_read_b128 a[120:123], v226 offset:14336\n"
"v_cvt_pk_bf16_f32 v17, v18, v19\n"
"v_mfma_f32_16x16x32_bf16 v[48:51], a[104:107], a[40:43], v[48:51]\n"
"s_cselect_b32 s40, s47, s40\n"
"v_exp_f32 v25, v25\n"
"v_mfma_f32_16x16x32_bf16 v[56:59], a[104:107], a[56:59], v[56:59]\n"
"v_cvt_pk_bf16_f32 v24, v24, v25\n"
"v_mfma_f32_16x16x32_bf16 v[32:35], a[108:111], a[12:15], v[32:35]\n"
"v_exp_f32 v26, v26\n"
"v_mfma_f32_16x16x32_bf16 v[40:43], a[108:111], a[28:31], v[40:43]\n"
"ds_read_b128 a[124:127], v226 offset:15360\n"
"v_mfma_f32_16x16x32_bf16 v[48:51], a[108:111], a[44:47], v[48:51]\n"
"v_exp_f32 v27, v27\n"
"v_mfma_f32_16x16x32_bf16 v[56:59], a[108:111], a[60:63], v[56:59]\n"
"s_waitcnt lgkmcnt(2)\n"
"buffer_load_dwordx4 v225, s[8:11], s44 offen offset:1024 sc1 lds\n"
"v_mfma_f32_16x16x32_bf16 v[36:39], a[112:115], a[0:3], v[220:223]\n"
"v_exp_f32 v4, v4\n"
"v_mfma_f32_16x16x32_bf16 v[44:47], a[112:115], a[16:19], 0\n"
"ds_read_b128 a[64:67], v227 offset:16384\n"
"v_cvt_pk_bf16_f32 v25, v26, v27\n"
"v_mfma_f32_16x16x32_bf16 v[52:55], a[112:115], a[32:35], 0\n"
"v_exp_f32 v5, v5\n"
"v_mfma_f32_16x16x32_bf16 v[60:63], a[112:115], a[48:51], 0\n"
"v_cvt_pk_bf16_f32 v2, v4, v5\n"
"v_mfma_f32_16x16x32_bf16 v[36:39], a[116:119], a[4:7], v[36:39]\n"
"v_exp_f32 v6, v6\n"
"v_mfma_f32_16x16x32_bf16 v[44:47], a[116:119], a[20:23], v[44:47]\n"
"ds_read_b128 a[68:71], v227 offset:17408\n"
"v_mfma_f32_16x16x32_bf16 v[52:55], a[116:119], a[36:39], v[52:55]\n"
"v_exp_f32 v7, v7\n"
"v_mfma_f32_16x16x32_bf16 v[60:63], a[116:119], a[52:55], v[60:63]\n"
"s_waitcnt lgkmcnt(2)\n"
"buffer_load_dwordx4 v225, s[8:11], s44 offen offset:2048 sc1 lds\n"
"v_mfma_f32_16x16x32_bf16 v[36:39], a[120:123], a[8:11], v[36:39]\n"
"v_exp_f32 v12, v12\n"
"v_mfma_f32_16x16x32_bf16 v[44:47], a[120:123], a[24:27], v[44:47]\n"
"ds_read_b128 a[72:75], v227 offset:18432\n"
"v_cvt_pk_bf16_f32 v3, v6, v7\n"
"v_mfma_f32_16x16x32_bf16 v[52:55], a[120:123], a[40:43], v[52:55]\n"
"v_exp_f32 v13, v13\n"
"v_mfma_f32_16x16x32_bf16 v[60:63], a[120:123], a[56:59], v[60:63]\n"
"v_cvt_pk_bf16_f32 v10, v12, v13\n"
"v_mfma_f32_16x16x32_bf16 v[36:39], a[124:127], a[12:15], v[36:39]\n"
"v_exp_f32 v14, v14\n"
"v_mfma_f32_16x16x32_bf16 v[44:47], a[124:127], a[28:31], v[44:47]\n"
"ds_read_b128 a[76:79], v227 offset:19456\n"
"v_mfma_f32_16x16x32_bf16 v[52:55], a[124:127], a[44:47], v[52:55]\n"
"v_exp_f32 v15, v15\n"
"v_mfma_f32_16x16x32_bf16 v[60:63], a[124:127], a[60:63], v[60:63]\n"
"s_waitcnt lgkmcnt(2)\n"
"buffer_load_dwordx4 v225, s[8:11], s44 offen offset:3072 sc1 lds\n"
"v_mfma_f32_16x16x32_bf16 v[80:83], a[64:67], v[64:67], v[80:83]\n"
"s_add_u32 s41, s41, 32768\n"
"v_exp_f32 v20, v20\n"
"v_mfma_f32_16x16x32_bf16 v[112:115], a[64:67], v[68:71], v[112:115]\n"
"s_cmp_eq_u32 s41, 163840\n"
"ds_read_b128 a[80:83], v227 offset:20480\n"
"v_cvt_pk_bf16_f32 v11, v14, v15\n"
"v_mfma_f32_16x16x32_bf16 v[144:147], a[64:67], v[72:75], v[144:147]\n"
"s_cselect_b32 s41, 0, s41\n"
"v_exp_f32 v21, v21\n"
"v_mfma_f32_16x16x32_bf16 v[176:179], a[64:67], v[76:79], v[176:179]\n"
"v_add_u32 v226, s41, v224\n"
"v_cvt_pk_bf16_f32 v18, v20, v21\n"
"v_mfma_f32_16x16x32_bf16 v[84:87], a[68:71], v[64:67], v[84:87]\n"
"v_exp_f32 v22, v22\n"
"v_mfma_f32_16x16x32_bf16 v[116:119], a[68:71], v[68:71], v[116:119]\n"
"ds_read_b128 a[84:87], v227 offset:21504\n"
"v_mfma_f32_16x16x32_bf16 v[148:151], a[68:71], v[72:75], v[148:151]\n"
"v_exp_f32 v23, v23\n"
"v_mfma_f32_16x16x32_bf16 v[180:183], a[68:71], v[76:79], v[180:183]\n"
"s_waitcnt lgkmcnt(2)\n"
"v_cvt_pk_bf16_f32 v19, v22, v23\n"
"v_mfma_f32_16x16x32_bf16 v[88:91], a[72:75], v[64:67], v[88:91]\n"
"v_exp_f32 v28, v28\n"
"v_mfma_f32_16x16x32_bf16 v[120:123], a[72:75], v[68:71], v[120:123]\n"
"ds_read_b128 a[88:91], v227 offset:22528\n"
"v_mfma_f32_16x16x32_bf16 v[152:155], a[72:75], v[72:75], v[152:155]\n"
"v_exp_f32 v29, v29\n"
"v_mfma_f32_16x16x32_bf16 v[184:187], a[72:75], v[76:79], v[184:187]\n"
"v_cvt_pk_bf16_f32 v26, v28, v29\n"
"v_mfma_f32_16x16x32_bf16 v[92:95], a[76:79], v[64:67], v[92:95]\n"
"v_exp_f32 v30, v30\n"
"v_mfma_f32_16x16x32_bf16 v[124:127], a[76:79], v[68:71], v[124:127]\n"
"ds_read_b128 a[92:95], v227 offset:23552\n"
"v_mfma_f32_16x16x32_bf16 v[156:159], a[76:79], v[72:75], v[156:159]\n"
"v_exp_f32 v31, v31\n"
"v_mfma_f32_16x16x32_bf16 v[188:191], a[76:79], v[76:79], v[188:191]\n"
"s_waitcnt lgkmcnt(2)\n"
"v_cvt_pk_bf16_f32 v27, v30, v31\n"
"v_mfma_f32_16x16x32_bf16 v[96:99], a[80:83], v[64:67], v[96:99]\n"
"v_exp_f32 v32, v32\n"
"v_mfma_f32_16x16x32_bf16 v[128:131], a[80:83], v[68:71], v[128:131]\n"
"v_mfma_f32_16x16x32_bf16 v[160:163], a[80:83], v[72:75], v[160:163]\n"
"v_exp_f32 v33, v33\n"
"v_mfma_f32_16x16x32_bf16 v[192:195], a[80:83], v[76:79], v[192:195]\n"
"v_mfma_f32_16x16x32_bf16 v[100:103], a[84:87], v[64:67], v[100:103]\n"
"v_exp_f32 v34, v34\n"
"v_mfma_f32_16x16x32_bf16 v[132:135], a[84:87], v[68:71], v[132:135]\n"
"ds_read_b128 a[96:99], v227 offset:24576\n"
"v_mfma_f32_16x16x32_bf16 v[164:167], a[84:87], v[72:75], v[164:167]\n"
"v_exp_f32 v35, v35\n"
"v_mfma_f32_16x16x32_bf16 v[196:199], a[84:87], v[76:79], v[196:199]\n"
"s_waitcnt lgkmcnt(1)\n"
"v_mfma_f32_16x16x32_bf16 v[104:107], a[88:91], v[64:67], v[104:107]\n"
"v_exp_f32 v40, v40\n"
"v_mfma_f32_16x16x32_bf16 v[136:139], a[88:91], v[68:71], v[136:139]\n"
"ds_read_b128 a[100:103], v227 offset:25600\n"
"v_mfma_f32_16x16x32_bf16 v[168:171], a[88:91], v[72:75], v[168:171]\n"
"v_exp_f32 v41, v41\n"
"v_mfma_f32_16x16x32_bf16 v[200:203], a[88:91], v[76:79], v[200:203]\n"
"v_mfma_f32_16x16x32_bf16 v[108:111], a[92:95], v[64:67], v[108:111]\n"
"v_exp_f32 v42, v42\n"
"v_mfma_f32_16x16x32_bf16 v[140:143], a[92:95], v[68:71], v[140:143]\n"
"ds_read_b128 a[104:107], v227 offset:26624\n"
"v_mfma_f32_16x16x32_bf16 v[172:175], a[92:95], v[72:75], v[172:175]\n"
"v_exp_f32 v43, v43\n"
"v_mfma_f32_16x16x32_bf16 v[204:207], a[92:95], v[76:79], v[204:207]\n"
"v_mfma_f32_16x16x32_bf16 a[132:135], a[128:131], v[64:67], a[132:135]\n"
"v_exp_f32 v48, v48\n"
"v_mfma_f32_16x16x32_bf16 a[136:139], a[128:131], v[68:71], a[136:139]\n"
"ds_read_b128 a[108:111], v227 offset:27648\n"
"v_cvt_pk_bf16_f32 v64, v32, v33\n"
"v_mfma_f32_16x16x32_bf16 a[140:143], a[128:131], v[72:75], a[140:143]\n"
"v_exp_f32 v49, v49\n"
"v_mfma_f32_16x16x32_bf16 a[144:147], a[128:131], v[76:79], a[144:147]\n"
"s_waitcnt lgkmcnt(2)\n"
"v_cvt_pk_bf16_f32 v65, v34, v35\n"
"v_cvt_pk_bf16_f32 v68, v40, v41\n"
"v_mfma_f32_16x16x32_bf16 v[80:83], a[96:99], v[0:3], v[80:83]\n"
"v_exp_f32 v50, v50\n"
"v_mfma_f32_16x16x32_bf16 v[112:115], a[96:99], v[8:11], v[112:115]\n"
"ds_read_b128 a[112:115], v227 offset:28672\n"
"v_cvt_pk_bf16_f32 v69, v42, v43\n"
"v_mfma_f32_16x16x32_bf16 v[144:147], a[96:99], v[16:19], v[144:147]\n"
"v_exp_f32 v51, v51\n"
"v_mfma_f32_16x16x32_bf16 v[176:179], a[96:99], v[24:27], v[176:179]\n"
"v_cvt_pk_bf16_f32 v72, v48, v49\n"
"v_cvt_pk_bf16_f32 v73, v50, v51\n"
"v_mfma_f32_16x16x32_bf16 v[84:87], a[100:103], v[0:3], v[84:87]\n"
"v_exp_f32 v36, v36\n"
"v_mfma_f32_16x16x32_bf16 v[116:119], a[100:103], v[8:11], v[116:119]\n"
"ds_read_b128 a[116:119], v227 offset:29696\n"
"v_mfma_f32_16x16x32_bf16 v[148:151], a[100:103], v[16:19], v[148:151]\n"
"v_exp_f32 v37, v37\n"
"v_mfma_f32_16x16x32_bf16 v[180:183], a[100:103], v[24:27], v[180:183]\n"
"s_waitcnt lgkmcnt(2)\n"
"v_cvt_pk_bf16_f32 v66, v36, v37\n"
"v_mfma_f32_16x16x32_bf16 v[88:91], a[104:107], v[0:3], v[88:91]\n"
"v_exp_f32 v38, v38\n"
"v_mfma_f32_16x16x32_bf16 v[120:123], a[104:107], v[8:11], v[120:123]\n"
"ds_read_b128 a[120:123], v227 offset:30720\n"
"v_mfma_f32_16x16x32_bf16 v[152:155], a[104:107], v[16:19], v[152:155]\n"
"v_exp_f32 v39, v39\n"
"v_mfma_f32_16x16x32_bf16 v[184:187], a[104:107], v[24:27], v[184:187]\n"
"v_cvt_pk_bf16_f32 v67, v38, v39\n"
"v_mfma_f32_16x16x32_bf16 v[92:95], a[108:111], v[0:3], v[92:95]\n"
"v_exp_f32 v44, v44\n"
"v_mfma_f32_16x16x32_bf16 v[124:127], a[108:111], v[8:11], v[124:127]\n"
"ds_read_b128 a[124:127], v227 offset:31744\n"
"v_mfma_f32_16x16x32_bf16 v[156:159], a[108:111], v[16:19], v[156:159]\n"
"v_exp_f32 v45, v45\n"
"v_mfma_f32_16x16x32_bf16 v[188:191], a[108:111], v[24:27], v[188:191]\n"
"s_waitcnt lgkmcnt(2)\n"
"v_cvt_pk_bf16_f32 v70, v44, v45\n"
"v_mfma_f32_16x16x32_bf16 v[96:99], a[112:115], v[0:3], v[96:99]\n"
"v_exp_f32 v46, v46\n"
"v_mfma_f32_16x16x32_bf16 v[128:131], a[112:115], v[8:11], v[128:131]\n"
"v_mfma_f32_16x16x32_bf16 v[160:163], a[112:115], v[16:19], v[160:163]\n"
"v_exp_f32 v47, v47\n"
"v_mfma_f32_16x16x32_bf16 v[192:195], a[112:115], v[24:27], v[192:195]\n"
"v_cvt_pk_bf16_f32 v71, v46, v47\n"
"v_mfma_f32_16x16x32_bf16 v[100:103], a[116:119], v[0:3], v[100:103]\n"
"s_waitcnt vmcnt(16)\n"
"s_barrier\n"
"v_mfma_f32_16x16x32_bf16 v[132:135], a[116:119], v[8:11], v[132:135]\n"
"ds_read_b128 a[64:67], v226 offset:0\n"
"v_mfma_f32_16x16x32_bf16 v[164:167], a[116:119], v[16:19], v[164:167]\n"
"v_exp_f32 v52, v52\n"
"v_mfma_f32_16x16x32_bf16 v[196:199], a[116:119], v[24:27], v[196:199]\n"
"s_waitcnt lgkmcnt(1)\n"
"ds_read_b128 a[68:71], v226 offset:1024\n"
"v_mfma_f32_16x16x32_bf16 v[104:107], a[120:123], v[0:3], v[104:107]\n"
"v_exp_f32 v53, v53\n"
"v_mfma_f32_16x16x32_bf16 v[136:139], a[120:123], v[8:11], v[136:139]\n"
"ds_read_b128 a[72:75], v226 offset:2048\n"
"v_cvt_pk_bf16_f32 v74, v52, v53\n"
"v_mfma_f32_16x16x32_bf16 v[168:171], a[120:123], v[16:19], v[168:171]\n"
"v_exp_f32 v54, v54\n"
"v_mfma_f32_16x16x32_bf16 v[200:203], a[120:123], v[24:27], v[200:203]\n"
"v_mfma_f32_16x16x32_bf16 v[108:111], a[124:127], v[0:3], v[108:111]\n"
"v_exp_f32 v55, v55\n"
"v_mfma_f32_16x16x32_bf16 v[140:143], a[124:127], v[8:11], v[140:143]\n"
"ds_read_b128 a[76:79], v226 offset:3072\n"
"v_cvt_pk_bf16_f32 v75, v54, v55\n"
"v_mfma_f32_16x16x32_bf16 v[172:175], a[124:127], v[16:19], v[172:175]\n"
"v_mfma_f32_16x16x32_bf16 v[204:207], a[124:127], v[24:27], v[204:207]\n"
"ds_read_b128 a[80:83], v226 offset:4096\n"
"v_mfma_f32_16x16x32_bf16 a[132:135], a[128:131], v[0:3], a[132:135]\n"
"v_mfma_f32_16x16x32_bf16 a[136:139], a[128:131], v[8:11], a[136:139]\n"
"ds_read_b128 a[84:87], v226 offset:5120\n"
"v_mfma_f32_16x16x32_bf16 a[140:143], a[128:131], v[16:19], a[140:143]\n"
"v_mfma_f32_16x16x32_bf16 a[144:147], a[128:131], v[24:27], a[144:147]\n"
"LDB_%=:\n"
"s_waitcnt lgkmcnt(5)\n"
"v_mfma_f32_16x16x32_bf16 v[8:11], a[64:67], a[16:19], v[208:211]\n"
"v_exp_f32 v56, v56\n"
"v_mfma_f32_16x16x32_bf16 v[16:19], a[64:67], a[32:35], 0\n"
"v_readlane_b32 s43, v228, s42\n"
"v_mfma_f32_16x16x32_bf16 v[24:27], a[64:67], a[48:51], 0\n"
"s_waitcnt lgkmcnt(4)\n"
"v_exp_f32 v57, v57\n"
"v_mfma_f32_16x16x32_bf16 v[8:11], a[68:71], a[20:23], v[8:11]\n"
"s_lshl_b32 s50, s35, 6\n"
"ds_read_b128 a[88:91], v226 offset:6144\n"
"v_cvt_pk_bf16_f32 v76, v56, v57\n"
"v_mfma_f32_16x16x32_bf16 v[16:19], a[68:71], a[36:39], v[16:19]\n"
"s_mov_b32 m0, s40\n"
"v_exp_f32 v58, v58\n"
"v_mfma_f32_16x16x32_bf16 v[24:27], a[68:71], a[52:55], v[24:27]\n"
"s_waitcnt lgkmcnt(1)\n"
"ds_read_b128 a[92:95], v226 offset:7168\n"
"v_readlane_b32 s44, v229, s42\n"
"v_mfma_f32_16x16x32_bf16 v[8:11], a[72:75], a[24:27], v[8:11]\n"
"s_add_u32 s50, s50, 4352\n"
"v_exp_f32 v59, v59\n"
"v_mfma_f32_16x16x32_bf16 v[16:19], a[72:75], a[40:43], v[16:19]\n"
"s_add_u32 s42, s42, 1\n"
"buffer_load_dwordx4 v225, s[8:11], s43 offen offset:0 sc1 lds\n"
"v_mov_b32 v227, v226\n"
"v_mfma_f32_16x16x32_bf16 v[24:27], a[72:75], a[56:59], v[24:27]\n"
"v_exp_f32 v60, v60\n"
"v_lshrrev_b32 v235, 1, v224\n"
"v_mfma_f32_16x16x32_bf16 v[8:11], a[76:79], a[28:31], v[8:11]\n"
"ds_read_b128 a[96:99], v226 offset:8192\n"
"v_and_b32 v235, 56, v235\n"
"v_mfma_f32_16x16x32_bf16 v[16:19], a[76:79], a[44:47], v[16:19]\n"
"v_exp_f32 v61, v61\n"
"v_cvt_pk_bf16_f32 v77, v58, v59\n"
"v_mfma_f32_16x16x32_bf16 v[24:27], a[76:79], a[60:63], v[24:27]\n"
"ds_read_b128 a[100:103], v226 offset:9216\n"
"v_cvt_pk_bf16_f32 v78, v60, v61\n"
"v_mfma_f32_16x16x32_bf16 v[12:15], a[80:83], a[16:19], v[212:215]\n"
"v_exp_f32 v62, v62\n"
"v_mfma_f32_16x16x32_bf16 v[20:23], a[80:83], a[32:35], 0\n"
"v_mfma_f32_16x16x32_bf16 v[28:31], a[80:83], a[48:51], 0\n"
"v_exp_f32 v63, v63\n"
"v_mfma_f32_16x16x32_bf16 v[12:15], a[84:87], a[20:23], v[12:15]\n"
"buffer_load_dwordx4 v225, s[8:11], s43 offen offset:1024 sc1 lds\n"
"v_mfma_f32_16x16x32_bf16 v[20:23], a[84:87], a[36:39], v[20:23]\n"
"v_exp_f32 v8, v8\n"
"v_mfma_f32_16x16x32_bf16 v[28:31], a[84:87], a[52:55], v[28:31]\n"
"s_waitcnt lgkmcnt(1)\n"
"ds_read_b128 a[104:107], v226 offset:10240\n"
"v_cvt_pk_bf16_f32 v79, v62, v63\n"
"v_mfma_f32_16x16x32_bf16 v[12:15], a[88:91], a[24:27], v[12:15]\n"
"v_exp_f32 v9, v9\n"
"v_mfma_f32_16x16x32_bf16 v[20:23], a[88:91], a[40:43], v[20:23]\n"
"ds_read_b128 a[108:111], v226 offset:11264\n"
"v_cvt_pk_bf16_f32 v8, v8, v9\n"
"v_mfma_f32_16x16x32_bf16 v[28:31], a[88:91], a[56:59], v[28:31]\n"
"s_lshl_b32 s56, s34, 1\n"
"v_exp_f32 v10, v10\n"
"v_mfma_f32_16x16x32_bf16 v[12:15], a[92:95], a[28:31], v[12:15]\n"
"s_sub_u32 s56, 12, s56\n"
"ds_read_b128 a[112:115], v226 offset:12288\n"
"v_mfma_f32_16x16x32_bf16 v[20:23], a[92:95], a[44:47], v[20:23]\n"
"v_exp_f32 v11, v11\n"
"v_mfma_f32_16x16x32_bf16 v[28:31], a[92:95], a[60:63], v[28:31]\n"
"buffer_load_dwordx4 v225, s[8:11], s43 offen offset:2048 sc1 lds\n"
"v_mfma_f32_16x16x32_bf16 v[40:43], a[96:99], a[16:19], v[216:219]\n"
"v_exp_f32 v16, v16\n"
"v_mfma_f32_16x16x32_bf16 v[48:51], a[96:99], a[32:35], 0\n"
"ds_read_b128 a[116:119], v226 offset:13312\n"
"v_cvt_pk_bf16_f32 v9, v10, v11\n"
"v_mfma_f32_16x16x32_bf16 v[56:59], a[96:99], a[48:51], 0\n"
"s_waitcnt lgkmcnt(2)\n"
"v_exp_f32 v17, v17\n"
"v_mfma_f32_16x16x32_bf16 v[40:43], a[100:103], a[20:23], v[40:43]\n"
"ds_read_b128 a[120:123], v226 offset:14336\n"
"v_cvt_pk_bf16_f32 v16, v16, v17\n"
"v_mfma_f32_16x16x32_bf16 v[48:51], a[100:103], a[36:39], v[48:51]\n"
"v_exp_f32 v18, v18\n"
"v_mfma_f32_16x16x32_bf16 v[56:59], a[100:103], a[52:55], v[56:59]\n"
"ds_read_b128 a[124:127], v226 offset:15360\n"
"v_mfma_f32_16x16x32_bf16 v[40:43], a[104:107], a[24:27], v[40:43]\n"
"v_exp_f32 v19, v19\n"
"v_mfma_f32_16x16x32_bf16 v[48:51], a[104:107], a[40:43], v[48:51]\n"
"buffer_load_dwordx4 v225, s[8:11], s43 offen offset:3072 sc1 lds\n"
"v_mfma_f32_16x16x32_bf16 v[56:59], a[104:107], a[56:59], v[56:59]\n"
"v_exp_f32 v24, v24\n"
"v_mfma_f32_16x16x32_bf16 v[40:43], a[108:111], a[28:31], v[40:43]\n"
"ds_read_b128 a[64:67], v227 offset:16384\n"
"v_cvt_pk_bf16_f32 v17, v18, v19\n"
"v_mfma_f32_16x16x32_bf16 v[48:51], a[108:111], a[44:47], v[48:51]\n"
"v_exp_f32 v25, v25\n"
"v_mfma_f32_16x16x32_bf16 v[56:59], a[108:111], a[60:63], v[56:59]\n"
"s_waitcnt lgkmcnt(2)\n"
"v_cvt_pk_bf16_f32 v24, v24, v25\n"
"s_add_u32 m0, s40, 16384\n"
"v_mfma_f32_16x16x32_bf16 v[44:47], a[112:115], a[16:19], v[220:223]\n"
"v_exp_f32 v26, v26\n"
"v_mfma_f32_16x16x32_bf16 v[52:55], a[112:115], a[32:35], 0\n"
"ds_read_b128 a[68:71], v227 offset:17408\n"
"v_mfma_f32_16x16x32_bf16 v[60:63], a[112:115], a[48:51], 0\n"
"v_exp_f32 v27, v27\n"
"v_mfma_f32_16x16x32_bf16 v[44:47], a[116:119], a[20:23], v[44:47]\n"
"buffer_load_dwordx4 v225, s[8:11], s44 offen offset:0 sc1 lds\n"
"v_mfma_f32_16x16x32_bf16 v[52:55], a[116:119], a[36:39], v[52:55]\n"
"s_add_u32 s40, s40, 32768\n"
"v_exp_f32 v12, v12\n"
"v_mfma_f32_16x16x32_bf16 v[60:63], a[116:119], a[52:55], v[60:63]\n"
"s_cmp_eq_u32 s40, s46\n"
"buffer_load_dwordx2 v[236:237], v235, s[8:11], s50 offen sc1\n"
"v_mfma_f32_16x16x32_bf16 v[44:47], a[120:123], a[24:27], v[44:47]\n"
"s_cselect_b32 s40, s47, s40\n"
"ds_read_b128 a[72:75], v227 offset:18432\n"
"v_cvt_pk_bf16_f32 v25, v26, v27\n"
"v_mfma_f32_16x16x32_bf16 v[52:55], a[120:123], a[40:43], v[52:55]\n"
"v_exp_f32 v13, v13\n"
"v_mfma_f32_16x16x32_bf16 v[60:63], a[120:123], a[56:59], v[60:63]\n"
"s_waitcnt lgkmcnt(1)\n"
"v_exp_f32 v14, v14\n"
"v_mfma_f32_16x16x32_bf16 v[44:47], a[124:127], a[28:31], v[44:47]\n"
"ds_read_b128 a[76:79], v227 offset:19456\n"
"v_cvt_pk_bf16_f32 v10, v12, v13\n"
"v_mfma_f32_16x16x32_bf16 v[52:55], a[124:127], a[44:47], v[52:55]\n"
"v_exp_f32 v15, v15\n"
"v_mfma_f32_16x16x32_bf16 v[60:63], a[124:127], a[60:63], v[60:63]\n"
"s_add_u32 s41, s41, 32768\n"
"buffer_load_dwordx4 v225, s[8:11], s44 offen offset:1024 sc1 lds\n"
"v_mfma_f32_16x16x32_bf16 v[80:83], a[64:67], v[64:67], v[80:83]\n"
"s_cmp_eq_u32 s41, 163840\n"
"v_exp_f32 v20, v20\n"
"v_mfma_f32_16x16x32_bf16 v[112:115], a[64:67], v[68:71], v[112:115]\n"
"s_cselect_b32 s41, 0, s41\n"
"ds_read_b128 a[80:83], v227 offset:20480\n"
"v_add_u32 v226, s41, v224\n"
"v_mfma_f32_16x16x32_bf16 v[144:147], a[64:67], v[72:75], v[144:147]\n"
"v_exp_f32 v21, v21\n"
"v_cvt_pk_bf16_f32 v11, v14, v15\n"
"v_mfma_f32_16x16x32_bf16 v[176:179], a[64:67], v[76:79], v[176:179]\n"
"v_cvt_pk_bf16_f32 v18, v20, v21\n"
"v_mfma_f32_16x16x32_bf16 v[84:87], a[68:71], v[64:67], v[84:87]\n"
"v_exp_f32 v22, v22\n"
"v_mfma_f32_16x16x32_bf16 v[116:119], a[68:71], v[68:71], v[116:119]\n"
"ds_read_b128 a[84:87], v227 offset:21504\n"
"v_mfma_f32_16x16x32_bf16 v[148:151], a[68:71], v[72:75], v[148:151]\n"
"v_exp_f32 v23, v23\n"
"v_mfma_f32_16x16x32_bf16 v[180:183], a[68:71], v[76:79], v[180:183]\n"
"s_waitcnt lgkmcnt(2)\n"
"buffer_load_dwordx4 v225, s[8:11], s44 offen offset:2048 sc1 lds\n"
"v_mfma_f32_16x16x32_bf16 v[88:91], a[72:75], v[64:67], v[88:91]\n"
"v_exp_f32 v28, v28\n"
"v_mfma_f32_16x16x32_bf16 v[120:123], a[72:75], v[68:71], v[120:123]\n"
"ds_read_b128 a[88:91], v227 offset:22528\n"
"v_cvt_pk_bf16_f32 v19, v22, v23\n"
"v_mfma_f32_16x16x32_bf16 v[152:155], a[72:75], v[72:75], v[152:155]\n"
"v_exp_f32 v29, v29\n"
"v_mfma_f32_16x16x32_bf16 v[184:187], a[72:75], v[76:79], v[184:187]\n"
"v_cvt_pk_bf16_f32 v26, v28, v29\n"
"v_mfma_f32_16x16x32_bf16 v[92:95], a[76:79], v[64:67], v[92:95]\n"
"v_exp_f32 v30, v30\n"
"v_mfma_f32_16x16x32_bf16 v[124:127], a[76:79], v[68:71], v[124:127]\n"
"ds_read_b128 a[92:95], v227 offset:23552\n"
"v_mfma_f32_16x16x32_bf16 v[156:159], a[76:79], v[72:75], v[156:159]\n"
"v_exp_f32 v31, v31\n"
"v_mfma_f32_16x16x32_bf16 v[188:191], a[76:79], v[76:79], v[188:191]\n"
"s_waitcnt lgkmcnt(2)\n"
"buffer_load_dwordx4 v225, s[8:11], s44 offen offset:3072 sc1 lds\n"
"v_mfma_f32_16x16x32_bf16 v[96:99], a[80:83], v[64:67], v[96:99]\n"
"v_exp_f32 v40, v40\n"
"v_mfma_f32_16x16x32_bf16 v[128:131], a[80:83], v[68:71], v[128:131]\n"
"v_cvt_pk_bf16_f32 v27, v30, v31\n"
"v_mfma_f32_16x16x32_bf16 v[160:163], a[80:83], v[72:75], v[160:163]\n"
"v_exp_f32 v41, v41\n"
"v_mfma_f32_16x16x32_bf16 v[192:195], a[80:83], v[76:79], v[192:195]\n"
"v_mfma_f32_16x16x32_bf16 v[100:103], a[84:87], v[64:67], v[100:103]\n"
"v_exp_f32 v42, v42\n"
"v_mfma_f32_16x16x32_bf16 v[132:135], a[84:87], v[68:71], v[132:135]\n"
"ds_read_b128 a[96:99], v227 offset:24576\n"
"v_mfma_f32_16x16x32_bf16 v[164:167], a[84:87], v[72:75], v[164:167]\n"
"v_exp_f32 v43, v43\n"
"v_mfma_f32_16x16x32_bf16 v[196:199], a[84:87], v[76:79], v[196:199]\n"
"s_waitcnt lgkmcnt(1)\n"
"v_mfma_f32_16x16x32_bf16 v[104:107], a[88:91], v[64:67], v[104:107]\n"
"v_exp_f32 v48, v48\n"
"v_mfma_f32_16x16x32_bf16 v[136:139], a[88:91], v[68:71], v[136:139]\n"
"ds_read_b128 a[100:103], v227 offset:25600\n"
"v_mfma_f32_16x16x32_bf16 v[168:171], a[88:91], v[72:75], v[168:171]\n"
"v_exp_f32 v49, v49\n"
"v_mfma_f32_16x16x32_bf16 v[200:203], a[88:91], v[76:79], v[200:203]\n"
"ds_read_b128 a[104:107], v227 offset:26624\n"
"v_mfma_f32_16x16x32_bf16 v[108:111], a[92:95], v[64:67], v[108:111]\n"
"v_exp_f32 v50, v50\n"
"v_mfma_f32_16x16x32_bf16 v[140:143], a[92:95], v[68:71], v[140:143]\n"
"ds_read_b128 a[108:111], v227 offset:27648\n"
"v_mfma_f32_16x16x32_bf16 v[172:175], a[92:95], v[72:75], v[172:175]\n"
"v_exp_f32 v51, v51\n"
"v_mfma_f32_16x16x32_bf16 v[204:207], a[92:95], v[76:79], v[204:207]\n"
"v_mfma_f32_16x16x32_bf16 a[132:135], a[128:131], v[64:67], a[132:135]\n"
"v_exp_f32 v44, v44\n"
"v_mfma_f32_16x16x32_bf16 a[136:139], a[128:131], v[68:71], a[136:139]\n"
"ds_read_b128 a[112:115], v227 offset:28672\n"
"v_cvt_pk_bf16_f32 v68, v40, v41\n"
"v_cvt_pk_bf16_f32 v69, v42, v43\n"
"v_mfma_f32_16x16x32_bf16 a[140:143], a[128:131], v[72:75], a[140:143]\n"
"v_exp_f32 v45, v45\n"
"v_cvt_pk_bf16_f32 v72, v48, v49\n"
"v_mfma_f32_16x16x32_bf16 a[144:147], a[128:131], v[76:79], a[144:147]\n"
"s_waitcnt lgkmcnt(2)\n"
"ds_read_b128 a[116:119], v227 offset:29696\n"
"v_cvt_pk_bf16_f32 v73, v50, v51\n"
"v_mfma_f32_16x16x32_bf16 v[112:115], a[96:99], v[8:11], v[112:115]\n"
"v_exp_f32 v46, v46\n"
"v_mfma_f32_16x16x32_bf16 v[144:147], a[96:99], v[16:19], v[144:147]\n"
"v_cvt_pk_bf16_f32 v70, v44, v45\n"
"v_mfma_f32_16x16x32_bf16 v[176:179], a[96:99], v[24:27], v[176:179]\n"
"v_exp_f32 v47, v47\n"
"v_mfma_f32_16x16x32_bf16 v[116:119], a[100:103], v[8:11], v[116:119]\n"
"v_accvgpr_read_b32 v251, a132\n"
"v_subrev_u32 v253, 0x2b800000, v251\n"
"v_mfma_f32_16x16x32_bf16 v[148:151], a[100:103], v[16:19], v[148:151]\n"
"v_cmp_le_u32 vcc, 0x34000000, v253\n"
"s_cmp_lg_u64 vcc, 0\n"
"s_cbranch_scc1 L1014_%=\n"
"L1013_%=:\n"
"v_mfma_f32_16x16x32_bf16 v[180:183], a[100:103], v[24:27], v[180:183]\n"
"v_exp_f32 v52, v52\n"
"v_mfma_f32_16x16x32_bf16 v[120:123], a[104:107], v[8:11], v[120:123]\n"
"v_rcp_f32 v251, v251\n"
"v_cvt_pk_bf16_f32 v71, v46, v47\n"
"v_mfma_f32_16x16x32_bf16 v[152:155], a[104:107], v[16:19], v[152:155]\n"
"ds_read_b128 a[120:123], v227 offset:30720\n"
"ds_read_b128 a[124:127], v227 offset:31744\n"
"v_mfma_f32_16x16x32_bf16 v[184:187], a[104:107], v[24:27], v[184:187]\n"
"s_waitcnt lgkmcnt(2)\n"
"v_exp_f32 v53, v53\n"
"v_mul_f32 v80, v251, v80\n"
"v_mfma_f32_16x16x32_bf16 v[124:127], a[108:111], v[8:11], v[124:127]\n"
"v_exp_f32 v54, v54\n"
"v_mul_f32 v81, v251, v81\n"
"v_mfma_f32_16x16x32_bf16 v[156:159], a[108:111], v[16:19], v[156:159]\n"
"v_exp_f32 v55, v55\n"
"v_mul_f32 v82, v251, v82\n"
"v_mfma_f32_16x16x32_bf16 v[188:191], a[108:111], v[24:27], v[188:191]\n"
"v_mul_f32 v83, v251, v83\n"
"v_cvt_pk_bf16_f32 v74, v52, v53\n"
"v_mul_f32 v84, v251, v84\n"
"v_mfma_f32_16x16x32_bf16 v[128:131], a[112:115], v[8:11], v[128:131]\n"
"s_waitcnt vmcnt(17)\n"
"s_barrier\n"
"v_mfma_f32_16x16x32_bf16 v[160:163], a[112:115], v[16:19], v[160:163]\n"
"ds_read_b128 a[64:67], v226 offset:0\n"
"v_mul_f32 v85, v251, v85\n"
"v_mul_f32 v86, v251, v86\n"
"v_mfma_f32_16x16x32_bf16 v[192:195], a[112:115], v[24:27], v[192:195]\n"
"v_mul_f32 v87, v251, v87\n"
"v_cvt_pk_bf16_f32 v80, v80, v81\n"
"v_cvt_pk_bf16_f32 v81, v82, v83\n"
"v_mfma_f32_16x16x32_bf16 v[132:135], a[116:119], v[8:11], v[132:135]\n"
"ds_read_b128 a[68:71], v226 offset:1024\n"
"v_cvt_pk_bf16_f32 v82, v84, v85\n"
"v_cvt_pk_bf16_f32 v83, v86, v87\n"
"v_mfma_f32_16x16x32_bf16 v[164:167], a[116:119], v[16:19], v[164:167]\n"
"buffer_store_dwordx4 v[80:83], v224, s[8:11], s59 offen offset:0 sc1\n"
"v_cvt_pk_bf16_f32 v75, v54, v55\n"
"v_mfma_f32_16x16x32_bf16 v[196:199], a[116:119], v[24:27], v[196:199]\n"
"s_waitcnt lgkmcnt(2)\n"
"s_waitcnt vmcnt(4)\n"
"v_cmp_eq_u64 vcc, s[28:29], v[236:237]\n"
"s_cmp_eq_u64 vcc, exec\n"
"s_cbranch_scc0 L1019_%=\n"
"L1020_%=:\n"
"v_mfma_f32_16x16x32_bf16 v[136:139], a[120:123], v[8:11], v[136:139]\n"
"ds_read_b128 a[72:75], v226 offset:2048\n"
"v_mul_f32 v88, v251, v88\n"
"v_mul_f32 v89, v251, v89\n"
"v_mfma_f32_16x16x32_bf16 v[168:171], a[120:123], v[16:19], v[168:171]\n"
"v_mul_f32 v90, v251, v90\n"
"v_mul_f32 v91, v251, v91\n"
"v_mul_f32 v92, v251, v92\n"
"v_mfma_f32_16x16x32_bf16 v[200:203], a[120:123], v[24:27], v[200:203]\n"
"v_mul_f32 v93, v251, v93\n"
"v_mul_f32 v94, v251, v94\n"
"v_mul_f32 v95, v251, v95\n"
"v_mfma_f32_16x16x32_bf16 v[140:143], a[124:127], v[8:11], v[140:143]\n"
"ds_read_b128 a[76:79], v226 offset:3072\n"
"v_cvt_pk_bf16_f32 v88, v88, v89\n"
"v_cvt_pk_bf16_f32 v89, v90, v91\n"
"v_mfma_f32_16x16x32_bf16 v[172:175], a[124:127], v[16:19], v[172:175]\n"
"v_cvt_pk_bf16_f32 v90, v92, v93\n"
"v_cvt_pk_bf16_f32 v91, v94, v95\n"
"v_mul_f32 v96, v251, v96\n"
"v_mfma_f32_16x16x32_bf16 v[204:207], a[124:127], v[24:27], v[204:207]\n"
"v_mul_f32 v97, v251, v97\n"
"v_mul_f32 v104, v251, v104\n"
"v_mul_f32 v98, v251, v98\n"
"v_mfma_f32_16x16x32_bf16 a[136:139], a[128:131], v[8:11], a[136:139]\n"
"v_mul_f32 v105, v251, v105\n"
"v_mul_f32 v99, v251, v99\n"
"v_cvt_pk_bf16_f32 v96, v96, v97\n"
"v_mfma_f32_16x16x32_bf16 a[140:143], a[128:131], v[16:19], a[140:143]\n"
"v_mul_f32 v106, v251, v106\n"
"v_mul_f32 v107, v251, v107\n"
"v_mul_f32 v100, v251, v100\n"
"v_mfma_f32_16x16x32_bf16 a[144:147], a[128:131], v[24:27], a[144:147]\n"
"v_mul_f32 v101, v251, v101\n"
"v_mul_f32 v102, v251, v102\n"
"v_mul_f32 v103, v251, v103\n"
"buffer_store_dwordx4 v[88:91], v224, s[8:11], s59 offen offset:1024 sc1\n"
"v_cvt_pk_bf16_f32 v97, v98, v99\n"
"v_cvt_pk_bf16_f32 v98, v100, v101\n"
"v_cvt_pk_bf16_f32 v99, v102, v103\n"
"buffer_store_dwordx4 v[96:99], v224, s[8:11], s59 offen offset:2048 sc1\n"
"v_mul_f32 v108, v251, v108\n"
"v_mul_f32 v109, v251, v109\n"
"v_mul_f32 v110, v251, v110\n"
"v_mul_f32 v111, v251, v111\n"
"v_cvt_pk_bf16_f32 v104, v104, v105\n"
"v_cvt_pk_bf16_f32 v105, v106, v107\n"
"v_cvt_pk_bf16_f32 v106, v108, v109\n"
"v_cvt_pk_bf16_f32 v107, v110, v111\n"
"buffer_store_dwordx4 v[104:107], v224, s[8:11], s59 offen offset:3072 sc1\n"
"ds_read_b128 a[80:83], v226 offset:4096\n"
"ds_read_b128 a[84:87], v226 offset:5120\n"
"s_cmp_eq_u32 s34, 7\n"
"s_cbranch_scc1 LD2_%=\n"
"s_waitcnt lgkmcnt(5)\n"
"v_mfma_f32_16x16x32_bf16 v[0:3], a[64:67], a[32:35], 0\n"
"s_mov_b32 m0, s40\n"
"v_exp_f32 v56, v56\n"
"v_mfma_f32_16x16x32_bf16 v[8:11], a[64:67], a[48:51], 0\n"
"s_waitcnt lgkmcnt(4)\n"
"ds_read_b128 a[88:91], v226 offset:6144\n"
"v_readlane_b32 s43, v228, s42\n"
"v_mfma_f32_16x16x32_bf16 v[0:3], a[68:71], a[36:39], v[0:3]\n"
"v_exp_f32 v57, v57\n"
"v_mfma_f32_16x16x32_bf16 v[8:11], a[68:71], a[52:55], v[8:11]\n"
"s_waitcnt lgkmcnt(4)\n"
"ds_read_b128 a[92:95], v226 offset:7168\n"
"ds_read_b128 a[96:99], v226 offset:8192\n"
"v_mfma_f32_16x16x32_bf16 v[0:3], a[72:75], a[40:43], v[0:3]\n"
"s_add_u32 s41, s41, 32768\n"
"buffer_load_dwordx4 v225, s[8:11], s43 offen offset:0 sc1 lds\n"
"v_readlane_b32 s44, v229, s42\n"
"v_mfma_f32_16x16x32_bf16 v[8:11], a[72:75], a[56:59], v[8:11]\n"
"s_waitcnt lgkmcnt(3)\n"
"v_exp_f32 v58, v58\n"
"v_mfma_f32_16x16x32_bf16 v[0:3], a[76:79], a[44:47], v[0:3]\n"
"s_add_u32 s42, s42, 1\n"
"ds_read_b128 a[100:103], v226 offset:9216\n"
"v_cvt_pk_bf16_f32 v76, v56, v57\n"
"v_mfma_f32_16x16x32_bf16 v[8:11], a[76:79], a[60:63], v[8:11]\n"
"s_cmp_eq_u32 s41, 163840\n"
"v_exp_f32 v59, v59\n"
"v_mfma_f32_16x16x32_bf16 v[4:7], a[80:83], a[32:35], 0\n"
"s_cselect_b32 s41, 0, s41\n"
"v_exp_f32 v60, v60\n"
"v_add_u32 v227, s41, v224\n"
"v_mfma_f32_16x16x32_bf16 v[12:15], a[80:83], a[48:51], 0\n"
"ds_read_b128 a[104:107], v226 offset:10240\n"
"v_cvt_pk_bf16_f32 v77, v58, v59\n"
"v_mfma_f32_16x16x32_bf16 v[4:7], a[84:87], a[36:39], v[4:7]\n"
"v_exp_f32 v61, v61\n"
"v_mfma_f32_16x16x32_bf16 v[12:15], a[84:87], a[52:55], v[12:15]\n"
"s_waitcnt lgkmcnt(2)\n"
"buffer_load_dwordx4 v225, s[8:11], s43 offen offset:1024 sc1 lds\n"
"v_mfma_f32_16x16x32_bf16 v[4:7], a[88:91], a[40:43], v[4:7]\n"
"ds_read_b128 a[108:111], v226 offset:11264\n"
"v_cvt_pk_bf16_f32 v78, v60, v61\n"
"v_mfma_f32_16x16x32_bf16 v[12:15], a[88:91], a[56:59], v[12:15]\n"
"ds_read_b128 a[112:115], v226 offset:12288\n"
"v_mfma_f32_16x16x32_bf16 v[4:7], a[92:95], a[44:47], v[4:7]\n"
"v_exp_f32 v62, v62\n"
"v_mfma_f32_16x16x32_bf16 v[12:15], a[92:95], a[60:63], v[12:15]\n"
"ds_read_b128 a[116:119], v226 offset:13312\n"
"v_mfma_f32_16x16x32_bf16 v[16:19], a[96:99], a[32:35], 0\n"
"v_exp_f32 v63, v63\n"
"v_mfma_f32_16x16x32_bf16 v[24:27], a[96:99], a[48:51], 0\n"
"s_waitcnt lgkmcnt(3)\n"
"v_exp_f32 v0, v0\n"
"v_mfma_f32_16x16x32_bf16 v[16:19], a[100:103], a[36:39], v[16:19]\n"
"ds_read_b128 a[120:123], v226 offset:14336\n"
"v_cvt_pk_bf16_f32 v79, v62, v63\n"
"v_mfma_f32_16x16x32_bf16 v[24:27], a[100:103], a[52:55], v[24:27]\n"
"buffer_load_dwordx4 v225, s[8:11], s43 offen offset:2048 sc1 lds\n"
"v_mfma_f32_16x16x32_bf16 v[16:19], a[104:107], a[40:43], v[16:19]\n"
"v_exp_f32 v1, v1\n"
"v_mfma_f32_16x16x32_bf16 v[24:27], a[104:107], a[56:59], v[24:27]\n"
"s_waitcnt lgkmcnt(2)\n"
"ds_read_b128 a[124:127], v226 offset:15360\n"
"ds_read_b128 a[64:67], v226 offset:16384\n"
"v_mfma_f32_16x16x32_bf16 v[16:19], a[108:111], a[44:47], v[16:19]\n"
"v_exp_f32 v2, v2\n"
"v_cvt_pk_bf16_f32 v0, v0, v1\n"
"v_mfma_f32_16x16x32_bf16 v[24:27], a[108:111], a[60:63], v[24:27]\n"
"ds_read_b128 a[68:71], v226 offset:17408\n"
"v_mfma_f32_16x16x32_bf16 v[20:23], a[112:115], a[32:35], 0\n"
"v_exp_f32 v3, v3\n"
"v_mfma_f32_16x16x32_bf16 v[28:31], a[112:115], a[48:51], 0\n"
"s_waitcnt lgkmcnt(3)\n"
"v_exp_f32 v8, v8\n"
"v_mfma_f32_16x16x32_bf16 v[20:23], a[116:119], a[36:39], v[20:23]\n"
"buffer_load_dwordx4 v225, s[8:11], s43 offen offset:3072 sc1 lds\n"
"v_cvt_pk_bf16_f32 v1, v2, v3\n"
"v_mfma_f32_16x16x32_bf16 v[28:31], a[116:119], a[52:55], v[28:31]\n"
"ds_read_b128 a[72:75], v226 offset:18432\n"
"v_mfma_f32_16x16x32_bf16 v[20:23], a[120:123], a[40:43], v[20:23]\n"
"v_exp_f32 v9, v9\n"
"v_mfma_f32_16x16x32_bf16 v[28:31], a[120:123], a[56:59], v[28:31]\n"
"s_waitcnt lgkmcnt(2)\n"
"ds_read_b128 a[76:79], v226 offset:19456\n"
"ds_read_b128 a[80:83], v226 offset:20480\n"
"v_mfma_f32_16x16x32_bf16 v[20:23], a[124:127], a[44:47], v[20:23]\n"
"s_add_u32 m0, s40, 16384\n"
"v_exp_f32 v10, v10\n"
"v_cvt_pk_bf16_f32 v8, v8, v9\n"
"v_mfma_f32_16x16x32_bf16 v[28:31], a[124:127], a[60:63], v[28:31]\n"
"ds_read_b128 a[84:87], v226 offset:21504\n"
"v_mfma_f32_16x16x32_bf16 v[32:35], a[64:67], a[32:35], 0\n"
"v_exp_f32 v11, v11\n"
"v_mfma_f32_16x16x32_bf16 v[40:43], a[64:67], a[48:51], 0\n"
"s_waitcnt lgkmcnt(4)\n"
"v_exp_f32 v4, v4\n"
"v_mfma_f32_16x16x32_bf16 v[32:35], a[68:71], a[36:39], v[32:35]\n"
"s_add_u32 s40, s40, 32768\n"
"buffer_load_dwordx4 v225, s[8:11], s44 offen offset:0 sc1 lds\n"
"v_cvt_pk_bf16_f32 v9, v10, v11\n"
"v_mfma_f32_16x16x32_bf16 v[40:43], a[68:71], a[52:55], v[40:43]\n"
"s_waitcnt lgkmcnt(3)\n"
"ds_read_b128 a[88:91], v226 offset:22528\n"
"s_cmp_eq_u32 s40, s46\n"
"v_mfma_f32_16x16x32_bf16 v[32:35], a[72:75], a[40:43], v[32:35]\n"
"s_cselect_b32 s40, s47, s40\n"
"v_exp_f32 v5, v5\n"
"v_mfma_f32_16x16x32_bf16 v[40:43], a[72:75], a[56:59], v[40:43]\n"
"s_waitcnt lgkmcnt(2)\n"
"ds_read_b128 a[92:95], v226 offset:23552\n"
"ds_read_b128 a[96:99], v226 offset:24576\n"
"v_mfma_f32_16x16x32_bf16 v[32:35], a[76:79], a[44:47], v[32:35]\n"
"v_exp_f32 v6, v6\n"
"v_cvt_pk_bf16_f32 v2, v4, v5\n"
"v_mfma_f32_16x16x32_bf16 v[40:43], a[76:79], a[60:63], v[40:43]\n"
"ds_read_b128 a[100:103], v226 offset:25600\n"
"v_mfma_f32_16x16x32_bf16 v[36:39], a[80:83], a[32:35], 0\n"
"v_exp_f32 v7, v7\n"
"v_mfma_f32_16x16x32_bf16 v[44:47], a[80:83], a[48:51], 0\n"
"s_waitcnt lgkmcnt(4)\n"
"buffer_load_dwordx4 v225, s[8:11], s44 offen offset:1024 sc1 lds\n"
"v_mfma_f32_16x16x32_bf16 v[36:39], a[84:87], a[36:39], v[36:39]\n"
"v_exp_f32 v12, v12\n"
"v_cvt_pk_bf16_f32 v3, v6, v7\n"
"v_mfma_f32_16x16x32_bf16 v[44:47], a[84:87], a[52:55], v[44:47]\n"
"s_waitcnt lgkmcnt(3)\n"
"ds_read_b128 a[104:107], v226 offset:26624\n"
"ds_read_b128 a[108:111], v226 offset:27648\n"
"v_mfma_f32_16x16x32_bf16 v[36:39], a[88:91], a[40:43], v[36:39]\n"
"v_exp_f32 v13, v13\n"
"v_mfma_f32_16x16x32_bf16 v[44:47], a[88:91], a[56:59], v[44:47]\n"
"s_waitcnt lgkmcnt(3)\n"
"ds_read_b128 a[112:115], v226 offset:28672\n"
"ds_read_b128 a[116:119], v226 offset:29696\n"
"v_mfma_f32_16x16x32_bf16 v[36:39], a[92:95], a[44:47], v[36:39]\n"
"v_exp_f32 v14, v14\n"
"v_cvt_pk_bf16_f32 v10, v12, v13\n"
"v_mfma_f32_16x16x32_bf16 v[44:47], a[92:95], a[60:63], v[44:47]\n"
"v_exp_f32 v15, v15\n"
"v_mfma_f32_16x16x32_bf16 v[48:51], a[96:99], a[32:35], 0\n"
"buffer_load_dwordx4 v225, s[8:11], s44 offen offset:2048 sc1 lds\n"
"v_mfma_f32_16x16x32_bf16 v[56:59], a[96:99], a[48:51], 0\n"
"s_waitcnt lgkmcnt(4)\n"
"v_exp_f32 v16, v16\n"
"v_mfma_f32_16x16x32_bf16 v[48:51], a[100:103], a[36:39], v[48:51]\n"
"ds_read_b128 a[120:123], v226 offset:30720\n"
"v_cvt_pk_bf16_f32 v11, v14, v15\n"
"v_mfma_f32_16x16x32_bf16 v[56:59], a[100:103], a[52:55], v[56:59]\n"
"s_waitcnt lgkmcnt(3)\n"
"v_exp_f32 v17, v17\n"
"v_mfma_f32_16x16x32_bf16 v[48:51], a[104:107], a[40:43], v[48:51]\n"
"ds_read_b128 a[124:127], v226 offset:31744\n"
"v_cvt_pk_bf16_f32 v16, v16, v17\n"
"v_mfma_f32_16x16x32_bf16 v[56:59], a[104:107], a[56:59], v[56:59]\n"
"s_waitcnt vmcnt(15)\n"
"s_barrier\n"
"v_mfma_f32_16x16x32_bf16 v[48:51], a[108:111], a[44:47], v[48:51]\n"
"ds_read_b128 a[96:99], v227 offset:8192\n"
"v_mfma_f32_16x16x32_bf16 v[56:59], a[108:111], a[60:63], v[56:59]\n"
"s_waitcnt lgkmcnt(3)\n"
"v_exp_f32 v18, v18\n"
"v_mfma_f32_16x16x32_bf16 v[52:55], a[112:115], a[32:35], 0\n"
"buffer_load_dwordx4 v225, s[8:11], s44 offen offset:3072 sc1 lds\n"
"v_mfma_f32_16x16x32_bf16 v[60:63], a[112:115], a[48:51], 0\n"
"v_exp_f32 v19, v19\n"
"v_mfma_f32_16x16x32_bf16 v[52:55], a[116:119], a[36:39], v[52:55]\n"
"ds_read_b128 a[100:103], v227 offset:9216\n"
"v_readlane_b32 s43, v228, s42\n"
"v_mfma_f32_16x16x32_bf16 v[60:63], a[116:119], a[52:55], v[60:63]\n"
"s_waitcnt lgkmcnt(2)\n"
"v_exp_f32 v24, v24\n"
"v_mfma_f32_16x16x32_bf16 v[52:55], a[120:123], a[40:43], v[52:55]\n"
"s_mov_b32 m0, s40\n"
"ds_read_b128 a[104:107], v227 offset:10240\n"
"ds_read_b128 a[108:111], v227 offset:11264\n"
"v_cvt_pk_bf16_f32 v17, v18, v19\n"
"v_mfma_f32_16x16x32_bf16 v[60:63], a[120:123], a[56:59], v[60:63]\n"
"v_exp_f32 v25, v25\n"
"v_mfma_f32_16x16x32_bf16 v[52:55], a[124:127], a[44:47], v[52:55]\n"
"v_exp_f32 v26, v26\n"
"v_mfma_f32_16x16x32_bf16 v[60:63], a[124:127], a[60:63], v[60:63]\n"
"s_waitcnt lgkmcnt(3)\n"
"buffer_load_dwordx4 v225, s[8:11], s43 offen offset:0 sc1 lds\n"
"v_mfma_f32_16x16x32_bf16 v[112:115], a[96:99], v[68:71], v[112:115]\n"
"s_add_u32 s41, s41, 32768\n"
"v_exp_f32 v27, v27\n"
"v_readlane_b32 s44, v229, s42\n"
"v_mfma_f32_16x16x32_bf16 v[144:147], a[96:99], v[72:75], v[144:147]\n"
"s_cmp_eq_u32 s41, 163840\n"
"ds_read_b128 a[112:115], v227 offset:12288\n"
"v_cvt_pk_bf16_f32 v24, v24, v25\n"
"v_mfma_f32_16x16x32_bf16 v[176:179], a[96:99], v[76:79], v[176:179]\n"
"s_waitcnt lgkmcnt(3)\n"
"v_exp_f32 v20, v20\n"
"v_mfma_f32_16x16x32_bf16 v[116:119], a[100:103], v[68:71], v[116:119]\n"
"s_cselect_b32 s41, 0, s41\n"
"ds_read_b128 a[116:119], v227 offset:13312\n"
"v_add_u32 v226, s41, v224\n"
"v_mfma_f32_16x16x32_bf16 v[148:151], a[100:103], v[72:75], v[148:151]\n"
"s_add_u32 s42, s42, 1\n"
"v_exp_f32 v21, v21\n"
"v_cvt_pk_bf16_f32 v25, v26, v27\n"
"v_mfma_f32_16x16x32_bf16 v[180:183], a[100:103], v[76:79], v[180:183]\n"
"s_waitcnt lgkmcnt(2)\n"
"ds_read_b128 a[120:123], v227 offset:14336\n"
"v_cvt_pk_bf16_f32 v18, v20, v21\n"
"v_mfma_f32_16x16x32_bf16 v[120:123], a[104:107], v[68:71], v[120:123]\n"
"v_exp_f32 v22, v22\n"
"v_mfma_f32_16x16x32_bf16 v[152:155], a[104:107], v[72:75], v[152:155]\n"
"buffer_load_dwordx4 v225, s[8:11], s43 offen offset:1024 sc1 lds\n"
"v_mfma_f32_16x16x32_bf16 v[184:187], a[104:107], v[76:79], v[184:187]\n"
"v_exp_f32 v23, v23\n"
"v_mfma_f32_16x16x32_bf16 v[124:127], a[108:111], v[68:71], v[124:127]\n"
"ds_read_b128 a[124:127], v227 offset:15360\n"
"v_cvt_pk_bf16_f32 v19, v22, v23\n"
"v_mfma_f32_16x16x32_bf16 v[156:159], a[108:111], v[72:75], v[156:159]\n"
"v_exp_f32 v28, v28\n"
"v_mfma_f32_16x16x32_bf16 v[188:191], a[108:111], v[76:79], v[188:191]\n"
"s_waitcnt lgkmcnt(2)\n"
"v_mfma_f32_16x16x32_bf16 v[128:131], a[112:115], v[68:71], v[128:131]\n"
"v_exp_f32 v29, v29\n"
"v_mfma_f32_16x16x32_bf16 v[160:163], a[112:115], v[72:75], v[160:163]\n"
"ds_read_b128 a[64:67], v227 offset:16384\n"
"v_cvt_pk_bf16_f32 v26, v28, v29\n"
"v_mfma_f32_16x16x32_bf16 v[192:195], a[112:115], v[76:79], v[192:195]\n"
"v_exp_f32 v30, v30\n"
"v_mfma_f32_16x16x32_bf16 v[132:135], a[116:119], v[68:71], v[132:135]\n"
"buffer_load_dwordx4 v225, s[8:11], s43 offen offset:2048 sc1 lds\n"
"v_mfma_f32_16x16x32_bf16 v[164:167], a[116:119], v[72:75], v[164:167]\n"
"v_exp_f32 v31, v31\n"
"v_mfma_f32_16x16x32_bf16 v[196:199], a[116:119], v[76:79], v[196:199]\n"
"s_waitcnt lgkmcnt(1)\n"
"ds_read_b128 a[68:71], v227 offset:17408\n"
"v_cvt_pk_bf16_f32 v27, v30, v31\n"
"v_mfma_f32_16x16x32_bf16 v[136:139], a[120:123], v[68:71], v[136:139]\n"
"v_exp_f32 v32, v32\n"
"v_mfma_f32_16x16x32_bf16 v[168:171], a[120:123], v[72:75], v[168:171]\n"
"ds_read_b128 a[72:75], v227 offset:18432\n"
"ds_read_b128 a[76:79], v227 offset:19456\n"
"v_mfma_f32_16x16x32_bf16 v[200:203], a[120:123], v[76:79], v[200:203]\n"
"v_exp_f32 v33, v33\n"
"v_mfma_f32_16x16x32_bf16 v[140:143], a[124:127], v[68:71], v[140:143]\n"
"ds_read_b128 a[80:83], v227 offset:20480\n"
"v_cvt_pk_bf16_f32 v64, v32, v33\n"
"v_mfma_f32_16x16x32_bf16 v[172:175], a[124:127], v[72:75], v[172:175]\n"
"buffer_load_dwordx4 v225, s[8:11], s43 offen offset:3072 sc1 lds\n"
"v_mfma_f32_16x16x32_bf16 v[204:207], a[124:127], v[76:79], v[204:207]\n"
"v_exp_f32 v34, v34\n"
"v_mfma_f32_16x16x32_bf16 a[136:139], a[128:131], v[68:71], a[136:139]\n"
"v_exp_f32 v35, v35\n"
"v_mfma_f32_16x16x32_bf16 a[140:143], a[128:131], v[72:75], a[140:143]\n"
"s_add_u32 m0, s40, 16384\n"
"ds_read_b128 a[84:87], v227 offset:21504\n"
"v_cvt_pk_bf16_f32 v65, v34, v35\n"
"v_mfma_f32_16x16x32_bf16 a[144:147], a[128:131], v[76:79], a[144:147]\n"
"s_waitcnt lgkmcnt(2)\n"
"v_exp_f32 v40, v40\n"
"v_mfma_f32_16x16x32_bf16 v[144:147], a[64:67], v[0:3], v[144:147]\n"
"ds_read_b128 a[88:91], v227 offset:22528\n"
"ds_read_b128 a[92:95], v227 offset:23552\n"
"v_mfma_f32_16x16x32_bf16 v[176:179], a[64:67], v[8:11], v[176:179]\n"
"v_exp_f32 v41, v41\n"
"v_mfma_f32_16x16x32_bf16 v[148:151], a[68:71], v[0:3], v[148:151]\n"
"s_add_u32 s51, s59, 16384\n"
"buffer_load_dwordx4 v225, s[8:11], s44 offen offset:0 sc1 lds\n"
"v_mfma_f32_16x16x32_bf16 v[180:183], a[68:71], v[8:11], v[180:183]\n"
"s_add_u32 s40, s40, 32768\n"
"v_exp_f32 v42, v42\n"
"v_mfma_f32_16x16x32_bf16 v[152:155], a[72:75], v[0:3], v[152:155]\n"
"s_cmp_eq_u32 s40, s46\n"
"v_accvgpr_read_b32 v252, a136\n"
"v_subrev_u32 v253, 0x2b800000, v252\n"
"v_mfma_f32_16x16x32_bf16 v[184:187], a[72:75], v[8:11], v[184:187]\n"
"s_cselect_b32 s40, s47, s40\n"
"v_exp_f32 v43, v43\n"
"v_cvt_pk_bf16_f32 v68, v40, v41\n"
"v_mfma_f32_16x16x32_bf16 v[156:159], a[76:79], v[0:3], v[156:159]\n"
"v_cmp_le_u32 vcc, 0x34000000, v253\n"
"s_cmp_lg_u64 vcc, 0\n"
"s_cbranch_scc1 L1023_%=\n"
"L1022_%=:\n"
"v_mfma_f32_16x16x32_bf16 v[188:191], a[76:79], v[8:11], v[188:191]\n"
"s_waitcnt lgkmcnt(0)\n"
"v_rcp_f32 v252, v252\n"
"v_mfma_f32_16x16x32_bf16 v[160:163], a[80:83], v[0:3], v[160:163]\n"
"ds_read_b128 a[96:99], v227 offset:24576\n"
"ds_read_b128 a[100:103], v227 offset:25600\n"
"v_mul_f32 v112, v252, v112\n"
"v_mfma_f32_16x16x32_bf16 v[192:195], a[80:83], v[8:11], v[192:195]\n"
"v_exp_f32 v36, v36\n"
"v_mul_f32 v113, v252, v113\n"
"v_mfma_f32_16x16x32_bf16 v[164:167], a[84:87], v[0:3], v[164:167]\n"
"ds_read_b128 a[104:107], v227 offset:26624\n"
"ds_read_b128 a[108:111], v227 offset:27648\n"
"v_cvt_pk_bf16_f32 v69, v42, v43\n"
"v_mfma_f32_16x16x32_bf16 v[196:199], a[84:87], v[8:11], v[196:199]\n"
"v_exp_f32 v37, v37\n"
"v_mul_f32 v114, v252, v114\n"
"v_mfma_f32_16x16x32_bf16 v[168:171], a[88:91], v[0:3], v[168:171]\n"
"buffer_load_dwordx4 v225, s[8:11], s44 offen offset:1024 sc1 lds\n"
"v_mul_f32 v115, v252, v115\n"
"v_mfma_f32_16x16x32_bf16 v[200:203], a[88:91], v[8:11], v[200:203]\n"
"v_exp_f32 v38, v38\n"
"v_mul_f32 v116, v252, v116\n"
"v_mfma_f32_16x16x32_bf16 v[172:175], a[92:95], v[0:3], v[172:175]\n"
"v_exp_f32 v39, v39\n"
"v_mul_f32 v117, v252, v117\n"
"v_mfma_f32_16x16x32_bf16 v[204:207], a[92:95], v[8:11], v[204:207]\n"
"ds_read_b128 a[112:115], v227 offset:28672\n"
"v_mul_f32 v118, v252, v118\n"
"v_mul_f32 v119, v252, v119\n"
"v_mfma_f32_16x16x32_bf16 a[140:143], a[128:131], v[0:3], a[140:143]\n"
"v_exp_f32 v44, v44\n"
"v_cvt_pk_bf16_f32 v112, v112, v113\n"
"v_mfma_f32_16x16x32_bf16 a[144:147], a[128:131], v[8:11], a[144:147]\n"
"s_waitcnt lgkmcnt(1)\n"
"ds_read_b128 a[116:119], v227 offset:29696\n"
"ds_read_b128 a[120:123], v227 offset:30720\n"
"v_cvt_pk_bf16_f32 v66, v36, v37\n"
"v_mfma_f32_16x16x32_bf16 v[144:147], a[96:99], v[16:19], v[144:147]\n"
"buffer_load_dwordx4 v225, s[8:11], s44 offen offset:2048 sc1 lds\n"
"v_cvt_pk_bf16_f32 v113, v114, v115\n"
"v_mfma_f32_16x16x32_bf16 v[176:179], a[96:99], v[24:27], v[176:179]\n"
"v_exp_f32 v45, v45\n"
"v_cvt_pk_bf16_f32 v114, v116, v117\n"
"v_mfma_f32_16x16x32_bf16 v[148:151], a[100:103], v[16:19], v[148:151]\n"
"ds_read_b128 a[124:127], v227 offset:31744\n"
"v_cvt_pk_bf16_f32 v115, v118, v119\n"
"v_mul_f32 v120, v252, v120\n"
"v_mfma_f32_16x16x32_bf16 v[180:183], a[100:103], v[24:27], v[180:183]\n"
"buffer_store_dwordx4 v[112:115], v224, s[8:11], s51 offen offset:0 sc1\n"
"v_mul_f32 v121, v252, v121\n"
"v_mfma_f32_16x16x32_bf16 v[152:155], a[104:107], v[16:19], v[152:155]\n"
"v_exp_f32 v46, v46\n"
"v_mul_f32 v122, v252, v122\n"
"v_mfma_f32_16x16x32_bf16 v[184:187], a[104:107], v[24:27], v[184:187]\n"
"s_waitcnt vmcnt(16)\n"
"s_barrier\n"
"v_mfma_f32_16x16x32_bf16 v[156:159], a[108:111], v[16:19], v[156:159]\n"
"ds_read_b128 a[64:67], v226 offset:0\n"
"v_cvt_pk_bf16_f32 v67, v38, v39\n"
"v_mul_f32 v123, v252, v123\n"
"v_mfma_f32_16x16x32_bf16 v[188:191], a[108:111], v[24:27], v[188:191]\n"
"s_waitcnt lgkmcnt(2)\n"
"v_exp_f32 v47, v47\n"
"v_mul_f32 v124, v252, v124\n"
"v_mfma_f32_16x16x32_bf16 v[160:163], a[112:115], v[16:19], v[160:163]\n"
"ds_read_b128 a[68:71], v226 offset:1024\n"
"ds_read_b128 a[72:75], v226 offset:2048\n"
"v_mul_f32 v125, v252, v125\n"
"v_mfma_f32_16x16x32_bf16 v[192:195], a[112:115], v[24:27], v[192:195]\n"
"buffer_load_dwordx4 v225, s[8:11], s44 offen offset:3072 sc1 lds\n"
"v_mul_f32 v126, v252, v126\n"
"v_mfma_f32_16x16x32_bf16 v[164:167], a[116:119], v[16:19], v[164:167]\n"
"v_exp_f32 v48, v48\n"
"v_mul_f32 v127, v252, v127\n"
"v_mfma_f32_16x16x32_bf16 v[196:199], a[116:119], v[24:27], v[196:199]\n"
"v_exp_f32 v49, v49\n"
"v_cvt_pk_bf16_f32 v120, v120, v121\n"
"v_mfma_f32_16x16x32_bf16 v[168:171], a[120:123], v[16:19], v[168:171]\n"
"v_exp_f32 v50, v50\n"
"v_cvt_pk_bf16_f32 v121, v122, v123\n"
"v_mfma_f32_16x16x32_bf16 v[200:203], a[120:123], v[24:27], v[200:203]\n"
"s_waitcnt lgkmcnt(2)\n"
"ds_read_b128 a[76:79], v226 offset:3072\n"
"v_cvt_pk_bf16_f32 v70, v44, v45\n"
"v_cvt_pk_bf16_f32 v122, v124, v125\n"
"v_mfma_f32_16x16x32_bf16 v[172:175], a[124:127], v[16:19], v[172:175]\n"
"v_mul_f32 v128, v252, v128\n"
"v_mul_f32 v129, v252, v129\n"
"v_mul_f32 v136, v252, v136\n"
"v_mfma_f32_16x16x32_bf16 v[204:207], a[124:127], v[24:27], v[204:207]\n"
"v_mul_f32 v137, v252, v137\n"
"v_mul_f32 v130, v252, v130\n"
"v_mul_f32 v131, v252, v131\n"
"v_mfma_f32_16x16x32_bf16 a[140:143], a[128:131], v[16:19], a[140:143]\n"
"v_cvt_pk_bf16_f32 v128, v128, v129\n"
"v_mul_f32 v138, v252, v138\n"
"v_mul_f32 v139, v252, v139\n"
"v_mfma_f32_16x16x32_bf16 a[144:147], a[128:131], v[24:27], a[144:147]\n"
"v_cvt_pk_bf16_f32 v136, v136, v137\n"
"v_mul_f32 v132, v252, v132\n"
"v_mul_f32 v133, v252, v133\n"
"v_cvt_pk_bf16_f32 v71, v46, v47\n"
"v_cvt_pk_bf16_f32 v72, v48, v49\n"
"v_exp_f32 v51, v51\n"
"s_nop 0\n"
"v_cvt_pk_bf16_f32 v73, v50, v51\n"
"v_exp_f32 v52, v52\n"
"v_exp_f32 v53, v53\n"
"s_nop 0\n"
"v_cvt_pk_bf16_f32 v74, v52, v53\n"
"v_exp_f32 v54, v54\n"
"v_exp_f32 v55, v55\n"
"s_nop 0\n"
"v_cvt_pk_bf16_f32 v75, v54, v55\n"
"v_cvt_pk_bf16_f32 v123, v126, v127\n"
"buffer_store_dwordx4 v[120:123], v224, s[8:11], s51 offen offset:1024 sc1\n"
"v_mul_f32 v134, v252, v134\n"
"v_mul_f32 v135, v252, v135\n"
"v_cvt_pk_bf16_f32 v129, v130, v131\n"
"v_cvt_pk_bf16_f32 v130, v132, v133\n"
"v_cvt_pk_bf16_f32 v131, v134, v135\n"
"buffer_store_dwordx4 v[128:131], v224, s[8:11], s51 offen offset:2048 sc1\n"
"v_mul_f32 v140, v252, v140\n"
"v_mul_f32 v141, v252, v141\n"
"v_mul_f32 v142, v252, v142\n"
"v_mul_f32 v143, v252, v143\n"
"v_cvt_pk_bf16_f32 v137, v138, v139\n"
"v_cvt_pk_bf16_f32 v138, v140, v141\n"
"v_cvt_pk_bf16_f32 v139, v142, v143\n"
"buffer_store_dwordx4 v[136:139], v224, s[8:11], s51 offen offset:3072 sc1\n"
"ds_read_b128 a[80:83], v226 offset:4096\n"
"ds_read_b128 a[84:87], v226 offset:5120\n"
".p2align 6\n"
"LP2_%=:\n"
"s_waitcnt lgkmcnt(5)\n"
"v_mfma_f32_16x16x32_bf16 v[0:3], a[64:67], a[32:35], 0\n"
"v_exp_f32 v56, v56\n"
"v_mfma_f32_16x16x32_bf16 v[8:11], a[64:67], a[48:51], 0\n"
"s_waitcnt lgkmcnt(4)\n"
"ds_read_b128 a[88:91], v226 offset:6144\n"
"v_readlane_b32 s43, v228, s42\n"
"v_mfma_f32_16x16x32_bf16 v[0:3], a[68:71], a[36:39], v[0:3]\n"
"v_exp_f32 v57, v57\n"
"v_mfma_f32_16x16x32_bf16 v[8:11], a[68:71], a[52:55], v[8:11]\n"
"s_waitcnt lgkmcnt(4)\n"
"ds_read_b128 a[92:95], v226 offset:7168\n"
"ds_read_b128 a[96:99], v226 offset:8192\n"
"v_mfma_f32_16x16x32_bf16 v[0:3], a[72:75], a[40:43], v[0:3]\n"
"s_mov_b32 m0, s40\n"
"v_exp_f32 v58, v58\n"
"v_mfma_f32_16x16x32_bf16 v[8:11], a[72:75], a[56:59], v[8:11]\n"
"s_waitcnt lgkmcnt(3)\n"
"ds_read_b128 a[100:103], v226 offset:9216\n"
"v_readlane_b32 s44, v229, s42\n"
"v_mfma_f32_16x16x32_bf16 v[0:3], a[76:79], a[44:47], v[0:3]\n"
"s_add_u32 s41, s41, 32768\n"
"v_exp_f32 v59, v59\n"
"v_mfma_f32_16x16x32_bf16 v[8:11], a[76:79], a[60:63], v[8:11]\n"
"s_cmp_eq_u32 s41, 163840\n"
"buffer_load_dwordx4 v225, s[8:11], s43 offen offset:0 sc1 lds\n"
"v_mfma_f32_16x16x32_bf16 v[4:7], a[80:83], a[32:35], 0\n"
"s_cselect_b32 s41, 0, s41\n"
"v_exp_f32 v60, v60\n"
"v_mfma_f32_16x16x32_bf16 v[12:15], a[80:83], a[48:51], 0\n"
"s_add_u32 s42, s42, 1\n"
"ds_read_b128 a[104:107], v226 offset:10240\n"
"v_add_u32 v227, s41, v224\n"
"v_mfma_f32_16x16x32_bf16 v[4:7], a[84:87], a[36:39], v[4:7]\n"
"v_exp_f32 v61, v61\n"
"v_mfma_f32_16x16x32_bf16 v[12:15], a[84:87], a[52:55], v[12:15]\n"
"s_waitcnt lgkmcnt(2)\n"
"ds_read_b128 a[108:111], v226 offset:11264\n"
"ds_read_b128 a[112:115], v226 offset:12288\n"
"v_mfma_f32_16x16x32_bf16 v[4:7], a[88:91], a[40:43], v[4:7]\n"
"v_exp_f32 v62, v62\n"
"v_mfma_f32_16x16x32_bf16 v[12:15], a[88:91], a[56:59], v[12:15]\n"
"ds_read_b128 a[116:119], v226 offset:13312\n"
"v_cvt_pk_bf16_f32 v76, v56, v57\n"
"v_mfma_f32_16x16x32_bf16 v[4:7], a[92:95], a[44:47], v[4:7]\n"
"v_exp_f32 v63, v63\n"
"v_mfma_f32_16x16x32_bf16 v[12:15], a[92:95], a[60:63], v[12:15]\n"
"buffer_load_dwordx4 v225, s[8:11], s43 offen offset:1024 sc1 lds\n"
"v_mfma_f32_16x16x32_bf16 v[16:19], a[96:99], a[32:35], 0\n"
"v_exp_f32 v0, v0\n"
"v_mfma_f32_16x16x32_bf16 v[24:27], a[96:99], a[48:51], 0\n"
"s_waitcnt lgkmcnt(3)\n"
"ds_read_b128 a[120:123], v226 offset:14336\n"
"v_cvt_pk_bf16_f32 v77, v58, v59\n"
"v_mfma_f32_16x16x32_bf16 v[16:19], a[100:103], a[36:39], v[16:19]\n"
"v_exp_f32 v1, v1\n"
"v_mfma_f32_16x16x32_bf16 v[24:27], a[100:103], a[52:55], v[24:27]\n"
"ds_read_b128 a[124:127], v226 offset:15360\n"
"ds_read_b128 a[64:67], v226 offset:16384\n"
"v_mfma_f32_16x16x32_bf16 v[16:19], a[104:107], a[40:43], v[16:19]\n"
"v_exp_f32 v2, v2\n"
"v_mfma_f32_16x16x32_bf16 v[24:27], a[104:107], a[56:59], v[24:27]\n"
"s_waitcnt lgkmcnt(3)\n"
"ds_read_b128 a[68:71], v226 offset:17408\n"
"v_cvt_pk_bf16_f32 v78, v60, v61\n"
"v_mfma_f32_16x16x32_bf16 v[16:19], a[108:111], a[44:47], v[16:19]\n"
"v_exp_f32 v3, v3\n"
"v_mfma_f32_16x16x32_bf16 v[24:27], a[108:111], a[60:63], v[24:27]\n"
"buffer_load_dwordx4 v225, s[8:11], s43 offen offset:2048 sc1 lds\n"
"v_mfma_f32_16x16x32_bf16 v[20:23], a[112:115], a[32:35], 0\n"
"v_exp_f32 v8, v8\n"
"v_mfma_f32_16x16x32_bf16 v[28:31], a[112:115], a[48:51], 0\n"
"ds_read_b128 a[72:75], v226 offset:18432\n"
"v_cvt_pk_bf16_f32 v79, v62, v63\n"
"v_mfma_f32_16x16x32_bf16 v[20:23], a[116:119], a[36:39], v[20:23]\n"
"v_exp_f32 v9, v9\n"
"v_mfma_f32_16x16x32_bf16 v[28:31], a[116:119], a[52:55], v[28:31]\n"
"s_waitcnt lgkmcnt(2)\n"
"ds_read_b128 a[76:79], v226 offset:19456\n"
"ds_read_b128 a[80:83], v226 offset:20480\n"
"v_mfma_f32_16x16x32_bf16 v[20:23], a[120:123], a[40:43], v[20:23]\n"
"v_exp_f32 v10, v10\n"
"v_mfma_f32_16x16x32_bf16 v[28:31], a[120:123], a[56:59], v[28:31]\n"
"ds_read_b128 a[84:87], v226 offset:21504\n"
"v_cvt_pk_bf16_f32 v0, v0, v1\n"
"v_mfma_f32_16x16x32_bf16 v[20:23], a[124:127], a[44:47], v[20:23]\n"
"v_exp_f32 v11, v11\n"
"v_mfma_f32_16x16x32_bf16 v[28:31], a[124:127], a[60:63], v[28:31]\n"
"buffer_load_dwordx4 v225, s[8:11], s43 offen offset:3072 sc1 lds\n"
"v_mfma_f32_16x16x32_bf16 v[32:35], a[64:67], a[32:35], 0\n"
"v_exp_f32 v4, v4\n"
"v_mfma_f32_16x16x32_bf16 v[40:43], a[64:67], a[48:51], 0\n"
"s_waitcnt lgkmcnt(3)\n"
"ds_read_b128 a[88:91], v226 offset:22528\n"
"v_cvt_pk_bf16_f32 v1, v2, v3\n"
"v_mfma_f32_16x16x32_bf16 v[32:35], a[68:71], a[36:39], v[32:35]\n"
"v_exp_f32 v5, v5\n"
"v_mfma_f32_16x16x32_bf16 v[40:43], a[68:71], a[52:55], v[40:43]\n"
"s_add_u32 m0, s40, 16384\n"
"ds_read_b128 a[92:95], v226 offset:23552\n"
"ds_read_b128 a[96:99], v226 offset:24576\n"
"v_mfma_f32_16x16x32_bf16 v[32:35], a[72:75], a[40:43], v[32:35]\n"
"v_exp_f32 v6, v6\n"
"v_mfma_f32_16x16x32_bf16 v[40:43], a[72:75], a[56:59], v[40:43]\n"
"s_waitcnt lgkmcnt(3)\n"
"ds_read_b128 a[100:103], v226 offset:25600\n"
"v_cvt_pk_bf16_f32 v8, v8, v9\n"
"v_mfma_f32_16x16x32_bf16 v[32:35], a[76:79], a[44:47], v[32:35]\n"
"v_exp_f32 v7, v7\n"
"v_mfma_f32_16x16x32_bf16 v[40:43], a[76:79], a[60:63], v[40:43]\n"
"buffer_load_dwordx4 v225, s[8:11], s44 offen offset:0 sc1 lds\n"
"v_mfma_f32_16x16x32_bf16 v[36:39], a[80:83], a[32:35], 0\n"
"s_add_u32 s40, s40, 32768\n"
"v_exp_f32 v12, v12\n"
"v_mfma_f32_16x16x32_bf16 v[44:47], a[80:83], a[48:51], 0\n"
"s_cmp_eq_u32 s40, s46\n"
"ds_read_b128 a[104:107], v226 offset:26624\n"
"v_cvt_pk_bf16_f32 v9, v10, v11\n"
"v_mfma_f32_16x16x32_bf16 v[36:39], a[84:87], a[36:39], v[36:39]\n"
"s_cselect_b32 s40, s47, s40\n"
"v_exp_f32 v13, v13\n"
"v_mfma_f32_16x16x32_bf16 v[44:47], a[84:87], a[52:55], v[44:47]\n"
"s_waitcnt lgkmcnt(2)\n"
"ds_read_b128 a[108:111], v226 offset:27648\n"
"ds_read_b128 a[112:115], v226 offset:28672\n"
"v_mfma_f32_16x16x32_bf16 v[36:39], a[88:91], a[40:43], v[36:39]\n"
"v_exp_f32 v14, v14\n"
"v_mfma_f32_16x16x32_bf16 v[44:47], a[88:91], a[56:59], v[44:47]\n"
"ds_read_b128 a[116:119], v226 offset:29696\n"
"v_cvt_pk_bf16_f32 v2, v4, v5\n"
"v_mfma_f32_16x16x32_bf16 v[36:39], a[92:95], a[44:47], v[36:39]\n"
"v_exp_f32 v15, v15\n"
"v_mfma_f32_16x16x32_bf16 v[44:47], a[92:95], a[60:63], v[44:47]\n"
"buffer_load_dwordx4 v225, s[8:11], s44 offen offset:1024 sc1 lds\n"
"v_mfma_f32_16x16x32_bf16 v[48:51], a[96:99], a[32:35], 0\n"
"v_exp_f32 v16, v16\n"
"v_mfma_f32_16x16x32_bf16 v[56:59], a[96:99], a[48:51], 0\n"
"s_waitcnt lgkmcnt(3)\n"
"ds_read_b128 a[120:123], v226 offset:30720\n"
"v_cvt_pk_bf16_f32 v3, v6, v7\n"
"v_mfma_f32_16x16x32_bf16 v[48:51], a[100:103], a[36:39], v[48:51]\n"
"v_exp_f32 v17, v17\n"
"v_mfma_f32_16x16x32_bf16 v[56:59], a[100:103], a[52:55], v[56:59]\n"
"ds_read_b128 a[124:127], v226 offset:31744\n"
"v_cvt_pk_bf16_f32 v10, v12, v13\n"
"v_mfma_f32_16x16x32_bf16 v[48:51], a[104:107], a[40:43], v[48:51]\n"
"s_waitcnt vmcnt(14)\n"
"s_barrier\n"
"v_mfma_f32_16x16x32_bf16 v[56:59], a[104:107], a[56:59], v[56:59]\n"
"s_waitcnt lgkmcnt(2)\n"
"ds_read_b128 a[64:67], v227 offset:0\n"
"v_cvt_pk_bf16_f32 v11, v14, v15\n"
"v_mfma_f32_16x16x32_bf16 v[48:51], a[108:111], a[44:47], v[48:51]\n"
"v_exp_f32 v18, v18\n"
"v_mfma_f32_16x16x32_bf16 v[56:59], a[108:111], a[60:63], v[56:59]\n"
"buffer_load_dwordx4 v225, s[8:11], s44 offen offset:2048 sc1 lds\n"
"v_mfma_f32_16x16x32_bf16 v[52:55], a[112:115], a[32:35], 0\n"
"v_exp_f32 v19, v19\n"
"v_mfma_f32_16x16x32_bf16 v[60:63], a[112:115], a[48:51], 0\n"
"ds_read_b128 a[68:71], v227 offset:1024\n"
"v_cvt_pk_bf16_f32 v16, v16, v17\n"
"v_mfma_f32_16x16x32_bf16 v[52:55], a[116:119], a[36:39], v[52:55]\n"
"v_exp_f32 v24, v24\n"
"v_mfma_f32_16x16x32_bf16 v[60:63], a[116:119], a[52:55], v[60:63]\n"
"s_waitcnt lgkmcnt(2)\n"
"ds_read_b128 a[72:75], v227 offset:2048\n"
"ds_read_b128 a[76:79], v227 offset:3072\n"
"v_mfma_f32_16x16x32_bf16 v[52:55], a[120:123], a[40:43], v[52:55]\n"
"v_exp_f32 v25, v25\n"
"v_mfma_f32_16x16x32_bf16 v[60:63], a[120:123], a[56:59], v[60:63]\n"
"ds_read_b128 a[80:83], v227 offset:4096\n"
"v_cvt_pk_bf16_f32 v17, v18, v19\n"
"v_mfma_f32_16x16x32_bf16 v[52:55], a[124:127], a[44:47], v[52:55]\n"
"v_exp_f32 v26, v26\n"
"v_mfma_f32_16x16x32_bf16 v[60:63], a[124:127], a[60:63], v[60:63]\n"
"s_waitcnt lgkmcnt(4)\n"
"buffer_load_dwordx4 v225, s[8:11], s44 offen offset:3072 sc1 lds\n"
"v_mfma_f32_16x16x32_bf16 v[144:147], a[64:67], v[64:67], v[144:147]\n"
"s_add_u32 s41, s41, 32768\n"
"v_exp_f32 v27, v27\n"
"v_mfma_f32_16x16x32_bf16 v[176:179], a[64:67], v[68:71], v[176:179]\n"
"s_waitcnt lgkmcnt(3)\n"
"ds_read_b128 a[84:87], v227 offset:5120\n"
"v_readlane_b32 s43, v228, s42\n"
"v_mfma_f32_16x16x32_bf16 v[148:151], a[68:71], v[64:67], v[148:151]\n"
"s_cmp_eq_u32 s41, 163840\n"
"v_exp_f32 v20, v20\n"
"v_mfma_f32_16x16x32_bf16 v[180:183], a[68:71], v[68:71], v[180:183]\n"
"s_waitcnt lgkmcnt(2)\n"
"ds_read_b128 a[88:91], v227 offset:6144\n"
"ds_read_b128 a[92:95], v227 offset:7168\n"
"v_mfma_f32_16x16x32_bf16 v[152:155], a[72:75], v[64:67], v[152:155]\n"
"s_cselect_b32 s41, 0, s41\n"
"v_exp_f32 v21, v21\n"
"v_mfma_f32_16x16x32_bf16 v[184:187], a[72:75], v[68:71], v[184:187]\n"
"s_mov_b32 m0, s40\n"
"ds_read_b128 a[96:99], v227 offset:8192\n"
"v_add_u32 v226, s41, v224\n"
"v_mfma_f32_16x16x32_bf16 v[156:159], a[76:79], v[64:67], v[156:159]\n"
"v_exp_f32 v22, v22\n"
"v_mfma_f32_16x16x32_bf16 v[188:191], a[76:79], v[68:71], v[188:191]\n"
"s_waitcnt lgkmcnt(4)\n"
"buffer_load_dwordx4 v225, s[8:11], s43 offen offset:0 sc1 lds\n"
"v_mfma_f32_16x16x32_bf16 v[160:163], a[80:83], v[64:67], v[160:163]\n"
"v_exp_f32 v23, v23\n"
"v_mfma_f32_16x16x32_bf16 v[192:195], a[80:83], v[68:71], v[192:195]\n"
"s_waitcnt lgkmcnt(3)\n"
"ds_read_b128 a[100:103], v227 offset:9216\n"
"v_readlane_b32 s44, v229, s42\n"
"v_mfma_f32_16x16x32_bf16 v[164:167], a[84:87], v[64:67], v[164:167]\n"
"s_add_u32 s42, s42, 1\n"
"v_exp_f32 v28, v28\n"
"v_mfma_f32_16x16x32_bf16 v[196:199], a[84:87], v[68:71], v[196:199]\n"
"s_waitcnt lgkmcnt(2)\n"
"ds_read_b128 a[104:107], v227 offset:10240\n"
"ds_read_b128 a[108:111], v227 offset:11264\n"
"v_mfma_f32_16x16x32_bf16 v[168:171], a[88:91], v[64:67], v[168:171]\n"
"v_exp_f32 v29, v29\n"
"v_mfma_f32_16x16x32_bf16 v[200:203], a[88:91], v[68:71], v[200:203]\n"
"ds_read_b128 a[112:115], v227 offset:12288\n"
"v_cvt_pk_bf16_f32 v24, v24, v25\n"
"v_mfma_f32_16x16x32_bf16 v[172:175], a[92:95], v[64:67], v[172:175]\n"
"v_exp_f32 v30, v30\n"
"v_mfma_f32_16x16x32_bf16 v[204:207], a[92:95], v[68:71], v[204:207]\n"
"buffer_load_dwordx4 v225, s[8:11], s43 offen offset:1024 sc1 lds\n"
"v_mfma_f32_16x16x32_bf16 a[140:143], a[128:131], v[64:67], a[140:143]\n"
"v_exp_f32 v31, v31\n"
"v_mfma_f32_16x16x32_bf16 a[144:147], a[128:131], v[68:71], a[144:147]\n"
"s_waitcnt lgkmcnt(3)\n"
"ds_read_b128 a[116:119], v227 offset:13312\n"
"v_cvt_pk_bf16_f32 v25, v26, v27\n"
"v_mfma_f32_16x16x32_bf16 v[144:147], a[96:99], v[72:75], v[144:147]\n"
"v_exp_f32 v32, v32\n"
"v_mfma_f32_16x16x32_bf16 v[176:179], a[96:99], v[76:79], v[176:179]\n"
"ds_read_b128 a[120:123], v227 offset:14336\n"
"ds_read_b128 a[124:127], v227 offset:15360\n"
"v_mfma_f32_16x16x32_bf16 v[148:151], a[100:103], v[72:75], v[148:151]\n"
"v_exp_f32 v33, v33\n"
"v_mfma_f32_16x16x32_bf16 v[180:183], a[100:103], v[76:79], v[180:183]\n"
"s_waitcnt lgkmcnt(3)\n"
"v_cvt_pk_bf16_f32 v18, v20, v21\n"
"v_cvt_pk_bf16_f32 v19, v22, v23\n"
"v_mfma_f32_16x16x32_bf16 v[152:155], a[104:107], v[72:75], v[152:155]\n"
"v_exp_f32 v34, v34\n"
"v_mfma_f32_16x16x32_bf16 v[184:187], a[104:107], v[76:79], v[184:187]\n"
"buffer_load_dwordx4 v225, s[8:11], s43 offen offset:2048 sc1 lds\n"
"v_mfma_f32_16x16x32_bf16 v[156:159], a[108:111], v[72:75], v[156:159]\n"
"v_exp_f32 v35, v35\n"
"v_mfma_f32_16x16x32_bf16 v[188:191], a[108:111], v[76:79], v[188:191]\n"
"ds_read_b128 a[64:67], v227 offset:16384\n"
"v_cvt_pk_bf16_f32 v26, v28, v29\n"
"v_mfma_f32_16x16x32_bf16 v[160:163], a[112:115], v[72:75], v[160:163]\n"
"v_exp_f32 v40, v40\n"
"v_mfma_f32_16x16x32_bf16 v[192:195], a[112:115], v[76:79], v[192:195]\n"
"s_waitcnt lgkmcnt(1)\n"
"ds_read_b128 a[68:71], v227 offset:17408\n"
"ds_read_b128 a[72:75], v227 offset:18432\n"
"v_mfma_f32_16x16x32_bf16 v[164:167], a[116:119], v[72:75], v[164:167]\n"
"v_exp_f32 v41, v41\n"
"v_mfma_f32_16x16x32_bf16 v[196:199], a[116:119], v[76:79], v[196:199]\n"
"ds_read_b128 a[76:79], v227 offset:19456\n"
"v_cvt_pk_bf16_f32 v27, v30, v31\n"
"v_mfma_f32_16x16x32_bf16 v[168:171], a[120:123], v[72:75], v[168:171]\n"
"v_exp_f32 v42, v42\n"
"v_mfma_f32_16x16x32_bf16 v[200:203], a[120:123], v[76:79], v[200:203]\n"
"buffer_load_dwordx4 v225, s[8:11], s43 offen offset:3072 sc1 lds\n"
"v_mfma_f32_16x16x32_bf16 v[172:175], a[124:127], v[72:75], v[172:175]\n"
"v_exp_f32 v43, v43\n"
"v_mfma_f32_16x16x32_bf16 v[204:207], a[124:127], v[76:79], v[204:207]\n"
"ds_read_b128 a[80:83], v227 offset:20480\n"
"v_cvt_pk_bf16_f32 v64, v32, v33\n"
"v_mfma_f32_16x16x32_bf16 a[140:143], a[128:131], v[72:75], a[140:143]\n"
"v_exp_f32 v36, v36\n"
"v_mfma_f32_16x16x32_bf16 a[144:147], a[128:131], v[76:79], a[144:147]\n"
"s_waitcnt lgkmcnt(2)\n"
"ds_read_b128 a[84:87], v227 offset:21504\n"
"ds_read_b128 a[88:91], v227 offset:22528\n"
"v_mfma_f32_16x16x32_bf16 v[144:147], a[64:67], v[0:3], v[144:147]\n"
"s_add_u32 m0, s40, 16384\n"
"v_exp_f32 v37, v37\n"
"v_mfma_f32_16x16x32_bf16 v[176:179], a[64:67], v[8:11], v[176:179]\n"
"ds_read_b128 a[92:95], v227 offset:23552\n"
"v_cvt_pk_bf16_f32 v65, v34, v35\n"
"v_mfma_f32_16x16x32_bf16 v[148:151], a[68:71], v[0:3], v[148:151]\n"
"v_exp_f32 v38, v38\n"
"v_mfma_f32_16x16x32_bf16 v[180:183], a[68:71], v[8:11], v[180:183]\n"
"buffer_load_dwordx4 v225, s[8:11], s44 offen offset:0 sc1 lds\n"
"v_mfma_f32_16x16x32_bf16 v[152:155], a[72:75], v[0:3], v[152:155]\n"
"s_add_u32 s40, s40, 32768\n"
"v_exp_f32 v39, v39\n"
"v_mfma_f32_16x16x32_bf16 v[184:187], a[72:75], v[8:11], v[184:187]\n"
"s_waitcnt lgkmcnt(3)\n"
"ds_read_b128 a[96:99], v227 offset:24576\n"
"v_cvt_pk_bf16_f32 v68, v40, v41\n"
"v_mfma_f32_16x16x32_bf16 v[156:159], a[76:79], v[0:3], v[156:159]\n"
"s_cmp_eq_u32 s40, s46\n"
"v_exp_f32 v44, v44\n"
"v_mfma_f32_16x16x32_bf16 v[188:191], a[76:79], v[8:11], v[188:191]\n"
"s_cselect_b32 s40, s47, s40\n"
"ds_read_b128 a[100:103], v227 offset:25600\n"
"v_cvt_pk_bf16_f32 v69, v42, v43\n"
"v_mfma_f32_16x16x32_bf16 v[160:163], a[80:83], v[0:3], v[160:163]\n"
"v_exp_f32 v45, v45\n"
"v_mfma_f32_16x16x32_bf16 v[192:195], a[80:83], v[8:11], v[192:195]\n"
"s_waitcnt lgkmcnt(2)\n"
"ds_read_b128 a[104:107], v227 offset:26624\n"
"v_cvt_pk_bf16_f32 v66, v36, v37\n"
"v_mfma_f32_16x16x32_bf16 v[164:167], a[84:87], v[0:3], v[164:167]\n"
"v_exp_f32 v46, v46\n"
"v_mfma_f32_16x16x32_bf16 v[196:199], a[84:87], v[8:11], v[196:199]\n"
"buffer_load_dwordx4 v225, s[8:11], s44 offen offset:1024 sc1 lds\n"
"v_mfma_f32_16x16x32_bf16 v[168:171], a[88:91], v[0:3], v[168:171]\n"
"v_exp_f32 v47, v47\n"
"v_mfma_f32_16x16x32_bf16 v[200:203], a[88:91], v[8:11], v[200:203]\n"
"ds_read_b128 a[108:111], v227 offset:27648\n"
"v_cvt_pk_bf16_f32 v67, v38, v39\n"
"v_mfma_f32_16x16x32_bf16 v[172:175], a[92:95], v[0:3], v[172:175]\n"
"v_exp_f32 v48, v48\n"
"v_mfma_f32_16x16x32_bf16 v[204:207], a[92:95], v[8:11], v[204:207]\n"
"ds_read_b128 a[112:115], v227 offset:28672\n"
"ds_read_b128 a[116:119], v227 offset:29696\n"
"v_mfma_f32_16x16x32_bf16 a[140:143], a[128:131], v[0:3], a[140:143]\n"
"v_exp_f32 v49, v49\n"
"v_mfma_f32_16x16x32_bf16 a[144:147], a[128:131], v[8:11], a[144:147]\n"
"s_waitcnt lgkmcnt(3)\n"
"ds_read_b128 a[120:123], v227 offset:30720\n"
"v_cvt_pk_bf16_f32 v70, v44, v45\n"
"v_mfma_f32_16x16x32_bf16 v[144:147], a[96:99], v[16:19], v[144:147]\n"
"v_exp_f32 v50, v50\n"
"v_mfma_f32_16x16x32_bf16 v[176:179], a[96:99], v[24:27], v[176:179]\n"
"buffer_load_dwordx4 v225, s[8:11], s44 offen offset:2048 sc1 lds\n"
"v_mfma_f32_16x16x32_bf16 v[148:151], a[100:103], v[16:19], v[148:151]\n"
"v_exp_f32 v51, v51\n"
"v_mfma_f32_16x16x32_bf16 v[180:183], a[100:103], v[24:27], v[180:183]\n"
"ds_read_b128 a[124:127], v227 offset:31744\n"
"v_cvt_pk_bf16_f32 v71, v46, v47\n"
"v_mfma_f32_16x16x32_bf16 v[152:155], a[104:107], v[16:19], v[152:155]\n"
"s_waitcnt vmcnt(15)\n"
"s_barrier\n"
"v_mfma_f32_16x16x32_bf16 v[184:187], a[104:107], v[24:27], v[184:187]\n"
"s_waitcnt lgkmcnt(2)\n"
"ds_read_b128 a[64:67], v226 offset:0\n"
"v_cvt_pk_bf16_f32 v72, v48, v49\n"
"v_mfma_f32_16x16x32_bf16 v[156:159], a[108:111], v[16:19], v[156:159]\n"
"v_exp_f32 v52, v52\n"
"v_mfma_f32_16x16x32_bf16 v[188:191], a[108:111], v[24:27], v[188:191]\n"
"ds_read_b128 a[68:71], v226 offset:1024\n"
"v_cvt_pk_bf16_f32 v73, v50, v51\n"
"v_mfma_f32_16x16x32_bf16 v[160:163], a[112:115], v[16:19], v[160:163]\n"
"v_exp_f32 v53, v53\n"
"v_mfma_f32_16x16x32_bf16 v[192:195], a[112:115], v[24:27], v[192:195]\n"
"buffer_load_dwordx4 v225, s[8:11], s44 offen offset:3072 sc1 lds\n"
"v_mfma_f32_16x16x32_bf16 v[164:167], a[116:119], v[16:19], v[164:167]\n"
"v_exp_f32 v54, v54\n"
"v_mfma_f32_16x16x32_bf16 v[196:199], a[116:119], v[24:27], v[196:199]\n"
"s_waitcnt lgkmcnt(2)\n"
"ds_read_b128 a[72:75], v226 offset:2048\n"
"v_cvt_pk_bf16_f32 v74, v52, v53\n"
"v_mfma_f32_16x16x32_bf16 v[168:171], a[120:123], v[16:19], v[168:171]\n"
"v_exp_f32 v55, v55\n"
"v_mfma_f32_16x16x32_bf16 v[200:203], a[120:123], v[24:27], v[200:203]\n"
"ds_read_b128 a[76:79], v226 offset:3072\n"
"ds_read_b128 a[80:83], v226 offset:4096\n"
"v_mfma_f32_16x16x32_bf16 v[172:175], a[124:127], v[16:19], v[172:175]\n"
"v_cvt_pk_bf16_f32 v75, v54, v55\n"
"v_mfma_f32_16x16x32_bf16 v[204:207], a[124:127], v[24:27], v[204:207]\n"
"ds_read_b128 a[84:87], v226 offset:5120\n"
"v_mfma_f32_16x16x32_bf16 a[140:143], a[128:131], v[16:19], a[140:143]\n"
"v_mfma_f32_16x16x32_bf16 a[144:147], a[128:131], v[24:27], a[144:147]\n"
"s_sub_u32 s56, s56, 1\n"
"s_cbranch_scc0 LP2_%=\n"
"s_waitcnt lgkmcnt(5)\n"
"v_mfma_f32_16x16x32_bf16 v[0:3], a[64:67], a[32:35], v[208:211]\n"
"v_exp_f32 v56, v56\n"
"v_mfma_f32_16x16x32_bf16 v[8:11], a[64:67], a[48:51], 0\n"
"s_waitcnt lgkmcnt(4)\n"
"ds_read_b128 a[88:91], v226 offset:6144\n"
"v_readlane_b32 s43, v228, s42\n"
"v_mfma_f32_16x16x32_bf16 v[0:3], a[68:71], a[36:39], v[0:3]\n"
"s_waitcnt vmcnt(16)\n"
"buffer_store_dwordx2 v[232:233], v230, s[8:11], s61 offen sc1\n"
"v_mfma_f32_16x16x32_bf16 v[8:11], a[68:71], a[52:55], v[8:11]\n"
"s_waitcnt lgkmcnt(4)\n"
"v_exp_f32 v57, v57\n"
"v_mfma_f32_16x16x32_bf16 v[0:3], a[72:75], a[40:43], v[0:3]\n"
"s_mov_b32 m0, s40\n"
"ds_read_b128 a[92:95], v226 offset:7168\n"
"ds_read_b128 a[96:99], v226 offset:8192\n"
"v_mfma_f32_16x16x32_bf16 v[8:11], a[72:75], a[56:59], v[8:11]\n"
"s_waitcnt lgkmcnt(3)\n"
"v_exp_f32 v58, v58\n"
"v_mfma_f32_16x16x32_bf16 v[0:3], a[76:79], a[44:47], v[0:3]\n"
"s_add_u32 s41, s41, 32768\n"
"ds_read_b128 a[100:103], v226 offset:9216\n"
"v_readlane_b32 s44, v229, s42\n"
"v_cvt_pk_bf16_f32 v76, v56, v57\n"
"v_mfma_f32_16x16x32_bf16 v[8:11], a[76:79], a[60:63], v[8:11]\n"
"s_cmp_eq_u32 s41, 163840\n"
"v_exp_f32 v59, v59\n"
"v_mfma_f32_16x16x32_bf16 v[4:7], a[80:83], a[32:35], v[212:215]\n"
"s_cselect_b32 s41, 0, s41\n"
"buffer_load_dwordx4 v225, s[8:11], s43 offen offset:0 sc1 lds\n"
"v_add_u32 v227, s41, v224\n"
"v_mfma_f32_16x16x32_bf16 v[12:15], a[80:83], a[48:51], 0\n"
"s_add_u32 s42, s42, 1\n"
"v_exp_f32 v60, v60\n"
"v_mfma_f32_16x16x32_bf16 v[4:7], a[84:87], a[36:39], v[4:7]\n"
"ds_read_b128 a[104:107], v226 offset:10240\n"
"v_cvt_pk_bf16_f32 v77, v58, v59\n"
"v_mfma_f32_16x16x32_bf16 v[12:15], a[84:87], a[52:55], v[12:15]\n"
"s_waitcnt lgkmcnt(2)\n"
"v_exp_f32 v61, v61\n"
"v_mfma_f32_16x16x32_bf16 v[4:7], a[88:91], a[40:43], v[4:7]\n"
"ds_read_b128 a[108:111], v226 offset:11264\n"
"ds_read_b128 a[112:115], v226 offset:12288\n"
"v_mfma_f32_16x16x32_bf16 v[12:15], a[88:91], a[56:59], v[12:15]\n"
"v_exp_f32 v62, v62\n"
"v_mfma_f32_16x16x32_bf16 v[4:7], a[92:95], a[44:47], v[4:7]\n"
"ds_read_b128 a[116:119], v226 offset:13312\n"
"v_cvt_pk_bf16_f32 v78, v60, v61\n"
"v_mfma_f32_16x16x32_bf16 v[12:15], a[92:95], a[60:63], v[12:15]\n"
"v_exp_f32 v63, v63\n"
"v_mfma_f32_16x16x32_bf16 v[16:19], a[96:99], a[32:35], v[216:219]\n"
"buffer_load_dwordx4 v225, s[8:11], s43 offen offset:1024 sc1 lds\n"
"v_mfma_f32_16x16x32_bf16 v[24:27], a[96:99], a[48:51], 0\n"
"s_waitcnt lgkmcnt(3)\n"
"ds_read_b128 a[120:123], v226 offset:14336\n"
"v_cvt_pk_bf16_f32 v79, v62, v63\n"
"v_mfma_f32_16x16x32_bf16 v[16:19], a[100:103], a[36:39], v[16:19]\n"
"ds_read_b128 a[124:127], v226 offset:15360\n"
"v_mfma_f32_16x16x32_bf16 v[24:27], a[100:103], a[52:55], v[24:27]\n"
"ds_read_b128 a[64:67], v226 offset:16384\n"
"v_mfma_f32_16x16x32_bf16 v[16:19], a[104:107], a[40:43], v[16:19]\n"
"ds_read_b128 a[68:71], v226 offset:17408\n"
"v_mfma_f32_16x16x32_bf16 v[24:27], a[104:107], a[56:59], v[24:27]\n"
"s_waitcnt lgkmcnt(4)\n"
"ds_read_b128 a[72:75], v226 offset:18432\n"
"v_mfma_f32_16x16x32_bf16 v[16:19], a[108:111], a[44:47], v[16:19]\n"
"ds_read_b128 a[76:79], v226 offset:19456\n"
"v_mfma_f32_16x16x32_bf16 v[24:27], a[108:111], a[60:63], v[24:27]\n"
"ds_read_b128 a[80:83], v226 offset:20480\n"
"v_mfma_f32_16x16x32_bf16 v[20:23], a[112:115], a[32:35], v[220:223]\n"
"buffer_load_dwordx4 v225, s[8:11], s43 offen offset:2048 sc1 lds\n"
"v_mfma_f32_16x16x32_bf16 v[28:31], a[112:115], a[48:51], 0\n"
"ds_read_b128 a[84:87], v226 offset:21504\n"
"v_mfma_f32_16x16x32_bf16 v[20:23], a[116:119], a[36:39], v[20:23]\n"
"ds_read_b128 a[88:91], v226 offset:22528\n"
"v_mfma_f32_16x16x32_bf16 v[28:31], a[116:119], a[52:55], v[28:31]\n"
"s_waitcnt lgkmcnt(5)\n"
"ds_read_b128 a[92:95], v226 offset:23552\n"
"v_mfma_f32_16x16x32_bf16 v[20:23], a[120:123], a[40:43], v[20:23]\n"
"ds_read_b128 a[96:99], v226 offset:24576\n"
"v_mfma_f32_16x16x32_bf16 v[28:31], a[120:123], a[56:59], v[28:31]\n"
"ds_read_b128 a[100:103], v226 offset:25600\n"
"v_mfma_f32_16x16x32_bf16 v[20:23], a[124:127], a[44:47], v[20:23]\n"
"ds_read_b128 a[104:107], v226 offset:26624\n"
"v_mfma_f32_16x16x32_bf16 v[28:31], a[124:127], a[60:63], v[28:31]\n"
"ds_read_b128 a[108:111], v226 offset:27648\n"
"v_mfma_f32_16x16x32_bf16 v[40:43], a[64:67], a[48:51], v[208:211]\n"
"ds_read_b128 a[112:115], v226 offset:28672\n"
"v_mfma_f32_16x16x32_bf16 v[40:43], a[68:71], a[52:55], v[40:43]\n"
"s_waitcnt lgkmcnt(6)\n"
"buffer_load_dwordx4 v225, s[8:11], s43 offen offset:3072 sc1 lds\n"
"v_mfma_f32_16x16x32_bf16 v[40:43], a[72:75], a[56:59], v[40:43]\n"
"ds_read_b128 a[116:119], v226 offset:29696\n"
"v_mfma_f32_16x16x32_bf16 v[40:43], a[76:79], a[60:63], v[40:43]\n"
"s_add_u32 m0, s40, 16384\n"
"ds_read_b128 a[120:123], v226 offset:30720\n"
"v_mfma_f32_16x16x32_bf16 v[44:47], a[80:83], a[48:51], v[212:215]\n"
"ds_read_b128 a[124:127], v226 offset:31744\n"
"v_mfma_f32_16x16x32_bf16 v[44:47], a[84:87], a[52:55], v[44:47]\n"
"v_exp_f32 v0, v0\n"
"v_mfma_f32_16x16x32_bf16 v[44:47], a[88:91], a[56:59], v[44:47]\n"
"s_waitcnt lgkmcnt(4)\n"
"s_waitcnt vmcnt(13)\n"
"s_barrier\n"
"v_mfma_f32_16x16x32_bf16 v[44:47], a[92:95], a[60:63], v[44:47]\n"
"ds_read_b128 a[64:67], v227 offset:0\n"
"v_mfma_f32_16x16x32_bf16 v[56:59], a[96:99], a[48:51], v[216:219]\n"
"s_add_u32 s40, s40, 32768\n"
"v_exp_f32 v1, v1\n"
"v_mfma_f32_16x16x32_bf16 v[56:59], a[100:103], a[52:55], v[56:59]\n"
"s_cmp_eq_u32 s40, s46\n"
"buffer_load_dwordx4 v225, s[8:11], s44 offen offset:0 sc1 lds\n"
"v_mfma_f32_16x16x32_bf16 v[56:59], a[104:107], a[56:59], v[56:59]\n"
"s_cselect_b32 s40, s47, s40\n"
"v_exp_f32 v2, v2\n"
"v_cvt_pk_bf16_f32 v0, v0, v1\n"
"v_mfma_f32_16x16x32_bf16 v[56:59], a[108:111], a[60:63], v[56:59]\n"
"s_waitcnt lgkmcnt(1)\n"
"ds_read_b128 a[68:71], v227 offset:1024\n"
"ds_read_b128 a[72:75], v227 offset:2048\n"
"v_mfma_f32_16x16x32_bf16 v[60:63], a[112:115], a[48:51], v[220:223]\n"
"v_exp_f32 v3, v3\n"
"v_mfma_f32_16x16x32_bf16 v[60:63], a[116:119], a[52:55], v[60:63]\n"
"ds_read_b128 a[76:79], v227 offset:3072\n"
"ds_read_b128 a[80:83], v227 offset:4096\n"
"v_mfma_f32_16x16x32_bf16 v[60:63], a[120:123], a[56:59], v[60:63]\n"
"v_exp_f32 v8, v8\n"
"v_cvt_pk_bf16_f32 v1, v2, v3\n"
"v_mfma_f32_16x16x32_bf16 v[60:63], a[124:127], a[60:63], v[60:63]\n"
"s_waitcnt lgkmcnt(4)\n"
"v_exp_f32 v9, v9\n"
"v_mfma_f32_16x16x32_bf16 v[144:147], a[64:67], v[64:67], v[144:147]\n"
"s_add_u32 s41, s41, 32768\n"
"buffer_load_dwordx4 v225, s[8:11], s44 offen offset:1024 sc1 lds\n"
"v_mfma_f32_16x16x32_bf16 v[176:179], a[64:67], v[68:71], v[176:179]\n"
"s_waitcnt lgkmcnt(3)\n"
"v_exp_f32 v10, v10\n"
"v_mfma_f32_16x16x32_bf16 v[148:151], a[68:71], v[64:67], v[148:151]\n"
"s_cmp_eq_u32 s41, 163840\n"
"ds_read_b128 a[84:87], v227 offset:5120\n"
"v_cvt_pk_bf16_f32 v8, v8, v9\n"
"v_mfma_f32_16x16x32_bf16 v[180:183], a[68:71], v[68:71], v[180:183]\n"
"s_waitcnt lgkmcnt(3)\n"
"v_exp_f32 v11, v11\n"
"v_mfma_f32_16x16x32_bf16 v[152:155], a[72:75], v[64:67], v[152:155]\n"
"s_cselect_b32 s41, 0, s41\n"
"ds_read_b128 a[88:91], v227 offset:6144\n"
"ds_read_b128 a[92:95], v227 offset:7168\n"
"v_add_u32 v226, s41, v224\n"
"v_mfma_f32_16x16x32_bf16 v[184:187], a[72:75], v[68:71], v[184:187]\n"
"s_waitcnt lgkmcnt(3)\n"
"v_exp_f32 v4, v4\n"
"v_mfma_f32_16x16x32_bf16 v[156:159], a[76:79], v[64:67], v[156:159]\n"
"ds_read_b128 a[96:99], v227 offset:8192\n"
"v_cvt_pk_bf16_f32 v9, v10, v11\n"
"v_mfma_f32_16x16x32_bf16 v[188:191], a[76:79], v[68:71], v[188:191]\n"
"v_exp_f32 v5, v5\n"
"v_mfma_f32_16x16x32_bf16 v[160:163], a[80:83], v[64:67], v[160:163]\n"
"buffer_load_dwordx4 v225, s[8:11], s44 offen offset:2048 sc1 lds\n"
"v_mfma_f32_16x16x32_bf16 v[192:195], a[80:83], v[68:71], v[192:195]\n"
"s_waitcnt lgkmcnt(3)\n"
"v_exp_f32 v6, v6\n"
"v_mfma_f32_16x16x32_bf16 v[164:167], a[84:87], v[64:67], v[164:167]\n"
"ds_read_b128 a[100:103], v227 offset:9216\n"
"v_cvt_pk_bf16_f32 v2, v4, v5\n"
"v_mfma_f32_16x16x32_bf16 v[196:199], a[84:87], v[68:71], v[196:199]\n"
"s_waitcnt lgkmcnt(2)\n"
"v_exp_f32 v7, v7\n"
"v_mfma_f32_16x16x32_bf16 v[168:171], a[88:91], v[64:67], v[168:171]\n"
"ds_read_b128 a[104:107], v227 offset:10240\n"
"ds_read_b128 a[108:111], v227 offset:11264\n"
"v_mfma_f32_16x16x32_bf16 v[200:203], a[88:91], v[68:71], v[200:203]\n"
"v_exp_f32 v12, v12\n"
"v_mfma_f32_16x16x32_bf16 v[172:175], a[92:95], v[64:67], v[172:175]\n"
"ds_read_b128 a[112:115], v227 offset:12288\n"
"v_cvt_pk_bf16_f32 v3, v6, v7\n"
"v_mfma_f32_16x16x32_bf16 v[204:207], a[92:95], v[68:71], v[204:207]\n"
"v_exp_f32 v13, v13\n"
"v_mfma_f32_16x16x32_bf16 a[140:143], a[128:131], v[64:67], a[140:143]\n"
"buffer_load_dwordx4 v225, s[8:11], s44 offen offset:3072 sc1 lds\n"
"v_mfma_f32_16x16x32_bf16 a[144:147], a[128:131], v[68:71], a[144:147]\n"
"s_waitcnt lgkmcnt(3)\n"
"v_exp_f32 v14, v14\n"
"v_mfma_f32_16x16x32_bf16 v[144:147], a[96:99], v[72:75], v[144:147]\n"
"ds_read_b128 a[116:119], v227 offset:13312\n"
"v_readlane_b32 s43, v228, s42\n"
"v_cvt_pk_bf16_f32 v10, v12, v13\n"
"v_mfma_f32_16x16x32_bf16 v[176:179], a[96:99], v[76:79], v[176:179]\n"
"s_mov_b32 m0, s40\n"
"v_exp_f32 v15, v15\n"
"v_mfma_f32_16x16x32_bf16 v[148:151], a[100:103], v[72:75], v[148:151]\n"
"ds_read_b128 a[120:123], v227 offset:14336\n"
"ds_read_b128 a[124:127], v227 offset:15360\n"
"v_mfma_f32_16x16x32_bf16 v[180:183], a[100:103], v[76:79], v[180:183]\n"
"s_waitcnt lgkmcnt(3)\n"
"v_exp_f32 v16, v16\n"
"v_mfma_f32_16x16x32_bf16 v[152:155], a[104:107], v[72:75], v[152:155]\n"
"v_exp_f32 v17, v17\n"
"v_cvt_pk_bf16_f32 v11, v14, v15\n"
"v_mfma_f32_16x16x32_bf16 v[184:187], a[104:107], v[76:79], v[184:187]\n"
"v_exp_f32 v18, v18\n"
"v_mfma_f32_16x16x32_bf16 v[156:159], a[108:111], v[72:75], v[156:159]\n"
"buffer_load_dwordx4 v225, s[8:11], s43 offen offset:0 sc1 lds\n"
"v_readlane_b32 s44, v229, s42\n"
"v_mfma_f32_16x16x32_bf16 v[188:191], a[108:111], v[76:79], v[188:191]\n"
"s_add_u32 s42, s42, 1\n"
"v_exp_f32 v19, v19\n"
"v_cvt_pk_bf16_f32 v16, v16, v17\n"
"v_mfma_f32_16x16x32_bf16 v[160:163], a[112:115], v[72:75], v[160:163]\n"
"ds_read_b128 a[64:67], v227 offset:16384\n"
"v_cvt_pk_bf16_f32 v17, v18, v19\n"
"v_mfma_f32_16x16x32_bf16 v[192:195], a[112:115], v[76:79], v[192:195]\n"
"s_waitcnt lgkmcnt(1)\n"
"v_exp_f32 v24, v24\n"
"v_mfma_f32_16x16x32_bf16 v[164:167], a[116:119], v[72:75], v[164:167]\n"
"ds_read_b128 a[68:71], v227 offset:17408\n"
"ds_read_b128 a[72:75], v227 offset:18432\n"
"v_mfma_f32_16x16x32_bf16 v[196:199], a[116:119], v[76:79], v[196:199]\n"
"v_exp_f32 v25, v25\n"
"v_mfma_f32_16x16x32_bf16 v[168:171], a[120:123], v[72:75], v[168:171]\n"
"ds_read_b128 a[76:79], v227 offset:19456\n"
"v_cvt_pk_bf16_f32 v24, v24, v25\n"
"v_mfma_f32_16x16x32_bf16 v[200:203], a[120:123], v[76:79], v[200:203]\n"
"v_exp_f32 v26, v26\n"
"v_mfma_f32_16x16x32_bf16 v[172:175], a[124:127], v[72:75], v[172:175]\n"
"buffer_load_dwordx4 v225, s[8:11], s43 offen offset:1024 sc1 lds\n"
"v_mfma_f32_16x16x32_bf16 v[204:207], a[124:127], v[76:79], v[204:207]\n"
"v_exp_f32 v27, v27\n"
"v_mfma_f32_16x16x32_bf16 a[140:143], a[128:131], v[72:75], a[140:143]\n"
"ds_read_b128 a[80:83], v227 offset:20480\n"
"v_cvt_pk_bf16_f32 v25, v26, v27\n"
"v_mfma_f32_16x16x32_bf16 a[144:147], a[128:131], v[76:79], a[144:147]\n"
"s_waitcnt lgkmcnt(2)\n"
"v_exp_f32 v20, v20\n"
"v_mfma_f32_16x16x32_bf16 v[144:147], a[64:67], v[0:3], v[144:147]\n"
"ds_read_b128 a[84:87], v227 offset:21504\n"
"ds_read_b128 a[88:91], v227 offset:22528\n"
"v_mfma_f32_16x16x32_bf16 v[176:179], a[64:67], v[8:11], v[176:179]\n"
"v_exp_f32 v21, v21\n"
"v_mfma_f32_16x16x32_bf16 v[148:151], a[68:71], v[0:3], v[148:151]\n"
"ds_read_b128 a[92:95], v227 offset:23552\n"
"v_cvt_pk_bf16_f32 v18, v20, v21\n"
"v_mfma_f32_16x16x32_bf16 v[180:183], a[68:71], v[8:11], v[180:183]\n"
"v_exp_f32 v22, v22\n"
"v_mfma_f32_16x16x32_bf16 v[152:155], a[72:75], v[0:3], v[152:155]\n"
"buffer_load_dwordx4 v225, s[8:11], s43 offen offset:2048 sc1 lds\n"
"v_mfma_f32_16x16x32_bf16 v[184:187], a[72:75], v[8:11], v[184:187]\n"
"s_waitcnt lgkmcnt(3)\n"
"v_exp_f32 v23, v23\n"
"v_mfma_f32_16x16x32_bf16 v[156:159], a[76:79], v[0:3], v[156:159]\n"
"ds_read_b128 a[96:99], v227 offset:24576\n"
"v_cvt_pk_bf16_f32 v19, v22, v23\n"
"v_mfma_f32_16x16x32_bf16 v[188:191], a[76:79], v[8:11], v[188:191]\n"
"v_exp_f32 v28, v28\n"
"v_mfma_f32_16x16x32_bf16 v[160:163], a[80:83], v[0:3], v[160:163]\n"
"ds_read_b128 a[100:103], v227 offset:25600\n"
"v_mfma_f32_16x16x32_bf16 v[192:195], a[80:83], v[8:11], v[192:195]\n"
"s_waitcnt lgkmcnt(2)\n"
"v_exp_f32 v29, v29\n"
"v_mfma_f32_16x16x32_bf16 v[164:167], a[84:87], v[0:3], v[164:167]\n"
"ds_read_b128 a[104:107], v227 offset:26624\n"
"v_cvt_pk_bf16_f32 v26, v28, v29\n"
"v_mfma_f32_16x16x32_bf16 v[196:199], a[84:87], v[8:11], v[196:199]\n"
"v_exp_f32 v30, v30\n"
"v_mfma_f32_16x16x32_bf16 v[168:171], a[88:91], v[0:3], v[168:171]\n"
"buffer_load_dwordx4 v225, s[8:11], s43 offen offset:3072 sc1 lds\n"
"v_mfma_f32_16x16x32_bf16 v[200:203], a[88:91], v[8:11], v[200:203]\n"
"v_exp_f32 v31, v31\n"
"v_mfma_f32_16x16x32_bf16 v[172:175], a[92:95], v[0:3], v[172:175]\n"
"ds_read_b128 a[108:111], v227 offset:27648\n"
"v_cvt_pk_bf16_f32 v27, v30, v31\n"
"v_mfma_f32_16x16x32_bf16 v[204:207], a[92:95], v[8:11], v[204:207]\n"
"s_add_u32 m0, s40, 16384\n"
"v_exp_f32 v40, v40\n"
"v_mfma_f32_16x16x32_bf16 a[140:143], a[128:131], v[0:3], a[140:143]\n"
"ds_read_b128 a[112:115], v227 offset:28672\n"
"ds_read_b128 a[116:119], v227 offset:29696\n"
"v_mfma_f32_16x16x32_bf16 a[144:147], a[128:131], v[8:11], a[144:147]\n"
"s_waitcnt lgkmcnt(3)\n"
"v_exp_f32 v41, v41\n"
"v_mfma_f32_16x16x32_bf16 v[144:147], a[96:99], v[16:19], v[144:147]\n"
"ds_read_b128 a[120:123], v227 offset:30720\n"
"v_cvt_pk_bf16_f32 v68, v40, v41\n"
"v_mfma_f32_16x16x32_bf16 v[176:179], a[96:99], v[24:27], v[176:179]\n"
"v_exp_f32 v42, v42\n"
"v_mfma_f32_16x16x32_bf16 v[148:151], a[100:103], v[16:19], v[148:151]\n"
"s_add_u32 s40, s40, 32768\n"
"buffer_load_dwordx4 v225, s[8:11], s44 offen offset:0 sc1 lds\n"
"v_mfma_f32_16x16x32_bf16 v[180:183], a[100:103], v[24:27], v[180:183]\n"
"s_cmp_eq_u32 s40, s46\n"
"ds_read_b128 a[124:127], v227 offset:31744\n"
"s_cselect_b32 s40, s47, s40\n"
"v_mfma_f32_16x16x32_bf16 v[152:155], a[104:107], v[16:19], v[152:155]\n"
"s_add_u32 s41, s41, 32768\n"
"s_waitcnt vmcnt(14)\n"
"s_barrier\n"
"v_mfma_f32_16x16x32_bf16 v[184:187], a[104:107], v[24:27], v[184:187]\n"
"s_waitcnt lgkmcnt(2)\n"
"ds_read_b128 a[64:67], v226 offset:0\n"
"s_cmp_eq_u32 s41, 163840\n"
"v_mfma_f32_16x16x32_bf16 v[156:159], a[108:111], v[16:19], v[156:159]\n"
"s_cselect_b32 s41, 0, s41\n"
"ds_read_b128 a[68:71], v226 offset:1024\n"
"v_add_u32 v227, s41, v224\n"
"v_mfma_f32_16x16x32_bf16 v[188:191], a[108:111], v[24:27], v[188:191]\n"
"ds_read_b128 a[72:75], v226 offset:2048\n"
"v_mfma_f32_16x16x32_bf16 v[160:163], a[112:115], v[16:19], v[160:163]\n"
"ds_read_b128 a[76:79], v226 offset:3072\n"
"v_mfma_f32_16x16x32_bf16 v[192:195], a[112:115], v[24:27], v[192:195]\n"
"ds_read_b128 a[80:83], v226 offset:4096\n"
"v_mfma_f32_16x16x32_bf16 v[164:167], a[116:119], v[16:19], v[164:167]\n"
"buffer_load_dwordx4 v225, s[8:11], s44 offen offset:1024 sc1 lds\n"
"v_mfma_f32_16x16x32_bf16 v[196:199], a[116:119], v[24:27], v[196:199]\n"
"s_waitcnt lgkmcnt(5)\n"
"ds_read_b128 a[84:87], v226 offset:5120\n"
"v_mfma_f32_16x16x32_bf16 v[168:171], a[120:123], v[16:19], v[168:171]\n"
"ds_read_b128 a[88:91], v226 offset:6144\n"
"v_mfma_f32_16x16x32_bf16 v[200:203], a[120:123], v[24:27], v[200:203]\n"
"ds_read_b128 a[92:95], v226 offset:7168\n"
"v_mfma_f32_16x16x32_bf16 v[172:175], a[124:127], v[16:19], v[172:175]\n"
"ds_read_b128 a[96:99], v226 offset:8192\n"
"v_mfma_f32_16x16x32_bf16 v[204:207], a[124:127], v[24:27], v[204:207]\n"
"ds_read_b128 a[100:103], v226 offset:9216\n"
"v_mfma_f32_16x16x32_bf16 a[140:143], a[128:131], v[16:19], a[140:143]\n"
"ds_read_b128 a[104:107], v226 offset:10240\n"
"v_mfma_f32_16x16x32_bf16 a[144:147], a[128:131], v[24:27], a[144:147]\n"
"ds_read_b128 a[108:111], v226 offset:11264\n"
"v_exp_f32 v43, v43\n"
"s_nop 0\n"
"v_cvt_pk_bf16_f32 v69, v42, v43\n"
"v_exp_f32 v44, v44\n"
"v_exp_f32 v45, v45\n"
"s_nop 0\n"
"v_cvt_pk_bf16_f32 v70, v44, v45\n"
"v_exp_f32 v46, v46\n"
"v_exp_f32 v47, v47\n"
"s_nop 0\n"
"v_cvt_pk_bf16_f32 v71, v46, v47\n"
"s_waitcnt lgkmcnt(11)\n"
"s_nop 0\n"
"v_mfma_f32_16x16x32_bf16 v[176:179], a[64:67], v[68:71], v[176:179]\n"
"s_waitcnt lgkmcnt(6)\n"
"buffer_load_dwordx4 v225, s[8:11], s44 offen offset:2048 sc1 lds\n"
"v_mfma_f32_16x16x32_bf16 v[180:183], a[68:71], v[68:71], v[180:183]\n"
"ds_read_b128 a[112:115], v226 offset:12288\n"
"v_mfma_f32_16x16x32_bf16 v[184:187], a[72:75], v[68:71], v[184:187]\n"
"ds_read_b128 a[116:119], v226 offset:13312\n"
"v_mfma_f32_16x16x32_bf16 v[188:191], a[76:79], v[68:71], v[188:191]\n"
"ds_read_b128 a[120:123], v226 offset:14336\n"
"v_mfma_f32_16x16x32_bf16 v[192:195], a[80:83], v[68:71], v[192:195]\n"
"ds_read_b128 a[124:127], v226 offset:15360\n"
"v_mfma_f32_16x16x32_bf16 v[196:199], a[84:87], v[68:71], v[196:199]\n"
"s_waitcnt lgkmcnt(5)\n"
"s_waitcnt vmcnt(7)\n"
"s_barrier\n"
"v_mfma_f32_16x16x32_bf16 v[200:203], a[88:91], v[68:71], v[200:203]\n"
"ds_read_b128 a[64:67], v227 offset:0\n"
"v_mfma_f32_16x16x32_bf16 v[204:207], a[92:95], v[68:71], v[204:207]\n"
"buffer_load_dwordx4 v225, s[8:11], s44 offen offset:3072 sc1 lds\n"
"v_mfma_f32_16x16x32_bf16 a[144:147], a[128:131], v[68:71], a[144:147]\n"
"v_exp_f32 v56, v56\n"
"v_exp_f32 v57, v57\n"
"s_nop 0\n"
"v_cvt_pk_bf16_f32 v76, v56, v57\n"
"v_exp_f32 v58, v58\n"
"v_exp_f32 v59, v59\n"
"s_nop 0\n"
"v_cvt_pk_bf16_f32 v77, v58, v59\n"
"v_exp_f32 v60, v60\n"
"v_exp_f32 v61, v61\n"
"s_nop 0\n"
"v_cvt_pk_bf16_f32 v78, v60, v61\n"
"v_exp_f32 v62, v62\n"
"v_exp_f32 v63, v63\n"
"s_nop 0\n"
"v_cvt_pk_bf16_f32 v79, v62, v63\n"
"s_nop 1\n"
"v_mfma_f32_16x16x32_bf16 v[176:179], a[96:99], v[76:79], v[176:179]\n"
"ds_read_b128 a[68:71], v227 offset:1024\n"
"v_mfma_f32_16x16x32_bf16 v[180:183], a[100:103], v[76:79], v[180:183]\n"
"v_mfma_f32_16x16x32_bf16 v[184:187], a[104:107], v[76:79], v[184:187]\n"
"s_waitcnt lgkmcnt(2)\n"
"ds_read_b128 a[72:75], v227 offset:2048\n"
"v_mfma_f32_16x16x32_bf16 v[188:191], a[108:111], v[76:79], v[188:191]\n"
"ds_read_b128 a[76:79], v227 offset:3072\n"
"v_mfma_f32_16x16x32_bf16 v[192:195], a[112:115], v[76:79], v[192:195]\n"
"ds_read_b128 a[80:83], v227 offset:4096\n"
"v_mfma_f32_16x16x32_bf16 v[196:199], a[116:119], v[76:79], v[196:199]\n"
"v_mfma_f32_16x16x32_bf16 v[200:203], a[120:123], v[76:79], v[200:203]\n"
"v_mfma_f32_16x16x32_bf16 v[204:207], a[124:127], v[76:79], v[204:207]\n"
"v_mfma_f32_16x16x32_bf16 a[144:147], a[128:131], v[76:79], a[144:147]\n"
"ds_read_b128 a[84:87], v227 offset:5120\n"
"LTL_%=:\n"
"ds_read_b128 a[88:91], v227 offset:6144\n"
"s_waitcnt lgkmcnt(6)\n"
"v_mfma_f32_16x16x32_bf16 v[0:3], a[64:67], v[80:83], 0\n"
"v_accvgpr_read_b32 v251, a140\n"
"v_subrev_u32 v253, 0x2b800000, v251\n"
"v_mfma_f32_16x16x32_bf16 v[40:43], a[64:67], v[112:115], 0\n"
"s_waitcnt lgkmcnt(5)\n"
"ds_read_b128 a[92:95], v227 offset:7168\n"
"v_mfma_f32_16x16x32_bf16 v[4:7], a[68:71], v[80:83], 0\n"
"v_cmp_le_u32 vcc, 0x34000000, v253\n"
"s_cmp_lg_u64 vcc, 0\n"
"s_cbranch_scc1 L1035_%=\n"
"L1034_%=:\n"
"v_mfma_f32_16x16x32_bf16 v[44:47], a[68:71], v[112:115], 0\n"
"s_waitcnt lgkmcnt(5)\n"
"v_rcp_f32 v251, v251\n"
"v_mfma_f32_16x16x32_bf16 v[8:11], a[72:75], v[80:83], 0\n"
"s_add_u32 s41, s41, 32768\n"
"ds_read_b128 a[96:99], v227 offset:8192\n"
"v_mul_f32 v144, v251, v144\n"
"v_mfma_f32_16x16x32_bf16 v[48:51], a[72:75], v[112:115], 0\n"
"s_waitcnt lgkmcnt(4)\n"
"ds_read_b128 a[100:103], v227 offset:9216\n"
"v_mul_f32 v145, v251, v145\n"
"v_mfma_f32_16x16x32_bf16 v[12:15], a[76:79], v[80:83], 0\n"
"s_cmp_eq_u32 s41, 163840\n"
"v_mul_f32 v146, v251, v146\n"
"v_mul_f32 v147, v251, v147\n"
"v_mfma_f32_16x16x32_bf16 v[52:55], a[76:79], v[112:115], 0\n"
"s_cselect_b32 s41, 0, s41\n"
"ds_read_b128 a[104:107], v227 offset:10240\n"
"v_add_u32 v226, s41, v224\n"
"v_mfma_f32_16x16x32_bf16 v[16:19], a[80:83], v[80:83], 0\n"
"s_add_u32 s41, s41, 32768\n"
"v_mul_f32 v148, v251, v148\n"
"v_mul_f32 v149, v251, v149\n"
"v_mfma_f32_16x16x32_bf16 v[56:59], a[80:83], v[112:115], 0\n"
"s_waitcnt lgkmcnt(3)\n"
"ds_read_b128 a[108:111], v227 offset:11264\n"
"v_mul_f32 v150, v251, v150\n"
"v_mul_f32 v151, v251, v151\n"
"v_mfma_f32_16x16x32_bf16 v[20:23], a[84:87], v[80:83], 0\n"
"s_cmp_eq_u32 s41, 163840\n"
"v_cvt_pk_bf16_f32 v144, v144, v145\n"
"v_cvt_pk_bf16_f32 v145, v146, v147\n"
"v_mfma_f32_16x16x32_bf16 v[60:63], a[84:87], v[112:115], 0\n"
"s_cselect_b32 s41, 0, s41\n"
"ds_read_b128 a[112:115], v227 offset:12288\n"
"v_add_u32 v235, s41, v224\n"
"v_cvt_pk_bf16_f32 v146, v148, v149\n"
"v_mfma_f32_16x16x32_bf16 v[24:27], a[88:91], v[80:83], 0\n"
"s_add_u32 s41, s41, 32768\n"
"v_cvt_pk_bf16_f32 v147, v150, v151\n"
"v_mul_f32 v152, v251, v152\n"
"v_mfma_f32_16x16x32_bf16 v[64:67], a[88:91], v[112:115], 0\n"
"s_cmp_eq_u32 s41, 163840\n"
"buffer_store_dwordx4 v[144:147], v224, s[8:11], s60 offen offset:0 sc1\n"
"v_mul_f32 v153, v251, v153\n"
"v_mfma_f32_16x16x32_bf16 v[28:31], a[92:95], v[80:83], 0\n"
"s_cselect_b32 s41, 0, s41\n"
"ds_read_b128 a[116:119], v227 offset:13312\n"
"v_add_u32 v236, s41, v224\n"
"v_mul_f32 v154, v251, v154\n"
"v_mfma_f32_16x16x32_bf16 v[68:71], a[92:95], v[112:115], 0\n"
"s_waitcnt lgkmcnt(3)\n"
"ds_read_b128 a[120:123], v227 offset:14336\n"
"v_mul_f32 v155, v251, v155\n"
"v_mul_f32 v156, v251, v156\n"
"v_mfma_f32_16x16x32_bf16 v[32:35], a[96:99], v[80:83], 0\n"
"s_add_u32 s41, s41, 32768\n"
"v_mul_f32 v157, v251, v157\n"
"v_mul_f32 v158, v251, v158\n"
"v_mul_f32 v159, v251, v159\n"
"v_mfma_f32_16x16x32_bf16 v[72:75], a[96:99], v[112:115], 0\n"
"s_cmp_eq_u32 s41, 163840\n"
"ds_read_b128 a[124:127], v227 offset:15360\n"
"v_cvt_pk_bf16_f32 v152, v152, v153\n"
"v_cvt_pk_bf16_f32 v153, v154, v155\n"
"v_mfma_f32_16x16x32_bf16 v[36:39], a[100:103], v[80:83], 0\n"
"s_cselect_b32 s41, 0, s41\n"
"v_cvt_pk_bf16_f32 v154, v156, v157\n"
"v_add_u32 v237, s41, v224\n"
"v_cvt_pk_bf16_f32 v155, v158, v159\n"
"v_mfma_f32_16x16x32_bf16 v[76:79], a[100:103], v[112:115], 0\n"
"buffer_store_dwordx4 v[152:155], v224, s[8:11], s60 offen offset:1024 sc1\n"
"v_mul_f32 v160, v251, v160\n"
"v_mfma_f32_16x16x32_bf16 v[0:3], a[104:107], v[88:91], v[0:3]\n"
"ds_read_b128 a[64:67], v227 offset:16384\n"
"v_mul_f32 v161, v251, v161\n"
"v_mul_f32 v162, v251, v162\n"
"v_mfma_f32_16x16x32_bf16 v[40:43], a[104:107], v[120:123], v[40:43]\n"
"s_waitcnt lgkmcnt(3)\n"
"ds_read_b128 a[68:71], v227 offset:17408\n"
"v_mul_f32 v163, v251, v163\n"
"v_mul_f32 v164, v251, v164\n"
"v_mfma_f32_16x16x32_bf16 v[4:7], a[108:111], v[88:91], v[4:7]\n"
"s_waitcnt vmcnt(4)\n"
"buffer_store_dwordx2 v[232:233], v230, s[8:11], s61 offen sc1\n"
"v_mul_f32 v165, v251, v165\n"
"v_mfma_f32_16x16x32_bf16 v[44:47], a[108:111], v[120:123], v[44:47]\n"
"ds_read_b128 a[72:75], v227 offset:18432\n"
"v_mul_f32 v166, v251, v166\n"
"v_mul_f32 v167, v251, v167\n"
"v_mfma_f32_16x16x32_bf16 v[8:11], a[112:115], v[88:91], v[8:11]\n"
"s_add_u32 s51, s60, 16384\n"
"v_accvgpr_read_b32 v252, a144\n"
"v_subrev_u32 v253, 0x2b800000, v252\n"
"v_cvt_pk_bf16_f32 v160, v160, v161\n"
"v_mfma_f32_16x16x32_bf16 v[48:51], a[112:115], v[120:123], v[48:51]\n"
"s_lshl_b32 s62, s34, 11\n"
"ds_read_b128 a[76:79], v227 offset:19456\n"
"v_cvt_pk_bf16_f32 v161, v162, v163\n"
"v_cvt_pk_bf16_f32 v162, v164, v165\n"
"v_mfma_f32_16x16x32_bf16 v[12:15], a[116:119], v[88:91], v[12:15]\n"
"s_sub_u32 s62, s61, s62\n"
"v_cmp_le_u32 vcc, 0x34000000, v253\n"
"s_cmp_lg_u64 vcc, 0\n"
"s_cbranch_scc1 L1041_%=\n"
"L1040_%=:\n"
"v_mfma_f32_16x16x32_bf16 v[52:55], a[116:119], v[120:123], v[52:55]\n"
"s_waitcnt lgkmcnt(3)\n"
"v_rcp_f32 v252, v252\n"
"v_cvt_pk_bf16_f32 v163, v166, v167\n"
"v_mfma_f32_16x16x32_bf16 v[16:19], a[120:123], v[88:91], v[16:19]\n"
"s_add_u32 s62, s62, 15360\n"
"buffer_store_dwordx4 v[160:163], v224, s[8:11], s60 offen offset:2048 sc1\n"
"v_mul_f32 v168, v251, v168\n"
"v_mfma_f32_16x16x32_bf16 v[56:59], a[120:123], v[120:123], v[56:59]\n"
"ds_read_b128 a[80:83], v227 offset:20480\n"
"v_mul_f32 v169, v251, v169\n"
"v_mul_f32 v170, v251, v170\n"
"v_mfma_f32_16x16x32_bf16 v[20:23], a[124:127], v[88:91], v[20:23]\n"
"ds_read_b128 a[84:87], v227 offset:21504\n"
"v_mul_f32 v171, v251, v171\n"
"v_mul_f32 v172, v251, v172\n"
"v_mfma_f32_16x16x32_bf16 v[60:63], a[124:127], v[120:123], v[60:63]\n"
"ds_read_b128 a[88:91], v227 offset:22528\n"
"v_mul_f32 v173, v251, v173\n"
"v_mul_f32 v174, v251, v174\n"
"v_mfma_f32_16x16x32_bf16 v[24:27], a[64:67], v[88:91], v[24:27]\n"
"v_mul_f32 v175, v251, v175\n"
"v_cvt_pk_bf16_f32 v168, v168, v169\n"
"v_cvt_pk_bf16_f32 v169, v170, v171\n"
"v_mfma_f32_16x16x32_bf16 v[64:67], a[64:67], v[120:123], v[64:67]\n"
"s_waitcnt lgkmcnt(3)\n"
"ds_read_b128 a[92:95], v227 offset:23552\n"
"v_mul_f32 v176, v252, v176\n"
"v_mul_f32 v177, v252, v177\n"
"v_mfma_f32_16x16x32_bf16 v[28:31], a[68:71], v[88:91], v[28:31]\n"
"v_lshrrev_b32 v238, 1, v224\n"
"v_cvt_pk_bf16_f32 v170, v172, v173\n"
"v_cvt_pk_bf16_f32 v171, v174, v175\n"
"v_mfma_f32_16x16x32_bf16 v[68:71], a[68:71], v[120:123], v[68:71]\n"
"buffer_store_dwordx4 v[168:171], v224, s[8:11], s60 offen offset:3072 sc1\n"
"v_mul_f32 v178, v252, v178\n"
"v_mfma_f32_16x16x32_bf16 v[32:35], a[72:75], v[88:91], v[32:35]\n"
"ds_read_b128 a[96:99], v227 offset:24576\n"
"v_mul_f32 v179, v252, v179\n"
"v_and_b32 v238, 24, v238\n"
"v_mfma_f32_16x16x32_bf16 v[72:75], a[72:75], v[120:123], v[72:75]\n"
"ds_read_b128 a[100:103], v227 offset:25600\n"
"v_mul_f32 v180, v252, v180\n"
"v_mul_f32 v181, v252, v181\n"
"v_mfma_f32_16x16x32_bf16 v[36:39], a[76:79], v[88:91], v[36:39]\n"
"v_mul_f32 v182, v252, v182\n"
"v_mul_f32 v183, v252, v183\n"
"v_cvt_pk_bf16_f32 v176, v176, v177\n"
"v_mfma_f32_16x16x32_bf16 v[76:79], a[76:79], v[120:123], v[76:79]\n"
"s_waitcnt lgkmcnt(3)\n"
"ds_read_b128 a[104:107], v227 offset:26624\n"
"v_cvt_pk_bf16_f32 v177, v178, v179\n"
"v_cvt_pk_bf16_f32 v178, v180, v181\n"
"v_mfma_f32_16x16x32_bf16 v[0:3], a[80:83], v[96:99], v[0:3]\n"
"v_cvt_pk_bf16_f32 v179, v182, v183\n"
"v_mul_f32 v184, v252, v184\n"
"v_mul_f32 v185, v252, v185\n"
"v_mfma_f32_16x16x32_bf16 v[40:43], a[80:83], v[128:131], v[40:43]\n"
"buffer_store_dwordx4 v[176:179], v224, s[8:11], s51 offen offset:0 sc1\n"
"v_mul_f32 v186, v252, v186\n"
"v_mfma_f32_16x16x32_bf16 v[4:7], a[84:87], v[96:99], v[4:7]\n"
"ds_read_b128 a[108:111], v227 offset:27648\n"
"v_mul_f32 v187, v252, v187\n"
"v_mul_f32 v188, v252, v188\n"
"v_mfma_f32_16x16x32_bf16 v[44:47], a[84:87], v[128:131], v[44:47]\n"
"ds_read_b128 a[112:115], v227 offset:28672\n"
"v_mul_f32 v189, v252, v189\n"
"v_mul_f32 v190, v252, v190\n"
"v_mfma_f32_16x16x32_bf16 v[8:11], a[88:91], v[96:99], v[8:11]\n"
"v_mul_f32 v191, v252, v191\n"
"v_cvt_pk_bf16_f32 v184, v184, v185\n"
"v_cvt_pk_bf16_f32 v185, v186, v187\n"
"v_mfma_f32_16x16x32_bf16 v[48:51], a[88:91], v[128:131], v[48:51]\n"
"s_waitcnt lgkmcnt(3)\n"
"ds_read_b128 a[116:119], v227 offset:29696\n"
"v_cvt_pk_bf16_f32 v186, v188, v189\n"
"v_cvt_pk_bf16_f32 v187, v190, v191\n"
"v_mfma_f32_16x16x32_bf16 v[12:15], a[92:95], v[96:99], v[12:15]\n"
"buffer_store_dwordx4 v[184:187], v224, s[8:11], s51 offen offset:1024 sc1\n"
"v_mul_f32 v192, v252, v192\n"
"v_mfma_f32_16x16x32_bf16 v[52:55], a[92:95], v[128:131], v[52:55]\n"
"ds_read_b128 a[120:123], v227 offset:30720\n"
"v_mul_f32 v193, v252, v193\n"
"v_mul_f32 v194, v252, v194\n"
"v_mfma_f32_16x16x32_bf16 v[16:19], a[96:99], v[96:99], v[16:19]\n"
"s_andn2_b32 s63, s61, 31\n"
"v_mul_f32 v195, v252, v195\n"
"v_mul_f32 v196, v252, v196\n"
"v_mul_f32 v197, v252, v197\n"
"v_mfma_f32_16x16x32_bf16 v[56:59], a[96:99], v[128:131], v[56:59]\n"
"ds_read_b128 a[124:127], v227 offset:31744\n"
"v_mul_f32 v198, v252, v198\n"
"v_mul_f32 v199, v252, v199\n"
"v_mfma_f32_16x16x32_bf16 v[20:23], a[100:103], v[96:99], v[20:23]\n"
"s_waitcnt vmcnt(7)\n"
"s_barrier\n"
"v_mfma_f32_16x16x32_bf16 v[60:63], a[100:103], v[128:131], v[60:63]\n"
"s_waitcnt lgkmcnt(3)\n"
"buffer_load_dwordx2 v[240:241], v238, s[8:11], s63 offen sc1\n"
"v_cvt_pk_bf16_f32 v192, v192, v193\n"
"v_mfma_f32_16x16x32_bf16 v[24:27], a[104:107], v[96:99], v[24:27]\n"
"ds_read_b128 a[64:67], v226 offset:0\n"
"v_cvt_pk_bf16_f32 v193, v194, v195\n"
"v_cvt_pk_bf16_f32 v194, v196, v197\n"
"v_mfma_f32_16x16x32_bf16 v[64:67], a[104:107], v[128:131], v[64:67]\n"
"ds_read_b128 a[68:71], v226 offset:1024\n"
"v_cvt_pk_bf16_f32 v195, v198, v199\n"
"v_mul_f32 v200, v252, v200\n"
"v_mfma_f32_16x16x32_bf16 v[28:31], a[108:111], v[96:99], v[28:31]\n"
"buffer_store_dwordx4 v[192:195], v224, s[8:11], s51 offen offset:2048 sc1\n"
"v_mul_f32 v201, v252, v201\n"
"v_mfma_f32_16x16x32_bf16 v[68:71], a[108:111], v[128:131], v[68:71]\n"
"ds_read_b128 a[72:75], v226 offset:2048\n"
"v_readlane_b32 s43, v228, s42\n"
"v_mul_f32 v202, v252, v202\n"
"v_mfma_f32_16x16x32_bf16 v[32:35], a[112:115], v[96:99], v[32:35]\n"
"v_mul_f32 v203, v252, v203\n"
"v_mul_f32 v204, v252, v204\n"
"v_mul_f32 v205, v252, v205\n"
"v_mfma_f32_16x16x32_bf16 v[72:75], a[112:115], v[128:131], v[72:75]\n"
"s_waitcnt lgkmcnt(3)\n"
"ds_read_b128 a[76:79], v226 offset:3072\n"
"v_mul_f32 v206, v252, v206\n"
"v_mul_f32 v207, v252, v207\n"
"v_mfma_f32_16x16x32_bf16 v[36:39], a[116:119], v[96:99], v[36:39]\n"
"s_mov_b32 m0, s40\n"
"v_cvt_pk_bf16_f32 v200, v200, v201\n"
"v_cvt_pk_bf16_f32 v201, v202, v203\n"
"v_cvt_pk_bf16_f32 v202, v204, v205\n"
"v_mfma_f32_16x16x32_bf16 v[76:79], a[116:119], v[128:131], v[76:79]\n"
"ds_read_b128 a[80:83], v226 offset:4096\n"
"v_cvt_pk_bf16_f32 v203, v206, v207\n"
"v_mfma_f32_16x16x32_bf16 v[0:3], a[120:123], v[104:107], v[0:3]\n"
"buffer_store_dwordx4 v[200:203], v224, s[8:11], s51 offen offset:3072 sc1\n"
"v_mfma_f32_16x16x32_bf16 v[40:43], a[120:123], v[136:139], v[40:43]\n"
"ds_read_b128 a[84:87], v226 offset:5120\n"
"v_readlane_b32 s44, v229, s42\n"
"v_mfma_f32_16x16x32_bf16 v[4:7], a[124:127], v[104:107], v[4:7]\n"
"s_add_u32 s42, s42, 1\n"
"buffer_load_dwordx4 v225, s[8:11], s43 offen offset:0 sc1 lds\n"
"v_mfma_f32_16x16x32_bf16 v[44:47], a[124:127], v[136:139], v[44:47]\n"
"s_waitcnt lgkmcnt(3)\n"
"ds_read_b128 a[88:91], v226 offset:6144\n"
"v_mfma_f32_16x16x32_bf16 v[8:11], a[64:67], v[104:107], v[8:11]\n"
"v_mfma_f32_16x16x32_bf16 v[48:51], a[64:67], v[136:139], v[48:51]\n"
"ds_read_b128 a[92:95], v226 offset:7168\n"
"v_mfma_f32_16x16x32_bf16 v[12:15], a[68:71], v[104:107], v[12:15]\n"
"buffer_load_dwordx4 v225, s[8:11], s43 offen offset:1024 sc1 lds\n"
"v_mfma_f32_16x16x32_bf16 v[52:55], a[68:71], v[136:139], v[52:55]\n"
"ds_read_b128 a[96:99], v227 offset:0\n"
"v_mfma_f32_16x16x32_bf16 v[16:19], a[72:75], v[104:107], v[16:19]\n"
"v_mfma_f32_16x16x32_bf16 v[56:59], a[72:75], v[136:139], v[56:59]\n"
"s_waitcnt lgkmcnt(3)\n"
"ds_read_b128 a[100:103], v227 offset:1024\n"
"v_mfma_f32_16x16x32_bf16 v[20:23], a[76:79], v[104:107], v[20:23]\n"
"buffer_load_dwordx4 v225, s[8:11], s43 offen offset:2048 sc1 lds\n"
"v_mfma_f32_16x16x32_bf16 v[60:63], a[76:79], v[136:139], v[60:63]\n"
"ds_read_b128 a[104:107], v227 offset:2048\n"
"v_mfma_f32_16x16x32_bf16 v[24:27], a[80:83], v[104:107], v[24:27]\n"
"v_mfma_f32_16x16x32_bf16 v[64:67], a[80:83], v[136:139], v[64:67]\n"
"ds_read_b128 a[108:111], v227 offset:3072\n"
"v_mfma_f32_16x16x32_bf16 v[28:31], a[84:87], v[104:107], v[28:31]\n"
"buffer_load_dwordx4 v225, s[8:11], s43 offen offset:3072 sc1 lds\n"
"v_mfma_f32_16x16x32_bf16 v[68:71], a[84:87], v[136:139], v[68:71]\n"
"s_waitcnt lgkmcnt(3)\n"
"ds_read_b128 a[112:115], v227 offset:4096\n"
"s_add_u32 m0, s40, 16384\n"
"v_mfma_f32_16x16x32_bf16 v[32:35], a[88:91], v[104:107], v[32:35]\n"
"v_mfma_f32_16x16x32_bf16 v[72:75], a[88:91], v[136:139], v[72:75]\n"
"ds_read_b128 a[116:119], v227 offset:5120\n"
"v_mfma_f32_16x16x32_bf16 v[36:39], a[92:95], v[104:107], v[36:39]\n"
"buffer_load_dwordx4 v225, s[8:11], s44 offen offset:0 sc1 lds\n"
"v_mfma_f32_16x16x32_bf16 v[76:79], a[92:95], v[136:139], v[76:79]\n"
"s_add_u32 s40, s40, 32768\n"
"ds_read_b128 a[120:123], v227 offset:6144\n"
"v_mfma_f32_16x16x32_bf16 v[84:87], a[96:99], v[144:147], 0\n"
"s_cmp_eq_u32 s40, s46\n"
"s_cselect_b32 s40, s47, s40\n"
"v_mfma_f32_16x16x32_bf16 v[164:167], a[96:99], v[176:179], 0\n"
"s_waitcnt lgkmcnt(3)\n"
"ds_read_b128 a[124:127], v227 offset:7168\n"
"v_mfma_f32_16x16x32_bf16 v[92:95], a[100:103], v[144:147], 0\n"
"buffer_load_dwordx4 v225, s[8:11], s44 offen offset:1024 sc1 lds\n"
"v_mfma_f32_16x16x32_bf16 v[172:175], a[100:103], v[176:179], 0\n"
"ds_read_b128 a[64:67], v227 offset:8192\n"
"v_mfma_f32_16x16x32_bf16 v[100:103], a[104:107], v[144:147], 0\n"
"v_mfma_f32_16x16x32_bf16 v[180:183], a[104:107], v[176:179], 0\n"
"ds_read_b128 a[68:71], v227 offset:9216\n"
"v_mfma_f32_16x16x32_bf16 v[108:111], a[108:111], v[144:147], 0\n"
"buffer_load_dwordx4 v225, s[8:11], s44 offen offset:2048 sc1 lds\n"
"v_mfma_f32_16x16x32_bf16 v[188:191], a[108:111], v[176:179], 0\n"
"s_waitcnt lgkmcnt(3)\n"
"ds_read_b128 a[72:75], v227 offset:10240\n"
"v_readlane_b32 s43, v228, s42\n"
"v_mfma_f32_16x16x32_bf16 v[116:119], a[112:115], v[144:147], 0\n"
"v_mfma_f32_16x16x32_bf16 v[196:199], a[112:115], v[176:179], 0\n"
"s_add_u32 s48, s59, 32768\n"
"ds_read_b128 a[76:79], v227 offset:11264\n"
"v_mfma_f32_16x16x32_bf16 v[124:127], a[116:119], v[144:147], 0\n"
"s_and_b32 s48, s48, 0x18000\n"
"buffer_load_dwordx4 v225, s[8:11], s44 offen offset:3072 sc1 lds\n"
"v_mfma_f32_16x16x32_bf16 v[204:207], a[116:119], v[176:179], 0\n"
"s_mov_b32 m0, s40\n"
"ds_read_b128 a[80:83], v227 offset:12288\n"
"s_andn2_b32 s49, s59, 0x18000\n"
"v_mfma_f32_16x16x32_bf16 v[132:135], a[120:123], v[144:147], 0\n"
"s_or_b32 s52, s48, s49\n"
"s_waitcnt vmcnt(10)\n"
"v_cmp_eq_u64 vcc, s[28:29], v[240:241]\n"
"s_cmp_eq_u64 vcc, exec\n"
"s_cbranch_scc0 L1046_%=\n"
"L1047_%=:\n"
"v_mfma_f32_16x16x32_bf16 v[208:211], a[120:123], v[176:179], 0\n"
"s_waitcnt lgkmcnt(3)\n"
"ds_read_b128 a[84:87], v227 offset:13312\n"
"v_readlane_b32 s44, v229, s42\n"
"v_mfma_f32_16x16x32_bf16 v[140:143], a[124:127], v[144:147], 0\n"
"s_add_u32 s53, s52, 16384\n"
"buffer_load_dwordx4 v225, s[8:11], s43 offen offset:0 sc1 lds\n"
"v_mfma_f32_16x16x32_bf16 v[212:215], a[124:127], v[176:179], 0\n"
"s_add_u32 s42, s42, 1\n"
"buffer_load_dwordx4 a[0:3], v224, s[8:11], s52 offen offset:0 sc1\n"
"v_mfma_f32_16x16x32_bf16 v[148:151], a[64:67], v[144:147], 0\n"
"ds_read_b128 a[88:91], v227 offset:14336\n"
"v_mfma_f32_16x16x32_bf16 v[216:219], a[64:67], v[176:179], 0\n"
"ds_read_b128 a[92:95], v227 offset:15360\n"
"v_mfma_f32_16x16x32_bf16 v[156:159], a[68:71], v[144:147], 0\n"
"buffer_load_dwordx4 v225, s[8:11], s43 offen offset:1024 sc1 lds\n"
"v_mfma_f32_16x16x32_bf16 v[220:223], a[68:71], v[176:179], 0\n"
"s_waitcnt lgkmcnt(3)\n"
"ds_read_b128 a[96:99], v227 offset:16384\n"
"v_mfma_f32_16x16x32_bf16 v[84:87], a[72:75], v[152:155], v[84:87]\n"
"buffer_load_dwordx4 a[4:7], v224, s[8:11], s53 offen offset:0 sc1\n"
"v_mfma_f32_16x16x32_bf16 v[164:167], a[72:75], v[184:187], v[164:167]\n"
"ds_read_b128 a[100:103], v227 offset:17408\n"
"v_mfma_f32_16x16x32_bf16 v[92:95], a[76:79], v[152:155], v[92:95]\n"
"buffer_load_dwordx4 v225, s[8:11], s43 offen offset:2048 sc1 lds\n"
"v_mfma_f32_16x16x32_bf16 v[172:175], a[76:79], v[184:187], v[172:175]\n"
"ds_read_b128 a[104:107], v227 offset:18432\n"
"v_mfma_f32_16x16x32_bf16 v[100:103], a[80:83], v[152:155], v[100:103]\n"
"buffer_load_dwordx4 a[8:11], v224, s[8:11], s52 offen offset:1024 sc1\n"
"v_mfma_f32_16x16x32_bf16 v[180:183], a[80:83], v[184:187], v[180:183]\n"
"s_waitcnt lgkmcnt(3)\n"
"ds_read_b128 a[108:111], v227 offset:19456\n"
"v_mfma_f32_16x16x32_bf16 v[108:111], a[84:87], v[152:155], v[108:111]\n"
"buffer_load_dwordx4 v225, s[8:11], s43 offen offset:3072 sc1 lds\n"
"v_mfma_f32_16x16x32_bf16 v[188:191], a[84:87], v[184:187], v[188:191]\n"
"s_add_u32 m0, s40, 16384\n"
"ds_read_b128 a[112:115], v227 offset:20480\n"
"v_mfma_f32_16x16x32_bf16 v[116:119], a[88:91], v[152:155], v[116:119]\n"
"v_mfma_f32_16x16x32_bf16 v[196:199], a[88:91], v[184:187], v[196:199]\n"
"ds_read_b128 a[116:119], v227 offset:21504\n"
"v_mfma_f32_16x16x32_bf16 v[124:127], a[92:95], v[152:155], v[124:127]\n"
"buffer_load_dwordx4 a[12:15], v224, s[8:11], s53 offen offset:1024 sc1\n"
"v_mfma_f32_16x16x32_bf16 v[204:207], a[92:95], v[184:187], v[204:207]\n"
"s_waitcnt lgkmcnt(3)\n"
"buffer_load_dwordx4 v225, s[8:11], s44 offen offset:0 sc1 lds\n"
"v_mfma_f32_16x16x32_bf16 v[132:135], a[96:99], v[152:155], v[132:135]\n"
"s_add_u32 s40, s40, 32768\n"
"ds_read_b128 a[120:123], v227 offset:22528\n"
"s_cmp_eq_u32 s40, s46\n"
"v_mfma_f32_16x16x32_bf16 v[208:211], a[96:99], v[184:187], v[208:211]\n"
"s_cselect_b32 s40, s47, s40\n"
"ds_read_b128 a[124:127], v227 offset:23552\n"
"v_mfma_f32_16x16x32_bf16 v[140:143], a[100:103], v[152:155], v[140:143]\n"
"buffer_load_dwordx4 v225, s[8:11], s44 offen offset:1024 sc1 lds\n"
"v_mfma_f32_16x16x32_bf16 v[212:215], a[100:103], v[184:187], v[212:215]\n"
"buffer_load_dwordx4 a[16:19], v224, s[8:11], s52 offen offset:2048 sc1\n"
"v_mfma_f32_16x16x32_bf16 v[148:151], a[104:107], v[152:155], v[148:151]\n"
"ds_read_b128 a[64:67], v227 offset:24576\n"
"v_mfma_f32_16x16x32_bf16 v[216:219], a[104:107], v[184:187], v[216:219]\n"
"s_waitcnt lgkmcnt(3)\n"
"ds_read_b128 a[68:71], v227 offset:25600\n"
"v_mfma_f32_16x16x32_bf16 v[156:159], a[108:111], v[152:155], v[156:159]\n"
"buffer_load_dwordx4 v225, s[8:11], s44 offen offset:2048 sc1 lds\n"
"v_mfma_f32_16x16x32_bf16 v[220:223], a[108:111], v[184:187], v[220:223]\n"
"ds_read_b128 a[72:75], v227 offset:26624\n"
"v_readlane_b32 s43, v228, s42\n"
"v_mfma_f32_16x16x32_bf16 v[84:87], a[112:115], v[160:163], v[84:87]\n"
"s_lshr_b32 s89, s31, 3\n"
"buffer_load_dwordx4 a[20:23], v224, s[8:11], s53 offen offset:2048 sc1\n"
"v_mfma_f32_16x16x32_bf16 v[164:167], a[112:115], v[192:195], v[164:167]\n"
"s_waitcnt vmcnt(21)\n"
"buffer_store_dwordx2 v[232:233], v230, s[8:11], s62 offen sc1\n"
"v_mfma_f32_16x16x32_bf16 v[92:95], a[116:119], v[160:163], v[92:95]\n"
"s_lshl_b32 s89, s89, 4\n"
"ds_read_b128 a[76:79], v227 offset:27648\n"
"s_add_u32 s89, s89, s30\n"
"v_mfma_f32_16x16x32_bf16 v[172:175], a[116:119], v[192:195], v[172:175]\n"
"s_waitcnt lgkmcnt(3)\n"
"buffer_load_dwordx4 v225, s[8:11], s44 offen offset:3072 sc1 lds\n"
"v_mfma_f32_16x16x32_bf16 v[100:103], a[120:123], v[160:163], v[100:103]\n"
"s_mov_b32 m0, s40\n"
"ds_read_b128 a[80:83], v227 offset:28672\n"
"v_readlane_b32 s44, v229, s42\n"
"v_mfma_f32_16x16x32_bf16 v[180:183], a[120:123], v[192:195], v[180:183]\n"
"s_lshl_b32 s89, s89, 3\n"
"buffer_load_dwordx4 a[24:27], v224, s[8:11], s52 offen offset:3072 sc1\n"
"v_mfma_f32_16x16x32_bf16 v[108:111], a[124:127], v[160:163], v[108:111]\n"
"s_add_u32 s89, s89, s32\n"
"ds_read_b128 a[84:87], v227 offset:29696\n"
"s_mul_i32 s88, s89, 5120\n"
"v_mfma_f32_16x16x32_bf16 v[188:191], a[124:127], v[192:195], v[188:191]\n"
"s_add_u32 s42, s42, 1\n"
"buffer_load_dwordx4 v225, s[8:11], s43 offen offset:0 sc1 lds\n"
"v_mfma_f32_16x16x32_bf16 v[116:119], a[64:67], v[160:163], v[116:119]\n"
"s_add_u32 s88, s88, 60817408\n"
"ds_read_b128 a[88:91], v227 offset:30720\n"
"s_lshl_b32 s89, s89, 3\n"
"v_mfma_f32_16x16x32_bf16 v[196:199], a[64:67], v[192:195], v[196:199]\n"
"s_waitcnt lgkmcnt(3)\n"
"ds_read_b128 a[92:95], v227 offset:31744\n"
"v_lshlrev_b32 v239, 4, v224\n"
"v_mfma_f32_16x16x32_bf16 v[124:127], a[68:71], v[160:163], v[124:127]\n"
"s_add_u32 s89, s89, 23296\n"
"buffer_load_dwordx4 a[28:31], v224, s[8:11], s53 offen offset:3072 sc1\n"
"v_mfma_f32_16x16x32_bf16 v[204:207], a[68:71], v[192:195], v[204:207]\n"
"s_add_u32 s48, s59, 65536\n"
"buffer_load_dwordx4 v225, s[8:11], s43 offen offset:1024 sc1 lds\n"
"v_mfma_f32_16x16x32_bf16 v[132:135], a[72:75], v[160:163], v[132:135]\n"
"s_and_b32 s48, s48, 0x18000\n"
"ds_read_b128 a[96:99], v226 offset:0\n"
"v_and_b32 v242, 0x3e0, v224\n"
"v_mfma_f32_16x16x32_bf16 v[208:211], a[72:75], v[192:195], v[208:211]\n"
"s_andn2_b32 s49, s59, 0x18000\n"
"ds_read_b128 a[100:103], v226 offset:1024\n"
"v_lshl_add_u32 v239, v242, 22, v239\n"
"v_mfma_f32_16x16x32_bf16 v[140:143], a[76:79], v[160:163], v[140:143]\n"
"s_or_b32 s52, s48, s49\n"
"buffer_load_dwordx4 v225, s[8:11], s43 offen offset:2048 sc1 lds\n"
"v_mfma_f32_16x16x32_bf16 v[212:215], a[76:79], v[192:195], v[212:215]\n"
"s_waitcnt lgkmcnt(3)\n"
"buffer_load_dwordx4 a[32:35], v224, s[8:11], s52 offen offset:0 sc1\n"
"v_mfma_f32_16x16x32_bf16 v[148:151], a[80:83], v[160:163], v[148:151]\n"
"s_add_u32 s53, s52, 16384\n"
"ds_read_b128 a[104:107], v226 offset:2048\n"
"v_mfma_f32_16x16x32_bf16 v[216:219], a[80:83], v[192:195], v[216:219]\n"
"ds_read_b128 a[108:111], v226 offset:3072\n"
"v_mfma_f32_16x16x32_bf16 v[156:159], a[84:87], v[160:163], v[156:159]\n"
"buffer_load_dwordx4 v225, s[8:11], s43 offen offset:3072 sc1 lds\n"
"v_mfma_f32_16x16x32_bf16 v[220:223], a[84:87], v[192:195], v[220:223]\n"
"s_add_u32 m0, s40, 16384\n"
"ds_read_b128 a[112:115], v226 offset:4096\n"
"v_mfma_f32_16x16x32_bf16 v[84:87], a[88:91], v[168:171], v[84:87]\n"
"buffer_load_dwordx4 a[36:39], v224, s[8:11], s53 offen offset:0 sc1\n"
"v_mfma_f32_16x16x32_bf16 v[164:167], a[88:91], v[200:203], v[164:167]\n"
"s_waitcnt lgkmcnt(3)\n"
"ds_read_b128 a[116:119], v226 offset:5120\n"
"v_mfma_f32_16x16x32_bf16 v[92:95], a[92:95], v[168:171], v[92:95]\n"
"buffer_load_dwordx4 v225, s[8:11], s44 offen offset:0 sc1 lds\n"
"v_mfma_f32_16x16x32_bf16 v[172:175], a[92:95], v[200:203], v[172:175]\n"
"s_add_u32 s40, s40, 32768\n"
"ds_read_b128 a[120:123], v226 offset:6144\n"
"v_mfma_f32_16x16x32_bf16 v[100:103], a[96:99], v[168:171], v[100:103]\n"
"s_cmp_eq_u32 s40, s46\n"
"buffer_load_dwordx4 a[40:43], v224, s[8:11], s52 offen offset:1024 sc1\n"
"v_mfma_f32_16x16x32_bf16 v[180:183], a[96:99], v[200:203], v[180:183]\n"
"s_cselect_b32 s40, s47, s40\n"
"ds_read_b128 a[124:127], v226 offset:7168\n"
"v_mfma_f32_16x16x32_bf16 v[108:111], a[100:103], v[168:171], v[108:111]\n"
"buffer_load_dwordx4 v225, s[8:11], s44 offen offset:1024 sc1 lds\n"
"v_mfma_f32_16x16x32_bf16 v[188:191], a[100:103], v[200:203], v[188:191]\n"
"s_waitcnt lgkmcnt(3)\n"
"ds_read_b128 a[64:67], v226 offset:8192\n"
"v_mfma_f32_16x16x32_bf16 v[116:119], a[104:107], v[168:171], v[116:119]\n"
"v_mfma_f32_16x16x32_bf16 v[196:199], a[104:107], v[200:203], v[196:199]\n"
"ds_read_b128 a[68:71], v226 offset:9216\n"
"v_mfma_f32_16x16x32_bf16 v[124:127], a[108:111], v[168:171], v[124:127]\n"
"buffer_load_dwordx4 a[44:47], v224, s[8:11], s53 offen offset:1024 sc1\n"
"v_mfma_f32_16x16x32_bf16 v[204:207], a[108:111], v[200:203], v[204:207]\n"
"buffer_load_dwordx4 v225, s[8:11], s44 offen offset:2048 sc1 lds\n"
"v_mfma_f32_16x16x32_bf16 v[132:135], a[112:115], v[168:171], v[132:135]\n"
"ds_read_b128 a[72:75], v226 offset:10240\n"
"v_mfma_f32_16x16x32_bf16 v[208:211], a[112:115], v[200:203], v[208:211]\n"
"s_waitcnt lgkmcnt(3)\n"
"ds_read_b128 a[76:79], v226 offset:11264\n"
"v_mfma_f32_16x16x32_bf16 v[140:143], a[116:119], v[168:171], v[140:143]\n"
"buffer_load_dwordx4 v225, s[8:11], s44 offen offset:3072 sc1 lds\n"
"v_mfma_f32_16x16x32_bf16 v[212:215], a[116:119], v[200:203], v[212:215]\n"
"buffer_load_dwordx4 a[48:51], v224, s[8:11], s52 offen offset:2048 sc1\n"
"v_mfma_f32_16x16x32_bf16 v[148:151], a[120:123], v[168:171], v[148:151]\n"
"ds_read_b128 a[80:83], v226 offset:12288\n"
"v_mfma_f32_16x16x32_bf16 v[216:219], a[120:123], v[200:203], v[216:219]\n"
"ds_read_b128 a[84:87], v226 offset:13312\n"
"v_mfma_f32_16x16x32_bf16 v[156:159], a[124:127], v[168:171], v[156:159]\n"
"v_mfma_f32_16x16x32_bf16 v[220:223], a[124:127], v[200:203], v[220:223]\n"
"s_waitcnt vmcnt(28) lgkmcnt(3)\n"
"ds_read_b128 a[88:91], v226 offset:14336\n"
"v_mfma_f32_16x16x32_bf16 v[0:3], a[64:67], a[0:3], v[0:3]\n"
"s_waitcnt vmcnt(26)\n"
"buffer_load_dwordx4 a[52:55], v224, s[8:11], s53 offen offset:2048 sc1\n"
"v_mfma_f32_16x16x32_bf16 v[40:43], a[64:67], a[4:7], v[40:43]\n"
"ds_read_b128 a[92:95], v226 offset:15360\n"
"v_mfma_f32_16x16x32_bf16 v[4:7], a[68:71], a[0:3], v[4:7]\n"
"v_mfma_f32_16x16x32_bf16 v[44:47], a[68:71], a[4:7], v[44:47]\n"
"ds_read_b128 a[96:99], v226 offset:16384\n"
"v_mfma_f32_16x16x32_bf16 v[8:11], a[72:75], a[0:3], v[8:11]\n"
"buffer_load_dwordx4 a[56:59], v224, s[8:11], s52 offen offset:3072 sc1\n"
"v_mfma_f32_16x16x32_bf16 v[48:51], a[72:75], a[4:7], v[48:51]\n"
"s_waitcnt lgkmcnt(3)\n"
"ds_read_b128 a[100:103], v226 offset:17408\n"
"v_mfma_f32_16x16x32_bf16 v[12:15], a[76:79], a[0:3], v[12:15]\n"
"v_mfma_f32_16x16x32_bf16 v[52:55], a[76:79], a[4:7], v[52:55]\n"
"ds_read_b128 a[104:107], v226 offset:18432\n"
"v_mfma_f32_16x16x32_bf16 v[16:19], a[80:83], a[0:3], v[16:19]\n"
"v_mfma_f32_16x16x32_bf16 v[56:59], a[80:83], a[4:7], v[56:59]\n"
"s_add_u32 s48, s59, 98304\n"
"ds_read_b128 a[108:111], v226 offset:19456\n"
"v_mfma_f32_16x16x32_bf16 v[20:23], a[84:87], a[0:3], v[20:23]\n"
"s_and_b32 s48, s48, 0x18000\n"
"buffer_load_dwordx4 a[60:63], v224, s[8:11], s53 offen offset:3072 sc1\n"
"v_mfma_f32_16x16x32_bf16 v[60:63], a[84:87], a[4:7], v[60:63]\n"
"s_waitcnt lgkmcnt(3)\n"
"ds_read_b128 a[112:115], v226 offset:20480\n"
"s_andn2_b32 s49, s59, 0x18000\n"
"v_mfma_f32_16x16x32_bf16 v[24:27], a[88:91], a[0:3], v[24:27]\n"
"s_or_b32 s52, s48, s49\n"
"s_add_u32 s53, s52, 16384\n"
"v_mfma_f32_16x16x32_bf16 v[64:67], a[88:91], a[4:7], v[64:67]\n"
"ds_read_b128 a[116:119], v226 offset:21504\n"
"v_mfma_f32_16x16x32_bf16 v[28:31], a[92:95], a[0:3], v[28:31]\n"
"buffer_load_dwordx4 a[180:183], v224, s[8:11], s52 offen offset:0 sc1\n"
"v_mfma_f32_16x16x32_bf16 v[68:71], a[92:95], a[4:7], v[68:71]\n"
"ds_read_b128 a[120:123], v226 offset:22528\n"
"v_mfma_f32_16x16x32_bf16 v[32:35], a[96:99], a[0:3], v[32:35]\n"
"v_mfma_f32_16x16x32_bf16 v[72:75], a[96:99], a[4:7], v[72:75]\n"
"s_waitcnt lgkmcnt(3)\n"
"ds_read_b128 a[124:127], v226 offset:23552\n"
"v_mfma_f32_16x16x32_bf16 v[36:39], a[100:103], a[0:3], v[36:39]\n"
"v_mfma_f32_16x16x32_bf16 v[76:79], a[100:103], a[4:7], v[76:79]\n"
"s_waitcnt vmcnt(28)\n"
"ds_read_b128 a[64:67], v226 offset:24576\n"
"v_mfma_f32_16x16x32_bf16 v[0:3], a[104:107], a[8:11], v[0:3]\n"
"s_waitcnt vmcnt(26)\n"
"buffer_load_dwordx4 a[184:187], v224, s[8:11], s53 offen offset:0 sc1\n"
"v_mfma_f32_16x16x32_bf16 v[40:43], a[104:107], a[12:15], v[40:43]\n"
"ds_read_b128 a[68:71], v226 offset:25600\n"
"v_mfma_f32_16x16x32_bf16 v[4:7], a[108:111], a[8:11], v[4:7]\n"
"v_mfma_f32_16x16x32_bf16 v[44:47], a[108:111], a[12:15], v[44:47]\n"
"s_waitcnt lgkmcnt(3)\n"
"ds_read_b128 a[72:75], v226 offset:26624\n"
"v_mfma_f32_16x16x32_bf16 v[8:11], a[112:115], a[8:11], v[8:11]\n"
"buffer_load_dwordx4 a[188:191], v224, s[8:11], s52 offen offset:1024 sc1\n"
"v_mfma_f32_16x16x32_bf16 v[48:51], a[112:115], a[12:15], v[48:51]\n"
"ds_read_b128 a[76:79], v226 offset:27648\n"
"v_mfma_f32_16x16x32_bf16 v[12:15], a[116:119], a[8:11], v[12:15]\n"
"v_mfma_f32_16x16x32_bf16 v[52:55], a[116:119], a[12:15], v[52:55]\n"
"ds_read_b128 a[80:83], v226 offset:28672\n"
"v_mfma_f32_16x16x32_bf16 v[16:19], a[120:123], a[8:11], v[16:19]\n"
"v_mfma_f32_16x16x32_bf16 v[56:59], a[120:123], a[12:15], v[56:59]\n"
"s_waitcnt lgkmcnt(3)\n"
"ds_read_b128 a[84:87], v226 offset:29696\n"
"v_mfma_f32_16x16x32_bf16 v[20:23], a[124:127], a[8:11], v[20:23]\n"
"buffer_load_dwordx4 a[192:195], v224, s[8:11], s53 offen offset:1024 sc1\n"
"v_mfma_f32_16x16x32_bf16 v[60:63], a[124:127], a[12:15], v[60:63]\n"
"ds_read_b128 a[88:91], v226 offset:30720\n"
"v_mfma_f32_16x16x32_bf16 v[24:27], a[64:67], a[8:11], v[24:27]\n"
"v_mfma_f32_16x16x32_bf16 v[64:67], a[64:67], a[12:15], v[64:67]\n"
"ds_read_b128 a[92:95], v226 offset:31744\n"
"v_mfma_f32_16x16x32_bf16 v[28:31], a[68:71], a[8:11], v[28:31]\n"
"s_barrier\n"
"v_mfma_f32_16x16x32_bf16 v[68:71], a[68:71], a[12:15], v[68:71]\n"
"s_waitcnt lgkmcnt(3)\n"
"buffer_load_dwordx4 a[196:199], v224, s[8:11], s52 offen offset:2048 sc1\n"
"v_mfma_f32_16x16x32_bf16 v[32:35], a[72:75], a[8:11], v[32:35]\n"
"ds_read_b128 a[96:99], v235 offset:0\n"
"v_mfma_f32_16x16x32_bf16 v[72:75], a[72:75], a[12:15], v[72:75]\n"
"ds_read_b128 a[100:103], v235 offset:1024\n"
"v_mfma_f32_16x16x32_bf16 v[36:39], a[76:79], a[8:11], v[36:39]\n"
"s_andn2_b32 s63, s62, 31\n"
"v_mfma_f32_16x16x32_bf16 v[76:79], a[76:79], a[12:15], v[76:79]\n"
"s_waitcnt vmcnt(27)\n"
"ds_read_b128 a[104:107], v235 offset:2048\n"
"v_mfma_f32_16x16x32_bf16 v[0:3], a[80:83], a[16:19], v[0:3]\n"
"s_waitcnt vmcnt(25)\n"
"buffer_load_dwordx4 a[200:203], v224, s[8:11], s53 offen offset:2048 sc1\n"
"v_mfma_f32_16x16x32_bf16 v[40:43], a[80:83], a[20:23], v[40:43]\n"
"s_waitcnt lgkmcnt(3)\n"
"buffer_load_dwordx2 v[240:241], v238, s[8:11], s63 offen sc1\n"
"v_mfma_f32_16x16x32_bf16 v[4:7], a[84:87], a[16:19], v[4:7]\n"
"ds_read_b128 a[108:111], v235 offset:3072\n"
"v_mfma_f32_16x16x32_bf16 v[44:47], a[84:87], a[20:23], v[44:47]\n"
"ds_read_b128 a[112:115], v235 offset:4096\n"
"v_mfma_f32_16x16x32_bf16 v[8:11], a[88:91], a[16:19], v[8:11]\n"
"buffer_load_dwordx4 a[204:207], v224, s[8:11], s52 offen offset:3072 sc1\n"
"v_mfma_f32_16x16x32_bf16 v[48:51], a[88:91], a[20:23], v[48:51]\n"
"ds_read_b128 a[116:119], v235 offset:5120\n"
"v_mfma_f32_16x16x32_bf16 v[12:15], a[92:95], a[16:19], v[12:15]\n"
"v_mfma_f32_16x16x32_bf16 v[52:55], a[92:95], a[20:23], v[52:55]\n"
"s_waitcnt lgkmcnt(3)\n"
"ds_read_b128 a[120:123], v235 offset:6144\n"
"v_mfma_f32_16x16x32_bf16 v[16:19], a[96:99], a[16:19], v[16:19]\n"
"v_mfma_f32_16x16x32_bf16 v[56:59], a[96:99], a[20:23], v[56:59]\n"
"ds_read_b128 a[124:127], v235 offset:7168\n"
"v_mfma_f32_16x16x32_bf16 v[20:23], a[100:103], a[16:19], v[20:23]\n"
"buffer_load_dwordx4 a[208:211], v224, s[8:11], s53 offen offset:3072 sc1\n"
"v_mfma_f32_16x16x32_bf16 v[60:63], a[100:103], a[20:23], v[60:63]\n"
"ds_read_b128 a[64:67], v235 offset:8192\n"
"v_mfma_f32_16x16x32_bf16 v[24:27], a[104:107], a[16:19], v[24:27]\n"
"v_mfma_f32_16x16x32_bf16 v[64:67], a[104:107], a[20:23], v[64:67]\n"
"s_waitcnt lgkmcnt(3)\n"
"ds_read_b128 a[68:71], v235 offset:9216\n"
"v_mfma_f32_16x16x32_bf16 v[28:31], a[108:111], a[16:19], v[28:31]\n"
"v_mfma_f32_16x16x32_bf16 v[68:71], a[108:111], a[20:23], v[68:71]\n"
"ds_read_b128 a[72:75], v235 offset:10240\n"
"v_mfma_f32_16x16x32_bf16 v[32:35], a[112:115], a[16:19], v[32:35]\n"
"v_mfma_f32_16x16x32_bf16 v[72:75], a[112:115], a[20:23], v[72:75]\n"
"ds_read_b128 a[76:79], v235 offset:11264\n"
"v_mfma_f32_16x16x32_bf16 v[36:39], a[116:119], a[16:19], v[36:39]\n"
"v_mfma_f32_16x16x32_bf16 v[76:79], a[116:119], a[20:23], v[76:79]\n"
"s_waitcnt vmcnt(26) lgkmcnt(3)\n"
"ds_read_b128 a[80:83], v235 offset:12288\n"
"v_mfma_f32_16x16x32_bf16 v[0:3], a[120:123], a[24:27], v[0:3]\n"
"s_waitcnt vmcnt(24)\n"
"v_mfma_f32_16x16x32_bf16 v[40:43], a[120:123], a[28:31], v[40:43]\n"
"ds_read_b128 a[84:87], v235 offset:13312\n"
"v_mfma_f32_16x16x32_bf16 v[4:7], a[124:127], a[24:27], v[4:7]\n"
"v_mfma_f32_16x16x32_bf16 v[44:47], a[124:127], a[28:31], v[44:47]\n"
"ds_read_b128 a[88:91], v235 offset:14336\n"
"v_mfma_f32_16x16x32_bf16 v[8:11], a[64:67], a[24:27], v[8:11]\n"
"v_mfma_f32_16x16x32_bf16 v[48:51], a[64:67], a[28:31], v[48:51]\n"
"s_waitcnt lgkmcnt(3)\n"
"ds_read_b128 a[92:95], v235 offset:15360\n"
"v_mfma_f32_16x16x32_bf16 v[12:15], a[68:71], a[24:27], v[12:15]\n"
"v_mfma_f32_16x16x32_bf16 v[52:55], a[68:71], a[28:31], v[52:55]\n"
"ds_read_b128 a[96:99], v235 offset:16384\n"
"v_mfma_f32_16x16x32_bf16 v[16:19], a[72:75], a[24:27], v[16:19]\n"
"v_mfma_f32_16x16x32_bf16 v[56:59], a[72:75], a[28:31], v[56:59]\n"
"ds_read_b128 a[100:103], v235 offset:17408\n"
"v_mfma_f32_16x16x32_bf16 v[20:23], a[76:79], a[24:27], v[20:23]\n"
"v_mfma_f32_16x16x32_bf16 v[60:63], a[76:79], a[28:31], v[60:63]\n"
"s_waitcnt lgkmcnt(3)\n"
"ds_read_b128 a[104:107], v235 offset:26624\n"
"v_mfma_f32_16x16x32_bf16 v[24:27], a[80:83], a[24:27], v[24:27]\n"
"v_mfma_f32_16x16x32_bf16 v[64:67], a[80:83], a[28:31], v[64:67]\n"
"ds_read_b128 a[108:111], v235 offset:27648\n"
"v_mfma_f32_16x16x32_bf16 v[28:31], a[84:87], a[24:27], v[28:31]\n"
"s_waitcnt vmcnt(13)\n"
"s_barrier\n"
"v_mfma_f32_16x16x32_bf16 v[68:71], a[84:87], a[28:31], v[68:71]\n"
"ds_read_b128 a[112:115], v236 offset:4096\n"
"v_mfma_f32_16x16x32_bf16 v[32:35], a[88:91], a[24:27], v[32:35]\n"
"v_mfma_f32_16x16x32_bf16 v[72:75], a[88:91], a[28:31], v[72:75]\n"
"s_waitcnt lgkmcnt(3)\n"
"ds_read_b128 a[116:119], v236 offset:5120\n"
"v_mfma_f32_16x16x32_bf16 v[36:39], a[92:95], a[24:27], v[36:39]\n"
"v_mfma_f32_16x16x32_bf16 v[76:79], a[92:95], a[28:31], v[76:79]\n"
"ds_read_b128 a[120:123], v236 offset:14336\n"
"v_mfma_f32_16x16x32_bf16 v[0:3], a[96:99], a[32:35], v[0:3]\n"
"v_mfma_f32_16x16x32_bf16 v[40:43], a[96:99], a[36:39], v[40:43]\n"
"ds_read_b128 a[124:127], v236 offset:15360\n"
"v_mfma_f32_16x16x32_bf16 v[4:7], a[100:103], a[32:35], v[4:7]\n"
"v_mfma_f32_16x16x32_bf16 v[44:47], a[100:103], a[36:39], v[44:47]\n"
"s_waitcnt lgkmcnt(3)\n"
"ds_read_b128 a[64:67], v236 offset:24576\n"
"v_mfma_f32_16x16x32_bf16 v[0:3], a[104:107], a[40:43], v[0:3]\n"
"v_mfma_f32_16x16x32_bf16 v[40:43], a[104:107], a[44:47], v[40:43]\n"
"ds_read_b128 a[68:71], v236 offset:25600\n"
"v_mfma_f32_16x16x32_bf16 v[4:7], a[108:111], a[40:43], v[4:7]\n"
"v_mfma_f32_16x16x32_bf16 v[44:47], a[108:111], a[44:47], v[44:47]\n"
"s_waitcnt vmcnt(12)\n"
"ds_read_b128 a[72:75], v237 offset:2048\n"
"s_add_u32 s48, s60, 32768\n"
"v_mfma_f32_16x16x32_bf16 v[0:3], a[112:115], a[48:51], v[0:3]\n"
"s_waitcnt vmcnt(11)\n"
"s_and_b32 s48, s48, 0x18000\n"
"v_mfma_f32_16x16x32_bf16 v[40:43], a[112:115], a[52:55], v[40:43]\n"
"s_waitcnt lgkmcnt(3)\n"
"ds_read_b128 a[76:79], v237 offset:3072\n"
"s_andn2_b32 s49, s60, 0x18000\n"
"v_mfma_f32_16x16x32_bf16 v[4:7], a[116:119], a[48:51], v[4:7]\n"
"s_or_b32 s52, s48, s49\n"
"s_waitcnt vmcnt(2)\n"
"v_cmp_eq_u64 vcc, s[28:29], v[240:241]\n"
"s_cmp_eq_u64 vcc, exec\n"
"s_cbranch_scc0 L1049_%=\n"
"L1050_%=:\n"
"v_mfma_f32_16x16x32_bf16 v[44:47], a[116:119], a[52:55], v[44:47]\n"
"s_add_u32 s53, s52, 16384\n"
"ds_read_b128 a[80:83], v237 offset:12288\n"
"v_mfma_f32_16x16x32_bf16 v[0:3], a[120:123], a[56:59], v[0:3]\n"
"buffer_load_dwordx4 a[212:215], v224, s[8:11], s52 offen offset:0 sc1\n"
"v_mfma_f32_16x16x32_bf16 v[40:43], a[120:123], a[60:63], v[40:43]\n"
"ds_read_b128 a[84:87], v237 offset:13312\n"
"v_mfma_f32_16x16x32_bf16 v[4:7], a[124:127], a[56:59], v[4:7]\n"
"v_mfma_f32_16x16x32_bf16 v[44:47], a[124:127], a[60:63], v[44:47]\n"
"s_waitcnt lgkmcnt(3)\n"
"ds_read_b128 a[88:91], v237 offset:22528\n"
"v_mfma_f32_16x16x32_bf16 v[0:3], a[64:67], a[180:183], v[0:3]\n"
"v_mfma_f32_16x16x32_bf16 v[40:43], a[64:67], a[184:187], v[40:43]\n"
"ds_read_b128 a[92:95], v237 offset:23552\n"
"v_mfma_f32_16x16x32_bf16 v[4:7], a[68:71], a[180:183], v[4:7]\n"
"buffer_load_dwordx4 a[216:219], v224, s[8:11], s53 offen offset:0 sc1\n"
"v_mfma_f32_16x16x32_bf16 v[44:47], a[68:71], a[184:187], v[44:47]\n"
"ds_read_b128 a[96:99], v235 offset:18432\n"
"v_mfma_f32_16x16x32_bf16 v[0:3], a[72:75], a[188:191], v[0:3]\n"
"v_mfma_f32_16x16x32_bf16 v[40:43], a[72:75], a[192:195], v[40:43]\n"
"s_waitcnt lgkmcnt(3)\n"
"ds_read_b128 a[100:103], v235 offset:19456\n"
"v_mfma_f32_16x16x32_bf16 v[4:7], a[76:79], a[188:191], v[4:7]\n"
"buffer_load_dwordx4 a[220:223], v224, s[8:11], s52 offen offset:1024 sc1\n"
"v_mfma_f32_16x16x32_bf16 v[44:47], a[76:79], a[192:195], v[44:47]\n"
"ds_read_b128 a[104:107], v235 offset:28672\n"
"v_bfe_u32 v243, v224, 6, 2\n"
"v_mfma_f32_16x16x32_bf16 v[0:3], a[80:83], a[196:199], v[0:3]\n"
"v_bfe_u32 v244, v224, 4, 2\n"
"v_lshrrev_b32 v245, 8, v224\n"
"v_mfma_f32_16x16x32_bf16 v[40:43], a[80:83], a[200:203], v[40:43]\n"
"ds_read_b128 a[108:111], v235 offset:29696\n"
"v_lshlrev_b32 v245, 3, v245\n"
"v_mfma_f32_16x16x32_bf16 v[4:7], a[84:87], a[196:199], v[4:7]\n"
"v_sub_u32 v244, v244, v245\n"
"v_mfma_f32_16x16x32_bf16 v[44:47], a[84:87], a[200:203], v[44:47]\n"
"s_waitcnt vmcnt(4) lgkmcnt(3)\n"
"ds_read_b128 a[112:115], v236 offset:6144\n"
"v_and_b32 v245, 16, v224\n"
"v_mfma_f32_16x16x32_bf16 v[0:3], a[88:91], a[204:207], v[0:3]\n"
"s_waitcnt vmcnt(3)\n"
"buffer_load_dwordx4 a[224:227], v224, s[8:11], s53 offen offset:1024 sc1\n"
"v_mfma_f32_16x16x32_bf16 v[40:43], a[88:91], a[208:211], v[40:43]\n"
"ds_read_b128 a[116:119], v236 offset:7168\n"
"v_mov_b32 v246, 0x3f80\n"
"v_mfma_f32_16x16x32_bf16 v[4:7], a[92:95], a[204:207], v[4:7]\n"
"v_lshlrev_b32 v245, v245, v246\n"
"v_lshlrev_b32 v246, 4, v243\n"
"v_mfma_f32_16x16x32_bf16 v[44:47], a[92:95], a[208:211], v[44:47]\n"
"ds_read_b128 a[120:123], v236 offset:16384\n"
"v_and_b32 v246, 16, v246\n"
"v_mfma_f32_16x16x32_bf16 v[8:11], a[96:99], a[32:35], v[8:11]\n"
"buffer_load_dwordx4 a[228:231], v224, s[8:11], s52 offen offset:2048 sc1\n"
"v_mfma_f32_16x16x32_bf16 v[48:51], a[96:99], a[36:39], v[48:51]\n"
"s_waitcnt lgkmcnt(3)\n"
"ds_read_b128 a[124:127], v236 offset:17408\n"
"v_and_b32 v247, 2, v243\n"
"v_mfma_f32_16x16x32_bf16 v[12:15], a[100:103], a[32:35], v[12:15]\n"
"s_add_u32 s90, s88, 0\n"
"v_lshl_add_u32 v246, v247, 1, v246\n"
"v_cvt_pk_bf16_f32 v0, v0, v1\n"
"v_mfma_f32_16x16x32_bf16 v[52:55], a[100:103], a[36:39], v[52:55]\n"
"ds_read_b128 a[64:67], v236 offset:26624\n"
"v_cvt_pk_bf16_f32 v1, v2, v3\n"
"v_mfma_f32_16x16x32_bf16 v[8:11], a[104:107], a[40:43], v[8:11]\n"
"v_add3_u32 v246, v246, v244, 0\n"
"v_cvt_pk_bf16_f32 v2, v4, v5\n"
"v_mfma_f32_16x16x32_bf16 v[48:51], a[104:107], a[44:47], v[48:51]\n"
"ds_read_b128 a[68:71], v236 offset:27648\n"
"v_cvt_pk_bf16_f32 v3, v6, v7\n"
"v_mfma_f32_16x16x32_bf16 v[12:15], a[108:111], a[40:43], v[12:15]\n"
"buffer_load_dwordx4 a[232:235], v224, s[8:11], s53 offen offset:2048 sc1\n"
"v_ashrrev_i32 v246, 1, v246\n"
"v_mfma_f32_16x16x32_bf16 v[52:55], a[108:111], a[44:47], v[52:55]\n"
"s_waitcnt lgkmcnt(3)\n"
"buffer_store_dwordx4 v[0:3], v224, s[8:11], s90 offen sc1\n"
"v_cmp_eq_u32_e64 vcc, 0, v246\n"
"v_mfma_f32_16x16x32_bf16 v[8:11], a[112:115], a[48:51], v[8:11]\n"
"ds_read_b128 a[72:75], v237 offset:4096\n"
"v_cndmask_b32_e64 v80, 0, v245, vcc\n"
"v_mfma_f32_16x16x32_bf16 v[48:51], a[112:115], a[52:55], v[48:51]\n"
"s_add_u32 s91, s88, 163840\n"
"ds_read_b128 a[76:79], v237 offset:5120\n"
"v_cmp_eq_u32_e64 s[92:93], 1, v246\n"
"v_mfma_f32_16x16x32_bf16 v[12:15], a[116:119], a[48:51], v[12:15]\n"
"buffer_load_dwordx4 a[236:239], v224, s[8:11], s52 offen offset:3072 sc1\n"
"v_cndmask_b32_e64 v81, 0, v245, s[92:93]\n"
"v_mfma_f32_16x16x32_bf16 v[52:55], a[116:119], a[52:55], v[52:55]\n"
"ds_read_b128 a[80:83], v237 offset:14336\n"
"v_cmp_eq_u32_e64 s[94:95], 2, v246\n"
"v_mfma_f32_16x16x32_bf16 v[8:11], a[120:123], a[56:59], v[8:11]\n"
"v_cvt_pk_bf16_f32 v40, v40, v41\n"
"v_cvt_pk_bf16_f32 v41, v42, v43\n"
"v_cndmask_b32_e64 v82, 0, v245, s[94:95]\n"
"v_mfma_f32_16x16x32_bf16 v[48:51], a[120:123], a[60:63], v[48:51]\n"
"s_waitcnt lgkmcnt(3)\n"
"ds_read_b128 a[84:87], v237 offset:15360\n"
"v_cvt_pk_bf16_f32 v42, v44, v45\n"
"v_cvt_pk_bf16_f32 v43, v46, v47\n"
"v_mfma_f32_16x16x32_bf16 v[12:15], a[124:127], a[56:59], v[12:15]\n"
"buffer_store_dwordx4 v[40:43], v224, s[8:11], s91 offen sc1\n"
"v_cmp_eq_u32_e64 vcc, 3, v246\n"
"v_mfma_f32_16x16x32_bf16 v[52:55], a[124:127], a[60:63], v[52:55]\n"
"s_add_u32 s48, s60, 65536\n"
"ds_read_b128 a[88:91], v237 offset:24576\n"
"v_cndmask_b32_e64 v83, 0, v245, vcc\n"
"v_lshlrev_b32 v246, 4, v243\n"
"v_mfma_f32_16x16x32_bf16 v[8:11], a[64:67], a[180:183], v[8:11]\n"
"s_and_b32 s48, s48, 0x18000\n"
"buffer_load_dwordx4 a[240:243], v224, s[8:11], s53 offen offset:3072 sc1\n"
"v_and_b32 v246, 16, v246\n"
"v_mfma_f32_16x16x32_bf16 v[48:51], a[64:67], a[184:187], v[48:51]\n"
"s_andn2_b32 s49, s60, 0x18000\n"
"ds_read_b128 a[92:95], v237 offset:25600\n"
"v_and_b32 v247, 2, v243\n"
"v_mfma_f32_16x16x32_bf16 v[12:15], a[68:71], a[180:183], v[12:15]\n"
"s_or_b32 s52, s48, s49\n"
"v_lshl_add_u32 v246, v247, 1, v246\n"
"v_add3_u32 v246, v246, v244, 8\n"
"v_mfma_f32_16x16x32_bf16 v[52:55], a[68:71], a[184:187], v[52:55]\n"
"s_waitcnt lgkmcnt(3)\n"
"ds_read_b128 a[96:99], v235 offset:20480\n"
"v_ashrrev_i32 v246, 1, v246\n"
"v_mfma_f32_16x16x32_bf16 v[8:11], a[72:75], a[188:191], v[8:11]\n"
"s_add_u32 s53, s52, 16384\n"
"buffer_load_dwordx4 a[244:247], v224, s[8:11], s52 offen offset:0 sc1\n"
"v_mfma_f32_16x16x32_bf16 v[48:51], a[72:75], a[192:195], v[48:51]\n"
"ds_read_b128 a[100:103], v235 offset:21504\n"
"v_cmp_eq_u32_e64 vcc, 0, v246\n"
"v_mfma_f32_16x16x32_bf16 v[12:15], a[76:79], a[188:191], v[12:15]\n"
"v_cmp_eq_u32_e64 s[92:93], 1, v246\n"
"v_cndmask_b32_e64 v88, 0, v245, vcc\n"
"v_mfma_f32_16x16x32_bf16 v[52:55], a[76:79], a[192:195], v[52:55]\n"
"ds_read_b128 a[104:107], v235 offset:30720\n"
"v_cndmask_b32_e64 v89, 0, v245, s[92:93]\n"
"v_mfma_f32_16x16x32_bf16 v[8:11], a[80:83], a[196:199], v[8:11]\n"
"v_cmp_eq_u32_e64 s[94:95], 2, v246\n"
"v_cmp_eq_u32_e64 vcc, 3, v246\n"
"v_mfma_f32_16x16x32_bf16 v[48:51], a[80:83], a[200:203], v[48:51]\n"
"s_waitcnt lgkmcnt(3)\n"
"ds_read_b128 a[108:111], v235 offset:31744\n"
"v_cndmask_b32_e64 v90, 0, v245, s[94:95]\n"
"v_mfma_f32_16x16x32_bf16 v[12:15], a[84:87], a[196:199], v[12:15]\n"
"buffer_load_dwordx4 a[248:251], v224, s[8:11], s53 offen offset:0 sc1\n"
"v_mfma_f32_16x16x32_bf16 v[52:55], a[84:87], a[200:203], v[52:55]\n"
"ds_read_b128 a[112:115], v236 offset:8192\n"
"v_cndmask_b32_e64 v91, 0, v245, vcc\n"
"v_mfma_f32_16x16x32_bf16 v[8:11], a[88:91], a[204:207], v[8:11]\n"
"v_lshl_add_u32 v246, v243, 3, v244\n"
"v_add_u32 v246, 0, v246\n"
"v_mfma_f32_16x16x32_bf16 v[48:51], a[88:91], a[208:211], v[48:51]\n"
"ds_read_b128 a[116:119], v236 offset:9216\n"
"v_ashrrev_i32 v246, 1, v246\n"
"v_mfma_f32_16x16x32_bf16 v[12:15], a[92:95], a[204:207], v[12:15]\n"
"buffer_load_dwordx4 a[252:255], v224, s[8:11], s52 offen offset:1024 sc1\n"
"v_mfma_f32_16x16x32_bf16 v[52:55], a[92:95], a[208:211], v[52:55]\n"
"s_waitcnt lgkmcnt(3)\n"
"ds_read_b128 a[120:123], v236 offset:18432\n"
"v_cmp_eq_u32_e64 vcc, 0, v246\n"
"v_mfma_f32_16x16x32_bf16 v[16:19], a[96:99], a[32:35], v[16:19]\n"
"v_cmp_eq_u32_e64 s[92:93], 1, v246\n"
"v_cndmask_b32_e64 v96, 0, v245, vcc\n"
"v_mfma_f32_16x16x32_bf16 v[56:59], a[96:99], a[36:39], v[56:59]\n"
"ds_read_b128 a[124:127], v236 offset:19456\n"
"v_cndmask_b32_e64 v97, 0, v245, s[92:93]\n"
"v_mfma_f32_16x16x32_bf16 v[20:23], a[100:103], a[32:35], v[20:23]\n"
"s_add_u32 s90, s88, 1024\n"
"v_cmp_eq_u32_e64 s[94:95], 2, v246\n"
"v_cvt_pk_bf16_f32 v8, v8, v9\n"
"v_mfma_f32_16x16x32_bf16 v[60:63], a[100:103], a[36:39], v[60:63]\n"
"ds_read_b128 a[64:67], v236 offset:28672\n"
"v_cvt_pk_bf16_f32 v9, v10, v11\n"
"v_mfma_f32_16x16x32_bf16 v[16:19], a[104:107], a[40:43], v[16:19]\n"
"buffer_load_dwordx4 a[128:131], v224, s[8:11], s53 offen offset:1024 sc1\n"
"v_mfma_f32_16x16x32_bf16 v[56:59], a[104:107], a[44:47], v[56:59]\n"
"s_waitcnt lgkmcnt(3)\n"
"ds_read_b128 a[68:71], v236 offset:29696\n"
"v_cndmask_b32_e64 v98, 0, v245, s[94:95]\n"
"v_mfma_f32_16x16x32_bf16 v[20:23], a[108:111], a[40:43], v[20:23]\n"
"v_cvt_pk_bf16_f32 v10, v12, v13\n"
"v_cvt_pk_bf16_f32 v11, v14, v15\n"
"v_cmp_eq_u32_e64 vcc, 3, v246\n"
"v_mfma_f32_16x16x32_bf16 v[60:63], a[108:111], a[44:47], v[60:63]\n"
"buffer_store_dwordx4 v[8:11], v224, s[8:11], s90 offen sc1\n"
"v_cndmask_b32_e64 v99, 0, v245, vcc\n"
"v_mfma_f32_16x16x32_bf16 v[16:19], a[112:115], a[48:51], v[16:19]\n"
"ds_read_b128 a[72:75], v237 offset:6144\n"
"v_lshl_add_u32 v246, v243, 3, v244\n"
"v_mfma_f32_16x16x32_bf16 v[56:59], a[112:115], a[52:55], v[56:59]\n"
"s_add_u32 s91, s88, 164864\n"
"buffer_load_dwordx4 a[132:135], v224, s[8:11], s52 offen offset:2048 sc1\n"
"v_add_u32 v246, 4, v246\n"
"v_mfma_f32_16x16x32_bf16 v[20:23], a[116:119], a[48:51], v[20:23]\n"
"ds_read_b128 a[76:79], v237 offset:7168\n"
"v_ashrrev_i32 v246, 1, v246\n"
"v_mfma_f32_16x16x32_bf16 v[60:63], a[116:119], a[52:55], v[60:63]\n"
"s_waitcnt lgkmcnt(3)\n"
"ds_read_b128 a[80:83], v237 offset:16384\n"
"v_cmp_eq_u32_e64 vcc, 0, v246\n"
"v_mfma_f32_16x16x32_bf16 v[16:19], a[120:123], a[56:59], v[16:19]\n"
"v_cvt_pk_bf16_f32 v48, v48, v49\n"
"v_cvt_pk_bf16_f32 v49, v50, v51\n"
"v_cndmask_b32_e64 v104, 0, v245, vcc\n"
"v_mfma_f32_16x16x32_bf16 v[56:59], a[120:123], a[60:63], v[56:59]\n"
"ds_read_b128 a[84:87], v237 offset:17408\n"
"v_cvt_pk_bf16_f32 v50, v52, v53\n"
"v_cvt_pk_bf16_f32 v51, v54, v55\n"
"v_mfma_f32_16x16x32_bf16 v[20:23], a[124:127], a[56:59], v[20:23]\n"
"buffer_store_dwordx4 v[48:51], v224, s[8:11], s91 offen sc1\n"
"v_cmp_eq_u32_e64 s[92:93], 1, v246\n"
"v_mfma_f32_16x16x32_bf16 v[60:63], a[124:127], a[60:63], v[60:63]\n"
"buffer_load_dwordx4 a[136:139], v224, s[8:11], s53 offen offset:2048 sc1\n"
"v_cndmask_b32_e64 v105, 0, v245, s[92:93]\n"
"v_mfma_f32_16x16x32_bf16 v[16:19], a[64:67], a[180:183], v[16:19]\n"
"s_mov_b32 s12, s64\n"
"ds_read_b128 a[88:91], v237 offset:26624\n"
"v_cmp_eq_u32_e64 s[94:95], 2, v246\n"
"v_mfma_f32_16x16x32_bf16 v[56:59], a[64:67], a[184:187], v[56:59]\n"
"s_waitcnt lgkmcnt(3)\n"
"ds_read_b128 a[92:95], v237 offset:27648\n"
"v_cndmask_b32_e64 v106, 0, v245, s[94:95]\n"
"v_cmp_eq_u32_e64 vcc, 3, v246\n"
"v_mfma_f32_16x16x32_bf16 v[20:23], a[68:71], a[180:183], v[20:23]\n"
"s_and_b32 s13, s65, 0xffff\n"
"buffer_load_dwordx4 a[140:143], v224, s[8:11], s52 offen offset:3072 sc1\n"
"v_cndmask_b32_e64 v107, 0, v245, vcc\n"
"v_mfma_f32_16x16x32_bf16 v[60:63], a[68:71], a[184:187], v[60:63]\n"
"s_mov_b32 s14, 2621440\n"
"ds_read_b128 a[96:99], v235 offset:22528\n"
"s_mov_b32 s15, 0x20000\n"
"v_mfma_f32_16x16x32_bf16 v[16:19], a[72:75], a[188:191], v[16:19]\n"
"s_mov_b32 s16, s86\n"
"v_mfma_f32_16x16x32_bf16 v[56:59], a[72:75], a[192:195], v[56:59]\n"
"s_and_b32 s17, s87, 0xffff\n"
"ds_read_b128 a[100:103], v235 offset:23552\n"
"v_mfma_f32_16x16x32_bf16 v[20:23], a[76:79], a[188:191], v[20:23]\n"
"s_mov_b32 s18, 2621440\n"
"v_mfma_f32_16x16x32_bf16 v[60:63], a[76:79], a[192:195], v[60:63]\n"
"s_waitcnt lgkmcnt(3)\n"
"ds_read_b128 a[104:107], v236 offset:0\n"
"s_add_u32 s48, s60, 98304\n"
"v_mfma_f32_16x16x32_bf16 v[16:19], a[80:83], a[196:199], v[16:19]\n"
"s_mov_b32 s19, 0x20000\n"
"buffer_load_dwordx4 a[144:147], v224, s[8:11], s53 offen offset:3072 sc1\n"
"v_mfma_f32_16x16x32_bf16 v[56:59], a[80:83], a[200:203], v[56:59]\n"
"s_and_b32 s48, s48, 0x18000\n"
"ds_read_b128 a[108:111], v236 offset:1024\n"
"v_bfe_u32 v243, v224, 4, 4\n"
"v_mfma_f32_16x16x32_bf16 v[20:23], a[84:87], a[196:199], v[20:23]\n"
"s_andn2_b32 s49, s60, 0x18000\n"
"s_lshl_b32 s92, s34, 3\n"
"s_or_b32 s52, s48, s49\n"
"v_mfma_f32_16x16x32_bf16 v[60:63], a[84:87], a[200:203], v[60:63]\n"
"s_add_u32 s53, s52, 16384\n"
"ds_read_b128 a[112:115], v236 offset:10240\n"
"s_add_u32 s93, s92, s32\n"
"v_mfma_f32_16x16x32_bf16 v[16:19], a[88:91], a[204:207], v[16:19]\n"
"s_sub_u32 s92, s32, s92\n"
"buffer_load_dwordx4 a[148:151], v224, s[8:11], s52 offen offset:0 sc1\n"
"v_mfma_f32_16x16x32_bf16 v[56:59], a[88:91], a[208:211], v[56:59]\n"
"s_waitcnt lgkmcnt(3)\n"
"ds_read_b128 a[116:119], v236 offset:11264\n"
"v_lshl_add_u32 v244, s93, 4, v243\n"
"v_mfma_f32_16x16x32_bf16 v[20:23], a[92:95], a[204:207], v[20:23]\n"
"s_add_u32 s92, s92, 120\n"
"v_lshl_add_u32 v245, s92, 4, v243\n"
"v_mfma_f32_16x16x32_bf16 v[60:63], a[92:95], a[208:211], v[60:63]\n"
"s_mul_i32 s93, s93, 20480\n"
"ds_read_b128 a[120:123], v236 offset:20480\n"
"v_mfma_f32_16x16x32_bf16 v[24:27], a[96:99], a[32:35], v[24:27]\n"
"s_mul_i32 s92, s92, 20480\n"
"v_mfma_f32_16x16x32_bf16 v[64:67], a[96:99], a[36:39], v[64:67]\n"
"ds_read_b128 a[124:127], v236 offset:21504\n"
"v_add_u32 v178, s93, v224\n"
"v_mfma_f32_16x16x32_bf16 v[28:31], a[100:103], a[32:35], v[28:31]\n"
"s_add_u32 s90, s88, 2048\n"
"buffer_load_dwordx4 a[152:155], v224, s[8:11], s53 offen offset:0 sc1\n"
"v_mfma_f32_16x16x32_bf16 v[68:71], a[100:103], a[36:39], v[68:71]\n"
"s_waitcnt lgkmcnt(3)\n"
"ds_read_b128 a[64:67], v236 offset:30720\n"
"v_cvt_pk_bf16_f32 v16, v16, v17\n"
"v_mfma_f32_16x16x32_bf16 v[24:27], a[104:107], a[40:43], v[24:27]\n"
"v_cvt_pk_bf16_f32 v17, v18, v19\n"
"v_add_u32 v179, s92, v224\n"
"v_mfma_f32_16x16x32_bf16 v[64:67], a[104:107], a[44:47], v[64:67]\n"
"ds_read_b128 a[68:71], v236 offset:31744\n"
"v_cvt_pk_bf16_f32 v18, v20, v21\n"
"v_mfma_f32_16x16x32_bf16 v[28:31], a[108:111], a[40:43], v[28:31]\n"
"buffer_load_dwordx4 a[156:159], v224, s[8:11], s52 offen offset:1024 sc1\n"
"v_cvt_pk_bf16_f32 v19, v22, v23\n"
"v_mfma_f32_16x16x32_bf16 v[68:71], a[108:111], a[44:47], v[68:71]\n"
"buffer_store_dwordx4 v[16:19], v224, s[8:11], s90 offen sc1\n"
"v_lshrrev_b32 v243, 4, v224\n"
"v_mfma_f32_16x16x32_bf16 v[24:27], a[112:115], a[48:51], v[24:27]\n"
"ds_read_b128 a[72:75], v237 offset:8192\n"
"v_and_b32 v243, 48, v243\n"
"v_mul_u32_u24 v176, 1280, v244\n"
"v_mfma_f32_16x16x32_bf16 v[64:67], a[112:115], a[52:55], v[64:67]\n"
"s_waitcnt lgkmcnt(3)\n"
"ds_read_b128 a[76:79], v237 offset:9216\n"
"v_mul_u32_u24 v177, 1280, v245\n"
"v_mfma_f32_16x16x32_bf16 v[28:31], a[116:119], a[48:51], v[28:31]\n"
"s_add_u32 s91, s88, 165888\n"
"v_add_u32 v176, v176, v243\n"
"v_add_u32 v177, v177, v243\n"
"v_mfma_f32_16x16x32_bf16 v[68:71], a[116:119], a[52:55], v[68:71]\n"
"ds_read_b128 a[80:83], v237 offset:18432\n"
"v_cvt_pk_bf16_f32 v56, v56, v57\n"
"v_mfma_f32_16x16x32_bf16 v[24:27], a[120:123], a[56:59], v[24:27]\n"
"buffer_load_dwordx4 a[160:163], v224, s[8:11], s53 offen offset:1024 sc1\n"
"v_cvt_pk_bf16_f32 v57, v58, v59\n"
"v_mfma_f32_16x16x32_bf16 v[64:67], a[120:123], a[60:63], v[64:67]\n"
"ds_read_b128 a[84:87], v237 offset:19456\n"
"v_and_b32 v243, 0x300, v224\n"
"v_cvt_pk_bf16_f32 v58, v60, v61\n"
"v_mfma_f32_16x16x32_bf16 v[28:31], a[124:127], a[56:59], v[28:31]\n"
"v_cvt_pk_bf16_f32 v59, v62, v63\n"
"v_add_u32 v243, 2657280, v243\n"
"v_sub_u32 v244, 2047, v244\n"
"v_mfma_f32_16x16x32_bf16 v[68:71], a[124:127], a[60:63], v[68:71]\n"
"s_waitcnt lgkmcnt(3)\n"
"buffer_store_dwordx4 v[56:59], v224, s[8:11], s91 offen sc1\n"
"v_sub_u32 v245, 2047, v245\n"
"v_mfma_f32_16x16x32_bf16 v[24:27], a[64:67], a[180:183], v[24:27]\n"
"ds_read_b128 a[88:91], v237 offset:28672\n"
"v_readlane_b32 s92, v234, 0\n"
"v_mfma_f32_16x16x32_bf16 v[64:67], a[64:67], a[184:187], v[64:67]\n"
"s_and_b32 s92, s92, 0xffff\n"
"buffer_load_dwordx4 a[164:167], v224, s[8:11], s52 offen offset:2048 sc1\n"
"v_mfma_f32_16x16x32_bf16 v[28:31], a[68:71], a[180:183], v[28:31]\n"
"ds_read_b128 a[92:95], v237 offset:29696\n"
"v_cmp_eq_u32 vcc, s92, v244\n"
"v_mfma_f32_16x16x32_bf16 v[68:71], a[68:71], a[184:187], v[68:71]\n"
"ds_read_b128 a[96:99], v235 offset:24576\n"
"v_cmp_eq_u32_e64 s[94:95], s92, v245\n"
"v_mfma_f32_16x16x32_bf16 v[24:27], a[72:75], a[188:191], v[24:27]\n"
"v_cndmask_b32 v178, v178, v243, vcc\n"
"v_cndmask_b32_e64 v179, v179, v243, s[94:95]\n"
"v_mfma_f32_16x16x32_bf16 v[64:67], a[72:75], a[192:195], v[64:67]\n"
"s_waitcnt lgkmcnt(3)\n"
"ds_read_b128 a[100:103], v235 offset:25600\n"
"v_add_u32 v243, 16, v243\n"
"v_mfma_f32_16x16x32_bf16 v[28:31], a[76:79], a[188:191], v[28:31]\n"
"buffer_load_dwordx4 a[168:171], v224, s[8:11], s53 offen offset:2048 sc1\n"
"v_mfma_f32_16x16x32_bf16 v[68:71], a[76:79], a[192:195], v[68:71]\n"
"ds_read_b128 a[104:107], v236 offset:2048\n"
"v_readlane_b32 s92, v234, 1\n"
"v_mfma_f32_16x16x32_bf16 v[24:27], a[80:83], a[196:199], v[24:27]\n"
"s_and_b32 s92, s92, 0xffff\n"
"v_cmp_eq_u32 vcc, s92, v244\n"
"v_cmp_eq_u32_e64 s[94:95], s92, v245\n"
"v_mfma_f32_16x16x32_bf16 v[64:67], a[80:83], a[200:203], v[64:67]\n"
"ds_read_b128 a[108:111], v236 offset:3072\n"
"v_cndmask_b32 v178, v178, v243, vcc\n"
"v_mfma_f32_16x16x32_bf16 v[28:31], a[84:87], a[196:199], v[28:31]\n"
"buffer_load_dwordx4 a[172:175], v224, s[8:11], s52 offen offset:3072 sc1\n"
"v_mfma_f32_16x16x32_bf16 v[68:71], a[84:87], a[200:203], v[68:71]\n"
"s_waitcnt lgkmcnt(3)\n"
"ds_read_b128 a[112:115], v236 offset:12288\n"
"v_cndmask_b32_e64 v179, v179, v243, s[94:95]\n"
"v_mfma_f32_16x16x32_bf16 v[24:27], a[88:91], a[204:207], v[24:27]\n"
"v_add_u32 v243, 16, v243\n"
"v_readlane_b32 s92, v234, 2\n"
"v_mfma_f32_16x16x32_bf16 v[64:67], a[88:91], a[208:211], v[64:67]\n"
"s_and_b32 s92, s92, 0xffff\n"
"ds_read_b128 a[116:119], v236 offset:13312\n"
"v_cmp_eq_u32 vcc, s92, v244\n"
"v_mfma_f32_16x16x32_bf16 v[28:31], a[92:95], a[204:207], v[28:31]\n"
"v_cmp_eq_u32_e64 s[94:95], s92, v245\n"
"v_mfma_f32_16x16x32_bf16 v[68:71], a[92:95], a[208:211], v[68:71]\n"
"ds_read_b128 a[120:123], v236 offset:22528\n"
"v_cndmask_b32 v178, v178, v243, vcc\n"
"v_mfma_f32_16x16x32_bf16 v[32:35], a[96:99], a[32:35], v[32:35]\n"
"buffer_load_dwordx4 a[176:179], v224, s[8:11], s53 offen offset:3072 sc1\n"
"v_mfma_f32_16x16x32_bf16 v[72:75], a[96:99], a[36:39], v[72:75]\n"
"s_waitcnt lgkmcnt(3)\n"
"ds_read_b128 a[124:127], v236 offset:23552\n"
"v_cndmask_b32_e64 v179, v179, v243, s[94:95]\n"
"v_mfma_f32_16x16x32_bf16 v[36:39], a[100:103], a[32:35], v[36:39]\n"
"s_add_u32 s90, s88, 3072\n"
"v_add_u32 v243, 16, v243\n"
"v_cvt_pk_bf16_f32 v24, v24, v25\n"
"v_mfma_f32_16x16x32_bf16 v[76:79], a[100:103], a[36:39], v[76:79]\n"
"ds_read_b128 a[64:67], v237 offset:0\n"
"v_cvt_pk_bf16_f32 v25, v26, v27\n"
"v_mfma_f32_16x16x32_bf16 v[32:35], a[104:107], a[40:43], v[32:35]\n"
"v_readlane_b32 s92, v234, 3\n"
"v_cvt_pk_bf16_f32 v26, v28, v29\n"
"v_mfma_f32_16x16x32_bf16 v[72:75], a[104:107], a[44:47], v[72:75]\n"
"s_and_b32 s92, s92, 0xffff\n"
"ds_read_b128 a[68:71], v237 offset:1024\n"
"v_cvt_pk_bf16_f32 v27, v30, v31\n"
"v_mfma_f32_16x16x32_bf16 v[36:39], a[108:111], a[40:43], v[36:39]\n"
"buffer_store_dwordx4 v[24:27], v224, s[8:11], s90 offen sc1\n"
"v_mfma_f32_16x16x32_bf16 v[76:79], a[108:111], a[44:47], v[76:79]\n"
"s_waitcnt lgkmcnt(3)\n"
"ds_read_b128 a[72:75], v237 offset:10240\n"
"v_cmp_eq_u32 vcc, s92, v244\n"
"v_mfma_f32_16x16x32_bf16 v[32:35], a[112:115], a[48:51], v[32:35]\n"
"v_cmp_eq_u32_e64 s[94:95], s92, v245\n"
"v_cndmask_b32 v178, v178, v243, vcc\n"
"v_mfma_f32_16x16x32_bf16 v[72:75], a[112:115], a[52:55], v[72:75]\n"
"s_add_u32 s91, s88, 166912\n"
"ds_read_b128 a[76:79], v237 offset:11264\n"
"v_cndmask_b32_e64 v179, v179, v243, s[94:95]\n"
"v_mfma_f32_16x16x32_bf16 v[36:39], a[116:119], a[48:51], v[36:39]\n"
"v_add_u32 v243, 16, v243\n"
"v_cvt_pk_bf16_f32 v64, v64, v65\n"
"v_mfma_f32_16x16x32_bf16 v[76:79], a[116:119], a[52:55], v[76:79]\n"
"ds_read_b128 a[80:83], v237 offset:20480\n"
"v_cvt_pk_bf16_f32 v65, v66, v67\n"
"v_mfma_f32_16x16x32_bf16 v[32:35], a[120:123], a[56:59], v[32:35]\n"
"v_readlane_b32 s92, v234, 4\n"
"v_cvt_pk_bf16_f32 v66, v68, v69\n"
"v_mfma_f32_16x16x32_bf16 v[72:75], a[120:123], a[60:63], v[72:75]\n"
"s_waitcnt lgkmcnt(3)\n"
"ds_read_b128 a[84:87], v237 offset:21504\n"
"v_cvt_pk_bf16_f32 v67, v70, v71\n"
"v_mfma_f32_16x16x32_bf16 v[36:39], a[124:127], a[56:59], v[36:39]\n"
"s_and_b32 s92, s92, 0xffff\n"
"buffer_store_dwordx4 v[64:67], v224, s[8:11], s91 offen sc1\n"
"v_cmp_eq_u32 vcc, s92, v244\n"
"v_mfma_f32_16x16x32_bf16 v[76:79], a[124:127], a[60:63], v[76:79]\n"
"ds_read_b128 a[88:91], v237 offset:30720\n"
"v_cmp_eq_u32_e64 s[94:95], s92, v245\n"
"v_mfma_f32_16x16x32_bf16 v[32:35], a[64:67], a[180:183], v[32:35]\n"
"v_cndmask_b32 v178, v178, v243, vcc\n"
"v_cndmask_b32_e64 v179, v179, v243, s[94:95]\n"
"v_mfma_f32_16x16x32_bf16 v[72:75], a[64:67], a[184:187], v[72:75]\n"
"ds_read_b128 a[92:95], v237 offset:31744\n"
"v_add_u32 v243, 16, v243\n"
"v_mfma_f32_16x16x32_bf16 v[36:39], a[68:71], a[180:183], v[36:39]\n"
"v_readlane_b32 s92, v234, 5\n"
"s_and_b32 s92, s92, 0xffff\n"
"v_mfma_f32_16x16x32_bf16 v[76:79], a[68:71], a[184:187], v[76:79]\n"
"s_waitcnt lgkmcnt(3)\n"
"ds_read_b128 a[96:99], v226 offset:8192\n"
"v_cmp_eq_u32 vcc, s92, v244\n"
"v_mfma_f32_16x16x32_bf16 v[32:35], a[72:75], a[188:191], v[32:35]\n"
"v_cmp_eq_u32_e64 s[94:95], s92, v245\n"
"v_cndmask_b32 v178, v178, v243, vcc\n"
"v_mfma_f32_16x16x32_bf16 v[72:75], a[72:75], a[192:195], v[72:75]\n"
"ds_read_b128 a[100:103], v226 offset:9216\n"
"v_cndmask_b32_e64 v179, v179, v243, s[94:95]\n"
"v_mfma_f32_16x16x32_bf16 v[36:39], a[76:79], a[188:191], v[36:39]\n"
"v_add_u32 v243, 16, v243\n"
"v_mfma_f32_16x16x32_bf16 v[76:79], a[76:79], a[192:195], v[76:79]\n"
"ds_read_b128 a[104:107], v226 offset:18432\n"
"v_readlane_b32 s92, v234, 6\n"
"v_mfma_f32_16x16x32_bf16 v[32:35], a[80:83], a[196:199], v[32:35]\n"
"s_and_b32 s92, s92, 0xffff\n"
"v_mfma_f32_16x16x32_bf16 v[72:75], a[80:83], a[200:203], v[72:75]\n"
"s_waitcnt lgkmcnt(3)\n"
"ds_read_b128 a[108:111], v226 offset:19456\n"
"v_cmp_eq_u32 vcc, s92, v244\n"
"v_mfma_f32_16x16x32_bf16 v[36:39], a[84:87], a[196:199], v[36:39]\n"
"v_cmp_eq_u32_e64 s[94:95], s92, v245\n"
"v_mfma_f32_16x16x32_bf16 v[76:79], a[84:87], a[200:203], v[76:79]\n"
"ds_read_b128 a[112:115], v226 offset:28672\n"
"v_cndmask_b32 v178, v178, v243, vcc\n"
"v_mfma_f32_16x16x32_bf16 v[32:35], a[88:91], a[204:207], v[32:35]\n"
"v_cndmask_b32_e64 v179, v179, v243, s[94:95]\n"
"v_mfma_f32_16x16x32_bf16 v[72:75], a[88:91], a[208:211], v[72:75]\n"
"ds_read_b128 a[116:119], v226 offset:29696\n"
"v_add_u32 v243, 16, v243\n"
"v_mfma_f32_16x16x32_bf16 v[36:39], a[92:95], a[204:207], v[36:39]\n"
"v_readlane_b32 s92, v234, 7\n"
"v_mfma_f32_16x16x32_bf16 v[76:79], a[92:95], a[208:211], v[76:79]\n"
"s_waitcnt vmcnt(31) lgkmcnt(3)\n"
"ds_read_b128 a[120:123], v235 offset:6144\n"
"s_and_b32 s92, s92, 0xffff\n"
"v_mfma_f32_16x16x32_bf16 v[84:87], a[96:99], a[212:215], v[84:87]\n"
"s_waitcnt vmcnt(30)\n"
"v_cmp_eq_u32 vcc, s92, v244\n"
"v_mfma_f32_16x16x32_bf16 v[164:167], a[96:99], a[216:219], v[164:167]\n"
"ds_read_b128 a[124:127], v235 offset:7168\n"
"v_cmp_eq_u32_e64 s[94:95], s92, v245\n"
"v_mfma_f32_16x16x32_bf16 v[92:95], a[100:103], a[212:215], v[92:95]\n"
"s_add_u32 s90, s88, 4096\n"
"v_cvt_pk_bf16_f32 v32, v32, v33\n"
"v_cvt_pk_bf16_f32 v33, v34, v35\n"
"v_mfma_f32_16x16x32_bf16 v[172:175], a[100:103], a[216:219], v[172:175]\n"
"s_waitcnt vmcnt(29)\n"
"ds_read_b128 a[64:67], v235 offset:16384\n"
"v_cndmask_b32 v178, v178, v243, vcc\n"
"v_mfma_f32_16x16x32_bf16 v[84:87], a[104:107], a[220:223], v[84:87]\n"
"s_waitcnt vmcnt(28)\n"
"v_cvt_pk_bf16_f32 v34, v36, v37\n"
"v_cvt_pk_bf16_f32 v35, v38, v39\n"
"v_mfma_f32_16x16x32_bf16 v[164:167], a[104:107], a[224:227], v[164:167]\n"
"s_waitcnt lgkmcnt(3)\n"
"ds_read_b128 a[68:71], v235 offset:17408\n"
"v_cndmask_b32_e64 v179, v179, v243, s[94:95]\n"
"v_mfma_f32_16x16x32_bf16 v[92:95], a[108:111], a[220:223], v[92:95]\n"
"s_mul_i32 s92, s33, 5\n"
"buffer_store_dwordx4 v[32:35], v224, s[8:11], s90 offen sc1\n"
"v_mfma_f32_16x16x32_bf16 v[172:175], a[108:111], a[224:227], v[172:175]\n"
"s_waitcnt vmcnt(28)\n"
"ds_read_b128 a[72:75], v235 offset:26624\n"
"s_add_u32 s93, s92, s30\n"
"v_mfma_f32_16x16x32_bf16 v[84:87], a[112:115], a[228:231], v[84:87]\n"
"s_waitcnt vmcnt(27)\n"
"s_add_u32 s92, s92, 4\n"
"s_lshl_b32 s38, s93, 6\n"
"v_mfma_f32_16x16x32_bf16 v[164:167], a[112:115], a[232:235], v[164:167]\n"
"s_lshl_b32 s55, s92, 6\n"
"ds_read_b128 a[76:79], v235 offset:27648\n"
"v_cvt_pk_bf16_f32 v72, v72, v73\n"
"v_mfma_f32_16x16x32_bf16 v[92:95], a[116:119], a[228:231], v[92:95]\n"
"s_add_u32 s91, s88, 167936\n"
"v_cvt_pk_bf16_f32 v73, v74, v75\n"
"v_cvt_pk_bf16_f32 v74, v76, v77\n"
"v_mfma_f32_16x16x32_bf16 v[172:175], a[116:119], a[232:235], v[172:175]\n"
"s_waitcnt vmcnt(25) lgkmcnt(3)\n"
"ds_read_b128 a[80:83], v236 offset:4096\n"
"v_cvt_pk_bf16_f32 v75, v78, v79\n"
"v_mfma_f32_16x16x32_bf16 v[84:87], a[120:123], a[236:239], v[84:87]\n"
"s_waitcnt vmcnt(23)\n"
"buffer_store_dwordx4 v[72:75], v224, s[8:11], s91 offen sc1\n"
"v_mfma_f32_16x16x32_bf16 v[164:167], a[120:123], a[240:243], v[164:167]\n"
"s_add_u32 s55, s55, s45\n"
"ds_read_b128 a[84:87], v236 offset:5120\n"
"s_lshl_b32 s36, s93, 10\n"
"v_mfma_f32_16x16x32_bf16 v[92:95], a[124:127], a[236:239], v[92:95]\n"
"s_lshl_b32 s37, s92, 10\n"
"s_add_u32 s36, s36, 65536\n"
"s_add_u32 s37, s37, 65536\n"
"v_mfma_f32_16x16x32_bf16 v[172:175], a[124:127], a[240:243], v[172:175]\n"
"s_waitcnt vmcnt(23)\n"
"ds_read_b128 a[88:91], v236 offset:14336\n"
"v_lshrrev_b32 v243, 1, v224\n"
"v_mfma_f32_16x16x32_bf16 v[84:87], a[64:67], a[244:247], v[84:87]\n"
"s_waitcnt vmcnt(22)\n"
"v_min_u32 v244, 312, v243\n"
"s_add_u32 s37, s37, s45\n"
"v_mfma_f32_16x16x32_bf16 v[164:167], a[64:67], a[248:251], v[164:167]\n"
"s_waitcnt lgkmcnt(3)\n"
"ds_read_b128 a[92:95], v236 offset:15360\n"
"v_and_b32 v243, 56, v243\n"
"v_mfma_f32_16x16x32_bf16 v[92:95], a[68:71], a[244:247], v[92:95]\n"
"s_mov_b32 s92, 6400\n"
"v_mfma_f32_16x16x32_bf16 v[172:175], a[68:71], a[248:251], v[172:175]\n"
"s_waitcnt vmcnt(21)\n"
"ds_read_b128 a[96:99], v236 offset:24576\n"
"v_mfma_f32_16x16x32_bf16 v[84:87], a[72:75], a[252:255], v[84:87]\n"
"s_waitcnt vmcnt(20)\n"
"v_mfma_f32_16x16x32_bf16 v[164:167], a[72:75], a[128:131], v[164:167]\n"
"ds_read_b128 a[100:103], v236 offset:25600\n"
"v_mfma_f32_16x16x32_bf16 v[92:95], a[76:79], a[252:255], v[92:95]\n"
"v_mfma_f32_16x16x32_bf16 v[172:175], a[76:79], a[128:131], v[172:175]\n"
"s_waitcnt vmcnt(18) lgkmcnt(3)\n"
"ds_read_b128 a[104:107], v237 offset:2048\n"
"v_mfma_f32_16x16x32_bf16 v[84:87], a[80:83], a[132:135], v[84:87]\n"
"s_waitcnt vmcnt(16)\n"
"v_mfma_f32_16x16x32_bf16 v[164:167], a[80:83], a[136:139], v[164:167]\n"
"ds_read_b128 a[108:111], v237 offset:3072\n"
"v_mfma_f32_16x16x32_bf16 v[92:95], a[84:87], a[132:135], v[92:95]\n"
"v_mfma_f32_16x16x32_bf16 v[172:175], a[84:87], a[136:139], v[172:175]\n"
"s_waitcnt vmcnt(15)\n"
"ds_read_b128 a[112:115], v237 offset:12288\n"
"v_mfma_f32_16x16x32_bf16 v[84:87], a[88:91], a[140:143], v[84:87]\n"
"s_waitcnt vmcnt(14)\n"
"v_mfma_f32_16x16x32_bf16 v[164:167], a[88:91], a[144:147], v[164:167]\n"
"s_waitcnt lgkmcnt(3)\n"
"ds_read_b128 a[116:119], v237 offset:13312\n"
"v_mfma_f32_16x16x32_bf16 v[92:95], a[92:95], a[140:143], v[92:95]\n"
"v_mfma_f32_16x16x32_bf16 v[172:175], a[92:95], a[144:147], v[172:175]\n"
"s_waitcnt vmcnt(13)\n"
"ds_read_b128 a[120:123], v237 offset:22528\n"
"v_mfma_f32_16x16x32_bf16 v[84:87], a[96:99], a[148:151], v[84:87]\n"
"s_waitcnt vmcnt(12)\n"
"v_mfma_f32_16x16x32_bf16 v[164:167], a[96:99], a[152:155], v[164:167]\n"
"ds_read_b128 a[124:127], v237 offset:23552\n"
"v_mfma_f32_16x16x32_bf16 v[92:95], a[100:103], a[148:151], v[92:95]\n"
"v_mfma_f32_16x16x32_bf16 v[172:175], a[100:103], a[152:155], v[172:175]\n"
"s_waitcnt vmcnt(11) lgkmcnt(3)\n"
"ds_read_b128 a[64:67], v226 offset:10240\n"
"v_mfma_f32_16x16x32_bf16 v[84:87], a[104:107], a[156:159], v[84:87]\n"
"s_waitcnt vmcnt(9)\n"
"v_mfma_f32_16x16x32_bf16 v[164:167], a[104:107], a[160:163], v[164:167]\n"
"ds_read_b128 a[68:71], v226 offset:11264\n"
"v_mfma_f32_16x16x32_bf16 v[92:95], a[108:111], a[156:159], v[92:95]\n"
"v_mfma_f32_16x16x32_bf16 v[172:175], a[108:111], a[160:163], v[172:175]\n"
"s_waitcnt vmcnt(7)\n"
"ds_read_b128 a[72:75], v226 offset:20480\n"
"v_mfma_f32_16x16x32_bf16 v[84:87], a[112:115], a[164:167], v[84:87]\n"
"s_waitcnt vmcnt(6)\n"
"buffer_load_dwordx2 v[240:241], v244, s[8:11], s92 offen sc1\n"
"v_mfma_f32_16x16x32_bf16 v[164:167], a[112:115], a[168:171], v[164:167]\n"
"s_waitcnt lgkmcnt(3)\n"
"ds_read_b128 a[76:79], v226 offset:21504\n"
"v_mfma_f32_16x16x32_bf16 v[92:95], a[116:119], a[164:167], v[92:95]\n"
"v_mfma_f32_16x16x32_bf16 v[172:175], a[116:119], a[168:171], v[172:175]\n"
"s_waitcnt vmcnt(6)\n"
"ds_read_b128 a[80:83], v226 offset:30720\n"
"v_mfma_f32_16x16x32_bf16 v[84:87], a[120:123], a[172:175], v[84:87]\n"
"s_waitcnt vmcnt(5)\n"
"v_mfma_f32_16x16x32_bf16 v[164:167], a[120:123], a[176:179], v[164:167]\n"
"ds_read_b128 a[84:87], v226 offset:31744\n"
"v_mfma_f32_16x16x32_bf16 v[92:95], a[124:127], a[172:175], v[92:95]\n"
"v_mfma_f32_16x16x32_bf16 v[172:175], a[124:127], a[176:179], v[172:175]\n"
"s_waitcnt lgkmcnt(3)\n"
"ds_read_b128 a[88:91], v235 offset:8192\n"
"v_mfma_f32_16x16x32_bf16 v[100:103], a[64:67], a[212:215], v[100:103]\n"
"v_mfma_f32_16x16x32_bf16 v[180:183], a[64:67], a[216:219], v[180:183]\n"
"ds_read_b128 a[92:95], v235 offset:9216\n"
"v_mfma_f32_16x16x32_bf16 v[108:111], a[68:71], a[212:215], v[108:111]\n"
"s_add_u32 s90, s88, 327680\n"
"v_cvt_pk_bf16_f32 v84, v84, v85\n"
"v_cvt_pk_bf16_f32 v85, v86, v87\n"
"v_mfma_f32_16x16x32_bf16 v[188:191], a[68:71], a[216:219], v[188:191]\n"
"ds_read_b128 a[96:99], v235 offset:18432\n"
"v_cvt_pk_bf16_f32 v86, v92, v93\n"
"v_mfma_f32_16x16x32_bf16 v[100:103], a[72:75], a[220:223], v[100:103]\n"
"v_cvt_pk_bf16_f32 v87, v94, v95\n"
"v_mfma_f32_16x16x32_bf16 v[180:183], a[72:75], a[224:227], v[180:183]\n"
"s_waitcnt lgkmcnt(3)\n"
"ds_read_b128 a[100:103], v235 offset:19456\n"
"v_mfma_f32_16x16x32_bf16 v[108:111], a[76:79], a[220:223], v[108:111]\n"
"buffer_store_dwordx4 v[84:87], v224, s[8:11], s90 offen sc1\n"
"v_mfma_f32_16x16x32_bf16 v[188:191], a[76:79], a[224:227], v[188:191]\n"
"ds_read_b128 a[104:107], v235 offset:28672\n"
"v_mfma_f32_16x16x32_bf16 v[100:103], a[80:83], a[228:231], v[100:103]\n"
"v_mfma_f32_16x16x32_bf16 v[180:183], a[80:83], a[232:235], v[180:183]\n"
"s_add_u32 s91, s88, 491520\n"
"ds_read_b128 a[108:111], v235 offset:29696\n"
"v_cvt_pk_bf16_f32 v164, v164, v165\n"
"v_mfma_f32_16x16x32_bf16 v[108:111], a[84:87], a[228:231], v[108:111]\n"
"v_cvt_pk_bf16_f32 v165, v166, v167\n"
"v_cvt_pk_bf16_f32 v166, v172, v173\n"
"v_mfma_f32_16x16x32_bf16 v[188:191], a[84:87], a[232:235], v[188:191]\n"
"s_waitcnt lgkmcnt(3)\n"
"ds_read_b128 a[112:115], v236 offset:6144\n"
"v_cvt_pk_bf16_f32 v167, v174, v175\n"
"v_mfma_f32_16x16x32_bf16 v[100:103], a[88:91], a[236:239], v[100:103]\n"
"buffer_store_dwordx4 v[164:167], v224, s[8:11], s91 offen sc1\n"
"v_mfma_f32_16x16x32_bf16 v[180:183], a[88:91], a[240:243], v[180:183]\n"
"ds_read_b128 a[116:119], v236 offset:7168\n"
"v_mfma_f32_16x16x32_bf16 v[108:111], a[92:95], a[236:239], v[108:111]\n"
"v_mfma_f32_16x16x32_bf16 v[188:191], a[92:95], a[240:243], v[188:191]\n"
"ds_read_b128 a[120:123], v236 offset:16384\n"
"v_mfma_f32_16x16x32_bf16 v[100:103], a[96:99], a[244:247], v[100:103]\n"
"v_mfma_f32_16x16x32_bf16 v[180:183], a[96:99], a[248:251], v[180:183]\n"
"s_waitcnt lgkmcnt(3)\n"
"ds_read_b128 a[124:127], v236 offset:17408\n"
"v_mfma_f32_16x16x32_bf16 v[108:111], a[100:103], a[244:247], v[108:111]\n"
"v_mfma_f32_16x16x32_bf16 v[188:191], a[100:103], a[248:251], v[188:191]\n"
"ds_read_b128 a[64:67], v236 offset:26624\n"
"v_mfma_f32_16x16x32_bf16 v[100:103], a[104:107], a[252:255], v[100:103]\n"
"v_mfma_f32_16x16x32_bf16 v[180:183], a[104:107], a[128:131], v[180:183]\n"
"ds_read_b128 a[68:71], v236 offset:27648\n"
"v_mfma_f32_16x16x32_bf16 v[108:111], a[108:111], a[252:255], v[108:111]\n"
"v_mfma_f32_16x16x32_bf16 v[188:191], a[108:111], a[128:131], v[188:191]\n"
"s_waitcnt lgkmcnt(3)\n"
"ds_read_b128 a[72:75], v237 offset:4096\n"
"v_mfma_f32_16x16x32_bf16 v[100:103], a[112:115], a[132:135], v[100:103]\n"
"v_mfma_f32_16x16x32_bf16 v[180:183], a[112:115], a[136:139], v[180:183]\n"
"ds_read_b128 a[76:79], v237 offset:5120\n"
"v_mfma_f32_16x16x32_bf16 v[108:111], a[116:119], a[132:135], v[108:111]\n"
"v_mfma_f32_16x16x32_bf16 v[188:191], a[116:119], a[136:139], v[188:191]\n"
"ds_read_b128 a[80:83], v237 offset:14336\n"
"v_mfma_f32_16x16x32_bf16 v[100:103], a[120:123], a[140:143], v[100:103]\n"
"v_mfma_f32_16x16x32_bf16 v[180:183], a[120:123], a[144:147], v[180:183]\n"
"s_waitcnt lgkmcnt(3)\n"
"ds_read_b128 a[84:87], v237 offset:15360\n"
"v_mfma_f32_16x16x32_bf16 v[108:111], a[124:127], a[140:143], v[108:111]\n"
"v_mfma_f32_16x16x32_bf16 v[188:191], a[124:127], a[144:147], v[188:191]\n"
"ds_read_b128 a[88:91], v237 offset:24576\n"
"v_mfma_f32_16x16x32_bf16 v[100:103], a[64:67], a[148:151], v[100:103]\n"
"s_waitcnt vmcnt(3)\n"
"buffer_store_dwordx2 v[232:233], v239, s[8:11], s89 offen sc1\n"
"v_mfma_f32_16x16x32_bf16 v[180:183], a[64:67], a[152:155], v[180:183]\n"
"ds_read_b128 a[92:95], v237 offset:25600\n"
"v_mfma_f32_16x16x32_bf16 v[108:111], a[68:71], a[148:151], v[108:111]\n"
"v_mfma_f32_16x16x32_bf16 v[188:191], a[68:71], a[152:155], v[188:191]\n"
"s_waitcnt lgkmcnt(3)\n"
"ds_read_b128 a[96:99], v226 offset:12288\n"
"v_mfma_f32_16x16x32_bf16 v[100:103], a[72:75], a[156:159], v[100:103]\n"
"v_mfma_f32_16x16x32_bf16 v[180:183], a[72:75], a[160:163], v[180:183]\n"
"ds_read_b128 a[100:103], v226 offset:13312\n"
"v_mfma_f32_16x16x32_bf16 v[108:111], a[76:79], a[156:159], v[108:111]\n"
"v_mfma_f32_16x16x32_bf16 v[188:191], a[76:79], a[160:163], v[188:191]\n"
"ds_read_b128 a[104:107], v226 offset:22528\n"
"v_mfma_f32_16x16x32_bf16 v[100:103], a[80:83], a[164:167], v[100:103]\n"
"v_mfma_f32_16x16x32_bf16 v[180:183], a[80:83], a[168:171], v[180:183]\n"
"s_waitcnt lgkmcnt(3)\n"
"ds_read_b128 a[108:111], v226 offset:23552\n"
"v_mfma_f32_16x16x32_bf16 v[108:111], a[84:87], a[164:167], v[108:111]\n"
"v_mfma_f32_16x16x32_bf16 v[188:191], a[84:87], a[168:171], v[188:191]\n"
"ds_read_b128 a[112:115], v235 offset:0\n"
"v_mfma_f32_16x16x32_bf16 v[100:103], a[88:91], a[172:175], v[100:103]\n"
"v_mfma_f32_16x16x32_bf16 v[180:183], a[88:91], a[176:179], v[180:183]\n"
"ds_read_b128 a[116:119], v235 offset:1024\n"
"v_mfma_f32_16x16x32_bf16 v[108:111], a[92:95], a[172:175], v[108:111]\n"
"v_mfma_f32_16x16x32_bf16 v[188:191], a[92:95], a[176:179], v[188:191]\n"
"s_waitcnt lgkmcnt(3)\n"
"ds_read_b128 a[120:123], v235 offset:10240\n"
"v_mfma_f32_16x16x32_bf16 v[116:119], a[96:99], a[212:215], v[116:119]\n"
"v_mfma_f32_16x16x32_bf16 v[196:199], a[96:99], a[216:219], v[196:199]\n"
"ds_read_b128 a[124:127], v235 offset:11264\n"
"v_mfma_f32_16x16x32_bf16 v[124:127], a[100:103], a[212:215], v[124:127]\n"
"s_add_u32 s90, s88, 328704\n"
"v_cvt_pk_bf16_f32 v100, v100, v101\n"
"v_cvt_pk_bf16_f32 v101, v102, v103\n"
"v_mfma_f32_16x16x32_bf16 v[204:207], a[100:103], a[216:219], v[204:207]\n"
"ds_read_b128 a[64:67], v235 offset:20480\n"
"v_cvt_pk_bf16_f32 v102, v108, v109\n"
"v_mfma_f32_16x16x32_bf16 v[116:119], a[104:107], a[220:223], v[116:119]\n"
"s_waitcnt vmcnt(3)\n"
"v_cmp_eq_u64 vcc, s[28:29], v[240:241]\n"
"s_cmp_eq_u64 vcc, exec\n"
"s_cbranch_scc0 L1052_%=\n"
"L1053_%=:\n"
"v_mfma_f32_16x16x32_bf16 v[196:199], a[104:107], a[224:227], v[196:199]\n"
"s_waitcnt lgkmcnt(3)\n"
"ds_read_b128 a[68:71], v235 offset:21504\n"
"v_cvt_pk_bf16_f32 v103, v110, v111\n"
"v_mfma_f32_16x16x32_bf16 v[124:127], a[108:111], a[220:223], v[124:127]\n"
"buffer_store_dwordx4 v[100:103], v224, s[8:11], s90 offen sc1\n"
"v_mfma_f32_16x16x32_bf16 v[204:207], a[108:111], a[224:227], v[204:207]\n"
"ds_read_b128 a[72:75], v235 offset:30720\n"
"v_mfma_f32_16x16x32_bf16 v[116:119], a[112:115], a[228:231], v[116:119]\n"
"buffer_load_dwordx4 v[112:115], v176, s[12:15], s38 offen\n"
"v_mfma_f32_16x16x32_bf16 v[196:199], a[112:115], a[232:235], v[196:199]\n"
"s_add_u32 s91, s88, 492544\n"
"ds_read_b128 a[76:79], v235 offset:31744\n"
"v_cvt_pk_bf16_f32 v180, v180, v181\n"
"v_mfma_f32_16x16x32_bf16 v[124:127], a[116:119], a[228:231], v[124:127]\n"
"buffer_load_dwordx4 v[144:147], v178, s[8:11], s36 offen sc1\n"
"v_mfma_f32_16x16x32_bf16 v[204:207], a[116:119], a[232:235], v[204:207]\n"
"s_waitcnt lgkmcnt(3)\n"
"ds_read_b128 a[80:83], v236 offset:8192\n"
"v_cvt_pk_bf16_f32 v181, v182, v183\n"
"v_mfma_f32_16x16x32_bf16 v[116:119], a[120:123], a[236:239], v[116:119]\n"
"v_cvt_pk_bf16_f32 v182, v188, v189\n"
"v_cvt_pk_bf16_f32 v183, v190, v191\n"
"v_mfma_f32_16x16x32_bf16 v[196:199], a[120:123], a[240:243], v[196:199]\n"
"buffer_store_dwordx4 v[180:183], v224, s[8:11], s91 offen sc1\n"
"v_mfma_f32_16x16x32_bf16 v[124:127], a[124:127], a[236:239], v[124:127]\n"
"ds_read_b128 a[84:87], v236 offset:9216\n"
"v_mfma_f32_16x16x32_bf16 v[204:207], a[124:127], a[240:243], v[204:207]\n"
"buffer_load_dwordx4 v[120:123], v176, s[12:15], s55 offen\n"
"v_mfma_f32_16x16x32_bf16 v[116:119], a[64:67], a[244:247], v[116:119]\n"
"ds_read_b128 a[88:91], v236 offset:18432\n"
"v_mfma_f32_16x16x32_bf16 v[196:199], a[64:67], a[248:251], v[196:199]\n"
"s_waitcnt lgkmcnt(3)\n"
"buffer_load_dwordx4 v[152:155], v178, s[8:11], s37 offen sc1\n"
"v_mfma_f32_16x16x32_bf16 v[124:127], a[68:71], a[244:247], v[124:127]\n"
"ds_read_b128 a[92:95], v236 offset:19456\n"
"v_mfma_f32_16x16x32_bf16 v[204:207], a[68:71], a[248:251], v[204:207]\n"
"ds_read_b128 a[96:99], v236 offset:28672\n"
"v_mfma_f32_16x16x32_bf16 v[116:119], a[72:75], a[252:255], v[116:119]\n"
"buffer_load_dwordx4 v[128:131], v177, s[12:15], s38 offen\n"
"v_mfma_f32_16x16x32_bf16 v[196:199], a[72:75], a[128:131], v[196:199]\n"
"ds_read_b128 a[100:103], v236 offset:29696\n"
"v_mfma_f32_16x16x32_bf16 v[124:127], a[76:79], a[252:255], v[124:127]\n"
"buffer_load_dwordx4 v[160:163], v179, s[8:11], s36 offen sc1\n"
"v_mfma_f32_16x16x32_bf16 v[204:207], a[76:79], a[128:131], v[204:207]\n"
"s_waitcnt lgkmcnt(3)\n"
"ds_read_b128 a[104:107], v237 offset:6144\n"
"v_mfma_f32_16x16x32_bf16 v[116:119], a[80:83], a[132:135], v[116:119]\n"
"v_mfma_f32_16x16x32_bf16 v[196:199], a[80:83], a[136:139], v[196:199]\n"
"ds_read_b128 a[108:111], v237 offset:7168\n"
"v_mfma_f32_16x16x32_bf16 v[124:127], a[84:87], a[132:135], v[124:127]\n"
"buffer_load_dwordx4 v[136:139], v177, s[12:15], s55 offen\n"
"v_mfma_f32_16x16x32_bf16 v[204:207], a[84:87], a[136:139], v[204:207]\n"
"ds_read_b128 a[112:115], v237 offset:16384\n"
"v_mfma_f32_16x16x32_bf16 v[116:119], a[88:91], a[140:143], v[116:119]\n"
"buffer_load_dwordx4 v[168:171], v179, s[8:11], s37 offen sc1\n"
"v_mfma_f32_16x16x32_bf16 v[196:199], a[88:91], a[144:147], v[196:199]\n"
"s_waitcnt lgkmcnt(3)\n"
"ds_read_b128 a[116:119], v237 offset:17408\n"
"v_mfma_f32_16x16x32_bf16 v[124:127], a[92:95], a[140:143], v[124:127]\n"
"v_mfma_f32_16x16x32_bf16 v[204:207], a[92:95], a[144:147], v[204:207]\n"
"ds_read_b128 a[120:123], v237 offset:26624\n"
"v_mfma_f32_16x16x32_bf16 v[116:119], a[96:99], a[148:151], v[116:119]\n"
"v_mfma_f32_16x16x32_bf16 v[196:199], a[96:99], a[152:155], v[196:199]\n"
"ds_read_b128 a[124:127], v237 offset:27648\n"
"v_mfma_f32_16x16x32_bf16 v[124:127], a[100:103], a[148:151], v[124:127]\n"
"s_lshr_b32 s93, s31, 3\n"
"v_mfma_f32_16x16x32_bf16 v[204:207], a[100:103], a[152:155], v[204:207]\n"
"s_waitcnt lgkmcnt(3)\n"
"ds_read_b128 a[64:67], v226 offset:14336\n"
"s_lshl_b32 s93, s93, 4\n"
"v_mfma_f32_16x16x32_bf16 v[116:119], a[104:107], a[156:159], v[116:119]\n"
"s_add_u32 s93, s93, s32\n"
"v_mfma_f32_16x16x32_bf16 v[196:199], a[104:107], a[160:163], v[196:199]\n"
"s_mul_i32 s94, s93, 40960\n"
"ds_read_b128 a[68:71], v226 offset:15360\n"
"v_mfma_f32_16x16x32_bf16 v[124:127], a[108:111], a[156:159], v[124:127]\n"
"s_lshl_b32 s93, s93, 6\n"
"v_mfma_f32_16x16x32_bf16 v[204:207], a[108:111], a[160:163], v[204:207]\n"
"s_add_u32 s93, s93, 23296\n"
"ds_read_b128 a[72:75], v226 offset:24576\n"
"v_mfma_f32_16x16x32_bf16 v[116:119], a[112:115], a[164:167], v[116:119]\n"
"s_add_u32 s94, s94, 60817408\n"
"v_mfma_f32_16x16x32_bf16 v[196:199], a[112:115], a[168:171], v[196:199]\n"
"s_waitcnt lgkmcnt(3)\n"
"ds_read_b128 a[76:79], v226 offset:25600\n"
"s_lshl_b32 s92, s30, 10\n"
"v_mfma_f32_16x16x32_bf16 v[124:127], a[116:119], a[164:167], v[124:127]\n"
"v_add_u32 v244, s92, v224\n"
"v_mfma_f32_16x16x32_bf16 v[204:207], a[116:119], a[168:171], v[204:207]\n"
"s_add_u32 s92, s45, 4096\n"
"ds_read_b128 a[80:83], v235 offset:2048\n"
"v_mfma_f32_16x16x32_bf16 v[116:119], a[120:123], a[172:175], v[116:119]\n"
"v_add_u32 v245, s92, v224\n"
"v_mfma_f32_16x16x32_bf16 v[196:199], a[120:123], a[176:179], v[196:199]\n"
"ds_read_b128 a[84:87], v235 offset:3072\n"
"v_mfma_f32_16x16x32_bf16 v[124:127], a[124:127], a[172:175], v[124:127]\n"
"v_mfma_f32_16x16x32_bf16 v[204:207], a[124:127], a[176:179], v[204:207]\n"
"s_waitcnt lgkmcnt(3)\n"
"ds_read_b128 a[88:91], v235 offset:12288\n"
"v_mfma_f32_16x16x32_bf16 v[132:135], a[64:67], a[212:215], v[132:135]\n"
"v_mfma_f32_16x16x32_bf16 v[208:211], a[64:67], a[216:219], v[208:211]\n"
"ds_read_b128 a[92:95], v235 offset:13312\n"
"v_mfma_f32_16x16x32_bf16 v[140:143], a[68:71], a[212:215], v[140:143]\n"
"s_add_u32 s90, s88, 329728\n"
"v_cvt_pk_bf16_f32 v116, v116, v117\n"
"v_cvt_pk_bf16_f32 v117, v118, v119\n"
"v_mfma_f32_16x16x32_bf16 v[212:215], a[68:71], a[216:219], v[212:215]\n"
"ds_read_b128 a[96:99], v235 offset:22528\n"
"v_cvt_pk_bf16_f32 v118, v124, v125\n"
"v_mfma_f32_16x16x32_bf16 v[132:135], a[72:75], a[220:223], v[132:135]\n"
"v_cvt_pk_bf16_f32 v119, v126, v127\n"
"v_mfma_f32_16x16x32_bf16 v[208:211], a[72:75], a[224:227], v[208:211]\n"
"s_waitcnt lgkmcnt(3)\n"
"ds_read_b128 a[100:103], v235 offset:23552\n"
"v_mfma_f32_16x16x32_bf16 v[140:143], a[76:79], a[220:223], v[140:143]\n"
"buffer_store_dwordx4 v[116:119], v224, s[8:11], s90 offen sc1\n"
"v_mfma_f32_16x16x32_bf16 v[212:215], a[76:79], a[224:227], v[212:215]\n"
"ds_read_b128 a[104:107], v236 offset:0\n"
"v_mfma_f32_16x16x32_bf16 v[132:135], a[80:83], a[228:231], v[132:135]\n"
"v_mfma_f32_16x16x32_bf16 v[208:211], a[80:83], a[232:235], v[208:211]\n"
"s_add_u32 s91, s88, 493568\n"
"ds_read_b128 a[108:111], v236 offset:1024\n"
"v_cvt_pk_bf16_f32 v196, v196, v197\n"
"v_mfma_f32_16x16x32_bf16 v[140:143], a[84:87], a[228:231], v[140:143]\n"
"v_cvt_pk_bf16_f32 v197, v198, v199\n"
"v_cvt_pk_bf16_f32 v198, v204, v205\n"
"v_mfma_f32_16x16x32_bf16 v[212:215], a[84:87], a[232:235], v[212:215]\n"
"s_waitcnt lgkmcnt(3)\n"
"ds_read_b128 a[112:115], v236 offset:10240\n"
"v_cvt_pk_bf16_f32 v199, v206, v207\n"
"v_mfma_f32_16x16x32_bf16 v[132:135], a[88:91], a[236:239], v[132:135]\n"
"buffer_store_dwordx4 v[196:199], v224, s[8:11], s91 offen sc1\n"
"v_mfma_f32_16x16x32_bf16 v[208:211], a[88:91], a[240:243], v[208:211]\n"
"ds_read_b128 a[116:119], v236 offset:11264\n"
"v_mfma_f32_16x16x32_bf16 v[140:143], a[92:95], a[236:239], v[140:143]\n"
"v_mfma_f32_16x16x32_bf16 v[212:215], a[92:95], a[240:243], v[212:215]\n"
"ds_read_b128 a[120:123], v236 offset:20480\n"
"v_mfma_f32_16x16x32_bf16 v[132:135], a[96:99], a[244:247], v[132:135]\n"
"v_mfma_f32_16x16x32_bf16 v[208:211], a[96:99], a[248:251], v[208:211]\n"
"s_waitcnt lgkmcnt(3)\n"
"ds_read_b128 a[124:127], v236 offset:21504\n"
"v_mfma_f32_16x16x32_bf16 v[140:143], a[100:103], a[244:247], v[140:143]\n"
"v_mfma_f32_16x16x32_bf16 v[212:215], a[100:103], a[248:251], v[212:215]\n"
"ds_read_b128 a[64:67], v236 offset:30720\n"
"v_mfma_f32_16x16x32_bf16 v[132:135], a[104:107], a[252:255], v[132:135]\n"
"v_mfma_f32_16x16x32_bf16 v[208:211], a[104:107], a[128:131], v[208:211]\n"
"ds_read_b128 a[68:71], v236 offset:31744\n"
"v_mfma_f32_16x16x32_bf16 v[140:143], a[108:111], a[252:255], v[140:143]\n"
"v_mfma_f32_16x16x32_bf16 v[212:215], a[108:111], a[128:131], v[212:215]\n"
"s_waitcnt lgkmcnt(3)\n"
"ds_read_b128 a[72:75], v237 offset:8192\n"
"v_mfma_f32_16x16x32_bf16 v[132:135], a[112:115], a[132:135], v[132:135]\n"
"v_mfma_f32_16x16x32_bf16 v[208:211], a[112:115], a[136:139], v[208:211]\n"
"ds_read_b128 a[76:79], v237 offset:9216\n"
"v_mfma_f32_16x16x32_bf16 v[140:143], a[116:119], a[132:135], v[140:143]\n"
"v_mfma_f32_16x16x32_bf16 v[212:215], a[116:119], a[136:139], v[212:215]\n"
"ds_read_b128 a[80:83], v237 offset:18432\n"
"v_mfma_f32_16x16x32_bf16 v[132:135], a[120:123], a[140:143], v[132:135]\n"
"v_mfma_f32_16x16x32_bf16 v[208:211], a[120:123], a[144:147], v[208:211]\n"
"s_waitcnt lgkmcnt(3)\n"
"ds_read_b128 a[84:87], v237 offset:19456\n"
"v_mfma_f32_16x16x32_bf16 v[140:143], a[124:127], a[140:143], v[140:143]\n"
"v_mfma_f32_16x16x32_bf16 v[212:215], a[124:127], a[144:147], v[212:215]\n"
"ds_read_b128 a[88:91], v237 offset:28672\n"
"v_mfma_f32_16x16x32_bf16 v[132:135], a[64:67], a[148:151], v[132:135]\n"
"v_mfma_f32_16x16x32_bf16 v[208:211], a[64:67], a[152:155], v[208:211]\n"
"ds_read_b128 a[92:95], v237 offset:29696\n"
"v_mfma_f32_16x16x32_bf16 v[140:143], a[68:71], a[148:151], v[140:143]\n"
"v_mfma_f32_16x16x32_bf16 v[212:215], a[68:71], a[152:155], v[212:215]\n"
"s_waitcnt lgkmcnt(3)\n"
"ds_read_b128 a[96:99], v226 offset:16384\n"
"v_mfma_f32_16x16x32_bf16 v[132:135], a[72:75], a[156:159], v[132:135]\n"
"v_mfma_f32_16x16x32_bf16 v[208:211], a[72:75], a[160:163], v[208:211]\n"
"ds_read_b128 a[100:103], v226 offset:17408\n"
"v_mfma_f32_16x16x32_bf16 v[140:143], a[76:79], a[156:159], v[140:143]\n"
"v_mfma_f32_16x16x32_bf16 v[212:215], a[76:79], a[160:163], v[212:215]\n"
"ds_read_b128 a[104:107], v226 offset:26624\n"
"v_mfma_f32_16x16x32_bf16 v[132:135], a[80:83], a[164:167], v[132:135]\n"
"v_mfma_f32_16x16x32_bf16 v[208:211], a[80:83], a[168:171], v[208:211]\n"
"s_waitcnt lgkmcnt(3)\n"
"ds_read_b128 a[108:111], v226 offset:27648\n"
"v_mfma_f32_16x16x32_bf16 v[140:143], a[84:87], a[164:167], v[140:143]\n"
"v_mfma_f32_16x16x32_bf16 v[212:215], a[84:87], a[168:171], v[212:215]\n"
"ds_read_b128 a[112:115], v235 offset:4096\n"
"v_mfma_f32_16x16x32_bf16 v[132:135], a[88:91], a[172:175], v[132:135]\n"
"v_mfma_f32_16x16x32_bf16 v[208:211], a[88:91], a[176:179], v[208:211]\n"
"ds_read_b128 a[116:119], v235 offset:5120\n"
"v_mfma_f32_16x16x32_bf16 v[140:143], a[92:95], a[172:175], v[140:143]\n"
"buffer_load_dwordx2 v[240:241], v243, s[8:11], s93 offen sc1\n"
"v_mfma_f32_16x16x32_bf16 v[212:215], a[92:95], a[176:179], v[212:215]\n"
"s_waitcnt lgkmcnt(3)\n"
"ds_read_b128 a[120:123], v235 offset:14336\n"
"v_mfma_f32_16x16x32_bf16 v[148:151], a[96:99], a[212:215], v[148:151]\n"
"v_mfma_f32_16x16x32_bf16 v[216:219], a[96:99], a[216:219], v[216:219]\n"
"ds_read_b128 a[124:127], v235 offset:15360\n"
"v_mfma_f32_16x16x32_bf16 v[156:159], a[100:103], a[212:215], v[156:159]\n"
"s_add_u32 s90, s88, 330752\n"
"v_cvt_pk_bf16_f32 v132, v132, v133\n"
"v_cvt_pk_bf16_f32 v133, v134, v135\n"
"v_mfma_f32_16x16x32_bf16 v[220:223], a[100:103], a[216:219], v[220:223]\n"
"ds_read_b128 a[64:67], v235 offset:24576\n"
"v_cvt_pk_bf16_f32 v134, v140, v141\n"
"v_mfma_f32_16x16x32_bf16 v[148:151], a[104:107], a[220:223], v[148:151]\n"
"v_cvt_pk_bf16_f32 v135, v142, v143\n"
"v_mfma_f32_16x16x32_bf16 v[216:219], a[104:107], a[224:227], v[216:219]\n"
"s_waitcnt lgkmcnt(3)\n"
"ds_read_b128 a[68:71], v235 offset:25600\n"
"v_mfma_f32_16x16x32_bf16 v[156:159], a[108:111], a[220:223], v[156:159]\n"
"buffer_store_dwordx4 v[132:135], v224, s[8:11], s90 offen sc1\n"
"v_mfma_f32_16x16x32_bf16 v[220:223], a[108:111], a[224:227], v[220:223]\n"
"ds_read_b128 a[72:75], v236 offset:2048\n"
"v_mfma_f32_16x16x32_bf16 v[148:151], a[112:115], a[228:231], v[148:151]\n"
"v_mfma_f32_16x16x32_bf16 v[216:219], a[112:115], a[232:235], v[216:219]\n"
"s_add_u32 s91, s88, 494592\n"
"ds_read_b128 a[76:79], v236 offset:3072\n"
"v_cvt_pk_bf16_f32 v208, v208, v209\n"
"v_mfma_f32_16x16x32_bf16 v[156:159], a[116:119], a[228:231], v[156:159]\n"
"v_cvt_pk_bf16_f32 v209, v210, v211\n"
"v_cvt_pk_bf16_f32 v210, v212, v213\n"
"v_mfma_f32_16x16x32_bf16 v[220:223], a[116:119], a[232:235], v[220:223]\n"
"s_waitcnt lgkmcnt(3)\n"
"ds_read_b128 a[80:83], v236 offset:12288\n"
"v_cvt_pk_bf16_f32 v211, v214, v215\n"
"v_mfma_f32_16x16x32_bf16 v[148:151], a[120:123], a[236:239], v[148:151]\n"
"buffer_store_dwordx4 v[208:211], v224, s[8:11], s91 offen sc1\n"
"v_mfma_f32_16x16x32_bf16 v[216:219], a[120:123], a[240:243], v[216:219]\n"
"ds_read_b128 a[84:87], v236 offset:13312\n"
"v_mfma_f32_16x16x32_bf16 v[156:159], a[124:127], a[236:239], v[156:159]\n"
"v_mfma_f32_16x16x32_bf16 v[220:223], a[124:127], a[240:243], v[220:223]\n"
"ds_read_b128 a[88:91], v236 offset:22528\n"
"v_mfma_f32_16x16x32_bf16 v[148:151], a[64:67], a[244:247], v[148:151]\n"
"v_mfma_f32_16x16x32_bf16 v[216:219], a[64:67], a[248:251], v[216:219]\n"
"s_waitcnt lgkmcnt(3)\n"
"ds_read_b128 a[92:95], v236 offset:23552\n"
"v_mfma_f32_16x16x32_bf16 v[156:159], a[68:71], a[244:247], v[156:159]\n"
"v_mfma_f32_16x16x32_bf16 v[220:223], a[68:71], a[248:251], v[220:223]\n"
"ds_read_b128 a[96:99], v237 offset:0\n"
"v_mfma_f32_16x16x32_bf16 v[148:151], a[72:75], a[252:255], v[148:151]\n"
"v_mfma_f32_16x16x32_bf16 v[216:219], a[72:75], a[128:131], v[216:219]\n"
"ds_read_b128 a[100:103], v237 offset:1024\n"
"v_mfma_f32_16x16x32_bf16 v[156:159], a[76:79], a[252:255], v[156:159]\n"
"v_mfma_f32_16x16x32_bf16 v[220:223], a[76:79], a[128:131], v[220:223]\n"
"s_waitcnt lgkmcnt(3)\n"
"ds_read_b128 a[104:107], v237 offset:10240\n"
"v_mfma_f32_16x16x32_bf16 v[148:151], a[80:83], a[132:135], v[148:151]\n"
"v_mfma_f32_16x16x32_bf16 v[216:219], a[80:83], a[136:139], v[216:219]\n"
"ds_read_b128 a[108:111], v237 offset:11264\n"
"v_mfma_f32_16x16x32_bf16 v[156:159], a[84:87], a[132:135], v[156:159]\n"
"v_mfma_f32_16x16x32_bf16 v[220:223], a[84:87], a[136:139], v[220:223]\n"
"ds_read_b128 a[112:115], v237 offset:20480\n"
"v_mfma_f32_16x16x32_bf16 v[148:151], a[88:91], a[140:143], v[148:151]\n"
"v_mfma_f32_16x16x32_bf16 v[216:219], a[88:91], a[144:147], v[216:219]\n"
"s_waitcnt lgkmcnt(3)\n"
"ds_read_b128 a[116:119], v237 offset:21504\n"
"v_mfma_f32_16x16x32_bf16 v[156:159], a[92:95], a[140:143], v[156:159]\n"
"v_mfma_f32_16x16x32_bf16 v[220:223], a[92:95], a[144:147], v[220:223]\n"
"ds_read_b128 a[120:123], v237 offset:30720\n"
"v_mfma_f32_16x16x32_bf16 v[148:151], a[96:99], a[148:151], v[148:151]\n"
"v_mfma_f32_16x16x32_bf16 v[216:219], a[96:99], a[152:155], v[216:219]\n"
"ds_read_b128 a[124:127], v237 offset:31744\n"
"v_mfma_f32_16x16x32_bf16 v[156:159], a[100:103], a[148:151], v[156:159]\n"
"v_mfma_f32_16x16x32_bf16 v[220:223], a[100:103], a[152:155], v[220:223]\n"
"s_waitcnt lgkmcnt(3)\n"
"v_mfma_f32_16x16x32_bf16 v[148:151], a[104:107], a[156:159], v[148:151]\n"
"v_mfma_f32_16x16x32_bf16 v[216:219], a[104:107], a[160:163], v[216:219]\n"
"v_mfma_f32_16x16x32_bf16 v[156:159], a[108:111], a[156:159], v[156:159]\n"
"v_mfma_f32_16x16x32_bf16 v[220:223], a[108:111], a[160:163], v[220:223]\n"
"v_mfma_f32_16x16x32_bf16 v[148:151], a[112:115], a[164:167], v[148:151]\n"
"v_mfma_f32_16x16x32_bf16 v[216:219], a[112:115], a[168:171], v[216:219]\n"
"s_waitcnt lgkmcnt(0)\n"
"v_mfma_f32_16x16x32_bf16 v[156:159], a[116:119], a[164:167], v[156:159]\n"
"v_mfma_f32_16x16x32_bf16 v[220:223], a[116:119], a[168:171], v[220:223]\n"
"v_mfma_f32_16x16x32_bf16 v[148:151], a[120:123], a[172:175], v[148:151]\n"
"v_mfma_f32_16x16x32_bf16 v[216:219], a[120:123], a[176:179], v[216:219]\n"
"v_mfma_f32_16x16x32_bf16 v[156:159], a[124:127], a[172:175], v[156:159]\n"
"v_mfma_f32_16x16x32_bf16 v[220:223], a[124:127], a[176:179], v[220:223]\n"
"s_nop 5\n"
"v_cvt_pk_bf16_f32 v148, v148, v149\n"
"v_cvt_pk_bf16_f32 v149, v150, v151\n"
"v_cvt_pk_bf16_f32 v150, v156, v157\n"
"v_cvt_pk_bf16_f32 v151, v158, v159\n"
"s_add_u32 s90, s88, 331776\n"
"buffer_store_dwordx4 v[148:151], v224, s[8:11], s90 offen sc1\n"
"v_cvt_pk_bf16_f32 v216, v216, v217\n"
"v_cvt_pk_bf16_f32 v217, v218, v219\n"
"v_cvt_pk_bf16_f32 v218, v220, v221\n"
"v_cvt_pk_bf16_f32 v219, v222, v223\n"
"s_add_u32 s91, s88, 495616\n"
"buffer_store_dwordx4 v[216:219], v224, s[8:11], s91 offen sc1\n"
"s_waitcnt vmcnt(4)\n"
"v_cmp_eq_u64 vcc, s[28:29], v[240:241]\n"
"s_cmp_eq_u64 vcc, exec\n"
"s_cbranch_scc0 L1055_%=\n"
"L1056_%=:\n"
"s_add_u32 s92, s94, 0\n"
"buffer_load_dwordx4 a[0:3], v244, s[8:11], s92 offen sc1\n"
"buffer_load_dwordx4 a[32:35], v245, s[8:11], s92 offen sc1\n"
"s_add_u32 s92, s94, 5120\n"
"buffer_load_dwordx4 a[4:7], v244, s[8:11], s92 offen sc1\n"
"buffer_load_dwordx4 a[36:39], v245, s[8:11], s92 offen sc1\n"
"s_add_u32 s92, s94, 10240\n"
"buffer_load_dwordx4 a[8:11], v244, s[8:11], s92 offen sc1\n"
"buffer_load_dwordx4 a[40:43], v245, s[8:11], s92 offen sc1\n"
"s_add_u32 s92, s94, 15360\n"
"buffer_load_dwordx4 a[12:15], v244, s[8:11], s92 offen sc1\n"
"buffer_load_dwordx4 a[44:47], v245, s[8:11], s92 offen sc1\n"
"s_add_u32 s92, s94, 20480\n"
"buffer_load_dwordx4 a[16:19], v244, s[8:11], s92 offen sc1\n"
"buffer_load_dwordx4 a[48:51], v245, s[8:11], s92 offen sc1\n"
"s_add_u32 s92, s94, 25600\n"
"buffer_load_dwordx4 a[20:23], v244, s[8:11], s92 offen sc1\n"
"buffer_load_dwordx4 a[52:55], v245, s[8:11], s92 offen sc1\n"
"s_add_u32 s92, s94, 30720\n"
"buffer_load_dwordx4 a[24:27], v244, s[8:11], s92 offen sc1\n"
"buffer_load_dwordx4 a[56:59], v245, s[8:11], s92 offen sc1\n"
"s_add_u32 s92, s94, 35840\n"
"buffer_load_dwordx4 a[28:31], v244, s[8:11], s92 offen sc1\n"
"buffer_load_dwordx4 a[60:63], v245, s[8:11], s92 offen sc1\n"
"s_add_u32 s89, s89, 512\n"
"s_waitcnt vmcnt(16)\n"
"buffer_store_dwordx2 v[232:233], v239, s[8:11], s89 offen sc1\n"
"buffer_load_dwordx2 v[240:241], v243, s[8:11], s93 offen offset:512 sc1\n"
"v_mfma_f32_16x16x32_bf16 v[0:3], v[96:99], v[112:115], 0\n"
"v_mfma_f32_16x16x32_bf16 v[4:7], v[104:107], v[112:115], 0\n"
"v_mfma_f32_16x16x32_bf16 v[8:11], v[96:99], v[120:123], 0\n"
"v_mfma_f32_16x16x32_bf16 v[12:15], v[104:107], v[120:123], 0\n"
"v_mfma_f32_16x16x32_bf16 v[0:3], v[96:99], v[144:147], v[0:3]\n"
"v_mfma_f32_16x16x32_bf16 v[4:7], v[104:107], v[144:147], v[4:7]\n"
"v_mfma_f32_16x16x32_bf16 v[8:11], v[96:99], v[152:155], v[8:11]\n"
"v_mfma_f32_16x16x32_bf16 v[12:15], v[104:107], v[152:155], v[12:15]\n"
"v_mfma_f32_16x16x32_bf16 v[16:19], v[96:99], v[128:131], 0\n"
"v_mfma_f32_16x16x32_bf16 v[20:23], v[104:107], v[128:131], 0\n"
"v_mfma_f32_16x16x32_bf16 v[24:27], v[96:99], v[136:139], 0\n"
"v_mfma_f32_16x16x32_bf16 v[28:31], v[104:107], v[136:139], 0\n"
"v_mfma_f32_16x16x32_bf16 v[16:19], v[96:99], v[160:163], v[16:19]\n"
"v_mfma_f32_16x16x32_bf16 v[20:23], v[104:107], v[160:163], v[20:23]\n"
"v_mfma_f32_16x16x32_bf16 v[24:27], v[96:99], v[168:171], v[24:27]\n"
"v_mfma_f32_16x16x32_bf16 v[28:31], v[104:107], v[168:171], v[28:31]\n"
"s_waitcnt vmcnt(16)\n"
"v_mfma_f32_16x16x32_bf16 v[0:3], v[80:83], a[0:3], v[0:3]\n"
"v_mfma_f32_16x16x32_bf16 v[4:7], v[88:91], a[0:3], v[4:7]\n"
"v_mfma_f32_16x16x32_bf16 v[8:11], v[80:83], a[32:35], v[8:11]\n"
"v_mfma_f32_16x16x32_bf16 v[12:15], v[88:91], a[32:35], v[12:15]\n"
"s_waitcnt vmcnt(14)\n"
"v_mfma_f32_16x16x32_bf16 v[0:3], v[80:83], a[4:7], v[0:3]\n"
"v_mfma_f32_16x16x32_bf16 v[4:7], v[88:91], a[4:7], v[4:7]\n"
"v_mfma_f32_16x16x32_bf16 v[8:11], v[80:83], a[36:39], v[8:11]\n"
"v_mfma_f32_16x16x32_bf16 v[12:15], v[88:91], a[36:39], v[12:15]\n"
"s_waitcnt vmcnt(12)\n"
"v_mfma_f32_16x16x32_bf16 v[0:3], v[80:83], a[8:11], v[0:3]\n"
"v_mfma_f32_16x16x32_bf16 v[4:7], v[88:91], a[8:11], v[4:7]\n"
"v_mfma_f32_16x16x32_bf16 v[8:11], v[80:83], a[40:43], v[8:11]\n"
"v_mfma_f32_16x16x32_bf16 v[12:15], v[88:91], a[40:43], v[12:15]\n"
"s_waitcnt vmcnt(10)\n"
"v_mfma_f32_16x16x32_bf16 v[0:3], v[80:83], a[12:15], v[0:3]\n"
"v_mfma_f32_16x16x32_bf16 v[4:7], v[88:91], a[12:15], v[4:7]\n"
"v_mfma_f32_16x16x32_bf16 v[8:11], v[80:83], a[44:47], v[8:11]\n"
"v_mfma_f32_16x16x32_bf16 v[12:15], v[88:91], a[44:47], v[12:15]\n"
"s_waitcnt vmcnt(8)\n"
"v_mfma_f32_16x16x32_bf16 v[0:3], v[80:83], a[16:19], v[0:3]\n"
"v_mfma_f32_16x16x32_bf16 v[4:7], v[88:91], a[16:19], v[4:7]\n"
"v_mfma_f32_16x16x32_bf16 v[8:11], v[80:83], a[48:51], v[8:11]\n"
"v_mfma_f32_16x16x32_bf16 v[12:15], v[88:91], a[48:51], v[12:15]\n"
"s_waitcnt vmcnt(6)\n"
"v_mfma_f32_16x16x32_bf16 v[0:3], v[80:83], a[20:23], v[0:3]\n"
"v_mfma_f32_16x16x32_bf16 v[4:7], v[88:91], a[20:23], v[4:7]\n"
"v_mfma_f32_16x16x32_bf16 v[8:11], v[80:83], a[52:55], v[8:11]\n"
"v_mfma_f32_16x16x32_bf16 v[12:15], v[88:91], a[52:55], v[12:15]\n"
"s_waitcnt vmcnt(4)\n"
"v_mfma_f32_16x16x32_bf16 v[0:3], v[80:83], a[24:27], v[0:3]\n"
"v_mfma_f32_16x16x32_bf16 v[4:7], v[88:91], a[24:27], v[4:7]\n"
"v_mfma_f32_16x16x32_bf16 v[8:11], v[80:83], a[56:59], v[8:11]\n"
"v_mfma_f32_16x16x32_bf16 v[12:15], v[88:91], a[56:59], v[12:15]\n"
"s_waitcnt vmcnt(2)\n"
"v_mfma_f32_16x16x32_bf16 v[0:3], v[80:83], a[28:31], v[0:3]\n"
"v_mfma_f32_16x16x32_bf16 v[4:7], v[88:91], a[28:31], v[4:7]\n"
"v_mfma_f32_16x16x32_bf16 v[8:11], v[80:83], a[60:63], v[8:11]\n"
"v_mfma_f32_16x16x32_bf16 v[12:15], v[88:91], a[60:63], v[12:15]\n"
"s_nop 5\n"
"v_cvt_pk_bf16_f32 v0, v0, v1\n"
"v_cvt_pk_bf16_f32 v1, v2, v3\n"
"v_cvt_pk_bf16_f32 v2, v4, v5\n"
"v_cvt_pk_bf16_f32 v3, v6, v7\n"
"buffer_store_dwordx4 v[0:3], v176, s[16:19], s38 offen\n"
"v_cvt_pk_bf16_f32 v8, v8, v9\n"
"v_cvt_pk_bf16_f32 v9, v10, v11\n"
"v_cvt_pk_bf16_f32 v10, v12, v13\n"
"v_cvt_pk_bf16_f32 v11, v14, v15\n"
"buffer_store_dwordx4 v[8:11], v176, s[16:19], s55 offen\n"
"s_waitcnt vmcnt(2)\n"
"v_cmp_eq_u64 vcc, s[28:29], v[240:241]\n"
"s_cmp_eq_u64 vcc, exec\n"
"s_cbranch_scc0 L1058_%=\n"
"L1059_%=:\n"
"s_add_u32 s92, s94, 327680\n"
"buffer_load_dwordx4 a[64:67], v244, s[8:11], s92 offen sc1\n"
"buffer_load_dwordx4 a[96:99], v245, s[8:11], s92 offen sc1\n"
"s_add_u32 s92, s94, 332800\n"
"buffer_load_dwordx4 a[68:71], v244, s[8:11], s92 offen sc1\n"
"buffer_load_dwordx4 a[100:103], v245, s[8:11], s92 offen sc1\n"
"s_add_u32 s92, s94, 337920\n"
"buffer_load_dwordx4 a[72:75], v244, s[8:11], s92 offen sc1\n"
"buffer_load_dwordx4 a[104:107], v245, s[8:11], s92 offen sc1\n"
"s_add_u32 s92, s94, 343040\n"
"buffer_load_dwordx4 a[76:79], v244, s[8:11], s92 offen sc1\n"
"buffer_load_dwordx4 a[108:111], v245, s[8:11], s92 offen sc1\n"
"s_add_u32 s92, s94, 348160\n"
"buffer_load_dwordx4 a[80:83], v244, s[8:11], s92 offen sc1\n"
"buffer_load_dwordx4 a[112:115], v245, s[8:11], s92 offen sc1\n"
"s_add_u32 s92, s94, 353280\n"
"buffer_load_dwordx4 a[84:87], v244, s[8:11], s92 offen sc1\n"
"buffer_load_dwordx4 a[116:119], v245, s[8:11], s92 offen sc1\n"
"s_add_u32 s92, s94, 358400\n"
"buffer_load_dwordx4 a[88:91], v244, s[8:11], s92 offen sc1\n"
"buffer_load_dwordx4 a[120:123], v245, s[8:11], s92 offen sc1\n"
"s_add_u32 s92, s94, 363520\n"
"buffer_load_dwordx4 a[92:95], v244, s[8:11], s92 offen sc1\n"
"buffer_load_dwordx4 a[124:127], v245, s[8:11], s92 offen sc1\n"
"s_waitcnt vmcnt(14)\n"
"v_mfma_f32_16x16x32_bf16 v[16:19], v[80:83], a[64:67], v[16:19]\n"
"v_mfma_f32_16x16x32_bf16 v[20:23], v[88:91], a[64:67], v[20:23]\n"
"v_mfma_f32_16x16x32_bf16 v[24:27], v[80:83], a[96:99], v[24:27]\n"
"v_mfma_f32_16x16x32_bf16 v[28:31], v[88:91], a[96:99], v[28:31]\n"
"s_waitcnt vmcnt(12)\n"
"v_mfma_f32_16x16x32_bf16 v[16:19], v[80:83], a[68:71], v[16:19]\n"
"v_mfma_f32_16x16x32_bf16 v[20:23], v[88:91], a[68:71], v[20:23]\n"
"v_mfma_f32_16x16x32_bf16 v[24:27], v[80:83], a[100:103], v[24:27]\n"
"v_mfma_f32_16x16x32_bf16 v[28:31], v[88:91], a[100:103], v[28:31]\n"
"s_waitcnt vmcnt(10)\n"
"v_mfma_f32_16x16x32_bf16 v[16:19], v[80:83], a[72:75], v[16:19]\n"
"v_mfma_f32_16x16x32_bf16 v[20:23], v[88:91], a[72:75], v[20:23]\n"
"v_mfma_f32_16x16x32_bf16 v[24:27], v[80:83], a[104:107], v[24:27]\n"
"v_mfma_f32_16x16x32_bf16 v[28:31], v[88:91], a[104:107], v[28:31]\n"
"s_waitcnt vmcnt(8)\n"
"v_mfma_f32_16x16x32_bf16 v[16:19], v[80:83], a[76:79], v[16:19]\n"
"v_mfma_f32_16x16x32_bf16 v[20:23], v[88:91], a[76:79], v[20:23]\n"
"v_mfma_f32_16x16x32_bf16 v[24:27], v[80:83], a[108:111], v[24:27]\n"
"v_mfma_f32_16x16x32_bf16 v[28:31], v[88:91], a[108:111], v[28:31]\n"
"s_waitcnt vmcnt(6)\n"
"v_mfma_f32_16x16x32_bf16 v[16:19], v[80:83], a[80:83], v[16:19]\n"
"v_mfma_f32_16x16x32_bf16 v[20:23], v[88:91], a[80:83], v[20:23]\n"
"v_mfma_f32_16x16x32_bf16 v[24:27], v[80:83], a[112:115], v[24:27]\n"
"v_mfma_f32_16x16x32_bf16 v[28:31], v[88:91], a[112:115], v[28:31]\n"
"s_waitcnt vmcnt(4)\n"
"v_mfma_f32_16x16x32_bf16 v[16:19], v[80:83], a[84:87], v[16:19]\n"
"v_mfma_f32_16x16x32_bf16 v[20:23], v[88:91], a[84:87], v[20:23]\n"
"v_mfma_f32_16x16x32_bf16 v[24:27], v[80:83], a[116:119], v[24:27]\n"
"v_mfma_f32_16x16x32_bf16 v[28:31], v[88:91], a[116:119], v[28:31]\n"
"s_waitcnt vmcnt(2)\n"
"v_mfma_f32_16x16x32_bf16 v[16:19], v[80:83], a[88:91], v[16:19]\n"
"v_mfma_f32_16x16x32_bf16 v[20:23], v[88:91], a[88:91], v[20:23]\n"
"v_mfma_f32_16x16x32_bf16 v[24:27], v[80:83], a[120:123], v[24:27]\n"
"v_mfma_f32_16x16x32_bf16 v[28:31], v[88:91], a[120:123], v[28:31]\n"
"s_waitcnt vmcnt(0)\n"
"v_mfma_f32_16x16x32_bf16 v[16:19], v[80:83], a[92:95], v[16:19]\n"
"v_mfma_f32_16x16x32_bf16 v[20:23], v[88:91], a[92:95], v[20:23]\n"
"v_mfma_f32_16x16x32_bf16 v[24:27], v[80:83], a[124:127], v[24:27]\n"
"v_mfma_f32_16x16x32_bf16 v[28:31], v[88:91], a[124:127], v[28:31]\n"
"s_nop 5\n"
"v_cvt_pk_bf16_f32 v16, v16, v17\n"
"v_cvt_pk_bf16_f32 v17, v18, v19\n"
"v_cvt_pk_bf16_f32 v18, v20, v21\n"
"v_cvt_pk_bf16_f32 v19, v22, v23\n"
"buffer_store_dwordx4 v[16:19], v177, s[16:19], s38 offen\n"
"v_cvt_pk_bf16_f32 v24, v24, v25\n"
"v_cvt_pk_bf16_f32 v25, v26, v27\n"
"v_cvt_pk_bf16_f32 v26, v28, v29\n"
"v_cvt_pk_bf16_f32 v27, v30, v31\n"
"buffer_store_dwordx4 v[24:27], v177, s[16:19], s55 offen\n"
"s_waitcnt vmcnt(0) lgkmcnt(0)\n"
"s_endpgm\n"
"L1004_%=:\n"
"s_mov_b32 s54, 0\n"
"L1006_%=:\n"
"s_sleep 2\n"
"buffer_load_dwordx4 v[160:163], v224, s[8:11], 0 offen offset:256 sc1\n"
"buffer_load_dwordx4 v[164:167], v224, s[8:11], 0 offen offset:1280 sc1\n"
"s_waitcnt vmcnt(0)\n"
"s_add_u32 s54, s54, 1\n"
"v_cmp_eq_u64 vcc, s[28:29], v[160:161]\n"
"v_cmp_eq_u64_e64 s[62:63], s[28:29], v[162:163]\n"
"s_and_b64 vcc, vcc, s[62:63]\n"
"v_cmp_eq_u64_e64 s[62:63], s[28:29], v[164:165]\n"
"s_and_b64 vcc, vcc, s[62:63]\n"
"v_cmp_eq_u64_e64 s[62:63], s[28:29], v[166:167]\n"
"s_and_b64 vcc, vcc, s[62:63]\n"
"s_cmp_eq_u64 vcc, exec\n"
"s_cbranch_scc1 L1005_%=\n"
"s_cmp_lt_u32 s54, 1048576\n"
"s_cbranch_scc1 L1006_%=\n"
"s_branch L1005_%=\n"
"L1007_%=:\n"
"s_mov_b32 s54, 0\n"
"L1009_%=:\n"
"v_max3_u32 v226, v160, v161, v162\n"
"v_max3_u32 v226, v226, v163, v164\n"
"v_max3_u32 v226, v226, v165, v166\n"
"v_max3_u32 v226, v226, v167, v168\n"
"v_max3_u32 v226, v226, v169, v170\n"
"v_max3_u32 v226, v226, v171, v172\n"
"v_max3_u32 v226, v226, v173, v174\n"
"v_max3_u32 v226, v226, v175, v176\n"
"v_max3_u32 v226, v226, v177, v178\n"
"v_max3_u32 v226, v226, v179, v180\n"
"v_max3_u32 v226, v226, v181, v182\n"
"v_max3_u32 v226, v226, v183, v184\n"
"v_max3_u32 v226, v226, v185, v186\n"
"v_max3_u32 v226, v226, v187, v188\n"
"v_max3_u32 v226, v226, v189, v190\n"
"v_max_u32 v226, v226, v191\n"
"v_mov_b32 v229, v226\n"
"s_nop 1\n"
"v_max_u32_dpp v229, v229, v229 row_shr:1 row_mask:0xf bank_mask:0xf\n"
"s_nop 1\n"
"v_max_u32_dpp v229, v229, v229 row_shr:2 row_mask:0xf bank_mask:0xf\n"
"s_nop 1\n"
"v_max_u32_dpp v229, v229, v229 row_shr:4 row_mask:0xf bank_mask:0xf\n"
"s_nop 1\n"
"v_max_u32_dpp v229, v229, v229 row_shr:8 row_mask:0xf bank_mask:0xf\n"
"s_nop 1\n"
"v_max_u32_dpp v229, v229, v229 row_bcast:15 row_mask:0xa bank_mask:0xf\n"
"s_nop 1\n"
"v_max_u32_dpp v229, v229, v229 row_bcast:31 row_mask:0xc bank_mask:0xf\n"
"s_nop 1\n"
"v_readlane_b32 s62, v229, 63\n"
"s_nop 1\n"
"v_cmp_eq_u32 vcc, s62, v160\n"
"s_nop 1\n"
"v_cndmask_b32_e64 v160, v160, 0, vcc\n"
"v_cmp_eq_u32 vcc, s62, v161\n"
"s_nop 1\n"
"v_cndmask_b32_e64 v161, v161, 0, vcc\n"
"v_cmp_eq_u32 vcc, s62, v162\n"
"s_nop 1\n"
"v_cndmask_b32_e64 v162, v162, 0, vcc\n"
"v_cmp_eq_u32 vcc, s62, v163\n"
"s_nop 1\n"
"v_cndmask_b32_e64 v163, v163, 0, vcc\n"
"v_cmp_eq_u32 vcc, s62, v164\n"
"s_nop 1\n"
"v_cndmask_b32_e64 v164, v164, 0, vcc\n"
"v_cmp_eq_u32 vcc, s62, v165\n"
"s_nop 1\n"
"v_cndmask_b32_e64 v165, v165, 0, vcc\n"
"v_cmp_eq_u32 vcc, s62, v166\n"
"s_nop 1\n"
"v_cndmask_b32_e64 v166, v166, 0, vcc\n"
"v_cmp_eq_u32 vcc, s62, v167\n"
"s_nop 1\n"
"v_cndmask_b32_e64 v167, v167, 0, vcc\n"
"v_cmp_eq_u32 vcc, s62, v168\n"
"s_nop 1\n"
"v_cndmask_b32_e64 v168, v168, 0, vcc\n"
"v_cmp_eq_u32 vcc, s62, v169\n"
"s_nop 1\n"
"v_cndmask_b32_e64 v169, v169, 0, vcc\n"
"v_cmp_eq_u32 vcc, s62, v170\n"
"s_nop 1\n"
"v_cndmask_b32_e64 v170, v170, 0, vcc\n"
"v_cmp_eq_u32 vcc, s62, v171\n"
"s_nop 1\n"
"v_cndmask_b32_e64 v171, v171, 0, vcc\n"
"v_cmp_eq_u32 vcc, s62, v172\n"
"s_nop 1\n"
"v_cndmask_b32_e64 v172, v172, 0, vcc\n"
"v_cmp_eq_u32 vcc, s62, v173\n"
"s_nop 1\n"
"v_cndmask_b32_e64 v173, v173, 0, vcc\n"
"v_cmp_eq_u32 vcc, s62, v174\n"
"s_nop 1\n"
"v_cndmask_b32_e64 v174, v174, 0, vcc\n"
"v_cmp_eq_u32 vcc, s62, v175\n"
"s_nop 1\n"
"v_cndmask_b32_e64 v175, v175, 0, vcc\n"
"v_cmp_eq_u32 vcc, s62, v176\n"
"s_nop 1\n"
"v_cndmask_b32_e64 v176, v176, 0, vcc\n"
"v_cmp_eq_u32 vcc, s62, v177\n"
"s_nop 1\n"
"v_cndmask_b32_e64 v177, v177, 0, vcc\n"
"v_cmp_eq_u32 vcc, s62, v178\n"
"s_nop 1\n"
"v_cndmask_b32_e64 v178, v178, 0, vcc\n"
"v_cmp_eq_u32 vcc, s62, v179\n"
"s_nop 1\n"
"v_cndmask_b32_e64 v179, v179, 0, vcc\n"
"v_cmp_eq_u32 vcc, s62, v180\n"
"s_nop 1\n"
"v_cndmask_b32_e64 v180, v180, 0, vcc\n"
"v_cmp_eq_u32 vcc, s62, v181\n"
"s_nop 1\n"
"v_cndmask_b32_e64 v181, v181, 0, vcc\n"
"v_cmp_eq_u32 vcc, s62, v182\n"
"s_nop 1\n"
"v_cndmask_b32_e64 v182, v182, 0, vcc\n"
"v_cmp_eq_u32 vcc, s62, v183\n"
"s_nop 1\n"
"v_cndmask_b32_e64 v183, v183, 0, vcc\n"
"v_cmp_eq_u32 vcc, s62, v184\n"
"s_nop 1\n"
"v_cndmask_b32_e64 v184, v184, 0, vcc\n"
"v_cmp_eq_u32 vcc, s62, v185\n"
"s_nop 1\n"
"v_cndmask_b32_e64 v185, v185, 0, vcc\n"
"v_cmp_eq_u32 vcc, s62, v186\n"
"s_nop 1\n"
"v_cndmask_b32_e64 v186, v186, 0, vcc\n"
"v_cmp_eq_u32 vcc, s62, v187\n"
"s_nop 1\n"
"v_cndmask_b32_e64 v187, v187, 0, vcc\n"
"v_cmp_eq_u32 vcc, s62, v188\n"
"s_nop 1\n"
"v_cndmask_b32_e64 v188, v188, 0, vcc\n"
"v_cmp_eq_u32 vcc, s62, v189\n"
"s_nop 1\n"
"v_cndmask_b32_e64 v189, v189, 0, vcc\n"
"v_cmp_eq_u32 vcc, s62, v190\n"
"s_nop 1\n"
"v_cndmask_b32_e64 v190, v190, 0, vcc\n"
"v_cmp_eq_u32 vcc, s62, v191\n"
"s_nop 1\n"
"v_cndmask_b32_e64 v191, v191, 0, vcc\n"
"v_mov_b32 v229, s62\n"
"s_lshl_b64 exec, 1, s54\n"
"v_mov_b32 v225, v229\n"
"s_mov_b64 exec, -1\n"
"s_add_u32 s54, s54, 1\n"
"s_cmp_lt_u32 s54, 8\n"
"s_cbranch_scc1 L1009_%=\n"
"s_nop 4\n"
"s_branch L1008_%=\n"
"L1010_%=:\n"
"s_mov_b32 s54, 0\n"
"L1012_%=:\n"
"s_sleep 2\n"
"buffer_load_dwordx2 v[72:73], v252, s[8:11], s50 offen sc1\n"
"s_waitcnt vmcnt(0)\n"
"s_add_u32 s54, s54, 1\n"
"v_cmp_eq_u64 vcc, s[28:29], v[72:73]\n"
"s_cmp_eq_u64 vcc, exec\n"
"s_cbranch_scc1 L1011_%=\n"
"s_cmp_lt_u32 s54, 1048576\n"
"s_cbranch_scc1 L1012_%=\n"
"s_branch L1011_%=\n"
"L1014_%=:\n"
"s_nop 15\n"
"s_lshl_b32 s89, s34, 3\n"
"s_add_u32 s89, s89, s30\n"
"s_add_u32 s89, s89, 0\n"
"s_lshl_b32 s91, s89, 4\n"
"s_lshr_b32 s89, s89, 1\n"
"v_bfe_u32 v249, v224, 4, 4\n"
"v_lshrrev_b32 v254, 8, v224\n"
"v_lshlrev_b32 v254, 2, v254\n"
"v_sub_u32 v249, v249, v254\n"
"v_add_u32 v249, s91, v249\n"
"v_mov_b32 v255, 0xff800000\n"
"v_mov_b32 v248, v255\n"
"s_mov_b32 s90, 0\n"
"s_mov_b32 s88, 0\n"
"v_mov_b32 v80, 0\n"
"v_mov_b32 v81, 0\n"
"v_mov_b32 v82, 0\n"
"v_mov_b32 v83, 0\n"
"v_mov_b32 v84, 0\n"
"v_mov_b32 v85, 0\n"
"v_mov_b32 v86, 0\n"
"v_mov_b32 v87, 0\n"
"v_mov_b32 v88, 0\n"
"v_mov_b32 v89, 0\n"
"v_mov_b32 v90, 0\n"
"v_mov_b32 v91, 0\n"
"v_mov_b32 v92, 0\n"
"v_mov_b32 v93, 0\n"
"v_mov_b32 v94, 0\n"
"v_mov_b32 v95, 0\n"
"v_mov_b32 v96, 0\n"
"v_mov_b32 v97, 0\n"
"v_mov_b32 v98, 0\n"
"v_mov_b32 v99, 0\n"
"v_mov_b32 v100, 0\n"
"v_mov_b32 v101, 0\n"
"v_mov_b32 v102, 0\n"
"v_mov_b32 v103, 0\n"
"v_mov_b32 v104, 0\n"
"v_mov_b32 v105, 0\n"
"v_mov_b32 v106, 0\n"
"v_mov_b32 v107, 0\n"
"v_mov_b32 v108, 0\n"
"v_mov_b32 v109, 0\n"
"v_mov_b32 v110, 0\n"
"v_mov_b32 v111, 0\n"
"v_accvgpr_write_b32 a132, 0\n"
"v_accvgpr_write_b32 a133, 0\n"
"v_accvgpr_write_b32 a134, 0\n"
"v_accvgpr_write_b32 a135, 0\n"
"L1015_%=:\n"
"s_lshl_b32 s91, s88, 13\n"
"s_add_u32 s91, s91, s39\n"
"s_add_u32 s91, s91, 0\n"
"buffer_load_dwordx4 a[148:151], v224, s[8:11], s91 offen offset:0 sc1\n"
"buffer_load_dwordx4 a[152:155], v224, s[8:11], s91 offen offset:1024 sc1\n"
"buffer_load_dwordx4 a[156:159], v224, s[8:11], s91 offen offset:2048 sc1\n"
"buffer_load_dwordx4 a[160:163], v224, s[8:11], s91 offen offset:3072 sc1\n"
"s_add_u32 s91, s91, 4096\n"
"buffer_load_dwordx4 a[164:167], v224, s[8:11], s91 offen offset:0 sc1\n"
"buffer_load_dwordx4 a[168:171], v224, s[8:11], s91 offen offset:1024 sc1\n"
"buffer_load_dwordx4 a[172:175], v224, s[8:11], s91 offen offset:2048 sc1\n"
"buffer_load_dwordx4 a[176:179], v224, s[8:11], s91 offen offset:3072 sc1\n"
"s_waitcnt vmcnt(0)\n"
"v_mfma_f32_16x16x32_bf16 v[240:243], a[148:151], a[0:3], 0\n"
"v_mfma_f32_16x16x32_bf16 v[240:243], a[152:155], a[4:7], v[240:243]\n"
"v_mfma_f32_16x16x32_bf16 v[240:243], a[156:159], a[8:11], v[240:243]\n"
"v_mfma_f32_16x16x32_bf16 v[240:243], a[160:163], a[12:15], v[240:243]\n"
"v_mfma_f32_16x16x32_bf16 v[244:247], a[164:167], a[0:3], 0\n"
"v_mfma_f32_16x16x32_bf16 v[244:247], a[168:171], a[4:7], v[244:247]\n"
"v_mfma_f32_16x16x32_bf16 v[244:247], a[172:175], a[8:11], v[244:247]\n"
"v_mfma_f32_16x16x32_bf16 v[244:247], a[176:179], a[12:15], v[244:247]\n"
"s_lshl_b32 s91, s88, 5\n"
"v_subrev_u32 v250, s91, v249\n"
"s_nop 7\n"
"v_cmp_gt_i32 vcc, 0, v250\n"
"s_nop 1\n"
"v_cndmask_b32 v240, v240, v255, vcc\n"
"v_cmp_gt_i32 vcc, 1, v250\n"
"s_nop 1\n"
"v_cndmask_b32 v241, v241, v255, vcc\n"
"v_cmp_gt_i32 vcc, 2, v250\n"
"s_nop 1\n"
"v_cndmask_b32 v242, v242, v255, vcc\n"
"v_cmp_gt_i32 vcc, 3, v250\n"
"s_nop 1\n"
"v_cndmask_b32 v243, v243, v255, vcc\n"
"v_cmp_gt_i32 vcc, 16, v250\n"
"s_nop 1\n"
"v_cndmask_b32 v244, v244, v255, vcc\n"
"v_cmp_gt_i32 vcc, 17, v250\n"
"s_nop 1\n"
"v_cndmask_b32 v245, v245, v255, vcc\n"
"v_cmp_gt_i32 vcc, 18, v250\n"
"s_nop 1\n"
"v_cndmask_b32 v246, v246, v255, vcc\n"
"v_cmp_gt_i32 vcc, 19, v250\n"
"s_nop 1\n"
"v_cndmask_b32 v247, v247, v255, vcc\n"
"s_cmp_lg_u32 s90, 0\n"
"s_cbranch_scc1 L1016_%=\n"
"v_max3_f32 v248, v248, v240, v241\n"
"v_max3_f32 v248, v248, v242, v243\n"
"v_max3_f32 v248, v248, v244, v245\n"
"v_max3_f32 v248, v248, v246, v247\n"
"s_branch L1017_%=\n"
"L1016_%=:\n"
"s_lshl_b32 s91, s88, 13\n"
"s_add_u32 s91, s91, s39\n"
"s_add_u32 s91, s91, 524288\n"
"buffer_load_dwordx4 a[148:151], v224, s[8:11], s91 offen offset:0 sc1\n"
"buffer_load_dwordx4 a[152:155], v224, s[8:11], s91 offen offset:1024 sc1\n"
"buffer_load_dwordx4 a[156:159], v224, s[8:11], s91 offen offset:2048 sc1\n"
"buffer_load_dwordx4 a[160:163], v224, s[8:11], s91 offen offset:3072 sc1\n"
"s_add_u32 s91, s91, 4096\n"
"buffer_load_dwordx4 a[164:167], v224, s[8:11], s91 offen offset:0 sc1\n"
"buffer_load_dwordx4 a[168:171], v224, s[8:11], s91 offen offset:1024 sc1\n"
"buffer_load_dwordx4 a[172:175], v224, s[8:11], s91 offen offset:2048 sc1\n"
"buffer_load_dwordx4 a[176:179], v224, s[8:11], s91 offen offset:3072 sc1\n"
"s_waitcnt vmcnt(0)\n"
"v_sub_f32 v240, v240, v248\n"
"v_sub_f32 v241, v241, v248\n"
"v_sub_f32 v242, v242, v248\n"
"v_sub_f32 v243, v243, v248\n"
"v_sub_f32 v244, v244, v248\n"
"v_sub_f32 v245, v245, v248\n"
"v_sub_f32 v246, v246, v248\n"
"v_sub_f32 v247, v247, v248\n"
"v_exp_f32 v240, v240\n"
"v_exp_f32 v241, v241\n"
"v_exp_f32 v242, v242\n"
"v_exp_f32 v243, v243\n"
"v_exp_f32 v244, v244\n"
"v_exp_f32 v245, v245\n"
"v_exp_f32 v246, v246\n"
"v_exp_f32 v247, v247\n"
"s_nop 0\n"
"v_cvt_pk_bf16_f32 v240, v240, v241\n"
"v_cvt_pk_bf16_f32 v241, v242, v243\n"
"v_cvt_pk_bf16_f32 v242, v244, v245\n"
"v_cvt_pk_bf16_f32 v243, v246, v247\n"
"s_nop 1\n"
"v_mfma_f32_16x16x32_bf16 v[80:83], a[148:151], v[240:243], v[80:83]\n"
"v_mfma_f32_16x16x32_bf16 v[84:87], a[152:155], v[240:243], v[84:87]\n"
"v_mfma_f32_16x16x32_bf16 v[88:91], a[156:159], v[240:243], v[88:91]\n"
"v_mfma_f32_16x16x32_bf16 v[92:95], a[160:163], v[240:243], v[92:95]\n"
"v_mfma_f32_16x16x32_bf16 v[96:99], a[164:167], v[240:243], v[96:99]\n"
"v_mfma_f32_16x16x32_bf16 v[100:103], a[168:171], v[240:243], v[100:103]\n"
"v_mfma_f32_16x16x32_bf16 v[104:107], a[172:175], v[240:243], v[104:107]\n"
"v_mfma_f32_16x16x32_bf16 v[108:111], a[176:179], v[240:243], v[108:111]\n"
"v_mfma_f32_16x16x32_bf16 a[132:135], a[128:131], v[240:243], a[132:135]\n"
"s_nop 7\n"
"L1017_%=:\n"
"s_add_u32 s88, s88, 1\n"
"s_cmp_le_u32 s88, s89\n"
"s_cbranch_scc1 L1015_%=\n"
"s_cmp_lg_u32 s90, 0\n"
"s_cbranch_scc1 L1018_%=\n"
"v_mov_b32 v254, v248\n"
"s_nop 1\n"
"v_permlane16_swap_b32 v254, v248\n"
"s_nop 1\n"
"v_max_f32 v248, v248, v254\n"
"v_mov_b32 v254, v248\n"
"s_nop 1\n"
"v_permlane32_swap_b32 v254, v248\n"
"s_nop 1\n"
"v_max_f32 v248, v248, v254\n"
"s_mov_b32 s90, 1\n"
"s_mov_b32 s88, 0\n"
"s_branch L1015_%=\n"
"L1018_%=:\n"
"s_nop 7\n"
"v_accvgpr_read_b32 v251, a132\n"
"s_nop 1\n"
"s_branch L1013_%=\n"
"L1019_%=:\n"
"s_mov_b32 s54, 0\n"
"L1021_%=:\n"
"s_sleep 2\n"
"buffer_load_dwordx2 v[236:237], v235, s[8:11], s50 offen sc1\n"
"s_waitcnt vmcnt(0)\n"
"s_add_u32 s54, s54, 1\n"
"v_cmp_eq_u64 vcc, s[28:29], v[236:237]\n"
"s_cmp_eq_u64 vcc, exec\n"
"s_cbranch_scc1 L1020_%=\n"
"s_cmp_lt_u32 s54, 1048576\n"
"s_cbranch_scc1 L1021_%=\n"
"s_branch L1020_%=\n"
"L1023_%=:\n"
"s_nop 15\n"
"s_lshl_b32 s89, s34, 3\n"
"s_add_u32 s89, s89, s30\n"
"s_add_u32 s89, s89, 4\n"
"s_lshl_b32 s91, s89, 4\n"
"s_lshr_b32 s89, s89, 1\n"
"v_bfe_u32 v249, v224, 4, 4\n"
"v_lshrrev_b32 v254, 8, v224\n"
"v_lshlrev_b32 v254, 2, v254\n"
"v_sub_u32 v249, v249, v254\n"
"v_add_u32 v249, s91, v249\n"
"v_mov_b32 v255, 0xff800000\n"
"v_mov_b32 v248, v255\n"
"s_mov_b32 s90, 0\n"
"s_mov_b32 s88, 0\n"
"v_mov_b32 v112, 0\n"
"v_mov_b32 v113, 0\n"
"v_mov_b32 v114, 0\n"
"v_mov_b32 v115, 0\n"
"v_mov_b32 v116, 0\n"
"v_mov_b32 v117, 0\n"
"v_mov_b32 v118, 0\n"
"v_mov_b32 v119, 0\n"
"v_mov_b32 v120, 0\n"
"v_mov_b32 v121, 0\n"
"v_mov_b32 v122, 0\n"
"v_mov_b32 v123, 0\n"
"v_mov_b32 v124, 0\n"
"v_mov_b32 v125, 0\n"
"v_mov_b32 v126, 0\n"
"v_mov_b32 v127, 0\n"
"v_mov_b32 v128, 0\n"
"v_mov_b32 v129, 0\n"
"v_mov_b32 v130, 0\n"
"v_mov_b32 v131, 0\n"
"v_mov_b32 v132, 0\n"
"v_mov_b32 v133, 0\n"
"v_mov_b32 v134, 0\n"
"v_mov_b32 v135, 0\n"
"v_mov_b32 v136, 0\n"
"v_mov_b32 v137, 0\n"
"v_mov_b32 v138, 0\n"
"v_mov_b32 v139, 0\n"
"v_mov_b32 v140, 0\n"
"v_mov_b32 v141, 0\n"
"v_mov_b32 v142, 0\n"
"v_mov_b32 v143, 0\n"
"v_accvgpr_write_b32 a136, 0\n"
"v_accvgpr_write_b32 a137, 0\n"
"v_accvgpr_write_b32 a138, 0\n"
"v_accvgpr_write_b32 a139, 0\n"
"L1024_%=:\n"
"s_lshl_b32 s91, s88, 13\n"
"s_add_u32 s91, s91, s39\n"
"s_add_u32 s91, s91, 0\n"
"buffer_load_dwordx4 a[148:151], v224, s[8:11], s91 offen offset:0 sc1\n"
"buffer_load_dwordx4 a[152:155], v224, s[8:11], s91 offen offset:1024 sc1\n"
"buffer_load_dwordx4 a[156:159], v224, s[8:11], s91 offen offset:2048 sc1\n"
"buffer_load_dwordx4 a[160:163], v224, s[8:11], s91 offen offset:3072 sc1\n"
"s_add_u32 s91, s91, 4096\n"
"buffer_load_dwordx4 a[164:167], v224, s[8:11], s91 offen offset:0 sc1\n"
"buffer_load_dwordx4 a[168:171], v224, s[8:11], s91 offen offset:1024 sc1\n"
"buffer_load_dwordx4 a[172:175], v224, s[8:11], s91 offen offset:2048 sc1\n"
"buffer_load_dwordx4 a[176:179], v224, s[8:11], s91 offen offset:3072 sc1\n"
"s_waitcnt vmcnt(0)\n"
"v_mfma_f32_16x16x32_bf16 v[240:243], a[148:151], a[16:19], 0\n"
"v_mfma_f32_16x16x32_bf16 v[240:243], a[152:155], a[20:23], v[240:243]\n"
"v_mfma_f32_16x16x32_bf16 v[240:243], a[156:159], a[24:27], v[240:243]\n"
"v_mfma_f32_16x16x32_bf16 v[240:243], a[160:163], a[28:31], v[240:243]\n"
"v_mfma_f32_16x16x32_bf16 v[244:247], a[164:167], a[16:19], 0\n"
"v_mfma_f32_16x16x32_bf16 v[244:247], a[168:171], a[20:23], v[244:247]\n"
"v_mfma_f32_16x16x32_bf16 v[244:247], a[172:175], a[24:27], v[244:247]\n"
"v_mfma_f32_16x16x32_bf16 v[244:247], a[176:179], a[28:31], v[244:247]\n"
"s_lshl_b32 s91, s88, 5\n"
"v_subrev_u32 v250, s91, v249\n"
"s_nop 7\n"
"v_cmp_gt_i32 vcc, 0, v250\n"
"s_nop 1\n"
"v_cndmask_b32 v240, v240, v255, vcc\n"
"v_cmp_gt_i32 vcc, 1, v250\n"
"s_nop 1\n"
"v_cndmask_b32 v241, v241, v255, vcc\n"
"v_cmp_gt_i32 vcc, 2, v250\n"
"s_nop 1\n"
"v_cndmask_b32 v242, v242, v255, vcc\n"
"v_cmp_gt_i32 vcc, 3, v250\n"
"s_nop 1\n"
"v_cndmask_b32 v243, v243, v255, vcc\n"
"v_cmp_gt_i32 vcc, 16, v250\n"
"s_nop 1\n"
"v_cndmask_b32 v244, v244, v255, vcc\n"
"v_cmp_gt_i32 vcc, 17, v250\n"
"s_nop 1\n"
"v_cndmask_b32 v245, v245, v255, vcc\n"
"v_cmp_gt_i32 vcc, 18, v250\n"
"s_nop 1\n"
"v_cndmask_b32 v246, v246, v255, vcc\n"
"v_cmp_gt_i32 vcc, 19, v250\n"
"s_nop 1\n"
"v_cndmask_b32 v247, v247, v255, vcc\n"
"s_cmp_lg_u32 s90, 0\n"
"s_cbranch_scc1 L1025_%=\n"
"v_max3_f32 v248, v248, v240, v241\n"
"v_max3_f32 v248, v248, v242, v243\n"
"v_max3_f32 v248, v248, v244, v245\n"
"v_max3_f32 v248, v248, v246, v247\n"
"s_branch L1026_%=\n"
"L1025_%=:\n"
"s_lshl_b32 s91, s88, 13\n"
"s_add_u32 s91, s91, s39\n"
"s_add_u32 s91, s91, 524288\n"
"buffer_load_dwordx4 a[148:151], v224, s[8:11], s91 offen offset:0 sc1\n"
"buffer_load_dwordx4 a[152:155], v224, s[8:11], s91 offen offset:1024 sc1\n"
"buffer_load_dwordx4 a[156:159], v224, s[8:11], s91 offen offset:2048 sc1\n"
"buffer_load_dwordx4 a[160:163], v224, s[8:11], s91 offen offset:3072 sc1\n"
"s_add_u32 s91, s91, 4096\n"
"buffer_load_dwordx4 a[164:167], v224, s[8:11], s91 offen offset:0 sc1\n"
"buffer_load_dwordx4 a[168:171], v224, s[8:11], s91 offen offset:1024 sc1\n"
"buffer_load_dwordx4 a[172:175], v224, s[8:11], s91 offen offset:2048 sc1\n"
"buffer_load_dwordx4 a[176:179], v224, s[8:11], s91 offen offset:3072 sc1\n"
"s_waitcnt vmcnt(0)\n"
"v_sub_f32 v240, v240, v248\n"
"v_sub_f32 v241, v241, v248\n"
"v_sub_f32 v242, v242, v248\n"
"v_sub_f32 v243, v243, v248\n"
"v_sub_f32 v244, v244, v248\n"
"v_sub_f32 v245, v245, v248\n"
"v_sub_f32 v246, v246, v248\n"
"v_sub_f32 v247, v247, v248\n"
"v_exp_f32 v240, v240\n"
"v_exp_f32 v241, v241\n"
"v_exp_f32 v242, v242\n"
"v_exp_f32 v243, v243\n"
"v_exp_f32 v244, v244\n"
"v_exp_f32 v245, v245\n"
"v_exp_f32 v246, v246\n"
"v_exp_f32 v247, v247\n"
"s_nop 0\n"
"v_cvt_pk_bf16_f32 v240, v240, v241\n"
"v_cvt_pk_bf16_f32 v241, v242, v243\n"
"v_cvt_pk_bf16_f32 v242, v244, v245\n"
"v_cvt_pk_bf16_f32 v243, v246, v247\n"
"s_nop 1\n"
"v_mfma_f32_16x16x32_bf16 v[112:115], a[148:151], v[240:243], v[112:115]\n"
"v_mfma_f32_16x16x32_bf16 v[116:119], a[152:155], v[240:243], v[116:119]\n"
"v_mfma_f32_16x16x32_bf16 v[120:123], a[156:159], v[240:243], v[120:123]\n"
"v_mfma_f32_16x16x32_bf16 v[124:127], a[160:163], v[240:243], v[124:127]\n"
"v_mfma_f32_16x16x32_bf16 v[128:131], a[164:167], v[240:243], v[128:131]\n"
"v_mfma_f32_16x16x32_bf16 v[132:135], a[168:171], v[240:243], v[132:135]\n"
"v_mfma_f32_16x16x32_bf16 v[136:139], a[172:175], v[240:243], v[136:139]\n"
"v_mfma_f32_16x16x32_bf16 v[140:143], a[176:179], v[240:243], v[140:143]\n"
"v_mfma_f32_16x16x32_bf16 a[136:139], a[128:131], v[240:243], a[136:139]\n"
"s_nop 7\n"
"L1026_%=:\n"
"s_add_u32 s88, s88, 1\n"
"s_cmp_le_u32 s88, s89\n"
"s_cbranch_scc1 L1024_%=\n"
"s_cmp_lg_u32 s90, 0\n"
"s_cbranch_scc1 L1027_%=\n"
"v_mov_b32 v254, v248\n"
"s_nop 1\n"
"v_permlane16_swap_b32 v254, v248\n"
"s_nop 1\n"
"v_max_f32 v248, v248, v254\n"
"v_mov_b32 v254, v248\n"
"s_nop 1\n"
"v_permlane32_swap_b32 v254, v248\n"
"s_nop 1\n"
"v_max_f32 v248, v248, v254\n"
"s_mov_b32 s90, 1\n"
"s_mov_b32 s88, 0\n"
"s_branch L1024_%=\n"
"L1027_%=:\n"
"s_nop 7\n"
"v_accvgpr_read_b32 v252, a136\n"
"s_nop 1\n"
"s_branch L1022_%=\n"
"L1029_%=:\n"
"s_nop 15\n"
"s_lshl_b32 s89, s34, 3\n"
"s_add_u32 s89, s89, s30\n"
"s_add_u32 s89, s89, 4\n"
"s_lshl_b32 s91, s89, 4\n"
"s_lshr_b32 s89, s89, 1\n"
"v_bfe_u32 v249, v224, 4, 4\n"
"v_lshrrev_b32 v254, 8, v224\n"
"v_lshlrev_b32 v254, 2, v254\n"
"v_sub_u32 v249, v249, v254\n"
"v_add_u32 v249, s91, v249\n"
"v_mov_b32 v255, 0xff800000\n"
"v_mov_b32 v248, v255\n"
"s_mov_b32 s90, 0\n"
"s_mov_b32 s88, 0\n"
"v_mov_b32 v112, 0\n"
"v_mov_b32 v113, 0\n"
"v_mov_b32 v114, 0\n"
"v_mov_b32 v115, 0\n"
"v_mov_b32 v116, 0\n"
"v_mov_b32 v117, 0\n"
"v_mov_b32 v118, 0\n"
"v_mov_b32 v119, 0\n"
"v_mov_b32 v120, 0\n"
"v_mov_b32 v121, 0\n"
"v_mov_b32 v122, 0\n"
"v_mov_b32 v123, 0\n"
"v_mov_b32 v124, 0\n"
"v_mov_b32 v125, 0\n"
"v_mov_b32 v126, 0\n"
"v_mov_b32 v127, 0\n"
"v_mov_b32 v128, 0\n"
"v_mov_b32 v129, 0\n"
"v_mov_b32 v130, 0\n"
"v_mov_b32 v131, 0\n"
"v_mov_b32 v132, 0\n"
"v_mov_b32 v133, 0\n"
"v_mov_b32 v134, 0\n"
"v_mov_b32 v135, 0\n"
"v_mov_b32 v136, 0\n"
"v_mov_b32 v137, 0\n"
"v_mov_b32 v138, 0\n"
"v_mov_b32 v139, 0\n"
"v_mov_b32 v140, 0\n"
"v_mov_b32 v141, 0\n"
"v_mov_b32 v142, 0\n"
"v_mov_b32 v143, 0\n"
"v_accvgpr_write_b32 a136, 0\n"
"v_accvgpr_write_b32 a137, 0\n"
"v_accvgpr_write_b32 a138, 0\n"
"v_accvgpr_write_b32 a139, 0\n"
"L1030_%=:\n"
"s_lshl_b32 s91, s88, 13\n"
"s_add_u32 s91, s91, s39\n"
"s_add_u32 s91, s91, 0\n"
"buffer_load_dwordx4 a[148:151], v224, s[8:11], s91 offen offset:0 sc1\n"
"buffer_load_dwordx4 a[152:155], v224, s[8:11], s91 offen offset:1024 sc1\n"
"buffer_load_dwordx4 a[156:159], v224, s[8:11], s91 offen offset:2048 sc1\n"
"buffer_load_dwordx4 a[160:163], v224, s[8:11], s91 offen offset:3072 sc1\n"
"s_add_u32 s91, s91, 4096\n"
"buffer_load_dwordx4 a[164:167], v224, s[8:11], s91 offen offset:0 sc1\n"
"buffer_load_dwordx4 a[168:171], v224, s[8:11], s91 offen offset:1024 sc1\n"
"buffer_load_dwordx4 a[172:175], v224, s[8:11], s91 offen offset:2048 sc1\n"
"buffer_load_dwordx4 a[176:179], v224, s[8:11], s91 offen offset:3072 sc1\n"
"s_waitcnt vmcnt(0)\n"
"v_mfma_f32_16x16x32_bf16 v[240:243], a[148:151], a[16:19], 0\n"
"v_mfma_f32_16x16x32_bf16 v[240:243], a[152:155], a[20:23], v[240:243]\n"
"v_mfma_f32_16x16x32_bf16 v[240:243], a[156:159], a[24:27], v[240:243]\n"
"v_mfma_f32_16x16x32_bf16 v[240:243], a[160:163], a[28:31], v[240:243]\n"
"v_mfma_f32_16x16x32_bf16 v[244:247], a[164:167], a[16:19], 0\n"
"v_mfma_f32_16x16x32_bf16 v[244:247], a[168:171], a[20:23], v[244:247]\n"
"v_mfma_f32_16x16x32_bf16 v[244:247], a[172:175], a[24:27], v[244:247]\n"
"v_mfma_f32_16x16x32_bf16 v[244:247], a[176:179], a[28:31], v[244:247]\n"
"s_lshl_b32 s91, s88, 5\n"
"v_subrev_u32 v250, s91, v249\n"
"s_nop 7\n"
"v_cmp_gt_i32 vcc, 0, v250\n"
"s_nop 1\n"
"v_cndmask_b32 v240, v240, v255, vcc\n"
"v_cmp_gt_i32 vcc, 1, v250\n"
"s_nop 1\n"
"v_cndmask_b32 v241, v241, v255, vcc\n"
"v_cmp_gt_i32 vcc, 2, v250\n"
"s_nop 1\n"
"v_cndmask_b32 v242, v242, v255, vcc\n"
"v_cmp_gt_i32 vcc, 3, v250\n"
"s_nop 1\n"
"v_cndmask_b32 v243, v243, v255, vcc\n"
"v_cmp_gt_i32 vcc, 16, v250\n"
"s_nop 1\n"
"v_cndmask_b32 v244, v244, v255, vcc\n"
"v_cmp_gt_i32 vcc, 17, v250\n"
"s_nop 1\n"
"v_cndmask_b32 v245, v245, v255, vcc\n"
"v_cmp_gt_i32 vcc, 18, v250\n"
"s_nop 1\n"
"v_cndmask_b32 v246, v246, v255, vcc\n"
"v_cmp_gt_i32 vcc, 19, v250\n"
"s_nop 1\n"
"v_cndmask_b32 v247, v247, v255, vcc\n"
"s_cmp_lg_u32 s90, 0\n"
"s_cbranch_scc1 L1031_%=\n"
"v_max3_f32 v248, v248, v240, v241\n"
"v_max3_f32 v248, v248, v242, v243\n"
"v_max3_f32 v248, v248, v244, v245\n"
"v_max3_f32 v248, v248, v246, v247\n"
"s_branch L1032_%=\n"
"L1031_%=:\n"
"s_lshl_b32 s91, s88, 13\n"
"s_add_u32 s91, s91, s39\n"
"s_add_u32 s91, s91, 524288\n"
"buffer_load_dwordx4 a[148:151], v224, s[8:11], s91 offen offset:0 sc1\n"
"buffer_load_dwordx4 a[152:155], v224, s[8:11], s91 offen offset:1024 sc1\n"
"buffer_load_dwordx4 a[156:159], v224, s[8:11], s91 offen offset:2048 sc1\n"
"buffer_load_dwordx4 a[160:163], v224, s[8:11], s91 offen offset:3072 sc1\n"
"s_add_u32 s91, s91, 4096\n"
"buffer_load_dwordx4 a[164:167], v224, s[8:11], s91 offen offset:0 sc1\n"
"buffer_load_dwordx4 a[168:171], v224, s[8:11], s91 offen offset:1024 sc1\n"
"buffer_load_dwordx4 a[172:175], v224, s[8:11], s91 offen offset:2048 sc1\n"
"buffer_load_dwordx4 a[176:179], v224, s[8:11], s91 offen offset:3072 sc1\n"
"s_waitcnt vmcnt(0)\n"
"v_sub_f32 v240, v240, v248\n"
"v_sub_f32 v241, v241, v248\n"
"v_sub_f32 v242, v242, v248\n"
"v_sub_f32 v243, v243, v248\n"
"v_sub_f32 v244, v244, v248\n"
"v_sub_f32 v245, v245, v248\n"
"v_sub_f32 v246, v246, v248\n"
"v_sub_f32 v247, v247, v248\n"
"v_exp_f32 v240, v240\n"
"v_exp_f32 v241, v241\n"
"v_exp_f32 v242, v242\n"
"v_exp_f32 v243, v243\n"
"v_exp_f32 v244, v244\n"
"v_exp_f32 v245, v245\n"
"v_exp_f32 v246, v246\n"
"v_exp_f32 v247, v247\n"
"s_nop 0\n"
"v_cvt_pk_bf16_f32 v240, v240, v241\n"
"v_cvt_pk_bf16_f32 v241, v242, v243\n"
"v_cvt_pk_bf16_f32 v242, v244, v245\n"
"v_cvt_pk_bf16_f32 v243, v246, v247\n"
"s_nop 1\n"
"v_mfma_f32_16x16x32_bf16 v[112:115], a[148:151], v[240:243], v[112:115]\n"
"v_mfma_f32_16x16x32_bf16 v[116:119], a[152:155], v[240:243], v[116:119]\n"
"v_mfma_f32_16x16x32_bf16 v[120:123], a[156:159], v[240:243], v[120:123]\n"
"v_mfma_f32_16x16x32_bf16 v[124:127], a[160:163], v[240:243], v[124:127]\n"
"v_mfma_f32_16x16x32_bf16 v[128:131], a[164:167], v[240:243], v[128:131]\n"
"v_mfma_f32_16x16x32_bf16 v[132:135], a[168:171], v[240:243], v[132:135]\n"
"v_mfma_f32_16x16x32_bf16 v[136:139], a[172:175], v[240:243], v[136:139]\n"
"v_mfma_f32_16x16x32_bf16 v[140:143], a[176:179], v[240:243], v[140:143]\n"
"v_mfma_f32_16x16x32_bf16 a[136:139], a[128:131], v[240:243], a[136:139]\n"
"s_nop 7\n"
"L1032_%=:\n"
"s_add_u32 s88, s88, 1\n"
"s_cmp_le_u32 s88, s89\n"
"s_cbranch_scc1 L1030_%=\n"
"s_cmp_lg_u32 s90, 0\n"
"s_cbranch_scc1 L1033_%=\n"
"v_mov_b32 v254, v248\n"
"s_nop 1\n"
"v_permlane16_swap_b32 v254, v248\n"
"s_nop 1\n"
"v_max_f32 v248, v248, v254\n"
"v_mov_b32 v254, v248\n"
"s_nop 1\n"
"v_permlane32_swap_b32 v254, v248\n"
"s_nop 1\n"
"v_max_f32 v248, v248, v254\n"
"s_mov_b32 s90, 1\n"
"s_mov_b32 s88, 0\n"
"s_branch L1030_%=\n"
"L1033_%=:\n"
"s_nop 7\n"
"v_accvgpr_read_b32 v252, a136\n"
"s_nop 1\n"
"s_branch L1028_%=\n"
"L1035_%=:\n"
"s_nop 15\n"
"s_lshl_b32 s89, s34, 3\n"
"s_sub_u32 s89, 120, s89\n"
"s_add_u32 s89, s89, s30\n"
"s_add_u32 s89, s89, 0\n"
"s_lshl_b32 s91, s89, 4\n"
"s_lshr_b32 s89, s89, 1\n"
"v_bfe_u32 v249, v224, 4, 4\n"
"v_lshrrev_b32 v254, 8, v224\n"
"v_lshlrev_b32 v254, 2, v254\n"
"v_sub_u32 v249, v249, v254\n"
"v_add_u32 v249, s91, v249\n"
"v_mov_b32 v255, 0xff800000\n"
"v_mov_b32 v248, v255\n"
"s_mov_b32 s90, 0\n"
"s_mov_b32 s88, 0\n"
"v_mov_b32 v144, 0\n"
"v_mov_b32 v145, 0\n"
"v_mov_b32 v146, 0\n"
"v_mov_b32 v147, 0\n"
"v_mov_b32 v148, 0\n"
"v_mov_b32 v149, 0\n"
"v_mov_b32 v150, 0\n"
"v_mov_b32 v151, 0\n"
"v_mov_b32 v152, 0\n"
"v_mov_b32 v153, 0\n"
"v_mov_b32 v154, 0\n"
"v_mov_b32 v155, 0\n"
"v_mov_b32 v156, 0\n"
"v_mov_b32 v157, 0\n"
"v_mov_b32 v158, 0\n"
"v_mov_b32 v159, 0\n"
"v_mov_b32 v160, 0\n"
"v_mov_b32 v161, 0\n"
"v_mov_b32 v162, 0\n"
"v_mov_b32 v163, 0\n"
"v_mov_b32 v164, 0\n"
"v_mov_b32 v165, 0\n"
"v_mov_b32 v166, 0\n"
"v_mov_b32 v167, 0\n"
"v_mov_b32 v168, 0\n"
"v_mov_b32 v169, 0\n"
"v_mov_b32 v170, 0\n"
"v_mov_b32 v171, 0\n"
"v_mov_b32 v172, 0\n"
"v_mov_b32 v173, 0\n"
"v_mov_b32 v174, 0\n"
"v_mov_b32 v175, 0\n"
"v_accvgpr_write_b32 a140, 0\n"
"v_accvgpr_write_b32 a141, 0\n"
"v_accvgpr_write_b32 a142, 0\n"
"v_accvgpr_write_b32 a143, 0\n"
"L1036_%=:\n"
"s_lshl_b32 s91, s88, 13\n"
"s_add_u32 s91, s91, s39\n"
"s_add_u32 s91, s91, 0\n"
"buffer_load_dwordx4 a[148:151], v224, s[8:11], s91 offen offset:0 sc1\n"
"buffer_load_dwordx4 a[152:155], v224, s[8:11], s91 offen offset:1024 sc1\n"
"buffer_load_dwordx4 a[156:159], v224, s[8:11], s91 offen offset:2048 sc1\n"
"buffer_load_dwordx4 a[160:163], v224, s[8:11], s91 offen offset:3072 sc1\n"
"s_add_u32 s91, s91, 4096\n"
"buffer_load_dwordx4 a[164:167], v224, s[8:11], s91 offen offset:0 sc1\n"
"buffer_load_dwordx4 a[168:171], v224, s[8:11], s91 offen offset:1024 sc1\n"
"buffer_load_dwordx4 a[172:175], v224, s[8:11], s91 offen offset:2048 sc1\n"
"buffer_load_dwordx4 a[176:179], v224, s[8:11], s91 offen offset:3072 sc1\n"
"s_waitcnt vmcnt(0)\n"
"v_mfma_f32_16x16x32_bf16 v[240:243], a[148:151], a[32:35], 0\n"
"v_mfma_f32_16x16x32_bf16 v[240:243], a[152:155], a[36:39], v[240:243]\n"
"v_mfma_f32_16x16x32_bf16 v[240:243], a[156:159], a[40:43], v[240:243]\n"
"v_mfma_f32_16x16x32_bf16 v[240:243], a[160:163], a[44:47], v[240:243]\n"
"v_mfma_f32_16x16x32_bf16 v[244:247], a[164:167], a[32:35], 0\n"
"v_mfma_f32_16x16x32_bf16 v[244:247], a[168:171], a[36:39], v[244:247]\n"
"v_mfma_f32_16x16x32_bf16 v[244:247], a[172:175], a[40:43], v[244:247]\n"
"v_mfma_f32_16x16x32_bf16 v[244:247], a[176:179], a[44:47], v[244:247]\n"
"s_lshl_b32 s91, s88, 5\n"
"v_subrev_u32 v250, s91, v249\n"
"s_nop 7\n"
"v_cmp_gt_i32 vcc, 0, v250\n"
"s_nop 1\n"
"v_cndmask_b32 v240, v240, v255, vcc\n"
"v_cmp_gt_i32 vcc, 1, v250\n"
"s_nop 1\n"
"v_cndmask_b32 v241, v241, v255, vcc\n"
"v_cmp_gt_i32 vcc, 2, v250\n"
"s_nop 1\n"
"v_cndmask_b32 v242, v242, v255, vcc\n"
"v_cmp_gt_i32 vcc, 3, v250\n"
"s_nop 1\n"
"v_cndmask_b32 v243, v243, v255, vcc\n"
"v_cmp_gt_i32 vcc, 16, v250\n"
"s_nop 1\n"
"v_cndmask_b32 v244, v244, v255, vcc\n"
"v_cmp_gt_i32 vcc, 17, v250\n"
"s_nop 1\n"
"v_cndmask_b32 v245, v245, v255, vcc\n"
"v_cmp_gt_i32 vcc, 18, v250\n"
"s_nop 1\n"
"v_cndmask_b32 v246, v246, v255, vcc\n"
"v_cmp_gt_i32 vcc, 19, v250\n"
"s_nop 1\n"
"v_cndmask_b32 v247, v247, v255, vcc\n"
"s_cmp_lg_u32 s90, 0\n"
"s_cbranch_scc1 L1037_%=\n"
"v_max3_f32 v248, v248, v240, v241\n"
"v_max3_f32 v248, v248, v242, v243\n"
"v_max3_f32 v248, v248, v244, v245\n"
"v_max3_f32 v248, v248, v246, v247\n"
"s_branch L1038_%=\n"
"L1037_%=:\n"
"s_lshl_b32 s91, s88, 13\n"
"s_add_u32 s91, s91, s39\n"
"s_add_u32 s91, s91, 524288\n"
"buffer_load_dwordx4 a[148:151], v224, s[8:11], s91 offen offset:0 sc1\n"
"buffer_load_dwordx4 a[152:155], v224, s[8:11], s91 offen offset:1024 sc1\n"
"buffer_load_dwordx4 a[156:159], v224, s[8:11], s91 offen offset:2048 sc1\n"
"buffer_load_dwordx4 a[160:163], v224, s[8:11], s91 offen offset:3072 sc1\n"
"s_add_u32 s91, s91, 4096\n"
"buffer_load_dwordx4 a[164:167], v224, s[8:11], s91 offen offset:0 sc1\n"
"buffer_load_dwordx4 a[168:171], v224, s[8:11], s91 offen offset:1024 sc1\n"
"buffer_load_dwordx4 a[172:175], v224, s[8:11], s91 offen offset:2048 sc1\n"
"buffer_load_dwordx4 a[176:179], v224, s[8:11], s91 offen offset:3072 sc1\n"
"s_waitcnt vmcnt(0)\n"
"v_sub_f32 v240, v240, v248\n"
"v_sub_f32 v241, v241, v248\n"
"v_sub_f32 v242, v242, v248\n"
"v_sub_f32 v243, v243, v248\n"
"v_sub_f32 v244, v244, v248\n"
"v_sub_f32 v245, v245, v248\n"
"v_sub_f32 v246, v246, v248\n"
"v_sub_f32 v247, v247, v248\n"
"v_exp_f32 v240, v240\n"
"v_exp_f32 v241, v241\n"
"v_exp_f32 v242, v242\n"
"v_exp_f32 v243, v243\n"
"v_exp_f32 v244, v244\n"
"v_exp_f32 v245, v245\n"
"v_exp_f32 v246, v246\n"
"v_exp_f32 v247, v247\n"
"s_nop 0\n"
"v_cvt_pk_bf16_f32 v240, v240, v241\n"
"v_cvt_pk_bf16_f32 v241, v242, v243\n"
"v_cvt_pk_bf16_f32 v242, v244, v245\n"
"v_cvt_pk_bf16_f32 v243, v246, v247\n"
"s_nop 1\n"
"v_mfma_f32_16x16x32_bf16 v[144:147], a[148:151], v[240:243], v[144:147]\n"
"v_mfma_f32_16x16x32_bf16 v[148:151], a[152:155], v[240:243], v[148:151]\n"
"v_mfma_f32_16x16x32_bf16 v[152:155], a[156:159], v[240:243], v[152:155]\n"
"v_mfma_f32_16x16x32_bf16 v[156:159], a[160:163], v[240:243], v[156:159]\n"
"v_mfma_f32_16x16x32_bf16 v[160:163], a[164:167], v[240:243], v[160:163]\n"
"v_mfma_f32_16x16x32_bf16 v[164:167], a[168:171], v[240:243], v[164:167]\n"
"v_mfma_f32_16x16x32_bf16 v[168:171], a[172:175], v[240:243], v[168:171]\n"
"v_mfma_f32_16x16x32_bf16 v[172:175], a[176:179], v[240:243], v[172:175]\n"
"v_mfma_f32_16x16x32_bf16 a[140:143], a[128:131], v[240:243], a[140:143]\n"
"s_nop 7\n"
"L1038_%=:\n"
"s_add_u32 s88, s88, 1\n"
"s_cmp_le_u32 s88, s89\n"
"s_cbranch_scc1 L1036_%=\n"
"s_cmp_lg_u32 s90, 0\n"
"s_cbranch_scc1 L1039_%=\n"
"v_mov_b32 v254, v248\n"
"s_nop 1\n"
"v_permlane16_swap_b32 v254, v248\n"
"s_nop 1\n"
"v_max_f32 v248, v248, v254\n"
"v_mov_b32 v254, v248\n"
"s_nop 1\n"
"v_permlane32_swap_b32 v254, v248\n"
"s_nop 1\n"
"v_max_f32 v248, v248, v254\n"
"s_mov_b32 s90, 1\n"
"s_mov_b32 s88, 0\n"
"s_branch L1036_%=\n"
"L1039_%=:\n"
"s_nop 7\n"
"v_accvgpr_read_b32 v251, a140\n"
"s_nop 1\n"
"s_branch L1034_%=\n"
"L1041_%=:\n"
"s_nop 15\n"
"s_lshl_b32 s89, s34, 3\n"
"s_sub_u32 s89, 120, s89\n"
"s_add_u32 s89, s89, s30\n"
"s_add_u32 s89, s89, 4\n"
"s_lshl_b32 s91, s89, 4\n"
"s_lshr_b32 s89, s89, 1\n"
"v_bfe_u32 v249, v224, 4, 4\n"
"v_lshrrev_b32 v254, 8, v224\n"
"v_lshlrev_b32 v254, 2, v254\n"
"v_sub_u32 v249, v249, v254\n"
"v_add_u32 v249, s91, v249\n"
"v_mov_b32 v255, 0xff800000\n"
"v_mov_b32 v248, v255\n"
"s_mov_b32 s90, 0\n"
"s_mov_b32 s88, 0\n"
"v_mov_b32 v176, 0\n"
"v_mov_b32 v177, 0\n"
"v_mov_b32 v178, 0\n"
"v_mov_b32 v179, 0\n"
"v_mov_b32 v180, 0\n"
"v_mov_b32 v181, 0\n"
"v_mov_b32 v182, 0\n"
"v_mov_b32 v183, 0\n"
"v_mov_b32 v184, 0\n"
"v_mov_b32 v185, 0\n"
"v_mov_b32 v186, 0\n"
"v_mov_b32 v187, 0\n"
"v_mov_b32 v188, 0\n"
"v_mov_b32 v189, 0\n"
"v_mov_b32 v190, 0\n"
"v_mov_b32 v191, 0\n"
"v_mov_b32 v192, 0\n"
"v_mov_b32 v193, 0\n"
"v_mov_b32 v194, 0\n"
"v_mov_b32 v195, 0\n"
"v_mov_b32 v196, 0\n"
"v_mov_b32 v197, 0\n"
"v_mov_b32 v198, 0\n"
"v_mov_b32 v199, 0\n"
"v_mov_b32 v200, 0\n"
"v_mov_b32 v201, 0\n"
"v_mov_b32 v202, 0\n"
"v_mov_b32 v203, 0\n"
"v_mov_b32 v204, 0\n"
"v_mov_b32 v205, 0\n"
"v_mov_b32 v206, 0\n"
"v_mov_b32 v207, 0\n"
"v_accvgpr_write_b32 a144, 0\n"
"v_accvgpr_write_b32 a145, 0\n"
"v_accvgpr_write_b32 a146, 0\n"
"v_accvgpr_write_b32 a147, 0\n"
"L1042_%=:\n"
"s_lshl_b32 s91, s88, 13\n"
"s_add_u32 s91, s91, s39\n"
"s_add_u32 s91, s91, 0\n"
"buffer_load_dwordx4 a[148:151], v224, s[8:11], s91 offen offset:0 sc1\n"
"buffer_load_dwordx4 a[152:155], v224, s[8:11], s91 offen offset:1024 sc1\n"
"buffer_load_dwordx4 a[156:159], v224, s[8:11], s91 offen offset:2048 sc1\n"
"buffer_load_dwordx4 a[160:163], v224, s[8:11], s91 offen offset:3072 sc1\n"
"s_add_u32 s91, s91, 4096\n"
"buffer_load_dwordx4 a[164:167], v224, s[8:11], s91 offen offset:0 sc1\n"
"buffer_load_dwordx4 a[168:171], v224, s[8:11], s91 offen offset:1024 sc1\n"
"buffer_load_dwordx4 a[172:175], v224, s[8:11], s91 offen offset:2048 sc1\n"
"buffer_load_dwordx4 a[176:179], v224, s[8:11], s91 offen offset:3072 sc1\n"
"s_waitcnt vmcnt(0)\n"
"v_mfma_f32_16x16x32_bf16 v[240:243], a[148:151], a[48:51], 0\n"
"v_mfma_f32_16x16x32_bf16 v[240:243], a[152:155], a[52:55], v[240:243]\n"
"v_mfma_f32_16x16x32_bf16 v[240:243], a[156:159], a[56:59], v[240:243]\n"
"v_mfma_f32_16x16x32_bf16 v[240:243], a[160:163], a[60:63], v[240:243]\n"
"v_mfma_f32_16x16x32_bf16 v[244:247], a[164:167], a[48:51], 0\n"
"v_mfma_f32_16x16x32_bf16 v[244:247], a[168:171], a[52:55], v[244:247]\n"
"v_mfma_f32_16x16x32_bf16 v[244:247], a[172:175], a[56:59], v[244:247]\n"
"v_mfma_f32_16x16x32_bf16 v[244:247], a[176:179], a[60:63], v[244:247]\n"
"s_lshl_b32 s91, s88, 5\n"
"v_subrev_u32 v250, s91, v249\n"
"s_nop 7\n"
"v_cmp_gt_i32 vcc, 0, v250\n"
"s_nop 1\n"
"v_cndmask_b32 v240, v240, v255, vcc\n"
"v_cmp_gt_i32 vcc, 1, v250\n"
"s_nop 1\n"
"v_cndmask_b32 v241, v241, v255, vcc\n"
"v_cmp_gt_i32 vcc, 2, v250\n"
"s_nop 1\n"
"v_cndmask_b32 v242, v242, v255, vcc\n"
"v_cmp_gt_i32 vcc, 3, v250\n"
"s_nop 1\n"
"v_cndmask_b32 v243, v243, v255, vcc\n"
"v_cmp_gt_i32 vcc, 16, v250\n"
"s_nop 1\n"
"v_cndmask_b32 v244, v244, v255, vcc\n"
"v_cmp_gt_i32 vcc, 17, v250\n"
"s_nop 1\n"
"v_cndmask_b32 v245, v245, v255, vcc\n"
"v_cmp_gt_i32 vcc, 18, v250\n"
"s_nop 1\n"
"v_cndmask_b32 v246, v246, v255, vcc\n"
"v_cmp_gt_i32 vcc, 19, v250\n"
"s_nop 1\n"
"v_cndmask_b32 v247, v247, v255, vcc\n"
"s_cmp_lg_u32 s90, 0\n"
"s_cbranch_scc1 L1043_%=\n"
"v_max3_f32 v248, v248, v240, v241\n"
"v_max3_f32 v248, v248, v242, v243\n"
"v_max3_f32 v248, v248, v244, v245\n"
"v_max3_f32 v248, v248, v246, v247\n"
"s_branch L1044_%=\n"
"L1043_%=:\n"
"s_lshl_b32 s91, s88, 13\n"
"s_add_u32 s91, s91, s39\n"
"s_add_u32 s91, s91, 524288\n"
"buffer_load_dwordx4 a[148:151], v224, s[8:11], s91 offen offset:0 sc1\n"
"buffer_load_dwordx4 a[152:155], v224, s[8:11], s91 offen offset:1024 sc1\n"
"buffer_load_dwordx4 a[156:159], v224, s[8:11], s91 offen offset:2048 sc1\n"
"buffer_load_dwordx4 a[160:163], v224, s[8:11], s91 offen offset:3072 sc1\n"
"s_add_u32 s91, s91, 4096\n"
"buffer_load_dwordx4 a[164:167], v224, s[8:11], s91 offen offset:0 sc1\n"
"buffer_load_dwordx4 a[168:171], v224, s[8:11], s91 offen offset:1024 sc1\n"
"buffer_load_dwordx4 a[172:175], v224, s[8:11], s91 offen offset:2048 sc1\n"
"buffer_load_dwordx4 a[176:179], v224, s[8:11], s91 offen offset:3072 sc1\n"
"s_waitcnt vmcnt(0)\n"
"v_sub_f32 v240, v240, v248\n"
"v_sub_f32 v241, v241, v248\n"
"v_sub_f32 v242, v242, v248\n"
"v_sub_f32 v243, v243, v248\n"
"v_sub_f32 v244, v244, v248\n"
"v_sub_f32 v245, v245, v248\n"
"v_sub_f32 v246, v246, v248\n"
"v_sub_f32 v247, v247, v248\n"
"v_exp_f32 v240, v240\n"
"v_exp_f32 v241, v241\n"
"v_exp_f32 v242, v242\n"
"v_exp_f32 v243, v243\n"
"v_exp_f32 v244, v244\n"
"v_exp_f32 v245, v245\n"
"v_exp_f32 v246, v246\n"
"v_exp_f32 v247, v247\n"
"s_nop 0\n"
"v_cvt_pk_bf16_f32 v240, v240, v241\n"
"v_cvt_pk_bf16_f32 v241, v242, v243\n"
"v_cvt_pk_bf16_f32 v242, v244, v245\n"
"v_cvt_pk_bf16_f32 v243, v246, v247\n"
"s_nop 1\n"
"v_mfma_f32_16x16x32_bf16 v[176:179], a[148:151], v[240:243], v[176:179]\n"
"v_mfma_f32_16x16x32_bf16 v[180:183], a[152:155], v[240:243], v[180:183]\n"
"v_mfma_f32_16x16x32_bf16 v[184:187], a[156:159], v[240:243], v[184:187]\n"
"v_mfma_f32_16x16x32_bf16 v[188:191], a[160:163], v[240:243], v[188:191]\n"
"v_mfma_f32_16x16x32_bf16 v[192:195], a[164:167], v[240:243], v[192:195]\n"
"v_mfma_f32_16x16x32_bf16 v[196:199], a[168:171], v[240:243], v[196:199]\n"
"v_mfma_f32_16x16x32_bf16 v[200:203], a[172:175], v[240:243], v[200:203]\n"
"v_mfma_f32_16x16x32_bf16 v[204:207], a[176:179], v[240:243], v[204:207]\n"
"v_mfma_f32_16x16x32_bf16 a[144:147], a[128:131], v[240:243], a[144:147]\n"
"s_nop 7\n"
"L1044_%=:\n"
"s_add_u32 s88, s88, 1\n"
"s_cmp_le_u32 s88, s89\n"
"s_cbranch_scc1 L1042_%=\n"
"s_cmp_lg_u32 s90, 0\n"
"s_cbranch_scc1 L1045_%=\n"
"v_mov_b32 v254, v248\n"
"s_nop 1\n"
"v_permlane16_swap_b32 v254, v248\n"
"s_nop 1\n"
"v_max_f32 v248, v248, v254\n"
"v_mov_b32 v254, v248\n"
"s_nop 1\n"
"v_permlane32_swap_b32 v254, v248\n"
"s_nop 1\n"
"v_max_f32 v248, v248, v254\n"
"s_mov_b32 s90, 1\n"
"s_mov_b32 s88, 0\n"
"s_branch L1042_%=\n"
"L1045_%=:\n"
"s_nop 7\n"
"v_accvgpr_read_b32 v252, a144\n"
"s_nop 1\n"
"s_branch L1040_%=\n"
"L1046_%=:\n"
"s_mov_b32 s54, 0\n"
"L1048_%=:\n"
"s_sleep 2\n"
"buffer_load_dwordx2 v[240:241], v238, s[8:11], s63 offen sc1\n"
"s_waitcnt vmcnt(0)\n"
"s_add_u32 s54, s54, 1\n"
"v_cmp_eq_u64 vcc, s[28:29], v[240:241]\n"
"s_cmp_eq_u64 vcc, exec\n"
"s_cbranch_scc1 L1047_%=\n"
"s_cmp_lt_u32 s54, 1048576\n"
"s_cbranch_scc1 L1048_%=\n"
"s_branch L1047_%=\n"
"L1049_%=:\n"
"s_mov_b32 s54, 0\n"
"L1051_%=:\n"
"s_sleep 2\n"
"buffer_load_dwordx2 v[240:241], v238, s[8:11], s63 offen sc1\n"
"s_waitcnt vmcnt(0)\n"
"s_add_u32 s54, s54, 1\n"
"v_cmp_eq_u64 vcc, s[28:29], v[240:241]\n"
"s_cmp_eq_u64 vcc, exec\n"
"s_cbranch_scc1 L1050_%=\n"
"s_cmp_lt_u32 s54, 1048576\n"
"s_cbranch_scc1 L1051_%=\n"
"s_branch L1050_%=\n"
"L1052_%=:\n"
"s_mov_b32 s54, 0\n"
"L1054_%=:\n"
"s_sleep 2\n"
"buffer_load_dwordx2 v[240:241], v244, s[8:11], s92 offen sc1\n"
"s_waitcnt vmcnt(0)\n"
"s_add_u32 s54, s54, 1\n"
"v_cmp_eq_u64 vcc, s[28:29], v[240:241]\n"
"s_cmp_eq_u64 vcc, exec\n"
"s_cbranch_scc1 L1053_%=\n"
"s_cmp_lt_u32 s54, 1048576\n"
"s_cbranch_scc1 L1054_%=\n"
"s_branch L1053_%=\n"
"L1055_%=:\n"
"s_mov_b32 s54, 0\n"
"L1057_%=:\n"
"s_sleep 2\n"
"buffer_load_dwordx2 v[240:241], v243, s[8:11], s93 offen sc1\n"
"s_waitcnt vmcnt(0)\n"
"s_add_u32 s54, s54, 1\n"
"v_cmp_eq_u64 vcc, s[28:29], v[240:241]\n"
"s_cmp_eq_u64 vcc, exec\n"
"s_cbranch_scc1 L1056_%=\n"
"s_cmp_lt_u32 s54, 1048576\n"
"s_cbranch_scc1 L1057_%=\n"
"s_branch L1056_%=\n"
"L1058_%=:\n"
"s_mov_b32 s54, 0\n"
"L1060_%=:\n"
"s_sleep 2\n"
"buffer_load_dwordx2 v[240:241], v243, s[8:11], s93 offen offset:512 sc1\n"
"s_waitcnt vmcnt(0)\n"
"s_add_u32 s54, s54, 1\n"
"v_cmp_eq_u64 vcc, s[28:29], v[240:241]\n"
"s_cmp_eq_u64 vcc, exec\n"
"s_cbranch_scc1 L1059_%=\n"
"s_cmp_lt_u32 s54, 1048576\n"
"s_cbranch_scc1 L1060_%=\n"
"s_branch L1059_%=\n"
"L1066_%=:\n"
"s_mov_b32 s54, 0\n"
"L1068_%=:\n"
"s_sleep 2\n"
"buffer_load_dwordx4 v[240:243], v224, s[8:11], s48 offen offset:0 sc1\n"
"buffer_load_dwordx4 v[244:247], v224, s[8:11], s48 offen offset:1024 sc1\n"
"s_waitcnt vmcnt(0)\n"
"s_add_u32 s54, s54, 1\n"
"v_cmp_eq_u64 vcc, s[28:29], v[240:241]\n"
"v_cmp_eq_u64_e64 s[52:53], s[28:29], v[242:243]\n"
"s_and_b64 vcc, vcc, s[52:53]\n"
"v_cmp_eq_u64_e64 s[52:53], s[28:29], v[244:245]\n"
"s_and_b64 vcc, vcc, s[52:53]\n"
"v_cmp_eq_u64_e64 s[52:53], s[28:29], v[246:247]\n"
"s_and_b64 vcc, vcc, s[52:53]\n"
"s_cmp_eq_u64 vcc, exec\n"
"s_cbranch_scc1 L1067_%=\n"
"s_cmp_lt_u32 s54, 1048576\n"
"s_cbranch_scc1 L1068_%=\n"
"s_branch L1067_%=\n"
"LFA_%=:\n"
"s_waitcnt lgkmcnt(5)\n"
"v_mfma_f32_16x16x32_bf16 v[0:3], a[64:67], a[0:3], v[208:211]\n"
"s_mov_b32 m0, s40\n"
"v_cvt_pk_bf16_f32 v192, v192, v193\n"
"v_cvt_pk_bf16_f32 v193, v194, v195\n"
"v_mfma_f32_16x16x32_bf16 v[8:11], a[64:67], a[16:19], 0\n"
"v_readlane_b32 s43, v228, s42\n"
"v_cvt_pk_bf16_f32 v194, v196, v197\n"
"v_mfma_f32_16x16x32_bf16 v[16:19], a[64:67], a[32:35], 0\n"
"v_cvt_pk_bf16_f32 v195, v198, v199\n"
"v_readlane_b32 s44, v229, s42\n"
"v_mfma_f32_16x16x32_bf16 v[24:27], a[64:67], a[48:51], 0\n"
"s_waitcnt lgkmcnt(0)\n"
"buffer_load_dwordx4 v225, s[8:11], s43 offen offset:0 sc1 lds\n"
"v_mfma_f32_16x16x32_bf16 v[0:3], a[68:71], a[4:7], v[0:3]\n"
"s_add_u32 s42, s42, 1\n"
"v_accvgpr_write_b32 a44, v192\n"
"v_accvgpr_write_b32 a45, v193\n"
"v_mfma_f32_16x16x32_bf16 v[8:11], a[68:71], a[20:23], v[8:11]\n"
"v_accvgpr_write_b32 a46, v194\n"
"v_accvgpr_write_b32 a47, v195\n"
"v_mfma_f32_16x16x32_bf16 v[16:19], a[68:71], a[36:39], v[16:19]\n"
"v_cvt_pk_bf16_f32 v200, v200, v201\n"
"v_cvt_pk_bf16_f32 v201, v202, v203\n"
"v_mfma_f32_16x16x32_bf16 v[24:27], a[68:71], a[52:55], v[24:27]\n"
"v_cvt_pk_bf16_f32 v202, v204, v205\n"
"v_cvt_pk_bf16_f32 v203, v206, v207\n"
"v_mov_b32 v227, v226\n"
"v_mfma_f32_16x16x32_bf16 v[0:3], a[72:75], a[8:11], v[0:3]\n"
"buffer_load_dwordx4 v225, s[8:11], s43 offen offset:1024 sc1 lds\n"
"v_mfma_f32_16x16x32_bf16 v[8:11], a[72:75], a[24:27], v[8:11]\n"
"ds_read_b128 a[88:91], v226 offset:6144\n"
"v_accvgpr_write_b32 a60, v200\n"
"v_accvgpr_write_b32 a61, v201\n"
"v_mfma_f32_16x16x32_bf16 v[16:19], a[72:75], a[40:43], v[16:19]\n"
"s_lshl_b32 s59, s34, 5\n"
"v_accvgpr_write_b32 a62, v202\n"
"v_accvgpr_write_b32 a63, v203\n"
"v_mfma_f32_16x16x32_bf16 v[24:27], a[72:75], a[56:59], v[24:27]\n"
"s_sub_u32 s60, 480, s59\n"
"v_mfma_f32_16x16x32_bf16 v[0:3], a[76:79], a[12:15], v[0:3]\n"
"s_add_u32 s59, s59, s35\n"
"v_mfma_f32_16x16x32_bf16 v[8:11], a[76:79], a[28:31], v[8:11]\n"
"s_add_u32 s60, s60, s35\n"
"ds_read_b128 a[92:95], v226 offset:7168\n"
"v_mfma_f32_16x16x32_bf16 v[16:19], a[76:79], a[44:47], v[16:19]\n"
"s_lshl_b32 s59, s59, 15\n"
"buffer_load_dwordx4 v225, s[8:11], s43 offen offset:2048 sc1 lds\n"
"v_mfma_f32_16x16x32_bf16 v[24:27], a[76:79], a[60:63], v[24:27]\n"
"s_lshl_b32 s60, s60, 15\n"
"v_mfma_f32_16x16x32_bf16 v[4:7], a[80:83], a[0:3], v[212:215]\n"
"s_add_u32 s48, s36, 44040192\n"
"v_exp_f32 v0, v0\n"
"v_mfma_f32_16x16x32_bf16 v[12:15], a[80:83], a[16:19], 0\n"
"s_add_u32 s59, s59, s48\n"
"ds_read_b128 a[96:99], v226 offset:8192\n"
"v_mfma_f32_16x16x32_bf16 v[20:23], a[80:83], a[32:35], 0\n"
"s_add_u32 s60, s60, s48\n"
"v_exp_f32 v1, v1\n"
"v_mfma_f32_16x16x32_bf16 v[28:31], a[80:83], a[48:51], 0\n"
"s_lshl_b32 s61, s34, 3\n"
"v_exp_f32 v2, v2\n"
"v_mfma_f32_16x16x32_bf16 v[4:7], a[84:87], a[4:7], v[4:7]\n"
"s_add_u32 s61, s61, s32\n"
"buffer_load_dwordx4 v225, s[8:11], s43 offen offset:3072 sc1 lds\n"
"v_mfma_f32_16x16x32_bf16 v[12:15], a[84:87], a[20:23], v[12:15]\n"
"s_lshl_b32 s61, s61, 2\n"
"v_exp_f32 v3, v3\n"
"v_mfma_f32_16x16x32_bf16 v[20:23], a[84:87], a[36:39], v[20:23]\n"
"s_add_u32 s61, s61, s30\n"
"ds_read_b128 a[100:103], v226 offset:9216\n"
"v_cvt_pk_bf16_f32 v0, v0, v1\n"
"v_mfma_f32_16x16x32_bf16 v[28:31], a[84:87], a[52:55], v[28:31]\n"
"s_waitcnt lgkmcnt(2)\n"
"v_exp_f32 v8, v8\n"
"v_mfma_f32_16x16x32_bf16 v[4:7], a[88:91], a[8:11], v[4:7]\n"
"s_add_u32 m0, s40, 16384\n"
"v_exp_f32 v9, v9\n"
"v_cvt_pk_bf16_f32 v1, v2, v3\n"
"v_mfma_f32_16x16x32_bf16 v[12:15], a[88:91], a[24:27], v[12:15]\n"
"s_lshl_b32 s61, s61, 2\n"
"v_exp_f32 v10, v10\n"
"v_mfma_f32_16x16x32_bf16 v[20:23], a[88:91], a[40:43], v[20:23]\n"
"s_add_u32 s61, s61, s33\n"
"ds_read_b128 a[104:107], v226 offset:10240\n"
"v_cvt_pk_bf16_f32 v8, v8, v9\n"
"v_mfma_f32_16x16x32_bf16 v[28:31], a[88:91], a[56:59], v[28:31]\n"
"s_lshl_b32 s61, s61, 3\n"
"buffer_load_dwordx4 v225, s[8:11], s44 offen offset:0 sc1 lds\n"
"v_mfma_f32_16x16x32_bf16 v[4:7], a[92:95], a[12:15], v[4:7]\n"
"s_add_u32 s61, s61, 6912\n"
"v_exp_f32 v11, v11\n"
"v_mfma_f32_16x16x32_bf16 v[12:15], a[92:95], a[28:31], v[12:15]\n"
"s_lshr_b32 s48, s31, 3\n"
"v_exp_f32 v16, v16\n"
"v_mfma_f32_16x16x32_bf16 v[20:23], a[92:95], a[44:47], v[20:23]\n"
"s_add_u32 s40, s40, 32768\n"
"v_exp_f32 v17, v17\n"
"v_cvt_pk_bf16_f32 v9, v10, v11\n"
"v_mfma_f32_16x16x32_bf16 v[28:31], a[92:95], a[60:63], v[28:31]\n"
"s_waitcnt lgkmcnt(1)\n"
"ds_read_b128 a[108:111], v226 offset:11264\n"
"v_cvt_pk_bf16_f32 v16, v16, v17\n"
"v_mfma_f32_16x16x32_bf16 v[32:35], a[96:99], a[0:3], v[216:219]\n"
"s_add_u32 s58, s32, 32\n"
"v_exp_f32 v18, v18\n"
"v_mfma_f32_16x16x32_bf16 v[40:43], a[96:99], a[16:19], 0\n"
"s_cmp_eq_u32 s40, s46\n"
"v_exp_f32 v19, v19\n"
"v_mfma_f32_16x16x32_bf16 v[48:51], a[96:99], a[32:35], 0\n"
"s_cselect_b32 s40, s47, s40\n"
"buffer_load_dwordx4 v225, s[8:11], s44 offen offset:1024 sc1 lds\n"
"v_mfma_f32_16x16x32_bf16 v[56:59], a[96:99], a[48:51], 0\n"
"s_cmp_eq_u32 s48, 5\n"
"v_exp_f32 v24, v24\n"
"v_cvt_pk_bf16_f32 v17, v18, v19\n"
"v_mfma_f32_16x16x32_bf16 v[32:35], a[100:103], a[4:7], v[32:35]\n"
"s_cselect_b32 s58, s58, 99\n"
"ds_read_b128 a[112:115], v226 offset:12288\n"
"s_cmp_eq_u32 s34, 6\n"
"v_mfma_f32_16x16x32_bf16 v[40:43], a[100:103], a[20:23], v[40:43]\n"
"s_cselect_b32 s58, s35, s58\n"
"v_exp_f32 v25, v25\n"
"v_mfma_f32_16x16x32_bf16 v[48:51], a[100:103], a[36:39], v[48:51]\n"
"s_lshl_b32 s56, s34, 1\n"
"v_exp_f32 v26, v26\n"
"v_cvt_pk_bf16_f32 v24, v24, v25\n"
"v_mfma_f32_16x16x32_bf16 v[56:59], a[100:103], a[52:55], v[56:59]\n"
"s_waitcnt lgkmcnt(1)\n"
"v_exp_f32 v27, v27\n"
"v_mfma_f32_16x16x32_bf16 v[32:35], a[104:107], a[8:11], v[32:35]\n"
"s_sub_u32 s56, s56, 2\n"
"ds_read_b128 a[116:119], v226 offset:13312\n"
"v_cvt_pk_bf16_f32 v25, v26, v27\n"
"v_mfma_f32_16x16x32_bf16 v[40:43], a[104:107], a[24:27], v[40:43]\n"
"s_cmp_ge_u32 s34, 5\n"
"buffer_load_dwordx4 v225, s[8:11], s44 offen offset:2048 sc1 lds\n"
"v_mfma_f32_16x16x32_bf16 v[48:51], a[104:107], a[40:43], v[48:51]\n"
"s_cselect_b32 s56, 1, s56\n"
"v_exp_f32 v4, v4\n"
"v_mfma_f32_16x16x32_bf16 v[56:59], a[104:107], a[56:59], v[56:59]\n"
"s_cmp_ge_u32 s34, 5\n"
"v_exp_f32 v5, v5\n"
"v_mfma_f32_16x16x32_bf16 v[32:35], a[108:111], a[12:15], v[32:35]\n"
"s_cselect_b32 s57, 1, 0\n"
"v_exp_f32 v6, v6\n"
"v_cvt_pk_bf16_f32 v2, v4, v5\n"
"v_mfma_f32_16x16x32_bf16 v[40:43], a[108:111], a[28:31], v[40:43]\n"
"s_lshl_b32 s48, s35, 3\n"
"ds_read_b128 a[120:123], v226 offset:14336\n"
"ds_read_b128 a[124:127], v226 offset:15360\n"
"v_readlane_b32 s43, v228, s42\n"
"v_mfma_f32_16x16x32_bf16 v[48:51], a[108:111], a[44:47], v[48:51]\n"
"s_add_u32 s48, s48, s34\n"
"v_exp_f32 v7, v7\n"
"v_mfma_f32_16x16x32_bf16 v[56:59], a[108:111], a[60:63], v[56:59]\n"
"s_waitcnt lgkmcnt(2)\n"
"v_exp_f32 v12, v12\n"
"v_cvt_pk_bf16_f32 v3, v6, v7\n"
"v_mfma_f32_16x16x32_bf16 v[36:39], a[112:115], a[0:3], v[220:223]\n"
"s_lshl_b32 s48, s48, 3\n"
"buffer_load_dwordx4 v225, s[8:11], s44 offen offset:3072 sc1 lds\n"
"v_mfma_f32_16x16x32_bf16 v[44:47], a[112:115], a[16:19], 0\n"
"s_add_u32 s48, s48, 4352\n"
"v_exp_f32 v13, v13\n"
"v_mfma_f32_16x16x32_bf16 v[52:55], a[112:115], a[32:35], 0\n"
"s_add_u32 s48, s48, s45\n"
"v_exp_f32 v14, v14\n"
"v_cvt_pk_bf16_f32 v10, v12, v13\n"
"v_mfma_f32_16x16x32_bf16 v[60:63], a[112:115], a[48:51], 0\n"
"s_mov_b32 m0, s40\n"
"v_exp_f32 v15, v15\n"
"v_readlane_b32 s44, v229, s42\n"
"v_mfma_f32_16x16x32_bf16 v[36:39], a[116:119], a[4:7], v[36:39]\n"
"s_add_u32 s42, s42, 1\n"
"v_exp_f32 v20, v20\n"
"v_cvt_pk_bf16_f32 v11, v14, v15\n"
"v_mfma_f32_16x16x32_bf16 v[44:47], a[116:119], a[20:23], v[44:47]\n"
"ds_read_b128 a[96:99], v227 offset:24576\n"
"ds_read_b128 a[100:103], v227 offset:25600\n"
"v_mfma_f32_16x16x32_bf16 v[52:55], a[116:119], a[36:39], v[52:55]\n"
"buffer_load_dwordx4 v225, s[8:11], s43 offen offset:0 sc1 lds\n"
"v_mfma_f32_16x16x32_bf16 v[60:63], a[116:119], a[52:55], v[60:63]\n"
"s_waitcnt lgkmcnt(2)\n"
"v_exp_f32 v21, v21\n"
"v_mfma_f32_16x16x32_bf16 v[36:39], a[120:123], a[8:11], v[36:39]\n"
"v_exp_f32 v22, v22\n"
"v_cvt_pk_bf16_f32 v18, v20, v21\n"
"v_mfma_f32_16x16x32_bf16 v[44:47], a[120:123], a[24:27], v[44:47]\n"
"v_exp_f32 v23, v23\n"
"v_mfma_f32_16x16x32_bf16 v[52:55], a[120:123], a[40:43], v[52:55]\n"
"buffer_load_dwordx4 v225, s[8:11], s43 offen offset:1024 sc1 lds\n"
"v_cvt_pk_bf16_f32 v19, v22, v23\n"
"v_mfma_f32_16x16x32_bf16 v[60:63], a[120:123], a[56:59], v[60:63]\n"
"v_exp_f32 v28, v28\n"
"v_mfma_f32_16x16x32_bf16 v[36:39], a[124:127], a[12:15], v[36:39]\n"
"ds_read_b128 a[104:107], v227 offset:26624\n"
"v_mfma_f32_16x16x32_bf16 v[44:47], a[124:127], a[28:31], v[44:47]\n"
"v_exp_f32 v29, v29\n"
"v_mfma_f32_16x16x32_bf16 v[52:55], a[124:127], a[44:47], v[52:55]\n"
"v_exp_f32 v30, v30\n"
"v_cvt_pk_bf16_f32 v26, v28, v29\n"
"v_mfma_f32_16x16x32_bf16 v[60:63], a[124:127], a[60:63], v[60:63]\n"
"s_waitcnt lgkmcnt(1)\n"
"v_exp_f32 v31, v31\n"
"v_mfma_f32_16x16x32_bf16 v[80:83], a[96:99], v[0:3], 0\n"
"s_add_u32 s41, s41, 32768\n"
"v_cvt_pk_bf16_f32 v27, v30, v31\n"
"s_cmp_eq_u32 s41, 163840\n"
"v_mfma_f32_16x16x32_bf16 v[112:115], a[96:99], v[8:11], 0\n"
"s_cselect_b32 s41, 0, s41\n"
"buffer_load_dwordx4 v225, s[8:11], s43 offen offset:2048 sc1 lds\n"
"v_mfma_f32_16x16x32_bf16 v[144:147], a[96:99], v[16:19], 0\n"
"ds_read_b128 a[108:111], v227 offset:27648\n"
"v_add_u32 v226, s41, v224\n"
"v_mfma_f32_16x16x32_bf16 v[176:179], a[96:99], v[24:27], 0\n"
"v_exp_f32 v32, v32\n"
"v_mfma_f32_16x16x32_bf16 v[84:87], a[100:103], v[0:3], 0\n"
"v_exp_f32 v33, v33\n"
"v_mfma_f32_16x16x32_bf16 v[116:119], a[100:103], v[8:11], 0\n"
"v_exp_f32 v34, v34\n"
"v_cvt_pk_bf16_f32 v64, v32, v33\n"
"v_mfma_f32_16x16x32_bf16 v[148:151], a[100:103], v[16:19], 0\n"
"ds_read_b128 a[112:115], v227 offset:28672\n"
"ds_read_b128 a[116:119], v227 offset:29696\n"
"v_mfma_f32_16x16x32_bf16 v[180:183], a[100:103], v[24:27], 0\n"
"s_waitcnt lgkmcnt(3)\n"
"v_exp_f32 v35, v35\n"
"v_mfma_f32_16x16x32_bf16 v[88:91], a[104:107], v[0:3], 0\n"
"buffer_load_dwordx4 v225, s[8:11], s43 offen offset:3072 sc1 lds\n"
"v_cvt_pk_bf16_f32 v65, v34, v35\n"
"v_mfma_f32_16x16x32_bf16 v[120:123], a[104:107], v[8:11], 0\n"
"s_add_u32 m0, s40, 16384\n"
"v_exp_f32 v40, v40\n"
"v_mfma_f32_16x16x32_bf16 v[152:155], a[104:107], v[16:19], 0\n"
"s_add_u32 s40, s40, 32768\n"
"v_exp_f32 v41, v41\n"
"v_mfma_f32_16x16x32_bf16 v[184:187], a[104:107], v[24:27], 0\n"
"s_waitcnt lgkmcnt(2)\n"
"v_exp_f32 v42, v42\n"
"v_cvt_pk_bf16_f32 v68, v40, v41\n"
"v_mfma_f32_16x16x32_bf16 v[92:95], a[108:111], v[0:3], 0\n"
"s_cmp_eq_u32 s40, s46\n"
"v_exp_f32 v43, v43\n"
"v_mfma_f32_16x16x32_bf16 v[124:127], a[108:111], v[8:11], 0\n"
"s_cselect_b32 s40, s47, s40\n"
"buffer_load_dwordx4 v225, s[8:11], s44 offen offset:0 sc1 lds\n"
"v_cvt_pk_bf16_f32 v69, v42, v43\n"
"v_mfma_f32_16x16x32_bf16 v[156:159], a[108:111], v[16:19], 0\n"
"ds_read_b128 a[120:123], v227 offset:30720\n"
"v_mfma_f32_16x16x32_bf16 v[188:191], a[108:111], v[24:27], 0\n"
"s_waitcnt lgkmcnt(1)\n"
"v_exp_f32 v48, v48\n"
"v_mfma_f32_16x16x32_bf16 v[96:99], a[112:115], v[0:3], 0\n"
"v_exp_f32 v49, v49\n"
"v_mfma_f32_16x16x32_bf16 v[128:131], a[112:115], v[8:11], 0\n"
"v_exp_f32 v50, v50\n"
"v_cvt_pk_bf16_f32 v72, v48, v49\n"
"v_mfma_f32_16x16x32_bf16 v[160:163], a[112:115], v[16:19], 0\n"
"ds_read_b128 a[124:127], v227 offset:31744\n"
"v_mfma_f32_16x16x32_bf16 v[192:195], a[112:115], v[24:27], 0\n"
"v_exp_f32 v51, v51\n"
"v_mfma_f32_16x16x32_bf16 v[100:103], a[116:119], v[0:3], 0\n"
"buffer_load_dwordx4 v225, s[8:11], s44 offen offset:1024 sc1 lds\n"
"v_cvt_pk_bf16_f32 v73, v50, v51\n"
"v_mfma_f32_16x16x32_bf16 v[132:135], a[116:119], v[8:11], 0\n"
"v_exp_f32 v36, v36\n"
"v_mfma_f32_16x16x32_bf16 v[164:167], a[116:119], v[16:19], 0\n"
"v_exp_f32 v37, v37\n"
"v_mfma_f32_16x16x32_bf16 v[196:199], a[116:119], v[24:27], 0\n"
"s_waitcnt lgkmcnt(1)\n"
"v_exp_f32 v38, v38\n"
"v_cvt_pk_bf16_f32 v66, v36, v37\n"
"v_mfma_f32_16x16x32_bf16 v[104:107], a[120:123], v[0:3], 0\n"
"v_exp_f32 v39, v39\n"
"v_mfma_f32_16x16x32_bf16 v[136:139], a[120:123], v[8:11], 0\n"
"s_waitcnt vmcnt(14)\n"
"s_barrier\n"
"v_mfma_f32_16x16x32_bf16 v[168:171], a[120:123], v[16:19], 0\n"
"buffer_load_dwordx4 v225, s[8:11], s44 offen offset:2048 sc1 lds\n"
"v_cvt_pk_bf16_f32 v67, v38, v39\n"
"v_mfma_f32_16x16x32_bf16 v[200:203], a[120:123], v[24:27], 0\n"
"s_waitcnt lgkmcnt(0)\n"
"ds_read_b128 a[64:67], v226 offset:0\n"
"v_mfma_f32_16x16x32_bf16 v[108:111], a[124:127], v[0:3], 0\n"
"buffer_store_dwordx2 v[232:233], v230, s[8:11], s48 offen sc1\n"
"v_mfma_f32_16x16x32_bf16 v[140:143], a[124:127], v[8:11], 0\n"
"v_exp_f32 v44, v44\n"
"v_mfma_f32_16x16x32_bf16 v[172:175], a[124:127], v[16:19], 0\n"
"v_exp_f32 v45, v45\n"
"v_mfma_f32_16x16x32_bf16 v[204:207], a[124:127], v[24:27], 0\n"
"v_exp_f32 v46, v46\n"
"v_cvt_pk_bf16_f32 v70, v44, v45\n"
"v_mfma_f32_16x16x32_bf16 a[132:135], a[128:131], v[0:3], 0\n"
"ds_read_b128 a[68:71], v226 offset:1024\n"
"ds_read_b128 a[72:75], v226 offset:2048\n"
"v_mfma_f32_16x16x32_bf16 a[136:139], a[128:131], v[8:11], 0\n"
"v_exp_f32 v47, v47\n"
"v_mfma_f32_16x16x32_bf16 a[140:143], a[128:131], v[16:19], 0\n"
"v_exp_f32 v52, v52\n"
"v_cvt_pk_bf16_f32 v71, v46, v47\n"
"v_mfma_f32_16x16x32_bf16 a[144:147], a[128:131], v[24:27], 0\n"
"v_exp_f32 v53, v53\n"
"buffer_load_dwordx4 v225, s[8:11], s44 offen offset:3072 sc1 lds\n"
"v_cvt_pk_bf16_f32 v74, v52, v53\n"
"v_exp_f32 v54, v54\n"
"v_exp_f32 v55, v55\n"
"s_nop 0\n"
"v_cvt_pk_bf16_f32 v75, v54, v55\n"
"ds_read_b128 a[76:79], v226 offset:3072\n"
"ds_read_b128 a[80:83], v226 offset:4096\n"
"ds_read_b128 a[84:87], v226 offset:5120\n"
"s_branch LDB_%=\n"
"LD2_%=:\n"
"s_waitcnt lgkmcnt(5)\n"
"v_mfma_f32_16x16x32_bf16 v[0:3], a[64:67], a[32:35], v[208:211]\n"
"v_exp_f32 v56, v56\n"
"v_mfma_f32_16x16x32_bf16 v[8:11], a[64:67], a[48:51], 0\n"
"s_waitcnt lgkmcnt(2)\n"
"ds_read_b128 a[88:91], v226 offset:6144\n"
"v_readlane_b32 s43, v228, s42\n"
"v_mfma_f32_16x16x32_bf16 v[0:3], a[68:71], a[36:39], v[0:3]\n"
"v_exp_f32 v57, v57\n"
"v_mfma_f32_16x16x32_bf16 v[8:11], a[68:71], a[52:55], v[8:11]\n"
"s_mov_b32 m0, s40\n"
"ds_read_b128 a[92:95], v226 offset:7168\n"
"ds_read_b128 a[96:99], v226 offset:8192\n"
"v_mfma_f32_16x16x32_bf16 v[0:3], a[72:75], a[40:43], v[0:3]\n"
"s_add_u32 s41, s41, 32768\n"
"v_exp_f32 v58, v58\n"
"v_mfma_f32_16x16x32_bf16 v[8:11], a[72:75], a[56:59], v[8:11]\n"
"s_cmp_eq_u32 s41, 163840\n"
"ds_read_b128 a[100:103], v226 offset:9216\n"
"v_readlane_b32 s44, v229, s42\n"
"v_mfma_f32_16x16x32_bf16 v[0:3], a[76:79], a[44:47], v[0:3]\n"
"s_cselect_b32 s41, 0, s41\n"
"v_exp_f32 v59, v59\n"
"v_cvt_pk_bf16_f32 v76, v56, v57\n"
"v_mfma_f32_16x16x32_bf16 v[8:11], a[76:79], a[60:63], v[8:11]\n"
"s_waitcnt lgkmcnt(4)\n"
"buffer_load_dwordx4 v225, s[8:11], s43 offen offset:0 sc1 lds\n"
"v_add_u32 v227, s41, v224\n"
"v_mfma_f32_16x16x32_bf16 v[4:7], a[80:83], a[32:35], v[212:215]\n"
"s_add_u32 s42, s42, 1\n"
"v_exp_f32 v60, v60\n"
"v_mfma_f32_16x16x32_bf16 v[12:15], a[80:83], a[48:51], 0\n"
"ds_read_b128 a[104:107], v226 offset:10240\n"
"v_cvt_pk_bf16_f32 v77, v58, v59\n"
"v_mfma_f32_16x16x32_bf16 v[4:7], a[84:87], a[36:39], v[4:7]\n"
"v_exp_f32 v61, v61\n"
"v_mfma_f32_16x16x32_bf16 v[12:15], a[84:87], a[52:55], v[12:15]\n"
"s_waitcnt lgkmcnt(2)\n"
"ds_read_b128 a[108:111], v226 offset:11264\n"
"ds_read_b128 a[112:115], v226 offset:12288\n"
"v_mfma_f32_16x16x32_bf16 v[4:7], a[88:91], a[40:43], v[4:7]\n"
"v_exp_f32 v62, v62\n"
"v_mfma_f32_16x16x32_bf16 v[12:15], a[88:91], a[56:59], v[12:15]\n"
"ds_read_b128 a[116:119], v226 offset:13312\n"
"v_cvt_pk_bf16_f32 v78, v60, v61\n"
"v_mfma_f32_16x16x32_bf16 v[4:7], a[92:95], a[44:47], v[4:7]\n"
"v_exp_f32 v63, v63\n"
"v_mfma_f32_16x16x32_bf16 v[12:15], a[92:95], a[60:63], v[12:15]\n"
"buffer_load_dwordx4 v225, s[8:11], s43 offen offset:1024 sc1 lds\n"
"v_mfma_f32_16x16x32_bf16 v[16:19], a[96:99], a[32:35], v[216:219]\n"
"ds_read_b128 a[120:123], v226 offset:14336\n"
"v_cvt_pk_bf16_f32 v79, v62, v63\n"
"v_mfma_f32_16x16x32_bf16 v[24:27], a[96:99], a[48:51], 0\n"
"s_waitcnt lgkmcnt(4)\n"
"ds_read_b128 a[124:127], v226 offset:15360\n"
"v_mfma_f32_16x16x32_bf16 v[16:19], a[100:103], a[36:39], v[16:19]\n"
"ds_read_b128 a[64:67], v226 offset:16384\n"
"v_mfma_f32_16x16x32_bf16 v[24:27], a[100:103], a[52:55], v[24:27]\n"
"ds_read_b128 a[68:71], v226 offset:17408\n"
"v_mfma_f32_16x16x32_bf16 v[16:19], a[104:107], a[40:43], v[16:19]\n"
"ds_read_b128 a[72:75], v226 offset:18432\n"
"v_mfma_f32_16x16x32_bf16 v[24:27], a[104:107], a[56:59], v[24:27]\n"
"s_waitcnt lgkmcnt(5)\n"
"ds_read_b128 a[76:79], v226 offset:19456\n"
"v_mfma_f32_16x16x32_bf16 v[16:19], a[108:111], a[44:47], v[16:19]\n"
"ds_read_b128 a[80:83], v226 offset:20480\n"
"v_mfma_f32_16x16x32_bf16 v[24:27], a[108:111], a[60:63], v[24:27]\n"
"buffer_load_dwordx4 v225, s[8:11], s43 offen offset:2048 sc1 lds\n"
"v_mfma_f32_16x16x32_bf16 v[20:23], a[112:115], a[32:35], v[220:223]\n"
"ds_read_b128 a[84:87], v226 offset:21504\n"
"v_mfma_f32_16x16x32_bf16 v[28:31], a[112:115], a[48:51], 0\n"
"ds_read_b128 a[88:91], v226 offset:22528\n"
"v_mfma_f32_16x16x32_bf16 v[20:23], a[116:119], a[36:39], v[20:23]\n"
"ds_read_b128 a[92:95], v226 offset:23552\n"
"v_mfma_f32_16x16x32_bf16 v[28:31], a[116:119], a[52:55], v[28:31]\n"
"s_waitcnt lgkmcnt(5)\n"
"ds_read_b128 a[96:99], v226 offset:24576\n"
"v_mfma_f32_16x16x32_bf16 v[20:23], a[120:123], a[40:43], v[20:23]\n"
"ds_read_b128 a[100:103], v226 offset:25600\n"
"v_mfma_f32_16x16x32_bf16 v[28:31], a[120:123], a[56:59], v[28:31]\n"
"ds_read_b128 a[104:107], v226 offset:26624\n"
"v_mfma_f32_16x16x32_bf16 v[20:23], a[124:127], a[44:47], v[20:23]\n"
"ds_read_b128 a[108:111], v226 offset:27648\n"
"v_mfma_f32_16x16x32_bf16 v[28:31], a[124:127], a[60:63], v[28:31]\n"
"ds_read_b128 a[112:115], v226 offset:28672\n"
"v_mfma_f32_16x16x32_bf16 v[40:43], a[64:67], a[48:51], v[208:211]\n"
"buffer_load_dwordx4 v225, s[8:11], s43 offen offset:3072 sc1 lds\n"
"v_mfma_f32_16x16x32_bf16 v[40:43], a[68:71], a[52:55], v[40:43]\n"
"ds_read_b128 a[116:119], v226 offset:29696\n"
"v_mfma_f32_16x16x32_bf16 v[40:43], a[72:75], a[56:59], v[40:43]\n"
"s_waitcnt lgkmcnt(5)\n"
"ds_read_b128 a[120:123], v226 offset:30720\n"
"v_mfma_f32_16x16x32_bf16 v[40:43], a[76:79], a[60:63], v[40:43]\n"
"s_add_u32 m0, s40, 16384\n"
"ds_read_b128 a[124:127], v226 offset:31744\n"
"v_mfma_f32_16x16x32_bf16 v[44:47], a[80:83], a[48:51], v[212:215]\n"
"v_exp_f32 v0, v0\n"
"v_mfma_f32_16x16x32_bf16 v[44:47], a[84:87], a[52:55], v[44:47]\n"
"s_barrier\n"
"v_mfma_f32_16x16x32_bf16 v[44:47], a[88:91], a[56:59], v[44:47]\n"
"v_exp_f32 v1, v1\n"
"v_mfma_f32_16x16x32_bf16 v[44:47], a[92:95], a[60:63], v[44:47]\n"
"buffer_load_dwordx4 v225, s[8:11], s44 offen offset:0 sc1 lds\n"
"v_mfma_f32_16x16x32_bf16 v[56:59], a[96:99], a[48:51], v[216:219]\n"
"s_waitcnt lgkmcnt(2)\n"
"ds_read_b128 a[96:99], v227 offset:8192\n"
"v_cvt_pk_bf16_f32 v0, v0, v1\n"
"v_mfma_f32_16x16x32_bf16 v[56:59], a[100:103], a[52:55], v[56:59]\n"
"s_add_u32 s40, s40, 32768\n"
"v_exp_f32 v2, v2\n"
"v_mfma_f32_16x16x32_bf16 v[56:59], a[104:107], a[56:59], v[56:59]\n"
"s_cmp_eq_u32 s40, s46\n"
"ds_read_b128 a[100:103], v227 offset:9216\n"
"s_cselect_b32 s40, s47, s40\n"
"v_mfma_f32_16x16x32_bf16 v[56:59], a[108:111], a[60:63], v[56:59]\n"
"v_exp_f32 v3, v3\n"
"v_mfma_f32_16x16x32_bf16 v[60:63], a[112:115], a[48:51], v[220:223]\n"
"ds_read_b128 a[104:107], v227 offset:10240\n"
"ds_read_b128 a[108:111], v227 offset:11264\n"
"v_mfma_f32_16x16x32_bf16 v[60:63], a[116:119], a[52:55], v[60:63]\n"
"s_waitcnt lgkmcnt(4)\n"
"v_exp_f32 v8, v8\n"
"v_mfma_f32_16x16x32_bf16 v[60:63], a[120:123], a[56:59], v[60:63]\n"
"v_exp_f32 v9, v9\n"
"v_cvt_pk_bf16_f32 v1, v2, v3\n"
"v_mfma_f32_16x16x32_bf16 v[60:63], a[124:127], a[60:63], v[60:63]\n"
"s_waitcnt lgkmcnt(3)\n"
"v_exp_f32 v10, v10\n"
"v_mfma_f32_16x16x32_bf16 v[112:115], a[96:99], v[68:71], v[112:115]\n"
"s_add_u32 s41, s41, 32768\n"
"buffer_load_dwordx4 v225, s[8:11], s44 offen offset:1024 sc1 lds\n"
"v_mfma_f32_16x16x32_bf16 v[144:147], a[96:99], v[72:75], v[144:147]\n"
"s_cmp_eq_u32 s41, 163840\n"
"v_exp_f32 v11, v11\n"
"v_cvt_pk_bf16_f32 v8, v8, v9\n"
"v_mfma_f32_16x16x32_bf16 v[176:179], a[96:99], v[76:79], v[176:179]\n"
"s_waitcnt lgkmcnt(2)\n"
"ds_read_b128 a[112:115], v227 offset:12288\n"
"v_cvt_pk_bf16_f32 v9, v10, v11\n"
"v_mfma_f32_16x16x32_bf16 v[116:119], a[100:103], v[68:71], v[116:119]\n"
"s_cselect_b32 s41, 0, s41\n"
"v_exp_f32 v4, v4\n"
"v_add_u32 v226, s41, v224\n"
"v_mfma_f32_16x16x32_bf16 v[148:151], a[100:103], v[72:75], v[148:151]\n"
"ds_read_b128 a[116:119], v227 offset:13312\n"
"v_mfma_f32_16x16x32_bf16 v[180:183], a[100:103], v[76:79], v[180:183]\n"
"s_waitcnt lgkmcnt(2)\n"
"v_exp_f32 v5, v5\n"
"v_mfma_f32_16x16x32_bf16 v[120:123], a[104:107], v[68:71], v[120:123]\n"
"ds_read_b128 a[120:123], v227 offset:14336\n"
"v_cvt_pk_bf16_f32 v2, v4, v5\n"
"v_mfma_f32_16x16x32_bf16 v[152:155], a[104:107], v[72:75], v[152:155]\n"
"v_exp_f32 v6, v6\n"
"v_mfma_f32_16x16x32_bf16 v[184:187], a[104:107], v[76:79], v[184:187]\n"
"buffer_load_dwordx4 v225, s[8:11], s44 offen offset:2048 sc1 lds\n"
"v_mfma_f32_16x16x32_bf16 v[124:127], a[108:111], v[68:71], v[124:127]\n"
"v_exp_f32 v7, v7\n"
"v_mfma_f32_16x16x32_bf16 v[156:159], a[108:111], v[72:75], v[156:159]\n"
"ds_read_b128 a[124:127], v227 offset:15360\n"
"v_cvt_pk_bf16_f32 v3, v6, v7\n"
"v_mfma_f32_16x16x32_bf16 v[188:191], a[108:111], v[76:79], v[188:191]\n"
"s_waitcnt lgkmcnt(2)\n"
"v_exp_f32 v12, v12\n"
"v_mfma_f32_16x16x32_bf16 v[128:131], a[112:115], v[68:71], v[128:131]\n"
"v_exp_f32 v13, v13\n"
"v_mfma_f32_16x16x32_bf16 v[160:163], a[112:115], v[72:75], v[160:163]\n"
"v_exp_f32 v14, v14\n"
"v_mfma_f32_16x16x32_bf16 v[192:195], a[112:115], v[76:79], v[192:195]\n"
"ds_read_b128 a[64:67], v227 offset:16384\n"
"v_cvt_pk_bf16_f32 v10, v12, v13\n"
"v_mfma_f32_16x16x32_bf16 v[132:135], a[116:119], v[68:71], v[132:135]\n"
"v_exp_f32 v15, v15\n"
"v_mfma_f32_16x16x32_bf16 v[164:167], a[116:119], v[72:75], v[164:167]\n"
"buffer_load_dwordx4 v225, s[8:11], s44 offen offset:3072 sc1 lds\n"
"v_mfma_f32_16x16x32_bf16 v[196:199], a[116:119], v[76:79], v[196:199]\n"
"s_waitcnt lgkmcnt(1)\n"
"v_exp_f32 v16, v16\n"
"v_mfma_f32_16x16x32_bf16 v[136:139], a[120:123], v[68:71], v[136:139]\n"
"ds_read_b128 a[68:71], v227 offset:17408\n"
"v_readlane_b32 s43, v228, s42\n"
"v_cvt_pk_bf16_f32 v11, v14, v15\n"
"v_mfma_f32_16x16x32_bf16 v[168:171], a[120:123], v[72:75], v[168:171]\n"
"s_mov_b32 m0, s40\n"
"v_exp_f32 v17, v17\n"
"v_mfma_f32_16x16x32_bf16 v[200:203], a[120:123], v[76:79], v[200:203]\n"
"ds_read_b128 a[72:75], v227 offset:18432\n"
"ds_read_b128 a[76:79], v227 offset:19456\n"
"v_mfma_f32_16x16x32_bf16 v[140:143], a[124:127], v[68:71], v[140:143]\n"
"v_exp_f32 v18, v18\n"
"v_mfma_f32_16x16x32_bf16 v[172:175], a[124:127], v[72:75], v[172:175]\n"
"ds_read_b128 a[80:83], v227 offset:20480\n"
"v_readlane_b32 s44, v229, s42\n"
"v_cvt_pk_bf16_f32 v16, v16, v17\n"
"v_mfma_f32_16x16x32_bf16 v[204:207], a[124:127], v[76:79], v[204:207]\n"
"s_add_u32 s42, s42, 1\n"
"v_exp_f32 v19, v19\n"
"v_mfma_f32_16x16x32_bf16 a[136:139], a[128:131], v[68:71], a[136:139]\n"
"buffer_load_dwordx4 v225, s[8:11], s43 offen offset:0 sc1 lds\n"
"v_mfma_f32_16x16x32_bf16 a[140:143], a[128:131], v[72:75], a[140:143]\n"
"v_exp_f32 v24, v24\n"
"v_mfma_f32_16x16x32_bf16 a[144:147], a[128:131], v[76:79], a[144:147]\n"
"s_waitcnt lgkmcnt(3)\n"
"ds_read_b128 a[84:87], v227 offset:21504\n"
"v_cvt_pk_bf16_f32 v17, v18, v19\n"
"v_mfma_f32_16x16x32_bf16 v[144:147], a[64:67], v[0:3], v[144:147]\n"
"v_exp_f32 v25, v25\n"
"v_mfma_f32_16x16x32_bf16 v[176:179], a[64:67], v[8:11], v[176:179]\n"
"ds_read_b128 a[88:91], v227 offset:22528\n"
"ds_read_b128 a[92:95], v227 offset:23552\n"
"v_mfma_f32_16x16x32_bf16 v[148:151], a[68:71], v[0:3], v[148:151]\n"
"s_add_u32 s51, s59, 16384\n"
"v_exp_f32 v26, v26\n"
"v_mfma_f32_16x16x32_bf16 v[180:183], a[68:71], v[8:11], v[180:183]\n"
"s_waitcnt lgkmcnt(3)\n"
"v_exp_f32 v27, v27\n"
"v_cvt_pk_bf16_f32 v24, v24, v25\n"
"v_mfma_f32_16x16x32_bf16 v[152:155], a[72:75], v[0:3], v[152:155]\n"
"v_exp_f32 v20, v20\n"
"v_mfma_f32_16x16x32_bf16 v[184:187], a[72:75], v[8:11], v[184:187]\n"
"buffer_load_dwordx4 v225, s[8:11], s43 offen offset:1024 sc1 lds\n"
"v_accvgpr_read_b32 v252, a136\n"
"v_mfma_f32_16x16x32_bf16 v[156:159], a[76:79], v[0:3], v[156:159]\n"
"v_exp_f32 v21, v21\n"
"v_subrev_u32 v253, 0x2b800000, v252\n"
"v_mfma_f32_16x16x32_bf16 v[188:191], a[76:79], v[8:11], v[188:191]\n"
"v_cmp_le_u32 vcc, 0x34000000, v253\n"
"s_cmp_lg_u64 vcc, 0\n"
"s_cbranch_scc1 L1029_%=\n"
"L1028_%=:\n"
"v_mfma_f32_16x16x32_bf16 v[160:163], a[80:83], v[0:3], v[160:163]\n"
"v_rcp_f32 v252, v252\n"
"v_cvt_pk_bf16_f32 v25, v26, v27\n"
"v_mfma_f32_16x16x32_bf16 v[192:195], a[80:83], v[8:11], v[192:195]\n"
"s_waitcnt lgkmcnt(0)\n"
"ds_read_b128 a[96:99], v227 offset:24576\n"
"ds_read_b128 a[100:103], v227 offset:25600\n"
"v_mul_f32 v112, v252, v112\n"
"v_mfma_f32_16x16x32_bf16 v[164:167], a[84:87], v[0:3], v[164:167]\n"
"v_exp_f32 v22, v22\n"
"v_mul_f32 v113, v252, v113\n"
"v_mfma_f32_16x16x32_bf16 v[196:199], a[84:87], v[8:11], v[196:199]\n"
"ds_read_b128 a[104:107], v227 offset:26624\n"
"ds_read_b128 a[108:111], v227 offset:27648\n"
"v_cvt_pk_bf16_f32 v18, v20, v21\n"
"v_mfma_f32_16x16x32_bf16 v[168:171], a[88:91], v[0:3], v[168:171]\n"
"v_exp_f32 v23, v23\n"
"v_mul_f32 v114, v252, v114\n"
"v_mfma_f32_16x16x32_bf16 v[200:203], a[88:91], v[8:11], v[200:203]\n"
"v_exp_f32 v28, v28\n"
"v_mul_f32 v115, v252, v115\n"
"v_mfma_f32_16x16x32_bf16 v[172:175], a[92:95], v[0:3], v[172:175]\n"
"buffer_load_dwordx4 v225, s[8:11], s43 offen offset:2048 sc1 lds\n"
"v_mul_f32 v116, v252, v116\n"
"v_mfma_f32_16x16x32_bf16 v[204:207], a[92:95], v[8:11], v[204:207]\n"
"v_cvt_pk_bf16_f32 v19, v22, v23\n"
"v_mul_f32 v117, v252, v117\n"
"v_mul_f32 v118, v252, v118\n"
"v_mfma_f32_16x16x32_bf16 a[140:143], a[128:131], v[0:3], a[140:143]\n"
"v_exp_f32 v29, v29\n"
"v_mul_f32 v119, v252, v119\n"
"v_mfma_f32_16x16x32_bf16 a[144:147], a[128:131], v[8:11], a[144:147]\n"
"s_waitcnt lgkmcnt(2)\n"
"v_exp_f32 v30, v30\n"
"v_cvt_pk_bf16_f32 v26, v28, v29\n"
"v_mfma_f32_16x16x32_bf16 v[144:147], a[96:99], v[16:19], v[144:147]\n"
"v_exp_f32 v31, v31\n"
"v_cvt_pk_bf16_f32 v112, v112, v113\n"
"v_cvt_pk_bf16_f32 v27, v30, v31\n"
"s_nop 1\n"
"v_mfma_f32_16x16x32_bf16 v[176:179], a[96:99], v[24:27], v[176:179]\n"
"ds_read_b128 a[112:115], v227 offset:28672\n"
"ds_read_b128 a[116:119], v227 offset:29696\n"
"v_cvt_pk_bf16_f32 v113, v114, v115\n"
"v_mfma_f32_16x16x32_bf16 v[148:151], a[100:103], v[16:19], v[148:151]\n"
"ds_read_b128 a[120:123], v227 offset:30720\n"
"v_cvt_pk_bf16_f32 v114, v116, v117\n"
"v_cvt_pk_bf16_f32 v115, v118, v119\n"
"v_mfma_f32_16x16x32_bf16 v[180:183], a[100:103], v[24:27], v[180:183]\n"
"s_waitcnt lgkmcnt(3)\n"
"buffer_store_dwordx4 v[112:115], v224, s[8:11], s51 offen offset:0 sc1\n"
"v_mul_f32 v120, v252, v120\n"
"v_mfma_f32_16x16x32_bf16 v[152:155], a[104:107], v[16:19], v[152:155]\n"
"s_add_u32 s41, s41, 32768\n"
"ds_read_b128 a[124:127], v227 offset:31744\n"
"v_mul_f32 v121, v252, v121\n"
"v_mul_f32 v122, v252, v122\n"
"v_mfma_f32_16x16x32_bf16 v[184:187], a[104:107], v[24:27], v[184:187]\n"
"s_cmp_eq_u32 s41, 163840\n"
"v_exp_f32 v40, v40\n"
"v_mul_f32 v123, v252, v123\n"
"v_mfma_f32_16x16x32_bf16 v[156:159], a[108:111], v[16:19], v[156:159]\n"
"s_cselect_b32 s41, 0, s41\n"
"buffer_load_dwordx4 v225, s[8:11], s43 offen offset:3072 sc1 lds\n"
"v_mul_f32 v124, v252, v124\n"
"v_mfma_f32_16x16x32_bf16 v[188:191], a[108:111], v[24:27], v[188:191]\n"
"s_waitcnt lgkmcnt(3)\n"
"s_waitcnt vmcnt(17)\n"
"s_barrier\n"
"v_mfma_f32_16x16x32_bf16 v[160:163], a[112:115], v[16:19], v[160:163]\n"
"s_add_u32 m0, s40, 16384\n"
"ds_read_b128 a[64:67], v226 offset:0\n"
"v_add_u32 v227, s41, v224\n"
"v_mul_f32 v125, v252, v125\n"
"v_mfma_f32_16x16x32_bf16 v[192:195], a[112:115], v[24:27], v[192:195]\n"
"s_waitcnt lgkmcnt(2)\n"
"ds_read_b128 a[68:71], v226 offset:1024\n"
"v_mul_f32 v126, v252, v126\n"
"v_mul_f32 v127, v252, v127\n"
"v_mfma_f32_16x16x32_bf16 v[164:167], a[116:119], v[16:19], v[164:167]\n"
"ds_read_b128 a[72:75], v226 offset:2048\n"
"v_cvt_pk_bf16_f32 v120, v120, v121\n"
"v_cvt_pk_bf16_f32 v121, v122, v123\n"
"v_mfma_f32_16x16x32_bf16 v[196:199], a[116:119], v[24:27], v[196:199]\n"
"s_add_u32 s40, s40, 32768\n"
"ds_read_b128 a[76:79], v226 offset:3072\n"
"v_cvt_pk_bf16_f32 v122, v124, v125\n"
"v_cvt_pk_bf16_f32 v123, v126, v127\n"
"v_mfma_f32_16x16x32_bf16 v[168:171], a[120:123], v[16:19], v[168:171]\n"
"s_cmp_eq_u32 s40, s46\n"
"buffer_store_dwordx4 v[120:123], v224, s[8:11], s51 offen offset:1024 sc1\n"
"v_mul_f32 v128, v252, v128\n"
"v_mfma_f32_16x16x32_bf16 v[200:203], a[120:123], v[24:27], v[200:203]\n"
"s_waitcnt lgkmcnt(4)\n"
"ds_read_b128 a[80:83], v226 offset:4096\n"
"v_mul_f32 v129, v252, v129\n"
"v_mul_f32 v130, v252, v130\n"
"v_mfma_f32_16x16x32_bf16 v[172:175], a[124:127], v[16:19], v[172:175]\n"
"s_cselect_b32 s40, s47, s40\n"
"ds_read_b128 a[84:87], v226 offset:5120\n"
"v_mul_f32 v131, v252, v131\n"
"v_mul_f32 v132, v252, v132\n"
"v_mfma_f32_16x16x32_bf16 v[204:207], a[124:127], v[24:27], v[204:207]\n"
"buffer_load_dwordx4 v225, s[8:11], s44 offen offset:0 sc1 lds\n"
"v_mul_f32 v133, v252, v133\n"
"v_mfma_f32_16x16x32_bf16 a[140:143], a[128:131], v[16:19], a[140:143]\n"
"ds_read_b128 a[88:91], v226 offset:6144\n"
"v_mul_f32 v134, v252, v134\n"
"v_mul_f32 v135, v252, v135\n"
"v_mfma_f32_16x16x32_bf16 a[144:147], a[128:131], v[24:27], a[144:147]\n"
"ds_read_b128 a[92:95], v226 offset:7168\n"
"v_cvt_pk_bf16_f32 v128, v128, v129\n"
"v_cvt_pk_bf16_f32 v129, v130, v131\n"
"v_exp_f32 v41, v41\n"
"s_nop 0\n"
"v_cvt_pk_bf16_f32 v68, v40, v41\n"
"v_exp_f32 v42, v42\n"
"v_exp_f32 v43, v43\n"
"s_nop 0\n"
"v_cvt_pk_bf16_f32 v69, v42, v43\n"
"v_exp_f32 v44, v44\n"
"v_exp_f32 v45, v45\n"
"s_nop 0\n"
"v_cvt_pk_bf16_f32 v70, v44, v45\n"
"v_exp_f32 v46, v46\n"
"v_exp_f32 v47, v47\n"
"s_nop 0\n"
"v_cvt_pk_bf16_f32 v71, v46, v47\n"
"s_waitcnt lgkmcnt(7)\n"
"s_nop 0\n"
"v_mfma_f32_16x16x32_bf16 v[176:179], a[64:67], v[68:71], v[176:179]\n"
"s_waitcnt lgkmcnt(4)\n"
"ds_read_b128 a[96:99], v226 offset:8192\n"
"ds_read_b128 a[100:103], v226 offset:9216\n"
"v_cvt_pk_bf16_f32 v130, v132, v133\n"
"v_mfma_f32_16x16x32_bf16 v[180:183], a[68:71], v[68:71], v[180:183]\n"
"ds_read_b128 a[104:107], v226 offset:10240\n"
"v_cvt_pk_bf16_f32 v131, v134, v135\n"
"v_mul_f32 v136, v252, v136\n"
"v_mfma_f32_16x16x32_bf16 v[184:187], a[72:75], v[68:71], v[184:187]\n"
"buffer_store_dwordx4 v[128:131], v224, s[8:11], s51 offen offset:2048 sc1\n"
"v_mul_f32 v137, v252, v137\n"
"v_mfma_f32_16x16x32_bf16 v[188:191], a[76:79], v[68:71], v[188:191]\n"
"s_waitcnt lgkmcnt(5)\n"
"ds_read_b128 a[108:111], v226 offset:11264\n"
"v_mul_f32 v138, v252, v138\n"
"v_mul_f32 v139, v252, v139\n"
"v_mfma_f32_16x16x32_bf16 v[192:195], a[80:83], v[68:71], v[192:195]\n"
"ds_read_b128 a[112:115], v226 offset:12288\n"
"v_mul_f32 v140, v252, v140\n"
"v_mul_f32 v141, v252, v141\n"
"v_mfma_f32_16x16x32_bf16 v[196:199], a[84:87], v[68:71], v[196:199]\n"
"s_waitcnt lgkmcnt(6)\n"
"buffer_load_dwordx4 v225, s[8:11], s44 offen offset:1024 sc1 lds\n"
"v_mul_f32 v142, v252, v142\n"
"v_mfma_f32_16x16x32_bf16 v[200:203], a[88:91], v[68:71], v[200:203]\n"
"s_waitcnt lgkmcnt(5)\n"
"ds_read_b128 a[116:119], v226 offset:13312\n"
"v_mul_f32 v143, v252, v143\n"
"v_cvt_pk_bf16_f32 v136, v136, v137\n"
"v_mfma_f32_16x16x32_bf16 v[204:207], a[92:95], v[68:71], v[204:207]\n"
"ds_read_b128 a[120:123], v226 offset:14336\n"
"v_cvt_pk_bf16_f32 v137, v138, v139\n"
"v_cvt_pk_bf16_f32 v138, v140, v141\n"
"v_mfma_f32_16x16x32_bf16 a[144:147], a[128:131], v[68:71], a[144:147]\n"
"ds_read_b128 a[124:127], v226 offset:15360\n"
"v_cvt_pk_bf16_f32 v139, v142, v143\n"
"v_exp_f32 v56, v56\n"
"v_exp_f32 v57, v57\n"
"s_nop 0\n"
"v_cvt_pk_bf16_f32 v76, v56, v57\n"
"v_exp_f32 v58, v58\n"
"v_exp_f32 v59, v59\n"
"s_nop 0\n"
"v_cvt_pk_bf16_f32 v77, v58, v59\n"
"v_exp_f32 v60, v60\n"
"v_exp_f32 v61, v61\n"
"s_nop 0\n"
"v_cvt_pk_bf16_f32 v78, v60, v61\n"
"v_exp_f32 v62, v62\n"
"v_exp_f32 v63, v63\n"
"s_nop 0\n"
"v_cvt_pk_bf16_f32 v79, v62, v63\n"
"s_waitcnt lgkmcnt(7)\n"
"s_nop 0\n"
"v_mfma_f32_16x16x32_bf16 v[176:179], a[96:99], v[76:79], v[176:179]\n"
"s_waitcnt lgkmcnt(5)\n"
"buffer_store_dwordx4 v[136:139], v224, s[8:11], s51 offen offset:3072 sc1\n"
"v_mfma_f32_16x16x32_bf16 v[180:183], a[100:103], v[76:79], v[180:183]\n"
"s_waitcnt vmcnt(10)\n"
"s_barrier\n"
"v_mfma_f32_16x16x32_bf16 v[184:187], a[104:107], v[76:79], v[184:187]\n"
"s_waitcnt lgkmcnt(3)\n"
"ds_read_b128 a[64:67], v227 offset:0\n"
"v_mfma_f32_16x16x32_bf16 v[188:191], a[108:111], v[76:79], v[188:191]\n"
"ds_read_b128 a[68:71], v227 offset:1024\n"
"v_mfma_f32_16x16x32_bf16 v[192:195], a[112:115], v[76:79], v[192:195]\n"
"s_waitcnt lgkmcnt(4)\n"
"buffer_load_dwordx4 v225, s[8:11], s44 offen offset:2048 sc1 lds\n"
"v_mfma_f32_16x16x32_bf16 v[196:199], a[116:119], v[76:79], v[196:199]\n"
"s_waitcnt lgkmcnt(3)\n"
"ds_read_b128 a[72:75], v227 offset:2048\n"
"v_mfma_f32_16x16x32_bf16 v[200:203], a[120:123], v[76:79], v[200:203]\n"
"s_waitcnt lgkmcnt(3)\n"
"ds_read_b128 a[76:79], v227 offset:3072\n"
"v_mfma_f32_16x16x32_bf16 v[204:207], a[124:127], v[76:79], v[204:207]\n"
"ds_read_b128 a[80:83], v227 offset:4096\n"
"v_mfma_f32_16x16x32_bf16 a[144:147], a[128:131], v[76:79], a[144:147]\n"
"ds_read_b128 a[84:87], v227 offset:5120\n"
"buffer_load_dwordx4 v225, s[8:11], s44 offen offset:3072 sc1 lds\n"
"s_branch LTL_%=\n"
"LEV_%=:\n"
"s_mov_b32 s56, 0\n"
"s_cmp_eq_u32 s57, 1\n"
"s_cbranch_scc0 L1061_%=\n"
"s_mov_b32 s48, 4352\n"
"buffer_load_dwordx4 v[240:243], v224, s[8:11], s48 offen offset:0 sc1\n"
"buffer_load_dwordx4 v[244:247], v224, s[8:11], s48 offen offset:1024 sc1\n"
"s_branch L1064_%=\n"
"L1061_%=:\n"
"s_cmp_eq_u32 s57, 2\n"
"s_cbranch_scc0 L1062_%=\n"
"s_mov_b32 s48, 4352\n"
"s_waitcnt vmcnt(8)\n"
"v_cmp_eq_u64 vcc, s[28:29], v[240:241]\n"
"v_cmp_eq_u64_e64 s[52:53], s[28:29], v[242:243]\n"
"s_and_b64 vcc, vcc, s[52:53]\n"
"v_cmp_eq_u64_e64 s[52:53], s[28:29], v[244:245]\n"
"s_and_b64 vcc, vcc, s[52:53]\n"
"v_cmp_eq_u64_e64 s[52:53], s[28:29], v[246:247]\n"
"s_and_b64 vcc, vcc, s[52:53]\n"
"s_cmp_eq_u64 vcc, exec\n"
"s_cbranch_scc0 L1066_%=\n"
"L1067_%=:\n"
"s_cmp_lt_u32 s58, 40\n"
"s_cbranch_scc0 L1065_%=\n"
"s_mul_i32 s48, s30, 14\n"
"s_cmp_eq_u32 s30, 3\n"
"s_subb_u32 s48, s48, 0\n"
"s_mul_i32 s49, s58, 54\n"
"s_add_u32 s49, s49, s48\n"
"s_lshl_b32 s49, s49, 10\n"
"s_add_u32 s49, s49, 2744320\n"
"s_lshl_b32 s48, s48, 9\n"
"s_add_u32 s48, s48, 2695168\n"
"s_cmp_lt_u32 s30, 2\n"
"s_cselect_b32 s50, 0, 2130706432\n"
"v_lshrrev_b32 v235, 1, v224\n"
"v_and_b32 v235, 0x180, v235\n"
"v_and_b32 v239, 0x70, v224\n"
"v_add_u32 v235, v235, v239\n"
"buffer_load_dwordx4 a[148:151], v224, s[8:11], s49 offen offset:0 sc1\n"
"buffer_load_dwordx4 a[204:207], v235, s[8:11], s48 offen offset:0 sc1\n"
"buffer_load_dwordx4 a[152:155], v224, s[8:11], s49 offen offset:1024 sc1\n"
"buffer_load_dwordx4 a[208:211], v235, s[8:11], s48 offen offset:512 sc1\n"
"buffer_load_dwordx4 a[156:159], v224, s[8:11], s49 offen offset:2048 sc1\n"
"buffer_load_dwordx4 a[212:215], v235, s[8:11], s48 offen offset:1024 sc1\n"
"buffer_load_dwordx4 a[160:163], v224, s[8:11], s49 offen offset:3072 sc1\n"
"buffer_load_dwordx4 a[216:219], v235, s[8:11], s48 offen offset:1536 sc1\n"
"s_add_u32 s49, s49, 4096\n"
"buffer_load_dwordx4 a[164:167], v224, s[8:11], s49 offen offset:0 sc1\n"
"buffer_load_dwordx4 a[220:223], v235, s[8:11], s48 offen offset:2048 sc1\n"
"buffer_load_dwordx4 a[168:171], v224, s[8:11], s49 offen offset:1024 sc1\n"
"buffer_load_dwordx4 a[224:227], v235, s[8:11], s48 offen offset:2560 sc1\n"
"buffer_load_dwordx4 a[172:175], v224, s[8:11], s49 offen offset:2048 sc1\n"
"buffer_load_dwordx4 a[228:231], v235, s[8:11], s48 offen offset:3072 sc1\n"
"buffer_load_dwordx4 a[176:179], v224, s[8:11], s49 offen offset:3072 sc1\n"
"buffer_load_dwordx4 a[232:235], v235, s[8:11], s48 offen offset:3584 sc1\n"
"s_add_u32 s49, s49, 4096\n"
"s_add_u32 s48, s48, 4096\n"
"buffer_load_dwordx4 a[180:183], v224, s[8:11], s49 offen offset:0 sc1\n"
"buffer_load_dwordx4 a[236:239], v235, s[8:11], s48 offen offset:0 sc1\n"
"buffer_load_dwordx4 a[184:187], v224, s[8:11], s49 offen offset:1024 sc1\n"
"buffer_load_dwordx4 a[240:243], v235, s[8:11], s48 offen offset:512 sc1\n"
"buffer_load_dwordx4 a[188:191], v224, s[8:11], s49 offen offset:2048 sc1\n"
"buffer_load_dwordx4 a[244:247], v235, s[8:11], s48 offen offset:1024 sc1\n"
"buffer_load_dwordx4 a[192:195], v224, s[8:11], s49 offen offset:3072 sc1\n"
"buffer_load_dwordx4 a[248:251], v235, s[8:11], s48 offen offset:1536 sc1\n"
"s_add_u32 s49, s49, 4096\n"
"buffer_load_dwordx4 a[196:199], v224, s[8:11], s49 offen offset:0 sc1\n"
"buffer_load_dwordx4 a[252:255], v235, s[8:11], s48 offen offset:2048 sc1\n"
"s_add_u32 s48, s48, s50\n"
"s_add_u32 s49, s49, s50\n"
"buffer_load_dwordx4 a[200:203], v224, s[8:11], s49 offen offset:1024 sc1\n"
"buffer_load_dwordx4 v[244:247], v235, s[8:11], s48 offen offset:2560 sc1\n"
"v_lshrrev_b32 v235, 9, v224\n"
"v_lshlrev_b32 v235, 8, v235\n"
"v_bfe_u32 v238, v224, 8, 1\n"
"v_lshl_add_u32 v235, v238, 3, v235\n"
"s_lshl_b32 s48, s58, 9\n"
"v_add_u32 v235, s48, v235\n"
"v_add_u32 v239, v239, v235\n"
"v_and_b32 v238, 0x80, v224\n"
"v_lshl_add_u32 v239, v238, 23, v239\n"
"v_not_b32 v238, v234\n"
"v_and_b32 v240, 15, v238\n"
"v_lshl_add_u32 v235, v240, 4, v235\n"
"v_bfe_u32 v238, v238, 4, 7\n"
"v_mul_u32_u24 v238, 20480, v238\n"
"v_add_u32 v238, v238, v235\n"
"s_mov_b32 s48, 65536\n"
"buffer_load_dwordx2 v[236:237], v238, s[8:11], s48 offen sc1\n"
"s_branch L1064_%=\n"
"L1062_%=:\n"
"s_cmp_eq_u32 s57, 3\n"
"s_cbranch_scc0 L1063_%=\n"
"s_waitcnt vmcnt(8)\n"
"v_mfma_f32_16x16x32_bf16 v[240:243], a[148:151], a[204:207], 0\n"
"v_mfma_f32_16x16x32_bf16 v[240:243], a[152:155], a[208:211], v[240:243]\n"
"v_mfma_f32_16x16x32_bf16 v[240:243], a[156:159], a[212:215], v[240:243]\n"
"v_mfma_f32_16x16x32_bf16 v[240:243], a[160:163], a[216:219], v[240:243]\n"
"v_mfma_f32_16x16x32_bf16 v[240:243], a[164:167], a[220:223], v[240:243]\n"
"v_mfma_f32_16x16x32_bf16 v[240:243], a[168:171], a[224:227], v[240:243]\n"
"v_mfma_f32_16x16x32_bf16 v[240:243], a[172:175], a[228:231], v[240:243]\n"
"v_mfma_f32_16x16x32_bf16 v[240:243], a[176:179], a[232:235], v[240:243]\n"
"v_mfma_f32_16x16x32_bf16 v[240:243], a[180:183], a[236:239], v[240:243]\n"
"v_mfma_f32_16x16x32_bf16 v[240:243], a[184:187], a[240:243], v[240:243]\n"
"v_mfma_f32_16x16x32_bf16 v[240:243], a[188:191], a[244:247], v[240:243]\n"
"v_mfma_f32_16x16x32_bf16 v[240:243], a[192:195], a[248:251], v[240:243]\n"
"v_mfma_f32_16x16x32_bf16 v[240:243], a[196:199], a[252:255], v[240:243]\n"
"v_mfma_f32_16x16x32_bf16 v[240:243], a[200:203], v[244:247], v[240:243]\n"
"s_sub_u32 s48, s41, 32768\n"
"s_cselect_b32 s48, 131072, s48\n"
"v_add_u32 v235, s48, v224\n"
"s_lshl_b32 s49, s30, 10\n"
"v_add_u32 v238, s49, v235\n"
"s_barrier\n"
"s_nop 3\n"
"ds_write_b128 v238, v[240:243]\n"
"s_waitcnt lgkmcnt(0)\n"
"s_barrier\n"
"ds_read_b128 v[240:243], v235 offset:0\n"
"ds_read_b128 v[244:247], v235 offset:1024\n"
"ds_read_b128 v[248:251], v235 offset:2048\n"
"ds_read_b128 v[252:255], v235 offset:3072\n"
"v_ashrrev_i32 v235, 31, v234\n"
"v_not_b32 v235, v235\n"
"v_or_b32 v235, 0x80000000, v235\n"
"v_xor_b32 v235, v235, v234\n"
"v_and_b32 v235, 0xffff0000, v235\n"
"v_mul_f32 v235, 0xbfb8aa3b, v235\n"
"v_exp_f32 v235, v235\n"
"s_nop 0\n"
"v_add_f32 v235, 1.0, v235\n"
"v_rcp_f32 v235, v235\n"
"s_nop 0\n"
"v_cvt_pk_bf16_f32 v235, v235, v235\n"
"v_lshlrev_b32 v235, 16, v235\n"
"s_waitcnt lgkmcnt(0)\n"
"v_add_f32 v240, v240, v244\n"
"v_add_f32 v248, v248, v252\n"
"v_add_f32 v241, v241, v245\n"
"v_add_f32 v249, v249, v253\n"
"v_add_f32 v242, v242, v246\n"
"v_add_f32 v250, v250, v254\n"
"v_add_f32 v243, v243, v247\n"
"v_add_f32 v251, v251, v255\n"
"v_add_f32 v240, v240, v248\n"
"v_add_f32 v241, v241, v249\n"
"v_add_f32 v242, v242, v250\n"
"v_add_f32 v243, v243, v251\n"
"v_cvt_pk_bf16_f32 v240, v240, v240\n"
"v_lshlrev_b32 v240, 16, v240\n"
"v_mul_f32 v240, v240, v235\n"
"v_cvt_pk_bf16_f32 v240, v240, v240\n"
"v_lshlrev_b32 v240, 16, v240\n"
"v_cvt_pk_bf16_f32 v241, v241, v241\n"
"v_lshlrev_b32 v241, 16, v241\n"
"v_mul_f32 v241, v241, v235\n"
"v_cvt_pk_bf16_f32 v241, v241, v241\n"
"v_lshlrev_b32 v241, 16, v241\n"
"v_cvt_pk_bf16_f32 v242, v242, v242\n"
"v_lshlrev_b32 v242, 16, v242\n"
"v_mul_f32 v242, v242, v235\n"
"v_cvt_pk_bf16_f32 v242, v242, v242\n"
"v_lshlrev_b32 v242, 16, v242\n"
"v_cvt_pk_bf16_f32 v243, v243, v243\n"
"v_lshlrev_b32 v243, 16, v243\n"
"v_mul_f32 v243, v243, v235\n"
"v_cvt_pk_bf16_f32 v243, v243, v243\n"
"v_lshlrev_b32 v243, 16, v243\n"
"v_lshlrev_b32 v244, 16, v236\n"
"v_and_b32 v245, 0xffff0000, v236\n"
"v_lshlrev_b32 v246, 16, v237\n"
"v_and_b32 v247, 0xffff0000, v237\n"
"v_add_f32 v240, v240, v244\n"
"v_add_f32 v241, v241, v245\n"
"v_add_f32 v242, v242, v246\n"
"v_add_f32 v243, v243, v247\n"
"v_cvt_pk_bf16_f32 v240, v240, v241\n"
"v_cvt_pk_bf16_f32 v241, v242, v243\n"
"s_add_u32 s48, s45, 2722816\n"
"buffer_store_dwordx2 v[240:241], v239, s[8:11], s48 offen sc1\n"
"s_branch L1064_%=\n"
"L1063_%=:\n"
"s_lshl_b32 s48, s58, 3\n"
"s_add_u32 s48, s48, s45\n"
"s_add_u32 s48, s48, 6400\n"
"s_waitcnt vmcnt(8)\n"
"buffer_store_dwordx2 v[232:233], v230, s[8:11], s48 offen sc1\n"
"L1065_%=:\n"
"s_lshl_b32 s56, s34, 1\n"
"s_sub_u32 s56, s56, s57\n"
"s_sub_u32 s56, s56, 3\n"
"s_mov_b32 s57, -1\n"
"L1064_%=:\n"
"s_add_u32 s57, s57, 1\n"
"s_branch LP1_%=\n"
"LEND_%=:\n"
      : : [ka] "s"(__builtin_amdgcn_kernarg_segment_ptr()), [b] "s"(blockIdx.x), [t] "v"(threadIdx.x)
      : "v1", "v2", "v3", "v4", "v5", "v6", "v7", "v8", "v9", "v10", "v11", "v12", "v13", "v14", "v15", "v16", "v17", "v18", "v19", "v20", "v21", "v22", "v23", "v24", "v25", "v26", "v27", "v28", "v29", "v30", "v31", "v32", "v33", "v34", "v35", "v36", "v37", "v38", "v39", "v40", "v41", "v42", "v43", "v44", "v45", "v46", "v47", "v48", "v49", "v50", "v51", "v52", "v53", "v54", "v55", "v56", "v57", "v58", "v59", "v60", "v61", "v62", "v63", "v64", "v65", "v66", "v67", "v68", "v69", "v70", "v71", "v72", "v73", "v74", "v75", "v76", "v77", "v78", "v79", "v80", "v81", "v82", "v83", "v84", "v85", "v86", "v87", "v88", "v89", "v90", "v91", "v92", "v93", "v94", "v95", "v96", "v97", "v98", "v99", "v100", "v101", "v102", "v103", "v104", "v105", "v106", "v107", "v108", "v109", "v110", "v111", "v112", "v113", "v114", "v115", "v116", "v117", "v118", "v119", "v120", "v121", "v122", "v123", "v124", "v125", "v126", "v127", "v128", "v129", "v130", "v131", "v132", "v133", "v134", "v135", "v136", "v137", "v138", "v139", "v140", "v141", "v142", "v143", "v144", "v145", "v146", "v147", "v148", "v149", "v150", "v151", "v152", "v153", "v154", "v155", "v156", "v157", "v158", "v159", "v160", "v161", "v162", "v163", "v164", "v165", "v166", "v167", "v168", "v169", "v170", "v171", "v172", "v173", "v174", "v175", "v176", "v177", "v178", "v179", "v180", "v181", "v182", "v183", "v184", "v185", "v186", "v187", "v188", "v189", "v190", "v191", "v192", "v193", "v194", "v195", "v196", "v197", "v198", "v199", "v200", "v201", "v202", "v203", "v204", "v205", "v206", "v207", "v208", "v209", "v210", "v211", "v212", "v213", "v214", "v215", "v216", "v217", "v218", "v219", "v220", "v221", "v222", "v223", "v224", "v225", "v226", "v227", "v228", "v229", "v230", "v231", "v232", "v233", "v234", "v235", "v236", "v237", "v238", "v239", "v240", "v241", "v242", "v243", "v244", "v245", "v246", "v247", "v248", "v249", "v250", "v251", "v252", "v253", "v254", "v255", "a0", "a1", "a2", "a3", "a4", "a5", "a6", "a7", "a8", "a9", "a10", "a11", "a12", "a13", "a14", "a15", "a16", "a17", "a18", "a19", "a20", "a21", "a22", "a23", "a24", "a25", "a26", "a27", "a28", "a29", "a30", "a31", "a32", "a33", "a34", "a35", "a36", "a37", "a38", "a39", "a40", "a41", "a42", "a43", "a44", "a45", "a46", "a47", "a48", "a49", "a50", "a51", "a52", "a53", "a54", "a55", "a56", "a57", "a58", "a59", "a60", "a61", "a62", "a63", "a64", "a65", "a66", "a67", "a68", "a69", "a70", "a71", "a72", "a73", "a74", "a75", "a76", "a77", "a78", "a79", "a80", "a81", "a82", "a83", "a84", "a85", "a86", "a87", "a88", "a89", "a90", "a91", "a92", "a93", "a94", "a95", "a96", "a97", "a98", "a99", "a100", "a101", "a102", "a103", "a104", "a105", "a106", "a107", "a108", "a109", "a110", "a111", "a112", "a113", "a114", "a115", "a116", "a117", "a118", "a119", "a120", "a121", "a122", "a123", "a124", "a125", "a126", "a127", "a128", "a129", "a130", "a131", "a132", "a133", "a134", "a135", "a136", "a137", "a138", "a139", "a140", "a141", "a142", "a143", "a144", "a145", "a146", "a147", "a148", "a149", "a150", "a151", "a152", "a153", "a154", "a155", "a156", "a157", "a158", "a159", "a160", "a161", "a162", "a163", "a164", "a165", "a166", "a167", "a168", "a169", "a170", "a171", "a172", "a173", "a174", "a175", "a176", "a177", "a178", "a179", "a180", "a181", "a182", "a183", "a184", "a185", "a186", "a187", "a188", "a189", "a190", "a191", "a192", "a193", "a194", "a195", "a196", "a197", "a198", "a199", "a200", "a201", "a202", "a203", "a204", "a205", "a206", "a207", "a208", "a209", "a210", "a211", "a212", "a213", "a214", "a215", "a216", "a217", "a218", "a219", "a220", "a221", "a222", "a223", "a224", "a225", "a226", "a227", "a228", "a229", "a230", "a231", "a232", "a233", "a234", "a235", "a236", "a237", "a238", "a239", "a240", "a241", "a242", "a243", "a244", "a245", "a246", "a247", "a248", "a249", "a250", "a251", "a252", "a253", "a254", "a255", "s8", "s9", "s10", "s11", "s12", "s13", "s14", "s15", "s16", "s17", "s18", "s19", "s20", "s21", "s22", "s23", "s24", "s25", "s26", "s27", "s28", "s29", "s30", "s31", "s32", "s33", "s34", "s35", "s36", "s37", "s38", "s39", "s40", "s41", "s42", "s43", "s44", "s45", "s46", "s47", "s48", "s49", "s50", "s51", "s52", "s53", "s54", "s55", "s56", "s57", "s58", "s59", "s60", "s61", "s62", "s63", "s64", "s65", "s66", "s67", "s68", "s69", "s70", "s71", "s72", "s73", "s74", "s75", "s76", "s77", "s78", "s79", "s80", "s81", "s82", "s83", "s84", "s85", "s86", "s87", "s88", "s89", "s90", "s91", "s92", "s93", "s94", "s95", "s96", "s97", "s98", "s99", "scc", "vcc", "memory");
}

extern "C" void kernel_launch(void* const* d_in, const int* in_sizes, int n_in, void* d_out, int out_size, void* d_ws, size_t ws_size, hipStream_t stream) {
  (void)hipFuncSetAttribute((const void*)mega_kernel, hipFuncAttributeMaxDynamicSharedMemorySize, 163840);
  mega_kernel<<<256, 256, 163840, stream>>>((const bf16*)d_in[0], (const bf16*)d_in[1], (const bf16*)d_in[2], (const bf16*)d_in[3], (const bf16*)d_in[4], (const bf16*)d_in[5],
      (const bf16*)d_in[7], (const bf16*)d_in[8], (const bf16*)d_in[9], (const bf16*)d_in[10], (char*)d_ws + (size_t)0 * 1048576, (bf16*)d_out);
}
```

```cpp
#include <hip/hip_runtime.h>
#include <hip/hip_bf16.h>
typedef __hip_bfloat16 bf16;
#define MF "v_mfma_f32_16x16x32_bf16 "
#define RD "ds_read_b128 "
#define RT "ds_read_b64_tr_b16 "
#define BL "buffer_load_dwordx4 "
#define BS "buffer_store_dwordx4 "
#define PK "v_cvt_pk_bf16_f32 "
#define EX "v_exp_f32 "
#define WT "s_waitcnt "
#define MU "v_mul_f32 "
#define SA "s_add_u32 "

__global__ __launch_bounds__(256, 1) void mega_kernel(const bf16* x, const bf16* ckv, const bf16* wq, const bf16* wuk, const bf16* wuv, const bf16* wo, const bf16* w_r,
                                                      const bf16* wg, const bf16* wu, const bf16* wd, char* ws, bf16* out) {
  asm volatile(
"s_load_dwordx16 s[64:79],%[ka],0\n"
"s_load_dwordx8 s[80:87],%[ka],64\n"
"s_mov_b32 s31,%[b]\n"
"v_lshrrev_b32 v1,6,%[t]\n"
"v_and_b32 v2,63,%[t]\n"
"v_readfirstlane_b32 s30,v1\n"
"v_lshlrev_b32 v224,4,v2\n"
"s_and_b32 s32,s31,7\n"
"s_lshr_b32 s33,s31,6\n"
"s_bfe_u32 s34,s31,0x30003\n"
"s_lshl_b32 s35,s32,2\n"
SA"s35,s35,s33\n"
SA"s46,s34,s32\n"
"s_and_b32 s46,s46,6\n"
"s_lshl_b32 s47,s46,19\n"
"s_lshl_b32 s46,s46,7\n"
"s_lshl_b32 s36,s30,12\n"
"s_mul_i32 s37,s30,24576\n"
SA"s37,s37,65536\n"
"v_lshrrev_b32 v3,3,v2\n"
"v_lshrrev_b32 v4,4,v2\n"
"v_and_b32 v5,7,v2\n"
"v_xor_b32 v6,v5,v4\n"
"v_lshlrev_b32 v6,4,v6\n"
"v_lshl_add_u32 v6,v3,10,v6\n"
"s_lshl_b32 s50,s30,15\n"
"v_add_u32 v234,s50,v6\n"
"v_xor_b32 v235,64,v234\n"
"v_add_u32 v235,7168,v235\n"
"v_add_u32 v236,14336,v234\n"
"v_add_u32 v237,14336,v235\n"
"v_and_b32 v7,1,v4\n"
"v_lshlrev_b32 v7,1,v7\n"
"v_xor_b32 v7,v7,v5\n"
"v_lshlrev_b32 v7,4,v7\n"
"v_lshl_add_u32 v238,v3,13,v7\n"
"v_xor_b32 v239,64,v238\n"
"v_add_u32 v239,64512,v239\n"
"v_add_u32 v240,129024,v238\n"
"v_add_u32 v241,129024,v239\n"
"v_and_b32 v8,31,v2\n"
"v_lshlrev_b32 v8,3,v8\n"
"v_lshrrev_b32 v9,5,v2\n"
"s_lshl_b32 s50,s31,2\n"
SA"s50,s50,s30\n"
"s_mul_i32 s50,s50,2560\n"
"v_mul_u32_u24 v9,1280,v9\n"
"v_add3_u32 v9,v9,v8,s50\n"
"s_lshl_b32 s50,s34,17\n"
"s_lshl_b32 s51,s35,8\n"
"s_and_b32 s52,s30,1\n"
"s_lshl_b32 s52,s52,7\n"
SA"s52,s52,s51\n"
"s_mov_b32 s14,0x120000\n"
"s_mov_b32 s15,0x20000\n"
"s_sub_u32 s18,4194304,s52\n"
"s_mov_b32 s19,0x20000\n"
WT"lgkmcnt(0)\n"
"s_load_dwordx2 s[28:29],s[84:85],0\n"
SA"s12,s66,s50\n"
"s_addc_u32 s13,s67,0\n"
"s_and_b32 s13,s13,0xffff\n"
"s_cmp_lt_u32 s30,2\n"
"s_cselect_b64 s[16:17],s[70:71],s[72:73]\n"
SA"s16,s16,s52\n"
"s_addc_u32 s17,s17,0\n"
"s_and_b32 s17,s17,0xffff\n"
"global_load_dwordx2 v[128:129],v9,s[64:65] offset:0\n"
"global_load_dwordx2 v[130:131],v9,s[64:65] offset:256\n"
"global_load_dwordx2 v[132:133],v9,s[64:65] offset:512\n"
"global_load_dwordx2 v[134:135],v9,s[64:65] offset:768\n"
"global_load_dwordx2 v[136:137],v9,s[64:65] offset:1024\n"
"global_load_dwordx2 v[138:139],v8,s[76:77] offset:0\n"
"global_load_dwordx2 v[140:141],v8,s[76:77] offset:256\n"
"global_load_dwordx2 v[142:143],v8,s[76:77] offset:512\n"
"global_load_dwordx2 v[144:145],v8,s[76:77] offset:768\n"
"global_load_dwordx2 v[146:147],v8,s[76:77] offset:1024\n"
SA"m0,s37,0\n"
SA"s48,s47,0\n"
"s_and_b32 s48,s48,4194303\n"
BL"v238,s[16:19],s48 offen offset:0 lds\n"
BL"v239,s[16:19],s48 offen offset:1024 lds\n"
BL"v240,s[16:19],s48 offen offset:2048 lds\n"
BL"v241,s[16:19],s48 offen offset:3072 lds\n"
"s_getpc_b64 s[24:25]\n"
"LPC_%=:\n"
"s_and_b32 s25,s25,0xffff\n"
"s_mov_b32 s26,LEND_%=-LPC_%=\n"
"s_mov_b32 s27,0x20000\n"
"s_lshr_b32 s49,s31,3\n"
"s_lshl_b32 s49,s49,2\n"
SA"s49,s49,s30\n"
"s_lshl_b32 s49,s49,7\n"
"v_lshlrev_b32 v10,14,v2\n"
"buffer_load_dword v255,v10,s[24:27],s49 offen\n"
SA"m0,s36,0\n"
SA"s48,s46,0\n"
"s_and_b32 s48,s48,1023\n"
BL"v234,s[12:15],s48 offen offset:0 lds\n"
BL"v235,s[12:15],s48 offen offset:1024 lds\n"
BL"v236,s[12:15],s48 offen offset:2048 lds\n"
BL"v237,s[12:15],s48 offen offset:3072 lds\n"
SA"m0,s37,4096\n"
SA"s48,s47,262144\n"
"s_and_b32 s48,s48,4194303\n"
BL"v238,s[16:19],s48 offen offset:0 lds\n"
BL"v239,s[16:19],s48 offen offset:1024 lds\n"
BL"v240,s[16:19],s48 offen offset:2048 lds\n"
BL"v241,s[16:19],s48 offen offset:3072 lds\n"
SA"m0,s37,8192\n"
SA"s48,s47,524288\n"
"s_and_b32 s48,s48,4194303\n"
BL"v238,s[16:19],s48 offen offset:0 lds\n"
BL"v239,s[16:19],s48 offen offset:1024 lds\n"
BL"v240,s[16:19],s48 offen offset:2048 lds\n"
BL"v241,s[16:19],s48 offen offset:3072 lds\n"
"v_lshlrev_b32 v253,7,%[t]\n"
"s_mov_b32 s8,s84\n"
"s_and_b32 s9,s85,0xffff\n"
"s_mov_b32 s10,81272832\n"
"s_mov_b32 s11,0x20000\n"
SA"s20,s68,s51\n"
"s_addc_u32 s21,s69,0\n"
"s_and_b32 s21,s21,0xffff\n"
"s_sub_u32 s22,5242880,s51\n"
"s_mov_b32 s23,0x20000\n"
"v_and_b32 v11,15,v2\n"
"v_lshrrev_b32 v12,1,v11\n"
"v_xor_b32 v12,v12,v4\n"
"v_lshlrev_b32 v12,4,v12\n"
"v_lshl_add_u32 v242,v11,7,v12\n"
"v_xor_b32 v243,64,v242\n"
"v_lshrrev_b32 v12,2,v11\n"
"v_lshl_add_u32 v12,v4,3,v12\n"
"v_lshlrev_b32 v12,7,v12\n"
"v_and_b32 v13,3,v2\n"
"v_lshl_add_u32 v12,v13,3,v12\n"
"v_and_b32 v13,3,v3\n"
"v_lshl_add_u32 v12,v13,5,v12\n"
"v_add_u32 v246,s37,v12\n"
"v_xor_b32 v247,32,v246\n"
"v_xor_b32 v248,64,v246\n"
"v_xor_b32 v249,96,v246\n"
"v_mov_b32 v13,2130706432\n"
"v_cmp_eq_u32 vcc,0,v2\n"
"s_cmp_eq_u32 s30,0\n"
"s_cselect_b32 s45,0,2130706432\n"
"v_cndmask_b32_e64 v230,v13,0,vcc\n"
"s_lshl_b32 s39,s30,8\n"
SA"s39,s39,s46\n"
SA"s98,s39,128\n"
"s_and_b32 s39,s39,1023\n"
"s_and_b32 s98,s98,1023\n"
"s_lshl_b32 s50,s30,15\n"
"s_sub_u32 s50,1048576,s50\n"
SA"s39,s39,s50\n"
SA"s98,s98,s50\n"
"s_mov_b32 s55,0x3e0293ee\n"
WT"lgkmcnt(0)\n"
SA"s28,s28,0x7f4a7c15\n"
"s_addc_u32 s29,s29,0x9e3779b9\n"
"v_mov_b32 v232,s28\n"
"v_mov_b32 v233,s29\n"
"s_cmp_lt_u32 s30,2\n"
"s_cbranch_scc0 LV_%=\n"
WT"vmcnt(26)\n"
"v_lshlrev_b32 v148,16,v128\n"
"v_and_b32 v149,0xffff0000,v128\n"
"v_lshlrev_b32 v150,16,v129\n"
"v_and_b32 v151,0xffff0000,v129\n"
WT"vmcnt(25)\n"
"v_lshlrev_b32 v152,16,v130\n"
"v_and_b32 v153,0xffff0000,v130\n"
"v_lshlrev_b32 v154,16,v131\n"
"v_and_b32 v155,0xffff0000,v131\n"
WT"vmcnt(24)\n"
"v_lshlrev_b32 v156,16,v132\n"
"v_and_b32 v157,0xffff0000,v132\n"
"v_lshlrev_b32 v158,16,v133\n"
"v_and_b32 v159,0xffff0000,v133\n"
WT"vmcnt(23)\n"
"v_lshlrev_b32 v160,16,v134\n"
"v_and_b32 v161,0xffff0000,v134\n"
"v_lshlrev_b32 v162,16,v135\n"
"v_and_b32 v163,0xffff0000,v135\n"
WT"vmcnt(22)\n"
"v_lshlrev_b32 v164,16,v136\n"
"v_and_b32 v165,0xffff0000,v136\n"
"v_lshlrev_b32 v166,16,v137\n"
"v_and_b32 v167,0xffff0000,v137\n"
"v_add_f32 v188,v148,v149\n"
"v_add_f32 v189,v150,v151\n"
"v_add_f32 v188,v188,v152\n"
"v_add_f32 v189,v189,v153\n"
"v_add_f32 v188,v188,v154\n"
"v_add_f32 v189,v189,v155\n"
"v_add_f32 v188,v188,v156\n"
"v_add_f32 v189,v189,v157\n"
"v_add_f32 v188,v188,v158\n"
"v_add_f32 v189,v189,v159\n"
"v_add_f32 v188,v188,v160\n"
"v_add_f32 v189,v189,v161\n"
"v_add_f32 v188,v188,v162\n"
"v_add_f32 v189,v189,v163\n"
"v_add_f32 v188,v188,v164\n"
"v_add_f32 v189,v189,v165\n"
"v_add_f32 v188,v188,v166\n"
"v_add_f32 v189,v189,v167\n"
"v_add_f32 v188,v188,v189\n"
"s_nop 1\n"
"v_add_f32_dpp v188,v188,v188 quad_perm:[1,0,3,2] row_mask:0xf bank_mask:0xf\n"
WT"vmcnt(21)\n"
"v_lshlrev_b32 v168,16,v138\n"
"v_and_b32 v169,0xffff0000,v138\n"
"v_add_f32_dpp v188,v188,v188 quad_perm:[2,3,0,1] row_mask:0xf bank_mask:0xf\n"
"v_lshlrev_b32 v170,16,v139\n"
"v_and_b32 v171,0xffff0000,v139\n"
"v_add_f32_dpp v188,v188,v188 row_half_mirror row_mask:0xf bank_mask:0xf\n"
WT"vmcnt(20)\n"
"v_lshlrev_b32 v172,16,v140\n"
"v_and_b32 v173,0xffff0000,v140\n"
"v_add_f32_dpp v188,v188,v188 row_mirror row_mask:0xf bank_mask:0xf\n"
"v_lshlrev_b32 v174,16,v141\n"
"v_and_b32 v175,0xffff0000,v141\n"
"v_mov_b32 v189,v188\n"
WT"vmcnt(19)\n"
"v_lshlrev_b32 v176,16,v142\n"
"v_and_b32 v177,0xffff0000,v142\n"
"v_permlane16_swap_b32 v189,v188\n"
"v_lshlrev_b32 v178,16,v143\n"
"v_and_b32 v179,0xffff0000,v143\n"
"v_add_f32 v188,v188,v189\n"
WT"vmcnt(18)\n"
"v_lshlrev_b32 v180,16,v144\n"
"v_and_b32 v181,0xffff0000,v144\n"
MU"v191,0x3acccccd,v188\n"
"v_lshlrev_b32 v182,16,v145\n"
"v_and_b32 v183,0xffff0000,v145\n"
PK"v128,v191,v191\n"
WT"vmcnt(17)\n"
"v_lshlrev_b32 v184,16,v146\n"
"v_and_b32 v185,0xffff0000,v146\n"
"v_lshlrev_b32 v128,16,v128\n"
"v_lshlrev_b32 v186,16,v147\n"
"v_and_b32 v187,0xffff0000,v147\n"
"v_sub_f32 v148,v148,v128\n"
"v_sub_f32 v149,v149,v128\n"
"v_sub_f32 v150,v150,v128\n"
"v_sub_f32 v151,v151,v128\n"
"v_sub_f32 v152,v152,v128\n"
"v_sub_f32 v153,v153,v128\n"
"v_sub_f32 v154,v154,v128\n"
"v_sub_f32 v155,v155,v128\n"
"v_sub_f32 v156,v156,v128\n"
"v_sub_f32 v157,v157,v128\n"
SA"m0,s36,16384\n"
SA"s48,s46,128\n"
"s_and_b32 s48,s48,1023\n"
BL"v234,s[12:15],s48 offen offset:0 lds\n"
BL"v235,s[12:15],s48 offen offset:1024 lds\n"
BL"v236,s[12:15],s48 offen offset:2048 lds\n"
BL"v237,s[12:15],s48 offen offset:3072 lds\n"
"v_sub_f32 v158,v158,v128\n"
"v_sub_f32 v159,v159,v128\n"
"v_sub_f32 v160,v160,v128\n"
"v_sub_f32 v161,v161,v128\n"
"v_sub_f32 v162,v162,v128\n"
"v_sub_f32 v163,v163,v128\n"
"v_sub_f32 v164,v164,v128\n"
"v_sub_f32 v165,v165,v128\n"
"v_sub_f32 v166,v166,v128\n"
"v_sub_f32 v167,v167,v128\n"
MU"v188,v148,v148\n"
MU"v189,v149,v149\n"
"v_fmac_f32 v188,v150,v150\n"
"v_fmac_f32 v189,v151,v151\n"
"v_fmac_f32 v188,v152,v152\n"
"v_fmac_f32 v189,v153,v153\n"
"v_fmac_f32 v188,v154,v154\n"
"v_fmac_f32 v189,v155,v155\n"
"v_fmac_f32 v188,v156,v156\n"
"v_fmac_f32 v189,v157,v157\n"
"v_fmac_f32 v188,v158,v158\n"
"v_fmac_f32 v189,v159,v159\n"
"v_fmac_f32 v188,v160,v160\n"
"v_fmac_f32 v189,v161,v161\n"
"v_fmac_f32 v188,v162,v162\n"
"v_fmac_f32 v189,v163,v163\n"
"v_fmac_f32 v188,v164,v164\n"
"v_fmac_f32 v189,v165,v165\n"
"v_fmac_f32 v188,v166,v166\n"
"v_fmac_f32 v189,v167,v167\n"
"v_add_f32 v188,v188,v189\n"
PK"v149,v148,v149\n"
"v_lshlrev_b32 v148,16,v149\n"
"v_add_f32_dpp v188,v188,v188 quad_perm:[1,0,3,2] row_mask:0xf bank_mask:0xf\n"
"v_and_b32 v149,0xffff0000,v149\n"
PK"v151,v150,v151\n"
"v_add_f32_dpp v188,v188,v188 quad_perm:[2,3,0,1] row_mask:0xf bank_mask:0xf\n"
"v_lshlrev_b32 v150,16,v151\n"
"v_and_b32 v151,0xffff0000,v151\n"
"v_add_f32_dpp v188,v188,v188 row_half_mirror row_mask:0xf bank_mask:0xf\n"
PK"v153,v152,v153\n"
"v_lshlrev_b32 v152,16,v153\n"
"v_add_f32_dpp v188,v188,v188 row_mirror row_mask:0xf bank_mask:0xf\n"
"v_and_b32 v153,0xffff0000,v153\n"
PK"v155,v154,v155\n"
"v_mov_b32 v189,v188\n"
"v_lshlrev_b32 v154,16,v155\n"
"v_and_b32 v155,0xffff0000,v155\n"
"v_permlane16_swap_b32 v189,v188\n"
PK"v157,v156,v157\n"
"v_lshlrev_b32 v156,16,v157\n"
"v_add_f32 v188,v188,v189\n"
"v_and_b32 v157,0xffff0000,v157\n"
PK"v159,v158,v159\n"
"v_sub_f32 v191,v191,v128\n"
"v_lshlrev_b32 v158,16,v159\n"
"v_and_b32 v159,0xffff0000,v159\n"
MU"v188,0x3acccccd,v188\n"
PK"v161,v160,v161\n"
"v_lshlrev_b32 v160,16,v161\n"
"v_fma_f32 v188,-v191,v191,v188\n"
"v_and_b32 v161,0xffff0000,v161\n"
PK"v163,v162,v163\n"
"v_max_f32 v188,0,v188\n"
"v_lshlrev_b32 v162,16,v163\n"
"v_and_b32 v163,0xffff0000,v163\n"
PK"v188,v188,v188\n"
PK"v165,v164,v165\n"
"v_lshlrev_b32 v164,16,v165\n"
"v_lshlrev_b32 v188,16,v188\n"
"v_and_b32 v165,0xffff0000,v165\n"
PK"v167,v166,v167\n"
"v_add_f32 v188,0x3727c5ac,v188\n"
"v_lshlrev_b32 v166,16,v167\n"
"v_and_b32 v167,0xffff0000,v167\n"
PK"v188,v188,v188\n"
"v_bfe_u32 v130,v224,5,4\n"
"v_mul_u32_u24 v130,144,v130\n"
"v_lshlrev_b32 v188,16,v188\n"
SA"m0,s37,12288\n"
SA"s48,s47,786432\n"
"s_and_b32 s48,s48,4194303\n"
BL"v238,s[16:19],s48 offen offset:0 lds\n"
BL"v239,s[16:19],s48 offen offset:1024 lds\n"
BL"v240,s[16:19],s48 offen offset:2048 lds\n"
BL"v241,s[16:19],s48 offen offset:3072 lds\n"
"v_bfe_u32 v131,v224,4,1\n"
"v_lshl_add_u32 v130,v131,3,v130\n"
"v_sqrt_f32 v189,v188\n"
"v_lshrrev_b32 v131,9,v224\n"
"v_lshl_add_u32 v130,v131,4,v130\n"
"v_rcp_f32 v190,v189\n"
"s_lshl_b32 s52,s30,5\n"
"v_add_u32 v130,s52,v130\n"
"v_fma_f32 v188,-v189,v189,v188\n"
MU"v190,0.5,v190\n"
"v_fma_f32 v189,v188,v190,v189\n"
PK"v189,v189,v189\n"
"v_lshlrev_b32 v189,16,v189\n"
"v_rcp_f32 v129,v189\n"
"s_nop 0\n"
MU"v148,v148,v129\n"
MU"v149,v149,v129\n"
PK"v138,v148,v149\n"
MU"v150,v150,v129\n"
MU"v151,v151,v129\n"
PK"v139,v150,v151\n"
"ds_write_b64 v130,v[138:139] offset:49152\n"
MU"v152,v152,v129\n"
MU"v153,v153,v129\n"
PK"v140,v152,v153\n"
MU"v154,v154,v129\n"
MU"v155,v155,v129\n"
PK"v141,v154,v155\n"
"ds_write_b64 v130,v[140:141] offset:51456\n"
MU"v156,v156,v129\n"
MU"v157,v157,v129\n"
PK"v142,v156,v157\n"
MU"v158,v158,v129\n"
MU"v159,v159,v129\n"
PK"v143,v158,v159\n"
"ds_write_b64 v130,v[142:143] offset:53760\n"
MU"v160,v160,v129\n"
MU"v161,v161,v129\n"
PK"v144,v160,v161\n"
MU"v162,v162,v129\n"
MU"v163,v163,v129\n"
PK"v145,v162,v163\n"
"ds_write_b64 v130,v[144:145] offset:56064\n"
MU"v164,v164,v129\n"
MU"v165,v165,v129\n"
PK"v146,v164,v165\n"
MU"v166,v166,v129\n"
MU"v167,v167,v129\n"
PK"v147,v166,v167\n"
"ds_write_b64 v130,v[146:147] offset:58368\n"
"v_lshlrev_b32 v148,16,v138\n"
"v_and_b32 v149,0xffff0000,v138\n"
MU"v188,v148,v168\n"
MU"v189,v149,v169\n"
"v_lshlrev_b32 v150,16,v139\n"
"v_and_b32 v151,0xffff0000,v139\n"
"v_fmac_f32 v188,v150,v170\n"
"v_fmac_f32 v189,v151,v171\n"
"v_lshlrev_b32 v152,16,v140\n"
"v_and_b32 v153,0xffff0000,v140\n"
"v_fmac_f32 v188,v152,v172\n"
"v_fmac_f32 v189,v153,v173\n"
"v_lshlrev_b32 v154,16,v141\n"
"v_and_b32 v155,0xffff0000,v141\n"
"v_fmac_f32 v188,v154,v174\n"
"v_fmac_f32 v189,v155,v175\n"
"v_lshlrev_b32 v156,16,v142\n"
"v_and_b32 v157,0xffff0000,v142\n"
"v_fmac_f32 v188,v156,v176\n"
"v_fmac_f32 v189,v157,v177\n"
"v_lshlrev_b32 v158,16,v143\n"
"v_and_b32 v159,0xffff0000,v143\n"
"v_fmac_f32 v188,v158,v178\n"
"v_fmac_f32 v189,v159,v179\n"
"v_lshlrev_b32 v160,16,v144\n"
"v_and_b32 v161,0xffff0000,v144\n"
"v_fmac_f32 v188,v160,v180\n"
"v_fmac_f32 v189,v161,v181\n"
"v_lshlrev_b32 v162,16,v145\n"
"v_and_b32 v163,0xffff0000,v145\n"
WT"vmcnt(21)\n"
RT"a[0:1],v246 offset:0\n"
RT"a[2:3],v246 offset:512\n"
RT"a[4:5],v247 offset:0\n"
RT"a[6:7],v247 offset:512\n"
RT"a[8:9],v248 offset:0\n"
RT"a[10:11],v248 offset:512\n"
RT"a[12:13],v249 offset:0\n"
RT"a[14:15],v249 offset:512\n"
WT"vmcnt(12)\n"
RT"a[16:17],v246 offset:4096\n"
RT"a[18:19],v246 offset:4608\n"
WT"lgkmcnt(10)\n"
"s_barrier\n"
RD"v[192:195],v242 offset:0\n"
RD"v[196:199],v242 offset:2048\n"
RD"v[200:203],v242 offset:4096\n"
RD"v[204:207],v242 offset:6144\n"
RD"v[208:211],v242 offset:8192\n"
"v_lshrrev_b32 v131,7,v224\n"
WT"lgkmcnt(4)\n"
MF"v[0:3],a[0:3],v[192:195],0\n"
SA"m0,s37,16384\n"
RD"v[212:215],v242 offset:10240\n"
"v_lshl_add_u32 v131,v131,4,v224\n"
MF"v[4:7],a[4:7],v[192:195],0\n"
SA"s48,s47,1048576\n"
"s_and_b32 s48,s48,4194303\n"
"s_mul_i32 s52,s30,1152\n"
MF"v[8:11],a[8:11],v[192:195],0\n"
RT"a[20:21],v247 offset:4096\n"
"v_add_u32 v128,s52,v131\n"
MF"v[12:15],a[12:15],v[192:195],0\n"
WT"lgkmcnt(5)\n"
BL"v238,s[16:19],s48 offen offset:0 lds\n"
MF"v[16:19],a[0:3],v[196:199],0\n"
"s_and_b32 s52,s30,1\n"
RD"v[216:219],v242 offset:12288\n"
"v_fmac_f32 v188,v162,v182\n"
MF"v[20:23],a[4:7],v[196:199],0\n"
"s_mul_i32 s51,s52,1152\n"
"v_fmac_f32 v189,v163,v183\n"
"v_add_u32 v131,s51,v131\n"
MF"v[24:27],a[8:11],v[196:199],0\n"
"s_lshl_b32 s52,s52,10\n"
RT"a[22:23],v247 offset:4608\n"
"v_lshlrev_b32 v164,16,v146\n"
MF"v[28:31],a[12:15],v[196:199],0\n"
WT"lgkmcnt(3)\n"
BL"v239,s[16:19],s48 offen offset:1024 lds\n"
MF"v[32:35],a[0:3],v[200:203],0\n"
RD"v[220:223],v242 offset:14336\n"
"v_and_b32 v129,0x70,v224\n"
MF"v[36:39],a[4:7],v[200:203],0\n"
"s_lshr_b32 s50,s31,1\n"
RD"v[132:135],v128 offset:49152\n"
"v_and_b32 v165,0xffff0000,v146\n"
MF"v[40:43],a[8:11],v[200:203],0\n"
"s_mul_i32 s50,s50,20480\n"
RD"a[240:243],v128 offset:53760\n"
"v_and_b32 v130,0x380,v224\n"
MF"v[44:47],a[12:15],v[200:203],0\n"
"s_and_b32 s51,s31,1\n"
RD"a[244:247],v131 offset:58368\n"
"v_fmac_f32 v188,v164,v184\n"
MF"v[48:51],a[0:3],v[204:207],0\n"
"s_lshl_b32 s51,s51,7\n"
RD"v[192:195],v243 offset:0\n"
"v_lshl_add_u32 v129,v130,1,v129\n"
MF"v[52:55],a[4:7],v[204:207],0\n"
SA"s50,s50,s51\n"
RT"a[24:25],v248 offset:4096\n"
"v_fmac_f32 v189,v165,v185\n"
MF"v[56:59],a[8:11],v[204:207],0\n"
SA"s50,s50,65536\n"
BL"v240,s[16:19],s48 offen offset:2048 lds\n"
MF"v[60:63],a[12:15],v[204:207],0\n"
"s_lshl_b32 s52,s52,1\n"
RD"v[196:199],v243 offset:2048\n"
"v_lshlrev_b32 v166,16,v147\n"
MF"v[64:67],a[0:3],v[208:211],0\n"
SA"s52,s52,s50\n"
RT"a[26:27],v248 offset:4608\n"
"v_and_b32 v167,0xffff0000,v147\n"
MF"v[68:71],a[4:7],v[208:211],0\n"
SA"s52,s52,16384\n"
BL"v241,s[16:19],s48 offen offset:3072 lds\n"
MF"v[72:75],a[8:11],v[208:211],0\n"
SA"m0,s36,32768\n"
RD"v[200:203],v243 offset:4096\n"
"v_fmac_f32 v188,v166,v186\n"
MF"v[76:79],a[12:15],v[208:211],0\n"
SA"s48,s46,256\n"
RT"a[28:29],v249 offset:4096\n"
"v_fmac_f32 v189,v167,v187\n"
MF"v[80:83],a[0:3],v[212:215],0\n"
"s_and_b32 s48,s48,1023\n"
"v_add_f32 v188,v188,v189\n"
"s_lshl_b32 s51,s30,11\n"
MF"v[84:87],a[4:7],v[212:215],0\n"
SA"s50,s50,s51\n"
BL"v234,s[12:15],s48 offen offset:0 lds\n"
MF"v[88:91],a[8:11],v[212:215],0\n"
SA"s51,s50,8192\n"
RD"v[204:207],v243 offset:6144\n"
"v_add_f32_dpp v188,v188,v188 quad_perm:[1,0,3,2] row_mask:0xf bank_mask:0xf\n"
MF"v[92:95],a[12:15],v[212:215],0\n"
WT"lgkmcnt(3)\n"
RT"a[30:31],v249 offset:4608\n"
"v_add_f32_dpp v188,v188,v188 quad_perm:[2,3,0,1] row_mask:0xf bank_mask:0xf\n"
MF"v[96:99],a[0:3],v[216:219],0\n"
BS"v[132:135],v129,s[8:11],s50 offen sc1\n"
MF"v[100:103],a[4:7],v[216:219],0\n"
"s_lshl_b32 s90,s31,2\n"
RD"v[208:211],v243 offset:8192\n"
"v_add_f32_dpp v188,v188,v188 row_half_mirror row_mask:0xf bank_mask:0xf\n"
MF"v[104:107],a[8:11],v[216:219],0\n"
SA"s90,s90,s30\n"
BL"v235,s[12:15],s48 offen offset:1024 lds\n"
MF"v[108:111],a[12:15],v[216:219],0\n"
"s_lshl_b32 s90,s90,1\n"
BS"a[240:243],v129,s[8:11],s51 offen sc1\n"
MF"v[112:115],a[0:3],v[220:223],0\n"
"s_sub_u32 s91,2047,s90\n"
WT"vmcnt(16)\n"
RT"a[32:33],v246 offset:8192\n"
MF"v[116:119],a[4:7],v[220:223],0\n"
BS"a[244:247],v129,s[8:11],s52 offen sc1\n"
MF"v[120:123],a[8:11],v[220:223],0\n"
RD"v[212:215],v243 offset:10240\n"
"v_add_f32_dpp v188,v188,v188 row_mirror row_mask:0xf bank_mask:0xf\n"
MF"v[124:127],a[12:15],v[220:223],0\n"
BL"v236,s[12:15],s48 offen offset:2048 lds\n"
MF"v[0:3],a[16:19],v[192:195],v[0:3]\n"
RT"a[34:35],v246 offset:8704\n"
"v_mov_b32 v189,v188\n"
MF"v[4:7],a[20:23],v[192:195],v[4:7]\n"
RD"v[216:219],v243 offset:12288\n"
"v_permlane16_swap_b32 v189,v188\n"
MF"v[8:11],a[24:27],v[192:195],v[8:11]\n"
WT"lgkmcnt(4)\n"
BL"v237,s[12:15],s48 offen offset:3072 lds\n"
MF"v[12:15],a[28:31],v[192:195],v[12:15]\n"
"s_lshl_b32 s90,s90,2\n"
RT"a[36:37],v247 offset:8192\n"
"v_add_f32 v188,v188,v189\n"
MF"v[16:19],a[16:19],v[196:199],v[16:19]\n"
SA"s90,s90,2686976\n"
RD"v[220:223],v243 offset:14336\n"
PK"v188,v188,v188\n"
MF"v[20:23],a[20:23],v[196:199],v[20:23]\n"
SA"m0,s37,20480\n"
"v_lshlrev_b32 v188,16,v188\n"
"v_ashrrev_i32 v189,31,v188\n"
MF"v[24:27],a[24:27],v[196:199],v[24:27]\n"
SA"s48,s47,1310720\n"
RT"a[38:39],v247 offset:8704\n"
"v_or_b32 v189,0x80000000,v189\n"
MF"v[28:31],a[28:31],v[196:199],v[28:31]\n"
"s_and_b32 s48,s48,4194303\n"
"v_xor_b32 v188,v188,v189\n"
"v_and_b32 v188,0xffff0000,v188\n"
MF"v[32:35],a[16:19],v[200:203],v[32:35]\n"
BL"v238,s[16:19],s48 offen offset:0 lds\n"
MF"v[36:39],a[20:23],v[200:203],v[36:39]\n"
"v_lshrrev_b32 v189,9,v224\n"
"v_sub_u32 v190,s91,v189\n"
MF"v[40:43],a[24:27],v[200:203],v[40:43]\n"
RT"a[40:41],v248 offset:8192\n"
"v_or_b32 v188,v188,v190\n"
MF"v[44:47],a[28:31],v[200:203],v[44:47]\n"
BL"v239,s[16:19],s48 offen offset:1024 lds\n"
MF"v[48:51],a[16:19],v[204:207],v[48:51]\n"
"v_and_b32 v190,0x1f0,v224\n"
"v_lshlrev_b32 v190,23,v190\n"
MF"v[52:55],a[20:23],v[204:207],v[52:55]\n"
WT"vmcnt(17) lgkmcnt(2)\n"
"s_barrier\n"
MF"v[56:59],a[24:27],v[204:207],v[56:59]\n"
RD"v[192:195],v242 offset:16384\n"
"v_lshl_add_u32 v190,v189,2,v190\n"
MF"v[60:63],a[28:31],v[204:207],v[60:63]\n"
"buffer_store_dword v188,v190,s[8:11],s90 offen sc1\n"
MF"v[64:67],a[16:19],v[208:211],v[64:67]\n"
RT"a[42:43],v248 offset:8704\n"
MF"v[68:71],a[20:23],v[208:211],v[68:71]\n"
BL"v240,s[16:19],s48 offen offset:2048 lds\n"
MF"v[72:75],a[24:27],v[208:211],v[72:75]\n"
RD"v[196:199],v242 offset:18432\n"
MF"v[76:79],a[28:31],v[208:211],v[76:79]\n"
RT"a[44:45],v249 offset:8192\n"
MF"v[80:83],a[16:19],v[212:215],v[80:83]\n"
BL"v241,s[16:19],s48 offen offset:3072 lds\n"
MF"v[84:87],a[20:23],v[212:215],v[84:87]\n"
RD"v[200:203],v242 offset:20480\n"
MF"v[88:91],a[24:27],v[212:215],v[88:91]\n"
SA"m0,s37,0\n"
RT"a[46:47],v249 offset:8704\n"
SA"s48,s47,1572864\n"
MF"v[92:95],a[28:31],v[212:215],v[92:95]\n"
"s_and_b32 s48,s48,4194303\n"
MF"v[96:99],a[16:19],v[216:219],v[96:99]\n"
BL"v238,s[16:19],s48 offen offset:0 lds\n"
MF"v[100:103],a[20:23],v[216:219],v[100:103]\n"
RD"v[204:207],v242 offset:22528\n"
MF"v[104:107],a[24:27],v[216:219],v[104:107]\n"
WT"vmcnt(17)\n"
RT"a[48:49],v246 offset:12288\n"
MF"v[108:111],a[28:31],v[216:219],v[108:111]\n"
MF"v[112:115],a[16:19],v[220:223],v[112:115]\n"
RD"v[208:211],v242 offset:24576\n"
MF"v[116:119],a[20:23],v[220:223],v[116:119]\n"
BL"v239,s[16:19],s48 offen offset:1024 lds\n"
MF"v[120:123],a[24:27],v[220:223],v[120:123]\n"
RT"a[50:51],v246 offset:12800\n"
MF"v[124:127],a[28:31],v[220:223],v[124:127]\n"
WT"lgkmcnt(4)\n"
MF"v[0:3],a[32:35],v[192:195],v[0:3]\n"
RD"v[212:215],v242 offset:26624\n"
MF"v[4:7],a[36:39],v[192:195],v[4:7]\n"
BL"v240,s[16:19],s48 offen offset:2048 lds\n"
MF"v[8:11],a[40:43],v[192:195],v[8:11]\n"
RT"a[52:53],v247 offset:12288\n"
MF"v[12:15],a[44:47],v[192:195],v[12:15]\n"
RD"v[216:219],v242 offset:28672\n"
MF"v[16:19],a[32:35],v[196:199],v[16:19]\n"
MF"v[20:23],a[36:39],v[196:199],v[20:23]\n"
BL"v241,s[16:19],s48 offen offset:3072 lds\n"
MF"v[24:27],a[40:43],v[196:199],v[24:27]\n"
RT"a[54:55],v247 offset:12800\n"
MF"v[28:31],a[44:47],v[196:199],v[28:31]\n"
RD"v[220:223],v242 offset:30720\n"
MF"v[32:35],a[32:35],v[200:203],v[32:35]\n"
SA"m0,s36,0\n"
SA"s48,s46,384\n"
"s_and_b32 s48,s48,1023\n"
MF"v[36:39],a[36:39],v[200:203],v[36:39]\n"
MF"v[40:43],a[40:43],v[200:203],v[40:43]\n"
RD"v[192:195],v243 offset:16384\n"
MF"v[44:47],a[44:47],v[200:203],v[44:47]\n"
WT"lgkmcnt(3)\n"
RT"a[56:57],v248 offset:12288\n"
MF"v[48:51],a[32:35],v[204:207],v[48:51]\n"
BL"v234,s[12:15],s48 offen offset:0 lds\n"
MF"v[52:55],a[36:39],v[204:207],v[52:55]\n"
MF"v[56:59],a[40:43],v[204:207],v[56:59]\n"
RD"v[196:199],v243 offset:18432\n"
MF"v[60:63],a[44:47],v[204:207],v[60:63]\n"
RT"a[58:59],v248 offset:12800\n"
MF"v[64:67],a[32:35],v[208:211],v[64:67]\n"
BL"v235,s[12:15],s48 offen offset:1024 lds\n"
MF"v[68:71],a[36:39],v[208:211],v[68:71]\n"
RD"v[200:203],v243 offset:20480\n"
MF"v[72:75],a[40:43],v[208:211],v[72:75]\n"
RT"a[60:61],v249 offset:12288\n"
MF"v[76:79],a[44:47],v[208:211],v[76:79]\n"
BL"v236,s[12:15],s48 offen offset:2048 lds\n"
MF"v[80:83],a[32:35],v[212:215],v[80:83]\n"
MF"v[84:87],a[36:39],v[212:215],v[84:87]\n"
RD"v[204:207],v243 offset:22528\n"
MF"v[88:91],a[40:43],v[212:215],v[88:91]\n"
RT"a[62:63],v249 offset:12800\n"
MF"v[92:95],a[44:47],v[212:215],v[92:95]\n"
BL"v237,s[12:15],s48 offen offset:3072 lds\n"
MF"v[96:99],a[32:35],v[216:219],v[96:99]\n"
RD"v[208:211],v243 offset:24576\n"
MF"v[100:103],a[36:39],v[216:219],v[100:103]\n"
MF"v[104:107],a[40:43],v[216:219],v[104:107]\n"
SA"m0,s37,4096\n"
WT"vmcnt(20)\n"
RT"a[64:65],v246 offset:16384\n"
MF"v[108:111],a[44:47],v[216:219],v[108:111]\n"
WT"lgkmcnt(4)\n"
SA"s48,s47,1835008\n"
"s_and_b32 s48,s48,4194303\n"
MF"v[112:115],a[32:35],v[220:223],v[112:115]\n"
RD"v[212:215],v243 offset:26624\n"
MF"v[116:119],a[36:39],v[220:223],v[116:119]\n"
BL"v238,s[16:19],s48 offen offset:0 lds\n"
MF"v[120:123],a[40:43],v[220:223],v[120:123]\n"
RT"a[66:67],v246 offset:16896\n"
MF"v[124:127],a[44:47],v[220:223],v[124:127]\n"
RD"v[216:219],v243 offset:28672\n"
MF"v[0:3],a[48:51],v[192:195],v[0:3]\n"
BL"v239,s[16:19],s48 offen offset:1024 lds\n"
MF"v[4:7],a[52:55],v[192:195],v[4:7]\n"
MF"v[8:11],a[56:59],v[192:195],v[8:11]\n"
WT"lgkmcnt(3)\n"
RT"a[68:69],v247 offset:16384\n"
MF"v[12:15],a[60:63],v[192:195],v[12:15]\n"
RD"v[220:223],v243 offset:30720\n"
MF"v[16:19],a[48:51],v[196:199],v[16:19]\n"
MF"v[20:23],a[52:55],v[196:199],v[20:23]\n"
BL"v240,s[16:19],s48 offen offset:2048 lds\n"
MF"v[24:27],a[56:59],v[196:199],v[24:27]\n"
RT"a[70:71],v247 offset:16896\n"
MF"v[28:31],a[60:63],v[196:199],v[28:31]\n"
MF"v[32:35],a[48:51],v[200:203],v[32:35]\n"
"s_lshl_b32 s51,s32,8\n"
"s_andn2_b32 s50,s31,7\n"
SA"s50,s50,s51\n"
MF"v[36:39],a[52:55],v[200:203],v[36:39]\n"
SA"s50,s50,256\n"
BL"v241,s[16:19],s48 offen offset:3072 lds\n"
MF"v[40:43],a[56:59],v[200:203],v[40:43]\n"
SA"s50,s50,s45\n"
RT"a[72:73],v248 offset:16384\n"
MF"v[44:47],a[60:63],v[200:203],v[44:47]\n"
MF"v[48:51],a[48:51],v[204:207],v[48:51]\n"
SA"m0,s37,8192\n"
SA"s48,s47,2097152\n"
"s_and_b32 s48,s48,4194303\n"
MF"v[52:55],a[52:55],v[204:207],v[52:55]\n"
WT"vmcnt(14) lgkmcnt(2)\n"
"s_barrier\n"
MF"v[56:59],a[56:59],v[204:207],v[56:59]\n"
RD"v[192:195],v242 offset:32768\n"
MF"v[60:63],a[60:63],v[204:207],v[60:63]\n"
RT"a[74:75],v248 offset:16896\n"
MF"v[64:67],a[48:51],v[208:211],v[64:67]\n"
BL"v238,s[16:19],s48 offen offset:0 lds\n"
MF"v[68:71],a[52:55],v[208:211],v[68:71]\n"
"buffer_store_dwordx2 v[232:233],v230,s[8:11],s50 offen sc1\n"
MF"v[72:75],a[56:59],v[208:211],v[72:75]\n"
RD"v[196:199],v242 offset:34816\n"
MF"v[76:79],a[60:63],v[208:211],v[76:79]\n"
RT"a[76:77],v249 offset:16384\n"
MF"v[80:83],a[48:51],v[212:215],v[80:83]\n"
BL"v239,s[16:19],s48 offen offset:1024 lds\n"
MF"v[84:87],a[52:55],v[212:215],v[84:87]\n"
RD"v[200:203],v242 offset:36864\n"
MF"v[88:91],a[56:59],v[212:215],v[88:91]\n"
RT"a[78:79],v249 offset:16896\n"
MF"v[92:95],a[60:63],v[212:215],v[92:95]\n"
BL"v240,s[16:19],s48 offen offset:2048 lds\n"
MF"v[96:99],a[48:51],v[216:219],v[96:99]\n"
MF"v[100:103],a[52:55],v[216:219],v[100:103]\n"
RD"v[204:207],v242 offset:38912\n"
MF"v[104:107],a[56:59],v[216:219],v[104:107]\n"
WT"vmcnt(16)\n"
RT"a[80:81],v246 offset:20480\n"
MF"v[108:111],a[60:63],v[216:219],v[108:111]\n"
BL"v241,s[16:19],s48 offen offset:3072 lds\n"
MF"v[112:115],a[48:51],v[220:223],v[112:115]\n"
RD"v[208:211],v242 offset:40960\n"
MF"v[116:119],a[52:55],v[220:223],v[116:119]\n"
MF"v[120:123],a[56:59],v[220:223],v[120:123]\n"
SA"m0,s36,16384\n"
RT"a[82:83],v246 offset:20992\n"
SA"s48,s46,512\n"
MF"v[124:127],a[60:63],v[220:223],v[124:127]\n"
WT"lgkmcnt(4)\n"
"s_and_b32 s48,s48,1023\n"
MF"v[0:3],a[64:67],v[192:195],v[0:3]\n"
RD"v[212:215],v242 offset:43008\n"
MF"v[4:7],a[68:71],v[192:195],v[4:7]\n"
BL"v234,s[12:15],s48 offen offset:0 lds\n"
MF"v[8:11],a[72:75],v[192:195],v[8:11]\n"
RT"a[84:85],v247 offset:20480\n"
MF"v[12:15],a[76:79],v[192:195],v[12:15]\n"
RD"v[216:219],v242 offset:45056\n"
MF"v[16:19],a[64:67],v[196:199],v[16:19]\n"
BL"v235,s[12:15],s48 offen offset:1024 lds\n"
MF"v[20:23],a[68:71],v[196:199],v[20:23]\n"
MF"v[24:27],a[72:75],v[196:199],v[24:27]\n"
RT"a[86:87],v247 offset:20992\n"
MF"v[28:31],a[76:79],v[196:199],v[28:31]\n"
RD"v[220:223],v242 offset:47104\n"
MF"v[32:35],a[64:67],v[200:203],v[32:35]\n"
MF"v[36:39],a[68:71],v[200:203],v[36:39]\n"
BL"v236,s[12:15],s48 offen offset:2048 lds\n"
MF"v[40:43],a[72:75],v[200:203],v[40:43]\n"
RD"v[192:195],v243 offset:32768\n"
MF"v[44:47],a[76:79],v[200:203],v[44:47]\n"
WT"lgkmcnt(3)\n"
RT"a[88:89],v248 offset:20480\n"
MF"v[48:51],a[64:67],v[204:207],v[48:51]\n"
MF"v[52:55],a[68:71],v[204:207],v[52:55]\n"
BL"v237,s[12:15],s48 offen offset:3072 lds\n"
MF"v[56:59],a[72:75],v[204:207],v[56:59]\n"
RD"v[196:199],v243 offset:34816\n"
MF"v[60:63],a[76:79],v[204:207],v[60:63]\n"
RT"a[90:91],v248 offset:20992\n"
MF"v[64:67],a[64:67],v[208:211],v[64:67]\n"
SA"m0,s37,12288\n"
SA"s48,s47,2359296\n"
"s_and_b32 s48,s48,4194303\n"
MF"v[68:71],a[68:71],v[208:211],v[68:71]\n"
RD"v[200:203],v243 offset:36864\n"
MF"v[72:75],a[72:75],v[208:211],v[72:75]\n"
RT"a[92:93],v249 offset:20480\n"
MF"v[76:79],a[76:79],v[208:211],v[76:79]\n"
BL"v238,s[16:19],s48 offen offset:0 lds\n"
MF"v[80:83],a[64:67],v[212:215],v[80:83]\n"
MF"v[84:87],a[68:71],v[212:215],v[84:87]\n"
RD"v[204:207],v243 offset:38912\n"
MF"v[88:91],a[72:75],v[212:215],v[88:91]\n"
RT"a[94:95],v249 offset:20992\n"
MF"v[92:95],a[76:79],v[212:215],v[92:95]\n"
BL"v239,s[16:19],s48 offen offset:1024 lds\n"
MF"v[96:99],a[64:67],v[216:219],v[96:99]\n"
RD"v[208:211],v243 offset:40960\n"
MF"v[100:103],a[68:71],v[216:219],v[100:103]\n"
MF"v[104:107],a[72:75],v[216:219],v[104:107]\n"
WT"vmcnt(19)\n"
RT"a[96:97],v246 offset:0\n"
MF"v[108:111],a[76:79],v[216:219],v[108:111]\n"
WT"lgkmcnt(4)\n"
BL"v240,s[16:19],s48 offen offset:2048 lds\n"
MF"v[112:115],a[64:67],v[220:223],v[112:115]\n"
RD"v[212:215],v243 offset:43008\n"
MF"v[116:119],a[68:71],v[220:223],v[116:119]\n"
MF"v[120:123],a[72:75],v[220:223],v[120:123]\n"
RT"a[98:99],v246 offset:512\n"
MF"v[124:127],a[76:79],v[220:223],v[124:127]\n"
RD"v[216:219],v243 offset:45056\n"
MF"v[0:3],a[80:83],v[192:195],v[0:3]\n"
BL"v241,s[16:19],s48 offen offset:3072 lds\n"
MF"v[4:7],a[84:87],v[192:195],v[4:7]\n"
MF"v[8:11],a[88:91],v[192:195],v[8:11]\n"
WT"lgkmcnt(3)\n"
RT"a[100:101],v247 offset:0\n"
SA"m0,s37,16384\n"
MF"v[12:15],a[92:95],v[192:195],v[12:15]\n"
SA"s48,s47,2621440\n"
RD"v[220:223],v243 offset:47104\n"
"s_and_b32 s48,s48,4194303\n"
MF"v[16:19],a[80:83],v[196:199],v[16:19]\n"
BL"v238,s[16:19],s48 offen offset:0 lds\n"
MF"v[20:23],a[84:87],v[196:199],v[20:23]\n"
MF"v[24:27],a[88:91],v[196:199],v[24:27]\n"
RT"a[102:103],v247 offset:512\n"
MF"v[28:31],a[92:95],v[196:199],v[28:31]\n"
MF"v[32:35],a[80:83],v[200:203],v[32:35]\n"
BL"v239,s[16:19],s48 offen offset:1024 lds\n"
MF"v[36:39],a[84:87],v[200:203],v[36:39]\n"
MF"v[40:43],a[88:91],v[200:203],v[40:43]\n"
RT"a[104:105],v248 offset:0\n"
MF"v[44:47],a[92:95],v[200:203],v[44:47]\n"
MF"v[48:51],a[80:83],v[204:207],v[48:51]\n"
MF"v[52:55],a[84:87],v[204:207],v[52:55]\n"
WT"vmcnt(19) lgkmcnt(2)\n"
"s_barrier\n"
MF"v[56:59],a[88:91],v[204:207],v[56:59]\n"
BL"v240,s[16:19],s48 offen offset:2048 lds\n"
MF"v[60:63],a[92:95],v[204:207],v[60:63]\n"
RD"v[192:195],v242 offset:0\n"
MF"v[64:67],a[80:83],v[208:211],v[64:67]\n"
RT"a[106:107],v248 offset:512\n"
MF"v[68:71],a[84:87],v[208:211],v[68:71]\n"
BL"v241,s[16:19],s48 offen offset:3072 lds\n"
MF"v[72:75],a[88:91],v[208:211],v[72:75]\n"
RD"v[196:199],v242 offset:2048\n"
MF"v[76:79],a[92:95],v[208:211],v[76:79]\n"
RT"a[108:109],v249 offset:0\n"
MF"v[80:83],a[80:83],v[212:215],v[80:83]\n"
SA"m0,s36,32768\n"
SA"s48,s46,640\n"
"s_and_b32 s48,s48,1023\n"
MF"v[84:87],a[84:87],v[212:215],v[84:87]\n"
RD"v[200:203],v242 offset:4096\n"
MF"v[88:91],a[88:91],v[212:215],v[88:91]\n"
RT"a[110:111],v249 offset:512\n"
MF"v[92:95],a[92:95],v[212:215],v[92:95]\n"
BL"v234,s[12:15],s48 offen offset:0 lds\n"
MF"v[96:99],a[80:83],v[216:219],v[96:99]\n"
MF"v[100:103],a[84:87],v[216:219],v[100:103]\n"
RD"v[204:207],v242 offset:6144\n"
MF"v[104:107],a[88:91],v[216:219],v[104:107]\n"
WT"vmcnt(18)\n"
RT"a[112:113],v246 offset:4096\n"
MF"v[108:111],a[92:95],v[216:219],v[108:111]\n"
BL"v235,s[12:15],s48 offen offset:1024 lds\n"
MF"v[112:115],a[80:83],v[220:223],v[112:115]\n"
RD"v[208:211],v242 offset:8192\n"
MF"v[116:119],a[84:87],v[220:223],v[116:119]\n"
MF"v[120:123],a[88:91],v[220:223],v[120:123]\n"
RT"a[114:115],v246 offset:4608\n"
MF"v[124:127],a[92:95],v[220:223],v[124:127]\n"
WT"lgkmcnt(4)\n"
BL"v236,s[12:15],s48 offen offset:2048 lds\n"
MF"v[0:3],a[96:99],v[192:195],v[0:3]\n"
RD"v[212:215],v242 offset:10240\n"
MF"v[4:7],a[100:103],v[192:195],v[4:7]\n"
MF"v[8:11],a[104:107],v[192:195],v[8:11]\n"
RT"a[116:117],v247 offset:4096\n"
MF"v[12:15],a[108:111],v[192:195],v[12:15]\n"
RD"v[216:219],v242 offset:12288\n"
MF"v[16:19],a[96:99],v[196:199],v[16:19]\n"
BL"v237,s[12:15],s48 offen offset:3072 lds\n"
MF"v[20:23],a[100:103],v[196:199],v[20:23]\n"
MF"v[24:27],a[104:107],v[196:199],v[24:27]\n"
SA"m0,s37,20480\n"
RT"a[118:119],v247 offset:4608\n"
SA"s48,s47,2883584\n"
MF"v[28:31],a[108:111],v[196:199],v[28:31]\n"
"s_and_b32 s48,s48,4194303\n"
RD"v[220:223],v242 offset:14336\n"
MF"v[32:35],a[96:99],v[200:203],v[32:35]\n"
BL"v238,s[16:19],s48 offen offset:0 lds\n"
MF"v[36:39],a[100:103],v[200:203],v[36:39]\n"
MF"v[40:43],a[104:107],v[200:203],v[40:43]\n"
RD"v[192:195],v243 offset:0\n"
MF"v[44:47],a[108:111],v[200:203],v[44:47]\n"
WT"lgkmcnt(3)\n"
RT"a[120:121],v248 offset:4096\n"
MF"v[48:51],a[96:99],v[204:207],v[48:51]\n"
BL"v239,s[16:19],s48 offen offset:1024 lds\n"
MF"v[52:55],a[100:103],v[204:207],v[52:55]\n"
MF"v[56:59],a[104:107],v[204:207],v[56:59]\n"
RD"v[196:199],v243 offset:2048\n"
MF"v[60:63],a[108:111],v[204:207],v[60:63]\n"
RT"a[122:123],v248 offset:4608\n"
MF"v[64:67],a[96:99],v[208:211],v[64:67]\n"
MF"v[68:71],a[100:103],v[208:211],v[68:71]\n"
RD"v[200:203],v243 offset:4096\n"
MF"v[72:75],a[104:107],v[208:211],v[72:75]\n"
BL"v240,s[16:19],s48 offen offset:2048 lds\n"
MF"v[76:79],a[108:111],v[208:211],v[76:79]\n"
RT"a[124:125],v249 offset:4096\n"
MF"v[80:83],a[96:99],v[212:215],v[80:83]\n"
MF"v[84:87],a[100:103],v[212:215],v[84:87]\n"
RD"v[204:207],v243 offset:6144\n"
MF"v[88:91],a[104:107],v[212:215],v[88:91]\n"
BL"v241,s[16:19],s48 offen offset:3072 lds\n"
MF"v[92:95],a[108:111],v[212:215],v[92:95]\n"
RT"a[126:127],v249 offset:4608\n"
MF"v[96:99],a[96:99],v[216:219],v[96:99]\n"
SA"m0,s37,0\n"
RD"v[208:211],v243 offset:8192\n"
SA"s48,s47,3145728\n"
MF"v[100:103],a[100:103],v[216:219],v[100:103]\n"
"s_and_b32 s48,s48,4194303\n"
MF"v[104:107],a[104:107],v[216:219],v[104:107]\n"
WT"vmcnt(20)\n"
RT"a[128:129],v246 offset:8192\n"
MF"v[108:111],a[108:111],v[216:219],v[108:111]\n"
WT"lgkmcnt(4)\n"
BL"v238,s[16:19],s48 offen offset:0 lds\n"
MF"v[112:115],a[96:99],v[220:223],v[112:115]\n"
RD"v[212:215],v243 offset:10240\n"
MF"v[116:119],a[100:103],v[220:223],v[116:119]\n"
MF"v[120:123],a[104:107],v[220:223],v[120:123]\n"
RT"a[130:131],v246 offset:8704\n"
MF"v[124:127],a[108:111],v[220:223],v[124:127]\n"
BL"v239,s[16:19],s48 offen offset:1024 lds\n"
MF"v[0:3],a[112:115],v[192:195],v[0:3]\n"
RD"v[216:219],v243 offset:12288\n"
MF"v[4:7],a[116:119],v[192:195],v[4:7]\n"
MF"v[8:11],a[120:123],v[192:195],v[8:11]\n"
WT"lgkmcnt(3)\n"
RT"a[132:133],v247 offset:8192\n"
MF"v[12:15],a[124:127],v[192:195],v[12:15]\n"
RD"v[220:223],v243 offset:14336\n"
MF"v[16:19],a[112:115],v[196:199],v[16:19]\n"
BL"v240,s[16:19],s48 offen offset:2048 lds\n"
MF"v[20:23],a[116:119],v[196:199],v[20:23]\n"
MF"v[24:27],a[120:123],v[196:199],v[24:27]\n"
RT"a[134:135],v247 offset:8704\n"
MF"v[28:31],a[124:127],v[196:199],v[28:31]\n"
BL"v241,s[16:19],s48 offen offset:3072 lds\n"
MF"v[32:35],a[112:115],v[200:203],v[32:35]\n"
MF"v[36:39],a[116:119],v[200:203],v[36:39]\n"
MF"v[40:43],a[120:123],v[200:203],v[40:43]\n"
RT"a[136:137],v248 offset:8192\n"
MF"v[44:47],a[124:127],v[200:203],v[44:47]\n"
MF"v[48:51],a[112:115],v[204:207],v[48:51]\n"
MF"v[52:55],a[116:119],v[204:207],v[52:55]\n"
WT"vmcnt(20) lgkmcnt(2)\n"
"s_barrier\n"
MF"v[56:59],a[120:123],v[204:207],v[56:59]\n"
SA"m0,s36,0\n"
RD"v[192:195],v242 offset:16384\n"
SA"s48,s46,768\n"
MF"v[60:63],a[124:127],v[204:207],v[60:63]\n"
"s_and_b32 s48,s48,1023\n"
RT"a[138:139],v248 offset:8704\n"
MF"v[64:67],a[112:115],v[208:211],v[64:67]\n"
BL"v234,s[12:15],s48 offen offset:0 lds\n"
MF"v[68:71],a[116:119],v[208:211],v[68:71]\n"
MF"v[72:75],a[120:123],v[208:211],v[72:75]\n"
RD"v[196:199],v242 offset:18432\n"
MF"v[76:79],a[124:127],v[208:211],v[76:79]\n"
RT"a[140:141],v249 offset:8192\n"
MF"v[80:83],a[112:115],v[212:215],v[80:83]\n"
BL"v235,s[12:15],s48 offen offset:1024 lds\n"
MF"v[84:87],a[116:119],v[212:215],v[84:87]\n"
RD"v[200:203],v242 offset:20480\n"
MF"v[88:91],a[120:123],v[212:215],v[88:91]\n"
RT"a[142:143],v249 offset:8704\n"
MF"v[92:95],a[124:127],v[212:215],v[92:95]\n"
MF"v[96:99],a[112:115],v[216:219],v[96:99]\n"
MF"v[100:103],a[116:119],v[216:219],v[100:103]\n"
RD"v[204:207],v242 offset:22528\n"
MF"v[104:107],a[120:123],v[216:219],v[104:107]\n"
BL"v236,s[12:15],s48 offen offset:2048 lds\n"
MF"v[108:111],a[124:127],v[216:219],v[108:111]\n"
WT"vmcnt(19)\n"
RT"a[144:145],v246 offset:12288\n"
MF"v[112:115],a[112:115],v[220:223],v[112:115]\n"
RD"v[208:211],v242 offset:24576\n"
MF"v[116:119],a[116:119],v[220:223],v[116:119]\n"
BL"v237,s[12:15],s48 offen offset:3072 lds\n"
MF"v[120:123],a[120:123],v[220:223],v[120:123]\n"
RT"a[146:147],v246 offset:12800\n"
MF"v[124:127],a[124:127],v[220:223],v[124:127]\n"
WT"lgkmcnt(4)\n"
MF"v[0:3],a[128:131],v[192:195],v[0:3]\n"
SA"m0,s37,4096\n"
RD"v[212:215],v242 offset:26624\n"
SA"s48,s47,3407872\n"
MF"v[4:7],a[132:135],v[192:195],v[4:7]\n"
"s_and_b32 s48,s48,4194303\n"
MF"v[8:11],a[136:139],v[192:195],v[8:11]\n"
RT"a[148:149],v247 offset:12288\n"
MF"v[12:15],a[140:143],v[192:195],v[12:15]\n"
BL"v238,s[16:19],s48 offen offset:0 lds\n"
MF"v[16:19],a[128:131],v[196:199],v[16:19]\n"
RD"v[216:219],v242 offset:28672\n"
MF"v[20:23],a[132:135],v[196:199],v[20:23]\n"
MF"v[24:27],a[136:139],v[196:199],v[24:27]\n"
RT"a[150:151],v247 offset:12800\n"
MF"v[28:31],a[140:143],v[196:199],v[28:31]\n"
BL"v239,s[16:19],s48 offen offset:1024 lds\n"
MF"v[32:35],a[128:131],v[200:203],v[32:35]\n"
RD"v[220:223],v242 offset:30720\n"
MF"v[36:39],a[132:135],v[200:203],v[36:39]\n"
MF"v[40:43],a[136:139],v[200:203],v[40:43]\n"
RD"v[192:195],v243 offset:16384\n"
MF"v[44:47],a[140:143],v[200:203],v[44:47]\n"
WT"lgkmcnt(3)\n"
RT"a[152:153],v248 offset:12288\n"
MF"v[48:51],a[128:131],v[204:207],v[48:51]\n"
BL"v240,s[16:19],s48 offen offset:2048 lds\n"
MF"v[52:55],a[132:135],v[204:207],v[52:55]\n"
MF"v[56:59],a[136:139],v[204:207],v[56:59]\n"
RD"v[196:199],v243 offset:18432\n"
MF"v[60:63],a[140:143],v[204:207],v[60:63]\n"
RT"a[154:155],v248 offset:12800\n"
MF"v[64:67],a[128:131],v[208:211],v[64:67]\n"
BL"v241,s[16:19],s48 offen offset:3072 lds\n"
MF"v[68:71],a[132:135],v[208:211],v[68:71]\n"
RD"v[200:203],v243 offset:20480\n"
MF"v[72:75],a[136:139],v[208:211],v[72:75]\n"
SA"m0,s37,8192\n"
RT"a[156:157],v249 offset:12288\n"
SA"s48,s47,3670016\n"
MF"v[76:79],a[140:143],v[208:211],v[76:79]\n"
"s_and_b32 s48,s48,4194303\n"
MF"v[80:83],a[128:131],v[212:215],v[80:83]\n"
BL"v238,s[16:19],s48 offen offset:0 lds\n"
MF"v[84:87],a[132:135],v[212:215],v[84:87]\n"
RD"v[204:207],v243 offset:22528\n"
MF"v[88:91],a[136:139],v[212:215],v[88:91]\n"
RT"a[158:159],v249 offset:12800\n"
MF"v[92:95],a[140:143],v[212:215],v[92:95]\n"
MF"v[96:99],a[128:131],v[216:219],v[96:99]\n"
RD"v[208:211],v243 offset:24576\n"
MF"v[100:103],a[132:135],v[216:219],v[100:103]\n"
BL"v239,s[16:19],s48 offen offset:1024 lds\n"
MF"v[104:107],a[136:139],v[216:219],v[104:107]\n"
WT"vmcnt(22)\n"
RT"a[160:161],v246 offset:16384\n"
MF"v[108:111],a[140:143],v[216:219],v[108:111]\n"
WT"lgkmcnt(4)\n"
MF"v[112:115],a[128:131],v[220:223],v[112:115]\n"
RD"v[212:215],v243 offset:26624\n"
MF"v[116:119],a[132:135],v[220:223],v[116:119]\n"
BL"v240,s[16:19],s48 offen offset:2048 lds\n"
MF"v[120:123],a[136:139],v[220:223],v[120:123]\n"
RT"a[162:163],v246 offset:16896\n"
MF"v[124:127],a[140:143],v[220:223],v[124:127]\n"
RD"v[216:219],v243 offset:28672\n"
MF"v[0:3],a[144:147],v[192:195],v[0:3]\n"
MF"v[4:7],a[148:151],v[192:195],v[4:7]\n"
BL"v241,s[16:19],s48 offen offset:3072 lds\n"
MF"v[8:11],a[152:155],v[192:195],v[8:11]\n"
WT"lgkmcnt(3)\n"
RT"a[164:165],v247 offset:16384\n"
MF"v[12:15],a[156:159],v[192:195],v[12:15]\n"
RD"v[220:223],v243 offset:30720\n"
MF"v[16:19],a[144:147],v[196:199],v[16:19]\n"
MF"v[20:23],a[148:151],v[196:199],v[20:23]\n"
MF"v[24:27],a[152:155],v[196:199],v[24:27]\n"
RT"a[166:167],v247 offset:16896\n"
MF"v[28:31],a[156:159],v[196:199],v[28:31]\n"
MF"v[32:35],a[144:147],v[200:203],v[32:35]\n"
MF"v[36:39],a[148:151],v[200:203],v[36:39]\n"
MF"v[40:43],a[152:155],v[200:203],v[40:43]\n"
RT"a[168:169],v248 offset:16384\n"
MF"v[44:47],a[156:159],v[200:203],v[44:47]\n"
MF"v[48:51],a[144:147],v[204:207],v[48:51]\n"
MF"v[52:55],a[148:151],v[204:207],v[52:55]\n"
WT"vmcnt(20) lgkmcnt(2)\n"
"s_barrier\n"
MF"v[56:59],a[152:155],v[204:207],v[56:59]\n"
SA"m0,s36,16384\n"
RD"v[192:195],v242 offset:32768\n"
SA"s48,s46,896\n"
MF"v[60:63],a[156:159],v[204:207],v[60:63]\n"
"s_and_b32 s48,s48,1023\n"
RT"a[170:171],v248 offset:16896\n"
MF"v[64:67],a[144:147],v[208:211],v[64:67]\n"
BL"v234,s[12:15],s48 offen offset:0 lds\n"
MF"v[68:71],a[148:151],v[208:211],v[68:71]\n"
MF"v[72:75],a[152:155],v[208:211],v[72:75]\n"
RD"v[196:199],v242 offset:34816\n"
MF"v[76:79],a[156:159],v[208:211],v[76:79]\n"
RT"a[172:173],v249 offset:16384\n"
MF"v[80:83],a[144:147],v[212:215],v[80:83]\n"
BL"v235,s[12:15],s48 offen offset:1024 lds\n"
MF"v[84:87],a[148:151],v[212:215],v[84:87]\n"
RD"v[200:203],v242 offset:36864\n"
MF"v[88:91],a[152:155],v[212:215],v[88:91]\n"
RT"a[174:175],v249 offset:16896\n"
MF"v[92:95],a[156:159],v[212:215],v[92:95]\n"
MF"v[96:99],a[144:147],v[216:219],v[96:99]\n"
MF"v[100:103],a[148:151],v[216:219],v[100:103]\n"
RD"v[204:207],v242 offset:38912\n"
MF"v[104:107],a[152:155],v[216:219],v[104:107]\n"
BL"v236,s[12:15],s48 offen offset:2048 lds\n"
MF"v[108:111],a[156:159],v[216:219],v[108:111]\n"
WT"vmcnt(19)\n"
RT"a[176:177],v246 offset:20480\n"
MF"v[112:115],a[144:147],v[220:223],v[112:115]\n"
RD"v[208:211],v242 offset:40960\n"
MF"v[116:119],a[148:151],v[220:223],v[116:119]\n"
BL"v237,s[12:15],s48 offen offset:3072 lds\n"
MF"v[120:123],a[152:155],v[220:223],v[120:123]\n"
RT"a[178:179],v246 offset:20992\n"
MF"v[124:127],a[156:159],v[220:223],v[124:127]\n"
WT"lgkmcnt(4)\n"
MF"v[0:3],a[160:163],v[192:195],v[0:3]\n"
SA"m0,s37,12288\n"
RD"v[212:215],v242 offset:43008\n"
SA"s48,s47,3932160\n"
MF"v[4:7],a[164:167],v[192:195],v[4:7]\n"
"s_and_b32 s48,s48,4194303\n"
MF"v[8:11],a[168:171],v[192:195],v[8:11]\n"
RT"a[180:181],v247 offset:20480\n"
MF"v[12:15],a[172:175],v[192:195],v[12:15]\n"
BL"v238,s[16:19],s48 offen offset:0 lds\n"
MF"v[16:19],a[160:163],v[196:199],v[16:19]\n"
RD"v[216:219],v242 offset:45056\n"
MF"v[20:23],a[164:167],v[196:199],v[20:23]\n"
MF"v[24:27],a[168:171],v[196:199],v[24:27]\n"
RT"a[182:183],v247 offset:20992\n"
MF"v[28:31],a[172:175],v[196:199],v[28:31]\n"
BL"v239,s[16:19],s48 offen offset:1024 lds\n"
MF"v[32:35],a[160:163],v[200:203],v[32:35]\n"
RD"v[220:223],v242 offset:47104\n"
MF"v[36:39],a[164:167],v[200:203],v[36:39]\n"
MF"v[40:43],a[168:171],v[200:203],v[40:43]\n"
RD"v[192:195],v243 offset:32768\n"
MF"v[44:47],a[172:175],v[200:203],v[44:47]\n"
WT"lgkmcnt(3)\n"
RT"a[184:185],v248 offset:20480\n"
MF"v[48:51],a[160:163],v[204:207],v[48:51]\n"
BL"v240,s[16:19],s48 offen offset:2048 lds\n"
MF"v[52:55],a[164:167],v[204:207],v[52:55]\n"
MF"v[56:59],a[168:171],v[204:207],v[56:59]\n"
RD"v[196:199],v243 offset:34816\n"
MF"v[60:63],a[172:175],v[204:207],v[60:63]\n"
RT"a[186:187],v248 offset:20992\n"
MF"v[64:67],a[160:163],v[208:211],v[64:67]\n"
BL"v241,s[16:19],s48 offen offset:3072 lds\n"
MF"v[68:71],a[164:167],v[208:211],v[68:71]\n"
RD"v[200:203],v243 offset:36864\n"
MF"v[72:75],a[168:171],v[208:211],v[72:75]\n"
SA"m0,s37,16384\n"
RT"a[188:189],v249 offset:20480\n"
SA"s48,s39,0\n"
MF"v[76:79],a[172:175],v[208:211],v[76:79]\n"
MF"v[80:83],a[160:163],v[212:215],v[80:83]\n"
BL"v234,s[12:15],s48 offen offset:0 lds\n"
MF"v[84:87],a[164:167],v[212:215],v[84:87]\n"
RD"v[204:207],v243 offset:38912\n"
MF"v[88:91],a[168:171],v[212:215],v[88:91]\n"
RT"a[190:191],v249 offset:20992\n"
MF"v[92:95],a[172:175],v[212:215],v[92:95]\n"
MF"v[96:99],a[160:163],v[216:219],v[96:99]\n"
RD"v[208:211],v243 offset:40960\n"
MF"v[100:103],a[164:167],v[216:219],v[100:103]\n"
BL"v235,s[12:15],s48 offen offset:1024 lds\n"
MF"v[104:107],a[168:171],v[216:219],v[104:107]\n"
WT"vmcnt(22)\n"
RT"a[192:193],v246 offset:0\n"
MF"v[108:111],a[172:175],v[216:219],v[108:111]\n"
WT"lgkmcnt(4)\n"
MF"v[112:115],a[160:163],v[220:223],v[112:115]\n"
RD"v[212:215],v243 offset:43008\n"
MF"v[116:119],a[164:167],v[220:223],v[116:119]\n"
BL"v236,s[12:15],s48 offen offset:2048 lds\n"
MF"v[120:123],a[168:171],v[220:223],v[120:123]\n"
RT"a[194:195],v246 offset:512\n"
MF"v[124:127],a[172:175],v[220:223],v[124:127]\n"
RD"v[216:219],v243 offset:45056\n"
MF"v[0:3],a[176:179],v[192:195],v[0:3]\n"
MF"v[4:7],a[180:183],v[192:195],v[4:7]\n"
BL"v237,s[12:15],s48 offen offset:3072 lds\n"
MF"v[8:11],a[184:187],v[192:195],v[8:11]\n"
WT"lgkmcnt(3)\n"
RT"a[196:197],v247 offset:0\n"
MF"v[12:15],a[188:191],v[192:195],v[12:15]\n"
RD"v[220:223],v243 offset:47104\n"
MF"v[16:19],a[176:179],v[196:199],v[16:19]\n"
SA"m0,s37,20480\n"
SA"s48,s98,0\n"
MF"v[20:23],a[180:183],v[196:199],v[20:23]\n"
MF"v[24:27],a[184:187],v[196:199],v[24:27]\n"
RT"a[198:199],v247 offset:512\n"
MF"v[28:31],a[188:191],v[196:199],v[28:31]\n"
BL"v234,s[12:15],s48 offen offset:0 lds\n"
MF"v[32:35],a[176:179],v[200:203],v[32:35]\n"
MF"v[36:39],a[180:183],v[200:203],v[36:39]\n"
MF"v[40:43],a[184:187],v[200:203],v[40:43]\n"
RT"a[200:201],v248 offset:0\n"
MF"v[44:47],a[188:191],v[200:203],v[44:47]\n"
BL"v235,s[12:15],s48 offen offset:1024 lds\n"
MF"v[48:51],a[176:179],v[204:207],v[48:51]\n"
MF"v[52:55],a[180:183],v[204:207],v[52:55]\n"
WT"vmcnt(22) lgkmcnt(2)\n"
"s_barrier\n"
MF"v[56:59],a[184:187],v[204:207],v[56:59]\n"
RD"v[192:195],v242 offset:0\n"
MF"v[60:63],a[188:191],v[204:207],v[60:63]\n"
RT"a[202:203],v248 offset:512\n"
MF"v[64:67],a[176:179],v[208:211],v[64:67]\n"
BL"v236,s[12:15],s48 offen offset:2048 lds\n"
MF"v[68:71],a[180:183],v[208:211],v[68:71]\n"
MF"v[72:75],a[184:187],v[208:211],v[72:75]\n"
RD"v[196:199],v242 offset:2048\n"
MF"v[76:79],a[188:191],v[208:211],v[76:79]\n"
RT"a[204:205],v249 offset:0\n"
MF"v[80:83],a[176:179],v[212:215],v[80:83]\n"
BL"v237,s[12:15],s48 offen offset:3072 lds\n"
MF"v[84:87],a[180:183],v[212:215],v[84:87]\n"
RD"v[200:203],v242 offset:4096\n"
MF"v[88:91],a[184:187],v[212:215],v[88:91]\n"
RT"a[206:207],v249 offset:512\n"
MF"v[92:95],a[188:191],v[212:215],v[92:95]\n"
MF"v[96:99],a[176:179],v[216:219],v[96:99]\n"
MF"v[100:103],a[180:183],v[216:219],v[100:103]\n"
RD"v[204:207],v242 offset:6144\n"
MF"v[104:107],a[184:187],v[216:219],v[104:107]\n"
SA"m0,s37,0\n"
WT"vmcnt(20)\n"
RT"a[208:209],v246 offset:4096\n"
MF"v[108:111],a[188:191],v[216:219],v[108:111]\n"
SA"s48,s39,32768\n"
MF"v[112:115],a[176:179],v[220:223],v[112:115]\n"
RD"v[208:211],v242 offset:8192\n"
MF"v[116:119],a[180:183],v[220:223],v[116:119]\n"
WT"lgkmcnt(3)\n"
BL"v234,s[12:15],s48 offen offset:0 lds\n"
MF"v[120:123],a[184:187],v[220:223],v[120:123]\n"
RT"a[210:211],v246 offset:4608\n"
MF"v[124:127],a[188:191],v[220:223],v[124:127]\n"
MF"v[0:3],a[192:195],v[192:195],v[0:3]\n"
RD"v[212:215],v242 offset:10240\n"
MF"v[4:7],a[196:199],v[192:195],v[4:7]\n"
BL"v235,s[12:15],s48 offen offset:1024 lds\n"
MF"v[8:11],a[200:203],v[192:195],v[8:11]\n"
RT"a[212:213],v247 offset:4096\n"
MF"v[12:15],a[204:207],v[192:195],v[12:15]\n"
RD"v[216:219],v242 offset:12288\n"
MF"v[16:19],a[192:195],v[196:199],v[16:19]\n"
MF"v[20:23],a[196:199],v[196:199],v[20:23]\n"
BL"v236,s[12:15],s48 offen offset:2048 lds\n"
MF"v[24:27],a[200:203],v[196:199],v[24:27]\n"
RT"a[214:215],v247 offset:4608\n"
MF"v[28:31],a[204:207],v[196:199],v[28:31]\n"
RD"v[220:223],v242 offset:14336\n"
MF"v[32:35],a[192:195],v[200:203],v[32:35]\n"
MF"v[36:39],a[196:199],v[200:203],v[36:39]\n"
BL"v237,s[12:15],s48 offen offset:3072 lds\n"
MF"v[40:43],a[200:203],v[200:203],v[40:43]\n"
RD"v[192:195],v243 offset:0\n"
MF"v[44:47],a[204:207],v[200:203],v[44:47]\n"
WT"lgkmcnt(3)\n"
RT"a[216:217],v248 offset:4096\n"
MF"v[48:51],a[192:195],v[204:207],v[48:51]\n"
MF"v[52:55],a[196:199],v[204:207],v[52:55]\n"
MF"v[56:59],a[200:203],v[204:207],v[56:59]\n"
RD"v[196:199],v243 offset:2048\n"
MF"v[60:63],a[204:207],v[204:207],v[60:63]\n"
RT"a[218:219],v248 offset:4608\n"
MF"v[64:67],a[192:195],v[208:211],v[64:67]\n"
MF"v[68:71],a[196:199],v[208:211],v[68:71]\n"
RD"v[200:203],v243 offset:4096\n"
MF"v[72:75],a[200:203],v[208:211],v[72:75]\n"
RT"a[220:221],v249 offset:4096\n"
MF"v[76:79],a[204:207],v[208:211],v[76:79]\n"
MF"v[80:83],a[192:195],v[212:215],v[80:83]\n"
MF"v[84:87],a[196:199],v[212:215],v[84:87]\n"
RD"v[204:207],v243 offset:6144\n"
MF"v[88:91],a[200:203],v[212:215],v[88:91]\n"
RT"a[222:223],v249 offset:4608\n"
MF"v[92:95],a[204:207],v[212:215],v[92:95]\n"
MF"v[96:99],a[192:195],v[216:219],v[96:99]\n"
RD"v[208:211],v243 offset:8192\n"
MF"v[100:103],a[196:199],v[216:219],v[100:103]\n"
MF"v[104:107],a[200:203],v[216:219],v[104:107]\n"
SA"m0,s37,4096\n"
WT"vmcnt(20)\n"
RT"a[224:225],v246 offset:8192\n"
MF"v[108:111],a[204:207],v[216:219],v[108:111]\n"
WT"lgkmcnt(4)\n"
SA"s48,s98,32768\n"
MF"v[112:115],a[192:195],v[220:223],v[112:115]\n"
RD"v[212:215],v243 offset:10240\n"
MF"v[116:119],a[196:199],v[220:223],v[116:119]\n"
WT"lgkmcnt(3)\n"
BL"v234,s[12:15],s48 offen offset:0 lds\n"
MF"v[120:123],a[200:203],v[220:223],v[120:123]\n"
RT"a[226:227],v246 offset:8704\n"
MF"v[124:127],a[204:207],v[220:223],v[124:127]\n"
RD"v[216:219],v243 offset:12288\n"
MF"v[0:3],a[208:211],v[192:195],v[0:3]\n"
BL"v235,s[12:15],s48 offen offset:1024 lds\n"
MF"v[4:7],a[212:215],v[192:195],v[4:7]\n"
MF"v[8:11],a[216:219],v[192:195],v[8:11]\n"
RT"a[228:229],v247 offset:8192\n"
MF"v[12:15],a[220:223],v[192:195],v[12:15]\n"
RD"v[220:223],v243 offset:14336\n"
MF"v[16:19],a[208:211],v[196:199],v[16:19]\n"
MF"v[20:23],a[212:215],v[196:199],v[20:23]\n"
BL"v236,s[12:15],s48 offen offset:2048 lds\n"
MF"v[24:27],a[216:219],v[196:199],v[24:27]\n"
RT"a[230:231],v247 offset:8704\n"
MF"v[28:31],a[220:223],v[196:199],v[28:31]\n"
MF"v[32:35],a[208:211],v[200:203],v[32:35]\n"
MF"v[36:39],a[212:215],v[200:203],v[36:39]\n"
BL"v237,s[12:15],s48 offen offset:3072 lds\n"
MF"v[40:43],a[216:219],v[200:203],v[40:43]\n"
RT"a[232:233],v248 offset:8192\n"
MF"v[44:47],a[220:223],v[200:203],v[44:47]\n"
MF"v[48:51],a[208:211],v[204:207],v[48:51]\n"
MF"v[52:55],a[212:215],v[204:207],v[52:55]\n"
WT"vmcnt(20) lgkmcnt(2)\n"
"s_barrier\n"
MF"v[56:59],a[216:219],v[204:207],v[56:59]\n"
RD"v[192:195],v242 offset:16384\n"
MF"v[60:63],a[220:223],v[204:207],v[60:63]\n"
RT"a[234:235],v248 offset:8704\n"
MF"v[64:67],a[208:211],v[208:211],v[64:67]\n"
MF"v[68:71],a[212:215],v[208:211],v[68:71]\n"
MF"v[72:75],a[216:219],v[208:211],v[72:75]\n"
RD"v[196:199],v242 offset:18432\n"
MF"v[76:79],a[220:223],v[208:211],v[76:79]\n"
RT"a[236:237],v249 offset:8192\n"
MF"v[80:83],a[208:211],v[212:215],v[80:83]\n"
MF"v[84:87],a[212:215],v[212:215],v[84:87]\n"
RD"v[200:203],v242 offset:20480\n"
MF"v[88:91],a[216:219],v[212:215],v[88:91]\n"
RT"a[238:239],v249 offset:8704\n"
MF"v[92:95],a[220:223],v[212:215],v[92:95]\n"
MF"v[96:99],a[208:211],v[216:219],v[96:99]\n"
"s_lshl_b32 s50,s35,20\n"
MF"v[100:103],a[212:215],v[216:219],v[100:103]\n"
"s_lshl_b32 s51,s34,15\n"
RD"v[204:207],v242 offset:22528\n"
MF"v[104:107],a[216:219],v[216:219],v[104:107]\n"
SA"s50,s50,s51\n"
WT"vmcnt(16)\n"
RT"a[240:241],v246 offset:12288\n"
MF"v[108:111],a[220:223],v[216:219],v[108:111]\n"
SA"m0,s37,8192\n"
SA"s48,s39,65536\n"
"s_and_b32 s51,s30,1\n"
MF"v[112:115],a[208:211],v[220:223],v[112:115]\n"
"s_lshl_b32 s51,s51,11\n"
RD"v[208:211],v242 offset:24576\n"
MF"v[116:119],a[212:215],v[220:223],v[116:119]\n"
WT"lgkmcnt(3)\n"
BL"v234,s[12:15],s48 offen offset:0 lds\n"
MF"v[120:123],a[216:219],v[220:223],v[120:123]\n"
SA"s50,s50,s51\n"
RT"a[242:243],v246 offset:12800\n"
SA"s50,s50,9175040\n"
MF"v[124:127],a[220:223],v[220:223],v[124:127]\n"
"v_add_u32 v250,s50,v224\n"
MF"v[0:3],a[224:227],v[192:195],v[0:3]\n"
RD"v[212:215],v242 offset:26624\n"
MF"v[4:7],a[228:231],v[192:195],v[4:7]\n"
BL"v235,s[12:15],s48 offen offset:1024 lds\n"
MF"v[8:11],a[232:235],v[192:195],v[8:11]\n"
RT"a[244:245],v247 offset:12288\n"
MF"v[12:15],a[236:239],v[192:195],v[12:15]\n"
RD"v[216:219],v242 offset:28672\n"
MF"v[16:19],a[224:227],v[196:199],v[16:19]\n"
MF"v[20:23],a[228:231],v[196:199],v[20:23]\n"
BL"v236,s[12:15],s48 offen offset:2048 lds\n"
MF"v[24:27],a[232:235],v[196:199],v[24:27]\n"
RT"a[246:247],v247 offset:12800\n"
MF"v[28:31],a[236:239],v[196:199],v[28:31]\n"
RD"v[220:223],v242 offset:30720\n"
MF"v[32:35],a[224:227],v[200:203],v[32:35]\n"
MF"v[36:39],a[228:231],v[200:203],v[36:39]\n"
BL"v237,s[12:15],s48 offen offset:3072 lds\n"
MF"v[40:43],a[232:235],v[200:203],v[40:43]\n"
RD"v[192:195],v243 offset:16384\n"
MF"v[44:47],a[236:239],v[200:203],v[44:47]\n"
WT"lgkmcnt(3)\n"
RT"a[248:249],v248 offset:12288\n"
MF"v[48:51],a[224:227],v[204:207],v[48:51]\n"
MF"v[52:55],a[228:231],v[204:207],v[52:55]\n"
MF"v[56:59],a[232:235],v[204:207],v[56:59]\n"
RD"v[196:199],v243 offset:18432\n"
MF"v[60:63],a[236:239],v[204:207],v[60:63]\n"
RT"a[250:251],v248 offset:12800\n"
MF"v[64:67],a[224:227],v[208:211],v[64:67]\n"
MF"v[68:71],a[228:231],v[208:211],v[68:71]\n"
RD"v[200:203],v243 offset:20480\n"
MF"v[72:75],a[232:235],v[208:211],v[72:75]\n"
RT"a[252:253],v249 offset:12288\n"
MF"v[76:79],a[236:239],v[208:211],v[76:79]\n"
MF"v[80:83],a[224:227],v[212:215],v[80:83]\n"
MF"v[84:87],a[228:231],v[212:215],v[84:87]\n"
RD"v[204:207],v243 offset:22528\n"
MF"v[88:91],a[232:235],v[212:215],v[88:91]\n"
RT"a[254:255],v249 offset:12800\n"
MF"v[92:95],a[236:239],v[212:215],v[92:95]\n"
"s_lshr_b32 s50,s30,1\n"
MF"v[96:99],a[224:227],v[216:219],v[96:99]\n"
"s_and_b32 s51,s30,1\n"
RD"v[208:211],v243 offset:24576\n"
MF"v[100:103],a[228:231],v[216:219],v[100:103]\n"
"s_mul_i32 s38,s50,24576\n"
MF"v[104:107],a[232:235],v[216:219],v[104:107]\n"
SA"m0,s37,12288\n"
SA"s48,s98,65536\n"
"s_lshl_b32 s60,s50,18\n"
MF"v[108:111],a[236:239],v[216:219],v[108:111]\n"
WT"lgkmcnt(3)\n"
"s_lshl_b32 s50,s51,12\n"
MF"v[112:115],a[224:227],v[220:223],v[112:115]\n"
SA"s38,s38,s50\n"
RD"v[212:215],v243 offset:26624\n"
MF"v[116:119],a[228:231],v[220:223],v[116:119]\n"
WT"lgkmcnt(2)\n"
BL"v234,s[12:15],s48 offen offset:0 lds\n"
MF"v[120:123],a[232:235],v[220:223],v[120:123]\n"
"s_lshl_b32 s50,s51,7\n"
SA"s60,s60,s50\n"
MF"v[124:127],a[236:239],v[220:223],v[124:127]\n"
RD"v[216:219],v243 offset:28672\n"
MF"v[0:3],a[240:243],v[192:195],v[0:3]\n"
BL"v235,s[12:15],s48 offen offset:1024 lds\n"
MF"v[4:7],a[244:247],v[192:195],v[4:7]\n"
"s_sub_u32 s99,s34,5\n"
MF"v[8:11],a[248:251],v[192:195],v[8:11]\n"
"s_cmp_lt_u32 s34,5\n"
"s_cselect_b32 s99,s34,s99\n"
MF"v[12:15],a[252:255],v[192:195],v[12:15]\n"
"s_mul_i32 s99,s99,1\n"
RD"v[220:223],v243 offset:30720\n"
MF"v[16:19],a[240:243],v[196:199],v[16:19]\n"
"s_lshl_b32 s61,s99,20\n"
MF"v[20:23],a[244:247],v[196:199],v[20:23]\n"
BL"v236,s[12:15],s48 offen offset:2048 lds\n"
MF"v[24:27],a[248:251],v[196:199],v[24:27]\n"
MF"v[28:31],a[252:255],v[196:199],v[28:31]\n"
MF"v[32:35],a[240:243],v[200:203],v[32:35]\n"
MF"v[36:39],a[244:247],v[200:203],v[36:39]\n"
BL"v237,s[12:15],s48 offen offset:3072 lds\n"
MF"v[40:43],a[248:251],v[200:203],v[40:43]\n"
"v_add_u32 v244,114688,v242\n"
"v_add_u32 v245,114688,v243\n"
MF"v[44:47],a[252:255],v[200:203],v[44:47]\n"
"v_add_u32 v242,65536,v242\n"
"v_add_u32 v243,65536,v243\n"
MF"v[48:51],a[240:243],v[204:207],v[48:51]\n"
MF"v[52:55],a[244:247],v[204:207],v[52:55]\n"
WT"vmcnt(16) lgkmcnt(0)\n"
"s_barrier\n"
MF"v[56:59],a[248:251],v[204:207],v[56:59]\n"
RD"v[192:195],v242 offset:16384\n"
MF"v[60:63],a[252:255],v[204:207],v[60:63]\n"
MF"v[64:67],a[240:243],v[208:211],v[64:67]\n"
MF"v[68:71],a[244:247],v[208:211],v[68:71]\n"
MF"v[72:75],a[248:251],v[208:211],v[72:75]\n"
RD"v[196:199],v242 offset:18432\n"
MF"v[76:79],a[252:255],v[208:211],v[76:79]\n"
MF"v[80:83],a[240:243],v[212:215],v[80:83]\n"
MF"v[84:87],a[244:247],v[212:215],v[84:87]\n"
MF"v[88:91],a[248:251],v[212:215],v[88:91]\n"
RD"v[200:203],v243 offset:16384\n"
MF"v[92:95],a[252:255],v[212:215],v[92:95]\n"
MF"v[96:99],a[240:243],v[216:219],v[96:99]\n"
MF"v[100:103],a[244:247],v[216:219],v[100:103]\n"
MF"v[104:107],a[248:251],v[216:219],v[104:107]\n"
RD"v[204:207],v243 offset:18432\n"
MF"v[108:111],a[252:255],v[216:219],v[108:111]\n"
MF"v[112:115],a[240:243],v[220:223],v[112:115]\n"
MF"v[116:119],a[244:247],v[220:223],v[116:119]\n"
RD"v[208:211],v242 offset:20480\n"
MF"v[120:123],a[248:251],v[220:223],v[120:123]\n"
MF"v[124:127],a[252:255],v[220:223],v[124:127]\n"
WT"lgkmcnt(2)\n"
MF"v[128:131],a[0:3],v[192:195],0\n"
MF"v[132:135],a[4:7],v[192:195],0\n"
RD"v[212:215],v242 offset:22528\n"
MF"v[136:139],a[8:11],v[192:195],0\n"
MU"v0,s55,v0\n"
MU"v1,s55,v1\n"
MF"v[140:143],a[12:15],v[192:195],0\n"
MU"v2,s55,v2\n"
MU"v3,s55,v3\n"
MF"v[144:147],a[0:3],v[196:199],0\n"
MU"v4,s55,v4\n"
MU"v5,s55,v5\n"
MF"v[148:151],a[4:7],v[196:199],0\n"
RD"v[216:219],v243 offset:20480\n"
MU"v6,s55,v6\n"
MF"v[152:155],a[8:11],v[196:199],0\n"
MU"v7,s55,v7\n"
PK"v0,v0,v1\n"
MF"v[156:159],a[12:15],v[196:199],0\n"
PK"v1,v2,v3\n"
PK"v2,v4,v5\n"
MF"v[128:131],a[16:19],v[200:203],v[128:131]\n"
PK"v3,v6,v7\n"
MF"v[132:135],a[20:23],v[200:203],v[132:135]\n"
RD"v[220:223],v243 offset:22528\n"
MF"v[136:139],a[24:27],v[200:203],v[136:139]\n"
MF"v[140:143],a[28:31],v[200:203],v[140:143]\n"
WT"lgkmcnt(2)\n"
MF"v[144:147],a[16:19],v[204:207],v[144:147]\n"
RD"v[192:195],v242 offset:40960\n"
MU"v8,s55,v8\n"
MF"v[148:151],a[20:23],v[204:207],v[148:151]\n"
MU"v9,s55,v9\n"
MU"v10,s55,v10\n"
MF"v[152:155],a[24:27],v[204:207],v[152:155]\n"
MU"v11,s55,v11\n"
MU"v12,s55,v12\n"
MF"v[156:159],a[28:31],v[204:207],v[156:159]\n"
MU"v13,s55,v13\n"
MU"v14,s55,v14\n"
MF"v[128:131],a[32:35],v[208:211],v[128:131]\n"
RD"v[196:199],v242 offset:43008\n"
MU"v15,s55,v15\n"
MF"v[132:135],a[36:39],v[208:211],v[132:135]\n"
PK"v8,v8,v9\n"
PK"v9,v10,v11\n"
MF"v[136:139],a[40:43],v[208:211],v[136:139]\n"
PK"v10,v12,v13\n"
PK"v11,v14,v15\n"
MF"v[140:143],a[44:47],v[208:211],v[140:143]\n"
MF"v[144:147],a[32:35],v[212:215],v[144:147]\n"
RD"v[200:203],v243 offset:40960\n"
MF"v[148:151],a[36:39],v[212:215],v[148:151]\n"
MF"v[152:155],a[40:43],v[212:215],v[152:155]\n"
MU"v16,s55,v16\n"
MU"v17,s55,v17\n"
MF"v[156:159],a[44:47],v[212:215],v[156:159]\n"
WT"lgkmcnt(1)\n"
MU"v18,s55,v18\n"
MU"v19,s55,v19\n"
MF"v[128:131],a[48:51],v[216:219],v[128:131]\n"
RD"v[204:207],v243 offset:43008\n"
MU"v20,s55,v20\n"
MF"v[132:135],a[52:55],v[216:219],v[132:135]\n"
MU"v21,s55,v21\n"
MU"v22,s55,v22\n"
MF"v[136:139],a[56:59],v[216:219],v[136:139]\n"
SA"m0,s36,0\n"
MU"v23,s55,v23\n"
PK"v16,v16,v17\n"
MF"v[140:143],a[60:63],v[216:219],v[140:143]\n"
SA"s48,s60,s61\n"
RD"v[208:211],v242 offset:45056\n"
PK"v17,v18,v19\n"
MF"v[144:147],a[48:51],v[220:223],v[144:147]\n"
BL"v238,s[20:23],s48 offen offset:0 lds\n"
MF"v[148:151],a[52:55],v[220:223],v[148:151]\n"
PK"v18,v20,v21\n"
PK"v19,v22,v23\n"
MF"v[152:155],a[56:59],v[220:223],v[152:155]\n"
MF"v[156:159],a[60:63],v[220:223],v[156:159]\n"
"s_mov_b32 s49,0\n"
RD"v[212:215],v242 offset:47104\n"
MF"v[128:131],a[64:67],v[192:195],v[128:131]\n"
MU"v24,s55,v24\n"
MU"v25,s55,v25\n"
MF"v[132:135],a[68:71],v[192:195],v[132:135]\n"
BS"v[0:3],v250,s[8:11],s49 offen offset:0 sc1\n"
MF"v[136:139],a[72:75],v[192:195],v[136:139]\n"
MU"v26,s55,v26\n"
MU"v27,s55,v27\n"
MF"v[140:143],a[76:79],v[192:195],v[140:143]\n"
RD"v[216:219],v243 offset:45056\n"
MU"v28,s55,v28\n"
MF"v[144:147],a[64:67],v[196:199],v[144:147]\n"
BL"v239,s[20:23],s48 offen offset:1024 lds\n"
MF"v[148:151],a[68:71],v[196:199],v[148:151]\n"
MU"v29,s55,v29\n"
MU"v30,s55,v30\n"
MF"v[152:155],a[72:75],v[196:199],v[152:155]\n"
MU"v31,s55,v31\n"
PK"v24,v24,v25\n"
MF"v[156:159],a[76:79],v[196:199],v[156:159]\n"
WT"lgkmcnt(1)\n"
RD"v[220:223],v243 offset:47104\n"
PK"v25,v26,v27\n"
MF"v[128:131],a[80:83],v[200:203],v[128:131]\n"
PK"v26,v28,v29\n"
PK"v27,v30,v31\n"
MF"v[132:135],a[84:87],v[200:203],v[132:135]\n"
MF"v[136:139],a[88:91],v[200:203],v[136:139]\n"
RD"v[192:195],v244 offset:16384\n"
MU"v32,s55,v32\n"
MF"v[140:143],a[92:95],v[200:203],v[140:143]\n"
MU"v33,s55,v33\n"
MU"v34,s55,v34\n"
MF"v[144:147],a[80:83],v[204:207],v[144:147]\n"
BL"v240,s[20:23],s48 offen offset:2048 lds\n"
MF"v[148:151],a[84:87],v[204:207],v[148:151]\n"
MU"v35,s55,v35\n"
MU"v36,s55,v36\n"
MF"v[152:155],a[88:91],v[204:207],v[152:155]\n"
RD"v[196:199],v244 offset:18432\n"
MU"v37,s55,v37\n"
MF"v[156:159],a[92:95],v[204:207],v[156:159]\n"
MU"v38,s55,v38\n"
MU"v39,s55,v39\n"
MF"v[128:131],a[96:99],v[208:211],v[128:131]\n"
PK"v32,v32,v33\n"
PK"v33,v34,v35\n"
MF"v[132:135],a[100:103],v[208:211],v[132:135]\n"
PK"v34,v36,v37\n"
PK"v35,v38,v39\n"
MF"v[136:139],a[104:107],v[208:211],v[136:139]\n"
RD"v[200:203],v245 offset:16384\n"
MF"v[140:143],a[108:111],v[208:211],v[140:143]\n"
MF"v[144:147],a[96:99],v[212:215],v[144:147]\n"
SA"s61,s61,524288\n"
BL"v241,s[20:23],s48 offen offset:3072 lds\n"
MF"v[148:151],a[100:103],v[212:215],v[148:151]\n"
MU"v40,s55,v40\n"
MU"v41,s55,v41\n"
MF"v[152:155],a[104:107],v[212:215],v[152:155]\n"
RD"v[204:207],v245 offset:18432\n"
MU"v42,s55,v42\n"
MF"v[156:159],a[108:111],v[212:215],v[156:159]\n"
WT"lgkmcnt(2)\n"
MU"v43,s55,v43\n"
MU"v44,s55,v44\n"
MF"v[128:131],a[112:115],v[216:219],v[128:131]\n"
MU"v45,s55,v45\n"
MU"v46,s55,v46\n"
MF"v[132:135],a[116:119],v[216:219],v[132:135]\n"
RD"v[208:211],v244 offset:20480\n"
MU"v47,s55,v47\n"
MF"v[136:139],a[120:123],v[216:219],v[136:139]\n"
PK"v40,v40,v41\n"
PK"v41,v42,v43\n"
MF"v[140:143],a[124:127],v[216:219],v[140:143]\n"
PK"v42,v44,v45\n"
PK"v43,v46,v47\n"
MF"v[144:147],a[112:115],v[220:223],v[144:147]\n"
MF"v[148:151],a[116:119],v[220:223],v[148:151]\n"
RD"v[212:215],v244 offset:22528\n"
MF"v[152:155],a[120:123],v[220:223],v[152:155]\n"
MU"v48,s55,v48\n"
MU"v49,s55,v49\n"
MF"v[156:159],a[124:127],v[220:223],v[156:159]\n"
MU"v50,s55,v50\n"
MU"v51,s55,v51\n"
MF"v[128:131],a[128:131],v[192:195],v[128:131]\n"
BS"v[8:11],v250,s[8:11],s49 offen offset:1024 sc1\n"
MF"v[132:135],a[132:135],v[192:195],v[132:135]\n"
RD"v[216:219],v245 offset:20480\n"
MU"v52,s55,v52\n"
MF"v[136:139],a[136:139],v[192:195],v[136:139]\n"
SA"m0,s36,16384\n"
MU"v53,s55,v53\n"
MU"v54,s55,v54\n"
MF"v[140:143],a[140:143],v[192:195],v[140:143]\n"
SA"s48,s60,s61\n"
MU"v55,s55,v55\n"
PK"v48,v48,v49\n"
MF"v[144:147],a[128:131],v[196:199],v[144:147]\n"
BL"v238,s[20:23],s48 offen offset:0 lds\n"
MF"v[148:151],a[132:135],v[196:199],v[148:151]\n"
RD"v[220:223],v245 offset:22528\n"
PK"v49,v50,v51\n"
MF"v[152:155],a[136:139],v[196:199],v[152:155]\n"
PK"v50,v52,v53\n"
PK"v51,v54,v55\n"
MF"v[156:159],a[140:143],v[196:199],v[156:159]\n"
WT"lgkmcnt(2)\n"
MF"v[128:131],a[144:147],v[200:203],v[128:131]\n"
RD"v[192:195],v244 offset:40960\n"
MU"v56,s55,v56\n"
MF"v[132:135],a[148:151],v[200:203],v[132:135]\n"
MU"v57,s55,v57\n"
MU"v58,s55,v58\n"
MF"v[136:139],a[152:155],v[200:203],v[136:139]\n"
MU"v59,s55,v59\n"
MU"v60,s55,v60\n"
MF"v[140:143],a[156:159],v[200:203],v[140:143]\n"
MU"v61,s55,v61\n"
MU"v62,s55,v62\n"
MF"v[144:147],a[144:147],v[204:207],v[144:147]\n"
RD"v[196:199],v244 offset:43008\n"
MU"v63,s55,v63\n"
MF"v[148:151],a[148:151],v[204:207],v[148:151]\n"
BL"v239,s[20:23],s48 offen offset:1024 lds\n"
MF"v[152:155],a[152:155],v[204:207],v[152:155]\n"
PK"v56,v56,v57\n"
PK"v57,v58,v59\n"
MF"v[156:159],a[156:159],v[204:207],v[156:159]\n"
PK"v58,v60,v61\n"
PK"v59,v62,v63\n"
MF"v[128:131],a[160:163],v[208:211],v[128:131]\n"
RD"v[200:203],v245 offset:40960\n"
MF"v[132:135],a[164:167],v[208:211],v[132:135]\n"
MF"v[136:139],a[168:171],v[208:211],v[136:139]\n"
MU"v64,s55,v64\n"
MU"v65,s55,v65\n"
MF"v[140:143],a[172:175],v[208:211],v[140:143]\n"
MU"v66,s55,v66\n"
MU"v67,s55,v67\n"
MF"v[144:147],a[160:163],v[212:215],v[144:147]\n"
RD"v[204:207],v245 offset:43008\n"
MU"v68,s55,v68\n"
MF"v[148:151],a[164:167],v[212:215],v[148:151]\n"
BL"v240,s[20:23],s48 offen offset:2048 lds\n"
MF"v[152:155],a[168:171],v[212:215],v[152:155]\n"
MU"v69,s55,v69\n"
MU"v70,s55,v70\n"
MF"v[156:159],a[172:175],v[212:215],v[156:159]\n"
WT"lgkmcnt(1)\n"
RD"v[208:211],v244 offset:45056\n"
MU"v71,s55,v71\n"
MF"v[128:131],a[176:179],v[216:219],v[128:131]\n"
PK"v64,v64,v65\n"
PK"v65,v66,v67\n"
MF"v[132:135],a[180:183],v[216:219],v[132:135]\n"
PK"v66,v68,v69\n"
PK"v67,v70,v71\n"
MF"v[136:139],a[184:187],v[216:219],v[136:139]\n"
MF"v[140:143],a[188:191],v[216:219],v[140:143]\n"
RD"v[212:215],v244 offset:47104\n"
MF"v[144:147],a[176:179],v[220:223],v[144:147]\n"
SA"s61,s61,524288\n"
BL"v241,s[20:23],s48 offen offset:3072 lds\n"
MF"v[148:151],a[180:183],v[220:223],v[148:151]\n"
"s_mov_b32 s49,4096\n"
MU"v72,s55,v72\n"
MU"v73,s55,v73\n"
MF"v[152:155],a[184:187],v[220:223],v[152:155]\n"
"s_cmp_eq_u32 s61,5242880\n"
MU"v74,s55,v74\n"
MU"v75,s55,v75\n"
MF"v[156:159],a[188:191],v[220:223],v[156:159]\n"
"s_cselect_b32 s61,0,s61\n"
RD"v[216:219],v245 offset:45056\n"
MU"v76,s55,v76\n"
MF"v[128:131],a[192:195],v[192:195],v[128:131]\n"
BS"v[16:19],v250,s[8:11],s49 offen offset:0 sc1\n"
MF"v[132:135],a[196:199],v[192:195],v[132:135]\n"
MU"v77,s55,v77\n"
MU"v78,s55,v78\n"
MF"v[136:139],a[200:203],v[192:195],v[136:139]\n"
MU"v79,s55,v79\n"
PK"v72,v72,v73\n"
MF"v[140:143],a[204:207],v[192:195],v[140:143]\n"
RD"v[220:223],v245 offset:47104\n"
PK"v73,v74,v75\n"
MF"v[144:147],a[192:195],v[196:199],v[144:147]\n"
PK"v74,v76,v77\n"
PK"v75,v78,v79\n"
MF"v[148:151],a[196:199],v[196:199],v[148:151]\n"
MF"v[152:155],a[200:203],v[196:199],v[152:155]\n"
MU"v80,s55,v80\n"
MU"v81,s55,v81\n"
MF"v[156:159],a[204:207],v[196:199],v[156:159]\n"
MU"v82,s55,v82\n"
MU"v83,s55,v83\n"
MF"v[128:131],a[208:211],v[200:203],v[128:131]\n"
MU"v84,s55,v84\n"
MU"v85,s55,v85\n"
MF"v[132:135],a[212:215],v[200:203],v[132:135]\n"
MU"v86,s55,v86\n"
MU"v87,s55,v87\n"
MF"v[136:139],a[216:219],v[200:203],v[136:139]\n"
PK"v80,v80,v81\n"
PK"v81,v82,v83\n"
MF"v[140:143],a[220:223],v[200:203],v[140:143]\n"
WT"lgkmcnt(0)\n"
PK"v82,v84,v85\n"
PK"v83,v86,v87\n"
MF"v[144:147],a[208:211],v[204:207],v[144:147]\n"
MF"v[148:151],a[212:215],v[204:207],v[148:151]\n"
WT"vmcnt(19)\n"
"s_barrier\n"
MF"v[152:155],a[216:219],v[204:207],v[152:155]\n"
SA"m0,s37,16384\n"
RD"v[192:195],v242 offset:0\n"
SA"s48,s39,98304\n"
MF"v[156:159],a[220:223],v[204:207],v[156:159]\n"
MF"v[128:131],a[224:227],v[208:211],v[128:131]\n"
BL"v234,s[12:15],s48 offen offset:0 lds\n"
MF"v[132:135],a[228:231],v[208:211],v[132:135]\n"
MU"v88,s55,v88\n"
MU"v89,s55,v89\n"
MF"v[136:139],a[232:235],v[208:211],v[136:139]\n"
RD"v[196:199],v242 offset:2048\n"
MU"v90,s55,v90\n"
MF"v[140:143],a[236:239],v[208:211],v[140:143]\n"
MU"v91,s55,v91\n"
MU"v92,s55,v92\n"
MF"v[144:147],a[224:227],v[212:215],v[144:147]\n"
MU"v93,s55,v93\n"
MU"v94,s55,v94\n"
MF"v[148:151],a[228:231],v[212:215],v[148:151]\n"
MU"v95,s55,v95\n"
PK"v88,v88,v89\n"
MF"v[152:155],a[232:235],v[212:215],v[152:155]\n"
RD"v[200:203],v243 offset:0\n"
PK"v89,v90,v91\n"
MF"v[156:159],a[236:239],v[212:215],v[156:159]\n"
BL"v235,s[12:15],s48 offen offset:1024 lds\n"
MF"v[128:131],a[240:243],v[216:219],v[128:131]\n"
PK"v90,v92,v93\n"
PK"v91,v94,v95\n"
MF"v[132:135],a[244:247],v[216:219],v[132:135]\n"
MF"v[136:139],a[248:251],v[216:219],v[136:139]\n"
RD"v[204:207],v243 offset:2048\n"
MF"v[140:143],a[252:255],v[216:219],v[140:143]\n"
MF"v[144:147],a[240:243],v[220:223],v[144:147]\n"
BL"v236,s[12:15],s48 offen offset:2048 lds\n"
MF"v[148:151],a[244:247],v[220:223],v[148:151]\n"
RD"v[208:211],v242 offset:4096\n"
MF"v[152:155],a[248:251],v[220:223],v[152:155]\n"
BS"v[24:27],v250,s[8:11],s49 offen offset:1024 sc1\n"
MF"v[156:159],a[252:255],v[220:223],v[156:159]\n"
WT"lgkmcnt(2)\n"
MF"v[36:39],a[0:3],v[192:195],0\n"
MF"v[68:71],a[4:7],v[192:195],0\n"
RD"v[212:215],v242 offset:6144\n"
MF"v[44:47],a[8:11],v[192:195],0\n"
BL"v237,s[12:15],s48 offen offset:3072 lds\n"
MF"v[76:79],a[12:15],v[192:195],0\n"
MU"v128,s55,v128\n"
MU"v129,s55,v129\n"
MF"v[52:55],a[0:3],v[196:199],0\n"
MU"v130,s55,v130\n"
MU"v131,s55,v131\n"
MF"v[84:87],a[4:7],v[196:199],0\n"
RD"v[216:219],v243 offset:4096\n"
MU"v132,s55,v132\n"
MF"v[60:63],a[8:11],v[196:199],0\n"
SA"m0,s37,20480\n"
MU"v133,s55,v133\n"
MU"v134,s55,v134\n"
MF"v[92:95],a[12:15],v[196:199],0\n"
SA"s48,s98,98304\n"
MU"v135,s55,v135\n"
PK"v4,v128,v129\n"
MF"v[36:39],a[16:19],v[200:203],v[36:39]\n"
BL"v234,s[12:15],s48 offen offset:0 lds\n"
MF"v[68:71],a[20:23],v[200:203],v[68:71]\n"
RD"v[220:223],v243 offset:6144\n"
PK"v5,v130,v131\n"
MF"v[44:47],a[24:27],v[200:203],v[44:47]\n"
PK"v6,v132,v133\n"
PK"v7,v134,v135\n"
MF"v[76:79],a[28:31],v[200:203],v[76:79]\n"
WT"lgkmcnt(2)\n"
MF"v[52:55],a[16:19],v[204:207],v[52:55]\n"
RD"v[192:195],v242 offset:24576\n"
MF"v[84:87],a[20:23],v[204:207],v[84:87]\n"
MU"v136,s55,v136\n"
MU"v137,s55,v137\n"
MF"v[60:63],a[24:27],v[204:207],v[60:63]\n"
BL"v235,s[12:15],s48 offen offset:1024 lds\n"
MF"v[92:95],a[28:31],v[204:207],v[92:95]\n"
MU"v138,s55,v138\n"
MU"v139,s55,v139\n"
MF"v[36:39],a[32:35],v[208:211],v[36:39]\n"
RD"v[196:199],v242 offset:26624\n"
MU"v140,s55,v140\n"
MF"v[68:71],a[36:39],v[208:211],v[68:71]\n"
MU"v141,s55,v141\n"
MU"v142,s55,v142\n"
MF"v[44:47],a[40:43],v[208:211],v[44:47]\n"
MU"v143,s55,v143\n"
PK"v12,v136,v137\n"
MF"v[76:79],a[44:47],v[208:211],v[76:79]\n"
PK"v13,v138,v139\n"
PK"v14,v140,v141\n"
MF"v[52:55],a[32:35],v[212:215],v[52:55]\n"
RD"v[200:203],v243 offset:24576\n"
PK"v15,v142,v143\n"
MF"v[84:87],a[36:39],v[212:215],v[84:87]\n"
BL"v236,s[12:15],s48 offen offset:2048 lds\n"
MF"v[60:63],a[40:43],v[212:215],v[60:63]\n"
MF"v[92:95],a[44:47],v[212:215],v[92:95]\n"
WT"lgkmcnt(1)\n"
MU"v144,s55,v144\n"
MU"v145,s55,v145\n"
MF"v[36:39],a[48:51],v[216:219],v[36:39]\n"
RD"v[204:207],v243 offset:26624\n"
MU"v146,s55,v146\n"
MF"v[68:71],a[52:55],v[216:219],v[68:71]\n"
MU"v147,s55,v147\n"
MU"v148,s55,v148\n"
MF"v[44:47],a[56:59],v[216:219],v[44:47]\n"
BL"v237,s[12:15],s48 offen offset:3072 lds\n"
MF"v[76:79],a[60:63],v[216:219],v[76:79]\n"
"s_mov_b32 s49,8192\n"
RD"v[208:211],v242 offset:28672\n"
MU"v149,s55,v149\n"
MF"v[52:55],a[48:51],v[220:223],v[52:55]\n"
MU"v150,s55,v150\n"
MU"v151,s55,v151\n"
MF"v[84:87],a[52:55],v[220:223],v[84:87]\n"
BS"v[32:35],v250,s[8:11],s49 offen offset:0 sc1\n"
MF"v[60:63],a[56:59],v[220:223],v[60:63]\n"
PK"v20,v144,v145\n"
PK"v21,v146,v147\n"
MF"v[92:95],a[60:63],v[220:223],v[92:95]\n"
RD"v[212:215],v242 offset:30720\n"
PK"v22,v148,v149\n"
MF"v[36:39],a[64:67],v[192:195],v[36:39]\n"
PK"v23,v150,v151\n"
MF"v[68:71],a[68:71],v[192:195],v[68:71]\n"
MU"v152,s55,v152\n"
MU"v153,s55,v153\n"
MF"v[44:47],a[72:75],v[192:195],v[44:47]\n"
MU"v154,s55,v154\n"
MU"v155,s55,v155\n"
MF"v[76:79],a[76:79],v[192:195],v[76:79]\n"
RD"v[216:219],v243 offset:28672\n"
MU"v156,s55,v156\n"
MF"v[52:55],a[64:67],v[196:199],v[52:55]\n"
MU"v157,s55,v157\n"
MU"v158,s55,v158\n"
MF"v[84:87],a[68:71],v[196:199],v[84:87]\n"
MU"v159,s55,v159\n"
PK"v28,v152,v153\n"
MF"v[60:63],a[72:75],v[196:199],v[60:63]\n"
PK"v29,v154,v155\n"
PK"v30,v156,v157\n"
MF"v[92:95],a[76:79],v[196:199],v[92:95]\n"
WT"lgkmcnt(1)\n"
RD"v[220:223],v243 offset:30720\n"
PK"v31,v158,v159\n"
MF"v[36:39],a[80:83],v[200:203],v[36:39]\n"
MF"v[68:71],a[84:87],v[200:203],v[68:71]\n"
MF"v[44:47],a[88:91],v[200:203],v[44:47]\n"
RD"v[192:195],v244 offset:0\n"
MF"v[76:79],a[92:95],v[200:203],v[76:79]\n"
MF"v[52:55],a[80:83],v[204:207],v[52:55]\n"
MF"v[84:87],a[84:87],v[204:207],v[84:87]\n"
MF"v[60:63],a[88:91],v[204:207],v[60:63]\n"
RD"v[196:199],v244 offset:2048\n"
MF"v[92:95],a[92:95],v[204:207],v[92:95]\n"
MF"v[36:39],a[96:99],v[208:211],v[36:39]\n"
MF"v[68:71],a[100:103],v[208:211],v[68:71]\n"
MF"v[44:47],a[104:107],v[208:211],v[44:47]\n"
RD"v[200:203],v245 offset:0\n"
MF"v[76:79],a[108:111],v[208:211],v[76:79]\n"
MF"v[52:55],a[96:99],v[212:215],v[52:55]\n"
MF"v[84:87],a[100:103],v[212:215],v[84:87]\n"
MF"v[60:63],a[104:107],v[212:215],v[60:63]\n"
RD"v[204:207],v245 offset:2048\n"
MF"v[92:95],a[108:111],v[212:215],v[92:95]\n"
WT"lgkmcnt(2)\n"
MF"v[36:39],a[112:115],v[216:219],v[36:39]\n"
MF"v[68:71],a[116:119],v[216:219],v[68:71]\n"
RD"v[208:211],v244 offset:4096\n"
MF"v[44:47],a[120:123],v[216:219],v[44:47]\n"
SA"m0,s36,49152\n"
SA"s48,s60,s61\n"
MF"v[76:79],a[124:127],v[216:219],v[76:79]\n"
MF"v[52:55],a[112:115],v[220:223],v[52:55]\n"
BS"v[40:43],v250,s[8:11],s49 offen offset:1024 sc1\n"
MF"v[84:87],a[116:119],v[220:223],v[84:87]\n"
BL"v238,s[20:23],s48 offen offset:0 lds\n"
MF"v[60:63],a[120:123],v[220:223],v[60:63]\n"
RD"v[212:215],v244 offset:6144\n"
MU"v96,s55,v96\n"
MF"v[92:95],a[124:127],v[220:223],v[92:95]\n"
MU"v97,s55,v97\n"
MU"v98,s55,v98\n"
MF"v[36:39],a[128:131],v[192:195],v[36:39]\n"
MU"v99,s55,v99\n"
MU"v100,s55,v100\n"
MF"v[68:71],a[132:135],v[192:195],v[68:71]\n"
RD"v[216:219],v245 offset:4096\n"
MU"v101,s55,v101\n"
MF"v[44:47],a[136:139],v[192:195],v[44:47]\n"
MU"v102,s55,v102\n"
MU"v103,s55,v103\n"
MF"v[76:79],a[140:143],v[192:195],v[76:79]\n"
PK"v96,v96,v97\n"
PK"v97,v98,v99\n"
MF"v[52:55],a[128:131],v[196:199],v[52:55]\n"
BL"v239,s[20:23],s48 offen offset:1024 lds\n"
MF"v[84:87],a[132:135],v[196:199],v[84:87]\n"
RD"v[220:223],v245 offset:6144\n"
PK"v98,v100,v101\n"
MF"v[60:63],a[136:139],v[196:199],v[60:63]\n"
PK"v99,v102,v103\n"
MU"v104,s55,v104\n"
MF"v[92:95],a[140:143],v[196:199],v[92:95]\n"
WT"lgkmcnt(2)\n"
MU"v105,s55,v105\n"
MU"v106,s55,v106\n"
MF"v[36:39],a[144:147],v[200:203],v[36:39]\n"
RD"v[192:195],v244 offset:24576\n"
MU"v107,s55,v107\n"
MF"v[68:71],a[148:151],v[200:203],v[68:71]\n"
MU"v108,s55,v108\n"
MU"v109,s55,v109\n"
MF"v[44:47],a[152:155],v[200:203],v[44:47]\n"
MU"v110,s55,v110\n"
MU"v111,s55,v111\n"
MF"v[76:79],a[156:159],v[200:203],v[76:79]\n"
PK"v104,v104,v105\n"
PK"v105,v106,v107\n"
MF"v[52:55],a[144:147],v[204:207],v[52:55]\n"
RD"v[196:199],v244 offset:26624\n"
PK"v106,v108,v109\n"
MF"v[84:87],a[148:151],v[204:207],v[84:87]\n"
BL"v240,s[20:23],s48 offen offset:2048 lds\n"
MF"v[60:63],a[152:155],v[204:207],v[60:63]\n"
PK"v107,v110,v111\n"
MF"v[92:95],a[156:159],v[204:207],v[92:95]\n"
MF"v[36:39],a[160:163],v[208:211],v[36:39]\n"
RD"v[200:203],v245 offset:24576\n"
MU"v112,s55,v112\n"
MF"v[68:71],a[164:167],v[208:211],v[68:71]\n"
MU"v113,s55,v113\n"
MU"v114,s55,v114\n"
MF"v[44:47],a[168:171],v[208:211],v[44:47]\n"
MU"v115,s55,v115\n"
MU"v116,s55,v116\n"
MF"v[76:79],a[172:175],v[208:211],v[76:79]\n"
MU"v117,s55,v117\n"
MU"v118,s55,v118\n"
MF"v[52:55],a[160:163],v[212:215],v[52:55]\n"
SA"s61,s61,524288\n"
RD"v[204:207],v245 offset:26624\n"
MU"v119,s55,v119\n"
MF"v[84:87],a[164:167],v[212:215],v[84:87]\n"
BL"v241,s[20:23],s48 offen offset:3072 lds\n"
MF"v[60:63],a[168:171],v[212:215],v[60:63]\n"
PK"v112,v112,v113\n"
PK"v113,v114,v115\n"
MF"v[92:95],a[172:175],v[212:215],v[92:95]\n"
WT"lgkmcnt(1)\n"
RD"v[208:211],v244 offset:28672\n"
PK"v114,v116,v117\n"
MF"v[36:39],a[176:179],v[216:219],v[36:39]\n"
PK"v115,v118,v119\n"
MF"v[68:71],a[180:183],v[216:219],v[68:71]\n"
"s_mov_b32 s49,12288\n"
MF"v[44:47],a[184:187],v[216:219],v[44:47]\n"
MU"v120,s55,v120\n"
MU"v121,s55,v121\n"
MF"v[76:79],a[188:191],v[216:219],v[76:79]\n"
RD"v[212:215],v244 offset:30720\n"
MU"v122,s55,v122\n"
MF"v[52:55],a[176:179],v[220:223],v[52:55]\n"
BS"v[48:51],v250,s[8:11],s49 offen offset:0 sc1\n"
MF"v[84:87],a[180:183],v[220:223],v[84:87]\n"
MU"v123,s55,v123\n"
MU"v124,s55,v124\n"
MF"v[60:63],a[184:187],v[220:223],v[60:63]\n"
MU"v125,s55,v125\n"
MU"v126,s55,v126\n"
MF"v[92:95],a[188:191],v[220:223],v[92:95]\n"
RD"v[216:219],v245 offset:28672\n"
MU"v127,s55,v127\n"
MF"v[36:39],a[192:195],v[192:195],v[36:39]\n"
PK"v120,v120,v121\n"
PK"v121,v122,v123\n"
MF"v[68:71],a[196:199],v[192:195],v[68:71]\n"
PK"v122,v124,v125\n"
PK"v123,v126,v127\n"
MF"v[44:47],a[200:203],v[192:195],v[44:47]\n"
MF"v[76:79],a[204:207],v[192:195],v[76:79]\n"
RD"v[220:223],v245 offset:30720\n"
MF"v[52:55],a[192:195],v[196:199],v[52:55]\n"
MF"v[84:87],a[196:199],v[196:199],v[84:87]\n"
MF"v[60:63],a[200:203],v[196:199],v[60:63]\n"
MF"v[92:95],a[204:207],v[196:199],v[92:95]\n"
MF"v[36:39],a[208:211],v[200:203],v[36:39]\n"
MF"v[68:71],a[212:215],v[200:203],v[68:71]\n"
MF"v[44:47],a[216:219],v[200:203],v[44:47]\n"
MF"v[76:79],a[220:223],v[200:203],v[76:79]\n"
WT"lgkmcnt(0)\n"
"v_lshrrev_b32 v251,2,v224\n"
MF"v[52:55],a[208:211],v[204:207],v[52:55]\n"
"v_and_b32 v251,28,v251\n"
MF"v[84:87],a[212:215],v[204:207],v[84:87]\n"
WT"vmcnt(27)\n"
"s_barrier\n"
MF"v[60:63],a[216:219],v[204:207],v[60:63]\n"
RD"v[192:195],v242 offset:8192\n"
MF"v[92:95],a[220:223],v[204:207],v[92:95]\n"
"ds_read_b32 v234,v251 offset:40960\n"
MF"v[36:39],a[224:227],v[208:211],v[36:39]\n"
MF"v[68:71],a[228:231],v[208:211],v[68:71]\n"
MF"v[44:47],a[232:235],v[208:211],v[44:47]\n"
RD"v[196:199],v242 offset:10240\n"
MF"v[76:79],a[236:239],v[208:211],v[76:79]\n"
MF"v[52:55],a[224:227],v[212:215],v[52:55]\n"
MF"v[84:87],a[228:231],v[212:215],v[84:87]\n"
MF"v[60:63],a[232:235],v[212:215],v[60:63]\n"
RD"v[200:203],v243 offset:8192\n"
MF"v[92:95],a[236:239],v[212:215],v[92:95]\n"
WT"lgkmcnt(2)\n"
"v_not_b32 v251,v234\n"
"v_bfe_u32 v252,v251,4,7\n"
MF"v[36:39],a[240:243],v[216:219],v[36:39]\n"
"v_mul_u32_u24 v252,20480,v252\n"
MF"v[68:71],a[244:247],v[216:219],v[68:71]\n"
"v_and_b32 v251,15,v251\n"
MF"v[44:47],a[248:251],v[216:219],v[44:47]\n"
RD"v[204:207],v243 offset:10240\n"
"v_lshl_add_u32 v251,v251,4,v252\n"
MF"v[76:79],a[252:255],v[216:219],v[76:79]\n"
BS"v[56:59],v250,s[8:11],s49 offen offset:1024 sc1\n"
MF"v[52:55],a[240:243],v[220:223],v[52:55]\n"
"v_and_b32 v252,0x380,v224\n"
"v_lshl_add_u32 v251,v252,1,v251\n"
MF"v[84:87],a[244:247],v[220:223],v[84:87]\n"
RD"v[208:211],v242 offset:12288\n"
"v_add_u32 v252,v252,v224\n"
MF"v[60:63],a[248:251],v[220:223],v[60:63]\n"
"s_lshl_b32 s92,s34,2\n"
MF"v[92:95],a[252:255],v[220:223],v[92:95]\n"
SA"s92,s92,s30\n"
MF"v[128:131],a[0:3],v[192:195],0\n"
SA"m0,s38,65536\n"
SA"s48,s60,s61\n"
"s_lshl_b32 s93,s92,11\n"
MF"v[132:135],a[4:7],v[192:195],0\n"
"s_cmp_lt_u32 s92,10\n"
RD"v[212:215],v242 offset:14336\n"
"s_cselect_b32 s93,s93,2130706432\n"
MF"v[136:139],a[8:11],v[192:195],0\n"
SA"s92,s93,65536\n"
BL"v238,s[20:23],s48 offen offset:0 lds\n"
MF"v[140:143],a[12:15],v[192:195],0\n"
WT"lgkmcnt(2)\n"
BL"v[100:103],v251,s[8:11],s92 offen sc1\n"
MF"v[144:147],a[0:3],v[196:199],0\n"
"s_mul_i32 s92,s35,20480\n"
MU"v36,s55,v36\n"
MU"v37,s55,v37\n"
MF"v[148:151],a[4:7],v[196:199],0\n"
SA"s92,s92,s93\n"
RD"v[216:219],v243 offset:12288\n"
MU"v38,s55,v38\n"
MF"v[152:155],a[8:11],v[196:199],0\n"
SA"s92,s92,2770944\n"
MU"v39,s55,v39\n"
MU"v68,s55,v68\n"
MF"v[156:159],a[12:15],v[196:199],0\n"
MU"v69,s55,v69\n"
MU"v70,s55,v70\n"
MF"v[128:131],a[16:19],v[200:203],v[128:131]\n"
MU"v71,s55,v71\n"
PK"v36,v36,v37\n"
MF"v[132:135],a[20:23],v[200:203],v[132:135]\n"
RD"v[220:223],v243 offset:14336\n"
PK"v37,v38,v39\n"
MF"v[136:139],a[24:27],v[200:203],v[136:139]\n"
BL"v239,s[20:23],s48 offen offset:1024 lds\n"
MF"v[140:143],a[28:31],v[200:203],v[140:143]\n"
PK"v38,v68,v69\n"
PK"v39,v70,v71\n"
MF"v[144:147],a[16:19],v[204:207],v[144:147]\n"
RD"v[192:195],v242 offset:32768\n"
MF"v[148:151],a[20:23],v[204:207],v[148:151]\n"
MU"v44,s55,v44\n"
MU"v45,s55,v45\n"
MF"v[152:155],a[24:27],v[204:207],v[152:155]\n"
MU"v46,s55,v46\n"
MU"v47,s55,v47\n"
MF"v[156:159],a[28:31],v[204:207],v[156:159]\n"
WT"lgkmcnt(2)\n"
MU"v76,s55,v76\n"
MU"v77,s55,v77\n"
MF"v[128:131],a[32:35],v[208:211],v[128:131]\n"
RD"v[196:199],v242 offset:34816\n"
MU"v78,s55,v78\n"
MF"v[132:135],a[36:39],v[208:211],v[132:135]\n"
MU"v79,s55,v79\n"
PK"v44,v44,v45\n"
MF"v[136:139],a[40:43],v[208:211],v[136:139]\n"
BL"v240,s[20:23],s48 offen offset:2048 lds\n"
MF"v[140:143],a[44:47],v[208:211],v[140:143]\n"
PK"v45,v46,v47\n"
PK"v46,v76,v77\n"
MF"v[144:147],a[32:35],v[212:215],v[144:147]\n"
RD"v[200:203],v243 offset:32768\n"
PK"v47,v78,v79\n"
MF"v[148:151],a[36:39],v[212:215],v[148:151]\n"
MF"v[152:155],a[40:43],v[212:215],v[152:155]\n"
MF"v[156:159],a[44:47],v[212:215],v[156:159]\n"
"s_mov_b32 s49,16384\n"
MU"v52,s55,v52\n"
MU"v53,s55,v53\n"
MF"v[128:131],a[48:51],v[216:219],v[128:131]\n"
RD"v[204:207],v243 offset:34816\n"
MU"v54,s55,v54\n"
MF"v[132:135],a[52:55],v[216:219],v[132:135]\n"
BS"v[64:67],v250,s[8:11],s49 offen offset:0 sc1\n"
MF"v[136:139],a[56:59],v[216:219],v[136:139]\n"
SA"s61,s61,524288\n"
BL"v241,s[20:23],s48 offen offset:3072 lds\n"
MF"v[140:143],a[60:63],v[216:219],v[140:143]\n"
WT"lgkmcnt(1)\n"
RD"v[208:211],v242 offset:36864\n"
MU"v55,s55,v55\n"
MF"v[144:147],a[48:51],v[220:223],v[144:147]\n"
"s_cmp_eq_u32 s61,5242880\n"
MU"v84,s55,v84\n"
MU"v85,s55,v85\n"
MF"v[148:151],a[52:55],v[220:223],v[148:151]\n"
"s_cselect_b32 s61,0,s61\n"
MU"v86,s55,v86\n"
MU"v87,s55,v87\n"
MF"v[152:155],a[56:59],v[220:223],v[152:155]\n"
PK"v52,v52,v53\n"
PK"v53,v54,v55\n"
MF"v[156:159],a[60:63],v[220:223],v[156:159]\n"
RD"v[212:215],v242 offset:38912\n"
PK"v54,v84,v85\n"
MF"v[128:131],a[64:67],v[192:195],v[128:131]\n"
PK"v55,v86,v87\n"
MF"v[132:135],a[68:71],v[192:195],v[132:135]\n"
MU"v60,s55,v60\n"
MU"v61,s55,v61\n"
MF"v[136:139],a[72:75],v[192:195],v[136:139]\n"
MU"v62,s55,v62\n"
MU"v63,s55,v63\n"
MF"v[140:143],a[76:79],v[192:195],v[140:143]\n"
RD"v[216:219],v243 offset:36864\n"
MU"v92,s55,v92\n"
MF"v[144:147],a[64:67],v[196:199],v[144:147]\n"
MU"v93,s55,v93\n"
MU"v94,s55,v94\n"
MF"v[148:151],a[68:71],v[196:199],v[148:151]\n"
MU"v95,s55,v95\n"
PK"v60,v60,v61\n"
MF"v[152:155],a[72:75],v[196:199],v[152:155]\n"
PK"v61,v62,v63\n"
PK"v62,v92,v93\n"
MF"v[156:159],a[76:79],v[196:199],v[156:159]\n"
RD"v[220:223],v243 offset:38912\n"
PK"v63,v94,v95\n"
MF"v[128:131],a[80:83],v[200:203],v[128:131]\n"
SA"m0,s38,114688\n"
SA"s48,s60,s61\n"
MF"v[132:135],a[84:87],v[200:203],v[132:135]\n"
MF"v[136:139],a[88:91],v[200:203],v[136:139]\n"
RD"v[192:195],v244 offset:8192\n"
MF"v[140:143],a[92:95],v[200:203],v[140:143]\n"
WT"lgkmcnt(2)\n"
BL"v238,s[20:23],s48 offen offset:0 lds\n"
MF"v[144:147],a[80:83],v[204:207],v[144:147]\n"
MF"v[148:151],a[84:87],v[204:207],v[148:151]\n"
MF"v[152:155],a[88:91],v[204:207],v[152:155]\n"
RD"v[196:199],v244 offset:10240\n"
MF"v[156:159],a[92:95],v[204:207],v[156:159]\n"
MF"v[128:131],a[96:99],v[208:211],v[128:131]\n"
MF"v[132:135],a[100:103],v[208:211],v[132:135]\n"
MF"v[136:139],a[104:107],v[208:211],v[136:139]\n"
RD"v[200:203],v245 offset:8192\n"
MF"v[140:143],a[108:111],v[208:211],v[140:143]\n"
BL"v239,s[20:23],s48 offen offset:1024 lds\n"
MF"v[144:147],a[96:99],v[212:215],v[144:147]\n"
MF"v[148:151],a[100:103],v[212:215],v[148:151]\n"
MF"v[152:155],a[104:107],v[212:215],v[152:155]\n"
RD"v[204:207],v245 offset:10240\n"
MF"v[156:159],a[108:111],v[212:215],v[156:159]\n"
MF"v[128:131],a[112:115],v[216:219],v[128:131]\n"
BS"v[72:75],v250,s[8:11],s49 offen offset:1024 sc1\n"
MF"v[132:135],a[116:119],v[216:219],v[132:135]\n"
RD"v[208:211],v244 offset:12288\n"
MF"v[136:139],a[120:123],v[216:219],v[136:139]\n"
BL"v240,s[20:23],s48 offen offset:2048 lds\n"
MF"v[140:143],a[124:127],v[216:219],v[140:143]\n"
WT"lgkmcnt(2)\n"
MF"v[144:147],a[112:115],v[220:223],v[144:147]\n"
MF"v[148:151],a[116:119],v[220:223],v[148:151]\n"
RD"v[212:215],v244 offset:14336\n"
MF"v[152:155],a[120:123],v[220:223],v[152:155]\n"
MF"v[156:159],a[124:127],v[220:223],v[156:159]\n"
MF"v[128:131],a[128:131],v[192:195],v[128:131]\n"
MF"v[132:135],a[132:135],v[192:195],v[132:135]\n"
RD"v[216:219],v245 offset:12288\n"
MF"v[136:139],a[136:139],v[192:195],v[136:139]\n"
SA"s61,s61,524288\n"
BL"v241,s[20:23],s48 offen offset:3072 lds\n"
MF"v[140:143],a[140:143],v[192:195],v[140:143]\n"
MF"v[144:147],a[128:131],v[196:199],v[144:147]\n"
MF"v[148:151],a[132:135],v[196:199],v[148:151]\n"
RD"v[220:223],v245 offset:14336\n"
MF"v[152:155],a[136:139],v[196:199],v[152:155]\n"
MF"v[156:159],a[140:143],v[196:199],v[156:159]\n"
MF"v[128:131],a[144:147],v[200:203],v[128:131]\n"
RD"v[192:195],v244 offset:32768\n"
MF"v[132:135],a[148:151],v[200:203],v[132:135]\n"
MF"v[136:139],a[152:155],v[200:203],v[136:139]\n"
MF"v[140:143],a[156:159],v[200:203],v[140:143]\n"
WT"lgkmcnt(2)\n"
MF"v[144:147],a[144:147],v[204:207],v[144:147]\n"
RD"v[196:199],v244 offset:34816\n"
MF"v[148:151],a[148:151],v[204:207],v[148:151]\n"
MF"v[152:155],a[152:155],v[204:207],v[152:155]\n"
MF"v[156:159],a[156:159],v[204:207],v[156:159]\n"
MF"v[128:131],a[160:163],v[208:211],v[128:131]\n"
RD"v[200:203],v245 offset:32768\n"
MF"v[132:135],a[164:167],v[208:211],v[132:135]\n"
MF"v[136:139],a[168:171],v[208:211],v[136:139]\n"
MF"v[140:143],a[172:175],v[208:211],v[140:143]\n"
MF"v[144:147],a[160:163],v[212:215],v[144:147]\n"
RD"v[204:207],v245 offset:34816\n"
MF"v[148:151],a[164:167],v[212:215],v[148:151]\n"
"s_mov_b32 s49,20480\n"
MF"v[152:155],a[168:171],v[212:215],v[152:155]\n"
MF"v[156:159],a[172:175],v[212:215],v[156:159]\n"
RD"v[208:211],v244 offset:36864\n"
MF"v[128:131],a[176:179],v[216:219],v[128:131]\n"
BS"v[80:83],v250,s[8:11],s49 offen offset:0 sc1\n"
MF"v[132:135],a[180:183],v[216:219],v[132:135]\n"
MF"v[136:139],a[184:187],v[216:219],v[136:139]\n"
MF"v[140:143],a[188:191],v[216:219],v[140:143]\n"
WT"lgkmcnt(1)\n"
RD"v[212:215],v244 offset:38912\n"
MF"v[144:147],a[176:179],v[220:223],v[144:147]\n"
MF"v[148:151],a[180:183],v[220:223],v[148:151]\n"
MF"v[152:155],a[184:187],v[220:223],v[152:155]\n"
MF"v[156:159],a[188:191],v[220:223],v[156:159]\n"
RD"v[216:219],v245 offset:36864\n"
MF"v[128:131],a[192:195],v[192:195],v[128:131]\n"
MF"v[132:135],a[196:199],v[192:195],v[132:135]\n"
MF"v[136:139],a[200:203],v[192:195],v[136:139]\n"
MF"v[140:143],a[204:207],v[192:195],v[140:143]\n"
RD"v[220:223],v245 offset:38912\n"
MF"v[144:147],a[192:195],v[196:199],v[144:147]\n"
MF"v[148:151],a[196:199],v[196:199],v[148:151]\n"
MF"v[152:155],a[200:203],v[196:199],v[152:155]\n"
MF"v[156:159],a[204:207],v[196:199],v[156:159]\n"
WT"vmcnt(10)\n"
BS"v[100:103],v252,s[8:11],s92 offen sc1\n"
MF"v[128:131],a[208:211],v[200:203],v[128:131]\n"
MF"v[132:135],a[212:215],v[200:203],v[132:135]\n"
MF"v[136:139],a[216:219],v[200:203],v[136:139]\n"
MF"v[140:143],a[220:223],v[200:203],v[140:143]\n"
MF"v[144:147],a[208:211],v[204:207],v[144:147]\n"
MF"v[148:151],a[212:215],v[204:207],v[148:151]\n"
WT"lgkmcnt(0)\n"
"s_barrier\n"
MF"v[152:155],a[216:219],v[204:207],v[152:155]\n"
SA"m0,s36,32768\n"
RD"v[192:195],v242 offset:16384\n"
SA"s48,s60,s61\n"
MF"v[156:159],a[220:223],v[204:207],v[156:159]\n"
MF"v[128:131],a[224:227],v[208:211],v[128:131]\n"
BL"v238,s[20:23],s48 offen offset:0 lds\n"
MF"v[132:135],a[228:231],v[208:211],v[132:135]\n"
MF"v[136:139],a[232:235],v[208:211],v[136:139]\n"
RD"v[196:199],v242 offset:18432\n"
MF"v[140:143],a[236:239],v[208:211],v[140:143]\n"
MF"v[144:147],a[224:227],v[212:215],v[144:147]\n"
MF"v[148:151],a[228:231],v[212:215],v[148:151]\n"
MF"v[152:155],a[232:235],v[212:215],v[152:155]\n"
RD"v[200:203],v243 offset:16384\n"
MF"v[156:159],a[236:239],v[212:215],v[156:159]\n"
BS"v[88:91],v250,s[8:11],s49 offen offset:1024 sc1\n"
MF"v[128:131],a[240:243],v[216:219],v[128:131]\n"
"s_lshl_b32 s50,s34,3\n"
BL"v239,s[20:23],s48 offen offset:1024 lds\n"
MF"v[132:135],a[244:247],v[216:219],v[132:135]\n"
SA"s51,s50,s30\n"
MF"v[136:139],a[248:251],v[216:219],v[136:139]\n"
"s_mul_i32 s51,s51,20480\n"
RD"v[204:207],v243 offset:18432\n"
MF"v[140:143],a[252:255],v[216:219],v[140:143]\n"
SA"s56,s51,65536\n"
MF"v[144:147],a[240:243],v[220:223],v[144:147]\n"
SA"s57,s56,81920\n"
MF"v[148:151],a[244:247],v[220:223],v[148:151]\n"
"s_sub_u32 s51,s30,s50\n"
RD"v[208:211],v242 offset:20480\n"
MF"v[152:155],a[248:251],v[220:223],v[152:155]\n"
SA"s51,s51,120\n"
MF"v[156:159],a[252:255],v[220:223],v[156:159]\n"
WT"lgkmcnt(2)\n"
"s_mul_i32 s51,s51,20480\n"
MF"v[0:3],a[0:3],v[192:195],0\n"
SA"s58,s51,65536\n"
BL"v240,s[20:23],s48 offen offset:2048 lds\n"
MF"v[8:11],a[4:7],v[192:195],0\n"
"s_lshl_b32 s99,s99,12\n"
RD"v[212:215],v242 offset:22528\n"
SA"s59,s58,81920\n"
MF"v[16:19],a[8:11],v[192:195],0\n"
"v_add_u32 v235,s99,v224\n"
MF"v[24:27],a[12:15],v[192:195],0\n"
MU"v128,s55,v128\n"
MU"v129,s55,v129\n"
MF"v[32:35],a[0:3],v[196:199],0\n"
MU"v130,s55,v130\n"
MU"v131,s55,v131\n"
MF"v[40:43],a[4:7],v[196:199],0\n"
RD"v[216:219],v243 offset:20480\n"
MU"v132,s55,v132\n"
MF"v[48:51],a[8:11],v[196:199],0\n"
MU"v133,s55,v133\n"
MU"v134,s55,v134\n"
MF"v[56:59],a[12:15],v[196:199],0\n"
MU"v135,s55,v135\n"
PK"v64,v128,v129\n"
MF"v[0:3],a[16:19],v[200:203],v[0:3]\n"
SA"s61,s61,524288\n"
BL"v241,s[20:23],s48 offen offset:3072 lds\n"
MF"v[8:11],a[20:23],v[200:203],v[8:11]\n"
"s_cmp_eq_u32 s61,5242880\n"
BL"a[0:3],v235,s[8:11],s56 offen offset:0 sc1\n"
MF"v[16:19],a[24:27],v[200:203],v[16:19]\n"
"s_cselect_b32 s61,0,s61\n"
RD"v[220:223],v243 offset:22528\n"
PK"v65,v130,v131\n"
MF"v[24:27],a[28:31],v[200:203],v[24:27]\n"
WT"lgkmcnt(2)\n"
PK"v66,v132,v133\n"
PK"v67,v134,v135\n"
MF"v[32:35],a[16:19],v[204:207],v[32:35]\n"
RD"v[192:195],v242 offset:40960\n"
MF"v[40:43],a[20:23],v[204:207],v[40:43]\n"
MU"v136,s55,v136\n"
MU"v137,s55,v137\n"
MF"v[48:51],a[24:27],v[204:207],v[48:51]\n"
BL"a[4:7],v235,s[8:11],s57 offen offset:0 sc1\n"
MF"v[56:59],a[28:31],v[204:207],v[56:59]\n"
MU"v138,s55,v138\n"
MU"v139,s55,v139\n"
MF"v[0:3],a[32:35],v[208:211],v[0:3]\n"
RD"v[196:199],v242 offset:43008\n"
MU"v140,s55,v140\n"
MF"v[8:11],a[36:39],v[208:211],v[8:11]\n"
MU"v141,s55,v141\n"
MU"v142,s55,v142\n"
MF"v[16:19],a[40:43],v[208:211],v[16:19]\n"
MU"v143,s55,v143\n"
PK"v68,v136,v137\n"
MF"v[24:27],a[44:47],v[208:211],v[24:27]\n"
"s_mov_b32 s49,24576\n"
PK"v69,v138,v139\n"
PK"v70,v140,v141\n"
MF"v[32:35],a[32:35],v[212:215],v[32:35]\n"
RD"v[200:203],v243 offset:40960\n"
PK"v71,v142,v143\n"
MF"v[40:43],a[36:39],v[212:215],v[40:43]\n"
BL"a[8:11],v235,s[8:11],s58 offen offset:0 sc1\n"
MF"v[48:51],a[40:43],v[212:215],v[48:51]\n"
BS"v[96:99],v250,s[8:11],s49 offen offset:0 sc1\n"
MF"v[56:59],a[44:47],v[212:215],v[56:59]\n"
WT"lgkmcnt(1)\n"
MU"v144,s55,v144\n"
MU"v145,s55,v145\n"
MF"v[0:3],a[48:51],v[216:219],v[0:3]\n"
RD"v[204:207],v243 offset:43008\n"
MU"v146,s55,v146\n"
MF"v[8:11],a[52:55],v[216:219],v[8:11]\n"
MU"v147,s55,v147\n"
MU"v148,s55,v148\n"
MF"v[16:19],a[56:59],v[216:219],v[16:19]\n"
BL"a[12:15],v235,s[8:11],s59 offen offset:0 sc1\n"
MF"v[24:27],a[60:63],v[216:219],v[24:27]\n"
RD"v[208:211],v242 offset:45056\n"
MU"v149,s55,v149\n"
MF"v[32:35],a[48:51],v[220:223],v[32:35]\n"
MU"v150,s55,v150\n"
MU"v151,s55,v151\n"
MF"v[40:43],a[52:55],v[220:223],v[40:43]\n"
PK"v72,v144,v145\n"
PK"v73,v146,v147\n"
MF"v[48:51],a[56:59],v[220:223],v[48:51]\n"
PK"v74,v148,v149\n"
PK"v75,v150,v151\n"
MF"v[56:59],a[60:63],v[220:223],v[56:59]\n"
RD"v[212:215],v242 offset:47104\n"
MF"v[0:3],a[64:67],v[192:195],v[0:3]\n"
BL"a[16:19],v235,s[8:11],s56 offen offset:1024 sc1\n"
MF"v[8:11],a[68:71],v[192:195],v[8:11]\n"
MU"v152,s55,v152\n"
MU"v153,s55,v153\n"
MF"v[16:19],a[72:75],v[192:195],v[16:19]\n"
MU"v154,s55,v154\n"
MU"v155,s55,v155\n"
MF"v[24:27],a[76:79],v[192:195],v[24:27]\n"
RD"v[216:219],v243 offset:45056\n"
MU"v156,s55,v156\n"
MF"v[32:35],a[64:67],v[196:199],v[32:35]\n"
MU"v157,s55,v157\n"
MU"v158,s55,v158\n"
MF"v[40:43],a[68:71],v[196:199],v[40:43]\n"
MU"v159,s55,v159\n"
PK"v76,v152,v153\n"
MF"v[48:51],a[72:75],v[196:199],v[48:51]\n"
BL"a[20:23],v235,s[8:11],s57 offen offset:1024 sc1\n"
MF"v[56:59],a[76:79],v[196:199],v[56:59]\n"
WT"lgkmcnt(1)\n"
RD"v[220:223],v243 offset:47104\n"
PK"v77,v154,v155\n"
MF"v[0:3],a[80:83],v[200:203],v[0:3]\n"
PK"v78,v156,v157\n"
PK"v79,v158,v159\n"
MF"v[8:11],a[84:87],v[200:203],v[8:11]\n"
MF"v[16:19],a[88:91],v[200:203],v[16:19]\n"
RD"v[192:195],v244 offset:16384\n"
MF"v[24:27],a[92:95],v[200:203],v[24:27]\n"
MF"v[32:35],a[80:83],v[204:207],v[32:35]\n"
BL"a[24:27],v235,s[8:11],s58 offen offset:1024 sc1\n"
MF"v[40:43],a[84:87],v[204:207],v[40:43]\n"
MF"v[48:51],a[88:91],v[204:207],v[48:51]\n"
RD"v[196:199],v244 offset:18432\n"
MF"v[56:59],a[92:95],v[204:207],v[56:59]\n"
MF"v[0:3],a[96:99],v[208:211],v[0:3]\n"
MF"v[8:11],a[100:103],v[208:211],v[8:11]\n"
MF"v[16:19],a[104:107],v[208:211],v[16:19]\n"
RD"v[200:203],v245 offset:16384\n"
MF"v[24:27],a[108:111],v[208:211],v[24:27]\n"
BL"a[28:31],v235,s[8:11],s59 offen offset:1024 sc1\n"
MF"v[32:35],a[96:99],v[212:215],v[32:35]\n"
BS"v[104:107],v250,s[8:11],s49 offen offset:1024 sc1\n"
MF"v[40:43],a[100:103],v[212:215],v[40:43]\n"
MF"v[48:51],a[104:107],v[212:215],v[48:51]\n"
RD"v[204:207],v245 offset:18432\n"
MF"v[56:59],a[108:111],v[212:215],v[56:59]\n"
WT"lgkmcnt(2)\n"
MF"v[0:3],a[112:115],v[216:219],v[0:3]\n"
BL"a[32:35],v235,s[8:11],s56 offen offset:2048 sc1\n"
MF"v[8:11],a[116:119],v[216:219],v[8:11]\n"
RD"v[208:211],v244 offset:20480\n"
MF"v[16:19],a[120:123],v[216:219],v[16:19]\n"
MF"v[24:27],a[124:127],v[216:219],v[24:27]\n"
MF"v[32:35],a[112:115],v[220:223],v[32:35]\n"
MF"v[40:43],a[116:119],v[220:223],v[40:43]\n"
RD"v[212:215],v244 offset:22528\n"
MF"v[48:51],a[120:123],v[220:223],v[48:51]\n"
BL"a[36:39],v235,s[8:11],s57 offen offset:2048 sc1\n"
MF"v[56:59],a[124:127],v[220:223],v[56:59]\n"
MF"v[0:3],a[128:131],v[192:195],v[0:3]\n"
MF"v[8:11],a[132:135],v[192:195],v[8:11]\n"
RD"v[216:219],v245 offset:20480\n"
MF"v[16:19],a[136:139],v[192:195],v[16:19]\n"
MF"v[24:27],a[140:143],v[192:195],v[24:27]\n"
MF"v[32:35],a[128:131],v[196:199],v[32:35]\n"
BL"a[40:43],v235,s[8:11],s58 offen offset:2048 sc1\n"
MF"v[40:43],a[132:135],v[196:199],v[40:43]\n"
RD"v[220:223],v245 offset:22528\n"
MF"v[48:51],a[136:139],v[196:199],v[48:51]\n"
MF"v[56:59],a[140:143],v[196:199],v[56:59]\n"
WT"lgkmcnt(2)\n"
MF"v[0:3],a[144:147],v[200:203],v[0:3]\n"
RD"v[192:195],v244 offset:40960\n"
MF"v[8:11],a[148:151],v[200:203],v[8:11]\n"
MF"v[16:19],a[152:155],v[200:203],v[16:19]\n"
BL"a[44:47],v235,s[8:11],s59 offen offset:2048 sc1\n"
MF"v[24:27],a[156:159],v[200:203],v[24:27]\n"
MF"v[32:35],a[144:147],v[204:207],v[32:35]\n"
RD"v[196:199],v244 offset:43008\n"
MF"v[40:43],a[148:151],v[204:207],v[40:43]\n"
MF"v[48:51],a[152:155],v[204:207],v[48:51]\n"
MF"v[56:59],a[156:159],v[204:207],v[56:59]\n"
MF"v[0:3],a[160:163],v[208:211],v[0:3]\n"
"s_sub_u32 s53,s37,65536\n"
RD"v[200:203],v245 offset:40960\n"
MF"v[8:11],a[164:167],v[208:211],v[8:11]\n"
"s_mov_b32 s49,28672\n"
BL"a[48:51],v235,s[8:11],s56 offen offset:3072 sc1\n"
MF"v[16:19],a[168:171],v[208:211],v[16:19]\n"
"v_subrev_u32 v246,s53,v246\n"
"v_subrev_u32 v247,s53,v247\n"
MF"v[24:27],a[172:175],v[208:211],v[24:27]\n"
BS"v[112:115],v250,s[8:11],s49 offen offset:0 sc1\n"
MF"v[32:35],a[160:163],v[212:215],v[32:35]\n"
RD"v[204:207],v245 offset:43008\n"
"v_subrev_u32 v248,s53,v248\n"
MF"v[40:43],a[164:167],v[212:215],v[40:43]\n"
"v_subrev_u32 v249,s53,v249\n"
"v_subrev_u32 v236,65536,v246\n"
MF"v[48:51],a[168:171],v[212:215],v[48:51]\n"
BL"a[52:55],v235,s[8:11],s57 offen offset:3072 sc1\n"
MF"v[56:59],a[172:175],v[212:215],v[56:59]\n"
WT"lgkmcnt(1)\n"
RD"v[208:211],v244 offset:45056\n"
"v_subrev_u32 v237,65536,v247\n"
MF"v[0:3],a[176:179],v[216:219],v[0:3]\n"
"v_subrev_u32 v228,65536,v248\n"
"v_subrev_u32 v229,65536,v249\n"
MF"v[8:11],a[180:183],v[216:219],v[8:11]\n"
MF"v[16:19],a[184:187],v[216:219],v[16:19]\n"
MF"v[24:27],a[188:191],v[216:219],v[24:27]\n"
RD"v[212:215],v244 offset:47104\n"
MF"v[32:35],a[176:179],v[220:223],v[32:35]\n"
BL"a[56:59],v235,s[8:11],s58 offen offset:3072 sc1\n"
MF"v[40:43],a[180:183],v[220:223],v[40:43]\n"
MF"v[48:51],a[184:187],v[220:223],v[48:51]\n"
MF"v[56:59],a[188:191],v[220:223],v[56:59]\n"
RD"v[216:219],v245 offset:45056\n"
MF"v[0:3],a[192:195],v[192:195],v[0:3]\n"
SA"s99,s99,4096\n"
MF"v[8:11],a[196:199],v[192:195],v[8:11]\n"
"s_cmp_eq_u32 s99,20480\n"
"s_cselect_b32 s99,0,s99\n"
MF"v[16:19],a[200:203],v[192:195],v[16:19]\n"
BL"a[60:63],v235,s[8:11],s59 offen offset:3072 sc1\n"
MF"v[24:27],a[204:207],v[192:195],v[24:27]\n"
RD"v[220:223],v245 offset:47104\n"
MF"v[32:35],a[192:195],v[196:199],v[32:35]\n"
MF"v[40:43],a[196:199],v[196:199],v[40:43]\n"
MF"v[48:51],a[200:203],v[196:199],v[48:51]\n"
MF"v[56:59],a[204:207],v[196:199],v[56:59]\n"
"v_add_u32 v235,s99,v224\n"
MF"v[0:3],a[208:211],v[200:203],v[0:3]\n"
BL"a[64:67],v235,s[8:11],s56 offen offset:0 sc1\n"
MF"v[8:11],a[212:215],v[200:203],v[8:11]\n"
MF"v[16:19],a[216:219],v[200:203],v[16:19]\n"
MF"v[24:27],a[220:223],v[200:203],v[24:27]\n"
WT"lgkmcnt(0)\n"
MF"v[32:35],a[208:211],v[204:207],v[32:35]\n"
MF"v[40:43],a[212:215],v[204:207],v[40:43]\n"
"s_barrier\n"
MF"v[48:51],a[216:219],v[204:207],v[48:51]\n"
RT"a[128:129],v236 offset:0\n"
MF"v[56:59],a[220:223],v[204:207],v[56:59]\n"
BL"a[68:71],v235,s[8:11],s57 offen offset:0 sc1\n"
MF"v[0:3],a[224:227],v[208:211],v[0:3]\n"
SA"m0,s38,73728\n"
RT"a[130:131],v236 offset:512\n"
SA"s48,s60,s61\n"
MF"v[8:11],a[228:231],v[208:211],v[8:11]\n"
MF"v[16:19],a[232:235],v[208:211],v[16:19]\n"
RT"a[132:133],v237 offset:0\n"
"v_add_u32 v242,49152,v246\n"
MF"v[24:27],a[236:239],v[208:211],v[24:27]\n"
BS"v[120:123],v250,s[8:11],s49 offen offset:1024 sc1\n"
MF"v[32:35],a[224:227],v[212:215],v[32:35]\n"
BL"v238,s[20:23],s48 offen offset:0 lds\n"
MF"v[40:43],a[228:231],v[212:215],v[40:43]\n"
RT"a[134:135],v237 offset:512\n"
"v_add_u32 v243,49152,v247\n"
MF"v[48:51],a[232:235],v[212:215],v[48:51]\n"
BL"a[72:75],v235,s[8:11],s58 offen offset:0 sc1\n"
MF"v[56:59],a[236:239],v[212:215],v[56:59]\n"
RT"a[136:137],v228 offset:0\n"
"v_add_u32 v244,49152,v248\n"
MF"v[0:3],a[240:243],v[216:219],v[0:3]\n"
RT"a[138:139],v228 offset:512\n"
"v_add_u32 v245,49152,v249\n"
MF"v[8:11],a[244:247],v[216:219],v[8:11]\n"
MF"v[16:19],a[248:251],v[216:219],v[16:19]\n"
RT"a[140:141],v229 offset:0\n"
MF"v[24:27],a[252:255],v[216:219],v[24:27]\n"
BL"v239,s[20:23],s48 offen offset:1024 lds\n"
MF"v[32:35],a[240:243],v[220:223],v[32:35]\n"
BL"a[76:79],v235,s[8:11],s59 offen offset:0 sc1\n"
MF"v[40:43],a[244:247],v[220:223],v[40:43]\n"
RT"a[142:143],v229 offset:512\n"
MF"v[48:51],a[248:251],v[220:223],v[48:51]\n"
RT"a[144:145],v236 offset:4096\n"
MF"v[56:59],a[252:255],v[220:223],v[56:59]\n"
WT"vmcnt(25) lgkmcnt(3)\n"
MF"v[80:83],a[128:131],a[0:3],0\n"
WT"vmcnt(24)\n"
RT"a[146:147],v236 offset:4608\n"
MF"v[88:91],a[128:131],a[4:7],0\n"
WT"vmcnt(23)\n"
BL"a[80:83],v235,s[8:11],s56 offen offset:1024 sc1\n"
MF"v[96:99],a[128:131],a[8:11],0\n"
WT"vmcnt(22)\n"
RT"a[148:149],v237 offset:4096\n"
MF"v[104:107],a[128:131],a[12:15],0\n"
BL"v240,s[20:23],s48 offen offset:2048 lds\n"
MF"v[84:87],a[132:135],a[0:3],0\n"
RT"a[150:151],v237 offset:4608\n"
MU"v0,s55,v0\n"
MF"v[92:95],a[132:135],a[4:7],0\n"
RT"a[152:153],v228 offset:4096\n"
MU"v1,s55,v1\n"
MF"v[100:103],a[132:135],a[8:11],0\n"
MU"v2,s55,v2\n"
MU"v3,s55,v3\n"
MF"v[108:111],a[132:135],a[12:15],0\n"
RT"a[154:155],v228 offset:4608\n"
MU"v8,s55,v8\n"
MF"v[112:115],a[136:139],a[0:3],0\n"
BL"a[84:87],v235,s[8:11],s57 offen offset:1024 sc1\n"
MF"v[120:123],a[136:139],a[4:7],0\n"
RT"a[156:157],v229 offset:4096\n"
MU"v9,s55,v9\n"
MF"v[128:131],a[136:139],a[8:11],0\n"
SA"s61,s61,524288\n"
BL"v241,s[20:23],s48 offen offset:3072 lds\n"
MF"v[136:139],a[136:139],a[12:15],0\n"
WT"lgkmcnt(3)\n"
RT"a[158:159],v229 offset:4608\n"
MU"v10,s55,v10\n"
MF"v[116:119],a[140:143],a[0:3],0\n"
MU"v11,s55,v11\n"
PK"v0,v0,v1\n"
MF"v[124:127],a[140:143],a[4:7],0\n"
RT"a[160:161],v236 offset:8192\n"
PK"v1,v2,v3\n"
MF"v[132:135],a[140:143],a[8:11],0\n"
BL"a[88:91],v235,s[8:11],s58 offen offset:1024 sc1\n"
MF"v[140:143],a[140:143],a[12:15],0\n"
RT"a[162:163],v236 offset:8704\n"
PK"v2,v8,v9\n"
MF"v[144:147],a[144:147],a[0:3],0\n"
PK"v3,v10,v11\n"
MU"v16,s55,v16\n"
MF"v[152:155],a[144:147],a[4:7],0\n"
RT"a[164:165],v237 offset:8192\n"
MU"v17,s55,v17\n"
MF"v[160:163],a[144:147],a[8:11],0\n"
MU"v18,s55,v18\n"
MU"v19,s55,v19\n"
MF"v[168:171],a[144:147],a[12:15],0\n"
RT"a[166:167],v237 offset:8704\n"
MU"v24,s55,v24\n"
MF"v[148:151],a[148:151],a[0:3],0\n"
MU"v25,s55,v25\n"
MU"v26,s55,v26\n"
MF"v[156:159],a[148:151],a[4:7],0\n"
RT"a[168:169],v228 offset:8192\n"
MU"v27,s55,v27\n"
MF"v[164:167],a[148:151],a[8:11],0\n"
BL"a[92:95],v235,s[8:11],s59 offen offset:1024 sc1\n"
MF"v[172:175],a[148:151],a[12:15],0\n"
WT"lgkmcnt(3)\n"
RT"a[170:171],v228 offset:8704\n"
PK"v16,v16,v17\n"
MF"v[176:179],a[152:155],a[0:3],0\n"
"s_mov_b32 s49,262144\n"
RT"a[172:173],v229 offset:8192\n"
PK"v17,v18,v19\n"
MF"v[184:187],a[152:155],a[4:7],0\n"
PK"v18,v24,v25\n"
PK"v19,v26,v27\n"
MF"v[192:195],a[152:155],a[8:11],0\n"
RT"a[174:175],v229 offset:8704\n"
MU"v32,s55,v32\n"
MF"v[200:203],a[152:155],a[12:15],0\n"
BS"v[4:7],v250,s[8:11],s49 offen offset:0 sc1\n"
MF"v[180:183],a[156:159],a[0:3],0\n"
RT"a[176:177],v236 offset:12288\n"
MU"v33,s55,v33\n"
MF"v[188:191],a[156:159],a[4:7],0\n"
BL"a[96:99],v235,s[8:11],s56 offen offset:2048 sc1\n"
MF"v[196:199],a[156:159],a[8:11],0\n"
RT"a[178:179],v236 offset:12800\n"
MU"v34,s55,v34\n"
MF"v[204:207],a[156:159],a[12:15],0\n"
WT"vmcnt(28)\n"
MU"v35,s55,v35\n"
MU"v40,s55,v40\n"
MF"v[80:83],a[160:163],a[16:19],v[80:83]\n"
WT"vmcnt(27)\n"
RT"a[180:181],v237 offset:12288\n"
MU"v41,s55,v41\n"
MF"v[88:91],a[160:163],a[20:23],v[88:91]\n"
WT"vmcnt(26)\n"
MU"v42,s55,v42\n"
MU"v43,s55,v43\n"
MF"v[96:99],a[160:163],a[24:27],v[96:99]\n"
WT"vmcnt(25)\n"
RT"a[182:183],v237 offset:12800\n"
PK"v32,v32,v33\n"
MF"v[104:107],a[160:163],a[28:31],v[104:107]\n"
WT"lgkmcnt(3)\n"
BL"a[100:103],v235,s[8:11],s57 offen offset:2048 sc1\n"
MF"v[84:87],a[164:167],a[16:19],v[84:87]\n"
SA"m0,s38,122880\n"
RT"a[184:185],v228 offset:12288\n"
PK"v33,v34,v35\n"
MF"v[92:95],a[164:167],a[20:23],v[92:95]\n"
SA"s48,s60,s61\n"
PK"v34,v40,v41\n"
PK"v35,v42,v43\n"
MF"v[100:103],a[164:167],a[24:27],v[100:103]\n"
"s_mov_b32 s88,4096\n"
BL"v238,s[20:23],s48 offen offset:0 lds\n"
MF"v[108:111],a[164:167],a[28:31],v[108:111]\n"
RT"a[186:187],v228 offset:12800\n"
MU"v48,s55,v48\n"
MF"v[112:115],a[168:171],a[16:19],v[112:115]\n"
RT"a[188:189],v229 offset:12288\n"
MU"v49,s55,v49\n"
MF"v[120:123],a[168:171],a[20:23],v[120:123]\n"
MU"v50,s55,v50\n"
MU"v51,s55,v51\n"
MF"v[128:131],a[168:171],a[24:27],v[128:131]\n"
RT"a[190:191],v229 offset:12800\n"
MU"v56,s55,v56\n"
MF"v[136:139],a[168:171],a[28:31],v[136:139]\n"
BL"a[104:107],v235,s[8:11],s58 offen offset:2048 sc1\n"
MF"v[116:119],a[172:175],a[16:19],v[116:119]\n"
RT"a[128:129],v236 offset:16384\n"
MU"v57,s55,v57\n"
MF"v[124:127],a[172:175],a[20:23],v[124:127]\n"
BL"v239,s[20:23],s48 offen offset:1024 lds\n"
MF"v[132:135],a[172:175],a[24:27],v[132:135]\n"
RT"a[130:131],v236 offset:16896\n"
MU"v58,s55,v58\n"
MF"v[140:143],a[172:175],a[28:31],v[140:143]\n"
WT"lgkmcnt(3)\n"
RT"a[132:133],v237 offset:16384\n"
MU"v59,s55,v59\n"
MF"v[144:147],a[176:179],a[16:19],v[144:147]\n"
PK"v48,v48,v49\n"
PK"v49,v50,v51\n"
MF"v[152:155],a[176:179],a[20:23],v[152:155]\n"
RT"a[134:135],v237 offset:16896\n"
PK"v50,v56,v57\n"
MF"v[160:163],a[176:179],a[24:27],v[160:163]\n"
BL"a[108:111],v235,s[8:11],s59 offen offset:2048 sc1\n"
MF"v[168:171],a[176:179],a[28:31],v[168:171]\n"
RT"a[136:137],v228 offset:16384\n"
PK"v51,v58,v59\n"
MF"v[148:151],a[180:183],a[16:19],v[148:151]\n"
BL"v240,s[20:23],s48 offen offset:2048 lds\n"
MF"v[156:159],a[180:183],a[20:23],v[156:159]\n"
RT"a[138:139],v228 offset:16896\n"
MF"v[164:167],a[180:183],a[24:27],v[164:167]\n"
MF"v[172:175],a[180:183],a[28:31],v[172:175]\n"
RT"a[140:141],v229 offset:16384\n"
MF"v[176:179],a[184:187],a[16:19],v[176:179]\n"
BL"a[112:115],v235,s[8:11],s56 offen offset:3072 sc1\n"
MF"v[184:187],a[184:187],a[20:23],v[184:187]\n"
RT"a[142:143],v229 offset:16896\n"
MF"v[192:195],a[184:187],a[24:27],v[192:195]\n"
MF"v[200:203],a[184:187],a[28:31],v[200:203]\n"
WT"lgkmcnt(3)\n"
RT"a[144:145],v236 offset:20480\n"
MF"v[180:183],a[188:191],a[16:19],v[180:183]\n"
SA"s61,s61,524288\n"
BS"v[12:15],v250,s[8:11],s49 offen offset:1024 sc1\n"
MF"v[188:191],a[188:191],a[20:23],v[188:191]\n"
"s_cmp_eq_u32 s61,5242880\n"
BL"v241,s[20:23],s48 offen offset:3072 lds\n"
MF"v[196:199],a[188:191],a[24:27],v[196:199]\n"
"s_cselect_b32 s61,0,s61\n"
RT"a[146:147],v236 offset:20992\n"
MF"v[204:207],a[188:191],a[28:31],v[204:207]\n"
WT"vmcnt(32)\n"
RT"a[148:149],v237 offset:20480\n"
MF"v[80:83],a[128:131],a[32:35],v[80:83]\n"
WT"vmcnt(31)\n"
BL"a[116:119],v235,s[8:11],s57 offen offset:3072 sc1\n"
MF"v[88:91],a[128:131],a[36:39],v[88:91]\n"
WT"vmcnt(31)\n"
RT"a[150:151],v237 offset:20992\n"
MF"v[96:99],a[128:131],a[40:43],v[96:99]\n"
WT"vmcnt(30)\n"
RT"a[152:153],v228 offset:20480\n"
MF"v[104:107],a[128:131],a[44:47],v[104:107]\n"
MF"v[84:87],a[132:135],a[32:35],v[84:87]\n"
RT"a[154:155],v228 offset:20992\n"
MF"v[92:95],a[132:135],a[36:39],v[92:95]\n"
MF"v[100:103],a[132:135],a[40:43],v[100:103]\n"
RT"a[156:157],v229 offset:20480\n"
MF"v[108:111],a[132:135],a[44:47],v[108:111]\n"
WT"lgkmcnt(4)\n"
BL"a[120:123],v235,s[8:11],s58 offen offset:3072 sc1\n"
MF"v[112:115],a[136:139],a[32:35],v[112:115]\n"
RT"a[158:159],v229 offset:20992\n"
MF"v[120:123],a[136:139],a[36:39],v[120:123]\n"
MF"v[128:131],a[136:139],a[40:43],v[128:131]\n"
RT"a[160:161],v236 offset:24576\n"
MF"v[136:139],a[136:139],a[44:47],v[136:139]\n"
MF"v[116:119],a[140:143],a[32:35],v[116:119]\n"
SA"s99,s99,4096\n"
RT"a[162:163],v236 offset:25088\n"
MF"v[124:127],a[140:143],a[36:39],v[124:127]\n"
"s_cmp_eq_u32 s99,20480\n"
BL"a[124:127],v235,s[8:11],s59 offen offset:3072 sc1\n"
MF"v[132:135],a[140:143],a[40:43],v[132:135]\n"
"s_cselect_b32 s99,0,s99\n"
RT"a[164:165],v237 offset:24576\n"
MF"v[140:143],a[140:143],a[44:47],v[140:143]\n"
MF"v[144:147],a[144:147],a[32:35],v[144:147]\n"
RT"a[166:167],v237 offset:25088\n"
MF"v[152:155],a[144:147],a[36:39],v[152:155]\n"
MF"v[160:163],a[144:147],a[40:43],v[160:163]\n"
RT"a[168:169],v228 offset:24576\n"
MF"v[168:171],a[144:147],a[44:47],v[168:171]\n"
WT"lgkmcnt(3)\n"
"v_add_u32 v235,s99,v224\n"
MF"v[148:151],a[148:151],a[32:35],v[148:151]\n"
RT"a[170:171],v228 offset:25088\n"
MF"v[156:159],a[148:151],a[36:39],v[156:159]\n"
BL"a[0:3],v235,s[8:11],s56 offen offset:0 sc1\n"
MF"v[164:167],a[148:151],a[40:43],v[164:167]\n"
RT"a[172:173],v229 offset:24576\n"
MF"v[172:175],a[148:151],a[44:47],v[172:175]\n"
RT"a[174:175],v229 offset:25088\n"
MF"v[176:179],a[152:155],a[32:35],v[176:179]\n"
MF"v[184:187],a[152:155],a[36:39],v[184:187]\n"
RT"a[176:177],v236 offset:28672\n"
MF"v[192:195],a[152:155],a[40:43],v[192:195]\n"
MF"v[200:203],a[152:155],a[44:47],v[200:203]\n"
RT"a[178:179],v236 offset:29184\n"
MF"v[180:183],a[156:159],a[32:35],v[180:183]\n"
"s_mov_b32 s49,266240\n"
BL"a[4:7],v235,s[8:11],s57 offen offset:0 sc1\n"
MF"v[188:191],a[156:159],a[36:39],v[188:191]\n"
RT"a[180:181],v237 offset:28672\n"
MF"v[196:199],a[156:159],a[40:43],v[196:199]\n"
BS"v[20:23],v250,s[8:11],s49 offen offset:0 sc1\n"
MF"v[204:207],a[156:159],a[44:47],v[204:207]\n"
RT"a[182:183],v237 offset:29184\n"
"s_branch LQ_%=\n"
"LV_%=:\n"
WT"vmcnt(26)\n"
"v_lshlrev_b32 v148,16,v128\n"
"v_and_b32 v149,0xffff0000,v128\n"
"v_lshlrev_b32 v150,16,v129\n"
"v_and_b32 v151,0xffff0000,v129\n"
WT"vmcnt(25)\n"
"v_lshlrev_b32 v152,16,v130\n"
"v_and_b32 v153,0xffff0000,v130\n"
"v_lshlrev_b32 v154,16,v131\n"
"v_and_b32 v155,0xffff0000,v131\n"
WT"vmcnt(24)\n"
"v_lshlrev_b32 v156,16,v132\n"
"v_and_b32 v157,0xffff0000,v132\n"
"v_lshlrev_b32 v158,16,v133\n"
"v_and_b32 v159,0xffff0000,v133\n"
WT"vmcnt(23)\n"
"v_lshlrev_b32 v160,16,v134\n"
"v_and_b32 v161,0xffff0000,v134\n"
"v_lshlrev_b32 v162,16,v135\n"
"v_and_b32 v163,0xffff0000,v135\n"
WT"vmcnt(22)\n"
"v_lshlrev_b32 v164,16,v136\n"
"v_and_b32 v165,0xffff0000,v136\n"
"v_lshlrev_b32 v166,16,v137\n"
"v_and_b32 v167,0xffff0000,v137\n"
"v_add_f32 v188,v148,v149\n"
"v_add_f32 v189,v150,v151\n"
"v_add_f32 v188,v188,v152\n"
"v_add_f32 v189,v189,v153\n"
"v_add_f32 v188,v188,v154\n"
"v_add_f32 v189,v189,v155\n"
"v_add_f32 v188,v188,v156\n"
"v_add_f32 v189,v189,v157\n"
"v_add_f32 v188,v188,v158\n"
"v_add_f32 v189,v189,v159\n"
"v_add_f32 v188,v188,v160\n"
"v_add_f32 v189,v189,v161\n"
"v_add_f32 v188,v188,v162\n"
"v_add_f32 v189,v189,v163\n"
"v_add_f32 v188,v188,v164\n"
"v_add_f32 v189,v189,v165\n"
"v_add_f32 v188,v188,v166\n"
"v_add_f32 v189,v189,v167\n"
"v_add_f32 v188,v188,v189\n"
"s_nop 1\n"
"v_add_f32_dpp v188,v188,v188 quad_perm:[1,0,3,2] row_mask:0xf bank_mask:0xf\n"
WT"vmcnt(21)\n"
"v_lshlrev_b32 v168,16,v138\n"
"v_and_b32 v169,0xffff0000,v138\n"
"v_add_f32_dpp v188,v188,v188 quad_perm:[2,3,0,1] row_mask:0xf bank_mask:0xf\n"
"v_lshlrev_b32 v170,16,v139\n"
"v_and_b32 v171,0xffff0000,v139\n"
"v_add_f32_dpp v188,v188,v188 row_half_mirror row_mask:0xf bank_mask:0xf\n"
WT"vmcnt(20)\n"
"v_lshlrev_b32 v172,16,v140\n"
"v_and_b32 v173,0xffff0000,v140\n"
"v_add_f32_dpp v188,v188,v188 row_mirror row_mask:0xf bank_mask:0xf\n"
"v_lshlrev_b32 v174,16,v141\n"
"v_and_b32 v175,0xffff0000,v141\n"
"v_mov_b32 v189,v188\n"
WT"vmcnt(19)\n"
"v_lshlrev_b32 v176,16,v142\n"
"v_and_b32 v177,0xffff0000,v142\n"
"v_permlane16_swap_b32 v189,v188\n"
"v_lshlrev_b32 v178,16,v143\n"
"v_and_b32 v179,0xffff0000,v143\n"
"v_add_f32 v188,v188,v189\n"
WT"vmcnt(18)\n"
"v_lshlrev_b32 v180,16,v144\n"
"v_and_b32 v181,0xffff0000,v144\n"
MU"v191,0x3acccccd,v188\n"
"v_lshlrev_b32 v182,16,v145\n"
"v_and_b32 v183,0xffff0000,v145\n"
PK"v128,v191,v191\n"
WT"vmcnt(17)\n"
"v_lshlrev_b32 v184,16,v146\n"
"v_and_b32 v185,0xffff0000,v146\n"
"v_lshlrev_b32 v128,16,v128\n"
"v_lshlrev_b32 v186,16,v147\n"
"v_and_b32 v187,0xffff0000,v147\n"
"v_sub_f32 v148,v148,v128\n"
"v_sub_f32 v149,v149,v128\n"
"v_sub_f32 v150,v150,v128\n"
"v_sub_f32 v151,v151,v128\n"
"v_sub_f32 v152,v152,v128\n"
"v_sub_f32 v153,v153,v128\n"
"v_sub_f32 v154,v154,v128\n"
"v_sub_f32 v155,v155,v128\n"
"v_sub_f32 v156,v156,v128\n"
"v_sub_f32 v157,v157,v128\n"
SA"m0,s36,16384\n"
SA"s48,s46,128\n"
"s_and_b32 s48,s48,1023\n"
BL"v234,s[12:15],s48 offen offset:0 lds\n"
BL"v235,s[12:15],s48 offen offset:1024 lds\n"
BL"v236,s[12:15],s48 offen offset:2048 lds\n"
BL"v237,s[12:15],s48 offen offset:3072 lds\n"
"v_sub_f32 v158,v158,v128\n"
"v_sub_f32 v159,v159,v128\n"
"v_sub_f32 v160,v160,v128\n"
"v_sub_f32 v161,v161,v128\n"
"v_sub_f32 v162,v162,v128\n"
"v_sub_f32 v163,v163,v128\n"
"v_sub_f32 v164,v164,v128\n"
"v_sub_f32 v165,v165,v128\n"
"v_sub_f32 v166,v166,v128\n"
"v_sub_f32 v167,v167,v128\n"
MU"v188,v148,v148\n"
MU"v189,v149,v149\n"
"v_fmac_f32 v188,v150,v150\n"
"v_fmac_f32 v189,v151,v151\n"
"v_fmac_f32 v188,v152,v152\n"
"v_fmac_f32 v189,v153,v153\n"
"v_fmac_f32 v188,v154,v154\n"
"v_fmac_f32 v189,v155,v155\n"
"v_fmac_f32 v188,v156,v156\n"
"v_fmac_f32 v189,v157,v157\n"
"v_fmac_f32 v188,v158,v158\n"
"v_fmac_f32 v189,v159,v159\n"
"v_fmac_f32 v188,v160,v160\n"
"v_fmac_f32 v189,v161,v161\n"
"v_fmac_f32 v188,v162,v162\n"
"v_fmac_f32 v189,v163,v163\n"
"v_fmac_f32 v188,v164,v164\n"
"v_fmac_f32 v189,v165,v165\n"
"v_fmac_f32 v188,v166,v166\n"
"v_fmac_f32 v189,v167,v167\n"
"v_add_f32 v188,v188,v189\n"
PK"v149,v148,v149\n"
"v_lshlrev_b32 v148,16,v149\n"
"v_add_f32_dpp v188,v188,v188 quad_perm:[1,0,3,2] row_mask:0xf bank_mask:0xf\n"
"v_and_b32 v149,0xffff0000,v149\n"
PK"v151,v150,v151\n"
"v_add_f32_dpp v188,v188,v188 quad_perm:[2,3,0,1] row_mask:0xf bank_mask:0xf\n"
"v_lshlrev_b32 v150,16,v151\n"
"v_and_b32 v151,0xffff0000,v151\n"
"v_add_f32_dpp v188,v188,v188 row_half_mirror row_mask:0xf bank_mask:0xf\n"
PK"v153,v152,v153\n"
"v_lshlrev_b32 v152,16,v153\n"
"v_add_f32_dpp v188,v188,v188 row_mirror row_mask:0xf bank_mask:0xf\n"
"v_and_b32 v153,0xffff0000,v153\n"
PK"v155,v154,v155\n"
"v_mov_b32 v189,v188\n"
"v_lshlrev_b32 v154,16,v155\n"
"v_and_b32 v155,0xffff0000,v155\n"
"v_permlane16_swap_b32 v189,v188\n"
PK"v157,v156,v157\n"
"v_lshlrev_b32 v156,16,v157\n"
"v_add_f32 v188,v188,v189\n"
"v_and_b32 v157,0xffff0000,v157\n"
PK"v159,v158,v159\n"
"v_sub_f32 v191,v191,v128\n"
"v_lshlrev_b32 v158,16,v159\n"
"v_and_b32 v159,0xffff0000,v159\n"
MU"v188,0x3acccccd,v188\n"
PK"v161,v160,v161\n"
"v_lshlrev_b32 v160,16,v161\n"
"v_fma_f32 v188,-v191,v191,v188\n"
"v_and_b32 v161,0xffff0000,v161\n"
PK"v163,v162,v163\n"
"v_max_f32 v188,0,v188\n"
"v_lshlrev_b32 v162,16,v163\n"
"v_and_b32 v163,0xffff0000,v163\n"
PK"v188,v188,v188\n"
PK"v165,v164,v165\n"
"v_lshlrev_b32 v164,16,v165\n"
"v_lshlrev_b32 v188,16,v188\n"
"v_and_b32 v165,0xffff0000,v165\n"
PK"v167,v166,v167\n"
"v_add_f32 v188,0x3727c5ac,v188\n"
"v_lshlrev_b32 v166,16,v167\n"
"v_and_b32 v167,0xffff0000,v167\n"
PK"v188,v188,v188\n"
"v_bfe_u32 v130,v224,5,4\n"
"v_mul_u32_u24 v130,144,v130\n"
"v_lshlrev_b32 v188,16,v188\n"
SA"m0,s37,12288\n"
SA"s48,s47,786432\n"
"s_and_b32 s48,s48,4194303\n"
BL"v238,s[16:19],s48 offen offset:0 lds\n"
BL"v239,s[16:19],s48 offen offset:1024 lds\n"
BL"v240,s[16:19],s48 offen offset:2048 lds\n"
BL"v241,s[16:19],s48 offen offset:3072 lds\n"
"v_bfe_u32 v131,v224,4,1\n"
"v_lshl_add_u32 v130,v131,3,v130\n"
"v_sqrt_f32 v189,v188\n"
"v_lshrrev_b32 v131,9,v224\n"
"v_lshl_add_u32 v130,v131,4,v130\n"
"v_rcp_f32 v190,v189\n"
"s_lshl_b32 s52,s30,5\n"
"v_add_u32 v130,s52,v130\n"
"v_fma_f32 v188,-v189,v189,v188\n"
MU"v190,0.5,v190\n"
"v_fma_f32 v189,v188,v190,v189\n"
PK"v189,v189,v189\n"
"v_lshlrev_b32 v189,16,v189\n"
"v_rcp_f32 v129,v189\n"
"s_nop 0\n"
MU"v148,v148,v129\n"
MU"v149,v149,v129\n"
PK"v138,v148,v149\n"
MU"v150,v150,v129\n"
MU"v151,v151,v129\n"
PK"v139,v150,v151\n"
"ds_write_b64 v130,v[138:139] offset:49152\n"
MU"v152,v152,v129\n"
MU"v153,v153,v129\n"
PK"v140,v152,v153\n"
MU"v154,v154,v129\n"
MU"v155,v155,v129\n"
PK"v141,v154,v155\n"
"ds_write_b64 v130,v[140:141] offset:51456\n"
MU"v156,v156,v129\n"
MU"v157,v157,v129\n"
PK"v142,v156,v157\n"
MU"v158,v158,v129\n"
MU"v159,v159,v129\n"
PK"v143,v158,v159\n"
"ds_write_b64 v130,v[142:143] offset:53760\n"
MU"v160,v160,v129\n"
MU"v161,v161,v129\n"
PK"v144,v160,v161\n"
MU"v162,v162,v129\n"
MU"v163,v163,v129\n"
PK"v145,v162,v163\n"
"ds_write_b64 v130,v[144:145] offset:56064\n"
MU"v164,v164,v129\n"
MU"v165,v165,v129\n"
PK"v146,v164,v165\n"
MU"v166,v166,v129\n"
MU"v167,v167,v129\n"
PK"v147,v166,v167\n"
"ds_write_b64 v130,v[146:147] offset:58368\n"
"v_lshlrev_b32 v148,16,v138\n"
"v_and_b32 v149,0xffff0000,v138\n"
MU"v188,v148,v168\n"
MU"v189,v149,v169\n"
"v_lshlrev_b32 v150,16,v139\n"
"v_and_b32 v151,0xffff0000,v139\n"
"v_fmac_f32 v188,v150,v170\n"
"v_fmac_f32 v189,v151,v171\n"
"v_lshlrev_b32 v152,16,v140\n"
"v_and_b32 v153,0xffff0000,v140\n"
"v_fmac_f32 v188,v152,v172\n"
"v_fmac_f32 v189,v153,v173\n"
"v_lshlrev_b32 v154,16,v141\n"
"v_and_b32 v155,0xffff0000,v141\n"
"v_fmac_f32 v188,v154,v174\n"
"v_fmac_f32 v189,v155,v175\n"
"v_lshlrev_b32 v156,16,v142\n"
"v_and_b32 v157,0xffff0000,v142\n"
"v_fmac_f32 v188,v156,v176\n"
"v_fmac_f32 v189,v157,v177\n"
"v_lshlrev_b32 v158,16,v143\n"
"v_and_b32 v159,0xffff0000,v143\n"
"v_fmac_f32 v188,v158,v178\n"
"v_fmac_f32 v189,v159,v179\n"
"v_lshlrev_b32 v160,16,v144\n"
"v_and_b32 v161,0xffff0000,v144\n"
"v_fmac_f32 v188,v160,v180\n"
"v_fmac_f32 v189,v161,v181\n"
"v_lshlrev_b32 v162,16,v145\n"
"v_and_b32 v163,0xffff0000,v145\n"
WT"vmcnt(21)\n"
RT"a[0:1],v246 offset:0\n"
RT"a[2:3],v246 offset:512\n"
RT"a[4:5],v247 offset:0\n"
RT"a[6:7],v247 offset:512\n"
RT"a[8:9],v248 offset:0\n"
RT"a[10:11],v248 offset:512\n"
RT"a[12:13],v249 offset:0\n"
RT"a[14:15],v249 offset:512\n"
WT"vmcnt(12)\n"
RT"a[16:17],v246 offset:4096\n"
RT"a[18:19],v246 offset:4608\n"
WT"lgkmcnt(10)\n"
"s_barrier\n"
RD"v[192:195],v242 offset:0\n"
RD"v[196:199],v242 offset:2048\n"
RD"v[200:203],v242 offset:4096\n"
RD"v[204:207],v242 offset:6144\n"
RD"v[208:211],v242 offset:8192\n"
"v_lshrrev_b32 v131,7,v224\n"
WT"lgkmcnt(4)\n"
MF"v[0:3],v[192:195],a[0:3],0\n"
SA"m0,s37,16384\n"
RD"v[212:215],v242 offset:10240\n"
"v_lshl_add_u32 v131,v131,4,v224\n"
MF"v[8:11],v[192:195],a[4:7],0\n"
SA"s48,s47,1048576\n"
"s_and_b32 s48,s48,4194303\n"
"s_mul_i32 s52,s30,1152\n"
MF"v[16:19],v[192:195],a[8:11],0\n"
RT"a[20:21],v247 offset:4096\n"
"v_add_u32 v128,s52,v131\n"
MF"v[24:27],v[192:195],a[12:15],0\n"
WT"lgkmcnt(5)\n"
BL"v238,s[16:19],s48 offen offset:0 lds\n"
MF"v[4:7],v[196:199],a[0:3],0\n"
"s_and_b32 s52,s30,1\n"
RD"v[216:219],v242 offset:12288\n"
"v_fmac_f32 v188,v162,v182\n"
MF"v[12:15],v[196:199],a[4:7],0\n"
"s_mul_i32 s51,s52,1152\n"
"v_fmac_f32 v189,v163,v183\n"
"v_add_u32 v131,s51,v131\n"
MF"v[20:23],v[196:199],a[8:11],0\n"
"s_lshl_b32 s52,s52,10\n"
RT"a[22:23],v247 offset:4608\n"
"v_lshlrev_b32 v164,16,v146\n"
MF"v[28:31],v[196:199],a[12:15],0\n"
WT"lgkmcnt(3)\n"
BL"v239,s[16:19],s48 offen offset:1024 lds\n"
MF"v[32:35],v[200:203],a[0:3],0\n"
RD"v[220:223],v242 offset:14336\n"
"v_and_b32 v129,0x70,v224\n"
MF"v[40:43],v[200:203],a[4:7],0\n"
"s_lshr_b32 s50,s31,1\n"
RD"v[132:135],v128 offset:49152\n"
"v_and_b32 v165,0xffff0000,v146\n"
MF"v[48:51],v[200:203],a[8:11],0\n"
"s_mul_i32 s50,s50,20480\n"
RD"a[240:243],v128 offset:53760\n"
"v_and_b32 v130,0x380,v224\n"
MF"v[56:59],v[200:203],a[12:15],0\n"
"s_and_b32 s51,s31,1\n"
RD"a[244:247],v131 offset:58368\n"
"v_fmac_f32 v188,v164,v184\n"
MF"v[36:39],v[204:207],a[0:3],0\n"
"s_lshl_b32 s51,s51,7\n"
RD"v[192:195],v243 offset:0\n"
"v_lshl_add_u32 v129,v130,1,v129\n"
MF"v[44:47],v[204:207],a[4:7],0\n"
SA"s50,s50,s51\n"
RT"a[24:25],v248 offset:4096\n"
"v_fmac_f32 v189,v165,v185\n"
MF"v[52:55],v[204:207],a[8:11],0\n"
SA"s50,s50,65536\n"
BL"v240,s[16:19],s48 offen offset:2048 lds\n"
MF"v[60:63],v[204:207],a[12:15],0\n"
"s_lshl_b32 s52,s52,1\n"
RD"v[196:199],v243 offset:2048\n"
"v_lshlrev_b32 v166,16,v147\n"
MF"v[64:67],v[208:211],a[0:3],0\n"
SA"s52,s52,s50\n"
RT"a[26:27],v248 offset:4608\n"
"v_and_b32 v167,0xffff0000,v147\n"
MF"v[72:75],v[208:211],a[4:7],0\n"
SA"s52,s52,16384\n"
BL"v241,s[16:19],s48 offen offset:3072 lds\n"
MF"v[80:83],v[208:211],a[8:11],0\n"
SA"m0,s36,32768\n"
RD"v[200:203],v243 offset:4096\n"
"v_fmac_f32 v188,v166,v186\n"
MF"v[88:91],v[208:211],a[12:15],0\n"
SA"s48,s46,256\n"
RT"a[28:29],v249 offset:4096\n"
"v_fmac_f32 v189,v167,v187\n"
MF"v[68:71],v[212:215],a[0:3],0\n"
"s_and_b32 s48,s48,1023\n"
"v_add_f32 v188,v188,v189\n"
"s_lshl_b32 s51,s30,11\n"
MF"v[76:79],v[212:215],a[4:7],0\n"
SA"s50,s50,s51\n"
BL"v234,s[12:15],s48 offen offset:0 lds\n"
MF"v[84:87],v[212:215],a[8:11],0\n"
SA"s51,s50,8192\n"
RD"v[204:207],v243 offset:6144\n"
"v_add_f32_dpp v188,v188,v188 quad_perm:[1,0,3,2] row_mask:0xf bank_mask:0xf\n"
MF"v[92:95],v[212:215],a[12:15],0\n"
WT"lgkmcnt(3)\n"
RT"a[30:31],v249 offset:4608\n"
"v_add_f32_dpp v188,v188,v188 quad_perm:[2,3,0,1] row_mask:0xf bank_mask:0xf\n"
MF"v[96:99],v[216:219],a[0:3],0\n"
BS"v[132:135],v129,s[8:11],s50 offen sc1\n"
MF"v[104:107],v[216:219],a[4:7],0\n"
"s_lshl_b32 s90,s31,2\n"
RD"v[208:211],v243 offset:8192\n"
"v_add_f32_dpp v188,v188,v188 row_half_mirror row_mask:0xf bank_mask:0xf\n"
MF"v[112:115],v[216:219],a[8:11],0\n"
SA"s90,s90,s30\n"
BL"v235,s[12:15],s48 offen offset:1024 lds\n"
MF"v[120:123],v[216:219],a[12:15],0\n"
"s_lshl_b32 s90,s90,1\n"
BS"a[240:243],v129,s[8:11],s51 offen sc1\n"
MF"v[100:103],v[220:223],a[0:3],0\n"
"s_sub_u32 s91,2047,s90\n"
WT"vmcnt(16)\n"
RT"a[32:33],v246 offset:8192\n"
MF"v[108:111],v[220:223],a[4:7],0\n"
BS"a[244:247],v129,s[8:11],s52 offen sc1\n"
MF"v[116:119],v[220:223],a[8:11],0\n"
RD"v[212:215],v243 offset:10240\n"
"v_add_f32_dpp v188,v188,v188 row_mirror row_mask:0xf bank_mask:0xf\n"
MF"v[124:127],v[220:223],a[12:15],0\n"
BL"v236,s[12:15],s48 offen offset:2048 lds\n"
MF"v[0:3],v[192:195],a[16:19],v[0:3]\n"
RT"a[34:35],v246 offset:8704\n"
"v_mov_b32 v189,v188\n"
MF"v[8:11],v[192:195],a[20:23],v[8:11]\n"
RD"v[216:219],v243 offset:12288\n"
"v_permlane16_swap_b32 v189,v188\n"
MF"v[16:19],v[192:195],a[24:27],v[16:19]\n"
WT"lgkmcnt(4)\n"
BL"v237,s[12:15],s48 offen offset:3072 lds\n"
MF"v[24:27],v[192:195],a[28:31],v[24:27]\n"
"s_lshl_b32 s90,s90,2\n"
RT"a[36:37],v247 offset:8192\n"
"v_add_f32 v188,v188,v189\n"
MF"v[4:7],v[196:199],a[16:19],v[4:7]\n"
SA"s90,s90,2686976\n"
RD"v[220:223],v243 offset:14336\n"
PK"v188,v188,v188\n"
MF"v[12:15],v[196:199],a[20:23],v[12:15]\n"
SA"m0,s37,20480\n"
"v_lshlrev_b32 v188,16,v188\n"
"v_ashrrev_i32 v189,31,v188\n"
MF"v[20:23],v[196:199],a[24:27],v[20:23]\n"
SA"s48,s47,1310720\n"
RT"a[38:39],v247 offset:8704\n"
"v_or_b32 v189,0x80000000,v189\n"
MF"v[28:31],v[196:199],a[28:31],v[28:31]\n"
"s_and_b32 s48,s48,4194303\n"
"v_xor_b32 v188,v188,v189\n"
"v_and_b32 v188,0xffff0000,v188\n"
MF"v[32:35],v[200:203],a[16:19],v[32:35]\n"
BL"v238,s[16:19],s48 offen offset:0 lds\n"
MF"v[40:43],v[200:203],a[20:23],v[40:43]\n"
"v_lshrrev_b32 v189,9,v224\n"
"v_sub_u32 v190,s91,v189\n"
MF"v[48:51],v[200:203],a[24:27],v[48:51]\n"
RT"a[40:41],v248 offset:8192\n"
"v_or_b32 v188,v188,v190\n"
MF"v[56:59],v[200:203],a[28:31],v[56:59]\n"
BL"v239,s[16:19],s48 offen offset:1024 lds\n"
MF"v[36:39],v[204:207],a[16:19],v[36:39]\n"
"v_and_b32 v190,0x1f0,v224\n"
"v_lshlrev_b32 v190,23,v190\n"
MF"v[44:47],v[204:207],a[20:23],v[44:47]\n"
WT"vmcnt(17) lgkmcnt(2)\n"
"s_barrier\n"
MF"v[52:55],v[204:207],a[24:27],v[52:55]\n"
RD"v[192:195],v242 offset:16384\n"
"v_lshl_add_u32 v190,v189,2,v190\n"
MF"v[60:63],v[204:207],a[28:31],v[60:63]\n"
"buffer_store_dword v188,v190,s[8:11],s90 offen sc1\n"
MF"v[64:67],v[208:211],a[16:19],v[64:67]\n"
RT"a[42:43],v248 offset:8704\n"
MF"v[72:75],v[208:211],a[20:23],v[72:75]\n"
BL"v240,s[16:19],s48 offen offset:2048 lds\n"
MF"v[80:83],v[208:211],a[24:27],v[80:83]\n"
RD"v[196:199],v242 offset:18432\n"
MF"v[88:91],v[208:211],a[28:31],v[88:91]\n"
RT"a[44:45],v249 offset:8192\n"
MF"v[68:71],v[212:215],a[16:19],v[68:71]\n"
BL"v241,s[16:19],s48 offen offset:3072 lds\n"
MF"v[76:79],v[212:215],a[20:23],v[76:79]\n"
RD"v[200:203],v242 offset:20480\n"
MF"v[84:87],v[212:215],a[24:27],v[84:87]\n"
SA"m0,s37,0\n"
RT"a[46:47],v249 offset:8704\n"
SA"s48,s47,1572864\n"
MF"v[92:95],v[212:215],a[28:31],v[92:95]\n"
"s_and_b32 s48,s48,4194303\n"
MF"v[96:99],v[216:219],a[16:19],v[96:99]\n"
BL"v238,s[16:19],s48 offen offset:0 lds\n"
MF"v[104:107],v[216:219],a[20:23],v[104:107]\n"
RD"v[204:207],v242 offset:22528\n"
MF"v[112:115],v[216:219],a[24:27],v[112:115]\n"
WT"vmcnt(17)\n"
RT"a[48:49],v246 offset:12288\n"
MF"v[120:123],v[216:219],a[28:31],v[120:123]\n"
MF"v[100:103],v[220:223],a[16:19],v[100:103]\n"
RD"v[208:211],v242 offset:24576\n"
MF"v[108:111],v[220:223],a[20:23],v[108:111]\n"
BL"v239,s[16:19],s48 offen offset:1024 lds\n"
MF"v[116:119],v[220:223],a[24:27],v[116:119]\n"
RT"a[50:51],v246 offset:12800\n"
MF"v[124:127],v[220:223],a[28:31],v[124:127]\n"
WT"lgkmcnt(4)\n"
MF"v[0:3],v[192:195],a[32:35],v[0:3]\n"
RD"v[212:215],v242 offset:26624\n"
MF"v[8:11],v[192:195],a[36:39],v[8:11]\n"
BL"v240,s[16:19],s48 offen offset:2048 lds\n"
MF"v[16:19],v[192:195],a[40:43],v[16:19]\n"
RT"a[52:53],v247 offset:12288\n"
MF"v[24:27],v[192:195],a[44:47],v[24:27]\n"
RD"v[216:219],v242 offset:28672\n"
MF"v[4:7],v[196:199],a[32:35],v[4:7]\n"
MF"v[12:15],v[196:199],a[36:39],v[12:15]\n"
BL"v241,s[16:19],s48 offen offset:3072 lds\n"
MF"v[20:23],v[196:199],a[40:43],v[20:23]\n"
RT"a[54:55],v247 offset:12800\n"
MF"v[28:31],v[196:199],a[44:47],v[28:31]\n"
RD"v[220:223],v242 offset:30720\n"
MF"v[32:35],v[200:203],a[32:35],v[32:35]\n"
SA"m0,s36,0\n"
SA"s48,s46,384\n"
"s_and_b32 s48,s48,1023\n"
MF"v[40:43],v[200:203],a[36:39],v[40:43]\n"
MF"v[48:51],v[200:203],a[40:43],v[48:51]\n"
RD"v[192:195],v243 offset:16384\n"
MF"v[56:59],v[200:203],a[44:47],v[56:59]\n"
WT"lgkmcnt(3)\n"
RT"a[56:57],v248 offset:12288\n"
MF"v[36:39],v[204:207],a[32:35],v[36:39]\n"
BL"v234,s[12:15],s48 offen offset:0 lds\n"
MF"v[44:47],v[204:207],a[36:39],v[44:47]\n"
MF"v[52:55],v[204:207],a[40:43],v[52:55]\n"
RD"v[196:199],v243 offset:18432\n"
MF"v[60:63],v[204:207],a[44:47],v[60:63]\n"
RT"a[58:59],v248 offset:12800\n"
MF"v[64:67],v[208:211],a[32:35],v[64:67]\n"
BL"v235,s[12:15],s48 offen offset:1024 lds\n"
MF"v[72:75],v[208:211],a[36:39],v[72:75]\n"
RD"v[200:203],v243 offset:20480\n"
MF"v[80:83],v[208:211],a[40:43],v[80:83]\n"
RT"a[60:61],v249 offset:12288\n"
MF"v[88:91],v[208:211],a[44:47],v[88:91]\n"
BL"v236,s[12:15],s48 offen offset:2048 lds\n"
MF"v[68:71],v[212:215],a[32:35],v[68:71]\n"
MF"v[76:79],v[212:215],a[36:39],v[76:79]\n"
RD"v[204:207],v243 offset:22528\n"
MF"v[84:87],v[212:215],a[40:43],v[84:87]\n"
RT"a[62:63],v249 offset:12800\n"
MF"v[92:95],v[212:215],a[44:47],v[92:95]\n"
BL"v237,s[12:15],s48 offen offset:3072 lds\n"
MF"v[96:99],v[216:219],a[32:35],v[96:99]\n"
RD"v[208:211],v243 offset:24576\n"
MF"v[104:107],v[216:219],a[36:39],v[104:107]\n"
MF"v[112:115],v[216:219],a[40:43],v[112:115]\n"
SA"m0,s37,4096\n"
WT"vmcnt(20)\n"
RT"a[64:65],v246 offset:16384\n"
MF"v[120:123],v[216:219],a[44:47],v[120:123]\n"
WT"lgkmcnt(4)\n"
SA"s48,s47,1835008\n"
"s_and_b32 s48,s48,4194303\n"
MF"v[100:103],v[220:223],a[32:35],v[100:103]\n"
RD"v[212:215],v243 offset:26624\n"
MF"v[108:111],v[220:223],a[36:39],v[108:111]\n"
BL"v238,s[16:19],s48 offen offset:0 lds\n"
MF"v[116:119],v[220:223],a[40:43],v[116:119]\n"
RT"a[66:67],v246 offset:16896\n"
MF"v[124:127],v[220:223],a[44:47],v[124:127]\n"
RD"v[216:219],v243 offset:28672\n"
MF"v[0:3],v[192:195],a[48:51],v[0:3]\n"
BL"v239,s[16:19],s48 offen offset:1024 lds\n"
MF"v[8:11],v[192:195],a[52:55],v[8:11]\n"
MF"v[16:19],v[192:195],a[56:59],v[16:19]\n"
WT"lgkmcnt(3)\n"
RT"a[68:69],v247 offset:16384\n"
MF"v[24:27],v[192:195],a[60:63],v[24:27]\n"
RD"v[220:223],v243 offset:30720\n"
MF"v[4:7],v[196:199],a[48:51],v[4:7]\n"
MF"v[12:15],v[196:199],a[52:55],v[12:15]\n"
BL"v240,s[16:19],s48 offen offset:2048 lds\n"
MF"v[20:23],v[196:199],a[56:59],v[20:23]\n"
RT"a[70:71],v247 offset:16896\n"
MF"v[28:31],v[196:199],a[60:63],v[28:31]\n"
MF"v[32:35],v[200:203],a[48:51],v[32:35]\n"
"s_lshl_b32 s51,s32,8\n"
"s_andn2_b32 s50,s31,7\n"
SA"s50,s50,s51\n"
MF"v[40:43],v[200:203],a[52:55],v[40:43]\n"
SA"s50,s50,256\n"
BL"v241,s[16:19],s48 offen offset:3072 lds\n"
MF"v[48:51],v[200:203],a[56:59],v[48:51]\n"
SA"s50,s50,s45\n"
RT"a[72:73],v248 offset:16384\n"
MF"v[56:59],v[200:203],a[60:63],v[56:59]\n"
MF"v[36:39],v[204:207],a[48:51],v[36:39]\n"
SA"m0,s37,8192\n"
SA"s48,s47,2097152\n"
"s_and_b32 s48,s48,4194303\n"
MF"v[44:47],v[204:207],a[52:55],v[44:47]\n"
WT"vmcnt(14) lgkmcnt(2)\n"
"s_barrier\n"
MF"v[52:55],v[204:207],a[56:59],v[52:55]\n"
RD"v[192:195],v242 offset:32768\n"
MF"v[60:63],v[204:207],a[60:63],v[60:63]\n"
RT"a[74:75],v248 offset:16896\n"
MF"v[64:67],v[208:211],a[48:51],v[64:67]\n"
BL"v238,s[16:19],s48 offen offset:0 lds\n"
MF"v[72:75],v[208:211],a[52:55],v[72:75]\n"
"buffer_store_dwordx2 v[232:233],v230,s[8:11],s50 offen sc1\n"
MF"v[80:83],v[208:211],a[56:59],v[80:83]\n"
RD"v[196:199],v242 offset:34816\n"
MF"v[88:91],v[208:211],a[60:63],v[88:91]\n"
RT"a[76:77],v249 offset:16384\n"
MF"v[68:71],v[212:215],a[48:51],v[68:71]\n"
BL"v239,s[16:19],s48 offen offset:1024 lds\n"
MF"v[76:79],v[212:215],a[52:55],v[76:79]\n"
RD"v[200:203],v242 offset:36864\n"
MF"v[84:87],v[212:215],a[56:59],v[84:87]\n"
RT"a[78:79],v249 offset:16896\n"
MF"v[92:95],v[212:215],a[60:63],v[92:95]\n"
BL"v240,s[16:19],s48 offen offset:2048 lds\n"
MF"v[96:99],v[216:219],a[48:51],v[96:99]\n"
MF"v[104:107],v[216:219],a[52:55],v[104:107]\n"
RD"v[204:207],v242 offset:38912\n"
MF"v[112:115],v[216:219],a[56:59],v[112:115]\n"
WT"vmcnt(16)\n"
RT"a[80:81],v246 offset:20480\n"
MF"v[120:123],v[216:219],a[60:63],v[120:123]\n"
BL"v241,s[16:19],s48 offen offset:3072 lds\n"
MF"v[100:103],v[220:223],a[48:51],v[100:103]\n"
RD"v[208:211],v242 offset:40960\n"
MF"v[108:111],v[220:223],a[52:55],v[108:111]\n"
MF"v[116:119],v[220:223],a[56:59],v[116:119]\n"
SA"m0,s36,16384\n"
RT"a[82:83],v246 offset:20992\n"
SA"s48,s46,512\n"
MF"v[124:127],v[220:223],a[60:63],v[124:127]\n"
WT"lgkmcnt(4)\n"
"s_and_b32 s48,s48,1023\n"
MF"v[0:3],v[192:195],a[64:67],v[0:3]\n"
RD"v[212:215],v242 offset:43008\n"
MF"v[8:11],v[192:195],a[68:71],v[8:11]\n"
BL"v234,s[12:15],s48 offen offset:0 lds\n"
MF"v[16:19],v[192:195],a[72:75],v[16:19]\n"
RT"a[84:85],v247 offset:20480\n"
MF"v[24:27],v[192:195],a[76:79],v[24:27]\n"
RD"v[216:219],v242 offset:45056\n"
MF"v[4:7],v[196:199],a[64:67],v[4:7]\n"
BL"v235,s[12:15],s48 offen offset:1024 lds\n"
MF"v[12:15],v[196:199],a[68:71],v[12:15]\n"
MF"v[20:23],v[196:199],a[72:75],v[20:23]\n"
RT"a[86:87],v247 offset:20992\n"
MF"v[28:31],v[196:199],a[76:79],v[28:31]\n"
RD"v[220:223],v242 offset:47104\n"
MF"v[32:35],v[200:203],a[64:67],v[32:35]\n"
MF"v[40:43],v[200:203],a[68:71],v[40:43]\n"
BL"v236,s[12:15],s48 offen offset:2048 lds\n"
MF"v[48:51],v[200:203],a[72:75],v[48:51]\n"
RD"v[192:195],v243 offset:32768\n"
MF"v[56:59],v[200:203],a[76:79],v[56:59]\n"
WT"lgkmcnt(3)\n"
RT"a[88:89],v248 offset:20480\n"
MF"v[36:39],v[204:207],a[64:67],v[36:39]\n"
MF"v[44:47],v[204:207],a[68:71],v[44:47]\n"
BL"v237,s[12:15],s48 offen offset:3072 lds\n"
MF"v[52:55],v[204:207],a[72:75],v[52:55]\n"
RD"v[196:199],v243 offset:34816\n"
MF"v[60:63],v[204:207],a[76:79],v[60:63]\n"
RT"a[90:91],v248 offset:20992\n"
MF"v[64:67],v[208:211],a[64:67],v[64:67]\n"
SA"m0,s37,12288\n"
SA"s48,s47,2359296\n"
"s_and_b32 s48,s48,4194303\n"
MF"v[72:75],v[208:211],a[68:71],v[72:75]\n"
RD"v[200:203],v243 offset:36864\n"
MF"v[80:83],v[208:211],a[72:75],v[80:83]\n"
RT"a[92:93],v249 offset:20480\n"
MF"v[88:91],v[208:211],a[76:79],v[88:91]\n"
BL"v238,s[16:19],s48 offen offset:0 lds\n"
MF"v[68:71],v[212:215],a[64:67],v[68:71]\n"
MF"v[76:79],v[212:215],a[68:71],v[76:79]\n"
RD"v[204:207],v243 offset:38912\n"
MF"v[84:87],v[212:215],a[72:75],v[84:87]\n"
RT"a[94:95],v249 offset:20992\n"
MF"v[92:95],v[212:215],a[76:79],v[92:95]\n"
BL"v239,s[16:19],s48 offen offset:1024 lds\n"
MF"v[96:99],v[216:219],a[64:67],v[96:99]\n"
RD"v[208:211],v243 offset:40960\n"
MF"v[104:107],v[216:219],a[68:71],v[104:107]\n"
MF"v[112:115],v[216:219],a[72:75],v[112:115]\n"
WT"vmcnt(19)\n"
RT"a[96:97],v246 offset:0\n"
MF"v[120:123],v[216:219],a[76:79],v[120:123]\n"
WT"lgkmcnt(4)\n"
BL"v240,s[16:19],s48 offen offset:2048 lds\n"
MF"v[100:103],v[220:223],a[64:67],v[100:103]\n"
RD"v[212:215],v243 offset:43008\n"
MF"v[108:111],v[220:223],a[68:71],v[108:111]\n"
MF"v[116:119],v[220:223],a[72:75],v[116:119]\n"
RT"a[98:99],v246 offset:512\n"
MF"v[124:127],v[220:223],a[76:79],v[124:127]\n"
RD"v[216:219],v243 offset:45056\n"
MF"v[0:3],v[192:195],a[80:83],v[0:3]\n"
BL"v241,s[16:19],s48 offen offset:3072 lds\n"
MF"v[8:11],v[192:195],a[84:87],v[8:11]\n"
MF"v[16:19],v[192:195],a[88:91],v[16:19]\n"
WT"lgkmcnt(3)\n"
RT"a[100:101],v247 offset:0\n"
SA"m0,s37,16384\n"
MF"v[24:27],v[192:195],a[92:95],v[24:27]\n"
SA"s48,s47,2621440\n"
RD"v[220:223],v243 offset:47104\n"
"s_and_b32 s48,s48,4194303\n"
MF"v[4:7],v[196:199],a[80:83],v[4:7]\n"
BL"v238,s[16:19],s48 offen offset:0 lds\n"
MF"v[12:15],v[196:199],a[84:87],v[12:15]\n"
MF"v[20:23],v[196:199],a[88:91],v[20:23]\n"
RT"a[102:103],v247 offset:512\n"
MF"v[28:31],v[196:199],a[92:95],v[28:31]\n"
MF"v[32:35],v[200:203],a[80:83],v[32:35]\n"
BL"v239,s[16:19],s48 offen offset:1024 lds\n"
MF"v[40:43],v[200:203],a[84:87],v[40:43]\n"
MF"v[48:51],v[200:203],a[88:91],v[48:51]\n"
RT"a[104:105],v248 offset:0\n"
MF"v[56:59],v[200:203],a[92:95],v[56:59]\n"
MF"v[36:39],v[204:207],a[80:83],v[36:39]\n"
MF"v[44:47],v[204:207],a[84:87],v[44:47]\n"
WT"vmcnt(19) lgkmcnt(2)\n"
"s_barrier\n"
MF"v[52:55],v[204:207],a[88:91],v[52:55]\n"
BL"v240,s[16:19],s48 offen offset:2048 lds\n"
MF"v[60:63],v[204:207],a[92:95],v[60:63]\n"
RD"v[192:195],v242 offset:0\n"
MF"v[64:67],v[208:211],a[80:83],v[64:67]\n"
RT"a[106:107],v248 offset:512\n"
MF"v[72:75],v[208:211],a[84:87],v[72:75]\n"
BL"v241,s[16:19],s48 offen offset:3072 lds\n"
MF"v[80:83],v[208:211],a[88:91],v[80:83]\n"
RD"v[196:199],v242 offset:2048\n"
MF"v[88:91],v[208:211],a[92:95],v[88:91]\n"
RT"a[108:109],v249 offset:0\n"
MF"v[68:71],v[212:215],a[80:83],v[68:71]\n"
SA"m0,s36,32768\n"
SA"s48,s46,640\n"
"s_and_b32 s48,s48,1023\n"
MF"v[76:79],v[212:215],a[84:87],v[76:79]\n"
RD"v[200:203],v242 offset:4096\n"
MF"v[84:87],v[212:215],a[88:91],v[84:87]\n"
RT"a[110:111],v249 offset:512\n"
MF"v[92:95],v[212:215],a[92:95],v[92:95]\n"
BL"v234,s[12:15],s48 offen offset:0 lds\n"
MF"v[96:99],v[216:219],a[80:83],v[96:99]\n"
MF"v[104:107],v[216:219],a[84:87],v[104:107]\n"
RD"v[204:207],v242 offset:6144\n"
MF"v[112:115],v[216:219],a[88:91],v[112:115]\n"
WT"vmcnt(18)\n"
RT"a[112:113],v246 offset:4096\n"
MF"v[120:123],v[216:219],a[92:95],v[120:123]\n"
BL"v235,s[12:15],s48 offen offset:1024 lds\n"
MF"v[100:103],v[220:223],a[80:83],v[100:103]\n"
RD"v[208:211],v242 offset:8192\n"
MF"v[108:111],v[220:223],a[84:87],v[108:111]\n"
MF"v[116:119],v[220:223],a[88:91],v[116:119]\n"
RT"a[114:115],v246 offset:4608\n"
MF"v[124:127],v[220:223],a[92:95],v[124:127]\n"
WT"lgkmcnt(4)\n"
BL"v236,s[12:15],s48 offen offset:2048 lds\n"
MF"v[0:3],v[192:195],a[96:99],v[0:3]\n"
RD"v[212:215],v242 offset:10240\n"
MF"v[8:11],v[192:195],a[100:103],v[8:11]\n"
MF"v[16:19],v[192:195],a[104:107],v[16:19]\n"
RT"a[116:117],v247 offset:4096\n"
MF"v[24:27],v[192:195],a[108:111],v[24:27]\n"
RD"v[216:219],v242 offset:12288\n"
MF"v[4:7],v[196:199],a[96:99],v[4:7]\n"
BL"v237,s[12:15],s48 offen offset:3072 lds\n"
MF"v[12:15],v[196:199],a[100:103],v[12:15]\n"
MF"v[20:23],v[196:199],a[104:107],v[20:23]\n"
SA"m0,s37,20480\n"
RT"a[118:119],v247 offset:4608\n"
SA"s48,s47,2883584\n"
MF"v[28:31],v[196:199],a[108:111],v[28:31]\n"
"s_and_b32 s48,s48,4194303\n"
RD"v[220:223],v242 offset:14336\n"
MF"v[32:35],v[200:203],a[96:99],v[32:35]\n"
BL"v238,s[16:19],s48 offen offset:0 lds\n"
MF"v[40:43],v[200:203],a[100:103],v[40:43]\n"
MF"v[48:51],v[200:203],a[104:107],v[48:51]\n"
RD"v[192:195],v243 offset:0\n"
MF"v[56:59],v[200:203],a[108:111],v[56:59]\n"
WT"lgkmcnt(3)\n"
RT"a[120:121],v248 offset:4096\n"
MF"v[36:39],v[204:207],a[96:99],v[36:39]\n"
BL"v239,s[16:19],s48 offen offset:1024 lds\n"
MF"v[44:47],v[204:207],a[100:103],v[44:47]\n"
MF"v[52:55],v[204:207],a[104:107],v[52:55]\n"
RD"v[196:199],v243 offset:2048\n"
MF"v[60:63],v[204:207],a[108:111],v[60:63]\n"
RT"a[122:123],v248 offset:4608\n"
MF"v[64:67],v[208:211],a[96:99],v[64:67]\n"
MF"v[72:75],v[208:211],a[100:103],v[72:75]\n"
RD"v[200:203],v243 offset:4096\n"
MF"v[80:83],v[208:211],a[104:107],v[80:83]\n"
BL"v240,s[16:19],s48 offen offset:2048 lds\n"
MF"v[88:91],v[208:211],a[108:111],v[88:91]\n"
RT"a[124:125],v249 offset:4096\n"
MF"v[68:71],v[212:215],a[96:99],v[68:71]\n"
MF"v[76:79],v[212:215],a[100:103],v[76:79]\n"
RD"v[204:207],v243 offset:6144\n"
MF"v[84:87],v[212:215],a[104:107],v[84:87]\n"
BL"v241,s[16:19],s48 offen offset:3072 lds\n"
MF"v[92:95],v[212:215],a[108:111],v[92:95]\n"
RT"a[126:127],v249 offset:4608\n"
MF"v[96:99],v[216:219],a[96:99],v[96:99]\n"
SA"m0,s37,0\n"
RD"v[208:211],v243 offset:8192\n"
SA"s48,s47,3145728\n"
MF"v[104:107],v[216:219],a[100:103],v[104:107]\n"
"s_and_b32 s48,s48,4194303\n"
MF"v[112:115],v[216:219],a[104:107],v[112:115]\n"
WT"vmcnt(20)\n"
RT"a[128:129],v246 offset:8192\n"
MF"v[120:123],v[216:219],a[108:111],v[120:123]\n"
WT"lgkmcnt(4)\n"
BL"v238,s[16:19],s48 offen offset:0 lds\n"
MF"v[100:103],v[220:223],a[96:99],v[100:103]\n"
RD"v[212:215],v243 offset:10240\n"
MF"v[108:111],v[220:223],a[100:103],v[108:111]\n"
MF"v[116:119],v[220:223],a[104:107],v[116:119]\n"
RT"a[130:131],v246 offset:8704\n"
MF"v[124:127],v[220:223],a[108:111],v[124:127]\n"
BL"v239,s[16:19],s48 offen offset:1024 lds\n"
MF"v[0:3],v[192:195],a[112:115],v[0:3]\n"
RD"v[216:219],v243 offset:12288\n"
MF"v[8:11],v[192:195],a[116:119],v[8:11]\n"
MF"v[16:19],v[192:195],a[120:123],v[16:19]\n"
WT"lgkmcnt(3)\n"
RT"a[132:133],v247 offset:8192\n"
MF"v[24:27],v[192:195],a[124:127],v[24:27]\n"
RD"v[220:223],v243 offset:14336\n"
MF"v[4:7],v[196:199],a[112:115],v[4:7]\n"
BL"v240,s[16:19],s48 offen offset:2048 lds\n"
MF"v[12:15],v[196:199],a[116:119],v[12:15]\n"
MF"v[20:23],v[196:199],a[120:123],v[20:23]\n"
RT"a[134:135],v247 offset:8704\n"
MF"v[28:31],v[196:199],a[124:127],v[28:31]\n"
BL"v241,s[16:19],s48 offen offset:3072 lds\n"
MF"v[32:35],v[200:203],a[112:115],v[32:35]\n"
MF"v[40:43],v[200:203],a[116:119],v[40:43]\n"
MF"v[48:51],v[200:203],a[120:123],v[48:51]\n"
RT"a[136:137],v248 offset:8192\n"
MF"v[56:59],v[200:203],a[124:127],v[56:59]\n"
MF"v[36:39],v[204:207],a[112:115],v[36:39]\n"
MF"v[44:47],v[204:207],a[116:119],v[44:47]\n"
WT"vmcnt(20) lgkmcnt(2)\n"
"s_barrier\n"
MF"v[52:55],v[204:207],a[120:123],v[52:55]\n"
SA"m0,s36,0\n"
RD"v[192:195],v242 offset:16384\n"
SA"s48,s46,768\n"
MF"v[60:63],v[204:207],a[124:127],v[60:63]\n"
"s_and_b32 s48,s48,1023\n"
RT"a[138:139],v248 offset:8704\n"
MF"v[64:67],v[208:211],a[112:115],v[64:67]\n"
BL"v234,s[12:15],s48 offen offset:0 lds\n"
MF"v[72:75],v[208:211],a[116:119],v[72:75]\n"
MF"v[80:83],v[208:211],a[120:123],v[80:83]\n"
RD"v[196:199],v242 offset:18432\n"
MF"v[88:91],v[208:211],a[124:127],v[88:91]\n"
RT"a[140:141],v249 offset:8192\n"
MF"v[68:71],v[212:215],a[112:115],v[68:71]\n"
BL"v235,s[12:15],s48 offen offset:1024 lds\n"
MF"v[76:79],v[212:215],a[116:119],v[76:79]\n"
RD"v[200:203],v242 offset:20480\n"
MF"v[84:87],v[212:215],a[120:123],v[84:87]\n"
RT"a[142:143],v249 offset:8704\n"
MF"v[92:95],v[212:215],a[124:127],v[92:95]\n"
MF"v[96:99],v[216:219],a[112:115],v[96:99]\n"
MF"v[104:107],v[216:219],a[116:119],v[104:107]\n"
RD"v[204:207],v242 offset:22528\n"
MF"v[112:115],v[216:219],a[120:123],v[112:115]\n"
BL"v236,s[12:15],s48 offen offset:2048 lds\n"
MF"v[120:123],v[216:219],a[124:127],v[120:123]\n"
WT"vmcnt(19)\n"
RT"a[144:145],v246 offset:12288\n"
MF"v[100:103],v[220:223],a[112:115],v[100:103]\n"
RD"v[208:211],v242 offset:24576\n"
MF"v[108:111],v[220:223],a[116:119],v[108:111]\n"
BL"v237,s[12:15],s48 offen offset:3072 lds\n"
MF"v[116:119],v[220:223],a[120:123],v[116:119]\n"
RT"a[146:147],v246 offset:12800\n"
MF"v[124:127],v[220:223],a[124:127],v[124:127]\n"
WT"lgkmcnt(4)\n"
MF"v[0:3],v[192:195],a[128:131],v[0:3]\n"
SA"m0,s37,4096\n"
RD"v[212:215],v242 offset:26624\n"
SA"s48,s47,3407872\n"
MF"v[8:11],v[192:195],a[132:135],v[8:11]\n"
"s_and_b32 s48,s48,4194303\n"
MF"v[16:19],v[192:195],a[136:139],v[16:19]\n"
RT"a[148:149],v247 offset:12288\n"
MF"v[24:27],v[192:195],a[140:143],v[24:27]\n"
BL"v238,s[16:19],s48 offen offset:0 lds\n"
MF"v[4:7],v[196:199],a[128:131],v[4:7]\n"
RD"v[216:219],v242 offset:28672\n"
MF"v[12:15],v[196:199],a[132:135],v[12:15]\n"
MF"v[20:23],v[196:199],a[136:139],v[20:23]\n"
RT"a[150:151],v247 offset:12800\n"
MF"v[28:31],v[196:199],a[140:143],v[28:31]\n"
BL"v239,s[16:19],s48 offen offset:1024 lds\n"
MF"v[32:35],v[200:203],a[128:131],v[32:35]\n"
RD"v[220:223],v242 offset:30720\n"
MF"v[40:43],v[200:203],a[132:135],v[40:43]\n"
MF"v[48:51],v[200:203],a[136:139],v[48:51]\n"
RD"v[192:195],v243 offset:16384\n"
MF"v[56:59],v[200:203],a[140:143],v[56:59]\n"
WT"lgkmcnt(3)\n"
RT"a[152:153],v248 offset:12288\n"
MF"v[36:39],v[204:207],a[128:131],v[36:39]\n"
BL"v240,s[16:19],s48 offen offset:2048 lds\n"
MF"v[44:47],v[204:207],a[132:135],v[44:47]\n"
MF"v[52:55],v[204:207],a[136:139],v[52:55]\n"
RD"v[196:199],v243 offset:18432\n"
MF"v[60:63],v[204:207],a[140:143],v[60:63]\n"
RT"a[154:155],v248 offset:12800\n"
MF"v[64:67],v[208:211],a[128:131],v[64:67]\n"
BL"v241,s[16:19],s48 offen offset:3072 lds\n"
MF"v[72:75],v[208:211],a[132:135],v[72:75]\n"
RD"v[200:203],v243 offset:20480\n"
MF"v[80:83],v[208:211],a[136:139],v[80:83]\n"
SA"m0,s37,8192\n"
RT"a[156:157],v249 offset:12288\n"
SA"s48,s47,3670016\n"
MF"v[88:91],v[208:211],a[140:143],v[88:91]\n"
"s_and_b32 s48,s48,4194303\n"
MF"v[68:71],v[212:215],a[128:131],v[68:71]\n"
BL"v238,s[16:19],s48 offen offset:0 lds\n"
MF"v[76:79],v[212:215],a[132:135],v[76:79]\n"
RD"v[204:207],v243 offset:22528\n"
MF"v[84:87],v[212:215],a[136:139],v[84:87]\n"
RT"a[158:159],v249 offset:12800\n"
MF"v[92:95],v[212:215],a[140:143],v[92:95]\n"
MF"v[96:99],v[216:219],a[128:131],v[96:99]\n"
RD"v[208:211],v243 offset:24576\n"
MF"v[104:107],v[216:219],a[132:135],v[104:107]\n"
BL"v239,s[16:19],s48 offen offset:1024 lds\n"
MF"v[112:115],v[216:219],a[136:139],v[112:115]\n"
WT"vmcnt(22)\n"
RT"a[160:161],v246 offset:16384\n"
MF"v[120:123],v[216:219],a[140:143],v[120:123]\n"
WT"lgkmcnt(4)\n"
MF"v[100:103],v[220:223],a[128:131],v[100:103]\n"
RD"v[212:215],v243 offset:26624\n"
MF"v[108:111],v[220:223],a[132:135],v[108:111]\n"
BL"v240,s[16:19],s48 offen offset:2048 lds\n"
MF"v[116:119],v[220:223],a[136:139],v[116:119]\n"
RT"a[162:163],v246 offset:16896\n"
MF"v[124:127],v[220:223],a[140:143],v[124:127]\n"
RD"v[216:219],v243 offset:28672\n"
MF"v[0:3],v[192:195],a[144:147],v[0:3]\n"
MF"v[8:11],v[192:195],a[148:151],v[8:11]\n"
BL"v241,s[16:19],s48 offen offset:3072 lds\n"
MF"v[16:19],v[192:195],a[152:155],v[16:19]\n"
WT"lgkmcnt(3)\n"
RT"a[164:165],v247 offset:16384\n"
MF"v[24:27],v[192:195],a[156:159],v[24:27]\n"
RD"v[220:223],v243 offset:30720\n"
MF"v[4:7],v[196:199],a[144:147],v[4:7]\n"
MF"v[12:15],v[196:199],a[148:151],v[12:15]\n"
MF"v[20:23],v[196:199],a[152:155],v[20:23]\n"
RT"a[166:167],v247 offset:16896\n"
MF"v[28:31],v[196:199],a[156:159],v[28:31]\n"
MF"v[32:35],v[200:203],a[144:147],v[32:35]\n"
MF"v[40:43],v[200:203],a[148:151],v[40:43]\n"
MF"v[48:51],v[200:203],a[152:155],v[48:51]\n"
RT"a[168:169],v248 offset:16384\n"
MF"v[56:59],v[200:203],a[156:159],v[56:59]\n"
MF"v[36:39],v[204:207],a[144:147],v[36:39]\n"
MF"v[44:47],v[204:207],a[148:151],v[44:47]\n"
WT"vmcnt(20) lgkmcnt(2)\n"
"s_barrier\n"
MF"v[52:55],v[204:207],a[152:155],v[52:55]\n"
SA"m0,s36,16384\n"
RD"v[192:195],v242 offset:32768\n"
SA"s48,s46,896\n"
MF"v[60:63],v[204:207],a[156:159],v[60:63]\n"
"s_and_b32 s48,s48,1023\n"
RT"a[170:171],v248 offset:16896\n"
MF"v[64:67],v[208:211],a[144:147],v[64:67]\n"
BL"v234,s[12:15],s48 offen offset:0 lds\n"
MF"v[72:75],v[208:211],a[148:151],v[72:75]\n"
MF"v[80:83],v[208:211],a[152:155],v[80:83]\n"
RD"v[196:199],v242 offset:34816\n"
MF"v[88:91],v[208:211],a[156:159],v[88:91]\n"
RT"a[172:173],v249 offset:16384\n"
MF"v[68:71],v[212:215],a[144:147],v[68:71]\n"
BL"v235,s[12:15],s48 offen offset:1024 lds\n"
MF"v[76:79],v[212:215],a[148:151],v[76:79]\n"
RD"v[200:203],v242 offset:36864\n"
MF"v[84:87],v[212:215],a[152:155],v[84:87]\n"
RT"a[174:175],v249 offset:16896\n"
MF"v[92:95],v[212:215],a[156:159],v[92:95]\n"
MF"v[96:99],v[216:219],a[144:147],v[96:99]\n"
MF"v[104:107],v[216:219],a[148:151],v[104:107]\n"
RD"v[204:207],v242 offset:38912\n"
MF"v[112:115],v[216:219],a[152:155],v[112:115]\n"
BL"v236,s[12:15],s48 offen offset:2048 lds\n"
MF"v[120:123],v[216:219],a[156:159],v[120:123]\n"
WT"vmcnt(19)\n"
RT"a[176:177],v246 offset:20480\n"
MF"v[100:103],v[220:223],a[144:147],v[100:103]\n"
RD"v[208:211],v242 offset:40960\n"
MF"v[108:111],v[220:223],a[148:151],v[108:111]\n"
BL"v237,s[12:15],s48 offen offset:3072 lds\n"
MF"v[116:119],v[220:223],a[152:155],v[116:119]\n"
RT"a[178:179],v246 offset:20992\n"
MF"v[124:127],v[220:223],a[156:159],v[124:127]\n"
WT"lgkmcnt(4)\n"
MF"v[0:3],v[192:195],a[160:163],v[0:3]\n"
SA"m0,s37,12288\n"
RD"v[212:215],v242 offset:43008\n"
SA"s48,s47,3932160\n"
MF"v[8:11],v[192:195],a[164:167],v[8:11]\n"
"s_and_b32 s48,s48,4194303\n"
MF"v[16:19],v[192:195],a[168:171],v[16:19]\n"
RT"a[180:181],v247 offset:20480\n"
MF"v[24:27],v[192:195],a[172:175],v[24:27]\n"
BL"v238,s[16:19],s48 offen offset:0 lds\n"
MF"v[4:7],v[196:199],a[160:163],v[4:7]\n"
RD"v[216:219],v242 offset:45056\n"
MF"v[12:15],v[196:199],a[164:167],v[12:15]\n"
MF"v[20:23],v[196:199],a[168:171],v[20:23]\n"
RT"a[182:183],v247 offset:20992\n"
MF"v[28:31],v[196:199],a[172:175],v[28:31]\n"
BL"v239,s[16:19],s48 offen offset:1024 lds\n"
MF"v[32:35],v[200:203],a[160:163],v[32:35]\n"
RD"v[220:223],v242 offset:47104\n"
MF"v[40:43],v[200:203],a[164:167],v[40:43]\n"
MF"v[48:51],v[200:203],a[168:171],v[48:51]\n"
RD"v[192:195],v243 offset:32768\n"
MF"v[56:59],v[200:203],a[172:175],v[56:59]\n"
WT"lgkmcnt(3)\n"
RT"a[184:185],v248 offset:20480\n"
MF"v[36:39],v[204:207],a[160:163],v[36:39]\n"
BL"v240,s[16:19],s48 offen offset:2048 lds\n"
MF"v[44:47],v[204:207],a[164:167],v[44:47]\n"
MF"v[52:55],v[204:207],a[168:171],v[52:55]\n"
RD"v[196:199],v243 offset:34816\n"
MF"v[60:63],v[204:207],a[172:175],v[60:63]\n"
RT"a[186:187],v248 offset:20992\n"
MF"v[64:67],v[208:211],a[160:163],v[64:67]\n"
BL"v241,s[16:19],s48 offen offset:3072 lds\n"
MF"v[72:75],v[208:211],a[164:167],v[72:75]\n"
RD"v[200:203],v243 offset:36864\n"
MF"v[80:83],v[208:211],a[168:171],v[80:83]\n"
SA"m0,s37,16384\n"
RT"a[188:189],v249 offset:20480\n"
SA"s48,s39,0\n"
MF"v[88:91],v[208:211],a[172:175],v[88:91]\n"
MF"v[68:71],v[212:215],a[160:163],v[68:71]\n"
BL"v234,s[12:15],s48 offen offset:0 lds\n"
MF"v[76:79],v[212:215],a[164:167],v[76:79]\n"
RD"v[204:207],v243 offset:38912\n"
MF"v[84:87],v[212:215],a[168:171],v[84:87]\n"
RT"a[190:191],v249 offset:20992\n"
MF"v[92:95],v[212:215],a[172:175],v[92:95]\n"
MF"v[96:99],v[216:219],a[160:163],v[96:99]\n"
RD"v[208:211],v243 offset:40960\n"
MF"v[104:107],v[216:219],a[164:167],v[104:107]\n"
BL"v235,s[12:15],s48 offen offset:1024 lds\n"
MF"v[112:115],v[216:219],a[168:171],v[112:115]\n"
WT"vmcnt(22)\n"
RT"a[192:193],v246 offset:0\n"
MF"v[120:123],v[216:219],a[172:175],v[120:123]\n"
WT"lgkmcnt(4)\n"
MF"v[100:103],v[220:223],a[160:163],v[100:103]\n"
RD"v[212:215],v243 offset:43008\n"
MF"v[108:111],v[220:223],a[164:167],v[108:111]\n"
BL"v236,s[12:15],s48 offen offset:2048 lds\n"
MF"v[116:119],v[220:223],a[168:171],v[116:119]\n"
RT"a[194:195],v246 offset:512\n"
MF"v[124:127],v[220:223],a[172:175],v[124:127]\n"
RD"v[216:219],v243 offset:45056\n"
MF"v[0:3],v[192:195],a[176:179],v[0:3]\n"
MF"v[8:11],v[192:195],a[180:183],v[8:11]\n"
BL"v237,s[12:15],s48 offen offset:3072 lds\n"
MF"v[16:19],v[192:195],a[184:187],v[16:19]\n"
WT"lgkmcnt(3)\n"
RT"a[196:197],v247 offset:0\n"
MF"v[24:27],v[192:195],a[188:191],v[24:27]\n"
RD"v[220:223],v243 offset:47104\n"
MF"v[4:7],v[196:199],a[176:179],v[4:7]\n"
SA"m0,s37,20480\n"
SA"s48,s98,0\n"
MF"v[12:15],v[196:199],a[180:183],v[12:15]\n"
MF"v[20:23],v[196:199],a[184:187],v[20:23]\n"
RT"a[198:199],v247 offset:512\n"
MF"v[28:31],v[196:199],a[188:191],v[28:31]\n"
BL"v234,s[12:15],s48 offen offset:0 lds\n"
MF"v[32:35],v[200:203],a[176:179],v[32:35]\n"
MF"v[40:43],v[200:203],a[180:183],v[40:43]\n"
MF"v[48:51],v[200:203],a[184:187],v[48:51]\n"
RT"a[200:201],v248 offset:0\n"
MF"v[56:59],v[200:203],a[188:191],v[56:59]\n"
BL"v235,s[12:15],s48 offen offset:1024 lds\n"
MF"v[36:39],v[204:207],a[176:179],v[36:39]\n"
MF"v[44:47],v[204:207],a[180:183],v[44:47]\n"
WT"vmcnt(22) lgkmcnt(2)\n"
"s_barrier\n"
MF"v[52:55],v[204:207],a[184:187],v[52:55]\n"
RD"v[192:195],v242 offset:0\n"
MF"v[60:63],v[204:207],a[188:191],v[60:63]\n"
RT"a[202:203],v248 offset:512\n"
MF"v[64:67],v[208:211],a[176:179],v[64:67]\n"
BL"v236,s[12:15],s48 offen offset:2048 lds\n"
MF"v[72:75],v[208:211],a[180:183],v[72:75]\n"
MF"v[80:83],v[208:211],a[184:187],v[80:83]\n"
RD"v[196:199],v242 offset:2048\n"
MF"v[88:91],v[208:211],a[188:191],v[88:91]\n"
RT"a[204:205],v249 offset:0\n"
MF"v[68:71],v[212:215],a[176:179],v[68:71]\n"
BL"v237,s[12:15],s48 offen offset:3072 lds\n"
MF"v[76:79],v[212:215],a[180:183],v[76:79]\n"
RD"v[200:203],v242 offset:4096\n"
MF"v[84:87],v[212:215],a[184:187],v[84:87]\n"
RT"a[206:207],v249 offset:512\n"
MF"v[92:95],v[212:215],a[188:191],v[92:95]\n"
MF"v[96:99],v[216:219],a[176:179],v[96:99]\n"
MF"v[104:107],v[216:219],a[180:183],v[104:107]\n"
RD"v[204:207],v242 offset:6144\n"
MF"v[112:115],v[216:219],a[184:187],v[112:115]\n"
SA"m0,s37,0\n"
WT"vmcnt(20)\n"
RT"a[208:209],v246 offset:4096\n"
MF"v[120:123],v[216:219],a[188:191],v[120:123]\n"
SA"s48,s39,32768\n"
MF"v[100:103],v[220:223],a[176:179],v[100:103]\n"
RD"v[208:211],v242 offset:8192\n"
MF"v[108:111],v[220:223],a[180:183],v[108:111]\n"
WT"lgkmcnt(3)\n"
BL"v234,s[12:15],s48 offen offset:0 lds\n"
MF"v[116:119],v[220:223],a[184:187],v[116:119]\n"
RT"a[210:211],v246 offset:4608\n"
MF"v[124:127],v[220:223],a[188:191],v[124:127]\n"
MF"v[0:3],v[192:195],a[192:195],v[0:3]\n"
RD"v[212:215],v242 offset:10240\n"
MF"v[8:11],v[192:195],a[196:199],v[8:11]\n"
BL"v235,s[12:15],s48 offen offset:1024 lds\n"
MF"v[16:19],v[192:195],a[200:203],v[16:19]\n"
RT"a[212:213],v247 offset:4096\n"
MF"v[24:27],v[192:195],a[204:207],v[24:27]\n"
RD"v[216:219],v242 offset:12288\n"
MF"v[4:7],v[196:199],a[192:195],v[4:7]\n"
MF"v[12:15],v[196:199],a[196:199],v[12:15]\n"
BL"v236,s[12:15],s48 offen offset:2048 lds\n"
MF"v[20:23],v[196:199],a[200:203],v[20:23]\n"
RT"a[214:215],v247 offset:4608\n"
MF"v[28:31],v[196:199],a[204:207],v[28:31]\n"
BL"v[160:163],v224,s[8:11],0 offen offset:256 sc1\n"
MF"v[32:35],v[200:203],a[192:195],v[32:35]\n"
RD"v[220:223],v242 offset:14336\n"
MF"v[40:43],v[200:203],a[196:199],v[40:43]\n"
BL"v[164:167],v224,s[8:11],0 offen offset:1280 sc1\n"
MF"v[48:51],v[200:203],a[200:203],v[48:51]\n"
BL"v237,s[12:15],s48 offen offset:3072 lds\n"
MF"v[56:59],v[200:203],a[204:207],v[56:59]\n"
WT"lgkmcnt(2)\n"
RD"v[192:195],v243 offset:0\n"
MF"v[36:39],v[204:207],a[192:195],v[36:39]\n"
RT"a[216:217],v248 offset:4096\n"
MF"v[44:47],v[204:207],a[196:199],v[44:47]\n"
MF"v[52:55],v[204:207],a[200:203],v[52:55]\n"
RD"v[196:199],v243 offset:2048\n"
MF"v[60:63],v[204:207],a[204:207],v[60:63]\n"
RT"a[218:219],v248 offset:4608\n"
MF"v[64:67],v[208:211],a[192:195],v[64:67]\n"
MF"v[72:75],v[208:211],a[196:199],v[72:75]\n"
RD"v[200:203],v243 offset:4096\n"
MF"v[80:83],v[208:211],a[200:203],v[80:83]\n"
RT"a[220:221],v249 offset:4096\n"
MF"v[88:91],v[208:211],a[204:207],v[88:91]\n"
MF"v[68:71],v[212:215],a[192:195],v[68:71]\n"
MF"v[76:79],v[212:215],a[196:199],v[76:79]\n"
RD"v[204:207],v243 offset:6144\n"
MF"v[84:87],v[212:215],a[200:203],v[84:87]\n"
RT"a[222:223],v249 offset:4608\n"
MF"v[92:95],v[212:215],a[204:207],v[92:95]\n"
MF"v[96:99],v[216:219],a[192:195],v[96:99]\n"
RD"v[208:211],v243 offset:8192\n"
MF"v[104:107],v[216:219],a[196:199],v[104:107]\n"
MF"v[112:115],v[216:219],a[200:203],v[112:115]\n"
SA"m0,s37,4096\n"
WT"vmcnt(22)\n"
RT"a[224:225],v246 offset:8192\n"
MF"v[120:123],v[216:219],a[204:207],v[120:123]\n"
WT"lgkmcnt(4)\n"
SA"s48,s98,32768\n"
MF"v[100:103],v[220:223],a[192:195],v[100:103]\n"
RD"v[212:215],v243 offset:10240\n"
MF"v[108:111],v[220:223],a[196:199],v[108:111]\n"
WT"lgkmcnt(3)\n"
BL"v234,s[12:15],s48 offen offset:0 lds\n"
MF"v[116:119],v[220:223],a[200:203],v[116:119]\n"
RT"a[226:227],v246 offset:8704\n"
MF"v[124:127],v[220:223],a[204:207],v[124:127]\n"
RD"v[216:219],v243 offset:12288\n"
MF"v[0:3],v[192:195],a[208:211],v[0:3]\n"
BL"v235,s[12:15],s48 offen offset:1024 lds\n"
MF"v[8:11],v[192:195],a[212:215],v[8:11]\n"
MF"v[16:19],v[192:195],a[216:219],v[16:19]\n"
RT"a[228:229],v247 offset:8192\n"
MF"v[24:27],v[192:195],a[220:223],v[24:27]\n"
RD"v[220:223],v243 offset:14336\n"
MF"v[4:7],v[196:199],a[208:211],v[4:7]\n"
MF"v[12:15],v[196:199],a[212:215],v[12:15]\n"
BL"v236,s[12:15],s48 offen offset:2048 lds\n"
MF"v[20:23],v[196:199],a[216:219],v[20:23]\n"
RT"a[230:231],v247 offset:8704\n"
MF"v[28:31],v[196:199],a[220:223],v[28:31]\n"
MF"v[32:35],v[200:203],a[208:211],v[32:35]\n"
MF"v[40:43],v[200:203],a[212:215],v[40:43]\n"
BL"v237,s[12:15],s48 offen offset:3072 lds\n"
MF"v[48:51],v[200:203],a[216:219],v[48:51]\n"
RT"a[232:233],v248 offset:8192\n"
MF"v[56:59],v[200:203],a[220:223],v[56:59]\n"
MF"v[36:39],v[204:207],a[208:211],v[36:39]\n"
MF"v[44:47],v[204:207],a[212:215],v[44:47]\n"
WT"vmcnt(22) lgkmcnt(2)\n"
"s_barrier\n"
MF"v[52:55],v[204:207],a[216:219],v[52:55]\n"
RD"v[192:195],v242 offset:16384\n"
MF"v[60:63],v[204:207],a[220:223],v[60:63]\n"
RT"a[234:235],v248 offset:8704\n"
MF"v[64:67],v[208:211],a[208:211],v[64:67]\n"
MF"v[72:75],v[208:211],a[212:215],v[72:75]\n"
MF"v[80:83],v[208:211],a[216:219],v[80:83]\n"
RD"v[196:199],v242 offset:18432\n"
MF"v[88:91],v[208:211],a[220:223],v[88:91]\n"
RT"a[236:237],v249 offset:8192\n"
MF"v[68:71],v[212:215],a[208:211],v[68:71]\n"
MF"v[76:79],v[212:215],a[212:215],v[76:79]\n"
RD"v[200:203],v242 offset:20480\n"
MF"v[84:87],v[212:215],a[216:219],v[84:87]\n"
RT"a[238:239],v249 offset:8704\n"
MF"v[92:95],v[212:215],a[220:223],v[92:95]\n"
MF"v[96:99],v[216:219],a[208:211],v[96:99]\n"
"s_lshl_b32 s50,s35,20\n"
MF"v[104:107],v[216:219],a[212:215],v[104:107]\n"
"s_lshl_b32 s51,s34,15\n"
RD"v[204:207],v242 offset:22528\n"
MF"v[112:115],v[216:219],a[216:219],v[112:115]\n"
SA"s50,s50,s51\n"
WT"vmcnt(18)\n"
RT"a[240:241],v246 offset:12288\n"
MF"v[120:123],v[216:219],a[220:223],v[120:123]\n"
SA"m0,s37,8192\n"
SA"s48,s39,65536\n"
"s_and_b32 s51,s30,1\n"
MF"v[100:103],v[220:223],a[208:211],v[100:103]\n"
"s_lshl_b32 s51,s51,12\n"
RD"v[208:211],v242 offset:24576\n"
MF"v[108:111],v[220:223],a[212:215],v[108:111]\n"
WT"lgkmcnt(3)\n"
BL"v234,s[12:15],s48 offen offset:0 lds\n"
MF"v[116:119],v[220:223],a[216:219],v[116:119]\n"
SA"s50,s50,s51\n"
RT"a[242:243],v246 offset:12800\n"
SA"s50,s50,9699328\n"
MF"v[124:127],v[220:223],a[220:223],v[124:127]\n"
"s_mov_b32 s94,2686976\n"
WT"vmcnt(6)\n"
"v_cmp_eq_u64 vcc,s[28:29],v[160:161]\n"
"v_cmp_eq_u64_e64 s[62:63],s[28:29],v[162:163]\n"
"s_and_b64 vcc,vcc,s[62:63]\n"
"v_cmp_eq_u64_e64 s[62:63],s[28:29],v[164:165]\n"
"s_and_b64 vcc,vcc,s[62:63]\n"
"v_cmp_eq_u64_e64 s[62:63],s[28:29],v[166:167]\n"
"s_and_b64 vcc,vcc,s[62:63]\n"
"s_cmp_eq_u64 vcc,exec\n"
"s_cbranch_scc0 L1004_%=\n"
"L1005_%=:\n"
MF"v[0:3],v[192:195],a[224:227],v[0:3]\n"
"s_mov_b32 s95,2691072\n"
RD"v[212:215],v242 offset:26624\n"
"v_add_u32 v250,s50,v224\n"
MF"v[8:11],v[192:195],a[228:231],v[8:11]\n"
BL"v235,s[12:15],s48 offen offset:1024 lds\n"
MF"v[16:19],v[192:195],a[232:235],v[16:19]\n"
BL"v[160:163],v224,s[8:11],s94 offen offset:0 sc1\n"
MF"v[24:27],v[192:195],a[236:239],v[24:27]\n"
RT"a[244:245],v247 offset:12288\n"
MF"v[4:7],v[196:199],a[224:227],v[4:7]\n"
BL"v[164:167],v224,s[8:11],s94 offen offset:1024 sc1\n"
MF"v[12:15],v[196:199],a[228:231],v[12:15]\n"
RD"v[216:219],v242 offset:28672\n"
MF"v[20:23],v[196:199],a[232:235],v[20:23]\n"
BL"v[168:171],v224,s[8:11],s94 offen offset:2048 sc1\n"
MF"v[28:31],v[196:199],a[236:239],v[28:31]\n"
BL"v236,s[12:15],s48 offen offset:2048 lds\n"
MF"v[32:35],v[200:203],a[224:227],v[32:35]\n"
RT"a[246:247],v247 offset:12800\n"
MF"v[40:43],v[200:203],a[228:231],v[40:43]\n"
BL"v[172:175],v224,s[8:11],s94 offen offset:3072 sc1\n"
MF"v[48:51],v[200:203],a[232:235],v[48:51]\n"
RD"v[220:223],v242 offset:30720\n"
MF"v[56:59],v[200:203],a[236:239],v[56:59]\n"
WT"lgkmcnt(3)\n"
BL"v[176:179],v224,s[8:11],s95 offen offset:0 sc1\n"
MF"v[36:39],v[204:207],a[224:227],v[36:39]\n"
BL"v237,s[12:15],s48 offen offset:3072 lds\n"
MF"v[44:47],v[204:207],a[228:231],v[44:47]\n"
RD"v[192:195],v243 offset:16384\n"
MF"v[52:55],v[204:207],a[232:235],v[52:55]\n"
RT"a[248:249],v248 offset:12288\n"
MF"v[60:63],v[204:207],a[236:239],v[60:63]\n"
BL"v[180:183],v224,s[8:11],s95 offen offset:1024 sc1\n"
MF"v[64:67],v[208:211],a[224:227],v[64:67]\n"
BL"v[184:187],v224,s[8:11],s95 offen offset:2048 sc1\n"
MF"v[72:75],v[208:211],a[228:231],v[72:75]\n"
RD"v[196:199],v243 offset:18432\n"
MF"v[80:83],v[208:211],a[232:235],v[80:83]\n"
RT"a[250:251],v248 offset:12800\n"
MF"v[88:91],v[208:211],a[236:239],v[88:91]\n"
BL"v[188:191],v224,s[8:11],s95 offen offset:3072 sc1\n"
MF"v[68:71],v[212:215],a[224:227],v[68:71]\n"
RD"v[200:203],v243 offset:20480\n"
MF"v[76:79],v[212:215],a[228:231],v[76:79]\n"
RT"a[252:253],v249 offset:12288\n"
MF"v[84:87],v[212:215],a[232:235],v[84:87]\n"
RD"v[204:207],v243 offset:22528\n"
MF"v[92:95],v[212:215],a[236:239],v[92:95]\n"
WT"lgkmcnt(5)\n"
RT"a[254:255],v249 offset:12800\n"
"s_lshr_b32 s50,s30,1\n"
MF"v[96:99],v[216:219],a[224:227],v[96:99]\n"
"s_and_b32 s51,s30,1\n"
RD"v[208:211],v243 offset:24576\n"
MF"v[104:107],v[216:219],a[228:231],v[104:107]\n"
"s_mul_i32 s38,s50,24576\n"
MF"v[112:115],v[216:219],a[232:235],v[112:115]\n"
SA"m0,s37,12288\n"
SA"s48,s98,65536\n"
"s_lshl_b32 s60,s50,18\n"
MF"v[120:123],v[216:219],a[236:239],v[120:123]\n"
"s_lshl_b32 s50,s51,12\n"
MF"v[100:103],v[220:223],a[224:227],v[100:103]\n"
SA"s38,s38,s50\n"
RD"v[212:215],v243 offset:26624\n"
MF"v[108:111],v[220:223],a[228:231],v[108:111]\n"
WT"lgkmcnt(2)\n"
BL"v234,s[12:15],s48 offen offset:0 lds\n"
MF"v[116:119],v[220:223],a[232:235],v[116:119]\n"
"s_lshl_b32 s50,s51,7\n"
SA"s60,s60,s50\n"
MF"v[124:127],v[220:223],a[236:239],v[124:127]\n"
RD"v[216:219],v243 offset:28672\n"
MF"v[0:3],v[192:195],a[240:243],v[0:3]\n"
BL"v235,s[12:15],s48 offen offset:1024 lds\n"
MF"v[8:11],v[192:195],a[244:247],v[8:11]\n"
"s_sub_u32 s99,s34,5\n"
MF"v[16:19],v[192:195],a[248:251],v[16:19]\n"
"s_cmp_lt_u32 s34,5\n"
"s_cselect_b32 s99,s34,s99\n"
MF"v[24:27],v[192:195],a[252:255],v[24:27]\n"
"s_mul_i32 s99,s99,1\n"
RD"v[220:223],v243 offset:30720\n"
MF"v[4:7],v[196:199],a[240:243],v[4:7]\n"
"s_lshl_b32 s61,s99,20\n"
MF"v[12:15],v[196:199],a[244:247],v[12:15]\n"
BL"v236,s[12:15],s48 offen offset:2048 lds\n"
MF"v[20:23],v[196:199],a[248:251],v[20:23]\n"
MF"v[28:31],v[196:199],a[252:255],v[28:31]\n"
MF"v[32:35],v[200:203],a[240:243],v[32:35]\n"
MF"v[40:43],v[200:203],a[244:247],v[40:43]\n"
BL"v237,s[12:15],s48 offen offset:3072 lds\n"
MF"v[48:51],v[200:203],a[248:251],v[48:51]\n"
"v_add_u32 v244,114688,v242\n"
"v_add_u32 v245,114688,v243\n"
MF"v[56:59],v[200:203],a[252:255],v[56:59]\n"
"v_add_u32 v242,65536,v242\n"
"v_add_u32 v243,65536,v243\n"
MF"v[36:39],v[204:207],a[240:243],v[36:39]\n"
MF"v[44:47],v[204:207],a[244:247],v[44:47]\n"
WT"lgkmcnt(0)\n"
"s_barrier\n"
MF"v[52:55],v[204:207],a[248:251],v[52:55]\n"
RD"v[192:195],v242 offset:16384\n"
MF"v[60:63],v[204:207],a[252:255],v[60:63]\n"
MF"v[64:67],v[208:211],a[240:243],v[64:67]\n"
MF"v[72:75],v[208:211],a[244:247],v[72:75]\n"
MF"v[80:83],v[208:211],a[248:251],v[80:83]\n"
RD"v[196:199],v242 offset:18432\n"
MF"v[88:91],v[208:211],a[252:255],v[88:91]\n"
MF"v[68:71],v[212:215],a[240:243],v[68:71]\n"
MF"v[76:79],v[212:215],a[244:247],v[76:79]\n"
MF"v[84:87],v[212:215],a[248:251],v[84:87]\n"
RD"v[200:203],v243 offset:16384\n"
MF"v[92:95],v[212:215],a[252:255],v[92:95]\n"
MF"v[96:99],v[216:219],a[240:243],v[96:99]\n"
MF"v[104:107],v[216:219],a[244:247],v[104:107]\n"
MF"v[112:115],v[216:219],a[248:251],v[112:115]\n"
RD"v[204:207],v243 offset:18432\n"
"v_mov_b32 v227,0\n"
MF"v[120:123],v[216:219],a[252:255],v[120:123]\n"
"s_cmp_eq_u32 s31,1\n"
WT"vmcnt(13)\n"
"v_mov_b32 v226,v160\n"
MF"v[100:103],v[220:223],a[240:243],v[100:103]\n"
"s_cselect_b32 s94,0,2130706432\n"
"v_mov_b32 v228,0\n"
"v_med3_u32 v228,v227,v228,v161\n"
MF"v[108:111],v[220:223],a[244:247],v[108:111]\n"
"buffer_store_dwordx2 v[232:233],v230,s[8:11],s94 offen sc1\n"
MF"v[116:119],v[220:223],a[248:251],v[116:119]\n"
RD"v[208:211],v242 offset:20480\n"
"v_med3_u32 v227,v226,v227,v161\n"
MF"v[124:127],v[220:223],a[252:255],v[124:127]\n"
WT"lgkmcnt(2)\n"
"v_max_u32 v226,v226,v161\n"
"v_med3_u32 v228,v227,v228,v162\n"
MF"v[128:131],v[192:195],a[0:3],0\n"
"v_med3_u32 v227,v226,v227,v162\n"
"v_max_u32 v226,v226,v162\n"
MF"v[136:139],v[192:195],a[4:7],0\n"
RD"v[212:215],v242 offset:22528\n"
"v_med3_u32 v228,v227,v228,v163\n"
MF"v[144:147],v[192:195],a[8:11],0\n"
"v_med3_u32 v227,v226,v227,v163\n"
"v_max_u32 v226,v226,v163\n"
MF"v[152:155],v[192:195],a[12:15],0\n"
PK"v0,v0,v1\n"
PK"v1,v2,v3\n"
MF"v[132:135],v[196:199],a[0:3],0\n"
WT"vmcnt(13)\n"
"v_med3_u32 v228,v227,v228,v164\n"
PK"v2,v4,v5\n"
MF"v[140:143],v[196:199],a[4:7],0\n"
RD"v[216:219],v243 offset:20480\n"
PK"v3,v6,v7\n"
MF"v[148:151],v[196:199],a[8:11],0\n"
"v_med3_u32 v227,v226,v227,v164\n"
"v_max_u32 v226,v226,v164\n"
MF"v[156:159],v[196:199],a[12:15],0\n"
"v_med3_u32 v228,v227,v228,v165\n"
"v_med3_u32 v227,v226,v227,v165\n"
MF"v[128:131],v[200:203],a[16:19],v[128:131]\n"
"v_max_u32 v226,v226,v165\n"
"v_med3_u32 v228,v227,v228,v166\n"
MF"v[136:139],v[200:203],a[20:23],v[136:139]\n"
RD"v[220:223],v243 offset:22528\n"
PK"v8,v8,v9\n"
MF"v[144:147],v[200:203],a[24:27],v[144:147]\n"
PK"v9,v10,v11\n"
"v_med3_u32 v227,v226,v227,v166\n"
MF"v[152:155],v[200:203],a[28:31],v[152:155]\n"
WT"lgkmcnt(2)\n"
PK"v10,v12,v13\n"
PK"v11,v14,v15\n"
MF"v[132:135],v[204:207],a[16:19],v[132:135]\n"
RD"v[192:195],v242 offset:40960\n"
"v_max_u32 v226,v226,v166\n"
MF"v[140:143],v[204:207],a[20:23],v[140:143]\n"
"v_med3_u32 v228,v227,v228,v167\n"
"v_med3_u32 v227,v226,v227,v167\n"
MF"v[148:151],v[204:207],a[24:27],v[148:151]\n"
"v_max_u32 v226,v226,v167\n"
PK"v16,v16,v17\n"
MF"v[156:159],v[204:207],a[28:31],v[156:159]\n"
WT"vmcnt(12)\n"
"v_med3_u32 v228,v227,v228,v168\n"
"v_med3_u32 v227,v226,v227,v168\n"
MF"v[128:131],v[208:211],a[32:35],v[128:131]\n"
"s_mov_b32 s49,0\n"
RD"v[196:199],v242 offset:43008\n"
PK"v17,v18,v19\n"
MF"v[136:139],v[208:211],a[36:39],v[136:139]\n"
"v_max_u32 v226,v226,v168\n"
"v_med3_u32 v228,v227,v228,v169\n"
MF"v[144:147],v[208:211],a[40:43],v[144:147]\n"
BS"v[0:3],v250,s[8:11],s49 offen offset:0 sc1\n"
MF"v[152:155],v[208:211],a[44:47],v[152:155]\n"
PK"v18,v20,v21\n"
PK"v19,v22,v23\n"
MF"v[132:135],v[212:215],a[32:35],v[132:135]\n"
RD"v[200:203],v243 offset:40960\n"
"v_med3_u32 v227,v226,v227,v169\n"
MF"v[140:143],v[212:215],a[36:39],v[140:143]\n"
"v_max_u32 v226,v226,v169\n"
"v_med3_u32 v228,v227,v228,v170\n"
MF"v[148:151],v[212:215],a[40:43],v[148:151]\n"
"v_med3_u32 v227,v226,v227,v170\n"
"v_max_u32 v226,v226,v170\n"
MF"v[156:159],v[212:215],a[44:47],v[156:159]\n"
WT"lgkmcnt(1)\n"
PK"v24,v24,v25\n"
PK"v25,v26,v27\n"
MF"v[128:131],v[216:219],a[48:51],v[128:131]\n"
RD"v[204:207],v243 offset:43008\n"
"v_med3_u32 v228,v227,v228,v171\n"
MF"v[136:139],v[216:219],a[52:55],v[136:139]\n"
"v_med3_u32 v227,v226,v227,v171\n"
PK"v26,v28,v29\n"
MF"v[144:147],v[216:219],a[56:59],v[144:147]\n"
SA"m0,s36,0\n"
PK"v27,v30,v31\n"
"v_max_u32 v226,v226,v171\n"
MF"v[152:155],v[216:219],a[60:63],v[152:155]\n"
SA"s48,s60,s61\n"
RD"v[208:211],v242 offset:45056\n"
PK"v32,v32,v33\n"
MF"v[132:135],v[220:223],a[48:51],v[132:135]\n"
BL"v238,s[20:23],s48 offen offset:0 lds\n"
MF"v[140:143],v[220:223],a[52:55],v[140:143]\n"
WT"vmcnt(12)\n"
"v_med3_u32 v228,v227,v228,v172\n"
"v_med3_u32 v227,v226,v227,v172\n"
MF"v[148:151],v[220:223],a[56:59],v[148:151]\n"
"v_max_u32 v226,v226,v172\n"
"v_med3_u32 v228,v227,v228,v173\n"
MF"v[156:159],v[220:223],a[60:63],v[156:159]\n"
RD"v[212:215],v242 offset:47104\n"
PK"v33,v34,v35\n"
MF"v[128:131],v[192:195],a[64:67],v[128:131]\n"
"v_med3_u32 v227,v226,v227,v173\n"
"v_max_u32 v226,v226,v173\n"
MF"v[136:139],v[192:195],a[68:71],v[136:139]\n"
PK"v34,v36,v37\n"
PK"v35,v38,v39\n"
MF"v[144:147],v[192:195],a[72:75],v[144:147]\n"
"v_med3_u32 v228,v227,v228,v174\n"
"v_med3_u32 v227,v226,v227,v174\n"
MF"v[152:155],v[192:195],a[76:79],v[152:155]\n"
RD"v[216:219],v243 offset:45056\n"
"v_max_u32 v226,v226,v174\n"
MF"v[132:135],v[196:199],a[64:67],v[132:135]\n"
BL"v239,s[20:23],s48 offen offset:1024 lds\n"
MF"v[140:143],v[196:199],a[68:71],v[140:143]\n"
"v_med3_u32 v228,v227,v228,v175\n"
"v_med3_u32 v227,v226,v227,v175\n"
MF"v[148:151],v[196:199],a[72:75],v[148:151]\n"
"v_max_u32 v226,v226,v175\n"
PK"v40,v40,v41\n"
MF"v[156:159],v[196:199],a[76:79],v[156:159]\n"
WT"lgkmcnt(1)\n"
RD"v[220:223],v243 offset:47104\n"
PK"v41,v42,v43\n"
MF"v[128:131],v[200:203],a[80:83],v[128:131]\n"
WT"vmcnt(12)\n"
"v_med3_u32 v228,v227,v228,v176\n"
PK"v42,v44,v45\n"
MF"v[136:139],v[200:203],a[84:87],v[136:139]\n"
PK"v43,v46,v47\n"
"v_med3_u32 v227,v226,v227,v176\n"
MF"v[144:147],v[200:203],a[88:91],v[144:147]\n"
RD"v[192:195],v244 offset:16384\n"
"v_max_u32 v226,v226,v176\n"
MF"v[152:155],v[200:203],a[92:95],v[152:155]\n"
"v_med3_u32 v228,v227,v228,v177\n"
"v_med3_u32 v227,v226,v227,v177\n"
MF"v[132:135],v[204:207],a[80:83],v[132:135]\n"
BL"v240,s[20:23],s48 offen offset:2048 lds\n"
MF"v[140:143],v[204:207],a[84:87],v[140:143]\n"
"v_max_u32 v226,v226,v177\n"
"v_med3_u32 v228,v227,v228,v178\n"
MF"v[148:151],v[204:207],a[88:91],v[148:151]\n"
RD"v[196:199],v244 offset:18432\n"
PK"v48,v48,v49\n"
MF"v[156:159],v[204:207],a[92:95],v[156:159]\n"
PK"v49,v50,v51\n"
"v_med3_u32 v227,v226,v227,v178\n"
MF"v[128:131],v[208:211],a[96:99],v[128:131]\n"
PK"v50,v52,v53\n"
PK"v51,v54,v55\n"
MF"v[136:139],v[208:211],a[100:103],v[136:139]\n"
BS"v[8:11],v250,s[8:11],s49 offen offset:1024 sc1\n"
MF"v[144:147],v[208:211],a[104:107],v[144:147]\n"
RD"v[200:203],v245 offset:16384\n"
"v_max_u32 v226,v226,v178\n"
MF"v[152:155],v[208:211],a[108:111],v[152:155]\n"
"v_med3_u32 v228,v227,v228,v179\n"
"v_med3_u32 v227,v226,v227,v179\n"
MF"v[132:135],v[212:215],a[96:99],v[132:135]\n"
SA"s61,s61,524288\n"
BL"v241,s[20:23],s48 offen offset:3072 lds\n"
MF"v[140:143],v[212:215],a[100:103],v[140:143]\n"
"v_max_u32 v226,v226,v179\n"
PK"v56,v56,v57\n"
MF"v[148:151],v[212:215],a[104:107],v[148:151]\n"
RD"v[204:207],v245 offset:18432\n"
PK"v57,v58,v59\n"
MF"v[156:159],v[212:215],a[108:111],v[156:159]\n"
WT"lgkmcnt(2)\n"
WT"vmcnt(13)\n"
"v_med3_u32 v228,v227,v228,v180\n"
MF"v[128:131],v[216:219],a[112:115],v[128:131]\n"
"v_med3_u32 v227,v226,v227,v180\n"
"v_max_u32 v226,v226,v180\n"
MF"v[136:139],v[216:219],a[116:119],v[136:139]\n"
RD"v[208:211],v244 offset:20480\n"
PK"v58,v60,v61\n"
MF"v[144:147],v[216:219],a[120:123],v[144:147]\n"
PK"v59,v62,v63\n"
"v_med3_u32 v228,v227,v228,v181\n"
MF"v[152:155],v[216:219],a[124:127],v[152:155]\n"
"v_med3_u32 v227,v226,v227,v181\n"
"v_max_u32 v226,v226,v181\n"
MF"v[132:135],v[220:223],a[112:115],v[132:135]\n"
"v_med3_u32 v228,v227,v228,v182\n"
"v_med3_u32 v227,v226,v227,v182\n"
MF"v[140:143],v[220:223],a[116:119],v[140:143]\n"
RD"v[212:215],v244 offset:22528\n"
"v_max_u32 v226,v226,v182\n"
MF"v[148:151],v[220:223],a[120:123],v[148:151]\n"
PK"v64,v64,v65\n"
PK"v65,v66,v67\n"
MF"v[156:159],v[220:223],a[124:127],v[156:159]\n"
"v_med3_u32 v228,v227,v228,v183\n"
PK"v66,v68,v69\n"
MF"v[128:131],v[192:195],a[128:131],v[128:131]\n"
PK"v67,v70,v71\n"
"v_med3_u32 v227,v226,v227,v183\n"
MF"v[136:139],v[192:195],a[132:135],v[136:139]\n"
RD"v[216:219],v245 offset:20480\n"
"v_max_u32 v226,v226,v183\n"
MF"v[144:147],v[192:195],a[136:139],v[144:147]\n"
SA"m0,s36,16384\n"
WT"vmcnt(12)\n"
"v_med3_u32 v228,v227,v228,v184\n"
MF"v[152:155],v[192:195],a[140:143],v[152:155]\n"
SA"s48,s60,s61\n"
"v_med3_u32 v227,v226,v227,v184\n"
"v_max_u32 v226,v226,v184\n"
MF"v[132:135],v[196:199],a[128:131],v[132:135]\n"
BL"v238,s[20:23],s48 offen offset:0 lds\n"
MF"v[140:143],v[196:199],a[132:135],v[140:143]\n"
RD"v[220:223],v245 offset:22528\n"
"v_med3_u32 v228,v227,v228,v185\n"
MF"v[148:151],v[196:199],a[136:139],v[148:151]\n"
PK"v72,v72,v73\n"
PK"v73,v74,v75\n"
MF"v[156:159],v[196:199],a[140:143],v[156:159]\n"
WT"lgkmcnt(2)\n"
"v_med3_u32 v227,v226,v227,v185\n"
"v_max_u32 v226,v226,v185\n"
MF"v[128:131],v[200:203],a[144:147],v[128:131]\n"
RD"v[192:195],v244 offset:40960\n"
PK"v74,v76,v77\n"
MF"v[136:139],v[200:203],a[148:151],v[136:139]\n"
PK"v75,v78,v79\n"
"v_med3_u32 v228,v227,v228,v186\n"
MF"v[144:147],v[200:203],a[152:155],v[144:147]\n"
"v_med3_u32 v227,v226,v227,v186\n"
"v_max_u32 v226,v226,v186\n"
MF"v[152:155],v[200:203],a[156:159],v[152:155]\n"
"v_med3_u32 v228,v227,v228,v187\n"
"v_med3_u32 v227,v226,v227,v187\n"
MF"v[132:135],v[204:207],a[144:147],v[132:135]\n"
RD"v[196:199],v244 offset:43008\n"
PK"v80,v80,v81\n"
MF"v[140:143],v[204:207],a[148:151],v[140:143]\n"
BL"v239,s[20:23],s48 offen offset:1024 lds\n"
MF"v[148:151],v[204:207],a[152:155],v[148:151]\n"
PK"v81,v82,v83\n"
"v_max_u32 v226,v226,v187\n"
MF"v[156:159],v[204:207],a[156:159],v[156:159]\n"
WT"vmcnt(13)\n"
"v_med3_u32 v228,v227,v228,v188\n"
PK"v82,v84,v85\n"
MF"v[128:131],v[208:211],a[160:163],v[128:131]\n"
RD"v[200:203],v245 offset:40960\n"
PK"v83,v86,v87\n"
MF"v[136:139],v[208:211],a[164:167],v[136:139]\n"
BS"v[16:19],v250,s[8:11],s49 offen offset:2048 sc1\n"
MF"v[144:147],v[208:211],a[168:171],v[144:147]\n"
"v_med3_u32 v227,v226,v227,v188\n"
"v_max_u32 v226,v226,v188\n"
MF"v[152:155],v[208:211],a[172:175],v[152:155]\n"
"v_med3_u32 v228,v227,v228,v189\n"
"v_med3_u32 v227,v226,v227,v189\n"
MF"v[132:135],v[212:215],a[160:163],v[132:135]\n"
RD"v[204:207],v245 offset:43008\n"
"v_max_u32 v226,v226,v189\n"
MF"v[140:143],v[212:215],a[164:167],v[140:143]\n"
BL"v240,s[20:23],s48 offen offset:2048 lds\n"
MF"v[148:151],v[212:215],a[168:171],v[148:151]\n"
PK"v88,v88,v89\n"
PK"v89,v90,v91\n"
MF"v[156:159],v[212:215],a[172:175],v[156:159]\n"
WT"lgkmcnt(1)\n"
RD"v[208:211],v244 offset:45056\n"
"v_med3_u32 v228,v227,v228,v190\n"
MF"v[128:131],v[216:219],a[176:179],v[128:131]\n"
"v_med3_u32 v227,v226,v227,v190\n"
PK"v90,v92,v93\n"
MF"v[136:139],v[216:219],a[180:183],v[136:139]\n"
PK"v91,v94,v95\n"
"v_max_u32 v226,v226,v190\n"
MF"v[144:147],v[216:219],a[184:187],v[144:147]\n"
"v_med3_u32 v228,v227,v228,v191\n"
"v_med3_u32 v227,v226,v227,v191\n"
MF"v[152:155],v[216:219],a[188:191],v[152:155]\n"
RD"v[212:215],v244 offset:47104\n"
"v_max_u32 v226,v226,v191\n"
MF"v[132:135],v[220:223],a[176:179],v[132:135]\n"
SA"s61,s61,524288\n"
BL"v241,s[20:23],s48 offen offset:3072 lds\n"
MF"v[140:143],v[220:223],a[180:183],v[140:143]\n"
"s_cmp_eq_u32 s61,5242880\n"
"v_mov_b32 v229,v226\n"
"s_cselect_b32 s61,0,s61\n"
MF"v[148:151],v[220:223],a[184:187],v[148:151]\n"
"v_max_u32_dpp v229,v229,v229 row_shr:1 row_mask:0xf bank_mask:0xf\n"
MF"v[156:159],v[220:223],a[188:191],v[156:159]\n"
RD"v[216:219],v245 offset:45056\n"
"v_max_u32_dpp v229,v229,v229 row_shr:2 row_mask:0xf bank_mask:0xf\n"
MF"v[128:131],v[192:195],a[192:195],v[128:131]\n"
MF"v[136:139],v[192:195],a[196:199],v[136:139]\n"
"v_max_u32_dpp v229,v229,v229 row_shr:4 row_mask:0xf bank_mask:0xf\n"
MF"v[144:147],v[192:195],a[200:203],v[144:147]\n"
MF"v[152:155],v[192:195],a[204:207],v[152:155]\n"
RD"v[220:223],v245 offset:47104\n"
"v_max_u32_dpp v229,v229,v229 row_shr:8 row_mask:0xf bank_mask:0xf\n"
MF"v[132:135],v[196:199],a[192:195],v[132:135]\n"
MF"v[140:143],v[196:199],a[196:199],v[140:143]\n"
"v_max_u32_dpp v229,v229,v229 row_bcast:15 row_mask:0xa bank_mask:0xf\n"
MF"v[148:151],v[196:199],a[200:203],v[148:151]\n"
MF"v[156:159],v[196:199],a[204:207],v[156:159]\n"
"v_max_u32_dpp v229,v229,v229 row_bcast:31 row_mask:0xc bank_mask:0xf\n"
MF"v[128:131],v[200:203],a[208:211],v[128:131]\n"
"v_readlane_b32 s62,v229,63\n"
MF"v[136:139],v[200:203],a[212:215],v[136:139]\n"
MF"v[144:147],v[200:203],a[216:219],v[144:147]\n"
"v_cmp_eq_u32 vcc,s62,v226\n"
"v_writelane_b32 v225,s62,0\n"
MF"v[152:155],v[200:203],a[220:223],v[152:155]\n"
WT"lgkmcnt(0)\n"
"v_cndmask_b32 v226,v226,v227,vcc\n"
"v_cndmask_b32_e64 v227,v227,0,vcc\n"
MF"v[132:135],v[204:207],a[208:211],v[132:135]\n"
"v_mov_b32 v229,v226\n"
MF"v[140:143],v[204:207],a[212:215],v[140:143]\n"
"s_barrier\n"
MF"v[148:151],v[204:207],a[216:219],v[148:151]\n"
SA"m0,s37,16384\n"
RD"v[192:195],v242 offset:0\n"
"v_max_u32_dpp v229,v229,v229 row_shr:1 row_mask:0xf bank_mask:0xf\n"
MF"v[156:159],v[204:207],a[220:223],v[156:159]\n"
SA"s48,s39,98304\n"
BS"v[24:27],v250,s[8:11],s49 offen offset:3072 sc1\n"
MF"v[128:131],v[208:211],a[224:227],v[128:131]\n"
BL"v234,s[12:15],s48 offen offset:0 lds\n"
MF"v[136:139],v[208:211],a[228:231],v[136:139]\n"
"v_max_u32_dpp v229,v229,v229 row_shr:2 row_mask:0xf bank_mask:0xf\n"
MF"v[144:147],v[208:211],a[232:235],v[144:147]\n"
RD"v[196:199],v242 offset:2048\n"
"v_max_u32_dpp v229,v229,v229 row_shr:4 row_mask:0xf bank_mask:0xf\n"
MF"v[152:155],v[208:211],a[236:239],v[152:155]\n"
MF"v[132:135],v[212:215],a[224:227],v[132:135]\n"
"v_max_u32_dpp v229,v229,v229 row_shr:8 row_mask:0xf bank_mask:0xf\n"
MF"v[140:143],v[212:215],a[228:231],v[140:143]\n"
MF"v[148:151],v[212:215],a[232:235],v[148:151]\n"
RD"v[200:203],v243 offset:0\n"
"v_max_u32_dpp v229,v229,v229 row_bcast:15 row_mask:0xa bank_mask:0xf\n"
MF"v[156:159],v[212:215],a[236:239],v[156:159]\n"
BL"v235,s[12:15],s48 offen offset:1024 lds\n"
MF"v[128:131],v[216:219],a[240:243],v[128:131]\n"
"v_max_u32_dpp v229,v229,v229 row_bcast:31 row_mask:0xc bank_mask:0xf\n"
MF"v[136:139],v[216:219],a[244:247],v[136:139]\n"
"v_readlane_b32 s62,v229,63\n"
MF"v[144:147],v[216:219],a[248:251],v[144:147]\n"
RD"v[204:207],v243 offset:2048\n"
"v_cmp_eq_u32 vcc,s62,v226\n"
MF"v[152:155],v[216:219],a[252:255],v[152:155]\n"
"v_writelane_b32 v225,s62,1\n"
"v_cndmask_b32 v226,v226,v227,vcc\n"
MF"v[132:135],v[220:223],a[240:243],v[132:135]\n"
BL"v236,s[12:15],s48 offen offset:2048 lds\n"
MF"v[140:143],v[220:223],a[244:247],v[140:143]\n"
RD"v[208:211],v242 offset:4096\n"
"v_cndmask_b32_e64 v227,v227,0,vcc\n"
MF"v[148:151],v[220:223],a[248:251],v[148:151]\n"
"v_mov_b32 v229,v226\n"
MF"v[156:159],v[220:223],a[252:255],v[156:159]\n"
WT"lgkmcnt(2)\n"
"v_max_u32_dpp v229,v229,v229 row_shr:1 row_mask:0xf bank_mask:0xf\n"
MF"v[36:39],v[192:195],a[0:3],0\n"
MF"v[44:47],v[192:195],a[4:7],0\n"
RD"v[212:215],v242 offset:6144\n"
"v_max_u32_dpp v229,v229,v229 row_shr:2 row_mask:0xf bank_mask:0xf\n"
MF"v[52:55],v[192:195],a[8:11],0\n"
BL"v237,s[12:15],s48 offen offset:3072 lds\n"
MF"v[60:63],v[192:195],a[12:15],0\n"
"v_max_u32_dpp v229,v229,v229 row_shr:4 row_mask:0xf bank_mask:0xf\n"
PK"v4,v128,v129\n"
MF"v[68:71],v[196:199],a[0:3],0\n"
"v_max_u32_dpp v229,v229,v229 row_shr:8 row_mask:0xf bank_mask:0xf\n"
PK"v5,v130,v131\n"
MF"v[76:79],v[196:199],a[4:7],0\n"
RD"v[216:219],v243 offset:4096\n"
"v_max_u32_dpp v229,v229,v229 row_bcast:15 row_mask:0xa bank_mask:0xf\n"
MF"v[84:87],v[196:199],a[8:11],0\n"
SA"m0,s37,20480\n"
"v_max_u32_dpp v229,v229,v229 row_bcast:31 row_mask:0xc bank_mask:0xf\n"
PK"v6,v132,v133\n"
MF"v[92:95],v[196:199],a[12:15],0\n"
SA"s48,s98,98304\n"
"v_readlane_b32 s62,v229,63\n"
PK"v7,v134,v135\n"
MF"v[36:39],v[200:203],a[16:19],v[36:39]\n"
BL"v234,s[12:15],s48 offen offset:0 lds\n"
MF"v[44:47],v[200:203],a[20:23],v[44:47]\n"
RD"v[220:223],v243 offset:6144\n"
"v_cmp_eq_u32 vcc,s62,v226\n"
MF"v[52:55],v[200:203],a[24:27],v[52:55]\n"
"v_writelane_b32 v225,s62,2\n"
"v_cndmask_b32 v226,v226,v227,vcc\n"
MF"v[60:63],v[200:203],a[28:31],v[60:63]\n"
WT"lgkmcnt(2)\n"
"v_cndmask_b32_e64 v227,v227,0,vcc\n"
"v_mov_b32 v229,v226\n"
MF"v[68:71],v[204:207],a[16:19],v[68:71]\n"
"s_mov_b32 s49,8192\n"
RD"v[192:195],v242 offset:24576\n"
"v_max_u32_dpp v229,v229,v229 row_shr:1 row_mask:0xf bank_mask:0xf\n"
MF"v[76:79],v[204:207],a[20:23],v[76:79]\n"
PK"v12,v136,v137\n"
"v_max_u32_dpp v229,v229,v229 row_shr:2 row_mask:0xf bank_mask:0xf\n"
MF"v[84:87],v[204:207],a[24:27],v[84:87]\n"
BS"v[32:35],v250,s[8:11],s49 offen offset:0 sc1\n"
MF"v[92:95],v[204:207],a[28:31],v[92:95]\n"
BL"v235,s[12:15],s48 offen offset:1024 lds\n"
MF"v[36:39],v[208:211],a[32:35],v[36:39]\n"
RD"v[196:199],v242 offset:26624\n"
"v_max_u32_dpp v229,v229,v229 row_shr:4 row_mask:0xf bank_mask:0xf\n"
MF"v[44:47],v[208:211],a[36:39],v[44:47]\n"
PK"v13,v138,v139\n"
"v_max_u32_dpp v229,v229,v229 row_shr:8 row_mask:0xf bank_mask:0xf\n"
MF"v[52:55],v[208:211],a[40:43],v[52:55]\n"
PK"v14,v140,v141\n"
"v_max_u32_dpp v229,v229,v229 row_bcast:15 row_mask:0xa bank_mask:0xf\n"
MF"v[60:63],v[208:211],a[44:47],v[60:63]\n"
PK"v15,v142,v143\n"
"v_max_u32_dpp v229,v229,v229 row_bcast:31 row_mask:0xc bank_mask:0xf\n"
MF"v[68:71],v[212:215],a[32:35],v[68:71]\n"
RD"v[200:203],v243 offset:24576\n"
"v_readlane_b32 s62,v229,63\n"
MF"v[76:79],v[212:215],a[36:39],v[76:79]\n"
BL"v236,s[12:15],s48 offen offset:2048 lds\n"
MF"v[84:87],v[212:215],a[40:43],v[84:87]\n"
"v_cmp_eq_u32 vcc,s62,v226\n"
"v_writelane_b32 v225,s62,3\n"
MF"v[92:95],v[212:215],a[44:47],v[92:95]\n"
WT"lgkmcnt(1)\n"
"v_cndmask_b32 v226,v226,v227,vcc\n"
"v_cndmask_b32_e64 v227,v227,0,vcc\n"
MF"v[36:39],v[216:219],a[48:51],v[36:39]\n"
RD"v[204:207],v243 offset:26624\n"
"v_mov_b32 v229,v226\n"
MF"v[44:47],v[216:219],a[52:55],v[44:47]\n"
PK"v20,v144,v145\n"
"v_max_u32_dpp v229,v229,v229 row_shr:1 row_mask:0xf bank_mask:0xf\n"
MF"v[52:55],v[216:219],a[56:59],v[52:55]\n"
BL"v237,s[12:15],s48 offen offset:3072 lds\n"
MF"v[60:63],v[216:219],a[60:63],v[60:63]\n"
RD"v[208:211],v242 offset:28672\n"
"v_max_u32_dpp v229,v229,v229 row_shr:2 row_mask:0xf bank_mask:0xf\n"
MF"v[68:71],v[220:223],a[48:51],v[68:71]\n"
PK"v21,v146,v147\n"
"v_max_u32_dpp v229,v229,v229 row_shr:4 row_mask:0xf bank_mask:0xf\n"
MF"v[76:79],v[220:223],a[52:55],v[76:79]\n"
PK"v22,v148,v149\n"
"v_max_u32_dpp v229,v229,v229 row_shr:8 row_mask:0xf bank_mask:0xf\n"
MF"v[84:87],v[220:223],a[56:59],v[84:87]\n"
PK"v23,v150,v151\n"
"v_max_u32_dpp v229,v229,v229 row_bcast:15 row_mask:0xa bank_mask:0xf\n"
MF"v[92:95],v[220:223],a[60:63],v[92:95]\n"
RD"v[212:215],v242 offset:30720\n"
"v_max_u32_dpp v229,v229,v229 row_bcast:31 row_mask:0xc bank_mask:0xf\n"
MF"v[36:39],v[192:195],a[64:67],v[36:39]\n"
"v_readlane_b32 s62,v229,63\n"
PK"v28,v152,v153\n"
MF"v[44:47],v[192:195],a[68:71],v[44:47]\n"
"v_cmp_eq_u32 vcc,s62,v226\n"
"v_writelane_b32 v225,s62,4\n"
MF"v[52:55],v[192:195],a[72:75],v[52:55]\n"
"v_cndmask_b32 v226,v226,v227,vcc\n"
"v_cndmask_b32_e64 v227,v227,0,vcc\n"
MF"v[60:63],v[192:195],a[76:79],v[60:63]\n"
RD"v[216:219],v243 offset:28672\n"
"v_mov_b32 v229,v226\n"
MF"v[68:71],v[196:199],a[64:67],v[68:71]\n"
PK"v29,v154,v155\n"
"v_max_u32_dpp v229,v229,v229 row_shr:1 row_mask:0xf bank_mask:0xf\n"
MF"v[76:79],v[196:199],a[68:71],v[76:79]\n"
PK"v30,v156,v157\n"
"v_max_u32_dpp v229,v229,v229 row_shr:2 row_mask:0xf bank_mask:0xf\n"
MF"v[84:87],v[196:199],a[72:75],v[84:87]\n"
PK"v31,v158,v159\n"
"v_max_u32_dpp v229,v229,v229 row_shr:4 row_mask:0xf bank_mask:0xf\n"
MF"v[92:95],v[196:199],a[76:79],v[92:95]\n"
WT"lgkmcnt(1)\n"
RD"v[220:223],v243 offset:30720\n"
"v_max_u32_dpp v229,v229,v229 row_shr:8 row_mask:0xf bank_mask:0xf\n"
MF"v[36:39],v[200:203],a[80:83],v[36:39]\n"
MF"v[44:47],v[200:203],a[84:87],v[44:47]\n"
"v_max_u32_dpp v229,v229,v229 row_bcast:15 row_mask:0xa bank_mask:0xf\n"
MF"v[52:55],v[200:203],a[88:91],v[52:55]\n"
RD"v[192:195],v244 offset:0\n"
"v_max_u32_dpp v229,v229,v229 row_bcast:31 row_mask:0xc bank_mask:0xf\n"
MF"v[60:63],v[200:203],a[92:95],v[60:63]\n"
"v_readlane_b32 s62,v229,63\n"
MF"v[68:71],v[204:207],a[80:83],v[68:71]\n"
MF"v[76:79],v[204:207],a[84:87],v[76:79]\n"
BS"v[40:43],v250,s[8:11],s49 offen offset:1024 sc1\n"
MF"v[84:87],v[204:207],a[88:91],v[84:87]\n"
RD"v[196:199],v244 offset:2048\n"
"v_cmp_eq_u32 vcc,s62,v226\n"
MF"v[92:95],v[204:207],a[92:95],v[92:95]\n"
"v_writelane_b32 v225,s62,5\n"
"v_cndmask_b32 v226,v226,v227,vcc\n"
MF"v[36:39],v[208:211],a[96:99],v[36:39]\n"
"v_cndmask_b32_e64 v227,v227,0,vcc\n"
"v_mov_b32 v229,v226\n"
MF"v[44:47],v[208:211],a[100:103],v[44:47]\n"
MF"v[52:55],v[208:211],a[104:107],v[52:55]\n"
RD"v[200:203],v245 offset:0\n"
"v_max_u32_dpp v229,v229,v229 row_shr:1 row_mask:0xf bank_mask:0xf\n"
MF"v[60:63],v[208:211],a[108:111],v[60:63]\n"
MF"v[68:71],v[212:215],a[96:99],v[68:71]\n"
"v_max_u32_dpp v229,v229,v229 row_shr:2 row_mask:0xf bank_mask:0xf\n"
MF"v[76:79],v[212:215],a[100:103],v[76:79]\n"
MF"v[84:87],v[212:215],a[104:107],v[84:87]\n"
RD"v[204:207],v245 offset:2048\n"
"v_max_u32_dpp v229,v229,v229 row_shr:4 row_mask:0xf bank_mask:0xf\n"
MF"v[92:95],v[212:215],a[108:111],v[92:95]\n"
WT"lgkmcnt(2)\n"
"v_max_u32_dpp v229,v229,v229 row_shr:8 row_mask:0xf bank_mask:0xf\n"
MF"v[36:39],v[216:219],a[112:115],v[36:39]\n"
MF"v[44:47],v[216:219],a[116:119],v[44:47]\n"
RD"v[208:211],v244 offset:4096\n"
"v_max_u32_dpp v229,v229,v229 row_bcast:15 row_mask:0xa bank_mask:0xf\n"
MF"v[52:55],v[216:219],a[120:123],v[52:55]\n"
SA"m0,s36,49152\n"
"v_max_u32_dpp v229,v229,v229 row_bcast:31 row_mask:0xc bank_mask:0xf\n"
SA"s48,s60,s61\n"
MF"v[60:63],v[216:219],a[124:127],v[60:63]\n"
"v_readlane_b32 s62,v229,63\n"
MF"v[68:71],v[220:223],a[112:115],v[68:71]\n"
BL"v238,s[20:23],s48 offen offset:0 lds\n"
MF"v[76:79],v[220:223],a[116:119],v[76:79]\n"
RD"v[212:215],v244 offset:6144\n"
"v_cmp_eq_u32 vcc,s62,v226\n"
MF"v[84:87],v[220:223],a[120:123],v[84:87]\n"
"v_writelane_b32 v225,s62,6\n"
"v_cndmask_b32 v226,v226,v227,vcc\n"
MF"v[92:95],v[220:223],a[124:127],v[92:95]\n"
"v_cndmask_b32_e64 v227,v227,0,vcc\n"
"v_mov_b32 v229,v226\n"
MF"v[36:39],v[192:195],a[128:131],v[36:39]\n"
PK"v96,v96,v97\n"
"v_max_u32_dpp v229,v229,v229 row_shr:1 row_mask:0xf bank_mask:0xf\n"
MF"v[44:47],v[192:195],a[132:135],v[44:47]\n"
RD"v[216:219],v245 offset:4096\n"
"v_max_u32_dpp v229,v229,v229 row_shr:2 row_mask:0xf bank_mask:0xf\n"
MF"v[52:55],v[192:195],a[136:139],v[52:55]\n"
PK"v97,v98,v99\n"
"v_max_u32_dpp v229,v229,v229 row_shr:4 row_mask:0xf bank_mask:0xf\n"
MF"v[60:63],v[192:195],a[140:143],v[60:63]\n"
PK"v98,v100,v101\n"
"v_max_u32_dpp v229,v229,v229 row_shr:8 row_mask:0xf bank_mask:0xf\n"
MF"v[68:71],v[196:199],a[128:131],v[68:71]\n"
BL"v239,s[20:23],s48 offen offset:1024 lds\n"
MF"v[76:79],v[196:199],a[132:135],v[76:79]\n"
RD"v[220:223],v245 offset:6144\n"
"v_max_u32_dpp v229,v229,v229 row_bcast:15 row_mask:0xa bank_mask:0xf\n"
MF"v[84:87],v[196:199],a[136:139],v[84:87]\n"
PK"v99,v102,v103\n"
"v_max_u32_dpp v229,v229,v229 row_bcast:31 row_mask:0xc bank_mask:0xf\n"
MF"v[92:95],v[196:199],a[140:143],v[92:95]\n"
WT"lgkmcnt(2)\n"
"v_readlane_b32 s62,v229,63\n"
"v_mov_b32 v229,v228\n"
MF"v[36:39],v[200:203],a[144:147],v[36:39]\n"
RD"v[192:195],v244 offset:24576\n"
"v_cmp_eq_u32 vcc,s62,v226\n"
MF"v[44:47],v[200:203],a[148:151],v[44:47]\n"
"v_writelane_b32 v225,s62,7\n"
"v_cndmask_b32 v226,v226,v227,vcc\n"
MF"v[52:55],v[200:203],a[152:155],v[52:55]\n"
"v_cndmask_b32_e64 v227,v227,0,vcc\n"
"v_max_u32_dpp v229,v229,v229 row_shr:1 row_mask:0xf bank_mask:0xf\n"
MF"v[60:63],v[200:203],a[156:159],v[60:63]\n"
PK"v104,v104,v105\n"
"v_max_u32_dpp v229,v229,v229 row_shr:2 row_mask:0xf bank_mask:0xf\n"
MF"v[68:71],v[204:207],a[144:147],v[68:71]\n"
RD"v[196:199],v244 offset:26624\n"
"v_max_u32_dpp v229,v229,v229 row_shr:4 row_mask:0xf bank_mask:0xf\n"
MF"v[76:79],v[204:207],a[148:151],v[76:79]\n"
BS"v[48:51],v250,s[8:11],s49 offen offset:2048 sc1\n"
MF"v[84:87],v[204:207],a[152:155],v[84:87]\n"
BL"v240,s[20:23],s48 offen offset:2048 lds\n"
MF"v[92:95],v[204:207],a[156:159],v[92:95]\n"
"v_max_u32_dpp v229,v229,v229 row_shr:8 row_mask:0xf bank_mask:0xf\n"
PK"v105,v106,v107\n"
MF"v[36:39],v[208:211],a[160:163],v[36:39]\n"
RD"v[200:203],v245 offset:24576\n"
"v_max_u32_dpp v229,v229,v229 row_bcast:15 row_mask:0xa bank_mask:0xf\n"
MF"v[44:47],v[208:211],a[164:167],v[44:47]\n"
PK"v106,v108,v109\n"
"v_max_u32_dpp v229,v229,v229 row_bcast:31 row_mask:0xc bank_mask:0xf\n"
MF"v[52:55],v[208:211],a[168:171],v[52:55]\n"
"v_readlane_b32 s63,v229,63\n"
PK"v107,v110,v111\n"
MF"v[60:63],v[208:211],a[172:175],v[60:63]\n"
"s_cmp_gt_u32 s63,s62\n"
"s_cbranch_scc1 L1007_%=\n"
"L1008_%=:\n"
MF"v[68:71],v[212:215],a[160:163],v[68:71]\n"
SA"s61,s61,524288\n"
RD"v[204:207],v245 offset:26624\n"
"v_lshrrev_b32 v229,2,v224\n"
MF"v[76:79],v[212:215],a[164:167],v[76:79]\n"
"ds_write_b32 v229,v225 offset:40960\n"
MF"v[84:87],v[212:215],a[168:171],v[84:87]\n"
BL"v241,s[20:23],s48 offen offset:3072 lds\n"
MF"v[92:95],v[212:215],a[172:175],v[92:95]\n"
WT"lgkmcnt(2)\n"
RD"v[208:211],v244 offset:28672\n"
PK"v112,v112,v113\n"
MF"v[36:39],v[216:219],a[176:179],v[36:39]\n"
PK"v113,v114,v115\n"
PK"v114,v116,v117\n"
MF"v[44:47],v[216:219],a[180:183],v[44:47]\n"
PK"v115,v118,v119\n"
PK"v120,v120,v121\n"
MF"v[52:55],v[216:219],a[184:187],v[52:55]\n"
PK"v121,v122,v123\n"
PK"v122,v124,v125\n"
MF"v[60:63],v[216:219],a[188:191],v[60:63]\n"
RD"v[212:215],v244 offset:30720\n"
PK"v123,v126,v127\n"
MF"v[68:71],v[220:223],a[176:179],v[68:71]\n"
MF"v[76:79],v[220:223],a[180:183],v[76:79]\n"
MF"v[84:87],v[220:223],a[184:187],v[84:87]\n"
MF"v[92:95],v[220:223],a[188:191],v[92:95]\n"
RD"v[216:219],v245 offset:28672\n"
MF"v[36:39],v[192:195],a[192:195],v[36:39]\n"
MF"v[44:47],v[192:195],a[196:199],v[44:47]\n"
MF"v[52:55],v[192:195],a[200:203],v[52:55]\n"
MF"v[60:63],v[192:195],a[204:207],v[60:63]\n"
RD"v[220:223],v245 offset:30720\n"
MF"v[68:71],v[196:199],a[192:195],v[68:71]\n"
MF"v[76:79],v[196:199],a[196:199],v[76:79]\n"
MF"v[84:87],v[196:199],a[200:203],v[84:87]\n"
MF"v[92:95],v[196:199],a[204:207],v[92:95]\n"
MF"v[36:39],v[200:203],a[208:211],v[36:39]\n"
MF"v[44:47],v[200:203],a[212:215],v[44:47]\n"
MF"v[52:55],v[200:203],a[216:219],v[52:55]\n"
MF"v[60:63],v[200:203],a[220:223],v[60:63]\n"
WT"lgkmcnt(0)\n"
BS"v[56:59],v250,s[8:11],s49 offen offset:3072 sc1\n"
MF"v[68:71],v[204:207],a[208:211],v[68:71]\n"
"v_lshrrev_b32 v251,2,v224\n"
"v_and_b32 v251,28,v251\n"
MF"v[76:79],v[204:207],a[212:215],v[76:79]\n"
WT"vmcnt(29)\n"
"s_barrier\n"
MF"v[84:87],v[204:207],a[216:219],v[84:87]\n"
RD"v[192:195],v242 offset:8192\n"
MF"v[92:95],v[204:207],a[220:223],v[92:95]\n"
"ds_read_b32 v234,v251 offset:40960\n"
MF"v[36:39],v[208:211],a[224:227],v[36:39]\n"
MF"v[44:47],v[208:211],a[228:231],v[44:47]\n"
MF"v[52:55],v[208:211],a[232:235],v[52:55]\n"
RD"v[196:199],v242 offset:10240\n"
MF"v[60:63],v[208:211],a[236:239],v[60:63]\n"
MF"v[68:71],v[212:215],a[224:227],v[68:71]\n"
MF"v[76:79],v[212:215],a[228:231],v[76:79]\n"
MF"v[84:87],v[212:215],a[232:235],v[84:87]\n"
RD"v[200:203],v243 offset:8192\n"
MF"v[92:95],v[212:215],a[236:239],v[92:95]\n"
WT"lgkmcnt(2)\n"
"v_not_b32 v251,v234\n"
"v_bfe_u32 v252,v251,4,7\n"
MF"v[36:39],v[216:219],a[240:243],v[36:39]\n"
"v_mul_u32_u24 v252,20480,v252\n"
MF"v[44:47],v[216:219],a[244:247],v[44:47]\n"
"v_and_b32 v251,15,v251\n"
MF"v[52:55],v[216:219],a[248:251],v[52:55]\n"
RD"v[204:207],v243 offset:10240\n"
"v_lshl_add_u32 v251,v251,4,v252\n"
MF"v[60:63],v[216:219],a[252:255],v[60:63]\n"
"v_and_b32 v252,0x380,v224\n"
MF"v[68:71],v[220:223],a[240:243],v[68:71]\n"
"v_lshl_add_u32 v251,v252,1,v251\n"
MF"v[76:79],v[220:223],a[244:247],v[76:79]\n"
RD"v[208:211],v242 offset:12288\n"
"v_add_u32 v252,v252,v224\n"
MF"v[84:87],v[220:223],a[248:251],v[84:87]\n"
"s_lshl_b32 s92,s34,2\n"
MF"v[92:95],v[220:223],a[252:255],v[92:95]\n"
SA"s92,s92,s30\n"
MF"v[128:131],v[192:195],a[0:3],0\n"
SA"m0,s38,65536\n"
SA"s48,s60,s61\n"
"s_lshl_b32 s93,s92,11\n"
MF"v[136:139],v[192:195],a[4:7],0\n"
"s_cmp_lt_u32 s92,10\n"
RD"v[212:215],v242 offset:14336\n"
"s_cselect_b32 s93,s93,2130706432\n"
MF"v[144:147],v[192:195],a[8:11],0\n"
SA"s92,s93,65536\n"
BL"v238,s[20:23],s48 offen offset:0 lds\n"
MF"v[152:155],v[192:195],a[12:15],0\n"
WT"lgkmcnt(2)\n"
BL"v[100:103],v251,s[8:11],s92 offen sc1\n"
MF"v[132:135],v[196:199],a[0:3],0\n"
"s_mul_i32 s92,s35,20480\n"
PK"v36,v36,v37\n"
PK"v37,v38,v39\n"
MF"v[140:143],v[196:199],a[4:7],0\n"
SA"s92,s92,s93\n"
RD"v[216:219],v243 offset:12288\n"
PK"v38,v68,v69\n"
MF"v[148:151],v[196:199],a[8:11],0\n"
SA"s92,s92,2770944\n"
PK"v39,v70,v71\n"
MF"v[156:159],v[196:199],a[12:15],0\n"
MF"v[128:131],v[200:203],a[16:19],v[128:131]\n"
"s_mov_b32 s49,16384\n"
PK"v44,v44,v45\n"
PK"v45,v46,v47\n"
MF"v[136:139],v[200:203],a[20:23],v[136:139]\n"
RD"v[220:223],v243 offset:14336\n"
PK"v46,v76,v77\n"
MF"v[144:147],v[200:203],a[24:27],v[144:147]\n"
BS"v[64:67],v250,s[8:11],s49 offen offset:0 sc1\n"
MF"v[152:155],v[200:203],a[28:31],v[152:155]\n"
BL"v239,s[20:23],s48 offen offset:1024 lds\n"
MF"v[132:135],v[204:207],a[16:19],v[132:135]\n"
RD"v[192:195],v242 offset:32768\n"
PK"v47,v78,v79\n"
MF"v[140:143],v[204:207],a[20:23],v[140:143]\n"
PK"v52,v52,v53\n"
PK"v53,v54,v55\n"
MF"v[148:151],v[204:207],a[24:27],v[148:151]\n"
PK"v54,v84,v85\n"
PK"v55,v86,v87\n"
MF"v[156:159],v[204:207],a[28:31],v[156:159]\n"
WT"lgkmcnt(2)\n"
MF"v[128:131],v[208:211],a[32:35],v[128:131]\n"
RD"v[196:199],v242 offset:34816\n"
MF"v[136:139],v[208:211],a[36:39],v[136:139]\n"
MF"v[144:147],v[208:211],a[40:43],v[144:147]\n"
BL"v240,s[20:23],s48 offen offset:2048 lds\n"
MF"v[152:155],v[208:211],a[44:47],v[152:155]\n"
PK"v60,v60,v61\n"
PK"v61,v62,v63\n"
MF"v[132:135],v[212:215],a[32:35],v[132:135]\n"
RD"v[200:203],v243 offset:32768\n"
PK"v62,v92,v93\n"
MF"v[140:143],v[212:215],a[36:39],v[140:143]\n"
PK"v63,v94,v95\n"
MF"v[148:151],v[212:215],a[40:43],v[148:151]\n"
MF"v[156:159],v[212:215],a[44:47],v[156:159]\n"
MF"v[128:131],v[216:219],a[48:51],v[128:131]\n"
RD"v[204:207],v243 offset:34816\n"
MF"v[136:139],v[216:219],a[52:55],v[136:139]\n"
MF"v[144:147],v[216:219],a[56:59],v[144:147]\n"
SA"s61,s61,524288\n"
BL"v241,s[20:23],s48 offen offset:3072 lds\n"
MF"v[152:155],v[216:219],a[60:63],v[152:155]\n"
WT"lgkmcnt(1)\n"
RD"v[208:211],v242 offset:36864\n"
"s_cmp_eq_u32 s61,5242880\n"
MF"v[132:135],v[220:223],a[48:51],v[132:135]\n"
"s_cselect_b32 s61,0,s61\n"
MF"v[140:143],v[220:223],a[52:55],v[140:143]\n"
MF"v[148:151],v[220:223],a[56:59],v[148:151]\n"
MF"v[156:159],v[220:223],a[60:63],v[156:159]\n"
RD"v[212:215],v242 offset:38912\n"
MF"v[128:131],v[192:195],a[64:67],v[128:131]\n"
MF"v[136:139],v[192:195],a[68:71],v[136:139]\n"
MF"v[144:147],v[192:195],a[72:75],v[144:147]\n"
MF"v[152:155],v[192:195],a[76:79],v[152:155]\n"
RD"v[216:219],v243 offset:36864\n"
MF"v[132:135],v[196:199],a[64:67],v[132:135]\n"
MF"v[140:143],v[196:199],a[68:71],v[140:143]\n"
MF"v[148:151],v[196:199],a[72:75],v[148:151]\n"
MF"v[156:159],v[196:199],a[76:79],v[156:159]\n"
RD"v[220:223],v243 offset:38912\n"
MF"v[128:131],v[200:203],a[80:83],v[128:131]\n"
SA"m0,s38,114688\n"
SA"s48,s60,s61\n"
MF"v[136:139],v[200:203],a[84:87],v[136:139]\n"
BS"v[72:75],v250,s[8:11],s49 offen offset:1024 sc1\n"
MF"v[144:147],v[200:203],a[88:91],v[144:147]\n"
RD"v[192:195],v244 offset:8192\n"
MF"v[152:155],v[200:203],a[92:95],v[152:155]\n"
WT"lgkmcnt(2)\n"
BL"v238,s[20:23],s48 offen offset:0 lds\n"
MF"v[132:135],v[204:207],a[80:83],v[132:135]\n"
MF"v[140:143],v[204:207],a[84:87],v[140:143]\n"
MF"v[148:151],v[204:207],a[88:91],v[148:151]\n"
RD"v[196:199],v244 offset:10240\n"
MF"v[156:159],v[204:207],a[92:95],v[156:159]\n"
MF"v[128:131],v[208:211],a[96:99],v[128:131]\n"
MF"v[136:139],v[208:211],a[100:103],v[136:139]\n"
MF"v[144:147],v[208:211],a[104:107],v[144:147]\n"
RD"v[200:203],v245 offset:8192\n"
MF"v[152:155],v[208:211],a[108:111],v[152:155]\n"
BL"v239,s[20:23],s48 offen offset:1024 lds\n"
MF"v[132:135],v[212:215],a[96:99],v[132:135]\n"
MF"v[140:143],v[212:215],a[100:103],v[140:143]\n"
MF"v[148:151],v[212:215],a[104:107],v[148:151]\n"
RD"v[204:207],v245 offset:10240\n"
MF"v[156:159],v[212:215],a[108:111],v[156:159]\n"
MF"v[128:131],v[216:219],a[112:115],v[128:131]\n"
MF"v[136:139],v[216:219],a[116:119],v[136:139]\n"
RD"v[208:211],v244 offset:12288\n"
MF"v[144:147],v[216:219],a[120:123],v[144:147]\n"
BL"v240,s[20:23],s48 offen offset:2048 lds\n"
MF"v[152:155],v[216:219],a[124:127],v[152:155]\n"
WT"lgkmcnt(2)\n"
MF"v[132:135],v[220:223],a[112:115],v[132:135]\n"
MF"v[140:143],v[220:223],a[116:119],v[140:143]\n"
RD"v[212:215],v244 offset:14336\n"
MF"v[148:151],v[220:223],a[120:123],v[148:151]\n"
MF"v[156:159],v[220:223],a[124:127],v[156:159]\n"
MF"v[128:131],v[192:195],a[128:131],v[128:131]\n"
MF"v[136:139],v[192:195],a[132:135],v[136:139]\n"
RD"v[216:219],v245 offset:12288\n"
MF"v[144:147],v[192:195],a[136:139],v[144:147]\n"
SA"s61,s61,524288\n"
BL"v241,s[20:23],s48 offen offset:3072 lds\n"
MF"v[152:155],v[192:195],a[140:143],v[152:155]\n"
MF"v[132:135],v[196:199],a[128:131],v[132:135]\n"
MF"v[140:143],v[196:199],a[132:135],v[140:143]\n"
RD"v[220:223],v245 offset:14336\n"
MF"v[148:151],v[196:199],a[136:139],v[148:151]\n"
MF"v[156:159],v[196:199],a[140:143],v[156:159]\n"
MF"v[128:131],v[200:203],a[144:147],v[128:131]\n"
RD"v[192:195],v244 offset:32768\n"
MF"v[136:139],v[200:203],a[148:151],v[136:139]\n"
BS"v[80:83],v250,s[8:11],s49 offen offset:2048 sc1\n"
MF"v[144:147],v[200:203],a[152:155],v[144:147]\n"
MF"v[152:155],v[200:203],a[156:159],v[152:155]\n"
WT"lgkmcnt(2)\n"
MF"v[132:135],v[204:207],a[144:147],v[132:135]\n"
RD"v[196:199],v244 offset:34816\n"
MF"v[140:143],v[204:207],a[148:151],v[140:143]\n"
MF"v[148:151],v[204:207],a[152:155],v[148:151]\n"
MF"v[156:159],v[204:207],a[156:159],v[156:159]\n"
MF"v[128:131],v[208:211],a[160:163],v[128:131]\n"
RD"v[200:203],v245 offset:32768\n"
MF"v[136:139],v[208:211],a[164:167],v[136:139]\n"
MF"v[144:147],v[208:211],a[168:171],v[144:147]\n"
MF"v[152:155],v[208:211],a[172:175],v[152:155]\n"
MF"v[132:135],v[212:215],a[160:163],v[132:135]\n"
RD"v[204:207],v245 offset:34816\n"
MF"v[140:143],v[212:215],a[164:167],v[140:143]\n"
MF"v[148:151],v[212:215],a[168:171],v[148:151]\n"
MF"v[156:159],v[212:215],a[172:175],v[156:159]\n"
RD"v[208:211],v244 offset:36864\n"
MF"v[128:131],v[216:219],a[176:179],v[128:131]\n"
MF"v[136:139],v[216:219],a[180:183],v[136:139]\n"
MF"v[144:147],v[216:219],a[184:187],v[144:147]\n"
MF"v[152:155],v[216:219],a[188:191],v[152:155]\n"
WT"lgkmcnt(1)\n"
RD"v[212:215],v244 offset:38912\n"
MF"v[132:135],v[220:223],a[176:179],v[132:135]\n"
MF"v[140:143],v[220:223],a[180:183],v[140:143]\n"
MF"v[148:151],v[220:223],a[184:187],v[148:151]\n"
MF"v[156:159],v[220:223],a[188:191],v[156:159]\n"
RD"v[216:219],v245 offset:36864\n"
MF"v[128:131],v[192:195],a[192:195],v[128:131]\n"
MF"v[136:139],v[192:195],a[196:199],v[136:139]\n"
MF"v[144:147],v[192:195],a[200:203],v[144:147]\n"
MF"v[152:155],v[192:195],a[204:207],v[152:155]\n"
RD"v[220:223],v245 offset:38912\n"
MF"v[132:135],v[196:199],a[192:195],v[132:135]\n"
MF"v[140:143],v[196:199],a[196:199],v[140:143]\n"
MF"v[148:151],v[196:199],a[200:203],v[148:151]\n"
MF"v[156:159],v[196:199],a[204:207],v[156:159]\n"
BS"v[88:91],v250,s[8:11],s49 offen offset:3072 sc1\n"
MF"v[128:131],v[200:203],a[208:211],v[128:131]\n"
WT"vmcnt(11)\n"
BS"v[100:103],v252,s[8:11],s92 offen sc1\n"
MF"v[136:139],v[200:203],a[212:215],v[136:139]\n"
MF"v[144:147],v[200:203],a[216:219],v[144:147]\n"
MF"v[152:155],v[200:203],a[220:223],v[152:155]\n"
MF"v[132:135],v[204:207],a[208:211],v[132:135]\n"
MF"v[140:143],v[204:207],a[212:215],v[140:143]\n"
WT"lgkmcnt(0)\n"
"s_barrier\n"
MF"v[148:151],v[204:207],a[216:219],v[148:151]\n"
SA"m0,s36,32768\n"
RD"v[192:195],v242 offset:16384\n"
SA"s48,s60,s61\n"
MF"v[156:159],v[204:207],a[220:223],v[156:159]\n"
MF"v[128:131],v[208:211],a[224:227],v[128:131]\n"
BL"v238,s[20:23],s48 offen offset:0 lds\n"
MF"v[136:139],v[208:211],a[228:231],v[136:139]\n"
MF"v[144:147],v[208:211],a[232:235],v[144:147]\n"
RD"v[196:199],v242 offset:18432\n"
MF"v[152:155],v[208:211],a[236:239],v[152:155]\n"
MF"v[132:135],v[212:215],a[224:227],v[132:135]\n"
MF"v[140:143],v[212:215],a[228:231],v[140:143]\n"
MF"v[148:151],v[212:215],a[232:235],v[148:151]\n"
RD"v[200:203],v243 offset:16384\n"
MF"v[156:159],v[212:215],a[236:239],v[156:159]\n"
MF"v[128:131],v[216:219],a[240:243],v[128:131]\n"
"s_lshl_b32 s50,s34,3\n"
BL"v239,s[20:23],s48 offen offset:1024 lds\n"
MF"v[136:139],v[216:219],a[244:247],v[136:139]\n"
SA"s51,s50,s30\n"
MF"v[144:147],v[216:219],a[248:251],v[144:147]\n"
"s_mul_i32 s51,s51,20480\n"
RD"v[204:207],v243 offset:18432\n"
MF"v[152:155],v[216:219],a[252:255],v[152:155]\n"
SA"s56,s51,65536\n"
MF"v[132:135],v[220:223],a[240:243],v[132:135]\n"
SA"s57,s56,81920\n"
MF"v[140:143],v[220:223],a[244:247],v[140:143]\n"
"s_sub_u32 s51,s30,s50\n"
RD"v[208:211],v242 offset:20480\n"
MF"v[148:151],v[220:223],a[248:251],v[148:151]\n"
SA"s51,s51,120\n"
MF"v[156:159],v[220:223],a[252:255],v[156:159]\n"
WT"lgkmcnt(2)\n"
"s_mul_i32 s51,s51,20480\n"
MF"v[0:3],v[192:195],a[0:3],0\n"
SA"s58,s51,65536\n"
BL"v240,s[20:23],s48 offen offset:2048 lds\n"
MF"v[16:19],v[192:195],a[4:7],0\n"
"s_lshl_b32 s99,s99,12\n"
RD"v[212:215],v242 offset:22528\n"
SA"s59,s58,81920\n"
MF"v[32:35],v[192:195],a[8:11],0\n"
"v_add_u32 v235,s99,v224\n"
MF"v[48:51],v[192:195],a[12:15],0\n"
PK"v64,v128,v129\n"
PK"v65,v130,v131\n"
MF"v[8:11],v[196:199],a[0:3],0\n"
"s_mov_b32 s49,24576\n"
PK"v66,v132,v133\n"
PK"v67,v134,v135\n"
MF"v[24:27],v[196:199],a[4:7],0\n"
RD"v[216:219],v243 offset:20480\n"
MF"v[40:43],v[196:199],a[8:11],0\n"
BS"v[96:99],v250,s[8:11],s49 offen offset:0 sc1\n"
MF"v[56:59],v[196:199],a[12:15],0\n"
MF"v[0:3],v[200:203],a[16:19],v[0:3]\n"
SA"s61,s61,524288\n"
BL"v241,s[20:23],s48 offen offset:3072 lds\n"
MF"v[16:19],v[200:203],a[20:23],v[16:19]\n"
"s_cmp_eq_u32 s61,5242880\n"
BL"a[0:3],v235,s[8:11],s56 offen offset:0 sc1\n"
MF"v[32:35],v[200:203],a[24:27],v[32:35]\n"
"s_cselect_b32 s61,0,s61\n"
RD"v[220:223],v243 offset:22528\n"
PK"v68,v136,v137\n"
MF"v[48:51],v[200:203],a[28:31],v[48:51]\n"
WT"lgkmcnt(2)\n"
PK"v69,v138,v139\n"
PK"v70,v140,v141\n"
MF"v[8:11],v[204:207],a[16:19],v[8:11]\n"
RD"v[192:195],v242 offset:40960\n"
PK"v71,v142,v143\n"
MF"v[24:27],v[204:207],a[20:23],v[24:27]\n"
PK"v72,v144,v145\n"
PK"v73,v146,v147\n"
MF"v[40:43],v[204:207],a[24:27],v[40:43]\n"
BL"a[4:7],v235,s[8:11],s57 offen offset:0 sc1\n"
MF"v[56:59],v[204:207],a[28:31],v[56:59]\n"
PK"v74,v148,v149\n"
PK"v75,v150,v151\n"
MF"v[0:3],v[208:211],a[32:35],v[0:3]\n"
RD"v[196:199],v242 offset:43008\n"
MF"v[16:19],v[208:211],a[36:39],v[16:19]\n"
MF"v[32:35],v[208:211],a[40:43],v[32:35]\n"
PK"v76,v152,v153\n"
PK"v77,v154,v155\n"
MF"v[48:51],v[208:211],a[44:47],v[48:51]\n"
PK"v78,v156,v157\n"
PK"v79,v158,v159\n"
MF"v[8:11],v[212:215],a[32:35],v[8:11]\n"
RD"v[200:203],v243 offset:40960\n"
MF"v[24:27],v[212:215],a[36:39],v[24:27]\n"
BL"a[8:11],v235,s[8:11],s58 offen offset:0 sc1\n"
MF"v[40:43],v[212:215],a[40:43],v[40:43]\n"
MF"v[56:59],v[212:215],a[44:47],v[56:59]\n"
WT"lgkmcnt(1)\n"
MF"v[0:3],v[216:219],a[48:51],v[0:3]\n"
RD"v[204:207],v243 offset:43008\n"
MF"v[16:19],v[216:219],a[52:55],v[16:19]\n"
MF"v[32:35],v[216:219],a[56:59],v[32:35]\n"
BL"a[12:15],v235,s[8:11],s59 offen offset:0 sc1\n"
MF"v[48:51],v[216:219],a[60:63],v[48:51]\n"
RD"v[208:211],v242 offset:45056\n"
MF"v[8:11],v[220:223],a[48:51],v[8:11]\n"
MF"v[24:27],v[220:223],a[52:55],v[24:27]\n"
MF"v[40:43],v[220:223],a[56:59],v[40:43]\n"
MF"v[56:59],v[220:223],a[60:63],v[56:59]\n"
RD"v[212:215],v242 offset:47104\n"
MF"v[0:3],v[192:195],a[64:67],v[0:3]\n"
BL"a[16:19],v235,s[8:11],s56 offen offset:1024 sc1\n"
MF"v[16:19],v[192:195],a[68:71],v[16:19]\n"
MF"v[32:35],v[192:195],a[72:75],v[32:35]\n"
MF"v[48:51],v[192:195],a[76:79],v[48:51]\n"
RD"v[216:219],v243 offset:45056\n"
MF"v[8:11],v[196:199],a[64:67],v[8:11]\n"
MF"v[24:27],v[196:199],a[68:71],v[24:27]\n"
BS"v[104:107],v250,s[8:11],s49 offen offset:1024 sc1\n"
MF"v[40:43],v[196:199],a[72:75],v[40:43]\n"
BL"a[20:23],v235,s[8:11],s57 offen offset:1024 sc1\n"
MF"v[56:59],v[196:199],a[76:79],v[56:59]\n"
WT"lgkmcnt(1)\n"
RD"v[220:223],v243 offset:47104\n"
MF"v[0:3],v[200:203],a[80:83],v[0:3]\n"
MF"v[16:19],v[200:203],a[84:87],v[16:19]\n"
MF"v[32:35],v[200:203],a[88:91],v[32:35]\n"
RD"v[192:195],v244 offset:16384\n"
MF"v[48:51],v[200:203],a[92:95],v[48:51]\n"
MF"v[8:11],v[204:207],a[80:83],v[8:11]\n"
BL"a[24:27],v235,s[8:11],s58 offen offset:1024 sc1\n"
MF"v[24:27],v[204:207],a[84:87],v[24:27]\n"
MF"v[40:43],v[204:207],a[88:91],v[40:43]\n"
RD"v[196:199],v244 offset:18432\n"
MF"v[56:59],v[204:207],a[92:95],v[56:59]\n"
MF"v[0:3],v[208:211],a[96:99],v[0:3]\n"
MF"v[16:19],v[208:211],a[100:103],v[16:19]\n"
MF"v[32:35],v[208:211],a[104:107],v[32:35]\n"
RD"v[200:203],v245 offset:16384\n"
MF"v[48:51],v[208:211],a[108:111],v[48:51]\n"
BL"a[28:31],v235,s[8:11],s59 offen offset:1024 sc1\n"
MF"v[8:11],v[212:215],a[96:99],v[8:11]\n"
MF"v[24:27],v[212:215],a[100:103],v[24:27]\n"
MF"v[40:43],v[212:215],a[104:107],v[40:43]\n"
RD"v[204:207],v245 offset:18432\n"
MF"v[56:59],v[212:215],a[108:111],v[56:59]\n"
WT"lgkmcnt(2)\n"
MF"v[0:3],v[216:219],a[112:115],v[0:3]\n"
BL"a[32:35],v235,s[8:11],s56 offen offset:2048 sc1\n"
MF"v[16:19],v[216:219],a[116:119],v[16:19]\n"
RD"v[208:211],v244 offset:20480\n"
MF"v[32:35],v[216:219],a[120:123],v[32:35]\n"
MF"v[48:51],v[216:219],a[124:127],v[48:51]\n"
MF"v[8:11],v[220:223],a[112:115],v[8:11]\n"
MF"v[24:27],v[220:223],a[116:119],v[24:27]\n"
RD"v[212:215],v244 offset:22528\n"
MF"v[40:43],v[220:223],a[120:123],v[40:43]\n"
BL"a[36:39],v235,s[8:11],s57 offen offset:2048 sc1\n"
MF"v[56:59],v[220:223],a[124:127],v[56:59]\n"
MF"v[0:3],v[192:195],a[128:131],v[0:3]\n"
MF"v[16:19],v[192:195],a[132:135],v[16:19]\n"
RD"v[216:219],v245 offset:20480\n"
MF"v[32:35],v[192:195],a[136:139],v[32:35]\n"
MF"v[48:51],v[192:195],a[140:143],v[48:51]\n"
MF"v[8:11],v[196:199],a[128:131],v[8:11]\n"
BS"v[112:115],v250,s[8:11],s49 offen offset:2048 sc1\n"
MF"v[24:27],v[196:199],a[132:135],v[24:27]\n"
BL"a[40:43],v235,s[8:11],s58 offen offset:2048 sc1\n"
MF"v[40:43],v[196:199],a[136:139],v[40:43]\n"
RD"v[220:223],v245 offset:22528\n"
MF"v[56:59],v[196:199],a[140:143],v[56:59]\n"
WT"lgkmcnt(2)\n"
MF"v[0:3],v[200:203],a[144:147],v[0:3]\n"
RD"v[192:195],v244 offset:40960\n"
MF"v[16:19],v[200:203],a[148:151],v[16:19]\n"
MF"v[32:35],v[200:203],a[152:155],v[32:35]\n"
BL"a[44:47],v235,s[8:11],s59 offen offset:2048 sc1\n"
MF"v[48:51],v[200:203],a[156:159],v[48:51]\n"
MF"v[8:11],v[204:207],a[144:147],v[8:11]\n"
RD"v[196:199],v244 offset:43008\n"
MF"v[24:27],v[204:207],a[148:151],v[24:27]\n"
MF"v[40:43],v[204:207],a[152:155],v[40:43]\n"
MF"v[56:59],v[204:207],a[156:159],v[56:59]\n"
MF"v[0:3],v[208:211],a[160:163],v[0:3]\n"
"s_sub_u32 s53,s37,65536\n"
RD"v[200:203],v245 offset:40960\n"
MF"v[16:19],v[208:211],a[164:167],v[16:19]\n"
BL"a[48:51],v235,s[8:11],s56 offen offset:3072 sc1\n"
MF"v[32:35],v[208:211],a[168:171],v[32:35]\n"
"v_subrev_u32 v246,s53,v246\n"
"v_subrev_u32 v247,s53,v247\n"
MF"v[48:51],v[208:211],a[172:175],v[48:51]\n"
"v_subrev_u32 v248,s53,v248\n"
"v_subrev_u32 v249,s53,v249\n"
MF"v[8:11],v[212:215],a[160:163],v[8:11]\n"
RD"v[204:207],v245 offset:43008\n"
"v_subrev_u32 v236,65536,v246\n"
MF"v[24:27],v[212:215],a[164:167],v[24:27]\n"
"v_subrev_u32 v237,65536,v247\n"
"v_subrev_u32 v228,65536,v248\n"
MF"v[40:43],v[212:215],a[168:171],v[40:43]\n"
BL"a[52:55],v235,s[8:11],s57 offen offset:3072 sc1\n"
MF"v[56:59],v[212:215],a[172:175],v[56:59]\n"
WT"lgkmcnt(1)\n"
RD"v[208:211],v244 offset:45056\n"
"v_subrev_u32 v229,65536,v249\n"
MF"v[0:3],v[216:219],a[176:179],v[0:3]\n"
MF"v[16:19],v[216:219],a[180:183],v[16:19]\n"
MF"v[32:35],v[216:219],a[184:187],v[32:35]\n"
MF"v[48:51],v[216:219],a[188:191],v[48:51]\n"
RD"v[212:215],v244 offset:47104\n"
MF"v[8:11],v[220:223],a[176:179],v[8:11]\n"
BL"a[56:59],v235,s[8:11],s58 offen offset:3072 sc1\n"
MF"v[24:27],v[220:223],a[180:183],v[24:27]\n"
MF"v[40:43],v[220:223],a[184:187],v[40:43]\n"
MF"v[56:59],v[220:223],a[188:191],v[56:59]\n"
RD"v[216:219],v245 offset:45056\n"
MF"v[0:3],v[192:195],a[192:195],v[0:3]\n"
SA"s99,s99,4096\n"
MF"v[16:19],v[192:195],a[196:199],v[16:19]\n"
"s_cmp_eq_u32 s99,20480\n"
"s_cselect_b32 s99,0,s99\n"
MF"v[32:35],v[192:195],a[200:203],v[32:35]\n"
BL"a[60:63],v235,s[8:11],s59 offen offset:3072 sc1\n"
MF"v[48:51],v[192:195],a[204:207],v[48:51]\n"
RD"v[220:223],v245 offset:47104\n"
MF"v[8:11],v[196:199],a[192:195],v[8:11]\n"
BS"v[120:123],v250,s[8:11],s49 offen offset:3072 sc1\n"
MF"v[24:27],v[196:199],a[196:199],v[24:27]\n"
MF"v[40:43],v[196:199],a[200:203],v[40:43]\n"
MF"v[56:59],v[196:199],a[204:207],v[56:59]\n"
"v_add_u32 v235,s99,v224\n"
MF"v[0:3],v[200:203],a[208:211],v[0:3]\n"
BL"a[64:67],v235,s[8:11],s56 offen offset:0 sc1\n"
MF"v[16:19],v[200:203],a[212:215],v[16:19]\n"
MF"v[32:35],v[200:203],a[216:219],v[32:35]\n"
MF"v[48:51],v[200:203],a[220:223],v[48:51]\n"
WT"lgkmcnt(0)\n"
MF"v[8:11],v[204:207],a[208:211],v[8:11]\n"
MF"v[24:27],v[204:207],a[212:215],v[24:27]\n"
"s_barrier\n"
MF"v[40:43],v[204:207],a[216:219],v[40:43]\n"
RT"a[128:129],v236 offset:0\n"
MF"v[56:59],v[204:207],a[220:223],v[56:59]\n"
BL"a[68:71],v235,s[8:11],s57 offen offset:0 sc1\n"
MF"v[0:3],v[208:211],a[224:227],v[0:3]\n"
SA"m0,s38,73728\n"
RT"a[130:131],v236 offset:512\n"
SA"s48,s60,s61\n"
MF"v[16:19],v[208:211],a[228:231],v[16:19]\n"
MF"v[32:35],v[208:211],a[232:235],v[32:35]\n"
RT"a[132:133],v237 offset:0\n"
"v_add_u32 v242,49152,v246\n"
MF"v[48:51],v[208:211],a[236:239],v[48:51]\n"
BL"v238,s[20:23],s48 offen offset:0 lds\n"
MF"v[8:11],v[212:215],a[224:227],v[8:11]\n"
RT"a[134:135],v237 offset:512\n"
"v_add_u32 v243,49152,v247\n"
MF"v[24:27],v[212:215],a[228:231],v[24:27]\n"
BL"a[72:75],v235,s[8:11],s58 offen offset:0 sc1\n"
MF"v[40:43],v[212:215],a[232:235],v[40:43]\n"
RT"a[136:137],v228 offset:0\n"
"v_add_u32 v244,49152,v248\n"
MF"v[56:59],v[212:215],a[236:239],v[56:59]\n"
"v_add_u32 v245,49152,v249\n"
MF"v[0:3],v[216:219],a[240:243],v[0:3]\n"
RT"a[138:139],v228 offset:512\n"
MF"v[16:19],v[216:219],a[244:247],v[16:19]\n"
MF"v[32:35],v[216:219],a[248:251],v[32:35]\n"
RT"a[140:141],v229 offset:0\n"
MF"v[48:51],v[216:219],a[252:255],v[48:51]\n"
BL"v239,s[20:23],s48 offen offset:1024 lds\n"
MF"v[8:11],v[220:223],a[240:243],v[8:11]\n"
BL"a[76:79],v235,s[8:11],s59 offen offset:0 sc1\n"
MF"v[24:27],v[220:223],a[244:247],v[24:27]\n"
RT"a[142:143],v229 offset:512\n"
MF"v[40:43],v[220:223],a[248:251],v[40:43]\n"
RT"a[144:145],v236 offset:4096\n"
MF"v[56:59],v[220:223],a[252:255],v[56:59]\n"
WT"vmcnt(24) lgkmcnt(3)\n"
MF"v[80:83],a[128:131],a[0:3],0\n"
WT"vmcnt(23)\n"
RT"a[146:147],v236 offset:4608\n"
MF"v[88:91],a[128:131],a[4:7],0\n"
WT"vmcnt(22)\n"
BL"a[80:83],v235,s[8:11],s56 offen offset:1024 sc1\n"
MF"v[96:99],a[128:131],a[8:11],0\n"
WT"vmcnt(22)\n"
RT"a[148:149],v237 offset:4096\n"
MF"v[104:107],a[128:131],a[12:15],0\n"
BL"v240,s[20:23],s48 offen offset:2048 lds\n"
MF"v[84:87],a[132:135],a[0:3],0\n"
RT"a[150:151],v237 offset:4608\n"
PK"v0,v0,v1\n"
MF"v[92:95],a[132:135],a[4:7],0\n"
RT"a[152:153],v228 offset:4096\n"
PK"v1,v2,v3\n"
MF"v[100:103],a[132:135],a[8:11],0\n"
PK"v2,v8,v9\n"
PK"v3,v10,v11\n"
MF"v[108:111],a[132:135],a[12:15],0\n"
RT"a[154:155],v228 offset:4608\n"
MF"v[112:115],a[136:139],a[0:3],0\n"
BL"a[84:87],v235,s[8:11],s57 offen offset:1024 sc1\n"
MF"v[120:123],a[136:139],a[4:7],0\n"
RT"a[156:157],v229 offset:4096\n"
PK"v16,v16,v17\n"
MF"v[128:131],a[136:139],a[8:11],0\n"
SA"s61,s61,524288\n"
BL"v241,s[20:23],s48 offen offset:3072 lds\n"
MF"v[136:139],a[136:139],a[12:15],0\n"
WT"lgkmcnt(3)\n"
RT"a[158:159],v229 offset:4608\n"
PK"v17,v18,v19\n"
MF"v[116:119],a[140:143],a[0:3],0\n"
PK"v18,v24,v25\n"
PK"v19,v26,v27\n"
MF"v[124:127],a[140:143],a[4:7],0\n"
RT"a[160:161],v236 offset:8192\n"
PK"v32,v32,v33\n"
MF"v[132:135],a[140:143],a[8:11],0\n"
BL"a[88:91],v235,s[8:11],s58 offen offset:1024 sc1\n"
MF"v[140:143],a[140:143],a[12:15],0\n"
RT"a[162:163],v236 offset:8704\n"
PK"v33,v34,v35\n"
MF"v[144:147],a[144:147],a[0:3],0\n"
PK"v34,v40,v41\n"
PK"v35,v42,v43\n"
MF"v[152:155],a[144:147],a[4:7],0\n"
RT"a[164:165],v237 offset:8192\n"
MF"v[160:163],a[144:147],a[8:11],0\n"
PK"v48,v48,v49\n"
PK"v49,v50,v51\n"
MF"v[168:171],a[144:147],a[12:15],0\n"
RT"a[166:167],v237 offset:8704\n"
PK"v50,v56,v57\n"
MF"v[148:151],a[148:151],a[0:3],0\n"
PK"v51,v58,v59\n"
MF"v[156:159],a[148:151],a[4:7],0\n"
RT"a[168:169],v228 offset:8192\n"
MF"v[164:167],a[148:151],a[8:11],0\n"
BL"a[92:95],v235,s[8:11],s59 offen offset:1024 sc1\n"
MF"v[172:175],a[148:151],a[12:15],0\n"
WT"lgkmcnt(3)\n"
RT"a[170:171],v228 offset:8704\n"
MF"v[176:179],a[152:155],a[0:3],0\n"
"s_mov_b32 s49,262144\n"
RT"a[172:173],v229 offset:8192\n"
MF"v[184:187],a[152:155],a[4:7],0\n"
MF"v[192:195],a[152:155],a[8:11],0\n"
RT"a[174:175],v229 offset:8704\n"
MF"v[200:203],a[152:155],a[12:15],0\n"
BS"v[4:7],v250,s[8:11],s49 offen offset:0 sc1\n"
MF"v[180:183],a[156:159],a[0:3],0\n"
RT"a[176:177],v236 offset:12288\n"
MF"v[188:191],a[156:159],a[4:7],0\n"
BL"a[96:99],v235,s[8:11],s56 offen offset:2048 sc1\n"
MF"v[196:199],a[156:159],a[8:11],0\n"
RT"a[178:179],v236 offset:12800\n"
MF"v[204:207],a[156:159],a[12:15],0\n"
WT"vmcnt(28)\n"
MF"v[80:83],a[160:163],a[16:19],v[80:83]\n"
WT"vmcnt(26)\n"
RT"a[180:181],v237 offset:12288\n"
MF"v[88:91],a[160:163],a[20:23],v[88:91]\n"
WT"vmcnt(25)\n"
MF"v[96:99],a[160:163],a[24:27],v[96:99]\n"
WT"vmcnt(24)\n"
RT"a[182:183],v237 offset:12800\n"
SA"m0,s38,122880\n"
MF"v[104:107],a[160:163],a[28:31],v[104:107]\n"
WT"lgkmcnt(3)\n"
BL"a[100:103],v235,s[8:11],s57 offen offset:2048 sc1\n"
MF"v[84:87],a[164:167],a[16:19],v[84:87]\n"
SA"s48,s60,s61\n"
RT"a[184:185],v228 offset:12288\n"
"s_mov_b32 s88,2048\n"
MF"v[92:95],a[164:167],a[20:23],v[92:95]\n"
BL"v238,s[20:23],s48 offen offset:0 lds\n"
MF"v[100:103],a[164:167],a[24:27],v[100:103]\n"
RT"a[186:187],v228 offset:12800\n"
MF"v[108:111],a[164:167],a[28:31],v[108:111]\n"
MF"v[112:115],a[168:171],a[16:19],v[112:115]\n"
RT"a[188:189],v229 offset:12288\n"
MF"v[120:123],a[168:171],a[20:23],v[120:123]\n"
MF"v[128:131],a[168:171],a[24:27],v[128:131]\n"
RT"a[190:191],v229 offset:12800\n"
MF"v[136:139],a[168:171],a[28:31],v[136:139]\n"
BL"a[104:107],v235,s[8:11],s58 offen offset:2048 sc1\n"
MF"v[116:119],a[172:175],a[16:19],v[116:119]\n"
RT"a[128:129],v236 offset:16384\n"
MF"v[124:127],a[172:175],a[20:23],v[124:127]\n"
BL"v239,s[20:23],s48 offen offset:1024 lds\n"
MF"v[132:135],a[172:175],a[24:27],v[132:135]\n"
RT"a[130:131],v236 offset:16896\n"
MF"v[140:143],a[172:175],a[28:31],v[140:143]\n"
WT"lgkmcnt(3)\n"
RT"a[132:133],v237 offset:16384\n"
MF"v[144:147],a[176:179],a[16:19],v[144:147]\n"
MF"v[152:155],a[176:179],a[20:23],v[152:155]\n"
RT"a[134:135],v237 offset:16896\n"
MF"v[160:163],a[176:179],a[24:27],v[160:163]\n"
BL"a[108:111],v235,s[8:11],s59 offen offset:2048 sc1\n"
MF"v[168:171],a[176:179],a[28:31],v[168:171]\n"
RT"a[136:137],v228 offset:16384\n"
MF"v[148:151],a[180:183],a[16:19],v[148:151]\n"
BL"v240,s[20:23],s48 offen offset:2048 lds\n"
MF"v[156:159],a[180:183],a[20:23],v[156:159]\n"
RT"a[138:139],v228 offset:16896\n"
MF"v[164:167],a[180:183],a[24:27],v[164:167]\n"
MF"v[172:175],a[180:183],a[28:31],v[172:175]\n"
RT"a[140:141],v229 offset:16384\n"
MF"v[176:179],a[184:187],a[16:19],v[176:179]\n"
BL"a[112:115],v235,s[8:11],s56 offen offset:3072 sc1\n"
MF"v[184:187],a[184:187],a[20:23],v[184:187]\n"
RT"a[142:143],v229 offset:16896\n"
MF"v[192:195],a[184:187],a[24:27],v[192:195]\n"
MF"v[200:203],a[184:187],a[28:31],v[200:203]\n"
WT"lgkmcnt(3)\n"
RT"a[144:145],v236 offset:20480\n"
MF"v[180:183],a[188:191],a[16:19],v[180:183]\n"
SA"s61,s61,524288\n"
BS"v[12:15],v250,s[8:11],s49 offen offset:1024 sc1\n"
MF"v[188:191],a[188:191],a[20:23],v[188:191]\n"
"s_cmp_eq_u32 s61,5242880\n"
BL"v241,s[20:23],s48 offen offset:3072 lds\n"
MF"v[196:199],a[188:191],a[24:27],v[196:199]\n"
"s_cselect_b32 s61,0,s61\n"
RT"a[146:147],v236 offset:20992\n"
MF"v[204:207],a[188:191],a[28:31],v[204:207]\n"
WT"vmcnt(32)\n"
RT"a[148:149],v237 offset:20480\n"
MF"v[80:83],a[128:131],a[32:35],v[80:83]\n"
WT"vmcnt(31)\n"
BL"a[116:119],v235,s[8:11],s57 offen offset:3072 sc1\n"
MF"v[88:91],a[128:131],a[36:39],v[88:91]\n"
WT"vmcnt(30)\n"
RT"a[150:151],v237 offset:20992\n"
MF"v[96:99],a[128:131],a[40:43],v[96:99]\n"
WT"vmcnt(29)\n"
RT"a[152:153],v228 offset:20480\n"
MF"v[104:107],a[128:131],a[44:47],v[104:107]\n"
MF"v[84:87],a[132:135],a[32:35],v[84:87]\n"
RT"a[154:155],v228 offset:20992\n"
MF"v[92:95],a[132:135],a[36:39],v[92:95]\n"
MF"v[100:103],a[132:135],a[40:43],v[100:103]\n"
RT"a[156:157],v229 offset:20480\n"
MF"v[108:111],a[132:135],a[44:47],v[108:111]\n"
WT"lgkmcnt(4)\n"
BL"a[120:123],v235,s[8:11],s58 offen offset:3072 sc1\n"
MF"v[112:115],a[136:139],a[32:35],v[112:115]\n"
RT"a[158:159],v229 offset:20992\n"
MF"v[120:123],a[136:139],a[36:39],v[120:123]\n"
MF"v[128:131],a[136:139],a[40:43],v[128:131]\n"
RT"a[160:161],v236 offset:24576\n"
MF"v[136:139],a[136:139],a[44:47],v[136:139]\n"
MF"v[116:119],a[140:143],a[32:35],v[116:119]\n"
SA"s99,s99,4096\n"
RT"a[162:163],v236 offset:25088\n"
MF"v[124:127],a[140:143],a[36:39],v[124:127]\n"
"s_cmp_eq_u32 s99,20480\n"
BL"a[124:127],v235,s[8:11],s59 offen offset:3072 sc1\n"
MF"v[132:135],a[140:143],a[40:43],v[132:135]\n"
"s_cselect_b32 s99,0,s99\n"
RT"a[164:165],v237 offset:24576\n"
MF"v[140:143],a[140:143],a[44:47],v[140:143]\n"
MF"v[144:147],a[144:147],a[32:35],v[144:147]\n"
RT"a[166:167],v237 offset:25088\n"
MF"v[152:155],a[144:147],a[36:39],v[152:155]\n"
MF"v[160:163],a[144:147],a[40:43],v[160:163]\n"
RT"a[168:169],v228 offset:24576\n"
MF"v[168:171],a[144:147],a[44:47],v[168:171]\n"
WT"lgkmcnt(3)\n"
"v_add_u32 v235,s99,v224\n"
MF"v[148:151],a[148:151],a[32:35],v[148:151]\n"
RT"a[170:171],v228 offset:25088\n"
MF"v[156:159],a[148:151],a[36:39],v[156:159]\n"
BL"a[0:3],v235,s[8:11],s56 offen offset:0 sc1\n"
MF"v[164:167],a[148:151],a[40:43],v[164:167]\n"
RT"a[172:173],v229 offset:24576\n"
MF"v[172:175],a[148:151],a[44:47],v[172:175]\n"
RT"a[174:175],v229 offset:25088\n"
MF"v[176:179],a[152:155],a[32:35],v[176:179]\n"
MF"v[184:187],a[152:155],a[36:39],v[184:187]\n"
RT"a[176:177],v236 offset:28672\n"
MF"v[192:195],a[152:155],a[40:43],v[192:195]\n"
MF"v[200:203],a[152:155],a[44:47],v[200:203]\n"
RT"a[178:179],v236 offset:29184\n"
MF"v[180:183],a[156:159],a[32:35],v[180:183]\n"
BL"a[4:7],v235,s[8:11],s57 offen offset:0 sc1\n"
MF"v[188:191],a[156:159],a[36:39],v[188:191]\n"
RT"a[180:181],v237 offset:28672\n"
MF"v[196:199],a[156:159],a[40:43],v[196:199]\n"
BS"v[20:23],v250,s[8:11],s49 offen offset:2048 sc1\n"
MF"v[204:207],a[156:159],a[44:47],v[204:207]\n"
RT"a[182:183],v237 offset:29184\n"
"LQ_%=:\n"
WT"vmcnt(33)\n"
MF"v[80:83],a[160:163],a[48:51],v[80:83]\n"
WT"vmcnt(32)\n"
MF"v[88:91],a[160:163],a[52:55],v[88:91]\n"
WT"vmcnt(31)\n"
RT"a[184:185],v228 offset:28672\n"
MF"v[96:99],a[160:163],a[56:59],v[96:99]\n"
WT"vmcnt(30)\n"
BL"a[8:11],v235,s[8:11],s58 offen offset:0 sc1\n"
MF"v[104:107],a[160:163],a[60:63],v[104:107]\n"
WT"lgkmcnt(3)\n"
RT"a[186:187],v228 offset:29184\n"
MF"v[84:87],a[164:167],a[48:51],v[84:87]\n"
MF"v[92:95],a[164:167],a[52:55],v[92:95]\n"
RT"a[188:189],v229 offset:28672\n"
MF"v[100:103],a[164:167],a[56:59],v[100:103]\n"
MF"v[108:111],a[164:167],a[60:63],v[108:111]\n"
RT"a[190:191],v229 offset:29184\n"
MF"v[112:115],a[168:171],a[48:51],v[112:115]\n"
"s_lshl_b32 s50,s35,3\n"
SA"s50,s50,s34\n"
"s_lshl_b32 s50,s50,3\n"
MF"v[120:123],a[168:171],a[52:55],v[120:123]\n"
SA"s50,s50,2304\n"
BL"a[12:15],v235,s[8:11],s59 offen offset:0 sc1\n"
MF"v[128:131],a[168:171],a[56:59],v[128:131]\n"
SA"s51,s50,s45\n"
MF"v[136:139],a[168:171],a[60:63],v[136:139]\n"
MF"v[116:119],a[172:175],a[48:51],v[116:119]\n"
MF"v[124:127],a[172:175],a[52:55],v[124:127]\n"
WT"vmcnt(29) lgkmcnt(0)\n"
"s_barrier\n"
MF"v[132:135],a[172:175],a[56:59],v[132:135]\n"
RT"a[128:129],v236 offset:49152\n"
MF"v[140:143],a[172:175],a[60:63],v[140:143]\n"
"buffer_store_dwordx2 v[232:233],v230,s[8:11],s51 offen sc1\n"
MF"v[144:147],a[176:179],a[48:51],v[144:147]\n"
RT"a[130:131],v236 offset:49664\n"
MF"v[152:155],a[176:179],a[52:55],v[152:155]\n"
BL"a[16:19],v235,s[8:11],s56 offen offset:1024 sc1\n"
MF"v[160:163],a[176:179],a[56:59],v[160:163]\n"
SA"m0,s38,81920\n"
RT"a[132:133],v237 offset:49152\n"
SA"s48,s60,s61\n"
MF"v[168:171],a[176:179],a[60:63],v[168:171]\n"
MF"v[148:151],a[180:183],a[48:51],v[148:151]\n"
RT"a[134:135],v237 offset:49664\n"
MF"v[156:159],a[180:183],a[52:55],v[156:159]\n"
BL"v238,s[20:23],s48 offen offset:0 lds\n"
MF"v[164:167],a[180:183],a[56:59],v[164:167]\n"
RT"a[136:137],v228 offset:49152\n"
MF"v[172:175],a[180:183],a[60:63],v[172:175]\n"
BL"a[20:23],v235,s[8:11],s57 offen offset:1024 sc1\n"
MF"v[176:179],a[184:187],a[48:51],v[176:179]\n"
RT"a[138:139],v228 offset:49664\n"
MF"v[184:187],a[184:187],a[52:55],v[184:187]\n"
MF"v[192:195],a[184:187],a[56:59],v[192:195]\n"
RT"a[140:141],v229 offset:49152\n"
MF"v[200:203],a[184:187],a[60:63],v[200:203]\n"
MF"v[180:183],a[188:191],a[48:51],v[180:183]\n"
RT"a[142:143],v229 offset:49664\n"
MF"v[188:191],a[188:191],a[52:55],v[188:191]\n"
BL"v239,s[20:23],s48 offen offset:1024 lds\n"
MF"v[196:199],a[188:191],a[56:59],v[196:199]\n"
SA"s49,s88,262144\n"
RT"a[144:145],v236 offset:53248\n"
MF"v[204:207],a[188:191],a[60:63],v[204:207]\n"
WT"lgkmcnt(3)\n"
BL"a[24:27],v235,s[8:11],s58 offen offset:1024 sc1\n"
MF"v[80:83],a[128:131],a[64:67],v[80:83]\n"
RT"a[146:147],v236 offset:53760\n"
MF"v[88:91],a[128:131],a[68:71],v[88:91]\n"
WT"vmcnt(33)\n"
BS"v[28:31],v250,s[8:11],s49 offen offset:1024 sc1\n"
MF"v[96:99],a[128:131],a[72:75],v[96:99]\n"
WT"vmcnt(32)\n"
RT"a[148:149],v237 offset:53248\n"
MF"v[104:107],a[128:131],a[76:79],v[104:107]\n"
MF"v[84:87],a[132:135],a[64:67],v[84:87]\n"
RT"a[150:151],v237 offset:53760\n"
MF"v[92:95],a[132:135],a[68:71],v[92:95]\n"
BL"v240,s[20:23],s48 offen offset:2048 lds\n"
MF"v[100:103],a[132:135],a[72:75],v[100:103]\n"
RT"a[152:153],v228 offset:53248\n"
MF"v[108:111],a[132:135],a[76:79],v[108:111]\n"
BL"a[28:31],v235,s[8:11],s59 offen offset:1024 sc1\n"
MF"v[112:115],a[136:139],a[64:67],v[112:115]\n"
RT"a[154:155],v228 offset:53760\n"
MF"v[120:123],a[136:139],a[68:71],v[120:123]\n"
RT"a[156:157],v229 offset:53248\n"
MF"v[128:131],a[136:139],a[72:75],v[128:131]\n"
MF"v[136:139],a[136:139],a[76:79],v[136:139]\n"
WT"lgkmcnt(3)\n"
RT"a[158:159],v229 offset:53760\n"
MF"v[116:119],a[140:143],a[64:67],v[116:119]\n"
SA"s61,s61,524288\n"
BL"v241,s[20:23],s48 offen offset:3072 lds\n"
MF"v[124:127],a[140:143],a[68:71],v[124:127]\n"
BL"a[32:35],v235,s[8:11],s56 offen offset:2048 sc1\n"
MF"v[132:135],a[140:143],a[72:75],v[132:135]\n"
RT"a[160:161],v236 offset:57344\n"
MF"v[140:143],a[140:143],a[76:79],v[140:143]\n"
RT"a[162:163],v236 offset:57856\n"
MF"v[144:147],a[144:147],a[64:67],v[144:147]\n"
MF"v[152:155],a[144:147],a[68:71],v[152:155]\n"
RT"a[164:165],v237 offset:57344\n"
MF"v[160:163],a[144:147],a[72:75],v[160:163]\n"
MF"v[168:171],a[144:147],a[76:79],v[168:171]\n"
RT"a[166:167],v237 offset:57856\n"
MF"v[148:151],a[148:151],a[64:67],v[148:151]\n"
BL"a[36:39],v235,s[8:11],s57 offen offset:2048 sc1\n"
MF"v[156:159],a[148:151],a[68:71],v[156:159]\n"
RT"a[168:169],v228 offset:57344\n"
MF"v[164:167],a[148:151],a[72:75],v[164:167]\n"
MF"v[172:175],a[148:151],a[76:79],v[172:175]\n"
WT"lgkmcnt(3)\n"
RT"a[170:171],v228 offset:57856\n"
MF"v[176:179],a[152:155],a[64:67],v[176:179]\n"
RT"a[172:173],v229 offset:57344\n"
MF"v[184:187],a[152:155],a[68:71],v[184:187]\n"
MF"v[192:195],a[152:155],a[72:75],v[192:195]\n"
RT"a[174:175],v229 offset:57856\n"
MF"v[200:203],a[152:155],a[76:79],v[200:203]\n"
BL"a[40:43],v235,s[8:11],s58 offen offset:2048 sc1\n"
MF"v[180:183],a[156:159],a[64:67],v[180:183]\n"
RT"a[176:177],v236 offset:61440\n"
MF"v[188:191],a[156:159],a[68:71],v[188:191]\n"
MF"v[196:199],a[156:159],a[72:75],v[196:199]\n"
RT"a[178:179],v236 offset:61952\n"
MF"v[204:207],a[156:159],a[76:79],v[204:207]\n"
WT"vmcnt(37)\n"
MF"v[80:83],a[160:163],a[80:83],v[80:83]\n"
WT"vmcnt(35)\n"
RT"a[180:181],v237 offset:61440\n"
"s_mov_b32 s49,286720\n"
MF"v[88:91],a[160:163],a[84:87],v[88:91]\n"
WT"vmcnt(33)\n"
BL"a[44:47],v235,s[8:11],s59 offen offset:2048 sc1\n"
MF"v[96:99],a[160:163],a[88:91],v[96:99]\n"
WT"vmcnt(33)\n"
RT"a[182:183],v237 offset:61952\n"
MF"v[104:107],a[160:163],a[92:95],v[104:107]\n"
WT"lgkmcnt(3)\n"
BS"v[0:3],v250,s[8:11],s49 offen offset:0 sc1\n"
MF"v[84:87],a[164:167],a[80:83],v[84:87]\n"
RT"a[184:185],v228 offset:61440\n"
MF"v[92:95],a[164:167],a[84:87],v[92:95]\n"
MF"v[100:103],a[164:167],a[88:91],v[100:103]\n"
RT"a[186:187],v228 offset:61952\n"
MF"v[108:111],a[164:167],a[92:95],v[108:111]\n"
MF"v[112:115],a[168:171],a[80:83],v[112:115]\n"
RT"a[188:189],v229 offset:61440\n"
MF"v[120:123],a[168:171],a[84:87],v[120:123]\n"
BL"a[48:51],v235,s[8:11],s56 offen offset:3072 sc1\n"
MF"v[128:131],a[168:171],a[88:91],v[128:131]\n"
RT"a[190:191],v229 offset:61952\n"
MF"v[136:139],a[168:171],a[92:95],v[136:139]\n"
RT"a[128:129],v246 offset:0\n"
MF"v[116:119],a[172:175],a[80:83],v[116:119]\n"
MF"v[124:127],a[172:175],a[84:87],v[124:127]\n"
RT"a[130:131],v246 offset:512\n"
MF"v[132:135],a[172:175],a[88:91],v[132:135]\n"
SA"m0,s38,131072\n"
SA"s48,s60,s61\n"
MF"v[140:143],a[172:175],a[92:95],v[140:143]\n"
WT"lgkmcnt(3)\n"
RT"a[132:133],v247 offset:0\n"
MF"v[144:147],a[176:179],a[80:83],v[144:147]\n"
BL"a[52:55],v235,s[8:11],s57 offen offset:3072 sc1\n"
MF"v[152:155],a[176:179],a[84:87],v[152:155]\n"
BL"v238,s[20:23],s48 offen offset:0 lds\n"
MF"v[160:163],a[176:179],a[88:91],v[160:163]\n"
RT"a[134:135],v247 offset:512\n"
MF"v[168:171],a[176:179],a[92:95],v[168:171]\n"
RT"a[136:137],v248 offset:0\n"
MF"v[148:151],a[180:183],a[80:83],v[148:151]\n"
MF"v[156:159],a[180:183],a[84:87],v[156:159]\n"
RT"a[138:139],v248 offset:512\n"
MF"v[164:167],a[180:183],a[88:91],v[164:167]\n"
BL"a[56:59],v235,s[8:11],s58 offen offset:3072 sc1\n"
MF"v[172:175],a[180:183],a[92:95],v[172:175]\n"
RT"a[140:141],v249 offset:0\n"
MF"v[176:179],a[184:187],a[80:83],v[176:179]\n"
BL"v239,s[20:23],s48 offen offset:1024 lds\n"
MF"v[184:187],a[184:187],a[84:87],v[184:187]\n"
RT"a[142:143],v249 offset:512\n"
MF"v[192:195],a[184:187],a[88:91],v[192:195]\n"
MF"v[200:203],a[184:187],a[92:95],v[200:203]\n"
WT"lgkmcnt(3)\n"
RT"a[144:145],v246 offset:4096\n"
MF"v[180:183],a[188:191],a[80:83],v[180:183]\n"
SA"s99,s99,4096\n"
MF"v[188:191],a[188:191],a[84:87],v[188:191]\n"
"s_cmp_eq_u32 s99,20480\n"
RT"a[146:147],v246 offset:4608\n"
"s_cselect_b32 s99,0,s99\n"
MF"v[196:199],a[188:191],a[88:91],v[196:199]\n"
BL"a[60:63],v235,s[8:11],s59 offen offset:3072 sc1\n"
MF"v[204:207],a[188:191],a[92:95],v[204:207]\n"
WT"vmcnt(38)\n"
RT"a[148:149],v247 offset:4096\n"
MF"v[80:83],a[128:131],a[96:99],v[80:83]\n"
WT"vmcnt(37)\n"
BL"v240,s[20:23],s48 offen offset:2048 lds\n"
MF"v[88:91],a[128:131],a[100:103],v[88:91]\n"
WT"vmcnt(36)\n"
RT"a[150:151],v247 offset:4608\n"
MF"v[96:99],a[128:131],a[104:107],v[96:99]\n"
WT"vmcnt(34)\n"
RT"a[152:153],v248 offset:4096\n"
MF"v[104:107],a[128:131],a[108:111],v[104:107]\n"
"v_add_u32 v235,s99,v224\n"
MF"v[84:87],a[132:135],a[96:99],v[84:87]\n"
RT"a[154:155],v248 offset:4608\n"
MF"v[92:95],a[132:135],a[100:103],v[92:95]\n"
BS"v[16:19],v250,s[8:11],s49 offen offset:1024 sc1\n"
MF"v[100:103],a[132:135],a[104:107],v[100:103]\n"
BL"a[64:67],v235,s[8:11],s56 offen offset:0 sc1\n"
MF"v[108:111],a[132:135],a[108:111],v[108:111]\n"
WT"lgkmcnt(3)\n"
RT"a[156:157],v249 offset:4096\n"
MF"v[112:115],a[136:139],a[96:99],v[112:115]\n"
SA"s61,s61,524288\n"
RT"a[158:159],v249 offset:4608\n"
MF"v[120:123],a[136:139],a[100:103],v[120:123]\n"
"s_cmp_eq_u32 s61,5242880\n"
BL"v241,s[20:23],s48 offen offset:3072 lds\n"
MF"v[128:131],a[136:139],a[104:107],v[128:131]\n"
"s_cselect_b32 s61,0,s61\n"
RT"a[160:161],v246 offset:24576\n"
MF"v[136:139],a[136:139],a[108:111],v[136:139]\n"
BL"a[68:71],v235,s[8:11],s57 offen offset:0 sc1\n"
MF"v[116:119],a[140:143],a[96:99],v[116:119]\n"
RT"a[162:163],v246 offset:25088\n"
MF"v[124:127],a[140:143],a[100:103],v[124:127]\n"
MF"v[132:135],a[140:143],a[104:107],v[132:135]\n"
RT"a[164:165],v247 offset:24576\n"
MF"v[140:143],a[140:143],a[108:111],v[140:143]\n"
MF"v[144:147],a[144:147],a[96:99],v[144:147]\n"
"s_lshl_b32 s39,s35,20\n"
RT"a[166:167],v247 offset:25088\n"
"v_add_u32 v225,s36,v224\n"
MF"v[152:155],a[144:147],a[100:103],v[152:155]\n"
SA"s39,s39,9175040\n"
MF"v[160:163],a[144:147],a[104:107],v[160:163]\n"
RT"a[168:169],v248 offset:24576\n"
MF"v[168:171],a[144:147],a[108:111],v[168:171]\n"
WT"lgkmcnt(3)\n"
BL"a[72:75],v235,s[8:11],s58 offen offset:0 sc1\n"
MF"v[148:151],a[148:151],a[96:99],v[148:151]\n"
RT"a[170:171],v248 offset:25088\n"
MF"v[156:159],a[148:151],a[100:103],v[156:159]\n"
RT"a[172:173],v249 offset:24576\n"
MF"v[164:167],a[148:151],a[104:107],v[164:167]\n"
MF"v[172:175],a[148:151],a[108:111],v[172:175]\n"
RT"a[174:175],v249 offset:25088\n"
MF"v[176:179],a[152:155],a[96:99],v[176:179]\n"
MF"v[184:187],a[152:155],a[100:103],v[184:187]\n"
RT"a[176:177],v246 offset:28672\n"
MF"v[192:195],a[152:155],a[104:107],v[192:195]\n"
BL"a[76:79],v235,s[8:11],s59 offen offset:0 sc1\n"
MF"v[200:203],a[152:155],a[108:111],v[200:203]\n"
RT"a[178:179],v246 offset:29184\n"
MF"v[180:183],a[156:159],a[96:99],v[180:183]\n"
MF"v[188:191],a[156:159],a[100:103],v[188:191]\n"
RT"a[180:181],v247 offset:28672\n"
MF"v[196:199],a[156:159],a[104:107],v[196:199]\n"
MF"v[204:207],a[156:159],a[108:111],v[204:207]\n"
WT"vmcnt(38)\n"
RT"a[182:183],v247 offset:29184\n"
MF"v[80:83],a[160:163],a[112:115],v[80:83]\n"
WT"vmcnt(35)\n"
BL"a[80:83],v235,s[8:11],s56 offen offset:1024 sc1\n"
MF"v[88:91],a[160:163],a[116:119],v[88:91]\n"
WT"vmcnt(35)\n"
RT"a[184:185],v248 offset:28672\n"
MF"v[96:99],a[160:163],a[120:123],v[96:99]\n"
WT"vmcnt(34)\n"
MF"v[104:107],a[160:163],a[124:127],v[104:107]\n"
WT"lgkmcnt(3)\n"
RT"a[186:187],v248 offset:29184\n"
MF"v[84:87],a[164:167],a[112:115],v[84:87]\n"
SA"s49,s88,286720\n"
MF"v[92:95],a[164:167],a[116:119],v[92:95]\n"
RT"a[188:189],v249 offset:28672\n"
MF"v[100:103],a[164:167],a[120:123],v[100:103]\n"
BS"v[32:35],v250,s[8:11],s49 offen offset:0 sc1\n"
MF"v[108:111],a[164:167],a[124:127],v[108:111]\n"
RT"a[190:191],v249 offset:29184\n"
MF"v[112:115],a[168:171],a[112:115],v[112:115]\n"
BL"a[84:87],v235,s[8:11],s57 offen offset:1024 sc1\n"
MF"v[120:123],a[168:171],a[116:119],v[120:123]\n"
MF"v[128:131],a[168:171],a[120:123],v[128:131]\n"
MF"v[136:139],a[168:171],a[124:127],v[136:139]\n"
MF"v[116:119],a[172:175],a[112:115],v[116:119]\n"
MF"v[124:127],a[172:175],a[116:119],v[124:127]\n"
"s_barrier\n"
MF"v[132:135],a[172:175],a[120:123],v[132:135]\n"
RT"a[128:129],v242 offset:0\n"
MF"v[140:143],a[172:175],a[124:127],v[140:143]\n"
BL"a[88:91],v235,s[8:11],s58 offen offset:1024 sc1\n"
MF"v[144:147],a[176:179],a[112:115],v[144:147]\n"
RT"a[130:131],v242 offset:512\n"
MF"v[152:155],a[176:179],a[116:119],v[152:155]\n"
MF"v[160:163],a[176:179],a[120:123],v[160:163]\n"
RT"a[132:133],v243 offset:0\n"
MF"v[168:171],a[176:179],a[124:127],v[168:171]\n"
WT"lgkmcnt(3)\n"
MF"v[148:151],a[180:183],a[112:115],v[148:151]\n"
RT"a[134:135],v243 offset:512\n"
MF"v[156:159],a[180:183],a[116:119],v[156:159]\n"
BL"a[92:95],v235,s[8:11],s59 offen offset:1024 sc1\n"
MF"v[164:167],a[180:183],a[120:123],v[164:167]\n"
RT"a[136:137],v244 offset:0\n"
MF"v[172:175],a[180:183],a[124:127],v[172:175]\n"
MF"v[176:179],a[184:187],a[112:115],v[176:179]\n"
RT"a[138:139],v244 offset:512\n"
MF"v[184:187],a[184:187],a[116:119],v[184:187]\n"
MF"v[192:195],a[184:187],a[120:123],v[192:195]\n"
RT"a[140:141],v245 offset:0\n"
MF"v[200:203],a[184:187],a[124:127],v[200:203]\n"
MF"v[180:183],a[188:191],a[112:115],v[180:183]\n"
RT"a[142:143],v245 offset:512\n"
MF"v[188:191],a[188:191],a[116:119],v[188:191]\n"
BL"a[96:99],v235,s[8:11],s56 offen offset:2048 sc1\n"
MF"v[196:199],a[188:191],a[120:123],v[196:199]\n"
RT"a[144:145],v242 offset:4096\n"
MF"v[204:207],a[188:191],a[124:127],v[204:207]\n"
WT"vmcnt(38) lgkmcnt(3)\n"
MF"v[80:83],a[128:131],a[0:3],v[80:83]\n"
WT"vmcnt(37)\n"
RT"a[146:147],v242 offset:4608\n"
MF"v[88:91],a[128:131],a[4:7],v[88:91]\n"
WT"vmcnt(35)\n"
MF"v[96:99],a[128:131],a[8:11],v[96:99]\n"
WT"vmcnt(34)\n"
RT"a[148:149],v243 offset:4096\n"
MF"v[104:107],a[128:131],a[12:15],v[104:107]\n"
BL"a[100:103],v235,s[8:11],s57 offen offset:2048 sc1\n"
MF"v[84:87],a[132:135],a[0:3],v[84:87]\n"
RT"a[150:151],v243 offset:4608\n"
MF"v[92:95],a[132:135],a[4:7],v[92:95]\n"
RT"a[152:153],v244 offset:4096\n"
MF"v[100:103],a[132:135],a[8:11],v[100:103]\n"
MF"v[108:111],a[132:135],a[12:15],v[108:111]\n"
RT"a[154:155],v244 offset:4608\n"
MF"v[112:115],a[136:139],a[0:3],v[112:115]\n"
BS"v[48:51],v250,s[8:11],s49 offen offset:1024 sc1\n"
MF"v[120:123],a[136:139],a[4:7],v[120:123]\n"
RT"a[156:157],v245 offset:4096\n"
MF"v[128:131],a[136:139],a[8:11],v[128:131]\n"
BL"a[104:107],v235,s[8:11],s58 offen offset:2048 sc1\n"
MF"v[136:139],a[136:139],a[12:15],v[136:139]\n"
WT"lgkmcnt(3)\n"
RT"a[158:159],v245 offset:4608\n"
MF"v[116:119],a[140:143],a[0:3],v[116:119]\n"
MF"v[124:127],a[140:143],a[4:7],v[124:127]\n"
RT"a[160:161],v242 offset:24576\n"
MF"v[132:135],a[140:143],a[8:11],v[132:135]\n"
MF"v[140:143],a[140:143],a[12:15],v[140:143]\n"
RT"a[162:163],v242 offset:25088\n"
MF"v[144:147],a[144:147],a[0:3],v[144:147]\n"
MF"v[152:155],a[144:147],a[4:7],v[152:155]\n"
RT"a[164:165],v243 offset:24576\n"
MF"v[160:163],a[144:147],a[8:11],v[160:163]\n"
BL"a[108:111],v235,s[8:11],s59 offen offset:2048 sc1\n"
MF"v[168:171],a[144:147],a[12:15],v[168:171]\n"
RT"a[166:167],v243 offset:25088\n"
MF"v[148:151],a[148:151],a[0:3],v[148:151]\n"
MF"v[156:159],a[148:151],a[4:7],v[156:159]\n"
RT"a[168:169],v244 offset:24576\n"
MF"v[164:167],a[148:151],a[8:11],v[164:167]\n"
MF"v[172:175],a[148:151],a[12:15],v[172:175]\n"
WT"lgkmcnt(3)\n"
RT"a[170:171],v244 offset:25088\n"
MF"v[176:179],a[152:155],a[0:3],v[176:179]\n"
RT"a[172:173],v245 offset:24576\n"
MF"v[184:187],a[152:155],a[4:7],v[184:187]\n"
BL"a[112:115],v235,s[8:11],s56 offen offset:3072 sc1\n"
MF"v[192:195],a[152:155],a[8:11],v[192:195]\n"
RT"a[174:175],v245 offset:25088\n"
MF"v[200:203],a[152:155],a[12:15],v[200:203]\n"
MF"v[180:183],a[156:159],a[0:3],v[180:183]\n"
RT"a[176:177],v242 offset:28672\n"
MF"v[188:191],a[156:159],a[4:7],v[188:191]\n"
MF"v[196:199],a[156:159],a[8:11],v[196:199]\n"
RT"a[178:179],v242 offset:29184\n"
MF"v[204:207],a[156:159],a[12:15],v[204:207]\n"
WT"vmcnt(37)\n"
BL"a[116:119],v235,s[8:11],s57 offen offset:3072 sc1\n"
MF"v[80:83],a[160:163],a[16:19],v[80:83]\n"
WT"vmcnt(36)\n"
RT"a[180:181],v243 offset:28672\n"
MF"v[88:91],a[160:163],a[20:23],v[88:91]\n"
WT"vmcnt(34)\n"
MF"v[96:99],a[160:163],a[24:27],v[96:99]\n"
WT"vmcnt(31)\n"
RT"a[182:183],v243 offset:29184\n"
MF"v[104:107],a[160:163],a[28:31],v[104:107]\n"
WT"lgkmcnt(3)\n"
MF"v[84:87],a[164:167],a[16:19],v[84:87]\n"
RT"a[184:185],v244 offset:28672\n"
MF"v[92:95],a[164:167],a[20:23],v[92:95]\n"
MF"v[100:103],a[164:167],a[24:27],v[100:103]\n"
RT"a[186:187],v244 offset:29184\n"
MF"v[108:111],a[164:167],a[28:31],v[108:111]\n"
BL"a[120:123],v235,s[8:11],s58 offen offset:3072 sc1\n"
MF"v[112:115],a[168:171],a[16:19],v[112:115]\n"
"s_mov_b32 s49,270336\n"
RT"a[188:189],v245 offset:28672\n"
MF"v[120:123],a[168:171],a[20:23],v[120:123]\n"
MF"v[128:131],a[168:171],a[24:27],v[128:131]\n"
RT"a[190:191],v245 offset:29184\n"
MF"v[136:139],a[168:171],a[28:31],v[136:139]\n"
BS"v[36:39],v250,s[8:11],s49 offen offset:0 sc1\n"
MF"v[116:119],a[172:175],a[16:19],v[116:119]\n"
SA"s99,s99,4096\n"
RT"a[128:129],v236 offset:32768\n"
MF"v[124:127],a[172:175],a[20:23],v[124:127]\n"
"s_cmp_eq_u32 s99,20480\n"
RT"a[130:131],v236 offset:33280\n"
"s_cselect_b32 s99,0,s99\n"
MF"v[132:135],a[172:175],a[24:27],v[132:135]\n"
BL"a[124:127],v235,s[8:11],s59 offen offset:3072 sc1\n"
MF"v[140:143],a[172:175],a[28:31],v[140:143]\n"
WT"lgkmcnt(3)\n"
RT"a[132:133],v237 offset:32768\n"
MF"v[144:147],a[176:179],a[16:19],v[144:147]\n"
MF"v[152:155],a[176:179],a[20:23],v[152:155]\n"
RT"a[134:135],v237 offset:33280\n"
MF"v[160:163],a[176:179],a[24:27],v[160:163]\n"
MF"v[168:171],a[176:179],a[28:31],v[168:171]\n"
RT"a[136:137],v228 offset:32768\n"
"v_add_u32 v235,s99,v224\n"
MF"v[148:151],a[180:183],a[16:19],v[148:151]\n"
BL"a[192:195],v235,s[8:11],s56 offen offset:0 sc1\n"
MF"v[156:159],a[180:183],a[20:23],v[156:159]\n"
RT"a[138:139],v228 offset:33280\n"
MF"v[164:167],a[180:183],a[24:27],v[164:167]\n"
MF"v[172:175],a[180:183],a[28:31],v[172:175]\n"
RT"a[140:141],v229 offset:32768\n"
MF"v[176:179],a[184:187],a[16:19],v[176:179]\n"
"v_lshrrev_b32 v252,1,v224\n"
MF"v[184:187],a[184:187],a[20:23],v[184:187]\n"
RT"a[142:143],v229 offset:33280\n"
"v_and_b32 v252,56,v252\n"
MF"v[192:195],a[184:187],a[24:27],v[192:195]\n"
"s_lshl_b32 s50,s35,6\n"
MF"v[200:203],a[184:187],a[28:31],v[200:203]\n"
WT"lgkmcnt(3)\n"
RT"a[144:145],v236 offset:36864\n"
SA"s50,s50,2304\n"
MF"v[180:183],a[188:191],a[16:19],v[180:183]\n"
BL"a[196:199],v235,s[8:11],s57 offen offset:0 sc1\n"
MF"v[188:191],a[188:191],a[20:23],v[188:191]\n"
"buffer_load_dwordx2 v[208:209],v252,s[8:11],s50 offen sc1\n"
MF"v[196:199],a[188:191],a[24:27],v[196:199]\n"
RT"a[146:147],v236 offset:37376\n"
MF"v[204:207],a[188:191],a[28:31],v[204:207]\n"
WT"vmcnt(35)\n"
RT"a[148:149],v237 offset:36864\n"
MF"v[80:83],a[128:131],a[32:35],v[80:83]\n"
WT"vmcnt(34)\n"
MF"v[88:91],a[128:131],a[36:39],v[88:91]\n"
WT"vmcnt(33)\n"
RT"a[150:151],v237 offset:37376\n"
MF"v[96:99],a[128:131],a[40:43],v[96:99]\n"
WT"vmcnt(32)\n"
RT"a[152:153],v228 offset:36864\n"
MF"v[104:107],a[128:131],a[44:47],v[104:107]\n"
BL"a[200:203],v235,s[8:11],s58 offen offset:0 sc1\n"
MF"v[84:87],a[132:135],a[32:35],v[84:87]\n"
RT"a[154:155],v228 offset:37376\n"
MF"v[92:95],a[132:135],a[36:39],v[92:95]\n"
MF"v[100:103],a[132:135],a[40:43],v[100:103]\n"
RT"a[156:157],v229 offset:36864\n"
MF"v[108:111],a[132:135],a[44:47],v[108:111]\n"
WT"lgkmcnt(4)\n"
MF"v[112:115],a[136:139],a[32:35],v[112:115]\n"
RT"a[158:159],v229 offset:37376\n"
MF"v[120:123],a[136:139],a[36:39],v[120:123]\n"
BL"a[204:207],v235,s[8:11],s59 offen offset:0 sc1\n"
MF"v[128:131],a[136:139],a[40:43],v[128:131]\n"
RT"a[160:161],v236 offset:40960\n"
MF"v[136:139],a[136:139],a[44:47],v[136:139]\n"
MF"v[116:119],a[140:143],a[32:35],v[116:119]\n"
RT"a[162:163],v236 offset:41472\n"
MF"v[124:127],a[140:143],a[36:39],v[124:127]\n"
BS"v[44:47],v250,s[8:11],s49 offen offset:1024 sc1\n"
MF"v[132:135],a[140:143],a[40:43],v[132:135]\n"
RT"a[164:165],v237 offset:40960\n"
MF"v[140:143],a[140:143],a[44:47],v[140:143]\n"
MF"v[144:147],a[144:147],a[32:35],v[144:147]\n"
RT"a[166:167],v237 offset:41472\n"
MF"v[152:155],a[144:147],a[36:39],v[152:155]\n"
BL"a[208:211],v235,s[8:11],s56 offen offset:1024 sc1\n"
MF"v[160:163],a[144:147],a[40:43],v[160:163]\n"
RT"a[168:169],v228 offset:40960\n"
MF"v[168:171],a[144:147],a[44:47],v[168:171]\n"
WT"lgkmcnt(3)\n"
MF"v[148:151],a[148:151],a[32:35],v[148:151]\n"
RT"a[170:171],v228 offset:41472\n"
MF"v[156:159],a[148:151],a[36:39],v[156:159]\n"
RT"a[172:173],v229 offset:40960\n"
MF"v[164:167],a[148:151],a[40:43],v[164:167]\n"
MF"v[172:175],a[148:151],a[44:47],v[172:175]\n"
RT"a[174:175],v229 offset:41472\n"
MF"v[176:179],a[152:155],a[32:35],v[176:179]\n"
BL"a[212:215],v235,s[8:11],s57 offen offset:1024 sc1\n"
MF"v[184:187],a[152:155],a[36:39],v[184:187]\n"
RT"a[176:177],v236 offset:45056\n"
MF"v[192:195],a[152:155],a[40:43],v[192:195]\n"
MF"v[200:203],a[152:155],a[44:47],v[200:203]\n"
RT"a[178:179],v236 offset:45568\n"
MF"v[180:183],a[156:159],a[32:35],v[180:183]\n"
MF"v[188:191],a[156:159],a[36:39],v[188:191]\n"
RT"a[180:181],v237 offset:45056\n"
MF"v[196:199],a[156:159],a[40:43],v[196:199]\n"
BL"a[216:219],v235,s[8:11],s58 offen offset:1024 sc1\n"
MF"v[204:207],a[156:159],a[44:47],v[204:207]\n"
WT"vmcnt(36)\n"
RT"a[182:183],v237 offset:45568\n"
MF"v[80:83],a[160:163],a[48:51],v[80:83]\n"
WT"vmcnt(35)\n"
MF"v[88:91],a[160:163],a[52:55],v[88:91]\n"
WT"vmcnt(33)\n"
RT"a[184:185],v228 offset:45056\n"
MF"v[96:99],a[160:163],a[56:59],v[96:99]\n"
WT"vmcnt(31)\n"
MF"v[104:107],a[160:163],a[60:63],v[104:107]\n"
WT"lgkmcnt(3)\n"
RT"a[186:187],v228 offset:45568\n"
MF"v[84:87],a[164:167],a[48:51],v[84:87]\n"
MF"v[92:95],a[164:167],a[52:55],v[92:95]\n"
RT"a[188:189],v229 offset:45056\n"
MF"v[100:103],a[164:167],a[56:59],v[100:103]\n"
BL"a[220:223],v235,s[8:11],s59 offen offset:1024 sc1\n"
MF"v[108:111],a[164:167],a[60:63],v[108:111]\n"
RT"a[190:191],v229 offset:45568\n"
MF"v[112:115],a[168:171],a[48:51],v[112:115]\n"
MF"v[120:123],a[168:171],a[52:55],v[120:123]\n"
WT"vmcnt(7)\n"
"v_cmp_eq_u64 vcc,s[28:29],v[208:209]\n"
"s_cmp_eq_u64 vcc,exec\n"
"s_cbranch_scc0 L1010_%=\n"
"L1011_%=:\n"
MF"v[128:131],a[168:171],a[56:59],v[128:131]\n"
MF"v[136:139],a[168:171],a[60:63],v[136:139]\n"
SA"m0,s36,0\n"
SA"s48,s39,0\n"
MF"v[116:119],a[172:175],a[48:51],v[116:119]\n"
SA"s49,s88,270336\n"
BL"a[224:227],v235,s[8:11],s56 offen offset:2048 sc1\n"
MF"v[124:127],a[172:175],a[52:55],v[124:127]\n"
WT"lgkmcnt(0)\n"
"s_barrier\n"
MF"v[132:135],a[172:175],a[56:59],v[132:135]\n"
BL"v225,s[8:11],s48 offen offset:0 sc1 lds\n"
MF"v[140:143],a[172:175],a[60:63],v[140:143]\n"
RT"a[128:129],v246 offset:8192\n"
MF"v[144:147],a[176:179],a[48:51],v[144:147]\n"
BS"v[52:55],v250,s[8:11],s49 offen offset:0 sc1\n"
MF"v[152:155],a[176:179],a[52:55],v[152:155]\n"
RT"a[130:131],v246 offset:8704\n"
MF"v[160:163],a[176:179],a[56:59],v[160:163]\n"
RT"a[132:133],v247 offset:8192\n"
"v_lshlrev_b32 v254,10,v224\n"
MF"v[168:171],a[176:179],a[60:63],v[168:171]\n"
BL"a[228:231],v235,s[8:11],s57 offen offset:2048 sc1\n"
MF"v[148:151],a[180:183],a[48:51],v[148:151]\n"
"s_lshl_b32 s94,s34,1\n"
RT"a[134:135],v247 offset:8704\n"
"v_lshrrev_b32 v255,4,v224\n"
MF"v[156:159],a[180:183],a[52:55],v[156:159]\n"
SA"s95,s94,2\n"
BL"v225,s[8:11],s48 offen offset:1024 sc1 lds\n"
MF"v[164:167],a[180:183],a[56:59],v[164:167]\n"
"s_sub_u32 s94,32,s94\n"
RT"a[136:137],v248 offset:8192\n"
MF"v[172:175],a[180:183],a[60:63],v[172:175]\n"
"v_add_u32 v228,s39,v254\n"
MF"v[176:179],a[184:187],a[48:51],v[176:179]\n"
RT"a[138:139],v248 offset:8704\n"
"v_and_b32 v254,1,v255\n"
MF"v[184:187],a[184:187],a[52:55],v[184:187]\n"
"v_mul_u32_u24 v254,491520,v254\n"
MF"v[192:195],a[184:187],a[56:59],v[192:195]\n"
RT"a[140:141],v249 offset:8192\n"
"v_cmp_gt_u32 vcc,s95,v255\n"
MF"v[200:203],a[184:187],a[60:63],v[200:203]\n"
BL"a[232:235],v235,s[8:11],s58 offen offset:2048 sc1\n"
MF"v[180:183],a[188:191],a[48:51],v[180:183]\n"
RT"a[142:143],v249 offset:8704\n"
"v_mov_b32 v229,16384\n"
MF"v[188:191],a[188:191],a[52:55],v[188:191]\n"
BL"v225,s[8:11],s48 offen offset:2048 sc1 lds\n"
MF"v[196:199],a[188:191],a[56:59],v[196:199]\n"
RT"a[144:145],v246 offset:12288\n"
"v_mov_b32 v253,516096\n"
MF"v[204:207],a[188:191],a[60:63],v[204:207]\n"
WT"lgkmcnt(3)\n"
"v_cndmask_b32_e64 v254,v254,0,vcc\n"
"v_cndmask_b32 v229,v229,v253,vcc\n"
MF"v[80:83],a[128:131],a[64:67],v[80:83]\n"
RT"a[146:147],v246 offset:12800\n"
"v_cmp_eq_u32 vcc,s94,v255\n"
MF"v[88:91],a[128:131],a[68:71],v[88:91]\n"
BL"a[236:239],v235,s[8:11],s59 offen offset:2048 sc1\n"
MF"v[96:99],a[128:131],a[72:75],v[96:99]\n"
RT"a[148:149],v247 offset:12288\n"
"v_mov_b32 v253,507904\n"
MF"v[104:107],a[128:131],a[76:79],v[104:107]\n"
"v_cndmask_b32 v254,v254,v253,vcc\n"
"v_mov_b32 v253,1862270976\n"
MF"v[84:87],a[132:135],a[64:67],v[84:87]\n"
RT"a[150:151],v247 offset:12800\n"
"v_cndmask_b32 v229,v229,v253,vcc\n"
MF"v[92:95],a[132:135],a[68:71],v[92:95]\n"
"s_mul_i32 s95,s35,163840\n"
RT"a[152:153],v248 offset:12288\n"
"v_add_u32 v228,v228,v254\n"
MF"v[100:103],a[132:135],a[72:75],v[100:103]\n"
SA"s95,s95,3395584\n"
BL"v225,s[8:11],s48 offen offset:3072 sc1 lds\n"
MF"v[108:111],a[132:135],a[76:79],v[108:111]\n"
RT"a[154:155],v248 offset:12800\n"
"v_subrev_u32 v254,s94,v255\n"
MF"v[112:115],a[136:139],a[64:67],v[112:115]\n"
BL"a[240:243],v235,s[8:11],s56 offen offset:3072 sc1\n"
MF"v[120:123],a[136:139],a[68:71],v[120:123]\n"
RT"a[156:157],v249 offset:12288\n"
"v_lshlrev_b32 v254,15,v254\n"
MF"v[128:131],a[136:139],a[72:75],v[128:131]\n"
"v_add_u32 v254,s95,v254\n"
"v_cmp_lt_u32 vcc,s94,v255\n"
MF"v[136:139],a[136:139],a[76:79],v[136:139]\n"
WT"lgkmcnt(3)\n"
RT"a[158:159],v249 offset:12800\n"
"v_cndmask_b32 v228,v228,v254,vcc\n"
MF"v[116:119],a[140:143],a[64:67],v[116:119]\n"
SA"m0,s36,16384\n"
"v_add_u32 v229,v229,v228\n"
SA"s48,s39,516096\n"
MF"v[124:127],a[140:143],a[68:71],v[124:127]\n"
"s_mov_b32 s47,s36\n"
RT"a[160:161],v246 offset:32768\n"
MF"v[132:135],a[140:143],a[72:75],v[132:135]\n"
SA"s46,s36,163840\n"
BL"v225,s[8:11],s48 offen offset:0 sc1 lds\n"
MF"v[140:143],a[140:143],a[76:79],v[140:143]\n"
SA"s40,s36,65536\n"
RT"a[162:163],v246 offset:33280\n"
MF"v[144:147],a[144:147],a[64:67],v[144:147]\n"
"s_mov_b32 s41,0\n"
BL"a[244:247],v235,s[8:11],s57 offen offset:3072 sc1\n"
MF"v[152:155],a[144:147],a[68:71],v[152:155]\n"
"s_mov_b32 s42,2\n"
BS"v[60:63],v250,s[8:11],s49 offen offset:1024 sc1\n"
MF"v[160:163],a[144:147],a[72:75],v[160:163]\n"
RT"a[164:165],v247 offset:32768\n"
"v_mov_b32 v226,v224\n"
MF"v[168:171],a[144:147],a[76:79],v[168:171]\n"
RT"a[166:167],v247 offset:33280\n"
"v_bfe_u32 v254,v224,4,4\n"
MF"v[148:151],a[148:151],a[64:67],v[148:151]\n"
"v_lshrrev_b32 v255,8,v224\n"
"v_lshlrev_b32 v255,2,v255\n"
MF"v[156:159],a[148:151],a[68:71],v[156:159]\n"
RT"a[168:169],v248 offset:32768\n"
"v_sub_u32 v254,v254,v255\n"
MF"v[164:167],a[148:151],a[72:75],v[164:167]\n"
"s_lshl_b32 s94,s30,4\n"
BL"v225,s[8:11],s48 offen offset:1024 sc1 lds\n"
MF"v[172:175],a[148:151],a[76:79],v[172:175]\n"
WT"lgkmcnt(3)\n"
BL"a[248:251],v235,s[8:11],s58 offen offset:3072 sc1\n"
MF"v[176:179],a[152:155],a[64:67],v[176:179]\n"
RT"a[170:171],v248 offset:33280\n"
"v_add_u32 v254,s94,v254\n"
MF"v[184:187],a[152:155],a[68:71],v[184:187]\n"
RT"a[172:173],v249 offset:32768\n"
"v_mov_b32 v255,0xff800000\n"
MF"v[192:195],a[152:155],a[72:75],v[192:195]\n"
RT"a[174:175],v249 offset:33280\n"
"v_cmp_gt_i32_e64 vcc,0,v254\n"
MF"v[200:203],a[152:155],a[76:79],v[200:203]\n"
"v_cmp_gt_i32_e64 s[52:53],1,v254\n"
"v_cndmask_b32_e64 v208,0,v255,vcc\n"
MF"v[180:183],a[156:159],a[64:67],v[180:183]\n"
RT"a[176:177],v246 offset:36864\n"
"v_cndmask_b32_e64 v209,0,v255,s[52:53]\n"
MF"v[188:191],a[156:159],a[68:71],v[188:191]\n"
BL"a[252:255],v235,s[8:11],s59 offen offset:3072 sc1\n"
MF"v[196:199],a[156:159],a[72:75],v[196:199]\n"
BL"v225,s[8:11],s48 offen offset:2048 sc1 lds\n"
MF"v[204:207],a[156:159],a[76:79],v[204:207]\n"
RT"a[178:179],v246 offset:37376\n"
"v_cmp_gt_i32_e64 s[62:63],2,v254\n"
MF"v[80:83],a[160:163],a[80:83],v[80:83]\n"
RT"a[180:181],v247 offset:36864\n"
"v_cndmask_b32_e64 v210,0,v255,s[62:63]\n"
MF"v[88:91],a[160:163],a[84:87],v[88:91]\n"
"v_cmp_gt_i32_e64 vcc,3,v254\n"
"v_cmp_gt_i32_e64 s[52:53],16,v254\n"
MF"v[96:99],a[160:163],a[88:91],v[96:99]\n"
RT"a[182:183],v247 offset:37376\n"
"v_cndmask_b32_e64 v211,0,v255,vcc\n"
MF"v[104:107],a[160:163],a[92:95],v[104:107]\n"
WT"lgkmcnt(3)\n"
"v_cndmask_b32_e64 v212,0,v255,s[52:53]\n"
"v_cmp_gt_i32_e64 s[62:63],17,v254\n"
MF"v[84:87],a[164:167],a[80:83],v[84:87]\n"
RT"a[184:185],v248 offset:36864\n"
"v_cndmask_b32_e64 v213,0,v255,s[62:63]\n"
MF"v[92:95],a[164:167],a[84:87],v[92:95]\n"
BL"v225,s[8:11],s48 offen offset:3072 sc1 lds\n"
MF"v[100:103],a[164:167],a[88:91],v[100:103]\n"
RT"a[186:187],v248 offset:37376\n"
"v_cmp_gt_i32_e64 vcc,18,v254\n"
MF"v[108:111],a[164:167],a[92:95],v[108:111]\n"
"v_cmp_gt_i32_e64 s[52:53],19,v254\n"
"v_cndmask_b32_e64 v214,0,v255,vcc\n"
MF"v[112:115],a[168:171],a[80:83],v[112:115]\n"
RT"a[188:189],v249 offset:36864\n"
"v_cndmask_b32_e64 v215,0,v255,s[52:53]\n"
MF"v[120:123],a[168:171],a[84:87],v[120:123]\n"
"v_cmp_gt_i32_e64 s[62:63],32,v254\n"
"v_cmp_gt_i32_e64 vcc,33,v254\n"
MF"v[128:131],a[168:171],a[88:91],v[128:131]\n"
RT"a[190:191],v249 offset:37376\n"
"v_cndmask_b32_e64 v216,0,v255,s[62:63]\n"
MF"v[136:139],a[168:171],a[92:95],v[136:139]\n"
SA"m0,s36,32768\n"
RT"a[128:129],v242 offset:8192\n"
"v_cndmask_b32_e64 v217,0,v255,vcc\n"
MF"v[116:119],a[172:175],a[80:83],v[116:119]\n"
SA"s48,s39,16384\n"
"v_cmp_gt_i32_e64 s[52:53],34,v254\n"
"v_cmp_gt_i32_e64 s[62:63],35,v254\n"
MF"v[124:127],a[172:175],a[84:87],v[124:127]\n"
RT"a[130:131],v242 offset:8704\n"
"v_cndmask_b32_e64 v218,0,v255,s[52:53]\n"
MF"v[132:135],a[172:175],a[88:91],v[132:135]\n"
BL"v225,s[8:11],s48 offen offset:0 sc1 lds\n"
MF"v[140:143],a[172:175],a[92:95],v[140:143]\n"
WT"lgkmcnt(3)\n"
RT"a[132:133],v243 offset:8192\n"
"v_cndmask_b32_e64 v219,0,v255,s[62:63]\n"
MF"v[144:147],a[176:179],a[80:83],v[144:147]\n"
"s_mov_b32 s49,278528\n"
"v_cmp_gt_i32_e64 vcc,48,v254\n"
"v_cmp_gt_i32_e64 s[52:53],49,v254\n"
MF"v[152:155],a[176:179],a[84:87],v[152:155]\n"
RT"a[134:135],v243 offset:8704\n"
"v_cndmask_b32_e64 v220,0,v255,vcc\n"
MF"v[160:163],a[176:179],a[88:91],v[160:163]\n"
BS"v[64:67],v250,s[8:11],s49 offen offset:0 sc1\n"
MF"v[168:171],a[176:179],a[92:95],v[168:171]\n"
RT"a[136:137],v244 offset:8192\n"
"v_cndmask_b32_e64 v221,0,v255,s[52:53]\n"
MF"v[148:151],a[180:183],a[80:83],v[148:151]\n"
"s_lshl_b32 s59,s34,5\n"
"v_cmp_gt_i32_e64 s[62:63],50,v254\n"
"v_cmp_gt_i32_e64 vcc,51,v254\n"
MF"v[156:159],a[180:183],a[84:87],v[156:159]\n"
"s_sub_u32 s60,480,s59\n"
RT"a[138:139],v244 offset:8704\n"
"v_cndmask_b32_e64 v222,0,v255,s[62:63]\n"
MF"v[164:167],a[180:183],a[88:91],v[164:167]\n"
SA"s59,s59,s35\n"
BL"v225,s[8:11],s48 offen offset:1024 sc1 lds\n"
MF"v[172:175],a[180:183],a[92:95],v[172:175]\n"
SA"s60,s60,s35\n"
RT"a[140:141],v245 offset:8192\n"
"v_cndmask_b32_e64 v223,0,v255,vcc\n"
MF"v[176:179],a[184:187],a[80:83],v[176:179]\n"
"s_lshl_b32 s59,s59,15\n"
"s_lshl_b32 s60,s60,15\n"
SA"s50,s36,42729472\n"
MF"v[184:187],a[184:187],a[84:87],v[184:187]\n"
SA"s59,s59,s50\n"
RT"a[142:143],v245 offset:8704\n"
SA"s60,s60,s50\n"
MF"v[192:195],a[184:187],a[88:91],v[192:195]\n"
"s_lshl_b32 s61,s34,3\n"
MF"v[200:203],a[184:187],a[92:95],v[200:203]\n"
WT"lgkmcnt(3)\n"
RT"a[144:145],v242 offset:12288\n"
SA"s61,s61,s32\n"
MF"v[180:183],a[188:191],a[80:83],v[180:183]\n"
"s_lshl_b32 s61,s61,2\n"
SA"s61,s61,s30\n"
MF"v[188:191],a[188:191],a[84:87],v[188:191]\n"
"s_lshl_b32 s61,s61,2\n"
RT"a[146:147],v242 offset:12800\n"
SA"s61,s61,s33\n"
MF"v[196:199],a[188:191],a[88:91],v[196:199]\n"
"s_lshl_b32 s61,s61,6\n"
BL"v225,s[8:11],s48 offen offset:2048 sc1 lds\n"
MF"v[204:207],a[188:191],a[92:95],v[204:207]\n"
SA"s61,s61,81002496\n"
RT"a[148:149],v243 offset:12288\n"
"s_lshl_b32 s98,s35,3\n"
MF"v[80:83],a[128:131],a[96:99],v[80:83]\n"
SA"s98,s98,s34\n"
MF"v[88:91],a[128:131],a[100:103],v[88:91]\n"
"s_lshl_b32 s98,s98,3\n"
RT"a[150:151],v243 offset:12800\n"
MF"v[96:99],a[128:131],a[104:107],v[96:99]\n"
SA"s98,s98,4352\n"
RT"a[152:153],v244 offset:12288\n"
SA"s98,s98,s45\n"
MF"v[104:107],a[128:131],a[108:111],v[104:107]\n"
"s_lshl_b32 s50,s34,2\n"
MF"v[84:87],a[132:135],a[96:99],v[84:87]\n"
"s_mul_i32 s51,s33,0x1011100\n"
RT"a[154:155],v244 offset:12800\n"
SA"s51,s51,0xf48c00\n"
MF"v[92:95],a[132:135],a[100:103],v[92:95]\n"
"s_lshr_b32 s51,s51,s50\n"
BL"v225,s[8:11],s48 offen offset:3072 sc1 lds\n"
MF"v[100:103],a[132:135],a[104:107],v[100:103]\n"
"s_and_b32 s51,s51,15\n"
RT"a[156:157],v245 offset:12288\n"
"s_lshr_b32 s50,s31,3\n"
MF"v[108:111],a[132:135],a[108:111],v[108:111]\n"
WT"lgkmcnt(4)\n"
"s_cmp_eq_u32 s50,6\n"
"s_cselect_b32 s51,15,s51\n"
MF"v[112:115],a[136:139],a[96:99],v[112:115]\n"
"s_cmp_eq_u32 s50,26\n"
RT"a[158:159],v245 offset:12800\n"
"s_cselect_b32 s51,0,s51\n"
MF"v[120:123],a[136:139],a[100:103],v[120:123]\n"
"s_mul_i32 s58,s32,14\n"
SA"s58,s58,s51\n"
MF"v[128:131],a[136:139],a[104:107],v[128:131]\n"
"s_sub_u32 s58,s58,1\n"
RT"a[160:161],v242 offset:32768\n"
MF"v[136:139],a[136:139],a[108:111],v[136:139]\n"
SA"m0,s36,49152\n"
SA"s48,s39,532480\n"
"s_cmp_lt_u32 s58,108\n"
MF"v[116:119],a[140:143],a[96:99],v[116:119]\n"
"s_cselect_b32 s90,s51,0\n"
RT"a[162:163],v242 offset:33280\n"
"s_cmp_lg_u32 s90,0\n"
MF"v[124:127],a[140:143],a[100:103],v[124:127]\n"
"s_cselect_b32 s57,1,0\n"
BL"v225,s[8:11],s48 offen offset:0 sc1 lds\n"
MF"v[132:135],a[140:143],a[104:107],v[132:135]\n"
"s_cmp_eq_u32 s51,15\n"
RT"a[164:165],v243 offset:32768\n"
"s_cselect_b32 s57,2,s57\n"
MF"v[140:143],a[140:143],a[108:111],v[140:143]\n"
"s_cmp_eq_u32 s50,6\n"
"s_cselect_b32 s90,4,s33\n"
"s_mul_i32 s50,s32,5\n"
MF"v[144:147],a[144:147],a[96:99],v[144:147]\n"
SA"s90,s90,s50\n"
RT"a[166:167],v243 offset:33280\n"
"s_cmp_eq_u32 s57,2\n"
MF"v[152:155],a[144:147],a[100:103],v[152:155]\n"
"s_cselect_b32 s58,s90,s58\n"
"s_lshl_b32 s56,s34,1\n"
"s_sub_u32 s56,s56,2\n"
MF"v[160:163],a[144:147],a[104:107],v[160:163]\n"
"s_cmp_eq_u32 s34,7\n"
RT"a[168:169],v244 offset:32768\n"
"s_cselect_b32 s56,3,s56\n"
MF"v[168:171],a[144:147],a[108:111],v[168:171]\n"
WT"lgkmcnt(3)\n"
"s_cmp_eq_u32 s57,2\n"
"s_cselect_b32 s56,5,s56\n"
MF"v[148:151],a[148:151],a[96:99],v[148:151]\n"
"s_cmp_lt_u32 s30,2\n"
RT"a[170:171],v244 offset:33280\n"
"s_cselect_b64 s[24:25],s[78:79],s[80:81]\n"
MF"v[156:159],a[148:151],a[100:103],v[156:159]\n"
"s_cmp_eq_u32 s57,2\n"
BS"v[68:71],v250,s[8:11],s49 offen offset:1024 sc1\n"
MF"v[164:167],a[148:151],a[104:107],v[164:167]\n"
"s_cselect_b64 s[24:25],s[82:83],s[24:25]\n"
RT"a[172:173],v245 offset:32768\n"
"s_and_b32 s50,s30,1\n"
MF"v[172:175],a[148:151],a[108:111],v[172:175]\n"
"s_mul_i32 s50,s50,10\n"
BL"v225,s[8:11],s48 offen offset:1024 sc1 lds\n"
MF"v[176:179],a[152:155],a[96:99],v[176:179]\n"
"s_mul_i32 s51,s30,14\n"
RT"a[174:175],v245 offset:33280\n"
"s_min_u32 s51,s51,41\n"
MF"v[184:187],a[152:155],a[100:103],v[184:187]\n"
"s_cmp_eq_u32 s57,2\n"
RT"a[176:177],v242 offset:36864\n"
"s_cselect_b32 s50,s51,s50\n"
MF"v[192:195],a[152:155],a[104:107],v[192:195]\n"
"s_cmp_lt_u32 s30,2\n"
"s_cselect_b32 s51,14,13\n"
"s_cmp_lt_u32 s58,108\n"
MF"v[200:203],a[152:155],a[108:111],v[200:203]\n"
"s_cselect_b32 s92,10,0\n"
RT"a[178:179],v242 offset:37376\n"
"s_cmp_eq_u32 s57,2\n"
MF"v[180:183],a[156:159],a[96:99],v[180:183]\n"
"s_cselect_b32 s92,s51,s92\n"
"s_mov_b32 s51,3456\n"
"s_cmp_eq_u32 s57,2\n"
MF"v[188:191],a[156:159],a[100:103],v[188:191]\n"
"s_cselect_b32 s51,1280,s51\n"
RT"a[180:181],v243 offset:36864\n"
"s_lshl_b32 s62,s51,5\n"
MF"v[196:199],a[156:159],a[104:107],v[196:199]\n"
"s_mul_i32 s26,s62,s92\n"
BL"v225,s[8:11],s48 offen offset:2048 sc1 lds\n"
MF"v[204:207],a[156:159],a[108:111],v[204:207]\n"
"s_mul_i32 s90,s62,s50\n"
RT"a[182:183],v243 offset:37376\n"
"s_lshl_b32 s91,s58,5\n"
MF"v[80:83],a[160:163],a[112:115],v[80:83]\n"
SA"s90,s90,s91\n"
SA"s24,s24,s90\n"
"s_addc_u32 s25,s25,0\n"
MF"v[88:91],a[160:163],a[116:119],v[88:91]\n"
"s_and_b32 s25,s25,0xffff\n"
RT"a[184:185],v244 offset:36864\n"
"s_sub_u32 s90,s51,32\n"
MF"v[96:99],a[160:163],a[120:123],v[96:99]\n"
"s_sub_u32 s26,s26,s90\n"
"v_lshrrev_b32 v235,5,v224\n"
"v_mul_u32_u24 v235,s51,v235\n"
MF"v[104:107],a[160:163],a[124:127],v[104:107]\n"
WT"lgkmcnt(3)\n"
RT"a[186:187],v244 offset:37376\n"
"v_and_b32 v239,16,v224\n"
MF"v[84:87],a[164:167],a[112:115],v[84:87]\n"
"s_cselect_b32 s26,0,s26\n"
"v_add_u32 v235,v235,v239\n"
"s_mov_b32 s27,0x20000\n"
MF"v[92:95],a[164:167],a[116:119],v[92:95]\n"
"s_mul_i32 s63,s35,20480\n"
RT"a[188:189],v245 offset:36864\n"
SA"s63,s63,2770944\n"
MF"v[100:103],a[164:167],a[120:123],v[100:103]\n"
"s_cmp_eq_u32 s57,2\n"
BL"v225,s[8:11],s48 offen offset:3072 sc1 lds\n"
MF"v[108:111],a[164:167],a[124:127],v[108:111]\n"
"s_cselect_b32 s63,2695168,s63\n"
RT"a[190:191],v245 offset:37376\n"
"s_lshl_b32 s50,s50,10\n"
MF"v[112:115],a[168:171],a[112:115],v[112:115]\n"
SA"s63,s63,s50\n"
"s_mov_b32 s50,1065353216\n"
MF"v[120:123],a[168:171],a[116:119],v[120:123]\n"
"s_cmp_lg_u32 s92,0\n"
"s_cselect_b32 s50,s45,s50\n"
"s_mov_b32 s93,2695168\n"
MF"v[128:131],a[168:171],a[120:123],v[128:131]\n"
"s_cmp_eq_u32 s57,2\n"
"s_cselect_b32 s93,2750464,s93\n"
MF"v[136:139],a[168:171],a[124:127],v[136:139]\n"
"s_lshl_b32 s51,s58,9\n"
MF"v[116:119],a[172:175],a[112:115],v[116:119]\n"
SA"s93,s93,s51\n"
SA"s93,s93,s50\n"
MF"v[124:127],a[172:175],a[116:119],v[124:127]\n"
"s_mov_b32 s94,6912\n"
"s_barrier\n"
MF"v[132:135],a[172:175],a[120:123],v[132:135]\n"
"s_cmp_eq_u32 s57,2\n"
RT"a[128:129],v246 offset:16384\n"
"s_cselect_b32 s94,6400,s94\n"
MF"v[140:143],a[172:175],a[124:127],v[140:143]\n"
"s_lshl_b32 s51,s58,3\n"
SA"s94,s94,s51\n"
MF"v[144:147],a[176:179],a[112:115],v[144:147]\n"
SA"s94,s94,s50\n"
RT"a[130:131],v246 offset:16896\n"
"s_mov_b32 s52,0\n"
MF"v[152:155],a[176:179],a[116:119],v[152:155]\n"
"s_lshl_b32 s50,s35,6\n"
RT"a[132:133],v247 offset:16384\n"
MF"v[160:163],a[176:179],a[120:123],v[160:163]\n"
SA"s50,s50,2304\n"
MF"v[168:171],a[176:179],a[124:127],v[168:171]\n"
WT"lgkmcnt(3)\n"
RT"a[134:135],v247 offset:16896\n"
"s_cmp_eq_u32 s57,2\n"
MF"v[148:151],a[180:183],a[112:115],v[148:151]\n"
"s_cselect_b32 s50,6912,s50\n"
RT"a[136:137],v246 offset:40960\n"
"s_mov_b32 s51,48\n"
MF"v[156:159],a[180:183],a[116:119],v[156:159]\n"
SA"s49,s88,278528\n"
"s_cmp_eq_u32 s57,2\n"
"s_cselect_b32 s51,848,s51\n"
MF"v[164:167],a[180:183],a[120:123],v[164:167]\n"
RT"a[138:139],v246 offset:41472\n"
"v_min_u32 v238,s51,v224\n"
MF"v[172:175],a[180:183],a[124:127],v[172:175]\n"
"s_mov_b32 s16,0\n"
BS"v[72:75],v250,s[8:11],s49 offen offset:0 sc1\n"
MF"v[176:179],a[184:187],a[112:115],v[176:179]\n"
"s_mov_b32 s20,8\n"
RT"a[140:141],v247 offset:40960\n"
"v_add_u32 v238,s50,v238\n"
MF"v[184:187],a[184:187],a[116:119],v[184:187]\n"
"s_mov_b32 s18,2130706432\n"
RT"a[142:143],v247 offset:41472\n"
"v_and_b32 v239,0x80,v224\n"
MF"v[192:195],a[184:187],a[120:123],v[192:195]\n"
"s_lshl_b32 s21,s32,4\n"
"v_lshl_add_u32 v236,v239,23,v224\n"
"s_lshr_b32 s22,s30,1\n"
MF"v[200:203],a[184:187],a[124:127],v[200:203]\n"
SA"s21,s21,s22\n"
RT"a[144:145],v242 offset:16384\n"
MF"v[180:183],a[188:191],a[112:115],v[180:183]\n"
"s_mul_i32 s21,s21,40960\n"
"s_lshl_b32 s22,s33,1\n"
MF"v[188:191],a[188:191],a[116:119],v[188:191]\n"
"s_and_b32 s23,s30,1\n"
RT"a[146:147],v242 offset:16896\n"
MF"v[196:199],a[188:191],a[120:123],v[196:199]\n"
SA"s22,s22,s23\n"
RT"a[148:149],v243 offset:16384\n"
"s_mul_i32 s22,s22,160\n"
MF"v[204:207],a[188:191],a[124:127],v[204:207]\n"
WT"lgkmcnt(4)\n"
SA"s17,s21,s22\n"
MF"v[80:83],a[128:131],a[192:195],v[80:83]\n"
RT"a[150:151],v243 offset:16896\n"
"v_lshrrev_b32 v240,4,v224\n"
MF"v[88:91],a[128:131],a[196:199],v[88:91]\n"
WT"vmcnt(35)\n"
RT"a[152:153],v242 offset:40960\n"
"v_add_u32 v240,0,v240\n"
MF"v[96:99],a[128:131],a[200:203],v[96:99]\n"
WT"vmcnt(34)\n"
"v_mul_u32_u24 v240,205,v240\n"
"v_lshrrev_b32 v240,11,v240\n"
MF"v[104:107],a[128:131],a[204:207],v[104:107]\n"
RT"a[154:155],v242 offset:41472\n"
"v_mul_u32_u24 v240,1120,v240\n"
MF"v[84:87],a[132:135],a[192:195],v[84:87]\n"
RT"a[156:157],v243 offset:40960\n"
"v_add_u32 v240,v240,v224\n"
MF"v[92:95],a[132:135],a[196:199],v[92:95]\n"
"v_lshrrev_b32 v241,4,v224\n"
"v_add_u32 v241,64,v241\n"
MF"v[100:103],a[132:135],a[200:203],v[100:103]\n"
RT"a[158:159],v243 offset:41472\n"
"v_mul_u32_u24 v241,205,v241\n"
MF"v[108:111],a[132:135],a[204:207],v[108:111]\n"
WT"vmcnt(32)\n"
"v_lshrrev_b32 v241,11,v241\n"
"v_mul_u32_u24 v241,1120,v241\n"
MF"v[80:83],a[136:139],a[208:211],v[80:83]\n"
WT"vmcnt(31)\n"
RT"a[160:161],v248 offset:16384\n"
"v_add_u32 v241,v241,v224\n"
MF"v[88:91],a[136:139],a[212:215],v[88:91]\n"
WT"vmcnt(30)\n"
"v_lshrrev_b32 v251,4,v224\n"
"v_add_u32 v251,128,v251\n"
MF"v[96:99],a[136:139],a[216:219],v[96:99]\n"
WT"vmcnt(29)\n"
RT"a[162:163],v248 offset:16896\n"
"v_mul_u32_u24 v251,205,v251\n"
MF"v[104:107],a[136:139],a[220:223],v[104:107]\n"
WT"lgkmcnt(3)\n"
RT"a[164:165],v249 offset:16384\n"
"v_lshrrev_b32 v251,11,v251\n"
MF"v[84:87],a[140:143],a[208:211],v[84:87]\n"
"v_mul_u32_u24 v251,1120,v251\n"
"v_add_u32 v251,v251,v224\n"
MF"v[92:95],a[140:143],a[212:215],v[92:95]\n"
RT"a[166:167],v249 offset:16896\n"
"v_and_b32 v237,512,v224\n"
MF"v[100:103],a[140:143],a[216:219],v[100:103]\n"
RT"a[168:169],v248 offset:40960\n"
"v_lshl_add_u32 v251,v237,20,v251\n"
MF"v[108:111],a[140:143],a[220:223],v[108:111]\n"
WT"vmcnt(28)\n"
"v_lshrrev_b32 v252,6,v224\n"
"v_mul_u32_u24 v252,160,v252\n"
MF"v[80:83],a[144:147],a[224:227],v[80:83]\n"
WT"vmcnt(25)\n"
RT"a[170:171],v248 offset:41472\n"
"v_bfe_u32 v237,v224,4,2\n"
MF"v[88:91],a[144:147],a[228:231],v[88:91]\n"
WT"vmcnt(23)\n"
RT"a[172:173],v249 offset:40960\n"
"v_lshl_add_u32 v252,v237,3,v252\n"
MF"v[96:99],a[144:147],a[232:235],v[96:99]\n"
WT"vmcnt(21)\n"
MF"v[104:107],a[144:147],a[236:239],v[104:107]\n"
RT"a[174:175],v249 offset:41472\n"
MF"v[84:87],a[148:151],a[224:227],v[84:87]\n"
MF"v[92:95],a[148:151],a[228:231],v[92:95]\n"
RT"a[176:177],v244 offset:16384\n"
MF"v[100:103],a[148:151],a[232:235],v[100:103]\n"
MF"v[108:111],a[148:151],a[236:239],v[108:111]\n"
WT"vmcnt(19)\n"
RT"a[178:179],v244 offset:16896\n"
MF"v[80:83],a[152:155],a[240:243],v[80:83]\n"
WT"vmcnt(17)\n"
RT"a[180:181],v245 offset:16384\n"
"v_lshrrev_b32 v253,4,v224\n"
MF"v[88:91],a[152:155],a[244:247],v[88:91]\n"
WT"vmcnt(14)\n"
BS"v[76:79],v250,s[8:11],s49 offen offset:1024 sc1\n"
MF"v[96:99],a[152:155],a[248:251],v[96:99]\n"
WT"vmcnt(14)\n"
RT"a[182:183],v245 offset:16896\n"
"v_min_u32 v253,49,v253\n"
MF"v[104:107],a[152:155],a[252:255],v[104:107]\n"
WT"lgkmcnt(4)\n"
RT"a[184:185],v244 offset:40960\n"
"v_mul_u32_u24 v254,52,v253\n"
MF"v[84:87],a[156:159],a[240:243],v[84:87]\n"
"v_lshrrev_b32 v254,8,v254\n"
"v_mul_u32_u24 v254,2901,v254\n"
MF"v[92:95],a[156:159],a[244:247],v[92:95]\n"
RT"a[186:187],v244 offset:41472\n"
"v_mul_u32_u24 v253,111,v253\n"
MF"v[100:103],a[156:159],a[248:251],v[100:103]\n"
RT"a[188:189],v245 offset:40960\n"
"v_add_u32 v253,v253,v254\n"
MF"v[108:111],a[156:159],a[252:255],v[108:111]\n"
"v_and_b32 v253,-4,v253\n"
"v_lshrrev_b32 v254,5,v224\n"
MF"v[112:115],a[160:163],a[192:195],v[112:115]\n"
RT"a[190:191],v245 offset:41472\n"
"v_min_u32 v254,13,v254\n"
MF"v[120:123],a[160:163],a[196:199],v[120:123]\n"
"v_mul_u32_u24 v254,1280,v254\n"
"v_bfe_u32 v237,v224,4,1\n"
MF"v[128:131],a[160:163],a[200:203],v[128:131]\n"
RT"a[128:129],v246 offset:20480\n"
"v_mul_u32_u24 v237,156,v237\n"
MF"v[136:139],a[160:163],a[204:207],v[136:139]\n"
"s_cmp_lt_u32 s30,2\n"
"v_add_u32 v254,v254,v237\n"
PK"v80,v80,v81\n"
MF"v[116:119],a[164:167],a[192:195],v[116:119]\n"
"s_cselect_b64 s[12:13],s[78:79],s[80:81]\n"
RT"a[130:131],v246 offset:20992\n"
PK"v81,v82,v83\n"
MF"v[124:127],a[164:167],a[196:199],v[124:127]\n"
"s_and_b32 s13,s13,0xffff\n"
RT"a[132:133],v247 offset:20480\n"
PK"v82,v84,v85\n"
MF"v[132:135],a[164:167],a[200:203],v[132:135]\n"
"s_mov_b32 s14,2211840\n"
PK"v83,v86,v87\n"
"v_accvgpr_write_b32 a0,v80\n"
MF"v[140:143],a[164:167],a[204:207],v[140:143]\n"
"s_mov_b32 s15,0x20000\n"
RT"a[134:135],v247 offset:20992\n"
"v_accvgpr_write_b32 a1,v81\n"
MF"v[112:115],a[168:171],a[208:211],v[112:115]\n"
"s_lshr_b32 s90,s31,3\n"
RT"a[136:137],v246 offset:45056\n"
"v_accvgpr_write_b32 a2,v82\n"
MF"v[120:123],a[168:171],a[212:215],v[120:123]\n"
"s_lshl_b32 s91,s90,1\n"
"v_accvgpr_write_b32 a3,v83\n"
PK"v88,v88,v89\n"
MF"v[128:131],a[168:171],a[216:219],v[128:131]\n"
"s_and_b32 s50,s30,1\n"
RT"a[138:139],v246 offset:45568\n"
PK"v89,v90,v91\n"
MF"v[136:139],a[168:171],a[220:223],v[136:139]\n"
SA"s91,s91,s50\n"
RT"a[140:141],v247 offset:45056\n"
PK"v90,v92,v93\n"
MF"v[116:119],a[172:175],a[208:211],v[116:119]\n"
"s_mul_i32 s91,s91,34560\n"
PK"v91,v94,v95\n"
"v_accvgpr_write_b32 a16,v88\n"
MF"v[124:127],a[172:175],a[212:215],v[124:127]\n"
"s_mul_i32 s50,s32,448\n"
RT"a[142:143],v247 offset:45568\n"
"v_accvgpr_write_b32 a17,v89\n"
MF"v[132:135],a[172:175],a[216:219],v[132:135]\n"
SA"s91,s91,s50\n"
"v_accvgpr_write_b32 a18,v90\n"
"v_accvgpr_write_b32 a19,v91\n"
MF"v[140:143],a[172:175],a[220:223],v[140:143]\n"
WT"lgkmcnt(3)\n"
"buffer_load_dword v255,v253,s[12:15],s91 offen\n"
MF"v[112:115],a[176:179],a[224:227],v[112:115]\n"
"s_mov_b32 s12,s82\n"
RT"a[144:145],v242 offset:20480\n"
PK"v96,v96,v97\n"
MF"v[120:123],a[176:179],a[228:231],v[120:123]\n"
"s_and_b32 s13,s83,0xffff\n"
RT"a[146:147],v242 offset:20992\n"
PK"v97,v98,v99\n"
MF"v[128:131],a[176:179],a[232:235],v[128:131]\n"
"s_lshl_b32 s90,s90,2\n"
RT"a[148:149],v243 offset:20480\n"
PK"v98,v100,v101\n"
MF"v[136:139],a[176:179],a[236:239],v[136:139]\n"
SA"s90,s90,s30\n"
PK"v99,v102,v103\n"
"v_accvgpr_write_b32 a32,v96\n"
MF"v[116:119],a[180:183],a[224:227],v[116:119]\n"
"s_mul_i32 s90,s90,17920\n"
RT"a[150:151],v243 offset:20992\n"
"v_accvgpr_write_b32 a33,v97\n"
MF"v[124:127],a[180:183],a[228:231],v[124:127]\n"
"s_mul_i32 s50,s32,160\n"
RT"a[152:153],v242 offset:45056\n"
"v_accvgpr_write_b32 a34,v98\n"
MF"v[132:135],a[180:183],a[232:235],v[132:135]\n"
SA"s90,s90,s50\n"
"v_accvgpr_write_b32 a35,v99\n"
PK"v104,v104,v105\n"
MF"v[140:143],a[180:183],a[236:239],v[140:143]\n"
"s_lshl_b32 s91,s32,5\n"
"buffer_load_dword v255,v254,s[12:15],s90 offen\n"
MF"v[112:115],a[184:187],a[240:243],v[112:115]\n"
"s_mov_b32 s12,s74\n"
RT"a[154:155],v242 offset:45568\n"
PK"v105,v106,v107\n"
MF"v[120:123],a[184:187],a[244:247],v[120:123]\n"
"s_and_b32 s13,s75,0xffff\n"
RT"a[156:157],v243 offset:45056\n"
PK"v106,v108,v109\n"
MF"v[128:131],a[184:187],a[248:251],v[128:131]\n"
"s_mov_b32 s14,5242880\n"
RT"a[158:159],v243 offset:45568\n"
PK"v107,v110,v111\n"
MF"v[136:139],a[184:187],a[252:255],v[136:139]\n"
"s_lshr_b32 s90,s31,3\n"
"v_accvgpr_write_b32 a48,v104\n"
"v_accvgpr_write_b32 a49,v105\n"
MF"v[116:119],a[188:191],a[240:243],v[116:119]\n"
SA"s90,s90,s91\n"
RT"a[160:161],v248 offset:20480\n"
"v_accvgpr_write_b32 a50,v106\n"
MF"v[124:127],a[188:191],a[244:247],v[124:127]\n"
"s_lshl_b32 s90,s90,2\n"
"v_accvgpr_write_b32 a51,v107\n"
"v_min_u32 v237,624,v224\n"
MF"v[132:135],a[188:191],a[248:251],v[132:135]\n"
SA"s90,s90,s30\n"
RT"a[162:163],v248 offset:20992\n"
"v_lshlrev_b32 v237,3,v237\n"
MF"v[140:143],a[188:191],a[252:255],v[140:143]\n"
"s_mul_i32 s90,s90,5120\n"
RT"a[164:165],v249 offset:20480\n"
MF"v[144:147],a[128:131],a[192:195],v[144:147]\n"
"buffer_load_dword v255,v237,s[12:15],s90 offen\n"
MF"v[152:155],a[128:131],a[196:199],v[152:155]\n"
RT"a[166:167],v249 offset:20992\n"
MF"v[160:163],a[128:131],a[200:203],v[160:163]\n"
RT"a[168:169],v248 offset:45056\n"
PK"v112,v112,v113\n"
MF"v[168:171],a[128:131],a[204:207],v[168:171]\n"
PK"v113,v114,v115\n"
PK"v114,v116,v117\n"
MF"v[148:151],a[132:135],a[192:195],v[148:151]\n"
RT"a[170:171],v248 offset:45568\n"
PK"v115,v118,v119\n"
MF"v[156:159],a[132:135],a[196:199],v[156:159]\n"
RT"a[172:173],v249 offset:45056\n"
"v_accvgpr_write_b32 a4,v112\n"
MF"v[164:167],a[132:135],a[200:203],v[164:167]\n"
"v_accvgpr_write_b32 a5,v113\n"
"v_accvgpr_write_b32 a6,v114\n"
MF"v[172:175],a[132:135],a[204:207],v[172:175]\n"
WT"lgkmcnt(4)\n"
RT"a[174:175],v249 offset:45568\n"
"v_accvgpr_write_b32 a7,v115\n"
MF"v[144:147],a[136:139],a[208:211],v[144:147]\n"
PK"v120,v120,v121\n"
PK"v121,v122,v123\n"
MF"v[152:155],a[136:139],a[212:215],v[152:155]\n"
RT"a[176:177],v244 offset:20480\n"
PK"v122,v124,v125\n"
MF"v[160:163],a[136:139],a[216:219],v[160:163]\n"
PK"v123,v126,v127\n"
"v_accvgpr_write_b32 a20,v120\n"
MF"v[168:171],a[136:139],a[220:223],v[168:171]\n"
RT"a[178:179],v244 offset:20992\n"
"v_accvgpr_write_b32 a21,v121\n"
MF"v[148:151],a[140:143],a[208:211],v[148:151]\n"
RT"a[180:181],v245 offset:20480\n"
"v_accvgpr_write_b32 a22,v122\n"
MF"v[156:159],a[140:143],a[212:215],v[156:159]\n"
"v_accvgpr_write_b32 a23,v123\n"
MF"v[164:167],a[140:143],a[216:219],v[164:167]\n"
RT"a[182:183],v245 offset:20992\n"
PK"v128,v128,v129\n"
MF"v[172:175],a[140:143],a[220:223],v[172:175]\n"
RT"a[184:185],v244 offset:45056\n"
PK"v129,v130,v131\n"
MF"v[144:147],a[144:147],a[224:227],v[144:147]\n"
PK"v130,v132,v133\n"
PK"v131,v134,v135\n"
MF"v[152:155],a[144:147],a[228:231],v[152:155]\n"
RT"a[186:187],v244 offset:45568\n"
"v_accvgpr_write_b32 a36,v128\n"
MF"v[160:163],a[144:147],a[232:235],v[160:163]\n"
RT"a[188:189],v245 offset:45056\n"
"v_accvgpr_write_b32 a37,v129\n"
MF"v[168:171],a[144:147],a[236:239],v[168:171]\n"
"v_accvgpr_write_b32 a38,v130\n"
"v_accvgpr_write_b32 a39,v131\n"
MF"v[148:151],a[148:151],a[224:227],v[148:151]\n"
RT"a[190:191],v245 offset:45568\n"
PK"v136,v136,v137\n"
MF"v[156:159],a[148:151],a[228:231],v[156:159]\n"
PK"v137,v138,v139\n"
PK"v138,v140,v141\n"
MF"v[164:167],a[148:151],a[232:235],v[164:167]\n"
PK"v139,v142,v143\n"
"v_accvgpr_write_b32 a52,v136\n"
MF"v[172:175],a[148:151],a[236:239],v[172:175]\n"
"v_accvgpr_write_b32 a53,v137\n"
"v_accvgpr_write_b32 a54,v138\n"
MF"v[144:147],a[152:155],a[240:243],v[144:147]\n"
"v_accvgpr_write_b32 a55,v139\n"
MF"v[152:155],a[152:155],a[244:247],v[152:155]\n"
MF"v[160:163],a[152:155],a[248:251],v[160:163]\n"
MF"v[168:171],a[152:155],a[252:255],v[168:171]\n"
MF"v[148:151],a[156:159],a[240:243],v[148:151]\n"
MF"v[156:159],a[156:159],a[244:247],v[156:159]\n"
MF"v[164:167],a[156:159],a[248:251],v[164:167]\n"
MF"v[172:175],a[156:159],a[252:255],v[172:175]\n"
MF"v[176:179],a[160:163],a[192:195],v[176:179]\n"
WT"lgkmcnt(0)\n"
MF"v[180:183],a[164:167],a[192:195],v[180:183]\n"
MF"v[176:179],a[168:171],a[208:211],v[176:179]\n"
PK"v144,v144,v145\n"
PK"v145,v146,v147\n"
MF"v[180:183],a[172:175],a[208:211],v[180:183]\n"
PK"v146,v148,v149\n"
PK"v147,v150,v151\n"
MF"v[176:179],a[176:179],a[224:227],v[176:179]\n"
"v_mov_b32 v254,0x3f803f80\n"
"v_accvgpr_write_b32 a8,v144\n"
MF"v[180:183],a[180:183],a[224:227],v[180:183]\n"
"v_accvgpr_write_b32 a9,v145\n"
"v_accvgpr_write_b32 a128,v254\n"
MF"v[176:179],a[184:187],a[240:243],v[176:179]\n"
"v_accvgpr_write_b32 a129,v254\n"
"v_accvgpr_write_b32 a130,v254\n"
MF"v[180:183],a[188:191],a[240:243],v[180:183]\n"
"v_accvgpr_write_b32 a131,v254\n"
"v_accvgpr_write_b32 a10,v146\n"
MF"v[184:187],a[160:163],a[196:199],v[184:187]\n"
"v_accvgpr_write_b32 a11,v147\n"
PK"v152,v152,v153\n"
MF"v[188:191],a[164:167],a[196:199],v[188:191]\n"
PK"v153,v154,v155\n"
PK"v154,v156,v157\n"
MF"v[184:187],a[168:171],a[212:215],v[184:187]\n"
PK"v155,v158,v159\n"
"v_accvgpr_write_b32 a24,v152\n"
MF"v[188:191],a[172:175],a[212:215],v[188:191]\n"
"v_accvgpr_write_b32 a25,v153\n"
PK"v176,v176,v177\n"
MF"v[184:187],a[176:179],a[228:231],v[184:187]\n"
PK"v177,v178,v179\n"
"v_accvgpr_write_b32 a26,v154\n"
MF"v[188:191],a[180:183],a[228:231],v[188:191]\n"
"v_accvgpr_write_b32 a27,v155\n"
PK"v178,v180,v181\n"
MF"v[184:187],a[184:187],a[244:247],v[184:187]\n"
PK"v179,v182,v183\n"
"v_accvgpr_write_b32 a12,v176\n"
MF"v[188:191],a[188:191],a[244:247],v[188:191]\n"
"v_accvgpr_write_b32 a13,v177\n"
"v_accvgpr_write_b32 a14,v178\n"
MF"v[192:195],a[160:163],a[200:203],v[192:195]\n"
"v_accvgpr_write_b32 a15,v179\n"
PK"v160,v160,v161\n"
MF"v[196:199],a[164:167],a[200:203],v[196:199]\n"
WT"vmcnt(15)\n"
"s_barrier\n"
MF"v[192:195],a[168:171],a[216:219],v[192:195]\n"
RD"a[64:67],v226 offset:0\n"
PK"v161,v162,v163\n"
MF"v[196:199],a[172:175],a[216:219],v[196:199]\n"
PK"v162,v164,v165\n"
PK"v163,v166,v167\n"
MF"v[192:195],a[176:179],a[232:235],v[192:195]\n"
RD"a[68:71],v226 offset:1024\n"
"v_accvgpr_write_b32 a40,v160\n"
MF"v[196:199],a[180:183],a[232:235],v[196:199]\n"
"v_accvgpr_write_b32 a41,v161\n"
"v_accvgpr_write_b32 a42,v162\n"
MF"v[192:195],a[184:187],a[248:251],v[192:195]\n"
RD"a[72:75],v226 offset:2048\n"
"v_accvgpr_write_b32 a43,v163\n"
MF"v[196:199],a[188:191],a[248:251],v[196:199]\n"
PK"v184,v184,v185\n"
PK"v185,v186,v187\n"
MF"v[200:203],a[160:163],a[204:207],v[200:203]\n"
RD"a[76:79],v226 offset:3072\n"
PK"v186,v188,v189\n"
MF"v[204:207],a[164:167],a[204:207],v[204:207]\n"
PK"v187,v190,v191\n"
PK"v168,v168,v169\n"
MF"v[200:203],a[168:171],a[220:223],v[200:203]\n"
RD"a[80:83],v226 offset:4096\n"
PK"v169,v170,v171\n"
MF"v[204:207],a[172:175],a[220:223],v[204:207]\n"
"v_accvgpr_write_b32 a28,v184\n"
"v_accvgpr_write_b32 a29,v185\n"
MF"v[200:203],a[176:179],a[236:239],v[200:203]\n"
RD"a[84:87],v226 offset:5120\n"
PK"v170,v172,v173\n"
MF"v[204:207],a[180:183],a[236:239],v[204:207]\n"
PK"v171,v174,v175\n"
"v_accvgpr_write_b32 a30,v186\n"
MF"v[200:203],a[184:187],a[252:255],v[200:203]\n"
"v_accvgpr_write_b32 a31,v187\n"
"v_accvgpr_write_b32 a56,v168\n"
MF"v[204:207],a[188:191],a[252:255],v[204:207]\n"
"v_accvgpr_write_b32 a57,v169\n"
"v_accvgpr_write_b32 a58,v170\n"
"v_accvgpr_write_b32 a59,v171\n"
"s_cmp_eq_u32 s34,0\n"
"s_cbranch_scc1 LFA_%=\n"
WT"lgkmcnt(5)\n"
MF"v[0:3],a[64:67],a[0:3],0\n"
"s_mov_b32 m0,s40\n"
PK"v192,v192,v193\n"
PK"v193,v194,v195\n"
MF"v[8:11],a[64:67],a[16:19],0\n"
"v_readlane_b32 s43,v228,s42\n"
PK"v194,v196,v197\n"
MF"v[16:19],a[64:67],a[32:35],0\n"
PK"v195,v198,v199\n"
"v_readlane_b32 s44,v229,s42\n"
MF"v[24:27],a[64:67],a[48:51],0\n"
WT"lgkmcnt(0)\n"
BL"v225,s[8:11],s43 offen offset:0 sc1 lds\n"
MF"v[0:3],a[68:71],a[4:7],v[0:3]\n"
SA"s42,s42,1\n"
"v_accvgpr_write_b32 a44,v192\n"
"v_accvgpr_write_b32 a45,v193\n"
MF"v[8:11],a[68:71],a[20:23],v[8:11]\n"
"v_accvgpr_write_b32 a46,v194\n"
"v_accvgpr_write_b32 a47,v195\n"
MF"v[16:19],a[68:71],a[36:39],v[16:19]\n"
PK"v200,v200,v201\n"
PK"v201,v202,v203\n"
MF"v[24:27],a[68:71],a[52:55],v[24:27]\n"
PK"v202,v204,v205\n"
PK"v203,v206,v207\n"
"v_mov_b32 v227,v226\n"
MF"v[0:3],a[72:75],a[8:11],v[0:3]\n"
BL"v225,s[8:11],s43 offen offset:1024 sc1 lds\n"
MF"v[8:11],a[72:75],a[24:27],v[8:11]\n"
RD"a[88:91],v226 offset:6144\n"
"v_accvgpr_write_b32 a60,v200\n"
"v_accvgpr_write_b32 a61,v201\n"
MF"v[16:19],a[72:75],a[40:43],v[16:19]\n"
"v_accvgpr_write_b32 a62,v202\n"
"v_accvgpr_write_b32 a63,v203\n"
MF"v[24:27],a[72:75],a[56:59],v[24:27]\n"
MF"v[0:3],a[76:79],a[12:15],v[0:3]\n"
MF"v[8:11],a[76:79],a[28:31],v[8:11]\n"
RD"a[92:95],v226 offset:7168\n"
MF"v[16:19],a[76:79],a[44:47],v[16:19]\n"
BL"v225,s[8:11],s43 offen offset:2048 sc1 lds\n"
MF"v[24:27],a[76:79],a[60:63],v[24:27]\n"
MF"v[4:7],a[80:83],a[0:3],0\n"
MF"v[12:15],a[80:83],a[16:19],0\n"
RD"a[96:99],v226 offset:8192\n"
MF"v[20:23],a[80:83],a[32:35],0\n"
EX"v0,v0\n"
MF"v[28:31],a[80:83],a[48:51],0\n"
EX"v1,v1\n"
MF"v[4:7],a[84:87],a[4:7],v[4:7]\n"
EX"v2,v2\n"
MF"v[12:15],a[84:87],a[20:23],v[12:15]\n"
BL"v225,s[8:11],s43 offen offset:3072 sc1 lds\n"
MF"v[20:23],a[84:87],a[36:39],v[20:23]\n"
SA"m0,s40,16384\n"
EX"v3,v3\n"
PK"v0,v0,v1\n"
MF"v[28:31],a[84:87],a[52:55],v[28:31]\n"
WT"lgkmcnt(1)\n"
RD"a[100:103],v226 offset:9216\n"
PK"v1,v2,v3\n"
MF"v[4:7],a[88:91],a[8:11],v[4:7]\n"
EX"v8,v8\n"
MF"v[12:15],a[88:91],a[24:27],v[12:15]\n"
EX"v9,v9\n"
MF"v[20:23],a[88:91],a[40:43],v[20:23]\n"
EX"v10,v10\n"
MF"v[28:31],a[88:91],a[56:59],v[28:31]\n"
SA"s40,s40,32768\n"
RD"a[104:107],v226 offset:10240\n"
PK"v8,v8,v9\n"
MF"v[4:7],a[92:95],a[12:15],v[4:7]\n"
"s_cmp_eq_u32 s40,s46\n"
BL"v225,s[8:11],s44 offen offset:0 sc1 lds\n"
EX"v11,v11\n"
MF"v[12:15],a[92:95],a[28:31],v[12:15]\n"
"s_cselect_b32 s40,s47,s40\n"
EX"v16,v16\n"
MF"v[20:23],a[92:95],a[44:47],v[20:23]\n"
EX"v17,v17\n"
PK"v9,v10,v11\n"
MF"v[28:31],a[92:95],a[60:63],v[28:31]\n"
WT"lgkmcnt(1)\n"
RD"a[108:111],v226 offset:11264\n"
PK"v16,v16,v17\n"
MF"v[32:35],a[96:99],a[0:3],0\n"
EX"v18,v18\n"
MF"v[40:43],a[96:99],a[16:19],0\n"
EX"v19,v19\n"
MF"v[48:51],a[96:99],a[32:35],0\n"
BL"v225,s[8:11],s44 offen offset:1024 sc1 lds\n"
EX"v24,v24\n"
MF"v[56:59],a[96:99],a[48:51],0\n"
RD"a[112:115],v226 offset:12288\n"
PK"v17,v18,v19\n"
MF"v[32:35],a[100:103],a[4:7],v[32:35]\n"
EX"v25,v25\n"
MF"v[40:43],a[100:103],a[20:23],v[40:43]\n"
EX"v26,v26\n"
MF"v[48:51],a[100:103],a[36:39],v[48:51]\n"
EX"v27,v27\n"
PK"v24,v24,v25\n"
MF"v[56:59],a[100:103],a[52:55],v[56:59]\n"
WT"lgkmcnt(1)\n"
RD"a[116:119],v226 offset:13312\n"
PK"v25,v26,v27\n"
MF"v[32:35],a[104:107],a[8:11],v[32:35]\n"
BL"v225,s[8:11],s44 offen offset:2048 sc1 lds\n"
EX"v4,v4\n"
MF"v[40:43],a[104:107],a[24:27],v[40:43]\n"
EX"v5,v5\n"
MF"v[48:51],a[104:107],a[40:43],v[48:51]\n"
EX"v6,v6\n"
MF"v[56:59],a[104:107],a[56:59],v[56:59]\n"
RD"a[120:123],v226 offset:14336\n"
"v_readlane_b32 s43,v228,s42\n"
PK"v2,v4,v5\n"
MF"v[32:35],a[108:111],a[12:15],v[32:35]\n"
EX"v7,v7\n"
MF"v[40:43],a[108:111],a[28:31],v[40:43]\n"
EX"v12,v12\n"
MF"v[48:51],a[108:111],a[44:47],v[48:51]\n"
BL"v225,s[8:11],s44 offen offset:3072 sc1 lds\n"
EX"v13,v13\n"
MF"v[56:59],a[108:111],a[60:63],v[56:59]\n"
WT"lgkmcnt(1)\n"
RD"a[124:127],v226 offset:15360\n"
PK"v3,v6,v7\n"
MF"v[36:39],a[112:115],a[0:3],0\n"
"s_mov_b32 m0,s40\n"
EX"v14,v14\n"
PK"v10,v12,v13\n"
MF"v[44:47],a[112:115],a[16:19],0\n"
EX"v15,v15\n"
MF"v[52:55],a[112:115],a[32:35],0\n"
EX"v20,v20\n"
MF"v[60:63],a[112:115],a[48:51],0\n"
RD"a[96:99],v227 offset:24576\n"
"v_readlane_b32 s44,v229,s42\n"
PK"v11,v14,v15\n"
MF"v[36:39],a[116:119],a[4:7],v[36:39]\n"
SA"s42,s42,1\n"
BL"v225,s[8:11],s43 offen offset:0 sc1 lds\n"
MF"v[44:47],a[116:119],a[20:23],v[44:47]\n"
EX"v21,v21\n"
MF"v[52:55],a[116:119],a[36:39],v[52:55]\n"
EX"v22,v22\n"
MF"v[60:63],a[116:119],a[52:55],v[60:63]\n"
WT"lgkmcnt(1)\n"
RD"a[100:103],v227 offset:25600\n"
PK"v18,v20,v21\n"
MF"v[36:39],a[120:123],a[8:11],v[36:39]\n"
EX"v23,v23\n"
MF"v[44:47],a[120:123],a[24:27],v[44:47]\n"
BL"v225,s[8:11],s43 offen offset:1024 sc1 lds\n"
MF"v[52:55],a[120:123],a[40:43],v[52:55]\n"
EX"v28,v28\n"
PK"v19,v22,v23\n"
MF"v[60:63],a[120:123],a[56:59],v[60:63]\n"
RD"a[104:107],v227 offset:26624\n"
MF"v[36:39],a[124:127],a[12:15],v[36:39]\n"
EX"v29,v29\n"
MF"v[44:47],a[124:127],a[28:31],v[44:47]\n"
EX"v30,v30\n"
MF"v[52:55],a[124:127],a[44:47],v[52:55]\n"
EX"v31,v31\n"
PK"v26,v28,v29\n"
MF"v[60:63],a[124:127],a[60:63],v[60:63]\n"
WT"lgkmcnt(1)\n"
BL"v225,s[8:11],s43 offen offset:2048 sc1 lds\n"
RD"a[108:111],v227 offset:27648\n"
MF"v[80:83],a[96:99],v[0:3],0\n"
SA"s41,s41,32768\n"
PK"v27,v30,v31\n"
EX"v32,v32\n"
MF"v[112:115],a[96:99],v[8:11],0\n"
"s_cmp_eq_u32 s41,163840\n"
EX"v33,v33\n"
MF"v[144:147],a[96:99],v[16:19],0\n"
"s_cselect_b32 s41,0,s41\n"
EX"v34,v34\n"
"v_add_u32 v226,s41,v224\n"
MF"v[176:179],a[96:99],v[24:27],0\n"
RD"a[112:115],v227 offset:28672\n"
PK"v64,v32,v33\n"
MF"v[84:87],a[100:103],v[0:3],0\n"
EX"v35,v35\n"
MF"v[116:119],a[100:103],v[8:11],0\n"
BL"v225,s[8:11],s43 offen offset:3072 sc1 lds\n"
MF"v[148:151],a[100:103],v[16:19],0\n"
SA"m0,s40,16384\n"
EX"v40,v40\n"
PK"v65,v34,v35\n"
MF"v[180:183],a[100:103],v[24:27],0\n"
WT"lgkmcnt(1)\n"
RD"a[116:119],v227 offset:29696\n"
MF"v[88:91],a[104:107],v[0:3],0\n"
EX"v41,v41\n"
MF"v[120:123],a[104:107],v[8:11],0\n"
EX"v42,v42\n"
MF"v[152:155],a[104:107],v[16:19],0\n"
SA"s40,s40,32768\n"
EX"v43,v43\n"
PK"v68,v40,v41\n"
MF"v[184:187],a[104:107],v[24:27],0\n"
"s_cmp_eq_u32 s40,s46\n"
BL"v225,s[8:11],s44 offen offset:0 sc1 lds\n"
RD"a[120:123],v227 offset:30720\n"
MF"v[92:95],a[108:111],v[0:3],0\n"
"s_cselect_b32 s40,s47,s40\n"
EX"v48,v48\n"
PK"v69,v42,v43\n"
MF"v[124:127],a[108:111],v[8:11],0\n"
EX"v49,v49\n"
MF"v[156:159],a[108:111],v[16:19],0\n"
EX"v50,v50\n"
MF"v[188:191],a[108:111],v[24:27],0\n"
WT"lgkmcnt(1)\n"
RD"a[124:127],v227 offset:31744\n"
PK"v72,v48,v49\n"
MF"v[96:99],a[112:115],v[0:3],0\n"
EX"v51,v51\n"
MF"v[128:131],a[112:115],v[8:11],0\n"
BL"v225,s[8:11],s44 offen offset:1024 sc1 lds\n"
MF"v[160:163],a[112:115],v[16:19],0\n"
EX"v36,v36\n"
PK"v73,v50,v51\n"
MF"v[192:195],a[112:115],v[24:27],0\n"
EX"v37,v37\n"
MF"v[100:103],a[116:119],v[0:3],0\n"
EX"v38,v38\n"
MF"v[132:135],a[116:119],v[8:11],0\n"
EX"v39,v39\n"
PK"v66,v36,v37\n"
MF"v[164:167],a[116:119],v[16:19],0\n"
WT"vmcnt(17) lgkmcnt(0)\n"
"s_barrier\n"
MF"v[196:199],a[116:119],v[24:27],0\n"
BL"v225,s[8:11],s44 offen offset:2048 sc1 lds\n"
RD"a[64:67],v226 offset:0\n"
MF"v[104:107],a[120:123],v[0:3],0\n"
"buffer_store_dwordx2 v[232:233],v230,s[8:11],s98 offen sc1\n"
EX"v44,v44\n"
MF"v[136:139],a[120:123],v[8:11],0\n"
EX"v45,v45\n"
PK"v67,v38,v39\n"
MF"v[168:171],a[120:123],v[16:19],0\n"
EX"v46,v46\n"
MF"v[200:203],a[120:123],v[24:27],0\n"
RD"a[68:71],v226 offset:1024\n"
RD"a[72:75],v226 offset:2048\n"
PK"v70,v44,v45\n"
MF"v[108:111],a[124:127],v[0:3],0\n"
EX"v47,v47\n"
MF"v[140:143],a[124:127],v[8:11],0\n"
BL"v225,s[8:11],s44 offen offset:3072 sc1 lds\n"
MF"v[172:175],a[124:127],v[16:19],0\n"
EX"v52,v52\n"
PK"v71,v46,v47\n"
MF"v[204:207],a[124:127],v[24:27],0\n"
RD"a[76:79],v226 offset:3072\n"
MF"a[132:135],a[128:131],v[0:3],0\n"
EX"v53,v53\n"
MF"a[136:139],a[128:131],v[8:11],0\n"
EX"v54,v54\n"
MF"a[140:143],a[128:131],v[16:19],0\n"
EX"v55,v55\n"
PK"v74,v52,v53\n"
MF"a[144:147],a[128:131],v[24:27],0\n"
PK"v75,v54,v55\n"
RD"a[80:83],v226 offset:4096\n"
RD"a[84:87],v226 offset:5120\n"
"s_cmp_eq_u32 s34,7\n"
"s_cbranch_scc1 LRT_%=\n"
"s_cmp_eq_u32 s57,1\n"
"s_cbranch_scc1 LEV_%=\n"
".p2align 6\n"
"LP1_%=:\n"
WT"lgkmcnt(5)\n"
MF"v[0:3],a[64:67],a[0:3],0\n"
EX"v56,v56\n"
MF"v[8:11],a[64:67],a[16:19],0\n"
"v_readlane_b32 s43,v228,s42\n"
MF"v[16:19],a[64:67],a[32:35],0\n"
EX"v57,v57\n"
MF"v[24:27],a[64:67],a[48:51],0\n"
WT"lgkmcnt(4)\n"
PK"v76,v56,v57\n"
"s_mov_b32 m0,s40\n"
MF"v[0:3],a[68:71],a[4:7],v[0:3]\n"
EX"v58,v58\n"
MF"v[8:11],a[68:71],a[20:23],v[8:11]\n"
"v_readlane_b32 s44,v229,s42\n"
"v_mov_b32 v227,v226\n"
MF"v[16:19],a[68:71],a[36:39],v[16:19]\n"
EX"v59,v59\n"
MF"v[24:27],a[68:71],a[52:55],v[24:27]\n"
WT"lgkmcnt(0)\n"
BL"v225,s[8:11],s43 offen offset:0 sc1 lds\n"
MF"v[0:3],a[72:75],a[8:11],v[0:3]\n"
SA"s42,s42,1\n"
EX"v60,v60\n"
MF"v[8:11],a[72:75],a[24:27],v[8:11]\n"
RD"a[88:91],v226 offset:6144\n"
PK"v77,v58,v59\n"
MF"v[16:19],a[72:75],a[40:43],v[16:19]\n"
EX"v61,v61\n"
MF"v[24:27],a[72:75],a[56:59],v[24:27]\n"
PK"v78,v60,v61\n"
MF"v[0:3],a[76:79],a[12:15],v[0:3]\n"
EX"v62,v62\n"
MF"v[8:11],a[76:79],a[28:31],v[8:11]\n"
RD"a[92:95],v226 offset:7168\n"
MF"v[16:19],a[76:79],a[44:47],v[16:19]\n"
EX"v63,v63\n"
MF"v[24:27],a[76:79],a[60:63],v[24:27]\n"
BL"v225,s[8:11],s43 offen offset:1024 sc1 lds\n"
MF"v[4:7],a[80:83],a[0:3],0\n"
EX"v0,v0\n"
MF"v[12:15],a[80:83],a[16:19],0\n"
RD"a[96:99],v226 offset:8192\n"
PK"v79,v62,v63\n"
MF"v[20:23],a[80:83],a[32:35],0\n"
EX"v1,v1\n"
MF"v[28:31],a[80:83],a[48:51],0\n"
PK"v0,v0,v1\n"
MF"v[4:7],a[84:87],a[4:7],v[4:7]\n"
EX"v2,v2\n"
MF"v[12:15],a[84:87],a[20:23],v[12:15]\n"
RD"a[100:103],v226 offset:9216\n"
MF"v[20:23],a[84:87],a[36:39],v[20:23]\n"
EX"v3,v3\n"
MF"v[28:31],a[84:87],a[52:55],v[28:31]\n"
WT"lgkmcnt(2)\n"
BL"v225,s[8:11],s43 offen offset:2048 sc1 lds\n"
MF"v[4:7],a[88:91],a[8:11],v[4:7]\n"
EX"v8,v8\n"
MF"v[12:15],a[88:91],a[24:27],v[12:15]\n"
RD"a[104:107],v226 offset:10240\n"
PK"v1,v2,v3\n"
MF"v[20:23],a[88:91],a[40:43],v[20:23]\n"
EX"v9,v9\n"
MF"v[28:31],a[88:91],a[56:59],v[28:31]\n"
PK"v8,v8,v9\n"
MF"v[4:7],a[92:95],a[12:15],v[4:7]\n"
EX"v10,v10\n"
MF"v[12:15],a[92:95],a[28:31],v[12:15]\n"
RD"a[108:111],v226 offset:11264\n"
MF"v[20:23],a[92:95],a[44:47],v[20:23]\n"
EX"v11,v11\n"
MF"v[28:31],a[92:95],a[60:63],v[28:31]\n"
WT"lgkmcnt(2)\n"
BL"v225,s[8:11],s43 offen offset:3072 sc1 lds\n"
MF"v[32:35],a[96:99],a[0:3],0\n"
EX"v16,v16\n"
MF"v[40:43],a[96:99],a[16:19],0\n"
RD"a[112:115],v226 offset:12288\n"
PK"v9,v10,v11\n"
MF"v[48:51],a[96:99],a[32:35],0\n"
EX"v17,v17\n"
MF"v[56:59],a[96:99],a[48:51],0\n"
SA"m0,s40,16384\n"
PK"v16,v16,v17\n"
MF"v[32:35],a[100:103],a[4:7],v[32:35]\n"
EX"v18,v18\n"
MF"v[40:43],a[100:103],a[20:23],v[40:43]\n"
RD"a[116:119],v226 offset:13312\n"
MF"v[48:51],a[100:103],a[36:39],v[48:51]\n"
EX"v19,v19\n"
MF"v[56:59],a[100:103],a[52:55],v[56:59]\n"
WT"lgkmcnt(2)\n"
BL"v225,s[8:11],s44 offen offset:0 sc1 lds\n"
MF"v[32:35],a[104:107],a[8:11],v[32:35]\n"
SA"s40,s40,32768\n"
EX"v24,v24\n"
MF"v[40:43],a[104:107],a[24:27],v[40:43]\n"
"s_cmp_eq_u32 s40,s46\n"
RD"a[120:123],v226 offset:14336\n"
PK"v17,v18,v19\n"
MF"v[48:51],a[104:107],a[40:43],v[48:51]\n"
"s_cselect_b32 s40,s47,s40\n"
EX"v25,v25\n"
MF"v[56:59],a[104:107],a[56:59],v[56:59]\n"
PK"v24,v24,v25\n"
MF"v[32:35],a[108:111],a[12:15],v[32:35]\n"
EX"v26,v26\n"
MF"v[40:43],a[108:111],a[28:31],v[40:43]\n"
RD"a[124:127],v226 offset:15360\n"
MF"v[48:51],a[108:111],a[44:47],v[48:51]\n"
EX"v27,v27\n"
MF"v[56:59],a[108:111],a[60:63],v[56:59]\n"
WT"lgkmcnt(2)\n"
BL"v225,s[8:11],s44 offen offset:1024 sc1 lds\n"
MF"v[36:39],a[112:115],a[0:3],0\n"
EX"v4,v4\n"
MF"v[44:47],a[112:115],a[16:19],0\n"
RD"a[64:67],v227 offset:16384\n"
PK"v25,v26,v27\n"
MF"v[52:55],a[112:115],a[32:35],0\n"
EX"v5,v5\n"
MF"v[60:63],a[112:115],a[48:51],0\n"
PK"v2,v4,v5\n"
MF"v[36:39],a[116:119],a[4:7],v[36:39]\n"
EX"v6,v6\n"
MF"v[44:47],a[116:119],a[20:23],v[44:47]\n"
RD"a[68:71],v227 offset:17408\n"
MF"v[52:55],a[116:119],a[36:39],v[52:55]\n"
EX"v7,v7\n"
MF"v[60:63],a[116:119],a[52:55],v[60:63]\n"
WT"lgkmcnt(2)\n"
BL"v225,s[8:11],s44 offen offset:2048 sc1 lds\n"
MF"v[36:39],a[120:123],a[8:11],v[36:39]\n"
EX"v12,v12\n"
MF"v[44:47],a[120:123],a[24:27],v[44:47]\n"
RD"a[72:75],v227 offset:18432\n"
PK"v3,v6,v7\n"
MF"v[52:55],a[120:123],a[40:43],v[52:55]\n"
EX"v13,v13\n"
MF"v[60:63],a[120:123],a[56:59],v[60:63]\n"
PK"v10,v12,v13\n"
MF"v[36:39],a[124:127],a[12:15],v[36:39]\n"
EX"v14,v14\n"
MF"v[44:47],a[124:127],a[28:31],v[44:47]\n"
RD"a[76:79],v227 offset:19456\n"
MF"v[52:55],a[124:127],a[44:47],v[52:55]\n"
EX"v15,v15\n"
MF"v[60:63],a[124:127],a[60:63],v[60:63]\n"
WT"lgkmcnt(2)\n"
BL"v225,s[8:11],s44 offen offset:3072 sc1 lds\n"
MF"v[80:83],a[64:67],v[64:67],v[80:83]\n"
SA"s41,s41,32768\n"
EX"v20,v20\n"
MF"v[112:115],a[64:67],v[68:71],v[112:115]\n"
"s_cmp_eq_u32 s41,163840\n"
RD"a[80:83],v227 offset:20480\n"
PK"v11,v14,v15\n"
MF"v[144:147],a[64:67],v[72:75],v[144:147]\n"
"s_cselect_b32 s41,0,s41\n"
EX"v21,v21\n"
MF"v[176:179],a[64:67],v[76:79],v[176:179]\n"
"v_add_u32 v226,s41,v224\n"
PK"v18,v20,v21\n"
MF"v[84:87],a[68:71],v[64:67],v[84:87]\n"
EX"v22,v22\n"
MF"v[116:119],a[68:71],v[68:71],v[116:119]\n"
RD"a[84:87],v227 offset:21504\n"
MF"v[148:151],a[68:71],v[72:75],v[148:151]\n"
EX"v23,v23\n"
MF"v[180:183],a[68:71],v[76:79],v[180:183]\n"
WT"lgkmcnt(2)\n"
PK"v19,v22,v23\n"
MF"v[88:91],a[72:75],v[64:67],v[88:91]\n"
EX"v28,v28\n"
MF"v[120:123],a[72:75],v[68:71],v[120:123]\n"
RD"a[88:91],v227 offset:22528\n"
MF"v[152:155],a[72:75],v[72:75],v[152:155]\n"
EX"v29,v29\n"
MF"v[184:187],a[72:75],v[76:79],v[184:187]\n"
PK"v26,v28,v29\n"
MF"v[92:95],a[76:79],v[64:67],v[92:95]\n"
EX"v30,v30\n"
MF"v[124:127],a[76:79],v[68:71],v[124:127]\n"
RD"a[92:95],v227 offset:23552\n"
MF"v[156:159],a[76:79],v[72:75],v[156:159]\n"
EX"v31,v31\n"
MF"v[188:191],a[76:79],v[76:79],v[188:191]\n"
WT"lgkmcnt(2)\n"
PK"v27,v30,v31\n"
MF"v[96:99],a[80:83],v[64:67],v[96:99]\n"
EX"v32,v32\n"
MF"v[128:131],a[80:83],v[68:71],v[128:131]\n"
RD"a[96:99],v227 offset:24576\n"
MF"v[160:163],a[80:83],v[72:75],v[160:163]\n"
EX"v33,v33\n"
MF"v[192:195],a[80:83],v[76:79],v[192:195]\n"
MF"v[100:103],a[84:87],v[64:67],v[100:103]\n"
EX"v34,v34\n"
MF"v[132:135],a[84:87],v[68:71],v[132:135]\n"
RD"a[100:103],v227 offset:25600\n"
MF"v[164:167],a[84:87],v[72:75],v[164:167]\n"
EX"v35,v35\n"
MF"v[196:199],a[84:87],v[76:79],v[196:199]\n"
WT"lgkmcnt(2)\n"
MF"v[104:107],a[88:91],v[64:67],v[104:107]\n"
EX"v40,v40\n"
MF"v[136:139],a[88:91],v[68:71],v[136:139]\n"
RD"a[104:107],v227 offset:26624\n"
MF"v[168:171],a[88:91],v[72:75],v[168:171]\n"
EX"v41,v41\n"
MF"v[200:203],a[88:91],v[76:79],v[200:203]\n"
MF"v[108:111],a[92:95],v[64:67],v[108:111]\n"
EX"v42,v42\n"
MF"v[140:143],a[92:95],v[68:71],v[140:143]\n"
RD"a[108:111],v227 offset:27648\n"
MF"v[172:175],a[92:95],v[72:75],v[172:175]\n"
EX"v43,v43\n"
MF"v[204:207],a[92:95],v[76:79],v[204:207]\n"
MF"a[132:135],a[128:131],v[64:67],a[132:135]\n"
EX"v48,v48\n"
MF"a[136:139],a[128:131],v[68:71],a[136:139]\n"
RD"a[112:115],v227 offset:28672\n"
PK"v64,v32,v33\n"
MF"a[140:143],a[128:131],v[72:75],a[140:143]\n"
EX"v49,v49\n"
MF"a[144:147],a[128:131],v[76:79],a[144:147]\n"
WT"lgkmcnt(2)\n"
PK"v65,v34,v35\n"
PK"v68,v40,v41\n"
MF"v[80:83],a[96:99],v[0:3],v[80:83]\n"
EX"v50,v50\n"
MF"v[112:115],a[96:99],v[8:11],v[112:115]\n"
RD"a[116:119],v227 offset:29696\n"
PK"v69,v42,v43\n"
MF"v[144:147],a[96:99],v[16:19],v[144:147]\n"
EX"v51,v51\n"
MF"v[176:179],a[96:99],v[24:27],v[176:179]\n"
PK"v72,v48,v49\n"
PK"v73,v50,v51\n"
MF"v[84:87],a[100:103],v[0:3],v[84:87]\n"
EX"v36,v36\n"
MF"v[116:119],a[100:103],v[8:11],v[116:119]\n"
RD"a[120:123],v227 offset:30720\n"
MF"v[148:151],a[100:103],v[16:19],v[148:151]\n"
EX"v37,v37\n"
MF"v[180:183],a[100:103],v[24:27],v[180:183]\n"
PK"v66,v36,v37\n"
MF"v[88:91],a[104:107],v[0:3],v[88:91]\n"
EX"v38,v38\n"
MF"v[120:123],a[104:107],v[8:11],v[120:123]\n"
RD"a[124:127],v227 offset:31744\n"
MF"v[152:155],a[104:107],v[16:19],v[152:155]\n"
EX"v39,v39\n"
MF"v[184:187],a[104:107],v[24:27],v[184:187]\n"
WT"lgkmcnt(2)\n"
PK"v67,v38,v39\n"
MF"v[92:95],a[108:111],v[0:3],v[92:95]\n"
EX"v44,v44\n"
MF"v[124:127],a[108:111],v[8:11],v[124:127]\n"
MF"v[156:159],a[108:111],v[16:19],v[156:159]\n"
EX"v45,v45\n"
MF"v[188:191],a[108:111],v[24:27],v[188:191]\n"
PK"v70,v44,v45\n"
MF"v[96:99],a[112:115],v[0:3],v[96:99]\n"
EX"v46,v46\n"
MF"v[128:131],a[112:115],v[8:11],v[128:131]\n"
MF"v[160:163],a[112:115],v[16:19],v[160:163]\n"
EX"v47,v47\n"
MF"v[192:195],a[112:115],v[24:27],v[192:195]\n"
PK"v71,v46,v47\n"
MF"v[100:103],a[116:119],v[0:3],v[100:103]\n"
WT"vmcnt(16) lgkmcnt(0)\n"
"s_barrier\n"
MF"v[132:135],a[116:119],v[8:11],v[132:135]\n"
RD"a[64:67],v226 offset:0\n"
MF"v[164:167],a[116:119],v[16:19],v[164:167]\n"
EX"v52,v52\n"
MF"v[196:199],a[116:119],v[24:27],v[196:199]\n"
RD"a[68:71],v226 offset:1024\n"
MF"v[104:107],a[120:123],v[0:3],v[104:107]\n"
EX"v53,v53\n"
MF"v[136:139],a[120:123],v[8:11],v[136:139]\n"
RD"a[72:75],v226 offset:2048\n"
PK"v74,v52,v53\n"
MF"v[168:171],a[120:123],v[16:19],v[168:171]\n"
EX"v54,v54\n"
MF"v[200:203],a[120:123],v[24:27],v[200:203]\n"
MF"v[108:111],a[124:127],v[0:3],v[108:111]\n"
EX"v55,v55\n"
MF"v[140:143],a[124:127],v[8:11],v[140:143]\n"
RD"a[76:79],v226 offset:3072\n"
PK"v75,v54,v55\n"
MF"v[172:175],a[124:127],v[16:19],v[172:175]\n"
MF"v[204:207],a[124:127],v[24:27],v[204:207]\n"
RD"a[80:83],v226 offset:4096\n"
MF"a[132:135],a[128:131],v[0:3],a[132:135]\n"
MF"a[136:139],a[128:131],v[8:11],a[136:139]\n"
RD"a[84:87],v226 offset:5120\n"
MF"a[140:143],a[128:131],v[16:19],a[140:143]\n"
MF"a[144:147],a[128:131],v[24:27],a[144:147]\n"
"s_sub_u32 s56,s56,1\n"
"s_cbranch_scc0 LP1_%=\n"
"s_cmp_lg_u32 s57,0\n"
"s_cbranch_scc1 LEV_%=\n"
"LDA_%=:\n"
WT"lgkmcnt(5)\n"
MF"v[0:3],a[64:67],a[0:3],v[208:211]\n"
EX"v56,v56\n"
MF"v[8:11],a[64:67],a[16:19],0\n"
"v_readlane_b32 s43,v228,s42\n"
MF"v[16:19],a[64:67],a[32:35],0\n"
EX"v57,v57\n"
MF"v[24:27],a[64:67],a[48:51],0\n"
WT"lgkmcnt(4)\n"
PK"v76,v56,v57\n"
"s_mov_b32 m0,s40\n"
MF"v[0:3],a[68:71],a[4:7],v[0:3]\n"
EX"v58,v58\n"
MF"v[8:11],a[68:71],a[20:23],v[8:11]\n"
"v_readlane_b32 s44,v229,s42\n"
"v_mov_b32 v227,v226\n"
MF"v[16:19],a[68:71],a[36:39],v[16:19]\n"
EX"v59,v59\n"
MF"v[24:27],a[68:71],a[52:55],v[24:27]\n"
WT"lgkmcnt(0)\n"
BL"v225,s[8:11],s43 offen offset:0 sc1 lds\n"
MF"v[0:3],a[72:75],a[8:11],v[0:3]\n"
SA"s42,s42,1\n"
EX"v60,v60\n"
MF"v[8:11],a[72:75],a[24:27],v[8:11]\n"
RD"a[88:91],v226 offset:6144\n"
PK"v77,v58,v59\n"
MF"v[16:19],a[72:75],a[40:43],v[16:19]\n"
EX"v61,v61\n"
MF"v[24:27],a[72:75],a[56:59],v[24:27]\n"
PK"v78,v60,v61\n"
MF"v[0:3],a[76:79],a[12:15],v[0:3]\n"
EX"v62,v62\n"
MF"v[8:11],a[76:79],a[28:31],v[8:11]\n"
RD"a[92:95],v226 offset:7168\n"
MF"v[16:19],a[76:79],a[44:47],v[16:19]\n"
EX"v63,v63\n"
MF"v[24:27],a[76:79],a[60:63],v[24:27]\n"
BL"v225,s[8:11],s43 offen offset:1024 sc1 lds\n"
MF"v[4:7],a[80:83],a[0:3],v[212:215]\n"
EX"v0,v0\n"
MF"v[12:15],a[80:83],a[16:19],0\n"
RD"a[96:99],v226 offset:8192\n"
PK"v79,v62,v63\n"
MF"v[20:23],a[80:83],a[32:35],0\n"
EX"v1,v1\n"
MF"v[28:31],a[80:83],a[48:51],0\n"
PK"v0,v0,v1\n"
MF"v[4:7],a[84:87],a[4:7],v[4:7]\n"
EX"v2,v2\n"
MF"v[12:15],a[84:87],a[20:23],v[12:15]\n"
RD"a[100:103],v226 offset:9216\n"
MF"v[20:23],a[84:87],a[36:39],v[20:23]\n"
EX"v3,v3\n"
MF"v[28:31],a[84:87],a[52:55],v[28:31]\n"
WT"lgkmcnt(2)\n"
BL"v225,s[8:11],s43 offen offset:2048 sc1 lds\n"
MF"v[4:7],a[88:91],a[8:11],v[4:7]\n"
EX"v8,v8\n"
MF"v[12:15],a[88:91],a[24:27],v[12:15]\n"
RD"a[104:107],v226 offset:10240\n"
PK"v1,v2,v3\n"
MF"v[20:23],a[88:91],a[40:43],v[20:23]\n"
EX"v9,v9\n"
MF"v[28:31],a[88:91],a[56:59],v[28:31]\n"
PK"v8,v8,v9\n"
MF"v[4:7],a[92:95],a[12:15],v[4:7]\n"
EX"v10,v10\n"
MF"v[12:15],a[92:95],a[28:31],v[12:15]\n"
RD"a[108:111],v226 offset:11264\n"
MF"v[20:23],a[92:95],a[44:47],v[20:23]\n"
EX"v11,v11\n"
MF"v[28:31],a[92:95],a[60:63],v[28:31]\n"
WT"lgkmcnt(2)\n"
BL"v225,s[8:11],s43 offen offset:3072 sc1 lds\n"
MF"v[32:35],a[96:99],a[0:3],v[216:219]\n"
EX"v16,v16\n"
MF"v[40:43],a[96:99],a[16:19],0\n"
RD"a[112:115],v226 offset:12288\n"
PK"v9,v10,v11\n"
MF"v[48:51],a[96:99],a[32:35],0\n"
EX"v17,v17\n"
MF"v[56:59],a[96:99],a[48:51],0\n"
SA"m0,s40,16384\n"
PK"v16,v16,v17\n"
MF"v[32:35],a[100:103],a[4:7],v[32:35]\n"
EX"v18,v18\n"
MF"v[40:43],a[100:103],a[20:23],v[40:43]\n"
RD"a[116:119],v226 offset:13312\n"
MF"v[48:51],a[100:103],a[36:39],v[48:51]\n"
EX"v19,v19\n"
MF"v[56:59],a[100:103],a[52:55],v[56:59]\n"
WT"lgkmcnt(2)\n"
BL"v225,s[8:11],s44 offen offset:0 sc1 lds\n"
MF"v[32:35],a[104:107],a[8:11],v[32:35]\n"
SA"s40,s40,32768\n"
EX"v24,v24\n"
MF"v[40:43],a[104:107],a[24:27],v[40:43]\n"
"s_cmp_eq_u32 s40,s46\n"
RD"a[120:123],v226 offset:14336\n"
PK"v17,v18,v19\n"
MF"v[48:51],a[104:107],a[40:43],v[48:51]\n"
"s_cselect_b32 s40,s47,s40\n"
EX"v25,v25\n"
MF"v[56:59],a[104:107],a[56:59],v[56:59]\n"
PK"v24,v24,v25\n"
MF"v[32:35],a[108:111],a[12:15],v[32:35]\n"
EX"v26,v26\n"
MF"v[40:43],a[108:111],a[28:31],v[40:43]\n"
RD"a[124:127],v226 offset:15360\n"
MF"v[48:51],a[108:111],a[44:47],v[48:51]\n"
EX"v27,v27\n"
MF"v[56:59],a[108:111],a[60:63],v[56:59]\n"
WT"lgkmcnt(2)\n"
BL"v225,s[8:11],s44 offen offset:1024 sc1 lds\n"
MF"v[36:39],a[112:115],a[0:3],v[220:223]\n"
EX"v4,v4\n"
MF"v[44:47],a[112:115],a[16:19],0\n"
RD"a[64:67],v227 offset:16384\n"
PK"v25,v26,v27\n"
MF"v[52:55],a[112:115],a[32:35],0\n"
EX"v5,v5\n"
MF"v[60:63],a[112:115],a[48:51],0\n"
PK"v2,v4,v5\n"
MF"v[36:39],a[116:119],a[4:7],v[36:39]\n"
EX"v6,v6\n"
MF"v[44:47],a[116:119],a[20:23],v[44:47]\n"
RD"a[68:71],v227 offset:17408\n"
MF"v[52:55],a[116:119],a[36:39],v[52:55]\n"
EX"v7,v7\n"
MF"v[60:63],a[116:119],a[52:55],v[60:63]\n"
WT"lgkmcnt(2)\n"
BL"v225,s[8:11],s44 offen offset:2048 sc1 lds\n"
MF"v[36:39],a[120:123],a[8:11],v[36:39]\n"
EX"v12,v12\n"
MF"v[44:47],a[120:123],a[24:27],v[44:47]\n"
RD"a[72:75],v227 offset:18432\n"
PK"v3,v6,v7\n"
MF"v[52:55],a[120:123],a[40:43],v[52:55]\n"
EX"v13,v13\n"
MF"v[60:63],a[120:123],a[56:59],v[60:63]\n"
PK"v10,v12,v13\n"
MF"v[36:39],a[124:127],a[12:15],v[36:39]\n"
EX"v14,v14\n"
MF"v[44:47],a[124:127],a[28:31],v[44:47]\n"
RD"a[76:79],v227 offset:19456\n"
MF"v[52:55],a[124:127],a[44:47],v[52:55]\n"
EX"v15,v15\n"
MF"v[60:63],a[124:127],a[60:63],v[60:63]\n"
WT"lgkmcnt(2)\n"
BL"v225,s[8:11],s44 offen offset:3072 sc1 lds\n"
MF"v[80:83],a[64:67],v[64:67],v[80:83]\n"
SA"s41,s41,32768\n"
EX"v20,v20\n"
MF"v[112:115],a[64:67],v[68:71],v[112:115]\n"
"s_cmp_eq_u32 s41,163840\n"
RD"a[80:83],v227 offset:20480\n"
PK"v11,v14,v15\n"
MF"v[144:147],a[64:67],v[72:75],v[144:147]\n"
"s_cselect_b32 s41,0,s41\n"
EX"v21,v21\n"
MF"v[176:179],a[64:67],v[76:79],v[176:179]\n"
"v_add_u32 v226,s41,v224\n"
PK"v18,v20,v21\n"
MF"v[84:87],a[68:71],v[64:67],v[84:87]\n"
EX"v22,v22\n"
MF"v[116:119],a[68:71],v[68:71],v[116:119]\n"
RD"a[84:87],v227 offset:21504\n"
MF"v[148:151],a[68:71],v[72:75],v[148:151]\n"
EX"v23,v23\n"
MF"v[180:183],a[68:71],v[76:79],v[180:183]\n"
WT"lgkmcnt(2)\n"
PK"v19,v22,v23\n"
MF"v[88:91],a[72:75],v[64:67],v[88:91]\n"
EX"v28,v28\n"
MF"v[120:123],a[72:75],v[68:71],v[120:123]\n"
RD"a[88:91],v227 offset:22528\n"
MF"v[152:155],a[72:75],v[72:75],v[152:155]\n"
EX"v29,v29\n"
MF"v[184:187],a[72:75],v[76:79],v[184:187]\n"
PK"v26,v28,v29\n"
MF"v[92:95],a[76:79],v[64:67],v[92:95]\n"
EX"v30,v30\n"
MF"v[124:127],a[76:79],v[68:71],v[124:127]\n"
RD"a[92:95],v227 offset:23552\n"
MF"v[156:159],a[76:79],v[72:75],v[156:159]\n"
EX"v31,v31\n"
MF"v[188:191],a[76:79],v[76:79],v[188:191]\n"
WT"lgkmcnt(2)\n"
PK"v27,v30,v31\n"
MF"v[96:99],a[80:83],v[64:67],v[96:99]\n"
EX"v32,v32\n"
MF"v[128:131],a[80:83],v[68:71],v[128:131]\n"
RD"a[96:99],v227 offset:24576\n"
MF"v[160:163],a[80:83],v[72:75],v[160:163]\n"
EX"v33,v33\n"
MF"v[192:195],a[80:83],v[76:79],v[192:195]\n"
MF"v[100:103],a[84:87],v[64:67],v[100:103]\n"
EX"v34,v34\n"
MF"v[132:135],a[84:87],v[68:71],v[132:135]\n"
RD"a[100:103],v227 offset:25600\n"
MF"v[164:167],a[84:87],v[72:75],v[164:167]\n"
EX"v35,v35\n"
MF"v[196:199],a[84:87],v[76:79],v[196:199]\n"
WT"lgkmcnt(2)\n"
MF"v[104:107],a[88:91],v[64:67],v[104:107]\n"
EX"v40,v40\n"
MF"v[136:139],a[88:91],v[68:71],v[136:139]\n"
RD"a[104:107],v227 offset:26624\n"
MF"v[168:171],a[88:91],v[72:75],v[168:171]\n"
EX"v41,v41\n"
MF"v[200:203],a[88:91],v[76:79],v[200:203]\n"
MF"v[108:111],a[92:95],v[64:67],v[108:111]\n"
EX"v42,v42\n"
MF"v[140:143],a[92:95],v[68:71],v[140:143]\n"
RD"a[108:111],v227 offset:27648\n"
MF"v[172:175],a[92:95],v[72:75],v[172:175]\n"
EX"v43,v43\n"
MF"v[204:207],a[92:95],v[76:79],v[204:207]\n"
MF"a[132:135],a[128:131],v[64:67],a[132:135]\n"
EX"v48,v48\n"
MF"a[136:139],a[128:131],v[68:71],a[136:139]\n"
RD"a[112:115],v227 offset:28672\n"
PK"v64,v32,v33\n"
MF"a[140:143],a[128:131],v[72:75],a[140:143]\n"
EX"v49,v49\n"
MF"a[144:147],a[128:131],v[76:79],a[144:147]\n"
WT"lgkmcnt(2)\n"
PK"v65,v34,v35\n"
PK"v68,v40,v41\n"
MF"v[80:83],a[96:99],v[0:3],v[80:83]\n"
EX"v50,v50\n"
MF"v[112:115],a[96:99],v[8:11],v[112:115]\n"
RD"a[116:119],v227 offset:29696\n"
PK"v69,v42,v43\n"
MF"v[144:147],a[96:99],v[16:19],v[144:147]\n"
EX"v51,v51\n"
MF"v[176:179],a[96:99],v[24:27],v[176:179]\n"
PK"v72,v48,v49\n"
PK"v73,v50,v51\n"
MF"v[84:87],a[100:103],v[0:3],v[84:87]\n"
EX"v36,v36\n"
MF"v[116:119],a[100:103],v[8:11],v[116:119]\n"
RD"a[120:123],v227 offset:30720\n"
MF"v[148:151],a[100:103],v[16:19],v[148:151]\n"
EX"v37,v37\n"
MF"v[180:183],a[100:103],v[24:27],v[180:183]\n"
PK"v66,v36,v37\n"
MF"v[88:91],a[104:107],v[0:3],v[88:91]\n"
EX"v38,v38\n"
MF"v[120:123],a[104:107],v[8:11],v[120:123]\n"
RD"a[124:127],v227 offset:31744\n"
MF"v[152:155],a[104:107],v[16:19],v[152:155]\n"
EX"v39,v39\n"
MF"v[184:187],a[104:107],v[24:27],v[184:187]\n"
WT"lgkmcnt(2)\n"
PK"v67,v38,v39\n"
MF"v[92:95],a[108:111],v[0:3],v[92:95]\n"
EX"v44,v44\n"
MF"v[124:127],a[108:111],v[8:11],v[124:127]\n"
MF"v[156:159],a[108:111],v[16:19],v[156:159]\n"
EX"v45,v45\n"
MF"v[188:191],a[108:111],v[24:27],v[188:191]\n"
PK"v70,v44,v45\n"
MF"v[96:99],a[112:115],v[0:3],v[96:99]\n"
EX"v46,v46\n"
MF"v[128:131],a[112:115],v[8:11],v[128:131]\n"
MF"v[160:163],a[112:115],v[16:19],v[160:163]\n"
EX"v47,v47\n"
MF"v[192:195],a[112:115],v[24:27],v[192:195]\n"
PK"v71,v46,v47\n"
MF"v[100:103],a[116:119],v[0:3],v[100:103]\n"
WT"vmcnt(16) lgkmcnt(0)\n"
"s_barrier\n"
MF"v[132:135],a[116:119],v[8:11],v[132:135]\n"
RD"a[64:67],v226 offset:0\n"
MF"v[164:167],a[116:119],v[16:19],v[164:167]\n"
EX"v52,v52\n"
MF"v[196:199],a[116:119],v[24:27],v[196:199]\n"
RD"a[68:71],v226 offset:1024\n"
MF"v[104:107],a[120:123],v[0:3],v[104:107]\n"
EX"v53,v53\n"
MF"v[136:139],a[120:123],v[8:11],v[136:139]\n"
RD"a[72:75],v226 offset:2048\n"
PK"v74,v52,v53\n"
MF"v[168:171],a[120:123],v[16:19],v[168:171]\n"
EX"v54,v54\n"
MF"v[200:203],a[120:123],v[24:27],v[200:203]\n"
MF"v[108:111],a[124:127],v[0:3],v[108:111]\n"
EX"v55,v55\n"
MF"v[140:143],a[124:127],v[8:11],v[140:143]\n"
RD"a[76:79],v226 offset:3072\n"
PK"v75,v54,v55\n"
MF"v[172:175],a[124:127],v[16:19],v[172:175]\n"
MF"v[204:207],a[124:127],v[24:27],v[204:207]\n"
RD"a[80:83],v226 offset:4096\n"
MF"a[132:135],a[128:131],v[0:3],a[132:135]\n"
MF"a[136:139],a[128:131],v[8:11],a[136:139]\n"
RD"a[84:87],v226 offset:5120\n"
MF"a[140:143],a[128:131],v[16:19],a[140:143]\n"
MF"a[144:147],a[128:131],v[24:27],a[144:147]\n"
"LDB_%=:\n"
WT"lgkmcnt(5)\n"
MF"v[8:11],a[64:67],a[16:19],v[208:211]\n"
EX"v56,v56\n"
MF"v[16:19],a[64:67],a[32:35],0\n"
"v_readlane_b32 s43,v228,s42\n"
MF"v[24:27],a[64:67],a[48:51],0\n"
WT"lgkmcnt(4)\n"
EX"v57,v57\n"
MF"v[8:11],a[68:71],a[20:23],v[8:11]\n"
"s_lshl_b32 s50,s35,8\n"
RD"a[88:91],v226 offset:6144\n"
PK"v76,v56,v57\n"
MF"v[16:19],a[68:71],a[36:39],v[16:19]\n"
SA"s50,s50,81264640\n"
EX"v58,v58\n"
MF"v[24:27],a[68:71],a[52:55],v[24:27]\n"
WT"lgkmcnt(1)\n"
RD"a[92:95],v226 offset:7168\n"
"v_readlane_b32 s44,v229,s42\n"
MF"v[8:11],a[72:75],a[24:27],v[8:11]\n"
"s_mov_b32 m0,s40\n"
EX"v59,v59\n"
MF"v[16:19],a[72:75],a[40:43],v[16:19]\n"
"s_cmp_ge_u32 s34,6\n"
BL"v225,s[8:11],s43 offen offset:0 sc1 lds\n"
"v_mov_b32 v227,v226\n"
MF"v[24:27],a[72:75],a[56:59],v[24:27]\n"
"s_cselect_b32 s50,s50,256\n"
EX"v60,v60\n"
"v_mov_b32 v237,0\n"
MF"v[8:11],a[76:79],a[28:31],v[8:11]\n"
SA"s42,s42,1\n"
RD"a[96:99],v226 offset:8192\n"
PK"v77,v58,v59\n"
MF"v[16:19],a[76:79],a[44:47],v[16:19]\n"
EX"v61,v61\n"
MF"v[24:27],a[76:79],a[60:63],v[24:27]\n"
RD"a[100:103],v226 offset:9216\n"
PK"v78,v60,v61\n"
MF"v[12:15],a[80:83],a[16:19],v[212:215]\n"
EX"v62,v62\n"
MF"v[20:23],a[80:83],a[32:35],0\n"
"buffer_load_dwordx2 v[238:239],v237,s[8:11],s50 offen sc1\n"
MF"v[28:31],a[80:83],a[48:51],0\n"
EX"v63,v63\n"
MF"v[12:15],a[84:87],a[20:23],v[12:15]\n"
BL"v225,s[8:11],s43 offen offset:1024 sc1 lds\n"
"v_lshrrev_b32 v235,7,v224\n"
MF"v[20:23],a[84:87],a[36:39],v[20:23]\n"
EX"v8,v8\n"
"v_lshrrev_b32 v230,1,v224\n"
MF"v[28:31],a[84:87],a[52:55],v[28:31]\n"
WT"lgkmcnt(1)\n"
RD"a[104:107],v226 offset:10240\n"
"v_lshl_add_u32 v230,v235,27,v230\n"
MF"v[12:15],a[88:91],a[24:27],v[12:15]\n"
EX"v9,v9\n"
PK"v79,v62,v63\n"
MF"v[20:23],a[88:91],a[40:43],v[20:23]\n"
RD"a[108:111],v226 offset:11264\n"
PK"v8,v8,v9\n"
MF"v[28:31],a[88:91],a[56:59],v[28:31]\n"
EX"v10,v10\n"
MF"v[12:15],a[92:95],a[28:31],v[12:15]\n"
RD"a[112:115],v226 offset:12288\n"
MF"v[20:23],a[92:95],a[44:47],v[20:23]\n"
EX"v11,v11\n"
MF"v[28:31],a[92:95],a[60:63],v[28:31]\n"
BL"v225,s[8:11],s43 offen offset:2048 sc1 lds\n"
MF"v[40:43],a[96:99],a[16:19],v[216:219]\n"
EX"v16,v16\n"
MF"v[48:51],a[96:99],a[32:35],0\n"
RD"a[116:119],v226 offset:13312\n"
PK"v9,v10,v11\n"
MF"v[56:59],a[96:99],a[48:51],0\n"
WT"lgkmcnt(2)\n"
EX"v17,v17\n"
MF"v[40:43],a[100:103],a[20:23],v[40:43]\n"
RD"a[120:123],v226 offset:14336\n"
PK"v16,v16,v17\n"
MF"v[48:51],a[100:103],a[36:39],v[48:51]\n"
EX"v18,v18\n"
MF"v[56:59],a[100:103],a[52:55],v[56:59]\n"
RD"a[124:127],v226 offset:15360\n"
MF"v[40:43],a[104:107],a[24:27],v[40:43]\n"
EX"v19,v19\n"
MF"v[48:51],a[104:107],a[40:43],v[48:51]\n"
BL"v225,s[8:11],s43 offen offset:3072 sc1 lds\n"
MF"v[56:59],a[104:107],a[56:59],v[56:59]\n"
EX"v24,v24\n"
MF"v[40:43],a[108:111],a[28:31],v[40:43]\n"
RD"a[64:67],v227 offset:16384\n"
PK"v17,v18,v19\n"
MF"v[48:51],a[108:111],a[44:47],v[48:51]\n"
EX"v25,v25\n"
MF"v[56:59],a[108:111],a[60:63],v[56:59]\n"
WT"lgkmcnt(2)\n"
PK"v24,v24,v25\n"
SA"m0,s40,16384\n"
MF"v[44:47],a[112:115],a[16:19],v[220:223]\n"
EX"v26,v26\n"
MF"v[52:55],a[112:115],a[32:35],0\n"
RD"a[68:71],v227 offset:17408\n"
MF"v[60:63],a[112:115],a[48:51],0\n"
EX"v27,v27\n"
MF"v[44:47],a[116:119],a[20:23],v[44:47]\n"
BL"v225,s[8:11],s44 offen offset:0 sc1 lds\n"
MF"v[52:55],a[116:119],a[36:39],v[52:55]\n"
SA"s40,s40,32768\n"
EX"v12,v12\n"
MF"v[60:63],a[116:119],a[52:55],v[60:63]\n"
"s_cmp_eq_u32 s40,s46\n"
RD"a[72:75],v227 offset:18432\n"
PK"v25,v26,v27\n"
MF"v[44:47],a[120:123],a[24:27],v[44:47]\n"
"s_cselect_b32 s40,s47,s40\n"
EX"v13,v13\n"
MF"v[52:55],a[120:123],a[40:43],v[52:55]\n"
PK"v10,v12,v13\n"
MF"v[60:63],a[120:123],a[56:59],v[60:63]\n"
WT"lgkmcnt(1)\n"
EX"v14,v14\n"
MF"v[44:47],a[124:127],a[28:31],v[44:47]\n"
RD"a[76:79],v227 offset:19456\n"
MF"v[52:55],a[124:127],a[44:47],v[52:55]\n"
EX"v15,v15\n"
MF"v[60:63],a[124:127],a[60:63],v[60:63]\n"
SA"s41,s41,32768\n"
BL"v225,s[8:11],s44 offen offset:1024 sc1 lds\n"
MF"v[80:83],a[64:67],v[64:67],v[80:83]\n"
"s_cmp_eq_u32 s41,163840\n"
EX"v20,v20\n"
MF"v[112:115],a[64:67],v[68:71],v[112:115]\n"
"s_cselect_b32 s41,0,s41\n"
RD"a[80:83],v227 offset:20480\n"
"v_add_u32 v226,s41,v224\n"
MF"v[144:147],a[64:67],v[72:75],v[144:147]\n"
EX"v21,v21\n"
PK"v11,v14,v15\n"
MF"v[176:179],a[64:67],v[76:79],v[176:179]\n"
PK"v18,v20,v21\n"
MF"v[84:87],a[68:71],v[64:67],v[84:87]\n"
EX"v22,v22\n"
MF"v[116:119],a[68:71],v[68:71],v[116:119]\n"
RD"a[84:87],v227 offset:21504\n"
MF"v[148:151],a[68:71],v[72:75],v[148:151]\n"
EX"v23,v23\n"
MF"v[180:183],a[68:71],v[76:79],v[180:183]\n"
WT"lgkmcnt(2)\n"
BL"v225,s[8:11],s44 offen offset:2048 sc1 lds\n"
MF"v[88:91],a[72:75],v[64:67],v[88:91]\n"
EX"v28,v28\n"
MF"v[120:123],a[72:75],v[68:71],v[120:123]\n"
RD"a[88:91],v227 offset:22528\n"
PK"v19,v22,v23\n"
MF"v[152:155],a[72:75],v[72:75],v[152:155]\n"
EX"v29,v29\n"
MF"v[184:187],a[72:75],v[76:79],v[184:187]\n"
PK"v26,v28,v29\n"
MF"v[92:95],a[76:79],v[64:67],v[92:95]\n"
EX"v30,v30\n"
MF"v[124:127],a[76:79],v[68:71],v[124:127]\n"
RD"a[92:95],v227 offset:23552\n"
MF"v[156:159],a[76:79],v[72:75],v[156:159]\n"
EX"v31,v31\n"
MF"v[188:191],a[76:79],v[76:79],v[188:191]\n"
WT"lgkmcnt(2)\n"
BL"v225,s[8:11],s44 offen offset:3072 sc1 lds\n"
MF"v[96:99],a[80:83],v[64:67],v[96:99]\n"
EX"v40,v40\n"
MF"v[128:131],a[80:83],v[68:71],v[128:131]\n"
RD"a[96:99],v227 offset:24576\n"
PK"v27,v30,v31\n"
MF"v[160:163],a[80:83],v[72:75],v[160:163]\n"
EX"v41,v41\n"
MF"v[192:195],a[80:83],v[76:79],v[192:195]\n"
RD"a[100:103],v227 offset:25600\n"
MF"v[100:103],a[84:87],v[64:67],v[100:103]\n"
WT"vmcnt(7)\n"
"v_cmp_eq_u64 vcc,s[28:29],v[238:239]\n"
"s_cmp_eq_u64 vcc,exec\n"
"s_cbranch_scc0 L1013_%=\n"
"L1014_%=:\n"
MF"v[132:135],a[84:87],v[68:71],v[132:135]\n"
EX"v42,v42\n"
MF"v[164:167],a[84:87],v[72:75],v[164:167]\n"
EX"v43,v43\n"
MF"v[196:199],a[84:87],v[76:79],v[196:199]\n"
WT"lgkmcnt(2)\n"
MF"v[104:107],a[88:91],v[64:67],v[104:107]\n"
EX"v48,v48\n"
MF"v[136:139],a[88:91],v[68:71],v[136:139]\n"
RD"a[104:107],v227 offset:26624\n"
MF"v[168:171],a[88:91],v[72:75],v[168:171]\n"
EX"v49,v49\n"
MF"v[200:203],a[88:91],v[76:79],v[200:203]\n"
RD"a[108:111],v227 offset:27648\n"
MF"v[108:111],a[92:95],v[64:67],v[108:111]\n"
EX"v50,v50\n"
MF"v[140:143],a[92:95],v[68:71],v[140:143]\n"
RD"a[112:115],v227 offset:28672\n"
MF"v[172:175],a[92:95],v[72:75],v[172:175]\n"
EX"v51,v51\n"
MF"v[204:207],a[92:95],v[76:79],v[204:207]\n"
MF"a[132:135],a[128:131],v[64:67],a[132:135]\n"
EX"v44,v44\n"
MF"a[136:139],a[128:131],v[68:71],a[136:139]\n"
RD"a[116:119],v227 offset:29696\n"
PK"v68,v40,v41\n"
PK"v69,v42,v43\n"
MF"a[140:143],a[128:131],v[72:75],a[140:143]\n"
EX"v45,v45\n"
PK"v72,v48,v49\n"
MF"a[144:147],a[128:131],v[76:79],a[144:147]\n"
WT"lgkmcnt(2)\n"
RD"a[120:123],v227 offset:30720\n"
PK"v73,v50,v51\n"
MF"v[112:115],a[96:99],v[8:11],v[112:115]\n"
EX"v46,v46\n"
MF"v[144:147],a[96:99],v[16:19],v[144:147]\n"
RD"a[124:127],v227 offset:31744\n"
PK"v70,v44,v45\n"
MF"v[176:179],a[96:99],v[24:27],v[176:179]\n"
EX"v47,v47\n"
MF"v[116:119],a[100:103],v[8:11],v[116:119]\n"
"v_accvgpr_read_b32 v251,a132\n"
"v_subrev_u32 v253,0x2b800000,v251\n"
MF"v[148:151],a[100:103],v[16:19],v[148:151]\n"
"v_cmp_le_u32 vcc,0x34000000,v253\n"
"s_cmp_lg_u64 vcc,0\n"
"s_cbranch_scc1 L1017_%=\n"
"L1016_%=:\n"
MF"v[180:183],a[100:103],v[24:27],v[180:183]\n"
EX"v52,v52\n"
MF"v[120:123],a[104:107],v[8:11],v[120:123]\n"
"v_rcp_f32 v251,v251\n"
PK"v71,v46,v47\n"
MF"v[152:155],a[104:107],v[16:19],v[152:155]\n"
EX"v53,v53\n"
MF"v[184:187],a[104:107],v[24:27],v[184:187]\n"
EX"v54,v54\n"
MU"v80,v251,v80\n"
MF"v[124:127],a[108:111],v[8:11],v[124:127]\n"
MU"v81,v251,v81\n"
MU"v82,v251,v82\n"
MU"v83,v251,v83\n"
MF"v[156:159],a[108:111],v[16:19],v[156:159]\n"
EX"v55,v55\n"
PK"v74,v52,v53\n"
MF"v[188:191],a[108:111],v[24:27],v[188:191]\n"
WT"lgkmcnt(0)\n"
MU"v84,v251,v84\n"
MU"v85,v251,v85\n"
MU"v86,v251,v86\n"
MF"v[128:131],a[112:115],v[8:11],v[128:131]\n"
"s_barrier\n"
MF"v[160:163],a[112:115],v[16:19],v[160:163]\n"
RD"a[64:67],v226 offset:0\n"
MU"v87,v251,v87\n"
PK"v80,v80,v81\n"
MF"v[192:195],a[112:115],v[24:27],v[192:195]\n"
PK"v81,v82,v83\n"
PK"v82,v84,v85\n"
PK"v83,v86,v87\n"
MF"v[132:135],a[116:119],v[8:11],v[132:135]\n"
BS"v[80:83],v224,s[8:11],s59 offen offset:0 sc1\n"
PK"v75,v54,v55\n"
MF"v[164:167],a[116:119],v[16:19],v[164:167]\n"
RD"a[68:71],v226 offset:1024\n"
MU"v88,v251,v88\n"
MU"v89,v251,v89\n"
MF"v[196:199],a[116:119],v[24:27],v[196:199]\n"
RD"a[72:75],v226 offset:2048\n"
MU"v90,v251,v90\n"
MU"v91,v251,v91\n"
MF"v[136:139],a[120:123],v[8:11],v[136:139]\n"
MU"v92,v251,v92\n"
MU"v93,v251,v93\n"
MU"v94,v251,v94\n"
MF"v[168:171],a[120:123],v[16:19],v[168:171]\n"
MU"v95,v251,v95\n"
PK"v88,v88,v89\n"
PK"v89,v90,v91\n"
MF"v[200:203],a[120:123],v[24:27],v[200:203]\n"
PK"v90,v92,v93\n"
PK"v91,v94,v95\n"
MU"v96,v251,v96\n"
MF"v[140:143],a[124:127],v[8:11],v[140:143]\n"
BS"v[88:91],v224,s[8:11],s59 offen offset:1024 sc1\n"
MU"v97,v251,v97\n"
MF"v[172:175],a[124:127],v[16:19],v[172:175]\n"
RD"a[76:79],v226 offset:3072\n"
MU"v98,v251,v98\n"
MU"v99,v251,v99\n"
MF"v[204:207],a[124:127],v[24:27],v[204:207]\n"
MU"v104,v251,v104\n"
MU"v105,v251,v105\n"
MU"v100,v251,v100\n"
MF"a[136:139],a[128:131],v[8:11],a[136:139]\n"
PK"v96,v96,v97\n"
MU"v106,v251,v106\n"
MU"v101,v251,v101\n"
MF"a[140:143],a[128:131],v[16:19],a[140:143]\n"
MU"v107,v251,v107\n"
MU"v102,v251,v102\n"
MU"v103,v251,v103\n"
MF"a[144:147],a[128:131],v[24:27],a[144:147]\n"
PK"v97,v98,v99\n"
MU"v108,v251,v108\n"
PK"v98,v100,v101\n"
PK"v99,v102,v103\n"
BS"v[96:99],v224,s[8:11],s59 offen offset:2048 sc1\n"
MU"v109,v251,v109\n"
MU"v110,v251,v110\n"
MU"v111,v251,v111\n"
PK"v104,v104,v105\n"
PK"v105,v106,v107\n"
PK"v106,v108,v109\n"
PK"v107,v110,v111\n"
BS"v[104:107],v224,s[8:11],s59 offen offset:3072 sc1\n"
RD"a[80:83],v226 offset:4096\n"
RD"a[84:87],v226 offset:5120\n"
"s_cmp_eq_u32 s34,7\n"
"s_cbranch_scc1 LD2_%=\n"
WT"lgkmcnt(5)\n"
MF"v[0:3],a[64:67],a[32:35],0\n"
"s_mov_b32 m0,s40\n"
EX"v56,v56\n"
MF"v[8:11],a[64:67],a[48:51],0\n"
WT"lgkmcnt(4)\n"
EX"v57,v57\n"
MF"v[0:3],a[68:71],a[36:39],v[0:3]\n"
"s_lshl_b32 s50,s35,6\n"
RD"a[88:91],v226 offset:6144\n"
"v_readlane_b32 s43,v228,s42\n"
MF"v[8:11],a[68:71],a[52:55],v[8:11]\n"
WT"lgkmcnt(4)\n"
EX"v58,v58\n"
MF"v[0:3],a[72:75],a[40:43],v[0:3]\n"
SA"s50,s50,4352\n"
BL"v225,s[8:11],s43 offen offset:0 sc1 lds\n"
"v_readlane_b32 s44,v229,s42\n"
MF"v[8:11],a[72:75],a[56:59],v[8:11]\n"
WT"lgkmcnt(1)\n"
RD"a[92:95],v226 offset:7168\n"
PK"v76,v56,v57\n"
MF"v[0:3],a[76:79],a[44:47],v[0:3]\n"
SA"s42,s42,1\n"
RD"a[96:99],v226 offset:8192\n"
"v_lshrrev_b32 v237,1,v224\n"
MF"v[8:11],a[76:79],a[60:63],v[8:11]\n"
SA"s41,s41,32768\n"
EX"v59,v59\n"
MF"v[4:7],a[80:83],a[32:35],0\n"
"s_cmp_eq_u32 s41,163840\n"
RD"a[100:103],v226 offset:9216\n"
"v_and_b32 v237,56,v237\n"
MF"v[12:15],a[80:83],a[48:51],0\n"
"s_cselect_b32 s41,0,s41\n"
EX"v60,v60\n"
"v_add_u32 v227,s41,v224\n"
MF"v[4:7],a[84:87],a[36:39],v[4:7]\n"
EX"v61,v61\n"
PK"v77,v58,v59\n"
MF"v[12:15],a[84:87],a[52:55],v[12:15]\n"
WT"lgkmcnt(3)\n"
RD"a[104:107],v226 offset:10240\n"
RD"a[108:111],v226 offset:11264\n"
MF"v[4:7],a[88:91],a[40:43],v[4:7]\n"
BL"v225,s[8:11],s43 offen offset:1024 sc1 lds\n"
MF"v[12:15],a[88:91],a[56:59],v[12:15]\n"
WT"lgkmcnt(3)\n"
EX"v62,v62\n"
PK"v78,v60,v61\n"
MF"v[4:7],a[92:95],a[44:47],v[4:7]\n"
RD"a[112:115],v226 offset:12288\n"
RD"a[116:119],v226 offset:13312\n"
MF"v[12:15],a[92:95],a[60:63],v[12:15]\n"
EX"v63,v63\n"
MF"v[16:19],a[96:99],a[32:35],0\n"
EX"v0,v0\n"
MF"v[24:27],a[96:99],a[48:51],0\n"
WT"lgkmcnt(4)\n"
EX"v1,v1\n"
PK"v79,v62,v63\n"
MF"v[16:19],a[100:103],a[36:39],v[16:19]\n"
RD"a[120:123],v226 offset:14336\n"
PK"v0,v0,v1\n"
MF"v[24:27],a[100:103],a[52:55],v[24:27]\n"
WT"lgkmcnt(3)\n"
BL"v225,s[8:11],s43 offen offset:2048 sc1 lds\n"
MF"v[16:19],a[104:107],a[40:43],v[16:19]\n"
"s_lshl_b32 s56,s34,1\n"
EX"v2,v2\n"
MF"v[24:27],a[104:107],a[56:59],v[24:27]\n"
"s_sub_u32 s56,11,s56\n"
RD"a[124:127],v226 offset:15360\n"
RD"a[64:67],v226 offset:16384\n"
MF"v[16:19],a[108:111],a[44:47],v[16:19]\n"
"s_max_i32 s56,s56,0\n"
EX"v3,v3\n"
MF"v[24:27],a[108:111],a[60:63],v[24:27]\n"
WT"lgkmcnt(3)\n"
RD"a[68:71],v226 offset:17408\n"
PK"v1,v2,v3\n"
MF"v[20:23],a[112:115],a[32:35],0\n"
"s_mov_b32 s57,s61\n"
EX"v8,v8\n"
MF"v[28:31],a[112:115],a[48:51],0\n"
EX"v9,v9\n"
MF"v[20:23],a[116:119],a[36:39],v[20:23]\n"
BL"v225,s[8:11],s43 offen offset:3072 sc1 lds\n"
MF"v[28:31],a[116:119],a[52:55],v[28:31]\n"
WT"lgkmcnt(3)\n"
RD"a[72:75],v226 offset:18432\n"
PK"v8,v8,v9\n"
MF"v[20:23],a[120:123],a[40:43],v[20:23]\n"
EX"v10,v10\n"
MF"v[28:31],a[120:123],a[56:59],v[28:31]\n"
WT"lgkmcnt(2)\n"
RD"a[76:79],v226 offset:19456\n"
RD"a[80:83],v226 offset:20480\n"
EX"v11,v11\n"
MF"v[20:23],a[124:127],a[44:47],v[20:23]\n"
SA"m0,s40,16384\n"
RD"a[84:87],v226 offset:21504\n"
PK"v9,v10,v11\n"
MF"v[28:31],a[124:127],a[60:63],v[28:31]\n"
EX"v4,v4\n"
MF"v[32:35],a[64:67],a[32:35],0\n"
"buffer_load_dwordx2 v[238:239],v237,s[8:11],s50 offen sc1\n"
MF"v[40:43],a[64:67],a[48:51],0\n"
WT"lgkmcnt(4)\n"
EX"v5,v5\n"
MF"v[32:35],a[68:71],a[36:39],v[32:35]\n"
SA"s40,s40,32768\n"
BL"v225,s[8:11],s44 offen offset:0 sc1 lds\n"
MF"v[40:43],a[68:71],a[52:55],v[40:43]\n"
WT"lgkmcnt(3)\n"
RD"a[88:91],v226 offset:22528\n"
PK"v2,v4,v5\n"
EX"v6,v6\n"
MF"v[32:35],a[72:75],a[40:43],v[32:35]\n"
"s_cmp_eq_u32 s40,s46\n"
RD"a[92:95],v226 offset:23552\n"
RD"a[96:99],v226 offset:24576\n"
MF"v[40:43],a[72:75],a[56:59],v[40:43]\n"
WT"lgkmcnt(3)\n"
EX"v7,v7\n"
MF"v[32:35],a[76:79],a[44:47],v[32:35]\n"
"s_cselect_b32 s40,s47,s40\n"
RD"a[100:103],v226 offset:25600\n"
PK"v3,v6,v7\n"
MF"v[40:43],a[76:79],a[60:63],v[40:43]\n"
EX"v12,v12\n"
MF"v[36:39],a[80:83],a[32:35],0\n"
EX"v13,v13\n"
MF"v[44:47],a[80:83],a[48:51],0\n"
BL"v225,s[8:11],s44 offen offset:1024 sc1 lds\n"
MF"v[36:39],a[84:87],a[36:39],v[36:39]\n"
EX"v14,v14\n"
MF"v[44:47],a[84:87],a[52:55],v[44:47]\n"
WT"lgkmcnt(1)\n"
RD"a[104:107],v226 offset:26624\n"
RD"a[108:111],v226 offset:27648\n"
PK"v10,v12,v13\n"
MF"v[36:39],a[88:91],a[40:43],v[36:39]\n"
EX"v15,v15\n"
MF"v[44:47],a[88:91],a[56:59],v[44:47]\n"
RD"a[112:115],v226 offset:28672\n"
RD"a[116:119],v226 offset:29696\n"
MF"v[36:39],a[92:95],a[44:47],v[36:39]\n"
EX"v16,v16\n"
PK"v11,v14,v15\n"
MF"v[44:47],a[92:95],a[60:63],v[44:47]\n"
EX"v17,v17\n"
MF"v[48:51],a[96:99],a[32:35],0\n"
BL"v225,s[8:11],s44 offen offset:2048 sc1 lds\n"
MF"v[56:59],a[96:99],a[48:51],0\n"
WT"lgkmcnt(4)\n"
EX"v18,v18\n"
MF"v[48:51],a[100:103],a[36:39],v[48:51]\n"
RD"a[120:123],v226 offset:30720\n"
PK"v16,v16,v17\n"
MF"v[56:59],a[100:103],a[52:55],v[56:59]\n"
WT"lgkmcnt(3)\n"
EX"v19,v19\n"
MF"v[48:51],a[104:107],a[40:43],v[48:51]\n"
RD"a[124:127],v226 offset:31744\n"
PK"v17,v18,v19\n"
MF"v[56:59],a[104:107],a[56:59],v[56:59]\n"
EX"v24,v24\n"
MF"v[48:51],a[108:111],a[44:47],v[48:51]\n"
WT"vmcnt(16)\n"
"s_barrier\n"
MF"v[56:59],a[108:111],a[60:63],v[56:59]\n"
WT"lgkmcnt(2)\n"
EX"v25,v25\n"
MF"v[52:55],a[112:115],a[32:35],0\n"
RD"a[96:99],v227 offset:8192\n"
"v_readlane_b32 s43,v228,s42\n"
MF"v[60:63],a[112:115],a[48:51],0\n"
BL"v225,s[8:11],s44 offen offset:3072 sc1 lds\n"
EX"v26,v26\n"
MF"v[52:55],a[116:119],a[36:39],v[52:55]\n"
EX"v27,v27\n"
PK"v24,v24,v25\n"
MF"v[60:63],a[116:119],a[52:55],v[60:63]\n"
WT"lgkmcnt(1)\n"
RD"a[100:103],v227 offset:9216\n"
RD"a[104:107],v227 offset:10240\n"
MF"v[52:55],a[120:123],a[40:43],v[52:55]\n"
"s_mov_b32 m0,s40\n"
EX"v20,v20\n"
MF"v[60:63],a[120:123],a[56:59],v[60:63]\n"
RD"a[108:111],v227 offset:11264\n"
"v_readlane_b32 s44,v229,s42\n"
PK"v25,v26,v27\n"
MF"v[52:55],a[124:127],a[44:47],v[52:55]\n"
EX"v21,v21\n"
MF"v[60:63],a[124:127],a[60:63],v[60:63]\n"
WT"lgkmcnt(3)\n"
EX"v22,v22\n"
MF"v[112:115],a[96:99],v[68:71],v[112:115]\n"
SA"s41,s41,32768\n"
BL"v225,s[8:11],s43 offen offset:0 sc1 lds\n"
MF"v[144:147],a[96:99],v[72:75],v[144:147]\n"
"s_cmp_eq_u32 s41,163840\n"
EX"v23,v23\n"
PK"v18,v20,v21\n"
MF"v[176:179],a[96:99],v[76:79],v[176:179]\n"
WT"lgkmcnt(1)\n"
RD"a[112:115],v227 offset:12288\n"
PK"v19,v22,v23\n"
MF"v[116:119],a[100:103],v[68:71],v[116:119]\n"
"s_cselect_b32 s41,0,s41\n"
EX"v28,v28\n"
"v_add_u32 v226,s41,v224\n"
MF"v[148:151],a[100:103],v[72:75],v[148:151]\n"
SA"s42,s42,1\n"
RD"a[116:119],v227 offset:13312\n"
MF"v[180:183],a[100:103],v[76:79],v[180:183]\n"
EX"v29,v29\n"
MF"v[120:123],a[104:107],v[68:71],v[120:123]\n"
RD"a[120:123],v227 offset:14336\n"
PK"v26,v28,v29\n"
MF"v[152:155],a[104:107],v[72:75],v[152:155]\n"
EX"v30,v30\n"
MF"v[184:187],a[104:107],v[76:79],v[184:187]\n"
WT"lgkmcnt(3)\n"
BL"v225,s[8:11],s43 offen offset:1024 sc1 lds\n"
MF"v[124:127],a[108:111],v[68:71],v[124:127]\n"
EX"v31,v31\n"
MF"v[156:159],a[108:111],v[72:75],v[156:159]\n"
RD"a[124:127],v227 offset:15360\n"
PK"v27,v30,v31\n"
MF"v[188:191],a[108:111],v[76:79],v[188:191]\n"
WT"lgkmcnt(2)\n"
EX"v32,v32\n"
MF"v[128:131],a[112:115],v[68:71],v[128:131]\n"
EX"v33,v33\n"
MF"v[160:163],a[112:115],v[72:75],v[160:163]\n"
RD"a[64:67],v227 offset:16384\n"
PK"v64,v32,v33\n"
MF"v[192:195],a[112:115],v[76:79],v[192:195]\n"
BL"v225,s[8:11],s43 offen offset:2048 sc1 lds\n"
MF"v[132:135],a[116:119],v[68:71],v[132:135]\n"
EX"v34,v34\n"
MF"v[164:167],a[116:119],v[72:75],v[164:167]\n"
EX"v35,v35\n"
MF"v[196:199],a[116:119],v[76:79],v[196:199]\n"
WT"lgkmcnt(1)\n"
EX"v40,v40\n"
MF"v[136:139],a[120:123],v[68:71],v[136:139]\n"
RD"a[68:71],v227 offset:17408\n"
PK"v65,v34,v35\n"
MF"v[168:171],a[120:123],v[72:75],v[168:171]\n"
EX"v41,v41\n"
MF"v[200:203],a[120:123],v[76:79],v[200:203]\n"
RD"a[72:75],v227 offset:18432\n"
RD"a[76:79],v227 offset:19456\n"
MF"v[140:143],a[124:127],v[68:71],v[140:143]\n"
WT"vmcnt(7)\n"
"v_cmp_eq_u64 vcc,s[28:29],v[238:239]\n"
"s_cmp_eq_u64 vcc,exec\n"
"s_cbranch_scc0 L1022_%=\n"
"L1023_%=:\n"
MF"v[172:175],a[124:127],v[72:75],v[172:175]\n"
EX"v42,v42\n"
MF"v[204:207],a[124:127],v[76:79],v[204:207]\n"
RD"a[80:83],v227 offset:20480\n"
MF"a[136:139],a[128:131],v[68:71],a[136:139]\n"
BL"v225,s[8:11],s43 offen offset:3072 sc1 lds\n"
PK"v68,v40,v41\n"
MF"a[140:143],a[128:131],v[72:75],a[140:143]\n"
SA"m0,s40,16384\n"
EX"v43,v43\n"
MF"a[144:147],a[128:131],v[76:79],a[144:147]\n"
WT"lgkmcnt(3)\n"
EX"v36,v36\n"
MF"v[144:147],a[64:67],v[0:3],v[144:147]\n"
RD"a[84:87],v227 offset:21504\n"
RD"a[88:91],v227 offset:22528\n"
PK"v69,v42,v43\n"
MF"v[176:179],a[64:67],v[8:11],v[176:179]\n"
EX"v37,v37\n"
MF"v[148:151],a[68:71],v[0:3],v[148:151]\n"
SA"s51,s59,16384\n"
RD"a[92:95],v227 offset:23552\n"
"v_accvgpr_read_b32 v252,a136\n"
MF"v[180:183],a[68:71],v[8:11],v[180:183]\n"
WT"lgkmcnt(3)\n"
EX"v38,v38\n"
MF"v[152:155],a[72:75],v[0:3],v[152:155]\n"
SA"s40,s40,32768\n"
BL"v225,s[8:11],s44 offen offset:0 sc1 lds\n"
PK"v66,v36,v37\n"
MF"v[184:187],a[72:75],v[8:11],v[184:187]\n"
"s_cmp_eq_u32 s40,s46\n"
EX"v39,v39\n"
"v_subrev_u32 v253,0x2b800000,v252\n"
MF"v[156:159],a[76:79],v[0:3],v[156:159]\n"
"s_cselect_b32 s40,s47,s40\n"
EX"v44,v44\n"
MF"v[188:191],a[76:79],v[8:11],v[188:191]\n"
"v_cmp_le_u32 vcc,0x34000000,v253\n"
"s_cmp_lg_u64 vcc,0\n"
"s_cbranch_scc1 L1026_%=\n"
"L1025_%=:\n"
MF"v[160:163],a[80:83],v[0:3],v[160:163]\n"
"v_rcp_f32 v252,v252\n"
PK"v67,v38,v39\n"
MF"v[192:195],a[80:83],v[8:11],v[192:195]\n"
WT"lgkmcnt(0)\n"
RD"a[96:99],v227 offset:24576\n"
RD"a[100:103],v227 offset:25600\n"
EX"v45,v45\n"
MF"v[164:167],a[84:87],v[0:3],v[164:167]\n"
RD"a[104:107],v227 offset:26624\n"
RD"a[108:111],v227 offset:27648\n"
MU"v112,v252,v112\n"
MF"v[196:199],a[84:87],v[8:11],v[196:199]\n"
EX"v46,v46\n"
MU"v113,v252,v113\n"
MF"v[168:171],a[88:91],v[0:3],v[168:171]\n"
BL"v225,s[8:11],s44 offen offset:1024 sc1 lds\n"
MU"v114,v252,v114\n"
MF"v[200:203],a[88:91],v[8:11],v[200:203]\n"
EX"v47,v47\n"
MU"v115,v252,v115\n"
MF"v[172:175],a[92:95],v[0:3],v[172:175]\n"
EX"v48,v48\n"
MU"v116,v252,v116\n"
MF"v[204:207],a[92:95],v[8:11],v[204:207]\n"
EX"v49,v49\n"
PK"v70,v44,v45\n"
MF"a[140:143],a[128:131],v[0:3],a[140:143]\n"
RD"a[112:115],v227 offset:28672\n"
RD"a[116:119],v227 offset:29696\n"
MU"v117,v252,v117\n"
MF"a[144:147],a[128:131],v[8:11],a[144:147]\n"
WT"lgkmcnt(2)\n"
EX"v50,v50\n"
MU"v118,v252,v118\n"
MF"v[144:147],a[96:99],v[16:19],v[144:147]\n"
RD"a[120:123],v227 offset:30720\n"
RD"a[124:127],v227 offset:31744\n"
MU"v119,v252,v119\n"
MF"v[176:179],a[96:99],v[24:27],v[176:179]\n"
BL"v225,s[8:11],s44 offen offset:2048 sc1 lds\n"
PK"v112,v112,v113\n"
MF"v[148:151],a[100:103],v[16:19],v[148:151]\n"
EX"v51,v51\n"
PK"v113,v114,v115\n"
MF"v[180:183],a[100:103],v[24:27],v[180:183]\n"
EX"v52,v52\n"
PK"v114,v116,v117\n"
MF"v[152:155],a[104:107],v[16:19],v[152:155]\n"
"s_barrier\n"
MF"v[184:187],a[104:107],v[24:27],v[184:187]\n"
EX"v53,v53\n"
PK"v115,v118,v119\n"
MF"v[156:159],a[108:111],v[16:19],v[156:159]\n"
BS"v[112:115],v224,s[8:11],s51 offen offset:0 sc1\n"
RD"a[64:67],v226 offset:0\n"
MF"v[188:191],a[108:111],v[24:27],v[188:191]\n"
WT"lgkmcnt(1)\n"
EX"v54,v54\n"
PK"v71,v46,v47\n"
MF"v[160:163],a[112:115],v[16:19],v[160:163]\n"
RD"a[68:71],v226 offset:1024\n"
RD"a[72:75],v226 offset:2048\n"
MU"v120,v252,v120\n"
MF"v[192:195],a[112:115],v[24:27],v[192:195]\n"
BL"v225,s[8:11],s44 offen offset:3072 sc1 lds\n"
MU"v121,v252,v121\n"
MF"v[164:167],a[116:119],v[16:19],v[164:167]\n"
EX"v55,v55\n"
MU"v122,v252,v122\n"
MF"v[196:199],a[116:119],v[24:27],v[196:199]\n"
MU"v123,v252,v123\n"
MU"v124,v252,v124\n"
PK"v72,v48,v49\n"
MF"v[168:171],a[120:123],v[16:19],v[168:171]\n"
RD"a[76:79],v226 offset:3072\n"
MU"v125,v252,v125\n"
MU"v126,v252,v126\n"
MF"v[200:203],a[120:123],v[24:27],v[200:203]\n"
MU"v127,v252,v127\n"
PK"v120,v120,v121\n"
PK"v121,v122,v123\n"
MF"v[172:175],a[124:127],v[16:19],v[172:175]\n"
MU"v128,v252,v128\n"
MU"v129,v252,v129\n"
MU"v136,v252,v136\n"
MF"v[204:207],a[124:127],v[24:27],v[204:207]\n"
MU"v137,v252,v137\n"
MU"v130,v252,v130\n"
MU"v131,v252,v131\n"
MF"a[140:143],a[128:131],v[16:19],a[140:143]\n"
PK"v128,v128,v129\n"
MU"v138,v252,v138\n"
MU"v139,v252,v139\n"
MF"a[144:147],a[128:131],v[24:27],a[144:147]\n"
PK"v136,v136,v137\n"
MU"v132,v252,v132\n"
MU"v133,v252,v133\n"
PK"v73,v50,v51\n"
PK"v74,v52,v53\n"
PK"v75,v54,v55\n"
PK"v122,v124,v125\n"
PK"v123,v126,v127\n"
BS"v[120:123],v224,s[8:11],s51 offen offset:1024 sc1\n"
MU"v134,v252,v134\n"
MU"v135,v252,v135\n"
PK"v129,v130,v131\n"
PK"v130,v132,v133\n"
PK"v131,v134,v135\n"
BS"v[128:131],v224,s[8:11],s51 offen offset:2048 sc1\n"
MU"v140,v252,v140\n"
MU"v141,v252,v141\n"
MU"v142,v252,v142\n"
MU"v143,v252,v143\n"
PK"v137,v138,v139\n"
PK"v138,v140,v141\n"
PK"v139,v142,v143\n"
BS"v[136:139],v224,s[8:11],s51 offen offset:3072 sc1\n"
RD"a[80:83],v226 offset:4096\n"
RD"a[84:87],v226 offset:5120\n"
".p2align 6\n"
"LP2_%=:\n"
WT"lgkmcnt(5)\n"
MF"v[0:3],a[64:67],a[32:35],0\n"
EX"v56,v56\n"
MF"v[8:11],a[64:67],a[48:51],0\n"
WT"lgkmcnt(4)\n"
RD"a[88:91],v226 offset:6144\n"
"v_readlane_b32 s43,v228,s42\n"
MF"v[0:3],a[68:71],a[36:39],v[0:3]\n"
EX"v57,v57\n"
MF"v[8:11],a[68:71],a[52:55],v[8:11]\n"
WT"lgkmcnt(4)\n"
RD"a[92:95],v226 offset:7168\n"
RD"a[96:99],v226 offset:8192\n"
MF"v[0:3],a[72:75],a[40:43],v[0:3]\n"
"s_mov_b32 m0,s40\n"
EX"v58,v58\n"
MF"v[8:11],a[72:75],a[56:59],v[8:11]\n"
WT"lgkmcnt(3)\n"
RD"a[100:103],v226 offset:9216\n"
"v_readlane_b32 s44,v229,s42\n"
MF"v[0:3],a[76:79],a[44:47],v[0:3]\n"
SA"s41,s41,32768\n"
EX"v59,v59\n"
MF"v[8:11],a[76:79],a[60:63],v[8:11]\n"
"s_cmp_eq_u32 s41,163840\n"
BL"v225,s[8:11],s43 offen offset:0 sc1 lds\n"
MF"v[4:7],a[80:83],a[32:35],0\n"
"s_cselect_b32 s41,0,s41\n"
EX"v60,v60\n"
MF"v[12:15],a[80:83],a[48:51],0\n"
SA"s42,s42,1\n"
RD"a[104:107],v226 offset:10240\n"
"v_add_u32 v227,s41,v224\n"
MF"v[4:7],a[84:87],a[36:39],v[4:7]\n"
EX"v61,v61\n"
MF"v[12:15],a[84:87],a[52:55],v[12:15]\n"
WT"lgkmcnt(2)\n"
RD"a[108:111],v226 offset:11264\n"
RD"a[112:115],v226 offset:12288\n"
MF"v[4:7],a[88:91],a[40:43],v[4:7]\n"
EX"v62,v62\n"
MF"v[12:15],a[88:91],a[56:59],v[12:15]\n"
RD"a[116:119],v226 offset:13312\n"
PK"v76,v56,v57\n"
MF"v[4:7],a[92:95],a[44:47],v[4:7]\n"
EX"v63,v63\n"
MF"v[12:15],a[92:95],a[60:63],v[12:15]\n"
BL"v225,s[8:11],s43 offen offset:1024 sc1 lds\n"
MF"v[16:19],a[96:99],a[32:35],0\n"
EX"v0,v0\n"
MF"v[24:27],a[96:99],a[48:51],0\n"
WT"lgkmcnt(3)\n"
RD"a[120:123],v226 offset:14336\n"
PK"v77,v58,v59\n"
MF"v[16:19],a[100:103],a[36:39],v[16:19]\n"
EX"v1,v1\n"
MF"v[24:27],a[100:103],a[52:55],v[24:27]\n"
RD"a[124:127],v226 offset:15360\n"
RD"a[64:67],v226 offset:16384\n"
MF"v[16:19],a[104:107],a[40:43],v[16:19]\n"
EX"v2,v2\n"
MF"v[24:27],a[104:107],a[56:59],v[24:27]\n"
WT"lgkmcnt(3)\n"
RD"a[68:71],v226 offset:17408\n"
PK"v78,v60,v61\n"
MF"v[16:19],a[108:111],a[44:47],v[16:19]\n"
EX"v3,v3\n"
MF"v[24:27],a[108:111],a[60:63],v[24:27]\n"
BL"v225,s[8:11],s43 offen offset:2048 sc1 lds\n"
MF"v[20:23],a[112:115],a[32:35],0\n"
EX"v8,v8\n"
MF"v[28:31],a[112:115],a[48:51],0\n"
RD"a[72:75],v226 offset:18432\n"
PK"v79,v62,v63\n"
MF"v[20:23],a[116:119],a[36:39],v[20:23]\n"
EX"v9,v9\n"
MF"v[28:31],a[116:119],a[52:55],v[28:31]\n"
WT"lgkmcnt(2)\n"
RD"a[76:79],v226 offset:19456\n"
RD"a[80:83],v226 offset:20480\n"
MF"v[20:23],a[120:123],a[40:43],v[20:23]\n"
EX"v10,v10\n"
MF"v[28:31],a[120:123],a[56:59],v[28:31]\n"
RD"a[84:87],v226 offset:21504\n"
PK"v0,v0,v1\n"
MF"v[20:23],a[124:127],a[44:47],v[20:23]\n"
EX"v11,v11\n"
MF"v[28:31],a[124:127],a[60:63],v[28:31]\n"
BL"v225,s[8:11],s43 offen offset:3072 sc1 lds\n"
MF"v[32:35],a[64:67],a[32:35],0\n"
EX"v4,v4\n"
MF"v[40:43],a[64:67],a[48:51],0\n"
WT"lgkmcnt(3)\n"
RD"a[88:91],v226 offset:22528\n"
PK"v1,v2,v3\n"
MF"v[32:35],a[68:71],a[36:39],v[32:35]\n"
EX"v5,v5\n"
MF"v[40:43],a[68:71],a[52:55],v[40:43]\n"
SA"m0,s40,16384\n"
RD"a[92:95],v226 offset:23552\n"
RD"a[96:99],v226 offset:24576\n"
MF"v[32:35],a[72:75],a[40:43],v[32:35]\n"
EX"v6,v6\n"
MF"v[40:43],a[72:75],a[56:59],v[40:43]\n"
WT"lgkmcnt(3)\n"
RD"a[100:103],v226 offset:25600\n"
PK"v8,v8,v9\n"
MF"v[32:35],a[76:79],a[44:47],v[32:35]\n"
EX"v7,v7\n"
MF"v[40:43],a[76:79],a[60:63],v[40:43]\n"
BL"v225,s[8:11],s44 offen offset:0 sc1 lds\n"
MF"v[36:39],a[80:83],a[32:35],0\n"
SA"s40,s40,32768\n"
EX"v12,v12\n"
MF"v[44:47],a[80:83],a[48:51],0\n"
"s_cmp_eq_u32 s40,s46\n"
RD"a[104:107],v226 offset:26624\n"
PK"v9,v10,v11\n"
MF"v[36:39],a[84:87],a[36:39],v[36:39]\n"
"s_cselect_b32 s40,s47,s40\n"
EX"v13,v13\n"
MF"v[44:47],a[84:87],a[52:55],v[44:47]\n"
WT"lgkmcnt(2)\n"
RD"a[108:111],v226 offset:27648\n"
RD"a[112:115],v226 offset:28672\n"
MF"v[36:39],a[88:91],a[40:43],v[36:39]\n"
EX"v14,v14\n"
MF"v[44:47],a[88:91],a[56:59],v[44:47]\n"
RD"a[116:119],v226 offset:29696\n"
PK"v2,v4,v5\n"
MF"v[36:39],a[92:95],a[44:47],v[36:39]\n"
EX"v15,v15\n"
MF"v[44:47],a[92:95],a[60:63],v[44:47]\n"
BL"v225,s[8:11],s44 offen offset:1024 sc1 lds\n"
MF"v[48:51],a[96:99],a[32:35],0\n"
EX"v16,v16\n"
MF"v[56:59],a[96:99],a[48:51],0\n"
WT"lgkmcnt(3)\n"
RD"a[120:123],v226 offset:30720\n"
PK"v3,v6,v7\n"
MF"v[48:51],a[100:103],a[36:39],v[48:51]\n"
EX"v17,v17\n"
MF"v[56:59],a[100:103],a[52:55],v[56:59]\n"
RD"a[124:127],v226 offset:31744\n"
PK"v10,v12,v13\n"
MF"v[48:51],a[104:107],a[40:43],v[48:51]\n"
WT"vmcnt(14)\n"
"s_barrier\n"
MF"v[56:59],a[104:107],a[56:59],v[56:59]\n"
WT"lgkmcnt(2)\n"
RD"a[64:67],v227 offset:0\n"
PK"v11,v14,v15\n"
MF"v[48:51],a[108:111],a[44:47],v[48:51]\n"
EX"v18,v18\n"
MF"v[56:59],a[108:111],a[60:63],v[56:59]\n"
BL"v225,s[8:11],s44 offen offset:2048 sc1 lds\n"
MF"v[52:55],a[112:115],a[32:35],0\n"
EX"v19,v19\n"
MF"v[60:63],a[112:115],a[48:51],0\n"
RD"a[68:71],v227 offset:1024\n"
PK"v16,v16,v17\n"
MF"v[52:55],a[116:119],a[36:39],v[52:55]\n"
EX"v24,v24\n"
MF"v[60:63],a[116:119],a[52:55],v[60:63]\n"
WT"lgkmcnt(2)\n"
RD"a[72:75],v227 offset:2048\n"
RD"a[76:79],v227 offset:3072\n"
MF"v[52:55],a[120:123],a[40:43],v[52:55]\n"
EX"v25,v25\n"
MF"v[60:63],a[120:123],a[56:59],v[60:63]\n"
RD"a[80:83],v227 offset:4096\n"
PK"v17,v18,v19\n"
MF"v[52:55],a[124:127],a[44:47],v[52:55]\n"
EX"v26,v26\n"
MF"v[60:63],a[124:127],a[60:63],v[60:63]\n"
WT"lgkmcnt(4)\n"
BL"v225,s[8:11],s44 offen offset:3072 sc1 lds\n"
MF"v[144:147],a[64:67],v[64:67],v[144:147]\n"
SA"s41,s41,32768\n"
EX"v27,v27\n"
MF"v[176:179],a[64:67],v[68:71],v[176:179]\n"
WT"lgkmcnt(3)\n"
RD"a[84:87],v227 offset:5120\n"
"v_readlane_b32 s43,v228,s42\n"
MF"v[148:151],a[68:71],v[64:67],v[148:151]\n"
"s_cmp_eq_u32 s41,163840\n"
EX"v20,v20\n"
MF"v[180:183],a[68:71],v[68:71],v[180:183]\n"
WT"lgkmcnt(2)\n"
RD"a[88:91],v227 offset:6144\n"
RD"a[92:95],v227 offset:7168\n"
MF"v[152:155],a[72:75],v[64:67],v[152:155]\n"
"s_cselect_b32 s41,0,s41\n"
EX"v21,v21\n"
MF"v[184:187],a[72:75],v[68:71],v[184:187]\n"
"s_mov_b32 m0,s40\n"
RD"a[96:99],v227 offset:8192\n"
"v_add_u32 v226,s41,v224\n"
MF"v[156:159],a[76:79],v[64:67],v[156:159]\n"
EX"v22,v22\n"
MF"v[188:191],a[76:79],v[68:71],v[188:191]\n"
WT"lgkmcnt(4)\n"
BL"v225,s[8:11],s43 offen offset:0 sc1 lds\n"
MF"v[160:163],a[80:83],v[64:67],v[160:163]\n"
EX"v23,v23\n"
MF"v[192:195],a[80:83],v[68:71],v[192:195]\n"
WT"lgkmcnt(3)\n"
RD"a[100:103],v227 offset:9216\n"
"v_readlane_b32 s44,v229,s42\n"
MF"v[164:167],a[84:87],v[64:67],v[164:167]\n"
SA"s42,s42,1\n"
EX"v28,v28\n"
MF"v[196:199],a[84:87],v[68:71],v[196:199]\n"
WT"lgkmcnt(2)\n"
RD"a[104:107],v227 offset:10240\n"
RD"a[108:111],v227 offset:11264\n"
MF"v[168:171],a[88:91],v[64:67],v[168:171]\n"
EX"v29,v29\n"
MF"v[200:203],a[88:91],v[68:71],v[200:203]\n"
RD"a[112:115],v227 offset:12288\n"
PK"v24,v24,v25\n"
MF"v[172:175],a[92:95],v[64:67],v[172:175]\n"
EX"v30,v30\n"
MF"v[204:207],a[92:95],v[68:71],v[204:207]\n"
BL"v225,s[8:11],s43 offen offset:1024 sc1 lds\n"
MF"a[140:143],a[128:131],v[64:67],a[140:143]\n"
EX"v31,v31\n"
MF"a[144:147],a[128:131],v[68:71],a[144:147]\n"
WT"lgkmcnt(3)\n"
RD"a[116:119],v227 offset:13312\n"
PK"v25,v26,v27\n"
MF"v[144:147],a[96:99],v[72:75],v[144:147]\n"
EX"v32,v32\n"
MF"v[176:179],a[96:99],v[76:79],v[176:179]\n"
RD"a[120:123],v227 offset:14336\n"
RD"a[124:127],v227 offset:15360\n"
MF"v[148:151],a[100:103],v[72:75],v[148:151]\n"
EX"v33,v33\n"
MF"v[180:183],a[100:103],v[76:79],v[180:183]\n"
WT"lgkmcnt(3)\n"
PK"v18,v20,v21\n"
PK"v19,v22,v23\n"
MF"v[152:155],a[104:107],v[72:75],v[152:155]\n"
EX"v34,v34\n"
MF"v[184:187],a[104:107],v[76:79],v[184:187]\n"
BL"v225,s[8:11],s43 offen offset:2048 sc1 lds\n"
MF"v[156:159],a[108:111],v[72:75],v[156:159]\n"
EX"v35,v35\n"
MF"v[188:191],a[108:111],v[76:79],v[188:191]\n"
RD"a[64:67],v227 offset:16384\n"
PK"v26,v28,v29\n"
MF"v[160:163],a[112:115],v[72:75],v[160:163]\n"
EX"v40,v40\n"
MF"v[192:195],a[112:115],v[76:79],v[192:195]\n"
WT"lgkmcnt(1)\n"
RD"a[68:71],v227 offset:17408\n"
RD"a[72:75],v227 offset:18432\n"
MF"v[164:167],a[116:119],v[72:75],v[164:167]\n"
EX"v41,v41\n"
MF"v[196:199],a[116:119],v[76:79],v[196:199]\n"
RD"a[76:79],v227 offset:19456\n"
PK"v27,v30,v31\n"
MF"v[168:171],a[120:123],v[72:75],v[168:171]\n"
EX"v42,v42\n"
MF"v[200:203],a[120:123],v[76:79],v[200:203]\n"
BL"v225,s[8:11],s43 offen offset:3072 sc1 lds\n"
MF"v[172:175],a[124:127],v[72:75],v[172:175]\n"
EX"v43,v43\n"
MF"v[204:207],a[124:127],v[76:79],v[204:207]\n"
RD"a[80:83],v227 offset:20480\n"
PK"v64,v32,v33\n"
MF"a[140:143],a[128:131],v[72:75],a[140:143]\n"
EX"v36,v36\n"
MF"a[144:147],a[128:131],v[76:79],a[144:147]\n"
WT"lgkmcnt(2)\n"
RD"a[84:87],v227 offset:21504\n"
RD"a[88:91],v227 offset:22528\n"
MF"v[144:147],a[64:67],v[0:3],v[144:147]\n"
SA"m0,s40,16384\n"
EX"v37,v37\n"
MF"v[176:179],a[64:67],v[8:11],v[176:179]\n"
RD"a[92:95],v227 offset:23552\n"
PK"v65,v34,v35\n"
MF"v[148:151],a[68:71],v[0:3],v[148:151]\n"
EX"v38,v38\n"
MF"v[180:183],a[68:71],v[8:11],v[180:183]\n"
BL"v225,s[8:11],s44 offen offset:0 sc1 lds\n"
MF"v[152:155],a[72:75],v[0:3],v[152:155]\n"
SA"s40,s40,32768\n"
EX"v39,v39\n"
MF"v[184:187],a[72:75],v[8:11],v[184:187]\n"
WT"lgkmcnt(3)\n"
RD"a[96:99],v227 offset:24576\n"
PK"v68,v40,v41\n"
MF"v[156:159],a[76:79],v[0:3],v[156:159]\n"
"s_cmp_eq_u32 s40,s46\n"
EX"v44,v44\n"
MF"v[188:191],a[76:79],v[8:11],v[188:191]\n"
"s_cselect_b32 s40,s47,s40\n"
RD"a[100:103],v227 offset:25600\n"
PK"v69,v42,v43\n"
MF"v[160:163],a[80:83],v[0:3],v[160:163]\n"
EX"v45,v45\n"
MF"v[192:195],a[80:83],v[8:11],v[192:195]\n"
WT"lgkmcnt(2)\n"
RD"a[104:107],v227 offset:26624\n"
PK"v66,v36,v37\n"
MF"v[164:167],a[84:87],v[0:3],v[164:167]\n"
EX"v46,v46\n"
MF"v[196:199],a[84:87],v[8:11],v[196:199]\n"
BL"v225,s[8:11],s44 offen offset:1024 sc1 lds\n"
MF"v[168:171],a[88:91],v[0:3],v[168:171]\n"
EX"v47,v47\n"
MF"v[200:203],a[88:91],v[8:11],v[200:203]\n"
RD"a[108:111],v227 offset:27648\n"
PK"v67,v38,v39\n"
MF"v[172:175],a[92:95],v[0:3],v[172:175]\n"
EX"v48,v48\n"
MF"v[204:207],a[92:95],v[8:11],v[204:207]\n"
RD"a[112:115],v227 offset:28672\n"
RD"a[116:119],v227 offset:29696\n"
MF"a[140:143],a[128:131],v[0:3],a[140:143]\n"
EX"v49,v49\n"
MF"a[144:147],a[128:131],v[8:11],a[144:147]\n"
WT"lgkmcnt(3)\n"
RD"a[120:123],v227 offset:30720\n"
PK"v70,v44,v45\n"
MF"v[144:147],a[96:99],v[16:19],v[144:147]\n"
EX"v50,v50\n"
MF"v[176:179],a[96:99],v[24:27],v[176:179]\n"
BL"v225,s[8:11],s44 offen offset:2048 sc1 lds\n"
MF"v[148:151],a[100:103],v[16:19],v[148:151]\n"
EX"v51,v51\n"
MF"v[180:183],a[100:103],v[24:27],v[180:183]\n"
RD"a[124:127],v227 offset:31744\n"
PK"v71,v46,v47\n"
MF"v[152:155],a[104:107],v[16:19],v[152:155]\n"
WT"vmcnt(15)\n"
"s_barrier\n"
MF"v[184:187],a[104:107],v[24:27],v[184:187]\n"
WT"lgkmcnt(2)\n"
RD"a[64:67],v226 offset:0\n"
PK"v72,v48,v49\n"
MF"v[156:159],a[108:111],v[16:19],v[156:159]\n"
EX"v52,v52\n"
MF"v[188:191],a[108:111],v[24:27],v[188:191]\n"
RD"a[68:71],v226 offset:1024\n"
PK"v73,v50,v51\n"
MF"v[160:163],a[112:115],v[16:19],v[160:163]\n"
EX"v53,v53\n"
MF"v[192:195],a[112:115],v[24:27],v[192:195]\n"
BL"v225,s[8:11],s44 offen offset:3072 sc1 lds\n"
MF"v[164:167],a[116:119],v[16:19],v[164:167]\n"
EX"v54,v54\n"
MF"v[196:199],a[116:119],v[24:27],v[196:199]\n"
WT"lgkmcnt(2)\n"
RD"a[72:75],v226 offset:2048\n"
PK"v74,v52,v53\n"
MF"v[168:171],a[120:123],v[16:19],v[168:171]\n"
EX"v55,v55\n"
MF"v[200:203],a[120:123],v[24:27],v[200:203]\n"
"buffer_store_dwordx2 v[232:233],v230,s[8:11],s57 offen sc1\n"
MF"v[172:175],a[124:127],v[16:19],v[172:175]\n"
"s_mov_b32 s57,2130706432\n"
RD"a[76:79],v226 offset:3072\n"
PK"v75,v54,v55\n"
MF"v[204:207],a[124:127],v[24:27],v[204:207]\n"
RD"a[80:83],v226 offset:4096\n"
MF"a[140:143],a[128:131],v[16:19],a[140:143]\n"
RD"a[84:87],v226 offset:5120\n"
MF"a[144:147],a[128:131],v[24:27],a[144:147]\n"
"s_sub_u32 s56,s56,1\n"
"s_cbranch_scc0 LP2_%=\n"
"s_cmp_eq_u32 s34,6\n"
"s_cbranch_scc1 LE2_%=\n"
WT"lgkmcnt(5)\n"
MF"v[0:3],a[64:67],a[32:35],0\n"
EX"v56,v56\n"
MF"v[8:11],a[64:67],a[48:51],0\n"
WT"lgkmcnt(4)\n"
RD"a[88:91],v226 offset:6144\n"
"v_readlane_b32 s43,v228,s42\n"
MF"v[0:3],a[68:71],a[36:39],v[0:3]\n"
EX"v57,v57\n"
MF"v[8:11],a[68:71],a[52:55],v[8:11]\n"
WT"lgkmcnt(4)\n"
RD"a[92:95],v226 offset:7168\n"
RD"a[96:99],v226 offset:8192\n"
MF"v[0:3],a[72:75],a[40:43],v[0:3]\n"
"s_andn2_b32 s63,s61,255\n"
EX"v58,v58\n"
MF"v[8:11],a[72:75],a[56:59],v[8:11]\n"
WT"lgkmcnt(3)\n"
RD"a[100:103],v226 offset:9216\n"
"v_bfe_u32 v238,v224,4,2\n"
MF"v[0:3],a[76:79],a[44:47],v[0:3]\n"
"s_mov_b32 m0,s40\n"
EX"v59,v59\n"
MF"v[8:11],a[76:79],a[60:63],v[8:11]\n"
"s_lshl_b32 s48,s35,8\n"
BL"v225,s[8:11],s43 offen offset:0 sc1 lds\n"
MF"v[4:7],a[80:83],a[32:35],0\n"
SA"s48,s48,81264640\n"
EX"v60,v60\n"
MF"v[12:15],a[80:83],a[48:51],0\n"
"s_sub_u32 s48,s48,s63\n"
RD"a[104:107],v226 offset:10240\n"
"v_lshlrev_b32 v238,6,v238\n"
MF"v[4:7],a[84:87],a[36:39],v[4:7]\n"
SA"s41,s41,32768\n"
EX"v61,v61\n"
MF"v[12:15],a[84:87],a[52:55],v[12:15]\n"
WT"lgkmcnt(2)\n"
RD"a[108:111],v226 offset:11264\n"
RD"a[112:115],v226 offset:12288\n"
PK"v76,v56,v57\n"
MF"v[4:7],a[88:91],a[40:43],v[4:7]\n"
"s_cmp_eq_u32 s41,163840\n"
EX"v62,v62\n"
"v_mov_b32 v239,s48\n"
MF"v[12:15],a[88:91],a[56:59],v[12:15]\n"
"s_cselect_b32 s41,0,s41\n"
RD"a[116:119],v226 offset:13312\n"
"v_cmp_lt_u32 vcc,511,v224\n"
"v_add_u32 v227,s41,v224\n"
MF"v[4:7],a[92:95],a[44:47],v[4:7]\n"
EX"v63,v63\n"
"v_cndmask_b32 v238,v238,v239,vcc\n"
MF"v[12:15],a[92:95],a[60:63],v[12:15]\n"
"buffer_load_dwordx2 v[240:241],v238,s[8:11],s63 offen sc1\n"
"v_readlane_b32 s44,v229,s42\n"
MF"v[16:19],a[96:99],a[32:35],0\n"
SA"s42,s42,1\n"
BL"v225,s[8:11],s43 offen offset:1024 sc1 lds\n"
PK"v77,v58,v59\n"
MF"v[24:27],a[96:99],a[48:51],0\n"
WT"lgkmcnt(3)\n"
EX"v0,v0\n"
MF"v[16:19],a[100:103],a[36:39],v[16:19]\n"
RD"a[120:123],v226 offset:14336\n"
PK"v78,v60,v61\n"
MF"v[24:27],a[100:103],a[52:55],v[24:27]\n"
EX"v1,v1\n"
MF"v[16:19],a[104:107],a[40:43],v[16:19]\n"
RD"a[124:127],v226 offset:15360\n"
RD"a[64:67],v226 offset:16384\n"
MF"v[24:27],a[104:107],a[56:59],v[24:27]\n"
WT"lgkmcnt(3)\n"
EX"v2,v2\n"
MF"v[16:19],a[108:111],a[44:47],v[16:19]\n"
RD"a[68:71],v226 offset:17408\n"
PK"v79,v62,v63\n"
MF"v[24:27],a[108:111],a[60:63],v[24:27]\n"
EX"v3,v3\n"
MF"v[20:23],a[112:115],a[32:35],0\n"
BL"v225,s[8:11],s43 offen offset:2048 sc1 lds\n"
MF"v[28:31],a[112:115],a[48:51],0\n"
EX"v8,v8\n"
MF"v[20:23],a[116:119],a[36:39],v[20:23]\n"
RD"a[72:75],v226 offset:18432\n"
PK"v0,v0,v1\n"
MF"v[28:31],a[116:119],a[52:55],v[28:31]\n"
WT"lgkmcnt(2)\n"
EX"v9,v9\n"
MF"v[20:23],a[120:123],a[40:43],v[20:23]\n"
RD"a[76:79],v226 offset:19456\n"
RD"a[80:83],v226 offset:20480\n"
MF"v[28:31],a[120:123],a[56:59],v[28:31]\n"
EX"v10,v10\n"
MF"v[20:23],a[124:127],a[44:47],v[20:23]\n"
RD"a[84:87],v226 offset:21504\n"
PK"v1,v2,v3\n"
MF"v[28:31],a[124:127],a[60:63],v[28:31]\n"
EX"v11,v11\n"
MF"v[32:35],a[64:67],a[32:35],0\n"
BL"v225,s[8:11],s43 offen offset:3072 sc1 lds\n"
MF"v[40:43],a[64:67],a[48:51],0\n"
WT"lgkmcnt(3)\n"
EX"v4,v4\n"
MF"v[32:35],a[68:71],a[36:39],v[32:35]\n"
RD"a[88:91],v226 offset:22528\n"
PK"v8,v8,v9\n"
MF"v[40:43],a[68:71],a[52:55],v[40:43]\n"
SA"m0,s40,16384\n"
EX"v5,v5\n"
MF"v[32:35],a[72:75],a[40:43],v[32:35]\n"
RD"a[92:95],v226 offset:23552\n"
RD"a[96:99],v226 offset:24576\n"
MF"v[40:43],a[72:75],a[56:59],v[40:43]\n"
WT"lgkmcnt(3)\n"
EX"v6,v6\n"
MF"v[32:35],a[76:79],a[44:47],v[32:35]\n"
RD"a[100:103],v226 offset:25600\n"
PK"v9,v10,v11\n"
MF"v[40:43],a[76:79],a[60:63],v[40:43]\n"
EX"v7,v7\n"
MF"v[36:39],a[80:83],a[32:35],0\n"
SA"s40,s40,32768\n"
BL"v225,s[8:11],s44 offen offset:0 sc1 lds\n"
MF"v[44:47],a[80:83],a[48:51],0\n"
SA"s48,s59,32768\n"
EX"v12,v12\n"
MF"v[36:39],a[84:87],a[36:39],v[36:39]\n"
"s_cmp_eq_u32 s40,s46\n"
RD"a[104:107],v226 offset:26624\n"
PK"v2,v4,v5\n"
MF"v[44:47],a[84:87],a[52:55],v[44:47]\n"
WT"lgkmcnt(2)\n"
EX"v13,v13\n"
MF"v[36:39],a[88:91],a[40:43],v[36:39]\n"
"s_cselect_b32 s40,s47,s40\n"
RD"a[108:111],v226 offset:27648\n"
RD"a[112:115],v226 offset:28672\n"
MF"v[44:47],a[88:91],a[56:59],v[44:47]\n"
"s_and_b32 s48,s48,0x18000\n"
WT"vmcnt(4)\n"
"v_cmp_eq_u64 vcc,s[28:29],v[240:241]\n"
"s_cmp_eq_u64 vcc,exec\n"
"s_cbranch_scc0 L1031_%=\n"
"L1032_%=:\n"
MF"v[36:39],a[92:95],a[44:47],v[36:39]\n"
"s_andn2_b32 s49,s59,0x18000\n"
EX"v14,v14\n"
MF"v[44:47],a[92:95],a[60:63],v[44:47]\n"
"s_or_b32 s52,s48,s49\n"
RD"a[116:119],v226 offset:29696\n"
PK"v3,v6,v7\n"
MF"v[48:51],a[96:99],a[32:35],0\n"
SA"s53,s52,16384\n"
BL"a[0:3],v224,s[8:11],s52 offen offset:0 sc1\n"
EX"v15,v15\n"
MF"v[56:59],a[96:99],a[48:51],0\n"
WT"lgkmcnt(3)\n"
BL"v225,s[8:11],s44 offen offset:1024 sc1 lds\n"
MF"v[48:51],a[100:103],a[36:39],v[48:51]\n"
EX"v16,v16\n"
MF"v[56:59],a[100:103],a[52:55],v[56:59]\n"
BL"a[4:7],v224,s[8:11],s53 offen offset:0 sc1\n"
RD"a[120:123],v226 offset:30720\n"
MF"v[48:51],a[104:107],a[40:43],v[48:51]\n"
EX"v17,v17\n"
PK"v10,v12,v13\n"
MF"v[56:59],a[104:107],a[56:59],v[56:59]\n"
WT"lgkmcnt(2)\n"
RD"a[124:127],v226 offset:31744\n"
PK"v11,v14,v15\n"
MF"v[48:51],a[108:111],a[44:47],v[48:51]\n"
"s_barrier\n"
MF"v[56:59],a[108:111],a[60:63],v[56:59]\n"
BL"a[8:11],v224,s[8:11],s52 offen offset:1024 sc1\n"
RD"a[64:67],v227 offset:0\n"
MF"v[52:55],a[112:115],a[32:35],0\n"
EX"v18,v18\n"
MF"v[60:63],a[112:115],a[48:51],0\n"
WT"lgkmcnt(3)\n"
BL"v225,s[8:11],s44 offen offset:2048 sc1 lds\n"
MF"v[52:55],a[116:119],a[36:39],v[52:55]\n"
BL"a[12:15],v224,s[8:11],s53 offen offset:1024 sc1\n"
EX"v19,v19\n"
MF"v[60:63],a[116:119],a[52:55],v[60:63]\n"
WT"lgkmcnt(2)\n"
RD"a[68:71],v227 offset:1024\n"
RD"a[72:75],v227 offset:2048\n"
PK"v16,v16,v17\n"
MF"v[52:55],a[120:123],a[40:43],v[52:55]\n"
EX"v24,v24\n"
MF"v[60:63],a[120:123],a[56:59],v[60:63]\n"
WT"lgkmcnt(3)\n"
RD"a[76:79],v227 offset:3072\n"
RD"a[80:83],v227 offset:4096\n"
MF"v[52:55],a[124:127],a[44:47],v[52:55]\n"
BL"a[16:19],v224,s[8:11],s52 offen offset:2048 sc1\n"
EX"v25,v25\n"
MF"v[60:63],a[124:127],a[60:63],v[60:63]\n"
WT"lgkmcnt(4)\n"
EX"v26,v26\n"
MF"v[144:147],a[64:67],v[64:67],v[144:147]\n"
SA"s41,s41,32768\n"
BL"v225,s[8:11],s44 offen offset:3072 sc1 lds\n"
MF"v[176:179],a[64:67],v[68:71],v[176:179]\n"
WT"lgkmcnt(3)\n"
BL"a[20:23],v224,s[8:11],s53 offen offset:2048 sc1\n"
PK"v17,v18,v19\n"
MF"v[148:151],a[68:71],v[64:67],v[148:151]\n"
"s_cmp_eq_u32 s41,163840\n"
EX"v27,v27\n"
MF"v[180:183],a[68:71],v[68:71],v[180:183]\n"
WT"lgkmcnt(2)\n"
RD"a[84:87],v227 offset:5120\n"
RD"a[88:91],v227 offset:6144\n"
MF"v[152:155],a[72:75],v[64:67],v[152:155]\n"
"s_cselect_b32 s41,0,s41\n"
EX"v20,v20\n"
MF"v[184:187],a[72:75],v[68:71],v[184:187]\n"
WT"lgkmcnt(2)\n"
BL"a[24:27],v224,s[8:11],s52 offen offset:3072 sc1\n"
RD"a[92:95],v227 offset:7168\n"
MF"v[156:159],a[76:79],v[64:67],v[156:159]\n"
"s_mov_b32 m0,s40\n"
EX"v21,v21\n"
PK"v24,v24,v25\n"
MF"v[188:191],a[76:79],v[68:71],v[188:191]\n"
SA"s48,s59,65536\n"
RD"a[96:99],v227 offset:8192\n"
"v_add_u32 v226,s41,v224\n"
"v_readlane_b32 s43,v228,s42\n"
MF"v[160:163],a[80:83],v[64:67],v[160:163]\n"
"s_and_b32 s48,s48,0x18000\n"
EX"v22,v22\n"
MF"v[192:195],a[80:83],v[68:71],v[192:195]\n"
WT"lgkmcnt(3)\n"
BL"a[28:31],v224,s[8:11],s53 offen offset:3072 sc1\n"
BL"v225,s[8:11],s43 offen offset:0 sc1 lds\n"
MF"v[164:167],a[84:87],v[64:67],v[164:167]\n"
"s_andn2_b32 s49,s59,0x18000\n"
EX"v23,v23\n"
PK"v25,v26,v27\n"
MF"v[196:199],a[84:87],v[68:71],v[196:199]\n"
WT"lgkmcnt(2)\n"
RD"a[100:103],v227 offset:9216\n"
RD"a[104:107],v227 offset:10240\n"
MF"v[168:171],a[88:91],v[64:67],v[168:171]\n"
"s_or_b32 s52,s48,s49\n"
EX"v28,v28\n"
MF"v[200:203],a[88:91],v[68:71],v[200:203]\n"
WT"lgkmcnt(3)\n"
BL"a[180:183],v224,s[8:11],s52 offen offset:0 sc1\n"
RD"a[108:111],v227 offset:11264\n"
MF"v[172:175],a[92:95],v[64:67],v[172:175]\n"
SA"s53,s52,16384\n"
EX"v29,v29\n"
"v_readlane_b32 s44,v229,s42\n"
MF"v[204:207],a[92:95],v[68:71],v[204:207]\n"
SA"s42,s42,1\n"
RD"a[112:115],v227 offset:12288\n"
PK"v18,v20,v21\n"
MF"a[140:143],a[128:131],v[64:67],a[140:143]\n"
BL"a[184:187],v224,s[8:11],s53 offen offset:0 sc1\n"
EX"v30,v30\n"
MF"a[144:147],a[128:131],v[68:71],a[144:147]\n"
WT"lgkmcnt(4)\n"
BL"v225,s[8:11],s43 offen offset:1024 sc1 lds\n"
PK"v19,v22,v23\n"
MF"v[144:147],a[96:99],v[72:75],v[144:147]\n"
EX"v31,v31\n"
MF"v[176:179],a[96:99],v[76:79],v[176:179]\n"
WT"lgkmcnt(2)\n"
BL"a[188:191],v224,s[8:11],s52 offen offset:1024 sc1\n"
RD"a[116:119],v227 offset:13312\n"
MF"v[148:151],a[100:103],v[72:75],v[148:151]\n"
EX"v32,v32\n"
MF"v[180:183],a[100:103],v[76:79],v[180:183]\n"
RD"a[120:123],v227 offset:14336\n"
RD"a[124:127],v227 offset:15360\n"
PK"v26,v28,v29\n"
MF"v[152:155],a[104:107],v[72:75],v[152:155]\n"
EX"v33,v33\n"
MF"v[184:187],a[104:107],v[76:79],v[184:187]\n"
WT"lgkmcnt(3)\n"
BL"a[192:195],v224,s[8:11],s53 offen offset:1024 sc1\n"
MF"v[156:159],a[108:111],v[72:75],v[156:159]\n"
EX"v34,v34\n"
MF"v[188:191],a[108:111],v[76:79],v[188:191]\n"
BL"v225,s[8:11],s43 offen offset:2048 sc1 lds\n"
PK"v27,v30,v31\n"
MF"v[160:163],a[112:115],v[72:75],v[160:163]\n"
EX"v35,v35\n"
MF"v[192:195],a[112:115],v[76:79],v[192:195]\n"
WT"lgkmcnt(2)\n"
BL"a[196:199],v224,s[8:11],s52 offen offset:2048 sc1\n"
RD"a[64:67],v227 offset:16384\n"
MF"v[164:167],a[116:119],v[72:75],v[164:167]\n"
EX"v40,v40\n"
MF"v[196:199],a[116:119],v[76:79],v[196:199]\n"
WT"lgkmcnt(1)\n"
RD"a[68:71],v227 offset:17408\n"
RD"a[72:75],v227 offset:18432\n"
PK"v64,v32,v33\n"
MF"v[168:171],a[120:123],v[72:75],v[168:171]\n"
BL"a[200:203],v224,s[8:11],s53 offen offset:2048 sc1\n"
EX"v41,v41\n"
MF"v[200:203],a[120:123],v[76:79],v[200:203]\n"
RD"a[76:79],v227 offset:19456\n"
PK"v65,v34,v35\n"
MF"v[172:175],a[124:127],v[72:75],v[172:175]\n"
EX"v42,v42\n"
MF"v[204:207],a[124:127],v[76:79],v[204:207]\n"
BL"v225,s[8:11],s43 offen offset:3072 sc1 lds\n"
MF"a[140:143],a[128:131],v[72:75],a[140:143]\n"
SA"s48,s59,98304\n"
BL"a[204:207],v224,s[8:11],s52 offen offset:3072 sc1\n"
EX"v43,v43\n"
MF"a[144:147],a[128:131],v[76:79],a[144:147]\n"
WT"lgkmcnt(3)\n"
RD"a[80:83],v227 offset:20480\n"
RD"a[84:87],v227 offset:21504\n"
MF"v[144:147],a[64:67],v[0:3],v[144:147]\n"
"s_and_b32 s48,s48,0x18000\n"
EX"v36,v36\n"
MF"v[176:179],a[64:67],v[8:11],v[176:179]\n"
WT"lgkmcnt(3)\n"
BL"a[208:211],v224,s[8:11],s53 offen offset:3072 sc1\n"
RD"a[88:91],v227 offset:22528\n"
MF"v[148:151],a[68:71],v[0:3],v[148:151]\n"
SA"m0,s40,16384\n"
EX"v37,v37\n"
PK"v68,v40,v41\n"
MF"v[180:183],a[68:71],v[8:11],v[180:183]\n"
"s_andn2_b32 s49,s59,0x18000\n"
RD"a[92:95],v227 offset:23552\n"
PK"v69,v42,v43\n"
MF"v[152:155],a[72:75],v[0:3],v[152:155]\n"
"s_or_b32 s52,s48,s49\n"
EX"v38,v38\n"
MF"v[184:187],a[72:75],v[8:11],v[184:187]\n"
WT"lgkmcnt(4)\n"
BL"a[212:215],v224,s[8:11],s52 offen offset:0 sc1\n"
BL"v225,s[8:11],s44 offen offset:0 sc1 lds\n"
MF"v[156:159],a[76:79],v[0:3],v[156:159]\n"
SA"s53,s52,16384\n"
EX"v39,v39\n"
MF"v[188:191],a[76:79],v[8:11],v[188:191]\n"
WT"lgkmcnt(2)\n"
RD"a[96:99],v227 offset:24576\n"
RD"a[100:103],v227 offset:25600\n"
MF"v[160:163],a[80:83],v[0:3],v[160:163]\n"
SA"s40,s40,32768\n"
EX"v44,v44\n"
MF"v[192:195],a[80:83],v[8:11],v[192:195]\n"
"s_cmp_eq_u32 s40,s46\n"
BL"a[216:219],v224,s[8:11],s53 offen offset:0 sc1\n"
PK"v66,v36,v37\n"
MF"v[164:167],a[84:87],v[0:3],v[164:167]\n"
"s_cselect_b32 s40,s47,s40\n"
EX"v45,v45\n"
MF"v[196:199],a[84:87],v[8:11],v[196:199]\n"
WT"lgkmcnt(2)\n"
RD"a[104:107],v227 offset:26624\n"
PK"v67,v38,v39\n"
MF"v[168:171],a[88:91],v[0:3],v[168:171]\n"
BL"a[220:223],v224,s[8:11],s52 offen offset:1024 sc1\n"
EX"v46,v46\n"
MF"v[200:203],a[88:91],v[8:11],v[200:203]\n"
BL"v225,s[8:11],s44 offen offset:1024 sc1 lds\n"
MF"v[172:175],a[92:95],v[0:3],v[172:175]\n"
EX"v47,v47\n"
MF"v[204:207],a[92:95],v[8:11],v[204:207]\n"
RD"a[108:111],v227 offset:27648\n"
RD"a[112:115],v227 offset:28672\n"
MF"a[140:143],a[128:131],v[0:3],a[140:143]\n"
BL"a[224:227],v224,s[8:11],s53 offen offset:1024 sc1\n"
EX"v48,v48\n"
MF"a[144:147],a[128:131],v[8:11],a[144:147]\n"
WT"lgkmcnt(3)\n"
RD"a[116:119],v227 offset:29696\n"
RD"a[120:123],v227 offset:30720\n"
PK"v70,v44,v45\n"
MF"v[144:147],a[96:99],v[16:19],v[144:147]\n"
EX"v49,v49\n"
MF"v[176:179],a[96:99],v[24:27],v[176:179]\n"
BL"a[228:231],v224,s[8:11],s52 offen offset:2048 sc1\n"
MF"v[148:151],a[100:103],v[16:19],v[148:151]\n"
EX"v50,v50\n"
MF"v[180:183],a[100:103],v[24:27],v[180:183]\n"
WT"lgkmcnt(4)\n"
BL"v225,s[8:11],s44 offen offset:2048 sc1 lds\n"
PK"v71,v46,v47\n"
MF"v[152:155],a[104:107],v[16:19],v[152:155]\n"
EX"v51,v51\n"
MF"v[184:187],a[104:107],v[24:27],v[184:187]\n"
WT"lgkmcnt(2)\n"
BL"a[232:235],v224,s[8:11],s53 offen offset:2048 sc1\n"
RD"a[124:127],v227 offset:31744\n"
MF"v[156:159],a[108:111],v[16:19],v[156:159]\n"
"s_barrier\n"
MF"v[188:191],a[108:111],v[24:27],v[188:191]\n"
RD"a[64:67],v226 offset:0\n"
PK"v72,v48,v49\n"
MF"v[160:163],a[112:115],v[16:19],v[160:163]\n"
EX"v52,v52\n"
MF"v[192:195],a[112:115],v[24:27],v[192:195]\n"
WT"lgkmcnt(2)\n"
BL"a[236:239],v224,s[8:11],s52 offen offset:3072 sc1\n"
RD"a[68:71],v226 offset:1024\n"
MF"v[164:167],a[116:119],v[16:19],v[164:167]\n"
EX"v53,v53\n"
MF"v[196:199],a[116:119],v[24:27],v[196:199]\n"
BL"v225,s[8:11],s44 offen offset:3072 sc1 lds\n"
PK"v73,v50,v51\n"
MF"v[168:171],a[120:123],v[16:19],v[168:171]\n"
BL"a[240:243],v224,s[8:11],s53 offen offset:3072 sc1\n"
EX"v54,v54\n"
MF"v[200:203],a[120:123],v[24:27],v[200:203]\n"
WT"lgkmcnt(2)\n"
RD"a[72:75],v226 offset:2048\n"
RD"a[76:79],v226 offset:3072\n"
MF"v[172:175],a[124:127],v[16:19],v[172:175]\n"
EX"v55,v55\n"
MF"v[204:207],a[124:127],v[24:27],v[204:207]\n"
RD"a[80:83],v226 offset:4096\n"
RD"a[84:87],v226 offset:5120\n"
MF"a[140:143],a[128:131],v[16:19],a[140:143]\n"
PK"v74,v52,v53\n"
PK"v75,v54,v55\n"
MF"a[144:147],a[128:131],v[24:27],a[144:147]\n"
"LE2_%=:\n"
WT"lgkmcnt(5)\n"
MF"v[0:3],a[64:67],a[32:35],v[208:211]\n"
"s_mov_b32 m0,s40\n"
EX"v56,v56\n"
MF"v[8:11],a[64:67],a[48:51],0\n"
WT"lgkmcnt(4)\n"
EX"v57,v57\n"
MF"v[0:3],a[68:71],a[36:39],v[0:3]\n"
RD"a[88:91],v226 offset:6144\n"
"v_readlane_b32 s43,v228,s42\n"
MF"v[8:11],a[68:71],a[52:55],v[8:11]\n"
WT"lgkmcnt(4)\n"
EX"v58,v58\n"
MF"v[0:3],a[72:75],a[40:43],v[0:3]\n"
SA"s41,s41,32768\n"
BL"v225,s[8:11],s43 offen offset:0 sc1 lds\n"
"v_readlane_b32 s44,v229,s42\n"
MF"v[8:11],a[72:75],a[56:59],v[8:11]\n"
WT"lgkmcnt(1)\n"
RD"a[92:95],v226 offset:7168\n"
PK"v76,v56,v57\n"
MF"v[0:3],a[76:79],a[44:47],v[0:3]\n"
SA"s42,s42,1\n"
RD"a[96:99],v226 offset:8192\n"
"s_cmp_eq_u32 s41,163840\n"
MF"v[8:11],a[76:79],a[60:63],v[8:11]\n"
"s_cselect_b32 s41,0,s41\n"
EX"v59,v59\n"
"v_add_u32 v227,s41,v224\n"
MF"v[4:7],a[80:83],a[32:35],v[212:215]\n"
RD"a[100:103],v226 offset:9216\n"
PK"v77,v58,v59\n"
MF"v[12:15],a[80:83],a[48:51],0\n"
EX"v60,v60\n"
MF"v[4:7],a[84:87],a[36:39],v[4:7]\n"
BL"v225,s[8:11],s43 offen offset:1024 sc1 lds\n"
MF"v[12:15],a[84:87],a[52:55],v[12:15]\n"
WT"lgkmcnt(3)\n"
EX"v61,v61\n"
MF"v[4:7],a[88:91],a[40:43],v[4:7]\n"
RD"a[104:107],v226 offset:10240\n"
RD"a[108:111],v226 offset:11264\n"
MF"v[12:15],a[88:91],a[56:59],v[12:15]\n"
WT"lgkmcnt(3)\n"
EX"v62,v62\n"
PK"v78,v60,v61\n"
MF"v[4:7],a[92:95],a[44:47],v[4:7]\n"
RD"a[112:115],v226 offset:12288\n"
RD"a[116:119],v226 offset:13312\n"
MF"v[12:15],a[92:95],a[60:63],v[12:15]\n"
EX"v63,v63\n"
MF"v[16:19],a[96:99],a[32:35],v[216:219]\n"
BL"v225,s[8:11],s43 offen offset:2048 sc1 lds\n"
MF"v[24:27],a[96:99],a[48:51],0\n"
WT"lgkmcnt(4)\n"
EX"v0,v0\n"
PK"v79,v62,v63\n"
MF"v[16:19],a[100:103],a[36:39],v[16:19]\n"
RD"a[120:123],v226 offset:14336\n"
RD"a[124:127],v226 offset:15360\n"
MF"v[24:27],a[100:103],a[52:55],v[24:27]\n"
WT"lgkmcnt(4)\n"
EX"v1,v1\n"
MF"v[16:19],a[104:107],a[40:43],v[16:19]\n"
RD"a[64:67],v226 offset:16384\n"
RD"a[68:71],v226 offset:17408\n"
MF"v[24:27],a[104:107],a[56:59],v[24:27]\n"
BL"v225,s[8:11],s43 offen offset:3072 sc1 lds\n"
EX"v2,v2\n"
MF"v[16:19],a[108:111],a[44:47],v[16:19]\n"
SA"m0,s40,16384\n"
RD"a[72:75],v226 offset:18432\n"
RD"a[76:79],v226 offset:19456\n"
PK"v0,v0,v1\n"
MF"v[24:27],a[108:111],a[60:63],v[24:27]\n"
WT"lgkmcnt(6)\n"
EX"v3,v3\n"
MF"v[20:23],a[112:115],a[32:35],v[220:223]\n"
RD"a[80:83],v226 offset:20480\n"
RD"a[84:87],v226 offset:21504\n"
MF"v[28:31],a[112:115],a[48:51],0\n"
EX"v8,v8\n"
PK"v1,v2,v3\n"
MF"v[20:23],a[116:119],a[36:39],v[20:23]\n"
SA"s40,s40,32768\n"
BL"v225,s[8:11],s44 offen offset:0 sc1 lds\n"
MF"v[28:31],a[116:119],a[52:55],v[28:31]\n"
WT"lgkmcnt(4)\n"
RD"a[88:91],v226 offset:22528\n"
EX"v9,v9\n"
MF"v[20:23],a[120:123],a[40:43],v[20:23]\n"
"s_cmp_eq_u32 s40,s46\n"
RD"a[92:95],v226 offset:23552\n"
RD"a[96:99],v226 offset:24576\n"
EX"v10,v10\n"
MF"v[28:31],a[120:123],a[56:59],v[28:31]\n"
"s_cselect_b32 s40,s47,s40\n"
RD"a[100:103],v226 offset:25600\n"
RD"a[104:107],v226 offset:26624\n"
PK"v8,v8,v9\n"
MF"v[20:23],a[124:127],a[44:47],v[20:23]\n"
EX"v11,v11\n"
MF"v[28:31],a[124:127],a[60:63],v[28:31]\n"
RD"a[108:111],v226 offset:27648\n"
RD"a[112:115],v226 offset:28672\n"
MF"v[40:43],a[64:67],a[48:51],v[208:211]\n"
BL"v225,s[8:11],s44 offen offset:1024 sc1 lds\n"
EX"v4,v4\n"
MF"v[40:43],a[68:71],a[52:55],v[40:43]\n"
WT"lgkmcnt(7)\n"
RD"a[116:119],v226 offset:29696\n"
RD"a[120:123],v226 offset:30720\n"
PK"v9,v10,v11\n"
MF"v[40:43],a[72:75],a[56:59],v[40:43]\n"
EX"v5,v5\n"
MF"v[40:43],a[76:79],a[60:63],v[40:43]\n"
RD"a[124:127],v226 offset:31744\n"
PK"v2,v4,v5\n"
MF"v[44:47],a[80:83],a[48:51],v[212:215]\n"
EX"v6,v6\n"
MF"v[44:47],a[84:87],a[52:55],v[44:47]\n"
WT"lgkmcnt(5)\n"
BL"v225,s[8:11],s44 offen offset:2048 sc1 lds\n"
MF"v[44:47],a[88:91],a[56:59],v[44:47]\n"
EX"v7,v7\n"
MF"v[44:47],a[92:95],a[60:63],v[44:47]\n"
WT"vmcnt(15)\n"
"s_barrier\n"
MF"v[56:59],a[96:99],a[48:51],v[216:219]\n"
RD"a[64:67],v227 offset:0\n"
"v_readlane_b32 s43,v228,s42\n"
EX"v12,v12\n"
MF"v[56:59],a[100:103],a[52:55],v[56:59]\n"
EX"v13,v13\n"
PK"v3,v6,v7\n"
MF"v[56:59],a[104:107],a[56:59],v[56:59]\n"
WT"lgkmcnt(1)\n"
BL"v225,s[8:11],s44 offen offset:3072 sc1 lds\n"
MF"v[56:59],a[108:111],a[60:63],v[56:59]\n"
"s_mov_b32 m0,s40\n"
RD"a[68:71],v227 offset:1024\n"
PK"v10,v12,v13\n"
MF"v[60:63],a[112:115],a[48:51],v[220:223]\n"
EX"v14,v14\n"
MF"v[60:63],a[116:119],a[52:55],v[60:63]\n"
RD"a[72:75],v227 offset:2048\n"
RD"a[76:79],v227 offset:3072\n"
MF"v[60:63],a[120:123],a[56:59],v[60:63]\n"
EX"v15,v15\n"
MF"v[60:63],a[124:127],a[60:63],v[60:63]\n"
WT"lgkmcnt(3)\n"
RD"a[80:83],v227 offset:4096\n"
"v_readlane_b32 s44,v229,s42\n"
MF"v[144:147],a[64:67],v[64:67],v[144:147]\n"
SA"s41,s41,32768\n"
EX"v16,v16\n"
PK"v11,v14,v15\n"
MF"v[176:179],a[64:67],v[68:71],v[176:179]\n"
WT"lgkmcnt(3)\n"
BL"v225,s[8:11],s43 offen offset:0 sc1 lds\n"
MF"v[148:151],a[68:71],v[64:67],v[148:151]\n"
"s_cmp_eq_u32 s41,163840\n"
EX"v17,v17\n"
MF"v[180:183],a[68:71],v[68:71],v[180:183]\n"
WT"lgkmcnt(2)\n"
RD"a[84:87],v227 offset:5120\n"
RD"a[88:91],v227 offset:6144\n"
MF"v[152:155],a[72:75],v[64:67],v[152:155]\n"
"s_cselect_b32 s41,0,s41\n"
EX"v18,v18\n"
"v_add_u32 v226,s41,v224\n"
MF"v[184:187],a[72:75],v[68:71],v[184:187]\n"
WT"lgkmcnt(3)\n"
RD"a[92:95],v227 offset:7168\n"
RD"a[96:99],v227 offset:8192\n"
PK"v16,v16,v17\n"
MF"v[156:159],a[76:79],v[64:67],v[156:159]\n"
SA"s42,s42,1\n"
EX"v19,v19\n"
MF"v[188:191],a[76:79],v[68:71],v[188:191]\n"
WT"lgkmcnt(4)\n"
BL"v225,s[8:11],s43 offen offset:1024 sc1 lds\n"
MF"v[160:163],a[80:83],v[64:67],v[160:163]\n"
EX"v24,v24\n"
PK"v17,v18,v19\n"
MF"v[192:195],a[80:83],v[68:71],v[192:195]\n"
WT"lgkmcnt(3)\n"
EX"v25,v25\n"
MF"v[164:167],a[84:87],v[64:67],v[164:167]\n"
EX"v26,v26\n"
MF"v[196:199],a[84:87],v[68:71],v[196:199]\n"
WT"lgkmcnt(2)\n"
RD"a[100:103],v227 offset:9216\n"
RD"a[104:107],v227 offset:10240\n"
PK"v24,v24,v25\n"
MF"v[168:171],a[88:91],v[64:67],v[168:171]\n"
BL"v225,s[8:11],s43 offen offset:2048 sc1 lds\n"
MF"v[200:203],a[88:91],v[68:71],v[200:203]\n"
WT"lgkmcnt(2)\n"
EX"v27,v27\n"
MF"v[172:175],a[92:95],v[64:67],v[172:175]\n"
RD"a[108:111],v227 offset:11264\n"
RD"a[112:115],v227 offset:12288\n"
MF"v[204:207],a[92:95],v[68:71],v[204:207]\n"
EX"v20,v20\n"
PK"v25,v26,v27\n"
MF"a[140:143],a[128:131],v[64:67],a[140:143]\n"
EX"v21,v21\n"
MF"a[144:147],a[128:131],v[68:71],a[144:147]\n"
BL"v225,s[8:11],s43 offen offset:3072 sc1 lds\n"
MF"v[144:147],a[96:99],v[72:75],v[144:147]\n"
SA"m0,s40,16384\n"
EX"v22,v22\n"
MF"v[176:179],a[96:99],v[76:79],v[176:179]\n"
WT"lgkmcnt(2)\n"
RD"a[116:119],v227 offset:13312\n"
RD"a[120:123],v227 offset:14336\n"
PK"v18,v20,v21\n"
MF"v[148:151],a[100:103],v[72:75],v[148:151]\n"
EX"v23,v23\n"
MF"v[180:183],a[100:103],v[76:79],v[180:183]\n"
RD"a[124:127],v227 offset:15360\n"
PK"v19,v22,v23\n"
MF"v[152:155],a[104:107],v[72:75],v[152:155]\n"
SA"s40,s40,32768\n"
EX"v28,v28\n"
MF"v[184:187],a[104:107],v[76:79],v[184:187]\n"
WT"lgkmcnt(3)\n"
BL"v225,s[8:11],s44 offen offset:0 sc1 lds\n"
MF"v[156:159],a[108:111],v[72:75],v[156:159]\n"
"s_cmp_eq_u32 s40,s46\n"
EX"v29,v29\n"
MF"v[188:191],a[108:111],v[76:79],v[188:191]\n"
"s_cselect_b32 s40,s47,s40\n"
EX"v30,v30\n"
MF"v[160:163],a[112:115],v[72:75],v[160:163]\n"
RD"a[64:67],v227 offset:16384\n"
PK"v26,v28,v29\n"
MF"v[192:195],a[112:115],v[76:79],v[192:195]\n"
WT"lgkmcnt(2)\n"
BL"v225,s[8:11],s44 offen offset:1024 sc1 lds\n"
MF"v[164:167],a[116:119],v[72:75],v[164:167]\n"
EX"v31,v31\n"
MF"v[196:199],a[116:119],v[76:79],v[196:199]\n"
RD"a[68:71],v227 offset:17408\n"
RD"a[72:75],v227 offset:18432\n"
MF"v[168:171],a[120:123],v[72:75],v[168:171]\n"
EX"v40,v40\n"
MF"v[200:203],a[120:123],v[76:79],v[200:203]\n"
WT"lgkmcnt(3)\n"
RD"a[76:79],v227 offset:19456\n"
PK"v27,v30,v31\n"
MF"v[172:175],a[124:127],v[72:75],v[172:175]\n"
EX"v41,v41\n"
MF"v[204:207],a[124:127],v[76:79],v[204:207]\n"
BL"v225,s[8:11],s44 offen offset:2048 sc1 lds\n"
MF"a[140:143],a[128:131],v[72:75],a[140:143]\n"
EX"v42,v42\n"
MF"a[144:147],a[128:131],v[76:79],a[144:147]\n"
WT"lgkmcnt(3)\n"
RD"a[80:83],v227 offset:20480\n"
RD"a[84:87],v227 offset:21504\n"
PK"v68,v40,v41\n"
MF"v[144:147],a[64:67],v[0:3],v[144:147]\n"
EX"v43,v43\n"
MF"v[176:179],a[64:67],v[8:11],v[176:179]\n"
WT"lgkmcnt(3)\n"
RD"a[88:91],v227 offset:22528\n"
RD"a[92:95],v227 offset:23552\n"
MF"v[148:151],a[68:71],v[0:3],v[148:151]\n"
EX"v44,v44\n"
"v_readlane_b32 s43,v228,s42\n"
MF"v[180:183],a[68:71],v[8:11],v[180:183]\n"
BL"v225,s[8:11],s44 offen offset:3072 sc1 lds\n"
PK"v69,v42,v43\n"
MF"v[152:155],a[72:75],v[0:3],v[152:155]\n"
"s_mov_b32 m0,s40\n"
EX"v45,v45\n"
MF"v[184:187],a[72:75],v[8:11],v[184:187]\n"
WT"lgkmcnt(4)\n"
EX"v46,v46\n"
MF"v[156:159],a[76:79],v[0:3],v[156:159]\n"
RD"a[96:99],v227 offset:24576\n"
"v_readlane_b32 s44,v229,s42\n"
PK"v70,v44,v45\n"
MF"v[188:191],a[76:79],v[8:11],v[188:191]\n"
WT"lgkmcnt(3)\n"
EX"v47,v47\n"
MF"v[160:163],a[80:83],v[0:3],v[160:163]\n"
SA"s42,s42,1\n"
RD"a[100:103],v227 offset:25600\n"
PK"v71,v46,v47\n"
MF"v[192:195],a[80:83],v[8:11],v[192:195]\n"
EX"v56,v56\n"
MF"v[164:167],a[84:87],v[0:3],v[164:167]\n"
RD"a[104:107],v227 offset:26624\n"
MF"v[196:199],a[84:87],v[8:11],v[196:199]\n"
WT"lgkmcnt(3)\n"
EX"v57,v57\n"
MF"v[168:171],a[88:91],v[0:3],v[168:171]\n"
EX"v58,v58\n"
MF"v[200:203],a[88:91],v[8:11],v[200:203]\n"
RD"a[108:111],v227 offset:27648\n"
PK"v76,v56,v57\n"
MF"v[172:175],a[92:95],v[0:3],v[172:175]\n"
EX"v59,v59\n"
MF"v[204:207],a[92:95],v[8:11],v[204:207]\n"
RD"a[112:115],v227 offset:28672\n"
RD"a[116:119],v227 offset:29696\n"
MF"a[140:143],a[128:131],v[0:3],a[140:143]\n"
EX"v60,v60\n"
MF"a[144:147],a[128:131],v[8:11],a[144:147]\n"
WT"lgkmcnt(3)\n"
RD"a[120:123],v227 offset:30720\n"
PK"v77,v58,v59\n"
MF"v[144:147],a[96:99],v[16:19],v[144:147]\n"
EX"v61,v61\n"
MF"v[176:179],a[96:99],v[24:27],v[176:179]\n"
PK"v78,v60,v61\n"
MF"v[148:151],a[100:103],v[16:19],v[148:151]\n"
EX"v62,v62\n"
MF"v[180:183],a[100:103],v[24:27],v[180:183]\n"
RD"a[124:127],v227 offset:31744\n"
MF"v[152:155],a[104:107],v[16:19],v[152:155]\n"
SA"s41,s41,32768\n"
WT"vmcnt(16)\n"
"s_barrier\n"
MF"v[184:187],a[104:107],v[24:27],v[184:187]\n"
WT"lgkmcnt(2)\n"
BL"v225,s[8:11],s43 offen offset:0 sc1 lds\n"
BL"v225,s[8:11],s43 offen offset:1024 sc1 lds\n"
MF"v[156:159],a[108:111],v[16:19],v[156:159]\n"
"s_cmp_eq_u32 s41,163840\n"
BL"v225,s[8:11],s43 offen offset:2048 sc1 lds\n"
EX"v63,v63\n"
MF"v[188:191],a[108:111],v[24:27],v[188:191]\n"
"s_cselect_b32 s41,0,s41\n"
RD"a[64:67],v226 offset:0\n"
RD"a[68:71],v226 offset:1024\n"
BL"v225,s[8:11],s43 offen offset:3072 sc1 lds\n"
MF"v[160:163],a[112:115],v[16:19],v[160:163]\n"
SA"m0,s40,16384\n"
RD"a[72:75],v226 offset:2048\n"
"v_add_u32 v227,s41,v224\n"
PK"v79,v62,v63\n"
MF"v[192:195],a[112:115],v[24:27],v[192:195]\n"
RD"a[76:79],v226 offset:3072\n"
MF"v[164:167],a[116:119],v[16:19],v[164:167]\n"
RD"a[80:83],v226 offset:4096\n"
MF"v[196:199],a[116:119],v[24:27],v[196:199]\n"
WT"lgkmcnt(5)\n"
RD"a[84:87],v226 offset:5120\n"
MF"v[168:171],a[120:123],v[16:19],v[168:171]\n"
SA"s40,s40,32768\n"
BL"v225,s[8:11],s44 offen offset:0 sc1 lds\n"
MF"v[200:203],a[120:123],v[24:27],v[200:203]\n"
"s_cmp_eq_u32 s40,s46\n"
RD"a[88:91],v226 offset:6144\n"
"s_cselect_b32 s40,s47,s40\n"
MF"v[172:175],a[124:127],v[16:19],v[172:175]\n"
RD"a[92:95],v226 offset:7168\n"
MF"v[204:207],a[124:127],v[24:27],v[204:207]\n"
RD"a[96:99],v226 offset:8192\n"
MF"a[140:143],a[128:131],v[16:19],a[140:143]\n"
RD"a[100:103],v226 offset:9216\n"
MF"a[144:147],a[128:131],v[24:27],a[144:147]\n"
WT"lgkmcnt(5)\n"
RD"a[104:107],v226 offset:10240\n"
MF"v[176:179],a[64:67],v[68:71],v[176:179]\n"
BL"v225,s[8:11],s44 offen offset:1024 sc1 lds\n"
RD"a[108:111],v226 offset:11264\n"
MF"v[180:183],a[68:71],v[68:71],v[180:183]\n"
RD"a[112:115],v226 offset:12288\n"
MF"v[184:187],a[72:75],v[68:71],v[184:187]\n"
RD"a[116:119],v226 offset:13312\n"
MF"v[188:191],a[76:79],v[68:71],v[188:191]\n"
RD"a[120:123],v226 offset:14336\n"
MF"v[192:195],a[80:83],v[68:71],v[192:195]\n"
WT"lgkmcnt(6)\n"
RD"a[124:127],v226 offset:15360\n"
MF"v[196:199],a[84:87],v[68:71],v[196:199]\n"
BL"v225,s[8:11],s44 offen offset:2048 sc1 lds\n"
MF"v[200:203],a[88:91],v[68:71],v[200:203]\n"
WT"vmcnt(15)\n"
"s_barrier\n"
MF"v[204:207],a[92:95],v[68:71],v[204:207]\n"
RD"a[64:67],v227 offset:0\n"
MF"a[144:147],a[128:131],v[68:71],a[144:147]\n"
MF"v[176:179],a[96:99],v[76:79],v[176:179]\n"
WT"lgkmcnt(3)\n"
BL"v225,s[8:11],s44 offen offset:3072 sc1 lds\n"
MF"v[180:183],a[100:103],v[76:79],v[180:183]\n"
RD"a[68:71],v227 offset:1024\n"
MF"v[184:187],a[104:107],v[76:79],v[184:187]\n"
RD"a[72:75],v227 offset:2048\n"
MF"v[188:191],a[108:111],v[76:79],v[188:191]\n"
RD"a[76:79],v227 offset:3072\n"
MF"v[192:195],a[112:115],v[76:79],v[192:195]\n"
RD"a[80:83],v227 offset:4096\n"
MF"v[196:199],a[116:119],v[76:79],v[196:199]\n"
WT"lgkmcnt(4)\n"
MF"v[200:203],a[120:123],v[76:79],v[200:203]\n"
MF"v[204:207],a[124:127],v[76:79],v[204:207]\n"
MF"a[144:147],a[128:131],v[76:79],a[144:147]\n"
RD"a[84:87],v227 offset:5120\n"
"LTL_%=:\n"
"s_cmp_ge_u32 s34,6\n"
"s_cbranch_scc1 LTH_%=\n"
RD"a[88:91],v227 offset:6144\n"
MF"v[0:3],a[64:67],v[80:83],0\n"
"v_accvgpr_read_b32 v251,a140\n"
"v_subrev_u32 v253,0x2b800000,v251\n"
MF"v[40:43],a[64:67],v[112:115],0\n"
WT"lgkmcnt(3)\n"
RD"a[92:95],v227 offset:7168\n"
MF"v[4:7],a[68:71],v[80:83],0\n"
"v_cmp_le_u32 vcc,0x34000000,v253\n"
"s_cmp_lg_u64 vcc,0\n"
"s_cbranch_scc1 L1041_%=\n"
"L1040_%=:\n"
MF"v[44:47],a[68:71],v[112:115],0\n"
"v_rcp_f32 v251,v251\n"
MF"v[8:11],a[72:75],v[80:83],0\n"
SA"s41,s41,32768\n"
RD"a[96:99],v227 offset:8192\n"
MU"v144,v251,v144\n"
MF"v[48:51],a[72:75],v[112:115],0\n"
"s_cmp_eq_u32 s41,163840\n"
RD"a[100:103],v227 offset:9216\n"
MU"v145,v251,v145\n"
MF"v[12:15],a[76:79],v[80:83],0\n"
"s_cselect_b32 s41,0,s41\n"
MU"v146,v251,v146\n"
"v_add_u32 v226,s41,v224\n"
MF"v[52:55],a[76:79],v[112:115],0\n"
WT"lgkmcnt(3)\n"
RD"a[104:107],v227 offset:10240\n"
MU"v147,v251,v147\n"
MF"v[16:19],a[80:83],v[80:83],0\n"
SA"s41,s41,32768\n"
MU"v148,v251,v148\n"
MU"v149,v251,v149\n"
MF"v[56:59],a[80:83],v[112:115],0\n"
"s_cmp_eq_u32 s41,163840\n"
RD"a[108:111],v227 offset:11264\n"
MU"v150,v251,v150\n"
MU"v151,v251,v151\n"
MF"v[20:23],a[84:87],v[80:83],0\n"
"s_cselect_b32 s41,0,s41\n"
PK"v144,v144,v145\n"
"v_add_u32 v235,s41,v224\n"
PK"v145,v146,v147\n"
MF"v[60:63],a[84:87],v[112:115],0\n"
RD"a[112:115],v227 offset:12288\n"
PK"v146,v148,v149\n"
MF"v[24:27],a[88:91],v[80:83],0\n"
SA"s41,s41,32768\n"
PK"v147,v150,v151\n"
MU"v152,v251,v152\n"
MF"v[64:67],a[88:91],v[112:115],0\n"
WT"lgkmcnt(3)\n"
BS"v[144:147],v224,s[8:11],s60 offen offset:0 sc1\n"
MU"v153,v251,v153\n"
MF"v[28:31],a[92:95],v[80:83],0\n"
"s_cmp_eq_u32 s41,163840\n"
RD"a[116:119],v227 offset:13312\n"
MU"v154,v251,v154\n"
MU"v155,v251,v155\n"
MF"v[68:71],a[92:95],v[112:115],0\n"
"s_cselect_b32 s41,0,s41\n"
RD"a[120:123],v227 offset:14336\n"
"v_add_u32 v236,s41,v224\n"
MU"v156,v251,v156\n"
MF"v[32:35],a[96:99],v[80:83],0\n"
SA"s41,s41,32768\n"
MU"v157,v251,v157\n"
MU"v158,v251,v158\n"
MU"v159,v251,v159\n"
MF"v[72:75],a[96:99],v[112:115],0\n"
"s_cmp_eq_u32 s41,163840\n"
RD"a[124:127],v227 offset:15360\n"
PK"v152,v152,v153\n"
PK"v153,v154,v155\n"
MF"v[36:39],a[100:103],v[80:83],0\n"
"s_cselect_b32 s41,0,s41\n"
PK"v154,v156,v157\n"
"v_add_u32 v237,s41,v224\n"
PK"v155,v158,v159\n"
MF"v[76:79],a[100:103],v[112:115],0\n"
WT"lgkmcnt(3)\n"
BS"v[152:155],v224,s[8:11],s60 offen offset:1024 sc1\n"
MU"v160,v251,v160\n"
MF"v[0:3],a[104:107],v[88:91],v[0:3]\n"
RD"a[64:67],v227 offset:16384\n"
MU"v161,v251,v161\n"
MU"v162,v251,v162\n"
MF"v[40:43],a[104:107],v[120:123],v[40:43]\n"
RD"a[68:71],v227 offset:17408\n"
MU"v163,v251,v163\n"
MU"v164,v251,v164\n"
MF"v[4:7],a[108:111],v[88:91],v[4:7]\n"
MU"v165,v251,v165\n"
MU"v166,v251,v166\n"
MF"v[44:47],a[108:111],v[120:123],v[44:47]\n"
RD"a[72:75],v227 offset:18432\n"
MU"v167,v251,v167\n"
PK"v160,v160,v161\n"
MF"v[8:11],a[112:115],v[88:91],v[8:11]\n"
SA"s51,s60,16384\n"
"v_accvgpr_read_b32 v252,a144\n"
"v_subrev_u32 v253,0x2b800000,v252\n"
PK"v161,v162,v163\n"
MF"v[48:51],a[112:115],v[120:123],v[48:51]\n"
WT"lgkmcnt(3)\n"
RD"a[76:79],v227 offset:19456\n"
PK"v162,v164,v165\n"
PK"v163,v166,v167\n"
MF"v[12:15],a[116:119],v[88:91],v[12:15]\n"
"s_lshl_b32 s62,s34,14\n"
BS"v[160:163],v224,s[8:11],s60 offen offset:2048 sc1\n"
MU"v168,v251,v168\n"
MF"v[52:55],a[116:119],v[120:123],v[52:55]\n"
"s_sub_u32 s62,s61,s62\n"
"v_cmp_le_u32 vcc,0x34000000,v253\n"
"s_cmp_lg_u64 vcc,0\n"
"s_cbranch_scc1 L1047_%=\n"
"L1046_%=:\n"
MF"v[16:19],a[120:123],v[88:91],v[16:19]\n"
SA"s62,s62,122880\n"
"v_rcp_f32 v252,v252\n"
MU"v169,v251,v169\n"
MF"v[56:59],a[120:123],v[120:123],v[56:59]\n"
RD"a[80:83],v227 offset:20480\n"
RD"a[84:87],v227 offset:21504\n"
MU"v170,v251,v170\n"
MF"v[20:23],a[124:127],v[88:91],v[20:23]\n"
MU"v171,v251,v171\n"
MU"v172,v251,v172\n"
MU"v173,v251,v173\n"
MF"v[60:63],a[124:127],v[120:123],v[60:63]\n"
WT"lgkmcnt(3)\n"
RD"a[88:91],v227 offset:22528\n"
MU"v174,v251,v174\n"
MU"v175,v251,v175\n"
MF"v[24:27],a[64:67],v[88:91],v[24:27]\n"
PK"v168,v168,v169\n"
PK"v169,v170,v171\n"
MU"v176,v252,v176\n"
MF"v[64:67],a[64:67],v[120:123],v[64:67]\n"
RD"a[92:95],v227 offset:23552\n"
MU"v177,v252,v177\n"
PK"v170,v172,v173\n"
MF"v[28:31],a[68:71],v[88:91],v[28:31]\n"
PK"v171,v174,v175\n"
MU"v178,v252,v178\n"
MU"v179,v252,v179\n"
MF"v[68:71],a[68:71],v[120:123],v[68:71]\n"
BS"v[168:171],v224,s[8:11],s60 offen offset:3072 sc1\n"
MU"v180,v252,v180\n"
MF"v[32:35],a[72:75],v[88:91],v[32:35]\n"
RD"a[96:99],v227 offset:24576\n"
MU"v181,v252,v181\n"
MU"v182,v252,v182\n"
MF"v[72:75],a[72:75],v[120:123],v[72:75]\n"
WT"lgkmcnt(3)\n"
RD"a[100:103],v227 offset:25600\n"
MU"v183,v252,v183\n"
PK"v176,v176,v177\n"
MF"v[36:39],a[76:79],v[88:91],v[36:39]\n"
PK"v177,v178,v179\n"
PK"v178,v180,v181\n"
PK"v179,v182,v183\n"
MF"v[76:79],a[76:79],v[120:123],v[76:79]\n"
BS"v[176:179],v224,s[8:11],s51 offen offset:0 sc1\n"
MU"v184,v252,v184\n"
MF"v[0:3],a[80:83],v[96:99],v[0:3]\n"
RD"a[104:107],v227 offset:26624\n"
MU"v185,v252,v185\n"
MU"v186,v252,v186\n"
MF"v[40:43],a[80:83],v[128:131],v[40:43]\n"
RD"a[108:111],v227 offset:27648\n"
MU"v187,v252,v187\n"
MU"v188,v252,v188\n"
MF"v[4:7],a[84:87],v[96:99],v[4:7]\n"
MU"v189,v252,v189\n"
MU"v190,v252,v190\n"
MU"v191,v252,v191\n"
MF"v[44:47],a[84:87],v[128:131],v[44:47]\n"
WT"lgkmcnt(3)\n"
RD"a[112:115],v227 offset:28672\n"
PK"v184,v184,v185\n"
PK"v185,v186,v187\n"
MF"v[8:11],a[88:91],v[96:99],v[8:11]\n"
PK"v186,v188,v189\n"
PK"v187,v190,v191\n"
MU"v192,v252,v192\n"
MF"v[48:51],a[88:91],v[128:131],v[48:51]\n"
BS"v[184:187],v224,s[8:11],s51 offen offset:1024 sc1\n"
MU"v193,v252,v193\n"
MF"v[12:15],a[92:95],v[96:99],v[12:15]\n"
RD"a[116:119],v227 offset:29696\n"
MU"v194,v252,v194\n"
MU"v195,v252,v195\n"
MF"v[52:55],a[92:95],v[128:131],v[52:55]\n"
RD"a[120:123],v227 offset:30720\n"
MU"v196,v252,v196\n"
MU"v197,v252,v197\n"
MF"v[16:19],a[96:99],v[96:99],v[16:19]\n"
MU"v198,v252,v198\n"
MU"v199,v252,v199\n"
PK"v192,v192,v193\n"
MF"v[56:59],a[96:99],v[128:131],v[56:59]\n"
WT"lgkmcnt(3)\n"
RD"a[124:127],v227 offset:31744\n"
PK"v193,v194,v195\n"
PK"v194,v196,v197\n"
MF"v[20:23],a[100:103],v[96:99],v[20:23]\n"
WT"vmcnt(14)\n"
"s_barrier\n"
MF"v[60:63],a[100:103],v[128:131],v[60:63]\n"
RD"a[64:67],v226 offset:0\n"
PK"v195,v198,v199\n"
MU"v200,v252,v200\n"
MF"v[24:27],a[104:107],v[96:99],v[24:27]\n"
BS"v[192:195],v224,s[8:11],s51 offen offset:2048 sc1\n"
MU"v201,v252,v201\n"
MF"v[64:67],a[104:107],v[128:131],v[64:67]\n"
RD"a[68:71],v226 offset:1024\n"
MU"v202,v252,v202\n"
MU"v203,v252,v203\n"
MF"v[28:31],a[108:111],v[96:99],v[28:31]\n"
MU"v204,v252,v204\n"
MU"v205,v252,v205\n"
MU"v206,v252,v206\n"
MF"v[68:71],a[108:111],v[128:131],v[68:71]\n"
WT"lgkmcnt(3)\n"
RD"a[72:75],v226 offset:2048\n"
MU"v207,v252,v207\n"
PK"v200,v200,v201\n"
MF"v[32:35],a[112:115],v[96:99],v[32:35]\n"
PK"v201,v202,v203\n"
PK"v202,v204,v205\n"
PK"v203,v206,v207\n"
MF"v[72:75],a[112:115],v[128:131],v[72:75]\n"
BS"v[200:203],v224,s[8:11],s51 offen offset:3072 sc1\n"
MF"v[36:39],a[116:119],v[96:99],v[36:39]\n"
RD"a[76:79],v226 offset:3072\n"
"v_readlane_b32 s43,v228,s42\n"
MF"v[76:79],a[116:119],v[128:131],v[76:79]\n"
RD"a[80:83],v226 offset:4096\n"
MF"v[0:3],a[120:123],v[104:107],v[0:3]\n"
MF"v[40:43],a[120:123],v[136:139],v[40:43]\n"
WT"lgkmcnt(3)\n"
RD"a[84:87],v226 offset:5120\n"
MF"v[4:7],a[124:127],v[104:107],v[4:7]\n"
"s_mov_b32 m0,s40\n"
MF"v[44:47],a[124:127],v[136:139],v[44:47]\n"
RD"a[88:91],v226 offset:6144\n"
MF"v[8:11],a[64:67],v[104:107],v[8:11]\n"
"v_readlane_b32 s44,v229,s42\n"
MF"v[48:51],a[64:67],v[136:139],v[48:51]\n"
RD"a[92:95],v226 offset:7168\n"
MF"v[12:15],a[68:71],v[104:107],v[12:15]\n"
SA"s42,s42,1\n"
BL"v225,s[8:11],s43 offen offset:0 sc1 lds\n"
MF"v[52:55],a[68:71],v[136:139],v[52:55]\n"
WT"lgkmcnt(3)\n"
RD"a[96:99],v227 offset:0\n"
MF"v[16:19],a[72:75],v[104:107],v[16:19]\n"
MF"v[56:59],a[72:75],v[136:139],v[56:59]\n"
RD"a[100:103],v227 offset:1024\n"
MF"v[20:23],a[76:79],v[104:107],v[20:23]\n"
MF"v[60:63],a[76:79],v[136:139],v[60:63]\n"
RD"a[104:107],v227 offset:2048\n"
MF"v[24:27],a[80:83],v[104:107],v[24:27]\n"
MF"v[64:67],a[80:83],v[136:139],v[64:67]\n"
WT"lgkmcnt(3)\n"
RD"a[108:111],v227 offset:3072\n"
MF"v[28:31],a[84:87],v[104:107],v[28:31]\n"
BL"v225,s[8:11],s43 offen offset:1024 sc1 lds\n"
MF"v[68:71],a[84:87],v[136:139],v[68:71]\n"
RD"a[112:115],v227 offset:4096\n"
MF"v[32:35],a[88:91],v[104:107],v[32:35]\n"
MF"v[72:75],a[88:91],v[136:139],v[72:75]\n"
RD"a[116:119],v227 offset:5120\n"
MF"v[36:39],a[92:95],v[104:107],v[36:39]\n"
MF"v[76:79],a[92:95],v[136:139],v[76:79]\n"
WT"lgkmcnt(3)\n"
RD"a[120:123],v227 offset:6144\n"
MF"v[84:87],a[96:99],v[144:147],0\n"
BL"v225,s[8:11],s43 offen offset:2048 sc1 lds\n"
MF"v[164:167],a[96:99],v[176:179],0\n"
RD"a[124:127],v227 offset:7168\n"
MF"v[92:95],a[100:103],v[144:147],0\n"
MF"v[172:175],a[100:103],v[176:179],0\n"
RD"a[64:67],v227 offset:8192\n"
MF"v[100:103],a[104:107],v[144:147],0\n"
MF"v[180:183],a[104:107],v[176:179],0\n"
WT"lgkmcnt(3)\n"
RD"a[68:71],v227 offset:9216\n"
MF"v[108:111],a[108:111],v[144:147],0\n"
MF"v[188:191],a[108:111],v[176:179],0\n"
RD"a[72:75],v227 offset:10240\n"
MF"v[116:119],a[112:115],v[144:147],0\n"
BL"v225,s[8:11],s43 offen offset:3072 sc1 lds\n"
MF"v[196:199],a[112:115],v[176:179],0\n"
RD"a[76:79],v227 offset:11264\n"
MF"v[124:127],a[116:119],v[144:147],0\n"
SA"m0,s40,16384\n"
MF"v[204:207],a[116:119],v[176:179],0\n"
WT"lgkmcnt(3)\n"
RD"a[80:83],v227 offset:12288\n"
MF"v[132:135],a[120:123],v[144:147],0\n"
MF"v[208:211],a[120:123],v[176:179],0\n"
RD"a[84:87],v227 offset:13312\n"
MF"v[140:143],a[124:127],v[144:147],0\n"
BL"v225,s[8:11],s44 offen offset:0 sc1 lds\n"
MF"v[212:215],a[124:127],v[176:179],0\n"
SA"s40,s40,32768\n"
RD"a[88:91],v227 offset:14336\n"
MF"v[148:151],a[64:67],v[144:147],0\n"
"s_cmp_eq_u32 s40,s46\n"
"s_cselect_b32 s40,s47,s40\n"
MF"v[216:219],a[64:67],v[176:179],0\n"
WT"lgkmcnt(3)\n"
RD"a[92:95],v227 offset:15360\n"
MF"v[156:159],a[68:71],v[144:147],0\n"
MF"v[220:223],a[68:71],v[176:179],0\n"
RD"a[96:99],v227 offset:16384\n"
MF"v[84:87],a[72:75],v[152:155],v[84:87]\n"
MF"v[164:167],a[72:75],v[184:187],v[164:167]\n"
RD"a[100:103],v227 offset:17408\n"
MF"v[92:95],a[76:79],v[152:155],v[92:95]\n"
BL"v225,s[8:11],s44 offen offset:1024 sc1 lds\n"
MF"v[172:175],a[76:79],v[184:187],v[172:175]\n"
WT"lgkmcnt(3)\n"
RD"a[104:107],v227 offset:18432\n"
MF"v[100:103],a[80:83],v[152:155],v[100:103]\n"
MF"v[180:183],a[80:83],v[184:187],v[180:183]\n"
RD"a[108:111],v227 offset:19456\n"
MF"v[108:111],a[84:87],v[152:155],v[108:111]\n"
MF"v[188:191],a[84:87],v[184:187],v[188:191]\n"
RD"a[112:115],v227 offset:20480\n"
MF"v[116:119],a[88:91],v[152:155],v[116:119]\n"
BL"v225,s[8:11],s44 offen offset:2048 sc1 lds\n"
MF"v[196:199],a[88:91],v[184:187],v[196:199]\n"
WT"lgkmcnt(3)\n"
RD"a[116:119],v227 offset:21504\n"
MF"v[124:127],a[92:95],v[152:155],v[124:127]\n"
WT"vmcnt(7)\n"
"buffer_store_dwordx2 v[232:233],v230,s[8:11],s62 offen sc1\n"
MF"v[204:207],a[92:95],v[184:187],v[204:207]\n"
RD"a[120:123],v227 offset:22528\n"
MF"v[132:135],a[96:99],v[152:155],v[132:135]\n"
MF"v[208:211],a[96:99],v[184:187],v[208:211]\n"
RD"a[124:127],v227 offset:23552\n"
MF"v[140:143],a[100:103],v[152:155],v[140:143]\n"
"v_readlane_b32 s43,v228,s42\n"
MF"v[212:215],a[100:103],v[184:187],v[212:215]\n"
WT"lgkmcnt(3)\n"
RD"a[64:67],v227 offset:24576\n"
MF"v[148:151],a[104:107],v[152:155],v[148:151]\n"
BL"v225,s[8:11],s44 offen offset:3072 sc1 lds\n"
MF"v[216:219],a[104:107],v[184:187],v[216:219]\n"
RD"a[68:71],v227 offset:25600\n"
MF"v[156:159],a[108:111],v[152:155],v[156:159]\n"
"s_mov_b32 m0,s40\n"
MF"v[220:223],a[108:111],v[184:187],v[220:223]\n"
RD"a[72:75],v227 offset:26624\n"
MF"v[84:87],a[112:115],v[160:163],v[84:87]\n"
"s_lshr_b32 s89,s31,3\n"
"v_readlane_b32 s44,v229,s42\n"
MF"v[164:167],a[112:115],v[192:195],v[164:167]\n"
WT"lgkmcnt(3)\n"
RD"a[76:79],v227 offset:27648\n"
"s_lshl_b32 s89,s89,4\n"
MF"v[92:95],a[116:119],v[160:163],v[92:95]\n"
SA"s42,s42,1\n"
BL"v225,s[8:11],s43 offen offset:0 sc1 lds\n"
MF"v[172:175],a[116:119],v[192:195],v[172:175]\n"
SA"s89,s89,s30\n"
RD"a[80:83],v227 offset:28672\n"
"s_lshl_b32 s89,s89,3\n"
MF"v[100:103],a[120:123],v[160:163],v[100:103]\n"
SA"s89,s89,s32\n"
MF"v[180:183],a[120:123],v[192:195],v[180:183]\n"
"s_mul_i32 s88,s89,5120\n"
RD"a[84:87],v227 offset:29696\n"
MF"v[108:111],a[124:127],v[160:163],v[108:111]\n"
SA"s88,s88,59506688\n"
MF"v[188:191],a[124:127],v[192:195],v[188:191]\n"
WT"lgkmcnt(3)\n"
RD"a[88:91],v227 offset:30720\n"
"s_lshl_b32 s89,s89,6\n"
MF"v[116:119],a[64:67],v[160:163],v[116:119]\n"
SA"s89,s89,80478208\n"
MF"v[196:199],a[64:67],v[192:195],v[196:199]\n"
RD"a[92:95],v227 offset:31744\n"
"v_bfe_u32 v239,v224,1,6\n"
MF"v[124:127],a[68:71],v[160:163],v[124:127]\n"
BL"v225,s[8:11],s43 offen offset:1024 sc1 lds\n"
MF"v[204:207],a[68:71],v[192:195],v[204:207]\n"
RD"a[96:99],v226 offset:0\n"
"v_bfe_u32 v242,v224,7,1\n"
MF"v[132:135],a[72:75],v[160:163],v[132:135]\n"
"v_lshl_add_u32 v239,v242,11,v239\n"
"v_lshrrev_b32 v242,8,v224\n"
MF"v[208:211],a[72:75],v[192:195],v[208:211]\n"
WT"lgkmcnt(3)\n"
RD"a[100:103],v226 offset:1024\n"
"v_lshl_add_u32 v239,v242,27,v239\n"
MF"v[140:143],a[76:79],v[160:163],v[140:143]\n"
MF"v[212:215],a[76:79],v[192:195],v[212:215]\n"
RD"a[104:107],v226 offset:2048\n"
MF"v[148:151],a[80:83],v[160:163],v[148:151]\n"
BL"v225,s[8:11],s43 offen offset:2048 sc1 lds\n"
MF"v[216:219],a[80:83],v[192:195],v[216:219]\n"
RD"a[108:111],v226 offset:3072\n"
MF"v[156:159],a[84:87],v[160:163],v[156:159]\n"
MF"v[220:223],a[84:87],v[192:195],v[220:223]\n"
WT"lgkmcnt(3)\n"
RD"a[112:115],v226 offset:4096\n"
MF"v[84:87],a[88:91],v[168:171],v[84:87]\n"
MF"v[164:167],a[88:91],v[200:203],v[164:167]\n"
RD"a[116:119],v226 offset:5120\n"
MF"v[92:95],a[92:95],v[168:171],v[92:95]\n"
MF"v[172:175],a[92:95],v[200:203],v[172:175]\n"
RD"a[120:123],v226 offset:6144\n"
MF"v[100:103],a[96:99],v[168:171],v[100:103]\n"
BL"v225,s[8:11],s43 offen offset:3072 sc1 lds\n"
MF"v[180:183],a[96:99],v[200:203],v[180:183]\n"
WT"lgkmcnt(3)\n"
RD"a[124:127],v226 offset:7168\n"
MF"v[108:111],a[100:103],v[168:171],v[108:111]\n"
SA"m0,s40,16384\n"
MF"v[188:191],a[100:103],v[200:203],v[188:191]\n"
RD"a[64:67],v226 offset:8192\n"
MF"v[116:119],a[104:107],v[168:171],v[116:119]\n"
MF"v[196:199],a[104:107],v[200:203],v[196:199]\n"
RD"a[68:71],v226 offset:9216\n"
MF"v[124:127],a[108:111],v[168:171],v[124:127]\n"
BL"v225,s[8:11],s44 offen offset:0 sc1 lds\n"
MF"v[204:207],a[108:111],v[200:203],v[204:207]\n"
WT"lgkmcnt(3)\n"
RD"a[72:75],v226 offset:10240\n"
SA"s40,s40,32768\n"
MF"v[132:135],a[112:115],v[168:171],v[132:135]\n"
"s_cmp_eq_u32 s40,s46\n"
"s_cselect_b32 s40,s47,s40\n"
MF"v[208:211],a[112:115],v[200:203],v[208:211]\n"
RD"a[76:79],v226 offset:11264\n"
MF"v[140:143],a[116:119],v[168:171],v[140:143]\n"
MF"v[212:215],a[116:119],v[200:203],v[212:215]\n"
RD"a[80:83],v226 offset:12288\n"
MF"v[148:151],a[120:123],v[168:171],v[148:151]\n"
MF"v[216:219],a[120:123],v[200:203],v[216:219]\n"
WT"lgkmcnt(3)\n"
RD"a[84:87],v226 offset:13312\n"
MF"v[156:159],a[124:127],v[168:171],v[156:159]\n"
BL"v225,s[8:11],s44 offen offset:1024 sc1 lds\n"
MF"v[220:223],a[124:127],v[200:203],v[220:223]\n"
RD"a[88:91],v226 offset:14336\n"
MF"v[0:3],a[64:67],a[0:3],v[0:3]\n"
MF"v[40:43],a[64:67],a[4:7],v[40:43]\n"
RD"a[92:95],v226 offset:15360\n"
MF"v[4:7],a[68:71],a[0:3],v[4:7]\n"
MF"v[44:47],a[68:71],a[4:7],v[44:47]\n"
WT"lgkmcnt(3)\n"
RD"a[96:99],v226 offset:16384\n"
MF"v[8:11],a[72:75],a[0:3],v[8:11]\n"
BL"v225,s[8:11],s44 offen offset:2048 sc1 lds\n"
MF"v[48:51],a[72:75],a[4:7],v[48:51]\n"
RD"a[100:103],v226 offset:17408\n"
MF"v[12:15],a[76:79],a[0:3],v[12:15]\n"
MF"v[52:55],a[76:79],a[4:7],v[52:55]\n"
RD"a[104:107],v226 offset:18432\n"
MF"v[16:19],a[80:83],a[0:3],v[16:19]\n"
MF"v[56:59],a[80:83],a[4:7],v[56:59]\n"
WT"lgkmcnt(3)\n"
RD"a[108:111],v226 offset:19456\n"
MF"v[20:23],a[84:87],a[0:3],v[20:23]\n"
MF"v[60:63],a[84:87],a[4:7],v[60:63]\n"
RD"a[112:115],v226 offset:20480\n"
MF"v[24:27],a[88:91],a[0:3],v[24:27]\n"
BL"v225,s[8:11],s44 offen offset:3072 sc1 lds\n"
MF"v[64:67],a[88:91],a[4:7],v[64:67]\n"
RD"a[116:119],v226 offset:21504\n"
MF"v[28:31],a[92:95],a[0:3],v[28:31]\n"
MF"v[68:71],a[92:95],a[4:7],v[68:71]\n"
WT"lgkmcnt(3)\n"
RD"a[120:123],v226 offset:22528\n"
MF"v[32:35],a[96:99],a[0:3],v[32:35]\n"
MF"v[72:75],a[96:99],a[4:7],v[72:75]\n"
RD"a[124:127],v226 offset:23552\n"
MF"v[36:39],a[100:103],a[0:3],v[36:39]\n"
MF"v[76:79],a[100:103],a[4:7],v[76:79]\n"
RD"a[64:67],v226 offset:24576\n"
MF"v[0:3],a[104:107],a[8:11],v[0:3]\n"
MF"v[40:43],a[104:107],a[12:15],v[40:43]\n"
WT"lgkmcnt(3)\n"
RD"a[68:71],v226 offset:25600\n"
MF"v[4:7],a[108:111],a[8:11],v[4:7]\n"
MF"v[44:47],a[108:111],a[12:15],v[44:47]\n"
RD"a[72:75],v226 offset:26624\n"
MF"v[8:11],a[112:115],a[8:11],v[8:11]\n"
MF"v[48:51],a[112:115],a[12:15],v[48:51]\n"
RD"a[76:79],v226 offset:27648\n"
MF"v[12:15],a[116:119],a[8:11],v[12:15]\n"
MF"v[52:55],a[116:119],a[12:15],v[52:55]\n"
WT"lgkmcnt(3)\n"
RD"a[80:83],v226 offset:28672\n"
MF"v[16:19],a[120:123],a[8:11],v[16:19]\n"
MF"v[56:59],a[120:123],a[12:15],v[56:59]\n"
RD"a[84:87],v226 offset:29696\n"
MF"v[20:23],a[124:127],a[8:11],v[20:23]\n"
MF"v[60:63],a[124:127],a[12:15],v[60:63]\n"
RD"a[88:91],v226 offset:30720\n"
MF"v[24:27],a[64:67],a[8:11],v[24:27]\n"
MF"v[64:67],a[64:67],a[12:15],v[64:67]\n"
WT"lgkmcnt(3)\n"
RD"a[92:95],v226 offset:31744\n"
MF"v[28:31],a[68:71],a[8:11],v[28:31]\n"
"s_barrier\n"
MF"v[68:71],a[68:71],a[12:15],v[68:71]\n"
RD"a[96:99],v235 offset:0\n"
MF"v[32:35],a[72:75],a[8:11],v[32:35]\n"
MF"v[72:75],a[72:75],a[12:15],v[72:75]\n"
RD"a[100:103],v235 offset:1024\n"
MF"v[36:39],a[76:79],a[8:11],v[36:39]\n"
MF"v[76:79],a[76:79],a[12:15],v[76:79]\n"
WT"lgkmcnt(3)\n"
RD"a[104:107],v235 offset:2048\n"
MF"v[0:3],a[80:83],a[16:19],v[0:3]\n"
MF"v[40:43],a[80:83],a[20:23],v[40:43]\n"
RD"a[108:111],v235 offset:3072\n"
MF"v[4:7],a[84:87],a[16:19],v[4:7]\n"
MF"v[44:47],a[84:87],a[20:23],v[44:47]\n"
RD"a[112:115],v235 offset:4096\n"
MF"v[8:11],a[88:91],a[16:19],v[8:11]\n"
MF"v[48:51],a[88:91],a[20:23],v[48:51]\n"
WT"lgkmcnt(3)\n"
RD"a[116:119],v235 offset:5120\n"
MF"v[12:15],a[92:95],a[16:19],v[12:15]\n"
MF"v[52:55],a[92:95],a[20:23],v[52:55]\n"
RD"a[120:123],v235 offset:6144\n"
MF"v[16:19],a[96:99],a[16:19],v[16:19]\n"
MF"v[56:59],a[96:99],a[20:23],v[56:59]\n"
RD"a[124:127],v235 offset:7168\n"
MF"v[20:23],a[100:103],a[16:19],v[20:23]\n"
MF"v[60:63],a[100:103],a[20:23],v[60:63]\n"
WT"lgkmcnt(3)\n"
RD"a[64:67],v235 offset:8192\n"
MF"v[24:27],a[104:107],a[16:19],v[24:27]\n"
MF"v[64:67],a[104:107],a[20:23],v[64:67]\n"
RD"a[68:71],v235 offset:9216\n"
"v_bfe_u32 v238,v224,4,2\n"
MF"v[28:31],a[108:111],a[16:19],v[28:31]\n"
"s_andn2_b32 s63,s62,255\n"
"v_lshlrev_b32 v238,6,v238\n"
MF"v[68:71],a[108:111],a[20:23],v[68:71]\n"
RD"a[72:75],v235 offset:10240\n"
MF"v[32:35],a[112:115],a[16:19],v[32:35]\n"
"buffer_load_dwordx2 v[240:241],v238,s[8:11],s63 offen sc1\n"
MF"v[72:75],a[112:115],a[20:23],v[72:75]\n"
WT"lgkmcnt(3)\n"
RD"a[76:79],v235 offset:11264\n"
MF"v[36:39],a[116:119],a[16:19],v[36:39]\n"
MF"v[76:79],a[116:119],a[20:23],v[76:79]\n"
RD"a[80:83],v235 offset:12288\n"
MF"v[0:3],a[120:123],a[24:27],v[0:3]\n"
MF"v[40:43],a[120:123],a[28:31],v[40:43]\n"
RD"a[84:87],v235 offset:13312\n"
MF"v[4:7],a[124:127],a[24:27],v[4:7]\n"
MF"v[44:47],a[124:127],a[28:31],v[44:47]\n"
RD"a[88:91],v235 offset:14336\n"
"LTJ_%=:\n"
WT"lgkmcnt(6)\n"
MF"v[8:11],a[64:67],a[24:27],v[8:11]\n"
MF"v[48:51],a[64:67],a[28:31],v[48:51]\n"
WT"lgkmcnt(3)\n"
RD"a[92:95],v235 offset:15360\n"
MF"v[12:15],a[68:71],a[24:27],v[12:15]\n"
MF"v[52:55],a[68:71],a[28:31],v[52:55]\n"
RD"a[96:99],v235 offset:16384\n"
MF"v[16:19],a[72:75],a[24:27],v[16:19]\n"
MF"v[56:59],a[72:75],a[28:31],v[56:59]\n"
RD"a[100:103],v235 offset:17408\n"
MF"v[20:23],a[76:79],a[24:27],v[20:23]\n"
MF"v[60:63],a[76:79],a[28:31],v[60:63]\n"
WT"lgkmcnt(3)\n"
RD"a[104:107],v235 offset:26624\n"
MF"v[24:27],a[80:83],a[24:27],v[24:27]\n"
MF"v[64:67],a[80:83],a[28:31],v[64:67]\n"
RD"a[108:111],v235 offset:27648\n"
MF"v[28:31],a[84:87],a[24:27],v[28:31]\n"
WT"vmcnt(1)\n"
"s_barrier\n"
MF"v[68:71],a[84:87],a[28:31],v[68:71]\n"
RD"a[112:115],v236 offset:4096\n"
MF"v[32:35],a[88:91],a[24:27],v[32:35]\n"
MF"v[72:75],a[88:91],a[28:31],v[72:75]\n"
WT"lgkmcnt(3)\n"
RD"a[116:119],v236 offset:5120\n"
MF"v[36:39],a[92:95],a[24:27],v[36:39]\n"
MF"v[76:79],a[92:95],a[28:31],v[76:79]\n"
RD"a[120:123],v236 offset:14336\n"
MF"v[0:3],a[96:99],a[180:183],v[0:3]\n"
MF"v[40:43],a[96:99],a[184:187],v[40:43]\n"
RD"a[124:127],v236 offset:15360\n"
MF"v[4:7],a[100:103],a[180:183],v[4:7]\n"
MF"v[44:47],a[100:103],a[184:187],v[44:47]\n"
WT"lgkmcnt(3)\n"
RD"a[64:67],v236 offset:24576\n"
MF"v[0:3],a[104:107],a[188:191],v[0:3]\n"
MF"v[40:43],a[104:107],a[192:195],v[40:43]\n"
RD"a[68:71],v236 offset:25600\n"
MF"v[4:7],a[108:111],a[188:191],v[4:7]\n"
MF"v[44:47],a[108:111],a[192:195],v[44:47]\n"
RD"a[72:75],v237 offset:2048\n"
MF"v[0:3],a[112:115],a[196:199],v[0:3]\n"
MF"v[40:43],a[112:115],a[200:203],v[40:43]\n"
WT"lgkmcnt(3)\n"
RD"a[76:79],v237 offset:3072\n"
MF"v[4:7],a[116:119],a[196:199],v[4:7]\n"
MF"v[44:47],a[116:119],a[200:203],v[44:47]\n"
RD"a[80:83],v237 offset:12288\n"
MF"v[0:3],a[120:123],a[204:207],v[0:3]\n"
MF"v[40:43],a[120:123],a[208:211],v[40:43]\n"
SA"s48,s60,32768\n"
RD"a[84:87],v237 offset:13312\n"
MF"v[4:7],a[124:127],a[204:207],v[4:7]\n"
"s_and_b32 s48,s48,0x18000\n"
MF"v[44:47],a[124:127],a[208:211],v[44:47]\n"
WT"lgkmcnt(3)\n"
RD"a[88:91],v237 offset:22528\n"
"s_andn2_b32 s49,s60,0x18000\n"
MF"v[0:3],a[64:67],a[212:215],v[0:3]\n"
"s_or_b32 s52,s48,s49\n"
WT"vmcnt(0)\n"
"v_cmp_eq_u64 vcc,s[28:29],v[240:241]\n"
"s_cmp_eq_u64 vcc,exec\n"
"s_cbranch_scc0 L1096_%=\n"
"L1097_%=:\n"
MF"v[40:43],a[64:67],a[216:219],v[40:43]\n"
SA"s53,s52,16384\n"
RD"a[92:95],v237 offset:23552\n"
MF"v[4:7],a[68:71],a[212:215],v[4:7]\n"
BL"a[244:247],v224,s[8:11],s52 offen offset:0 sc1\n"
MF"v[44:47],a[68:71],a[216:219],v[44:47]\n"
RD"a[96:99],v235 offset:18432\n"
MF"v[0:3],a[72:75],a[220:223],v[0:3]\n"
MF"v[40:43],a[72:75],a[224:227],v[40:43]\n"
WT"lgkmcnt(3)\n"
RD"a[100:103],v235 offset:19456\n"
MF"v[4:7],a[76:79],a[220:223],v[4:7]\n"
"v_bfe_u32 v243,v224,6,2\n"
MF"v[44:47],a[76:79],a[224:227],v[44:47]\n"
RD"a[104:107],v235 offset:28672\n"
"v_bfe_u32 v244,v224,4,2\n"
MF"v[0:3],a[80:83],a[228:231],v[0:3]\n"
BL"a[248:251],v224,s[8:11],s53 offen offset:0 sc1\n"
MF"v[40:43],a[80:83],a[232:235],v[40:43]\n"
RD"a[108:111],v235 offset:29696\n"
"v_lshrrev_b32 v245,8,v224\n"
MF"v[4:7],a[84:87],a[228:231],v[4:7]\n"
"v_lshlrev_b32 v245,3,v245\n"
"v_sub_u32 v244,v244,v245\n"
MF"v[44:47],a[84:87],a[232:235],v[44:47]\n"
WT"lgkmcnt(3)\n"
RD"a[112:115],v236 offset:6144\n"
"v_and_b32 v245,16,v224\n"
MF"v[0:3],a[88:91],a[236:239],v[0:3]\n"
"v_mov_b32 v246,0x3f80\n"
MF"v[40:43],a[88:91],a[240:243],v[40:43]\n"
RD"a[116:119],v236 offset:7168\n"
"v_lshlrev_b32 v245,v245,v246\n"
MF"v[4:7],a[92:95],a[236:239],v[4:7]\n"
BL"a[252:255],v224,s[8:11],s52 offen offset:1024 sc1\n"
MF"v[44:47],a[92:95],a[240:243],v[44:47]\n"
RD"a[120:123],v236 offset:16384\n"
"v_lshlrev_b32 v246,4,v243\n"
MF"v[8:11],a[96:99],a[180:183],v[8:11]\n"
"v_and_b32 v246,16,v246\n"
"v_and_b32 v247,2,v243\n"
MF"v[48:51],a[96:99],a[184:187],v[48:51]\n"
WT"lgkmcnt(3)\n"
RD"a[124:127],v236 offset:17408\n"
"v_lshl_add_u32 v246,v247,1,v246\n"
MF"v[12:15],a[100:103],a[180:183],v[12:15]\n"
SA"s90,s88,0\n"
BL"a[128:131],v224,s[8:11],s53 offen offset:1024 sc1\n"
MF"v[52:55],a[100:103],a[184:187],v[52:55]\n"
RD"a[64:67],v236 offset:26624\n"
PK"v0,v0,v1\n"
MF"v[8:11],a[104:107],a[188:191],v[8:11]\n"
PK"v1,v2,v3\n"
"v_add3_u32 v246,v246,v244,0\n"
MF"v[48:51],a[104:107],a[192:195],v[48:51]\n"
RD"a[68:71],v236 offset:27648\n"
PK"v2,v4,v5\n"
MF"v[12:15],a[108:111],a[188:191],v[12:15]\n"
PK"v3,v6,v7\n"
"v_ashrrev_i32 v246,1,v246\n"
MF"v[52:55],a[108:111],a[192:195],v[52:55]\n"
WT"lgkmcnt(3)\n"
BS"v[0:3],v224,s[8:11],s90 offen sc1\n"
"v_cmp_eq_u32_e64 vcc,0,v246\n"
MF"v[8:11],a[112:115],a[196:199],v[8:11]\n"
RD"a[72:75],v237 offset:4096\n"
"v_cndmask_b32_e64 v80,0,v245,vcc\n"
MF"v[48:51],a[112:115],a[200:203],v[48:51]\n"
SA"s91,s88,163840\n"
BL"a[132:135],v224,s[8:11],s52 offen offset:2048 sc1\n"
"v_cmp_eq_u32_e64 s[92:93],1,v246\n"
MF"v[12:15],a[116:119],a[196:199],v[12:15]\n"
RD"a[76:79],v237 offset:5120\n"
"v_cndmask_b32_e64 v81,0,v245,s[92:93]\n"
MF"v[52:55],a[116:119],a[200:203],v[52:55]\n"
RD"a[80:83],v237 offset:14336\n"
"v_cmp_eq_u32_e64 s[94:95],2,v246\n"
MF"v[8:11],a[120:123],a[204:207],v[8:11]\n"
PK"v40,v40,v41\n"
PK"v41,v42,v43\n"
"v_cndmask_b32_e64 v82,0,v245,s[94:95]\n"
MF"v[48:51],a[120:123],a[208:211],v[48:51]\n"
WT"lgkmcnt(3)\n"
RD"a[84:87],v237 offset:15360\n"
PK"v42,v44,v45\n"
PK"v43,v46,v47\n"
MF"v[12:15],a[124:127],a[204:207],v[12:15]\n"
BS"v[40:43],v224,s[8:11],s91 offen sc1\n"
"v_cmp_eq_u32_e64 vcc,3,v246\n"
MF"v[52:55],a[124:127],a[208:211],v[52:55]\n"
BL"a[136:139],v224,s[8:11],s53 offen offset:2048 sc1\n"
"v_cndmask_b32_e64 v83,0,v245,vcc\n"
MF"v[8:11],a[64:67],a[212:215],v[8:11]\n"
RD"a[88:91],v237 offset:24576\n"
"v_lshlrev_b32 v246,4,v243\n"
"v_and_b32 v246,16,v246\n"
MF"v[48:51],a[64:67],a[216:219],v[48:51]\n"
RD"a[92:95],v237 offset:25600\n"
"v_and_b32 v247,2,v243\n"
MF"v[12:15],a[68:71],a[212:215],v[12:15]\n"
"v_lshl_add_u32 v246,v247,1,v246\n"
"v_add3_u32 v246,v246,v244,8\n"
MF"v[52:55],a[68:71],a[216:219],v[52:55]\n"
WT"lgkmcnt(3)\n"
RD"a[96:99],v235 offset:20480\n"
"v_ashrrev_i32 v246,1,v246\n"
MF"v[8:11],a[72:75],a[220:223],v[8:11]\n"
BL"a[140:143],v224,s[8:11],s52 offen offset:3072 sc1\n"
MF"v[48:51],a[72:75],a[224:227],v[48:51]\n"
RD"a[100:103],v235 offset:21504\n"
"v_cmp_eq_u32_e64 vcc,0,v246\n"
MF"v[12:15],a[76:79],a[220:223],v[12:15]\n"
"v_cmp_eq_u32_e64 s[92:93],1,v246\n"
"v_cndmask_b32_e64 v88,0,v245,vcc\n"
MF"v[52:55],a[76:79],a[224:227],v[52:55]\n"
RD"a[104:107],v235 offset:30720\n"
"v_cndmask_b32_e64 v89,0,v245,s[92:93]\n"
MF"v[8:11],a[80:83],a[228:231],v[8:11]\n"
BL"a[144:147],v224,s[8:11],s53 offen offset:3072 sc1\n"
MF"v[48:51],a[80:83],a[232:235],v[48:51]\n"
WT"lgkmcnt(3)\n"
RD"a[108:111],v235 offset:31744\n"
"v_cmp_eq_u32_e64 s[94:95],2,v246\n"
MF"v[12:15],a[84:87],a[228:231],v[12:15]\n"
SA"s48,s60,65536\n"
"v_cndmask_b32_e64 v90,0,v245,s[94:95]\n"
"v_cmp_eq_u32_e64 vcc,3,v246\n"
MF"v[52:55],a[84:87],a[232:235],v[52:55]\n"
"s_and_b32 s48,s48,0x18000\n"
RD"a[112:115],v236 offset:8192\n"
"v_cndmask_b32_e64 v91,0,v245,vcc\n"
MF"v[8:11],a[88:91],a[236:239],v[8:11]\n"
"s_andn2_b32 s49,s60,0x18000\n"
"v_lshl_add_u32 v246,v243,3,v244\n"
"v_add_u32 v246,0,v246\n"
MF"v[48:51],a[88:91],a[240:243],v[48:51]\n"
"s_or_b32 s52,s48,s49\n"
RD"a[116:119],v236 offset:9216\n"
"v_ashrrev_i32 v246,1,v246\n"
MF"v[12:15],a[92:95],a[236:239],v[12:15]\n"
SA"s53,s52,16384\n"
BL"a[148:151],v224,s[8:11],s52 offen offset:0 sc1\n"
MF"v[52:55],a[92:95],a[240:243],v[52:55]\n"
WT"lgkmcnt(3)\n"
RD"a[120:123],v236 offset:18432\n"
"v_cmp_eq_u32_e64 vcc,0,v246\n"
MF"v[16:19],a[96:99],a[180:183],v[16:19]\n"
"v_cmp_eq_u32_e64 s[92:93],1,v246\n"
"v_cndmask_b32_e64 v96,0,v245,vcc\n"
MF"v[56:59],a[96:99],a[184:187],v[56:59]\n"
RD"a[124:127],v236 offset:19456\n"
"v_cndmask_b32_e64 v97,0,v245,s[92:93]\n"
MF"v[20:23],a[100:103],a[180:183],v[20:23]\n"
SA"s90,s88,1024\n"
"v_cmp_eq_u32_e64 s[94:95],2,v246\n"
PK"v8,v8,v9\n"
MF"v[60:63],a[100:103],a[184:187],v[60:63]\n"
RD"a[64:67],v236 offset:28672\n"
PK"v9,v10,v11\n"
MF"v[16:19],a[104:107],a[188:191],v[16:19]\n"
BL"a[152:155],v224,s[8:11],s53 offen offset:0 sc1\n"
MF"v[56:59],a[104:107],a[192:195],v[56:59]\n"
WT"lgkmcnt(3)\n"
RD"a[68:71],v236 offset:29696\n"
"v_cndmask_b32_e64 v98,0,v245,s[94:95]\n"
MF"v[20:23],a[108:111],a[188:191],v[20:23]\n"
PK"v10,v12,v13\n"
PK"v11,v14,v15\n"
"v_cmp_eq_u32_e64 vcc,3,v246\n"
MF"v[60:63],a[108:111],a[192:195],v[60:63]\n"
BS"v[8:11],v224,s[8:11],s90 offen sc1\n"
"v_cndmask_b32_e64 v99,0,v245,vcc\n"
MF"v[16:19],a[112:115],a[196:199],v[16:19]\n"
RD"a[72:75],v237 offset:6144\n"
"v_lshl_add_u32 v246,v243,3,v244\n"
MF"v[56:59],a[112:115],a[200:203],v[56:59]\n"
SA"s91,s88,164864\n"
RD"a[76:79],v237 offset:7168\n"
"v_add_u32 v246,4,v246\n"
MF"v[20:23],a[116:119],a[196:199],v[20:23]\n"
BL"a[156:159],v224,s[8:11],s52 offen offset:1024 sc1\n"
"v_ashrrev_i32 v246,1,v246\n"
MF"v[60:63],a[116:119],a[200:203],v[60:63]\n"
WT"lgkmcnt(3)\n"
RD"a[80:83],v237 offset:16384\n"
"v_cmp_eq_u32_e64 vcc,0,v246\n"
MF"v[16:19],a[120:123],a[204:207],v[16:19]\n"
PK"v48,v48,v49\n"
PK"v49,v50,v51\n"
"v_cndmask_b32_e64 v104,0,v245,vcc\n"
MF"v[56:59],a[120:123],a[208:211],v[56:59]\n"
RD"a[84:87],v237 offset:17408\n"
PK"v50,v52,v53\n"
PK"v51,v54,v55\n"
MF"v[20:23],a[124:127],a[204:207],v[20:23]\n"
BS"v[48:51],v224,s[8:11],s91 offen sc1\n"
"v_cmp_eq_u32_e64 s[92:93],1,v246\n"
MF"v[60:63],a[124:127],a[208:211],v[60:63]\n"
BL"a[160:163],v224,s[8:11],s53 offen offset:1024 sc1\n"
"v_cndmask_b32_e64 v105,0,v245,s[92:93]\n"
MF"v[16:19],a[64:67],a[212:215],v[16:19]\n"
"s_mov_b32 s12,s64\n"
RD"a[88:91],v237 offset:26624\n"
"v_cmp_eq_u32_e64 s[94:95],2,v246\n"
MF"v[56:59],a[64:67],a[216:219],v[56:59]\n"
WT"lgkmcnt(3)\n"
RD"a[92:95],v237 offset:27648\n"
"v_cndmask_b32_e64 v106,0,v245,s[94:95]\n"
"v_cmp_eq_u32_e64 vcc,3,v246\n"
MF"v[20:23],a[68:71],a[212:215],v[20:23]\n"
"s_and_b32 s13,s65,0xffff\n"
"v_cndmask_b32_e64 v107,0,v245,vcc\n"
"s_mov_b32 s14,2621440\n"
MF"v[60:63],a[68:71],a[216:219],v[60:63]\n"
"s_mov_b32 s15,0x20000\n"
RD"a[96:99],v235 offset:22528\n"
MF"v[16:19],a[72:75],a[220:223],v[16:19]\n"
"s_mov_b32 s16,s86\n"
BL"a[164:167],v224,s[8:11],s52 offen offset:2048 sc1\n"
MF"v[56:59],a[72:75],a[224:227],v[56:59]\n"
"s_and_b32 s17,s87,0xffff\n"
RD"a[100:103],v235 offset:23552\n"
MF"v[20:23],a[76:79],a[220:223],v[20:23]\n"
"s_mov_b32 s18,2621440\n"
MF"v[60:63],a[76:79],a[224:227],v[60:63]\n"
WT"lgkmcnt(3)\n"
RD"a[104:107],v236 offset:0\n"
"s_mov_b32 s19,0x20000\n"
MF"v[16:19],a[80:83],a[228:231],v[16:19]\n"
"v_bfe_u32 v243,v224,4,4\n"
MF"v[56:59],a[80:83],a[232:235],v[56:59]\n"
"s_lshl_b32 s92,s34,3\n"
RD"a[108:111],v236 offset:1024\n"
MF"v[20:23],a[84:87],a[228:231],v[20:23]\n"
SA"s93,s92,s32\n"
BL"a[168:171],v224,s[8:11],s53 offen offset:2048 sc1\n"
MF"v[60:63],a[84:87],a[232:235],v[60:63]\n"
"s_sub_u32 s92,s32,s92\n"
RD"a[112:115],v236 offset:10240\n"
MF"v[16:19],a[88:91],a[236:239],v[16:19]\n"
SA"s92,s92,120\n"
MF"v[56:59],a[88:91],a[240:243],v[56:59]\n"
WT"lgkmcnt(3)\n"
RD"a[116:119],v236 offset:11264\n"
"v_lshl_add_u32 v244,s93,4,v243\n"
MF"v[20:23],a[92:95],a[236:239],v[20:23]\n"
"v_lshl_add_u32 v245,s92,4,v243\n"
MF"v[60:63],a[92:95],a[240:243],v[60:63]\n"
"s_mul_i32 s93,s93,20480\n"
RD"a[120:123],v236 offset:20480\n"
MF"v[24:27],a[96:99],a[180:183],v[24:27]\n"
"s_mul_i32 s92,s92,20480\n"
BL"a[172:175],v224,s[8:11],s52 offen offset:3072 sc1\n"
MF"v[64:67],a[96:99],a[184:187],v[64:67]\n"
RD"a[124:127],v236 offset:21504\n"
"v_add_u32 v178,s93,v224\n"
MF"v[28:31],a[100:103],a[180:183],v[28:31]\n"
SA"s90,s88,2048\n"
PK"v16,v16,v17\n"
PK"v17,v18,v19\n"
MF"v[68:71],a[100:103],a[184:187],v[68:71]\n"
WT"lgkmcnt(3)\n"
RD"a[64:67],v236 offset:30720\n"
"v_add_u32 v179,s92,v224\n"
MF"v[24:27],a[104:107],a[188:191],v[24:27]\n"
BL"a[176:179],v224,s[8:11],s53 offen offset:3072 sc1\n"
MF"v[64:67],a[104:107],a[192:195],v[64:67]\n"
SA"s48,s60,98304\n"
RD"a[68:71],v236 offset:31744\n"
PK"v18,v20,v21\n"
MF"v[28:31],a[108:111],a[188:191],v[28:31]\n"
"s_and_b32 s48,s48,0x18000\n"
PK"v19,v22,v23\n"
"v_lshrrev_b32 v243,4,v224\n"
MF"v[68:71],a[108:111],a[192:195],v[68:71]\n"
"s_andn2_b32 s49,s60,0x18000\n"
BS"v[16:19],v224,s[8:11],s90 offen sc1\n"
"v_and_b32 v243,48,v243\n"
MF"v[24:27],a[112:115],a[196:199],v[24:27]\n"
"s_or_b32 s52,s48,s49\n"
RD"a[72:75],v237 offset:8192\n"
"v_mul_u32_u24 v176,1280,v244\n"
MF"v[64:67],a[112:115],a[200:203],v[64:67]\n"
WT"lgkmcnt(3)\n"
RD"a[76:79],v237 offset:9216\n"
"v_mul_u32_u24 v177,1280,v245\n"
MF"v[28:31],a[116:119],a[196:199],v[28:31]\n"
SA"s53,s52,16384\n"
BL"a[32:35],v224,s[8:11],s52 offen offset:0 sc1\n"
"v_add_u32 v176,v176,v243\n"
MF"v[68:71],a[116:119],a[200:203],v[68:71]\n"
SA"s91,s88,165888\n"
RD"a[80:83],v237 offset:18432\n"
"v_add_u32 v177,v177,v243\n"
MF"v[24:27],a[120:123],a[204:207],v[24:27]\n"
PK"v56,v56,v57\n"
PK"v57,v58,v59\n"
"v_and_b32 v243,0x300,v224\n"
MF"v[64:67],a[120:123],a[208:211],v[64:67]\n"
RD"a[84:87],v237 offset:19456\n"
PK"v58,v60,v61\n"
PK"v59,v62,v63\n"
MF"v[28:31],a[124:127],a[204:207],v[28:31]\n"
BS"v[56:59],v224,s[8:11],s91 offen sc1\n"
"v_add_u32 v243,2684928,v243\n"
MF"v[68:71],a[124:127],a[208:211],v[68:71]\n"
WT"lgkmcnt(3)\n"
RD"a[88:91],v237 offset:28672\n"
"v_sub_u32 v244,2047,v244\n"
"v_sub_u32 v245,2047,v245\n"
MF"v[24:27],a[64:67],a[212:215],v[24:27]\n"
BL"a[36:39],v224,s[8:11],s53 offen offset:0 sc1\n"
"v_readlane_b32 s92,v234,0\n"
MF"v[64:67],a[64:67],a[216:219],v[64:67]\n"
"s_and_b32 s92,s92,0xffff\n"
RD"a[92:95],v237 offset:29696\n"
"v_cmp_eq_u32 vcc,s92,v244\n"
MF"v[28:31],a[68:71],a[212:215],v[28:31]\n"
"v_cmp_eq_u32_e64 s[94:95],s92,v245\n"
"v_cndmask_b32 v178,v178,v243,vcc\n"
MF"v[68:71],a[68:71],a[216:219],v[68:71]\n"
RD"a[96:99],v235 offset:24576\n"
"v_cndmask_b32_e64 v179,v179,v243,s[94:95]\n"
MF"v[24:27],a[72:75],a[220:223],v[24:27]\n"
"v_add_u32 v243,16,v243\n"
"v_readlane_b32 s92,v234,1\n"
MF"v[64:67],a[72:75],a[224:227],v[64:67]\n"
WT"lgkmcnt(3)\n"
RD"a[100:103],v235 offset:25600\n"
"s_and_b32 s92,s92,0xffff\n"
MF"v[28:31],a[76:79],a[220:223],v[28:31]\n"
BL"a[40:43],v224,s[8:11],s52 offen offset:1024 sc1\n"
MF"v[68:71],a[76:79],a[224:227],v[68:71]\n"
RD"a[104:107],v236 offset:2048\n"
"v_cmp_eq_u32 vcc,s92,v244\n"
MF"v[24:27],a[80:83],a[228:231],v[24:27]\n"
"v_cmp_eq_u32_e64 s[94:95],s92,v245\n"
"v_cndmask_b32 v178,v178,v243,vcc\n"
MF"v[64:67],a[80:83],a[232:235],v[64:67]\n"
RD"a[108:111],v236 offset:3072\n"
"v_cndmask_b32_e64 v179,v179,v243,s[94:95]\n"
MF"v[28:31],a[84:87],a[228:231],v[28:31]\n"
BL"a[44:47],v224,s[8:11],s53 offen offset:1024 sc1\n"
MF"v[68:71],a[84:87],a[232:235],v[68:71]\n"
WT"lgkmcnt(3)\n"
RD"a[112:115],v236 offset:12288\n"
"v_add_u32 v243,16,v243\n"
MF"v[24:27],a[88:91],a[236:239],v[24:27]\n"
"v_readlane_b32 s92,v234,2\n"
"s_and_b32 s92,s92,0xffff\n"
MF"v[64:67],a[88:91],a[240:243],v[64:67]\n"
RD"a[116:119],v236 offset:13312\n"
"v_cmp_eq_u32 vcc,s92,v244\n"
MF"v[28:31],a[92:95],a[236:239],v[28:31]\n"
"v_cmp_eq_u32_e64 s[94:95],s92,v245\n"
MF"v[68:71],a[92:95],a[240:243],v[68:71]\n"
RD"a[120:123],v236 offset:22528\n"
"v_cndmask_b32 v178,v178,v243,vcc\n"
MF"v[32:35],a[96:99],a[180:183],v[32:35]\n"
BL"a[48:51],v224,s[8:11],s52 offen offset:2048 sc1\n"
MF"v[72:75],a[96:99],a[184:187],v[72:75]\n"
WT"lgkmcnt(3)\n"
RD"a[124:127],v236 offset:23552\n"
"v_cndmask_b32_e64 v179,v179,v243,s[94:95]\n"
MF"v[36:39],a[100:103],a[180:183],v[36:39]\n"
SA"s90,s88,3072\n"
"v_add_u32 v243,16,v243\n"
PK"v24,v24,v25\n"
MF"v[76:79],a[100:103],a[184:187],v[76:79]\n"
RD"a[64:67],v237 offset:0\n"
PK"v25,v26,v27\n"
MF"v[32:35],a[104:107],a[188:191],v[32:35]\n"
"v_readlane_b32 s92,v234,3\n"
PK"v26,v28,v29\n"
MF"v[72:75],a[104:107],a[192:195],v[72:75]\n"
"s_and_b32 s92,s92,0xffff\n"
RD"a[68:71],v237 offset:1024\n"
PK"v27,v30,v31\n"
MF"v[36:39],a[108:111],a[188:191],v[36:39]\n"
BL"a[52:55],v224,s[8:11],s53 offen offset:2048 sc1\n"
MF"v[76:79],a[108:111],a[192:195],v[76:79]\n"
WT"lgkmcnt(3)\n"
BS"v[24:27],v224,s[8:11],s90 offen sc1\n"
"v_cmp_eq_u32 vcc,s92,v244\n"
MF"v[32:35],a[112:115],a[196:199],v[32:35]\n"
RD"a[72:75],v237 offset:10240\n"
"v_cmp_eq_u32_e64 s[94:95],s92,v245\n"
MF"v[72:75],a[112:115],a[200:203],v[72:75]\n"
SA"s91,s88,166912\n"
RD"a[76:79],v237 offset:11264\n"
"v_cndmask_b32 v178,v178,v243,vcc\n"
MF"v[36:39],a[116:119],a[196:199],v[36:39]\n"
"v_cndmask_b32_e64 v179,v179,v243,s[94:95]\n"
"v_add_u32 v243,16,v243\n"
MF"v[76:79],a[116:119],a[200:203],v[76:79]\n"
RD"a[80:83],v237 offset:20480\n"
PK"v64,v64,v65\n"
MF"v[32:35],a[120:123],a[204:207],v[32:35]\n"
BL"a[56:59],v224,s[8:11],s52 offen offset:3072 sc1\n"
PK"v65,v66,v67\n"
MF"v[72:75],a[120:123],a[208:211],v[72:75]\n"
WT"lgkmcnt(3)\n"
RD"a[84:87],v237 offset:21504\n"
"v_readlane_b32 s92,v234,4\n"
PK"v66,v68,v69\n"
MF"v[36:39],a[124:127],a[204:207],v[36:39]\n"
"s_and_b32 s92,s92,0xffff\n"
PK"v67,v70,v71\n"
"v_cmp_eq_u32 vcc,s92,v244\n"
MF"v[76:79],a[124:127],a[208:211],v[76:79]\n"
BS"v[64:67],v224,s[8:11],s91 offen sc1\n"
"v_cmp_eq_u32_e64 s[94:95],s92,v245\n"
MF"v[32:35],a[64:67],a[212:215],v[32:35]\n"
RD"a[88:91],v237 offset:30720\n"
"v_cndmask_b32 v178,v178,v243,vcc\n"
MF"v[72:75],a[64:67],a[216:219],v[72:75]\n"
BL"a[60:63],v224,s[8:11],s53 offen offset:3072 sc1\n"
"v_cndmask_b32_e64 v179,v179,v243,s[94:95]\n"
MF"v[36:39],a[68:71],a[212:215],v[36:39]\n"
RD"a[92:95],v237 offset:31744\n"
"v_add_u32 v243,16,v243\n"
MF"v[76:79],a[68:71],a[216:219],v[76:79]\n"
WT"lgkmcnt(3)\n"
RD"a[96:99],v226 offset:8192\n"
"v_readlane_b32 s92,v234,5\n"
MF"v[32:35],a[72:75],a[220:223],v[32:35]\n"
"s_and_b32 s92,s92,0xffff\n"
"v_cmp_eq_u32 vcc,s92,v244\n"
"v_cmp_eq_u32_e64 s[94:95],s92,v245\n"
MF"v[72:75],a[72:75],a[224:227],v[72:75]\n"
RD"a[100:103],v226 offset:9216\n"
"v_cndmask_b32 v178,v178,v243,vcc\n"
MF"v[36:39],a[76:79],a[220:223],v[36:39]\n"
"v_cndmask_b32_e64 v179,v179,v243,s[94:95]\n"
"v_add_u32 v243,16,v243\n"
MF"v[76:79],a[76:79],a[224:227],v[76:79]\n"
RD"a[104:107],v226 offset:18432\n"
"v_readlane_b32 s92,v234,6\n"
MF"v[32:35],a[80:83],a[228:231],v[32:35]\n"
"s_and_b32 s92,s92,0xffff\n"
MF"v[72:75],a[80:83],a[232:235],v[72:75]\n"
WT"lgkmcnt(3)\n"
RD"a[108:111],v226 offset:19456\n"
"v_cmp_eq_u32 vcc,s92,v244\n"
MF"v[36:39],a[84:87],a[228:231],v[36:39]\n"
"v_cmp_eq_u32_e64 s[94:95],s92,v245\n"
MF"v[76:79],a[84:87],a[232:235],v[76:79]\n"
RD"a[112:115],v226 offset:28672\n"
"v_cndmask_b32 v178,v178,v243,vcc\n"
MF"v[32:35],a[88:91],a[236:239],v[32:35]\n"
"v_cndmask_b32_e64 v179,v179,v243,s[94:95]\n"
MF"v[72:75],a[88:91],a[240:243],v[72:75]\n"
RD"a[116:119],v226 offset:29696\n"
"v_add_u32 v243,16,v243\n"
MF"v[36:39],a[92:95],a[236:239],v[36:39]\n"
"v_readlane_b32 s92,v234,7\n"
MF"v[76:79],a[92:95],a[240:243],v[76:79]\n"
WT"vmcnt(31) lgkmcnt(3)\n"
RD"a[120:123],v235 offset:6144\n"
"s_and_b32 s92,s92,0xffff\n"
MF"v[84:87],a[96:99],a[244:247],v[84:87]\n"
WT"vmcnt(30)\n"
"v_cmp_eq_u32 vcc,s92,v244\n"
MF"v[164:167],a[96:99],a[248:251],v[164:167]\n"
RD"a[124:127],v235 offset:7168\n"
"v_cmp_eq_u32_e64 s[94:95],s92,v245\n"
MF"v[92:95],a[100:103],a[244:247],v[92:95]\n"
SA"s90,s88,4096\n"
PK"v32,v32,v33\n"
PK"v33,v34,v35\n"
MF"v[172:175],a[100:103],a[248:251],v[172:175]\n"
WT"vmcnt(29)\n"
RD"a[64:67],v235 offset:16384\n"
"v_cndmask_b32 v178,v178,v243,vcc\n"
MF"v[84:87],a[104:107],a[252:255],v[84:87]\n"
WT"vmcnt(28)\n"
PK"v34,v36,v37\n"
PK"v35,v38,v39\n"
MF"v[164:167],a[104:107],a[128:131],v[164:167]\n"
WT"lgkmcnt(3)\n"
RD"a[68:71],v235 offset:17408\n"
"v_cndmask_b32_e64 v179,v179,v243,s[94:95]\n"
MF"v[92:95],a[108:111],a[252:255],v[92:95]\n"
"s_mul_i32 s92,s33,5\n"
BS"v[32:35],v224,s[8:11],s90 offen sc1\n"
MF"v[172:175],a[108:111],a[128:131],v[172:175]\n"
WT"vmcnt(27)\n"
RD"a[72:75],v235 offset:26624\n"
SA"s93,s92,s30\n"
MF"v[84:87],a[112:115],a[132:135],v[84:87]\n"
WT"vmcnt(25)\n"
SA"s92,s92,4\n"
"s_lshl_b32 s38,s93,6\n"
MF"v[164:167],a[112:115],a[136:139],v[164:167]\n"
"s_lshl_b32 s55,s92,6\n"
RD"a[76:79],v235 offset:27648\n"
PK"v72,v72,v73\n"
MF"v[92:95],a[116:119],a[132:135],v[92:95]\n"
SA"s91,s88,167936\n"
PK"v73,v74,v75\n"
PK"v74,v76,v77\n"
MF"v[172:175],a[116:119],a[136:139],v[172:175]\n"
WT"vmcnt(24) lgkmcnt(3)\n"
RD"a[80:83],v236 offset:4096\n"
PK"v75,v78,v79\n"
MF"v[84:87],a[120:123],a[140:143],v[84:87]\n"
WT"vmcnt(23)\n"
BS"v[72:75],v224,s[8:11],s91 offen sc1\n"
MF"v[164:167],a[120:123],a[144:147],v[164:167]\n"
SA"s55,s55,s45\n"
RD"a[84:87],v236 offset:5120\n"
"s_lshl_b32 s36,s93,10\n"
MF"v[92:95],a[124:127],a[140:143],v[92:95]\n"
"s_lshl_b32 s37,s92,10\n"
SA"s36,s36,65536\n"
SA"s37,s37,65536\n"
MF"v[172:175],a[124:127],a[144:147],v[172:175]\n"
WT"vmcnt(23)\n"
RD"a[88:91],v236 offset:14336\n"
"v_lshrrev_b32 v243,1,v224\n"
MF"v[84:87],a[64:67],a[148:151],v[84:87]\n"
WT"vmcnt(22)\n"
"v_min_u32 v244,312,v243\n"
SA"s37,s37,s45\n"
MF"v[164:167],a[64:67],a[152:155],v[164:167]\n"
WT"lgkmcnt(3)\n"
RD"a[92:95],v236 offset:15360\n"
"v_and_b32 v243,56,v243\n"
MF"v[92:95],a[68:71],a[148:151],v[92:95]\n"
"v_lshlrev_b32 v243,3,v243\n"
MF"v[172:175],a[68:71],a[152:155],v[172:175]\n"
WT"vmcnt(20)\n"
RD"a[96:99],v236 offset:24576\n"
"s_mov_b32 s92,6400\n"
MF"v[84:87],a[72:75],a[156:159],v[84:87]\n"
WT"vmcnt(18)\n"
MF"v[164:167],a[72:75],a[160:163],v[164:167]\n"
RD"a[100:103],v236 offset:25600\n"
MF"v[92:95],a[76:79],a[156:159],v[92:95]\n"
MF"v[172:175],a[76:79],a[160:163],v[172:175]\n"
WT"vmcnt(17) lgkmcnt(3)\n"
RD"a[104:107],v237 offset:2048\n"
MF"v[84:87],a[80:83],a[164:167],v[84:87]\n"
WT"vmcnt(16)\n"
MF"v[164:167],a[80:83],a[168:171],v[164:167]\n"
RD"a[108:111],v237 offset:3072\n"
MF"v[92:95],a[84:87],a[164:167],v[92:95]\n"
MF"v[172:175],a[84:87],a[168:171],v[172:175]\n"
WT"vmcnt(15)\n"
RD"a[112:115],v237 offset:12288\n"
MF"v[84:87],a[88:91],a[172:175],v[84:87]\n"
WT"vmcnt(14)\n"
MF"v[164:167],a[88:91],a[176:179],v[164:167]\n"
WT"lgkmcnt(3)\n"
RD"a[116:119],v237 offset:13312\n"
MF"v[92:95],a[92:95],a[172:175],v[92:95]\n"
MF"v[172:175],a[92:95],a[176:179],v[172:175]\n"
WT"vmcnt(12)\n"
RD"a[120:123],v237 offset:22528\n"
MF"v[84:87],a[96:99],a[32:35],v[84:87]\n"
WT"vmcnt(10)\n"
MF"v[164:167],a[96:99],a[36:39],v[164:167]\n"
RD"a[124:127],v237 offset:23552\n"
MF"v[92:95],a[100:103],a[32:35],v[92:95]\n"
MF"v[172:175],a[100:103],a[36:39],v[172:175]\n"
WT"vmcnt(9) lgkmcnt(3)\n"
RD"a[64:67],v226 offset:10240\n"
MF"v[84:87],a[104:107],a[40:43],v[84:87]\n"
WT"vmcnt(8)\n"
MF"v[164:167],a[104:107],a[44:47],v[164:167]\n"
RD"a[68:71],v226 offset:11264\n"
MF"v[92:95],a[108:111],a[40:43],v[92:95]\n"
MF"v[172:175],a[108:111],a[44:47],v[172:175]\n"
WT"vmcnt(7)\n"
RD"a[72:75],v226 offset:20480\n"
MF"v[84:87],a[112:115],a[48:51],v[84:87]\n"
WT"vmcnt(6)\n"
"buffer_load_dwordx2 v[240:241],v244,s[8:11],s92 offen sc1\n"
MF"v[164:167],a[112:115],a[52:55],v[164:167]\n"
WT"lgkmcnt(3)\n"
RD"a[76:79],v226 offset:21504\n"
MF"v[92:95],a[116:119],a[48:51],v[92:95]\n"
MF"v[172:175],a[116:119],a[52:55],v[172:175]\n"
WT"vmcnt(5)\n"
RD"a[80:83],v226 offset:30720\n"
MF"v[84:87],a[120:123],a[56:59],v[84:87]\n"
WT"vmcnt(3)\n"
MF"v[164:167],a[120:123],a[60:63],v[164:167]\n"
RD"a[84:87],v226 offset:31744\n"
MF"v[92:95],a[124:127],a[56:59],v[92:95]\n"
MF"v[172:175],a[124:127],a[60:63],v[172:175]\n"
WT"lgkmcnt(3)\n"
RD"a[88:91],v235 offset:8192\n"
MF"v[100:103],a[64:67],a[244:247],v[100:103]\n"
MF"v[180:183],a[64:67],a[248:251],v[180:183]\n"
RD"a[92:95],v235 offset:9216\n"
MF"v[108:111],a[68:71],a[244:247],v[108:111]\n"
SA"s90,s88,327680\n"
PK"v84,v84,v85\n"
PK"v85,v86,v87\n"
MF"v[188:191],a[68:71],a[248:251],v[188:191]\n"
RD"a[96:99],v235 offset:18432\n"
PK"v86,v92,v93\n"
MF"v[100:103],a[72:75],a[252:255],v[100:103]\n"
PK"v87,v94,v95\n"
MF"v[180:183],a[72:75],a[128:131],v[180:183]\n"
WT"lgkmcnt(3)\n"
RD"a[100:103],v235 offset:19456\n"
MF"v[108:111],a[76:79],a[252:255],v[108:111]\n"
BS"v[84:87],v224,s[8:11],s90 offen sc1\n"
MF"v[188:191],a[76:79],a[128:131],v[188:191]\n"
RD"a[104:107],v235 offset:28672\n"
MF"v[100:103],a[80:83],a[132:135],v[100:103]\n"
MF"v[180:183],a[80:83],a[136:139],v[180:183]\n"
SA"s91,s88,491520\n"
RD"a[108:111],v235 offset:29696\n"
PK"v164,v164,v165\n"
MF"v[108:111],a[84:87],a[132:135],v[108:111]\n"
PK"v165,v166,v167\n"
PK"v166,v172,v173\n"
MF"v[188:191],a[84:87],a[136:139],v[188:191]\n"
WT"lgkmcnt(3)\n"
RD"a[112:115],v236 offset:6144\n"
PK"v167,v174,v175\n"
MF"v[100:103],a[88:91],a[140:143],v[100:103]\n"
BS"v[164:167],v224,s[8:11],s91 offen sc1\n"
MF"v[180:183],a[88:91],a[144:147],v[180:183]\n"
RD"a[116:119],v236 offset:7168\n"
MF"v[108:111],a[92:95],a[140:143],v[108:111]\n"
MF"v[188:191],a[92:95],a[144:147],v[188:191]\n"
RD"a[120:123],v236 offset:16384\n"
MF"v[100:103],a[96:99],a[148:151],v[100:103]\n"
MF"v[180:183],a[96:99],a[152:155],v[180:183]\n"
WT"lgkmcnt(3)\n"
RD"a[124:127],v236 offset:17408\n"
MF"v[108:111],a[100:103],a[148:151],v[108:111]\n"
MF"v[188:191],a[100:103],a[152:155],v[188:191]\n"
RD"a[64:67],v236 offset:26624\n"
MF"v[100:103],a[104:107],a[156:159],v[100:103]\n"
MF"v[180:183],a[104:107],a[160:163],v[180:183]\n"
RD"a[68:71],v236 offset:27648\n"
MF"v[108:111],a[108:111],a[156:159],v[108:111]\n"
MF"v[188:191],a[108:111],a[160:163],v[188:191]\n"
WT"lgkmcnt(3)\n"
RD"a[72:75],v237 offset:4096\n"
MF"v[100:103],a[112:115],a[164:167],v[100:103]\n"
MF"v[180:183],a[112:115],a[168:171],v[180:183]\n"
RD"a[76:79],v237 offset:5120\n"
MF"v[108:111],a[116:119],a[164:167],v[108:111]\n"
MF"v[188:191],a[116:119],a[168:171],v[188:191]\n"
RD"a[80:83],v237 offset:14336\n"
MF"v[100:103],a[120:123],a[172:175],v[100:103]\n"
MF"v[180:183],a[120:123],a[176:179],v[180:183]\n"
WT"lgkmcnt(3)\n"
RD"a[84:87],v237 offset:15360\n"
MF"v[108:111],a[124:127],a[172:175],v[108:111]\n"
MF"v[188:191],a[124:127],a[176:179],v[188:191]\n"
RD"a[88:91],v237 offset:24576\n"
MF"v[100:103],a[64:67],a[32:35],v[100:103]\n"
WT"vmcnt(3)\n"
"buffer_store_dwordx2 v[232:233],v239,s[8:11],s89 offen sc1\n"
MF"v[180:183],a[64:67],a[36:39],v[180:183]\n"
RD"a[92:95],v237 offset:25600\n"
MF"v[108:111],a[68:71],a[32:35],v[108:111]\n"
MF"v[188:191],a[68:71],a[36:39],v[188:191]\n"
WT"lgkmcnt(3)\n"
RD"a[96:99],v226 offset:12288\n"
MF"v[100:103],a[72:75],a[40:43],v[100:103]\n"
MF"v[180:183],a[72:75],a[44:47],v[180:183]\n"
RD"a[100:103],v226 offset:13312\n"
MF"v[108:111],a[76:79],a[40:43],v[108:111]\n"
MF"v[188:191],a[76:79],a[44:47],v[188:191]\n"
RD"a[104:107],v226 offset:22528\n"
MF"v[100:103],a[80:83],a[48:51],v[100:103]\n"
MF"v[180:183],a[80:83],a[52:55],v[180:183]\n"
WT"lgkmcnt(3)\n"
RD"a[108:111],v226 offset:23552\n"
MF"v[108:111],a[84:87],a[48:51],v[108:111]\n"
MF"v[188:191],a[84:87],a[52:55],v[188:191]\n"
RD"a[112:115],v235 offset:0\n"
MF"v[100:103],a[88:91],a[56:59],v[100:103]\n"
MF"v[180:183],a[88:91],a[60:63],v[180:183]\n"
RD"a[116:119],v235 offset:1024\n"
MF"v[108:111],a[92:95],a[56:59],v[108:111]\n"
MF"v[188:191],a[92:95],a[60:63],v[188:191]\n"
WT"lgkmcnt(3)\n"
RD"a[120:123],v235 offset:10240\n"
MF"v[116:119],a[96:99],a[244:247],v[116:119]\n"
MF"v[196:199],a[96:99],a[248:251],v[196:199]\n"
RD"a[124:127],v235 offset:11264\n"
MF"v[124:127],a[100:103],a[244:247],v[124:127]\n"
SA"s90,s88,328704\n"
PK"v100,v100,v101\n"
PK"v101,v102,v103\n"
MF"v[204:207],a[100:103],a[248:251],v[204:207]\n"
RD"a[64:67],v235 offset:20480\n"
PK"v102,v108,v109\n"
MF"v[116:119],a[104:107],a[252:255],v[116:119]\n"
WT"vmcnt(3)\n"
"v_cmp_eq_u64 vcc,s[28:29],v[240:241]\n"
"s_cmp_eq_u64 vcc,exec\n"
"s_cbranch_scc0 L1099_%=\n"
"L1100_%=:\n"
MF"v[196:199],a[104:107],a[128:131],v[196:199]\n"
WT"lgkmcnt(3)\n"
RD"a[68:71],v235 offset:21504\n"
PK"v103,v110,v111\n"
MF"v[124:127],a[108:111],a[252:255],v[124:127]\n"
BS"v[100:103],v224,s[8:11],s90 offen sc1\n"
MF"v[204:207],a[108:111],a[128:131],v[204:207]\n"
RD"a[72:75],v235 offset:30720\n"
MF"v[116:119],a[112:115],a[132:135],v[116:119]\n"
BL"v[112:115],v176,s[12:15],s38 offen\n"
MF"v[196:199],a[112:115],a[136:139],v[196:199]\n"
SA"s91,s88,492544\n"
RD"a[76:79],v235 offset:31744\n"
PK"v180,v180,v181\n"
MF"v[124:127],a[116:119],a[132:135],v[124:127]\n"
BL"v[144:147],v178,s[8:11],s36 offen sc1\n"
MF"v[204:207],a[116:119],a[136:139],v[204:207]\n"
WT"lgkmcnt(3)\n"
RD"a[80:83],v236 offset:8192\n"
PK"v181,v182,v183\n"
MF"v[116:119],a[120:123],a[140:143],v[116:119]\n"
PK"v182,v188,v189\n"
PK"v183,v190,v191\n"
MF"v[196:199],a[120:123],a[144:147],v[196:199]\n"
BS"v[180:183],v224,s[8:11],s91 offen sc1\n"
MF"v[124:127],a[124:127],a[140:143],v[124:127]\n"
RD"a[84:87],v236 offset:9216\n"
MF"v[204:207],a[124:127],a[144:147],v[204:207]\n"
BL"v[120:123],v176,s[12:15],s55 offen\n"
MF"v[116:119],a[64:67],a[148:151],v[116:119]\n"
RD"a[88:91],v236 offset:18432\n"
MF"v[196:199],a[64:67],a[152:155],v[196:199]\n"
WT"lgkmcnt(3)\n"
BL"v[152:155],v178,s[8:11],s37 offen sc1\n"
MF"v[124:127],a[68:71],a[148:151],v[124:127]\n"
RD"a[92:95],v236 offset:19456\n"
MF"v[204:207],a[68:71],a[152:155],v[204:207]\n"
RD"a[96:99],v236 offset:28672\n"
MF"v[116:119],a[72:75],a[156:159],v[116:119]\n"
BL"v[128:131],v177,s[12:15],s38 offen\n"
MF"v[196:199],a[72:75],a[160:163],v[196:199]\n"
RD"a[100:103],v236 offset:29696\n"
MF"v[124:127],a[76:79],a[156:159],v[124:127]\n"
BL"v[160:163],v179,s[8:11],s36 offen sc1\n"
MF"v[204:207],a[76:79],a[160:163],v[204:207]\n"
WT"lgkmcnt(3)\n"
RD"a[104:107],v237 offset:6144\n"
MF"v[116:119],a[80:83],a[164:167],v[116:119]\n"
MF"v[196:199],a[80:83],a[168:171],v[196:199]\n"
RD"a[108:111],v237 offset:7168\n"
MF"v[124:127],a[84:87],a[164:167],v[124:127]\n"
BL"v[136:139],v177,s[12:15],s55 offen\n"
MF"v[204:207],a[84:87],a[168:171],v[204:207]\n"
RD"a[112:115],v237 offset:16384\n"
MF"v[116:119],a[88:91],a[172:175],v[116:119]\n"
BL"v[168:171],v179,s[8:11],s37 offen sc1\n"
MF"v[196:199],a[88:91],a[176:179],v[196:199]\n"
WT"lgkmcnt(3)\n"
RD"a[116:119],v237 offset:17408\n"
MF"v[124:127],a[92:95],a[172:175],v[124:127]\n"
MF"v[204:207],a[92:95],a[176:179],v[204:207]\n"
RD"a[120:123],v237 offset:26624\n"
MF"v[116:119],a[96:99],a[32:35],v[116:119]\n"
MF"v[196:199],a[96:99],a[36:39],v[196:199]\n"
RD"a[124:127],v237 offset:27648\n"
MF"v[124:127],a[100:103],a[32:35],v[124:127]\n"
"s_lshr_b32 s93,s31,3\n"
MF"v[204:207],a[100:103],a[36:39],v[204:207]\n"
WT"lgkmcnt(3)\n"
RD"a[64:67],v226 offset:14336\n"
"s_lshl_b32 s93,s93,4\n"
MF"v[116:119],a[104:107],a[40:43],v[116:119]\n"
SA"s93,s93,s32\n"
MF"v[196:199],a[104:107],a[44:47],v[196:199]\n"
"s_mul_i32 s94,s93,40960\n"
RD"a[68:71],v226 offset:15360\n"
MF"v[124:127],a[108:111],a[40:43],v[124:127]\n"
"s_lshl_b32 s93,s93,9\n"
MF"v[204:207],a[108:111],a[44:47],v[204:207]\n"
SA"s93,s93,80478208\n"
RD"a[72:75],v226 offset:24576\n"
MF"v[116:119],a[112:115],a[48:51],v[116:119]\n"
SA"s94,s94,59506688\n"
MF"v[196:199],a[112:115],a[52:55],v[196:199]\n"
WT"lgkmcnt(3)\n"
RD"a[76:79],v226 offset:25600\n"
"s_lshl_b32 s92,s30,10\n"
MF"v[124:127],a[116:119],a[48:51],v[124:127]\n"
"v_add_u32 v244,s92,v224\n"
MF"v[204:207],a[116:119],a[52:55],v[204:207]\n"
SA"s92,s45,4096\n"
RD"a[80:83],v235 offset:2048\n"
MF"v[116:119],a[120:123],a[56:59],v[116:119]\n"
"v_add_u32 v245,s92,v224\n"
MF"v[196:199],a[120:123],a[60:63],v[196:199]\n"
RD"a[84:87],v235 offset:3072\n"
MF"v[124:127],a[124:127],a[56:59],v[124:127]\n"
MF"v[204:207],a[124:127],a[60:63],v[204:207]\n"
WT"lgkmcnt(3)\n"
RD"a[88:91],v235 offset:12288\n"
MF"v[132:135],a[64:67],a[244:247],v[132:135]\n"
MF"v[208:211],a[64:67],a[248:251],v[208:211]\n"
RD"a[92:95],v235 offset:13312\n"
MF"v[140:143],a[68:71],a[244:247],v[140:143]\n"
SA"s90,s88,329728\n"
"buffer_load_dwordx2 v[240:241],v243,s[8:11],s93 offen sc1\n"
MF"v[212:215],a[68:71],a[248:251],v[212:215]\n"
RD"a[96:99],v235 offset:22528\n"
PK"v116,v116,v117\n"
MF"v[132:135],a[72:75],a[252:255],v[132:135]\n"
PK"v117,v118,v119\n"
PK"v118,v124,v125\n"
MF"v[208:211],a[72:75],a[128:131],v[208:211]\n"
WT"lgkmcnt(3)\n"
RD"a[100:103],v235 offset:23552\n"
PK"v119,v126,v127\n"
MF"v[140:143],a[76:79],a[252:255],v[140:143]\n"
BS"v[116:119],v224,s[8:11],s90 offen sc1\n"
MF"v[212:215],a[76:79],a[128:131],v[212:215]\n"
RD"a[104:107],v236 offset:0\n"
MF"v[132:135],a[80:83],a[132:135],v[132:135]\n"
MF"v[208:211],a[80:83],a[136:139],v[208:211]\n"
SA"s91,s88,493568\n"
RD"a[108:111],v236 offset:1024\n"
PK"v196,v196,v197\n"
MF"v[140:143],a[84:87],a[132:135],v[140:143]\n"
PK"v197,v198,v199\n"
PK"v198,v204,v205\n"
MF"v[212:215],a[84:87],a[136:139],v[212:215]\n"
WT"lgkmcnt(3)\n"
RD"a[112:115],v236 offset:10240\n"
PK"v199,v206,v207\n"
MF"v[132:135],a[88:91],a[140:143],v[132:135]\n"
BS"v[196:199],v224,s[8:11],s91 offen sc1\n"
MF"v[208:211],a[88:91],a[144:147],v[208:211]\n"
RD"a[116:119],v236 offset:11264\n"
MF"v[140:143],a[92:95],a[140:143],v[140:143]\n"
MF"v[212:215],a[92:95],a[144:147],v[212:215]\n"
RD"a[120:123],v236 offset:20480\n"
MF"v[132:135],a[96:99],a[148:151],v[132:135]\n"
MF"v[208:211],a[96:99],a[152:155],v[208:211]\n"
WT"lgkmcnt(3)\n"
RD"a[124:127],v236 offset:21504\n"
MF"v[140:143],a[100:103],a[148:151],v[140:143]\n"
MF"v[212:215],a[100:103],a[152:155],v[212:215]\n"
RD"a[64:67],v236 offset:30720\n"
MF"v[132:135],a[104:107],a[156:159],v[132:135]\n"
MF"v[208:211],a[104:107],a[160:163],v[208:211]\n"
RD"a[68:71],v236 offset:31744\n"
MF"v[140:143],a[108:111],a[156:159],v[140:143]\n"
MF"v[212:215],a[108:111],a[160:163],v[212:215]\n"
WT"lgkmcnt(3)\n"
RD"a[72:75],v237 offset:8192\n"
MF"v[132:135],a[112:115],a[164:167],v[132:135]\n"
MF"v[208:211],a[112:115],a[168:171],v[208:211]\n"
RD"a[76:79],v237 offset:9216\n"
MF"v[140:143],a[116:119],a[164:167],v[140:143]\n"
MF"v[212:215],a[116:119],a[168:171],v[212:215]\n"
RD"a[80:83],v237 offset:18432\n"
MF"v[132:135],a[120:123],a[172:175],v[132:135]\n"
MF"v[208:211],a[120:123],a[176:179],v[208:211]\n"
WT"lgkmcnt(3)\n"
RD"a[84:87],v237 offset:19456\n"
MF"v[140:143],a[124:127],a[172:175],v[140:143]\n"
MF"v[212:215],a[124:127],a[176:179],v[212:215]\n"
RD"a[88:91],v237 offset:28672\n"
MF"v[132:135],a[64:67],a[32:35],v[132:135]\n"
MF"v[208:211],a[64:67],a[36:39],v[208:211]\n"
RD"a[92:95],v237 offset:29696\n"
MF"v[140:143],a[68:71],a[32:35],v[140:143]\n"
MF"v[212:215],a[68:71],a[36:39],v[212:215]\n"
WT"lgkmcnt(3)\n"
RD"a[96:99],v226 offset:16384\n"
MF"v[132:135],a[72:75],a[40:43],v[132:135]\n"
MF"v[208:211],a[72:75],a[44:47],v[208:211]\n"
RD"a[100:103],v226 offset:17408\n"
MF"v[140:143],a[76:79],a[40:43],v[140:143]\n"
MF"v[212:215],a[76:79],a[44:47],v[212:215]\n"
RD"a[104:107],v226 offset:26624\n"
MF"v[132:135],a[80:83],a[48:51],v[132:135]\n"
MF"v[208:211],a[80:83],a[52:55],v[208:211]\n"
WT"lgkmcnt(3)\n"
RD"a[108:111],v226 offset:27648\n"
MF"v[140:143],a[84:87],a[48:51],v[140:143]\n"
WT"vmcnt(2)\n"
"v_cmp_eq_u64 vcc,s[28:29],v[240:241]\n"
MF"v[212:215],a[84:87],a[52:55],v[212:215]\n"
"s_cmp_eq_u64 vcc,-1\n"
RD"a[112:115],v235 offset:4096\n"
"s_cselect_b32 s95,s94,1065353216\n"
MF"v[132:135],a[88:91],a[56:59],v[132:135]\n"
SA"s92,s95,0\n"
BL"a[180:183],v244,s[8:11],s92 offen sc1\n"
MF"v[208:211],a[88:91],a[60:63],v[208:211]\n"
RD"a[116:119],v235 offset:5120\n"
MF"v[140:143],a[92:95],a[56:59],v[140:143]\n"
BL"a[212:215],v245,s[8:11],s92 offen sc1\n"
MF"v[212:215],a[92:95],a[60:63],v[212:215]\n"
WT"lgkmcnt(3)\n"
RD"a[120:123],v235 offset:14336\n"
MF"v[148:151],a[96:99],a[244:247],v[148:151]\n"
MF"v[216:219],a[96:99],a[248:251],v[216:219]\n"
SA"s92,s95,5120\n"
RD"a[124:127],v235 offset:15360\n"
MF"v[156:159],a[100:103],a[244:247],v[156:159]\n"
SA"s90,s88,330752\n"
BL"a[184:187],v244,s[8:11],s92 offen sc1\n"
MF"v[220:223],a[100:103],a[248:251],v[220:223]\n"
RD"a[64:67],v235 offset:24576\n"
PK"v132,v132,v133\n"
MF"v[148:151],a[104:107],a[252:255],v[148:151]\n"
BL"a[216:219],v245,s[8:11],s92 offen sc1\n"
MF"v[216:219],a[104:107],a[128:131],v[216:219]\n"
WT"lgkmcnt(3)\n"
RD"a[68:71],v235 offset:25600\n"
PK"v133,v134,v135\n"
MF"v[156:159],a[108:111],a[252:255],v[156:159]\n"
PK"v134,v140,v141\n"
PK"v135,v142,v143\n"
MF"v[220:223],a[108:111],a[128:131],v[220:223]\n"
SA"s92,s95,10240\n"
BS"v[132:135],v224,s[8:11],s90 offen sc1\n"
MF"v[148:151],a[112:115],a[132:135],v[148:151]\n"
RD"a[72:75],v236 offset:2048\n"
MF"v[216:219],a[112:115],a[136:139],v[216:219]\n"
SA"s91,s88,494592\n"
BL"a[188:191],v244,s[8:11],s92 offen sc1\n"
MF"v[156:159],a[116:119],a[132:135],v[156:159]\n"
RD"a[76:79],v236 offset:3072\n"
PK"v208,v208,v209\n"
MF"v[220:223],a[116:119],a[136:139],v[220:223]\n"
WT"lgkmcnt(3)\n"
BL"a[220:223],v245,s[8:11],s92 offen sc1\n"
MF"v[148:151],a[120:123],a[140:143],v[148:151]\n"
RD"a[80:83],v236 offset:12288\n"
PK"v209,v210,v211\n"
MF"v[216:219],a[120:123],a[144:147],v[216:219]\n"
SA"s92,s95,15360\n"
RD"a[84:87],v236 offset:13312\n"
PK"v210,v212,v213\n"
PK"v211,v214,v215\n"
MF"v[156:159],a[124:127],a[140:143],v[156:159]\n"
BS"v[208:211],v224,s[8:11],s91 offen sc1\n"
MF"v[220:223],a[124:127],a[144:147],v[220:223]\n"
BL"a[192:195],v244,s[8:11],s92 offen sc1\n"
MF"v[148:151],a[64:67],a[148:151],v[148:151]\n"
RD"a[88:91],v236 offset:22528\n"
MF"v[216:219],a[64:67],a[152:155],v[216:219]\n"
WT"lgkmcnt(3)\n"
BL"a[224:227],v245,s[8:11],s92 offen sc1\n"
MF"v[156:159],a[68:71],a[148:151],v[156:159]\n"
RD"a[92:95],v236 offset:23552\n"
MF"v[220:223],a[68:71],a[152:155],v[220:223]\n"
SA"s92,s95,20480\n"
RD"a[96:99],v237 offset:0\n"
MF"v[148:151],a[72:75],a[156:159],v[148:151]\n"
BL"a[196:199],v244,s[8:11],s92 offen sc1\n"
MF"v[216:219],a[72:75],a[160:163],v[216:219]\n"
RD"a[100:103],v237 offset:1024\n"
MF"v[156:159],a[76:79],a[156:159],v[156:159]\n"
BL"a[228:231],v245,s[8:11],s92 offen sc1\n"
MF"v[220:223],a[76:79],a[160:163],v[220:223]\n"
WT"lgkmcnt(3)\n"
RD"a[104:107],v237 offset:10240\n"
MF"v[148:151],a[80:83],a[164:167],v[148:151]\n"
MF"v[216:219],a[80:83],a[168:171],v[216:219]\n"
SA"s92,s95,25600\n"
RD"a[108:111],v237 offset:11264\n"
MF"v[156:159],a[84:87],a[164:167],v[156:159]\n"
BL"a[200:203],v244,s[8:11],s92 offen sc1\n"
MF"v[220:223],a[84:87],a[168:171],v[220:223]\n"
RD"a[112:115],v237 offset:20480\n"
MF"v[148:151],a[88:91],a[172:175],v[148:151]\n"
BL"a[232:235],v245,s[8:11],s92 offen sc1\n"
MF"v[216:219],a[88:91],a[176:179],v[216:219]\n"
WT"lgkmcnt(3)\n"
RD"a[116:119],v237 offset:21504\n"
MF"v[156:159],a[92:95],a[172:175],v[156:159]\n"
MF"v[220:223],a[92:95],a[176:179],v[220:223]\n"
SA"s92,s95,30720\n"
RD"a[120:123],v237 offset:30720\n"
MF"v[148:151],a[96:99],a[32:35],v[148:151]\n"
BL"a[204:207],v244,s[8:11],s92 offen sc1\n"
MF"v[216:219],a[96:99],a[36:39],v[216:219]\n"
RD"a[124:127],v237 offset:31744\n"
MF"v[156:159],a[100:103],a[32:35],v[156:159]\n"
BL"a[236:239],v245,s[8:11],s92 offen sc1\n"
MF"v[220:223],a[100:103],a[36:39],v[220:223]\n"
WT"lgkmcnt(3)\n"
MF"v[148:151],a[104:107],a[40:43],v[148:151]\n"
MF"v[216:219],a[104:107],a[44:47],v[216:219]\n"
SA"s92,s95,35840\n"
MF"v[156:159],a[108:111],a[40:43],v[156:159]\n"
BL"a[208:211],v244,s[8:11],s92 offen sc1\n"
MF"v[220:223],a[108:111],a[44:47],v[220:223]\n"
MF"v[148:151],a[112:115],a[48:51],v[148:151]\n"
BL"a[240:243],v245,s[8:11],s92 offen sc1\n"
MF"v[216:219],a[112:115],a[52:55],v[216:219]\n"
WT"lgkmcnt(0)\n"
MF"v[156:159],a[116:119],a[48:51],v[156:159]\n"
MF"v[220:223],a[116:119],a[52:55],v[220:223]\n"
MF"v[148:151],a[120:123],a[56:59],v[148:151]\n"
MF"v[216:219],a[120:123],a[60:63],v[216:219]\n"
MF"v[156:159],a[124:127],a[56:59],v[156:159]\n"
MF"v[220:223],a[124:127],a[60:63],v[220:223]\n"
"s_nop 5\n"
PK"v148,v148,v149\n"
PK"v149,v150,v151\n"
PK"v150,v156,v157\n"
PK"v151,v158,v159\n"
SA"s90,s88,331776\n"
BS"v[148:151],v224,s[8:11],s90 offen sc1\n"
PK"v216,v216,v217\n"
PK"v217,v218,v219\n"
PK"v218,v220,v221\n"
PK"v219,v222,v223\n"
SA"s91,s88,495616\n"
BS"v[216:219],v224,s[8:11],s91 offen sc1\n"
"v_and_b32 v246,128,v224\n"
"v_lshl_add_u32 v243,v246,5,v243\n"
"v_lshrrev_b32 v246,8,v224\n"
"v_lshl_add_u32 v243,v246,27,v243\n"
"s_lshl_b32 s91,0x101,s32\n"
"s_or_b32 s91,s91,0xffff0000\n"
"s_mov_b32 s54,1048576\n"
MF"v[0:3],v[96:99],v[112:115],0\n"
MF"v[4:7],v[104:107],v[112:115],0\n"
MF"v[8:11],v[96:99],v[120:123],0\n"
MF"v[12:15],v[104:107],v[120:123],0\n"
MF"v[0:3],v[96:99],v[144:147],v[0:3]\n"
MF"v[4:7],v[104:107],v[144:147],v[4:7]\n"
MF"v[8:11],v[96:99],v[152:155],v[8:11]\n"
MF"v[12:15],v[104:107],v[152:155],v[12:15]\n"
MF"v[16:19],v[96:99],v[128:131],0\n"
MF"v[20:23],v[104:107],v[128:131],0\n"
MF"v[24:27],v[96:99],v[136:139],0\n"
MF"v[28:31],v[104:107],v[136:139],0\n"
MF"v[16:19],v[96:99],v[160:163],v[16:19]\n"
MF"v[20:23],v[104:107],v[160:163],v[20:23]\n"
MF"v[24:27],v[96:99],v[168:171],v[24:27]\n"
MF"v[28:31],v[104:107],v[168:171],v[28:31]\n"
SA"s89,s89,4096\n"
WT"vmcnt(0)\n"
"buffer_store_dwordx2 v[232:233],v239,s[8:11],s89 offen sc1\n"
"buffer_load_dwordx2 v[240:241],v243,s[8:11],s93 offen sc1\n"
"s_barrier\n"
"s_cmp_eq_u32 s95,s94\n"
"s_cbranch_scc0 L1102_%=\n"
MF"v[0:3],v[80:83],a[180:183],v[0:3]\n"
MF"v[4:7],v[88:91],a[180:183],v[4:7]\n"
MF"v[0:3],v[80:83],a[184:187],v[0:3]\n"
MF"v[4:7],v[88:91],a[184:187],v[4:7]\n"
MF"v[0:3],v[80:83],a[188:191],v[0:3]\n"
MF"v[4:7],v[88:91],a[188:191],v[4:7]\n"
MF"v[0:3],v[80:83],a[192:195],v[0:3]\n"
MF"v[4:7],v[88:91],a[192:195],v[4:7]\n"
MF"v[0:3],v[80:83],a[196:199],v[0:3]\n"
MF"v[4:7],v[88:91],a[196:199],v[4:7]\n"
MF"v[0:3],v[80:83],a[200:203],v[0:3]\n"
MF"v[4:7],v[88:91],a[200:203],v[4:7]\n"
MF"v[0:3],v[80:83],a[204:207],v[0:3]\n"
MF"v[4:7],v[88:91],a[204:207],v[4:7]\n"
MF"v[0:3],v[80:83],a[208:211],v[0:3]\n"
MF"v[4:7],v[88:91],a[208:211],v[4:7]\n"
MF"v[8:11],v[80:83],a[212:215],v[8:11]\n"
MF"v[12:15],v[88:91],a[212:215],v[12:15]\n"
MF"v[8:11],v[80:83],a[216:219],v[8:11]\n"
MF"v[12:15],v[88:91],a[216:219],v[12:15]\n"
MF"v[8:11],v[80:83],a[220:223],v[8:11]\n"
MF"v[12:15],v[88:91],a[220:223],v[12:15]\n"
MF"v[8:11],v[80:83],a[224:227],v[8:11]\n"
MF"v[12:15],v[88:91],a[224:227],v[12:15]\n"
PK"v0,v0,v1\n"
PK"v1,v2,v3\n"
PK"v2,v4,v5\n"
PK"v3,v6,v7\n"
BS"v[0:3],v176,s[16:19],s38 offen\n"
MF"v[8:11],v[80:83],a[228:231],v[8:11]\n"
MF"v[12:15],v[88:91],a[228:231],v[12:15]\n"
MF"v[8:11],v[80:83],a[232:235],v[8:11]\n"
MF"v[12:15],v[88:91],a[232:235],v[12:15]\n"
MF"v[8:11],v[80:83],a[236:239],v[8:11]\n"
MF"v[12:15],v[88:91],a[236:239],v[12:15]\n"
MF"v[8:11],v[80:83],a[240:243],v[8:11]\n"
MF"v[12:15],v[88:91],a[240:243],v[12:15]\n"
"s_nop 7\n"
PK"v8,v8,v9\n"
PK"v9,v10,v11\n"
PK"v10,v12,v13\n"
PK"v11,v14,v15\n"
BS"v[8:11],v176,s[16:19],s55 offen\n"
WT"vmcnt(2)\n"
"v_cmp_eq_u64 vcc,s[28:29],v[240:241]\n"
"s_or_b32 s90,vcc_lo,s91\n"
"s_branch L1104_%=\n"
"L1103_%=:\n"
"s_sleep 4\n"
"buffer_load_dwordx2 v[240:241],v243,s[8:11],s93 offen sc1\n"
"L1102_%=:\n"
WT"vmcnt(0)\n"
"v_cmp_eq_u64 vcc,s[28:29],v[240:241]\n"
"s_or_b32 s90,vcc_lo,s91\n"
"s_sub_u32 s54,s54,1\n"
"s_cselect_b32 s90,-1,s90\n"
"s_or_b32 s92,s90,0xff00\n"
"s_cmp_eq_u32 s92,-1\n"
"s_cbranch_scc0 L1103_%=\n"
SA"s92,s94,0\n"
BL"a[180:183],v244,s[8:11],s92 offen sc1\n"
BL"a[212:215],v245,s[8:11],s92 offen sc1\n"
SA"s92,s94,5120\n"
BL"a[184:187],v244,s[8:11],s92 offen sc1\n"
BL"a[216:219],v245,s[8:11],s92 offen sc1\n"
SA"s92,s94,10240\n"
BL"a[188:191],v244,s[8:11],s92 offen sc1\n"
BL"a[220:223],v245,s[8:11],s92 offen sc1\n"
SA"s92,s94,15360\n"
BL"a[192:195],v244,s[8:11],s92 offen sc1\n"
BL"a[224:227],v245,s[8:11],s92 offen sc1\n"
SA"s92,s94,20480\n"
BL"a[196:199],v244,s[8:11],s92 offen sc1\n"
BL"a[228:231],v245,s[8:11],s92 offen sc1\n"
SA"s92,s94,25600\n"
BL"a[200:203],v244,s[8:11],s92 offen sc1\n"
BL"a[232:235],v245,s[8:11],s92 offen sc1\n"
SA"s92,s94,30720\n"
BL"a[204:207],v244,s[8:11],s92 offen sc1\n"
BL"a[236:239],v245,s[8:11],s92 offen sc1\n"
SA"s92,s94,35840\n"
BL"a[208:211],v244,s[8:11],s92 offen sc1\n"
BL"a[240:243],v245,s[8:11],s92 offen sc1\n"
"L1104_%=:\n"
"s_bitcmp1_b32 s90,8\n"
"s_cbranch_scc0 L1109_%=\n"
SA"s92,s94,327680\n"
BL"a[64:67],v244,s[8:11],s92 offen sc1\n"
BL"a[96:99],v245,s[8:11],s92 offen sc1\n"
"L1109_%=:\n"
"s_bitcmp1_b32 s90,9\n"
"s_cbranch_scc0 L1110_%=\n"
SA"s92,s94,332800\n"
BL"a[68:71],v244,s[8:11],s92 offen sc1\n"
BL"a[100:103],v245,s[8:11],s92 offen sc1\n"
"L1110_%=:\n"
"s_bitcmp1_b32 s90,10\n"
"s_cbranch_scc0 L1111_%=\n"
SA"s92,s94,337920\n"
BL"a[72:75],v244,s[8:11],s92 offen sc1\n"
BL"a[104:107],v245,s[8:11],s92 offen sc1\n"
"L1111_%=:\n"
"s_bitcmp1_b32 s90,11\n"
"s_cbranch_scc0 L1112_%=\n"
SA"s92,s94,343040\n"
BL"a[76:79],v244,s[8:11],s92 offen sc1\n"
BL"a[108:111],v245,s[8:11],s92 offen sc1\n"
"L1112_%=:\n"
"s_bitcmp1_b32 s90,12\n"
"s_cbranch_scc0 L1113_%=\n"
SA"s92,s94,348160\n"
BL"a[80:83],v244,s[8:11],s92 offen sc1\n"
BL"a[112:115],v245,s[8:11],s92 offen sc1\n"
"L1113_%=:\n"
"s_bitcmp1_b32 s90,13\n"
"s_cbranch_scc0 L1114_%=\n"
SA"s92,s94,353280\n"
BL"a[84:87],v244,s[8:11],s92 offen sc1\n"
BL"a[116:119],v245,s[8:11],s92 offen sc1\n"
"L1114_%=:\n"
"s_bitcmp1_b32 s90,14\n"
"s_cbranch_scc0 L1115_%=\n"
SA"s92,s94,358400\n"
BL"a[88:91],v244,s[8:11],s92 offen sc1\n"
BL"a[120:123],v245,s[8:11],s92 offen sc1\n"
"L1115_%=:\n"
"s_bitcmp1_b32 s90,15\n"
"s_cbranch_scc0 L1116_%=\n"
SA"s92,s94,363520\n"
BL"a[92:95],v244,s[8:11],s92 offen sc1\n"
BL"a[124:127],v245,s[8:11],s92 offen sc1\n"
"L1116_%=:\n"
"s_mov_b32 s89,s90\n"
"buffer_load_dwordx2 v[240:241],v243,s[8:11],s93 offen sc1\n"
"s_cmp_eq_u32 s95,s94\n"
"s_cbranch_scc1 L1105_%=\n"
WT"vmcnt(0)\n"
MF"v[0:3],v[80:83],a[180:183],v[0:3]\n"
MF"v[4:7],v[88:91],a[180:183],v[4:7]\n"
MF"v[0:3],v[80:83],a[184:187],v[0:3]\n"
MF"v[4:7],v[88:91],a[184:187],v[4:7]\n"
MF"v[0:3],v[80:83],a[188:191],v[0:3]\n"
MF"v[4:7],v[88:91],a[188:191],v[4:7]\n"
MF"v[0:3],v[80:83],a[192:195],v[0:3]\n"
MF"v[4:7],v[88:91],a[192:195],v[4:7]\n"
MF"v[0:3],v[80:83],a[196:199],v[0:3]\n"
MF"v[4:7],v[88:91],a[196:199],v[4:7]\n"
MF"v[0:3],v[80:83],a[200:203],v[0:3]\n"
MF"v[4:7],v[88:91],a[200:203],v[4:7]\n"
MF"v[0:3],v[80:83],a[204:207],v[0:3]\n"
MF"v[4:7],v[88:91],a[204:207],v[4:7]\n"
MF"v[0:3],v[80:83],a[208:211],v[0:3]\n"
MF"v[4:7],v[88:91],a[208:211],v[4:7]\n"
MF"v[8:11],v[80:83],a[212:215],v[8:11]\n"
MF"v[12:15],v[88:91],a[212:215],v[12:15]\n"
MF"v[8:11],v[80:83],a[216:219],v[8:11]\n"
MF"v[12:15],v[88:91],a[216:219],v[12:15]\n"
MF"v[8:11],v[80:83],a[220:223],v[8:11]\n"
MF"v[12:15],v[88:91],a[220:223],v[12:15]\n"
MF"v[8:11],v[80:83],a[224:227],v[8:11]\n"
MF"v[12:15],v[88:91],a[224:227],v[12:15]\n"
PK"v0,v0,v1\n"
PK"v1,v2,v3\n"
PK"v2,v4,v5\n"
PK"v3,v6,v7\n"
BS"v[0:3],v176,s[16:19],s38 offen\n"
MF"v[8:11],v[80:83],a[228:231],v[8:11]\n"
MF"v[12:15],v[88:91],a[228:231],v[12:15]\n"
MF"v[8:11],v[80:83],a[232:235],v[8:11]\n"
MF"v[12:15],v[88:91],a[232:235],v[12:15]\n"
MF"v[8:11],v[80:83],a[236:239],v[8:11]\n"
MF"v[12:15],v[88:91],a[236:239],v[12:15]\n"
MF"v[8:11],v[80:83],a[240:243],v[8:11]\n"
MF"v[12:15],v[88:91],a[240:243],v[12:15]\n"
"s_nop 7\n"
PK"v8,v8,v9\n"
PK"v9,v10,v11\n"
PK"v10,v12,v13\n"
PK"v11,v14,v15\n"
BS"v[8:11],v176,s[16:19],s55 offen\n"
"L1105_%=:\n"
"s_cmp_eq_u32 s89,-1\n"
"s_cbranch_scc1 L1107_%=\n"
"L1106_%=:\n"
WT"vmcnt(0)\n"
"v_cmp_eq_u64 vcc,s[28:29],v[240:241]\n"
"s_or_b32 s90,vcc_lo,s91\n"
"s_sub_u32 s54,s54,1\n"
"s_cselect_b32 s90,-1,s90\n"
"s_cmp_eq_u32 s90,-1\n"
"s_cbranch_scc1 L1108_%=\n"
"s_sleep 4\n"
"buffer_load_dwordx2 v[240:241],v243,s[8:11],s93 offen sc1\n"
"s_branch L1106_%=\n"
"L1108_%=:\n"
"s_not_b32 s90,s89\n"
"s_bitcmp1_b32 s90,8\n"
"s_cbranch_scc0 L1117_%=\n"
SA"s92,s94,327680\n"
BL"a[64:67],v244,s[8:11],s92 offen sc1\n"
BL"a[96:99],v245,s[8:11],s92 offen sc1\n"
"L1117_%=:\n"
"s_bitcmp1_b32 s90,9\n"
"s_cbranch_scc0 L1118_%=\n"
SA"s92,s94,332800\n"
BL"a[68:71],v244,s[8:11],s92 offen sc1\n"
BL"a[100:103],v245,s[8:11],s92 offen sc1\n"
"L1118_%=:\n"
"s_bitcmp1_b32 s90,10\n"
"s_cbranch_scc0 L1119_%=\n"
SA"s92,s94,337920\n"
BL"a[72:75],v244,s[8:11],s92 offen sc1\n"
BL"a[104:107],v245,s[8:11],s92 offen sc1\n"
"L1119_%=:\n"
"s_bitcmp1_b32 s90,11\n"
"s_cbranch_scc0 L1120_%=\n"
SA"s92,s94,343040\n"
BL"a[76:79],v244,s[8:11],s92 offen sc1\n"
BL"a[108:111],v245,s[8:11],s92 offen sc1\n"
"L1120_%=:\n"
"s_bitcmp1_b32 s90,12\n"
"s_cbranch_scc0 L1121_%=\n"
SA"s92,s94,348160\n"
BL"a[80:83],v244,s[8:11],s92 offen sc1\n"
BL"a[112:115],v245,s[8:11],s92 offen sc1\n"
"L1121_%=:\n"
"s_bitcmp1_b32 s90,13\n"
"s_cbranch_scc0 L1122_%=\n"
SA"s92,s94,353280\n"
BL"a[84:87],v244,s[8:11],s92 offen sc1\n"
BL"a[116:119],v245,s[8:11],s92 offen sc1\n"
"L1122_%=:\n"
"s_bitcmp1_b32 s90,14\n"
"s_cbranch_scc0 L1123_%=\n"
SA"s92,s94,358400\n"
BL"a[88:91],v244,s[8:11],s92 offen sc1\n"
BL"a[120:123],v245,s[8:11],s92 offen sc1\n"
"L1123_%=:\n"
"s_bitcmp1_b32 s90,15\n"
"s_cbranch_scc0 L1124_%=\n"
SA"s92,s94,363520\n"
BL"a[92:95],v244,s[8:11],s92 offen sc1\n"
BL"a[124:127],v245,s[8:11],s92 offen sc1\n"
"L1124_%=:\n"
"L1107_%=:\n"
WT"vmcnt(0)\n"
MF"v[16:19],v[80:83],a[64:67],v[16:19]\n"
MF"v[20:23],v[88:91],a[64:67],v[20:23]\n"
MF"v[16:19],v[80:83],a[68:71],v[16:19]\n"
MF"v[20:23],v[88:91],a[68:71],v[20:23]\n"
MF"v[16:19],v[80:83],a[72:75],v[16:19]\n"
MF"v[20:23],v[88:91],a[72:75],v[20:23]\n"
MF"v[16:19],v[80:83],a[76:79],v[16:19]\n"
MF"v[20:23],v[88:91],a[76:79],v[20:23]\n"
MF"v[16:19],v[80:83],a[80:83],v[16:19]\n"
MF"v[20:23],v[88:91],a[80:83],v[20:23]\n"
MF"v[16:19],v[80:83],a[84:87],v[16:19]\n"
MF"v[20:23],v[88:91],a[84:87],v[20:23]\n"
MF"v[16:19],v[80:83],a[88:91],v[16:19]\n"
MF"v[20:23],v[88:91],a[88:91],v[20:23]\n"
MF"v[16:19],v[80:83],a[92:95],v[16:19]\n"
MF"v[20:23],v[88:91],a[92:95],v[20:23]\n"
MF"v[24:27],v[80:83],a[96:99],v[24:27]\n"
MF"v[28:31],v[88:91],a[96:99],v[28:31]\n"
MF"v[24:27],v[80:83],a[100:103],v[24:27]\n"
MF"v[28:31],v[88:91],a[100:103],v[28:31]\n"
MF"v[24:27],v[80:83],a[104:107],v[24:27]\n"
MF"v[28:31],v[88:91],a[104:107],v[28:31]\n"
MF"v[24:27],v[80:83],a[108:111],v[24:27]\n"
MF"v[28:31],v[88:91],a[108:111],v[28:31]\n"
PK"v16,v16,v17\n"
PK"v17,v18,v19\n"
PK"v18,v20,v21\n"
PK"v19,v22,v23\n"
BS"v[16:19],v177,s[16:19],s38 offen\n"
MF"v[24:27],v[80:83],a[112:115],v[24:27]\n"
MF"v[28:31],v[88:91],a[112:115],v[28:31]\n"
MF"v[24:27],v[80:83],a[116:119],v[24:27]\n"
MF"v[28:31],v[88:91],a[116:119],v[28:31]\n"
MF"v[24:27],v[80:83],a[120:123],v[24:27]\n"
MF"v[28:31],v[88:91],a[120:123],v[28:31]\n"
MF"v[24:27],v[80:83],a[124:127],v[24:27]\n"
MF"v[28:31],v[88:91],a[124:127],v[28:31]\n"
"s_nop 7\n"
PK"v24,v24,v25\n"
PK"v25,v26,v27\n"
PK"v26,v28,v29\n"
PK"v27,v30,v31\n"
BS"v[24:27],v177,s[16:19],s55 offen\n"
WT"vmcnt(0) lgkmcnt(0)\n"
"s_endpgm\n"
"L1004_%=:\n"
"s_mov_b32 s54,0\n"
"L1006_%=:\n"
"s_sleep 2\n"
BL"v[160:163],v224,s[8:11],0 offen offset:256 sc1\n"
BL"v[164:167],v224,s[8:11],0 offen offset:1280 sc1\n"
WT"vmcnt(0)\n"
SA"s54,s54,1\n"
"v_cmp_eq_u64 vcc,s[28:29],v[160:161]\n"
"v_cmp_eq_u64_e64 s[62:63],s[28:29],v[162:163]\n"
"s_and_b64 vcc,vcc,s[62:63]\n"
"v_cmp_eq_u64_e64 s[62:63],s[28:29],v[164:165]\n"
"s_and_b64 vcc,vcc,s[62:63]\n"
"v_cmp_eq_u64_e64 s[62:63],s[28:29],v[166:167]\n"
"s_and_b64 vcc,vcc,s[62:63]\n"
"s_cmp_eq_u64 vcc,exec\n"
"s_cbranch_scc1 L1005_%=\n"
"s_cmp_lt_u32 s54,1048576\n"
"s_cbranch_scc1 L1006_%=\n"
"s_branch L1005_%=\n"
"L1007_%=:\n"
"s_mov_b32 s54,0\n"
"L1009_%=:\n"
"v_max3_u32 v226,v160,v161,v162\n"
"v_max3_u32 v226,v226,v163,v164\n"
"v_max3_u32 v226,v226,v165,v166\n"
"v_max3_u32 v226,v226,v167,v168\n"
"v_max3_u32 v226,v226,v169,v170\n"
"v_max3_u32 v226,v226,v171,v172\n"
"v_max3_u32 v226,v226,v173,v174\n"
"v_max3_u32 v226,v226,v175,v176\n"
"v_max3_u32 v226,v226,v177,v178\n"
"v_max3_u32 v226,v226,v179,v180\n"
"v_max3_u32 v226,v226,v181,v182\n"
"v_max3_u32 v226,v226,v183,v184\n"
"v_max3_u32 v226,v226,v185,v186\n"
"v_max3_u32 v226,v226,v187,v188\n"
"v_max3_u32 v226,v226,v189,v190\n"
"v_max_u32 v226,v226,v191\n"
"v_mov_b32 v229,v226\n"
"s_nop 1\n"
"v_max_u32_dpp v229,v229,v229 row_shr:1 row_mask:0xf bank_mask:0xf\n"
"s_nop 1\n"
"v_max_u32_dpp v229,v229,v229 row_shr:2 row_mask:0xf bank_mask:0xf\n"
"s_nop 1\n"
"v_max_u32_dpp v229,v229,v229 row_shr:4 row_mask:0xf bank_mask:0xf\n"
"s_nop 1\n"
"v_max_u32_dpp v229,v229,v229 row_shr:8 row_mask:0xf bank_mask:0xf\n"
"s_nop 1\n"
"v_max_u32_dpp v229,v229,v229 row_bcast:15 row_mask:0xa bank_mask:0xf\n"
"s_nop 1\n"
"v_max_u32_dpp v229,v229,v229 row_bcast:31 row_mask:0xc bank_mask:0xf\n"
"s_nop 1\n"
"v_readlane_b32 s62,v229,63\n"
"s_nop 1\n"
"v_cmp_eq_u32 vcc,s62,v160\n"
"s_nop 1\n"
"v_cndmask_b32_e64 v160,v160,0,vcc\n"
"v_cmp_eq_u32 vcc,s62,v161\n"
"s_nop 1\n"
"v_cndmask_b32_e64 v161,v161,0,vcc\n"
"v_cmp_eq_u32 vcc,s62,v162\n"
"s_nop 1\n"
"v_cndmask_b32_e64 v162,v162,0,vcc\n"
"v_cmp_eq_u32 vcc,s62,v163\n"
"s_nop 1\n"
"v_cndmask_b32_e64 v163,v163,0,vcc\n"
"v_cmp_eq_u32 vcc,s62,v164\n"
"s_nop 1\n"
"v_cndmask_b32_e64 v164,v164,0,vcc\n"
"v_cmp_eq_u32 vcc,s62,v165\n"
"s_nop 1\n"
"v_cndmask_b32_e64 v165,v165,0,vcc\n"
"v_cmp_eq_u32 vcc,s62,v166\n"
"s_nop 1\n"
"v_cndmask_b32_e64 v166,v166,0,vcc\n"
"v_cmp_eq_u32 vcc,s62,v167\n"
"s_nop 1\n"
"v_cndmask_b32_e64 v167,v167,0,vcc\n"
"v_cmp_eq_u32 vcc,s62,v168\n"
"s_nop 1\n"
"v_cndmask_b32_e64 v168,v168,0,vcc\n"
"v_cmp_eq_u32 vcc,s62,v169\n"
"s_nop 1\n"
"v_cndmask_b32_e64 v169,v169,0,vcc\n"
"v_cmp_eq_u32 vcc,s62,v170\n"
"s_nop 1\n"
"v_cndmask_b32_e64 v170,v170,0,vcc\n"
"v_cmp_eq_u32 vcc,s62,v171\n"
"s_nop 1\n"
"v_cndmask_b32_e64 v171,v171,0,vcc\n"
"v_cmp_eq_u32 vcc,s62,v172\n"
"s_nop 1\n"
"v_cndmask_b32_e64 v172,v172,0,vcc\n"
"v_cmp_eq_u32 vcc,s62,v173\n"
"s_nop 1\n"
"v_cndmask_b32_e64 v173,v173,0,vcc\n"
"v_cmp_eq_u32 vcc,s62,v174\n"
"s_nop 1\n"
"v_cndmask_b32_e64 v174,v174,0,vcc\n"
"v_cmp_eq_u32 vcc,s62,v175\n"
"s_nop 1\n"
"v_cndmask_b32_e64 v175,v175,0,vcc\n"
"v_cmp_eq_u32 vcc,s62,v176\n"
"s_nop 1\n"
"v_cndmask_b32_e64 v176,v176,0,vcc\n"
"v_cmp_eq_u32 vcc,s62,v177\n"
"s_nop 1\n"
"v_cndmask_b32_e64 v177,v177,0,vcc\n"
"v_cmp_eq_u32 vcc,s62,v178\n"
"s_nop 1\n"
"v_cndmask_b32_e64 v178,v178,0,vcc\n"
"v_cmp_eq_u32 vcc,s62,v179\n"
"s_nop 1\n"
"v_cndmask_b32_e64 v179,v179,0,vcc\n"
"v_cmp_eq_u32 vcc,s62,v180\n"
"s_nop 1\n"
"v_cndmask_b32_e64 v180,v180,0,vcc\n"
"v_cmp_eq_u32 vcc,s62,v181\n"
"s_nop 1\n"
"v_cndmask_b32_e64 v181,v181,0,vcc\n"
"v_cmp_eq_u32 vcc,s62,v182\n"
"s_nop 1\n"
"v_cndmask_b32_e64 v182,v182,0,vcc\n"
"v_cmp_eq_u32 vcc,s62,v183\n"
"s_nop 1\n"
"v_cndmask_b32_e64 v183,v183,0,vcc\n"
"v_cmp_eq_u32 vcc,s62,v184\n"
"s_nop 1\n"
"v_cndmask_b32_e64 v184,v184,0,vcc\n"
"v_cmp_eq_u32 vcc,s62,v185\n"
"s_nop 1\n"
"v_cndmask_b32_e64 v185,v185,0,vcc\n"
"v_cmp_eq_u32 vcc,s62,v186\n"
"s_nop 1\n"
"v_cndmask_b32_e64 v186,v186,0,vcc\n"
"v_cmp_eq_u32 vcc,s62,v187\n"
"s_nop 1\n"
"v_cndmask_b32_e64 v187,v187,0,vcc\n"
"v_cmp_eq_u32 vcc,s62,v188\n"
"s_nop 1\n"
"v_cndmask_b32_e64 v188,v188,0,vcc\n"
"v_cmp_eq_u32 vcc,s62,v189\n"
"s_nop 1\n"
"v_cndmask_b32_e64 v189,v189,0,vcc\n"
"v_cmp_eq_u32 vcc,s62,v190\n"
"s_nop 1\n"
"v_cndmask_b32_e64 v190,v190,0,vcc\n"
"v_cmp_eq_u32 vcc,s62,v191\n"
"s_nop 1\n"
"v_cndmask_b32_e64 v191,v191,0,vcc\n"
"v_mov_b32 v229,s62\n"
"s_lshl_b64 exec,1,s54\n"
"v_mov_b32 v225,v229\n"
"s_mov_b64 exec,-1\n"
SA"s54,s54,1\n"
"s_cmp_lt_u32 s54,8\n"
"s_cbranch_scc1 L1009_%=\n"
"s_nop 4\n"
"s_branch L1008_%=\n"
"L1010_%=:\n"
"s_mov_b32 s54,0\n"
"L1012_%=:\n"
"s_sleep 2\n"
"buffer_load_dwordx2 v[208:209],v252,s[8:11],s50 offen sc1\n"
WT"vmcnt(0)\n"
SA"s54,s54,1\n"
"v_cmp_eq_u64 vcc,s[28:29],v[208:209]\n"
"s_cmp_eq_u64 vcc,exec\n"
"s_cbranch_scc1 L1011_%=\n"
"s_cmp_lt_u32 s54,1048576\n"
"s_cbranch_scc1 L1012_%=\n"
"s_branch L1011_%=\n"
"L1013_%=:\n"
"s_mov_b32 s54,0\n"
"L1015_%=:\n"
"s_sleep 2\n"
"buffer_load_dwordx2 v[238:239],v237,s[8:11],s50 offen sc1\n"
WT"vmcnt(0)\n"
SA"s54,s54,1\n"
"v_cmp_eq_u64 vcc,s[28:29],v[238:239]\n"
"s_cmp_eq_u64 vcc,exec\n"
"s_cbranch_scc1 L1014_%=\n"
"s_cmp_lt_u32 s54,1048576\n"
"s_cbranch_scc1 L1015_%=\n"
"s_branch L1014_%=\n"
"L1017_%=:\n"
"s_nop 15\n"
WT"vmcnt(0)\n"
"s_lshl_b32 s89,s34,3\n"
SA"s89,s89,s30\n"
SA"s89,s89,0\n"
"s_lshl_b32 s91,s89,4\n"
"s_lshr_b32 s89,s89,1\n"
"v_bfe_u32 v249,v224,4,4\n"
"v_lshrrev_b32 v254,8,v224\n"
"v_lshlrev_b32 v254,2,v254\n"
"v_sub_u32 v249,v249,v254\n"
"v_add_u32 v249,s91,v249\n"
"v_mov_b32 v255,0xff800000\n"
"v_mov_b32 v248,v255\n"
"s_mov_b32 s90,0\n"
"s_mov_b32 s88,0\n"
"v_mov_b32 v80,0\n"
"v_mov_b32 v81,0\n"
"v_mov_b32 v82,0\n"
"v_mov_b32 v83,0\n"
"v_mov_b32 v84,0\n"
"v_mov_b32 v85,0\n"
"v_mov_b32 v86,0\n"
"v_mov_b32 v87,0\n"
"v_mov_b32 v88,0\n"
"v_mov_b32 v89,0\n"
"v_mov_b32 v90,0\n"
"v_mov_b32 v91,0\n"
"v_mov_b32 v92,0\n"
"v_mov_b32 v93,0\n"
"v_mov_b32 v94,0\n"
"v_mov_b32 v95,0\n"
"v_mov_b32 v96,0\n"
"v_mov_b32 v97,0\n"
"v_mov_b32 v98,0\n"
"v_mov_b32 v99,0\n"
"v_mov_b32 v100,0\n"
"v_mov_b32 v101,0\n"
"v_mov_b32 v102,0\n"
"v_mov_b32 v103,0\n"
"v_mov_b32 v104,0\n"
"v_mov_b32 v105,0\n"
"v_mov_b32 v106,0\n"
"v_mov_b32 v107,0\n"
"v_mov_b32 v108,0\n"
"v_mov_b32 v109,0\n"
"v_mov_b32 v110,0\n"
"v_mov_b32 v111,0\n"
"v_accvgpr_write_b32 a132,0\n"
"v_accvgpr_write_b32 a133,0\n"
"v_accvgpr_write_b32 a134,0\n"
"v_accvgpr_write_b32 a135,0\n"
"L1018_%=:\n"
"s_lshl_b32 s91,s88,13\n"
SA"s91,s91,s39\n"
SA"s91,s91,0\n"
BL"a[148:151],v224,s[8:11],s91 offen offset:0 sc1\n"
BL"a[152:155],v224,s[8:11],s91 offen offset:1024 sc1\n"
BL"a[156:159],v224,s[8:11],s91 offen offset:2048 sc1\n"
BL"a[160:163],v224,s[8:11],s91 offen offset:3072 sc1\n"
SA"s91,s91,4096\n"
BL"a[164:167],v224,s[8:11],s91 offen offset:0 sc1\n"
BL"a[168:171],v224,s[8:11],s91 offen offset:1024 sc1\n"
BL"a[172:175],v224,s[8:11],s91 offen offset:2048 sc1\n"
BL"a[176:179],v224,s[8:11],s91 offen offset:3072 sc1\n"
WT"vmcnt(0)\n"
MF"v[240:243],a[148:151],a[0:3],0\n"
MF"v[240:243],a[152:155],a[4:7],v[240:243]\n"
MF"v[240:243],a[156:159],a[8:11],v[240:243]\n"
MF"v[240:243],a[160:163],a[12:15],v[240:243]\n"
MF"v[244:247],a[164:167],a[0:3],0\n"
MF"v[244:247],a[168:171],a[4:7],v[244:247]\n"
MF"v[244:247],a[172:175],a[8:11],v[244:247]\n"
MF"v[244:247],a[176:179],a[12:15],v[244:247]\n"
"s_lshl_b32 s91,s88,5\n"
"v_subrev_u32 v250,s91,v249\n"
"s_nop 7\n"
"v_cmp_gt_i32 vcc,0,v250\n"
"s_nop 1\n"
"v_cndmask_b32 v240,v240,v255,vcc\n"
"v_cmp_gt_i32 vcc,1,v250\n"
"s_nop 1\n"
"v_cndmask_b32 v241,v241,v255,vcc\n"
"v_cmp_gt_i32 vcc,2,v250\n"
"s_nop 1\n"
"v_cndmask_b32 v242,v242,v255,vcc\n"
"v_cmp_gt_i32 vcc,3,v250\n"
"s_nop 1\n"
"v_cndmask_b32 v243,v243,v255,vcc\n"
"v_cmp_gt_i32 vcc,16,v250\n"
"s_nop 1\n"
"v_cndmask_b32 v244,v244,v255,vcc\n"
"v_cmp_gt_i32 vcc,17,v250\n"
"s_nop 1\n"
"v_cndmask_b32 v245,v245,v255,vcc\n"
"v_cmp_gt_i32 vcc,18,v250\n"
"s_nop 1\n"
"v_cndmask_b32 v246,v246,v255,vcc\n"
"v_cmp_gt_i32 vcc,19,v250\n"
"s_nop 1\n"
"v_cndmask_b32 v247,v247,v255,vcc\n"
"s_cmp_lg_u32 s90,0\n"
"s_cbranch_scc1 L1019_%=\n"
"v_max3_f32 v248,v248,v240,v241\n"
"v_max3_f32 v248,v248,v242,v243\n"
"v_max3_f32 v248,v248,v244,v245\n"
"v_max3_f32 v248,v248,v246,v247\n"
"s_branch L1020_%=\n"
"L1019_%=:\n"
"s_lshl_b32 s91,s88,13\n"
SA"s91,s91,s39\n"
SA"s91,s91,524288\n"
BL"a[148:151],v224,s[8:11],s91 offen offset:0 sc1\n"
BL"a[152:155],v224,s[8:11],s91 offen offset:1024 sc1\n"
BL"a[156:159],v224,s[8:11],s91 offen offset:2048 sc1\n"
BL"a[160:163],v224,s[8:11],s91 offen offset:3072 sc1\n"
SA"s91,s91,4096\n"
BL"a[164:167],v224,s[8:11],s91 offen offset:0 sc1\n"
BL"a[168:171],v224,s[8:11],s91 offen offset:1024 sc1\n"
BL"a[172:175],v224,s[8:11],s91 offen offset:2048 sc1\n"
BL"a[176:179],v224,s[8:11],s91 offen offset:3072 sc1\n"
WT"vmcnt(0)\n"
"v_sub_f32 v240,v240,v248\n"
"v_sub_f32 v241,v241,v248\n"
"v_sub_f32 v242,v242,v248\n"
"v_sub_f32 v243,v243,v248\n"
"v_sub_f32 v244,v244,v248\n"
"v_sub_f32 v245,v245,v248\n"
"v_sub_f32 v246,v246,v248\n"
"v_sub_f32 v247,v247,v248\n"
EX"v240,v240\n"
EX"v241,v241\n"
EX"v242,v242\n"
EX"v243,v243\n"
EX"v244,v244\n"
EX"v245,v245\n"
EX"v246,v246\n"
EX"v247,v247\n"
"s_nop 0\n"
PK"v240,v240,v241\n"
PK"v241,v242,v243\n"
PK"v242,v244,v245\n"
PK"v243,v246,v247\n"
"s_nop 1\n"
MF"v[80:83],a[148:151],v[240:243],v[80:83]\n"
MF"v[84:87],a[152:155],v[240:243],v[84:87]\n"
MF"v[88:91],a[156:159],v[240:243],v[88:91]\n"
MF"v[92:95],a[160:163],v[240:243],v[92:95]\n"
MF"v[96:99],a[164:167],v[240:243],v[96:99]\n"
MF"v[100:103],a[168:171],v[240:243],v[100:103]\n"
MF"v[104:107],a[172:175],v[240:243],v[104:107]\n"
MF"v[108:111],a[176:179],v[240:243],v[108:111]\n"
MF"a[132:135],a[128:131],v[240:243],a[132:135]\n"
"s_nop 7\n"
"L1020_%=:\n"
SA"s88,s88,1\n"
"s_cmp_le_u32 s88,s89\n"
"s_cbranch_scc1 L1018_%=\n"
"s_cmp_lg_u32 s90,0\n"
"s_cbranch_scc1 L1021_%=\n"
"v_mov_b32 v254,v248\n"
"s_nop 1\n"
"v_permlane16_swap_b32 v254,v248\n"
"s_nop 1\n"
"v_max_f32 v248,v248,v254\n"
"v_mov_b32 v254,v248\n"
"s_nop 1\n"
"v_permlane32_swap_b32 v254,v248\n"
"s_nop 1\n"
"v_max_f32 v248,v248,v254\n"
"s_mov_b32 s90,1\n"
"s_mov_b32 s88,0\n"
"s_branch L1018_%=\n"
"L1021_%=:\n"
"s_nop 7\n"
"v_accvgpr_read_b32 v251,a132\n"
"s_nop 1\n"
"s_branch L1016_%=\n"
"L1022_%=:\n"
"s_mov_b32 s54,0\n"
"L1024_%=:\n"
"s_sleep 2\n"
"buffer_load_dwordx2 v[238:239],v237,s[8:11],s50 offen sc1\n"
WT"vmcnt(0)\n"
SA"s54,s54,1\n"
"v_cmp_eq_u64 vcc,s[28:29],v[238:239]\n"
"s_cmp_eq_u64 vcc,exec\n"
"s_cbranch_scc1 L1023_%=\n"
"s_cmp_lt_u32 s54,1048576\n"
"s_cbranch_scc1 L1024_%=\n"
"s_branch L1023_%=\n"
"L1026_%=:\n"
"s_nop 15\n"
WT"vmcnt(0)\n"
"s_lshl_b32 s89,s34,3\n"
SA"s89,s89,s30\n"
SA"s89,s89,4\n"
"s_lshl_b32 s91,s89,4\n"
"s_lshr_b32 s89,s89,1\n"
"v_bfe_u32 v249,v224,4,4\n"
"v_lshrrev_b32 v254,8,v224\n"
"v_lshlrev_b32 v254,2,v254\n"
"v_sub_u32 v249,v249,v254\n"
"v_add_u32 v249,s91,v249\n"
"v_mov_b32 v255,0xff800000\n"
"v_mov_b32 v248,v255\n"
"s_mov_b32 s90,0\n"
"s_mov_b32 s88,0\n"
"v_mov_b32 v112,0\n"
"v_mov_b32 v113,0\n"
"v_mov_b32 v114,0\n"
"v_mov_b32 v115,0\n"
"v_mov_b32 v116,0\n"
"v_mov_b32 v117,0\n"
"v_mov_b32 v118,0\n"
"v_mov_b32 v119,0\n"
"v_mov_b32 v120,0\n"
"v_mov_b32 v121,0\n"
"v_mov_b32 v122,0\n"
"v_mov_b32 v123,0\n"
"v_mov_b32 v124,0\n"
"v_mov_b32 v125,0\n"
"v_mov_b32 v126,0\n"
"v_mov_b32 v127,0\n"
"v_mov_b32 v128,0\n"
"v_mov_b32 v129,0\n"
"v_mov_b32 v130,0\n"
"v_mov_b32 v131,0\n"
"v_mov_b32 v132,0\n"
"v_mov_b32 v133,0\n"
"v_mov_b32 v134,0\n"
"v_mov_b32 v135,0\n"
"v_mov_b32 v136,0\n"
"v_mov_b32 v137,0\n"
"v_mov_b32 v138,0\n"
"v_mov_b32 v139,0\n"
"v_mov_b32 v140,0\n"
"v_mov_b32 v141,0\n"
"v_mov_b32 v142,0\n"
"v_mov_b32 v143,0\n"
"v_accvgpr_write_b32 a136,0\n"
"v_accvgpr_write_b32 a137,0\n"
"v_accvgpr_write_b32 a138,0\n"
"v_accvgpr_write_b32 a139,0\n"
"L1027_%=:\n"
"s_lshl_b32 s91,s88,13\n"
SA"s91,s91,s39\n"
SA"s91,s91,0\n"
BL"a[148:151],v224,s[8:11],s91 offen offset:0 sc1\n"
BL"a[152:155],v224,s[8:11],s91 offen offset:1024 sc1\n"
BL"a[156:159],v224,s[8:11],s91 offen offset:2048 sc1\n"
BL"a[160:163],v224,s[8:11],s91 offen offset:3072 sc1\n"
SA"s91,s91,4096\n"
BL"a[164:167],v224,s[8:11],s91 offen offset:0 sc1\n"
BL"a[168:171],v224,s[8:11],s91 offen offset:1024 sc1\n"
BL"a[172:175],v224,s[8:11],s91 offen offset:2048 sc1\n"
BL"a[176:179],v224,s[8:11],s91 offen offset:3072 sc1\n"
WT"vmcnt(0)\n"
MF"v[240:243],a[148:151],a[16:19],0\n"
MF"v[240:243],a[152:155],a[20:23],v[240:243]\n"
MF"v[240:243],a[156:159],a[24:27],v[240:243]\n"
MF"v[240:243],a[160:163],a[28:31],v[240:243]\n"
MF"v[244:247],a[164:167],a[16:19],0\n"
MF"v[244:247],a[168:171],a[20:23],v[244:247]\n"
MF"v[244:247],a[172:175],a[24:27],v[244:247]\n"
MF"v[244:247],a[176:179],a[28:31],v[244:247]\n"
"s_lshl_b32 s91,s88,5\n"
"v_subrev_u32 v250,s91,v249\n"
"s_nop 7\n"
"v_cmp_gt_i32 vcc,0,v250\n"
"s_nop 1\n"
"v_cndmask_b32 v240,v240,v255,vcc\n"
"v_cmp_gt_i32 vcc,1,v250\n"
"s_nop 1\n"
"v_cndmask_b32 v241,v241,v255,vcc\n"
"v_cmp_gt_i32 vcc,2,v250\n"
"s_nop 1\n"
"v_cndmask_b32 v242,v242,v255,vcc\n"
"v_cmp_gt_i32 vcc,3,v250\n"
"s_nop 1\n"
"v_cndmask_b32 v243,v243,v255,vcc\n"
"v_cmp_gt_i32 vcc,16,v250\n"
"s_nop 1\n"
"v_cndmask_b32 v244,v244,v255,vcc\n"
"v_cmp_gt_i32 vcc,17,v250\n"
"s_nop 1\n"
"v_cndmask_b32 v245,v245,v255,vcc\n"
"v_cmp_gt_i32 vcc,18,v250\n"
"s_nop 1\n"
"v_cndmask_b32 v246,v246,v255,vcc\n"
"v_cmp_gt_i32 vcc,19,v250\n"
"s_nop 1\n"
"v_cndmask_b32 v247,v247,v255,vcc\n"
"s_cmp_lg_u32 s90,0\n"
"s_cbranch_scc1 L1028_%=\n"
"v_max3_f32 v248,v248,v240,v241\n"
"v_max3_f32 v248,v248,v242,v243\n"
"v_max3_f32 v248,v248,v244,v245\n"
"v_max3_f32 v248,v248,v246,v247\n"
"s_branch L1029_%=\n"
"L1028_%=:\n"
"s_lshl_b32 s91,s88,13\n"
SA"s91,s91,s39\n"
SA"s91,s91,524288\n"
BL"a[148:151],v224,s[8:11],s91 offen offset:0 sc1\n"
BL"a[152:155],v224,s[8:11],s91 offen offset:1024 sc1\n"
BL"a[156:159],v224,s[8:11],s91 offen offset:2048 sc1\n"
BL"a[160:163],v224,s[8:11],s91 offen offset:3072 sc1\n"
SA"s91,s91,4096\n"
BL"a[164:167],v224,s[8:11],s91 offen offset:0 sc1\n"
BL"a[168:171],v224,s[8:11],s91 offen offset:1024 sc1\n"
BL"a[172:175],v224,s[8:11],s91 offen offset:2048 sc1\n"
BL"a[176:179],v224,s[8:11],s91 offen offset:3072 sc1\n"
WT"vmcnt(0)\n"
"v_sub_f32 v240,v240,v248\n"
"v_sub_f32 v241,v241,v248\n"
"v_sub_f32 v242,v242,v248\n"
"v_sub_f32 v243,v243,v248\n"
"v_sub_f32 v244,v244,v248\n"
"v_sub_f32 v245,v245,v248\n"
"v_sub_f32 v246,v246,v248\n"
"v_sub_f32 v247,v247,v248\n"
EX"v240,v240\n"
EX"v241,v241\n"
EX"v242,v242\n"
EX"v243,v243\n"
EX"v244,v244\n"
EX"v245,v245\n"
EX"v246,v246\n"
EX"v247,v247\n"
"s_nop 0\n"
PK"v240,v240,v241\n"
PK"v241,v242,v243\n"
PK"v242,v244,v245\n"
PK"v243,v246,v247\n"
"s_nop 1\n"
MF"v[112:115],a[148:151],v[240:243],v[112:115]\n"
MF"v[116:119],a[152:155],v[240:243],v[116:119]\n"
MF"v[120:123],a[156:159],v[240:243],v[120:123]\n"
MF"v[124:127],a[160:163],v[240:243],v[124:127]\n"
MF"v[128:131],a[164:167],v[240:243],v[128:131]\n"
MF"v[132:135],a[168:171],v[240:243],v[132:135]\n"
MF"v[136:139],a[172:175],v[240:243],v[136:139]\n"
MF"v[140:143],a[176:179],v[240:243],v[140:143]\n"
MF"a[136:139],a[128:131],v[240:243],a[136:139]\n"
"s_nop 7\n"
"L1029_%=:\n"
SA"s88,s88,1\n"
"s_cmp_le_u32 s88,s89\n"
"s_cbranch_scc1 L1027_%=\n"
"s_cmp_lg_u32 s90,0\n"
"s_cbranch_scc1 L1030_%=\n"
"v_mov_b32 v254,v248\n"
"s_nop 1\n"
"v_permlane16_swap_b32 v254,v248\n"
"s_nop 1\n"
"v_max_f32 v248,v248,v254\n"
"v_mov_b32 v254,v248\n"
"s_nop 1\n"
"v_permlane32_swap_b32 v254,v248\n"
"s_nop 1\n"
"v_max_f32 v248,v248,v254\n"
"s_mov_b32 s90,1\n"
"s_mov_b32 s88,0\n"
"s_branch L1027_%=\n"
"L1030_%=:\n"
"s_nop 7\n"
"v_accvgpr_read_b32 v252,a136\n"
"s_nop 1\n"
"s_branch L1025_%=\n"
"L1031_%=:\n"
"s_mov_b32 s54,0\n"
"L1033_%=:\n"
"s_sleep 2\n"
"buffer_load_dwordx2 v[240:241],v238,s[8:11],s63 offen sc1\n"
WT"vmcnt(0)\n"
SA"s54,s54,1\n"
"v_cmp_eq_u64 vcc,s[28:29],v[240:241]\n"
"s_cmp_eq_u64 vcc,exec\n"
"s_cbranch_scc1 L1032_%=\n"
"s_cmp_lt_u32 s54,1048576\n"
"s_cbranch_scc1 L1033_%=\n"
"s_branch L1032_%=\n"
"L1035_%=:\n"
"s_nop 15\n"
WT"vmcnt(0)\n"
"s_lshl_b32 s89,s34,3\n"
SA"s89,s89,s30\n"
SA"s89,s89,4\n"
"s_lshl_b32 s91,s89,4\n"
"s_lshr_b32 s89,s89,1\n"
"v_bfe_u32 v249,v224,4,4\n"
"v_lshrrev_b32 v254,8,v224\n"
"v_lshlrev_b32 v254,2,v254\n"
"v_sub_u32 v249,v249,v254\n"
"v_add_u32 v249,s91,v249\n"
"v_mov_b32 v255,0xff800000\n"
"v_mov_b32 v248,v255\n"
"s_mov_b32 s90,0\n"
"s_mov_b32 s88,0\n"
"v_mov_b32 v112,0\n"
"v_mov_b32 v113,0\n"
"v_mov_b32 v114,0\n"
"v_mov_b32 v115,0\n"
"v_mov_b32 v116,0\n"
"v_mov_b32 v117,0\n"
"v_mov_b32 v118,0\n"
"v_mov_b32 v119,0\n"
"v_mov_b32 v120,0\n"
"v_mov_b32 v121,0\n"
"v_mov_b32 v122,0\n"
"v_mov_b32 v123,0\n"
"v_mov_b32 v124,0\n"
"v_mov_b32 v125,0\n"
"v_mov_b32 v126,0\n"
"v_mov_b32 v127,0\n"
"v_mov_b32 v128,0\n"
"v_mov_b32 v129,0\n"
"v_mov_b32 v130,0\n"
"v_mov_b32 v131,0\n"
"v_mov_b32 v132,0\n"
"v_mov_b32 v133,0\n"
"v_mov_b32 v134,0\n"
"v_mov_b32 v135,0\n"
"v_mov_b32 v136,0\n"
"v_mov_b32 v137,0\n"
"v_mov_b32 v138,0\n"
"v_mov_b32 v139,0\n"
"v_mov_b32 v140,0\n"
"v_mov_b32 v141,0\n"
"v_mov_b32 v142,0\n"
"v_mov_b32 v143,0\n"
"v_accvgpr_write_b32 a136,0\n"
"v_accvgpr_write_b32 a137,0\n"
"v_accvgpr_write_b32 a138,0\n"
"v_accvgpr_write_b32 a139,0\n"
"L1036_%=:\n"
"s_lshl_b32 s91,s88,13\n"
SA"s91,s91,s39\n"
SA"s91,s91,0\n"
BL"a[148:151],v224,s[8:11],s91 offen offset:0 sc1\n"
BL"a[152:155],v224,s[8:11],s91 offen offset:1024 sc1\n"
BL"a[156:159],v224,s[8:11],s91 offen offset:2048 sc1\n"
BL"a[160:163],v224,s[8:11],s91 offen offset:3072 sc1\n"
SA"s91,s91,4096\n"
BL"a[164:167],v224,s[8:11],s91 offen offset:0 sc1\n"
BL"a[168:171],v224,s[8:11],s91 offen offset:1024 sc1\n"
BL"a[172:175],v224,s[8:11],s91 offen offset:2048 sc1\n"
BL"a[176:179],v224,s[8:11],s91 offen offset:3072 sc1\n"
WT"vmcnt(0)\n"
MF"v[240:243],a[148:151],a[16:19],0\n"
MF"v[240:243],a[152:155],a[20:23],v[240:243]\n"
MF"v[240:243],a[156:159],a[24:27],v[240:243]\n"
MF"v[240:243],a[160:163],a[28:31],v[240:243]\n"
MF"v[244:247],a[164:167],a[16:19],0\n"
MF"v[244:247],a[168:171],a[20:23],v[244:247]\n"
MF"v[244:247],a[172:175],a[24:27],v[244:247]\n"
MF"v[244:247],a[176:179],a[28:31],v[244:247]\n"
"s_lshl_b32 s91,s88,5\n"
"v_subrev_u32 v250,s91,v249\n"
"s_nop 7\n"
"v_cmp_gt_i32 vcc,0,v250\n"
"s_nop 1\n"
"v_cndmask_b32 v240,v240,v255,vcc\n"
"v_cmp_gt_i32 vcc,1,v250\n"
"s_nop 1\n"
"v_cndmask_b32 v241,v241,v255,vcc\n"
"v_cmp_gt_i32 vcc,2,v250\n"
"s_nop 1\n"
"v_cndmask_b32 v242,v242,v255,vcc\n"
"v_cmp_gt_i32 vcc,3,v250\n"
"s_nop 1\n"
"v_cndmask_b32 v243,v243,v255,vcc\n"
"v_cmp_gt_i32 vcc,16,v250\n"
"s_nop 1\n"
"v_cndmask_b32 v244,v244,v255,vcc\n"
"v_cmp_gt_i32 vcc,17,v250\n"
"s_nop 1\n"
"v_cndmask_b32 v245,v245,v255,vcc\n"
"v_cmp_gt_i32 vcc,18,v250\n"
"s_nop 1\n"
"v_cndmask_b32 v246,v246,v255,vcc\n"
"v_cmp_gt_i32 vcc,19,v250\n"
"s_nop 1\n"
"v_cndmask_b32 v247,v247,v255,vcc\n"
"s_cmp_lg_u32 s90,0\n"
"s_cbranch_scc1 L1037_%=\n"
"v_max3_f32 v248,v248,v240,v241\n"
"v_max3_f32 v248,v248,v242,v243\n"
"v_max3_f32 v248,v248,v244,v245\n"
"v_max3_f32 v248,v248,v246,v247\n"
"s_branch L1038_%=\n"
"L1037_%=:\n"
"s_lshl_b32 s91,s88,13\n"
SA"s91,s91,s39\n"
SA"s91,s91,524288\n"
BL"a[148:151],v224,s[8:11],s91 offen offset:0 sc1\n"
BL"a[152:155],v224,s[8:11],s91 offen offset:1024 sc1\n"
BL"a[156:159],v224,s[8:11],s91 offen offset:2048 sc1\n"
BL"a[160:163],v224,s[8:11],s91 offen offset:3072 sc1\n"
SA"s91,s91,4096\n"
BL"a[164:167],v224,s[8:11],s91 offen offset:0 sc1\n"
BL"a[168:171],v224,s[8:11],s91 offen offset:1024 sc1\n"
BL"a[172:175],v224,s[8:11],s91 offen offset:2048 sc1\n"
BL"a[176:179],v224,s[8:11],s91 offen offset:3072 sc1\n"
WT"vmcnt(0)\n"
"v_sub_f32 v240,v240,v248\n"
"v_sub_f32 v241,v241,v248\n"
"v_sub_f32 v242,v242,v248\n"
"v_sub_f32 v243,v243,v248\n"
"v_sub_f32 v244,v244,v248\n"
"v_sub_f32 v245,v245,v248\n"
"v_sub_f32 v246,v246,v248\n"
"v_sub_f32 v247,v247,v248\n"
EX"v240,v240\n"
EX"v241,v241\n"
EX"v242,v242\n"
EX"v243,v243\n"
EX"v244,v244\n"
EX"v245,v245\n"
EX"v246,v246\n"
EX"v247,v247\n"
"s_nop 0\n"
PK"v240,v240,v241\n"
PK"v241,v242,v243\n"
PK"v242,v244,v245\n"
PK"v243,v246,v247\n"
"s_nop 1\n"
MF"v[112:115],a[148:151],v[240:243],v[112:115]\n"
MF"v[116:119],a[152:155],v[240:243],v[116:119]\n"
MF"v[120:123],a[156:159],v[240:243],v[120:123]\n"
MF"v[124:127],a[160:163],v[240:243],v[124:127]\n"
MF"v[128:131],a[164:167],v[240:243],v[128:131]\n"
MF"v[132:135],a[168:171],v[240:243],v[132:135]\n"
MF"v[136:139],a[172:175],v[240:243],v[136:139]\n"
MF"v[140:143],a[176:179],v[240:243],v[140:143]\n"
MF"a[136:139],a[128:131],v[240:243],a[136:139]\n"
"s_nop 7\n"
"L1038_%=:\n"
SA"s88,s88,1\n"
"s_cmp_le_u32 s88,s89\n"
"s_cbranch_scc1 L1036_%=\n"
"s_cmp_lg_u32 s90,0\n"
"s_cbranch_scc1 L1039_%=\n"
"v_mov_b32 v254,v248\n"
"s_nop 1\n"
"v_permlane16_swap_b32 v254,v248\n"
"s_nop 1\n"
"v_max_f32 v248,v248,v254\n"
"v_mov_b32 v254,v248\n"
"s_nop 1\n"
"v_permlane32_swap_b32 v254,v248\n"
"s_nop 1\n"
"v_max_f32 v248,v248,v254\n"
"s_mov_b32 s90,1\n"
"s_mov_b32 s88,0\n"
"s_branch L1036_%=\n"
"L1039_%=:\n"
"s_nop 7\n"
"v_accvgpr_read_b32 v252,a136\n"
"s_nop 1\n"
"s_branch L1034_%=\n"
"L1041_%=:\n"
"s_nop 15\n"
WT"vmcnt(0)\n"
"s_lshl_b32 s89,s34,3\n"
"s_sub_u32 s89,120,s89\n"
SA"s89,s89,s30\n"
SA"s89,s89,0\n"
"s_lshl_b32 s91,s89,4\n"
"s_lshr_b32 s89,s89,1\n"
"v_bfe_u32 v249,v224,4,4\n"
"v_lshrrev_b32 v254,8,v224\n"
"v_lshlrev_b32 v254,2,v254\n"
"v_sub_u32 v249,v249,v254\n"
"v_add_u32 v249,s91,v249\n"
"v_mov_b32 v255,0xff800000\n"
"v_mov_b32 v248,v255\n"
"s_mov_b32 s90,0\n"
"s_mov_b32 s88,0\n"
"v_mov_b32 v144,0\n"
"v_mov_b32 v145,0\n"
"v_mov_b32 v146,0\n"
"v_mov_b32 v147,0\n"
"v_mov_b32 v148,0\n"
"v_mov_b32 v149,0\n"
"v_mov_b32 v150,0\n"
"v_mov_b32 v151,0\n"
"v_mov_b32 v152,0\n"
"v_mov_b32 v153,0\n"
"v_mov_b32 v154,0\n"
"v_mov_b32 v155,0\n"
"v_mov_b32 v156,0\n"
"v_mov_b32 v157,0\n"
"v_mov_b32 v158,0\n"
"v_mov_b32 v159,0\n"
"v_mov_b32 v160,0\n"
"v_mov_b32 v161,0\n"
"v_mov_b32 v162,0\n"
"v_mov_b32 v163,0\n"
"v_mov_b32 v164,0\n"
"v_mov_b32 v165,0\n"
"v_mov_b32 v166,0\n"
"v_mov_b32 v167,0\n"
"v_mov_b32 v168,0\n"
"v_mov_b32 v169,0\n"
"v_mov_b32 v170,0\n"
"v_mov_b32 v171,0\n"
"v_mov_b32 v172,0\n"
"v_mov_b32 v173,0\n"
"v_mov_b32 v174,0\n"
"v_mov_b32 v175,0\n"
"v_accvgpr_write_b32 a140,0\n"
"v_accvgpr_write_b32 a141,0\n"
"v_accvgpr_write_b32 a142,0\n"
"v_accvgpr_write_b32 a143,0\n"
"L1042_%=:\n"
"s_lshl_b32 s91,s88,13\n"
SA"s91,s91,s39\n"
SA"s91,s91,0\n"
BL"a[148:151],v224,s[8:11],s91 offen offset:0 sc1\n"
BL"a[152:155],v224,s[8:11],s91 offen offset:1024 sc1\n"
BL"a[156:159],v224,s[8:11],s91 offen offset:2048 sc1\n"
BL"a[160:163],v224,s[8:11],s91 offen offset:3072 sc1\n"
SA"s91,s91,4096\n"
BL"a[164:167],v224,s[8:11],s91 offen offset:0 sc1\n"
BL"a[168:171],v224,s[8:11],s91 offen offset:1024 sc1\n"
BL"a[172:175],v224,s[8:11],s91 offen offset:2048 sc1\n"
BL"a[176:179],v224,s[8:11],s91 offen offset:3072 sc1\n"
WT"vmcnt(0)\n"
MF"v[240:243],a[148:151],a[32:35],0\n"
MF"v[240:243],a[152:155],a[36:39],v[240:243]\n"
MF"v[240:243],a[156:159],a[40:43],v[240:243]\n"
MF"v[240:243],a[160:163],a[44:47],v[240:243]\n"
MF"v[244:247],a[164:167],a[32:35],0\n"
MF"v[244:247],a[168:171],a[36:39],v[244:247]\n"
MF"v[244:247],a[172:175],a[40:43],v[244:247]\n"
MF"v[244:247],a[176:179],a[44:47],v[244:247]\n"
"s_lshl_b32 s91,s88,5\n"
"v_subrev_u32 v250,s91,v249\n"
"s_nop 7\n"
"v_cmp_gt_i32 vcc,0,v250\n"
"s_nop 1\n"
"v_cndmask_b32 v240,v240,v255,vcc\n"
"v_cmp_gt_i32 vcc,1,v250\n"
"s_nop 1\n"
"v_cndmask_b32 v241,v241,v255,vcc\n"
"v_cmp_gt_i32 vcc,2,v250\n"
"s_nop 1\n"
"v_cndmask_b32 v242,v242,v255,vcc\n"
"v_cmp_gt_i32 vcc,3,v250\n"
"s_nop 1\n"
"v_cndmask_b32 v243,v243,v255,vcc\n"
"v_cmp_gt_i32 vcc,16,v250\n"
"s_nop 1\n"
"v_cndmask_b32 v244,v244,v255,vcc\n"
"v_cmp_gt_i32 vcc,17,v250\n"
"s_nop 1\n"
"v_cndmask_b32 v245,v245,v255,vcc\n"
"v_cmp_gt_i32 vcc,18,v250\n"
"s_nop 1\n"
"v_cndmask_b32 v246,v246,v255,vcc\n"
"v_cmp_gt_i32 vcc,19,v250\n"
"s_nop 1\n"
"v_cndmask_b32 v247,v247,v255,vcc\n"
"s_cmp_lg_u32 s90,0\n"
"s_cbranch_scc1 L1043_%=\n"
"v_max3_f32 v248,v248,v240,v241\n"
"v_max3_f32 v248,v248,v242,v243\n"
"v_max3_f32 v248,v248,v244,v245\n"
"v_max3_f32 v248,v248,v246,v247\n"
"s_branch L1044_%=\n"
"L1043_%=:\n"
"s_lshl_b32 s91,s88,13\n"
SA"s91,s91,s39\n"
SA"s91,s91,524288\n"
BL"a[148:151],v224,s[8:11],s91 offen offset:0 sc1\n"
BL"a[152:155],v224,s[8:11],s91 offen offset:1024 sc1\n"
BL"a[156:159],v224,s[8:11],s91 offen offset:2048 sc1\n"
BL"a[160:163],v224,s[8:11],s91 offen offset:3072 sc1\n"
SA"s91,s91,4096\n"
BL"a[164:167],v224,s[8:11],s91 offen offset:0 sc1\n"
BL"a[168:171],v224,s[8:11],s91 offen offset:1024 sc1\n"
BL"a[172:175],v224,s[8:11],s91 offen offset:2048 sc1\n"
BL"a[176:179],v224,s[8:11],s91 offen offset:3072 sc1\n"
WT"vmcnt(0)\n"
"v_sub_f32 v240,v240,v248\n"
"v_sub_f32 v241,v241,v248\n"
"v_sub_f32 v242,v242,v248\n"
"v_sub_f32 v243,v243,v248\n"
"v_sub_f32 v244,v244,v248\n"
"v_sub_f32 v245,v245,v248\n"
"v_sub_f32 v246,v246,v248\n"
"v_sub_f32 v247,v247,v248\n"
EX"v240,v240\n"
EX"v241,v241\n"
EX"v242,v242\n"
EX"v243,v243\n"
EX"v244,v244\n"
EX"v245,v245\n"
EX"v246,v246\n"
EX"v247,v247\n"
"s_nop 0\n"
PK"v240,v240,v241\n"
PK"v241,v242,v243\n"
PK"v242,v244,v245\n"
PK"v243,v246,v247\n"
"s_nop 1\n"
MF"v[144:147],a[148:151],v[240:243],v[144:147]\n"
MF"v[148:151],a[152:155],v[240:243],v[148:151]\n"
MF"v[152:155],a[156:159],v[240:243],v[152:155]\n"
MF"v[156:159],a[160:163],v[240:243],v[156:159]\n"
MF"v[160:163],a[164:167],v[240:243],v[160:163]\n"
MF"v[164:167],a[168:171],v[240:243],v[164:167]\n"
MF"v[168:171],a[172:175],v[240:243],v[168:171]\n"
MF"v[172:175],a[176:179],v[240:243],v[172:175]\n"
MF"a[140:143],a[128:131],v[240:243],a[140:143]\n"
"s_nop 7\n"
"L1044_%=:\n"
SA"s88,s88,1\n"
"s_cmp_le_u32 s88,s89\n"
"s_cbranch_scc1 L1042_%=\n"
"s_cmp_lg_u32 s90,0\n"
"s_cbranch_scc1 L1045_%=\n"
"v_mov_b32 v254,v248\n"
"s_nop 1\n"
"v_permlane16_swap_b32 v254,v248\n"
"s_nop 1\n"
"v_max_f32 v248,v248,v254\n"
"v_mov_b32 v254,v248\n"
"s_nop 1\n"
"v_permlane32_swap_b32 v254,v248\n"
"s_nop 1\n"
"v_max_f32 v248,v248,v254\n"
"s_mov_b32 s90,1\n"
"s_mov_b32 s88,0\n"
"s_branch L1042_%=\n"
"L1045_%=:\n"
"s_nop 7\n"
"v_accvgpr_read_b32 v251,a140\n"
"s_nop 1\n"
"s_branch L1040_%=\n"
"L1047_%=:\n"
"s_nop 15\n"
WT"vmcnt(0)\n"
"s_lshl_b32 s89,s34,3\n"
"s_sub_u32 s89,120,s89\n"
SA"s89,s89,s30\n"
SA"s89,s89,4\n"
"s_lshl_b32 s91,s89,4\n"
"s_lshr_b32 s89,s89,1\n"
"v_bfe_u32 v249,v224,4,4\n"
"v_lshrrev_b32 v254,8,v224\n"
"v_lshlrev_b32 v254,2,v254\n"
"v_sub_u32 v249,v249,v254\n"
"v_add_u32 v249,s91,v249\n"
"v_mov_b32 v255,0xff800000\n"
"v_mov_b32 v248,v255\n"
"s_mov_b32 s90,0\n"
"s_mov_b32 s88,0\n"
"v_mov_b32 v176,0\n"
"v_mov_b32 v177,0\n"
"v_mov_b32 v178,0\n"
"v_mov_b32 v179,0\n"
"v_mov_b32 v180,0\n"
"v_mov_b32 v181,0\n"
"v_mov_b32 v182,0\n"
"v_mov_b32 v183,0\n"
"v_mov_b32 v184,0\n"
"v_mov_b32 v185,0\n"
"v_mov_b32 v186,0\n"
"v_mov_b32 v187,0\n"
"v_mov_b32 v188,0\n"
"v_mov_b32 v189,0\n"
"v_mov_b32 v190,0\n"
"v_mov_b32 v191,0\n"
"v_mov_b32 v192,0\n"
"v_mov_b32 v193,0\n"
"v_mov_b32 v194,0\n"
"v_mov_b32 v195,0\n"
"v_mov_b32 v196,0\n"
"v_mov_b32 v197,0\n"
"v_mov_b32 v198,0\n"
"v_mov_b32 v199,0\n"
"v_mov_b32 v200,0\n"
"v_mov_b32 v201,0\n"
"v_mov_b32 v202,0\n"
"v_mov_b32 v203,0\n"
"v_mov_b32 v204,0\n"
"v_mov_b32 v205,0\n"
"v_mov_b32 v206,0\n"
"v_mov_b32 v207,0\n"
"v_accvgpr_write_b32 a144,0\n"
"v_accvgpr_write_b32 a145,0\n"
"v_accvgpr_write_b32 a146,0\n"
"v_accvgpr_write_b32 a147,0\n"
"L1048_%=:\n"
"s_lshl_b32 s91,s88,13\n"
SA"s91,s91,s39\n"
SA"s91,s91,0\n"
BL"a[148:151],v224,s[8:11],s91 offen offset:0 sc1\n"
BL"a[152:155],v224,s[8:11],s91 offen offset:1024 sc1\n"
BL"a[156:159],v224,s[8:11],s91 offen offset:2048 sc1\n"
BL"a[160:163],v224,s[8:11],s91 offen offset:3072 sc1\n"
SA"s91,s91,4096\n"
BL"a[164:167],v224,s[8:11],s91 offen offset:0 sc1\n"
BL"a[168:171],v224,s[8:11],s91 offen offset:1024 sc1\n"
BL"a[172:175],v224,s[8:11],s91 offen offset:2048 sc1\n"
BL"a[176:179],v224,s[8:11],s91 offen offset:3072 sc1\n"
WT"vmcnt(0)\n"
MF"v[240:243],a[148:151],a[48:51],0\n"
MF"v[240:243],a[152:155],a[52:55],v[240:243]\n"
MF"v[240:243],a[156:159],a[56:59],v[240:243]\n"
MF"v[240:243],a[160:163],a[60:63],v[240:243]\n"
MF"v[244:247],a[164:167],a[48:51],0\n"
MF"v[244:247],a[168:171],a[52:55],v[244:247]\n"
MF"v[244:247],a[172:175],a[56:59],v[244:247]\n"
MF"v[244:247],a[176:179],a[60:63],v[244:247]\n"
"s_lshl_b32 s91,s88,5\n"
"v_subrev_u32 v250,s91,v249\n"
"s_nop 7\n"
"v_cmp_gt_i32 vcc,0,v250\n"
"s_nop 1\n"
"v_cndmask_b32 v240,v240,v255,vcc\n"
"v_cmp_gt_i32 vcc,1,v250\n"
"s_nop 1\n"
"v_cndmask_b32 v241,v241,v255,vcc\n"
"v_cmp_gt_i32 vcc,2,v250\n"
"s_nop 1\n"
"v_cndmask_b32 v242,v242,v255,vcc\n"
"v_cmp_gt_i32 vcc,3,v250\n"
"s_nop 1\n"
"v_cndmask_b32 v243,v243,v255,vcc\n"
"v_cmp_gt_i32 vcc,16,v250\n"
"s_nop 1\n"
"v_cndmask_b32 v244,v244,v255,vcc\n"
"v_cmp_gt_i32 vcc,17,v250\n"
"s_nop 1\n"
"v_cndmask_b32 v245,v245,v255,vcc\n"
"v_cmp_gt_i32 vcc,18,v250\n"
"s_nop 1\n"
"v_cndmask_b32 v246,v246,v255,vcc\n"
"v_cmp_gt_i32 vcc,19,v250\n"
"s_nop 1\n"
"v_cndmask_b32 v247,v247,v255,vcc\n"
"s_cmp_lg_u32 s90,0\n"
"s_cbranch_scc1 L1049_%=\n"
"v_max3_f32 v248,v248,v240,v241\n"
"v_max3_f32 v248,v248,v242,v243\n"
"v_max3_f32 v248,v248,v244,v245\n"
"v_max3_f32 v248,v248,v246,v247\n"
"s_branch L1050_%=\n"
"L1049_%=:\n"
"s_lshl_b32 s91,s88,13\n"
SA"s91,s91,s39\n"
SA"s91,s91,524288\n"
BL"a[148:151],v224,s[8:11],s91 offen offset:0 sc1\n"
BL"a[152:155],v224,s[8:11],s91 offen offset:1024 sc1\n"
BL"a[156:159],v224,s[8:11],s91 offen offset:2048 sc1\n"
BL"a[160:163],v224,s[8:11],s91 offen offset:3072 sc1\n"
SA"s91,s91,4096\n"
BL"a[164:167],v224,s[8:11],s91 offen offset:0 sc1\n"
BL"a[168:171],v224,s[8:11],s91 offen offset:1024 sc1\n"
BL"a[172:175],v224,s[8:11],s91 offen offset:2048 sc1\n"
BL"a[176:179],v224,s[8:11],s91 offen offset:3072 sc1\n"
WT"vmcnt(0)\n"
"v_sub_f32 v240,v240,v248\n"
"v_sub_f32 v241,v241,v248\n"
"v_sub_f32 v242,v242,v248\n"
"v_sub_f32 v243,v243,v248\n"
"v_sub_f32 v244,v244,v248\n"
"v_sub_f32 v245,v245,v248\n"
"v_sub_f32 v246,v246,v248\n"
"v_sub_f32 v247,v247,v248\n"
EX"v240,v240\n"
EX"v241,v241\n"
EX"v242,v242\n"
EX"v243,v243\n"
EX"v244,v244\n"
EX"v245,v245\n"
EX"v246,v246\n"
EX"v247,v247\n"
"s_nop 0\n"
PK"v240,v240,v241\n"
PK"v241,v242,v243\n"
PK"v242,v244,v245\n"
PK"v243,v246,v247\n"
"s_nop 1\n"
MF"v[176:179],a[148:151],v[240:243],v[176:179]\n"
MF"v[180:183],a[152:155],v[240:243],v[180:183]\n"
MF"v[184:187],a[156:159],v[240:243],v[184:187]\n"
MF"v[188:191],a[160:163],v[240:243],v[188:191]\n"
MF"v[192:195],a[164:167],v[240:243],v[192:195]\n"
MF"v[196:199],a[168:171],v[240:243],v[196:199]\n"
MF"v[200:203],a[172:175],v[240:243],v[200:203]\n"
MF"v[204:207],a[176:179],v[240:243],v[204:207]\n"
MF"a[144:147],a[128:131],v[240:243],a[144:147]\n"
"s_nop 7\n"
"L1050_%=:\n"
SA"s88,s88,1\n"
"s_cmp_le_u32 s88,s89\n"
"s_cbranch_scc1 L1048_%=\n"
"s_cmp_lg_u32 s90,0\n"
"s_cbranch_scc1 L1051_%=\n"
"v_mov_b32 v254,v248\n"
"s_nop 1\n"
"v_permlane16_swap_b32 v254,v248\n"
"s_nop 1\n"
"v_max_f32 v248,v248,v254\n"
"v_mov_b32 v254,v248\n"
"s_nop 1\n"
"v_permlane32_swap_b32 v254,v248\n"
"s_nop 1\n"
"v_max_f32 v248,v248,v254\n"
"s_mov_b32 s90,1\n"
"s_mov_b32 s88,0\n"
"s_branch L1048_%=\n"
"L1051_%=:\n"
"s_nop 7\n"
"v_accvgpr_read_b32 v252,a144\n"
"s_nop 1\n"
"s_branch L1046_%=\n"
"L1082_%=:\n"
"s_nop 15\n"
WT"vmcnt(0)\n"
"s_lshl_b32 s89,s34,3\n"
"s_sub_u32 s89,120,s89\n"
SA"s89,s89,s30\n"
SA"s89,s89,0\n"
"s_lshl_b32 s91,s89,4\n"
"s_lshr_b32 s89,s89,1\n"
"v_bfe_u32 v249,v224,4,4\n"
"v_lshrrev_b32 v254,8,v224\n"
"v_lshlrev_b32 v254,2,v254\n"
"v_sub_u32 v249,v249,v254\n"
"v_add_u32 v249,s91,v249\n"
"v_mov_b32 v255,0xff800000\n"
"v_mov_b32 v248,v255\n"
"s_mov_b32 s90,0\n"
"s_mov_b32 s88,0\n"
"v_mov_b32 v144,0\n"
"v_mov_b32 v145,0\n"
"v_mov_b32 v146,0\n"
"v_mov_b32 v147,0\n"
"v_mov_b32 v148,0\n"
"v_mov_b32 v149,0\n"
"v_mov_b32 v150,0\n"
"v_mov_b32 v151,0\n"
"v_mov_b32 v152,0\n"
"v_mov_b32 v153,0\n"
"v_mov_b32 v154,0\n"
"v_mov_b32 v155,0\n"
"v_mov_b32 v156,0\n"
"v_mov_b32 v157,0\n"
"v_mov_b32 v158,0\n"
"v_mov_b32 v159,0\n"
"v_mov_b32 v160,0\n"
"v_mov_b32 v161,0\n"
"v_mov_b32 v162,0\n"
"v_mov_b32 v163,0\n"
"v_mov_b32 v164,0\n"
"v_mov_b32 v165,0\n"
"v_mov_b32 v166,0\n"
"v_mov_b32 v167,0\n"
"v_mov_b32 v168,0\n"
"v_mov_b32 v169,0\n"
"v_mov_b32 v170,0\n"
"v_mov_b32 v171,0\n"
"v_mov_b32 v172,0\n"
"v_mov_b32 v173,0\n"
"v_mov_b32 v174,0\n"
"v_mov_b32 v175,0\n"
"v_accvgpr_write_b32 a140,0\n"
"v_accvgpr_write_b32 a141,0\n"
"v_accvgpr_write_b32 a142,0\n"
"v_accvgpr_write_b32 a143,0\n"
"L1083_%=:\n"
"s_lshl_b32 s91,s88,13\n"
SA"s91,s91,s39\n"
SA"s91,s91,0\n"
BL"a[148:151],v224,s[8:11],s91 offen offset:0 sc1\n"
BL"a[152:155],v224,s[8:11],s91 offen offset:1024 sc1\n"
BL"a[156:159],v224,s[8:11],s91 offen offset:2048 sc1\n"
BL"a[160:163],v224,s[8:11],s91 offen offset:3072 sc1\n"
SA"s91,s91,4096\n"
BL"a[164:167],v224,s[8:11],s91 offen offset:0 sc1\n"
BL"a[168:171],v224,s[8:11],s91 offen offset:1024 sc1\n"
BL"a[172:175],v224,s[8:11],s91 offen offset:2048 sc1\n"
BL"a[176:179],v224,s[8:11],s91 offen offset:3072 sc1\n"
WT"vmcnt(0)\n"
MF"v[240:243],a[148:151],a[32:35],0\n"
MF"v[240:243],a[152:155],a[36:39],v[240:243]\n"
MF"v[240:243],a[156:159],a[40:43],v[240:243]\n"
MF"v[240:243],a[160:163],a[44:47],v[240:243]\n"
MF"v[244:247],a[164:167],a[32:35],0\n"
MF"v[244:247],a[168:171],a[36:39],v[244:247]\n"
MF"v[244:247],a[172:175],a[40:43],v[244:247]\n"
MF"v[244:247],a[176:179],a[44:47],v[244:247]\n"
"s_lshl_b32 s91,s88,5\n"
"v_subrev_u32 v250,s91,v249\n"
"s_nop 7\n"
"v_cmp_gt_i32 vcc,0,v250\n"
"s_nop 1\n"
"v_cndmask_b32 v240,v240,v255,vcc\n"
"v_cmp_gt_i32 vcc,1,v250\n"
"s_nop 1\n"
"v_cndmask_b32 v241,v241,v255,vcc\n"
"v_cmp_gt_i32 vcc,2,v250\n"
"s_nop 1\n"
"v_cndmask_b32 v242,v242,v255,vcc\n"
"v_cmp_gt_i32 vcc,3,v250\n"
"s_nop 1\n"
"v_cndmask_b32 v243,v243,v255,vcc\n"
"v_cmp_gt_i32 vcc,16,v250\n"
"s_nop 1\n"
"v_cndmask_b32 v244,v244,v255,vcc\n"
"v_cmp_gt_i32 vcc,17,v250\n"
"s_nop 1\n"
"v_cndmask_b32 v245,v245,v255,vcc\n"
"v_cmp_gt_i32 vcc,18,v250\n"
"s_nop 1\n"
"v_cndmask_b32 v246,v246,v255,vcc\n"
"v_cmp_gt_i32 vcc,19,v250\n"
"s_nop 1\n"
"v_cndmask_b32 v247,v247,v255,vcc\n"
"s_cmp_lg_u32 s90,0\n"
"s_cbranch_scc1 L1084_%=\n"
"v_max3_f32 v248,v248,v240,v241\n"
"v_max3_f32 v248,v248,v242,v243\n"
"v_max3_f32 v248,v248,v244,v245\n"
"v_max3_f32 v248,v248,v246,v247\n"
"s_branch L1085_%=\n"
"L1084_%=:\n"
"s_lshl_b32 s91,s88,13\n"
SA"s91,s91,s39\n"
SA"s91,s91,524288\n"
BL"a[148:151],v224,s[8:11],s91 offen offset:0 sc1\n"
BL"a[152:155],v224,s[8:11],s91 offen offset:1024 sc1\n"
BL"a[156:159],v224,s[8:11],s91 offen offset:2048 sc1\n"
BL"a[160:163],v224,s[8:11],s91 offen offset:3072 sc1\n"
SA"s91,s91,4096\n"
BL"a[164:167],v224,s[8:11],s91 offen offset:0 sc1\n"
BL"a[168:171],v224,s[8:11],s91 offen offset:1024 sc1\n"
BL"a[172:175],v224,s[8:11],s91 offen offset:2048 sc1\n"
BL"a[176:179],v224,s[8:11],s91 offen offset:3072 sc1\n"
WT"vmcnt(0)\n"
"v_sub_f32 v240,v240,v248\n"
"v_sub_f32 v241,v241,v248\n"
"v_sub_f32 v242,v242,v248\n"
"v_sub_f32 v243,v243,v248\n"
"v_sub_f32 v244,v244,v248\n"
"v_sub_f32 v245,v245,v248\n"
"v_sub_f32 v246,v246,v248\n"
"v_sub_f32 v247,v247,v248\n"
EX"v240,v240\n"
EX"v241,v241\n"
EX"v242,v242\n"
EX"v243,v243\n"
EX"v244,v244\n"
EX"v245,v245\n"
EX"v246,v246\n"
EX"v247,v247\n"
"s_nop 0\n"
PK"v240,v240,v241\n"
PK"v241,v242,v243\n"
PK"v242,v244,v245\n"
PK"v243,v246,v247\n"
"s_nop 1\n"
MF"v[144:147],a[148:151],v[240:243],v[144:147]\n"
MF"v[148:151],a[152:155],v[240:243],v[148:151]\n"
MF"v[152:155],a[156:159],v[240:243],v[152:155]\n"
MF"v[156:159],a[160:163],v[240:243],v[156:159]\n"
MF"v[160:163],a[164:167],v[240:243],v[160:163]\n"
MF"v[164:167],a[168:171],v[240:243],v[164:167]\n"
MF"v[168:171],a[172:175],v[240:243],v[168:171]\n"
MF"v[172:175],a[176:179],v[240:243],v[172:175]\n"
MF"a[140:143],a[128:131],v[240:243],a[140:143]\n"
"s_nop 7\n"
"L1085_%=:\n"
SA"s88,s88,1\n"
"s_cmp_le_u32 s88,s89\n"
"s_cbranch_scc1 L1083_%=\n"
"s_cmp_lg_u32 s90,0\n"
"s_cbranch_scc1 L1086_%=\n"
"v_mov_b32 v254,v248\n"
"s_nop 1\n"
"v_permlane16_swap_b32 v254,v248\n"
"s_nop 1\n"
"v_max_f32 v248,v248,v254\n"
"v_mov_b32 v254,v248\n"
"s_nop 1\n"
"v_permlane32_swap_b32 v254,v248\n"
"s_nop 1\n"
"v_max_f32 v248,v248,v254\n"
"s_mov_b32 s90,1\n"
"s_mov_b32 s88,0\n"
"s_branch L1083_%=\n"
"L1086_%=:\n"
"s_nop 7\n"
"v_accvgpr_read_b32 v251,a140\n"
"s_nop 1\n"
"s_branch L1081_%=\n"
"L1088_%=:\n"
"s_nop 15\n"
WT"vmcnt(0)\n"
"s_lshl_b32 s89,s34,3\n"
"s_sub_u32 s89,120,s89\n"
SA"s89,s89,s30\n"
SA"s89,s89,4\n"
"s_lshl_b32 s91,s89,4\n"
"s_lshr_b32 s89,s89,1\n"
"v_bfe_u32 v249,v224,4,4\n"
"v_lshrrev_b32 v254,8,v224\n"
"v_lshlrev_b32 v254,2,v254\n"
"v_sub_u32 v249,v249,v254\n"
"v_add_u32 v249,s91,v249\n"
"v_mov_b32 v255,0xff800000\n"
"v_mov_b32 v248,v255\n"
"s_mov_b32 s90,0\n"
"s_mov_b32 s88,0\n"
"v_mov_b32 v176,0\n"
"v_mov_b32 v177,0\n"
"v_mov_b32 v178,0\n"
"v_mov_b32 v179,0\n"
"v_mov_b32 v180,0\n"
"v_mov_b32 v181,0\n"
"v_mov_b32 v182,0\n"
"v_mov_b32 v183,0\n"
"v_mov_b32 v184,0\n"
"v_mov_b32 v185,0\n"
"v_mov_b32 v186,0\n"
"v_mov_b32 v187,0\n"
"v_mov_b32 v188,0\n"
"v_mov_b32 v189,0\n"
"v_mov_b32 v190,0\n"
"v_mov_b32 v191,0\n"
"v_mov_b32 v192,0\n"
"v_mov_b32 v193,0\n"
"v_mov_b32 v194,0\n"
"v_mov_b32 v195,0\n"
"v_mov_b32 v196,0\n"
"v_mov_b32 v197,0\n"
"v_mov_b32 v198,0\n"
"v_mov_b32 v199,0\n"
"v_mov_b32 v200,0\n"
"v_mov_b32 v201,0\n"
"v_mov_b32 v202,0\n"
"v_mov_b32 v203,0\n"
"v_mov_b32 v204,0\n"
"v_mov_b32 v205,0\n"
"v_mov_b32 v206,0\n"
"v_mov_b32 v207,0\n"
"v_accvgpr_write_b32 a144,0\n"
"v_accvgpr_write_b32 a145,0\n"
"v_accvgpr_write_b32 a146,0\n"
"v_accvgpr_write_b32 a147,0\n"
"L1089_%=:\n"
"s_lshl_b32 s91,s88,13\n"
SA"s91,s91,s39\n"
SA"s91,s91,0\n"
BL"a[148:151],v224,s[8:11],s91 offen offset:0 sc1\n"
BL"a[152:155],v224,s[8:11],s91 offen offset:1024 sc1\n"
BL"a[156:159],v224,s[8:11],s91 offen offset:2048 sc1\n"
BL"a[160:163],v224,s[8:11],s91 offen offset:3072 sc1\n"
SA"s91,s91,4096\n"
BL"a[164:167],v224,s[8:11],s91 offen offset:0 sc1\n"
BL"a[168:171],v224,s[8:11],s91 offen offset:1024 sc1\n"
BL"a[172:175],v224,s[8:11],s91 offen offset:2048 sc1\n"
BL"a[176:179],v224,s[8:11],s91 offen offset:3072 sc1\n"
WT"vmcnt(0)\n"
MF"v[240:243],a[148:151],a[48:51],0\n"
MF"v[240:243],a[152:155],a[52:55],v[240:243]\n"
MF"v[240:243],a[156:159],a[56:59],v[240:243]\n"
MF"v[240:243],a[160:163],a[60:63],v[240:243]\n"
MF"v[244:247],a[164:167],a[48:51],0\n"
MF"v[244:247],a[168:171],a[52:55],v[244:247]\n"
MF"v[244:247],a[172:175],a[56:59],v[244:247]\n"
MF"v[244:247],a[176:179],a[60:63],v[244:247]\n"
"s_lshl_b32 s91,s88,5\n"
"v_subrev_u32 v250,s91,v249\n"
"s_nop 7\n"
"v_cmp_gt_i32 vcc,0,v250\n"
"s_nop 1\n"
"v_cndmask_b32 v240,v240,v255,vcc\n"
"v_cmp_gt_i32 vcc,1,v250\n"
"s_nop 1\n"
"v_cndmask_b32 v241,v241,v255,vcc\n"
"v_cmp_gt_i32 vcc,2,v250\n"
"s_nop 1\n"
"v_cndmask_b32 v242,v242,v255,vcc\n"
"v_cmp_gt_i32 vcc,3,v250\n"
"s_nop 1\n"
"v_cndmask_b32 v243,v243,v255,vcc\n"
"v_cmp_gt_i32 vcc,16,v250\n"
"s_nop 1\n"
"v_cndmask_b32 v244,v244,v255,vcc\n"
"v_cmp_gt_i32 vcc,17,v250\n"
"s_nop 1\n"
"v_cndmask_b32 v245,v245,v255,vcc\n"
"v_cmp_gt_i32 vcc,18,v250\n"
"s_nop 1\n"
"v_cndmask_b32 v246,v246,v255,vcc\n"
"v_cmp_gt_i32 vcc,19,v250\n"
"s_nop 1\n"
"v_cndmask_b32 v247,v247,v255,vcc\n"
"s_cmp_lg_u32 s90,0\n"
"s_cbranch_scc1 L1090_%=\n"
"v_max3_f32 v248,v248,v240,v241\n"
"v_max3_f32 v248,v248,v242,v243\n"
"v_max3_f32 v248,v248,v244,v245\n"
"v_max3_f32 v248,v248,v246,v247\n"
"s_branch L1091_%=\n"
"L1090_%=:\n"
"s_lshl_b32 s91,s88,13\n"
SA"s91,s91,s39\n"
SA"s91,s91,524288\n"
BL"a[148:151],v224,s[8:11],s91 offen offset:0 sc1\n"
BL"a[152:155],v224,s[8:11],s91 offen offset:1024 sc1\n"
BL"a[156:159],v224,s[8:11],s91 offen offset:2048 sc1\n"
BL"a[160:163],v224,s[8:11],s91 offen offset:3072 sc1\n"
SA"s91,s91,4096\n"
BL"a[164:167],v224,s[8:11],s91 offen offset:0 sc1\n"
BL"a[168:171],v224,s[8:11],s91 offen offset:1024 sc1\n"
BL"a[172:175],v224,s[8:11],s91 offen offset:2048 sc1\n"
BL"a[176:179],v224,s[8:11],s91 offen offset:3072 sc1\n"
WT"vmcnt(0)\n"
"v_sub_f32 v240,v240,v248\n"
"v_sub_f32 v241,v241,v248\n"
"v_sub_f32 v242,v242,v248\n"
"v_sub_f32 v243,v243,v248\n"
"v_sub_f32 v244,v244,v248\n"
"v_sub_f32 v245,v245,v248\n"
"v_sub_f32 v246,v246,v248\n"
"v_sub_f32 v247,v247,v248\n"
EX"v240,v240\n"
EX"v241,v241\n"
EX"v242,v242\n"
EX"v243,v243\n"
EX"v244,v244\n"
EX"v245,v245\n"
EX"v246,v246\n"
EX"v247,v247\n"
"s_nop 0\n"
PK"v240,v240,v241\n"
PK"v241,v242,v243\n"
PK"v242,v244,v245\n"
PK"v243,v246,v247\n"
"s_nop 1\n"
MF"v[176:179],a[148:151],v[240:243],v[176:179]\n"
MF"v[180:183],a[152:155],v[240:243],v[180:183]\n"
MF"v[184:187],a[156:159],v[240:243],v[184:187]\n"
MF"v[188:191],a[160:163],v[240:243],v[188:191]\n"
MF"v[192:195],a[164:167],v[240:243],v[192:195]\n"
MF"v[196:199],a[168:171],v[240:243],v[196:199]\n"
MF"v[200:203],a[172:175],v[240:243],v[200:203]\n"
MF"v[204:207],a[176:179],v[240:243],v[204:207]\n"
MF"a[144:147],a[128:131],v[240:243],a[144:147]\n"
"s_nop 7\n"
"L1091_%=:\n"
SA"s88,s88,1\n"
"s_cmp_le_u32 s88,s89\n"
"s_cbranch_scc1 L1089_%=\n"
"s_cmp_lg_u32 s90,0\n"
"s_cbranch_scc1 L1092_%=\n"
"v_mov_b32 v254,v248\n"
"s_nop 1\n"
"v_permlane16_swap_b32 v254,v248\n"
"s_nop 1\n"
"v_max_f32 v248,v248,v254\n"
"v_mov_b32 v254,v248\n"
"s_nop 1\n"
"v_permlane32_swap_b32 v254,v248\n"
"s_nop 1\n"
"v_max_f32 v248,v248,v254\n"
"s_mov_b32 s90,1\n"
"s_mov_b32 s88,0\n"
"s_branch L1089_%=\n"
"L1092_%=:\n"
"s_nop 7\n"
"v_accvgpr_read_b32 v252,a144\n"
"s_nop 1\n"
"s_branch L1087_%=\n"
"L1093_%=:\n"
"s_mov_b32 s54,0\n"
"L1095_%=:\n"
"s_sleep 2\n"
"buffer_load_dwordx2 v[240:241],v238,s[8:11],s63 offen sc1\n"
WT"vmcnt(0)\n"
SA"s54,s54,1\n"
"v_cmp_eq_u64 vcc,s[28:29],v[240:241]\n"
"s_cmp_eq_u64 vcc,exec\n"
"s_cbranch_scc1 L1094_%=\n"
"s_cmp_lt_u32 s54,1048576\n"
"s_cbranch_scc1 L1095_%=\n"
"s_branch L1094_%=\n"
"L1096_%=:\n"
"s_mov_b32 s54,0\n"
"L1098_%=:\n"
"s_sleep 2\n"
"buffer_load_dwordx2 v[240:241],v238,s[8:11],s63 offen sc1\n"
WT"vmcnt(0)\n"
SA"s54,s54,1\n"
"v_cmp_eq_u64 vcc,s[28:29],v[240:241]\n"
"s_cmp_eq_u64 vcc,exec\n"
"s_cbranch_scc1 L1097_%=\n"
"s_cmp_lt_u32 s54,1048576\n"
"s_cbranch_scc1 L1098_%=\n"
"s_branch L1097_%=\n"
"L1099_%=:\n"
"s_mov_b32 s54,0\n"
"L1101_%=:\n"
"s_sleep 2\n"
"buffer_load_dwordx2 v[240:241],v244,s[8:11],s92 offen sc1\n"
WT"vmcnt(0)\n"
SA"s54,s54,1\n"
"v_cmp_eq_u64 vcc,s[28:29],v[240:241]\n"
"s_cmp_eq_u64 vcc,exec\n"
"s_cbranch_scc1 L1100_%=\n"
"s_cmp_lt_u32 s54,1048576\n"
"s_cbranch_scc1 L1101_%=\n"
"s_branch L1100_%=\n"
"LTH_%=:\n"
RD"a[88:91],v227 offset:6144\n"
WT"lgkmcnt(6)\n"
MF"v[0:3],a[64:67],v[80:83],0\n"
"v_accvgpr_read_b32 v251,a140\n"
"v_subrev_u32 v253,0x2b800000,v251\n"
MF"v[40:43],a[64:67],v[112:115],0\n"
WT"lgkmcnt(4)\n"
RD"a[92:95],v227 offset:7168\n"
MF"v[4:7],a[68:71],v[80:83],0\n"
"v_cmp_le_u32 vcc,0x34000000,v253\n"
"s_cmp_lg_u64 vcc,0\n"
"s_cbranch_scc1 L1082_%=\n"
"L1081_%=:\n"
MF"v[44:47],a[68:71],v[112:115],0\n"
"v_rcp_f32 v251,v251\n"
MF"v[8:11],a[72:75],v[80:83],0\n"
SA"s41,s41,32768\n"
RD"a[96:99],v227 offset:8192\n"
MU"v144,v251,v144\n"
MF"v[48:51],a[72:75],v[112:115],0\n"
WT"lgkmcnt(4)\n"
RD"a[100:103],v227 offset:9216\n"
MU"v145,v251,v145\n"
MF"v[12:15],a[76:79],v[80:83],0\n"
"s_cmp_eq_u32 s41,163840\n"
MU"v146,v251,v146\n"
MU"v147,v251,v147\n"
MF"v[52:55],a[76:79],v[112:115],0\n"
"s_cselect_b32 s41,0,s41\n"
RD"a[104:107],v227 offset:10240\n"
"v_add_u32 v226,s41,v224\n"
MF"v[16:19],a[80:83],v[80:83],0\n"
SA"s41,s41,32768\n"
MU"v148,v251,v148\n"
MU"v149,v251,v149\n"
MF"v[56:59],a[80:83],v[112:115],0\n"
WT"lgkmcnt(3)\n"
RD"a[108:111],v227 offset:11264\n"
MU"v150,v251,v150\n"
MU"v151,v251,v151\n"
MF"v[20:23],a[84:87],v[80:83],0\n"
"s_cmp_eq_u32 s41,163840\n"
PK"v144,v144,v145\n"
PK"v145,v146,v147\n"
MF"v[60:63],a[84:87],v[112:115],0\n"
"s_cselect_b32 s41,0,s41\n"
RD"a[112:115],v227 offset:12288\n"
"v_add_u32 v235,s41,v224\n"
PK"v146,v148,v149\n"
MF"v[24:27],a[88:91],v[80:83],0\n"
SA"s41,s41,32768\n"
PK"v147,v150,v151\n"
MU"v152,v251,v152\n"
MF"v[64:67],a[88:91],v[112:115],0\n"
"s_cmp_eq_u32 s41,163840\n"
BS"v[144:147],v224,s[8:11],s60 offen offset:0 sc1\n"
MU"v153,v251,v153\n"
MF"v[28:31],a[92:95],v[80:83],0\n"
"s_cselect_b32 s41,0,s41\n"
RD"a[116:119],v227 offset:13312\n"
"v_add_u32 v236,s41,v224\n"
MU"v154,v251,v154\n"
MF"v[68:71],a[92:95],v[112:115],0\n"
WT"lgkmcnt(3)\n"
RD"a[120:123],v227 offset:14336\n"
MU"v155,v251,v155\n"
MU"v156,v251,v156\n"
MF"v[32:35],a[96:99],v[80:83],0\n"
SA"s41,s41,32768\n"
MU"v157,v251,v157\n"
MU"v158,v251,v158\n"
MU"v159,v251,v159\n"
MF"v[72:75],a[96:99],v[112:115],0\n"
"s_cmp_eq_u32 s41,163840\n"
RD"a[124:127],v227 offset:15360\n"
PK"v152,v152,v153\n"
PK"v153,v154,v155\n"
MF"v[36:39],a[100:103],v[80:83],0\n"
"s_cselect_b32 s41,0,s41\n"
PK"v154,v156,v157\n"
"v_add_u32 v237,s41,v224\n"
PK"v155,v158,v159\n"
MF"v[76:79],a[100:103],v[112:115],0\n"
BS"v[152:155],v224,s[8:11],s60 offen offset:1024 sc1\n"
MU"v160,v251,v160\n"
MF"v[0:3],a[104:107],v[88:91],v[0:3]\n"
RD"a[64:67],v227 offset:16384\n"
MU"v161,v251,v161\n"
MU"v162,v251,v162\n"
MF"v[40:43],a[104:107],v[120:123],v[40:43]\n"
WT"lgkmcnt(3)\n"
RD"a[68:71],v227 offset:17408\n"
MU"v163,v251,v163\n"
MU"v164,v251,v164\n"
MF"v[4:7],a[108:111],v[88:91],v[4:7]\n"
WT"vmcnt(2)\n"
"buffer_store_dwordx2 v[232:233],v230,s[8:11],s61 offen sc1\n"
MU"v165,v251,v165\n"
MF"v[44:47],a[108:111],v[120:123],v[44:47]\n"
RD"a[72:75],v227 offset:18432\n"
MU"v166,v251,v166\n"
MU"v167,v251,v167\n"
MF"v[8:11],a[112:115],v[88:91],v[8:11]\n"
SA"s51,s60,16384\n"
"v_accvgpr_read_b32 v252,a144\n"
"v_subrev_u32 v253,0x2b800000,v252\n"
PK"v160,v160,v161\n"
MF"v[48:51],a[112:115],v[120:123],v[48:51]\n"
"s_lshl_b32 s62,s34,14\n"
RD"a[76:79],v227 offset:19456\n"
PK"v161,v162,v163\n"
PK"v162,v164,v165\n"
MF"v[12:15],a[116:119],v[88:91],v[12:15]\n"
"s_sub_u32 s62,s61,s62\n"
"v_cmp_le_u32 vcc,0x34000000,v253\n"
"s_cmp_lg_u64 vcc,0\n"
"s_cbranch_scc1 L1088_%=\n"
"L1087_%=:\n"
MF"v[52:55],a[116:119],v[120:123],v[52:55]\n"
WT"lgkmcnt(3)\n"
"v_rcp_f32 v252,v252\n"
PK"v163,v166,v167\n"
MF"v[16:19],a[120:123],v[88:91],v[16:19]\n"
SA"s62,s62,122880\n"
BS"v[160:163],v224,s[8:11],s60 offen offset:2048 sc1\n"
MU"v168,v251,v168\n"
MF"v[56:59],a[120:123],v[120:123],v[56:59]\n"
RD"a[80:83],v227 offset:20480\n"
MU"v169,v251,v169\n"
MU"v170,v251,v170\n"
MF"v[20:23],a[124:127],v[88:91],v[20:23]\n"
RD"a[84:87],v227 offset:21504\n"
MU"v171,v251,v171\n"
MU"v172,v251,v172\n"
MF"v[60:63],a[124:127],v[120:123],v[60:63]\n"
RD"a[88:91],v227 offset:22528\n"
MU"v173,v251,v173\n"
MU"v174,v251,v174\n"
MF"v[24:27],a[64:67],v[88:91],v[24:27]\n"
MU"v175,v251,v175\n"
PK"v168,v168,v169\n"
PK"v169,v170,v171\n"
MF"v[64:67],a[64:67],v[120:123],v[64:67]\n"
WT"lgkmcnt(3)\n"
RD"a[92:95],v227 offset:23552\n"
MU"v176,v252,v176\n"
MU"v177,v252,v177\n"
MF"v[28:31],a[68:71],v[88:91],v[28:31]\n"
PK"v170,v172,v173\n"
PK"v171,v174,v175\n"
MU"v178,v252,v178\n"
MF"v[68:71],a[68:71],v[120:123],v[68:71]\n"
BS"v[168:171],v224,s[8:11],s60 offen offset:3072 sc1\n"
MU"v179,v252,v179\n"
MF"v[32:35],a[72:75],v[88:91],v[32:35]\n"
RD"a[96:99],v227 offset:24576\n"
MU"v180,v252,v180\n"
MU"v181,v252,v181\n"
MF"v[72:75],a[72:75],v[120:123],v[72:75]\n"
RD"a[100:103],v227 offset:25600\n"
MU"v182,v252,v182\n"
MU"v183,v252,v183\n"
MF"v[36:39],a[76:79],v[88:91],v[36:39]\n"
PK"v176,v176,v177\n"
PK"v177,v178,v179\n"
PK"v178,v180,v181\n"
MF"v[76:79],a[76:79],v[120:123],v[76:79]\n"
WT"lgkmcnt(3)\n"
RD"a[104:107],v227 offset:26624\n"
PK"v179,v182,v183\n"
MU"v184,v252,v184\n"
MF"v[0:3],a[80:83],v[96:99],v[0:3]\n"
BS"v[176:179],v224,s[8:11],s51 offen offset:0 sc1\n"
MU"v185,v252,v185\n"
MF"v[40:43],a[80:83],v[128:131],v[40:43]\n"
RD"a[108:111],v227 offset:27648\n"
MU"v186,v252,v186\n"
MU"v187,v252,v187\n"
MF"v[4:7],a[84:87],v[96:99],v[4:7]\n"
MU"v188,v252,v188\n"
MU"v189,v252,v189\n"
MU"v190,v252,v190\n"
MF"v[44:47],a[84:87],v[128:131],v[44:47]\n"
RD"a[112:115],v227 offset:28672\n"
MU"v191,v252,v191\n"
PK"v184,v184,v185\n"
MF"v[8:11],a[88:91],v[96:99],v[8:11]\n"
PK"v185,v186,v187\n"
PK"v186,v188,v189\n"
PK"v187,v190,v191\n"
MF"v[48:51],a[88:91],v[128:131],v[48:51]\n"
WT"lgkmcnt(3)\n"
BS"v[184:187],v224,s[8:11],s51 offen offset:1024 sc1\n"
MU"v192,v252,v192\n"
MF"v[12:15],a[92:95],v[96:99],v[12:15]\n"
RD"a[116:119],v227 offset:29696\n"
MU"v193,v252,v193\n"
MU"v194,v252,v194\n"
MF"v[52:55],a[92:95],v[128:131],v[52:55]\n"
RD"a[120:123],v227 offset:30720\n"
"v_bfe_u32 v238,v224,4,2\n"
MU"v195,v252,v195\n"
MF"v[16:19],a[96:99],v[96:99],v[16:19]\n"
"s_andn2_b32 s63,s61,255\n"
"v_lshlrev_b32 v238,6,v238\n"
MU"v196,v252,v196\n"
MU"v197,v252,v197\n"
MF"v[56:59],a[96:99],v[128:131],v[56:59]\n"
RD"a[124:127],v227 offset:31744\n"
MU"v198,v252,v198\n"
MU"v199,v252,v199\n"
MF"v[20:23],a[100:103],v[96:99],v[20:23]\n"
"s_barrier\n"
MF"v[60:63],a[100:103],v[128:131],v[60:63]\n"
WT"lgkmcnt(3)\n"
"buffer_load_dwordx2 v[240:241],v238,s[8:11],s63 offen sc1\n"
PK"v192,v192,v193\n"
MF"v[24:27],a[104:107],v[96:99],v[24:27]\n"
RD"a[64:67],v226 offset:0\n"
PK"v193,v194,v195\n"
PK"v194,v196,v197\n"
MF"v[64:67],a[104:107],v[128:131],v[64:67]\n"
RD"a[68:71],v226 offset:1024\n"
PK"v195,v198,v199\n"
MU"v200,v252,v200\n"
MF"v[28:31],a[108:111],v[96:99],v[28:31]\n"
BS"v[192:195],v224,s[8:11],s51 offen offset:2048 sc1\n"
MU"v201,v252,v201\n"
MF"v[68:71],a[108:111],v[128:131],v[68:71]\n"
RD"a[72:75],v226 offset:2048\n"
MU"v202,v252,v202\n"
MU"v203,v252,v203\n"
MF"v[32:35],a[112:115],v[96:99],v[32:35]\n"
MU"v204,v252,v204\n"
MU"v205,v252,v205\n"
MU"v206,v252,v206\n"
MF"v[72:75],a[112:115],v[128:131],v[72:75]\n"
WT"lgkmcnt(3)\n"
RD"a[76:79],v226 offset:3072\n"
MU"v207,v252,v207\n"
PK"v200,v200,v201\n"
MF"v[36:39],a[116:119],v[96:99],v[36:39]\n"
"v_readlane_b32 s43,v228,s42\n"
PK"v201,v202,v203\n"
PK"v202,v204,v205\n"
MF"v[76:79],a[116:119],v[128:131],v[76:79]\n"
RD"a[80:83],v226 offset:4096\n"
PK"v203,v206,v207\n"
MF"v[0:3],a[120:123],v[104:107],v[0:3]\n"
BS"v[200:203],v224,s[8:11],s51 offen offset:3072 sc1\n"
MF"v[40:43],a[120:123],v[136:139],v[40:43]\n"
RD"a[84:87],v226 offset:5120\n"
MF"v[4:7],a[124:127],v[104:107],v[4:7]\n"
"s_mov_b32 m0,s40\n"
MF"v[44:47],a[124:127],v[136:139],v[44:47]\n"
WT"lgkmcnt(3)\n"
RD"a[88:91],v226 offset:6144\n"
MF"v[8:11],a[64:67],v[104:107],v[8:11]\n"
"v_readlane_b32 s44,v229,s42\n"
MF"v[48:51],a[64:67],v[136:139],v[48:51]\n"
RD"a[92:95],v226 offset:7168\n"
MF"v[12:15],a[68:71],v[104:107],v[12:15]\n"
SA"s42,s42,1\n"
BL"v225,s[8:11],s43 offen offset:0 sc1 lds\n"
MF"v[52:55],a[68:71],v[136:139],v[52:55]\n"
RD"a[96:99],v227 offset:0\n"
MF"v[16:19],a[72:75],v[104:107],v[16:19]\n"
MF"v[56:59],a[72:75],v[136:139],v[56:59]\n"
WT"lgkmcnt(3)\n"
RD"a[100:103],v227 offset:1024\n"
MF"v[20:23],a[76:79],v[104:107],v[20:23]\n"
MF"v[60:63],a[76:79],v[136:139],v[60:63]\n"
RD"a[104:107],v227 offset:2048\n"
MF"v[24:27],a[80:83],v[104:107],v[24:27]\n"
BL"v225,s[8:11],s43 offen offset:1024 sc1 lds\n"
MF"v[64:67],a[80:83],v[136:139],v[64:67]\n"
RD"a[108:111],v227 offset:3072\n"
MF"v[28:31],a[84:87],v[104:107],v[28:31]\n"
MF"v[68:71],a[84:87],v[136:139],v[68:71]\n"
WT"lgkmcnt(3)\n"
RD"a[112:115],v227 offset:4096\n"
MF"v[32:35],a[88:91],v[104:107],v[32:35]\n"
MF"v[72:75],a[88:91],v[136:139],v[72:75]\n"
RD"a[116:119],v227 offset:5120\n"
MF"v[36:39],a[92:95],v[104:107],v[36:39]\n"
BL"v225,s[8:11],s43 offen offset:2048 sc1 lds\n"
MF"v[76:79],a[92:95],v[136:139],v[76:79]\n"
RD"a[120:123],v227 offset:6144\n"
MF"v[84:87],a[96:99],v[144:147],0\n"
MF"v[164:167],a[96:99],v[176:179],0\n"
WT"lgkmcnt(3)\n"
RD"a[124:127],v227 offset:7168\n"
MF"v[92:95],a[100:103],v[144:147],0\n"
MF"v[172:175],a[100:103],v[176:179],0\n"
RD"a[64:67],v227 offset:8192\n"
MF"v[100:103],a[104:107],v[144:147],0\n"
BL"v225,s[8:11],s43 offen offset:3072 sc1 lds\n"
MF"v[180:183],a[104:107],v[176:179],0\n"
RD"a[68:71],v227 offset:9216\n"
MF"v[108:111],a[108:111],v[144:147],0\n"
SA"m0,s40,16384\n"
MF"v[188:191],a[108:111],v[176:179],0\n"
WT"lgkmcnt(3)\n"
RD"a[72:75],v227 offset:10240\n"
MF"v[116:119],a[112:115],v[144:147],0\n"
MF"v[196:199],a[112:115],v[176:179],0\n"
SA"s48,s59,32768\n"
RD"a[76:79],v227 offset:11264\n"
MF"v[124:127],a[116:119],v[144:147],0\n"
"s_and_b32 s48,s48,0x18000\n"
BL"v225,s[8:11],s44 offen offset:0 sc1 lds\n"
MF"v[204:207],a[116:119],v[176:179],0\n"
SA"s40,s40,32768\n"
RD"a[80:83],v227 offset:12288\n"
"s_andn2_b32 s49,s59,0x18000\n"
MF"v[132:135],a[120:123],v[144:147],0\n"
"s_cmp_eq_u32 s40,s46\n"
"s_cselect_b32 s40,s47,s40\n"
WT"vmcnt(7)\n"
"v_cmp_eq_u64 vcc,s[28:29],v[240:241]\n"
"s_cmp_eq_u64 vcc,exec\n"
"s_cbranch_scc0 L1093_%=\n"
"L1094_%=:\n"
MF"v[208:211],a[120:123],v[176:179],0\n"
WT"lgkmcnt(3)\n"
RD"a[84:87],v227 offset:13312\n"
"s_or_b32 s52,s48,s49\n"
MF"v[140:143],a[124:127],v[144:147],0\n"
SA"s53,s52,16384\n"
BL"a[0:3],v224,s[8:11],s52 offen offset:0 sc1\n"
MF"v[212:215],a[124:127],v[176:179],0\n"
RD"a[88:91],v227 offset:14336\n"
MF"v[148:151],a[64:67],v[144:147],0\n"
BL"v225,s[8:11],s44 offen offset:1024 sc1 lds\n"
MF"v[216:219],a[64:67],v[176:179],0\n"
RD"a[92:95],v227 offset:15360\n"
MF"v[156:159],a[68:71],v[144:147],0\n"
BL"a[4:7],v224,s[8:11],s53 offen offset:0 sc1\n"
MF"v[220:223],a[68:71],v[176:179],0\n"
WT"lgkmcnt(3)\n"
RD"a[96:99],v227 offset:16384\n"
MF"v[84:87],a[72:75],v[152:155],v[84:87]\n"
MF"v[164:167],a[72:75],v[184:187],v[164:167]\n"
RD"a[100:103],v227 offset:17408\n"
MF"v[92:95],a[76:79],v[152:155],v[92:95]\n"
BL"v225,s[8:11],s44 offen offset:2048 sc1 lds\n"
MF"v[172:175],a[76:79],v[184:187],v[172:175]\n"
RD"a[104:107],v227 offset:18432\n"
MF"v[100:103],a[80:83],v[152:155],v[100:103]\n"
BL"a[8:11],v224,s[8:11],s52 offen offset:1024 sc1\n"
MF"v[180:183],a[80:83],v[184:187],v[180:183]\n"
WT"lgkmcnt(3)\n"
RD"a[108:111],v227 offset:19456\n"
MF"v[108:111],a[84:87],v[152:155],v[108:111]\n"
"v_readlane_b32 s43,v228,s42\n"
MF"v[188:191],a[84:87],v[184:187],v[188:191]\n"
RD"a[112:115],v227 offset:20480\n"
MF"v[116:119],a[88:91],v[152:155],v[116:119]\n"
BL"a[12:15],v224,s[8:11],s53 offen offset:1024 sc1\n"
MF"v[196:199],a[88:91],v[184:187],v[196:199]\n"
BL"v225,s[8:11],s44 offen offset:3072 sc1 lds\n"
MF"v[124:127],a[92:95],v[152:155],v[124:127]\n"
"s_mov_b32 m0,s40\n"
RD"a[116:119],v227 offset:21504\n"
MF"v[204:207],a[92:95],v[184:187],v[204:207]\n"
WT"lgkmcnt(3)\n"
RD"a[120:123],v227 offset:22528\n"
MF"v[132:135],a[96:99],v[152:155],v[132:135]\n"
WT"vmcnt(12)\n"
"buffer_store_dwordx2 v[232:233],v230,s[8:11],s62 offen sc1\n"
MF"v[208:211],a[96:99],v[184:187],v[208:211]\n"
BL"a[16:19],v224,s[8:11],s52 offen offset:2048 sc1\n"
MF"v[140:143],a[100:103],v[152:155],v[140:143]\n"
RD"a[124:127],v227 offset:23552\n"
"v_readlane_b32 s44,v229,s42\n"
MF"v[212:215],a[100:103],v[184:187],v[212:215]\n"
SA"s42,s42,1\n"
BL"v225,s[8:11],s43 offen offset:0 sc1 lds\n"
MF"v[148:151],a[104:107],v[152:155],v[148:151]\n"
RD"a[64:67],v227 offset:24576\n"
MF"v[216:219],a[104:107],v[184:187],v[216:219]\n"
WT"lgkmcnt(3)\n"
BL"a[20:23],v224,s[8:11],s53 offen offset:2048 sc1\n"
MF"v[156:159],a[108:111],v[152:155],v[156:159]\n"
RD"a[68:71],v227 offset:25600\n"
MF"v[220:223],a[108:111],v[184:187],v[220:223]\n"
RD"a[72:75],v227 offset:26624\n"
MF"v[84:87],a[112:115],v[160:163],v[84:87]\n"
"s_lshr_b32 s89,s31,3\n"
BL"v225,s[8:11],s43 offen offset:1024 sc1 lds\n"
MF"v[164:167],a[112:115],v[192:195],v[164:167]\n"
"s_lshl_b32 s89,s89,4\n"
RD"a[76:79],v227 offset:27648\n"
MF"v[92:95],a[116:119],v[160:163],v[92:95]\n"
SA"s89,s89,s30\n"
BL"a[24:27],v224,s[8:11],s52 offen offset:3072 sc1\n"
MF"v[172:175],a[116:119],v[192:195],v[172:175]\n"
WT"lgkmcnt(3)\n"
RD"a[80:83],v227 offset:28672\n"
"s_lshl_b32 s89,s89,3\n"
MF"v[100:103],a[120:123],v[160:163],v[100:103]\n"
SA"s89,s89,s32\n"
MF"v[180:183],a[120:123],v[192:195],v[180:183]\n"
SA"s48,s59,65536\n"
RD"a[84:87],v227 offset:29696\n"
"s_mul_i32 s88,s89,5120\n"
MF"v[108:111],a[124:127],v[160:163],v[108:111]\n"
"s_and_b32 s48,s48,0x18000\n"
BL"a[28:31],v224,s[8:11],s53 offen offset:3072 sc1\n"
MF"v[188:191],a[124:127],v[192:195],v[188:191]\n"
SA"s88,s88,59506688\n"
BL"v225,s[8:11],s43 offen offset:2048 sc1 lds\n"
MF"v[116:119],a[64:67],v[160:163],v[116:119]\n"
"s_andn2_b32 s49,s59,0x18000\n"
RD"a[88:91],v227 offset:30720\n"
"s_lshl_b32 s89,s89,6\n"
MF"v[196:199],a[64:67],v[192:195],v[196:199]\n"
WT"lgkmcnt(3)\n"
RD"a[92:95],v227 offset:31744\n"
"v_bfe_u32 v239,v224,1,6\n"
MF"v[124:127],a[68:71],v[160:163],v[124:127]\n"
"s_or_b32 s52,s48,s49\n"
"v_bfe_u32 v242,v224,7,1\n"
SA"s53,s52,16384\n"
MF"v[204:207],a[68:71],v[192:195],v[204:207]\n"
SA"s89,s89,80478208\n"
BL"a[180:183],v224,s[8:11],s52 offen offset:0 sc1\n"
MF"v[132:135],a[72:75],v[160:163],v[132:135]\n"
RD"a[96:99],v226 offset:0\n"
"v_lshl_add_u32 v239,v242,11,v239\n"
MF"v[208:211],a[72:75],v[192:195],v[208:211]\n"
BL"v225,s[8:11],s43 offen offset:3072 sc1 lds\n"
MF"v[140:143],a[76:79],v[160:163],v[140:143]\n"
SA"m0,s40,16384\n"
RD"a[100:103],v226 offset:1024\n"
"v_lshrrev_b32 v242,8,v224\n"
MF"v[212:215],a[76:79],v[192:195],v[212:215]\n"
WT"lgkmcnt(3)\n"
BL"a[184:187],v224,s[8:11],s53 offen offset:0 sc1\n"
MF"v[148:151],a[80:83],v[160:163],v[148:151]\n"
RD"a[104:107],v226 offset:2048\n"
"v_lshl_add_u32 v239,v242,27,v239\n"
MF"v[216:219],a[80:83],v[192:195],v[216:219]\n"
RD"a[108:111],v226 offset:3072\n"
MF"v[156:159],a[84:87],v[160:163],v[156:159]\n"
BL"v225,s[8:11],s44 offen offset:0 sc1 lds\n"
MF"v[220:223],a[84:87],v[192:195],v[220:223]\n"
SA"s40,s40,32768\n"
RD"a[112:115],v226 offset:4096\n"
MF"v[84:87],a[88:91],v[168:171],v[84:87]\n"
"s_cmp_eq_u32 s40,s46\n"
BL"a[188:191],v224,s[8:11],s52 offen offset:1024 sc1\n"
MF"v[164:167],a[88:91],v[200:203],v[164:167]\n"
WT"lgkmcnt(3)\n"
RD"a[116:119],v226 offset:5120\n"
"s_cselect_b32 s40,s47,s40\n"
MF"v[92:95],a[92:95],v[168:171],v[92:95]\n"
MF"v[172:175],a[92:95],v[200:203],v[172:175]\n"
RD"a[120:123],v226 offset:6144\n"
MF"v[100:103],a[96:99],v[168:171],v[100:103]\n"
BL"a[192:195],v224,s[8:11],s53 offen offset:1024 sc1\n"
MF"v[180:183],a[96:99],v[200:203],v[180:183]\n"
BL"v225,s[8:11],s44 offen offset:1024 sc1 lds\n"
MF"v[108:111],a[100:103],v[168:171],v[108:111]\n"
RD"a[124:127],v226 offset:7168\n"
MF"v[188:191],a[100:103],v[200:203],v[188:191]\n"
WT"lgkmcnt(3)\n"
RD"a[64:67],v226 offset:8192\n"
MF"v[116:119],a[104:107],v[168:171],v[116:119]\n"
BL"a[196:199],v224,s[8:11],s52 offen offset:2048 sc1\n"
MF"v[196:199],a[104:107],v[200:203],v[196:199]\n"
RD"a[68:71],v226 offset:9216\n"
MF"v[124:127],a[108:111],v[168:171],v[124:127]\n"
BL"v225,s[8:11],s44 offen offset:2048 sc1 lds\n"
MF"v[204:207],a[108:111],v[200:203],v[204:207]\n"
RD"a[72:75],v226 offset:10240\n"
MF"v[132:135],a[112:115],v[168:171],v[132:135]\n"
BL"a[200:203],v224,s[8:11],s53 offen offset:2048 sc1\n"
MF"v[208:211],a[112:115],v[200:203],v[208:211]\n"
WT"lgkmcnt(3)\n"
RD"a[76:79],v226 offset:11264\n"
MF"v[140:143],a[116:119],v[168:171],v[140:143]\n"
MF"v[212:215],a[116:119],v[200:203],v[212:215]\n"
RD"a[80:83],v226 offset:12288\n"
MF"v[148:151],a[120:123],v[168:171],v[148:151]\n"
BL"v225,s[8:11],s44 offen offset:3072 sc1 lds\n"
MF"v[216:219],a[120:123],v[200:203],v[216:219]\n"
RD"a[84:87],v226 offset:13312\n"
MF"v[156:159],a[124:127],v[168:171],v[156:159]\n"
BL"a[204:207],v224,s[8:11],s52 offen offset:3072 sc1\n"
MF"v[220:223],a[124:127],v[200:203],v[220:223]\n"
WT"vmcnt(26) lgkmcnt(3)\n"
RD"a[88:91],v226 offset:14336\n"
MF"v[0:3],a[64:67],a[0:3],v[0:3]\n"
WT"vmcnt(24)\n"
MF"v[40:43],a[64:67],a[4:7],v[40:43]\n"
SA"s48,s59,98304\n"
RD"a[92:95],v226 offset:15360\n"
MF"v[4:7],a[68:71],a[0:3],v[4:7]\n"
"s_and_b32 s48,s48,0x18000\n"
BL"a[208:211],v224,s[8:11],s53 offen offset:3072 sc1\n"
MF"v[44:47],a[68:71],a[4:7],v[44:47]\n"
"s_andn2_b32 s49,s59,0x18000\n"
RD"a[96:99],v226 offset:16384\n"
MF"v[8:11],a[72:75],a[0:3],v[8:11]\n"
"s_or_b32 s52,s48,s49\n"
SA"s53,s52,16384\n"
MF"v[48:51],a[72:75],a[4:7],v[48:51]\n"
WT"lgkmcnt(3)\n"
RD"a[100:103],v226 offset:17408\n"
MF"v[12:15],a[76:79],a[0:3],v[12:15]\n"
BL"a[212:215],v224,s[8:11],s52 offen offset:0 sc1\n"
MF"v[52:55],a[76:79],a[4:7],v[52:55]\n"
RD"a[104:107],v226 offset:18432\n"
MF"v[16:19],a[80:83],a[0:3],v[16:19]\n"
MF"v[56:59],a[80:83],a[4:7],v[56:59]\n"
RD"a[108:111],v226 offset:19456\n"
MF"v[20:23],a[84:87],a[0:3],v[20:23]\n"
BL"a[216:219],v224,s[8:11],s53 offen offset:0 sc1\n"
MF"v[60:63],a[84:87],a[4:7],v[60:63]\n"
WT"lgkmcnt(3)\n"
RD"a[112:115],v226 offset:20480\n"
MF"v[24:27],a[88:91],a[0:3],v[24:27]\n"
MF"v[64:67],a[88:91],a[4:7],v[64:67]\n"
RD"a[116:119],v226 offset:21504\n"
MF"v[28:31],a[92:95],a[0:3],v[28:31]\n"
MF"v[68:71],a[92:95],a[4:7],v[68:71]\n"
RD"a[120:123],v226 offset:22528\n"
MF"v[32:35],a[96:99],a[0:3],v[32:35]\n"
BL"a[220:223],v224,s[8:11],s52 offen offset:1024 sc1\n"
MF"v[72:75],a[96:99],a[4:7],v[72:75]\n"
WT"lgkmcnt(3)\n"
RD"a[124:127],v226 offset:23552\n"
MF"v[36:39],a[100:103],a[0:3],v[36:39]\n"
MF"v[76:79],a[100:103],a[4:7],v[76:79]\n"
WT"vmcnt(26)\n"
RD"a[64:67],v226 offset:24576\n"
MF"v[0:3],a[104:107],a[8:11],v[0:3]\n"
WT"vmcnt(25)\n"
BL"a[224:227],v224,s[8:11],s53 offen offset:1024 sc1\n"
MF"v[40:43],a[104:107],a[12:15],v[40:43]\n"
RD"a[68:71],v226 offset:25600\n"
MF"v[4:7],a[108:111],a[8:11],v[4:7]\n"
MF"v[44:47],a[108:111],a[12:15],v[44:47]\n"
WT"lgkmcnt(3)\n"
RD"a[72:75],v226 offset:26624\n"
MF"v[8:11],a[112:115],a[8:11],v[8:11]\n"
BL"a[228:231],v224,s[8:11],s52 offen offset:2048 sc1\n"
MF"v[48:51],a[112:115],a[12:15],v[48:51]\n"
RD"a[76:79],v226 offset:27648\n"
MF"v[12:15],a[116:119],a[8:11],v[12:15]\n"
MF"v[52:55],a[116:119],a[12:15],v[52:55]\n"
RD"a[80:83],v226 offset:28672\n"
MF"v[16:19],a[120:123],a[8:11],v[16:19]\n"
BL"a[232:235],v224,s[8:11],s53 offen offset:2048 sc1\n"
MF"v[56:59],a[120:123],a[12:15],v[56:59]\n"
WT"lgkmcnt(3)\n"
RD"a[84:87],v226 offset:29696\n"
MF"v[20:23],a[124:127],a[8:11],v[20:23]\n"
MF"v[60:63],a[124:127],a[12:15],v[60:63]\n"
RD"a[88:91],v226 offset:30720\n"
MF"v[24:27],a[64:67],a[8:11],v[24:27]\n"
MF"v[64:67],a[64:67],a[12:15],v[64:67]\n"
RD"a[92:95],v226 offset:31744\n"
MF"v[28:31],a[68:71],a[8:11],v[28:31]\n"
"s_barrier\n"
MF"v[68:71],a[68:71],a[12:15],v[68:71]\n"
WT"lgkmcnt(3)\n"
BL"a[236:239],v224,s[8:11],s52 offen offset:3072 sc1\n"
MF"v[32:35],a[72:75],a[8:11],v[32:35]\n"
RD"a[96:99],v235 offset:0\n"
MF"v[72:75],a[72:75],a[12:15],v[72:75]\n"
RD"a[100:103],v235 offset:1024\n"
MF"v[36:39],a[76:79],a[8:11],v[36:39]\n"
BL"a[240:243],v224,s[8:11],s53 offen offset:3072 sc1\n"
MF"v[76:79],a[76:79],a[12:15],v[76:79]\n"
WT"vmcnt(27)\n"
RD"a[104:107],v235 offset:2048\n"
MF"v[0:3],a[80:83],a[16:19],v[0:3]\n"
WT"vmcnt(25)\n"
MF"v[40:43],a[80:83],a[20:23],v[40:43]\n"
WT"lgkmcnt(3)\n"
RD"a[108:111],v235 offset:3072\n"
MF"v[4:7],a[84:87],a[16:19],v[4:7]\n"
MF"v[44:47],a[84:87],a[20:23],v[44:47]\n"
RD"a[112:115],v235 offset:4096\n"
MF"v[8:11],a[88:91],a[16:19],v[8:11]\n"
MF"v[48:51],a[88:91],a[20:23],v[48:51]\n"
RD"a[116:119],v235 offset:5120\n"
MF"v[12:15],a[92:95],a[16:19],v[12:15]\n"
MF"v[52:55],a[92:95],a[20:23],v[52:55]\n"
WT"lgkmcnt(3)\n"
RD"a[120:123],v235 offset:6144\n"
MF"v[16:19],a[96:99],a[16:19],v[16:19]\n"
MF"v[56:59],a[96:99],a[20:23],v[56:59]\n"
RD"a[124:127],v235 offset:7168\n"
MF"v[20:23],a[100:103],a[16:19],v[20:23]\n"
MF"v[60:63],a[100:103],a[20:23],v[60:63]\n"
RD"a[64:67],v235 offset:8192\n"
MF"v[24:27],a[104:107],a[16:19],v[24:27]\n"
MF"v[64:67],a[104:107],a[20:23],v[64:67]\n"
WT"lgkmcnt(3)\n"
RD"a[68:71],v235 offset:9216\n"
"v_bfe_u32 v238,v224,4,2\n"
MF"v[28:31],a[108:111],a[16:19],v[28:31]\n"
"s_andn2_b32 s63,s62,255\n"
"v_lshlrev_b32 v238,6,v238\n"
MF"v[68:71],a[108:111],a[20:23],v[68:71]\n"
RD"a[72:75],v235 offset:10240\n"
MF"v[32:35],a[112:115],a[16:19],v[32:35]\n"
"buffer_load_dwordx2 v[240:241],v238,s[8:11],s63 offen sc1\n"
MF"v[72:75],a[112:115],a[20:23],v[72:75]\n"
RD"a[76:79],v235 offset:11264\n"
MF"v[36:39],a[116:119],a[16:19],v[36:39]\n"
MF"v[76:79],a[116:119],a[20:23],v[76:79]\n"
WT"vmcnt(24) lgkmcnt(3)\n"
RD"a[80:83],v235 offset:12288\n"
MF"v[0:3],a[120:123],a[24:27],v[0:3]\n"
WT"vmcnt(23)\n"
MF"v[40:43],a[120:123],a[28:31],v[40:43]\n"
RD"a[84:87],v235 offset:13312\n"
MF"v[4:7],a[124:127],a[24:27],v[4:7]\n"
MF"v[44:47],a[124:127],a[28:31],v[44:47]\n"
RD"a[88:91],v235 offset:14336\n"
"s_branch LTJ_%=\n"
"L1125_%=:\n"
"s_mov_b32 s54,0\n"
"L1127_%=:\n"
"s_sleep 2\n"
BL"v[240:243],v238,s[8:11],0 offen sc1\n"
WT"vmcnt(0)\n"
SA"s54,s54,1\n"
"v_cmp_eq_u64 vcc,s[28:29],v[240:241]\n"
"v_cmp_eq_u64_e64 s[50:51],s[28:29],v[242:243]\n"
"s_and_b64 vcc,vcc,s[50:51]\n"
"s_cmp_eq_u64 vcc,exec\n"
"s_cbranch_scc1 L1126_%=\n"
"s_cmp_lt_u32 s54,1048576\n"
"s_cbranch_scc1 L1127_%=\n"
"s_branch L1126_%=\n"
"L1128_%=:\n"
"s_mov_b32 s54,0\n"
"L1130_%=:\n"
"s_sleep 2\n"
"buffer_load_dwordx2 v[240:241],v238,s[8:11],s50 offen sc1\n"
WT"vmcnt(0)\n"
SA"s54,s54,1\n"
"v_cmp_eq_u64 vcc,s[28:29],v[240:241]\n"
"s_cmp_eq_u64 vcc,exec\n"
"s_cbranch_scc1 L1129_%=\n"
"s_cmp_lt_u32 s54,1048576\n"
"s_cbranch_scc1 L1130_%=\n"
"s_branch L1129_%=\n"
"L1131_%=:\n"
"s_mov_b32 s54,0\n"
"L1133_%=:\n"
"s_sleep 2\n"
"buffer_load_dwordx2 v[244:245],v242,s[8:11],s23 offen sc1\n"
WT"vmcnt(0)\n"
SA"s54,s54,1\n"
"v_cmp_eq_u64 vcc,s[28:29],v[244:245]\n"
"s_cmp_eq_u64 vcc,exec\n"
"s_cbranch_scc1 L1132_%=\n"
"s_cmp_lt_u32 s54,1048576\n"
"s_cbranch_scc1 L1133_%=\n"
"s_branch L1132_%=\n"
"LFA_%=:\n"
WT"lgkmcnt(5)\n"
MF"v[0:3],a[64:67],a[0:3],v[208:211]\n"
"s_mov_b32 m0,s40\n"
PK"v192,v192,v193\n"
PK"v193,v194,v195\n"
MF"v[8:11],a[64:67],a[16:19],0\n"
"v_readlane_b32 s43,v228,s42\n"
PK"v194,v196,v197\n"
MF"v[16:19],a[64:67],a[32:35],0\n"
PK"v195,v198,v199\n"
"v_readlane_b32 s44,v229,s42\n"
MF"v[24:27],a[64:67],a[48:51],0\n"
WT"lgkmcnt(0)\n"
BL"v225,s[8:11],s43 offen offset:0 sc1 lds\n"
MF"v[0:3],a[68:71],a[4:7],v[0:3]\n"
SA"s42,s42,1\n"
"v_accvgpr_write_b32 a44,v192\n"
"v_accvgpr_write_b32 a45,v193\n"
MF"v[8:11],a[68:71],a[20:23],v[8:11]\n"
"v_accvgpr_write_b32 a46,v194\n"
"v_accvgpr_write_b32 a47,v195\n"
MF"v[16:19],a[68:71],a[36:39],v[16:19]\n"
PK"v200,v200,v201\n"
PK"v201,v202,v203\n"
MF"v[24:27],a[68:71],a[52:55],v[24:27]\n"
PK"v202,v204,v205\n"
PK"v203,v206,v207\n"
"v_mov_b32 v227,v226\n"
MF"v[0:3],a[72:75],a[8:11],v[0:3]\n"
BL"v225,s[8:11],s43 offen offset:1024 sc1 lds\n"
MF"v[8:11],a[72:75],a[24:27],v[8:11]\n"
RD"a[88:91],v226 offset:6144\n"
"v_accvgpr_write_b32 a60,v200\n"
"v_accvgpr_write_b32 a61,v201\n"
MF"v[16:19],a[72:75],a[40:43],v[16:19]\n"
"v_accvgpr_write_b32 a62,v202\n"
"v_accvgpr_write_b32 a63,v203\n"
MF"v[24:27],a[72:75],a[56:59],v[24:27]\n"
MF"v[0:3],a[76:79],a[12:15],v[0:3]\n"
MF"v[8:11],a[76:79],a[28:31],v[8:11]\n"
RD"a[92:95],v226 offset:7168\n"
MF"v[16:19],a[76:79],a[44:47],v[16:19]\n"
BL"v225,s[8:11],s43 offen offset:2048 sc1 lds\n"
MF"v[24:27],a[76:79],a[60:63],v[24:27]\n"
MF"v[4:7],a[80:83],a[0:3],v[212:215]\n"
MF"v[12:15],a[80:83],a[16:19],0\n"
RD"a[96:99],v226 offset:8192\n"
MF"v[20:23],a[80:83],a[32:35],0\n"
EX"v0,v0\n"
MF"v[28:31],a[80:83],a[48:51],0\n"
EX"v1,v1\n"
MF"v[4:7],a[84:87],a[4:7],v[4:7]\n"
EX"v2,v2\n"
MF"v[12:15],a[84:87],a[20:23],v[12:15]\n"
BL"v225,s[8:11],s43 offen offset:3072 sc1 lds\n"
MF"v[20:23],a[84:87],a[36:39],v[20:23]\n"
SA"m0,s40,16384\n"
EX"v3,v3\n"
PK"v0,v0,v1\n"
MF"v[28:31],a[84:87],a[52:55],v[28:31]\n"
WT"lgkmcnt(1)\n"
RD"a[100:103],v226 offset:9216\n"
PK"v1,v2,v3\n"
MF"v[4:7],a[88:91],a[8:11],v[4:7]\n"
EX"v8,v8\n"
MF"v[12:15],a[88:91],a[24:27],v[12:15]\n"
EX"v9,v9\n"
MF"v[20:23],a[88:91],a[40:43],v[20:23]\n"
EX"v10,v10\n"
MF"v[28:31],a[88:91],a[56:59],v[28:31]\n"
SA"s40,s40,32768\n"
RD"a[104:107],v226 offset:10240\n"
PK"v8,v8,v9\n"
MF"v[4:7],a[92:95],a[12:15],v[4:7]\n"
"s_cmp_eq_u32 s40,s46\n"
BL"v225,s[8:11],s44 offen offset:0 sc1 lds\n"
EX"v11,v11\n"
MF"v[12:15],a[92:95],a[28:31],v[12:15]\n"
"s_cselect_b32 s40,s47,s40\n"
EX"v16,v16\n"
MF"v[20:23],a[92:95],a[44:47],v[20:23]\n"
EX"v17,v17\n"
PK"v9,v10,v11\n"
MF"v[28:31],a[92:95],a[60:63],v[28:31]\n"
WT"lgkmcnt(1)\n"
RD"a[108:111],v226 offset:11264\n"
PK"v16,v16,v17\n"
MF"v[32:35],a[96:99],a[0:3],v[216:219]\n"
EX"v18,v18\n"
MF"v[40:43],a[96:99],a[16:19],0\n"
EX"v19,v19\n"
MF"v[48:51],a[96:99],a[32:35],0\n"
BL"v225,s[8:11],s44 offen offset:1024 sc1 lds\n"
EX"v24,v24\n"
MF"v[56:59],a[96:99],a[48:51],0\n"
RD"a[112:115],v226 offset:12288\n"
PK"v17,v18,v19\n"
MF"v[32:35],a[100:103],a[4:7],v[32:35]\n"
EX"v25,v25\n"
MF"v[40:43],a[100:103],a[20:23],v[40:43]\n"
EX"v26,v26\n"
MF"v[48:51],a[100:103],a[36:39],v[48:51]\n"
EX"v27,v27\n"
PK"v24,v24,v25\n"
MF"v[56:59],a[100:103],a[52:55],v[56:59]\n"
WT"lgkmcnt(1)\n"
RD"a[116:119],v226 offset:13312\n"
PK"v25,v26,v27\n"
MF"v[32:35],a[104:107],a[8:11],v[32:35]\n"
BL"v225,s[8:11],s44 offen offset:2048 sc1 lds\n"
EX"v4,v4\n"
MF"v[40:43],a[104:107],a[24:27],v[40:43]\n"
EX"v5,v5\n"
MF"v[48:51],a[104:107],a[40:43],v[48:51]\n"
EX"v6,v6\n"
MF"v[56:59],a[104:107],a[56:59],v[56:59]\n"
RD"a[120:123],v226 offset:14336\n"
"v_readlane_b32 s43,v228,s42\n"
PK"v2,v4,v5\n"
MF"v[32:35],a[108:111],a[12:15],v[32:35]\n"
EX"v7,v7\n"
MF"v[40:43],a[108:111],a[28:31],v[40:43]\n"
EX"v12,v12\n"
MF"v[48:51],a[108:111],a[44:47],v[48:51]\n"
BL"v225,s[8:11],s44 offen offset:3072 sc1 lds\n"
EX"v13,v13\n"
MF"v[56:59],a[108:111],a[60:63],v[56:59]\n"
WT"lgkmcnt(1)\n"
RD"a[124:127],v226 offset:15360\n"
PK"v3,v6,v7\n"
MF"v[36:39],a[112:115],a[0:3],v[220:223]\n"
"s_mov_b32 m0,s40\n"
EX"v14,v14\n"
PK"v10,v12,v13\n"
MF"v[44:47],a[112:115],a[16:19],0\n"
EX"v15,v15\n"
MF"v[52:55],a[112:115],a[32:35],0\n"
EX"v20,v20\n"
MF"v[60:63],a[112:115],a[48:51],0\n"
RD"a[96:99],v227 offset:24576\n"
"v_readlane_b32 s44,v229,s42\n"
PK"v11,v14,v15\n"
MF"v[36:39],a[116:119],a[4:7],v[36:39]\n"
SA"s42,s42,1\n"
BL"v225,s[8:11],s43 offen offset:0 sc1 lds\n"
MF"v[44:47],a[116:119],a[20:23],v[44:47]\n"
EX"v21,v21\n"
MF"v[52:55],a[116:119],a[36:39],v[52:55]\n"
EX"v22,v22\n"
MF"v[60:63],a[116:119],a[52:55],v[60:63]\n"
WT"lgkmcnt(1)\n"
RD"a[100:103],v227 offset:25600\n"
PK"v18,v20,v21\n"
MF"v[36:39],a[120:123],a[8:11],v[36:39]\n"
EX"v23,v23\n"
MF"v[44:47],a[120:123],a[24:27],v[44:47]\n"
BL"v225,s[8:11],s43 offen offset:1024 sc1 lds\n"
MF"v[52:55],a[120:123],a[40:43],v[52:55]\n"
EX"v28,v28\n"
PK"v19,v22,v23\n"
MF"v[60:63],a[120:123],a[56:59],v[60:63]\n"
RD"a[104:107],v227 offset:26624\n"
MF"v[36:39],a[124:127],a[12:15],v[36:39]\n"
EX"v29,v29\n"
MF"v[44:47],a[124:127],a[28:31],v[44:47]\n"
EX"v30,v30\n"
MF"v[52:55],a[124:127],a[44:47],v[52:55]\n"
EX"v31,v31\n"
PK"v26,v28,v29\n"
MF"v[60:63],a[124:127],a[60:63],v[60:63]\n"
WT"lgkmcnt(1)\n"
BL"v225,s[8:11],s43 offen offset:2048 sc1 lds\n"
RD"a[108:111],v227 offset:27648\n"
MF"v[80:83],a[96:99],v[0:3],0\n"
SA"s41,s41,32768\n"
PK"v27,v30,v31\n"
EX"v32,v32\n"
MF"v[112:115],a[96:99],v[8:11],0\n"
"s_cmp_eq_u32 s41,163840\n"
EX"v33,v33\n"
MF"v[144:147],a[96:99],v[16:19],0\n"
"s_cselect_b32 s41,0,s41\n"
EX"v34,v34\n"
"v_add_u32 v226,s41,v224\n"
MF"v[176:179],a[96:99],v[24:27],0\n"
RD"a[112:115],v227 offset:28672\n"
PK"v64,v32,v33\n"
MF"v[84:87],a[100:103],v[0:3],0\n"
EX"v35,v35\n"
MF"v[116:119],a[100:103],v[8:11],0\n"
BL"v225,s[8:11],s43 offen offset:3072 sc1 lds\n"
MF"v[148:151],a[100:103],v[16:19],0\n"
SA"m0,s40,16384\n"
EX"v40,v40\n"
PK"v65,v34,v35\n"
MF"v[180:183],a[100:103],v[24:27],0\n"
WT"lgkmcnt(1)\n"
RD"a[116:119],v227 offset:29696\n"
MF"v[88:91],a[104:107],v[0:3],0\n"
EX"v41,v41\n"
MF"v[120:123],a[104:107],v[8:11],0\n"
EX"v42,v42\n"
MF"v[152:155],a[104:107],v[16:19],0\n"
SA"s40,s40,32768\n"
EX"v43,v43\n"
PK"v68,v40,v41\n"
MF"v[184:187],a[104:107],v[24:27],0\n"
"s_cmp_eq_u32 s40,s46\n"
BL"v225,s[8:11],s44 offen offset:0 sc1 lds\n"
RD"a[120:123],v227 offset:30720\n"
MF"v[92:95],a[108:111],v[0:3],0\n"
"s_cselect_b32 s40,s47,s40\n"
EX"v48,v48\n"
PK"v69,v42,v43\n"
MF"v[124:127],a[108:111],v[8:11],0\n"
EX"v49,v49\n"
MF"v[156:159],a[108:111],v[16:19],0\n"
EX"v50,v50\n"
MF"v[188:191],a[108:111],v[24:27],0\n"
WT"lgkmcnt(1)\n"
RD"a[124:127],v227 offset:31744\n"
PK"v72,v48,v49\n"
MF"v[96:99],a[112:115],v[0:3],0\n"
EX"v51,v51\n"
MF"v[128:131],a[112:115],v[8:11],0\n"
BL"v225,s[8:11],s44 offen offset:1024 sc1 lds\n"
MF"v[160:163],a[112:115],v[16:19],0\n"
EX"v36,v36\n"
PK"v73,v50,v51\n"
MF"v[192:195],a[112:115],v[24:27],0\n"
EX"v37,v37\n"
MF"v[100:103],a[116:119],v[0:3],0\n"
EX"v38,v38\n"
MF"v[132:135],a[116:119],v[8:11],0\n"
EX"v39,v39\n"
PK"v66,v36,v37\n"
MF"v[164:167],a[116:119],v[16:19],0\n"
WT"vmcnt(17) lgkmcnt(0)\n"
"s_barrier\n"
MF"v[196:199],a[116:119],v[24:27],0\n"
BL"v225,s[8:11],s44 offen offset:2048 sc1 lds\n"
RD"a[64:67],v226 offset:0\n"
MF"v[104:107],a[120:123],v[0:3],0\n"
"buffer_store_dwordx2 v[232:233],v230,s[8:11],s98 offen sc1\n"
EX"v44,v44\n"
MF"v[136:139],a[120:123],v[8:11],0\n"
EX"v45,v45\n"
PK"v67,v38,v39\n"
MF"v[168:171],a[120:123],v[16:19],0\n"
EX"v46,v46\n"
MF"v[200:203],a[120:123],v[24:27],0\n"
RD"a[68:71],v226 offset:1024\n"
RD"a[72:75],v226 offset:2048\n"
PK"v70,v44,v45\n"
MF"v[108:111],a[124:127],v[0:3],0\n"
EX"v47,v47\n"
MF"v[140:143],a[124:127],v[8:11],0\n"
BL"v225,s[8:11],s44 offen offset:3072 sc1 lds\n"
MF"v[172:175],a[124:127],v[16:19],0\n"
EX"v52,v52\n"
PK"v71,v46,v47\n"
MF"v[204:207],a[124:127],v[24:27],0\n"
RD"a[76:79],v226 offset:3072\n"
MF"a[132:135],a[128:131],v[0:3],0\n"
EX"v53,v53\n"
MF"a[136:139],a[128:131],v[8:11],0\n"
EX"v54,v54\n"
MF"a[140:143],a[128:131],v[16:19],0\n"
EX"v55,v55\n"
PK"v74,v52,v53\n"
MF"a[144:147],a[128:131],v[24:27],0\n"
PK"v75,v54,v55\n"
RD"a[80:83],v226 offset:4096\n"
RD"a[84:87],v226 offset:5120\n"
"s_branch LDB_%=\n"
"LD2_%=:\n"
WT"lgkmcnt(5)\n"
MF"v[0:3],a[64:67],a[32:35],v[208:211]\n"
"s_mov_b32 m0,s40\n"
EX"v56,v56\n"
MF"v[8:11],a[64:67],a[48:51],0\n"
WT"lgkmcnt(3)\n"
EX"v57,v57\n"
MF"v[0:3],a[68:71],a[36:39],v[0:3]\n"
RD"a[88:91],v226 offset:6144\n"
"v_readlane_b32 s43,v228,s42\n"
MF"v[8:11],a[68:71],a[52:55],v[8:11]\n"
EX"v58,v58\n"
MF"v[0:3],a[72:75],a[40:43],v[0:3]\n"
SA"s41,s41,32768\n"
RD"a[92:95],v226 offset:7168\n"
RD"a[96:99],v226 offset:8192\n"
"v_readlane_b32 s44,v229,s42\n"
MF"v[8:11],a[72:75],a[56:59],v[8:11]\n"
WT"lgkmcnt(5)\n"
BL"v225,s[8:11],s43 offen offset:0 sc1 lds\n"
PK"v76,v56,v57\n"
MF"v[0:3],a[76:79],a[44:47],v[0:3]\n"
SA"s42,s42,1\n"
EX"v59,v59\n"
MF"v[8:11],a[76:79],a[60:63],v[8:11]\n"
WT"lgkmcnt(3)\n"
RD"a[100:103],v226 offset:9216\n"
PK"v77,v58,v59\n"
MF"v[4:7],a[80:83],a[32:35],v[212:215]\n"
"s_cmp_eq_u32 s41,163840\n"
EX"v60,v60\n"
MF"v[12:15],a[80:83],a[48:51],0\n"
"s_cselect_b32 s41,0,s41\n"
EX"v61,v61\n"
"v_add_u32 v227,s41,v224\n"
MF"v[4:7],a[84:87],a[36:39],v[4:7]\n"
BL"v225,s[8:11],s43 offen offset:1024 sc1 lds\n"
MF"v[12:15],a[84:87],a[52:55],v[12:15]\n"
WT"lgkmcnt(1)\n"
RD"a[104:107],v226 offset:10240\n"
PK"v78,v60,v61\n"
MF"v[4:7],a[88:91],a[40:43],v[4:7]\n"
EX"v62,v62\n"
MF"v[12:15],a[88:91],a[56:59],v[12:15]\n"
RD"a[108:111],v226 offset:11264\n"
RD"a[112:115],v226 offset:12288\n"
MF"v[4:7],a[92:95],a[44:47],v[4:7]\n"
EX"v63,v63\n"
MF"v[12:15],a[92:95],a[60:63],v[12:15]\n"
BL"v225,s[8:11],s43 offen offset:2048 sc1 lds\n"
RD"a[116:119],v226 offset:13312\n"
MF"v[16:19],a[96:99],a[32:35],v[216:219]\n"
EX"v0,v0\n"
PK"v79,v62,v63\n"
MF"v[24:27],a[96:99],a[48:51],0\n"
WT"lgkmcnt(4)\n"
EX"v1,v1\n"
MF"v[16:19],a[100:103],a[36:39],v[16:19]\n"
RD"a[120:123],v226 offset:14336\n"
RD"a[124:127],v226 offset:15360\n"
MF"v[24:27],a[100:103],a[52:55],v[24:27]\n"
WT"lgkmcnt(5)\n"
EX"v2,v2\n"
PK"v0,v0,v1\n"
MF"v[16:19],a[104:107],a[40:43],v[16:19]\n"
BL"v225,s[8:11],s43 offen offset:3072 sc1 lds\n"
RD"a[64:67],v226 offset:16384\n"
MF"v[24:27],a[104:107],a[56:59],v[24:27]\n"
WT"lgkmcnt(4)\n"
RD"a[68:71],v226 offset:17408\n"
EX"v3,v3\n"
MF"v[16:19],a[108:111],a[44:47],v[16:19]\n"
SA"m0,s40,16384\n"
RD"a[72:75],v226 offset:18432\n"
RD"a[76:79],v226 offset:19456\n"
MF"v[24:27],a[108:111],a[60:63],v[24:27]\n"
EX"v8,v8\n"
PK"v1,v2,v3\n"
MF"v[20:23],a[112:115],a[32:35],v[220:223]\n"
SA"s40,s40,32768\n"
RD"a[80:83],v226 offset:20480\n"
RD"a[84:87],v226 offset:21504\n"
EX"v9,v9\n"
MF"v[28:31],a[112:115],a[48:51],0\n"
WT"lgkmcnt(6)\n"
BL"v225,s[8:11],s44 offen offset:0 sc1 lds\n"
MF"v[20:23],a[116:119],a[36:39],v[20:23]\n"
"s_cmp_eq_u32 s40,s46\n"
EX"v10,v10\n"
PK"v8,v8,v9\n"
MF"v[28:31],a[116:119],a[52:55],v[28:31]\n"
"s_cselect_b32 s40,s47,s40\n"
RD"a[88:91],v226 offset:22528\n"
RD"a[92:95],v226 offset:23552\n"
MF"v[20:23],a[120:123],a[40:43],v[20:23]\n"
EX"v11,v11\n"
MF"v[28:31],a[120:123],a[56:59],v[28:31]\n"
RD"a[96:99],v226 offset:24576\n"
RD"a[100:103],v226 offset:25600\n"
MF"v[20:23],a[124:127],a[44:47],v[20:23]\n"
BL"v225,s[8:11],s44 offen offset:1024 sc1 lds\n"
RD"a[104:107],v226 offset:26624\n"
MF"v[28:31],a[124:127],a[60:63],v[28:31]\n"
WT"lgkmcnt(5)\n"
EX"v4,v4\n"
PK"v9,v10,v11\n"
MF"v[40:43],a[64:67],a[48:51],v[208:211]\n"
RD"a[108:111],v226 offset:27648\n"
RD"a[112:115],v226 offset:28672\n"
EX"v5,v5\n"
MF"v[40:43],a[68:71],a[52:55],v[40:43]\n"
EX"v6,v6\n"
MF"v[40:43],a[72:75],a[56:59],v[40:43]\n"
RD"a[116:119],v226 offset:29696\n"
RD"a[120:123],v226 offset:30720\n"
PK"v2,v4,v5\n"
MF"v[40:43],a[76:79],a[60:63],v[40:43]\n"
BL"v225,s[8:11],s44 offen offset:2048 sc1 lds\n"
EX"v7,v7\n"
MF"v[44:47],a[80:83],a[48:51],v[212:215]\n"
RD"a[124:127],v226 offset:31744\n"
"v_readlane_b32 s43,v228,s42\n"
MF"v[44:47],a[84:87],a[52:55],v[44:47]\n"
WT"lgkmcnt(5)\n"
EX"v12,v12\n"
PK"v3,v6,v7\n"
MF"v[44:47],a[88:91],a[56:59],v[44:47]\n"
"s_barrier\n"
MF"v[44:47],a[92:95],a[60:63],v[44:47]\n"
EX"v13,v13\n"
MF"v[56:59],a[96:99],a[48:51],v[216:219]\n"
RD"a[96:99],v227 offset:8192\n"
PK"v10,v12,v13\n"
EX"v14,v14\n"
MF"v[56:59],a[100:103],a[52:55],v[56:59]\n"
BL"v225,s[8:11],s44 offen offset:3072 sc1 lds\n"
MF"v[56:59],a[104:107],a[56:59],v[56:59]\n"
WT"lgkmcnt(2)\n"
EX"v15,v15\n"
MF"v[56:59],a[108:111],a[60:63],v[56:59]\n"
"s_mov_b32 m0,s40\n"
RD"a[100:103],v227 offset:9216\n"
RD"a[104:107],v227 offset:10240\n"
MF"v[60:63],a[112:115],a[48:51],v[220:223]\n"
EX"v16,v16\n"
PK"v11,v14,v15\n"
MF"v[60:63],a[116:119],a[52:55],v[60:63]\n"
RD"a[108:111],v227 offset:11264\n"
"v_readlane_b32 s44,v229,s42\n"
MF"v[60:63],a[120:123],a[56:59],v[60:63]\n"
WT"lgkmcnt(4)\n"
EX"v17,v17\n"
MF"v[60:63],a[124:127],a[60:63],v[60:63]\n"
WT"lgkmcnt(3)\n"
BL"v225,s[8:11],s43 offen offset:0 sc1 lds\n"
EX"v18,v18\n"
MF"v[112:115],a[96:99],v[68:71],v[112:115]\n"
SA"s42,s42,1\n"
EX"v19,v19\n"
PK"v16,v16,v17\n"
MF"v[144:147],a[96:99],v[72:75],v[144:147]\n"
SA"s41,s41,32768\n"
EX"v24,v24\n"
MF"v[176:179],a[96:99],v[76:79],v[176:179]\n"
WT"lgkmcnt(1)\n"
BL"v225,s[8:11],s43 offen offset:1024 sc1 lds\n"
MF"v[116:119],a[100:103],v[68:71],v[116:119]\n"
"s_cmp_eq_u32 s41,163840\n"
RD"a[112:115],v227 offset:12288\n"
PK"v17,v18,v19\n"
MF"v[148:151],a[100:103],v[72:75],v[148:151]\n"
"s_cselect_b32 s41,0,s41\n"
EX"v25,v25\n"
"v_add_u32 v226,s41,v224\n"
MF"v[180:183],a[100:103],v[76:79],v[180:183]\n"
RD"a[116:119],v227 offset:13312\n"
RD"a[120:123],v227 offset:14336\n"
MF"v[120:123],a[104:107],v[68:71],v[120:123]\n"
EX"v26,v26\n"
MF"v[152:155],a[104:107],v[72:75],v[152:155]\n"
EX"v27,v27\n"
PK"v24,v24,v25\n"
MF"v[184:187],a[104:107],v[76:79],v[184:187]\n"
WT"lgkmcnt(3)\n"
BL"v225,s[8:11],s43 offen offset:2048 sc1 lds\n"
MF"v[124:127],a[108:111],v[68:71],v[124:127]\n"
EX"v20,v20\n"
MF"v[156:159],a[108:111],v[72:75],v[156:159]\n"
RD"a[124:127],v227 offset:15360\n"
PK"v25,v26,v27\n"
MF"v[188:191],a[108:111],v[76:79],v[188:191]\n"
WT"lgkmcnt(3)\n"
EX"v21,v21\n"
MF"v[128:131],a[112:115],v[68:71],v[128:131]\n"
BL"v225,s[8:11],s43 offen offset:3072 sc1 lds\n"
MF"v[160:163],a[112:115],v[72:75],v[160:163]\n"
SA"m0,s40,16384\n"
EX"v22,v22\n"
MF"v[192:195],a[112:115],v[76:79],v[192:195]\n"
WT"lgkmcnt(1)\n"
EX"v23,v23\n"
PK"v18,v20,v21\n"
MF"v[132:135],a[116:119],v[68:71],v[132:135]\n"
RD"a[64:67],v227 offset:16384\n"
PK"v19,v22,v23\n"
MF"v[164:167],a[116:119],v[72:75],v[164:167]\n"
EX"v28,v28\n"
MF"v[196:199],a[116:119],v[76:79],v[196:199]\n"
SA"s40,s40,32768\n"
BL"v225,s[8:11],s44 offen offset:0 sc1 lds\n"
MF"v[136:139],a[120:123],v[68:71],v[136:139]\n"
"s_cmp_eq_u32 s40,s46\n"
EX"v29,v29\n"
MF"v[168:171],a[120:123],v[72:75],v[168:171]\n"
"s_cselect_b32 s40,s47,s40\n"
RD"a[68:71],v227 offset:17408\n"
RD"a[72:75],v227 offset:18432\n"
MF"v[200:203],a[120:123],v[76:79],v[200:203]\n"
WT"lgkmcnt(3)\n"
EX"v30,v30\n"
MF"v[140:143],a[124:127],v[68:71],v[140:143]\n"
RD"a[76:79],v227 offset:19456\n"
RD"a[80:83],v227 offset:20480\n"
PK"v26,v28,v29\n"
MF"v[172:175],a[124:127],v[72:75],v[172:175]\n"
EX"v31,v31\n"
MF"v[204:207],a[124:127],v[76:79],v[204:207]\n"
BL"v225,s[8:11],s44 offen offset:1024 sc1 lds\n"
MF"a[136:139],a[128:131],v[68:71],a[136:139]\n"
EX"v40,v40\n"
PK"v27,v30,v31\n"
MF"a[140:143],a[128:131],v[72:75],a[140:143]\n"
EX"v41,v41\n"
MF"a[144:147],a[128:131],v[76:79],a[144:147]\n"
WT"lgkmcnt(2)\n"
RD"a[84:87],v227 offset:21504\n"
PK"v68,v40,v41\n"
MF"v[144:147],a[64:67],v[0:3],v[144:147]\n"
BL"v225,s[8:11],s44 offen offset:2048 sc1 lds\n"
MF"v[176:179],a[64:67],v[8:11],v[176:179]\n"
EX"v42,v42\n"
MF"v[148:151],a[68:71],v[0:3],v[148:151]\n"
SA"s51,s59,16384\n"
RD"a[88:91],v227 offset:22528\n"
RD"a[92:95],v227 offset:23552\n"
MF"v[180:183],a[68:71],v[8:11],v[180:183]\n"
EX"v43,v43\n"
MF"v[152:155],a[72:75],v[0:3],v[152:155]\n"
EX"v44,v44\n"
MF"v[184:187],a[72:75],v[8:11],v[184:187]\n"
WT"lgkmcnt(3)\n"
BL"v225,s[8:11],s44 offen offset:3072 sc1 lds\n"
"v_accvgpr_read_b32 v252,a136\n"
MF"v[156:159],a[76:79],v[0:3],v[156:159]\n"
"s_mov_b32 m0,s40\n"
EX"v45,v45\n"
"v_subrev_u32 v253,0x2b800000,v252\n"
MF"v[188:191],a[76:79],v[8:11],v[188:191]\n"
"v_cmp_le_u32 vcc,0x34000000,v253\n"
"s_cmp_lg_u64 vcc,0\n"
"s_cbranch_scc1 L1035_%=\n"
"L1034_%=:\n"
MF"v[160:163],a[80:83],v[0:3],v[160:163]\n"
"v_rcp_f32 v252,v252\n"
EX"v46,v46\n"
MF"v[192:195],a[80:83],v[8:11],v[192:195]\n"
WT"lgkmcnt(0)\n"
RD"a[96:99],v227 offset:24576\n"
RD"a[100:103],v227 offset:25600\n"
"v_readlane_b32 s43,v228,s42\n"
MF"v[164:167],a[84:87],v[0:3],v[164:167]\n"
EX"v47,v47\n"
PK"v69,v42,v43\n"
MF"v[196:199],a[84:87],v[8:11],v[196:199]\n"
RD"a[104:107],v227 offset:26624\n"
RD"a[108:111],v227 offset:27648\n"
EX"v56,v56\n"
MF"v[168:171],a[88:91],v[0:3],v[168:171]\n"
EX"v57,v57\n"
"v_readlane_b32 s44,v229,s42\n"
MF"v[200:203],a[88:91],v[8:11],v[200:203]\n"
SA"s42,s42,1\n"
EX"v58,v58\n"
PK"v70,v44,v45\n"
MF"v[172:175],a[92:95],v[0:3],v[172:175]\n"
RD"a[112:115],v227 offset:28672\n"
MU"v112,v252,v112\n"
MU"v113,v252,v113\n"
MF"v[204:207],a[92:95],v[8:11],v[204:207]\n"
EX"v59,v59\n"
MU"v114,v252,v114\n"
MF"a[140:143],a[128:131],v[0:3],a[140:143]\n"
EX"v60,v60\n"
MU"v115,v252,v115\n"
MF"a[144:147],a[128:131],v[8:11],a[144:147]\n"
WT"lgkmcnt(3)\n"
RD"a[116:119],v227 offset:29696\n"
RD"a[120:123],v227 offset:30720\n"
MU"v116,v252,v116\n"
MF"v[144:147],a[96:99],v[16:19],v[144:147]\n"
EX"v61,v61\n"
MU"v117,v252,v117\n"
MF"v[176:179],a[96:99],v[24:27],v[176:179]\n"
RD"a[124:127],v227 offset:31744\n"
MU"v118,v252,v118\n"
MU"v119,v252,v119\n"
MF"v[148:151],a[100:103],v[16:19],v[148:151]\n"
EX"v62,v62\n"
PK"v71,v46,v47\n"
MF"v[180:183],a[100:103],v[24:27],v[180:183]\n"
WT"lgkmcnt(3)\n"
WT"vmcnt(20)\n"
"s_barrier\n"
MF"v[152:155],a[104:107],v[16:19],v[152:155]\n"
SA"s41,s41,32768\n"
BL"v225,s[8:11],s43 offen offset:0 sc1 lds\n"
BL"v225,s[8:11],s43 offen offset:1024 sc1 lds\n"
MF"v[184:187],a[104:107],v[24:27],v[184:187]\n"
"s_cmp_eq_u32 s41,163840\n"
BL"v225,s[8:11],s43 offen offset:2048 sc1 lds\n"
PK"v112,v112,v113\n"
MF"v[156:159],a[108:111],v[16:19],v[156:159]\n"
"s_cselect_b32 s41,0,s41\n"
EX"v63,v63\n"
PK"v113,v114,v115\n"
MF"v[188:191],a[108:111],v[24:27],v[188:191]\n"
RD"a[64:67],v226 offset:0\n"
RD"a[68:71],v226 offset:1024\n"
"v_add_u32 v227,s41,v224\n"
MF"v[160:163],a[112:115],v[16:19],v[160:163]\n"
RD"a[72:75],v226 offset:2048\n"
PK"v114,v116,v117\n"
PK"v115,v118,v119\n"
MF"v[192:195],a[112:115],v[24:27],v[192:195]\n"
WT"lgkmcnt(3)\n"
BS"v[112:115],v224,s[8:11],s51 offen offset:0 sc1\n"
BL"v225,s[8:11],s43 offen offset:3072 sc1 lds\n"
MF"v[164:167],a[116:119],v[16:19],v[164:167]\n"
SA"m0,s40,16384\n"
RD"a[76:79],v226 offset:3072\n"
MU"v120,v252,v120\n"
MU"v121,v252,v121\n"
MF"v[196:199],a[116:119],v[24:27],v[196:199]\n"
RD"a[80:83],v226 offset:4096\n"
PK"v76,v56,v57\n"
MU"v122,v252,v122\n"
MF"v[168:171],a[120:123],v[16:19],v[168:171]\n"
RD"a[84:87],v226 offset:5120\n"
MU"v123,v252,v123\n"
MU"v124,v252,v124\n"
MF"v[200:203],a[120:123],v[24:27],v[200:203]\n"
SA"s40,s40,32768\n"
RD"a[88:91],v226 offset:6144\n"
MU"v125,v252,v125\n"
MU"v126,v252,v126\n"
MF"v[172:175],a[124:127],v[16:19],v[172:175]\n"
"s_cmp_eq_u32 s40,s46\n"
BL"v225,s[8:11],s44 offen offset:0 sc1 lds\n"
RD"a[92:95],v226 offset:7168\n"
MF"v[204:207],a[124:127],v[24:27],v[204:207]\n"
"s_cselect_b32 s40,s47,s40\n"
RD"a[96:99],v226 offset:8192\n"
MU"v127,v252,v127\n"
PK"v120,v120,v121\n"
MF"a[140:143],a[128:131],v[16:19],a[140:143]\n"
RD"a[100:103],v226 offset:9216\n"
PK"v121,v122,v123\n"
PK"v77,v58,v59\n"
MF"a[144:147],a[128:131],v[24:27],a[144:147]\n"
WT"lgkmcnt(6)\n"
RD"a[104:107],v226 offset:10240\n"
PK"v122,v124,v125\n"
PK"v123,v126,v127\n"
MF"v[176:179],a[64:67],v[68:71],v[176:179]\n"
BS"v[120:123],v224,s[8:11],s51 offen offset:1024 sc1\n"
RD"a[108:111],v226 offset:11264\n"
MF"v[180:183],a[68:71],v[68:71],v[180:183]\n"
BL"v225,s[8:11],s44 offen offset:1024 sc1 lds\n"
RD"a[112:115],v226 offset:12288\n"
MF"v[184:187],a[72:75],v[68:71],v[184:187]\n"
RD"a[116:119],v226 offset:13312\n"
MU"v128,v252,v128\n"
MU"v129,v252,v129\n"
MF"v[188:191],a[76:79],v[68:71],v[188:191]\n"
WT"lgkmcnt(6)\n"
RD"a[120:123],v226 offset:14336\n"
MU"v130,v252,v130\n"
MU"v131,v252,v131\n"
MF"v[192:195],a[80:83],v[68:71],v[192:195]\n"
RD"a[124:127],v226 offset:15360\n"
PK"v78,v60,v61\n"
MU"v132,v252,v132\n"
MF"v[196:199],a[84:87],v[68:71],v[196:199]\n"
BL"v225,s[8:11],s44 offen offset:2048 sc1 lds\n"
MU"v133,v252,v133\n"
MF"v[200:203],a[88:91],v[68:71],v[200:203]\n"
PK"v79,v62,v63\n"
MU"v134,v252,v134\n"
MU"v135,v252,v135\n"
MF"v[204:207],a[92:95],v[68:71],v[204:207]\n"
WT"vmcnt(17)\n"
"s_barrier\n"
MF"a[144:147],a[128:131],v[68:71],a[144:147]\n"
WT"lgkmcnt(3)\n"
RD"a[64:67],v227 offset:0\n"
PK"v128,v128,v129\n"
PK"v129,v130,v131\n"
MF"v[176:179],a[96:99],v[76:79],v[176:179]\n"
RD"a[68:71],v227 offset:1024\n"
PK"v130,v132,v133\n"
PK"v131,v134,v135\n"
MF"v[180:183],a[100:103],v[76:79],v[180:183]\n"
BS"v[128:131],v224,s[8:11],s51 offen offset:2048 sc1\n"
MU"v136,v252,v136\n"
MF"v[184:187],a[104:107],v[76:79],v[184:187]\n"
BL"v225,s[8:11],s44 offen offset:3072 sc1 lds\n"
MU"v137,v252,v137\n"
MF"v[188:191],a[108:111],v[76:79],v[188:191]\n"
RD"a[72:75],v227 offset:2048\n"
MU"v138,v252,v138\n"
MU"v139,v252,v139\n"
MF"v[192:195],a[112:115],v[76:79],v[192:195]\n"
WT"lgkmcnt(3)\n"
RD"a[76:79],v227 offset:3072\n"
MU"v140,v252,v140\n"
MU"v141,v252,v141\n"
MF"v[196:199],a[116:119],v[76:79],v[196:199]\n"
RD"a[80:83],v227 offset:4096\n"
MU"v142,v252,v142\n"
MU"v143,v252,v143\n"
MF"v[200:203],a[120:123],v[76:79],v[200:203]\n"
PK"v136,v136,v137\n"
PK"v137,v138,v139\n"
PK"v138,v140,v141\n"
MF"v[204:207],a[124:127],v[76:79],v[204:207]\n"
PK"v139,v142,v143\n"
MF"a[144:147],a[128:131],v[76:79],a[144:147]\n"
BS"v[136:139],v224,s[8:11],s51 offen offset:3072 sc1\n"
RD"a[84:87],v227 offset:5120\n"
"s_branch LTL_%=\n"
"LEV_%=:\n"
WT"lgkmcnt(5)\n"
MF"v[0:3],a[64:67],a[0:3],0\n"
SA"s95,s40,32768\n"
EX"v56,v56\n"
MF"v[8:11],a[64:67],a[16:19],0\n"
"s_cmp_eq_u32 s95,s46\n"
"s_cselect_b32 s95,s47,s95\n"
MF"v[16:19],a[64:67],a[32:35],0\n"
EX"v57,v57\n"
MF"v[24:27],a[64:67],a[48:51],0\n"
WT"lgkmcnt(4)\n"
PK"v76,v56,v57\n"
MF"v[0:3],a[68:71],a[4:7],v[0:3]\n"
SA"m0,s95,0\n"
EX"v58,v58\n"
MF"v[8:11],a[68:71],a[20:23],v[8:11]\n"
"v_mov_b32 v227,v226\n"
MF"v[16:19],a[68:71],a[36:39],v[16:19]\n"
EX"v59,v59\n"
MF"v[24:27],a[68:71],a[52:55],v[24:27]\n"
WT"lgkmcnt(0)\n"
BL"v235,s[24:27],s52 offen lds\n"
MF"v[0:3],a[72:75],a[8:11],v[0:3]\n"
SA"s52,s52,s62\n"
EX"v60,v60\n"
MF"v[8:11],a[72:75],a[24:27],v[8:11]\n"
RD"a[88:91],v226 offset:6144\n"
PK"v77,v58,v59\n"
MF"v[16:19],a[72:75],a[40:43],v[16:19]\n"
EX"v61,v61\n"
MF"v[24:27],a[72:75],a[56:59],v[24:27]\n"
PK"v78,v60,v61\n"
MF"v[0:3],a[76:79],a[12:15],v[0:3]\n"
SA"m0,s95,1024\n"
EX"v62,v62\n"
MF"v[8:11],a[76:79],a[28:31],v[8:11]\n"
RD"a[92:95],v226 offset:7168\n"
MF"v[16:19],a[76:79],a[44:47],v[16:19]\n"
EX"v63,v63\n"
MF"v[24:27],a[76:79],a[60:63],v[24:27]\n"
BL"v235,s[24:27],s52 offen lds\n"
MF"v[4:7],a[80:83],a[0:3],0\n"
SA"s52,s52,s62\n"
EX"v0,v0\n"
MF"v[12:15],a[80:83],a[16:19],0\n"
RD"a[96:99],v226 offset:8192\n"
PK"v79,v62,v63\n"
MF"v[20:23],a[80:83],a[32:35],0\n"
EX"v1,v1\n"
MF"v[28:31],a[80:83],a[48:51],0\n"
PK"v0,v0,v1\n"
MF"v[4:7],a[84:87],a[4:7],v[4:7]\n"
SA"m0,s95,2048\n"
EX"v2,v2\n"
MF"v[12:15],a[84:87],a[20:23],v[12:15]\n"
RD"a[100:103],v226 offset:9216\n"
MF"v[20:23],a[84:87],a[36:39],v[20:23]\n"
EX"v3,v3\n"
MF"v[28:31],a[84:87],a[52:55],v[28:31]\n"
WT"lgkmcnt(2)\n"
BL"v235,s[24:27],s52 offen lds\n"
MF"v[4:7],a[88:91],a[8:11],v[4:7]\n"
SA"s52,s52,s62\n"
EX"v8,v8\n"
MF"v[12:15],a[88:91],a[24:27],v[12:15]\n"
RD"a[104:107],v226 offset:10240\n"
PK"v1,v2,v3\n"
MF"v[20:23],a[88:91],a[40:43],v[20:23]\n"
EX"v9,v9\n"
MF"v[28:31],a[88:91],a[56:59],v[28:31]\n"
PK"v8,v8,v9\n"
MF"v[4:7],a[92:95],a[12:15],v[4:7]\n"
SA"m0,s95,3072\n"
EX"v10,v10\n"
MF"v[12:15],a[92:95],a[28:31],v[12:15]\n"
RD"a[108:111],v226 offset:11264\n"
MF"v[20:23],a[92:95],a[44:47],v[20:23]\n"
EX"v11,v11\n"
MF"v[28:31],a[92:95],a[60:63],v[28:31]\n"
WT"lgkmcnt(2)\n"
BL"v235,s[24:27],s52 offen lds\n"
MF"v[32:35],a[96:99],a[0:3],0\n"
SA"s52,s52,s62\n"
EX"v16,v16\n"
MF"v[40:43],a[96:99],a[16:19],0\n"
RD"a[112:115],v226 offset:12288\n"
PK"v9,v10,v11\n"
MF"v[48:51],a[96:99],a[32:35],0\n"
EX"v17,v17\n"
MF"v[56:59],a[96:99],a[48:51],0\n"
PK"v16,v16,v17\n"
MF"v[32:35],a[100:103],a[4:7],v[32:35]\n"
SA"m0,s95,16384\n"
EX"v18,v18\n"
MF"v[40:43],a[100:103],a[20:23],v[40:43]\n"
RD"a[116:119],v226 offset:13312\n"
MF"v[48:51],a[100:103],a[36:39],v[48:51]\n"
EX"v19,v19\n"
MF"v[56:59],a[100:103],a[52:55],v[56:59]\n"
WT"lgkmcnt(2)\n"
BL"v235,s[24:27],s52 offen lds\n"
MF"v[32:35],a[104:107],a[8:11],v[32:35]\n"
SA"s52,s52,s62\n"
EX"v24,v24\n"
MF"v[40:43],a[104:107],a[24:27],v[40:43]\n"
RD"a[120:123],v226 offset:14336\n"
PK"v17,v18,v19\n"
MF"v[48:51],a[104:107],a[40:43],v[48:51]\n"
EX"v25,v25\n"
MF"v[56:59],a[104:107],a[56:59],v[56:59]\n"
PK"v24,v24,v25\n"
MF"v[32:35],a[108:111],a[12:15],v[32:35]\n"
SA"m0,s95,17408\n"
EX"v26,v26\n"
MF"v[40:43],a[108:111],a[28:31],v[40:43]\n"
RD"a[124:127],v226 offset:15360\n"
MF"v[48:51],a[108:111],a[44:47],v[48:51]\n"
EX"v27,v27\n"
MF"v[56:59],a[108:111],a[60:63],v[56:59]\n"
WT"lgkmcnt(2)\n"
BL"v235,s[24:27],s52 offen lds\n"
MF"v[36:39],a[112:115],a[0:3],0\n"
SA"s52,s52,s62\n"
EX"v4,v4\n"
MF"v[44:47],a[112:115],a[16:19],0\n"
RD"a[64:67],v227 offset:16384\n"
PK"v25,v26,v27\n"
MF"v[52:55],a[112:115],a[32:35],0\n"
EX"v5,v5\n"
MF"v[60:63],a[112:115],a[48:51],0\n"
PK"v2,v4,v5\n"
MF"v[36:39],a[116:119],a[4:7],v[36:39]\n"
SA"m0,s95,18432\n"
EX"v6,v6\n"
MF"v[44:47],a[116:119],a[20:23],v[44:47]\n"
RD"a[68:71],v227 offset:17408\n"
MF"v[52:55],a[116:119],a[36:39],v[52:55]\n"
EX"v7,v7\n"
MF"v[60:63],a[116:119],a[52:55],v[60:63]\n"
WT"lgkmcnt(2)\n"
BL"v235,s[24:27],s52 offen lds\n"
MF"v[36:39],a[120:123],a[8:11],v[36:39]\n"
SA"s52,s52,s62\n"
EX"v12,v12\n"
MF"v[44:47],a[120:123],a[24:27],v[44:47]\n"
RD"a[72:75],v227 offset:18432\n"
"v_readlane_b32 s43,v228,s42\n"
MF"v[52:55],a[120:123],a[40:43],v[52:55]\n"
EX"v13,v13\n"
MF"v[60:63],a[120:123],a[56:59],v[60:63]\n"
"s_mov_b32 m0,s40\n"
PK"v3,v6,v7\n"
PK"v10,v12,v13\n"
MF"v[36:39],a[124:127],a[12:15],v[36:39]\n"
EX"v14,v14\n"
MF"v[44:47],a[124:127],a[28:31],v[44:47]\n"
RD"a[76:79],v227 offset:19456\n"
"v_readlane_b32 s44,v229,s42\n"
MF"v[52:55],a[124:127],a[44:47],v[52:55]\n"
EX"v15,v15\n"
MF"v[60:63],a[124:127],a[60:63],v[60:63]\n"
WT"lgkmcnt(2)\n"
BL"v225,s[8:11],s43 offen offset:0 sc1 lds\n"
MF"v[80:83],a[64:67],v[64:67],v[80:83]\n"
SA"s42,s42,1\n"
EX"v20,v20\n"
MF"v[112:115],a[64:67],v[68:71],v[112:115]\n"
SA"s41,s41,32768\n"
RD"a[80:83],v227 offset:20480\n"
PK"v11,v14,v15\n"
MF"v[144:147],a[64:67],v[72:75],v[144:147]\n"
"s_cmp_eq_u32 s41,163840\n"
EX"v21,v21\n"
MF"v[176:179],a[64:67],v[76:79],v[176:179]\n"
"s_cselect_b32 s41,0,s41\n"
PK"v18,v20,v21\n"
"v_add_u32 v226,s41,v224\n"
MF"v[84:87],a[68:71],v[64:67],v[84:87]\n"
EX"v22,v22\n"
MF"v[116:119],a[68:71],v[68:71],v[116:119]\n"
RD"a[84:87],v227 offset:21504\n"
MF"v[148:151],a[68:71],v[72:75],v[148:151]\n"
EX"v23,v23\n"
MF"v[180:183],a[68:71],v[76:79],v[180:183]\n"
WT"lgkmcnt(2)\n"
BL"v225,s[8:11],s43 offen offset:1024 sc1 lds\n"
MF"v[88:91],a[72:75],v[64:67],v[88:91]\n"
EX"v28,v28\n"
MF"v[120:123],a[72:75],v[68:71],v[120:123]\n"
RD"a[88:91],v227 offset:22528\n"
PK"v19,v22,v23\n"
MF"v[152:155],a[72:75],v[72:75],v[152:155]\n"
EX"v29,v29\n"
MF"v[184:187],a[72:75],v[76:79],v[184:187]\n"
PK"v26,v28,v29\n"
MF"v[92:95],a[76:79],v[64:67],v[92:95]\n"
EX"v30,v30\n"
MF"v[124:127],a[76:79],v[68:71],v[124:127]\n"
RD"a[92:95],v227 offset:23552\n"
MF"v[156:159],a[76:79],v[72:75],v[156:159]\n"
EX"v31,v31\n"
MF"v[188:191],a[76:79],v[76:79],v[188:191]\n"
WT"lgkmcnt(2)\n"
BL"v225,s[8:11],s43 offen offset:2048 sc1 lds\n"
MF"v[96:99],a[80:83],v[64:67],v[96:99]\n"
EX"v32,v32\n"
MF"v[128:131],a[80:83],v[68:71],v[128:131]\n"
RD"a[96:99],v227 offset:24576\n"
PK"v27,v30,v31\n"
MF"v[160:163],a[80:83],v[72:75],v[160:163]\n"
EX"v33,v33\n"
MF"v[192:195],a[80:83],v[76:79],v[192:195]\n"
MF"v[100:103],a[84:87],v[64:67],v[100:103]\n"
EX"v34,v34\n"
MF"v[132:135],a[84:87],v[68:71],v[132:135]\n"
RD"a[100:103],v227 offset:25600\n"
MF"v[164:167],a[84:87],v[72:75],v[164:167]\n"
EX"v35,v35\n"
MF"v[196:199],a[84:87],v[76:79],v[196:199]\n"
WT"lgkmcnt(2)\n"
BL"v225,s[8:11],s43 offen offset:3072 sc1 lds\n"
MF"v[104:107],a[88:91],v[64:67],v[104:107]\n"
EX"v40,v40\n"
MF"v[136:139],a[88:91],v[68:71],v[136:139]\n"
RD"a[104:107],v227 offset:26624\n"
MF"v[168:171],a[88:91],v[72:75],v[168:171]\n"
EX"v41,v41\n"
MF"v[200:203],a[88:91],v[76:79],v[200:203]\n"
SA"m0,s40,16384\n"
MF"v[108:111],a[92:95],v[64:67],v[108:111]\n"
EX"v42,v42\n"
MF"v[140:143],a[92:95],v[68:71],v[140:143]\n"
RD"a[108:111],v227 offset:27648\n"
MF"v[172:175],a[92:95],v[72:75],v[172:175]\n"
EX"v43,v43\n"
MF"v[204:207],a[92:95],v[76:79],v[204:207]\n"
BL"v225,s[8:11],s44 offen offset:0 sc1 lds\n"
MF"a[132:135],a[128:131],v[64:67],a[132:135]\n"
SA"s40,s40,32768\n"
EX"v48,v48\n"
PK"v64,v32,v33\n"
MF"a[136:139],a[128:131],v[68:71],a[136:139]\n"
"s_cmp_eq_u32 s40,s46\n"
RD"a[112:115],v227 offset:28672\n"
PK"v65,v34,v35\n"
MF"a[140:143],a[128:131],v[72:75],a[140:143]\n"
"s_cselect_b32 s40,s47,s40\n"
EX"v49,v49\n"
MF"a[144:147],a[128:131],v[76:79],a[144:147]\n"
WT"lgkmcnt(2)\n"
BL"v[240:243],v238,s[8:11],0 offen sc1\n"
PK"v68,v40,v41\n"
MF"v[80:83],a[96:99],v[0:3],v[80:83]\n"
EX"v50,v50\n"
MF"v[112:115],a[96:99],v[8:11],v[112:115]\n"
RD"a[116:119],v227 offset:29696\n"
PK"v69,v42,v43\n"
MF"v[144:147],a[96:99],v[16:19],v[144:147]\n"
EX"v51,v51\n"
MF"v[176:179],a[96:99],v[24:27],v[176:179]\n"
BL"v225,s[8:11],s44 offen offset:1024 sc1 lds\n"
MF"v[84:87],a[100:103],v[0:3],v[84:87]\n"
EX"v36,v36\n"
MF"v[116:119],a[100:103],v[8:11],v[116:119]\n"
RD"a[120:123],v227 offset:30720\n"
PK"v72,v48,v49\n"
MF"v[148:151],a[100:103],v[16:19],v[148:151]\n"
EX"v37,v37\n"
MF"v[180:183],a[100:103],v[24:27],v[180:183]\n"
PK"v73,v50,v51\n"
PK"v66,v36,v37\n"
MF"v[88:91],a[104:107],v[0:3],v[88:91]\n"
EX"v38,v38\n"
MF"v[120:123],a[104:107],v[8:11],v[120:123]\n"
RD"a[124:127],v227 offset:31744\n"
MF"v[152:155],a[104:107],v[16:19],v[152:155]\n"
EX"v39,v39\n"
MF"v[184:187],a[104:107],v[24:27],v[184:187]\n"
WT"lgkmcnt(2)\n"
BL"v225,s[8:11],s44 offen offset:2048 sc1 lds\n"
MF"v[92:95],a[108:111],v[0:3],v[92:95]\n"
EX"v44,v44\n"
MF"v[124:127],a[108:111],v[8:11],v[124:127]\n"
PK"v67,v38,v39\n"
MF"v[156:159],a[108:111],v[16:19],v[156:159]\n"
EX"v45,v45\n"
MF"v[188:191],a[108:111],v[24:27],v[188:191]\n"
PK"v70,v44,v45\n"
MF"v[96:99],a[112:115],v[0:3],v[96:99]\n"
EX"v46,v46\n"
MF"v[128:131],a[112:115],v[8:11],v[128:131]\n"
MF"v[160:163],a[112:115],v[16:19],v[160:163]\n"
EX"v47,v47\n"
MF"v[192:195],a[112:115],v[24:27],v[192:195]\n"
BL"v225,s[8:11],s44 offen offset:3072 sc1 lds\n"
MF"v[100:103],a[116:119],v[0:3],v[100:103]\n"
WT"vmcnt(24) lgkmcnt(0)\n"
"s_barrier\n"
MF"v[132:135],a[116:119],v[8:11],v[132:135]\n"
RD"a[64:67],v226 offset:0\n"
PK"v71,v46,v47\n"
MF"v[164:167],a[116:119],v[16:19],v[164:167]\n"
EX"v52,v52\n"
MF"v[196:199],a[116:119],v[24:27],v[196:199]\n"
RD"a[68:71],v226 offset:1024\n"
MF"v[104:107],a[120:123],v[0:3],v[104:107]\n"
EX"v53,v53\n"
MF"v[136:139],a[120:123],v[8:11],v[136:139]\n"
RD"a[72:75],v226 offset:2048\n"
PK"v74,v52,v53\n"
MF"v[168:171],a[120:123],v[16:19],v[168:171]\n"
EX"v54,v54\n"
MF"v[200:203],a[120:123],v[24:27],v[200:203]\n"
MF"v[108:111],a[124:127],v[0:3],v[108:111]\n"
EX"v55,v55\n"
MF"v[140:143],a[124:127],v[8:11],v[140:143]\n"
RD"a[76:79],v226 offset:3072\n"
PK"v75,v54,v55\n"
MF"v[172:175],a[124:127],v[16:19],v[172:175]\n"
MF"v[204:207],a[124:127],v[24:27],v[204:207]\n"
RD"a[80:83],v226 offset:4096\n"
MF"a[132:135],a[128:131],v[0:3],a[132:135]\n"
MF"a[136:139],a[128:131],v[8:11],a[136:139]\n"
RD"a[84:87],v226 offset:5120\n"
MF"a[140:143],a[128:131],v[16:19],a[140:143]\n"
MF"a[144:147],a[128:131],v[24:27],a[144:147]\n"
WT"lgkmcnt(5)\n"
MF"v[0:3],a[64:67],a[0:3],0\n"
SA"s95,s40,32768\n"
EX"v56,v56\n"
MF"v[8:11],a[64:67],a[16:19],0\n"
"s_cmp_eq_u32 s95,s46\n"
"v_lshrrev_b32 v237,1,v224\n"
"v_and_b32 v237,0x78,v237\n"
MF"v[16:19],a[64:67],a[32:35],0\n"
"s_cselect_b32 s95,s47,s95\n"
EX"v57,v57\n"
MF"v[24:27],a[64:67],a[48:51],0\n"
WT"lgkmcnt(1)\n"
"v_and_b32 v239,0x300,v224\n"
"v_add3_u32 v237,v237,v239,s40\n"
MF"v[0:3],a[68:71],a[4:7],v[0:3]\n"
SA"m0,s95,0\n"
EX"v58,v58\n"
MF"v[8:11],a[68:71],a[20:23],v[8:11]\n"
PK"v76,v56,v57\n"
"v_mov_b32 v227,v226\n"
MF"v[16:19],a[68:71],a[36:39],v[16:19]\n"
EX"v59,v59\n"
MF"v[24:27],a[68:71],a[52:55],v[24:27]\n"
BL"v235,s[24:27],s52 offen lds\n"
MF"v[0:3],a[72:75],a[8:11],v[0:3]\n"
SA"s52,s52,s62\n"
EX"v60,v60\n"
MF"v[8:11],a[72:75],a[24:27],v[8:11]\n"
RD"a[88:91],v226 offset:6144\n"
PK"v77,v58,v59\n"
MF"v[16:19],a[72:75],a[40:43],v[16:19]\n"
EX"v61,v61\n"
MF"v[24:27],a[72:75],a[56:59],v[24:27]\n"
WT"vmcnt(4)\n"
"v_cmp_eq_u64 vcc,s[28:29],v[240:241]\n"
"v_cmp_eq_u64_e64 s[50:51],s[28:29],v[242:243]\n"
"s_and_b64 vcc,vcc,s[50:51]\n"
"s_cmp_eq_u64 vcc,exec\n"
"s_cbranch_scc0 L1125_%=\n"
"L1126_%=:\n"
MF"v[0:3],a[76:79],a[12:15],v[0:3]\n"
SA"m0,s95,1024\n"
EX"v62,v62\n"
MF"v[8:11],a[76:79],a[28:31],v[8:11]\n"
RD"a[92:95],v226 offset:7168\n"
PK"v78,v60,v61\n"
MF"v[16:19],a[76:79],a[44:47],v[16:19]\n"
EX"v63,v63\n"
MF"v[24:27],a[76:79],a[60:63],v[24:27]\n"
BL"v235,s[24:27],s52 offen lds\n"
MF"v[4:7],a[80:83],a[0:3],0\n"
SA"s52,s52,s62\n"
EX"v0,v0\n"
MF"v[12:15],a[80:83],a[16:19],0\n"
RD"a[96:99],v226 offset:8192\n"
PK"v79,v62,v63\n"
MF"v[20:23],a[80:83],a[32:35],0\n"
EX"v1,v1\n"
MF"v[28:31],a[80:83],a[48:51],0\n"
WT"lgkmcnt(2)\n"
PK"v0,v0,v1\n"
MF"v[4:7],a[84:87],a[4:7],v[4:7]\n"
SA"m0,s95,2048\n"
EX"v2,v2\n"
MF"v[12:15],a[84:87],a[20:23],v[12:15]\n"
RD"a[100:103],v226 offset:9216\n"
MF"v[20:23],a[84:87],a[36:39],v[20:23]\n"
EX"v3,v3\n"
MF"v[28:31],a[84:87],a[52:55],v[28:31]\n"
BL"v235,s[24:27],s52 offen lds\n"
MF"v[4:7],a[88:91],a[8:11],v[4:7]\n"
SA"s52,s52,s62\n"
EX"v8,v8\n"
MF"v[12:15],a[88:91],a[24:27],v[12:15]\n"
RD"a[104:107],v226 offset:10240\n"
PK"v1,v2,v3\n"
MF"v[20:23],a[88:91],a[40:43],v[20:23]\n"
EX"v9,v9\n"
MF"v[28:31],a[88:91],a[56:59],v[28:31]\n"
WT"lgkmcnt(2)\n"
RT"a[148:149],v237 offset:0\n"
RT"a[150:151],v237 offset:128\n"
MF"v[4:7],a[92:95],a[12:15],v[4:7]\n"
SA"m0,s95,3072\n"
EX"v10,v10\n"
MF"v[12:15],a[92:95],a[28:31],v[12:15]\n"
RD"a[108:111],v226 offset:11264\n"
PK"v8,v8,v9\n"
MF"v[20:23],a[92:95],a[44:47],v[20:23]\n"
EX"v11,v11\n"
MF"v[28:31],a[92:95],a[60:63],v[28:31]\n"
RT"a[152:153],v237 offset:1024\n"
RT"a[154:155],v237 offset:1152\n"
MF"v[32:35],a[96:99],a[0:3],0\n"
BL"v235,s[24:27],s52 offen lds\n"
MF"v[40:43],a[96:99],a[16:19],0\n"
SA"s52,s52,s62\n"
EX"v16,v16\n"
MF"v[48:51],a[96:99],a[32:35],0\n"
RD"a[112:115],v226 offset:12288\n"
PK"v9,v10,v11\n"
MF"v[56:59],a[96:99],a[48:51],0\n"
WT"lgkmcnt(4)\n"
EX"v17,v17\n"
MF"v[32:35],a[100:103],a[4:7],v[32:35]\n"
SA"m0,s95,16384\n"
RT"a[156:157],v237 offset:2048\n"
RT"a[158:159],v237 offset:2176\n"
MF"v[40:43],a[100:103],a[20:23],v[40:43]\n"
EX"v18,v18\n"
MF"v[48:51],a[100:103],a[36:39],v[48:51]\n"
RD"a[116:119],v226 offset:13312\n"
PK"v16,v16,v17\n"
MF"v[56:59],a[100:103],a[52:55],v[56:59]\n"
EX"v19,v19\n"
MF"v[32:35],a[104:107],a[8:11],v[32:35]\n"
RT"a[160:161],v237 offset:3072\n"
RT"a[162:163],v237 offset:3200\n"
MF"v[40:43],a[104:107],a[24:27],v[40:43]\n"
BL"v235,s[24:27],s52 offen lds\n"
MF"v[48:51],a[104:107],a[40:43],v[48:51]\n"
SA"s52,s52,s62\n"
EX"v24,v24\n"
MF"v[56:59],a[104:107],a[56:59],v[56:59]\n"
WT"lgkmcnt(3)\n"
RD"a[120:123],v226 offset:14336\n"
RT"a[164:165],v237 offset:16384\n"
MF"v[32:35],a[108:111],a[12:15],v[32:35]\n"
SA"m0,s95,17408\n"
EX"v25,v25\n"
MF"v[40:43],a[108:111],a[28:31],v[40:43]\n"
RT"a[166:167],v237 offset:16512\n"
RD"a[124:127],v226 offset:15360\n"
MF"v[48:51],a[108:111],a[44:47],v[48:51]\n"
EX"v26,v26\n"
MF"v[56:59],a[108:111],a[60:63],v[56:59]\n"
EX"v27,v27\n"
PK"v17,v18,v19\n"
MF"v[36:39],a[112:115],a[0:3],0\n"
RT"a[168:169],v237 offset:17408\n"
RT"a[170:171],v237 offset:17536\n"
MF"v[44:47],a[112:115],a[16:19],0\n"
BL"v235,s[24:27],s52 offen lds\n"
MF"v[52:55],a[112:115],a[32:35],0\n"
SA"s52,s52,s62\n"
EX"v4,v4\n"
MF"v[60:63],a[112:115],a[48:51],0\n"
WT"lgkmcnt(4)\n"
RD"a[64:67],v227 offset:16384\n"
RT"a[172:173],v237 offset:18432\n"
PK"v24,v24,v25\n"
MF"v[36:39],a[116:119],a[4:7],v[36:39]\n"
SA"m0,s95,18432\n"
EX"v5,v5\n"
MF"v[44:47],a[116:119],a[20:23],v[44:47]\n"
RT"a[174:175],v237 offset:18560\n"
RD"a[68:71],v227 offset:17408\n"
MF"v[52:55],a[116:119],a[36:39],v[52:55]\n"
EX"v6,v6\n"
MF"v[60:63],a[116:119],a[52:55],v[60:63]\n"
EX"v7,v7\n"
PK"v25,v26,v27\n"
MF"v[36:39],a[120:123],a[8:11],v[36:39]\n"
"s_cmp_gt_u32 s92,0\n"
BL"v235,s[24:27],s52 offen lds\n"
MF"v[44:47],a[120:123],a[24:27],v[44:47]\n"
"s_cselect_b32 s63,s63,1065353216\n"
EX"v12,v12\n"
MF"v[52:55],a[120:123],a[40:43],v[52:55]\n"
SA"s52,s52,s62\n"
RD"a[72:75],v227 offset:18432\n"
"v_readlane_b32 s43,v228,s42\n"
MF"v[60:63],a[120:123],a[56:59],v[60:63]\n"
WT"lgkmcnt(3)\n"
EX"v13,v13\n"
PK"v2,v4,v5\n"
MF"v[36:39],a[124:127],a[12:15],v[36:39]\n"
"s_mov_b32 m0,s40\n"
BL"a[204:207],v236,s[8:11],s63 offen offset:0 sc1\n"
MF"v[44:47],a[124:127],a[28:31],v[44:47]\n"
EX"v14,v14\n"
MF"v[52:55],a[124:127],a[44:47],v[52:55]\n"
RD"a[76:79],v227 offset:19456\n"
"v_readlane_b32 s44,v229,s42\n"
MF"v[60:63],a[124:127],a[60:63],v[60:63]\n"
SA"s42,s42,1\n"
EX"v15,v15\n"
PK"v3,v6,v7\n"
MF"v[80:83],a[64:67],v[64:67],v[80:83]\n"
WT"lgkmcnt(3)\n"
BL"v225,s[8:11],s43 offen offset:0 sc1 lds\n"
MF"v[112:115],a[64:67],v[68:71],v[112:115]\n"
SA"s41,s41,32768\n"
EX"v20,v20\n"
MF"v[144:147],a[64:67],v[72:75],v[144:147]\n"
"s_cmp_eq_u32 s41,163840\n"
RD"a[80:83],v227 offset:20480\n"
PK"v10,v12,v13\n"
MF"v[176:179],a[64:67],v[76:79],v[176:179]\n"
WT"lgkmcnt(2)\n"
EX"v21,v21\n"
MF"v[84:87],a[68:71],v[64:67],v[84:87]\n"
"s_cselect_b32 s41,0,s41\n"
BL"a[208:211],v236,s[8:11],s63 offen offset:1024 sc1\n"
MF"v[116:119],a[68:71],v[68:71],v[116:119]\n"
EX"v22,v22\n"
MF"v[148:151],a[68:71],v[72:75],v[148:151]\n"
RD"a[84:87],v227 offset:21504\n"
"v_add_u32 v226,s41,v224\n"
MF"v[180:183],a[68:71],v[76:79],v[180:183]\n"
EX"v23,v23\n"
PK"v11,v14,v15\n"
MF"v[88:91],a[72:75],v[64:67],v[88:91]\n"
BL"v225,s[8:11],s43 offen offset:1024 sc1 lds\n"
MF"v[120:123],a[72:75],v[68:71],v[120:123]\n"
EX"v28,v28\n"
MF"v[152:155],a[72:75],v[72:75],v[152:155]\n"
RD"a[88:91],v227 offset:22528\n"
PK"v18,v20,v21\n"
MF"v[184:187],a[72:75],v[76:79],v[184:187]\n"
WT"lgkmcnt(2)\n"
EX"v29,v29\n"
MF"v[92:95],a[76:79],v[64:67],v[92:95]\n"
BL"a[212:215],v236,s[8:11],s63 offen offset:2048 sc1\n"
MF"v[124:127],a[76:79],v[68:71],v[124:127]\n"
EX"v30,v30\n"
MF"v[156:159],a[76:79],v[72:75],v[156:159]\n"
RD"a[92:95],v227 offset:23552\n"
PK"v19,v22,v23\n"
MF"v[188:191],a[76:79],v[76:79],v[188:191]\n"
EX"v31,v31\n"
MF"v[96:99],a[80:83],v[64:67],v[96:99]\n"
BL"v225,s[8:11],s43 offen offset:2048 sc1 lds\n"
MF"v[128:131],a[80:83],v[68:71],v[128:131]\n"
EX"v32,v32\n"
MF"v[160:163],a[80:83],v[72:75],v[160:163]\n"
RD"a[96:99],v227 offset:24576\n"
PK"v26,v28,v29\n"
MF"v[192:195],a[80:83],v[76:79],v[192:195]\n"
WT"lgkmcnt(2)\n"
EX"v33,v33\n"
MF"v[100:103],a[84:87],v[64:67],v[100:103]\n"
BL"a[216:219],v236,s[8:11],s63 offen offset:3072 sc1\n"
MF"v[132:135],a[84:87],v[68:71],v[132:135]\n"
EX"v34,v34\n"
MF"v[164:167],a[84:87],v[72:75],v[164:167]\n"
RD"a[100:103],v227 offset:25600\n"
PK"v27,v30,v31\n"
MF"v[196:199],a[84:87],v[76:79],v[196:199]\n"
SA"s63,s63,4096\n"
EX"v35,v35\n"
MF"v[104:107],a[88:91],v[64:67],v[104:107]\n"
BL"v225,s[8:11],s43 offen offset:3072 sc1 lds\n"
MF"v[136:139],a[88:91],v[68:71],v[136:139]\n"
EX"v40,v40\n"
MF"v[168:171],a[88:91],v[72:75],v[168:171]\n"
RD"a[104:107],v227 offset:26624\n"
MF"v[200:203],a[88:91],v[76:79],v[200:203]\n"
WT"lgkmcnt(2)\n"
EX"v41,v41\n"
MF"v[108:111],a[92:95],v[64:67],v[108:111]\n"
SA"m0,s40,16384\n"
BL"a[220:223],v236,s[8:11],s63 offen offset:0 sc1\n"
MF"v[140:143],a[92:95],v[68:71],v[140:143]\n"
EX"v42,v42\n"
MF"v[172:175],a[92:95],v[72:75],v[172:175]\n"
RD"a[108:111],v227 offset:27648\n"
MF"v[204:207],a[92:95],v[76:79],v[204:207]\n"
EX"v43,v43\n"
MF"a[132:135],a[128:131],v[64:67],a[132:135]\n"
SA"s40,s40,32768\n"
BL"v225,s[8:11],s44 offen offset:0 sc1 lds\n"
PK"v64,v32,v33\n"
MF"a[136:139],a[128:131],v[68:71],a[136:139]\n"
"s_cmp_eq_u32 s40,s46\n"
EX"v48,v48\n"
PK"v65,v34,v35\n"
MF"a[140:143],a[128:131],v[72:75],a[140:143]\n"
"s_cselect_b32 s40,s47,s40\n"
RD"a[112:115],v227 offset:28672\n"
PK"v68,v40,v41\n"
MF"a[144:147],a[128:131],v[76:79],a[144:147]\n"
EX"v49,v49\n"
MF"v[80:83],a[96:99],v[0:3],v[80:83]\n"
BL"a[224:227],v236,s[8:11],s63 offen offset:1024 sc1\n"
MF"v[112:115],a[96:99],v[8:11],v[112:115]\n"
EX"v50,v50\n"
MF"v[144:147],a[96:99],v[16:19],v[144:147]\n"
RD"a[116:119],v227 offset:29696\n"
PK"v69,v42,v43\n"
MF"v[176:179],a[96:99],v[24:27],v[176:179]\n"
WT"lgkmcnt(2)\n"
EX"v51,v51\n"
MF"v[84:87],a[100:103],v[0:3],v[84:87]\n"
BL"v225,s[8:11],s44 offen offset:1024 sc1 lds\n"
MF"v[116:119],a[100:103],v[8:11],v[116:119]\n"
EX"v36,v36\n"
MF"v[148:151],a[100:103],v[16:19],v[148:151]\n"
RD"a[120:123],v227 offset:30720\n"
PK"v72,v48,v49\n"
MF"v[180:183],a[100:103],v[24:27],v[180:183]\n"
EX"v37,v37\n"
MF"v[88:91],a[104:107],v[0:3],v[88:91]\n"
BL"a[228:231],v236,s[8:11],s63 offen offset:2048 sc1\n"
MF"v[120:123],a[104:107],v[8:11],v[120:123]\n"
EX"v38,v38\n"
MF"v[152:155],a[104:107],v[16:19],v[152:155]\n"
RD"a[124:127],v227 offset:31744\n"
PK"v73,v50,v51\n"
MF"v[184:187],a[104:107],v[24:27],v[184:187]\n"
EX"v39,v39\n"
MF"v[92:95],a[108:111],v[0:3],v[92:95]\n"
BL"v225,s[8:11],s44 offen offset:2048 sc1 lds\n"
MF"v[124:127],a[108:111],v[8:11],v[124:127]\n"
EX"v44,v44\n"
MF"v[156:159],a[108:111],v[16:19],v[156:159]\n"
EX"v45,v45\n"
MF"v[188:191],a[108:111],v[24:27],v[188:191]\n"
WT"lgkmcnt(1)\n"
BL"a[232:235],v236,s[8:11],s63 offen offset:3072 sc1\n"
PK"v66,v36,v37\n"
MF"v[96:99],a[112:115],v[0:3],v[96:99]\n"
EX"v46,v46\n"
MF"v[128:131],a[112:115],v[8:11],v[128:131]\n"
PK"v67,v38,v39\n"
PK"v70,v44,v45\n"
MF"v[160:163],a[112:115],v[16:19],v[160:163]\n"
EX"v47,v47\n"
MF"v[192:195],a[112:115],v[24:27],v[192:195]\n"
SA"s63,s63,4096\n"
BL"v225,s[8:11],s44 offen offset:3072 sc1 lds\n"
MF"v[100:103],a[116:119],v[0:3],v[100:103]\n"
WT"lgkmcnt(0)\n"
"s_barrier\n"
MF"v[132:135],a[116:119],v[8:11],v[132:135]\n"
RD"a[64:67],v226 offset:0\n"
PK"v71,v46,v47\n"
MF"v[164:167],a[116:119],v[16:19],v[164:167]\n"
EX"v52,v52\n"
MF"v[196:199],a[116:119],v[24:27],v[196:199]\n"
BL"a[236:239],v236,s[8:11],s63 offen offset:0 sc1\n"
MF"v[104:107],a[120:123],v[0:3],v[104:107]\n"
RD"a[68:71],v226 offset:1024\n"
MF"v[136:139],a[120:123],v[8:11],v[136:139]\n"
EX"v53,v53\n"
MF"v[168:171],a[120:123],v[16:19],v[168:171]\n"
RD"a[72:75],v226 offset:2048\n"
PK"v74,v52,v53\n"
MF"v[200:203],a[120:123],v[24:27],v[200:203]\n"
EX"v54,v54\n"
MF"v[108:111],a[124:127],v[0:3],v[108:111]\n"
EX"v55,v55\n"
MF"v[140:143],a[124:127],v[8:11],v[140:143]\n"
RD"a[76:79],v226 offset:3072\n"
PK"v75,v54,v55\n"
MF"v[172:175],a[124:127],v[16:19],v[172:175]\n"
MF"v[204:207],a[124:127],v[24:27],v[204:207]\n"
RD"a[80:83],v226 offset:4096\n"
MF"a[132:135],a[128:131],v[0:3],a[132:135]\n"
MF"a[136:139],a[128:131],v[8:11],a[136:139]\n"
RD"a[84:87],v226 offset:5120\n"
MF"a[140:143],a[128:131],v[16:19],a[140:143]\n"
MF"a[144:147],a[128:131],v[24:27],a[144:147]\n"
WT"lgkmcnt(5)\n"
MF"v[0:3],a[64:67],a[0:3],0\n"
SA"s95,s40,32768\n"
EX"v56,v56\n"
MF"v[8:11],a[64:67],a[16:19],0\n"
"s_cmp_eq_u32 s95,s46\n"
"v_lshrrev_b32 v237,1,v224\n"
"v_and_b32 v237,0x78,v237\n"
MF"v[16:19],a[64:67],a[32:35],0\n"
"s_cselect_b32 s95,s47,s95\n"
EX"v57,v57\n"
MF"v[24:27],a[64:67],a[48:51],0\n"
WT"lgkmcnt(1)\n"
BL"a[240:243],v236,s[8:11],s63 offen offset:1024 sc1\n"
MF"v[0:3],a[68:71],a[4:7],v[0:3]\n"
"s_sub_u32 s95,s95,s47\n"
EX"v58,v58\n"
MF"v[8:11],a[68:71],a[20:23],v[8:11]\n"
"s_lshr_b32 s53,s47,2\n"
"v_and_b32 v239,0x300,v224\n"
"v_add3_u32 v237,v237,v239,s40\n"
MF"v[16:19],a[68:71],a[36:39],v[16:19]\n"
"s_cmp_gt_u32 s92,10\n"
EX"v59,v59\n"
MF"v[24:27],a[68:71],a[52:55],v[24:27]\n"
"s_cselect_b32 s63,s63,1065353216\n"
PK"v76,v56,v57\n"
"v_add_u32 v235,s95,v224\n"
MF"v[0:3],a[72:75],a[8:11],v[0:3]\n"
"s_lshl_b32 s50,s35,6\n"
BL"a[244:247],v236,s[8:11],s63 offen offset:2048 sc1\n"
MF"v[8:11],a[72:75],a[24:27],v[8:11]\n"
SA"s50,s50,4352\n"
EX"v60,v60\n"
MF"v[16:19],a[72:75],a[40:43],v[16:19]\n"
RD"a[88:91],v226 offset:6144\n"
"v_mov_b32 v227,v226\n"
MF"v[24:27],a[72:75],a[56:59],v[24:27]\n"
EX"v61,v61\n"
MF"v[0:3],a[76:79],a[12:15],v[0:3]\n"
BL"a[248:251],v236,s[8:11],s63 offen offset:3072 sc1\n"
MF"v[8:11],a[76:79],a[28:31],v[8:11]\n"
SA"s63,s63,4096\n"
EX"v62,v62\n"
MF"v[16:19],a[76:79],a[44:47],v[16:19]\n"
RD"a[92:95],v226 offset:7168\n"
PK"v77,v58,v59\n"
MF"v[24:27],a[76:79],a[60:63],v[24:27]\n"
EX"v63,v63\n"
MF"v[4:7],a[80:83],a[0:3],0\n"
BL"a[252:255],v236,s[8:11],s63 offen offset:0 sc1\n"
"v_lshrrev_b32 v238,1,v224\n"
MF"v[12:15],a[80:83],a[16:19],0\n"
"s_cmp_gt_u32 s92,13\n"
EX"v0,v0\n"
"v_and_b32 v238,56,v238\n"
MF"v[20:23],a[80:83],a[32:35],0\n"
"s_cselect_b32 s63,s63,1065353216\n"
RD"a[96:99],v226 offset:8192\n"
PK"v78,v60,v61\n"
MF"v[28:31],a[80:83],a[48:51],0\n"
WT"lgkmcnt(2)\n"
EX"v1,v1\n"
MF"v[4:7],a[84:87],a[4:7],v[4:7]\n"
BL"v[244:247],v236,s[8:11],s63 offen offset:1024 sc1\n"
"v_add_u32 v236,s53,v235\n"
MF"v[12:15],a[84:87],a[20:23],v[12:15]\n"
EX"v2,v2\n"
MF"v[20:23],a[84:87],a[36:39],v[20:23]\n"
RD"a[100:103],v226 offset:9216\n"
PK"v79,v62,v63\n"
MF"v[28:31],a[84:87],a[52:55],v[28:31]\n"
EX"v3,v3\n"
MF"v[4:7],a[88:91],a[8:11],v[4:7]\n"
"buffer_load_dwordx2 v[240:241],v238,s[8:11],s50 offen sc1\n"
MF"v[12:15],a[88:91],a[24:27],v[12:15]\n"
EX"v8,v8\n"
MF"v[20:23],a[88:91],a[40:43],v[20:23]\n"
RD"a[104:107],v226 offset:10240\n"
PK"v0,v0,v1\n"
MF"v[28:31],a[88:91],a[56:59],v[28:31]\n"
WT"lgkmcnt(2)\n"
EX"v9,v9\n"
MF"v[4:7],a[92:95],a[12:15],v[4:7]\n"
WT"vmcnt(23)\n"
RT"a[176:177],v237 offset:0\n"
RT"a[178:179],v237 offset:128\n"
MF"v[12:15],a[92:95],a[28:31],v[12:15]\n"
EX"v10,v10\n"
MF"v[20:23],a[92:95],a[44:47],v[20:23]\n"
RD"a[108:111],v226 offset:11264\n"
PK"v1,v2,v3\n"
MF"v[28:31],a[92:95],a[60:63],v[28:31]\n"
EX"v11,v11\n"
MF"v[32:35],a[96:99],a[0:3],0\n"
RT"a[180:181],v237 offset:1024\n"
RT"a[182:183],v237 offset:1152\n"
MF"v[40:43],a[96:99],a[16:19],0\n"
EX"v16,v16\n"
MF"v[48:51],a[96:99],a[32:35],0\n"
RD"a[112:115],v226 offset:12288\n"
PK"v8,v8,v9\n"
MF"v[56:59],a[96:99],a[48:51],0\n"
WT"lgkmcnt(4)\n"
EX"v17,v17\n"
MF"v[32:35],a[100:103],a[4:7],v[32:35]\n"
RT"a[184:185],v237 offset:2048\n"
RT"a[186:187],v237 offset:2176\n"
MF"v[40:43],a[100:103],a[20:23],v[40:43]\n"
EX"v18,v18\n"
MF"v[48:51],a[100:103],a[36:39],v[48:51]\n"
RD"a[116:119],v226 offset:13312\n"
PK"v9,v10,v11\n"
MF"v[56:59],a[100:103],a[52:55],v[56:59]\n"
EX"v19,v19\n"
MF"v[32:35],a[104:107],a[8:11],v[32:35]\n"
RT"a[188:189],v237 offset:3072\n"
RT"a[190:191],v237 offset:3200\n"
MF"v[40:43],a[104:107],a[24:27],v[40:43]\n"
EX"v24,v24\n"
MF"v[48:51],a[104:107],a[40:43],v[48:51]\n"
RD"a[120:123],v226 offset:14336\n"
PK"v16,v16,v17\n"
MF"v[56:59],a[104:107],a[56:59],v[56:59]\n"
WT"lgkmcnt(4)\n"
EX"v25,v25\n"
MF"v[32:35],a[108:111],a[12:15],v[32:35]\n"
RT"a[192:193],v237 offset:16384\n"
RT"a[194:195],v237 offset:16512\n"
MF"v[40:43],a[108:111],a[28:31],v[40:43]\n"
EX"v26,v26\n"
MF"v[48:51],a[108:111],a[44:47],v[48:51]\n"
RD"a[124:127],v226 offset:15360\n"
PK"v17,v18,v19\n"
MF"v[56:59],a[108:111],a[60:63],v[56:59]\n"
EX"v27,v27\n"
MF"v[36:39],a[112:115],a[0:3],0\n"
RT"a[196:197],v237 offset:17408\n"
RT"a[198:199],v237 offset:17536\n"
MF"v[44:47],a[112:115],a[16:19],0\n"
EX"v4,v4\n"
MF"v[52:55],a[112:115],a[32:35],0\n"
RD"a[64:67],v227 offset:16384\n"
PK"v24,v24,v25\n"
MF"v[60:63],a[112:115],a[48:51],0\n"
WT"lgkmcnt(4)\n"
EX"v5,v5\n"
MF"v[36:39],a[116:119],a[4:7],v[36:39]\n"
RT"a[200:201],v237 offset:18432\n"
RT"a[202:203],v237 offset:18560\n"
MF"v[44:47],a[116:119],a[20:23],v[44:47]\n"
EX"v6,v6\n"
MF"v[52:55],a[116:119],a[36:39],v[52:55]\n"
RD"a[68:71],v227 offset:17408\n"
PK"v25,v26,v27\n"
MF"v[60:63],a[116:119],a[52:55],v[60:63]\n"
EX"v7,v7\n"
MF"v[36:39],a[120:123],a[8:11],v[36:39]\n"
EX"v12,v12\n"
MF"v[44:47],a[120:123],a[24:27],v[44:47]\n"
RD"a[72:75],v227 offset:18432\n"
"v_readlane_b32 s43,v228,s42\n"
MF"v[52:55],a[120:123],a[40:43],v[52:55]\n"
EX"v13,v13\n"
MF"v[60:63],a[120:123],a[56:59],v[60:63]\n"
WT"lgkmcnt(2)\n"
PK"v2,v4,v5\n"
PK"v3,v6,v7\n"
MF"v[36:39],a[124:127],a[12:15],v[36:39]\n"
"s_mov_b32 m0,s40\n"
EX"v14,v14\n"
MF"v[44:47],a[124:127],a[28:31],v[44:47]\n"
RD"a[76:79],v227 offset:19456\n"
"v_readlane_b32 s44,v229,s42\n"
MF"v[52:55],a[124:127],a[44:47],v[52:55]\n"
EX"v15,v15\n"
MF"v[60:63],a[124:127],a[60:63],v[60:63]\n"
SA"s42,s42,1\n"
BL"v225,s[8:11],s43 offen offset:0 sc1 lds\n"
MF"v[80:83],a[64:67],v[64:67],v[80:83]\n"
SA"s41,s41,32768\n"
EX"v20,v20\n"
MF"v[112:115],a[64:67],v[68:71],v[112:115]\n"
"s_cmp_eq_u32 s41,163840\n"
RD"a[80:83],v227 offset:20480\n"
PK"v10,v12,v13\n"
MF"v[144:147],a[64:67],v[72:75],v[144:147]\n"
"s_cselect_b32 s41,0,s41\n"
EX"v21,v21\n"
MF"v[176:179],a[64:67],v[76:79],v[176:179]\n"
WT"lgkmcnt(2)\n"
"v_add_u32 v226,s41,v224\n"
PK"v11,v14,v15\n"
MF"v[84:87],a[68:71],v[64:67],v[84:87]\n"
EX"v22,v22\n"
MF"v[116:119],a[68:71],v[68:71],v[116:119]\n"
RD"a[84:87],v227 offset:21504\n"
PK"v18,v20,v21\n"
MF"v[148:151],a[68:71],v[72:75],v[148:151]\n"
WT"vmcnt(1)\n"
"v_cmp_eq_u64 vcc,s[28:29],v[240:241]\n"
"s_cmp_eq_u64 vcc,exec\n"
"s_cbranch_scc0 L1128_%=\n"
"L1129_%=:\n"
MF"v[180:183],a[68:71],v[76:79],v[180:183]\n"
EX"v23,v23\n"
MF"v[88:91],a[72:75],v[64:67],v[88:91]\n"
BL"v225,s[8:11],s43 offen offset:1024 sc1 lds\n"
MF"v[120:123],a[72:75],v[68:71],v[120:123]\n"
EX"v28,v28\n"
MF"v[152:155],a[72:75],v[72:75],v[152:155]\n"
RD"a[88:91],v227 offset:22528\n"
PK"v19,v22,v23\n"
MF"v[184:187],a[72:75],v[76:79],v[184:187]\n"
WT"lgkmcnt(2)\n"
EX"v29,v29\n"
MF"v[92:95],a[76:79],v[64:67],v[92:95]\n"
EX"v30,v30\n"
MF"v[124:127],a[76:79],v[68:71],v[124:127]\n"
RD"a[92:95],v227 offset:23552\n"
PK"v26,v28,v29\n"
MF"v[156:159],a[76:79],v[72:75],v[156:159]\n"
EX"v31,v31\n"
MF"v[188:191],a[76:79],v[76:79],v[188:191]\n"
BL"v225,s[8:11],s43 offen offset:2048 sc1 lds\n"
MF"v[96:99],a[80:83],v[64:67],v[96:99]\n"
EX"v32,v32\n"
MF"v[128:131],a[80:83],v[68:71],v[128:131]\n"
RD"a[96:99],v227 offset:24576\n"
PK"v27,v30,v31\n"
MF"v[160:163],a[80:83],v[72:75],v[160:163]\n"
EX"v33,v33\n"
MF"v[192:195],a[80:83],v[76:79],v[192:195]\n"
WT"lgkmcnt(2)\n"
MF"v[100:103],a[84:87],v[64:67],v[100:103]\n"
EX"v34,v34\n"
MF"v[132:135],a[84:87],v[68:71],v[132:135]\n"
RD"a[100:103],v227 offset:25600\n"
MF"v[164:167],a[84:87],v[72:75],v[164:167]\n"
EX"v35,v35\n"
MF"v[196:199],a[84:87],v[76:79],v[196:199]\n"
BL"v225,s[8:11],s43 offen offset:3072 sc1 lds\n"
MF"v[104:107],a[88:91],v[64:67],v[104:107]\n"
EX"v40,v40\n"
MF"v[136:139],a[88:91],v[68:71],v[136:139]\n"
RD"a[104:107],v227 offset:26624\n"
MF"v[168:171],a[88:91],v[72:75],v[168:171]\n"
EX"v41,v41\n"
MF"v[200:203],a[88:91],v[76:79],v[200:203]\n"
WT"lgkmcnt(2)\n"
SA"m0,s40,16384\n"
MF"v[108:111],a[92:95],v[64:67],v[108:111]\n"
EX"v42,v42\n"
MF"v[140:143],a[92:95],v[68:71],v[140:143]\n"
RD"a[108:111],v227 offset:27648\n"
MF"v[172:175],a[92:95],v[72:75],v[172:175]\n"
EX"v43,v43\n"
MF"v[204:207],a[92:95],v[76:79],v[204:207]\n"
BL"v225,s[8:11],s44 offen offset:0 sc1 lds\n"
MF"a[132:135],a[128:131],v[64:67],a[132:135]\n"
SA"s40,s40,32768\n"
EX"v48,v48\n"
PK"v64,v32,v33\n"
MF"a[136:139],a[128:131],v[68:71],a[136:139]\n"
"s_cmp_eq_u32 s40,s46\n"
RD"a[112:115],v227 offset:28672\n"
PK"v65,v34,v35\n"
MF"a[140:143],a[128:131],v[72:75],a[140:143]\n"
"s_cselect_b32 s40,s47,s40\n"
EX"v49,v49\n"
MF"a[144:147],a[128:131],v[76:79],a[144:147]\n"
PK"v68,v40,v41\n"
PK"v69,v42,v43\n"
MF"v[240:243],a[148:151],a[204:207],0\n"
EX"v50,v50\n"
MF"v[240:243],a[152:155],a[208:211],v[240:243]\n"
RD"a[116:119],v227 offset:29696\n"
PK"v72,v48,v49\n"
MF"v[240:243],a[156:159],a[212:215],v[240:243]\n"
EX"v51,v51\n"
MF"v[240:243],a[160:163],a[216:219],v[240:243]\n"
BL"v225,s[8:11],s44 offen offset:1024 sc1 lds\n"
MF"v[240:243],a[164:167],a[220:223],v[240:243]\n"
EX"v36,v36\n"
MF"v[240:243],a[168:171],a[224:227],v[240:243]\n"
RD"a[120:123],v227 offset:30720\n"
PK"v73,v50,v51\n"
MF"v[240:243],a[172:175],a[228:231],v[240:243]\n"
EX"v37,v37\n"
MF"v[240:243],a[176:179],a[232:235],v[240:243]\n"
PK"v66,v36,v37\n"
MF"v[240:243],a[180:183],a[236:239],v[240:243]\n"
EX"v38,v38\n"
MF"v[240:243],a[184:187],a[240:243],v[240:243]\n"
RD"a[124:127],v227 offset:31744\n"
MF"v[240:243],a[188:191],a[244:247],v[240:243]\n"
EX"v39,v39\n"
MF"v[240:243],a[192:195],a[248:251],v[240:243]\n"
BL"v225,s[8:11],s44 offen offset:2048 sc1 lds\n"
MF"v[240:243],a[196:199],a[252:255],v[240:243]\n"
EX"v44,v44\n"
MF"v[240:243],a[200:203],v[244:247],v[240:243]\n"
PK"v67,v38,v39\n"
MF"v[80:83],a[96:99],v[0:3],v[80:83]\n"
EX"v45,v45\n"
MF"v[112:115],a[96:99],v[8:11],v[112:115]\n"
PK"v70,v44,v45\n"
MF"v[144:147],a[96:99],v[16:19],v[144:147]\n"
EX"v46,v46\n"
MF"v[176:179],a[96:99],v[24:27],v[176:179]\n"
WT"lgkmcnt(0)\n"
MF"v[84:87],a[100:103],v[0:3],v[84:87]\n"
EX"v47,v47\n"
MF"v[116:119],a[100:103],v[8:11],v[116:119]\n"
"ds_write_b128 v236,v[240:243]\n"
MF"v[148:151],a[100:103],v[16:19],v[148:151]\n"
BL"v225,s[8:11],s44 offen offset:3072 sc1 lds\n"
MF"v[180:183],a[100:103],v[24:27],v[180:183]\n"
PK"v71,v46,v47\n"
MF"v[88:91],a[104:107],v[0:3],v[88:91]\n"
EX"v52,v52\n"
MF"v[120:123],a[104:107],v[8:11],v[120:123]\n"
MF"v[152:155],a[104:107],v[16:19],v[152:155]\n"
EX"v53,v53\n"
MF"v[184:187],a[104:107],v[24:27],v[184:187]\n"
PK"v74,v52,v53\n"
MF"v[92:95],a[108:111],v[0:3],v[92:95]\n"
EX"v54,v54\n"
MF"v[124:127],a[108:111],v[8:11],v[124:127]\n"
MF"v[156:159],a[108:111],v[16:19],v[156:159]\n"
EX"v55,v55\n"
MF"v[188:191],a[108:111],v[24:27],v[188:191]\n"
PK"v75,v54,v55\n"
MF"v[96:99],a[112:115],v[0:3],v[96:99]\n"
MF"v[128:131],a[112:115],v[8:11],v[128:131]\n"
MF"v[160:163],a[112:115],v[16:19],v[160:163]\n"
MF"v[192:195],a[112:115],v[24:27],v[192:195]\n"
MF"v[100:103],a[116:119],v[0:3],v[100:103]\n"
WT"lgkmcnt(0)\n"
"s_barrier\n"
MF"v[132:135],a[116:119],v[8:11],v[132:135]\n"
RD"a[64:67],v226 offset:0\n"
MF"v[164:167],a[116:119],v[16:19],v[164:167]\n"
RD"v[240:243],v235 offset:0\n"
MF"v[196:199],a[116:119],v[24:27],v[196:199]\n"
RD"a[68:71],v226 offset:1024\n"
MF"v[104:107],a[120:123],v[0:3],v[104:107]\n"
RD"v[244:247],v235 offset:1024\n"
MF"v[136:139],a[120:123],v[8:11],v[136:139]\n"
RD"a[72:75],v226 offset:2048\n"
MF"v[168:171],a[120:123],v[16:19],v[168:171]\n"
RD"v[248:251],v235 offset:2048\n"
MF"v[200:203],a[120:123],v[24:27],v[200:203]\n"
MF"v[108:111],a[124:127],v[0:3],v[108:111]\n"
RD"v[252:255],v235 offset:3072\n"
MF"v[140:143],a[124:127],v[8:11],v[140:143]\n"
RD"a[76:79],v226 offset:3072\n"
MF"v[172:175],a[124:127],v[16:19],v[172:175]\n"
MF"v[204:207],a[124:127],v[24:27],v[204:207]\n"
RD"a[80:83],v226 offset:4096\n"
MF"a[132:135],a[128:131],v[0:3],a[132:135]\n"
MF"a[136:139],a[128:131],v[8:11],a[136:139]\n"
RD"a[84:87],v226 offset:5120\n"
MF"a[140:143],a[128:131],v[16:19],a[140:143]\n"
MF"a[144:147],a[128:131],v[24:27],a[144:147]\n"
"s_cmp_eq_u32 s57,2\n"
"s_cbranch_scc1 LMD_%=\n"
"s_cmp_eq_u32 s34,2\n"
"s_cbranch_scc1 LMA_%=\n"
WT"lgkmcnt(9)\n"
MF"v[0:3],a[64:67],a[0:3],0\n"
EX"v56,v56\n"
MF"v[8:11],a[64:67],a[16:19],0\n"
WT"lgkmcnt(6)\n"
"v_add_f32 v240,v240,v244\n"
MF"v[16:19],a[64:67],a[32:35],0\n"
EX"v57,v57\n"
MF"v[24:27],a[64:67],a[48:51],0\n"
WT"lgkmcnt(3)\n"
"v_add_f32 v248,v248,v252\n"
"v_add_f32 v241,v241,v245\n"
MF"v[0:3],a[68:71],a[4:7],v[0:3]\n"
EX"v58,v58\n"
MF"v[8:11],a[68:71],a[20:23],v[8:11]\n"
"v_add_f32 v249,v249,v253\n"
PK"v76,v56,v57\n"
MF"v[16:19],a[68:71],a[36:39],v[16:19]\n"
EX"v59,v59\n"
MF"v[24:27],a[68:71],a[52:55],v[24:27]\n"
"v_add_f32 v242,v242,v246\n"
"v_mov_b32 v227,v226\n"
MF"v[0:3],a[72:75],a[8:11],v[0:3]\n"
EX"v60,v60\n"
MF"v[8:11],a[72:75],a[24:27],v[8:11]\n"
RD"a[88:91],v226 offset:6144\n"
"v_add_f32 v250,v250,v254\n"
MF"v[16:19],a[72:75],a[40:43],v[16:19]\n"
EX"v61,v61\n"
MF"v[24:27],a[72:75],a[56:59],v[24:27]\n"
WT"lgkmcnt(1)\n"
"v_add_f32 v243,v243,v247\n"
"v_add_f32 v251,v251,v255\n"
MF"v[0:3],a[76:79],a[12:15],v[0:3]\n"
EX"v62,v62\n"
MF"v[8:11],a[76:79],a[28:31],v[8:11]\n"
RD"a[92:95],v226 offset:7168\n"
PK"v77,v58,v59\n"
MF"v[16:19],a[76:79],a[44:47],v[16:19]\n"
EX"v63,v63\n"
MF"v[24:27],a[76:79],a[60:63],v[24:27]\n"
PK"v240,v240,v240\n"
"v_lshlrev_b32 v240,16,v240\n"
MF"v[4:7],a[80:83],a[0:3],0\n"
EX"v0,v0\n"
MF"v[12:15],a[80:83],a[16:19],0\n"
RD"a[96:99],v226 offset:8192\n"
PK"v241,v241,v241\n"
MF"v[20:23],a[80:83],a[32:35],0\n"
EX"v1,v1\n"
MF"v[28:31],a[80:83],a[48:51],0\n"
"v_lshlrev_b32 v241,16,v241\n"
PK"v78,v60,v61\n"
MF"v[4:7],a[84:87],a[4:7],v[4:7]\n"
EX"v2,v2\n"
PK"v242,v242,v242\n"
MF"v[12:15],a[84:87],a[20:23],v[12:15]\n"
RD"a[100:103],v226 offset:9216\n"
"v_lshlrev_b32 v242,16,v242\n"
MF"v[20:23],a[84:87],a[36:39],v[20:23]\n"
EX"v3,v3\n"
PK"v243,v243,v243\n"
MF"v[28:31],a[84:87],a[52:55],v[28:31]\n"
WT"lgkmcnt(2)\n"
"v_lshlrev_b32 v243,16,v243\n"
PK"v79,v62,v63\n"
MF"v[4:7],a[88:91],a[8:11],v[4:7]\n"
EX"v8,v8\n"
MU"v244,0xbfb8aa3b,v240\n"
MF"v[12:15],a[88:91],a[24:27],v[12:15]\n"
EX"v244,v244\n"
MU"v245,0xbfb8aa3b,v241\n"
MF"v[20:23],a[88:91],a[40:43],v[20:23]\n"
EX"v245,v245\n"
MU"v246,0xbfb8aa3b,v242\n"
MF"v[28:31],a[88:91],a[56:59],v[28:31]\n"
RD"a[104:107],v226 offset:10240\n"
MU"v247,0xbfb8aa3b,v243\n"
PK"v0,v0,v1\n"
MF"v[4:7],a[92:95],a[12:15],v[4:7]\n"
EX"v246,v246\n"
EX"v9,v9\n"
MF"v[12:15],a[92:95],a[28:31],v[12:15]\n"
EX"v10,v10\n"
PK"v248,v248,v248\n"
MF"v[20:23],a[92:95],a[44:47],v[20:23]\n"
EX"v247,v247\n"
"v_lshlrev_b32 v248,16,v248\n"
MF"v[28:31],a[92:95],a[60:63],v[28:31]\n"
WT"lgkmcnt(1)\n"
RD"a[108:111],v226 offset:11264\n"
PK"v1,v2,v3\n"
MF"v[32:35],a[96:99],a[0:3],0\n"
EX"v11,v11\n"
PK"v249,v249,v249\n"
MF"v[40:43],a[96:99],a[16:19],0\n"
EX"v16,v16\n"
"v_lshlrev_b32 v249,16,v249\n"
MF"v[48:51],a[96:99],a[32:35],0\n"
RD"a[112:115],v226 offset:12288\n"
PK"v250,v250,v250\n"
MF"v[56:59],a[96:99],a[48:51],0\n"
EX"v17,v17\n"
PK"v8,v8,v9\n"
MF"v[32:35],a[100:103],a[4:7],v[32:35]\n"
EX"v18,v18\n"
"v_lshlrev_b32 v250,16,v250\n"
MF"v[40:43],a[100:103],a[20:23],v[40:43]\n"
RD"a[116:119],v226 offset:13312\n"
PK"v251,v251,v251\n"
MF"v[48:51],a[100:103],a[36:39],v[48:51]\n"
EX"v19,v19\n"
MF"v[56:59],a[100:103],a[52:55],v[56:59]\n"
WT"lgkmcnt(2)\n"
"v_lshlrev_b32 v251,16,v251\n"
PK"v9,v10,v11\n"
MF"v[32:35],a[104:107],a[8:11],v[32:35]\n"
EX"v24,v24\n"
"v_add_f32 v244,1.0,v244\n"
MF"v[40:43],a[104:107],a[24:27],v[40:43]\n"
"v_rcp_f32 v244,v244\n"
"v_add_f32 v245,1.0,v245\n"
MF"v[48:51],a[104:107],a[40:43],v[48:51]\n"
"v_rcp_f32 v245,v245\n"
"v_add_f32 v246,1.0,v246\n"
MF"v[56:59],a[104:107],a[56:59],v[56:59]\n"
"v_rcp_f32 v246,v246\n"
"v_add_f32 v247,1.0,v247\n"
MF"v[32:35],a[108:111],a[12:15],v[32:35]\n"
"v_rcp_f32 v247,v247\n"
EX"v25,v25\n"
MF"v[40:43],a[108:111],a[28:31],v[40:43]\n"
RD"a[120:123],v226 offset:14336\n"
RD"a[124:127],v226 offset:15360\n"
PK"v16,v16,v17\n"
MF"v[48:51],a[108:111],a[44:47],v[48:51]\n"
EX"v26,v26\n"
MF"v[56:59],a[108:111],a[60:63],v[56:59]\n"
WT"lgkmcnt(2)\n"
EX"v27,v27\n"
PK"v17,v18,v19\n"
MF"v[36:39],a[112:115],a[0:3],0\n"
EX"v4,v4\n"
MU"v240,v240,v244\n"
MF"v[44:47],a[112:115],a[16:19],0\n"
RD"a[64:67],v227 offset:16384\n"
MU"v241,v241,v245\n"
MF"v[52:55],a[112:115],a[32:35],0\n"
EX"v5,v5\n"
MU"v242,v242,v246\n"
MF"v[60:63],a[112:115],a[48:51],0\n"
MU"v243,v243,v247\n"
PK"v24,v24,v25\n"
MF"v[36:39],a[116:119],a[4:7],v[36:39]\n"
EX"v6,v6\n"
PK"v240,v240,v240\n"
MF"v[44:47],a[116:119],a[20:23],v[44:47]\n"
RD"a[68:71],v227 offset:17408\n"
"v_lshlrev_b32 v240,16,v240\n"
MF"v[52:55],a[116:119],a[36:39],v[52:55]\n"
EX"v7,v7\n"
PK"v241,v241,v241\n"
MF"v[60:63],a[116:119],a[52:55],v[60:63]\n"
WT"lgkmcnt(2)\n"
"v_readlane_b32 s43,v228,s42\n"
"v_lshlrev_b32 v241,16,v241\n"
PK"v25,v26,v27\n"
MF"v[36:39],a[120:123],a[8:11],v[36:39]\n"
EX"v12,v12\n"
PK"v242,v242,v242\n"
MF"v[44:47],a[120:123],a[24:27],v[44:47]\n"
RD"a[72:75],v227 offset:18432\n"
"v_lshlrev_b32 v242,16,v242\n"
MF"v[52:55],a[120:123],a[40:43],v[52:55]\n"
EX"v13,v13\n"
PK"v243,v243,v243\n"
MF"v[60:63],a[120:123],a[56:59],v[60:63]\n"
"s_mov_b32 m0,s40\n"
"v_lshlrev_b32 v243,16,v243\n"
PK"v2,v4,v5\n"
MF"v[36:39],a[124:127],a[12:15],v[36:39]\n"
EX"v14,v14\n"
MU"v240,v240,v248\n"
MF"v[44:47],a[124:127],a[28:31],v[44:47]\n"
RD"a[76:79],v227 offset:19456\n"
"v_readlane_b32 s44,v229,s42\n"
MU"v241,v241,v249\n"
MF"v[52:55],a[124:127],a[44:47],v[52:55]\n"
EX"v15,v15\n"
MU"v242,v242,v250\n"
MF"v[60:63],a[124:127],a[60:63],v[60:63]\n"
WT"lgkmcnt(2)\n"
BL"v225,s[8:11],s43 offen offset:0 sc1 lds\n"
MU"v243,v243,v251\n"
MF"v[80:83],a[64:67],v[64:67],v[80:83]\n"
SA"s42,s42,1\n"
EX"v20,v20\n"
PK"v3,v6,v7\n"
MF"v[112:115],a[64:67],v[68:71],v[112:115]\n"
SA"s41,s41,32768\n"
RD"a[80:83],v227 offset:20480\n"
PK"v240,v240,v241\n"
PK"v241,v242,v243\n"
MF"v[144:147],a[64:67],v[72:75],v[144:147]\n"
"s_cmp_eq_u32 s41,163840\n"
EX"v21,v21\n"
"v_lshrrev_b32 v239,9,v224\n"
MF"v[176:179],a[64:67],v[76:79],v[176:179]\n"
"s_cselect_b32 s41,0,s41\n"
"v_lshlrev_b32 v239,8,v239\n"
"v_add_u32 v226,s41,v224\n"
PK"v10,v12,v13\n"
MF"v[84:87],a[68:71],v[64:67],v[84:87]\n"
EX"v22,v22\n"
"v_bfe_u32 v237,v224,8,1\n"
MF"v[116:119],a[68:71],v[68:71],v[116:119]\n"
RD"a[84:87],v227 offset:21504\n"
"v_lshl_add_u32 v239,v237,3,v239\n"
MF"v[148:151],a[68:71],v[72:75],v[148:151]\n"
EX"v23,v23\n"
"v_and_b32 v237,0x80,v224\n"
MF"v[180:183],a[68:71],v[76:79],v[180:183]\n"
WT"lgkmcnt(2)\n"
BL"v225,s[8:11],s43 offen offset:1024 sc1 lds\n"
"v_lshl_add_u32 v239,v237,23,v239\n"
MF"v[88:91],a[72:75],v[64:67],v[88:91]\n"
EX"v28,v28\n"
PK"v11,v14,v15\n"
MF"v[120:123],a[72:75],v[68:71],v[120:123]\n"
RD"a[88:91],v227 offset:22528\n"
"v_and_b32 v237,0x70,v224\n"
"v_add_u32 v239,v239,v237\n"
MF"v[152:155],a[72:75],v[72:75],v[152:155]\n"
"buffer_store_dwordx2 v[240:241],v239,s[8:11],s93 offen sc1\n"
MF"v[184:187],a[72:75],v[76:79],v[184:187]\n"
EX"v29,v29\n"
PK"v18,v20,v21\n"
MF"v[92:95],a[76:79],v[64:67],v[92:95]\n"
EX"v30,v30\n"
MF"v[124:127],a[76:79],v[68:71],v[124:127]\n"
RD"a[92:95],v227 offset:23552\n"
PK"v19,v22,v23\n"
MF"v[156:159],a[76:79],v[72:75],v[156:159]\n"
EX"v31,v31\n"
MF"v[188:191],a[76:79],v[76:79],v[188:191]\n"
WT"lgkmcnt(2)\n"
BL"v225,s[8:11],s43 offen offset:2048 sc1 lds\n"
MF"v[96:99],a[80:83],v[64:67],v[96:99]\n"
EX"v32,v32\n"
MF"v[128:131],a[80:83],v[68:71],v[128:131]\n"
RD"a[96:99],v227 offset:24576\n"
PK"v26,v28,v29\n"
MF"v[160:163],a[80:83],v[72:75],v[160:163]\n"
EX"v33,v33\n"
MF"v[192:195],a[80:83],v[76:79],v[192:195]\n"
PK"v27,v30,v31\n"
MF"v[100:103],a[84:87],v[64:67],v[100:103]\n"
EX"v34,v34\n"
MF"v[132:135],a[84:87],v[68:71],v[132:135]\n"
RD"a[100:103],v227 offset:25600\n"
MF"v[164:167],a[84:87],v[72:75],v[164:167]\n"
EX"v35,v35\n"
MF"v[196:199],a[84:87],v[76:79],v[196:199]\n"
WT"lgkmcnt(2)\n"
BL"v225,s[8:11],s43 offen offset:3072 sc1 lds\n"
MF"v[104:107],a[88:91],v[64:67],v[104:107]\n"
EX"v40,v40\n"
MF"v[136:139],a[88:91],v[68:71],v[136:139]\n"
RD"a[104:107],v227 offset:26624\n"
MF"v[168:171],a[88:91],v[72:75],v[168:171]\n"
EX"v41,v41\n"
MF"v[200:203],a[88:91],v[76:79],v[200:203]\n"
SA"m0,s40,16384\n"
MF"v[108:111],a[92:95],v[64:67],v[108:111]\n"
EX"v42,v42\n"
MF"v[140:143],a[92:95],v[68:71],v[140:143]\n"
RD"a[108:111],v227 offset:27648\n"
MF"v[172:175],a[92:95],v[72:75],v[172:175]\n"
EX"v43,v43\n"
MF"v[204:207],a[92:95],v[76:79],v[204:207]\n"
BL"v225,s[8:11],s44 offen offset:0 sc1 lds\n"
MF"a[132:135],a[128:131],v[64:67],a[132:135]\n"
SA"s40,s40,32768\n"
EX"v48,v48\n"
PK"v64,v32,v33\n"
MF"a[136:139],a[128:131],v[68:71],a[136:139]\n"
"s_cmp_eq_u32 s40,s46\n"
RD"a[112:115],v227 offset:28672\n"
PK"v65,v34,v35\n"
MF"a[140:143],a[128:131],v[72:75],a[140:143]\n"
"s_cselect_b32 s40,s47,s40\n"
EX"v49,v49\n"
MF"a[144:147],a[128:131],v[76:79],a[144:147]\n"
WT"lgkmcnt(2)\n"
PK"v68,v40,v41\n"
PK"v69,v42,v43\n"
MF"v[80:83],a[96:99],v[0:3],v[80:83]\n"
EX"v50,v50\n"
MF"v[112:115],a[96:99],v[8:11],v[112:115]\n"
RD"a[116:119],v227 offset:29696\n"
PK"v72,v48,v49\n"
MF"v[144:147],a[96:99],v[16:19],v[144:147]\n"
EX"v51,v51\n"
MF"v[176:179],a[96:99],v[24:27],v[176:179]\n"
BL"v225,s[8:11],s44 offen offset:1024 sc1 lds\n"
MF"v[84:87],a[100:103],v[0:3],v[84:87]\n"
EX"v36,v36\n"
MF"v[116:119],a[100:103],v[8:11],v[116:119]\n"
RD"a[120:123],v227 offset:30720\n"
PK"v73,v50,v51\n"
MF"v[148:151],a[100:103],v[16:19],v[148:151]\n"
EX"v37,v37\n"
MF"v[180:183],a[100:103],v[24:27],v[180:183]\n"
PK"v66,v36,v37\n"
MF"v[88:91],a[104:107],v[0:3],v[88:91]\n"
EX"v38,v38\n"
MF"v[120:123],a[104:107],v[8:11],v[120:123]\n"
RD"a[124:127],v227 offset:31744\n"
MF"v[152:155],a[104:107],v[16:19],v[152:155]\n"
EX"v39,v39\n"
MF"v[184:187],a[104:107],v[24:27],v[184:187]\n"
WT"lgkmcnt(2)\n"
BL"v225,s[8:11],s44 offen offset:2048 sc1 lds\n"
MF"v[92:95],a[108:111],v[0:3],v[92:95]\n"
EX"v44,v44\n"
MF"v[124:127],a[108:111],v[8:11],v[124:127]\n"
PK"v67,v38,v39\n"
MF"v[156:159],a[108:111],v[16:19],v[156:159]\n"
EX"v45,v45\n"
MF"v[188:191],a[108:111],v[24:27],v[188:191]\n"
PK"v70,v44,v45\n"
MF"v[96:99],a[112:115],v[0:3],v[96:99]\n"
EX"v46,v46\n"
MF"v[128:131],a[112:115],v[8:11],v[128:131]\n"
MF"v[160:163],a[112:115],v[16:19],v[160:163]\n"
EX"v47,v47\n"
MF"v[192:195],a[112:115],v[24:27],v[192:195]\n"
BL"v225,s[8:11],s44 offen offset:3072 sc1 lds\n"
MF"v[100:103],a[116:119],v[0:3],v[100:103]\n"
WT"lgkmcnt(0)\n"
"s_barrier\n"
MF"v[132:135],a[116:119],v[8:11],v[132:135]\n"
RD"a[64:67],v226 offset:0\n"
PK"v71,v46,v47\n"
MF"v[164:167],a[116:119],v[16:19],v[164:167]\n"
EX"v52,v52\n"
MF"v[196:199],a[116:119],v[24:27],v[196:199]\n"
RD"a[68:71],v226 offset:1024\n"
MF"v[104:107],a[120:123],v[0:3],v[104:107]\n"
EX"v53,v53\n"
MF"v[136:139],a[120:123],v[8:11],v[136:139]\n"
RD"a[72:75],v226 offset:2048\n"
PK"v74,v52,v53\n"
MF"v[168:171],a[120:123],v[16:19],v[168:171]\n"
EX"v54,v54\n"
MF"v[200:203],a[120:123],v[24:27],v[200:203]\n"
MF"v[108:111],a[124:127],v[0:3],v[108:111]\n"
WT"vmcnt(6)\n"
"buffer_store_dwordx2 v[232:233],v230,s[8:11],s94 offen sc1\n"
MF"v[140:143],a[124:127],v[8:11],v[140:143]\n"
EX"v55,v55\n"
MF"v[172:175],a[124:127],v[16:19],v[172:175]\n"
RD"a[76:79],v226 offset:3072\n"
PK"v75,v54,v55\n"
MF"v[204:207],a[124:127],v[24:27],v[204:207]\n"
RD"a[80:83],v226 offset:4096\n"
MF"a[132:135],a[128:131],v[0:3],a[132:135]\n"
MF"a[136:139],a[128:131],v[8:11],a[136:139]\n"
RD"a[84:87],v226 offset:5120\n"
MF"a[140:143],a[128:131],v[16:19],a[140:143]\n"
MF"a[144:147],a[128:131],v[24:27],a[144:147]\n"
"s_mov_b32 s57,0\n"
"s_lshl_b32 s56,s34,1\n"
"s_sub_u32 s56,s56,6\n"
"s_branch LP1_%=\n"
"LMA_%=:\n"
WT"lgkmcnt(9)\n"
MF"v[0:3],a[64:67],a[0:3],v[208:211]\n"
EX"v56,v56\n"
MF"v[8:11],a[64:67],a[16:19],0\n"
WT"lgkmcnt(6)\n"
"v_add_f32 v240,v240,v244\n"
MF"v[16:19],a[64:67],a[32:35],0\n"
EX"v57,v57\n"
MF"v[24:27],a[64:67],a[48:51],0\n"
WT"lgkmcnt(3)\n"
"v_add_f32 v248,v248,v252\n"
"v_add_f32 v241,v241,v245\n"
MF"v[0:3],a[68:71],a[4:7],v[0:3]\n"
EX"v58,v58\n"
MF"v[8:11],a[68:71],a[20:23],v[8:11]\n"
"v_add_f32 v249,v249,v253\n"
PK"v76,v56,v57\n"
MF"v[16:19],a[68:71],a[36:39],v[16:19]\n"
EX"v59,v59\n"
MF"v[24:27],a[68:71],a[52:55],v[24:27]\n"
"v_add_f32 v242,v242,v246\n"
"v_mov_b32 v227,v226\n"
MF"v[0:3],a[72:75],a[8:11],v[0:3]\n"
EX"v60,v60\n"
MF"v[8:11],a[72:75],a[24:27],v[8:11]\n"
RD"a[88:91],v226 offset:6144\n"
"v_add_f32 v250,v250,v254\n"
MF"v[16:19],a[72:75],a[40:43],v[16:19]\n"
EX"v61,v61\n"
MF"v[24:27],a[72:75],a[56:59],v[24:27]\n"
WT"lgkmcnt(1)\n"
"v_add_f32 v243,v243,v247\n"
"v_add_f32 v251,v251,v255\n"
MF"v[0:3],a[76:79],a[12:15],v[0:3]\n"
EX"v62,v62\n"
MF"v[8:11],a[76:79],a[28:31],v[8:11]\n"
RD"a[92:95],v226 offset:7168\n"
PK"v77,v58,v59\n"
MF"v[16:19],a[76:79],a[44:47],v[16:19]\n"
EX"v63,v63\n"
MF"v[24:27],a[76:79],a[60:63],v[24:27]\n"
PK"v240,v240,v240\n"
"v_lshlrev_b32 v240,16,v240\n"
MF"v[4:7],a[80:83],a[0:3],v[212:215]\n"
EX"v0,v0\n"
MF"v[12:15],a[80:83],a[16:19],0\n"
RD"a[96:99],v226 offset:8192\n"
PK"v241,v241,v241\n"
MF"v[20:23],a[80:83],a[32:35],0\n"
EX"v1,v1\n"
MF"v[28:31],a[80:83],a[48:51],0\n"
"v_lshlrev_b32 v241,16,v241\n"
PK"v78,v60,v61\n"
MF"v[4:7],a[84:87],a[4:7],v[4:7]\n"
EX"v2,v2\n"
PK"v242,v242,v242\n"
MF"v[12:15],a[84:87],a[20:23],v[12:15]\n"
RD"a[100:103],v226 offset:9216\n"
"v_lshlrev_b32 v242,16,v242\n"
MF"v[20:23],a[84:87],a[36:39],v[20:23]\n"
EX"v3,v3\n"
PK"v243,v243,v243\n"
MF"v[28:31],a[84:87],a[52:55],v[28:31]\n"
WT"lgkmcnt(2)\n"
"v_lshlrev_b32 v243,16,v243\n"
PK"v79,v62,v63\n"
MF"v[4:7],a[88:91],a[8:11],v[4:7]\n"
EX"v8,v8\n"
MU"v244,0xbfb8aa3b,v240\n"
MF"v[12:15],a[88:91],a[24:27],v[12:15]\n"
EX"v244,v244\n"
MU"v245,0xbfb8aa3b,v241\n"
MF"v[20:23],a[88:91],a[40:43],v[20:23]\n"
EX"v245,v245\n"
MU"v246,0xbfb8aa3b,v242\n"
MF"v[28:31],a[88:91],a[56:59],v[28:31]\n"
RD"a[104:107],v226 offset:10240\n"
MU"v247,0xbfb8aa3b,v243\n"
PK"v0,v0,v1\n"
MF"v[4:7],a[92:95],a[12:15],v[4:7]\n"
EX"v246,v246\n"
EX"v9,v9\n"
MF"v[12:15],a[92:95],a[28:31],v[12:15]\n"
EX"v10,v10\n"
PK"v248,v248,v248\n"
MF"v[20:23],a[92:95],a[44:47],v[20:23]\n"
EX"v247,v247\n"
"v_lshlrev_b32 v248,16,v248\n"
MF"v[28:31],a[92:95],a[60:63],v[28:31]\n"
WT"lgkmcnt(1)\n"
RD"a[108:111],v226 offset:11264\n"
PK"v1,v2,v3\n"
MF"v[32:35],a[96:99],a[0:3],v[216:219]\n"
EX"v11,v11\n"
PK"v249,v249,v249\n"
MF"v[40:43],a[96:99],a[16:19],0\n"
EX"v16,v16\n"
"v_lshlrev_b32 v249,16,v249\n"
MF"v[48:51],a[96:99],a[32:35],0\n"
RD"a[112:115],v226 offset:12288\n"
PK"v250,v250,v250\n"
MF"v[56:59],a[96:99],a[48:51],0\n"
EX"v17,v17\n"
PK"v8,v8,v9\n"
MF"v[32:35],a[100:103],a[4:7],v[32:35]\n"
EX"v18,v18\n"
"v_lshlrev_b32 v250,16,v250\n"
MF"v[40:43],a[100:103],a[20:23],v[40:43]\n"
RD"a[116:119],v226 offset:13312\n"
PK"v251,v251,v251\n"
MF"v[48:51],a[100:103],a[36:39],v[48:51]\n"
EX"v19,v19\n"
MF"v[56:59],a[100:103],a[52:55],v[56:59]\n"
WT"lgkmcnt(2)\n"
"v_lshlrev_b32 v251,16,v251\n"
PK"v9,v10,v11\n"
MF"v[32:35],a[104:107],a[8:11],v[32:35]\n"
EX"v24,v24\n"
"v_add_f32 v244,1.0,v244\n"
MF"v[40:43],a[104:107],a[24:27],v[40:43]\n"
"v_rcp_f32 v244,v244\n"
"v_add_f32 v245,1.0,v245\n"
MF"v[48:51],a[104:107],a[40:43],v[48:51]\n"
"v_rcp_f32 v245,v245\n"
"v_add_f32 v246,1.0,v246\n"
MF"v[56:59],a[104:107],a[56:59],v[56:59]\n"
"v_rcp_f32 v246,v246\n"
"v_add_f32 v247,1.0,v247\n"
MF"v[32:35],a[108:111],a[12:15],v[32:35]\n"
"v_rcp_f32 v247,v247\n"
EX"v25,v25\n"
MF"v[40:43],a[108:111],a[28:31],v[40:43]\n"
RD"a[120:123],v226 offset:14336\n"
RD"a[124:127],v226 offset:15360\n"
PK"v16,v16,v17\n"
MF"v[48:51],a[108:111],a[44:47],v[48:51]\n"
EX"v26,v26\n"
MF"v[56:59],a[108:111],a[60:63],v[56:59]\n"
WT"lgkmcnt(2)\n"
EX"v27,v27\n"
PK"v17,v18,v19\n"
MF"v[36:39],a[112:115],a[0:3],v[220:223]\n"
EX"v4,v4\n"
MU"v240,v240,v244\n"
MF"v[44:47],a[112:115],a[16:19],0\n"
RD"a[64:67],v227 offset:16384\n"
MU"v241,v241,v245\n"
MF"v[52:55],a[112:115],a[32:35],0\n"
EX"v5,v5\n"
MU"v242,v242,v246\n"
MF"v[60:63],a[112:115],a[48:51],0\n"
MU"v243,v243,v247\n"
PK"v24,v24,v25\n"
MF"v[36:39],a[116:119],a[4:7],v[36:39]\n"
EX"v6,v6\n"
PK"v240,v240,v240\n"
MF"v[44:47],a[116:119],a[20:23],v[44:47]\n"
RD"a[68:71],v227 offset:17408\n"
"v_lshlrev_b32 v240,16,v240\n"
MF"v[52:55],a[116:119],a[36:39],v[52:55]\n"
EX"v7,v7\n"
PK"v241,v241,v241\n"
MF"v[60:63],a[116:119],a[52:55],v[60:63]\n"
WT"lgkmcnt(2)\n"
"v_readlane_b32 s43,v228,s42\n"
"v_lshlrev_b32 v241,16,v241\n"
PK"v25,v26,v27\n"
MF"v[36:39],a[120:123],a[8:11],v[36:39]\n"
EX"v12,v12\n"
PK"v242,v242,v242\n"
MF"v[44:47],a[120:123],a[24:27],v[44:47]\n"
RD"a[72:75],v227 offset:18432\n"
"v_lshlrev_b32 v242,16,v242\n"
MF"v[52:55],a[120:123],a[40:43],v[52:55]\n"
EX"v13,v13\n"
PK"v243,v243,v243\n"
MF"v[60:63],a[120:123],a[56:59],v[60:63]\n"
"s_mov_b32 m0,s40\n"
"v_lshlrev_b32 v243,16,v243\n"
PK"v2,v4,v5\n"
MF"v[36:39],a[124:127],a[12:15],v[36:39]\n"
EX"v14,v14\n"
MU"v240,v240,v248\n"
MF"v[44:47],a[124:127],a[28:31],v[44:47]\n"
RD"a[76:79],v227 offset:19456\n"
"v_readlane_b32 s44,v229,s42\n"
MU"v241,v241,v249\n"
MF"v[52:55],a[124:127],a[44:47],v[52:55]\n"
EX"v15,v15\n"
MU"v242,v242,v250\n"
MF"v[60:63],a[124:127],a[60:63],v[60:63]\n"
WT"lgkmcnt(2)\n"
BL"v225,s[8:11],s43 offen offset:0 sc1 lds\n"
MU"v243,v243,v251\n"
MF"v[80:83],a[64:67],v[64:67],v[80:83]\n"
SA"s42,s42,1\n"
EX"v20,v20\n"
PK"v3,v6,v7\n"
MF"v[112:115],a[64:67],v[68:71],v[112:115]\n"
SA"s41,s41,32768\n"
RD"a[80:83],v227 offset:20480\n"
PK"v240,v240,v241\n"
PK"v241,v242,v243\n"
MF"v[144:147],a[64:67],v[72:75],v[144:147]\n"
"s_cmp_eq_u32 s41,163840\n"
EX"v21,v21\n"
"v_lshrrev_b32 v239,9,v224\n"
MF"v[176:179],a[64:67],v[76:79],v[176:179]\n"
"s_cselect_b32 s41,0,s41\n"
"v_lshlrev_b32 v239,8,v239\n"
"v_add_u32 v226,s41,v224\n"
PK"v10,v12,v13\n"
MF"v[84:87],a[68:71],v[64:67],v[84:87]\n"
EX"v22,v22\n"
"v_bfe_u32 v237,v224,8,1\n"
MF"v[116:119],a[68:71],v[68:71],v[116:119]\n"
RD"a[84:87],v227 offset:21504\n"
"v_lshl_add_u32 v239,v237,3,v239\n"
MF"v[148:151],a[68:71],v[72:75],v[148:151]\n"
EX"v23,v23\n"
"v_and_b32 v237,0x80,v224\n"
MF"v[180:183],a[68:71],v[76:79],v[180:183]\n"
WT"lgkmcnt(2)\n"
BL"v225,s[8:11],s43 offen offset:1024 sc1 lds\n"
"v_lshl_add_u32 v239,v237,23,v239\n"
MF"v[88:91],a[72:75],v[64:67],v[88:91]\n"
EX"v28,v28\n"
PK"v11,v14,v15\n"
MF"v[120:123],a[72:75],v[68:71],v[120:123]\n"
RD"a[88:91],v227 offset:22528\n"
"v_and_b32 v237,0x70,v224\n"
"v_add_u32 v239,v239,v237\n"
MF"v[152:155],a[72:75],v[72:75],v[152:155]\n"
"buffer_store_dwordx2 v[240:241],v239,s[8:11],s93 offen sc1\n"
MF"v[184:187],a[72:75],v[76:79],v[184:187]\n"
EX"v29,v29\n"
PK"v18,v20,v21\n"
MF"v[92:95],a[76:79],v[64:67],v[92:95]\n"
EX"v30,v30\n"
MF"v[124:127],a[76:79],v[68:71],v[124:127]\n"
RD"a[92:95],v227 offset:23552\n"
PK"v19,v22,v23\n"
MF"v[156:159],a[76:79],v[72:75],v[156:159]\n"
EX"v31,v31\n"
MF"v[188:191],a[76:79],v[76:79],v[188:191]\n"
WT"lgkmcnt(2)\n"
BL"v225,s[8:11],s43 offen offset:2048 sc1 lds\n"
MF"v[96:99],a[80:83],v[64:67],v[96:99]\n"
EX"v32,v32\n"
MF"v[128:131],a[80:83],v[68:71],v[128:131]\n"
RD"a[96:99],v227 offset:24576\n"
PK"v26,v28,v29\n"
MF"v[160:163],a[80:83],v[72:75],v[160:163]\n"
EX"v33,v33\n"
MF"v[192:195],a[80:83],v[76:79],v[192:195]\n"
PK"v27,v30,v31\n"
MF"v[100:103],a[84:87],v[64:67],v[100:103]\n"
EX"v34,v34\n"
MF"v[132:135],a[84:87],v[68:71],v[132:135]\n"
RD"a[100:103],v227 offset:25600\n"
MF"v[164:167],a[84:87],v[72:75],v[164:167]\n"
EX"v35,v35\n"
MF"v[196:199],a[84:87],v[76:79],v[196:199]\n"
WT"lgkmcnt(2)\n"
BL"v225,s[8:11],s43 offen offset:3072 sc1 lds\n"
MF"v[104:107],a[88:91],v[64:67],v[104:107]\n"
EX"v40,v40\n"
MF"v[136:139],a[88:91],v[68:71],v[136:139]\n"
RD"a[104:107],v227 offset:26624\n"
MF"v[168:171],a[88:91],v[72:75],v[168:171]\n"
EX"v41,v41\n"
MF"v[200:203],a[88:91],v[76:79],v[200:203]\n"
SA"m0,s40,16384\n"
MF"v[108:111],a[92:95],v[64:67],v[108:111]\n"
EX"v42,v42\n"
MF"v[140:143],a[92:95],v[68:71],v[140:143]\n"
RD"a[108:111],v227 offset:27648\n"
MF"v[172:175],a[92:95],v[72:75],v[172:175]\n"
EX"v43,v43\n"
MF"v[204:207],a[92:95],v[76:79],v[204:207]\n"
BL"v225,s[8:11],s44 offen offset:0 sc1 lds\n"
MF"a[132:135],a[128:131],v[64:67],a[132:135]\n"
SA"s40,s40,32768\n"
EX"v48,v48\n"
PK"v64,v32,v33\n"
MF"a[136:139],a[128:131],v[68:71],a[136:139]\n"
"s_cmp_eq_u32 s40,s46\n"
RD"a[112:115],v227 offset:28672\n"
PK"v65,v34,v35\n"
MF"a[140:143],a[128:131],v[72:75],a[140:143]\n"
"s_cselect_b32 s40,s47,s40\n"
EX"v49,v49\n"
MF"a[144:147],a[128:131],v[76:79],a[144:147]\n"
WT"lgkmcnt(2)\n"
PK"v68,v40,v41\n"
PK"v69,v42,v43\n"
MF"v[80:83],a[96:99],v[0:3],v[80:83]\n"
EX"v50,v50\n"
MF"v[112:115],a[96:99],v[8:11],v[112:115]\n"
RD"a[116:119],v227 offset:29696\n"
PK"v72,v48,v49\n"
MF"v[144:147],a[96:99],v[16:19],v[144:147]\n"
EX"v51,v51\n"
MF"v[176:179],a[96:99],v[24:27],v[176:179]\n"
BL"v225,s[8:11],s44 offen offset:1024 sc1 lds\n"
MF"v[84:87],a[100:103],v[0:3],v[84:87]\n"
EX"v36,v36\n"
MF"v[116:119],a[100:103],v[8:11],v[116:119]\n"
RD"a[120:123],v227 offset:30720\n"
PK"v73,v50,v51\n"
MF"v[148:151],a[100:103],v[16:19],v[148:151]\n"
EX"v37,v37\n"
MF"v[180:183],a[100:103],v[24:27],v[180:183]\n"
PK"v66,v36,v37\n"
MF"v[88:91],a[104:107],v[0:3],v[88:91]\n"
EX"v38,v38\n"
MF"v[120:123],a[104:107],v[8:11],v[120:123]\n"
RD"a[124:127],v227 offset:31744\n"
MF"v[152:155],a[104:107],v[16:19],v[152:155]\n"
EX"v39,v39\n"
MF"v[184:187],a[104:107],v[24:27],v[184:187]\n"
WT"lgkmcnt(2)\n"
BL"v225,s[8:11],s44 offen offset:2048 sc1 lds\n"
MF"v[92:95],a[108:111],v[0:3],v[92:95]\n"
EX"v44,v44\n"
MF"v[124:127],a[108:111],v[8:11],v[124:127]\n"
PK"v67,v38,v39\n"
MF"v[156:159],a[108:111],v[16:19],v[156:159]\n"
EX"v45,v45\n"
MF"v[188:191],a[108:111],v[24:27],v[188:191]\n"
PK"v70,v44,v45\n"
MF"v[96:99],a[112:115],v[0:3],v[96:99]\n"
EX"v46,v46\n"
MF"v[128:131],a[112:115],v[8:11],v[128:131]\n"
MF"v[160:163],a[112:115],v[16:19],v[160:163]\n"
EX"v47,v47\n"
MF"v[192:195],a[112:115],v[24:27],v[192:195]\n"
BL"v225,s[8:11],s44 offen offset:3072 sc1 lds\n"
MF"v[100:103],a[116:119],v[0:3],v[100:103]\n"
WT"lgkmcnt(0)\n"
"s_barrier\n"
MF"v[132:135],a[116:119],v[8:11],v[132:135]\n"
RD"a[64:67],v226 offset:0\n"
PK"v71,v46,v47\n"
MF"v[164:167],a[116:119],v[16:19],v[164:167]\n"
EX"v52,v52\n"
MF"v[196:199],a[116:119],v[24:27],v[196:199]\n"
RD"a[68:71],v226 offset:1024\n"
MF"v[104:107],a[120:123],v[0:3],v[104:107]\n"
EX"v53,v53\n"
MF"v[136:139],a[120:123],v[8:11],v[136:139]\n"
RD"a[72:75],v226 offset:2048\n"
PK"v74,v52,v53\n"
MF"v[168:171],a[120:123],v[16:19],v[168:171]\n"
EX"v54,v54\n"
MF"v[200:203],a[120:123],v[24:27],v[200:203]\n"
MF"v[108:111],a[124:127],v[0:3],v[108:111]\n"
WT"vmcnt(6)\n"
"buffer_store_dwordx2 v[232:233],v230,s[8:11],s94 offen sc1\n"
MF"v[140:143],a[124:127],v[8:11],v[140:143]\n"
EX"v55,v55\n"
MF"v[172:175],a[124:127],v[16:19],v[172:175]\n"
RD"a[76:79],v226 offset:3072\n"
PK"v75,v54,v55\n"
MF"v[204:207],a[124:127],v[24:27],v[204:207]\n"
RD"a[80:83],v226 offset:4096\n"
MF"a[132:135],a[128:131],v[0:3],a[132:135]\n"
MF"a[136:139],a[128:131],v[8:11],a[136:139]\n"
RD"a[84:87],v226 offset:5120\n"
MF"a[140:143],a[128:131],v[16:19],a[140:143]\n"
MF"a[144:147],a[128:131],v[24:27],a[144:147]\n"
"s_branch LDB_%=\n"
"LMD_%=:\n"
"s_cmp_eq_u32 s34,6\n"
"s_cbranch_scc1 LME_%=\n"
WT"lgkmcnt(9)\n"
MF"v[0:3],a[64:67],a[0:3],v[208:211]\n"
EX"v56,v56\n"
MF"v[8:11],a[64:67],a[16:19],0\n"
"v_lshrrev_b32 v239,9,v224\n"
"v_lshlrev_b32 v239,8,v239\n"
MF"v[16:19],a[64:67],a[32:35],0\n"
EX"v57,v57\n"
MF"v[24:27],a[64:67],a[48:51],0\n"
WT"lgkmcnt(1)\n"
"v_bfe_u32 v237,v224,8,1\n"
"v_lshl_add_u32 v239,v237,3,v239\n"
MF"v[0:3],a[68:71],a[4:7],v[0:3]\n"
EX"v58,v58\n"
MF"v[8:11],a[68:71],a[20:23],v[8:11]\n"
"v_and_b32 v237,0x80,v224\n"
PK"v76,v56,v57\n"
MF"v[16:19],a[68:71],a[36:39],v[16:19]\n"
EX"v59,v59\n"
MF"v[24:27],a[68:71],a[52:55],v[24:27]\n"
"v_lshl_add_u32 v239,v237,23,v239\n"
"v_not_b32 v236,v234\n"
MF"v[0:3],a[72:75],a[8:11],v[0:3]\n"
EX"v60,v60\n"
MF"v[8:11],a[72:75],a[24:27],v[8:11]\n"
SA"s53,s93,4292282368\n"
RD"a[88:91],v226 offset:6144\n"
"v_mov_b32 v227,v226\n"
MF"v[16:19],a[72:75],a[40:43],v[16:19]\n"
EX"v61,v61\n"
MF"v[24:27],a[72:75],a[56:59],v[24:27]\n"
"v_and_b32 v237,15,v236\n"
"v_bfe_u32 v236,v236,4,7\n"
MF"v[0:3],a[76:79],a[12:15],v[0:3]\n"
EX"v62,v62\n"
MF"v[8:11],a[76:79],a[28:31],v[8:11]\n"
RD"a[92:95],v226 offset:7168\n"
"v_mul_u32_u24 v236,20480,v236\n"
MF"v[16:19],a[76:79],a[44:47],v[16:19]\n"
EX"v63,v63\n"
MF"v[24:27],a[76:79],a[60:63],v[24:27]\n"
PK"v77,v58,v59\n"
"v_lshl_add_u32 v236,v237,4,v236\n"
MF"v[4:7],a[80:83],a[0:3],v[212:215]\n"
EX"v0,v0\n"
"v_add_u32 v236,v236,v239\n"
MF"v[12:15],a[80:83],a[16:19],0\n"
RD"a[96:99],v226 offset:8192\n"
"v_and_b32 v237,0x70,v224\n"
MF"v[20:23],a[80:83],a[32:35],0\n"
EX"v1,v1\n"
MF"v[28:31],a[80:83],a[48:51],0\n"
WT"lgkmcnt(2)\n"
"v_add_u32 v239,v239,v237\n"
PK"v78,v60,v61\n"
MF"v[4:7],a[84:87],a[4:7],v[4:7]\n"
EX"v2,v2\n"
"v_add_f32 v240,v240,v244\n"
MF"v[12:15],a[84:87],a[20:23],v[12:15]\n"
RD"a[100:103],v226 offset:9216\n"
"v_add_f32 v248,v248,v252\n"
"v_add_f32 v241,v241,v245\n"
MF"v[20:23],a[84:87],a[36:39],v[20:23]\n"
EX"v3,v3\n"
"v_add_f32 v249,v249,v253\n"
MF"v[28:31],a[84:87],a[52:55],v[28:31]\n"
"v_add_f32 v242,v242,v246\n"
PK"v79,v62,v63\n"
MF"v[4:7],a[88:91],a[8:11],v[4:7]\n"
EX"v8,v8\n"
"v_add_f32 v250,v250,v254\n"
MF"v[12:15],a[88:91],a[24:27],v[12:15]\n"
RD"a[104:107],v226 offset:10240\n"
"v_add_f32 v243,v243,v247\n"
"v_add_f32 v251,v251,v255\n"
MF"v[20:23],a[88:91],a[40:43],v[20:23]\n"
"buffer_load_dwordx2 v[254:255],v236,s[8:11],s53 offen sc1\n"
"v_ashrrev_i32 v235,31,v234\n"
MF"v[28:31],a[88:91],a[56:59],v[28:31]\n"
WT"lgkmcnt(2)\n"
EX"v9,v9\n"
"v_not_b32 v235,v235\n"
MF"v[4:7],a[92:95],a[12:15],v[4:7]\n"
EX"v10,v10\n"
PK"v0,v0,v1\n"
MF"v[12:15],a[92:95],a[28:31],v[12:15]\n"
RD"a[108:111],v226 offset:11264\n"
"v_or_b32 v235,0x80000000,v235\n"
"v_xor_b32 v235,v235,v234\n"
MF"v[20:23],a[92:95],a[44:47],v[20:23]\n"
EX"v11,v11\n"
"v_and_b32 v235,0xffff0000,v235\n"
MF"v[28:31],a[92:95],a[60:63],v[28:31]\n"
MU"v235,0xbfb8aa3b,v235\n"
PK"v1,v2,v3\n"
EX"v235,v235\n"
MF"v[32:35],a[96:99],a[0:3],v[216:219]\n"
EX"v16,v16\n"
MF"v[40:43],a[96:99],a[16:19],0\n"
RD"a[112:115],v226 offset:12288\n"
"v_add_f32 v235,1.0,v235\n"
"v_rcp_f32 v235,v235\n"
MF"v[48:51],a[96:99],a[32:35],0\n"
EX"v17,v17\n"
MF"v[56:59],a[96:99],a[48:51],0\n"
WT"lgkmcnt(2)\n"
PK"v235,v235,v235\n"
PK"v8,v8,v9\n"
MF"v[32:35],a[100:103],a[4:7],v[32:35]\n"
EX"v18,v18\n"
"v_lshlrev_b32 v235,16,v235\n"
MF"v[40:43],a[100:103],a[20:23],v[40:43]\n"
RD"a[116:119],v226 offset:13312\n"
"v_add_f32 v240,v240,v248\n"
MF"v[48:51],a[100:103],a[36:39],v[48:51]\n"
EX"v19,v19\n"
"v_add_f32 v241,v241,v249\n"
MF"v[56:59],a[100:103],a[52:55],v[56:59]\n"
"v_add_f32 v242,v242,v250\n"
PK"v9,v10,v11\n"
MF"v[32:35],a[104:107],a[8:11],v[32:35]\n"
EX"v24,v24\n"
"v_add_f32 v243,v243,v251\n"
MF"v[40:43],a[104:107],a[24:27],v[40:43]\n"
RD"a[120:123],v226 offset:14336\n"
PK"v240,v240,v240\n"
MF"v[48:51],a[104:107],a[40:43],v[48:51]\n"
EX"v25,v25\n"
"v_lshlrev_b32 v240,16,v240\n"
MF"v[56:59],a[104:107],a[56:59],v[56:59]\n"
WT"lgkmcnt(2)\n"
MU"v240,v240,v235\n"
PK"v16,v16,v17\n"
MF"v[32:35],a[108:111],a[12:15],v[32:35]\n"
EX"v26,v26\n"
PK"v240,v240,v240\n"
MF"v[40:43],a[108:111],a[28:31],v[40:43]\n"
RD"a[124:127],v226 offset:15360\n"
"v_lshlrev_b32 v240,16,v240\n"
MF"v[48:51],a[108:111],a[44:47],v[48:51]\n"
EX"v27,v27\n"
PK"v241,v241,v241\n"
MF"v[56:59],a[108:111],a[60:63],v[56:59]\n"
"v_lshlrev_b32 v241,16,v241\n"
PK"v17,v18,v19\n"
MF"v[36:39],a[112:115],a[0:3],v[220:223]\n"
EX"v4,v4\n"
MU"v241,v241,v235\n"
MF"v[44:47],a[112:115],a[16:19],0\n"
RD"a[64:67],v227 offset:16384\n"
PK"v241,v241,v241\n"
MF"v[52:55],a[112:115],a[32:35],0\n"
EX"v5,v5\n"
"v_lshlrev_b32 v241,16,v241\n"
MF"v[60:63],a[112:115],a[48:51],0\n"
WT"lgkmcnt(2)\n"
PK"v242,v242,v242\n"
PK"v24,v24,v25\n"
MF"v[36:39],a[116:119],a[4:7],v[36:39]\n"
EX"v6,v6\n"
"v_lshlrev_b32 v242,16,v242\n"
MF"v[44:47],a[116:119],a[20:23],v[44:47]\n"
RD"a[68:71],v227 offset:17408\n"
MU"v242,v242,v235\n"
MF"v[52:55],a[116:119],a[36:39],v[52:55]\n"
EX"v7,v7\n"
PK"v242,v242,v242\n"
MF"v[60:63],a[116:119],a[52:55],v[60:63]\n"
"v_readlane_b32 s43,v228,s42\n"
"v_lshlrev_b32 v242,16,v242\n"
PK"v25,v26,v27\n"
MF"v[36:39],a[120:123],a[8:11],v[36:39]\n"
EX"v12,v12\n"
PK"v243,v243,v243\n"
MF"v[44:47],a[120:123],a[24:27],v[44:47]\n"
RD"a[72:75],v227 offset:18432\n"
"v_lshlrev_b32 v243,16,v243\n"
MF"v[52:55],a[120:123],a[40:43],v[52:55]\n"
EX"v13,v13\n"
MU"v243,v243,v235\n"
MF"v[60:63],a[120:123],a[56:59],v[60:63]\n"
WT"lgkmcnt(2)\n"
PK"v243,v243,v243\n"
PK"v2,v4,v5\n"
MF"v[36:39],a[124:127],a[12:15],v[36:39]\n"
"s_mov_b32 m0,s40\n"
EX"v14,v14\n"
"v_lshlrev_b32 v243,16,v243\n"
MF"v[44:47],a[124:127],a[28:31],v[44:47]\n"
RD"a[76:79],v227 offset:19456\n"
"v_readlane_b32 s44,v229,s42\n"
MF"v[52:55],a[124:127],a[44:47],v[52:55]\n"
EX"v15,v15\n"
MF"v[60:63],a[124:127],a[60:63],v[60:63]\n"
SA"s42,s42,1\n"
BL"v225,s[8:11],s43 offen offset:0 sc1 lds\n"
PK"v3,v6,v7\n"
MF"v[80:83],a[64:67],v[64:67],v[80:83]\n"
SA"s41,s41,32768\n"
EX"v20,v20\n"
MF"v[112:115],a[64:67],v[68:71],v[112:115]\n"
"s_cmp_eq_u32 s41,163840\n"
RD"a[80:83],v227 offset:20480\n"
PK"v10,v12,v13\n"
MF"v[144:147],a[64:67],v[72:75],v[144:147]\n"
"s_cselect_b32 s41,0,s41\n"
EX"v21,v21\n"
MF"v[176:179],a[64:67],v[76:79],v[176:179]\n"
WT"lgkmcnt(2)\n"
"v_add_u32 v226,s41,v224\n"
PK"v11,v14,v15\n"
MF"v[84:87],a[68:71],v[64:67],v[84:87]\n"
EX"v22,v22\n"
MF"v[116:119],a[68:71],v[68:71],v[116:119]\n"
RD"a[84:87],v227 offset:21504\n"
PK"v18,v20,v21\n"
MF"v[148:151],a[68:71],v[72:75],v[148:151]\n"
EX"v23,v23\n"
MF"v[180:183],a[68:71],v[76:79],v[180:183]\n"
BL"v225,s[8:11],s43 offen offset:1024 sc1 lds\n"
MF"v[88:91],a[72:75],v[64:67],v[88:91]\n"
EX"v28,v28\n"
MF"v[120:123],a[72:75],v[68:71],v[120:123]\n"
RD"a[88:91],v227 offset:22528\n"
PK"v19,v22,v23\n"
MF"v[152:155],a[72:75],v[72:75],v[152:155]\n"
EX"v29,v29\n"
MF"v[184:187],a[72:75],v[76:79],v[184:187]\n"
WT"lgkmcnt(2)\n"
WT"vmcnt(2)\n"
"v_lshlrev_b32 v244,16,v254\n"
"v_and_b32 v245,0xffff0000,v254\n"
MF"v[92:95],a[76:79],v[64:67],v[92:95]\n"
EX"v30,v30\n"
"v_lshlrev_b32 v246,16,v255\n"
MF"v[124:127],a[76:79],v[68:71],v[124:127]\n"
RD"a[92:95],v227 offset:23552\n"
"v_and_b32 v247,0xffff0000,v255\n"
"v_add_f32 v240,v240,v244\n"
MF"v[156:159],a[76:79],v[72:75],v[156:159]\n"
EX"v31,v31\n"
"v_add_f32 v241,v241,v245\n"
MF"v[188:191],a[76:79],v[76:79],v[188:191]\n"
BL"v225,s[8:11],s43 offen offset:2048 sc1 lds\n"
"v_add_f32 v242,v242,v246\n"
MF"v[96:99],a[80:83],v[64:67],v[96:99]\n"
EX"v32,v32\n"
"v_add_f32 v243,v243,v247\n"
MF"v[128:131],a[80:83],v[68:71],v[128:131]\n"
RD"a[96:99],v227 offset:24576\n"
PK"v240,v240,v241\n"
PK"v241,v242,v243\n"
MF"v[160:163],a[80:83],v[72:75],v[160:163]\n"
"buffer_store_dwordx2 v[240:241],v239,s[8:11],s93 offen sc1\n"
MF"v[192:195],a[80:83],v[76:79],v[192:195]\n"
WT"lgkmcnt(2)\n"
EX"v33,v33\n"
PK"v26,v28,v29\n"
MF"v[100:103],a[84:87],v[64:67],v[100:103]\n"
EX"v34,v34\n"
MF"v[132:135],a[84:87],v[68:71],v[132:135]\n"
RD"a[100:103],v227 offset:25600\n"
PK"v27,v30,v31\n"
MF"v[164:167],a[84:87],v[72:75],v[164:167]\n"
EX"v35,v35\n"
MF"v[196:199],a[84:87],v[76:79],v[196:199]\n"
BL"v225,s[8:11],s43 offen offset:3072 sc1 lds\n"
MF"v[104:107],a[88:91],v[64:67],v[104:107]\n"
EX"v40,v40\n"
MF"v[136:139],a[88:91],v[68:71],v[136:139]\n"
RD"a[104:107],v227 offset:26624\n"
MF"v[168:171],a[88:91],v[72:75],v[168:171]\n"
EX"v41,v41\n"
MF"v[200:203],a[88:91],v[76:79],v[200:203]\n"
WT"lgkmcnt(2)\n"
SA"m0,s40,16384\n"
MF"v[108:111],a[92:95],v[64:67],v[108:111]\n"
EX"v42,v42\n"
MF"v[140:143],a[92:95],v[68:71],v[140:143]\n"
RD"a[108:111],v227 offset:27648\n"
MF"v[172:175],a[92:95],v[72:75],v[172:175]\n"
EX"v43,v43\n"
MF"v[204:207],a[92:95],v[76:79],v[204:207]\n"
BL"v225,s[8:11],s44 offen offset:0 sc1 lds\n"
MF"a[132:135],a[128:131],v[64:67],a[132:135]\n"
SA"s40,s40,32768\n"
EX"v48,v48\n"
PK"v64,v32,v33\n"
MF"a[136:139],a[128:131],v[68:71],a[136:139]\n"
"s_cmp_eq_u32 s40,s46\n"
RD"a[112:115],v227 offset:28672\n"
PK"v65,v34,v35\n"
MF"a[140:143],a[128:131],v[72:75],a[140:143]\n"
"s_cselect_b32 s40,s47,s40\n"
EX"v49,v49\n"
MF"a[144:147],a[128:131],v[76:79],a[144:147]\n"
PK"v68,v40,v41\n"
PK"v69,v42,v43\n"
MF"v[80:83],a[96:99],v[0:3],v[80:83]\n"
EX"v50,v50\n"
MF"v[112:115],a[96:99],v[8:11],v[112:115]\n"
RD"a[116:119],v227 offset:29696\n"
PK"v72,v48,v49\n"
MF"v[144:147],a[96:99],v[16:19],v[144:147]\n"
EX"v51,v51\n"
MF"v[176:179],a[96:99],v[24:27],v[176:179]\n"
WT"lgkmcnt(2)\n"
BL"v225,s[8:11],s44 offen offset:1024 sc1 lds\n"
MF"v[84:87],a[100:103],v[0:3],v[84:87]\n"
EX"v36,v36\n"
MF"v[116:119],a[100:103],v[8:11],v[116:119]\n"
RD"a[120:123],v227 offset:30720\n"
PK"v73,v50,v51\n"
MF"v[148:151],a[100:103],v[16:19],v[148:151]\n"
EX"v37,v37\n"
MF"v[180:183],a[100:103],v[24:27],v[180:183]\n"
PK"v66,v36,v37\n"
MF"v[88:91],a[104:107],v[0:3],v[88:91]\n"
EX"v38,v38\n"
MF"v[120:123],a[104:107],v[8:11],v[120:123]\n"
RD"a[124:127],v227 offset:31744\n"
MF"v[152:155],a[104:107],v[16:19],v[152:155]\n"
EX"v39,v39\n"
MF"v[184:187],a[104:107],v[24:27],v[184:187]\n"
BL"v225,s[8:11],s44 offen offset:2048 sc1 lds\n"
MF"v[92:95],a[108:111],v[0:3],v[92:95]\n"
EX"v44,v44\n"
MF"v[124:127],a[108:111],v[8:11],v[124:127]\n"
PK"v67,v38,v39\n"
MF"v[156:159],a[108:111],v[16:19],v[156:159]\n"
EX"v45,v45\n"
MF"v[188:191],a[108:111],v[24:27],v[188:191]\n"
WT"lgkmcnt(1)\n"
PK"v70,v44,v45\n"
MF"v[96:99],a[112:115],v[0:3],v[96:99]\n"
EX"v46,v46\n"
MF"v[128:131],a[112:115],v[8:11],v[128:131]\n"
MF"v[160:163],a[112:115],v[16:19],v[160:163]\n"
EX"v47,v47\n"
MF"v[192:195],a[112:115],v[24:27],v[192:195]\n"
BL"v225,s[8:11],s44 offen offset:3072 sc1 lds\n"
MF"v[100:103],a[116:119],v[0:3],v[100:103]\n"
WT"lgkmcnt(0)\n"
"s_barrier\n"
MF"v[132:135],a[116:119],v[8:11],v[132:135]\n"
RD"a[64:67],v226 offset:0\n"
PK"v71,v46,v47\n"
MF"v[164:167],a[116:119],v[16:19],v[164:167]\n"
EX"v52,v52\n"
MF"v[196:199],a[116:119],v[24:27],v[196:199]\n"
RD"a[68:71],v226 offset:1024\n"
MF"v[104:107],a[120:123],v[0:3],v[104:107]\n"
EX"v53,v53\n"
MF"v[136:139],a[120:123],v[8:11],v[136:139]\n"
RD"a[72:75],v226 offset:2048\n"
PK"v74,v52,v53\n"
MF"v[168:171],a[120:123],v[16:19],v[168:171]\n"
EX"v54,v54\n"
MF"v[200:203],a[120:123],v[24:27],v[200:203]\n"
MF"v[108:111],a[124:127],v[0:3],v[108:111]\n"
WT"vmcnt(5)\n"
"buffer_store_dwordx2 v[232:233],v230,s[8:11],s94 offen sc1\n"
MF"v[140:143],a[124:127],v[8:11],v[140:143]\n"
EX"v55,v55\n"
MF"v[172:175],a[124:127],v[16:19],v[172:175]\n"
RD"a[76:79],v226 offset:3072\n"
PK"v75,v54,v55\n"
MF"v[204:207],a[124:127],v[24:27],v[204:207]\n"
RD"a[80:83],v226 offset:4096\n"
MF"a[132:135],a[128:131],v[0:3],a[132:135]\n"
MF"a[136:139],a[128:131],v[8:11],a[136:139]\n"
RD"a[84:87],v226 offset:5120\n"
MF"a[140:143],a[128:131],v[16:19],a[140:143]\n"
MF"a[144:147],a[128:131],v[24:27],a[144:147]\n"
"s_branch LDB_%=\n"
"LME_%=:\n"
WT"lgkmcnt(9)\n"
MF"v[0:3],a[64:67],a[0:3],0\n"
EX"v56,v56\n"
MF"v[8:11],a[64:67],a[16:19],0\n"
"v_lshrrev_b32 v239,9,v224\n"
"v_lshlrev_b32 v239,8,v239\n"
MF"v[16:19],a[64:67],a[32:35],0\n"
EX"v57,v57\n"
MF"v[24:27],a[64:67],a[48:51],0\n"
WT"lgkmcnt(1)\n"
"v_bfe_u32 v237,v224,8,1\n"
"v_lshl_add_u32 v239,v237,3,v239\n"
MF"v[0:3],a[68:71],a[4:7],v[0:3]\n"
EX"v58,v58\n"
MF"v[8:11],a[68:71],a[20:23],v[8:11]\n"
"v_and_b32 v237,0x80,v224\n"
PK"v76,v56,v57\n"
MF"v[16:19],a[68:71],a[36:39],v[16:19]\n"
EX"v59,v59\n"
MF"v[24:27],a[68:71],a[52:55],v[24:27]\n"
"v_lshl_add_u32 v239,v237,23,v239\n"
"v_not_b32 v236,v234\n"
MF"v[0:3],a[72:75],a[8:11],v[0:3]\n"
EX"v60,v60\n"
MF"v[8:11],a[72:75],a[24:27],v[8:11]\n"
SA"s53,s93,4292282368\n"
RD"a[88:91],v226 offset:6144\n"
"v_mov_b32 v227,v226\n"
MF"v[16:19],a[72:75],a[40:43],v[16:19]\n"
EX"v61,v61\n"
MF"v[24:27],a[72:75],a[56:59],v[24:27]\n"
"v_and_b32 v237,15,v236\n"
"v_bfe_u32 v236,v236,4,7\n"
MF"v[0:3],a[76:79],a[12:15],v[0:3]\n"
EX"v62,v62\n"
MF"v[8:11],a[76:79],a[28:31],v[8:11]\n"
RD"a[92:95],v226 offset:7168\n"
"v_mul_u32_u24 v236,20480,v236\n"
MF"v[16:19],a[76:79],a[44:47],v[16:19]\n"
EX"v63,v63\n"
MF"v[24:27],a[76:79],a[60:63],v[24:27]\n"
PK"v77,v58,v59\n"
"v_lshl_add_u32 v236,v237,4,v236\n"
MF"v[4:7],a[80:83],a[0:3],0\n"
EX"v0,v0\n"
"v_add_u32 v236,v236,v239\n"
MF"v[12:15],a[80:83],a[16:19],0\n"
RD"a[96:99],v226 offset:8192\n"
"v_and_b32 v237,0x70,v224\n"
MF"v[20:23],a[80:83],a[32:35],0\n"
EX"v1,v1\n"
MF"v[28:31],a[80:83],a[48:51],0\n"
WT"lgkmcnt(2)\n"
"v_add_u32 v239,v239,v237\n"
PK"v78,v60,v61\n"
MF"v[4:7],a[84:87],a[4:7],v[4:7]\n"
EX"v2,v2\n"
"v_add_f32 v240,v240,v244\n"
MF"v[12:15],a[84:87],a[20:23],v[12:15]\n"
RD"a[100:103],v226 offset:9216\n"
"v_add_f32 v248,v248,v252\n"
"v_add_f32 v241,v241,v245\n"
MF"v[20:23],a[84:87],a[36:39],v[20:23]\n"
EX"v3,v3\n"
"v_add_f32 v249,v249,v253\n"
MF"v[28:31],a[84:87],a[52:55],v[28:31]\n"
"v_add_f32 v242,v242,v246\n"
PK"v79,v62,v63\n"
MF"v[4:7],a[88:91],a[8:11],v[4:7]\n"
EX"v8,v8\n"
"v_add_f32 v250,v250,v254\n"
MF"v[12:15],a[88:91],a[24:27],v[12:15]\n"
RD"a[104:107],v226 offset:10240\n"
"v_add_f32 v243,v243,v247\n"
"v_add_f32 v251,v251,v255\n"
MF"v[20:23],a[88:91],a[40:43],v[20:23]\n"
"buffer_load_dwordx2 v[254:255],v236,s[8:11],s53 offen sc1\n"
"v_ashrrev_i32 v235,31,v234\n"
MF"v[28:31],a[88:91],a[56:59],v[28:31]\n"
WT"lgkmcnt(2)\n"
EX"v9,v9\n"
"v_not_b32 v235,v235\n"
MF"v[4:7],a[92:95],a[12:15],v[4:7]\n"
EX"v10,v10\n"
PK"v0,v0,v1\n"
MF"v[12:15],a[92:95],a[28:31],v[12:15]\n"
RD"a[108:111],v226 offset:11264\n"
"v_or_b32 v235,0x80000000,v235\n"
"v_xor_b32 v235,v235,v234\n"
MF"v[20:23],a[92:95],a[44:47],v[20:23]\n"
EX"v11,v11\n"
"v_and_b32 v235,0xffff0000,v235\n"
MF"v[28:31],a[92:95],a[60:63],v[28:31]\n"
MU"v235,0xbfb8aa3b,v235\n"
PK"v1,v2,v3\n"
EX"v235,v235\n"
MF"v[32:35],a[96:99],a[0:3],0\n"
EX"v16,v16\n"
MF"v[40:43],a[96:99],a[16:19],0\n"
RD"a[112:115],v226 offset:12288\n"
"v_add_f32 v235,1.0,v235\n"
"v_rcp_f32 v235,v235\n"
MF"v[48:51],a[96:99],a[32:35],0\n"
EX"v17,v17\n"
MF"v[56:59],a[96:99],a[48:51],0\n"
WT"lgkmcnt(2)\n"
PK"v235,v235,v235\n"
PK"v8,v8,v9\n"
MF"v[32:35],a[100:103],a[4:7],v[32:35]\n"
EX"v18,v18\n"
"v_lshlrev_b32 v235,16,v235\n"
MF"v[40:43],a[100:103],a[20:23],v[40:43]\n"
RD"a[116:119],v226 offset:13312\n"
"v_add_f32 v240,v240,v248\n"
MF"v[48:51],a[100:103],a[36:39],v[48:51]\n"
EX"v19,v19\n"
"v_add_f32 v241,v241,v249\n"
MF"v[56:59],a[100:103],a[52:55],v[56:59]\n"
"v_add_f32 v242,v242,v250\n"
PK"v9,v10,v11\n"
MF"v[32:35],a[104:107],a[8:11],v[32:35]\n"
EX"v24,v24\n"
"v_add_f32 v243,v243,v251\n"
MF"v[40:43],a[104:107],a[24:27],v[40:43]\n"
RD"a[120:123],v226 offset:14336\n"
PK"v240,v240,v240\n"
MF"v[48:51],a[104:107],a[40:43],v[48:51]\n"
EX"v25,v25\n"
"v_lshlrev_b32 v240,16,v240\n"
MF"v[56:59],a[104:107],a[56:59],v[56:59]\n"
WT"lgkmcnt(2)\n"
MU"v240,v240,v235\n"
PK"v16,v16,v17\n"
MF"v[32:35],a[108:111],a[12:15],v[32:35]\n"
EX"v26,v26\n"
PK"v240,v240,v240\n"
MF"v[40:43],a[108:111],a[28:31],v[40:43]\n"
RD"a[124:127],v226 offset:15360\n"
"v_lshlrev_b32 v240,16,v240\n"
MF"v[48:51],a[108:111],a[44:47],v[48:51]\n"
EX"v27,v27\n"
PK"v241,v241,v241\n"
MF"v[56:59],a[108:111],a[60:63],v[56:59]\n"
"v_lshlrev_b32 v241,16,v241\n"
PK"v17,v18,v19\n"
MF"v[36:39],a[112:115],a[0:3],0\n"
EX"v4,v4\n"
MU"v241,v241,v235\n"
MF"v[44:47],a[112:115],a[16:19],0\n"
RD"a[64:67],v227 offset:16384\n"
PK"v241,v241,v241\n"
MF"v[52:55],a[112:115],a[32:35],0\n"
EX"v5,v5\n"
"v_lshlrev_b32 v241,16,v241\n"
MF"v[60:63],a[112:115],a[48:51],0\n"
WT"lgkmcnt(2)\n"
PK"v242,v242,v242\n"
PK"v24,v24,v25\n"
MF"v[36:39],a[116:119],a[4:7],v[36:39]\n"
EX"v6,v6\n"
"v_lshlrev_b32 v242,16,v242\n"
MF"v[44:47],a[116:119],a[20:23],v[44:47]\n"
RD"a[68:71],v227 offset:17408\n"
MU"v242,v242,v235\n"
MF"v[52:55],a[116:119],a[36:39],v[52:55]\n"
EX"v7,v7\n"
PK"v242,v242,v242\n"
MF"v[60:63],a[116:119],a[52:55],v[60:63]\n"
"v_readlane_b32 s43,v228,s42\n"
"v_lshlrev_b32 v242,16,v242\n"
PK"v25,v26,v27\n"
MF"v[36:39],a[120:123],a[8:11],v[36:39]\n"
EX"v12,v12\n"
PK"v243,v243,v243\n"
MF"v[44:47],a[120:123],a[24:27],v[44:47]\n"
RD"a[72:75],v227 offset:18432\n"
"v_lshlrev_b32 v243,16,v243\n"
MF"v[52:55],a[120:123],a[40:43],v[52:55]\n"
EX"v13,v13\n"
MU"v243,v243,v235\n"
MF"v[60:63],a[120:123],a[56:59],v[60:63]\n"
WT"lgkmcnt(2)\n"
PK"v243,v243,v243\n"
PK"v2,v4,v5\n"
MF"v[36:39],a[124:127],a[12:15],v[36:39]\n"
"s_mov_b32 m0,s40\n"
EX"v14,v14\n"
"v_lshlrev_b32 v243,16,v243\n"
MF"v[44:47],a[124:127],a[28:31],v[44:47]\n"
RD"a[76:79],v227 offset:19456\n"
"v_readlane_b32 s44,v229,s42\n"
MF"v[52:55],a[124:127],a[44:47],v[52:55]\n"
EX"v15,v15\n"
MF"v[60:63],a[124:127],a[60:63],v[60:63]\n"
SA"s42,s42,1\n"
BL"v225,s[8:11],s43 offen offset:0 sc1 lds\n"
PK"v3,v6,v7\n"
MF"v[80:83],a[64:67],v[64:67],v[80:83]\n"
SA"s41,s41,32768\n"
EX"v20,v20\n"
MF"v[112:115],a[64:67],v[68:71],v[112:115]\n"
"s_cmp_eq_u32 s41,163840\n"
RD"a[80:83],v227 offset:20480\n"
PK"v10,v12,v13\n"
MF"v[144:147],a[64:67],v[72:75],v[144:147]\n"
"s_cselect_b32 s41,0,s41\n"
EX"v21,v21\n"
MF"v[176:179],a[64:67],v[76:79],v[176:179]\n"
WT"lgkmcnt(2)\n"
"v_add_u32 v226,s41,v224\n"
PK"v11,v14,v15\n"
MF"v[84:87],a[68:71],v[64:67],v[84:87]\n"
EX"v22,v22\n"
MF"v[116:119],a[68:71],v[68:71],v[116:119]\n"
RD"a[84:87],v227 offset:21504\n"
PK"v18,v20,v21\n"
MF"v[148:151],a[68:71],v[72:75],v[148:151]\n"
EX"v23,v23\n"
MF"v[180:183],a[68:71],v[76:79],v[180:183]\n"
BL"v225,s[8:11],s43 offen offset:1024 sc1 lds\n"
MF"v[88:91],a[72:75],v[64:67],v[88:91]\n"
EX"v28,v28\n"
MF"v[120:123],a[72:75],v[68:71],v[120:123]\n"
RD"a[88:91],v227 offset:22528\n"
PK"v19,v22,v23\n"
MF"v[152:155],a[72:75],v[72:75],v[152:155]\n"
EX"v29,v29\n"
MF"v[184:187],a[72:75],v[76:79],v[184:187]\n"
WT"lgkmcnt(2)\n"
WT"vmcnt(2)\n"
"v_lshlrev_b32 v244,16,v254\n"
"v_and_b32 v245,0xffff0000,v254\n"
MF"v[92:95],a[76:79],v[64:67],v[92:95]\n"
EX"v30,v30\n"
"v_lshlrev_b32 v246,16,v255\n"
MF"v[124:127],a[76:79],v[68:71],v[124:127]\n"
RD"a[92:95],v227 offset:23552\n"
"v_and_b32 v247,0xffff0000,v255\n"
"v_add_f32 v240,v240,v244\n"
MF"v[156:159],a[76:79],v[72:75],v[156:159]\n"
EX"v31,v31\n"
"v_add_f32 v241,v241,v245\n"
MF"v[188:191],a[76:79],v[76:79],v[188:191]\n"
BL"v225,s[8:11],s43 offen offset:2048 sc1 lds\n"
"v_add_f32 v242,v242,v246\n"
MF"v[96:99],a[80:83],v[64:67],v[96:99]\n"
EX"v32,v32\n"
"v_add_f32 v243,v243,v247\n"
MF"v[128:131],a[80:83],v[68:71],v[128:131]\n"
RD"a[96:99],v227 offset:24576\n"
PK"v240,v240,v241\n"
PK"v241,v242,v243\n"
MF"v[160:163],a[80:83],v[72:75],v[160:163]\n"
"buffer_store_dwordx2 v[240:241],v239,s[8:11],s93 offen sc1\n"
MF"v[192:195],a[80:83],v[76:79],v[192:195]\n"
WT"lgkmcnt(2)\n"
EX"v33,v33\n"
PK"v26,v28,v29\n"
MF"v[100:103],a[84:87],v[64:67],v[100:103]\n"
EX"v34,v34\n"
MF"v[132:135],a[84:87],v[68:71],v[132:135]\n"
RD"a[100:103],v227 offset:25600\n"
PK"v27,v30,v31\n"
MF"v[164:167],a[84:87],v[72:75],v[164:167]\n"
EX"v35,v35\n"
MF"v[196:199],a[84:87],v[76:79],v[196:199]\n"
BL"v225,s[8:11],s43 offen offset:3072 sc1 lds\n"
MF"v[104:107],a[88:91],v[64:67],v[104:107]\n"
EX"v40,v40\n"
MF"v[136:139],a[88:91],v[68:71],v[136:139]\n"
RD"a[104:107],v227 offset:26624\n"
MF"v[168:171],a[88:91],v[72:75],v[168:171]\n"
EX"v41,v41\n"
MF"v[200:203],a[88:91],v[76:79],v[200:203]\n"
WT"lgkmcnt(2)\n"
SA"m0,s40,16384\n"
MF"v[108:111],a[92:95],v[64:67],v[108:111]\n"
EX"v42,v42\n"
MF"v[140:143],a[92:95],v[68:71],v[140:143]\n"
RD"a[108:111],v227 offset:27648\n"
MF"v[172:175],a[92:95],v[72:75],v[172:175]\n"
EX"v43,v43\n"
MF"v[204:207],a[92:95],v[76:79],v[204:207]\n"
BL"v225,s[8:11],s44 offen offset:0 sc1 lds\n"
MF"a[132:135],a[128:131],v[64:67],a[132:135]\n"
SA"s40,s40,32768\n"
EX"v48,v48\n"
PK"v64,v32,v33\n"
MF"a[136:139],a[128:131],v[68:71],a[136:139]\n"
"s_cmp_eq_u32 s40,s46\n"
RD"a[112:115],v227 offset:28672\n"
PK"v65,v34,v35\n"
MF"a[140:143],a[128:131],v[72:75],a[140:143]\n"
"s_cselect_b32 s40,s47,s40\n"
EX"v49,v49\n"
MF"a[144:147],a[128:131],v[76:79],a[144:147]\n"
PK"v68,v40,v41\n"
PK"v69,v42,v43\n"
MF"v[80:83],a[96:99],v[0:3],v[80:83]\n"
EX"v50,v50\n"
MF"v[112:115],a[96:99],v[8:11],v[112:115]\n"
RD"a[116:119],v227 offset:29696\n"
PK"v72,v48,v49\n"
MF"v[144:147],a[96:99],v[16:19],v[144:147]\n"
EX"v51,v51\n"
MF"v[176:179],a[96:99],v[24:27],v[176:179]\n"
WT"lgkmcnt(2)\n"
BL"v225,s[8:11],s44 offen offset:1024 sc1 lds\n"
MF"v[84:87],a[100:103],v[0:3],v[84:87]\n"
EX"v36,v36\n"
MF"v[116:119],a[100:103],v[8:11],v[116:119]\n"
RD"a[120:123],v227 offset:30720\n"
PK"v73,v50,v51\n"
MF"v[148:151],a[100:103],v[16:19],v[148:151]\n"
EX"v37,v37\n"
MF"v[180:183],a[100:103],v[24:27],v[180:183]\n"
PK"v66,v36,v37\n"
MF"v[88:91],a[104:107],v[0:3],v[88:91]\n"
EX"v38,v38\n"
MF"v[120:123],a[104:107],v[8:11],v[120:123]\n"
RD"a[124:127],v227 offset:31744\n"
MF"v[152:155],a[104:107],v[16:19],v[152:155]\n"
EX"v39,v39\n"
MF"v[184:187],a[104:107],v[24:27],v[184:187]\n"
BL"v225,s[8:11],s44 offen offset:2048 sc1 lds\n"
MF"v[92:95],a[108:111],v[0:3],v[92:95]\n"
EX"v44,v44\n"
MF"v[124:127],a[108:111],v[8:11],v[124:127]\n"
PK"v67,v38,v39\n"
MF"v[156:159],a[108:111],v[16:19],v[156:159]\n"
EX"v45,v45\n"
MF"v[188:191],a[108:111],v[24:27],v[188:191]\n"
WT"lgkmcnt(1)\n"
PK"v70,v44,v45\n"
MF"v[96:99],a[112:115],v[0:3],v[96:99]\n"
EX"v46,v46\n"
MF"v[128:131],a[112:115],v[8:11],v[128:131]\n"
MF"v[160:163],a[112:115],v[16:19],v[160:163]\n"
EX"v47,v47\n"
MF"v[192:195],a[112:115],v[24:27],v[192:195]\n"
BL"v225,s[8:11],s44 offen offset:3072 sc1 lds\n"
MF"v[100:103],a[116:119],v[0:3],v[100:103]\n"
WT"lgkmcnt(0)\n"
"s_barrier\n"
MF"v[132:135],a[116:119],v[8:11],v[132:135]\n"
RD"a[64:67],v226 offset:0\n"
PK"v71,v46,v47\n"
MF"v[164:167],a[116:119],v[16:19],v[164:167]\n"
EX"v52,v52\n"
MF"v[196:199],a[116:119],v[24:27],v[196:199]\n"
RD"a[68:71],v226 offset:1024\n"
MF"v[104:107],a[120:123],v[0:3],v[104:107]\n"
EX"v53,v53\n"
MF"v[136:139],a[120:123],v[8:11],v[136:139]\n"
RD"a[72:75],v226 offset:2048\n"
PK"v74,v52,v53\n"
MF"v[168:171],a[120:123],v[16:19],v[168:171]\n"
EX"v54,v54\n"
MF"v[200:203],a[120:123],v[24:27],v[200:203]\n"
MF"v[108:111],a[124:127],v[0:3],v[108:111]\n"
WT"vmcnt(5)\n"
"buffer_store_dwordx2 v[232:233],v230,s[8:11],s94 offen sc1\n"
MF"v[140:143],a[124:127],v[8:11],v[140:143]\n"
EX"v55,v55\n"
MF"v[172:175],a[124:127],v[16:19],v[172:175]\n"
RD"a[76:79],v226 offset:3072\n"
PK"v75,v54,v55\n"
MF"v[204:207],a[124:127],v[24:27],v[204:207]\n"
RD"a[80:83],v226 offset:4096\n"
MF"a[132:135],a[128:131],v[0:3],a[132:135]\n"
MF"a[136:139],a[128:131],v[8:11],a[136:139]\n"
RD"a[84:87],v226 offset:5120\n"
MF"a[140:143],a[128:131],v[16:19],a[140:143]\n"
MF"a[144:147],a[128:131],v[24:27],a[144:147]\n"
"s_mov_b32 s57,0\n"
"s_lshl_b32 s56,s34,1\n"
"s_sub_u32 s56,s56,12\n"
"s_branch LP1_%=\n"
"LRT_%=:\n"
WT"lgkmcnt(5)\n"
MF"v[0:3],a[64:67],a[0:3],0\n"
"s_mov_b32 s12,s74\n"
EX"v56,v56\n"
MF"v[8:11],a[64:67],a[16:19],0\n"
"s_and_b32 s13,s75,0xffff\n"
"v_lshrrev_b32 v242,1,v224\n"
"v_and_b32 v242,56,v242\n"
MF"v[16:19],a[64:67],a[32:35],0\n"
"s_mov_b32 s14,5242880\n"
EX"v57,v57\n"
MF"v[24:27],a[64:67],a[48:51],0\n"
WT"lgkmcnt(4)\n"
PK"v76,v56,v57\n"
"s_lshl_b32 s23,s35,6\n"
MF"v[0:3],a[68:71],a[4:7],v[0:3]\n"
"s_mov_b32 s15,0x20000\n"
EX"v58,v58\n"
MF"v[8:11],a[68:71],a[20:23],v[8:11]\n"
SA"s23,s23,4352\n"
"v_add_u32 v243,s40,v252\n"
"v_mov_b32 v227,v226\n"
MF"v[16:19],a[68:71],a[36:39],v[16:19]\n"
SA"s19,s40,32768\n"
EX"v59,v59\n"
MF"v[24:27],a[68:71],a[52:55],v[24:27]\n"
WT"lgkmcnt(0)\n"
"buffer_load_dwordx2 v[244:245],v242,s[8:11],s23 offen sc1\n"
MF"v[0:3],a[72:75],a[8:11],v[0:3]\n"
"s_cmp_eq_u32 s19,s46\n"
EX"v60,v60\n"
MF"v[8:11],a[72:75],a[24:27],v[8:11]\n"
"s_cselect_b32 s19,s47,s19\n"
RD"a[88:91],v226 offset:6144\n"
PK"v77,v58,v59\n"
MF"v[16:19],a[72:75],a[40:43],v[16:19]\n"
"s_cmp_eq_u32 s20,0\n"
EX"v61,v61\n"
MF"v[24:27],a[72:75],a[56:59],v[24:27]\n"
"s_cselect_b32 s17,2130706432,s17\n"
PK"v78,v60,v61\n"
SA"m0,s19,0\n"
MF"v[0:3],a[76:79],a[12:15],v[0:3]\n"
BL"v240,s[12:15],s17 offen offset:0 lds\n"
MF"v[8:11],a[76:79],a[28:31],v[8:11]\n"
EX"v62,v62\n"
MF"v[16:19],a[76:79],a[44:47],v[16:19]\n"
RD"a[92:95],v226 offset:7168\n"
MF"v[24:27],a[76:79],a[60:63],v[24:27]\n"
EX"v63,v63\n"
MF"v[4:7],a[80:83],a[0:3],0\n"
BL"v241,s[12:15],s17 offen offset:1024 lds\n"
MF"v[12:15],a[80:83],a[16:19],0\n"
EX"v0,v0\n"
MF"v[20:23],a[80:83],a[32:35],0\n"
RD"a[96:99],v226 offset:8192\n"
PK"v79,v62,v63\n"
MF"v[28:31],a[80:83],a[48:51],0\n"
EX"v1,v1\n"
MF"v[4:7],a[84:87],a[4:7],v[4:7]\n"
BL"v251,s[12:15],s17 offen offset:2048 lds\n"
MF"v[12:15],a[84:87],a[20:23],v[12:15]\n"
SA"m0,s19,16384\n"
EX"v2,v2\n"
MF"v[20:23],a[84:87],a[36:39],v[20:23]\n"
SA"s17,s17,20480\n"
RD"a[100:103],v226 offset:9216\n"
PK"v0,v0,v1\n"
MF"v[28:31],a[84:87],a[52:55],v[28:31]\n"
WT"lgkmcnt(2)\n"
EX"v3,v3\n"
MF"v[4:7],a[88:91],a[8:11],v[4:7]\n"
BL"v240,s[12:15],s17 offen offset:0 lds\n"
MF"v[12:15],a[88:91],a[24:27],v[12:15]\n"
EX"v8,v8\n"
MF"v[20:23],a[88:91],a[40:43],v[20:23]\n"
RD"a[104:107],v226 offset:10240\n"
PK"v1,v2,v3\n"
MF"v[28:31],a[88:91],a[56:59],v[28:31]\n"
EX"v9,v9\n"
MF"v[4:7],a[92:95],a[12:15],v[4:7]\n"
WT"vmcnt(13)\n"
RT"a[148:149],v243 offset:0\n"
RT"a[150:151],v243 offset:16384\n"
MF"v[12:15],a[92:95],a[28:31],v[12:15]\n"
EX"v10,v10\n"
MF"v[20:23],a[92:95],a[44:47],v[20:23]\n"
RD"a[108:111],v226 offset:11264\n"
PK"v8,v8,v9\n"
MF"v[28:31],a[92:95],a[60:63],v[28:31]\n"
WT"lgkmcnt(4)\n"
EX"v11,v11\n"
MF"v[32:35],a[96:99],a[0:3],0\n"
BL"v241,s[12:15],s17 offen offset:1024 lds\n"
MF"v[40:43],a[96:99],a[16:19],0\n"
EX"v16,v16\n"
MF"v[48:51],a[96:99],a[32:35],0\n"
RD"a[112:115],v226 offset:12288\n"
PK"v9,v10,v11\n"
MF"v[56:59],a[96:99],a[48:51],0\n"
EX"v17,v17\n"
MF"v[32:35],a[100:103],a[4:7],v[32:35]\n"
RT"a[152:153],v243 offset:32\n"
RT"a[154:155],v243 offset:16416\n"
MF"v[40:43],a[100:103],a[20:23],v[40:43]\n"
EX"v18,v18\n"
MF"v[48:51],a[100:103],a[36:39],v[48:51]\n"
RD"a[116:119],v226 offset:13312\n"
PK"v16,v16,v17\n"
MF"v[56:59],a[100:103],a[52:55],v[56:59]\n"
WT"lgkmcnt(4)\n"
EX"v19,v19\n"
MF"v[32:35],a[104:107],a[8:11],v[32:35]\n"
BL"v251,s[12:15],s17 offen offset:2048 lds\n"
MF"v[40:43],a[104:107],a[24:27],v[40:43]\n"
EX"v24,v24\n"
MF"v[48:51],a[104:107],a[40:43],v[48:51]\n"
RD"a[120:123],v226 offset:14336\n"
PK"v17,v18,v19\n"
MF"v[56:59],a[104:107],a[56:59],v[56:59]\n"
EX"v25,v25\n"
MF"v[32:35],a[108:111],a[12:15],v[32:35]\n"
RT"a[156:157],v243 offset:64\n"
RT"a[158:159],v243 offset:16448\n"
MF"v[40:43],a[108:111],a[28:31],v[40:43]\n"
EX"v26,v26\n"
MF"v[48:51],a[108:111],a[44:47],v[48:51]\n"
RD"a[124:127],v226 offset:15360\n"
PK"v24,v24,v25\n"
MF"v[56:59],a[108:111],a[60:63],v[56:59]\n"
WT"lgkmcnt(4)\n"
EX"v27,v27\n"
MF"v[36:39],a[112:115],a[0:3],0\n"
EX"v4,v4\n"
MF"v[44:47],a[112:115],a[16:19],0\n"
RD"a[64:67],v227 offset:16384\n"
PK"v25,v26,v27\n"
MF"v[52:55],a[112:115],a[32:35],0\n"
EX"v5,v5\n"
MF"v[60:63],a[112:115],a[48:51],0\n"
RT"a[160:161],v243 offset:96\n"
RT"a[162:163],v243 offset:16480\n"
MF"v[36:39],a[116:119],a[4:7],v[36:39]\n"
EX"v6,v6\n"
MF"v[44:47],a[116:119],a[20:23],v[44:47]\n"
RD"a[68:71],v227 offset:17408\n"
PK"v2,v4,v5\n"
MF"v[52:55],a[116:119],a[36:39],v[52:55]\n"
EX"v7,v7\n"
MF"v[60:63],a[116:119],a[52:55],v[60:63]\n"
WT"lgkmcnt(4)\n"
"v_readlane_b32 s43,v228,s42\n"
PK"v3,v6,v7\n"
MF"v[36:39],a[120:123],a[8:11],v[36:39]\n"
EX"v12,v12\n"
MF"v[44:47],a[120:123],a[24:27],v[44:47]\n"
RD"a[72:75],v227 offset:18432\n"
MF"v[52:55],a[120:123],a[40:43],v[52:55]\n"
EX"v13,v13\n"
MF"v[60:63],a[120:123],a[56:59],v[60:63]\n"
"s_mov_b32 m0,s40\n"
RT"a[164:165],v243 offset:128\n"
RT"a[166:167],v243 offset:16512\n"
MF"v[36:39],a[124:127],a[12:15],v[36:39]\n"
SA"s17,s17,61440\n"
EX"v14,v14\n"
MF"v[44:47],a[124:127],a[28:31],v[44:47]\n"
RD"a[76:79],v227 offset:19456\n"
"v_readlane_b32 s44,v229,s42\n"
MF"v[52:55],a[124:127],a[44:47],v[52:55]\n"
EX"v15,v15\n"
MF"v[60:63],a[124:127],a[60:63],v[60:63]\n"
WT"lgkmcnt(4)\n"
PK"v10,v12,v13\n"
PK"v11,v14,v15\n"
MF"v[80:83],a[64:67],v[64:67],v[80:83]\n"
WT"lgkmcnt(1)\n"
BL"v225,s[8:11],s43 offen offset:0 sc1 lds\n"
MF"v[112:115],a[64:67],v[68:71],v[112:115]\n"
SA"s42,s42,1\n"
EX"v20,v20\n"
MF"v[144:147],a[64:67],v[72:75],v[144:147]\n"
SA"s41,s41,32768\n"
RD"a[80:83],v227 offset:20480\n"
"s_cmp_eq_u32 s41,163840\n"
MF"v[176:179],a[64:67],v[76:79],v[176:179]\n"
"s_cselect_b32 s41,0,s41\n"
EX"v21,v21\n"
MF"v[84:87],a[68:71],v[64:67],v[84:87]\n"
BS"a[148:151],v224,s[8:11],s18 offen offset:0 sc1\n"
MF"v[116:119],a[68:71],v[68:71],v[116:119]\n"
EX"v22,v22\n"
MF"v[148:151],a[68:71],v[72:75],v[148:151]\n"
RD"a[84:87],v227 offset:21504\n"
"v_add_u32 v226,s41,v224\n"
MF"v[180:183],a[68:71],v[76:79],v[180:183]\n"
EX"v23,v23\n"
MF"v[88:91],a[72:75],v[64:67],v[88:91]\n"
BL"v225,s[8:11],s43 offen offset:1024 sc1 lds\n"
MF"v[120:123],a[72:75],v[68:71],v[120:123]\n"
EX"v28,v28\n"
MF"v[152:155],a[72:75],v[72:75],v[152:155]\n"
RD"a[88:91],v227 offset:22528\n"
PK"v18,v20,v21\n"
MF"v[184:187],a[72:75],v[76:79],v[184:187]\n"
WT"lgkmcnt(2)\n"
EX"v29,v29\n"
MF"v[92:95],a[76:79],v[64:67],v[92:95]\n"
BS"a[152:155],v224,s[8:11],s18 offen offset:1024 sc1\n"
MF"v[124:127],a[76:79],v[68:71],v[124:127]\n"
EX"v30,v30\n"
MF"v[156:159],a[76:79],v[72:75],v[156:159]\n"
RD"a[92:95],v227 offset:23552\n"
PK"v19,v22,v23\n"
MF"v[188:191],a[76:79],v[76:79],v[188:191]\n"
EX"v31,v31\n"
MF"v[96:99],a[80:83],v[64:67],v[96:99]\n"
BL"v225,s[8:11],s43 offen offset:2048 sc1 lds\n"
MF"v[128:131],a[80:83],v[68:71],v[128:131]\n"
EX"v32,v32\n"
MF"v[160:163],a[80:83],v[72:75],v[160:163]\n"
RD"a[96:99],v227 offset:24576\n"
PK"v26,v28,v29\n"
MF"v[192:195],a[80:83],v[76:79],v[192:195]\n"
WT"lgkmcnt(2)\n"
EX"v33,v33\n"
MF"v[100:103],a[84:87],v[64:67],v[100:103]\n"
BS"a[156:159],v224,s[8:11],s18 offen offset:2048 sc1\n"
MF"v[132:135],a[84:87],v[68:71],v[132:135]\n"
EX"v34,v34\n"
MF"v[164:167],a[84:87],v[72:75],v[164:167]\n"
RD"a[100:103],v227 offset:25600\n"
PK"v27,v30,v31\n"
MF"v[196:199],a[84:87],v[76:79],v[196:199]\n"
EX"v35,v35\n"
MF"v[104:107],a[88:91],v[64:67],v[104:107]\n"
BL"v225,s[8:11],s43 offen offset:3072 sc1 lds\n"
MF"v[136:139],a[88:91],v[68:71],v[136:139]\n"
EX"v40,v40\n"
MF"v[168:171],a[88:91],v[72:75],v[168:171]\n"
RD"a[104:107],v227 offset:26624\n"
MF"v[200:203],a[88:91],v[76:79],v[200:203]\n"
WT"lgkmcnt(2)\n"
EX"v41,v41\n"
MF"v[108:111],a[92:95],v[64:67],v[108:111]\n"
SA"m0,s40,16384\n"
BS"a[160:163],v224,s[8:11],s18 offen offset:3072 sc1\n"
MF"v[140:143],a[92:95],v[68:71],v[140:143]\n"
EX"v42,v42\n"
MF"v[172:175],a[92:95],v[72:75],v[172:175]\n"
SA"s18,s18,4096\n"
RD"a[108:111],v227 offset:27648\n"
MF"v[204:207],a[92:95],v[76:79],v[204:207]\n"
EX"v43,v43\n"
MF"a[132:135],a[128:131],v[64:67],a[132:135]\n"
SA"s40,s40,32768\n"
BL"v225,s[8:11],s44 offen offset:0 sc1 lds\n"
PK"v64,v32,v33\n"
MF"a[136:139],a[128:131],v[68:71],a[136:139]\n"
"s_cmp_eq_u32 s40,s46\n"
EX"v48,v48\n"
PK"v65,v34,v35\n"
MF"a[140:143],a[128:131],v[72:75],a[140:143]\n"
"s_cselect_b32 s40,s47,s40\n"
RD"a[112:115],v227 offset:28672\n"
PK"v68,v40,v41\n"
MF"a[144:147],a[128:131],v[76:79],a[144:147]\n"
EX"v49,v49\n"
MF"v[80:83],a[96:99],v[0:3],v[80:83]\n"
"s_lshr_b32 s21,s16,1\n"
BS"a[164:167],v224,s[8:11],s18 offen offset:0 sc1\n"
MF"v[112:115],a[96:99],v[8:11],v[112:115]\n"
"s_sub_u32 s21,s21,s33\n"
EX"v50,v50\n"
MF"v[144:147],a[96:99],v[16:19],v[144:147]\n"
"s_and_b32 s21,s21,3\n"
RD"a[116:119],v227 offset:29696\n"
PK"v69,v42,v43\n"
MF"v[176:179],a[96:99],v[24:27],v[176:179]\n"
WT"lgkmcnt(2)\n"
EX"v51,v51\n"
MF"v[84:87],a[100:103],v[0:3],v[84:87]\n"
"s_lshl_b32 s21,s21,1\n"
BL"v225,s[8:11],s44 offen offset:1024 sc1 lds\n"
MF"v[116:119],a[100:103],v[8:11],v[116:119]\n"
"s_and_b32 s22,s16,1\n"
EX"v36,v36\n"
MF"v[148:151],a[100:103],v[16:19],v[148:151]\n"
SA"s21,s21,s22\n"
RD"a[120:123],v227 offset:30720\n"
PK"v72,v48,v49\n"
MF"v[180:183],a[100:103],v[24:27],v[180:183]\n"
"s_lshl_b32 s21,s21,2\n"
EX"v37,v37\n"
MF"v[88:91],a[104:107],v[0:3],v[88:91]\n"
SA"s21,s21,s30\n"
EX"v38,v38\n"
MF"v[120:123],a[104:107],v[8:11],v[120:123]\n"
"s_mul_i32 s21,s21,5120\n"
RD"a[124:127],v227 offset:31744\n"
PK"v73,v50,v51\n"
MF"v[152:155],a[104:107],v[16:19],v[152:155]\n"
"s_mul_i32 s22,s35,163840\n"
EX"v39,v39\n"
MF"v[184:187],a[104:107],v[24:27],v[184:187]\n"
SA"s21,s21,s22\n"
BL"v225,s[8:11],s44 offen offset:2048 sc1 lds\n"
MF"v[92:95],a[108:111],v[0:3],v[92:95]\n"
SA"s18,s21,3428352\n"
EX"v44,v44\n"
MF"v[124:127],a[108:111],v[8:11],v[124:127]\n"
SA"s16,s16,1\n"
PK"v66,v36,v37\n"
PK"v67,v38,v39\n"
MF"v[156:159],a[108:111],v[16:19],v[156:159]\n"
EX"v45,v45\n"
MF"v[188:191],a[108:111],v[24:27],v[188:191]\n"
WT"lgkmcnt(1)\n"
PK"v70,v44,v45\n"
MF"v[96:99],a[112:115],v[0:3],v[96:99]\n"
EX"v46,v46\n"
MF"v[128:131],a[112:115],v[8:11],v[128:131]\n"
MF"v[160:163],a[112:115],v[16:19],v[160:163]\n"
EX"v47,v47\n"
MF"v[192:195],a[112:115],v[24:27],v[192:195]\n"
BL"v225,s[8:11],s44 offen offset:3072 sc1 lds\n"
MF"v[100:103],a[116:119],v[0:3],v[100:103]\n"
WT"lgkmcnt(0)\n"
"s_barrier\n"
MF"v[132:135],a[116:119],v[8:11],v[132:135]\n"
RD"a[64:67],v226 offset:0\n"
PK"v71,v46,v47\n"
MF"v[164:167],a[116:119],v[16:19],v[164:167]\n"
EX"v52,v52\n"
MF"v[196:199],a[116:119],v[24:27],v[196:199]\n"
RD"a[68:71],v226 offset:1024\n"
MF"v[104:107],a[120:123],v[0:3],v[104:107]\n"
EX"v53,v53\n"
MF"v[136:139],a[120:123],v[8:11],v[136:139]\n"
RD"a[72:75],v226 offset:2048\n"
PK"v74,v52,v53\n"
MF"v[168:171],a[120:123],v[16:19],v[168:171]\n"
EX"v54,v54\n"
MF"v[200:203],a[120:123],v[24:27],v[200:203]\n"
MF"v[108:111],a[124:127],v[0:3],v[108:111]\n"
EX"v55,v55\n"
MF"v[140:143],a[124:127],v[8:11],v[140:143]\n"
RD"a[76:79],v226 offset:3072\n"
PK"v75,v54,v55\n"
MF"v[172:175],a[124:127],v[16:19],v[172:175]\n"
MF"v[204:207],a[124:127],v[24:27],v[204:207]\n"
RD"a[80:83],v226 offset:4096\n"
MF"a[132:135],a[128:131],v[0:3],a[132:135]\n"
MF"a[136:139],a[128:131],v[8:11],a[136:139]\n"
RD"a[84:87],v226 offset:5120\n"
MF"a[140:143],a[128:131],v[16:19],a[140:143]\n"
MF"a[144:147],a[128:131],v[24:27],a[144:147]\n"
"s_sub_u32 s20,s20,1\n"
"s_cbranch_scc0 LRT_%=\n"
"s_lshl_b32 s21,s35,8\n"
SA"s21,s21,81264640\n"
SA"s21,s21,s45\n"
WT"vmcnt(3)\n"
"s_barrier\n"
"buffer_store_dwordx2 v[232:233],v230,s[8:11],s21 offen sc1\n"
"v_cmp_eq_u64 vcc,s[28:29],v[244:245]\n"
"s_cmp_eq_u64 vcc,exec\n"
"s_cbranch_scc0 L1131_%=\n"
"L1132_%=:\n"
"s_branch LP1_%=\n"
"LEND_%=:\n"
      : : [ka] "s"(__builtin_amdgcn_kernarg_segment_ptr()), [b] "s"(blockIdx.x), [t] "v"(threadIdx.x)
      : "v1", "v2", "v3", "v4", "v5", "v6", "v7", "v8", "v9", "v10", "v11", "v12", "v13", "v14", "v15", "v16", "v17", "v18", "v19", "v20", "v21", "v22", "v23", "v24", "v25", "v26", "v27", "v28", "v29", "v30", "v31", "v32", "v33", "v34", "v35", "v36", "v37", "v38", "v39", "v40", "v41", "v42", "v43", "v44", "v45", "v46", "v47", "v48", "v49", "v50", "v51", "v52", "v53", "v54", "v55", "v56", "v57", "v58", "v59", "v60", "v61", "v62", "v63", "v64", "v65", "v66", "v67", "v68", "v69", "v70", "v71", "v72", "v73", "v74", "v75", "v76", "v77", "v78", "v79", "v80", "v81", "v82", "v83", "v84", "v85", "v86", "v87", "v88", "v89", "v90", "v91", "v92", "v93", "v94", "v95", "v96", "v97", "v98", "v99", "v100", "v101", "v102", "v103", "v104", "v105", "v106", "v107", "v108", "v109", "v110", "v111", "v112", "v113", "v114", "v115", "v116", "v117", "v118", "v119", "v120", "v121", "v122", "v123", "v124", "v125", "v126", "v127", "v128", "v129", "v130", "v131", "v132", "v133", "v134", "v135", "v136", "v137", "v138", "v139", "v140", "v141", "v142", "v143", "v144", "v145", "v146", "v147", "v148", "v149", "v150", "v151", "v152", "v153", "v154", "v155", "v156", "v157", "v158", "v159", "v160", "v161", "v162", "v163", "v164", "v165", "v166", "v167", "v168", "v169", "v170", "v171", "v172", "v173", "v174", "v175", "v176", "v177", "v178", "v179", "v180", "v181", "v182", "v183", "v184", "v185", "v186", "v187", "v188", "v189", "v190", "v191", "v192", "v193", "v194", "v195", "v196", "v197", "v198", "v199", "v200", "v201", "v202", "v203", "v204", "v205", "v206", "v207", "v208", "v209", "v210", "v211", "v212", "v213", "v214", "v215", "v216", "v217", "v218", "v219", "v220", "v221", "v222", "v223", "v224", "v225", "v226", "v227", "v228", "v229", "v230", "v231", "v232", "v233", "v234", "v235", "v236", "v237", "v238", "v239", "v240", "v241", "v242", "v243", "v244", "v245", "v246", "v247", "v248", "v249", "v250", "v251", "v252", "v253", "v254", "v255", "a0", "a1", "a2", "a3", "a4", "a5", "a6", "a7", "a8", "a9", "a10", "a11", "a12", "a13", "a14", "a15", "a16", "a17", "a18", "a19", "a20", "a21", "a22", "a23", "a24", "a25", "a26", "a27", "a28", "a29", "a30", "a31", "a32", "a33", "a34", "a35", "a36", "a37", "a38", "a39", "a40", "a41", "a42", "a43", "a44", "a45", "a46", "a47", "a48", "a49", "a50", "a51", "a52", "a53", "a54", "a55", "a56", "a57", "a58", "a59", "a60", "a61", "a62", "a63", "a64", "a65", "a66", "a67", "a68", "a69", "a70", "a71", "a72", "a73", "a74", "a75", "a76", "a77", "a78", "a79", "a80", "a81", "a82", "a83", "a84", "a85", "a86", "a87", "a88", "a89", "a90", "a91", "a92", "a93", "a94", "a95", "a96", "a97", "a98", "a99", "a100", "a101", "a102", "a103", "a104", "a105", "a106", "a107", "a108", "a109", "a110", "a111", "a112", "a113", "a114", "a115", "a116", "a117", "a118", "a119", "a120", "a121", "a122", "a123", "a124", "a125", "a126", "a127", "a128", "a129", "a130", "a131", "a132", "a133", "a134", "a135", "a136", "a137", "a138", "a139", "a140", "a141", "a142", "a143", "a144", "a145", "a146", "a147", "a148", "a149", "a150", "a151", "a152", "a153", "a154", "a155", "a156", "a157", "a158", "a159", "a160", "a161", "a162", "a163", "a164", "a165", "a166", "a167", "a168", "a169", "a170", "a171", "a172", "a173", "a174", "a175", "a176", "a177", "a178", "a179", "a180", "a181", "a182", "a183", "a184", "a185", "a186", "a187", "a188", "a189", "a190", "a191", "a192", "a193", "a194", "a195", "a196", "a197", "a198", "a199", "a200", "a201", "a202", "a203", "a204", "a205", "a206", "a207", "a208", "a209", "a210", "a211", "a212", "a213", "a214", "a215", "a216", "a217", "a218", "a219", "a220", "a221", "a222", "a223", "a224", "a225", "a226", "a227", "a228", "a229", "a230", "a231", "a232", "a233", "a234", "a235", "a236", "a237", "a238", "a239", "a240", "a241", "a242", "a243", "a244", "a245", "a246", "a247", "a248", "a249", "a250", "a251", "a252", "a253", "a254", "a255", "s8", "s9", "s10", "s11", "s12", "s13", "s14", "s15", "s16", "s17", "s18", "s19", "s20", "s21", "s22", "s23", "s24", "s25", "s26", "s27", "s28", "s29", "s30", "s31", "s32", "s33", "s34", "s35", "s36", "s37", "s38", "s39", "s40", "s41", "s42", "s43", "s44", "s45", "s46", "s47", "s48", "s49", "s50", "s51", "s52", "s53", "s54", "s55", "s56", "s57", "s58", "s59", "s60", "s61", "s62", "s63", "s64", "s65", "s66", "s67", "s68", "s69", "s70", "s71", "s72", "s73", "s74", "s75", "s76", "s77", "s78", "s79", "s80", "s81", "s82", "s83", "s84", "s85", "s86", "s87", "s88", "s89", "s90", "s91", "s92", "s93", "s94", "s95", "s96", "s97", "s98", "s99", "scc", "vcc", "memory");
}

extern "C" void kernel_launch(void* const* d_in, const int* in_sizes, int n_in, void* d_out, int out_size, void* d_ws, size_t ws_size, hipStream_t stream) {
  (void)hipFuncSetAttribute((const void*)mega_kernel, hipFuncAttributeMaxDynamicSharedMemorySize, 163840);
  mega_kernel<<<256, 256, 163840, stream>>>((const bf16*)d_in[0], (const bf16*)d_in[1], (const bf16*)d_in[2], (const bf16*)d_in[3], (const bf16*)d_in[4], (const bf16*)d_in[5],
      (const bf16*)d_in[7], (const bf16*)d_in[8], (const bf16*)d_in[9], (const bf16*)d_in[10], (char*)d_ws + (size_t)0 * 1048576, (bf16*)d_out);
}
```

```cpp
#include <hip/hip_runtime.h>
#include <hip/hip_bf16.h>
typedef __hip_bfloat16 bf16;
#define MF "v_mfma_f32_16x16x32_bf16 "
#define RD "ds_read_b128 "
#define RT "ds_read_b64_tr_b16 "
#define BL "buffer_load_dwordx4 "
#define BS "buffer_store_dwordx4 "
#define PK "v_cvt_pk_bf16_f32 "
#define EX "v_exp_f32 "
#define WT "s_waitcnt "
#define MU "v_mul_f32 "
#define SA "s_add_u32 "

__global__ __launch_bounds__(256, 1) void mega_kernel(const bf16* x, const bf16* ckv, const bf16* wq, const bf16* wuk, const bf16* wuv, const bf16* wo, const bf16* w_r,
                                                      const bf16* wg, const bf16* wu, const bf16* wd, char* ws, bf16* out) {
  asm volatile(
"s_load_dwordx16 s[64:79],%[ka],0\n"
"s_load_dwordx8 s[80:87],%[ka],64\n"
"s_mov_b32 s31,%[b]\n"
"v_lshrrev_b32 v1,6,%[t]\n"
"v_and_b32 v2,63,%[t]\n"
"v_readfirstlane_b32 s30,v1\n"
"v_lshlrev_b32 v224,4,v2\n"
"s_and_b32 s32,s31,7\n"
"s_lshr_b32 s33,s31,6\n"
"s_bfe_u32 s34,s31,0x30003\n"
"s_lshl_b32 s35,s32,2\n"
SA"s35,s35,s33\n"
SA"s46,s34,s32\n"
"s_and_b32 s46,s46,6\n"
"s_lshl_b32 s47,s46,19\n"
"s_lshl_b32 s46,s46,7\n"
"s_lshl_b32 s36,s30,12\n"
"s_mul_i32 s37,s30,24576\n"
SA"s37,s37,65536\n"
"v_lshrrev_b32 v3,3,v2\n"
"v_lshrrev_b32 v4,4,v2\n"
"v_and_b32 v5,7,v2\n"
"v_xor_b32 v6,v5,v4\n"
"v_lshlrev_b32 v6,4,v6\n"
"v_lshl_add_u32 v6,v3,10,v6\n"
"s_lshl_b32 s50,s30,15\n"
"v_add_u32 v234,s50,v6\n"
"v_xor_b32 v235,64,v234\n"
"v_add_u32 v235,7168,v235\n"
"v_add_u32 v236,14336,v234\n"
"v_add_u32 v237,14336,v235\n"
"v_and_b32 v7,1,v4\n"
"v_lshlrev_b32 v7,1,v7\n"
"v_xor_b32 v7,v7,v5\n"
"v_lshlrev_b32 v7,4,v7\n"
"v_lshl_add_u32 v238,v3,13,v7\n"
"v_xor_b32 v239,64,v238\n"
"v_add_u32 v239,64512,v239\n"
"v_add_u32 v240,129024,v238\n"
"v_add_u32 v241,129024,v239\n"
"v_and_b32 v8,31,v2\n"
"v_lshlrev_b32 v8,3,v8\n"
"v_lshrrev_b32 v9,5,v2\n"
"s_lshl_b32 s50,s31,2\n"
SA"s50,s50,s30\n"
"s_mul_i32 s50,s50,2560\n"
"v_mul_u32_u24 v9,1280,v9\n"
"v_add3_u32 v9,v9,v8,s50\n"
"s_lshl_b32 s50,s34,17\n"
"s_lshl_b32 s51,s35,8\n"
"s_and_b32 s52,s30,1\n"
"s_lshl_b32 s52,s52,7\n"
SA"s52,s52,s51\n"
"s_mov_b32 s14,0x120000\n"
"s_mov_b32 s15,0x20000\n"
"s_sub_u32 s18,4194304,s52\n"
"s_mov_b32 s19,0x20000\n"
WT"lgkmcnt(0)\n"
"s_load_dwordx2 s[28:29],s[84:85],0\n"
SA"s12,s66,s50\n"
"s_addc_u32 s13,s67,0\n"
"s_and_b32 s13,s13,0xffff\n"
"s_cmp_lt_u32 s30,2\n"
"s_cselect_b64 s[16:17],s[70:71],s[72:73]\n"
SA"s16,s16,s52\n"
"s_addc_u32 s17,s17,0\n"
"s_and_b32 s17,s17,0xffff\n"
"global_load_dwordx2 v[128:129],v9,s[64:65] offset:0\n"
"global_load_dwordx2 v[130:131],v9,s[64:65] offset:256\n"
"global_load_dwordx2 v[132:133],v9,s[64:65] offset:512\n"
"global_load_dwordx2 v[134:135],v9,s[64:65] offset:768\n"
"global_load_dwordx2 v[136:137],v9,s[64:65] offset:1024\n"
"global_load_dwordx2 v[138:139],v8,s[76:77] offset:0\n"
"global_load_dwordx2 v[140:141],v8,s[76:77] offset:256\n"
"global_load_dwordx2 v[142:143],v8,s[76:77] offset:512\n"
"global_load_dwordx2 v[144:145],v8,s[76:77] offset:768\n"
"global_load_dwordx2 v[146:147],v8,s[76:77] offset:1024\n"
SA"m0,s37,0\n"
SA"s48,s47,0\n"
"s_and_b32 s48,s48,4194303\n"
BL"v238,s[16:19],s48 offen offset:0 lds\n"
BL"v239,s[16:19],s48 offen offset:1024 lds\n"
BL"v240,s[16:19],s48 offen offset:2048 lds\n"
BL"v241,s[16:19],s48 offen offset:3072 lds\n"
"s_getpc_b64 s[24:25]\n"
"LPC_%=:\n"
"s_and_b32 s25,s25,0xffff\n"
"s_mov_b32 s26,LEND_%=-LPC_%=\n"
"s_mov_b32 s27,0x20000\n"
"s_lshr_b32 s49,s31,3\n"
"s_lshl_b32 s49,s49,2\n"
SA"s49,s49,s30\n"
"s_lshl_b32 s49,s49,7\n"
"v_lshlrev_b32 v10,14,v2\n"
"buffer_load_dword v255,v10,s[24:27],s49 offen\n"
SA"m0,s36,0\n"
SA"s48,s46,0\n"
"s_and_b32 s48,s48,1023\n"
BL"v234,s[12:15],s48 offen offset:0 lds\n"
BL"v235,s[12:15],s48 offen offset:1024 lds\n"
BL"v236,s[12:15],s48 offen offset:2048 lds\n"
BL"v237,s[12:15],s48 offen offset:3072 lds\n"
SA"m0,s37,4096\n"
SA"s48,s47,262144\n"
"s_and_b32 s48,s48,4194303\n"
BL"v238,s[16:19],s48 offen offset:0 lds\n"
BL"v239,s[16:19],s48 offen offset:1024 lds\n"
BL"v240,s[16:19],s48 offen offset:2048 lds\n"
BL"v241,s[16:19],s48 offen offset:3072 lds\n"
SA"m0,s37,8192\n"
SA"s48,s47,524288\n"
"s_and_b32 s48,s48,4194303\n"
BL"v238,s[16:19],s48 offen offset:0 lds\n"
BL"v239,s[16:19],s48 offen offset:1024 lds\n"
BL"v240,s[16:19],s48 offen offset:2048 lds\n"
BL"v241,s[16:19],s48 offen offset:3072 lds\n"
"v_lshlrev_b32 v253,7,%[t]\n"
"s_mov_b32 s8,s84\n"
"s_and_b32 s9,s85,0xffff\n"
"s_mov_b32 s10,81272832\n"
"s_mov_b32 s11,0x20000\n"
SA"s20,s68,s51\n"
"s_addc_u32 s21,s69,0\n"
"s_and_b32 s21,s21,0xffff\n"
"s_sub_u32 s22,5242880,s51\n"
"s_mov_b32 s23,0x20000\n"
"v_and_b32 v11,15,v2\n"
"v_lshrrev_b32 v12,1,v11\n"
"v_xor_b32 v12,v12,v4\n"
"v_lshlrev_b32 v12,4,v12\n"
"v_lshl_add_u32 v242,v11,7,v12\n"
"v_xor_b32 v243,64,v242\n"
"v_lshrrev_b32 v12,2,v11\n"
"v_lshl_add_u32 v12,v4,3,v12\n"
"v_lshlrev_b32 v12,7,v12\n"
"v_and_b32 v13,3,v2\n"
"v_lshl_add_u32 v12,v13,3,v12\n"
"v_and_b32 v13,3,v3\n"
"v_lshl_add_u32 v12,v13,5,v12\n"
"v_add_u32 v246,s37,v12\n"
"v_xor_b32 v247,32,v246\n"
"v_xor_b32 v248,64,v246\n"
"v_xor_b32 v249,96,v246\n"
"v_mov_b32 v13,2130706432\n"
"v_cmp_eq_u32 vcc,0,v2\n"
"s_cmp_eq_u32 s30,0\n"
"s_cselect_b32 s45,0,2130706432\n"
"v_cndmask_b32_e64 v230,v13,0,vcc\n"
"s_lshl_b32 s39,s30,8\n"
SA"s39,s39,s46\n"
SA"s98,s39,128\n"
"s_and_b32 s39,s39,1023\n"
"s_and_b32 s98,s98,1023\n"
"s_lshl_b32 s50,s30,15\n"
"s_sub_u32 s50,1048576,s50\n"
SA"s39,s39,s50\n"
SA"s98,s98,s50\n"
"s_mov_b32 s55,0x3e0293ee\n"
WT"lgkmcnt(0)\n"
SA"s28,s28,0x7f4a7c15\n"
"s_addc_u32 s29,s29,0x9e3779b9\n"
"v_mov_b32 v232,s28\n"
"v_mov_b32 v233,s29\n"
"s_cmp_lt_u32 s30,2\n"
"s_cbranch_scc0 LV_%=\n"
WT"vmcnt(26)\n"
"v_lshlrev_b32 v148,16,v128\n"
"v_and_b32 v149,0xffff0000,v128\n"
"v_lshlrev_b32 v150,16,v129\n"
"v_and_b32 v151,0xffff0000,v129\n"
WT"vmcnt(25)\n"
"v_lshlrev_b32 v152,16,v130\n"
"v_and_b32 v153,0xffff0000,v130\n"
"v_lshlrev_b32 v154,16,v131\n"
"v_and_b32 v155,0xffff0000,v131\n"
WT"vmcnt(24)\n"
"v_lshlrev_b32 v156,16,v132\n"
"v_and_b32 v157,0xffff0000,v132\n"
"v_lshlrev_b32 v158,16,v133\n"
"v_and_b32 v159,0xffff0000,v133\n"
WT"vmcnt(23)\n"
"v_lshlrev_b32 v160,16,v134\n"
"v_and_b32 v161,0xffff0000,v134\n"
"v_lshlrev_b32 v162,16,v135\n"
"v_and_b32 v163,0xffff0000,v135\n"
WT"vmcnt(22)\n"
"v_lshlrev_b32 v164,16,v136\n"
"v_and_b32 v165,0xffff0000,v136\n"
"v_lshlrev_b32 v166,16,v137\n"
"v_and_b32 v167,0xffff0000,v137\n"
"v_add_f32 v188,v148,v149\n"
"v_add_f32 v189,v150,v151\n"
"v_add_f32 v188,v188,v152\n"
"v_add_f32 v189,v189,v153\n"
"v_add_f32 v188,v188,v154\n"
"v_add_f32 v189,v189,v155\n"
"v_add_f32 v188,v188,v156\n"
"v_add_f32 v189,v189,v157\n"
"v_add_f32 v188,v188,v158\n"
"v_add_f32 v189,v189,v159\n"
"v_add_f32 v188,v188,v160\n"
"v_add_f32 v189,v189,v161\n"
"v_add_f32 v188,v188,v162\n"
"v_add_f32 v189,v189,v163\n"
"v_add_f32 v188,v188,v164\n"
"v_add_f32 v189,v189,v165\n"
"v_add_f32 v188,v188,v166\n"
"v_add_f32 v189,v189,v167\n"
"v_add_f32 v188,v188,v189\n"
"s_nop 1\n"
"v_add_f32_dpp v188,v188,v188 quad_perm:[1,0,3,2] row_mask:0xf bank_mask:0xf\n"
WT"vmcnt(21)\n"
"v_lshlrev_b32 v168,16,v138\n"
"v_and_b32 v169,0xffff0000,v138\n"
"v_add_f32_dpp v188,v188,v188 quad_perm:[2,3,0,1] row_mask:0xf bank_mask:0xf\n"
"v_lshlrev_b32 v170,16,v139\n"
"v_and_b32 v171,0xffff0000,v139\n"
"v_add_f32_dpp v188,v188,v188 row_half_mirror row_mask:0xf bank_mask:0xf\n"
WT"vmcnt(20)\n"
"v_lshlrev_b32 v172,16,v140\n"
"v_and_b32 v173,0xffff0000,v140\n"
"v_add_f32_dpp v188,v188,v188 row_mirror row_mask:0xf bank_mask:0xf\n"
"v_lshlrev_b32 v174,16,v141\n"
"v_and_b32 v175,0xffff0000,v141\n"
"v_mov_b32 v189,v188\n"
WT"vmcnt(19)\n"
"v_lshlrev_b32 v176,16,v142\n"
"v_and_b32 v177,0xffff0000,v142\n"
"v_permlane16_swap_b32 v189,v188\n"
"v_lshlrev_b32 v178,16,v143\n"
"v_and_b32 v179,0xffff0000,v143\n"
"v_add_f32 v188,v188,v189\n"
WT"vmcnt(18)\n"
"v_lshlrev_b32 v180,16,v144\n"
"v_and_b32 v181,0xffff0000,v144\n"
MU"v191,0x3acccccd,v188\n"
"v_lshlrev_b32 v182,16,v145\n"
"v_and_b32 v183,0xffff0000,v145\n"
PK"v128,v191,v191\n"
WT"vmcnt(17)\n"
"v_lshlrev_b32 v184,16,v146\n"
"v_and_b32 v185,0xffff0000,v146\n"
"v_lshlrev_b32 v128,16,v128\n"
"v_lshlrev_b32 v186,16,v147\n"
"v_and_b32 v187,0xffff0000,v147\n"
"v_sub_f32 v148,v148,v128\n"
"v_sub_f32 v149,v149,v128\n"
"v_sub_f32 v150,v150,v128\n"
"v_sub_f32 v151,v151,v128\n"
"v_sub_f32 v152,v152,v128\n"
"v_sub_f32 v153,v153,v128\n"
"v_sub_f32 v154,v154,v128\n"
"v_sub_f32 v155,v155,v128\n"
"v_sub_f32 v156,v156,v128\n"
"v_sub_f32 v157,v157,v128\n"
SA"m0,s36,16384\n"
SA"s48,s46,128\n"
"s_and_b32 s48,s48,1023\n"
BL"v234,s[12:15],s48 offen offset:0 lds\n"
BL"v235,s[12:15],s48 offen offset:1024 lds\n"
BL"v236,s[12:15],s48 offen offset:2048 lds\n"
BL"v237,s[12:15],s48 offen offset:3072 lds\n"
"v_sub_f32 v158,v158,v128\n"
"v_sub_f32 v159,v159,v128\n"
"v_sub_f32 v160,v160,v128\n"
"v_sub_f32 v161,v161,v128\n"
"v_sub_f32 v162,v162,v128\n"
"v_sub_f32 v163,v163,v128\n"
"v_sub_f32 v164,v164,v128\n"
"v_sub_f32 v165,v165,v128\n"
"v_sub_f32 v166,v166,v128\n"
"v_sub_f32 v167,v167,v128\n"
MU"v188,v148,v148\n"
MU"v189,v149,v149\n"
"v_fmac_f32 v188,v150,v150\n"
"v_fmac_f32 v189,v151,v151\n"
"v_fmac_f32 v188,v152,v152\n"
"v_fmac_f32 v189,v153,v153\n"
"v_fmac_f32 v188,v154,v154\n"
"v_fmac_f32 v189,v155,v155\n"
"v_fmac_f32 v188,v156,v156\n"
"v_fmac_f32 v189,v157,v157\n"
"v_fmac_f32 v188,v158,v158\n"
"v_fmac_f32 v189,v159,v159\n"
"v_fmac_f32 v188,v160,v160\n"
"v_fmac_f32 v189,v161,v161\n"
"v_fmac_f32 v188,v162,v162\n"
"v_fmac_f32 v189,v163,v163\n"
"v_fmac_f32 v188,v164,v164\n"
"v_fmac_f32 v189,v165,v165\n"
"v_fmac_f32 v188,v166,v166\n"
"v_fmac_f32 v189,v167,v167\n"
"v_add_f32 v188,v188,v189\n"
PK"v149,v148,v149\n"
"v_lshlrev_b32 v148,16,v149\n"
"v_add_f32_dpp v188,v188,v188 quad_perm:[1,0,3,2] row_mask:0xf bank_mask:0xf\n"
"v_and_b32 v149,0xffff0000,v149\n"
PK"v151,v150,v151\n"
"v_add_f32_dpp v188,v188,v188 quad_perm:[2,3,0,1] row_mask:0xf bank_mask:0xf\n"
"v_lshlrev_b32 v150,16,v151\n"
"v_and_b32 v151,0xffff0000,v151\n"
"v_add_f32_dpp v188,v188,v188 row_half_mirror row_mask:0xf bank_mask:0xf\n"
PK"v153,v152,v153\n"
"v_lshlrev_b32 v152,16,v153\n"
"v_add_f32_dpp v188,v188,v188 row_mirror row_mask:0xf bank_mask:0xf\n"
"v_and_b32 v153,0xffff0000,v153\n"
PK"v155,v154,v155\n"
"v_mov_b32 v189,v188\n"
"v_lshlrev_b32 v154,16,v155\n"
"v_and_b32 v155,0xffff0000,v155\n"
"v_permlane16_swap_b32 v189,v188\n"
PK"v157,v156,v157\n"
"v_lshlrev_b32 v156,16,v157\n"
"v_add_f32 v188,v188,v189\n"
"v_and_b32 v157,0xffff0000,v157\n"
PK"v159,v158,v159\n"
"v_sub_f32 v191,v191,v128\n"
"v_lshlrev_b32 v158,16,v159\n"
"v_and_b32 v159,0xffff0000,v159\n"
MU"v188,0x3acccccd,v188\n"
PK"v161,v160,v161\n"
"v_lshlrev_b32 v160,16,v161\n"
"v_fma_f32 v188,-v191,v191,v188\n"
"v_and_b32 v161,0xffff0000,v161\n"
PK"v163,v162,v163\n"
"v_max_f32 v188,0,v188\n"
"v_lshlrev_b32 v162,16,v163\n"
"v_and_b32 v163,0xffff0000,v163\n"
PK"v188,v188,v188\n"
PK"v165,v164,v165\n"
"v_lshlrev_b32 v164,16,v165\n"
"v_lshlrev_b32 v188,16,v188\n"
"v_and_b32 v165,0xffff0000,v165\n"
PK"v167,v166,v167\n"
"v_add_f32 v188,0x3727c5ac,v188\n"
"v_lshlrev_b32 v166,16,v167\n"
"v_and_b32 v167,0xffff0000,v167\n"
PK"v188,v188,v188\n"
"v_bfe_u32 v130,v224,5,4\n"
"v_mul_u32_u24 v130,144,v130\n"
"v_lshlrev_b32 v188,16,v188\n"
SA"m0,s37,12288\n"
SA"s48,s47,786432\n"
"s_and_b32 s48,s48,4194303\n"
BL"v238,s[16:19],s48 offen offset:0 lds\n"
BL"v239,s[16:19],s48 offen offset:1024 lds\n"
BL"v240,s[16:19],s48 offen offset:2048 lds\n"
BL"v241,s[16:19],s48 offen offset:3072 lds\n"
"v_bfe_u32 v131,v224,4,1\n"
"v_lshl_add_u32 v130,v131,3,v130\n"
"v_sqrt_f32 v189,v188\n"
"v_lshrrev_b32 v131,9,v224\n"
"v_lshl_add_u32 v130,v131,4,v130\n"
"v_rcp_f32 v190,v189\n"
"s_lshl_b32 s52,s30,5\n"
"v_add_u32 v130,s52,v130\n"
"v_fma_f32 v188,-v189,v189,v188\n"
MU"v190,0.5,v190\n"
"v_fma_f32 v189,v188,v190,v189\n"
PK"v189,v189,v189\n"
"v_lshlrev_b32 v189,16,v189\n"
"v_rcp_f32 v129,v189\n"
"s_nop 0\n"
MU"v148,v148,v129\n"
MU"v149,v149,v129\n"
PK"v138,v148,v149\n"
MU"v150,v150,v129\n"
MU"v151,v151,v129\n"
PK"v139,v150,v151\n"
"ds_write_b64 v130,v[138:139] offset:49152\n"
MU"v152,v152,v129\n"
MU"v153,v153,v129\n"
PK"v140,v152,v153\n"
MU"v154,v154,v129\n"
MU"v155,v155,v129\n"
PK"v141,v154,v155\n"
"ds_write_b64 v130,v[140:141] offset:51456\n"
MU"v156,v156,v129\n"
MU"v157,v157,v129\n"
PK"v142,v156,v157\n"
MU"v158,v158,v129\n"
MU"v159,v159,v129\n"
PK"v143,v158,v159\n"
"ds_write_b64 v130,v[142:143] offset:53760\n"
MU"v160,v160,v129\n"
MU"v161,v161,v129\n"
PK"v144,v160,v161\n"
MU"v162,v162,v129\n"
MU"v163,v163,v129\n"
PK"v145,v162,v163\n"
"ds_write_b64 v130,v[144:145] offset:56064\n"
MU"v164,v164,v129\n"
MU"v165,v165,v129\n"
PK"v146,v164,v165\n"
MU"v166,v166,v129\n"
MU"v167,v167,v129\n"
PK"v147,v166,v167\n"
"ds_write_b64 v130,v[146:147] offset:58368\n"
"v_lshlrev_b32 v148,16,v138\n"
"v_and_b32 v149,0xffff0000,v138\n"
MU"v188,v148,v168\n"
MU"v189,v149,v169\n"
"v_lshlrev_b32 v150,16,v139\n"
"v_and_b32 v151,0xffff0000,v139\n"
"v_fmac_f32 v188,v150,v170\n"
"v_fmac_f32 v189,v151,v171\n"
"v_lshlrev_b32 v152,16,v140\n"
"v_and_b32 v153,0xffff0000,v140\n"
"v_fmac_f32 v188,v152,v172\n"
"v_fmac_f32 v189,v153,v173\n"
"v_lshlrev_b32 v154,16,v141\n"
"v_and_b32 v155,0xffff0000,v141\n"
"v_fmac_f32 v188,v154,v174\n"
"v_fmac_f32 v189,v155,v175\n"
"v_lshlrev_b32 v156,16,v142\n"
"v_and_b32 v157,0xffff0000,v142\n"
"v_fmac_f32 v188,v156,v176\n"
"v_fmac_f32 v189,v157,v177\n"
"v_lshlrev_b32 v158,16,v143\n"
"v_and_b32 v159,0xffff0000,v143\n"
"v_fmac_f32 v188,v158,v178\n"
"v_fmac_f32 v189,v159,v179\n"
"v_lshlrev_b32 v160,16,v144\n"
"v_and_b32 v161,0xffff0000,v144\n"
"v_fmac_f32 v188,v160,v180\n"
"v_fmac_f32 v189,v161,v181\n"
"v_lshlrev_b32 v162,16,v145\n"
"v_and_b32 v163,0xffff0000,v145\n"
WT"vmcnt(21)\n"
RT"a[0:1],v246 offset:0\n"
RT"a[2:3],v246 offset:512\n"
RT"a[4:5],v247 offset:0\n"
RT"a[6:7],v247 offset:512\n"
RT"a[8:9],v248 offset:0\n"
RT"a[10:11],v248 offset:512\n"
RT"a[12:13],v249 offset:0\n"
RT"a[14:15],v249 offset:512\n"
WT"vmcnt(12)\n"
RT"a[16:17],v246 offset:4096\n"
RT"a[18:19],v246 offset:4608\n"
WT"lgkmcnt(10)\n"
"s_barrier\n"
RD"v[192:195],v242 offset:0\n"
RD"v[196:199],v242 offset:2048\n"
RD"v[200:203],v242 offset:4096\n"
RD"v[204:207],v242 offset:6144\n"
RD"v[208:211],v242 offset:8192\n"
"v_lshrrev_b32 v131,7,v224\n"
WT"lgkmcnt(4)\n"
MF"v[0:3],a[0:3],v[192:195],0\n"
SA"m0,s37,16384\n"
RD"v[212:215],v242 offset:10240\n"
"v_lshl_add_u32 v131,v131,4,v224\n"
MF"v[4:7],a[4:7],v[192:195],0\n"
SA"s48,s47,1048576\n"
"s_and_b32 s48,s48,4194303\n"
"s_mul_i32 s52,s30,1152\n"
MF"v[8:11],a[8:11],v[192:195],0\n"
RT"a[20:21],v247 offset:4096\n"
"v_add_u32 v128,s52,v131\n"
MF"v[12:15],a[12:15],v[192:195],0\n"
WT"lgkmcnt(5)\n"
BL"v238,s[16:19],s48 offen offset:0 lds\n"
MF"v[16:19],a[0:3],v[196:199],0\n"
"s_and_b32 s52,s30,1\n"
RD"v[216:219],v242 offset:12288\n"
"v_fmac_f32 v188,v162,v182\n"
MF"v[20:23],a[4:7],v[196:199],0\n"
"s_mul_i32 s51,s52,1152\n"
"v_fmac_f32 v189,v163,v183\n"
"v_add_u32 v131,s51,v131\n"
MF"v[24:27],a[8:11],v[196:199],0\n"
"s_lshl_b32 s52,s52,10\n"
RT"a[22:23],v247 offset:4608\n"
"v_lshlrev_b32 v164,16,v146\n"
MF"v[28:31],a[12:15],v[196:199],0\n"
WT"lgkmcnt(3)\n"
BL"v239,s[16:19],s48 offen offset:1024 lds\n"
MF"v[32:35],a[0:3],v[200:203],0\n"
RD"v[220:223],v242 offset:14336\n"
"v_and_b32 v129,0x70,v224\n"
MF"v[36:39],a[4:7],v[200:203],0\n"
"s_lshr_b32 s50,s31,1\n"
RD"v[132:135],v128 offset:49152\n"
"v_and_b32 v165,0xffff0000,v146\n"
MF"v[40:43],a[8:11],v[200:203],0\n"
"s_mul_i32 s50,s50,20480\n"
RD"a[240:243],v128 offset:53760\n"
"v_and_b32 v130,0x380,v224\n"
MF"v[44:47],a[12:15],v[200:203],0\n"
"s_and_b32 s51,s31,1\n"
RD"a[244:247],v131 offset:58368\n"
"v_fmac_f32 v188,v164,v184\n"
MF"v[48:51],a[0:3],v[204:207],0\n"
"s_lshl_b32 s51,s51,7\n"
RD"v[192:195],v243 offset:0\n"
"v_lshl_add_u32 v129,v130,1,v129\n"
MF"v[52:55],a[4:7],v[204:207],0\n"
SA"s50,s50,s51\n"
RT"a[24:25],v248 offset:4096\n"
"v_fmac_f32 v189,v165,v185\n"
MF"v[56:59],a[8:11],v[204:207],0\n"
SA"s50,s50,65536\n"
BL"v240,s[16:19],s48 offen offset:2048 lds\n"
MF"v[60:63],a[12:15],v[204:207],0\n"
"s_lshl_b32 s52,s52,1\n"
RD"v[196:199],v243 offset:2048\n"
"v_lshlrev_b32 v166,16,v147\n"
MF"v[64:67],a[0:3],v[208:211],0\n"
SA"s52,s52,s50\n"
RT"a[26:27],v248 offset:4608\n"
"v_and_b32 v167,0xffff0000,v147\n"
MF"v[68:71],a[4:7],v[208:211],0\n"
SA"s52,s52,16384\n"
BL"v241,s[16:19],s48 offen offset:3072 lds\n"
MF"v[72:75],a[8:11],v[208:211],0\n"
SA"m0,s36,32768\n"
RD"v[200:203],v243 offset:4096\n"
"v_fmac_f32 v188,v166,v186\n"
MF"v[76:79],a[12:15],v[208:211],0\n"
SA"s48,s46,256\n"
RT"a[28:29],v249 offset:4096\n"
"v_fmac_f32 v189,v167,v187\n"
MF"v[80:83],a[0:3],v[212:215],0\n"
"s_and_b32 s48,s48,1023\n"
"v_add_f32 v188,v188,v189\n"
"s_lshl_b32 s51,s30,11\n"
MF"v[84:87],a[4:7],v[212:215],0\n"
SA"s50,s50,s51\n"
BL"v234,s[12:15],s48 offen offset:0 lds\n"
MF"v[88:91],a[8:11],v[212:215],0\n"
SA"s51,s50,8192\n"
RD"v[204:207],v243 offset:6144\n"
"v_add_f32_dpp v188,v188,v188 quad_perm:[1,0,3,2] row_mask:0xf bank_mask:0xf\n"
MF"v[92:95],a[12:15],v[212:215],0\n"
WT"lgkmcnt(3)\n"
RT"a[30:31],v249 offset:4608\n"
"v_add_f32_dpp v188,v188,v188 quad_perm:[2,3,0,1] row_mask:0xf bank_mask:0xf\n"
MF"v[96:99],a[0:3],v[216:219],0\n"
BS"v[132:135],v129,s[8:11],s50 offen sc1\n"
MF"v[100:103],a[4:7],v[216:219],0\n"
"s_lshl_b32 s90,s31,2\n"
RD"v[208:211],v243 offset:8192\n"
"v_add_f32_dpp v188,v188,v188 row_half_mirror row_mask:0xf bank_mask:0xf\n"
MF"v[104:107],a[8:11],v[216:219],0\n"
SA"s90,s90,s30\n"
BL"v235,s[12:15],s48 offen offset:1024 lds\n"
MF"v[108:111],a[12:15],v[216:219],0\n"
"s_lshl_b32 s90,s90,1\n"
BS"a[240:243],v129,s[8:11],s51 offen sc1\n"
MF"v[112:115],a[0:3],v[220:223],0\n"
"s_sub_u32 s91,2047,s90\n"
WT"vmcnt(16)\n"
RT"a[32:33],v246 offset:8192\n"
MF"v[116:119],a[4:7],v[220:223],0\n"
BS"a[244:247],v129,s[8:11],s52 offen sc1\n"
MF"v[120:123],a[8:11],v[220:223],0\n"
RD"v[212:215],v243 offset:10240\n"
"v_add_f32_dpp v188,v188,v188 row_mirror row_mask:0xf bank_mask:0xf\n"
MF"v[124:127],a[12:15],v[220:223],0\n"
BL"v236,s[12:15],s48 offen offset:2048 lds\n"
MF"v[0:3],a[16:19],v[192:195],v[0:3]\n"
RT"a[34:35],v246 offset:8704\n"
"v_mov_b32 v189,v188\n"
MF"v[4:7],a[20:23],v[192:195],v[4:7]\n"
RD"v[216:219],v243 offset:12288\n"
"v_permlane16_swap_b32 v189,v188\n"
MF"v[8:11],a[24:27],v[192:195],v[8:11]\n"
WT"lgkmcnt(4)\n"
BL"v237,s[12:15],s48 offen offset:3072 lds\n"
MF"v[12:15],a[28:31],v[192:195],v[12:15]\n"
"s_lshl_b32 s90,s90,2\n"
RT"a[36:37],v247 offset:8192\n"
"v_add_f32 v188,v188,v189\n"
MF"v[16:19],a[16:19],v[196:199],v[16:19]\n"
SA"s90,s90,2686976\n"
RD"v[220:223],v243 offset:14336\n"
PK"v188,v188,v188\n"
MF"v[20:23],a[20:23],v[196:199],v[20:23]\n"
SA"m0,s37,20480\n"
"v_lshlrev_b32 v188,16,v188\n"
"v_ashrrev_i32 v189,31,v188\n"
MF"v[24:27],a[24:27],v[196:199],v[24:27]\n"
SA"s48,s47,1310720\n"
RT"a[38:39],v247 offset:8704\n"
"v_or_b32 v189,0x80000000,v189\n"
MF"v[28:31],a[28:31],v[196:199],v[28:31]\n"
"s_and_b32 s48,s48,4194303\n"
"v_xor_b32 v188,v188,v189\n"
"v_and_b32 v188,0xffff0000,v188\n"
MF"v[32:35],a[16:19],v[200:203],v[32:35]\n"
BL"v238,s[16:19],s48 offen offset:0 lds\n"
MF"v[36:39],a[20:23],v[200:203],v[36:39]\n"
"v_lshrrev_b32 v189,9,v224\n"
"v_sub_u32 v190,s91,v189\n"
MF"v[40:43],a[24:27],v[200:203],v[40:43]\n"
RT"a[40:41],v248 offset:8192\n"
"v_or_b32 v188,v188,v190\n"
MF"v[44:47],a[28:31],v[200:203],v[44:47]\n"
BL"v239,s[16:19],s48 offen offset:1024 lds\n"
MF"v[48:51],a[16:19],v[204:207],v[48:51]\n"
"v_and_b32 v190,0x1f0,v224\n"
"v_lshlrev_b32 v190,23,v190\n"
MF"v[52:55],a[20:23],v[204:207],v[52:55]\n"
WT"vmcnt(17) lgkmcnt(2)\n"
"s_barrier\n"
MF"v[56:59],a[24:27],v[204:207],v[56:59]\n"
RD"v[192:195],v242 offset:16384\n"
"v_lshl_add_u32 v190,v189,2,v190\n"
MF"v[60:63],a[28:31],v[204:207],v[60:63]\n"
"buffer_store_dword v188,v190,s[8:11],s90 offen sc1\n"
MF"v[64:67],a[16:19],v[208:211],v[64:67]\n"
RT"a[42:43],v248 offset:8704\n"
MF"v[68:71],a[20:23],v[208:211],v[68:71]\n"
BL"v240,s[16:19],s48 offen offset:2048 lds\n"
MF"v[72:75],a[24:27],v[208:211],v[72:75]\n"
RD"v[196:199],v242 offset:18432\n"
MF"v[76:79],a[28:31],v[208:211],v[76:79]\n"
RT"a[44:45],v249 offset:8192\n"
MF"v[80:83],a[16:19],v[212:215],v[80:83]\n"
BL"v241,s[16:19],s48 offen offset:3072 lds\n"
MF"v[84:87],a[20:23],v[212:215],v[84:87]\n"
RD"v[200:203],v242 offset:20480\n"
MF"v[88:91],a[24:27],v[212:215],v[88:91]\n"
SA"m0,s37,0\n"
RT"a[46:47],v249 offset:8704\n"
SA"s48,s47,1572864\n"
MF"v[92:95],a[28:31],v[212:215],v[92:95]\n"
"s_and_b32 s48,s48,4194303\n"
MF"v[96:99],a[16:19],v[216:219],v[96:99]\n"
BL"v238,s[16:19],s48 offen offset:0 lds\n"
MF"v[100:103],a[20:23],v[216:219],v[100:103]\n"
RD"v[204:207],v242 offset:22528\n"
MF"v[104:107],a[24:27],v[216:219],v[104:107]\n"
WT"vmcnt(17)\n"
RT"a[48:49],v246 offset:12288\n"
MF"v[108:111],a[28:31],v[216:219],v[108:111]\n"
MF"v[112:115],a[16:19],v[220:223],v[112:115]\n"
RD"v[208:211],v242 offset:24576\n"
MF"v[116:119],a[20:23],v[220:223],v[116:119]\n"
BL"v239,s[16:19],s48 offen offset:1024 lds\n"
MF"v[120:123],a[24:27],v[220:223],v[120:123]\n"
RT"a[50:51],v246 offset:12800\n"
MF"v[124:127],a[28:31],v[220:223],v[124:127]\n"
WT"lgkmcnt(4)\n"
MF"v[0:3],a[32:35],v[192:195],v[0:3]\n"
RD"v[212:215],v242 offset:26624\n"
MF"v[4:7],a[36:39],v[192:195],v[4:7]\n"
BL"v240,s[16:19],s48 offen offset:2048 lds\n"
MF"v[8:11],a[40:43],v[192:195],v[8:11]\n"
RT"a[52:53],v247 offset:12288\n"
MF"v[12:15],a[44:47],v[192:195],v[12:15]\n"
RD"v[216:219],v242 offset:28672\n"
MF"v[16:19],a[32:35],v[196:199],v[16:19]\n"
MF"v[20:23],a[36:39],v[196:199],v[20:23]\n"
BL"v241,s[16:19],s48 offen offset:3072 lds\n"
MF"v[24:27],a[40:43],v[196:199],v[24:27]\n"
RT"a[54:55],v247 offset:12800\n"
MF"v[28:31],a[44:47],v[196:199],v[28:31]\n"
RD"v[220:223],v242 offset:30720\n"
MF"v[32:35],a[32:35],v[200:203],v[32:35]\n"
SA"m0,s36,0\n"
SA"s48,s46,384\n"
"s_and_b32 s48,s48,1023\n"
MF"v[36:39],a[36:39],v[200:203],v[36:39]\n"
MF"v[40:43],a[40:43],v[200:203],v[40:43]\n"
RD"v[192:195],v243 offset:16384\n"
MF"v[44:47],a[44:47],v[200:203],v[44:47]\n"
WT"lgkmcnt(3)\n"
RT"a[56:57],v248 offset:12288\n"
MF"v[48:51],a[32:35],v[204:207],v[48:51]\n"
BL"v234,s[12:15],s48 offen offset:0 lds\n"
MF"v[52:55],a[36:39],v[204:207],v[52:55]\n"
MF"v[56:59],a[40:43],v[204:207],v[56:59]\n"
RD"v[196:199],v243 offset:18432\n"
MF"v[60:63],a[44:47],v[204:207],v[60:63]\n"
RT"a[58:59],v248 offset:12800\n"
MF"v[64:67],a[32:35],v[208:211],v[64:67]\n"
BL"v235,s[12:15],s48 offen offset:1024 lds\n"
MF"v[68:71],a[36:39],v[208:211],v[68:71]\n"
RD"v[200:203],v243 offset:20480\n"
MF"v[72:75],a[40:43],v[208:211],v[72:75]\n"
RT"a[60:61],v249 offset:12288\n"
MF"v[76:79],a[44:47],v[208:211],v[76:79]\n"
BL"v236,s[12:15],s48 offen offset:2048 lds\n"
MF"v[80:83],a[32:35],v[212:215],v[80:83]\n"
MF"v[84:87],a[36:39],v[212:215],v[84:87]\n"
RD"v[204:207],v243 offset:22528\n"
MF"v[88:91],a[40:43],v[212:215],v[88:91]\n"
RT"a[62:63],v249 offset:12800\n"
MF"v[92:95],a[44:47],v[212:215],v[92:95]\n"
BL"v237,s[12:15],s48 offen offset:3072 lds\n"
MF"v[96:99],a[32:35],v[216:219],v[96:99]\n"
RD"v[208:211],v243 offset:24576\n"
MF"v[100:103],a[36:39],v[216:219],v[100:103]\n"
MF"v[104:107],a[40:43],v[216:219],v[104:107]\n"
SA"m0,s37,4096\n"
WT"vmcnt(20)\n"
RT"a[64:65],v246 offset:16384\n"
MF"v[108:111],a[44:47],v[216:219],v[108:111]\n"
WT"lgkmcnt(4)\n"
SA"s48,s47,1835008\n"
"s_and_b32 s48,s48,4194303\n"
MF"v[112:115],a[32:35],v[220:223],v[112:115]\n"
RD"v[212:215],v243 offset:26624\n"
MF"v[116:119],a[36:39],v[220:223],v[116:119]\n"
BL"v238,s[16:19],s48 offen offset:0 lds\n"
MF"v[120:123],a[40:43],v[220:223],v[120:123]\n"
RT"a[66:67],v246 offset:16896\n"
MF"v[124:127],a[44:47],v[220:223],v[124:127]\n"
RD"v[216:219],v243 offset:28672\n"
MF"v[0:3],a[48:51],v[192:195],v[0:3]\n"
BL"v239,s[16:19],s48 offen offset:1024 lds\n"
MF"v[4:7],a[52:55],v[192:195],v[4:7]\n"
MF"v[8:11],a[56:59],v[192:195],v[8:11]\n"
WT"lgkmcnt(3)\n"
RT"a[68:69],v247 offset:16384\n"
MF"v[12:15],a[60:63],v[192:195],v[12:15]\n"
RD"v[220:223],v243 offset:30720\n"
MF"v[16:19],a[48:51],v[196:199],v[16:19]\n"
MF"v[20:23],a[52:55],v[196:199],v[20:23]\n"
BL"v240,s[16:19],s48 offen offset:2048 lds\n"
MF"v[24:27],a[56:59],v[196:199],v[24:27]\n"
RT"a[70:71],v247 offset:16896\n"
MF"v[28:31],a[60:63],v[196:199],v[28:31]\n"
MF"v[32:35],a[48:51],v[200:203],v[32:35]\n"
"s_lshl_b32 s51,s32,8\n"
"s_andn2_b32 s50,s31,7\n"
SA"s50,s50,s51\n"
MF"v[36:39],a[52:55],v[200:203],v[36:39]\n"
SA"s50,s50,256\n"
BL"v241,s[16:19],s48 offen offset:3072 lds\n"
MF"v[40:43],a[56:59],v[200:203],v[40:43]\n"
SA"s50,s50,s45\n"
RT"a[72:73],v248 offset:16384\n"
MF"v[44:47],a[60:63],v[200:203],v[44:47]\n"
MF"v[48:51],a[48:51],v[204:207],v[48:51]\n"
SA"m0,s37,8192\n"
SA"s48,s47,2097152\n"
"s_and_b32 s48,s48,4194303\n"
MF"v[52:55],a[52:55],v[204:207],v[52:55]\n"
WT"vmcnt(14) lgkmcnt(2)\n"
"s_barrier\n"
MF"v[56:59],a[56:59],v[204:207],v[56:59]\n"
RD"v[192:195],v242 offset:32768\n"
MF"v[60:63],a[60:63],v[204:207],v[60:63]\n"
RT"a[74:75],v248 offset:16896\n"
MF"v[64:67],a[48:51],v[208:211],v[64:67]\n"
BL"v238,s[16:19],s48 offen offset:0 lds\n"
MF"v[68:71],a[52:55],v[208:211],v[68:71]\n"
"buffer_store_dwordx2 v[232:233],v230,s[8:11],s50 offen sc1\n"
MF"v[72:75],a[56:59],v[208:211],v[72:75]\n"
RD"v[196:199],v242 offset:34816\n"
MF"v[76:79],a[60:63],v[208:211],v[76:79]\n"
RT"a[76:77],v249 offset:16384\n"
MF"v[80:83],a[48:51],v[212:215],v[80:83]\n"
BL"v239,s[16:19],s48 offen offset:1024 lds\n"
MF"v[84:87],a[52:55],v[212:215],v[84:87]\n"
RD"v[200:203],v242 offset:36864\n"
MF"v[88:91],a[56:59],v[212:215],v[88:91]\n"
RT"a[78:79],v249 offset:16896\n"
MF"v[92:95],a[60:63],v[212:215],v[92:95]\n"
BL"v240,s[16:19],s48 offen offset:2048 lds\n"
MF"v[96:99],a[48:51],v[216:219],v[96:99]\n"
MF"v[100:103],a[52:55],v[216:219],v[100:103]\n"
RD"v[204:207],v242 offset:38912\n"
MF"v[104:107],a[56:59],v[216:219],v[104:107]\n"
WT"vmcnt(16)\n"
RT"a[80:81],v246 offset:20480\n"
MF"v[108:111],a[60:63],v[216:219],v[108:111]\n"
BL"v241,s[16:19],s48 offen offset:3072 lds\n"
MF"v[112:115],a[48:51],v[220:223],v[112:115]\n"
RD"v[208:211],v242 offset:40960\n"
MF"v[116:119],a[52:55],v[220:223],v[116:119]\n"
MF"v[120:123],a[56:59],v[220:223],v[120:123]\n"
SA"m0,s36,16384\n"
RT"a[82:83],v246 offset:20992\n"
SA"s48,s46,512\n"
MF"v[124:127],a[60:63],v[220:223],v[124:127]\n"
WT"lgkmcnt(4)\n"
"s_and_b32 s48,s48,1023\n"
MF"v[0:3],a[64:67],v[192:195],v[0:3]\n"
RD"v[212:215],v242 offset:43008\n"
MF"v[4:7],a[68:71],v[192:195],v[4:7]\n"
BL"v234,s[12:15],s48 offen offset:0 lds\n"
MF"v[8:11],a[72:75],v[192:195],v[8:11]\n"
RT"a[84:85],v247 offset:20480\n"
MF"v[12:15],a[76:79],v[192:195],v[12:15]\n"
RD"v[216:219],v242 offset:45056\n"
MF"v[16:19],a[64:67],v[196:199],v[16:19]\n"
BL"v235,s[12:15],s48 offen offset:1024 lds\n"
MF"v[20:23],a[68:71],v[196:199],v[20:23]\n"
MF"v[24:27],a[72:75],v[196:199],v[24:27]\n"
RT"a[86:87],v247 offset:20992\n"
MF"v[28:31],a[76:79],v[196:199],v[28:31]\n"
RD"v[220:223],v242 offset:47104\n"
MF"v[32:35],a[64:67],v[200:203],v[32:35]\n"
MF"v[36:39],a[68:71],v[200:203],v[36:39]\n"
BL"v236,s[12:15],s48 offen offset:2048 lds\n"
MF"v[40:43],a[72:75],v[200:203],v[40:43]\n"
RD"v[192:195],v243 offset:32768\n"
MF"v[44:47],a[76:79],v[200:203],v[44:47]\n"
WT"lgkmcnt(3)\n"
RT"a[88:89],v248 offset:20480\n"
MF"v[48:51],a[64:67],v[204:207],v[48:51]\n"
MF"v[52:55],a[68:71],v[204:207],v[52:55]\n"
BL"v237,s[12:15],s48 offen offset:3072 lds\n"
MF"v[56:59],a[72:75],v[204:207],v[56:59]\n"
RD"v[196:199],v243 offset:34816\n"
MF"v[60:63],a[76:79],v[204:207],v[60:63]\n"
RT"a[90:91],v248 offset:20992\n"
MF"v[64:67],a[64:67],v[208:211],v[64:67]\n"
SA"m0,s37,12288\n"
SA"s48,s47,2359296\n"
"s_and_b32 s48,s48,4194303\n"
MF"v[68:71],a[68:71],v[208:211],v[68:71]\n"
RD"v[200:203],v243 offset:36864\n"
MF"v[72:75],a[72:75],v[208:211],v[72:75]\n"
RT"a[92:93],v249 offset:20480\n"
MF"v[76:79],a[76:79],v[208:211],v[76:79]\n"
BL"v238,s[16:19],s48 offen offset:0 lds\n"
MF"v[80:83],a[64:67],v[212:215],v[80:83]\n"
MF"v[84:87],a[68:71],v[212:215],v[84:87]\n"
RD"v[204:207],v243 offset:38912\n"
MF"v[88:91],a[72:75],v[212:215],v[88:91]\n"
RT"a[94:95],v249 offset:20992\n"
MF"v[92:95],a[76:79],v[212:215],v[92:95]\n"
BL"v239,s[16:19],s48 offen offset:1024 lds\n"
MF"v[96:99],a[64:67],v[216:219],v[96:99]\n"
RD"v[208:211],v243 offset:40960\n"
MF"v[100:103],a[68:71],v[216:219],v[100:103]\n"
MF"v[104:107],a[72:75],v[216:219],v[104:107]\n"
WT"vmcnt(19)\n"
RT"a[96:97],v246 offset:0\n"
MF"v[108:111],a[76:79],v[216:219],v[108:111]\n"
WT"lgkmcnt(4)\n"
BL"v240,s[16:19],s48 offen offset:2048 lds\n"
MF"v[112:115],a[64:67],v[220:223],v[112:115]\n"
RD"v[212:215],v243 offset:43008\n"
MF"v[116:119],a[68:71],v[220:223],v[116:119]\n"
MF"v[120:123],a[72:75],v[220:223],v[120:123]\n"
RT"a[98:99],v246 offset:512\n"
MF"v[124:127],a[76:79],v[220:223],v[124:127]\n"
RD"v[216:219],v243 offset:45056\n"
MF"v[0:3],a[80:83],v[192:195],v[0:3]\n"
BL"v241,s[16:19],s48 offen offset:3072 lds\n"
MF"v[4:7],a[84:87],v[192:195],v[4:7]\n"
MF"v[8:11],a[88:91],v[192:195],v[8:11]\n"
WT"lgkmcnt(3)\n"
RT"a[100:101],v247 offset:0\n"
SA"m0,s37,16384\n"
MF"v[12:15],a[92:95],v[192:195],v[12:15]\n"
SA"s48,s47,2621440\n"
RD"v[220:223],v243 offset:47104\n"
"s_and_b32 s48,s48,4194303\n"
MF"v[16:19],a[80:83],v[196:199],v[16:19]\n"
BL"v238,s[16:19],s48 offen offset:0 lds\n"
MF"v[20:23],a[84:87],v[196:199],v[20:23]\n"
MF"v[24:27],a[88:91],v[196:199],v[24:27]\n"
RT"a[102:103],v247 offset:512\n"
MF"v[28:31],a[92:95],v[196:199],v[28:31]\n"
MF"v[32:35],a[80:83],v[200:203],v[32:35]\n"
BL"v239,s[16:19],s48 offen offset:1024 lds\n"
MF"v[36:39],a[84:87],v[200:203],v[36:39]\n"
MF"v[40:43],a[88:91],v[200:203],v[40:43]\n"
RT"a[104:105],v248 offset:0\n"
MF"v[44:47],a[92:95],v[200:203],v[44:47]\n"
MF"v[48:51],a[80:83],v[204:207],v[48:51]\n"
MF"v[52:55],a[84:87],v[204:207],v[52:55]\n"
WT"vmcnt(19) lgkmcnt(2)\n"
"s_barrier\n"
MF"v[56:59],a[88:91],v[204:207],v[56:59]\n"
BL"v240,s[16:19],s48 offen offset:2048 lds\n"
MF"v[60:63],a[92:95],v[204:207],v[60:63]\n"
RD"v[192:195],v242 offset:0\n"
MF"v[64:67],a[80:83],v[208:211],v[64:67]\n"
RT"a[106:107],v248 offset:512\n"
MF"v[68:71],a[84:87],v[208:211],v[68:71]\n"
BL"v241,s[16:19],s48 offen offset:3072 lds\n"
MF"v[72:75],a[88:91],v[208:211],v[72:75]\n"
RD"v[196:199],v242 offset:2048\n"
MF"v[76:79],a[92:95],v[208:211],v[76:79]\n"
RT"a[108:109],v249 offset:0\n"
MF"v[80:83],a[80:83],v[212:215],v[80:83]\n"
SA"m0,s36,32768\n"
SA"s48,s46,640\n"
"s_and_b32 s48,s48,1023\n"
MF"v[84:87],a[84:87],v[212:215],v[84:87]\n"
RD"v[200:203],v242 offset:4096\n"
MF"v[88:91],a[88:91],v[212:215],v[88:91]\n"
RT"a[110:111],v249 offset:512\n"
MF"v[92:95],a[92:95],v[212:215],v[92:95]\n"
BL"v234,s[12:15],s48 offen offset:0 lds\n"
MF"v[96:99],a[80:83],v[216:219],v[96:99]\n"
MF"v[100:103],a[84:87],v[216:219],v[100:103]\n"
RD"v[204:207],v242 offset:6144\n"
MF"v[104:107],a[88:91],v[216:219],v[104:107]\n"
WT"vmcnt(18)\n"
RT"a[112:113],v246 offset:4096\n"
MF"v[108:111],a[92:95],v[216:219],v[108:111]\n"
BL"v235,s[12:15],s48 offen offset:1024 lds\n"
MF"v[112:115],a[80:83],v[220:223],v[112:115]\n"
RD"v[208:211],v242 offset:8192\n"
MF"v[116:119],a[84:87],v[220:223],v[116:119]\n"
MF"v[120:123],a[88:91],v[220:223],v[120:123]\n"
RT"a[114:115],v246 offset:4608\n"
MF"v[124:127],a[92:95],v[220:223],v[124:127]\n"
WT"lgkmcnt(4)\n"
BL"v236,s[12:15],s48 offen offset:2048 lds\n"
MF"v[0:3],a[96:99],v[192:195],v[0:3]\n"
RD"v[212:215],v242 offset:10240\n"
MF"v[4:7],a[100:103],v[192:195],v[4:7]\n"
MF"v[8:11],a[104:107],v[192:195],v[8:11]\n"
RT"a[116:117],v247 offset:4096\n"
MF"v[12:15],a[108:111],v[192:195],v[12:15]\n"
RD"v[216:219],v242 offset:12288\n"
MF"v[16:19],a[96:99],v[196:199],v[16:19]\n"
BL"v237,s[12:15],s48 offen offset:3072 lds\n"
MF"v[20:23],a[100:103],v[196:199],v[20:23]\n"
MF"v[24:27],a[104:107],v[196:199],v[24:27]\n"
SA"m0,s37,20480\n"
RT"a[118:119],v247 offset:4608\n"
SA"s48,s47,2883584\n"
MF"v[28:31],a[108:111],v[196:199],v[28:31]\n"
"s_and_b32 s48,s48,4194303\n"
RD"v[220:223],v242 offset:14336\n"
MF"v[32:35],a[96:99],v[200:203],v[32:35]\n"
BL"v238,s[16:19],s48 offen offset:0 lds\n"
MF"v[36:39],a[100:103],v[200:203],v[36:39]\n"
MF"v[40:43],a[104:107],v[200:203],v[40:43]\n"
RD"v[192:195],v243 offset:0\n"
MF"v[44:47],a[108:111],v[200:203],v[44:47]\n"
WT"lgkmcnt(3)\n"
RT"a[120:121],v248 offset:4096\n"
MF"v[48:51],a[96:99],v[204:207],v[48:51]\n"
BL"v239,s[16:19],s48 offen offset:1024 lds\n"
MF"v[52:55],a[100:103],v[204:207],v[52:55]\n"
MF"v[56:59],a[104:107],v[204:207],v[56:59]\n"
RD"v[196:199],v243 offset:2048\n"
MF"v[60:63],a[108:111],v[204:207],v[60:63]\n"
RT"a[122:123],v248 offset:4608\n"
MF"v[64:67],a[96:99],v[208:211],v[64:67]\n"
MF"v[68:71],a[100:103],v[208:211],v[68:71]\n"
RD"v[200:203],v243 offset:4096\n"
MF"v[72:75],a[104:107],v[208:211],v[72:75]\n"
BL"v240,s[16:19],s48 offen offset:2048 lds\n"
MF"v[76:79],a[108:111],v[208:211],v[76:79]\n"
RT"a[124:125],v249 offset:4096\n"
MF"v[80:83],a[96:99],v[212:215],v[80:83]\n"
MF"v[84:87],a[100:103],v[212:215],v[84:87]\n"
RD"v[204:207],v243 offset:6144\n"
MF"v[88:91],a[104:107],v[212:215],v[88:91]\n"
BL"v241,s[16:19],s48 offen offset:3072 lds\n"
MF"v[92:95],a[108:111],v[212:215],v[92:95]\n"
RT"a[126:127],v249 offset:4608\n"
MF"v[96:99],a[96:99],v[216:219],v[96:99]\n"
SA"m0,s37,0\n"
RD"v[208:211],v243 offset:8192\n"
SA"s48,s47,3145728\n"
MF"v[100:103],a[100:103],v[216:219],v[100:103]\n"
"s_and_b32 s48,s48,4194303\n"
MF"v[104:107],a[104:107],v[216:219],v[104:107]\n"
WT"vmcnt(20)\n"
RT"a[128:129],v246 offset:8192\n"
MF"v[108:111],a[108:111],v[216:219],v[108:111]\n"
WT"lgkmcnt(4)\n"
BL"v238,s[16:19],s48 offen offset:0 lds\n"
MF"v[112:115],a[96:99],v[220:223],v[112:115]\n"
RD"v[212:215],v243 offset:10240\n"
MF"v[116:119],a[100:103],v[220:223],v[116:119]\n"
MF"v[120:123],a[104:107],v[220:223],v[120:123]\n"
RT"a[130:131],v246 offset:8704\n"
MF"v[124:127],a[108:111],v[220:223],v[124:127]\n"
BL"v239,s[16:19],s48 offen offset:1024 lds\n"
MF"v[0:3],a[112:115],v[192:195],v[0:3]\n"
RD"v[216:219],v243 offset:12288\n"
MF"v[4:7],a[116:119],v[192:195],v[4:7]\n"
MF"v[8:11],a[120:123],v[192:195],v[8:11]\n"
WT"lgkmcnt(3)\n"
RT"a[132:133],v247 offset:8192\n"
MF"v[12:15],a[124:127],v[192:195],v[12:15]\n"
RD"v[220:223],v243 offset:14336\n"
MF"v[16:19],a[112:115],v[196:199],v[16:19]\n"
BL"v240,s[16:19],s48 offen offset:2048 lds\n"
MF"v[20:23],a[116:119],v[196:199],v[20:23]\n"
MF"v[24:27],a[120:123],v[196:199],v[24:27]\n"
RT"a[134:135],v247 offset:8704\n"
MF"v[28:31],a[124:127],v[196:199],v[28:31]\n"
BL"v241,s[16:19],s48 offen offset:3072 lds\n"
MF"v[32:35],a[112:115],v[200:203],v[32:35]\n"
MF"v[36:39],a[116:119],v[200:203],v[36:39]\n"
MF"v[40:43],a[120:123],v[200:203],v[40:43]\n"
RT"a[136:137],v248 offset:8192\n"
MF"v[44:47],a[124:127],v[200:203],v[44:47]\n"
MF"v[48:51],a[112:115],v[204:207],v[48:51]\n"
MF"v[52:55],a[116:119],v[204:207],v[52:55]\n"
WT"vmcnt(20) lgkmcnt(2)\n"
"s_barrier\n"
MF"v[56:59],a[120:123],v[204:207],v[56:59]\n"
SA"m0,s36,0\n"
RD"v[192:195],v242 offset:16384\n"
SA"s48,s46,768\n"
MF"v[60:63],a[124:127],v[204:207],v[60:63]\n"
"s_and_b32 s48,s48,1023\n"
RT"a[138:139],v248 offset:8704\n"
MF"v[64:67],a[112:115],v[208:211],v[64:67]\n"
BL"v234,s[12:15],s48 offen offset:0 lds\n"
MF"v[68:71],a[116:119],v[208:211],v[68:71]\n"
MF"v[72:75],a[120:123],v[208:211],v[72:75]\n"
RD"v[196:199],v242 offset:18432\n"
MF"v[76:79],a[124:127],v[208:211],v[76:79]\n"
RT"a[140:141],v249 offset:8192\n"
MF"v[80:83],a[112:115],v[212:215],v[80:83]\n"
BL"v235,s[12:15],s48 offen offset:1024 lds\n"
MF"v[84:87],a[116:119],v[212:215],v[84:87]\n"
RD"v[200:203],v242 offset:20480\n"
MF"v[88:91],a[120:123],v[212:215],v[88:91]\n"
RT"a[142:143],v249 offset:8704\n"
MF"v[92:95],a[124:127],v[212:215],v[92:95]\n"
MF"v[96:99],a[112:115],v[216:219],v[96:99]\n"
MF"v[100:103],a[116:119],v[216:219],v[100:103]\n"
RD"v[204:207],v242 offset:22528\n"
MF"v[104:107],a[120:123],v[216:219],v[104:107]\n"
BL"v236,s[12:15],s48 offen offset:2048 lds\n"
MF"v[108:111],a[124:127],v[216:219],v[108:111]\n"
WT"vmcnt(19)\n"
RT"a[144:145],v246 offset:12288\n"
MF"v[112:115],a[112:115],v[220:223],v[112:115]\n"
RD"v[208:211],v242 offset:24576\n"
MF"v[116:119],a[116:119],v[220:223],v[116:119]\n"
BL"v237,s[12:15],s48 offen offset:3072 lds\n"
MF"v[120:123],a[120:123],v[220:223],v[120:123]\n"
RT"a[146:147],v246 offset:12800\n"
MF"v[124:127],a[124:127],v[220:223],v[124:127]\n"
WT"lgkmcnt(4)\n"
MF"v[0:3],a[128:131],v[192:195],v[0:3]\n"
SA"m0,s37,4096\n"
RD"v[212:215],v242 offset:26624\n"
SA"s48,s47,3407872\n"
MF"v[4:7],a[132:135],v[192:195],v[4:7]\n"
"s_and_b32 s48,s48,4194303\n"
MF"v[8:11],a[136:139],v[192:195],v[8:11]\n"
RT"a[148:149],v247 offset:12288\n"
MF"v[12:15],a[140:143],v[192:195],v[12:15]\n"
BL"v238,s[16:19],s48 offen offset:0 lds\n"
MF"v[16:19],a[128:131],v[196:199],v[16:19]\n"
RD"v[216:219],v242 offset:28672\n"
MF"v[20:23],a[132:135],v[196:199],v[20:23]\n"
MF"v[24:27],a[136:139],v[196:199],v[24:27]\n"
RT"a[150:151],v247 offset:12800\n"
MF"v[28:31],a[140:143],v[196:199],v[28:31]\n"
BL"v239,s[16:19],s48 offen offset:1024 lds\n"
MF"v[32:35],a[128:131],v[200:203],v[32:35]\n"
RD"v[220:223],v242 offset:30720\n"
MF"v[36:39],a[132:135],v[200:203],v[36:39]\n"
MF"v[40:43],a[136:139],v[200:203],v[40:43]\n"
RD"v[192:195],v243 offset:16384\n"
MF"v[44:47],a[140:143],v[200:203],v[44:47]\n"
WT"lgkmcnt(3)\n"
RT"a[152:153],v248 offset:12288\n"
MF"v[48:51],a[128:131],v[204:207],v[48:51]\n"
BL"v240,s[16:19],s48 offen offset:2048 lds\n"
MF"v[52:55],a[132:135],v[204:207],v[52:55]\n"
MF"v[56:59],a[136:139],v[204:207],v[56:59]\n"
RD"v[196:199],v243 offset:18432\n"
MF"v[60:63],a[140:143],v[204:207],v[60:63]\n"
RT"a[154:155],v248 offset:12800\n"
MF"v[64:67],a[128:131],v[208:211],v[64:67]\n"
BL"v241,s[16:19],s48 offen offset:3072 lds\n"
MF"v[68:71],a[132:135],v[208:211],v[68:71]\n"
RD"v[200:203],v243 offset:20480\n"
MF"v[72:75],a[136:139],v[208:211],v[72:75]\n"
SA"m0,s37,8192\n"
RT"a[156:157],v249 offset:12288\n"
SA"s48,s47,3670016\n"
MF"v[76:79],a[140:143],v[208:211],v[76:79]\n"
"s_and_b32 s48,s48,4194303\n"
MF"v[80:83],a[128:131],v[212:215],v[80:83]\n"
BL"v238,s[16:19],s48 offen offset:0 lds\n"
MF"v[84:87],a[132:135],v[212:215],v[84:87]\n"
RD"v[204:207],v243 offset:22528\n"
MF"v[88:91],a[136:139],v[212:215],v[88:91]\n"
RT"a[158:159],v249 offset:12800\n"
MF"v[92:95],a[140:143],v[212:215],v[92:95]\n"
MF"v[96:99],a[128:131],v[216:219],v[96:99]\n"
RD"v[208:211],v243 offset:24576\n"
MF"v[100:103],a[132:135],v[216:219],v[100:103]\n"
BL"v239,s[16:19],s48 offen offset:1024 lds\n"
MF"v[104:107],a[136:139],v[216:219],v[104:107]\n"
WT"vmcnt(22)\n"
RT"a[160:161],v246 offset:16384\n"
MF"v[108:111],a[140:143],v[216:219],v[108:111]\n"
WT"lgkmcnt(4)\n"
MF"v[112:115],a[128:131],v[220:223],v[112:115]\n"
RD"v[212:215],v243 offset:26624\n"
MF"v[116:119],a[132:135],v[220:223],v[116:119]\n"
BL"v240,s[16:19],s48 offen offset:2048 lds\n"
MF"v[120:123],a[136:139],v[220:223],v[120:123]\n"
RT"a[162:163],v246 offset:16896\n"
MF"v[124:127],a[140:143],v[220:223],v[124:127]\n"
RD"v[216:219],v243 offset:28672\n"
MF"v[0:3],a[144:147],v[192:195],v[0:3]\n"
MF"v[4:7],a[148:151],v[192:195],v[4:7]\n"
BL"v241,s[16:19],s48 offen offset:3072 lds\n"
MF"v[8:11],a[152:155],v[192:195],v[8:11]\n"
WT"lgkmcnt(3)\n"
RT"a[164:165],v247 offset:16384\n"
MF"v[12:15],a[156:159],v[192:195],v[12:15]\n"
RD"v[220:223],v243 offset:30720\n"
MF"v[16:19],a[144:147],v[196:199],v[16:19]\n"
MF"v[20:23],a[148:151],v[196:199],v[20:23]\n"
MF"v[24:27],a[152:155],v[196:199],v[24:27]\n"
RT"a[166:167],v247 offset:16896\n"
MF"v[28:31],a[156:159],v[196:199],v[28:31]\n"
MF"v[32:35],a[144:147],v[200:203],v[32:35]\n"
MF"v[36:39],a[148:151],v[200:203],v[36:39]\n"
MF"v[40:43],a[152:155],v[200:203],v[40:43]\n"
RT"a[168:169],v248 offset:16384\n"
MF"v[44:47],a[156:159],v[200:203],v[44:47]\n"
MF"v[48:51],a[144:147],v[204:207],v[48:51]\n"
MF"v[52:55],a[148:151],v[204:207],v[52:55]\n"
WT"vmcnt(20) lgkmcnt(2)\n"
"s_barrier\n"
MF"v[56:59],a[152:155],v[204:207],v[56:59]\n"
SA"m0,s36,16384\n"
RD"v[192:195],v242 offset:32768\n"
SA"s48,s46,896\n"
MF"v[60:63],a[156:159],v[204:207],v[60:63]\n"
"s_and_b32 s48,s48,1023\n"
RT"a[170:171],v248 offset:16896\n"
MF"v[64:67],a[144:147],v[208:211],v[64:67]\n"
BL"v234,s[12:15],s48 offen offset:0 lds\n"
MF"v[68:71],a[148:151],v[208:211],v[68:71]\n"
MF"v[72:75],a[152:155],v[208:211],v[72:75]\n"
RD"v[196:199],v242 offset:34816\n"
MF"v[76:79],a[156:159],v[208:211],v[76:79]\n"
RT"a[172:173],v249 offset:16384\n"
MF"v[80:83],a[144:147],v[212:215],v[80:83]\n"
BL"v235,s[12:15],s48 offen offset:1024 lds\n"
MF"v[84:87],a[148:151],v[212:215],v[84:87]\n"
RD"v[200:203],v242 offset:36864\n"
MF"v[88:91],a[152:155],v[212:215],v[88:91]\n"
RT"a[174:175],v249 offset:16896\n"
MF"v[92:95],a[156:159],v[212:215],v[92:95]\n"
MF"v[96:99],a[144:147],v[216:219],v[96:99]\n"
MF"v[100:103],a[148:151],v[216:219],v[100:103]\n"
RD"v[204:207],v242 offset:38912\n"
MF"v[104:107],a[152:155],v[216:219],v[104:107]\n"
BL"v236,s[12:15],s48 offen offset:2048 lds\n"
MF"v[108:111],a[156:159],v[216:219],v[108:111]\n"
WT"vmcnt(19)\n"
RT"a[176:177],v246 offset:20480\n"
MF"v[112:115],a[144:147],v[220:223],v[112:115]\n"
RD"v[208:211],v242 offset:40960\n"
MF"v[116:119],a[148:151],v[220:223],v[116:119]\n"
BL"v237,s[12:15],s48 offen offset:3072 lds\n"
MF"v[120:123],a[152:155],v[220:223],v[120:123]\n"
RT"a[178:179],v246 offset:20992\n"
MF"v[124:127],a[156:159],v[220:223],v[124:127]\n"
WT"lgkmcnt(4)\n"
MF"v[0:3],a[160:163],v[192:195],v[0:3]\n"
SA"m0,s37,12288\n"
RD"v[212:215],v242 offset:43008\n"
SA"s48,s47,3932160\n"
MF"v[4:7],a[164:167],v[192:195],v[4:7]\n"
"s_and_b32 s48,s48,4194303\n"
MF"v[8:11],a[168:171],v[192:195],v[8:11]\n"
RT"a[180:181],v247 offset:20480\n"
MF"v[12:15],a[172:175],v[192:195],v[12:15]\n"
BL"v238,s[16:19],s48 offen offset:0 lds\n"
MF"v[16:19],a[160:163],v[196:199],v[16:19]\n"
RD"v[216:219],v242 offset:45056\n"
MF"v[20:23],a[164:167],v[196:199],v[20:23]\n"
MF"v[24:27],a[168:171],v[196:199],v[24:27]\n"
RT"a[182:183],v247 offset:20992\n"
MF"v[28:31],a[172:175],v[196:199],v[28:31]\n"
BL"v239,s[16:19],s48 offen offset:1024 lds\n"
MF"v[32:35],a[160:163],v[200:203],v[32:35]\n"
RD"v[220:223],v242 offset:47104\n"
MF"v[36:39],a[164:167],v[200:203],v[36:39]\n"
MF"v[40:43],a[168:171],v[200:203],v[40:43]\n"
RD"v[192:195],v243 offset:32768\n"
MF"v[44:47],a[172:175],v[200:203],v[44:47]\n"
WT"lgkmcnt(3)\n"
RT"a[184:185],v248 offset:20480\n"
MF"v[48:51],a[160:163],v[204:207],v[48:51]\n"
BL"v240,s[16:19],s48 offen offset:2048 lds\n"
MF"v[52:55],a[164:167],v[204:207],v[52:55]\n"
MF"v[56:59],a[168:171],v[204:207],v[56:59]\n"
RD"v[196:199],v243 offset:34816\n"
MF"v[60:63],a[172:175],v[204:207],v[60:63]\n"
RT"a[186:187],v248 offset:20992\n"
MF"v[64:67],a[160:163],v[208:211],v[64:67]\n"
BL"v241,s[16:19],s48 offen offset:3072 lds\n"
MF"v[68:71],a[164:167],v[208:211],v[68:71]\n"
RD"v[200:203],v243 offset:36864\n"
MF"v[72:75],a[168:171],v[208:211],v[72:75]\n"
SA"m0,s37,16384\n"
RT"a[188:189],v249 offset:20480\n"
SA"s48,s39,0\n"
MF"v[76:79],a[172:175],v[208:211],v[76:79]\n"
MF"v[80:83],a[160:163],v[212:215],v[80:83]\n"
BL"v234,s[12:15],s48 offen offset:0 lds\n"
MF"v[84:87],a[164:167],v[212:215],v[84:87]\n"
RD"v[204:207],v243 offset:38912\n"
MF"v[88:91],a[168:171],v[212:215],v[88:91]\n"
RT"a[190:191],v249 offset:20992\n"
MF"v[92:95],a[172:175],v[212:215],v[92:95]\n"
"s_lshr_b32 s50,s30,1\n"
MF"v[96:99],a[160:163],v[216:219],v[96:99]\n"
"s_and_b32 s51,s30,1\n"
RD"v[208:211],v243 offset:40960\n"
MF"v[100:103],a[164:167],v[216:219],v[100:103]\n"
"s_mul_i32 s38,s50,24576\n"
BL"v235,s[12:15],s48 offen offset:1024 lds\n"
MF"v[104:107],a[168:171],v[216:219],v[104:107]\n"
"s_lshl_b32 s60,s50,18\n"
WT"vmcnt(22)\n"
RT"a[192:193],v246 offset:0\n"
MF"v[108:111],a[172:175],v[216:219],v[108:111]\n"
WT"lgkmcnt(4)\n"
"s_lshl_b32 s50,s51,12\n"
MF"v[112:115],a[160:163],v[220:223],v[112:115]\n"
SA"s38,s38,s50\n"
RD"v[212:215],v243 offset:43008\n"
MF"v[116:119],a[164:167],v[220:223],v[116:119]\n"
"s_lshl_b32 s50,s51,7\n"
BL"v236,s[12:15],s48 offen offset:2048 lds\n"
MF"v[120:123],a[168:171],v[220:223],v[120:123]\n"
SA"s60,s60,s50\n"
RT"a[194:195],v246 offset:512\n"
MF"v[124:127],a[172:175],v[220:223],v[124:127]\n"
RD"v[216:219],v243 offset:45056\n"
MF"v[0:3],a[176:179],v[192:195],v[0:3]\n"
MF"v[4:7],a[180:183],v[192:195],v[4:7]\n"
"s_sub_u32 s99,s34,5\n"
BL"v237,s[12:15],s48 offen offset:3072 lds\n"
MF"v[8:11],a[184:187],v[192:195],v[8:11]\n"
WT"lgkmcnt(3)\n"
RT"a[196:197],v247 offset:0\n"
"s_cmp_lt_u32 s34,5\n"
MF"v[12:15],a[188:191],v[192:195],v[12:15]\n"
"s_cselect_b32 s99,s34,s99\n"
RD"v[220:223],v243 offset:47104\n"
"s_mul_i32 s99,s99,1\n"
MF"v[16:19],a[176:179],v[196:199],v[16:19]\n"
SA"m0,s37,20480\n"
SA"s48,s98,0\n"
"s_lshl_b32 s61,s99,20\n"
MF"v[20:23],a[180:183],v[196:199],v[20:23]\n"
MF"v[24:27],a[184:187],v[196:199],v[24:27]\n"
RT"a[198:199],v247 offset:512\n"
MF"v[28:31],a[188:191],v[196:199],v[28:31]\n"
BL"v234,s[12:15],s48 offen offset:0 lds\n"
MF"v[32:35],a[176:179],v[200:203],v[32:35]\n"
MF"v[36:39],a[180:183],v[200:203],v[36:39]\n"
MF"v[40:43],a[184:187],v[200:203],v[40:43]\n"
RT"a[200:201],v248 offset:0\n"
MF"v[44:47],a[188:191],v[200:203],v[44:47]\n"
BL"v235,s[12:15],s48 offen offset:1024 lds\n"
MF"v[48:51],a[176:179],v[204:207],v[48:51]\n"
MF"v[52:55],a[180:183],v[204:207],v[52:55]\n"
WT"vmcnt(22) lgkmcnt(2)\n"
"s_barrier\n"
MF"v[56:59],a[184:187],v[204:207],v[56:59]\n"
RD"v[192:195],v242 offset:0\n"
MF"v[60:63],a[188:191],v[204:207],v[60:63]\n"
RT"a[202:203],v248 offset:512\n"
MF"v[64:67],a[176:179],v[208:211],v[64:67]\n"
BL"v236,s[12:15],s48 offen offset:2048 lds\n"
MF"v[68:71],a[180:183],v[208:211],v[68:71]\n"
MF"v[72:75],a[184:187],v[208:211],v[72:75]\n"
RD"v[196:199],v242 offset:2048\n"
MF"v[76:79],a[188:191],v[208:211],v[76:79]\n"
RT"a[204:205],v249 offset:0\n"
MF"v[80:83],a[176:179],v[212:215],v[80:83]\n"
BL"v237,s[12:15],s48 offen offset:3072 lds\n"
MF"v[84:87],a[180:183],v[212:215],v[84:87]\n"
RD"v[200:203],v242 offset:4096\n"
MF"v[88:91],a[184:187],v[212:215],v[88:91]\n"
RT"a[206:207],v249 offset:512\n"
MF"v[92:95],a[188:191],v[212:215],v[92:95]\n"
MF"v[96:99],a[176:179],v[216:219],v[96:99]\n"
MF"v[100:103],a[180:183],v[216:219],v[100:103]\n"
RD"v[204:207],v242 offset:6144\n"
MF"v[104:107],a[184:187],v[216:219],v[104:107]\n"
SA"m0,s37,0\n"
WT"vmcnt(20)\n"
RT"a[208:209],v246 offset:4096\n"
MF"v[108:111],a[188:191],v[216:219],v[108:111]\n"
SA"s48,s39,32768\n"
MF"v[112:115],a[176:179],v[220:223],v[112:115]\n"
RD"v[208:211],v242 offset:8192\n"
MF"v[116:119],a[180:183],v[220:223],v[116:119]\n"
WT"lgkmcnt(3)\n"
BL"v234,s[12:15],s48 offen offset:0 lds\n"
MF"v[120:123],a[184:187],v[220:223],v[120:123]\n"
RT"a[210:211],v246 offset:4608\n"
MF"v[124:127],a[188:191],v[220:223],v[124:127]\n"
MF"v[0:3],a[192:195],v[192:195],v[0:3]\n"
RD"v[212:215],v242 offset:10240\n"
MF"v[4:7],a[196:199],v[192:195],v[4:7]\n"
BL"v235,s[12:15],s48 offen offset:1024 lds\n"
MF"v[8:11],a[200:203],v[192:195],v[8:11]\n"
RT"a[212:213],v247 offset:4096\n"
MF"v[12:15],a[204:207],v[192:195],v[12:15]\n"
RD"v[216:219],v242 offset:12288\n"
MF"v[16:19],a[192:195],v[196:199],v[16:19]\n"
MF"v[20:23],a[196:199],v[196:199],v[20:23]\n"
BL"v236,s[12:15],s48 offen offset:2048 lds\n"
MF"v[24:27],a[200:203],v[196:199],v[24:27]\n"
RT"a[214:215],v247 offset:4608\n"
MF"v[28:31],a[204:207],v[196:199],v[28:31]\n"
RD"v[220:223],v242 offset:14336\n"
MF"v[32:35],a[192:195],v[200:203],v[32:35]\n"
MF"v[36:39],a[196:199],v[200:203],v[36:39]\n"
BL"v237,s[12:15],s48 offen offset:3072 lds\n"
MF"v[40:43],a[200:203],v[200:203],v[40:43]\n"
RD"v[192:195],v243 offset:0\n"
MF"v[44:47],a[204:207],v[200:203],v[44:47]\n"
WT"lgkmcnt(3)\n"
RT"a[216:217],v248 offset:4096\n"
MF"v[48:51],a[192:195],v[204:207],v[48:51]\n"
MF"v[52:55],a[196:199],v[204:207],v[52:55]\n"
MF"v[56:59],a[200:203],v[204:207],v[56:59]\n"
RD"v[196:199],v243 offset:2048\n"
MF"v[60:63],a[204:207],v[204:207],v[60:63]\n"
RT"a[218:219],v248 offset:4608\n"
MF"v[64:67],a[192:195],v[208:211],v[64:67]\n"
MF"v[68:71],a[196:199],v[208:211],v[68:71]\n"
RD"v[200:203],v243 offset:4096\n"
MF"v[72:75],a[200:203],v[208:211],v[72:75]\n"
RT"a[220:221],v249 offset:4096\n"
MF"v[76:79],a[204:207],v[208:211],v[76:79]\n"
MF"v[80:83],a[192:195],v[212:215],v[80:83]\n"
MF"v[84:87],a[196:199],v[212:215],v[84:87]\n"
RD"v[204:207],v243 offset:6144\n"
MF"v[88:91],a[200:203],v[212:215],v[88:91]\n"
RT"a[222:223],v249 offset:4608\n"
MF"v[92:95],a[204:207],v[212:215],v[92:95]\n"
MF"v[96:99],a[192:195],v[216:219],v[96:99]\n"
RD"v[208:211],v243 offset:8192\n"
MF"v[100:103],a[196:199],v[216:219],v[100:103]\n"
MF"v[104:107],a[200:203],v[216:219],v[104:107]\n"
SA"m0,s37,4096\n"
WT"vmcnt(20)\n"
RT"a[224:225],v246 offset:8192\n"
MF"v[108:111],a[204:207],v[216:219],v[108:111]\n"
WT"lgkmcnt(4)\n"
SA"s48,s98,32768\n"
MF"v[112:115],a[192:195],v[220:223],v[112:115]\n"
RD"v[212:215],v243 offset:10240\n"
MF"v[116:119],a[196:199],v[220:223],v[116:119]\n"
WT"lgkmcnt(3)\n"
BL"v234,s[12:15],s48 offen offset:0 lds\n"
MF"v[120:123],a[200:203],v[220:223],v[120:123]\n"
RT"a[226:227],v246 offset:8704\n"
MF"v[124:127],a[204:207],v[220:223],v[124:127]\n"
RD"v[216:219],v243 offset:12288\n"
MF"v[0:3],a[208:211],v[192:195],v[0:3]\n"
BL"v235,s[12:15],s48 offen offset:1024 lds\n"
MF"v[4:7],a[212:215],v[192:195],v[4:7]\n"
MF"v[8:11],a[216:219],v[192:195],v[8:11]\n"
RT"a[228:229],v247 offset:8192\n"
MF"v[12:15],a[220:223],v[192:195],v[12:15]\n"
RD"v[220:223],v243 offset:14336\n"
MF"v[16:19],a[208:211],v[196:199],v[16:19]\n"
MF"v[20:23],a[212:215],v[196:199],v[20:23]\n"
BL"v236,s[12:15],s48 offen offset:2048 lds\n"
MF"v[24:27],a[216:219],v[196:199],v[24:27]\n"
RT"a[230:231],v247 offset:8704\n"
MF"v[28:31],a[220:223],v[196:199],v[28:31]\n"
MF"v[32:35],a[208:211],v[200:203],v[32:35]\n"
MF"v[36:39],a[212:215],v[200:203],v[36:39]\n"
BL"v237,s[12:15],s48 offen offset:3072 lds\n"
MF"v[40:43],a[216:219],v[200:203],v[40:43]\n"
RT"a[232:233],v248 offset:8192\n"
MF"v[44:47],a[220:223],v[200:203],v[44:47]\n"
MF"v[48:51],a[208:211],v[204:207],v[48:51]\n"
MF"v[52:55],a[212:215],v[204:207],v[52:55]\n"
WT"vmcnt(20) lgkmcnt(2)\n"
"s_barrier\n"
MF"v[56:59],a[216:219],v[204:207],v[56:59]\n"
RD"v[192:195],v242 offset:16384\n"
MF"v[60:63],a[220:223],v[204:207],v[60:63]\n"
RT"a[234:235],v248 offset:8704\n"
MF"v[64:67],a[208:211],v[208:211],v[64:67]\n"
MF"v[68:71],a[212:215],v[208:211],v[68:71]\n"
MF"v[72:75],a[216:219],v[208:211],v[72:75]\n"
RD"v[196:199],v242 offset:18432\n"
MF"v[76:79],a[220:223],v[208:211],v[76:79]\n"
RT"a[236:237],v249 offset:8192\n"
MF"v[80:83],a[208:211],v[212:215],v[80:83]\n"
MF"v[84:87],a[212:215],v[212:215],v[84:87]\n"
RD"v[200:203],v242 offset:20480\n"
MF"v[88:91],a[216:219],v[212:215],v[88:91]\n"
RT"a[238:239],v249 offset:8704\n"
MF"v[92:95],a[220:223],v[212:215],v[92:95]\n"
MF"v[96:99],a[208:211],v[216:219],v[96:99]\n"
"s_lshl_b32 s50,s35,20\n"
MF"v[100:103],a[212:215],v[216:219],v[100:103]\n"
"s_lshl_b32 s51,s34,15\n"
RD"v[204:207],v242 offset:22528\n"
MF"v[104:107],a[216:219],v[216:219],v[104:107]\n"
SA"s50,s50,s51\n"
WT"vmcnt(16)\n"
RT"a[240:241],v246 offset:12288\n"
MF"v[108:111],a[220:223],v[216:219],v[108:111]\n"
SA"m0,s37,8192\n"
SA"s48,s39,65536\n"
"s_and_b32 s51,s30,1\n"
MF"v[112:115],a[208:211],v[220:223],v[112:115]\n"
"s_lshl_b32 s51,s51,11\n"
RD"v[208:211],v242 offset:24576\n"
MF"v[116:119],a[212:215],v[220:223],v[116:119]\n"
WT"lgkmcnt(3)\n"
BL"v234,s[12:15],s48 offen offset:0 lds\n"
MF"v[120:123],a[216:219],v[220:223],v[120:123]\n"
SA"s50,s50,s51\n"
RT"a[242:243],v246 offset:12800\n"
SA"s50,s50,9175040\n"
MF"v[124:127],a[220:223],v[220:223],v[124:127]\n"
"v_add_u32 v250,s50,v224\n"
MF"v[0:3],a[224:227],v[192:195],v[0:3]\n"
RD"v[212:215],v242 offset:26624\n"
MF"v[4:7],a[228:231],v[192:195],v[4:7]\n"
BL"v235,s[12:15],s48 offen offset:1024 lds\n"
MF"v[8:11],a[232:235],v[192:195],v[8:11]\n"
RT"a[244:245],v247 offset:12288\n"
MF"v[12:15],a[236:239],v[192:195],v[12:15]\n"
RD"v[216:219],v242 offset:28672\n"
MF"v[16:19],a[224:227],v[196:199],v[16:19]\n"
MF"v[20:23],a[228:231],v[196:199],v[20:23]\n"
BL"v236,s[12:15],s48 offen offset:2048 lds\n"
MF"v[24:27],a[232:235],v[196:199],v[24:27]\n"
RT"a[246:247],v247 offset:12800\n"
MF"v[28:31],a[236:239],v[196:199],v[28:31]\n"
RD"v[220:223],v242 offset:30720\n"
MF"v[32:35],a[224:227],v[200:203],v[32:35]\n"
MF"v[36:39],a[228:231],v[200:203],v[36:39]\n"
BL"v237,s[12:15],s48 offen offset:3072 lds\n"
MF"v[40:43],a[232:235],v[200:203],v[40:43]\n"
RD"v[192:195],v243 offset:16384\n"
MF"v[44:47],a[236:239],v[200:203],v[44:47]\n"
WT"lgkmcnt(3)\n"
RT"a[248:249],v248 offset:12288\n"
MF"v[48:51],a[224:227],v[204:207],v[48:51]\n"
MF"v[52:55],a[228:231],v[204:207],v[52:55]\n"
MF"v[56:59],a[232:235],v[204:207],v[56:59]\n"
RD"v[196:199],v243 offset:18432\n"
MF"v[60:63],a[236:239],v[204:207],v[60:63]\n"
RT"a[250:251],v248 offset:12800\n"
MF"v[64:67],a[224:227],v[208:211],v[64:67]\n"
MF"v[68:71],a[228:231],v[208:211],v[68:71]\n"
RD"v[200:203],v243 offset:20480\n"
MF"v[72:75],a[232:235],v[208:211],v[72:75]\n"
RT"a[252:253],v249 offset:12288\n"
MF"v[76:79],a[236:239],v[208:211],v[76:79]\n"
MF"v[80:83],a[224:227],v[212:215],v[80:83]\n"
MF"v[84:87],a[228:231],v[212:215],v[84:87]\n"
RD"v[204:207],v243 offset:22528\n"
MF"v[88:91],a[232:235],v[212:215],v[88:91]\n"
RT"a[254:255],v249 offset:12800\n"
MF"v[92:95],a[236:239],v[212:215],v[92:95]\n"
MF"v[96:99],a[224:227],v[216:219],v[96:99]\n"
RD"v[208:211],v243 offset:24576\n"
MF"v[100:103],a[228:231],v[216:219],v[100:103]\n"
MF"v[104:107],a[232:235],v[216:219],v[104:107]\n"
SA"m0,s37,12288\n"
SA"s48,s98,65536\n"
MF"v[108:111],a[236:239],v[216:219],v[108:111]\n"
WT"lgkmcnt(3)\n"
MF"v[112:115],a[224:227],v[220:223],v[112:115]\n"
RD"v[212:215],v243 offset:26624\n"
MF"v[116:119],a[228:231],v[220:223],v[116:119]\n"
WT"lgkmcnt(2)\n"
BL"v234,s[12:15],s48 offen offset:0 lds\n"
MF"v[120:123],a[232:235],v[220:223],v[120:123]\n"
MF"v[124:127],a[236:239],v[220:223],v[124:127]\n"
RD"v[216:219],v243 offset:28672\n"
MF"v[0:3],a[240:243],v[192:195],v[0:3]\n"
BL"v235,s[12:15],s48 offen offset:1024 lds\n"
MF"v[4:7],a[244:247],v[192:195],v[4:7]\n"
MF"v[8:11],a[248:251],v[192:195],v[8:11]\n"
MF"v[12:15],a[252:255],v[192:195],v[12:15]\n"
RD"v[220:223],v243 offset:30720\n"
MF"v[16:19],a[240:243],v[196:199],v[16:19]\n"
MF"v[20:23],a[244:247],v[196:199],v[20:23]\n"
BL"v236,s[12:15],s48 offen offset:2048 lds\n"
MF"v[24:27],a[248:251],v[196:199],v[24:27]\n"
MF"v[28:31],a[252:255],v[196:199],v[28:31]\n"
MF"v[32:35],a[240:243],v[200:203],v[32:35]\n"
MF"v[36:39],a[244:247],v[200:203],v[36:39]\n"
BL"v237,s[12:15],s48 offen offset:3072 lds\n"
MF"v[40:43],a[248:251],v[200:203],v[40:43]\n"
"v_add_u32 v244,114688,v242\n"
"v_add_u32 v245,114688,v243\n"
MF"v[44:47],a[252:255],v[200:203],v[44:47]\n"
"v_add_u32 v242,65536,v242\n"
"v_add_u32 v243,65536,v243\n"
MF"v[48:51],a[240:243],v[204:207],v[48:51]\n"
MF"v[52:55],a[244:247],v[204:207],v[52:55]\n"
WT"vmcnt(16) lgkmcnt(0)\n"
"s_barrier\n"
MF"v[56:59],a[248:251],v[204:207],v[56:59]\n"
RD"v[192:195],v242 offset:16384\n"
MF"v[60:63],a[252:255],v[204:207],v[60:63]\n"
MF"v[64:67],a[240:243],v[208:211],v[64:67]\n"
MF"v[68:71],a[244:247],v[208:211],v[68:71]\n"
MF"v[72:75],a[248:251],v[208:211],v[72:75]\n"
SA"m0,s36,49152\n"
RD"v[196:199],v242 offset:18432\n"
SA"s48,s60,s61\n"
MF"v[76:79],a[252:255],v[208:211],v[76:79]\n"
MF"v[80:83],a[240:243],v[212:215],v[80:83]\n"
BL"v238,s[20:23],s48 offen offset:0 lds\n"
MF"v[84:87],a[244:247],v[212:215],v[84:87]\n"
MF"v[88:91],a[248:251],v[212:215],v[88:91]\n"
RD"v[200:203],v243 offset:16384\n"
MF"v[92:95],a[252:255],v[212:215],v[92:95]\n"
MF"v[96:99],a[240:243],v[216:219],v[96:99]\n"
BL"v239,s[20:23],s48 offen offset:1024 lds\n"
MF"v[100:103],a[244:247],v[216:219],v[100:103]\n"
MF"v[104:107],a[248:251],v[216:219],v[104:107]\n"
RD"v[204:207],v243 offset:18432\n"
MF"v[108:111],a[252:255],v[216:219],v[108:111]\n"
MF"v[112:115],a[240:243],v[220:223],v[112:115]\n"
MF"v[116:119],a[244:247],v[220:223],v[116:119]\n"
RD"v[208:211],v242 offset:20480\n"
MF"v[120:123],a[248:251],v[220:223],v[120:123]\n"
SA"s61,s61,524288\n"
BL"v240,s[20:23],s48 offen offset:2048 lds\n"
MF"v[124:127],a[252:255],v[220:223],v[124:127]\n"
WT"lgkmcnt(2)\n"
MF"v[128:131],a[0:3],v[192:195],0\n"
MF"v[132:135],a[4:7],v[192:195],0\n"
RD"v[212:215],v242 offset:22528\n"
MF"v[136:139],a[8:11],v[192:195],0\n"
BL"v241,s[20:23],s48 offen offset:3072 lds\n"
MF"v[140:143],a[12:15],v[192:195],0\n"
MU"v0,s55,v0\n"
MU"v1,s55,v1\n"
MF"v[144:147],a[0:3],v[196:199],0\n"
MU"v2,s55,v2\n"
MU"v3,s55,v3\n"
MF"v[148:151],a[4:7],v[196:199],0\n"
RD"v[216:219],v243 offset:20480\n"
MU"v4,s55,v4\n"
MF"v[152:155],a[8:11],v[196:199],0\n"
SA"m0,s36,32768\n"
MU"v5,s55,v5\n"
MU"v6,s55,v6\n"
MF"v[156:159],a[12:15],v[196:199],0\n"
SA"s48,s60,s61\n"
MU"v7,s55,v7\n"
PK"v0,v0,v1\n"
MF"v[128:131],a[16:19],v[200:203],v[128:131]\n"
BL"v238,s[20:23],s48 offen offset:0 lds\n"
MF"v[132:135],a[20:23],v[200:203],v[132:135]\n"
RD"v[220:223],v243 offset:22528\n"
PK"v1,v2,v3\n"
MF"v[136:139],a[24:27],v[200:203],v[136:139]\n"
PK"v2,v4,v5\n"
PK"v3,v6,v7\n"
MF"v[140:143],a[28:31],v[200:203],v[140:143]\n"
WT"lgkmcnt(2)\n"
MF"v[144:147],a[16:19],v[204:207],v[144:147]\n"
RD"v[192:195],v242 offset:40960\n"
MU"v8,s55,v8\n"
MF"v[148:151],a[20:23],v[204:207],v[148:151]\n"
BL"v239,s[20:23],s48 offen offset:1024 lds\n"
MF"v[152:155],a[24:27],v[204:207],v[152:155]\n"
MU"v9,s55,v9\n"
MU"v10,s55,v10\n"
MF"v[156:159],a[28:31],v[204:207],v[156:159]\n"
MU"v11,s55,v11\n"
MU"v12,s55,v12\n"
MF"v[128:131],a[32:35],v[208:211],v[128:131]\n"
RD"v[196:199],v242 offset:43008\n"
MU"v13,s55,v13\n"
MF"v[132:135],a[36:39],v[208:211],v[132:135]\n"
BL"v240,s[20:23],s48 offen offset:2048 lds\n"
MF"v[136:139],a[40:43],v[208:211],v[136:139]\n"
SA"s61,s61,524288\n"
MU"v14,s55,v14\n"
MU"v15,s55,v15\n"
MF"v[140:143],a[44:47],v[208:211],v[140:143]\n"
"s_cmp_eq_u32 s61,5242880\n"
PK"v8,v8,v9\n"
PK"v9,v10,v11\n"
MF"v[144:147],a[32:35],v[212:215],v[144:147]\n"
"s_cselect_b32 s61,0,s61\n"
RD"v[200:203],v243 offset:40960\n"
PK"v10,v12,v13\n"
MF"v[148:151],a[36:39],v[212:215],v[148:151]\n"
BL"v241,s[20:23],s48 offen offset:3072 lds\n"
MF"v[152:155],a[40:43],v[212:215],v[152:155]\n"
PK"v11,v14,v15\n"
MU"v16,s55,v16\n"
MF"v[156:159],a[44:47],v[212:215],v[156:159]\n"
WT"lgkmcnt(1)\n"
MU"v17,s55,v17\n"
MU"v18,s55,v18\n"
MF"v[128:131],a[48:51],v[216:219],v[128:131]\n"
RD"v[204:207],v243 offset:43008\n"
MU"v19,s55,v19\n"
MF"v[132:135],a[52:55],v[216:219],v[132:135]\n"
MU"v20,s55,v20\n"
MU"v21,s55,v21\n"
MF"v[136:139],a[56:59],v[216:219],v[136:139]\n"
MU"v22,s55,v22\n"
MU"v23,s55,v23\n"
MF"v[140:143],a[60:63],v[216:219],v[140:143]\n"
RD"v[208:211],v242 offset:45056\n"
PK"v16,v16,v17\n"
MF"v[144:147],a[48:51],v[220:223],v[144:147]\n"
PK"v17,v18,v19\n"
PK"v18,v20,v21\n"
MF"v[148:151],a[52:55],v[220:223],v[148:151]\n"
PK"v19,v22,v23\n"
MF"v[152:155],a[56:59],v[220:223],v[152:155]\n"
MF"v[156:159],a[60:63],v[220:223],v[156:159]\n"
"s_mov_b32 s49,0\n"
RD"v[212:215],v242 offset:47104\n"
MF"v[128:131],a[64:67],v[192:195],v[128:131]\n"
MU"v24,s55,v24\n"
MU"v25,s55,v25\n"
MF"v[132:135],a[68:71],v[192:195],v[132:135]\n"
BS"v[0:3],v250,s[8:11],s49 offen offset:0 sc1\n"
MF"v[136:139],a[72:75],v[192:195],v[136:139]\n"
MU"v26,s55,v26\n"
MU"v27,s55,v27\n"
MF"v[140:143],a[76:79],v[192:195],v[140:143]\n"
RD"v[216:219],v243 offset:45056\n"
MU"v28,s55,v28\n"
MF"v[144:147],a[64:67],v[196:199],v[144:147]\n"
MU"v29,s55,v29\n"
MU"v30,s55,v30\n"
MF"v[148:151],a[68:71],v[196:199],v[148:151]\n"
MU"v31,s55,v31\n"
PK"v24,v24,v25\n"
MF"v[152:155],a[72:75],v[196:199],v[152:155]\n"
PK"v25,v26,v27\n"
PK"v26,v28,v29\n"
MF"v[156:159],a[76:79],v[196:199],v[156:159]\n"
WT"lgkmcnt(1)\n"
RD"v[220:223],v243 offset:47104\n"
PK"v27,v30,v31\n"
MF"v[128:131],a[80:83],v[200:203],v[128:131]\n"
MF"v[132:135],a[84:87],v[200:203],v[132:135]\n"
MF"v[136:139],a[88:91],v[200:203],v[136:139]\n"
RD"v[192:195],v244 offset:16384\n"
MU"v32,s55,v32\n"
MF"v[140:143],a[92:95],v[200:203],v[140:143]\n"
MU"v33,s55,v33\n"
MU"v34,s55,v34\n"
MF"v[144:147],a[80:83],v[204:207],v[144:147]\n"
MU"v35,s55,v35\n"
MU"v36,s55,v36\n"
MF"v[148:151],a[84:87],v[204:207],v[148:151]\n"
MU"v37,s55,v37\n"
MU"v38,s55,v38\n"
MF"v[152:155],a[88:91],v[204:207],v[152:155]\n"
RD"v[196:199],v244 offset:18432\n"
MU"v39,s55,v39\n"
MF"v[156:159],a[92:95],v[204:207],v[156:159]\n"
PK"v32,v32,v33\n"
PK"v33,v34,v35\n"
MF"v[128:131],a[96:99],v[208:211],v[128:131]\n"
PK"v34,v36,v37\n"
PK"v35,v38,v39\n"
MF"v[132:135],a[100:103],v[208:211],v[132:135]\n"
MF"v[136:139],a[104:107],v[208:211],v[136:139]\n"
RD"v[200:203],v245 offset:16384\n"
MF"v[140:143],a[108:111],v[208:211],v[140:143]\n"
MF"v[144:147],a[96:99],v[212:215],v[144:147]\n"
MU"v40,s55,v40\n"
MU"v41,s55,v41\n"
MF"v[148:151],a[100:103],v[212:215],v[148:151]\n"
MU"v42,s55,v42\n"
MU"v43,s55,v43\n"
MF"v[152:155],a[104:107],v[212:215],v[152:155]\n"
RD"v[204:207],v245 offset:18432\n"
MU"v44,s55,v44\n"
MF"v[156:159],a[108:111],v[212:215],v[156:159]\n"
WT"lgkmcnt(2)\n"
MU"v45,s55,v45\n"
MU"v46,s55,v46\n"
MF"v[128:131],a[112:115],v[216:219],v[128:131]\n"
MU"v47,s55,v47\n"
PK"v40,v40,v41\n"
MF"v[132:135],a[116:119],v[216:219],v[132:135]\n"
RD"v[208:211],v244 offset:20480\n"
PK"v41,v42,v43\n"
MF"v[136:139],a[120:123],v[216:219],v[136:139]\n"
PK"v42,v44,v45\n"
PK"v43,v46,v47\n"
MF"v[140:143],a[124:127],v[216:219],v[140:143]\n"
MF"v[144:147],a[112:115],v[220:223],v[144:147]\n"
MF"v[148:151],a[116:119],v[220:223],v[148:151]\n"
RD"v[212:215],v244 offset:22528\n"
MF"v[152:155],a[120:123],v[220:223],v[152:155]\n"
MU"v48,s55,v48\n"
MU"v49,s55,v49\n"
MF"v[156:159],a[124:127],v[220:223],v[156:159]\n"
MU"v50,s55,v50\n"
MU"v51,s55,v51\n"
MF"v[128:131],a[128:131],v[192:195],v[128:131]\n"
BS"v[8:11],v250,s[8:11],s49 offen offset:1024 sc1\n"
MF"v[132:135],a[132:135],v[192:195],v[132:135]\n"
RD"v[216:219],v245 offset:20480\n"
MU"v52,s55,v52\n"
MF"v[136:139],a[136:139],v[192:195],v[136:139]\n"
MU"v53,s55,v53\n"
MU"v54,s55,v54\n"
MF"v[140:143],a[140:143],v[192:195],v[140:143]\n"
MU"v55,s55,v55\n"
PK"v48,v48,v49\n"
MF"v[144:147],a[128:131],v[196:199],v[144:147]\n"
PK"v49,v50,v51\n"
PK"v50,v52,v53\n"
MF"v[148:151],a[132:135],v[196:199],v[148:151]\n"
RD"v[220:223],v245 offset:22528\n"
PK"v51,v54,v55\n"
MF"v[152:155],a[136:139],v[196:199],v[152:155]\n"
MF"v[156:159],a[140:143],v[196:199],v[156:159]\n"
WT"lgkmcnt(2)\n"
MF"v[128:131],a[144:147],v[200:203],v[128:131]\n"
RD"v[192:195],v244 offset:40960\n"
MU"v56,s55,v56\n"
MF"v[132:135],a[148:151],v[200:203],v[132:135]\n"
MU"v57,s55,v57\n"
MU"v58,s55,v58\n"
MF"v[136:139],a[152:155],v[200:203],v[136:139]\n"
MU"v59,s55,v59\n"
MU"v60,s55,v60\n"
MF"v[140:143],a[156:159],v[200:203],v[140:143]\n"
MU"v61,s55,v61\n"
MU"v62,s55,v62\n"
MF"v[144:147],a[144:147],v[204:207],v[144:147]\n"
RD"v[196:199],v244 offset:43008\n"
MU"v63,s55,v63\n"
MF"v[148:151],a[148:151],v[204:207],v[148:151]\n"
PK"v56,v56,v57\n"
PK"v57,v58,v59\n"
MF"v[152:155],a[152:155],v[204:207],v[152:155]\n"
PK"v58,v60,v61\n"
PK"v59,v62,v63\n"
MF"v[156:159],a[156:159],v[204:207],v[156:159]\n"
MF"v[128:131],a[160:163],v[208:211],v[128:131]\n"
RD"v[200:203],v245 offset:40960\n"
MF"v[132:135],a[164:167],v[208:211],v[132:135]\n"
MF"v[136:139],a[168:171],v[208:211],v[136:139]\n"
MU"v64,s55,v64\n"
MU"v65,s55,v65\n"
MF"v[140:143],a[172:175],v[208:211],v[140:143]\n"
MU"v66,s55,v66\n"
MU"v67,s55,v67\n"
MF"v[144:147],a[160:163],v[212:215],v[144:147]\n"
RD"v[204:207],v245 offset:43008\n"
MU"v68,s55,v68\n"
MF"v[148:151],a[164:167],v[212:215],v[148:151]\n"
MU"v69,s55,v69\n"
MU"v70,s55,v70\n"
MF"v[152:155],a[168:171],v[212:215],v[152:155]\n"
MU"v71,s55,v71\n"
PK"v64,v64,v65\n"
MF"v[156:159],a[172:175],v[212:215],v[156:159]\n"
WT"lgkmcnt(1)\n"
RD"v[208:211],v244 offset:45056\n"
PK"v65,v66,v67\n"
MF"v[128:131],a[176:179],v[216:219],v[128:131]\n"
PK"v66,v68,v69\n"
PK"v67,v70,v71\n"
MF"v[132:135],a[180:183],v[216:219],v[132:135]\n"
MF"v[136:139],a[184:187],v[216:219],v[136:139]\n"
MF"v[140:143],a[188:191],v[216:219],v[140:143]\n"
RD"v[212:215],v244 offset:47104\n"
MF"v[144:147],a[176:179],v[220:223],v[144:147]\n"
MU"v72,s55,v72\n"
MU"v73,s55,v73\n"
MF"v[148:151],a[180:183],v[220:223],v[148:151]\n"
"s_mov_b32 s49,4096\n"
MU"v74,s55,v74\n"
MU"v75,s55,v75\n"
MF"v[152:155],a[184:187],v[220:223],v[152:155]\n"
MU"v76,s55,v76\n"
MU"v77,s55,v77\n"
MF"v[156:159],a[188:191],v[220:223],v[156:159]\n"
RD"v[216:219],v245 offset:45056\n"
MU"v78,s55,v78\n"
MF"v[128:131],a[192:195],v[192:195],v[128:131]\n"
BS"v[16:19],v250,s[8:11],s49 offen offset:0 sc1\n"
MF"v[132:135],a[196:199],v[192:195],v[132:135]\n"
MU"v79,s55,v79\n"
PK"v72,v72,v73\n"
MF"v[136:139],a[200:203],v[192:195],v[136:139]\n"
PK"v73,v74,v75\n"
PK"v74,v76,v77\n"
MF"v[140:143],a[204:207],v[192:195],v[140:143]\n"
RD"v[220:223],v245 offset:47104\n"
PK"v75,v78,v79\n"
MF"v[144:147],a[192:195],v[196:199],v[144:147]\n"
MF"v[148:151],a[196:199],v[196:199],v[148:151]\n"
MF"v[152:155],a[200:203],v[196:199],v[152:155]\n"
MU"v80,s55,v80\n"
MU"v81,s55,v81\n"
MF"v[156:159],a[204:207],v[196:199],v[156:159]\n"
MU"v82,s55,v82\n"
MU"v83,s55,v83\n"
MF"v[128:131],a[208:211],v[200:203],v[128:131]\n"
MU"v84,s55,v84\n"
MU"v85,s55,v85\n"
MF"v[132:135],a[212:215],v[200:203],v[132:135]\n"
MU"v86,s55,v86\n"
MU"v87,s55,v87\n"
MF"v[136:139],a[216:219],v[200:203],v[136:139]\n"
PK"v80,v80,v81\n"
PK"v81,v82,v83\n"
MF"v[140:143],a[220:223],v[200:203],v[140:143]\n"
WT"lgkmcnt(0)\n"
PK"v82,v84,v85\n"
PK"v83,v86,v87\n"
MF"v[144:147],a[208:211],v[204:207],v[144:147]\n"
MF"v[148:151],a[212:215],v[204:207],v[148:151]\n"
WT"vmcnt(19)\n"
"s_barrier\n"
MF"v[152:155],a[216:219],v[204:207],v[152:155]\n"
RD"v[192:195],v242 offset:0\n"
MF"v[156:159],a[220:223],v[204:207],v[156:159]\n"
MF"v[128:131],a[224:227],v[208:211],v[128:131]\n"
MU"v88,s55,v88\n"
MU"v89,s55,v89\n"
MF"v[132:135],a[228:231],v[208:211],v[132:135]\n"
MU"v90,s55,v90\n"
MU"v91,s55,v91\n"
MF"v[136:139],a[232:235],v[208:211],v[136:139]\n"
RD"v[196:199],v242 offset:2048\n"
MU"v92,s55,v92\n"
MF"v[140:143],a[236:239],v[208:211],v[140:143]\n"
MU"v93,s55,v93\n"
MU"v94,s55,v94\n"
MF"v[144:147],a[224:227],v[212:215],v[144:147]\n"
MU"v95,s55,v95\n"
PK"v88,v88,v89\n"
MF"v[148:151],a[228:231],v[212:215],v[148:151]\n"
PK"v89,v90,v91\n"
PK"v90,v92,v93\n"
MF"v[152:155],a[232:235],v[212:215],v[152:155]\n"
RD"v[200:203],v243 offset:0\n"
PK"v91,v94,v95\n"
MF"v[156:159],a[236:239],v[212:215],v[156:159]\n"
MF"v[128:131],a[240:243],v[216:219],v[128:131]\n"
MF"v[132:135],a[244:247],v[216:219],v[132:135]\n"
MF"v[136:139],a[248:251],v[216:219],v[136:139]\n"
RD"v[204:207],v243 offset:2048\n"
MF"v[140:143],a[252:255],v[216:219],v[140:143]\n"
MF"v[144:147],a[240:243],v[220:223],v[144:147]\n"
MF"v[148:151],a[244:247],v[220:223],v[148:151]\n"
RD"v[208:211],v242 offset:4096\n"
MF"v[152:155],a[248:251],v[220:223],v[152:155]\n"
BS"v[24:27],v250,s[8:11],s49 offen offset:1024 sc1\n"
MF"v[156:159],a[252:255],v[220:223],v[156:159]\n"
WT"lgkmcnt(2)\n"
MF"v[36:39],a[0:3],v[192:195],0\n"
MF"v[68:71],a[4:7],v[192:195],0\n"
RD"v[212:215],v242 offset:6144\n"
MF"v[44:47],a[8:11],v[192:195],0\n"
MF"v[76:79],a[12:15],v[192:195],0\n"
MU"v128,s55,v128\n"
MU"v129,s55,v129\n"
MF"v[52:55],a[0:3],v[196:199],0\n"
MU"v130,s55,v130\n"
MU"v131,s55,v131\n"
MF"v[84:87],a[4:7],v[196:199],0\n"
RD"v[216:219],v243 offset:4096\n"
MU"v132,s55,v132\n"
MF"v[60:63],a[8:11],v[196:199],0\n"
MU"v133,s55,v133\n"
MU"v134,s55,v134\n"
MF"v[92:95],a[12:15],v[196:199],0\n"
MU"v135,s55,v135\n"
PK"v4,v128,v129\n"
MF"v[36:39],a[16:19],v[200:203],v[36:39]\n"
PK"v5,v130,v131\n"
PK"v6,v132,v133\n"
MF"v[68:71],a[20:23],v[200:203],v[68:71]\n"
RD"v[220:223],v243 offset:6144\n"
PK"v7,v134,v135\n"
MF"v[44:47],a[24:27],v[200:203],v[44:47]\n"
MF"v[76:79],a[28:31],v[200:203],v[76:79]\n"
WT"lgkmcnt(2)\n"
MF"v[52:55],a[16:19],v[204:207],v[52:55]\n"
RD"v[192:195],v242 offset:24576\n"
MF"v[84:87],a[20:23],v[204:207],v[84:87]\n"
MU"v136,s55,v136\n"
MU"v137,s55,v137\n"
MF"v[60:63],a[24:27],v[204:207],v[60:63]\n"
MU"v138,s55,v138\n"
MU"v139,s55,v139\n"
MF"v[92:95],a[28:31],v[204:207],v[92:95]\n"
MU"v140,s55,v140\n"
MU"v141,s55,v141\n"
MF"v[36:39],a[32:35],v[208:211],v[36:39]\n"
RD"v[196:199],v242 offset:26624\n"
MU"v142,s55,v142\n"
MF"v[68:71],a[36:39],v[208:211],v[68:71]\n"
MU"v143,s55,v143\n"
PK"v12,v136,v137\n"
MF"v[44:47],a[40:43],v[208:211],v[44:47]\n"
PK"v13,v138,v139\n"
PK"v14,v140,v141\n"
MF"v[76:79],a[44:47],v[208:211],v[76:79]\n"
PK"v15,v142,v143\n"
MF"v[52:55],a[32:35],v[212:215],v[52:55]\n"
RD"v[200:203],v243 offset:24576\n"
MF"v[84:87],a[36:39],v[212:215],v[84:87]\n"
MF"v[60:63],a[40:43],v[212:215],v[60:63]\n"
MF"v[92:95],a[44:47],v[212:215],v[92:95]\n"
WT"lgkmcnt(1)\n"
MU"v144,s55,v144\n"
MU"v145,s55,v145\n"
MF"v[36:39],a[48:51],v[216:219],v[36:39]\n"
RD"v[204:207],v243 offset:26624\n"
MU"v146,s55,v146\n"
MF"v[68:71],a[52:55],v[216:219],v[68:71]\n"
MU"v147,s55,v147\n"
MU"v148,s55,v148\n"
MF"v[44:47],a[56:59],v[216:219],v[44:47]\n"
MU"v149,s55,v149\n"
MU"v150,s55,v150\n"
MF"v[76:79],a[60:63],v[216:219],v[76:79]\n"
"s_mov_b32 s49,8192\n"
RD"v[208:211],v242 offset:28672\n"
MU"v151,s55,v151\n"
MF"v[52:55],a[48:51],v[220:223],v[52:55]\n"
PK"v20,v144,v145\n"
PK"v21,v146,v147\n"
MF"v[84:87],a[52:55],v[220:223],v[84:87]\n"
BS"v[32:35],v250,s[8:11],s49 offen offset:0 sc1\n"
MF"v[60:63],a[56:59],v[220:223],v[60:63]\n"
PK"v22,v148,v149\n"
PK"v23,v150,v151\n"
MF"v[92:95],a[60:63],v[220:223],v[92:95]\n"
RD"v[212:215],v242 offset:30720\n"
MF"v[36:39],a[64:67],v[192:195],v[36:39]\n"
MF"v[68:71],a[68:71],v[192:195],v[68:71]\n"
MU"v152,s55,v152\n"
MU"v153,s55,v153\n"
MF"v[44:47],a[72:75],v[192:195],v[44:47]\n"
MU"v154,s55,v154\n"
MU"v155,s55,v155\n"
MF"v[76:79],a[76:79],v[192:195],v[76:79]\n"
RD"v[216:219],v243 offset:28672\n"
MU"v156,s55,v156\n"
MF"v[52:55],a[64:67],v[196:199],v[52:55]\n"
MU"v157,s55,v157\n"
MU"v158,s55,v158\n"
MF"v[84:87],a[68:71],v[196:199],v[84:87]\n"
MU"v159,s55,v159\n"
PK"v28,v152,v153\n"
MF"v[60:63],a[72:75],v[196:199],v[60:63]\n"
PK"v29,v154,v155\n"
PK"v30,v156,v157\n"
MF"v[92:95],a[76:79],v[196:199],v[92:95]\n"
WT"lgkmcnt(1)\n"
RD"v[220:223],v243 offset:30720\n"
PK"v31,v158,v159\n"
MF"v[36:39],a[80:83],v[200:203],v[36:39]\n"
MF"v[68:71],a[84:87],v[200:203],v[68:71]\n"
MF"v[44:47],a[88:91],v[200:203],v[44:47]\n"
RD"v[192:195],v244 offset:0\n"
MF"v[76:79],a[92:95],v[200:203],v[76:79]\n"
MF"v[52:55],a[80:83],v[204:207],v[52:55]\n"
MF"v[84:87],a[84:87],v[204:207],v[84:87]\n"
MF"v[60:63],a[88:91],v[204:207],v[60:63]\n"
RD"v[196:199],v244 offset:2048\n"
MF"v[92:95],a[92:95],v[204:207],v[92:95]\n"
MF"v[36:39],a[96:99],v[208:211],v[36:39]\n"
MF"v[68:71],a[100:103],v[208:211],v[68:71]\n"
MF"v[44:47],a[104:107],v[208:211],v[44:47]\n"
RD"v[200:203],v245 offset:0\n"
MF"v[76:79],a[108:111],v[208:211],v[76:79]\n"
MF"v[52:55],a[96:99],v[212:215],v[52:55]\n"
MF"v[84:87],a[100:103],v[212:215],v[84:87]\n"
MF"v[60:63],a[104:107],v[212:215],v[60:63]\n"
RD"v[204:207],v245 offset:2048\n"
MF"v[92:95],a[108:111],v[212:215],v[92:95]\n"
WT"lgkmcnt(2)\n"
MF"v[36:39],a[112:115],v[216:219],v[36:39]\n"
MF"v[68:71],a[116:119],v[216:219],v[68:71]\n"
RD"v[208:211],v244 offset:4096\n"
MF"v[44:47],a[120:123],v[216:219],v[44:47]\n"
SA"m0,s37,16384\n"
SA"s48,s39,98304\n"
MF"v[76:79],a[124:127],v[216:219],v[76:79]\n"
MF"v[52:55],a[112:115],v[220:223],v[52:55]\n"
BS"v[40:43],v250,s[8:11],s49 offen offset:1024 sc1\n"
MF"v[84:87],a[116:119],v[220:223],v[84:87]\n"
BL"v234,s[12:15],s48 offen offset:0 lds\n"
MF"v[60:63],a[120:123],v[220:223],v[60:63]\n"
RD"v[212:215],v244 offset:6144\n"
MU"v96,s55,v96\n"
MF"v[92:95],a[124:127],v[220:223],v[92:95]\n"
MU"v97,s55,v97\n"
MU"v98,s55,v98\n"
MF"v[36:39],a[128:131],v[192:195],v[36:39]\n"
MU"v99,s55,v99\n"
MU"v100,s55,v100\n"
MF"v[68:71],a[132:135],v[192:195],v[68:71]\n"
RD"v[216:219],v245 offset:4096\n"
MU"v101,s55,v101\n"
MF"v[44:47],a[136:139],v[192:195],v[44:47]\n"
BL"v235,s[12:15],s48 offen offset:1024 lds\n"
MF"v[76:79],a[140:143],v[192:195],v[76:79]\n"
MU"v102,s55,v102\n"
MU"v103,s55,v103\n"
MF"v[52:55],a[128:131],v[196:199],v[52:55]\n"
PK"v96,v96,v97\n"
PK"v97,v98,v99\n"
MF"v[84:87],a[132:135],v[196:199],v[84:87]\n"
RD"v[220:223],v245 offset:6144\n"
PK"v98,v100,v101\n"
MF"v[60:63],a[136:139],v[196:199],v[60:63]\n"
PK"v99,v102,v103\n"
MU"v104,s55,v104\n"
MF"v[92:95],a[140:143],v[196:199],v[92:95]\n"
WT"lgkmcnt(2)\n"
MU"v105,s55,v105\n"
MU"v106,s55,v106\n"
MF"v[36:39],a[144:147],v[200:203],v[36:39]\n"
RD"v[192:195],v244 offset:24576\n"
MU"v107,s55,v107\n"
MF"v[68:71],a[148:151],v[200:203],v[68:71]\n"
BL"v236,s[12:15],s48 offen offset:2048 lds\n"
MF"v[44:47],a[152:155],v[200:203],v[44:47]\n"
MU"v108,s55,v108\n"
MU"v109,s55,v109\n"
MF"v[76:79],a[156:159],v[200:203],v[76:79]\n"
MU"v110,s55,v110\n"
MU"v111,s55,v111\n"
MF"v[52:55],a[144:147],v[204:207],v[52:55]\n"
RD"v[196:199],v244 offset:26624\n"
PK"v104,v104,v105\n"
MF"v[84:87],a[148:151],v[204:207],v[84:87]\n"
PK"v105,v106,v107\n"
PK"v106,v108,v109\n"
MF"v[60:63],a[152:155],v[204:207],v[60:63]\n"
BL"v237,s[12:15],s48 offen offset:3072 lds\n"
MF"v[92:95],a[156:159],v[204:207],v[92:95]\n"
PK"v107,v110,v111\n"
MF"v[36:39],a[160:163],v[208:211],v[36:39]\n"
RD"v[200:203],v245 offset:24576\n"
MU"v112,s55,v112\n"
MF"v[68:71],a[164:167],v[208:211],v[68:71]\n"
MU"v113,s55,v113\n"
MU"v114,s55,v114\n"
MF"v[44:47],a[168:171],v[208:211],v[44:47]\n"
SA"m0,s37,20480\n"
MU"v115,s55,v115\n"
MU"v116,s55,v116\n"
MF"v[76:79],a[172:175],v[208:211],v[76:79]\n"
SA"s48,s98,98304\n"
MU"v117,s55,v117\n"
MU"v118,s55,v118\n"
MF"v[52:55],a[160:163],v[212:215],v[52:55]\n"
RD"v[204:207],v245 offset:26624\n"
MU"v119,s55,v119\n"
MF"v[84:87],a[164:167],v[212:215],v[84:87]\n"
BL"v234,s[12:15],s48 offen offset:0 lds\n"
MF"v[60:63],a[168:171],v[212:215],v[60:63]\n"
PK"v112,v112,v113\n"
PK"v113,v114,v115\n"
MF"v[92:95],a[172:175],v[212:215],v[92:95]\n"
WT"lgkmcnt(1)\n"
RD"v[208:211],v244 offset:28672\n"
PK"v114,v116,v117\n"
MF"v[36:39],a[176:179],v[216:219],v[36:39]\n"
PK"v115,v118,v119\n"
MF"v[68:71],a[180:183],v[216:219],v[68:71]\n"
"s_mov_b32 s49,12288\n"
MF"v[44:47],a[184:187],v[216:219],v[44:47]\n"
BL"v235,s[12:15],s48 offen offset:1024 lds\n"
MF"v[76:79],a[188:191],v[216:219],v[76:79]\n"
RD"v[212:215],v244 offset:30720\n"
MU"v120,s55,v120\n"
MF"v[52:55],a[176:179],v[220:223],v[52:55]\n"
BS"v[48:51],v250,s[8:11],s49 offen offset:0 sc1\n"
MF"v[84:87],a[180:183],v[220:223],v[84:87]\n"
MU"v121,s55,v121\n"
MU"v122,s55,v122\n"
MF"v[60:63],a[184:187],v[220:223],v[60:63]\n"
MU"v123,s55,v123\n"
MU"v124,s55,v124\n"
MF"v[92:95],a[188:191],v[220:223],v[92:95]\n"
RD"v[216:219],v245 offset:28672\n"
MU"v125,s55,v125\n"
MF"v[36:39],a[192:195],v[192:195],v[36:39]\n"
BL"v236,s[12:15],s48 offen offset:2048 lds\n"
MF"v[68:71],a[196:199],v[192:195],v[68:71]\n"
MU"v126,s55,v126\n"
MU"v127,s55,v127\n"
MF"v[44:47],a[200:203],v[192:195],v[44:47]\n"
PK"v120,v120,v121\n"
PK"v121,v122,v123\n"
MF"v[76:79],a[204:207],v[192:195],v[76:79]\n"
RD"v[220:223],v245 offset:30720\n"
PK"v122,v124,v125\n"
MF"v[52:55],a[192:195],v[196:199],v[52:55]\n"
PK"v123,v126,v127\n"
MF"v[84:87],a[196:199],v[196:199],v[84:87]\n"
MF"v[60:63],a[200:203],v[196:199],v[60:63]\n"
BL"v237,s[12:15],s48 offen offset:3072 lds\n"
MF"v[92:95],a[204:207],v[196:199],v[92:95]\n"
MF"v[36:39],a[208:211],v[200:203],v[36:39]\n"
MF"v[68:71],a[212:215],v[200:203],v[68:71]\n"
MF"v[44:47],a[216:219],v[200:203],v[44:47]\n"
MF"v[76:79],a[220:223],v[200:203],v[76:79]\n"
WT"lgkmcnt(0)\n"
"v_lshrrev_b32 v251,2,v224\n"
MF"v[52:55],a[208:211],v[204:207],v[52:55]\n"
"v_and_b32 v251,28,v251\n"
MF"v[84:87],a[212:215],v[204:207],v[84:87]\n"
WT"vmcnt(23)\n"
"s_barrier\n"
MF"v[60:63],a[216:219],v[204:207],v[60:63]\n"
RD"v[192:195],v242 offset:8192\n"
MF"v[92:95],a[220:223],v[204:207],v[92:95]\n"
"ds_read_b32 v234,v251 offset:16384\n"
MF"v[36:39],a[224:227],v[208:211],v[36:39]\n"
MF"v[68:71],a[228:231],v[208:211],v[68:71]\n"
MF"v[44:47],a[232:235],v[208:211],v[44:47]\n"
RD"v[196:199],v242 offset:10240\n"
MF"v[76:79],a[236:239],v[208:211],v[76:79]\n"
MF"v[52:55],a[224:227],v[212:215],v[52:55]\n"
MF"v[84:87],a[228:231],v[212:215],v[84:87]\n"
MF"v[60:63],a[232:235],v[212:215],v[60:63]\n"
RD"v[200:203],v243 offset:8192\n"
MF"v[92:95],a[236:239],v[212:215],v[92:95]\n"
WT"lgkmcnt(2)\n"
"v_not_b32 v251,v234\n"
"v_bfe_u32 v252,v251,4,7\n"
MF"v[36:39],a[240:243],v[216:219],v[36:39]\n"
"v_mul_u32_u24 v252,20480,v252\n"
MF"v[68:71],a[244:247],v[216:219],v[68:71]\n"
"v_and_b32 v251,15,v251\n"
MF"v[44:47],a[248:251],v[216:219],v[44:47]\n"
RD"v[204:207],v243 offset:10240\n"
"v_lshl_add_u32 v251,v251,4,v252\n"
MF"v[76:79],a[252:255],v[216:219],v[76:79]\n"
BS"v[56:59],v250,s[8:11],s49 offen offset:1024 sc1\n"
MF"v[52:55],a[240:243],v[220:223],v[52:55]\n"
"v_and_b32 v252,0x380,v224\n"
"v_lshl_add_u32 v251,v252,1,v251\n"
MF"v[84:87],a[244:247],v[220:223],v[84:87]\n"
RD"v[208:211],v242 offset:12288\n"
"v_add_u32 v252,v252,v224\n"
MF"v[60:63],a[248:251],v[220:223],v[60:63]\n"
"s_lshl_b32 s92,s34,2\n"
MF"v[92:95],a[252:255],v[220:223],v[92:95]\n"
SA"s92,s92,s30\n"
MF"v[128:131],a[0:3],v[192:195],0\n"
"s_lshl_b32 s93,s92,11\n"
MF"v[132:135],a[4:7],v[192:195],0\n"
"s_cmp_lt_u32 s92,10\n"
RD"v[212:215],v242 offset:14336\n"
"s_cselect_b32 s93,s93,2130706432\n"
MF"v[136:139],a[8:11],v[192:195],0\n"
SA"s92,s93,65536\n"
MF"v[140:143],a[12:15],v[192:195],0\n"
WT"lgkmcnt(2)\n"
BL"v[100:103],v251,s[8:11],s92 offen sc1\n"
MF"v[144:147],a[0:3],v[196:199],0\n"
"s_mul_i32 s92,s35,20480\n"
MU"v36,s55,v36\n"
MU"v37,s55,v37\n"
MF"v[148:151],a[4:7],v[196:199],0\n"
SA"s92,s92,s93\n"
RD"v[216:219],v243 offset:12288\n"
MU"v38,s55,v38\n"
MF"v[152:155],a[8:11],v[196:199],0\n"
SA"s92,s92,2770944\n"
MU"v39,s55,v39\n"
MU"v68,s55,v68\n"
MF"v[156:159],a[12:15],v[196:199],0\n"
MU"v69,s55,v69\n"
MU"v70,s55,v70\n"
MF"v[128:131],a[16:19],v[200:203],v[128:131]\n"
MU"v71,s55,v71\n"
PK"v36,v36,v37\n"
MF"v[132:135],a[20:23],v[200:203],v[132:135]\n"
RD"v[220:223],v243 offset:14336\n"
PK"v37,v38,v39\n"
MF"v[136:139],a[24:27],v[200:203],v[136:139]\n"
PK"v38,v68,v69\n"
PK"v39,v70,v71\n"
MF"v[140:143],a[28:31],v[200:203],v[140:143]\n"
MF"v[144:147],a[16:19],v[204:207],v[144:147]\n"
RD"v[192:195],v242 offset:32768\n"
MF"v[148:151],a[20:23],v[204:207],v[148:151]\n"
MU"v44,s55,v44\n"
MU"v45,s55,v45\n"
MF"v[152:155],a[24:27],v[204:207],v[152:155]\n"
MU"v46,s55,v46\n"
MU"v47,s55,v47\n"
MF"v[156:159],a[28:31],v[204:207],v[156:159]\n"
WT"lgkmcnt(2)\n"
MU"v76,s55,v76\n"
MU"v77,s55,v77\n"
MF"v[128:131],a[32:35],v[208:211],v[128:131]\n"
RD"v[196:199],v242 offset:34816\n"
MU"v78,s55,v78\n"
MF"v[132:135],a[36:39],v[208:211],v[132:135]\n"
MU"v79,s55,v79\n"
PK"v44,v44,v45\n"
MF"v[136:139],a[40:43],v[208:211],v[136:139]\n"
PK"v45,v46,v47\n"
PK"v46,v76,v77\n"
MF"v[140:143],a[44:47],v[208:211],v[140:143]\n"
PK"v47,v78,v79\n"
MF"v[144:147],a[32:35],v[212:215],v[144:147]\n"
SA"m0,s36,0\n"
RD"v[200:203],v243 offset:32768\n"
SA"s48,s60,s61\n"
MF"v[148:151],a[36:39],v[212:215],v[148:151]\n"
MF"v[152:155],a[40:43],v[212:215],v[152:155]\n"
BL"v238,s[20:23],s48 offen offset:0 lds\n"
MF"v[156:159],a[44:47],v[212:215],v[156:159]\n"
"s_mov_b32 s49,16384\n"
MU"v52,s55,v52\n"
MU"v53,s55,v53\n"
MF"v[128:131],a[48:51],v[216:219],v[128:131]\n"
RD"v[204:207],v243 offset:34816\n"
MU"v54,s55,v54\n"
MF"v[132:135],a[52:55],v[216:219],v[132:135]\n"
BS"v[64:67],v250,s[8:11],s49 offen offset:0 sc1\n"
MF"v[136:139],a[56:59],v[216:219],v[136:139]\n"
MU"v55,s55,v55\n"
MU"v84,s55,v84\n"
MF"v[140:143],a[60:63],v[216:219],v[140:143]\n"
WT"lgkmcnt(1)\n"
RD"v[208:211],v242 offset:36864\n"
MU"v85,s55,v85\n"
MF"v[144:147],a[48:51],v[220:223],v[144:147]\n"
MU"v86,s55,v86\n"
MU"v87,s55,v87\n"
MF"v[148:151],a[52:55],v[220:223],v[148:151]\n"
PK"v52,v52,v53\n"
PK"v53,v54,v55\n"
MF"v[152:155],a[56:59],v[220:223],v[152:155]\n"
BL"v239,s[20:23],s48 offen offset:1024 lds\n"
MF"v[156:159],a[60:63],v[220:223],v[156:159]\n"
RD"v[212:215],v242 offset:38912\n"
PK"v54,v84,v85\n"
MF"v[128:131],a[64:67],v[192:195],v[128:131]\n"
PK"v55,v86,v87\n"
MF"v[132:135],a[68:71],v[192:195],v[132:135]\n"
MU"v60,s55,v60\n"
MU"v61,s55,v61\n"
MF"v[136:139],a[72:75],v[192:195],v[136:139]\n"
MU"v62,s55,v62\n"
MU"v63,s55,v63\n"
MF"v[140:143],a[76:79],v[192:195],v[140:143]\n"
RD"v[216:219],v243 offset:36864\n"
MU"v92,s55,v92\n"
MF"v[144:147],a[64:67],v[196:199],v[144:147]\n"
MU"v93,s55,v93\n"
MU"v94,s55,v94\n"
MF"v[148:151],a[68:71],v[196:199],v[148:151]\n"
MU"v95,s55,v95\n"
PK"v60,v60,v61\n"
MF"v[152:155],a[72:75],v[196:199],v[152:155]\n"
BL"v240,s[20:23],s48 offen offset:2048 lds\n"
MF"v[156:159],a[76:79],v[196:199],v[156:159]\n"
RD"v[220:223],v243 offset:38912\n"
PK"v61,v62,v63\n"
MF"v[128:131],a[80:83],v[200:203],v[128:131]\n"
PK"v62,v92,v93\n"
PK"v63,v94,v95\n"
MF"v[132:135],a[84:87],v[200:203],v[132:135]\n"
MF"v[136:139],a[88:91],v[200:203],v[136:139]\n"
RD"v[192:195],v244 offset:8192\n"
MF"v[140:143],a[92:95],v[200:203],v[140:143]\n"
WT"lgkmcnt(2)\n"
MF"v[144:147],a[80:83],v[204:207],v[144:147]\n"
MF"v[148:151],a[84:87],v[204:207],v[148:151]\n"
MF"v[152:155],a[88:91],v[204:207],v[152:155]\n"
SA"s61,s61,524288\n"
RD"v[196:199],v244 offset:10240\n"
MF"v[156:159],a[92:95],v[204:207],v[156:159]\n"
BL"v241,s[20:23],s48 offen offset:3072 lds\n"
MF"v[128:131],a[96:99],v[208:211],v[128:131]\n"
MF"v[132:135],a[100:103],v[208:211],v[132:135]\n"
MF"v[136:139],a[104:107],v[208:211],v[136:139]\n"
RD"v[200:203],v245 offset:8192\n"
MF"v[140:143],a[108:111],v[208:211],v[140:143]\n"
MF"v[144:147],a[96:99],v[212:215],v[144:147]\n"
MF"v[148:151],a[100:103],v[212:215],v[148:151]\n"
MF"v[152:155],a[104:107],v[212:215],v[152:155]\n"
RD"v[204:207],v245 offset:10240\n"
MF"v[156:159],a[108:111],v[212:215],v[156:159]\n"
MF"v[128:131],a[112:115],v[216:219],v[128:131]\n"
BS"v[72:75],v250,s[8:11],s49 offen offset:1024 sc1\n"
MF"v[132:135],a[116:119],v[216:219],v[132:135]\n"
RD"v[208:211],v244 offset:12288\n"
MF"v[136:139],a[120:123],v[216:219],v[136:139]\n"
MF"v[140:143],a[124:127],v[216:219],v[140:143]\n"
WT"lgkmcnt(2)\n"
MF"v[144:147],a[112:115],v[220:223],v[144:147]\n"
SA"m0,s38,65536\n"
SA"s48,s60,s61\n"
MF"v[148:151],a[116:119],v[220:223],v[148:151]\n"
RD"v[212:215],v244 offset:14336\n"
MF"v[152:155],a[120:123],v[220:223],v[152:155]\n"
BL"v238,s[20:23],s48 offen offset:0 lds\n"
MF"v[156:159],a[124:127],v[220:223],v[156:159]\n"
MF"v[128:131],a[128:131],v[192:195],v[128:131]\n"
MF"v[132:135],a[132:135],v[192:195],v[132:135]\n"
RD"v[216:219],v245 offset:12288\n"
MF"v[136:139],a[136:139],v[192:195],v[136:139]\n"
MF"v[140:143],a[140:143],v[192:195],v[140:143]\n"
MF"v[144:147],a[128:131],v[196:199],v[144:147]\n"
MF"v[148:151],a[132:135],v[196:199],v[148:151]\n"
RD"v[220:223],v245 offset:14336\n"
MF"v[152:155],a[136:139],v[196:199],v[152:155]\n"
BL"v239,s[20:23],s48 offen offset:1024 lds\n"
MF"v[156:159],a[140:143],v[196:199],v[156:159]\n"
MF"v[128:131],a[144:147],v[200:203],v[128:131]\n"
RD"v[192:195],v244 offset:32768\n"
MF"v[132:135],a[148:151],v[200:203],v[132:135]\n"
MF"v[136:139],a[152:155],v[200:203],v[136:139]\n"
MF"v[140:143],a[156:159],v[200:203],v[140:143]\n"
WT"lgkmcnt(2)\n"
MF"v[144:147],a[144:147],v[204:207],v[144:147]\n"
RD"v[196:199],v244 offset:34816\n"
MF"v[148:151],a[148:151],v[204:207],v[148:151]\n"
MF"v[152:155],a[152:155],v[204:207],v[152:155]\n"
BL"v240,s[20:23],s48 offen offset:2048 lds\n"
MF"v[156:159],a[156:159],v[204:207],v[156:159]\n"
MF"v[128:131],a[160:163],v[208:211],v[128:131]\n"
RD"v[200:203],v245 offset:32768\n"
MF"v[132:135],a[164:167],v[208:211],v[132:135]\n"
MF"v[136:139],a[168:171],v[208:211],v[136:139]\n"
MF"v[140:143],a[172:175],v[208:211],v[140:143]\n"
MF"v[144:147],a[160:163],v[212:215],v[144:147]\n"
RD"v[204:207],v245 offset:34816\n"
MF"v[148:151],a[164:167],v[212:215],v[148:151]\n"
"s_mov_b32 s49,20480\n"
MF"v[152:155],a[168:171],v[212:215],v[152:155]\n"
SA"s61,s61,524288\n"
BL"v241,s[20:23],s48 offen offset:3072 lds\n"
MF"v[156:159],a[172:175],v[212:215],v[156:159]\n"
"s_cmp_eq_u32 s61,5242880\n"
RD"v[208:211],v244 offset:36864\n"
"s_cselect_b32 s61,0,s61\n"
MF"v[128:131],a[176:179],v[216:219],v[128:131]\n"
BS"v[80:83],v250,s[8:11],s49 offen offset:0 sc1\n"
MF"v[132:135],a[180:183],v[216:219],v[132:135]\n"
MF"v[136:139],a[184:187],v[216:219],v[136:139]\n"
MF"v[140:143],a[188:191],v[216:219],v[140:143]\n"
WT"lgkmcnt(1)\n"
RD"v[212:215],v244 offset:38912\n"
MF"v[144:147],a[176:179],v[220:223],v[144:147]\n"
SA"m0,s38,114688\n"
SA"s48,s60,s61\n"
MF"v[148:151],a[180:183],v[220:223],v[148:151]\n"
MF"v[152:155],a[184:187],v[220:223],v[152:155]\n"
BL"v238,s[20:23],s48 offen offset:0 lds\n"
MF"v[156:159],a[188:191],v[220:223],v[156:159]\n"
RD"v[216:219],v245 offset:36864\n"
MF"v[128:131],a[192:195],v[192:195],v[128:131]\n"
MF"v[132:135],a[196:199],v[192:195],v[132:135]\n"
MF"v[136:139],a[200:203],v[192:195],v[136:139]\n"
MF"v[140:143],a[204:207],v[192:195],v[140:143]\n"
RD"v[220:223],v245 offset:38912\n"
MF"v[144:147],a[192:195],v[196:199],v[144:147]\n"
MF"v[148:151],a[196:199],v[196:199],v[148:151]\n"
MF"v[152:155],a[200:203],v[196:199],v[152:155]\n"
BL"v239,s[20:23],s48 offen offset:1024 lds\n"
MF"v[156:159],a[204:207],v[196:199],v[156:159]\n"
WT"vmcnt(13)\n"
BS"v[100:103],v252,s[8:11],s92 offen sc1\n"
MF"v[128:131],a[208:211],v[200:203],v[128:131]\n"
MF"v[132:135],a[212:215],v[200:203],v[132:135]\n"
MF"v[136:139],a[216:219],v[200:203],v[136:139]\n"
MF"v[140:143],a[220:223],v[200:203],v[140:143]\n"
MF"v[144:147],a[208:211],v[204:207],v[144:147]\n"
MF"v[148:151],a[212:215],v[204:207],v[148:151]\n"
WT"lgkmcnt(0)\n"
"s_barrier\n"
MF"v[152:155],a[216:219],v[204:207],v[152:155]\n"
RD"v[192:195],v242 offset:16384\n"
MF"v[156:159],a[220:223],v[204:207],v[156:159]\n"
BL"v240,s[20:23],s48 offen offset:2048 lds\n"
MF"v[128:131],a[224:227],v[208:211],v[128:131]\n"
MF"v[132:135],a[228:231],v[208:211],v[132:135]\n"
MF"v[136:139],a[232:235],v[208:211],v[136:139]\n"
RD"v[196:199],v242 offset:18432\n"
MF"v[140:143],a[236:239],v[208:211],v[140:143]\n"
MF"v[144:147],a[224:227],v[212:215],v[144:147]\n"
MF"v[148:151],a[228:231],v[212:215],v[148:151]\n"
MF"v[152:155],a[232:235],v[212:215],v[152:155]\n"
SA"s61,s61,524288\n"
RD"v[200:203],v243 offset:16384\n"
MF"v[156:159],a[236:239],v[212:215],v[156:159]\n"
BS"v[88:91],v250,s[8:11],s49 offen offset:1024 sc1\n"
MF"v[128:131],a[240:243],v[216:219],v[128:131]\n"
"s_lshl_b32 s50,s34,3\n"
BL"v241,s[20:23],s48 offen offset:3072 lds\n"
MF"v[132:135],a[244:247],v[216:219],v[132:135]\n"
SA"s51,s50,s30\n"
MF"v[136:139],a[248:251],v[216:219],v[136:139]\n"
"s_mul_i32 s51,s51,20480\n"
RD"v[204:207],v243 offset:18432\n"
MF"v[140:143],a[252:255],v[216:219],v[140:143]\n"
SA"s56,s51,65536\n"
MF"v[144:147],a[240:243],v[220:223],v[144:147]\n"
SA"m0,s36,16384\n"
SA"s48,s60,s61\n"
SA"s57,s56,81920\n"
MF"v[148:151],a[244:247],v[220:223],v[148:151]\n"
"s_sub_u32 s51,s30,s50\n"
RD"v[208:211],v242 offset:20480\n"
MF"v[152:155],a[248:251],v[220:223],v[152:155]\n"
SA"s51,s51,120\n"
BL"v238,s[20:23],s48 offen offset:0 lds\n"
MF"v[156:159],a[252:255],v[220:223],v[156:159]\n"
WT"lgkmcnt(2)\n"
"s_mul_i32 s51,s51,20480\n"
MF"v[0:3],a[0:3],v[192:195],0\n"
SA"s58,s51,65536\n"
"s_lshl_b32 s99,s99,12\n"
MF"v[8:11],a[4:7],v[192:195],0\n"
SA"s59,s58,81920\n"
RD"v[212:215],v242 offset:22528\n"
MF"v[16:19],a[8:11],v[192:195],0\n"
"v_add_u32 v235,s99,v224\n"
MF"v[24:27],a[12:15],v[192:195],0\n"
MU"v128,s55,v128\n"
MU"v129,s55,v129\n"
MF"v[32:35],a[0:3],v[196:199],0\n"
MU"v130,s55,v130\n"
MU"v131,s55,v131\n"
MF"v[40:43],a[4:7],v[196:199],0\n"
RD"v[216:219],v243 offset:20480\n"
MU"v132,s55,v132\n"
MF"v[48:51],a[8:11],v[196:199],0\n"
BL"v239,s[20:23],s48 offen offset:1024 lds\n"
MF"v[56:59],a[12:15],v[196:199],0\n"
MU"v133,s55,v133\n"
MU"v134,s55,v134\n"
MF"v[0:3],a[16:19],v[200:203],v[0:3]\n"
BL"a[0:3],v235,s[8:11],s56 offen offset:0 sc1\n"
MF"v[8:11],a[20:23],v[200:203],v[8:11]\n"
RD"v[220:223],v243 offset:22528\n"
MU"v135,s55,v135\n"
MF"v[16:19],a[24:27],v[200:203],v[16:19]\n"
PK"v64,v128,v129\n"
PK"v65,v130,v131\n"
MF"v[24:27],a[28:31],v[200:203],v[24:27]\n"
WT"lgkmcnt(2)\n"
PK"v66,v132,v133\n"
PK"v67,v134,v135\n"
MF"v[32:35],a[16:19],v[204:207],v[32:35]\n"
RD"v[192:195],v242 offset:40960\n"
MF"v[40:43],a[20:23],v[204:207],v[40:43]\n"
MU"v136,s55,v136\n"
MU"v137,s55,v137\n"
MF"v[48:51],a[24:27],v[204:207],v[48:51]\n"
BL"v240,s[20:23],s48 offen offset:2048 lds\n"
MF"v[56:59],a[28:31],v[204:207],v[56:59]\n"
BL"a[4:7],v235,s[8:11],s57 offen offset:0 sc1\n"
MF"v[0:3],a[32:35],v[208:211],v[0:3]\n"
RD"v[196:199],v242 offset:43008\n"
MU"v138,s55,v138\n"
MF"v[8:11],a[36:39],v[208:211],v[8:11]\n"
MU"v139,s55,v139\n"
MU"v140,s55,v140\n"
MF"v[16:19],a[40:43],v[208:211],v[16:19]\n"
MU"v141,s55,v141\n"
MU"v142,s55,v142\n"
MF"v[24:27],a[44:47],v[208:211],v[24:27]\n"
"s_mov_b32 s49,24576\n"
MU"v143,s55,v143\n"
PK"v68,v136,v137\n"
MF"v[32:35],a[32:35],v[212:215],v[32:35]\n"
RD"v[200:203],v243 offset:40960\n"
PK"v69,v138,v139\n"
MF"v[40:43],a[36:39],v[212:215],v[40:43]\n"
BL"a[8:11],v235,s[8:11],s58 offen offset:0 sc1\n"
MF"v[48:51],a[40:43],v[212:215],v[48:51]\n"
SA"s61,s61,524288\n"
BS"v[96:99],v250,s[8:11],s49 offen offset:0 sc1\n"
MF"v[56:59],a[44:47],v[212:215],v[56:59]\n"
WT"lgkmcnt(1)\n"
BL"v241,s[20:23],s48 offen offset:3072 lds\n"
MF"v[0:3],a[48:51],v[216:219],v[0:3]\n"
"s_cmp_eq_u32 s61,5242880\n"
RD"v[204:207],v243 offset:43008\n"
PK"v70,v140,v141\n"
MF"v[8:11],a[52:55],v[216:219],v[8:11]\n"
"s_cselect_b32 s61,0,s61\n"
PK"v71,v142,v143\n"
MU"v144,s55,v144\n"
MF"v[16:19],a[56:59],v[216:219],v[16:19]\n"
BL"a[12:15],v235,s[8:11],s59 offen offset:0 sc1\n"
MF"v[24:27],a[60:63],v[216:219],v[24:27]\n"
RD"v[208:211],v242 offset:45056\n"
MU"v145,s55,v145\n"
MF"v[32:35],a[48:51],v[220:223],v[32:35]\n"
MU"v146,s55,v146\n"
MU"v147,s55,v147\n"
MF"v[40:43],a[52:55],v[220:223],v[40:43]\n"
MU"v148,s55,v148\n"
MU"v149,s55,v149\n"
MF"v[48:51],a[56:59],v[220:223],v[48:51]\n"
MU"v150,s55,v150\n"
MU"v151,s55,v151\n"
MF"v[56:59],a[60:63],v[220:223],v[56:59]\n"
RD"v[212:215],v242 offset:47104\n"
PK"v72,v144,v145\n"
MF"v[0:3],a[64:67],v[192:195],v[0:3]\n"
BL"a[16:19],v235,s[8:11],s56 offen offset:1024 sc1\n"
MF"v[8:11],a[68:71],v[192:195],v[8:11]\n"
PK"v73,v146,v147\n"
PK"v74,v148,v149\n"
MF"v[16:19],a[72:75],v[192:195],v[16:19]\n"
PK"v75,v150,v151\n"
MU"v152,s55,v152\n"
MF"v[24:27],a[76:79],v[192:195],v[24:27]\n"
RD"v[216:219],v243 offset:45056\n"
MU"v153,s55,v153\n"
MF"v[32:35],a[64:67],v[196:199],v[32:35]\n"
MU"v154,s55,v154\n"
MU"v155,s55,v155\n"
MF"v[40:43],a[68:71],v[196:199],v[40:43]\n"
MU"v156,s55,v156\n"
MU"v157,s55,v157\n"
MF"v[48:51],a[72:75],v[196:199],v[48:51]\n"
BL"a[20:23],v235,s[8:11],s57 offen offset:1024 sc1\n"
MF"v[56:59],a[76:79],v[196:199],v[56:59]\n"
WT"lgkmcnt(1)\n"
RD"v[220:223],v243 offset:47104\n"
MU"v158,s55,v158\n"
MF"v[0:3],a[80:83],v[200:203],v[0:3]\n"
MU"v159,s55,v159\n"
PK"v76,v152,v153\n"
MF"v[8:11],a[84:87],v[200:203],v[8:11]\n"
PK"v77,v154,v155\n"
PK"v78,v156,v157\n"
MF"v[16:19],a[88:91],v[200:203],v[16:19]\n"
RD"v[192:195],v244 offset:16384\n"
PK"v79,v158,v159\n"
MF"v[24:27],a[92:95],v[200:203],v[24:27]\n"
MF"v[32:35],a[80:83],v[204:207],v[32:35]\n"
BL"a[24:27],v235,s[8:11],s58 offen offset:1024 sc1\n"
MF"v[40:43],a[84:87],v[204:207],v[40:43]\n"
MF"v[48:51],a[88:91],v[204:207],v[48:51]\n"
RD"v[196:199],v244 offset:18432\n"
MF"v[56:59],a[92:95],v[204:207],v[56:59]\n"
MF"v[0:3],a[96:99],v[208:211],v[0:3]\n"
MF"v[8:11],a[100:103],v[208:211],v[8:11]\n"
MF"v[16:19],a[104:107],v[208:211],v[16:19]\n"
RD"v[200:203],v245 offset:16384\n"
MF"v[24:27],a[108:111],v[208:211],v[24:27]\n"
BL"a[28:31],v235,s[8:11],s59 offen offset:1024 sc1\n"
MF"v[32:35],a[96:99],v[212:215],v[32:35]\n"
BS"v[104:107],v250,s[8:11],s49 offen offset:1024 sc1\n"
MF"v[40:43],a[100:103],v[212:215],v[40:43]\n"
MF"v[48:51],a[104:107],v[212:215],v[48:51]\n"
RD"v[204:207],v245 offset:18432\n"
MF"v[56:59],a[108:111],v[212:215],v[56:59]\n"
WT"lgkmcnt(2)\n"
MF"v[0:3],a[112:115],v[216:219],v[0:3]\n"
BL"a[32:35],v235,s[8:11],s56 offen offset:2048 sc1\n"
MF"v[8:11],a[116:119],v[216:219],v[8:11]\n"
RD"v[208:211],v244 offset:20480\n"
MF"v[16:19],a[120:123],v[216:219],v[16:19]\n"
MF"v[24:27],a[124:127],v[216:219],v[24:27]\n"
MF"v[32:35],a[112:115],v[220:223],v[32:35]\n"
MF"v[40:43],a[116:119],v[220:223],v[40:43]\n"
RD"v[212:215],v244 offset:22528\n"
MF"v[48:51],a[120:123],v[220:223],v[48:51]\n"
BL"a[36:39],v235,s[8:11],s57 offen offset:2048 sc1\n"
MF"v[56:59],a[124:127],v[220:223],v[56:59]\n"
MF"v[0:3],a[128:131],v[192:195],v[0:3]\n"
MF"v[8:11],a[132:135],v[192:195],v[8:11]\n"
RD"v[216:219],v245 offset:20480\n"
MF"v[16:19],a[136:139],v[192:195],v[16:19]\n"
MF"v[24:27],a[140:143],v[192:195],v[24:27]\n"
MF"v[32:35],a[128:131],v[196:199],v[32:35]\n"
BL"a[40:43],v235,s[8:11],s58 offen offset:2048 sc1\n"
MF"v[40:43],a[132:135],v[196:199],v[40:43]\n"
RD"v[220:223],v245 offset:22528\n"
MF"v[48:51],a[136:139],v[196:199],v[48:51]\n"
MF"v[56:59],a[140:143],v[196:199],v[56:59]\n"
WT"lgkmcnt(2)\n"
MF"v[0:3],a[144:147],v[200:203],v[0:3]\n"
RD"v[192:195],v244 offset:40960\n"
MF"v[8:11],a[148:151],v[200:203],v[8:11]\n"
MF"v[16:19],a[152:155],v[200:203],v[16:19]\n"
BL"a[44:47],v235,s[8:11],s59 offen offset:2048 sc1\n"
MF"v[24:27],a[156:159],v[200:203],v[24:27]\n"
MF"v[32:35],a[144:147],v[204:207],v[32:35]\n"
RD"v[196:199],v244 offset:43008\n"
MF"v[40:43],a[148:151],v[204:207],v[40:43]\n"
MF"v[48:51],a[152:155],v[204:207],v[48:51]\n"
MF"v[56:59],a[156:159],v[204:207],v[56:59]\n"
MF"v[0:3],a[160:163],v[208:211],v[0:3]\n"
"s_sub_u32 s53,s37,65536\n"
RD"v[200:203],v245 offset:40960\n"
MF"v[8:11],a[164:167],v[208:211],v[8:11]\n"
"s_mov_b32 s49,28672\n"
BL"a[48:51],v235,s[8:11],s56 offen offset:3072 sc1\n"
MF"v[16:19],a[168:171],v[208:211],v[16:19]\n"
"v_subrev_u32 v246,s53,v246\n"
"v_subrev_u32 v247,s53,v247\n"
MF"v[24:27],a[172:175],v[208:211],v[24:27]\n"
BS"v[112:115],v250,s[8:11],s49 offen offset:0 sc1\n"
MF"v[32:35],a[160:163],v[212:215],v[32:35]\n"
RD"v[204:207],v245 offset:43008\n"
"v_subrev_u32 v248,s53,v248\n"
MF"v[40:43],a[164:167],v[212:215],v[40:43]\n"
"v_subrev_u32 v249,s53,v249\n"
"v_subrev_u32 v236,65536,v246\n"
MF"v[48:51],a[168:171],v[212:215],v[48:51]\n"
BL"a[52:55],v235,s[8:11],s57 offen offset:3072 sc1\n"
MF"v[56:59],a[172:175],v[212:215],v[56:59]\n"
WT"lgkmcnt(1)\n"
RD"v[208:211],v244 offset:45056\n"
"v_subrev_u32 v237,65536,v247\n"
MF"v[0:3],a[176:179],v[216:219],v[0:3]\n"
"v_subrev_u32 v228,65536,v248\n"
"v_subrev_u32 v229,65536,v249\n"
MF"v[8:11],a[180:183],v[216:219],v[8:11]\n"
MF"v[16:19],a[184:187],v[216:219],v[16:19]\n"
MF"v[24:27],a[188:191],v[216:219],v[24:27]\n"
RD"v[212:215],v244 offset:47104\n"
MF"v[32:35],a[176:179],v[220:223],v[32:35]\n"
BL"a[56:59],v235,s[8:11],s58 offen offset:3072 sc1\n"
MF"v[40:43],a[180:183],v[220:223],v[40:43]\n"
MF"v[48:51],a[184:187],v[220:223],v[48:51]\n"
MF"v[56:59],a[188:191],v[220:223],v[56:59]\n"
RD"v[216:219],v245 offset:45056\n"
MF"v[0:3],a[192:195],v[192:195],v[0:3]\n"
SA"s99,s99,4096\n"
MF"v[8:11],a[196:199],v[192:195],v[8:11]\n"
"s_cmp_eq_u32 s99,20480\n"
"s_cselect_b32 s99,0,s99\n"
MF"v[16:19],a[200:203],v[192:195],v[16:19]\n"
BL"a[60:63],v235,s[8:11],s59 offen offset:3072 sc1\n"
MF"v[24:27],a[204:207],v[192:195],v[24:27]\n"
RD"v[220:223],v245 offset:47104\n"
MF"v[32:35],a[192:195],v[196:199],v[32:35]\n"
MF"v[40:43],a[196:199],v[196:199],v[40:43]\n"
MF"v[48:51],a[200:203],v[196:199],v[48:51]\n"
MF"v[56:59],a[204:207],v[196:199],v[56:59]\n"
"v_add_u32 v235,s99,v224\n"
MF"v[0:3],a[208:211],v[200:203],v[0:3]\n"
BL"a[64:67],v235,s[8:11],s56 offen offset:0 sc1\n"
MF"v[8:11],a[212:215],v[200:203],v[8:11]\n"
MF"v[16:19],a[216:219],v[200:203],v[16:19]\n"
MF"v[24:27],a[220:223],v[200:203],v[24:27]\n"
WT"lgkmcnt(0)\n"
MF"v[32:35],a[208:211],v[204:207],v[32:35]\n"
MF"v[40:43],a[212:215],v[204:207],v[40:43]\n"
"s_barrier\n"
MF"v[48:51],a[216:219],v[204:207],v[48:51]\n"
RT"a[128:129],v236 offset:49152\n"
MF"v[56:59],a[220:223],v[204:207],v[56:59]\n"
BL"a[68:71],v235,s[8:11],s57 offen offset:0 sc1\n"
MF"v[0:3],a[224:227],v[208:211],v[0:3]\n"
SA"m0,s38,73728\n"
RT"a[130:131],v236 offset:49664\n"
SA"s48,s60,s61\n"
MF"v[8:11],a[228:231],v[208:211],v[8:11]\n"
MF"v[16:19],a[232:235],v[208:211],v[16:19]\n"
RT"a[132:133],v237 offset:49152\n"
"v_add_u32 v242,49152,v246\n"
MF"v[24:27],a[236:239],v[208:211],v[24:27]\n"
BS"v[120:123],v250,s[8:11],s49 offen offset:1024 sc1\n"
MF"v[32:35],a[224:227],v[212:215],v[32:35]\n"
BL"v238,s[20:23],s48 offen offset:0 lds\n"
MF"v[40:43],a[228:231],v[212:215],v[40:43]\n"
RT"a[134:135],v237 offset:49664\n"
"v_add_u32 v243,49152,v247\n"
MF"v[48:51],a[232:235],v[212:215],v[48:51]\n"
BL"a[72:75],v235,s[8:11],s58 offen offset:0 sc1\n"
MF"v[56:59],a[236:239],v[212:215],v[56:59]\n"
RT"a[136:137],v228 offset:49152\n"
"v_add_u32 v244,49152,v248\n"
MF"v[0:3],a[240:243],v[216:219],v[0:3]\n"
RT"a[138:139],v228 offset:49664\n"
"v_add_u32 v245,49152,v249\n"
MF"v[8:11],a[244:247],v[216:219],v[8:11]\n"
MF"v[16:19],a[248:251],v[216:219],v[16:19]\n"
RT"a[140:141],v229 offset:49152\n"
MF"v[24:27],a[252:255],v[216:219],v[24:27]\n"
BL"v239,s[20:23],s48 offen offset:1024 lds\n"
MF"v[32:35],a[240:243],v[220:223],v[32:35]\n"
BL"a[76:79],v235,s[8:11],s59 offen offset:0 sc1\n"
MF"v[40:43],a[244:247],v[220:223],v[40:43]\n"
RT"a[142:143],v229 offset:49664\n"
MF"v[48:51],a[248:251],v[220:223],v[48:51]\n"
RT"a[144:145],v236 offset:53248\n"
MF"v[56:59],a[252:255],v[220:223],v[56:59]\n"
WT"vmcnt(27) lgkmcnt(3)\n"
MF"v[80:83],a[128:131],a[0:3],0\n"
WT"vmcnt(25)\n"
RT"a[146:147],v236 offset:53760\n"
MF"v[88:91],a[128:131],a[4:7],0\n"
WT"vmcnt(24)\n"
BL"a[80:83],v235,s[8:11],s56 offen offset:1024 sc1\n"
MF"v[96:99],a[128:131],a[8:11],0\n"
WT"vmcnt(22)\n"
RT"a[148:149],v237 offset:53248\n"
MF"v[104:107],a[128:131],a[12:15],0\n"
BL"v240,s[20:23],s48 offen offset:2048 lds\n"
MF"v[84:87],a[132:135],a[0:3],0\n"
RT"a[150:151],v237 offset:53760\n"
MU"v0,s55,v0\n"
MF"v[92:95],a[132:135],a[4:7],0\n"
RT"a[152:153],v228 offset:53248\n"
MU"v1,s55,v1\n"
MF"v[100:103],a[132:135],a[8:11],0\n"
MU"v2,s55,v2\n"
MU"v3,s55,v3\n"
MF"v[108:111],a[132:135],a[12:15],0\n"
RT"a[154:155],v228 offset:53760\n"
MU"v8,s55,v8\n"
MF"v[112:115],a[136:139],a[0:3],0\n"
BL"a[84:87],v235,s[8:11],s57 offen offset:1024 sc1\n"
MF"v[120:123],a[136:139],a[4:7],0\n"
RT"a[156:157],v229 offset:53248\n"
MU"v9,s55,v9\n"
MF"v[128:131],a[136:139],a[8:11],0\n"
SA"s61,s61,524288\n"
BL"v241,s[20:23],s48 offen offset:3072 lds\n"
MF"v[136:139],a[136:139],a[12:15],0\n"
WT"lgkmcnt(3)\n"
RT"a[158:159],v229 offset:53760\n"
MU"v10,s55,v10\n"
MF"v[116:119],a[140:143],a[0:3],0\n"
MU"v11,s55,v11\n"
PK"v0,v0,v1\n"
MF"v[124:127],a[140:143],a[4:7],0\n"
RT"a[160:161],v236 offset:57344\n"
PK"v1,v2,v3\n"
MF"v[132:135],a[140:143],a[8:11],0\n"
BL"a[88:91],v235,s[8:11],s58 offen offset:1024 sc1\n"
MF"v[140:143],a[140:143],a[12:15],0\n"
RT"a[162:163],v236 offset:57856\n"
PK"v2,v8,v9\n"
MF"v[144:147],a[144:147],a[0:3],0\n"
PK"v3,v10,v11\n"
MU"v16,s55,v16\n"
MF"v[152:155],a[144:147],a[4:7],0\n"
RT"a[164:165],v237 offset:57344\n"
MU"v17,s55,v17\n"
MF"v[160:163],a[144:147],a[8:11],0\n"
MU"v18,s55,v18\n"
MU"v19,s55,v19\n"
MF"v[168:171],a[144:147],a[12:15],0\n"
RT"a[166:167],v237 offset:57856\n"
MU"v24,s55,v24\n"
MF"v[148:151],a[148:151],a[0:3],0\n"
MU"v25,s55,v25\n"
MU"v26,s55,v26\n"
MF"v[156:159],a[148:151],a[4:7],0\n"
RT"a[168:169],v228 offset:57344\n"
MU"v27,s55,v27\n"
MF"v[164:167],a[148:151],a[8:11],0\n"
BL"a[92:95],v235,s[8:11],s59 offen offset:1024 sc1\n"
MF"v[172:175],a[148:151],a[12:15],0\n"
WT"lgkmcnt(3)\n"
RT"a[170:171],v228 offset:57856\n"
PK"v16,v16,v17\n"
MF"v[176:179],a[152:155],a[0:3],0\n"
"s_mov_b32 s49,262144\n"
RT"a[172:173],v229 offset:57344\n"
PK"v17,v18,v19\n"
MF"v[184:187],a[152:155],a[4:7],0\n"
PK"v18,v24,v25\n"
PK"v19,v26,v27\n"
MF"v[192:195],a[152:155],a[8:11],0\n"
RT"a[174:175],v229 offset:57856\n"
MU"v32,s55,v32\n"
MF"v[200:203],a[152:155],a[12:15],0\n"
BS"v[4:7],v250,s[8:11],s49 offen offset:0 sc1\n"
MF"v[180:183],a[156:159],a[0:3],0\n"
RT"a[176:177],v236 offset:61440\n"
MU"v33,s55,v33\n"
MF"v[188:191],a[156:159],a[4:7],0\n"
BL"a[96:99],v235,s[8:11],s56 offen offset:2048 sc1\n"
MF"v[196:199],a[156:159],a[8:11],0\n"
RT"a[178:179],v236 offset:61952\n"
MU"v34,s55,v34\n"
MF"v[204:207],a[156:159],a[12:15],0\n"
WT"vmcnt(28)\n"
MU"v35,s55,v35\n"
MU"v40,s55,v40\n"
MF"v[80:83],a[160:163],a[16:19],v[80:83]\n"
WT"vmcnt(27)\n"
RT"a[180:181],v237 offset:61440\n"
MU"v41,s55,v41\n"
MF"v[88:91],a[160:163],a[20:23],v[88:91]\n"
WT"vmcnt(26)\n"
MU"v42,s55,v42\n"
MU"v43,s55,v43\n"
MF"v[96:99],a[160:163],a[24:27],v[96:99]\n"
WT"vmcnt(25)\n"
RT"a[182:183],v237 offset:61952\n"
PK"v32,v32,v33\n"
MF"v[104:107],a[160:163],a[28:31],v[104:107]\n"
WT"lgkmcnt(3)\n"
BL"a[100:103],v235,s[8:11],s57 offen offset:2048 sc1\n"
MF"v[84:87],a[164:167],a[16:19],v[84:87]\n"
SA"m0,s38,122880\n"
RT"a[184:185],v228 offset:61440\n"
PK"v33,v34,v35\n"
MF"v[92:95],a[164:167],a[20:23],v[92:95]\n"
SA"s48,s60,s61\n"
PK"v34,v40,v41\n"
PK"v35,v42,v43\n"
MF"v[100:103],a[164:167],a[24:27],v[100:103]\n"
"s_mov_b32 s88,4096\n"
BL"v238,s[20:23],s48 offen offset:0 lds\n"
MF"v[108:111],a[164:167],a[28:31],v[108:111]\n"
RT"a[186:187],v228 offset:61952\n"
MU"v48,s55,v48\n"
MF"v[112:115],a[168:171],a[16:19],v[112:115]\n"
RT"a[188:189],v229 offset:61440\n"
MU"v49,s55,v49\n"
MF"v[120:123],a[168:171],a[20:23],v[120:123]\n"
MU"v50,s55,v50\n"
MU"v51,s55,v51\n"
MF"v[128:131],a[168:171],a[24:27],v[128:131]\n"
RT"a[190:191],v229 offset:61952\n"
MU"v56,s55,v56\n"
MF"v[136:139],a[168:171],a[28:31],v[136:139]\n"
BL"a[104:107],v235,s[8:11],s58 offen offset:2048 sc1\n"
MF"v[116:119],a[172:175],a[16:19],v[116:119]\n"
RT"a[128:129],v236 offset:32768\n"
MU"v57,s55,v57\n"
MF"v[124:127],a[172:175],a[20:23],v[124:127]\n"
BL"v239,s[20:23],s48 offen offset:1024 lds\n"
MF"v[132:135],a[172:175],a[24:27],v[132:135]\n"
RT"a[130:131],v236 offset:33280\n"
MU"v58,s55,v58\n"
MF"v[140:143],a[172:175],a[28:31],v[140:143]\n"
WT"lgkmcnt(3)\n"
RT"a[132:133],v237 offset:32768\n"
MU"v59,s55,v59\n"
MF"v[144:147],a[176:179],a[16:19],v[144:147]\n"
PK"v48,v48,v49\n"
PK"v49,v50,v51\n"
MF"v[152:155],a[176:179],a[20:23],v[152:155]\n"
RT"a[134:135],v237 offset:33280\n"
PK"v50,v56,v57\n"
MF"v[160:163],a[176:179],a[24:27],v[160:163]\n"
BL"a[108:111],v235,s[8:11],s59 offen offset:2048 sc1\n"
MF"v[168:171],a[176:179],a[28:31],v[168:171]\n"
RT"a[136:137],v228 offset:32768\n"
PK"v51,v58,v59\n"
MF"v[148:151],a[180:183],a[16:19],v[148:151]\n"
BL"v240,s[20:23],s48 offen offset:2048 lds\n"
MF"v[156:159],a[180:183],a[20:23],v[156:159]\n"
RT"a[138:139],v228 offset:33280\n"
MF"v[164:167],a[180:183],a[24:27],v[164:167]\n"
MF"v[172:175],a[180:183],a[28:31],v[172:175]\n"
RT"a[140:141],v229 offset:32768\n"
MF"v[176:179],a[184:187],a[16:19],v[176:179]\n"
BL"a[112:115],v235,s[8:11],s56 offen offset:3072 sc1\n"
MF"v[184:187],a[184:187],a[20:23],v[184:187]\n"
RT"a[142:143],v229 offset:33280\n"
MF"v[192:195],a[184:187],a[24:27],v[192:195]\n"
MF"v[200:203],a[184:187],a[28:31],v[200:203]\n"
WT"lgkmcnt(3)\n"
RT"a[144:145],v236 offset:36864\n"
MF"v[180:183],a[188:191],a[16:19],v[180:183]\n"
SA"s61,s61,524288\n"
BS"v[12:15],v250,s[8:11],s49 offen offset:1024 sc1\n"
MF"v[188:191],a[188:191],a[20:23],v[188:191]\n"
"s_cmp_eq_u32 s61,5242880\n"
BL"v241,s[20:23],s48 offen offset:3072 lds\n"
MF"v[196:199],a[188:191],a[24:27],v[196:199]\n"
"s_cselect_b32 s61,0,s61\n"
RT"a[146:147],v236 offset:37376\n"
MF"v[204:207],a[188:191],a[28:31],v[204:207]\n"
WT"vmcnt(32)\n"
RT"a[148:149],v237 offset:36864\n"
MF"v[80:83],a[128:131],a[32:35],v[80:83]\n"
WT"vmcnt(31)\n"
BL"a[116:119],v235,s[8:11],s57 offen offset:3072 sc1\n"
MF"v[88:91],a[128:131],a[36:39],v[88:91]\n"
WT"vmcnt(31)\n"
RT"a[150:151],v237 offset:37376\n"
MF"v[96:99],a[128:131],a[40:43],v[96:99]\n"
WT"vmcnt(30)\n"
RT"a[152:153],v228 offset:36864\n"
MF"v[104:107],a[128:131],a[44:47],v[104:107]\n"
MF"v[84:87],a[132:135],a[32:35],v[84:87]\n"
RT"a[154:155],v228 offset:37376\n"
MF"v[92:95],a[132:135],a[36:39],v[92:95]\n"
MF"v[100:103],a[132:135],a[40:43],v[100:103]\n"
RT"a[156:157],v229 offset:36864\n"
MF"v[108:111],a[132:135],a[44:47],v[108:111]\n"
WT"lgkmcnt(4)\n"
BL"a[120:123],v235,s[8:11],s58 offen offset:3072 sc1\n"
MF"v[112:115],a[136:139],a[32:35],v[112:115]\n"
RT"a[158:159],v229 offset:37376\n"
MF"v[120:123],a[136:139],a[36:39],v[120:123]\n"
MF"v[128:131],a[136:139],a[40:43],v[128:131]\n"
RT"a[160:161],v236 offset:40960\n"
MF"v[136:139],a[136:139],a[44:47],v[136:139]\n"
MF"v[116:119],a[140:143],a[32:35],v[116:119]\n"
SA"s99,s99,4096\n"
RT"a[162:163],v236 offset:41472\n"
MF"v[124:127],a[140:143],a[36:39],v[124:127]\n"
"s_cmp_eq_u32 s99,20480\n"
BL"a[124:127],v235,s[8:11],s59 offen offset:3072 sc1\n"
MF"v[132:135],a[140:143],a[40:43],v[132:135]\n"
"s_cselect_b32 s99,0,s99\n"
RT"a[164:165],v237 offset:40960\n"
MF"v[140:143],a[140:143],a[44:47],v[140:143]\n"
MF"v[144:147],a[144:147],a[32:35],v[144:147]\n"
RT"a[166:167],v237 offset:41472\n"
MF"v[152:155],a[144:147],a[36:39],v[152:155]\n"
MF"v[160:163],a[144:147],a[40:43],v[160:163]\n"
RT"a[168:169],v228 offset:40960\n"
MF"v[168:171],a[144:147],a[44:47],v[168:171]\n"
WT"lgkmcnt(3)\n"
"v_add_u32 v235,s99,v224\n"
MF"v[148:151],a[148:151],a[32:35],v[148:151]\n"
RT"a[170:171],v228 offset:41472\n"
MF"v[156:159],a[148:151],a[36:39],v[156:159]\n"
BL"a[0:3],v235,s[8:11],s56 offen offset:0 sc1\n"
MF"v[164:167],a[148:151],a[40:43],v[164:167]\n"
RT"a[172:173],v229 offset:40960\n"
MF"v[172:175],a[148:151],a[44:47],v[172:175]\n"
RT"a[174:175],v229 offset:41472\n"
MF"v[176:179],a[152:155],a[32:35],v[176:179]\n"
MF"v[184:187],a[152:155],a[36:39],v[184:187]\n"
RT"a[176:177],v236 offset:45056\n"
MF"v[192:195],a[152:155],a[40:43],v[192:195]\n"
MF"v[200:203],a[152:155],a[44:47],v[200:203]\n"
RT"a[178:179],v236 offset:45568\n"
MF"v[180:183],a[156:159],a[32:35],v[180:183]\n"
"s_mov_b32 s49,266240\n"
BL"a[4:7],v235,s[8:11],s57 offen offset:0 sc1\n"
MF"v[188:191],a[156:159],a[36:39],v[188:191]\n"
RT"a[180:181],v237 offset:45056\n"
MF"v[196:199],a[156:159],a[40:43],v[196:199]\n"
BS"v[20:23],v250,s[8:11],s49 offen offset:0 sc1\n"
MF"v[204:207],a[156:159],a[44:47],v[204:207]\n"
RT"a[182:183],v237 offset:45568\n"
"s_branch LQ_%=\n"
"LV_%=:\n"
WT"vmcnt(26)\n"
"v_lshlrev_b32 v148,16,v128\n"
"v_and_b32 v149,0xffff0000,v128\n"
"v_lshlrev_b32 v150,16,v129\n"
"v_and_b32 v151,0xffff0000,v129\n"
WT"vmcnt(25)\n"
"v_lshlrev_b32 v152,16,v130\n"
"v_and_b32 v153,0xffff0000,v130\n"
"v_lshlrev_b32 v154,16,v131\n"
"v_and_b32 v155,0xffff0000,v131\n"
WT"vmcnt(24)\n"
"v_lshlrev_b32 v156,16,v132\n"
"v_and_b32 v157,0xffff0000,v132\n"
"v_lshlrev_b32 v158,16,v133\n"
"v_and_b32 v159,0xffff0000,v133\n"
WT"vmcnt(23)\n"
"v_lshlrev_b32 v160,16,v134\n"
"v_and_b32 v161,0xffff0000,v134\n"
"v_lshlrev_b32 v162,16,v135\n"
"v_and_b32 v163,0xffff0000,v135\n"
WT"vmcnt(22)\n"
"v_lshlrev_b32 v164,16,v136\n"
"v_and_b32 v165,0xffff0000,v136\n"
"v_lshlrev_b32 v166,16,v137\n"
"v_and_b32 v167,0xffff0000,v137\n"
"v_add_f32 v188,v148,v149\n"
"v_add_f32 v189,v150,v151\n"
"v_add_f32 v188,v188,v152\n"
"v_add_f32 v189,v189,v153\n"
"v_add_f32 v188,v188,v154\n"
"v_add_f32 v189,v189,v155\n"
"v_add_f32 v188,v188,v156\n"
"v_add_f32 v189,v189,v157\n"
"v_add_f32 v188,v188,v158\n"
"v_add_f32 v189,v189,v159\n"
"v_add_f32 v188,v188,v160\n"
"v_add_f32 v189,v189,v161\n"
"v_add_f32 v188,v188,v162\n"
"v_add_f32 v189,v189,v163\n"
"v_add_f32 v188,v188,v164\n"
"v_add_f32 v189,v189,v165\n"
"v_add_f32 v188,v188,v166\n"
"v_add_f32 v189,v189,v167\n"
"v_add_f32 v188,v188,v189\n"
"s_nop 1\n"
"v_add_f32_dpp v188,v188,v188 quad_perm:[1,0,3,2] row_mask:0xf bank_mask:0xf\n"
WT"vmcnt(21)\n"
"v_lshlrev_b32 v168,16,v138\n"
"v_and_b32 v169,0xffff0000,v138\n"
"v_add_f32_dpp v188,v188,v188 quad_perm:[2,3,0,1] row_mask:0xf bank_mask:0xf\n"
"v_lshlrev_b32 v170,16,v139\n"
"v_and_b32 v171,0xffff0000,v139\n"
"v_add_f32_dpp v188,v188,v188 row_half_mirror row_mask:0xf bank_mask:0xf\n"
WT"vmcnt(20)\n"
"v_lshlrev_b32 v172,16,v140\n"
"v_and_b32 v173,0xffff0000,v140\n"
"v_add_f32_dpp v188,v188,v188 row_mirror row_mask:0xf bank_mask:0xf\n"
"v_lshlrev_b32 v174,16,v141\n"
"v_and_b32 v175,0xffff0000,v141\n"
"v_mov_b32 v189,v188\n"
WT"vmcnt(19)\n"
"v_lshlrev_b32 v176,16,v142\n"
"v_and_b32 v177,0xffff0000,v142\n"
"v_permlane16_swap_b32 v189,v188\n"
"v_lshlrev_b32 v178,16,v143\n"
"v_and_b32 v179,0xffff0000,v143\n"
"v_add_f32 v188,v188,v189\n"
WT"vmcnt(18)\n"
"v_lshlrev_b32 v180,16,v144\n"
"v_and_b32 v181,0xffff0000,v144\n"
MU"v191,0x3acccccd,v188\n"
"v_lshlrev_b32 v182,16,v145\n"
"v_and_b32 v183,0xffff0000,v145\n"
PK"v128,v191,v191\n"
WT"vmcnt(17)\n"
"v_lshlrev_b32 v184,16,v146\n"
"v_and_b32 v185,0xffff0000,v146\n"
"v_lshlrev_b32 v128,16,v128\n"
"v_lshlrev_b32 v186,16,v147\n"
"v_and_b32 v187,0xffff0000,v147\n"
"v_sub_f32 v148,v148,v128\n"
"v_sub_f32 v149,v149,v128\n"
"v_sub_f32 v150,v150,v128\n"
"v_sub_f32 v151,v151,v128\n"
"v_sub_f32 v152,v152,v128\n"
"v_sub_f32 v153,v153,v128\n"
"v_sub_f32 v154,v154,v128\n"
"v_sub_f32 v155,v155,v128\n"
"v_sub_f32 v156,v156,v128\n"
"v_sub_f32 v157,v157,v128\n"
SA"m0,s36,16384\n"
SA"s48,s46,128\n"
"s_and_b32 s48,s48,1023\n"
BL"v234,s[12:15],s48 offen offset:0 lds\n"
BL"v235,s[12:15],s48 offen offset:1024 lds\n"
BL"v236,s[12:15],s48 offen offset:2048 lds\n"
BL"v237,s[12:15],s48 offen offset:3072 lds\n"
"v_sub_f32 v158,v158,v128\n"
"v_sub_f32 v159,v159,v128\n"
"v_sub_f32 v160,v160,v128\n"
"v_sub_f32 v161,v161,v128\n"
"v_sub_f32 v162,v162,v128\n"
"v_sub_f32 v163,v163,v128\n"
"v_sub_f32 v164,v164,v128\n"
"v_sub_f32 v165,v165,v128\n"
"v_sub_f32 v166,v166,v128\n"
"v_sub_f32 v167,v167,v128\n"
MU"v188,v148,v148\n"
MU"v189,v149,v149\n"
"v_fmac_f32 v188,v150,v150\n"
"v_fmac_f32 v189,v151,v151\n"
"v_fmac_f32 v188,v152,v152\n"
"v_fmac_f32 v189,v153,v153\n"
"v_fmac_f32 v188,v154,v154\n"
"v_fmac_f32 v189,v155,v155\n"
"v_fmac_f32 v188,v156,v156\n"
"v_fmac_f32 v189,v157,v157\n"
"v_fmac_f32 v188,v158,v158\n"
"v_fmac_f32 v189,v159,v159\n"
"v_fmac_f32 v188,v160,v160\n"
"v_fmac_f32 v189,v161,v161\n"
"v_fmac_f32 v188,v162,v162\n"
"v_fmac_f32 v189,v163,v163\n"
"v_fmac_f32 v188,v164,v164\n"
"v_fmac_f32 v189,v165,v165\n"
"v_fmac_f32 v188,v166,v166\n"
"v_fmac_f32 v189,v167,v167\n"
"v_add_f32 v188,v188,v189\n"
PK"v149,v148,v149\n"
"v_lshlrev_b32 v148,16,v149\n"
"v_add_f32_dpp v188,v188,v188 quad_perm:[1,0,3,2] row_mask:0xf bank_mask:0xf\n"
"v_and_b32 v149,0xffff0000,v149\n"
PK"v151,v150,v151\n"
"v_add_f32_dpp v188,v188,v188 quad_perm:[2,3,0,1] row_mask:0xf bank_mask:0xf\n"
"v_lshlrev_b32 v150,16,v151\n"
"v_and_b32 v151,0xffff0000,v151\n"
"v_add_f32_dpp v188,v188,v188 row_half_mirror row_mask:0xf bank_mask:0xf\n"
PK"v153,v152,v153\n"
"v_lshlrev_b32 v152,16,v153\n"
"v_add_f32_dpp v188,v188,v188 row_mirror row_mask:0xf bank_mask:0xf\n"
"v_and_b32 v153,0xffff0000,v153\n"
PK"v155,v154,v155\n"
"v_mov_b32 v189,v188\n"
"v_lshlrev_b32 v154,16,v155\n"
"v_and_b32 v155,0xffff0000,v155\n"
"v_permlane16_swap_b32 v189,v188\n"
PK"v157,v156,v157\n"
"v_lshlrev_b32 v156,16,v157\n"
"v_add_f32 v188,v188,v189\n"
"v_and_b32 v157,0xffff0000,v157\n"
PK"v159,v158,v159\n"
"v_sub_f32 v191,v191,v128\n"
"v_lshlrev_b32 v158,16,v159\n"
"v_and_b32 v159,0xffff0000,v159\n"
MU"v188,0x3acccccd,v188\n"
PK"v161,v160,v161\n"
"v_lshlrev_b32 v160,16,v161\n"
"v_fma_f32 v188,-v191,v191,v188\n"
"v_and_b32 v161,0xffff0000,v161\n"
PK"v163,v162,v163\n"
"v_max_f32 v188,0,v188\n"
"v_lshlrev_b32 v162,16,v163\n"
"v_and_b32 v163,0xffff0000,v163\n"
PK"v188,v188,v188\n"
PK"v165,v164,v165\n"
"v_lshlrev_b32 v164,16,v165\n"
"v_lshlrev_b32 v188,16,v188\n"
"v_and_b32 v165,0xffff0000,v165\n"
PK"v167,v166,v167\n"
"v_add_f32 v188,0x3727c5ac,v188\n"
"v_lshlrev_b32 v166,16,v167\n"
"v_and_b32 v167,0xffff0000,v167\n"
PK"v188,v188,v188\n"
"v_bfe_u32 v130,v224,5,4\n"
"v_mul_u32_u24 v130,144,v130\n"
"v_lshlrev_b32 v188,16,v188\n"
SA"m0,s37,12288\n"
SA"s48,s47,786432\n"
"s_and_b32 s48,s48,4194303\n"
BL"v238,s[16:19],s48 offen offset:0 lds\n"
BL"v239,s[16:19],s48 offen offset:1024 lds\n"
BL"v240,s[16:19],s48 offen offset:2048 lds\n"
BL"v241,s[16:19],s48 offen offset:3072 lds\n"
"v_bfe_u32 v131,v224,4,1\n"
"v_lshl_add_u32 v130,v131,3,v130\n"
"v_sqrt_f32 v189,v188\n"
"v_lshrrev_b32 v131,9,v224\n"
"v_lshl_add_u32 v130,v131,4,v130\n"
"v_rcp_f32 v190,v189\n"
"s_lshl_b32 s52,s30,5\n"
"v_add_u32 v130,s52,v130\n"
"v_fma_f32 v188,-v189,v189,v188\n"
MU"v190,0.5,v190\n"
"v_fma_f32 v189,v188,v190,v189\n"
PK"v189,v189,v189\n"
"v_lshlrev_b32 v189,16,v189\n"
"v_rcp_f32 v129,v189\n"
"s_nop 0\n"
MU"v148,v148,v129\n"
MU"v149,v149,v129\n"
PK"v138,v148,v149\n"
MU"v150,v150,v129\n"
MU"v151,v151,v129\n"
PK"v139,v150,v151\n"
"ds_write_b64 v130,v[138:139] offset:49152\n"
MU"v152,v152,v129\n"
MU"v153,v153,v129\n"
PK"v140,v152,v153\n"
MU"v154,v154,v129\n"
MU"v155,v155,v129\n"
PK"v141,v154,v155\n"
"ds_write_b64 v130,v[140:141] offset:51456\n"
MU"v156,v156,v129\n"
MU"v157,v157,v129\n"
PK"v142,v156,v157\n"
MU"v158,v158,v129\n"
MU"v159,v159,v129\n"
PK"v143,v158,v159\n"
"ds_write_b64 v130,v[142:143] offset:53760\n"
MU"v160,v160,v129\n"
MU"v161,v161,v129\n"
PK"v144,v160,v161\n"
MU"v162,v162,v129\n"
MU"v163,v163,v129\n"
PK"v145,v162,v163\n"
"ds_write_b64 v130,v[144:145] offset:56064\n"
MU"v164,v164,v129\n"
MU"v165,v165,v129\n"
PK"v146,v164,v165\n"
MU"v166,v166,v129\n"
MU"v167,v167,v129\n"
PK"v147,v166,v167\n"
"ds_write_b64 v130,v[146:147] offset:58368\n"
"v_lshlrev_b32 v148,16,v138\n"
"v_and_b32 v149,0xffff0000,v138\n"
MU"v188,v148,v168\n"
MU"v189,v149,v169\n"
"v_lshlrev_b32 v150,16,v139\n"
"v_and_b32 v151,0xffff0000,v139\n"
"v_fmac_f32 v188,v150,v170\n"
"v_fmac_f32 v189,v151,v171\n"
"v_lshlrev_b32 v152,16,v140\n"
"v_and_b32 v153,0xffff0000,v140\n"
"v_fmac_f32 v188,v152,v172\n"
"v_fmac_f32 v189,v153,v173\n"
"v_lshlrev_b32 v154,16,v141\n"
"v_and_b32 v155,0xffff0000,v141\n"
"v_fmac_f32 v188,v154,v174\n"
"v_fmac_f32 v189,v155,v175\n"
"v_lshlrev_b32 v156,16,v142\n"
"v_and_b32 v157,0xffff0000,v142\n"
"v_fmac_f32 v188,v156,v176\n"
"v_fmac_f32 v189,v157,v177\n"
"v_lshlrev_b32 v158,16,v143\n"
"v_and_b32 v159,0xffff0000,v143\n"
"v_fmac_f32 v188,v158,v178\n"
"v_fmac_f32 v189,v159,v179\n"
"v_lshlrev_b32 v160,16,v144\n"
"v_and_b32 v161,0xffff0000,v144\n"
"v_fmac_f32 v188,v160,v180\n"
"v_fmac_f32 v189,v161,v181\n"
"v_lshlrev_b32 v162,16,v145\n"
"v_and_b32 v163,0xffff0000,v145\n"
WT"vmcnt(21)\n"
RT"a[0:1],v246 offset:0\n"
RT"a[2:3],v246 offset:512\n"
RT"a[4:5],v247 offset:0\n"
RT"a[6:7],v247 offset:512\n"
RT"a[8:9],v248 offset:0\n"
RT"a[10:11],v248 offset:512\n"
RT"a[12:13],v249 offset:0\n"
RT"a[14:15],v249 offset:512\n"
WT"vmcnt(12)\n"
RT"a[16:17],v246 offset:4096\n"
RT"a[18:19],v246 offset:4608\n"
WT"lgkmcnt(10)\n"
"s_barrier\n"
RD"v[192:195],v242 offset:0\n"
RD"v[196:199],v242 offset:2048\n"
RD"v[200:203],v242 offset:4096\n"
RD"v[204:207],v242 offset:6144\n"
RD"v[208:211],v242 offset:8192\n"
"v_lshrrev_b32 v131,7,v224\n"
WT"lgkmcnt(4)\n"
MF"v[0:3],v[192:195],a[0:3],0\n"
SA"m0,s37,16384\n"
RD"v[212:215],v242 offset:10240\n"
"v_lshl_add_u32 v131,v131,4,v224\n"
MF"v[8:11],v[192:195],a[4:7],0\n"
SA"s48,s47,1048576\n"
"s_and_b32 s48,s48,4194303\n"
"s_mul_i32 s52,s30,1152\n"
MF"v[16:19],v[192:195],a[8:11],0\n"
RT"a[20:21],v247 offset:4096\n"
"v_add_u32 v128,s52,v131\n"
MF"v[24:27],v[192:195],a[12:15],0\n"
WT"lgkmcnt(5)\n"
BL"v238,s[16:19],s48 offen offset:0 lds\n"
MF"v[4:7],v[196:199],a[0:3],0\n"
"s_and_b32 s52,s30,1\n"
RD"v[216:219],v242 offset:12288\n"
"v_fmac_f32 v188,v162,v182\n"
MF"v[12:15],v[196:199],a[4:7],0\n"
"s_mul_i32 s51,s52,1152\n"
"v_fmac_f32 v189,v163,v183\n"
"v_add_u32 v131,s51,v131\n"
MF"v[20:23],v[196:199],a[8:11],0\n"
"s_lshl_b32 s52,s52,10\n"
RT"a[22:23],v247 offset:4608\n"
"v_lshlrev_b32 v164,16,v146\n"
MF"v[28:31],v[196:199],a[12:15],0\n"
WT"lgkmcnt(3)\n"
BL"v239,s[16:19],s48 offen offset:1024 lds\n"
MF"v[32:35],v[200:203],a[0:3],0\n"
RD"v[220:223],v242 offset:14336\n"
"v_and_b32 v129,0x70,v224\n"
MF"v[40:43],v[200:203],a[4:7],0\n"
"s_lshr_b32 s50,s31,1\n"
RD"v[132:135],v128 offset:49152\n"
"v_and_b32 v165,0xffff0000,v146\n"
MF"v[48:51],v[200:203],a[8:11],0\n"
"s_mul_i32 s50,s50,20480\n"
RD"a[240:243],v128 offset:53760\n"
"v_and_b32 v130,0x380,v224\n"
MF"v[56:59],v[200:203],a[12:15],0\n"
"s_and_b32 s51,s31,1\n"
RD"a[244:247],v131 offset:58368\n"
"v_fmac_f32 v188,v164,v184\n"
MF"v[36:39],v[204:207],a[0:3],0\n"
"s_lshl_b32 s51,s51,7\n"
RD"v[192:195],v243 offset:0\n"
"v_lshl_add_u32 v129,v130,1,v129\n"
MF"v[44:47],v[204:207],a[4:7],0\n"
SA"s50,s50,s51\n"
RT"a[24:25],v248 offset:4096\n"
"v_fmac_f32 v189,v165,v185\n"
MF"v[52:55],v[204:207],a[8:11],0\n"
SA"s50,s50,65536\n"
BL"v240,s[16:19],s48 offen offset:2048 lds\n"
MF"v[60:63],v[204:207],a[12:15],0\n"
"s_lshl_b32 s52,s52,1\n"
RD"v[196:199],v243 offset:2048\n"
"v_lshlrev_b32 v166,16,v147\n"
MF"v[64:67],v[208:211],a[0:3],0\n"
SA"s52,s52,s50\n"
RT"a[26:27],v248 offset:4608\n"
"v_and_b32 v167,0xffff0000,v147\n"
MF"v[72:75],v[208:211],a[4:7],0\n"
SA"s52,s52,16384\n"
BL"v241,s[16:19],s48 offen offset:3072 lds\n"
MF"v[80:83],v[208:211],a[8:11],0\n"
SA"m0,s36,32768\n"
RD"v[200:203],v243 offset:4096\n"
"v_fmac_f32 v188,v166,v186\n"
MF"v[88:91],v[208:211],a[12:15],0\n"
SA"s48,s46,256\n"
RT"a[28:29],v249 offset:4096\n"
"v_fmac_f32 v189,v167,v187\n"
MF"v[68:71],v[212:215],a[0:3],0\n"
"s_and_b32 s48,s48,1023\n"
"v_add_f32 v188,v188,v189\n"
"s_lshl_b32 s51,s30,11\n"
MF"v[76:79],v[212:215],a[4:7],0\n"
SA"s50,s50,s51\n"
BL"v234,s[12:15],s48 offen offset:0 lds\n"
MF"v[84:87],v[212:215],a[8:11],0\n"
SA"s51,s50,8192\n"
RD"v[204:207],v243 offset:6144\n"
"v_add_f32_dpp v188,v188,v188 quad_perm:[1,0,3,2] row_mask:0xf bank_mask:0xf\n"
MF"v[92:95],v[212:215],a[12:15],0\n"
WT"lgkmcnt(3)\n"
RT"a[30:31],v249 offset:4608\n"
"v_add_f32_dpp v188,v188,v188 quad_perm:[2,3,0,1] row_mask:0xf bank_mask:0xf\n"
MF"v[96:99],v[216:219],a[0:3],0\n"
BS"v[132:135],v129,s[8:11],s50 offen sc1\n"
MF"v[104:107],v[216:219],a[4:7],0\n"
"s_lshl_b32 s90,s31,2\n"
RD"v[208:211],v243 offset:8192\n"
"v_add_f32_dpp v188,v188,v188 row_half_mirror row_mask:0xf bank_mask:0xf\n"
MF"v[112:115],v[216:219],a[8:11],0\n"
SA"s90,s90,s30\n"
BL"v235,s[12:15],s48 offen offset:1024 lds\n"
MF"v[120:123],v[216:219],a[12:15],0\n"
"s_lshl_b32 s90,s90,1\n"
BS"a[240:243],v129,s[8:11],s51 offen sc1\n"
MF"v[100:103],v[220:223],a[0:3],0\n"
"s_sub_u32 s91,2047,s90\n"
WT"vmcnt(16)\n"
RT"a[32:33],v246 offset:8192\n"
MF"v[108:111],v[220:223],a[4:7],0\n"
BS"a[244:247],v129,s[8:11],s52 offen sc1\n"
MF"v[116:119],v[220:223],a[8:11],0\n"
RD"v[212:215],v243 offset:10240\n"
"v_add_f32_dpp v188,v188,v188 row_mirror row_mask:0xf bank_mask:0xf\n"
MF"v[124:127],v[220:223],a[12:15],0\n"
BL"v236,s[12:15],s48 offen offset:2048 lds\n"
MF"v[0:3],v[192:195],a[16:19],v[0:3]\n"
RT"a[34:35],v246 offset:8704\n"
"v_mov_b32 v189,v188\n"
MF"v[8:11],v[192:195],a[20:23],v[8:11]\n"
RD"v[216:219],v243 offset:12288\n"
"v_permlane16_swap_b32 v189,v188\n"
MF"v[16:19],v[192:195],a[24:27],v[16:19]\n"
WT"lgkmcnt(4)\n"
BL"v237,s[12:15],s48 offen offset:3072 lds\n"
MF"v[24:27],v[192:195],a[28:31],v[24:27]\n"
"s_lshl_b32 s90,s90,2\n"
RT"a[36:37],v247 offset:8192\n"
"v_add_f32 v188,v188,v189\n"
MF"v[4:7],v[196:199],a[16:19],v[4:7]\n"
SA"s90,s90,2686976\n"
RD"v[220:223],v243 offset:14336\n"
PK"v188,v188,v188\n"
MF"v[12:15],v[196:199],a[20:23],v[12:15]\n"
SA"m0,s37,20480\n"
"v_lshlrev_b32 v188,16,v188\n"
"v_ashrrev_i32 v189,31,v188\n"
MF"v[20:23],v[196:199],a[24:27],v[20:23]\n"
SA"s48,s47,1310720\n"
RT"a[38:39],v247 offset:8704\n"
"v_or_b32 v189,0x80000000,v189\n"
MF"v[28:31],v[196:199],a[28:31],v[28:31]\n"
"s_and_b32 s48,s48,4194303\n"
"v_xor_b32 v188,v188,v189\n"
"v_and_b32 v188,0xffff0000,v188\n"
MF"v[32:35],v[200:203],a[16:19],v[32:35]\n"
BL"v238,s[16:19],s48 offen offset:0 lds\n"
MF"v[40:43],v[200:203],a[20:23],v[40:43]\n"
"v_lshrrev_b32 v189,9,v224\n"
"v_sub_u32 v190,s91,v189\n"
MF"v[48:51],v[200:203],a[24:27],v[48:51]\n"
RT"a[40:41],v248 offset:8192\n"
"v_or_b32 v188,v188,v190\n"
MF"v[56:59],v[200:203],a[28:31],v[56:59]\n"
BL"v239,s[16:19],s48 offen offset:1024 lds\n"
MF"v[36:39],v[204:207],a[16:19],v[36:39]\n"
"v_and_b32 v190,0x1f0,v224\n"
"v_lshlrev_b32 v190,23,v190\n"
MF"v[44:47],v[204:207],a[20:23],v[44:47]\n"
WT"vmcnt(17) lgkmcnt(2)\n"
"s_barrier\n"
MF"v[52:55],v[204:207],a[24:27],v[52:55]\n"
RD"v[192:195],v242 offset:16384\n"
"v_lshl_add_u32 v190,v189,2,v190\n"
MF"v[60:63],v[204:207],a[28:31],v[60:63]\n"
"buffer_store_dword v188,v190,s[8:11],s90 offen sc1\n"
MF"v[64:67],v[208:211],a[16:19],v[64:67]\n"
RT"a[42:43],v248 offset:8704\n"
MF"v[72:75],v[208:211],a[20:23],v[72:75]\n"
BL"v240,s[16:19],s48 offen offset:2048 lds\n"
MF"v[80:83],v[208:211],a[24:27],v[80:83]\n"
RD"v[196:199],v242 offset:18432\n"
MF"v[88:91],v[208:211],a[28:31],v[88:91]\n"
RT"a[44:45],v249 offset:8192\n"
MF"v[68:71],v[212:215],a[16:19],v[68:71]\n"
BL"v241,s[16:19],s48 offen offset:3072 lds\n"
MF"v[76:79],v[212:215],a[20:23],v[76:79]\n"
RD"v[200:203],v242 offset:20480\n"
MF"v[84:87],v[212:215],a[24:27],v[84:87]\n"
SA"m0,s37,0\n"
RT"a[46:47],v249 offset:8704\n"
SA"s48,s47,1572864\n"
MF"v[92:95],v[212:215],a[28:31],v[92:95]\n"
"s_and_b32 s48,s48,4194303\n"
MF"v[96:99],v[216:219],a[16:19],v[96:99]\n"
BL"v238,s[16:19],s48 offen offset:0 lds\n"
MF"v[104:107],v[216:219],a[20:23],v[104:107]\n"
RD"v[204:207],v242 offset:22528\n"
MF"v[112:115],v[216:219],a[24:27],v[112:115]\n"
WT"vmcnt(17)\n"
RT"a[48:49],v246 offset:12288\n"
MF"v[120:123],v[216:219],a[28:31],v[120:123]\n"
MF"v[100:103],v[220:223],a[16:19],v[100:103]\n"
RD"v[208:211],v242 offset:24576\n"
MF"v[108:111],v[220:223],a[20:23],v[108:111]\n"
BL"v239,s[16:19],s48 offen offset:1024 lds\n"
MF"v[116:119],v[220:223],a[24:27],v[116:119]\n"
RT"a[50:51],v246 offset:12800\n"
MF"v[124:127],v[220:223],a[28:31],v[124:127]\n"
WT"lgkmcnt(4)\n"
MF"v[0:3],v[192:195],a[32:35],v[0:3]\n"
RD"v[212:215],v242 offset:26624\n"
MF"v[8:11],v[192:195],a[36:39],v[8:11]\n"
BL"v240,s[16:19],s48 offen offset:2048 lds\n"
MF"v[16:19],v[192:195],a[40:43],v[16:19]\n"
RT"a[52:53],v247 offset:12288\n"
MF"v[24:27],v[192:195],a[44:47],v[24:27]\n"
RD"v[216:219],v242 offset:28672\n"
MF"v[4:7],v[196:199],a[32:35],v[4:7]\n"
MF"v[12:15],v[196:199],a[36:39],v[12:15]\n"
BL"v241,s[16:19],s48 offen offset:3072 lds\n"
MF"v[20:23],v[196:199],a[40:43],v[20:23]\n"
RT"a[54:55],v247 offset:12800\n"
MF"v[28:31],v[196:199],a[44:47],v[28:31]\n"
RD"v[220:223],v242 offset:30720\n"
MF"v[32:35],v[200:203],a[32:35],v[32:35]\n"
SA"m0,s36,0\n"
SA"s48,s46,384\n"
"s_and_b32 s48,s48,1023\n"
MF"v[40:43],v[200:203],a[36:39],v[40:43]\n"
MF"v[48:51],v[200:203],a[40:43],v[48:51]\n"
RD"v[192:195],v243 offset:16384\n"
MF"v[56:59],v[200:203],a[44:47],v[56:59]\n"
WT"lgkmcnt(3)\n"
RT"a[56:57],v248 offset:12288\n"
MF"v[36:39],v[204:207],a[32:35],v[36:39]\n"
BL"v234,s[12:15],s48 offen offset:0 lds\n"
MF"v[44:47],v[204:207],a[36:39],v[44:47]\n"
MF"v[52:55],v[204:207],a[40:43],v[52:55]\n"
RD"v[196:199],v243 offset:18432\n"
MF"v[60:63],v[204:207],a[44:47],v[60:63]\n"
RT"a[58:59],v248 offset:12800\n"
MF"v[64:67],v[208:211],a[32:35],v[64:67]\n"
BL"v235,s[12:15],s48 offen offset:1024 lds\n"
MF"v[72:75],v[208:211],a[36:39],v[72:75]\n"
RD"v[200:203],v243 offset:20480\n"
MF"v[80:83],v[208:211],a[40:43],v[80:83]\n"
RT"a[60:61],v249 offset:12288\n"
MF"v[88:91],v[208:211],a[44:47],v[88:91]\n"
BL"v236,s[12:15],s48 offen offset:2048 lds\n"
MF"v[68:71],v[212:215],a[32:35],v[68:71]\n"
MF"v[76:79],v[212:215],a[36:39],v[76:79]\n"
RD"v[204:207],v243 offset:22528\n"
MF"v[84:87],v[212:215],a[40:43],v[84:87]\n"
RT"a[62:63],v249 offset:12800\n"
MF"v[92:95],v[212:215],a[44:47],v[92:95]\n"
BL"v237,s[12:15],s48 offen offset:3072 lds\n"
MF"v[96:99],v[216:219],a[32:35],v[96:99]\n"
RD"v[208:211],v243 offset:24576\n"
MF"v[104:107],v[216:219],a[36:39],v[104:107]\n"
MF"v[112:115],v[216:219],a[40:43],v[112:115]\n"
SA"m0,s37,4096\n"
WT"vmcnt(20)\n"
RT"a[64:65],v246 offset:16384\n"
MF"v[120:123],v[216:219],a[44:47],v[120:123]\n"
WT"lgkmcnt(4)\n"
SA"s48,s47,1835008\n"
"s_and_b32 s48,s48,4194303\n"
MF"v[100:103],v[220:223],a[32:35],v[100:103]\n"
RD"v[212:215],v243 offset:26624\n"
MF"v[108:111],v[220:223],a[36:39],v[108:111]\n"
BL"v238,s[16:19],s48 offen offset:0 lds\n"
MF"v[116:119],v[220:223],a[40:43],v[116:119]\n"
RT"a[66:67],v246 offset:16896\n"
MF"v[124:127],v[220:223],a[44:47],v[124:127]\n"
RD"v[216:219],v243 offset:28672\n"
MF"v[0:3],v[192:195],a[48:51],v[0:3]\n"
BL"v239,s[16:19],s48 offen offset:1024 lds\n"
MF"v[8:11],v[192:195],a[52:55],v[8:11]\n"
MF"v[16:19],v[192:195],a[56:59],v[16:19]\n"
WT"lgkmcnt(3)\n"
RT"a[68:69],v247 offset:16384\n"
MF"v[24:27],v[192:195],a[60:63],v[24:27]\n"
RD"v[220:223],v243 offset:30720\n"
MF"v[4:7],v[196:199],a[48:51],v[4:7]\n"
MF"v[12:15],v[196:199],a[52:55],v[12:15]\n"
BL"v240,s[16:19],s48 offen offset:2048 lds\n"
MF"v[20:23],v[196:199],a[56:59],v[20:23]\n"
RT"a[70:71],v247 offset:16896\n"
MF"v[28:31],v[196:199],a[60:63],v[28:31]\n"
MF"v[32:35],v[200:203],a[48:51],v[32:35]\n"
"s_lshl_b32 s51,s32,8\n"
"s_andn2_b32 s50,s31,7\n"
SA"s50,s50,s51\n"
MF"v[40:43],v[200:203],a[52:55],v[40:43]\n"
SA"s50,s50,256\n"
BL"v241,s[16:19],s48 offen offset:3072 lds\n"
MF"v[48:51],v[200:203],a[56:59],v[48:51]\n"
SA"s50,s50,s45\n"
RT"a[72:73],v248 offset:16384\n"
MF"v[56:59],v[200:203],a[60:63],v[56:59]\n"
MF"v[36:39],v[204:207],a[48:51],v[36:39]\n"
SA"m0,s37,8192\n"
SA"s48,s47,2097152\n"
"s_and_b32 s48,s48,4194303\n"
MF"v[44:47],v[204:207],a[52:55],v[44:47]\n"
WT"vmcnt(14) lgkmcnt(2)\n"
"s_barrier\n"
MF"v[52:55],v[204:207],a[56:59],v[52:55]\n"
RD"v[192:195],v242 offset:32768\n"
MF"v[60:63],v[204:207],a[60:63],v[60:63]\n"
RT"a[74:75],v248 offset:16896\n"
MF"v[64:67],v[208:211],a[48:51],v[64:67]\n"
BL"v238,s[16:19],s48 offen offset:0 lds\n"
MF"v[72:75],v[208:211],a[52:55],v[72:75]\n"
"buffer_store_dwordx2 v[232:233],v230,s[8:11],s50 offen sc1\n"
MF"v[80:83],v[208:211],a[56:59],v[80:83]\n"
RD"v[196:199],v242 offset:34816\n"
MF"v[88:91],v[208:211],a[60:63],v[88:91]\n"
RT"a[76:77],v249 offset:16384\n"
MF"v[68:71],v[212:215],a[48:51],v[68:71]\n"
BL"v239,s[16:19],s48 offen offset:1024 lds\n"
MF"v[76:79],v[212:215],a[52:55],v[76:79]\n"
RD"v[200:203],v242 offset:36864\n"
MF"v[84:87],v[212:215],a[56:59],v[84:87]\n"
RT"a[78:79],v249 offset:16896\n"
MF"v[92:95],v[212:215],a[60:63],v[92:95]\n"
BL"v240,s[16:19],s48 offen offset:2048 lds\n"
MF"v[96:99],v[216:219],a[48:51],v[96:99]\n"
MF"v[104:107],v[216:219],a[52:55],v[104:107]\n"
RD"v[204:207],v242 offset:38912\n"
MF"v[112:115],v[216:219],a[56:59],v[112:115]\n"
WT"vmcnt(16)\n"
RT"a[80:81],v246 offset:20480\n"
MF"v[120:123],v[216:219],a[60:63],v[120:123]\n"
BL"v241,s[16:19],s48 offen offset:3072 lds\n"
MF"v[100:103],v[220:223],a[48:51],v[100:103]\n"
RD"v[208:211],v242 offset:40960\n"
MF"v[108:111],v[220:223],a[52:55],v[108:111]\n"
MF"v[116:119],v[220:223],a[56:59],v[116:119]\n"
SA"m0,s36,16384\n"
RT"a[82:83],v246 offset:20992\n"
SA"s48,s46,512\n"
MF"v[124:127],v[220:223],a[60:63],v[124:127]\n"
WT"lgkmcnt(4)\n"
"s_and_b32 s48,s48,1023\n"
MF"v[0:3],v[192:195],a[64:67],v[0:3]\n"
RD"v[212:215],v242 offset:43008\n"
MF"v[8:11],v[192:195],a[68:71],v[8:11]\n"
BL"v234,s[12:15],s48 offen offset:0 lds\n"
MF"v[16:19],v[192:195],a[72:75],v[16:19]\n"
RT"a[84:85],v247 offset:20480\n"
MF"v[24:27],v[192:195],a[76:79],v[24:27]\n"
RD"v[216:219],v242 offset:45056\n"
MF"v[4:7],v[196:199],a[64:67],v[4:7]\n"
BL"v235,s[12:15],s48 offen offset:1024 lds\n"
MF"v[12:15],v[196:199],a[68:71],v[12:15]\n"
MF"v[20:23],v[196:199],a[72:75],v[20:23]\n"
RT"a[86:87],v247 offset:20992\n"
MF"v[28:31],v[196:199],a[76:79],v[28:31]\n"
RD"v[220:223],v242 offset:47104\n"
MF"v[32:35],v[200:203],a[64:67],v[32:35]\n"
MF"v[40:43],v[200:203],a[68:71],v[40:43]\n"
BL"v236,s[12:15],s48 offen offset:2048 lds\n"
MF"v[48:51],v[200:203],a[72:75],v[48:51]\n"
RD"v[192:195],v243 offset:32768\n"
MF"v[56:59],v[200:203],a[76:79],v[56:59]\n"
WT"lgkmcnt(3)\n"
RT"a[88:89],v248 offset:20480\n"
MF"v[36:39],v[204:207],a[64:67],v[36:39]\n"
MF"v[44:47],v[204:207],a[68:71],v[44:47]\n"
BL"v237,s[12:15],s48 offen offset:3072 lds\n"
MF"v[52:55],v[204:207],a[72:75],v[52:55]\n"
RD"v[196:199],v243 offset:34816\n"
MF"v[60:63],v[204:207],a[76:79],v[60:63]\n"
RT"a[90:91],v248 offset:20992\n"
MF"v[64:67],v[208:211],a[64:67],v[64:67]\n"
SA"m0,s37,12288\n"
SA"s48,s47,2359296\n"
"s_and_b32 s48,s48,4194303\n"
MF"v[72:75],v[208:211],a[68:71],v[72:75]\n"
RD"v[200:203],v243 offset:36864\n"
MF"v[80:83],v[208:211],a[72:75],v[80:83]\n"
RT"a[92:93],v249 offset:20480\n"
MF"v[88:91],v[208:211],a[76:79],v[88:91]\n"
BL"v238,s[16:19],s48 offen offset:0 lds\n"
MF"v[68:71],v[212:215],a[64:67],v[68:71]\n"
MF"v[76:79],v[212:215],a[68:71],v[76:79]\n"
RD"v[204:207],v243 offset:38912\n"
MF"v[84:87],v[212:215],a[72:75],v[84:87]\n"
RT"a[94:95],v249 offset:20992\n"
MF"v[92:95],v[212:215],a[76:79],v[92:95]\n"
BL"v239,s[16:19],s48 offen offset:1024 lds\n"
MF"v[96:99],v[216:219],a[64:67],v[96:99]\n"
RD"v[208:211],v243 offset:40960\n"
MF"v[104:107],v[216:219],a[68:71],v[104:107]\n"
MF"v[112:115],v[216:219],a[72:75],v[112:115]\n"
WT"vmcnt(19)\n"
RT"a[96:97],v246 offset:0\n"
MF"v[120:123],v[216:219],a[76:79],v[120:123]\n"
WT"lgkmcnt(4)\n"
BL"v240,s[16:19],s48 offen offset:2048 lds\n"
MF"v[100:103],v[220:223],a[64:67],v[100:103]\n"
RD"v[212:215],v243 offset:43008\n"
MF"v[108:111],v[220:223],a[68:71],v[108:111]\n"
MF"v[116:119],v[220:223],a[72:75],v[116:119]\n"
RT"a[98:99],v246 offset:512\n"
MF"v[124:127],v[220:223],a[76:79],v[124:127]\n"
RD"v[216:219],v243 offset:45056\n"
MF"v[0:3],v[192:195],a[80:83],v[0:3]\n"
BL"v241,s[16:19],s48 offen offset:3072 lds\n"
MF"v[8:11],v[192:195],a[84:87],v[8:11]\n"
MF"v[16:19],v[192:195],a[88:91],v[16:19]\n"
WT"lgkmcnt(3)\n"
RT"a[100:101],v247 offset:0\n"
SA"m0,s37,16384\n"
MF"v[24:27],v[192:195],a[92:95],v[24:27]\n"
SA"s48,s47,2621440\n"
RD"v[220:223],v243 offset:47104\n"
"s_and_b32 s48,s48,4194303\n"
MF"v[4:7],v[196:199],a[80:83],v[4:7]\n"
BL"v238,s[16:19],s48 offen offset:0 lds\n"
MF"v[12:15],v[196:199],a[84:87],v[12:15]\n"
MF"v[20:23],v[196:199],a[88:91],v[20:23]\n"
RT"a[102:103],v247 offset:512\n"
MF"v[28:31],v[196:199],a[92:95],v[28:31]\n"
MF"v[32:35],v[200:203],a[80:83],v[32:35]\n"
BL"v239,s[16:19],s48 offen offset:1024 lds\n"
MF"v[40:43],v[200:203],a[84:87],v[40:43]\n"
MF"v[48:51],v[200:203],a[88:91],v[48:51]\n"
RT"a[104:105],v248 offset:0\n"
MF"v[56:59],v[200:203],a[92:95],v[56:59]\n"
MF"v[36:39],v[204:207],a[80:83],v[36:39]\n"
MF"v[44:47],v[204:207],a[84:87],v[44:47]\n"
WT"vmcnt(19) lgkmcnt(2)\n"
"s_barrier\n"
MF"v[52:55],v[204:207],a[88:91],v[52:55]\n"
BL"v240,s[16:19],s48 offen offset:2048 lds\n"
MF"v[60:63],v[204:207],a[92:95],v[60:63]\n"
RD"v[192:195],v242 offset:0\n"
MF"v[64:67],v[208:211],a[80:83],v[64:67]\n"
RT"a[106:107],v248 offset:512\n"
MF"v[72:75],v[208:211],a[84:87],v[72:75]\n"
BL"v241,s[16:19],s48 offen offset:3072 lds\n"
MF"v[80:83],v[208:211],a[88:91],v[80:83]\n"
RD"v[196:199],v242 offset:2048\n"
MF"v[88:91],v[208:211],a[92:95],v[88:91]\n"
RT"a[108:109],v249 offset:0\n"
MF"v[68:71],v[212:215],a[80:83],v[68:71]\n"
SA"m0,s36,32768\n"
SA"s48,s46,640\n"
"s_and_b32 s48,s48,1023\n"
MF"v[76:79],v[212:215],a[84:87],v[76:79]\n"
RD"v[200:203],v242 offset:4096\n"
MF"v[84:87],v[212:215],a[88:91],v[84:87]\n"
RT"a[110:111],v249 offset:512\n"
MF"v[92:95],v[212:215],a[92:95],v[92:95]\n"
BL"v234,s[12:15],s48 offen offset:0 lds\n"
MF"v[96:99],v[216:219],a[80:83],v[96:99]\n"
MF"v[104:107],v[216:219],a[84:87],v[104:107]\n"
RD"v[204:207],v242 offset:6144\n"
MF"v[112:115],v[216:219],a[88:91],v[112:115]\n"
WT"vmcnt(18)\n"
RT"a[112:113],v246 offset:4096\n"
MF"v[120:123],v[216:219],a[92:95],v[120:123]\n"
BL"v235,s[12:15],s48 offen offset:1024 lds\n"
MF"v[100:103],v[220:223],a[80:83],v[100:103]\n"
RD"v[208:211],v242 offset:8192\n"
MF"v[108:111],v[220:223],a[84:87],v[108:111]\n"
MF"v[116:119],v[220:223],a[88:91],v[116:119]\n"
RT"a[114:115],v246 offset:4608\n"
MF"v[124:127],v[220:223],a[92:95],v[124:127]\n"
WT"lgkmcnt(4)\n"
BL"v236,s[12:15],s48 offen offset:2048 lds\n"
MF"v[0:3],v[192:195],a[96:99],v[0:3]\n"
RD"v[212:215],v242 offset:10240\n"
MF"v[8:11],v[192:195],a[100:103],v[8:11]\n"
MF"v[16:19],v[192:195],a[104:107],v[16:19]\n"
RT"a[116:117],v247 offset:4096\n"
MF"v[24:27],v[192:195],a[108:111],v[24:27]\n"
RD"v[216:219],v242 offset:12288\n"
MF"v[4:7],v[196:199],a[96:99],v[4:7]\n"
BL"v237,s[12:15],s48 offen offset:3072 lds\n"
MF"v[12:15],v[196:199],a[100:103],v[12:15]\n"
MF"v[20:23],v[196:199],a[104:107],v[20:23]\n"
SA"m0,s37,20480\n"
RT"a[118:119],v247 offset:4608\n"
SA"s48,s47,2883584\n"
MF"v[28:31],v[196:199],a[108:111],v[28:31]\n"
"s_and_b32 s48,s48,4194303\n"
RD"v[220:223],v242 offset:14336\n"
MF"v[32:35],v[200:203],a[96:99],v[32:35]\n"
BL"v238,s[16:19],s48 offen offset:0 lds\n"
MF"v[40:43],v[200:203],a[100:103],v[40:43]\n"
MF"v[48:51],v[200:203],a[104:107],v[48:51]\n"
RD"v[192:195],v243 offset:0\n"
MF"v[56:59],v[200:203],a[108:111],v[56:59]\n"
WT"lgkmcnt(3)\n"
RT"a[120:121],v248 offset:4096\n"
MF"v[36:39],v[204:207],a[96:99],v[36:39]\n"
BL"v239,s[16:19],s48 offen offset:1024 lds\n"
MF"v[44:47],v[204:207],a[100:103],v[44:47]\n"
MF"v[52:55],v[204:207],a[104:107],v[52:55]\n"
RD"v[196:199],v243 offset:2048\n"
MF"v[60:63],v[204:207],a[108:111],v[60:63]\n"
RT"a[122:123],v248 offset:4608\n"
MF"v[64:67],v[208:211],a[96:99],v[64:67]\n"
MF"v[72:75],v[208:211],a[100:103],v[72:75]\n"
RD"v[200:203],v243 offset:4096\n"
MF"v[80:83],v[208:211],a[104:107],v[80:83]\n"
BL"v240,s[16:19],s48 offen offset:2048 lds\n"
MF"v[88:91],v[208:211],a[108:111],v[88:91]\n"
RT"a[124:125],v249 offset:4096\n"
MF"v[68:71],v[212:215],a[96:99],v[68:71]\n"
MF"v[76:79],v[212:215],a[100:103],v[76:79]\n"
RD"v[204:207],v243 offset:6144\n"
MF"v[84:87],v[212:215],a[104:107],v[84:87]\n"
BL"v241,s[16:19],s48 offen offset:3072 lds\n"
MF"v[92:95],v[212:215],a[108:111],v[92:95]\n"
RT"a[126:127],v249 offset:4608\n"
MF"v[96:99],v[216:219],a[96:99],v[96:99]\n"
SA"m0,s37,0\n"
RD"v[208:211],v243 offset:8192\n"
SA"s48,s47,3145728\n"
MF"v[104:107],v[216:219],a[100:103],v[104:107]\n"
"s_and_b32 s48,s48,4194303\n"
MF"v[112:115],v[216:219],a[104:107],v[112:115]\n"
WT"vmcnt(20)\n"
RT"a[128:129],v246 offset:8192\n"
MF"v[120:123],v[216:219],a[108:111],v[120:123]\n"
WT"lgkmcnt(4)\n"
BL"v238,s[16:19],s48 offen offset:0 lds\n"
MF"v[100:103],v[220:223],a[96:99],v[100:103]\n"
RD"v[212:215],v243 offset:10240\n"
MF"v[108:111],v[220:223],a[100:103],v[108:111]\n"
MF"v[116:119],v[220:223],a[104:107],v[116:119]\n"
RT"a[130:131],v246 offset:8704\n"
MF"v[124:127],v[220:223],a[108:111],v[124:127]\n"
BL"v239,s[16:19],s48 offen offset:1024 lds\n"
MF"v[0:3],v[192:195],a[112:115],v[0:3]\n"
RD"v[216:219],v243 offset:12288\n"
MF"v[8:11],v[192:195],a[116:119],v[8:11]\n"
MF"v[16:19],v[192:195],a[120:123],v[16:19]\n"
WT"lgkmcnt(3)\n"
RT"a[132:133],v247 offset:8192\n"
MF"v[24:27],v[192:195],a[124:127],v[24:27]\n"
RD"v[220:223],v243 offset:14336\n"
MF"v[4:7],v[196:199],a[112:115],v[4:7]\n"
BL"v240,s[16:19],s48 offen offset:2048 lds\n"
MF"v[12:15],v[196:199],a[116:119],v[12:15]\n"
MF"v[20:23],v[196:199],a[120:123],v[20:23]\n"
RT"a[134:135],v247 offset:8704\n"
MF"v[28:31],v[196:199],a[124:127],v[28:31]\n"
BL"v241,s[16:19],s48 offen offset:3072 lds\n"
MF"v[32:35],v[200:203],a[112:115],v[32:35]\n"
MF"v[40:43],v[200:203],a[116:119],v[40:43]\n"
MF"v[48:51],v[200:203],a[120:123],v[48:51]\n"
RT"a[136:137],v248 offset:8192\n"
MF"v[56:59],v[200:203],a[124:127],v[56:59]\n"
MF"v[36:39],v[204:207],a[112:115],v[36:39]\n"
MF"v[44:47],v[204:207],a[116:119],v[44:47]\n"
WT"vmcnt(20) lgkmcnt(2)\n"
"s_barrier\n"
MF"v[52:55],v[204:207],a[120:123],v[52:55]\n"
SA"m0,s36,0\n"
RD"v[192:195],v242 offset:16384\n"
SA"s48,s46,768\n"
MF"v[60:63],v[204:207],a[124:127],v[60:63]\n"
"s_and_b32 s48,s48,1023\n"
RT"a[138:139],v248 offset:8704\n"
MF"v[64:67],v[208:211],a[112:115],v[64:67]\n"
BL"v234,s[12:15],s48 offen offset:0 lds\n"
MF"v[72:75],v[208:211],a[116:119],v[72:75]\n"
MF"v[80:83],v[208:211],a[120:123],v[80:83]\n"
RD"v[196:199],v242 offset:18432\n"
MF"v[88:91],v[208:211],a[124:127],v[88:91]\n"
RT"a[140:141],v249 offset:8192\n"
MF"v[68:71],v[212:215],a[112:115],v[68:71]\n"
BL"v235,s[12:15],s48 offen offset:1024 lds\n"
MF"v[76:79],v[212:215],a[116:119],v[76:79]\n"
RD"v[200:203],v242 offset:20480\n"
MF"v[84:87],v[212:215],a[120:123],v[84:87]\n"
RT"a[142:143],v249 offset:8704\n"
MF"v[92:95],v[212:215],a[124:127],v[92:95]\n"
MF"v[96:99],v[216:219],a[112:115],v[96:99]\n"
MF"v[104:107],v[216:219],a[116:119],v[104:107]\n"
RD"v[204:207],v242 offset:22528\n"
MF"v[112:115],v[216:219],a[120:123],v[112:115]\n"
BL"v236,s[12:15],s48 offen offset:2048 lds\n"
MF"v[120:123],v[216:219],a[124:127],v[120:123]\n"
WT"vmcnt(19)\n"
RT"a[144:145],v246 offset:12288\n"
MF"v[100:103],v[220:223],a[112:115],v[100:103]\n"
RD"v[208:211],v242 offset:24576\n"
MF"v[108:111],v[220:223],a[116:119],v[108:111]\n"
BL"v237,s[12:15],s48 offen offset:3072 lds\n"
MF"v[116:119],v[220:223],a[120:123],v[116:119]\n"
RT"a[146:147],v246 offset:12800\n"
MF"v[124:127],v[220:223],a[124:127],v[124:127]\n"
WT"lgkmcnt(4)\n"
MF"v[0:3],v[192:195],a[128:131],v[0:3]\n"
SA"m0,s37,4096\n"
RD"v[212:215],v242 offset:26624\n"
SA"s48,s47,3407872\n"
MF"v[8:11],v[192:195],a[132:135],v[8:11]\n"
"s_and_b32 s48,s48,4194303\n"
MF"v[16:19],v[192:195],a[136:139],v[16:19]\n"
RT"a[148:149],v247 offset:12288\n"
MF"v[24:27],v[192:195],a[140:143],v[24:27]\n"
BL"v238,s[16:19],s48 offen offset:0 lds\n"
MF"v[4:7],v[196:199],a[128:131],v[4:7]\n"
RD"v[216:219],v242 offset:28672\n"
MF"v[12:15],v[196:199],a[132:135],v[12:15]\n"
MF"v[20:23],v[196:199],a[136:139],v[20:23]\n"
RT"a[150:151],v247 offset:12800\n"
MF"v[28:31],v[196:199],a[140:143],v[28:31]\n"
BL"v239,s[16:19],s48 offen offset:1024 lds\n"
MF"v[32:35],v[200:203],a[128:131],v[32:35]\n"
RD"v[220:223],v242 offset:30720\n"
MF"v[40:43],v[200:203],a[132:135],v[40:43]\n"
MF"v[48:51],v[200:203],a[136:139],v[48:51]\n"
RD"v[192:195],v243 offset:16384\n"
MF"v[56:59],v[200:203],a[140:143],v[56:59]\n"
WT"lgkmcnt(3)\n"
RT"a[152:153],v248 offset:12288\n"
MF"v[36:39],v[204:207],a[128:131],v[36:39]\n"
BL"v240,s[16:19],s48 offen offset:2048 lds\n"
MF"v[44:47],v[204:207],a[132:135],v[44:47]\n"
MF"v[52:55],v[204:207],a[136:139],v[52:55]\n"
RD"v[196:199],v243 offset:18432\n"
MF"v[60:63],v[204:207],a[140:143],v[60:63]\n"
RT"a[154:155],v248 offset:12800\n"
MF"v[64:67],v[208:211],a[128:131],v[64:67]\n"
BL"v241,s[16:19],s48 offen offset:3072 lds\n"
MF"v[72:75],v[208:211],a[132:135],v[72:75]\n"
RD"v[200:203],v243 offset:20480\n"
MF"v[80:83],v[208:211],a[136:139],v[80:83]\n"
SA"m0,s37,8192\n"
RT"a[156:157],v249 offset:12288\n"
SA"s48,s47,3670016\n"
MF"v[88:91],v[208:211],a[140:143],v[88:91]\n"
"s_and_b32 s48,s48,4194303\n"
MF"v[68:71],v[212:215],a[128:131],v[68:71]\n"
BL"v238,s[16:19],s48 offen offset:0 lds\n"
MF"v[76:79],v[212:215],a[132:135],v[76:79]\n"
RD"v[204:207],v243 offset:22528\n"
MF"v[84:87],v[212:215],a[136:139],v[84:87]\n"
RT"a[158:159],v249 offset:12800\n"
MF"v[92:95],v[212:215],a[140:143],v[92:95]\n"
MF"v[96:99],v[216:219],a[128:131],v[96:99]\n"
RD"v[208:211],v243 offset:24576\n"
MF"v[104:107],v[216:219],a[132:135],v[104:107]\n"
BL"v239,s[16:19],s48 offen offset:1024 lds\n"
MF"v[112:115],v[216:219],a[136:139],v[112:115]\n"
WT"vmcnt(22)\n"
RT"a[160:161],v246 offset:16384\n"
MF"v[120:123],v[216:219],a[140:143],v[120:123]\n"
WT"lgkmcnt(4)\n"
MF"v[100:103],v[220:223],a[128:131],v[100:103]\n"
RD"v[212:215],v243 offset:26624\n"
MF"v[108:111],v[220:223],a[132:135],v[108:111]\n"
BL"v240,s[16:19],s48 offen offset:2048 lds\n"
MF"v[116:119],v[220:223],a[136:139],v[116:119]\n"
RT"a[162:163],v246 offset:16896\n"
MF"v[124:127],v[220:223],a[140:143],v[124:127]\n"
RD"v[216:219],v243 offset:28672\n"
MF"v[0:3],v[192:195],a[144:147],v[0:3]\n"
MF"v[8:11],v[192:195],a[148:151],v[8:11]\n"
BL"v241,s[16:19],s48 offen offset:3072 lds\n"
MF"v[16:19],v[192:195],a[152:155],v[16:19]\n"
WT"lgkmcnt(3)\n"
RT"a[164:165],v247 offset:16384\n"
MF"v[24:27],v[192:195],a[156:159],v[24:27]\n"
RD"v[220:223],v243 offset:30720\n"
MF"v[4:7],v[196:199],a[144:147],v[4:7]\n"
MF"v[12:15],v[196:199],a[148:151],v[12:15]\n"
MF"v[20:23],v[196:199],a[152:155],v[20:23]\n"
RT"a[166:167],v247 offset:16896\n"
MF"v[28:31],v[196:199],a[156:159],v[28:31]\n"
MF"v[32:35],v[200:203],a[144:147],v[32:35]\n"
MF"v[40:43],v[200:203],a[148:151],v[40:43]\n"
MF"v[48:51],v[200:203],a[152:155],v[48:51]\n"
RT"a[168:169],v248 offset:16384\n"
MF"v[56:59],v[200:203],a[156:159],v[56:59]\n"
MF"v[36:39],v[204:207],a[144:147],v[36:39]\n"
MF"v[44:47],v[204:207],a[148:151],v[44:47]\n"
WT"vmcnt(20) lgkmcnt(2)\n"
"s_barrier\n"
MF"v[52:55],v[204:207],a[152:155],v[52:55]\n"
SA"m0,s36,16384\n"
RD"v[192:195],v242 offset:32768\n"
SA"s48,s46,896\n"
MF"v[60:63],v[204:207],a[156:159],v[60:63]\n"
"s_and_b32 s48,s48,1023\n"
RT"a[170:171],v248 offset:16896\n"
MF"v[64:67],v[208:211],a[144:147],v[64:67]\n"
BL"v234,s[12:15],s48 offen offset:0 lds\n"
MF"v[72:75],v[208:211],a[148:151],v[72:75]\n"
MF"v[80:83],v[208:211],a[152:155],v[80:83]\n"
RD"v[196:199],v242 offset:34816\n"
MF"v[88:91],v[208:211],a[156:159],v[88:91]\n"
RT"a[172:173],v249 offset:16384\n"
MF"v[68:71],v[212:215],a[144:147],v[68:71]\n"
BL"v235,s[12:15],s48 offen offset:1024 lds\n"
MF"v[76:79],v[212:215],a[148:151],v[76:79]\n"
RD"v[200:203],v242 offset:36864\n"
MF"v[84:87],v[212:215],a[152:155],v[84:87]\n"
RT"a[174:175],v249 offset:16896\n"
MF"v[92:95],v[212:215],a[156:159],v[92:95]\n"
MF"v[96:99],v[216:219],a[144:147],v[96:99]\n"
MF"v[104:107],v[216:219],a[148:151],v[104:107]\n"
RD"v[204:207],v242 offset:38912\n"
MF"v[112:115],v[216:219],a[152:155],v[112:115]\n"
BL"v236,s[12:15],s48 offen offset:2048 lds\n"
MF"v[120:123],v[216:219],a[156:159],v[120:123]\n"
WT"vmcnt(19)\n"
RT"a[176:177],v246 offset:20480\n"
MF"v[100:103],v[220:223],a[144:147],v[100:103]\n"
RD"v[208:211],v242 offset:40960\n"
MF"v[108:111],v[220:223],a[148:151],v[108:111]\n"
BL"v237,s[12:15],s48 offen offset:3072 lds\n"
MF"v[116:119],v[220:223],a[152:155],v[116:119]\n"
RT"a[178:179],v246 offset:20992\n"
MF"v[124:127],v[220:223],a[156:159],v[124:127]\n"
WT"lgkmcnt(4)\n"
MF"v[0:3],v[192:195],a[160:163],v[0:3]\n"
SA"m0,s37,12288\n"
RD"v[212:215],v242 offset:43008\n"
SA"s48,s47,3932160\n"
MF"v[8:11],v[192:195],a[164:167],v[8:11]\n"
"s_and_b32 s48,s48,4194303\n"
MF"v[16:19],v[192:195],a[168:171],v[16:19]\n"
RT"a[180:181],v247 offset:20480\n"
MF"v[24:27],v[192:195],a[172:175],v[24:27]\n"
BL"v238,s[16:19],s48 offen offset:0 lds\n"
MF"v[4:7],v[196:199],a[160:163],v[4:7]\n"
RD"v[216:219],v242 offset:45056\n"
MF"v[12:15],v[196:199],a[164:167],v[12:15]\n"
MF"v[20:23],v[196:199],a[168:171],v[20:23]\n"
RT"a[182:183],v247 offset:20992\n"
MF"v[28:31],v[196:199],a[172:175],v[28:31]\n"
BL"v239,s[16:19],s48 offen offset:1024 lds\n"
MF"v[32:35],v[200:203],a[160:163],v[32:35]\n"
RD"v[220:223],v242 offset:47104\n"
MF"v[40:43],v[200:203],a[164:167],v[40:43]\n"
MF"v[48:51],v[200:203],a[168:171],v[48:51]\n"
RD"v[192:195],v243 offset:32768\n"
MF"v[56:59],v[200:203],a[172:175],v[56:59]\n"
WT"lgkmcnt(3)\n"
RT"a[184:185],v248 offset:20480\n"
MF"v[36:39],v[204:207],a[160:163],v[36:39]\n"
BL"v240,s[16:19],s48 offen offset:2048 lds\n"
MF"v[44:47],v[204:207],a[164:167],v[44:47]\n"
MF"v[52:55],v[204:207],a[168:171],v[52:55]\n"
RD"v[196:199],v243 offset:34816\n"
MF"v[60:63],v[204:207],a[172:175],v[60:63]\n"
RT"a[186:187],v248 offset:20992\n"
MF"v[64:67],v[208:211],a[160:163],v[64:67]\n"
BL"v241,s[16:19],s48 offen offset:3072 lds\n"
MF"v[72:75],v[208:211],a[164:167],v[72:75]\n"
RD"v[200:203],v243 offset:36864\n"
MF"v[80:83],v[208:211],a[168:171],v[80:83]\n"
SA"m0,s37,16384\n"
RT"a[188:189],v249 offset:20480\n"
SA"s48,s39,0\n"
MF"v[88:91],v[208:211],a[172:175],v[88:91]\n"
MF"v[68:71],v[212:215],a[160:163],v[68:71]\n"
BL"v234,s[12:15],s48 offen offset:0 lds\n"
MF"v[76:79],v[212:215],a[164:167],v[76:79]\n"
RD"v[204:207],v243 offset:38912\n"
MF"v[84:87],v[212:215],a[168:171],v[84:87]\n"
RT"a[190:191],v249 offset:20992\n"
MF"v[92:95],v[212:215],a[172:175],v[92:95]\n"
"s_lshr_b32 s50,s30,1\n"
MF"v[96:99],v[216:219],a[160:163],v[96:99]\n"
"s_and_b32 s51,s30,1\n"
RD"v[208:211],v243 offset:40960\n"
MF"v[104:107],v[216:219],a[164:167],v[104:107]\n"
"s_mul_i32 s38,s50,24576\n"
BL"v235,s[12:15],s48 offen offset:1024 lds\n"
MF"v[112:115],v[216:219],a[168:171],v[112:115]\n"
"s_lshl_b32 s60,s50,18\n"
WT"vmcnt(22)\n"
RT"a[192:193],v246 offset:0\n"
MF"v[120:123],v[216:219],a[172:175],v[120:123]\n"
WT"lgkmcnt(4)\n"
"s_lshl_b32 s50,s51,12\n"
MF"v[100:103],v[220:223],a[160:163],v[100:103]\n"
SA"s38,s38,s50\n"
RD"v[212:215],v243 offset:43008\n"
MF"v[108:111],v[220:223],a[164:167],v[108:111]\n"
"s_lshl_b32 s50,s51,7\n"
BL"v236,s[12:15],s48 offen offset:2048 lds\n"
MF"v[116:119],v[220:223],a[168:171],v[116:119]\n"
SA"s60,s60,s50\n"
RT"a[194:195],v246 offset:512\n"
MF"v[124:127],v[220:223],a[172:175],v[124:127]\n"
RD"v[216:219],v243 offset:45056\n"
MF"v[0:3],v[192:195],a[176:179],v[0:3]\n"
MF"v[8:11],v[192:195],a[180:183],v[8:11]\n"
"s_sub_u32 s99,s34,5\n"
BL"v237,s[12:15],s48 offen offset:3072 lds\n"
MF"v[16:19],v[192:195],a[184:187],v[16:19]\n"
WT"lgkmcnt(3)\n"
RT"a[196:197],v247 offset:0\n"
"s_cmp_lt_u32 s34,5\n"
MF"v[24:27],v[192:195],a[188:191],v[24:27]\n"
"s_cselect_b32 s99,s34,s99\n"
RD"v[220:223],v243 offset:47104\n"
"s_mul_i32 s99,s99,1\n"
MF"v[4:7],v[196:199],a[176:179],v[4:7]\n"
SA"m0,s37,20480\n"
SA"s48,s98,0\n"
"s_lshl_b32 s61,s99,20\n"
MF"v[12:15],v[196:199],a[180:183],v[12:15]\n"
MF"v[20:23],v[196:199],a[184:187],v[20:23]\n"
RT"a[198:199],v247 offset:512\n"
MF"v[28:31],v[196:199],a[188:191],v[28:31]\n"
BL"v234,s[12:15],s48 offen offset:0 lds\n"
MF"v[32:35],v[200:203],a[176:179],v[32:35]\n"
MF"v[40:43],v[200:203],a[180:183],v[40:43]\n"
MF"v[48:51],v[200:203],a[184:187],v[48:51]\n"
RT"a[200:201],v248 offset:0\n"
MF"v[56:59],v[200:203],a[188:191],v[56:59]\n"
BL"v235,s[12:15],s48 offen offset:1024 lds\n"
MF"v[36:39],v[204:207],a[176:179],v[36:39]\n"
MF"v[44:47],v[204:207],a[180:183],v[44:47]\n"
WT"vmcnt(22) lgkmcnt(2)\n"
"s_barrier\n"
MF"v[52:55],v[204:207],a[184:187],v[52:55]\n"
RD"v[192:195],v242 offset:0\n"
MF"v[60:63],v[204:207],a[188:191],v[60:63]\n"
RT"a[202:203],v248 offset:512\n"
MF"v[64:67],v[208:211],a[176:179],v[64:67]\n"
BL"v236,s[12:15],s48 offen offset:2048 lds\n"
MF"v[72:75],v[208:211],a[180:183],v[72:75]\n"
MF"v[80:83],v[208:211],a[184:187],v[80:83]\n"
RD"v[196:199],v242 offset:2048\n"
MF"v[88:91],v[208:211],a[188:191],v[88:91]\n"
RT"a[204:205],v249 offset:0\n"
MF"v[68:71],v[212:215],a[176:179],v[68:71]\n"
BL"v237,s[12:15],s48 offen offset:3072 lds\n"
MF"v[76:79],v[212:215],a[180:183],v[76:79]\n"
RD"v[200:203],v242 offset:4096\n"
MF"v[84:87],v[212:215],a[184:187],v[84:87]\n"
RT"a[206:207],v249 offset:512\n"
MF"v[92:95],v[212:215],a[188:191],v[92:95]\n"
MF"v[96:99],v[216:219],a[176:179],v[96:99]\n"
MF"v[104:107],v[216:219],a[180:183],v[104:107]\n"
RD"v[204:207],v242 offset:6144\n"
MF"v[112:115],v[216:219],a[184:187],v[112:115]\n"
SA"m0,s37,0\n"
WT"vmcnt(20)\n"
RT"a[208:209],v246 offset:4096\n"
MF"v[120:123],v[216:219],a[188:191],v[120:123]\n"
SA"s48,s39,32768\n"
MF"v[100:103],v[220:223],a[176:179],v[100:103]\n"
RD"v[208:211],v242 offset:8192\n"
MF"v[108:111],v[220:223],a[180:183],v[108:111]\n"
WT"lgkmcnt(3)\n"
BL"v234,s[12:15],s48 offen offset:0 lds\n"
MF"v[116:119],v[220:223],a[184:187],v[116:119]\n"
RT"a[210:211],v246 offset:4608\n"
MF"v[124:127],v[220:223],a[188:191],v[124:127]\n"
MF"v[0:3],v[192:195],a[192:195],v[0:3]\n"
RD"v[212:215],v242 offset:10240\n"
MF"v[8:11],v[192:195],a[196:199],v[8:11]\n"
BL"v235,s[12:15],s48 offen offset:1024 lds\n"
MF"v[16:19],v[192:195],a[200:203],v[16:19]\n"
RT"a[212:213],v247 offset:4096\n"
MF"v[24:27],v[192:195],a[204:207],v[24:27]\n"
RD"v[216:219],v242 offset:12288\n"
MF"v[4:7],v[196:199],a[192:195],v[4:7]\n"
MF"v[12:15],v[196:199],a[196:199],v[12:15]\n"
BL"v236,s[12:15],s48 offen offset:2048 lds\n"
MF"v[20:23],v[196:199],a[200:203],v[20:23]\n"
RT"a[214:215],v247 offset:4608\n"
MF"v[28:31],v[196:199],a[204:207],v[28:31]\n"
BL"v[160:163],v224,s[8:11],0 offen offset:256 sc1\n"
MF"v[32:35],v[200:203],a[192:195],v[32:35]\n"
RD"v[220:223],v242 offset:14336\n"
MF"v[40:43],v[200:203],a[196:199],v[40:43]\n"
BL"v[164:167],v224,s[8:11],0 offen offset:1280 sc1\n"
MF"v[48:51],v[200:203],a[200:203],v[48:51]\n"
BL"v237,s[12:15],s48 offen offset:3072 lds\n"
MF"v[56:59],v[200:203],a[204:207],v[56:59]\n"
WT"lgkmcnt(2)\n"
RD"v[192:195],v243 offset:0\n"
MF"v[36:39],v[204:207],a[192:195],v[36:39]\n"
RT"a[216:217],v248 offset:4096\n"
MF"v[44:47],v[204:207],a[196:199],v[44:47]\n"
MF"v[52:55],v[204:207],a[200:203],v[52:55]\n"
RD"v[196:199],v243 offset:2048\n"
MF"v[60:63],v[204:207],a[204:207],v[60:63]\n"
RT"a[218:219],v248 offset:4608\n"
MF"v[64:67],v[208:211],a[192:195],v[64:67]\n"
MF"v[72:75],v[208:211],a[196:199],v[72:75]\n"
RD"v[200:203],v243 offset:4096\n"
MF"v[80:83],v[208:211],a[200:203],v[80:83]\n"
RT"a[220:221],v249 offset:4096\n"
MF"v[88:91],v[208:211],a[204:207],v[88:91]\n"
MF"v[68:71],v[212:215],a[192:195],v[68:71]\n"
MF"v[76:79],v[212:215],a[196:199],v[76:79]\n"
RD"v[204:207],v243 offset:6144\n"
MF"v[84:87],v[212:215],a[200:203],v[84:87]\n"
RT"a[222:223],v249 offset:4608\n"
MF"v[92:95],v[212:215],a[204:207],v[92:95]\n"
MF"v[96:99],v[216:219],a[192:195],v[96:99]\n"
RD"v[208:211],v243 offset:8192\n"
MF"v[104:107],v[216:219],a[196:199],v[104:107]\n"
MF"v[112:115],v[216:219],a[200:203],v[112:115]\n"
SA"m0,s37,4096\n"
WT"vmcnt(22)\n"
RT"a[224:225],v246 offset:8192\n"
MF"v[120:123],v[216:219],a[204:207],v[120:123]\n"
WT"lgkmcnt(4)\n"
SA"s48,s98,32768\n"
MF"v[100:103],v[220:223],a[192:195],v[100:103]\n"
RD"v[212:215],v243 offset:10240\n"
MF"v[108:111],v[220:223],a[196:199],v[108:111]\n"
WT"lgkmcnt(3)\n"
BL"v234,s[12:15],s48 offen offset:0 lds\n"
MF"v[116:119],v[220:223],a[200:203],v[116:119]\n"
RT"a[226:227],v246 offset:8704\n"
MF"v[124:127],v[220:223],a[204:207],v[124:127]\n"
RD"v[216:219],v243 offset:12288\n"
MF"v[0:3],v[192:195],a[208:211],v[0:3]\n"
BL"v235,s[12:15],s48 offen offset:1024 lds\n"
MF"v[8:11],v[192:195],a[212:215],v[8:11]\n"
MF"v[16:19],v[192:195],a[216:219],v[16:19]\n"
RT"a[228:229],v247 offset:8192\n"
MF"v[24:27],v[192:195],a[220:223],v[24:27]\n"
RD"v[220:223],v243 offset:14336\n"
MF"v[4:7],v[196:199],a[208:211],v[4:7]\n"
MF"v[12:15],v[196:199],a[212:215],v[12:15]\n"
BL"v236,s[12:15],s48 offen offset:2048 lds\n"
MF"v[20:23],v[196:199],a[216:219],v[20:23]\n"
RT"a[230:231],v247 offset:8704\n"
MF"v[28:31],v[196:199],a[220:223],v[28:31]\n"
MF"v[32:35],v[200:203],a[208:211],v[32:35]\n"
MF"v[40:43],v[200:203],a[212:215],v[40:43]\n"
BL"v237,s[12:15],s48 offen offset:3072 lds\n"
MF"v[48:51],v[200:203],a[216:219],v[48:51]\n"
RT"a[232:233],v248 offset:8192\n"
MF"v[56:59],v[200:203],a[220:223],v[56:59]\n"
MF"v[36:39],v[204:207],a[208:211],v[36:39]\n"
MF"v[44:47],v[204:207],a[212:215],v[44:47]\n"
WT"vmcnt(22) lgkmcnt(2)\n"
"s_barrier\n"
MF"v[52:55],v[204:207],a[216:219],v[52:55]\n"
RD"v[192:195],v242 offset:16384\n"
MF"v[60:63],v[204:207],a[220:223],v[60:63]\n"
RT"a[234:235],v248 offset:8704\n"
MF"v[64:67],v[208:211],a[208:211],v[64:67]\n"
MF"v[72:75],v[208:211],a[212:215],v[72:75]\n"
MF"v[80:83],v[208:211],a[216:219],v[80:83]\n"
RD"v[196:199],v242 offset:18432\n"
MF"v[88:91],v[208:211],a[220:223],v[88:91]\n"
RT"a[236:237],v249 offset:8192\n"
MF"v[68:71],v[212:215],a[208:211],v[68:71]\n"
MF"v[76:79],v[212:215],a[212:215],v[76:79]\n"
RD"v[200:203],v242 offset:20480\n"
MF"v[84:87],v[212:215],a[216:219],v[84:87]\n"
RT"a[238:239],v249 offset:8704\n"
MF"v[92:95],v[212:215],a[220:223],v[92:95]\n"
MF"v[96:99],v[216:219],a[208:211],v[96:99]\n"
"s_lshl_b32 s50,s35,20\n"
MF"v[104:107],v[216:219],a[212:215],v[104:107]\n"
"s_lshl_b32 s51,s34,15\n"
RD"v[204:207],v242 offset:22528\n"
MF"v[112:115],v[216:219],a[216:219],v[112:115]\n"
SA"s50,s50,s51\n"
WT"vmcnt(18)\n"
RT"a[240:241],v246 offset:12288\n"
MF"v[120:123],v[216:219],a[220:223],v[120:123]\n"
SA"m0,s37,8192\n"
SA"s48,s39,65536\n"
"s_and_b32 s51,s30,1\n"
MF"v[100:103],v[220:223],a[208:211],v[100:103]\n"
"s_lshl_b32 s51,s51,12\n"
RD"v[208:211],v242 offset:24576\n"
MF"v[108:111],v[220:223],a[212:215],v[108:111]\n"
WT"lgkmcnt(3)\n"
BL"v234,s[12:15],s48 offen offset:0 lds\n"
MF"v[116:119],v[220:223],a[216:219],v[116:119]\n"
SA"s50,s50,s51\n"
RT"a[242:243],v246 offset:12800\n"
SA"s50,s50,9699328\n"
MF"v[124:127],v[220:223],a[220:223],v[124:127]\n"
"s_mov_b32 s94,2686976\n"
WT"vmcnt(6)\n"
"v_cmp_eq_u64 vcc,s[28:29],v[160:161]\n"
"v_cmp_eq_u64_e64 s[62:63],s[28:29],v[162:163]\n"
"s_and_b64 vcc,vcc,s[62:63]\n"
"v_cmp_eq_u64_e64 s[62:63],s[28:29],v[164:165]\n"
"s_and_b64 vcc,vcc,s[62:63]\n"
"v_cmp_eq_u64_e64 s[62:63],s[28:29],v[166:167]\n"
"s_and_b64 vcc,vcc,s[62:63]\n"
"s_cmp_eq_u64 vcc,exec\n"
"s_cbranch_scc0 L1004_%=\n"
"L1005_%=:\n"
MF"v[0:3],v[192:195],a[224:227],v[0:3]\n"
"s_mov_b32 s95,2691072\n"
RD"v[212:215],v242 offset:26624\n"
"v_add_u32 v250,s50,v224\n"
MF"v[8:11],v[192:195],a[228:231],v[8:11]\n"
BL"v235,s[12:15],s48 offen offset:1024 lds\n"
MF"v[16:19],v[192:195],a[232:235],v[16:19]\n"
BL"v[160:163],v224,s[8:11],s94 offen offset:0 sc1\n"
MF"v[24:27],v[192:195],a[236:239],v[24:27]\n"
RT"a[244:245],v247 offset:12288\n"
MF"v[4:7],v[196:199],a[224:227],v[4:7]\n"
BL"v[164:167],v224,s[8:11],s94 offen offset:1024 sc1\n"
MF"v[12:15],v[196:199],a[228:231],v[12:15]\n"
RD"v[216:219],v242 offset:28672\n"
MF"v[20:23],v[196:199],a[232:235],v[20:23]\n"
BL"v[168:171],v224,s[8:11],s94 offen offset:2048 sc1\n"
MF"v[28:31],v[196:199],a[236:239],v[28:31]\n"
BL"v236,s[12:15],s48 offen offset:2048 lds\n"
MF"v[32:35],v[200:203],a[224:227],v[32:35]\n"
RT"a[246:247],v247 offset:12800\n"
MF"v[40:43],v[200:203],a[228:231],v[40:43]\n"
BL"v[172:175],v224,s[8:11],s94 offen offset:3072 sc1\n"
MF"v[48:51],v[200:203],a[232:235],v[48:51]\n"
RD"v[220:223],v242 offset:30720\n"
MF"v[56:59],v[200:203],a[236:239],v[56:59]\n"
WT"lgkmcnt(3)\n"
BL"v[176:179],v224,s[8:11],s95 offen offset:0 sc1\n"
MF"v[36:39],v[204:207],a[224:227],v[36:39]\n"
BL"v237,s[12:15],s48 offen offset:3072 lds\n"
MF"v[44:47],v[204:207],a[228:231],v[44:47]\n"
RD"v[192:195],v243 offset:16384\n"
MF"v[52:55],v[204:207],a[232:235],v[52:55]\n"
RT"a[248:249],v248 offset:12288\n"
MF"v[60:63],v[204:207],a[236:239],v[60:63]\n"
BL"v[180:183],v224,s[8:11],s95 offen offset:1024 sc1\n"
MF"v[64:67],v[208:211],a[224:227],v[64:67]\n"
BL"v[184:187],v224,s[8:11],s95 offen offset:2048 sc1\n"
MF"v[72:75],v[208:211],a[228:231],v[72:75]\n"
RD"v[196:199],v243 offset:18432\n"
MF"v[80:83],v[208:211],a[232:235],v[80:83]\n"
RT"a[250:251],v248 offset:12800\n"
MF"v[88:91],v[208:211],a[236:239],v[88:91]\n"
BL"v[188:191],v224,s[8:11],s95 offen offset:3072 sc1\n"
MF"v[68:71],v[212:215],a[224:227],v[68:71]\n"
RD"v[200:203],v243 offset:20480\n"
MF"v[76:79],v[212:215],a[228:231],v[76:79]\n"
RT"a[252:253],v249 offset:12288\n"
MF"v[84:87],v[212:215],a[232:235],v[84:87]\n"
RD"v[204:207],v243 offset:22528\n"
MF"v[92:95],v[212:215],a[236:239],v[92:95]\n"
WT"lgkmcnt(5)\n"
RT"a[254:255],v249 offset:12800\n"
MF"v[96:99],v[216:219],a[224:227],v[96:99]\n"
RD"v[208:211],v243 offset:24576\n"
MF"v[104:107],v[216:219],a[228:231],v[104:107]\n"
MF"v[112:115],v[216:219],a[232:235],v[112:115]\n"
SA"m0,s37,12288\n"
SA"s48,s98,65536\n"
MF"v[120:123],v[216:219],a[236:239],v[120:123]\n"
MF"v[100:103],v[220:223],a[224:227],v[100:103]\n"
RD"v[212:215],v243 offset:26624\n"
MF"v[108:111],v[220:223],a[228:231],v[108:111]\n"
WT"lgkmcnt(2)\n"
BL"v234,s[12:15],s48 offen offset:0 lds\n"
MF"v[116:119],v[220:223],a[232:235],v[116:119]\n"
MF"v[124:127],v[220:223],a[236:239],v[124:127]\n"
RD"v[216:219],v243 offset:28672\n"
MF"v[0:3],v[192:195],a[240:243],v[0:3]\n"
BL"v235,s[12:15],s48 offen offset:1024 lds\n"
MF"v[8:11],v[192:195],a[244:247],v[8:11]\n"
MF"v[16:19],v[192:195],a[248:251],v[16:19]\n"
MF"v[24:27],v[192:195],a[252:255],v[24:27]\n"
RD"v[220:223],v243 offset:30720\n"
MF"v[4:7],v[196:199],a[240:243],v[4:7]\n"
MF"v[12:15],v[196:199],a[244:247],v[12:15]\n"
BL"v236,s[12:15],s48 offen offset:2048 lds\n"
MF"v[20:23],v[196:199],a[248:251],v[20:23]\n"
MF"v[28:31],v[196:199],a[252:255],v[28:31]\n"
MF"v[32:35],v[200:203],a[240:243],v[32:35]\n"
MF"v[40:43],v[200:203],a[244:247],v[40:43]\n"
BL"v237,s[12:15],s48 offen offset:3072 lds\n"
MF"v[48:51],v[200:203],a[248:251],v[48:51]\n"
"v_add_u32 v244,114688,v242\n"
"v_add_u32 v245,114688,v243\n"
MF"v[56:59],v[200:203],a[252:255],v[56:59]\n"
"v_add_u32 v242,65536,v242\n"
"v_add_u32 v243,65536,v243\n"
MF"v[36:39],v[204:207],a[240:243],v[36:39]\n"
MF"v[44:47],v[204:207],a[244:247],v[44:47]\n"
WT"lgkmcnt(0)\n"
"s_barrier\n"
MF"v[52:55],v[204:207],a[248:251],v[52:55]\n"
RD"v[192:195],v242 offset:16384\n"
MF"v[60:63],v[204:207],a[252:255],v[60:63]\n"
MF"v[64:67],v[208:211],a[240:243],v[64:67]\n"
MF"v[72:75],v[208:211],a[244:247],v[72:75]\n"
MF"v[80:83],v[208:211],a[248:251],v[80:83]\n"
SA"m0,s36,49152\n"
RD"v[196:199],v242 offset:18432\n"
SA"s48,s60,s61\n"
MF"v[88:91],v[208:211],a[252:255],v[88:91]\n"
MF"v[68:71],v[212:215],a[240:243],v[68:71]\n"
BL"v238,s[20:23],s48 offen offset:0 lds\n"
MF"v[76:79],v[212:215],a[244:247],v[76:79]\n"
MF"v[84:87],v[212:215],a[248:251],v[84:87]\n"
RD"v[200:203],v243 offset:16384\n"
MF"v[92:95],v[212:215],a[252:255],v[92:95]\n"
MF"v[96:99],v[216:219],a[240:243],v[96:99]\n"
BL"v239,s[20:23],s48 offen offset:1024 lds\n"
MF"v[104:107],v[216:219],a[244:247],v[104:107]\n"
MF"v[112:115],v[216:219],a[248:251],v[112:115]\n"
RD"v[204:207],v243 offset:18432\n"
"v_mov_b32 v227,0\n"
MF"v[120:123],v[216:219],a[252:255],v[120:123]\n"
"s_cmp_eq_u32 s31,1\n"
WT"vmcnt(15)\n"
"v_mov_b32 v226,v160\n"
MF"v[100:103],v[220:223],a[240:243],v[100:103]\n"
"s_cselect_b32 s94,0,2130706432\n"
"v_mov_b32 v228,0\n"
"v_med3_u32 v228,v227,v228,v161\n"
MF"v[108:111],v[220:223],a[244:247],v[108:111]\n"
"buffer_store_dwordx2 v[232:233],v230,s[8:11],s94 offen sc1\n"
MF"v[116:119],v[220:223],a[248:251],v[116:119]\n"
SA"s61,s61,524288\n"
RD"v[208:211],v242 offset:20480\n"
"v_med3_u32 v227,v226,v227,v161\n"
MF"v[124:127],v[220:223],a[252:255],v[124:127]\n"
WT"lgkmcnt(2)\n"
BL"v240,s[20:23],s48 offen offset:2048 lds\n"
MF"v[128:131],v[192:195],a[0:3],0\n"
"v_max_u32 v226,v226,v161\n"
"v_med3_u32 v228,v227,v228,v162\n"
MF"v[136:139],v[192:195],a[4:7],0\n"
RD"v[212:215],v242 offset:22528\n"
"v_med3_u32 v227,v226,v227,v162\n"
MF"v[144:147],v[192:195],a[8:11],0\n"
BL"v241,s[20:23],s48 offen offset:3072 lds\n"
MF"v[152:155],v[192:195],a[12:15],0\n"
"v_max_u32 v226,v226,v162\n"
"v_med3_u32 v228,v227,v228,v163\n"
MF"v[132:135],v[196:199],a[0:3],0\n"
"v_med3_u32 v227,v226,v227,v163\n"
"v_max_u32 v226,v226,v163\n"
MF"v[140:143],v[196:199],a[4:7],0\n"
RD"v[216:219],v243 offset:20480\n"
PK"v0,v0,v1\n"
MF"v[148:151],v[196:199],a[8:11],0\n"
SA"m0,s36,32768\n"
PK"v1,v2,v3\n"
PK"v2,v4,v5\n"
MF"v[156:159],v[196:199],a[12:15],0\n"
SA"s48,s60,s61\n"
WT"vmcnt(17)\n"
"v_med3_u32 v228,v227,v228,v164\n"
MF"v[128:131],v[200:203],a[16:19],v[128:131]\n"
BL"v238,s[20:23],s48 offen offset:0 lds\n"
MF"v[136:139],v[200:203],a[20:23],v[136:139]\n"
RD"v[220:223],v243 offset:22528\n"
PK"v3,v6,v7\n"
MF"v[144:147],v[200:203],a[24:27],v[144:147]\n"
"v_med3_u32 v227,v226,v227,v164\n"
"v_max_u32 v226,v226,v164\n"
MF"v[152:155],v[200:203],a[28:31],v[152:155]\n"
WT"lgkmcnt(2)\n"
"v_med3_u32 v228,v227,v228,v165\n"
"v_med3_u32 v227,v226,v227,v165\n"
MF"v[132:135],v[204:207],a[16:19],v[132:135]\n"
RD"v[192:195],v242 offset:40960\n"
"v_max_u32 v226,v226,v165\n"
MF"v[140:143],v[204:207],a[20:23],v[140:143]\n"
BL"v239,s[20:23],s48 offen offset:1024 lds\n"
MF"v[148:151],v[204:207],a[24:27],v[148:151]\n"
"v_med3_u32 v228,v227,v228,v166\n"
PK"v8,v8,v9\n"
MF"v[156:159],v[204:207],a[28:31],v[156:159]\n"
PK"v9,v10,v11\n"
"v_med3_u32 v227,v226,v227,v166\n"
MF"v[128:131],v[208:211],a[32:35],v[128:131]\n"
"s_mov_b32 s49,0\n"
RD"v[196:199],v242 offset:43008\n"
PK"v10,v12,v13\n"
MF"v[136:139],v[208:211],a[36:39],v[136:139]\n"
BL"v240,s[20:23],s48 offen offset:2048 lds\n"
MF"v[144:147],v[208:211],a[40:43],v[144:147]\n"
SA"s61,s61,524288\n"
BS"v[0:3],v250,s[8:11],s49 offen offset:0 sc1\n"
MF"v[152:155],v[208:211],a[44:47],v[152:155]\n"
"s_cmp_eq_u32 s61,5242880\n"
PK"v11,v14,v15\n"
"v_max_u32 v226,v226,v166\n"
MF"v[132:135],v[212:215],a[32:35],v[132:135]\n"
"s_cselect_b32 s61,0,s61\n"
RD"v[200:203],v243 offset:40960\n"
"v_med3_u32 v228,v227,v228,v167\n"
MF"v[140:143],v[212:215],a[36:39],v[140:143]\n"
BL"v241,s[20:23],s48 offen offset:3072 lds\n"
MF"v[148:151],v[212:215],a[40:43],v[148:151]\n"
"v_med3_u32 v227,v226,v227,v167\n"
"v_max_u32 v226,v226,v167\n"
MF"v[156:159],v[212:215],a[44:47],v[156:159]\n"
WT"lgkmcnt(1)\n"
WT"vmcnt(21)\n"
"v_med3_u32 v228,v227,v228,v168\n"
MF"v[128:131],v[216:219],a[48:51],v[128:131]\n"
RD"v[204:207],v243 offset:43008\n"
"v_med3_u32 v227,v226,v227,v168\n"
MF"v[136:139],v[216:219],a[52:55],v[136:139]\n"
PK"v16,v16,v17\n"
PK"v17,v18,v19\n"
MF"v[144:147],v[216:219],a[56:59],v[144:147]\n"
"v_max_u32 v226,v226,v168\n"
"v_med3_u32 v228,v227,v228,v169\n"
MF"v[152:155],v[216:219],a[60:63],v[152:155]\n"
RD"v[208:211],v242 offset:45056\n"
PK"v18,v20,v21\n"
MF"v[132:135],v[220:223],a[48:51],v[132:135]\n"
PK"v19,v22,v23\n"
"v_med3_u32 v227,v226,v227,v169\n"
MF"v[140:143],v[220:223],a[52:55],v[140:143]\n"
"v_max_u32 v226,v226,v169\n"
"v_med3_u32 v228,v227,v228,v170\n"
MF"v[148:151],v[220:223],a[56:59],v[148:151]\n"
"v_med3_u32 v227,v226,v227,v170\n"
"v_max_u32 v226,v226,v170\n"
MF"v[156:159],v[220:223],a[60:63],v[156:159]\n"
RD"v[212:215],v242 offset:47104\n"
PK"v24,v24,v25\n"
MF"v[128:131],v[192:195],a[64:67],v[128:131]\n"
PK"v25,v26,v27\n"
"v_med3_u32 v228,v227,v228,v171\n"
MF"v[136:139],v[192:195],a[68:71],v[136:139]\n"
"v_med3_u32 v227,v226,v227,v171\n"
PK"v26,v28,v29\n"
MF"v[144:147],v[192:195],a[72:75],v[144:147]\n"
PK"v27,v30,v31\n"
"v_max_u32 v226,v226,v171\n"
MF"v[152:155],v[192:195],a[76:79],v[152:155]\n"
RD"v[216:219],v243 offset:45056\n"
PK"v32,v32,v33\n"
MF"v[132:135],v[196:199],a[64:67],v[132:135]\n"
WT"vmcnt(19)\n"
"v_med3_u32 v228,v227,v228,v172\n"
"v_med3_u32 v227,v226,v227,v172\n"
MF"v[140:143],v[196:199],a[68:71],v[140:143]\n"
"v_max_u32 v226,v226,v172\n"
"v_med3_u32 v228,v227,v228,v173\n"
MF"v[148:151],v[196:199],a[72:75],v[148:151]\n"
PK"v33,v34,v35\n"
"v_med3_u32 v227,v226,v227,v173\n"
MF"v[156:159],v[196:199],a[76:79],v[156:159]\n"
WT"lgkmcnt(1)\n"
RD"v[220:223],v243 offset:47104\n"
"v_max_u32 v226,v226,v173\n"
MF"v[128:131],v[200:203],a[80:83],v[128:131]\n"
PK"v34,v36,v37\n"
PK"v35,v38,v39\n"
MF"v[136:139],v[200:203],a[84:87],v[136:139]\n"
"v_med3_u32 v228,v227,v228,v174\n"
"v_med3_u32 v227,v226,v227,v174\n"
MF"v[144:147],v[200:203],a[88:91],v[144:147]\n"
RD"v[192:195],v244 offset:16384\n"
"v_max_u32 v226,v226,v174\n"
MF"v[152:155],v[200:203],a[92:95],v[152:155]\n"
"v_med3_u32 v228,v227,v228,v175\n"
"v_med3_u32 v227,v226,v227,v175\n"
MF"v[132:135],v[204:207],a[80:83],v[132:135]\n"
"v_max_u32 v226,v226,v175\n"
PK"v40,v40,v41\n"
MF"v[140:143],v[204:207],a[84:87],v[140:143]\n"
PK"v41,v42,v43\n"
PK"v42,v44,v45\n"
MF"v[148:151],v[204:207],a[88:91],v[148:151]\n"
RD"v[196:199],v244 offset:18432\n"
PK"v43,v46,v47\n"
MF"v[156:159],v[204:207],a[92:95],v[156:159]\n"
WT"vmcnt(18)\n"
"v_med3_u32 v228,v227,v228,v176\n"
"v_med3_u32 v227,v226,v227,v176\n"
MF"v[128:131],v[208:211],a[96:99],v[128:131]\n"
"v_max_u32 v226,v226,v176\n"
"v_med3_u32 v228,v227,v228,v177\n"
MF"v[136:139],v[208:211],a[100:103],v[136:139]\n"
BS"v[8:11],v250,s[8:11],s49 offen offset:1024 sc1\n"
MF"v[144:147],v[208:211],a[104:107],v[144:147]\n"
RD"v[200:203],v245 offset:16384\n"
"v_med3_u32 v227,v226,v227,v177\n"
MF"v[152:155],v[208:211],a[108:111],v[152:155]\n"
"v_max_u32 v226,v226,v177\n"
"v_med3_u32 v228,v227,v228,v178\n"
MF"v[132:135],v[212:215],a[96:99],v[132:135]\n"
PK"v48,v48,v49\n"
PK"v49,v50,v51\n"
MF"v[140:143],v[212:215],a[100:103],v[140:143]\n"
"v_med3_u32 v227,v226,v227,v178\n"
PK"v50,v52,v53\n"
MF"v[148:151],v[212:215],a[104:107],v[148:151]\n"
RD"v[204:207],v245 offset:18432\n"
PK"v51,v54,v55\n"
MF"v[156:159],v[212:215],a[108:111],v[156:159]\n"
WT"lgkmcnt(2)\n"
"v_max_u32 v226,v226,v178\n"
"v_med3_u32 v228,v227,v228,v179\n"
MF"v[128:131],v[216:219],a[112:115],v[128:131]\n"
"v_med3_u32 v227,v226,v227,v179\n"
"v_max_u32 v226,v226,v179\n"
MF"v[136:139],v[216:219],a[116:119],v[136:139]\n"
RD"v[208:211],v244 offset:20480\n"
PK"v56,v56,v57\n"
MF"v[144:147],v[216:219],a[120:123],v[144:147]\n"
WT"vmcnt(17)\n"
"v_med3_u32 v228,v227,v228,v180\n"
"v_med3_u32 v227,v226,v227,v180\n"
MF"v[152:155],v[216:219],a[124:127],v[152:155]\n"
PK"v57,v58,v59\n"
"v_max_u32 v226,v226,v180\n"
MF"v[132:135],v[220:223],a[112:115],v[132:135]\n"
PK"v58,v60,v61\n"
PK"v59,v62,v63\n"
MF"v[140:143],v[220:223],a[116:119],v[140:143]\n"
RD"v[212:215],v244 offset:22528\n"
"v_med3_u32 v228,v227,v228,v181\n"
MF"v[148:151],v[220:223],a[120:123],v[148:151]\n"
"v_med3_u32 v227,v226,v227,v181\n"
"v_max_u32 v226,v226,v181\n"
MF"v[156:159],v[220:223],a[124:127],v[156:159]\n"
"v_med3_u32 v228,v227,v228,v182\n"
"v_med3_u32 v227,v226,v227,v182\n"
MF"v[128:131],v[192:195],a[128:131],v[128:131]\n"
"v_max_u32 v226,v226,v182\n"
PK"v64,v64,v65\n"
MF"v[136:139],v[192:195],a[132:135],v[136:139]\n"
RD"v[216:219],v245 offset:20480\n"
PK"v65,v66,v67\n"
MF"v[144:147],v[192:195],a[136:139],v[144:147]\n"
"v_med3_u32 v228,v227,v228,v183\n"
PK"v66,v68,v69\n"
MF"v[152:155],v[192:195],a[140:143],v[152:155]\n"
PK"v67,v70,v71\n"
"v_med3_u32 v227,v226,v227,v183\n"
MF"v[132:135],v[196:199],a[128:131],v[132:135]\n"
"v_max_u32 v226,v226,v183\n"
PK"v72,v72,v73\n"
MF"v[140:143],v[196:199],a[132:135],v[140:143]\n"
RD"v[220:223],v245 offset:22528\n"
PK"v73,v74,v75\n"
MF"v[148:151],v[196:199],a[136:139],v[148:151]\n"
WT"vmcnt(16)\n"
"v_med3_u32 v228,v227,v228,v184\n"
"v_med3_u32 v227,v226,v227,v184\n"
MF"v[156:159],v[196:199],a[140:143],v[156:159]\n"
WT"lgkmcnt(2)\n"
"v_max_u32 v226,v226,v184\n"
"v_med3_u32 v228,v227,v228,v185\n"
MF"v[128:131],v[200:203],a[144:147],v[128:131]\n"
RD"v[192:195],v244 offset:40960\n"
"v_med3_u32 v227,v226,v227,v185\n"
MF"v[136:139],v[200:203],a[148:151],v[136:139]\n"
"v_max_u32 v226,v226,v185\n"
PK"v74,v76,v77\n"
MF"v[144:147],v[200:203],a[152:155],v[144:147]\n"
PK"v75,v78,v79\n"
"v_med3_u32 v228,v227,v228,v186\n"
MF"v[152:155],v[200:203],a[156:159],v[152:155]\n"
"v_med3_u32 v227,v226,v227,v186\n"
"v_max_u32 v226,v226,v186\n"
MF"v[132:135],v[204:207],a[144:147],v[132:135]\n"
RD"v[196:199],v244 offset:43008\n"
"v_med3_u32 v228,v227,v228,v187\n"
MF"v[140:143],v[204:207],a[148:151],v[140:143]\n"
"v_med3_u32 v227,v226,v227,v187\n"
PK"v80,v80,v81\n"
MF"v[148:151],v[204:207],a[152:155],v[148:151]\n"
PK"v81,v82,v83\n"
"v_max_u32 v226,v226,v187\n"
MF"v[156:159],v[204:207],a[156:159],v[156:159]\n"
WT"vmcnt(15)\n"
"v_med3_u32 v228,v227,v228,v188\n"
PK"v82,v84,v85\n"
MF"v[128:131],v[208:211],a[160:163],v[128:131]\n"
RD"v[200:203],v245 offset:40960\n"
PK"v83,v86,v87\n"
MF"v[136:139],v[208:211],a[164:167],v[136:139]\n"
BS"v[16:19],v250,s[8:11],s49 offen offset:2048 sc1\n"
MF"v[144:147],v[208:211],a[168:171],v[144:147]\n"
"v_med3_u32 v227,v226,v227,v188\n"
"v_max_u32 v226,v226,v188\n"
MF"v[152:155],v[208:211],a[172:175],v[152:155]\n"
"v_med3_u32 v228,v227,v228,v189\n"
"v_med3_u32 v227,v226,v227,v189\n"
MF"v[132:135],v[212:215],a[160:163],v[132:135]\n"
RD"v[204:207],v245 offset:43008\n"
"v_max_u32 v226,v226,v189\n"
MF"v[140:143],v[212:215],a[164:167],v[140:143]\n"
PK"v88,v88,v89\n"
PK"v89,v90,v91\n"
MF"v[148:151],v[212:215],a[168:171],v[148:151]\n"
"v_med3_u32 v228,v227,v228,v190\n"
"v_med3_u32 v227,v226,v227,v190\n"
MF"v[156:159],v[212:215],a[172:175],v[156:159]\n"
WT"lgkmcnt(1)\n"
RD"v[208:211],v244 offset:45056\n"
PK"v90,v92,v93\n"
MF"v[128:131],v[216:219],a[176:179],v[128:131]\n"
PK"v91,v94,v95\n"
"v_max_u32 v226,v226,v190\n"
MF"v[136:139],v[216:219],a[180:183],v[136:139]\n"
"v_med3_u32 v228,v227,v228,v191\n"
"v_med3_u32 v227,v226,v227,v191\n"
MF"v[144:147],v[216:219],a[184:187],v[144:147]\n"
"v_max_u32 v226,v226,v191\n"
"v_mov_b32 v229,v226\n"
MF"v[152:155],v[216:219],a[188:191],v[152:155]\n"
RD"v[212:215],v244 offset:47104\n"
"v_max_u32_dpp v229,v229,v229 row_shr:1 row_mask:0xf bank_mask:0xf\n"
MF"v[132:135],v[220:223],a[176:179],v[132:135]\n"
MF"v[140:143],v[220:223],a[180:183],v[140:143]\n"
"v_max_u32_dpp v229,v229,v229 row_shr:2 row_mask:0xf bank_mask:0xf\n"
MF"v[148:151],v[220:223],a[184:187],v[148:151]\n"
MF"v[156:159],v[220:223],a[188:191],v[156:159]\n"
RD"v[216:219],v245 offset:45056\n"
"v_max_u32_dpp v229,v229,v229 row_shr:4 row_mask:0xf bank_mask:0xf\n"
MF"v[128:131],v[192:195],a[192:195],v[128:131]\n"
MF"v[136:139],v[192:195],a[196:199],v[136:139]\n"
"v_max_u32_dpp v229,v229,v229 row_shr:8 row_mask:0xf bank_mask:0xf\n"
MF"v[144:147],v[192:195],a[200:203],v[144:147]\n"
MF"v[152:155],v[192:195],a[204:207],v[152:155]\n"
RD"v[220:223],v245 offset:47104\n"
"v_max_u32_dpp v229,v229,v229 row_bcast:15 row_mask:0xa bank_mask:0xf\n"
MF"v[132:135],v[196:199],a[192:195],v[132:135]\n"
MF"v[140:143],v[196:199],a[196:199],v[140:143]\n"
"v_max_u32_dpp v229,v229,v229 row_bcast:31 row_mask:0xc bank_mask:0xf\n"
MF"v[148:151],v[196:199],a[200:203],v[148:151]\n"
"v_readlane_b32 s62,v229,63\n"
MF"v[156:159],v[196:199],a[204:207],v[156:159]\n"
MF"v[128:131],v[200:203],a[208:211],v[128:131]\n"
"v_cmp_eq_u32 vcc,s62,v226\n"
"v_writelane_b32 v225,s62,0\n"
MF"v[136:139],v[200:203],a[212:215],v[136:139]\n"
"v_cndmask_b32 v226,v226,v227,vcc\n"
"v_cndmask_b32_e64 v227,v227,0,vcc\n"
MF"v[144:147],v[200:203],a[216:219],v[144:147]\n"
"v_mov_b32 v229,v226\n"
MF"v[152:155],v[200:203],a[220:223],v[152:155]\n"
WT"lgkmcnt(0)\n"
"v_max_u32_dpp v229,v229,v229 row_shr:1 row_mask:0xf bank_mask:0xf\n"
MF"v[132:135],v[204:207],a[208:211],v[132:135]\n"
MF"v[140:143],v[204:207],a[212:215],v[140:143]\n"
"s_barrier\n"
MF"v[148:151],v[204:207],a[216:219],v[148:151]\n"
RD"v[192:195],v242 offset:0\n"
"v_max_u32_dpp v229,v229,v229 row_shr:2 row_mask:0xf bank_mask:0xf\n"
MF"v[156:159],v[204:207],a[220:223],v[156:159]\n"
BS"v[24:27],v250,s[8:11],s49 offen offset:3072 sc1\n"
MF"v[128:131],v[208:211],a[224:227],v[128:131]\n"
"v_max_u32_dpp v229,v229,v229 row_shr:4 row_mask:0xf bank_mask:0xf\n"
MF"v[136:139],v[208:211],a[228:231],v[136:139]\n"
MF"v[144:147],v[208:211],a[232:235],v[144:147]\n"
RD"v[196:199],v242 offset:2048\n"
"v_max_u32_dpp v229,v229,v229 row_shr:8 row_mask:0xf bank_mask:0xf\n"
MF"v[152:155],v[208:211],a[236:239],v[152:155]\n"
MF"v[132:135],v[212:215],a[224:227],v[132:135]\n"
"v_max_u32_dpp v229,v229,v229 row_bcast:15 row_mask:0xa bank_mask:0xf\n"
MF"v[140:143],v[212:215],a[228:231],v[140:143]\n"
MF"v[148:151],v[212:215],a[232:235],v[148:151]\n"
RD"v[200:203],v243 offset:0\n"
"v_max_u32_dpp v229,v229,v229 row_bcast:31 row_mask:0xc bank_mask:0xf\n"
MF"v[156:159],v[212:215],a[236:239],v[156:159]\n"
"v_readlane_b32 s62,v229,63\n"
MF"v[128:131],v[216:219],a[240:243],v[128:131]\n"
MF"v[136:139],v[216:219],a[244:247],v[136:139]\n"
"v_cmp_eq_u32 vcc,s62,v226\n"
"v_writelane_b32 v225,s62,1\n"
MF"v[144:147],v[216:219],a[248:251],v[144:147]\n"
RD"v[204:207],v243 offset:2048\n"
"v_cndmask_b32 v226,v226,v227,vcc\n"
MF"v[152:155],v[216:219],a[252:255],v[152:155]\n"
"v_cndmask_b32_e64 v227,v227,0,vcc\n"
"v_mov_b32 v229,v226\n"
MF"v[132:135],v[220:223],a[240:243],v[132:135]\n"
MF"v[140:143],v[220:223],a[244:247],v[140:143]\n"
RD"v[208:211],v242 offset:4096\n"
"v_max_u32_dpp v229,v229,v229 row_shr:1 row_mask:0xf bank_mask:0xf\n"
MF"v[148:151],v[220:223],a[248:251],v[148:151]\n"
MF"v[156:159],v[220:223],a[252:255],v[156:159]\n"
WT"lgkmcnt(2)\n"
"v_max_u32_dpp v229,v229,v229 row_shr:2 row_mask:0xf bank_mask:0xf\n"
MF"v[36:39],v[192:195],a[0:3],0\n"
MF"v[44:47],v[192:195],a[4:7],0\n"
RD"v[212:215],v242 offset:6144\n"
"v_max_u32_dpp v229,v229,v229 row_shr:4 row_mask:0xf bank_mask:0xf\n"
MF"v[52:55],v[192:195],a[8:11],0\n"
MF"v[60:63],v[192:195],a[12:15],0\n"
"v_max_u32_dpp v229,v229,v229 row_shr:8 row_mask:0xf bank_mask:0xf\n"
PK"v4,v128,v129\n"
MF"v[68:71],v[196:199],a[0:3],0\n"
"v_max_u32_dpp v229,v229,v229 row_bcast:15 row_mask:0xa bank_mask:0xf\n"
PK"v5,v130,v131\n"
MF"v[76:79],v[196:199],a[4:7],0\n"
RD"v[216:219],v243 offset:4096\n"
"v_max_u32_dpp v229,v229,v229 row_bcast:31 row_mask:0xc bank_mask:0xf\n"
MF"v[84:87],v[196:199],a[8:11],0\n"
"v_readlane_b32 s62,v229,63\n"
PK"v6,v132,v133\n"
MF"v[92:95],v[196:199],a[12:15],0\n"
"v_cmp_eq_u32 vcc,s62,v226\n"
"v_writelane_b32 v225,s62,2\n"
MF"v[36:39],v[200:203],a[16:19],v[36:39]\n"
"v_cndmask_b32 v226,v226,v227,vcc\n"
"v_cndmask_b32_e64 v227,v227,0,vcc\n"
MF"v[44:47],v[200:203],a[20:23],v[44:47]\n"
RD"v[220:223],v243 offset:6144\n"
"v_mov_b32 v229,v226\n"
MF"v[52:55],v[200:203],a[24:27],v[52:55]\n"
PK"v7,v134,v135\n"
"v_max_u32_dpp v229,v229,v229 row_shr:1 row_mask:0xf bank_mask:0xf\n"
MF"v[60:63],v[200:203],a[28:31],v[60:63]\n"
WT"lgkmcnt(2)\n"
"v_max_u32_dpp v229,v229,v229 row_shr:2 row_mask:0xf bank_mask:0xf\n"
PK"v12,v136,v137\n"
MF"v[68:71],v[204:207],a[16:19],v[68:71]\n"
"s_mov_b32 s49,8192\n"
RD"v[192:195],v242 offset:24576\n"
"v_max_u32_dpp v229,v229,v229 row_shr:4 row_mask:0xf bank_mask:0xf\n"
MF"v[76:79],v[204:207],a[20:23],v[76:79]\n"
PK"v13,v138,v139\n"
"v_max_u32_dpp v229,v229,v229 row_shr:8 row_mask:0xf bank_mask:0xf\n"
MF"v[84:87],v[204:207],a[24:27],v[84:87]\n"
BS"v[32:35],v250,s[8:11],s49 offen offset:0 sc1\n"
MF"v[92:95],v[204:207],a[28:31],v[92:95]\n"
"v_max_u32_dpp v229,v229,v229 row_bcast:15 row_mask:0xa bank_mask:0xf\n"
PK"v14,v140,v141\n"
MF"v[36:39],v[208:211],a[32:35],v[36:39]\n"
RD"v[196:199],v242 offset:26624\n"
"v_max_u32_dpp v229,v229,v229 row_bcast:31 row_mask:0xc bank_mask:0xf\n"
MF"v[44:47],v[208:211],a[36:39],v[44:47]\n"
"v_readlane_b32 s62,v229,63\n"
PK"v15,v142,v143\n"
MF"v[52:55],v[208:211],a[40:43],v[52:55]\n"
"v_cmp_eq_u32 vcc,s62,v226\n"
"v_writelane_b32 v225,s62,3\n"
MF"v[60:63],v[208:211],a[44:47],v[60:63]\n"
"v_cndmask_b32 v226,v226,v227,vcc\n"
"v_cndmask_b32_e64 v227,v227,0,vcc\n"
MF"v[68:71],v[212:215],a[32:35],v[68:71]\n"
RD"v[200:203],v243 offset:24576\n"
"v_mov_b32 v229,v226\n"
MF"v[76:79],v[212:215],a[36:39],v[76:79]\n"
PK"v20,v144,v145\n"
"v_max_u32_dpp v229,v229,v229 row_shr:1 row_mask:0xf bank_mask:0xf\n"
MF"v[84:87],v[212:215],a[40:43],v[84:87]\n"
PK"v21,v146,v147\n"
"v_max_u32_dpp v229,v229,v229 row_shr:2 row_mask:0xf bank_mask:0xf\n"
MF"v[92:95],v[212:215],a[44:47],v[92:95]\n"
WT"lgkmcnt(1)\n"
"v_max_u32_dpp v229,v229,v229 row_shr:4 row_mask:0xf bank_mask:0xf\n"
PK"v22,v148,v149\n"
MF"v[36:39],v[216:219],a[48:51],v[36:39]\n"
RD"v[204:207],v243 offset:26624\n"
"v_max_u32_dpp v229,v229,v229 row_shr:8 row_mask:0xf bank_mask:0xf\n"
MF"v[44:47],v[216:219],a[52:55],v[44:47]\n"
PK"v23,v150,v151\n"
"v_max_u32_dpp v229,v229,v229 row_bcast:15 row_mask:0xa bank_mask:0xf\n"
MF"v[52:55],v[216:219],a[56:59],v[52:55]\n"
PK"v28,v152,v153\n"
"v_max_u32_dpp v229,v229,v229 row_bcast:31 row_mask:0xc bank_mask:0xf\n"
MF"v[60:63],v[216:219],a[60:63],v[60:63]\n"
RD"v[208:211],v242 offset:28672\n"
"v_readlane_b32 s62,v229,63\n"
MF"v[68:71],v[220:223],a[48:51],v[68:71]\n"
PK"v29,v154,v155\n"
"v_cmp_eq_u32 vcc,s62,v226\n"
MF"v[76:79],v[220:223],a[52:55],v[76:79]\n"
"v_writelane_b32 v225,s62,4\n"
"v_cndmask_b32 v226,v226,v227,vcc\n"
MF"v[84:87],v[220:223],a[56:59],v[84:87]\n"
"v_cndmask_b32_e64 v227,v227,0,vcc\n"
"v_mov_b32 v229,v226\n"
MF"v[92:95],v[220:223],a[60:63],v[92:95]\n"
RD"v[212:215],v242 offset:30720\n"
"v_max_u32_dpp v229,v229,v229 row_shr:1 row_mask:0xf bank_mask:0xf\n"
MF"v[36:39],v[192:195],a[64:67],v[36:39]\n"
PK"v30,v156,v157\n"
"v_max_u32_dpp v229,v229,v229 row_shr:2 row_mask:0xf bank_mask:0xf\n"
MF"v[44:47],v[192:195],a[68:71],v[44:47]\n"
PK"v31,v158,v159\n"
"v_max_u32_dpp v229,v229,v229 row_shr:4 row_mask:0xf bank_mask:0xf\n"
MF"v[52:55],v[192:195],a[72:75],v[52:55]\n"
MF"v[60:63],v[192:195],a[76:79],v[60:63]\n"
RD"v[216:219],v243 offset:28672\n"
"v_max_u32_dpp v229,v229,v229 row_shr:8 row_mask:0xf bank_mask:0xf\n"
MF"v[68:71],v[196:199],a[64:67],v[68:71]\n"
MF"v[76:79],v[196:199],a[68:71],v[76:79]\n"
"v_max_u32_dpp v229,v229,v229 row_bcast:15 row_mask:0xa bank_mask:0xf\n"
MF"v[84:87],v[196:199],a[72:75],v[84:87]\n"
MF"v[92:95],v[196:199],a[76:79],v[92:95]\n"
WT"lgkmcnt(1)\n"
RD"v[220:223],v243 offset:30720\n"
"v_max_u32_dpp v229,v229,v229 row_bcast:31 row_mask:0xc bank_mask:0xf\n"
MF"v[36:39],v[200:203],a[80:83],v[36:39]\n"
"v_readlane_b32 s62,v229,63\n"
MF"v[44:47],v[200:203],a[84:87],v[44:47]\n"
MF"v[52:55],v[200:203],a[88:91],v[52:55]\n"
RD"v[192:195],v244 offset:0\n"
"v_cmp_eq_u32 vcc,s62,v226\n"
MF"v[60:63],v[200:203],a[92:95],v[60:63]\n"
"v_writelane_b32 v225,s62,5\n"
"v_cndmask_b32 v226,v226,v227,vcc\n"
MF"v[68:71],v[204:207],a[80:83],v[68:71]\n"
"v_cndmask_b32_e64 v227,v227,0,vcc\n"
"v_mov_b32 v229,v226\n"
MF"v[76:79],v[204:207],a[84:87],v[76:79]\n"
BS"v[40:43],v250,s[8:11],s49 offen offset:1024 sc1\n"
MF"v[84:87],v[204:207],a[88:91],v[84:87]\n"
RD"v[196:199],v244 offset:2048\n"
"v_max_u32_dpp v229,v229,v229 row_shr:1 row_mask:0xf bank_mask:0xf\n"
MF"v[92:95],v[204:207],a[92:95],v[92:95]\n"
MF"v[36:39],v[208:211],a[96:99],v[36:39]\n"
"v_max_u32_dpp v229,v229,v229 row_shr:2 row_mask:0xf bank_mask:0xf\n"
MF"v[44:47],v[208:211],a[100:103],v[44:47]\n"
MF"v[52:55],v[208:211],a[104:107],v[52:55]\n"
RD"v[200:203],v245 offset:0\n"
"v_max_u32_dpp v229,v229,v229 row_shr:4 row_mask:0xf bank_mask:0xf\n"
MF"v[60:63],v[208:211],a[108:111],v[60:63]\n"
MF"v[68:71],v[212:215],a[96:99],v[68:71]\n"
"v_max_u32_dpp v229,v229,v229 row_shr:8 row_mask:0xf bank_mask:0xf\n"
MF"v[76:79],v[212:215],a[100:103],v[76:79]\n"
MF"v[84:87],v[212:215],a[104:107],v[84:87]\n"
RD"v[204:207],v245 offset:2048\n"
"v_max_u32_dpp v229,v229,v229 row_bcast:15 row_mask:0xa bank_mask:0xf\n"
MF"v[92:95],v[212:215],a[108:111],v[92:95]\n"
WT"lgkmcnt(2)\n"
"v_max_u32_dpp v229,v229,v229 row_bcast:31 row_mask:0xc bank_mask:0xf\n"
MF"v[36:39],v[216:219],a[112:115],v[36:39]\n"
"v_readlane_b32 s62,v229,63\n"
MF"v[44:47],v[216:219],a[116:119],v[44:47]\n"
RD"v[208:211],v244 offset:4096\n"
"v_cmp_eq_u32 vcc,s62,v226\n"
MF"v[52:55],v[216:219],a[120:123],v[52:55]\n"
SA"m0,s37,16384\n"
"v_writelane_b32 v225,s62,6\n"
"v_cndmask_b32 v226,v226,v227,vcc\n"
MF"v[60:63],v[216:219],a[124:127],v[60:63]\n"
SA"s48,s39,98304\n"
"v_cndmask_b32_e64 v227,v227,0,vcc\n"
"v_mov_b32 v229,v226\n"
MF"v[68:71],v[220:223],a[112:115],v[68:71]\n"
BL"v234,s[12:15],s48 offen offset:0 lds\n"
MF"v[76:79],v[220:223],a[116:119],v[76:79]\n"
RD"v[212:215],v244 offset:6144\n"
"v_max_u32_dpp v229,v229,v229 row_shr:1 row_mask:0xf bank_mask:0xf\n"
MF"v[84:87],v[220:223],a[120:123],v[84:87]\n"
PK"v96,v96,v97\n"
"v_max_u32_dpp v229,v229,v229 row_shr:2 row_mask:0xf bank_mask:0xf\n"
MF"v[92:95],v[220:223],a[124:127],v[92:95]\n"
PK"v97,v98,v99\n"
"v_max_u32_dpp v229,v229,v229 row_shr:4 row_mask:0xf bank_mask:0xf\n"
MF"v[36:39],v[192:195],a[128:131],v[36:39]\n"
PK"v98,v100,v101\n"
"v_max_u32_dpp v229,v229,v229 row_shr:8 row_mask:0xf bank_mask:0xf\n"
MF"v[44:47],v[192:195],a[132:135],v[44:47]\n"
RD"v[216:219],v245 offset:4096\n"
"v_max_u32_dpp v229,v229,v229 row_bcast:15 row_mask:0xa bank_mask:0xf\n"
MF"v[52:55],v[192:195],a[136:139],v[52:55]\n"
BL"v235,s[12:15],s48 offen offset:1024 lds\n"
MF"v[60:63],v[192:195],a[140:143],v[60:63]\n"
"v_max_u32_dpp v229,v229,v229 row_bcast:31 row_mask:0xc bank_mask:0xf\n"
PK"v99,v102,v103\n"
MF"v[68:71],v[196:199],a[128:131],v[68:71]\n"
"v_readlane_b32 s62,v229,63\n"
"v_mov_b32 v229,v228\n"
MF"v[76:79],v[196:199],a[132:135],v[76:79]\n"
RD"v[220:223],v245 offset:6144\n"
"v_cmp_eq_u32 vcc,s62,v226\n"
MF"v[84:87],v[196:199],a[136:139],v[84:87]\n"
"v_writelane_b32 v225,s62,7\n"
"v_cndmask_b32 v226,v226,v227,vcc\n"
MF"v[92:95],v[196:199],a[140:143],v[92:95]\n"
WT"lgkmcnt(2)\n"
"v_cndmask_b32_e64 v227,v227,0,vcc\n"
"v_max_u32_dpp v229,v229,v229 row_shr:1 row_mask:0xf bank_mask:0xf\n"
MF"v[36:39],v[200:203],a[144:147],v[36:39]\n"
RD"v[192:195],v244 offset:24576\n"
"v_max_u32_dpp v229,v229,v229 row_shr:2 row_mask:0xf bank_mask:0xf\n"
MF"v[44:47],v[200:203],a[148:151],v[44:47]\n"
BL"v236,s[12:15],s48 offen offset:2048 lds\n"
MF"v[52:55],v[200:203],a[152:155],v[52:55]\n"
"v_max_u32_dpp v229,v229,v229 row_shr:4 row_mask:0xf bank_mask:0xf\n"
PK"v104,v104,v105\n"
MF"v[60:63],v[200:203],a[156:159],v[60:63]\n"
"v_max_u32_dpp v229,v229,v229 row_shr:8 row_mask:0xf bank_mask:0xf\n"
PK"v105,v106,v107\n"
MF"v[68:71],v[204:207],a[144:147],v[68:71]\n"
RD"v[196:199],v244 offset:26624\n"
"v_max_u32_dpp v229,v229,v229 row_bcast:15 row_mask:0xa bank_mask:0xf\n"
MF"v[76:79],v[204:207],a[148:151],v[76:79]\n"
BS"v[48:51],v250,s[8:11],s49 offen offset:2048 sc1\n"
MF"v[84:87],v[204:207],a[152:155],v[84:87]\n"
BL"v237,s[12:15],s48 offen offset:3072 lds\n"
MF"v[92:95],v[204:207],a[156:159],v[92:95]\n"
"v_max_u32_dpp v229,v229,v229 row_bcast:31 row_mask:0xc bank_mask:0xf\n"
PK"v106,v108,v109\n"
MF"v[36:39],v[208:211],a[160:163],v[36:39]\n"
RD"v[200:203],v245 offset:24576\n"
"v_readlane_b32 s63,v229,63\n"
MF"v[44:47],v[208:211],a[164:167],v[44:47]\n"
"s_cmp_gt_u32 s63,s62\n"
"s_cbranch_scc1 L1007_%=\n"
"L1008_%=:\n"
MF"v[52:55],v[208:211],a[168:171],v[52:55]\n"
SA"m0,s37,20480\n"
"v_lshrrev_b32 v229,2,v224\n"
PK"v107,v110,v111\n"
MF"v[60:63],v[208:211],a[172:175],v[60:63]\n"
SA"s48,s98,98304\n"
"ds_write_b32 v229,v225 offset:16384\n"
MF"v[68:71],v[212:215],a[160:163],v[68:71]\n"
RD"v[204:207],v245 offset:26624\n"
PK"v112,v112,v113\n"
MF"v[76:79],v[212:215],a[164:167],v[76:79]\n"
BL"v234,s[12:15],s48 offen offset:0 lds\n"
MF"v[84:87],v[212:215],a[168:171],v[84:87]\n"
PK"v113,v114,v115\n"
PK"v114,v116,v117\n"
MF"v[92:95],v[212:215],a[172:175],v[92:95]\n"
WT"lgkmcnt(2)\n"
RD"v[208:211],v244 offset:28672\n"
PK"v115,v118,v119\n"
MF"v[36:39],v[216:219],a[176:179],v[36:39]\n"
PK"v120,v120,v121\n"
PK"v121,v122,v123\n"
MF"v[44:47],v[216:219],a[180:183],v[44:47]\n"
PK"v122,v124,v125\n"
PK"v123,v126,v127\n"
MF"v[52:55],v[216:219],a[184:187],v[52:55]\n"
BL"v235,s[12:15],s48 offen offset:1024 lds\n"
MF"v[60:63],v[216:219],a[188:191],v[60:63]\n"
RD"v[212:215],v244 offset:30720\n"
MF"v[68:71],v[220:223],a[176:179],v[68:71]\n"
MF"v[76:79],v[220:223],a[180:183],v[76:79]\n"
MF"v[84:87],v[220:223],a[184:187],v[84:87]\n"
MF"v[92:95],v[220:223],a[188:191],v[92:95]\n"
RD"v[216:219],v245 offset:28672\n"
MF"v[36:39],v[192:195],a[192:195],v[36:39]\n"
BL"v236,s[12:15],s48 offen offset:2048 lds\n"
MF"v[44:47],v[192:195],a[196:199],v[44:47]\n"
MF"v[52:55],v[192:195],a[200:203],v[52:55]\n"
MF"v[60:63],v[192:195],a[204:207],v[60:63]\n"
RD"v[220:223],v245 offset:30720\n"
MF"v[68:71],v[196:199],a[192:195],v[68:71]\n"
MF"v[76:79],v[196:199],a[196:199],v[76:79]\n"
MF"v[84:87],v[196:199],a[200:203],v[84:87]\n"
BL"v237,s[12:15],s48 offen offset:3072 lds\n"
MF"v[92:95],v[196:199],a[204:207],v[92:95]\n"
MF"v[36:39],v[200:203],a[208:211],v[36:39]\n"
MF"v[44:47],v[200:203],a[212:215],v[44:47]\n"
MF"v[52:55],v[200:203],a[216:219],v[52:55]\n"
MF"v[60:63],v[200:203],a[220:223],v[60:63]\n"
WT"lgkmcnt(0)\n"
BS"v[56:59],v250,s[8:11],s49 offen offset:3072 sc1\n"
MF"v[68:71],v[204:207],a[208:211],v[68:71]\n"
"v_lshrrev_b32 v251,2,v224\n"
"v_and_b32 v251,28,v251\n"
MF"v[76:79],v[204:207],a[212:215],v[76:79]\n"
WT"vmcnt(25)\n"
"s_barrier\n"
MF"v[84:87],v[204:207],a[216:219],v[84:87]\n"
RD"v[192:195],v242 offset:8192\n"
MF"v[92:95],v[204:207],a[220:223],v[92:95]\n"
"ds_read_b32 v234,v251 offset:16384\n"
MF"v[36:39],v[208:211],a[224:227],v[36:39]\n"
MF"v[44:47],v[208:211],a[228:231],v[44:47]\n"
MF"v[52:55],v[208:211],a[232:235],v[52:55]\n"
RD"v[196:199],v242 offset:10240\n"
MF"v[60:63],v[208:211],a[236:239],v[60:63]\n"
MF"v[68:71],v[212:215],a[224:227],v[68:71]\n"
MF"v[76:79],v[212:215],a[228:231],v[76:79]\n"
MF"v[84:87],v[212:215],a[232:235],v[84:87]\n"
RD"v[200:203],v243 offset:8192\n"
MF"v[92:95],v[212:215],a[236:239],v[92:95]\n"
WT"lgkmcnt(2)\n"
"v_not_b32 v251,v234\n"
"v_bfe_u32 v252,v251,4,7\n"
MF"v[36:39],v[216:219],a[240:243],v[36:39]\n"
"v_mul_u32_u24 v252,20480,v252\n"
MF"v[44:47],v[216:219],a[244:247],v[44:47]\n"
"v_and_b32 v251,15,v251\n"
MF"v[52:55],v[216:219],a[248:251],v[52:55]\n"
RD"v[204:207],v243 offset:10240\n"
"v_lshl_add_u32 v251,v251,4,v252\n"
MF"v[60:63],v[216:219],a[252:255],v[60:63]\n"
"v_and_b32 v252,0x380,v224\n"
MF"v[68:71],v[220:223],a[240:243],v[68:71]\n"
"v_lshl_add_u32 v251,v252,1,v251\n"
MF"v[76:79],v[220:223],a[244:247],v[76:79]\n"
RD"v[208:211],v242 offset:12288\n"
"v_add_u32 v252,v252,v224\n"
MF"v[84:87],v[220:223],a[248:251],v[84:87]\n"
"s_lshl_b32 s92,s34,2\n"
MF"v[92:95],v[220:223],a[252:255],v[92:95]\n"
SA"s92,s92,s30\n"
MF"v[128:131],v[192:195],a[0:3],0\n"
"s_lshl_b32 s93,s92,11\n"
MF"v[136:139],v[192:195],a[4:7],0\n"
"s_cmp_lt_u32 s92,10\n"
RD"v[212:215],v242 offset:14336\n"
"s_cselect_b32 s93,s93,2130706432\n"
MF"v[144:147],v[192:195],a[8:11],0\n"
SA"s92,s93,65536\n"
MF"v[152:155],v[192:195],a[12:15],0\n"
WT"lgkmcnt(2)\n"
BL"v[100:103],v251,s[8:11],s92 offen sc1\n"
MF"v[132:135],v[196:199],a[0:3],0\n"
"s_mul_i32 s92,s35,20480\n"
PK"v36,v36,v37\n"
PK"v37,v38,v39\n"
MF"v[140:143],v[196:199],a[4:7],0\n"
SA"s92,s92,s93\n"
RD"v[216:219],v243 offset:12288\n"
PK"v38,v68,v69\n"
MF"v[148:151],v[196:199],a[8:11],0\n"
SA"s92,s92,2770944\n"
PK"v39,v70,v71\n"
MF"v[156:159],v[196:199],a[12:15],0\n"
MF"v[128:131],v[200:203],a[16:19],v[128:131]\n"
"s_mov_b32 s49,16384\n"
PK"v44,v44,v45\n"
PK"v45,v46,v47\n"
MF"v[136:139],v[200:203],a[20:23],v[136:139]\n"
RD"v[220:223],v243 offset:14336\n"
PK"v46,v76,v77\n"
MF"v[144:147],v[200:203],a[24:27],v[144:147]\n"
BS"v[64:67],v250,s[8:11],s49 offen offset:0 sc1\n"
MF"v[152:155],v[200:203],a[28:31],v[152:155]\n"
PK"v47,v78,v79\n"
MF"v[132:135],v[204:207],a[16:19],v[132:135]\n"
RD"v[192:195],v242 offset:32768\n"
MF"v[140:143],v[204:207],a[20:23],v[140:143]\n"
PK"v52,v52,v53\n"
PK"v53,v54,v55\n"
MF"v[148:151],v[204:207],a[24:27],v[148:151]\n"
PK"v54,v84,v85\n"
PK"v55,v86,v87\n"
MF"v[156:159],v[204:207],a[28:31],v[156:159]\n"
WT"lgkmcnt(2)\n"
MF"v[128:131],v[208:211],a[32:35],v[128:131]\n"
RD"v[196:199],v242 offset:34816\n"
MF"v[136:139],v[208:211],a[36:39],v[136:139]\n"
MF"v[144:147],v[208:211],a[40:43],v[144:147]\n"
PK"v60,v60,v61\n"
PK"v61,v62,v63\n"
MF"v[152:155],v[208:211],a[44:47],v[152:155]\n"
PK"v62,v92,v93\n"
PK"v63,v94,v95\n"
MF"v[132:135],v[212:215],a[32:35],v[132:135]\n"
SA"m0,s36,0\n"
RD"v[200:203],v243 offset:32768\n"
SA"s48,s60,s61\n"
MF"v[140:143],v[212:215],a[36:39],v[140:143]\n"
MF"v[148:151],v[212:215],a[40:43],v[148:151]\n"
BL"v238,s[20:23],s48 offen offset:0 lds\n"
MF"v[156:159],v[212:215],a[44:47],v[156:159]\n"
MF"v[128:131],v[216:219],a[48:51],v[128:131]\n"
RD"v[204:207],v243 offset:34816\n"
MF"v[136:139],v[216:219],a[52:55],v[136:139]\n"
MF"v[144:147],v[216:219],a[56:59],v[144:147]\n"
MF"v[152:155],v[216:219],a[60:63],v[152:155]\n"
WT"lgkmcnt(1)\n"
RD"v[208:211],v242 offset:36864\n"
MF"v[132:135],v[220:223],a[48:51],v[132:135]\n"
MF"v[140:143],v[220:223],a[52:55],v[140:143]\n"
MF"v[148:151],v[220:223],a[56:59],v[148:151]\n"
BL"v239,s[20:23],s48 offen offset:1024 lds\n"
MF"v[156:159],v[220:223],a[60:63],v[156:159]\n"
RD"v[212:215],v242 offset:38912\n"
MF"v[128:131],v[192:195],a[64:67],v[128:131]\n"
MF"v[136:139],v[192:195],a[68:71],v[136:139]\n"
MF"v[144:147],v[192:195],a[72:75],v[144:147]\n"
MF"v[152:155],v[192:195],a[76:79],v[152:155]\n"
RD"v[216:219],v243 offset:36864\n"
MF"v[132:135],v[196:199],a[64:67],v[132:135]\n"
MF"v[140:143],v[196:199],a[68:71],v[140:143]\n"
MF"v[148:151],v[196:199],a[72:75],v[148:151]\n"
BL"v240,s[20:23],s48 offen offset:2048 lds\n"
MF"v[156:159],v[196:199],a[76:79],v[156:159]\n"
RD"v[220:223],v243 offset:38912\n"
MF"v[128:131],v[200:203],a[80:83],v[128:131]\n"
MF"v[136:139],v[200:203],a[84:87],v[136:139]\n"
BS"v[72:75],v250,s[8:11],s49 offen offset:1024 sc1\n"
MF"v[144:147],v[200:203],a[88:91],v[144:147]\n"
RD"v[192:195],v244 offset:8192\n"
MF"v[152:155],v[200:203],a[92:95],v[152:155]\n"
WT"lgkmcnt(2)\n"
MF"v[132:135],v[204:207],a[80:83],v[132:135]\n"
MF"v[140:143],v[204:207],a[84:87],v[140:143]\n"
MF"v[148:151],v[204:207],a[88:91],v[148:151]\n"
SA"s61,s61,524288\n"
RD"v[196:199],v244 offset:10240\n"
MF"v[156:159],v[204:207],a[92:95],v[156:159]\n"
BL"v241,s[20:23],s48 offen offset:3072 lds\n"
MF"v[128:131],v[208:211],a[96:99],v[128:131]\n"
MF"v[136:139],v[208:211],a[100:103],v[136:139]\n"
MF"v[144:147],v[208:211],a[104:107],v[144:147]\n"
RD"v[200:203],v245 offset:8192\n"
MF"v[152:155],v[208:211],a[108:111],v[152:155]\n"
MF"v[132:135],v[212:215],a[96:99],v[132:135]\n"
MF"v[140:143],v[212:215],a[100:103],v[140:143]\n"
MF"v[148:151],v[212:215],a[104:107],v[148:151]\n"
RD"v[204:207],v245 offset:10240\n"
MF"v[156:159],v[212:215],a[108:111],v[156:159]\n"
MF"v[128:131],v[216:219],a[112:115],v[128:131]\n"
MF"v[136:139],v[216:219],a[116:119],v[136:139]\n"
RD"v[208:211],v244 offset:12288\n"
MF"v[144:147],v[216:219],a[120:123],v[144:147]\n"
MF"v[152:155],v[216:219],a[124:127],v[152:155]\n"
WT"lgkmcnt(2)\n"
MF"v[132:135],v[220:223],a[112:115],v[132:135]\n"
SA"m0,s38,65536\n"
SA"s48,s60,s61\n"
MF"v[140:143],v[220:223],a[116:119],v[140:143]\n"
RD"v[212:215],v244 offset:14336\n"
MF"v[148:151],v[220:223],a[120:123],v[148:151]\n"
BL"v238,s[20:23],s48 offen offset:0 lds\n"
MF"v[156:159],v[220:223],a[124:127],v[156:159]\n"
MF"v[128:131],v[192:195],a[128:131],v[128:131]\n"
MF"v[136:139],v[192:195],a[132:135],v[136:139]\n"
RD"v[216:219],v245 offset:12288\n"
MF"v[144:147],v[192:195],a[136:139],v[144:147]\n"
MF"v[152:155],v[192:195],a[140:143],v[152:155]\n"
MF"v[132:135],v[196:199],a[128:131],v[132:135]\n"
MF"v[140:143],v[196:199],a[132:135],v[140:143]\n"
RD"v[220:223],v245 offset:14336\n"
MF"v[148:151],v[196:199],a[136:139],v[148:151]\n"
BL"v239,s[20:23],s48 offen offset:1024 lds\n"
MF"v[156:159],v[196:199],a[140:143],v[156:159]\n"
MF"v[128:131],v[200:203],a[144:147],v[128:131]\n"
RD"v[192:195],v244 offset:32768\n"
MF"v[136:139],v[200:203],a[148:151],v[136:139]\n"
BS"v[80:83],v250,s[8:11],s49 offen offset:2048 sc1\n"
MF"v[144:147],v[200:203],a[152:155],v[144:147]\n"
MF"v[152:155],v[200:203],a[156:159],v[152:155]\n"
WT"lgkmcnt(2)\n"
MF"v[132:135],v[204:207],a[144:147],v[132:135]\n"
RD"v[196:199],v244 offset:34816\n"
MF"v[140:143],v[204:207],a[148:151],v[140:143]\n"
MF"v[148:151],v[204:207],a[152:155],v[148:151]\n"
BL"v240,s[20:23],s48 offen offset:2048 lds\n"
MF"v[156:159],v[204:207],a[156:159],v[156:159]\n"
MF"v[128:131],v[208:211],a[160:163],v[128:131]\n"
RD"v[200:203],v245 offset:32768\n"
MF"v[136:139],v[208:211],a[164:167],v[136:139]\n"
MF"v[144:147],v[208:211],a[168:171],v[144:147]\n"
MF"v[152:155],v[208:211],a[172:175],v[152:155]\n"
MF"v[132:135],v[212:215],a[160:163],v[132:135]\n"
RD"v[204:207],v245 offset:34816\n"
MF"v[140:143],v[212:215],a[164:167],v[140:143]\n"
MF"v[148:151],v[212:215],a[168:171],v[148:151]\n"
SA"s61,s61,524288\n"
BL"v241,s[20:23],s48 offen offset:3072 lds\n"
MF"v[156:159],v[212:215],a[172:175],v[156:159]\n"
"s_cmp_eq_u32 s61,5242880\n"
RD"v[208:211],v244 offset:36864\n"
"s_cselect_b32 s61,0,s61\n"
MF"v[128:131],v[216:219],a[176:179],v[128:131]\n"
MF"v[136:139],v[216:219],a[180:183],v[136:139]\n"
MF"v[144:147],v[216:219],a[184:187],v[144:147]\n"
MF"v[152:155],v[216:219],a[188:191],v[152:155]\n"
WT"lgkmcnt(1)\n"
RD"v[212:215],v244 offset:38912\n"
MF"v[132:135],v[220:223],a[176:179],v[132:135]\n"
SA"m0,s38,114688\n"
SA"s48,s60,s61\n"
MF"v[140:143],v[220:223],a[180:183],v[140:143]\n"
MF"v[148:151],v[220:223],a[184:187],v[148:151]\n"
BL"v238,s[20:23],s48 offen offset:0 lds\n"
MF"v[156:159],v[220:223],a[188:191],v[156:159]\n"
RD"v[216:219],v245 offset:36864\n"
MF"v[128:131],v[192:195],a[192:195],v[128:131]\n"
MF"v[136:139],v[192:195],a[196:199],v[136:139]\n"
MF"v[144:147],v[192:195],a[200:203],v[144:147]\n"
MF"v[152:155],v[192:195],a[204:207],v[152:155]\n"
RD"v[220:223],v245 offset:38912\n"
MF"v[132:135],v[196:199],a[192:195],v[132:135]\n"
MF"v[140:143],v[196:199],a[196:199],v[140:143]\n"
MF"v[148:151],v[196:199],a[200:203],v[148:151]\n"
BL"v239,s[20:23],s48 offen offset:1024 lds\n"
MF"v[156:159],v[196:199],a[204:207],v[156:159]\n"
BS"v[88:91],v250,s[8:11],s49 offen offset:3072 sc1\n"
MF"v[128:131],v[200:203],a[208:211],v[128:131]\n"
WT"vmcnt(14)\n"
BS"v[100:103],v252,s[8:11],s92 offen sc1\n"
MF"v[136:139],v[200:203],a[212:215],v[136:139]\n"
MF"v[144:147],v[200:203],a[216:219],v[144:147]\n"
MF"v[152:155],v[200:203],a[220:223],v[152:155]\n"
MF"v[132:135],v[204:207],a[208:211],v[132:135]\n"
MF"v[140:143],v[204:207],a[212:215],v[140:143]\n"
WT"lgkmcnt(0)\n"
"s_barrier\n"
MF"v[148:151],v[204:207],a[216:219],v[148:151]\n"
RD"v[192:195],v242 offset:16384\n"
MF"v[156:159],v[204:207],a[220:223],v[156:159]\n"
BL"v240,s[20:23],s48 offen offset:2048 lds\n"
MF"v[128:131],v[208:211],a[224:227],v[128:131]\n"
MF"v[136:139],v[208:211],a[228:231],v[136:139]\n"
MF"v[144:147],v[208:211],a[232:235],v[144:147]\n"
RD"v[196:199],v242 offset:18432\n"
MF"v[152:155],v[208:211],a[236:239],v[152:155]\n"
MF"v[132:135],v[212:215],a[224:227],v[132:135]\n"
MF"v[140:143],v[212:215],a[228:231],v[140:143]\n"
MF"v[148:151],v[212:215],a[232:235],v[148:151]\n"
SA"s61,s61,524288\n"
RD"v[200:203],v243 offset:16384\n"
MF"v[156:159],v[212:215],a[236:239],v[156:159]\n"
BL"v241,s[20:23],s48 offen offset:3072 lds\n"
MF"v[128:131],v[216:219],a[240:243],v[128:131]\n"
"s_lshl_b32 s50,s34,3\n"
MF"v[136:139],v[216:219],a[244:247],v[136:139]\n"
SA"s51,s50,s30\n"
MF"v[144:147],v[216:219],a[248:251],v[144:147]\n"
"s_mul_i32 s51,s51,20480\n"
RD"v[204:207],v243 offset:18432\n"
MF"v[152:155],v[216:219],a[252:255],v[152:155]\n"
SA"s56,s51,65536\n"
MF"v[132:135],v[220:223],a[240:243],v[132:135]\n"
SA"m0,s36,16384\n"
SA"s48,s60,s61\n"
SA"s57,s56,81920\n"
MF"v[140:143],v[220:223],a[244:247],v[140:143]\n"
"s_sub_u32 s51,s30,s50\n"
RD"v[208:211],v242 offset:20480\n"
MF"v[148:151],v[220:223],a[248:251],v[148:151]\n"
SA"s51,s51,120\n"
BL"v238,s[20:23],s48 offen offset:0 lds\n"
MF"v[156:159],v[220:223],a[252:255],v[156:159]\n"
WT"lgkmcnt(2)\n"
"s_mul_i32 s51,s51,20480\n"
MF"v[0:3],v[192:195],a[0:3],0\n"
SA"s58,s51,65536\n"
"s_lshl_b32 s99,s99,12\n"
MF"v[16:19],v[192:195],a[4:7],0\n"
SA"s59,s58,81920\n"
RD"v[212:215],v242 offset:22528\n"
MF"v[32:35],v[192:195],a[8:11],0\n"
"v_add_u32 v235,s99,v224\n"
MF"v[48:51],v[192:195],a[12:15],0\n"
PK"v64,v128,v129\n"
PK"v65,v130,v131\n"
MF"v[8:11],v[196:199],a[0:3],0\n"
"s_mov_b32 s49,24576\n"
PK"v66,v132,v133\n"
PK"v67,v134,v135\n"
MF"v[24:27],v[196:199],a[4:7],0\n"
RD"v[216:219],v243 offset:20480\n"
MF"v[40:43],v[196:199],a[8:11],0\n"
BS"v[96:99],v250,s[8:11],s49 offen offset:0 sc1\n"
MF"v[56:59],v[196:199],a[12:15],0\n"
BL"v239,s[20:23],s48 offen offset:1024 lds\n"
MF"v[0:3],v[200:203],a[16:19],v[0:3]\n"
BL"a[0:3],v235,s[8:11],s56 offen offset:0 sc1\n"
MF"v[16:19],v[200:203],a[20:23],v[16:19]\n"
RD"v[220:223],v243 offset:22528\n"
PK"v68,v136,v137\n"
MF"v[32:35],v[200:203],a[24:27],v[32:35]\n"
PK"v69,v138,v139\n"
PK"v70,v140,v141\n"
MF"v[48:51],v[200:203],a[28:31],v[48:51]\n"
WT"lgkmcnt(2)\n"
PK"v71,v142,v143\n"
MF"v[8:11],v[204:207],a[16:19],v[8:11]\n"
RD"v[192:195],v242 offset:40960\n"
MF"v[24:27],v[204:207],a[20:23],v[24:27]\n"
PK"v72,v144,v145\n"
PK"v73,v146,v147\n"
MF"v[40:43],v[204:207],a[24:27],v[40:43]\n"
BL"v240,s[20:23],s48 offen offset:2048 lds\n"
MF"v[56:59],v[204:207],a[28:31],v[56:59]\n"
BL"a[4:7],v235,s[8:11],s57 offen offset:0 sc1\n"
MF"v[0:3],v[208:211],a[32:35],v[0:3]\n"
RD"v[196:199],v242 offset:43008\n"
PK"v74,v148,v149\n"
MF"v[16:19],v[208:211],a[36:39],v[16:19]\n"
PK"v75,v150,v151\n"
MF"v[32:35],v[208:211],a[40:43],v[32:35]\n"
PK"v76,v152,v153\n"
PK"v77,v154,v155\n"
MF"v[48:51],v[208:211],a[44:47],v[48:51]\n"
PK"v78,v156,v157\n"
PK"v79,v158,v159\n"
MF"v[8:11],v[212:215],a[32:35],v[8:11]\n"
RD"v[200:203],v243 offset:40960\n"
MF"v[24:27],v[212:215],a[36:39],v[24:27]\n"
BL"a[8:11],v235,s[8:11],s58 offen offset:0 sc1\n"
MF"v[40:43],v[212:215],a[40:43],v[40:43]\n"
SA"s61,s61,524288\n"
BL"v241,s[20:23],s48 offen offset:3072 lds\n"
MF"v[56:59],v[212:215],a[44:47],v[56:59]\n"
WT"lgkmcnt(1)\n"
"s_cmp_eq_u32 s61,5242880\n"
"s_cselect_b32 s61,0,s61\n"
MF"v[0:3],v[216:219],a[48:51],v[0:3]\n"
RD"v[204:207],v243 offset:43008\n"
MF"v[16:19],v[216:219],a[52:55],v[16:19]\n"
MF"v[32:35],v[216:219],a[56:59],v[32:35]\n"
BL"a[12:15],v235,s[8:11],s59 offen offset:0 sc1\n"
MF"v[48:51],v[216:219],a[60:63],v[48:51]\n"
RD"v[208:211],v242 offset:45056\n"
MF"v[8:11],v[220:223],a[48:51],v[8:11]\n"
MF"v[24:27],v[220:223],a[52:55],v[24:27]\n"
MF"v[40:43],v[220:223],a[56:59],v[40:43]\n"
MF"v[56:59],v[220:223],a[60:63],v[56:59]\n"
RD"v[212:215],v242 offset:47104\n"
MF"v[0:3],v[192:195],a[64:67],v[0:3]\n"
BL"a[16:19],v235,s[8:11],s56 offen offset:1024 sc1\n"
MF"v[16:19],v[192:195],a[68:71],v[16:19]\n"
MF"v[32:35],v[192:195],a[72:75],v[32:35]\n"
MF"v[48:51],v[192:195],a[76:79],v[48:51]\n"
RD"v[216:219],v243 offset:45056\n"
MF"v[8:11],v[196:199],a[64:67],v[8:11]\n"
MF"v[24:27],v[196:199],a[68:71],v[24:27]\n"
BS"v[104:107],v250,s[8:11],s49 offen offset:1024 sc1\n"
MF"v[40:43],v[196:199],a[72:75],v[40:43]\n"
BL"a[20:23],v235,s[8:11],s57 offen offset:1024 sc1\n"
MF"v[56:59],v[196:199],a[76:79],v[56:59]\n"
WT"lgkmcnt(1)\n"
RD"v[220:223],v243 offset:47104\n"
MF"v[0:3],v[200:203],a[80:83],v[0:3]\n"
MF"v[16:19],v[200:203],a[84:87],v[16:19]\n"
MF"v[32:35],v[200:203],a[88:91],v[32:35]\n"
RD"v[192:195],v244 offset:16384\n"
MF"v[48:51],v[200:203],a[92:95],v[48:51]\n"
MF"v[8:11],v[204:207],a[80:83],v[8:11]\n"
BL"a[24:27],v235,s[8:11],s58 offen offset:1024 sc1\n"
MF"v[24:27],v[204:207],a[84:87],v[24:27]\n"
MF"v[40:43],v[204:207],a[88:91],v[40:43]\n"
RD"v[196:199],v244 offset:18432\n"
MF"v[56:59],v[204:207],a[92:95],v[56:59]\n"
MF"v[0:3],v[208:211],a[96:99],v[0:3]\n"
MF"v[16:19],v[208:211],a[100:103],v[16:19]\n"
MF"v[32:35],v[208:211],a[104:107],v[32:35]\n"
RD"v[200:203],v245 offset:16384\n"
MF"v[48:51],v[208:211],a[108:111],v[48:51]\n"
BL"a[28:31],v235,s[8:11],s59 offen offset:1024 sc1\n"
MF"v[8:11],v[212:215],a[96:99],v[8:11]\n"
MF"v[24:27],v[212:215],a[100:103],v[24:27]\n"
MF"v[40:43],v[212:215],a[104:107],v[40:43]\n"
RD"v[204:207],v245 offset:18432\n"
MF"v[56:59],v[212:215],a[108:111],v[56:59]\n"
WT"lgkmcnt(2)\n"
MF"v[0:3],v[216:219],a[112:115],v[0:3]\n"
BL"a[32:35],v235,s[8:11],s56 offen offset:2048 sc1\n"
MF"v[16:19],v[216:219],a[116:119],v[16:19]\n"
RD"v[208:211],v244 offset:20480\n"
MF"v[32:35],v[216:219],a[120:123],v[32:35]\n"
MF"v[48:51],v[216:219],a[124:127],v[48:51]\n"
MF"v[8:11],v[220:223],a[112:115],v[8:11]\n"
MF"v[24:27],v[220:223],a[116:119],v[24:27]\n"
RD"v[212:215],v244 offset:22528\n"
MF"v[40:43],v[220:223],a[120:123],v[40:43]\n"
BL"a[36:39],v235,s[8:11],s57 offen offset:2048 sc1\n"
MF"v[56:59],v[220:223],a[124:127],v[56:59]\n"
MF"v[0:3],v[192:195],a[128:131],v[0:3]\n"
MF"v[16:19],v[192:195],a[132:135],v[16:19]\n"
RD"v[216:219],v245 offset:20480\n"
MF"v[32:35],v[192:195],a[136:139],v[32:35]\n"
MF"v[48:51],v[192:195],a[140:143],v[48:51]\n"
MF"v[8:11],v[196:199],a[128:131],v[8:11]\n"
BS"v[112:115],v250,s[8:11],s49 offen offset:2048 sc1\n"
MF"v[24:27],v[196:199],a[132:135],v[24:27]\n"
BL"a[40:43],v235,s[8:11],s58 offen offset:2048 sc1\n"
MF"v[40:43],v[196:199],a[136:139],v[40:43]\n"
RD"v[220:223],v245 offset:22528\n"
MF"v[56:59],v[196:199],a[140:143],v[56:59]\n"
WT"lgkmcnt(2)\n"
MF"v[0:3],v[200:203],a[144:147],v[0:3]\n"
RD"v[192:195],v244 offset:40960\n"
MF"v[16:19],v[200:203],a[148:151],v[16:19]\n"
MF"v[32:35],v[200:203],a[152:155],v[32:35]\n"
BL"a[44:47],v235,s[8:11],s59 offen offset:2048 sc1\n"
MF"v[48:51],v[200:203],a[156:159],v[48:51]\n"
MF"v[8:11],v[204:207],a[144:147],v[8:11]\n"
RD"v[196:199],v244 offset:43008\n"
MF"v[24:27],v[204:207],a[148:151],v[24:27]\n"
MF"v[40:43],v[204:207],a[152:155],v[40:43]\n"
MF"v[56:59],v[204:207],a[156:159],v[56:59]\n"
MF"v[0:3],v[208:211],a[160:163],v[0:3]\n"
"s_sub_u32 s53,s37,65536\n"
RD"v[200:203],v245 offset:40960\n"
MF"v[16:19],v[208:211],a[164:167],v[16:19]\n"
BL"a[48:51],v235,s[8:11],s56 offen offset:3072 sc1\n"
MF"v[32:35],v[208:211],a[168:171],v[32:35]\n"
"v_subrev_u32 v246,s53,v246\n"
"v_subrev_u32 v247,s53,v247\n"
MF"v[48:51],v[208:211],a[172:175],v[48:51]\n"
"v_subrev_u32 v248,s53,v248\n"
"v_subrev_u32 v249,s53,v249\n"
MF"v[8:11],v[212:215],a[160:163],v[8:11]\n"
RD"v[204:207],v245 offset:43008\n"
"v_subrev_u32 v236,65536,v246\n"
MF"v[24:27],v[212:215],a[164:167],v[24:27]\n"
"v_subrev_u32 v237,65536,v247\n"
"v_subrev_u32 v228,65536,v248\n"
MF"v[40:43],v[212:215],a[168:171],v[40:43]\n"
BL"a[52:55],v235,s[8:11],s57 offen offset:3072 sc1\n"
MF"v[56:59],v[212:215],a[172:175],v[56:59]\n"
WT"lgkmcnt(1)\n"
RD"v[208:211],v244 offset:45056\n"
"v_subrev_u32 v229,65536,v249\n"
MF"v[0:3],v[216:219],a[176:179],v[0:3]\n"
MF"v[16:19],v[216:219],a[180:183],v[16:19]\n"
MF"v[32:35],v[216:219],a[184:187],v[32:35]\n"
MF"v[48:51],v[216:219],a[188:191],v[48:51]\n"
RD"v[212:215],v244 offset:47104\n"
MF"v[8:11],v[220:223],a[176:179],v[8:11]\n"
BL"a[56:59],v235,s[8:11],s58 offen offset:3072 sc1\n"
MF"v[24:27],v[220:223],a[180:183],v[24:27]\n"
MF"v[40:43],v[220:223],a[184:187],v[40:43]\n"
MF"v[56:59],v[220:223],a[188:191],v[56:59]\n"
RD"v[216:219],v245 offset:45056\n"
MF"v[0:3],v[192:195],a[192:195],v[0:3]\n"
SA"s99,s99,4096\n"
MF"v[16:19],v[192:195],a[196:199],v[16:19]\n"
"s_cmp_eq_u32 s99,20480\n"
"s_cselect_b32 s99,0,s99\n"
MF"v[32:35],v[192:195],a[200:203],v[32:35]\n"
BL"a[60:63],v235,s[8:11],s59 offen offset:3072 sc1\n"
MF"v[48:51],v[192:195],a[204:207],v[48:51]\n"
RD"v[220:223],v245 offset:47104\n"
MF"v[8:11],v[196:199],a[192:195],v[8:11]\n"
BS"v[120:123],v250,s[8:11],s49 offen offset:3072 sc1\n"
MF"v[24:27],v[196:199],a[196:199],v[24:27]\n"
MF"v[40:43],v[196:199],a[200:203],v[40:43]\n"
MF"v[56:59],v[196:199],a[204:207],v[56:59]\n"
"v_add_u32 v235,s99,v224\n"
MF"v[0:3],v[200:203],a[208:211],v[0:3]\n"
BL"a[64:67],v235,s[8:11],s56 offen offset:0 sc1\n"
MF"v[16:19],v[200:203],a[212:215],v[16:19]\n"
MF"v[32:35],v[200:203],a[216:219],v[32:35]\n"
MF"v[48:51],v[200:203],a[220:223],v[48:51]\n"
WT"lgkmcnt(0)\n"
MF"v[8:11],v[204:207],a[208:211],v[8:11]\n"
MF"v[24:27],v[204:207],a[212:215],v[24:27]\n"
"s_barrier\n"
MF"v[40:43],v[204:207],a[216:219],v[40:43]\n"
RT"a[128:129],v236 offset:49152\n"
MF"v[56:59],v[204:207],a[220:223],v[56:59]\n"
BL"a[68:71],v235,s[8:11],s57 offen offset:0 sc1\n"
MF"v[0:3],v[208:211],a[224:227],v[0:3]\n"
SA"m0,s38,73728\n"
RT"a[130:131],v236 offset:49664\n"
SA"s48,s60,s61\n"
MF"v[16:19],v[208:211],a[228:231],v[16:19]\n"
MF"v[32:35],v[208:211],a[232:235],v[32:35]\n"
RT"a[132:133],v237 offset:49152\n"
"v_add_u32 v242,49152,v246\n"
MF"v[48:51],v[208:211],a[236:239],v[48:51]\n"
BL"v238,s[20:23],s48 offen offset:0 lds\n"
MF"v[8:11],v[212:215],a[224:227],v[8:11]\n"
RT"a[134:135],v237 offset:49664\n"
"v_add_u32 v243,49152,v247\n"
MF"v[24:27],v[212:215],a[228:231],v[24:27]\n"
BL"a[72:75],v235,s[8:11],s58 offen offset:0 sc1\n"
MF"v[40:43],v[212:215],a[232:235],v[40:43]\n"
RT"a[136:137],v228 offset:49152\n"
"v_add_u32 v244,49152,v248\n"
MF"v[56:59],v[212:215],a[236:239],v[56:59]\n"
"v_add_u32 v245,49152,v249\n"
MF"v[0:3],v[216:219],a[240:243],v[0:3]\n"
RT"a[138:139],v228 offset:49664\n"
MF"v[16:19],v[216:219],a[244:247],v[16:19]\n"
MF"v[32:35],v[216:219],a[248:251],v[32:35]\n"
RT"a[140:141],v229 offset:49152\n"
MF"v[48:51],v[216:219],a[252:255],v[48:51]\n"
BL"v239,s[20:23],s48 offen offset:1024 lds\n"
MF"v[8:11],v[220:223],a[240:243],v[8:11]\n"
BL"a[76:79],v235,s[8:11],s59 offen offset:0 sc1\n"
MF"v[24:27],v[220:223],a[244:247],v[24:27]\n"
RT"a[142:143],v229 offset:49664\n"
MF"v[40:43],v[220:223],a[248:251],v[40:43]\n"
RT"a[144:145],v236 offset:53248\n"
MF"v[56:59],v[220:223],a[252:255],v[56:59]\n"
WT"vmcnt(26) lgkmcnt(3)\n"
MF"v[80:83],a[128:131],a[0:3],0\n"
WT"vmcnt(24)\n"
RT"a[146:147],v236 offset:53760\n"
MF"v[88:91],a[128:131],a[4:7],0\n"
WT"vmcnt(23)\n"
BL"a[80:83],v235,s[8:11],s56 offen offset:1024 sc1\n"
MF"v[96:99],a[128:131],a[8:11],0\n"
WT"vmcnt(22)\n"
RT"a[148:149],v237 offset:53248\n"
MF"v[104:107],a[128:131],a[12:15],0\n"
BL"v240,s[20:23],s48 offen offset:2048 lds\n"
MF"v[84:87],a[132:135],a[0:3],0\n"
RT"a[150:151],v237 offset:53760\n"
PK"v0,v0,v1\n"
MF"v[92:95],a[132:135],a[4:7],0\n"
RT"a[152:153],v228 offset:53248\n"
PK"v1,v2,v3\n"
MF"v[100:103],a[132:135],a[8:11],0\n"
PK"v2,v8,v9\n"
PK"v3,v10,v11\n"
MF"v[108:111],a[132:135],a[12:15],0\n"
RT"a[154:155],v228 offset:53760\n"
MF"v[112:115],a[136:139],a[0:3],0\n"
BL"a[84:87],v235,s[8:11],s57 offen offset:1024 sc1\n"
MF"v[120:123],a[136:139],a[4:7],0\n"
RT"a[156:157],v229 offset:53248\n"
PK"v16,v16,v17\n"
MF"v[128:131],a[136:139],a[8:11],0\n"
SA"s61,s61,524288\n"
BL"v241,s[20:23],s48 offen offset:3072 lds\n"
MF"v[136:139],a[136:139],a[12:15],0\n"
WT"lgkmcnt(3)\n"
RT"a[158:159],v229 offset:53760\n"
PK"v17,v18,v19\n"
MF"v[116:119],a[140:143],a[0:3],0\n"
PK"v18,v24,v25\n"
PK"v19,v26,v27\n"
MF"v[124:127],a[140:143],a[4:7],0\n"
RT"a[160:161],v236 offset:57344\n"
PK"v32,v32,v33\n"
MF"v[132:135],a[140:143],a[8:11],0\n"
BL"a[88:91],v235,s[8:11],s58 offen offset:1024 sc1\n"
MF"v[140:143],a[140:143],a[12:15],0\n"
RT"a[162:163],v236 offset:57856\n"
PK"v33,v34,v35\n"
MF"v[144:147],a[144:147],a[0:3],0\n"
PK"v34,v40,v41\n"
PK"v35,v42,v43\n"
MF"v[152:155],a[144:147],a[4:7],0\n"
RT"a[164:165],v237 offset:57344\n"
MF"v[160:163],a[144:147],a[8:11],0\n"
PK"v48,v48,v49\n"
PK"v49,v50,v51\n"
MF"v[168:171],a[144:147],a[12:15],0\n"
RT"a[166:167],v237 offset:57856\n"
PK"v50,v56,v57\n"
MF"v[148:151],a[148:151],a[0:3],0\n"
PK"v51,v58,v59\n"
MF"v[156:159],a[148:151],a[4:7],0\n"
RT"a[168:169],v228 offset:57344\n"
MF"v[164:167],a[148:151],a[8:11],0\n"
BL"a[92:95],v235,s[8:11],s59 offen offset:1024 sc1\n"
MF"v[172:175],a[148:151],a[12:15],0\n"
WT"lgkmcnt(3)\n"
RT"a[170:171],v228 offset:57856\n"
MF"v[176:179],a[152:155],a[0:3],0\n"
"s_mov_b32 s49,262144\n"
RT"a[172:173],v229 offset:57344\n"
MF"v[184:187],a[152:155],a[4:7],0\n"
MF"v[192:195],a[152:155],a[8:11],0\n"
RT"a[174:175],v229 offset:57856\n"
MF"v[200:203],a[152:155],a[12:15],0\n"
BS"v[4:7],v250,s[8:11],s49 offen offset:0 sc1\n"
MF"v[180:183],a[156:159],a[0:3],0\n"
RT"a[176:177],v236 offset:61440\n"
MF"v[188:191],a[156:159],a[4:7],0\n"
BL"a[96:99],v235,s[8:11],s56 offen offset:2048 sc1\n"
MF"v[196:199],a[156:159],a[8:11],0\n"
RT"a[178:179],v236 offset:61952\n"
MF"v[204:207],a[156:159],a[12:15],0\n"
WT"vmcnt(28)\n"
MF"v[80:83],a[160:163],a[16:19],v[80:83]\n"
WT"vmcnt(26)\n"
RT"a[180:181],v237 offset:61440\n"
MF"v[88:91],a[160:163],a[20:23],v[88:91]\n"
WT"vmcnt(25)\n"
MF"v[96:99],a[160:163],a[24:27],v[96:99]\n"
WT"vmcnt(24)\n"
RT"a[182:183],v237 offset:61952\n"
SA"m0,s38,122880\n"
MF"v[104:107],a[160:163],a[28:31],v[104:107]\n"
WT"lgkmcnt(3)\n"
BL"a[100:103],v235,s[8:11],s57 offen offset:2048 sc1\n"
MF"v[84:87],a[164:167],a[16:19],v[84:87]\n"
SA"s48,s60,s61\n"
RT"a[184:185],v228 offset:61440\n"
"s_mov_b32 s88,2048\n"
MF"v[92:95],a[164:167],a[20:23],v[92:95]\n"
BL"v238,s[20:23],s48 offen offset:0 lds\n"
MF"v[100:103],a[164:167],a[24:27],v[100:103]\n"
RT"a[186:187],v228 offset:61952\n"
MF"v[108:111],a[164:167],a[28:31],v[108:111]\n"
MF"v[112:115],a[168:171],a[16:19],v[112:115]\n"
RT"a[188:189],v229 offset:61440\n"
MF"v[120:123],a[168:171],a[20:23],v[120:123]\n"
MF"v[128:131],a[168:171],a[24:27],v[128:131]\n"
RT"a[190:191],v229 offset:61952\n"
MF"v[136:139],a[168:171],a[28:31],v[136:139]\n"
BL"a[104:107],v235,s[8:11],s58 offen offset:2048 sc1\n"
MF"v[116:119],a[172:175],a[16:19],v[116:119]\n"
RT"a[128:129],v236 offset:32768\n"
MF"v[124:127],a[172:175],a[20:23],v[124:127]\n"
BL"v239,s[20:23],s48 offen offset:1024 lds\n"
MF"v[132:135],a[172:175],a[24:27],v[132:135]\n"
RT"a[130:131],v236 offset:33280\n"
MF"v[140:143],a[172:175],a[28:31],v[140:143]\n"
WT"lgkmcnt(3)\n"
RT"a[132:133],v237 offset:32768\n"
MF"v[144:147],a[176:179],a[16:19],v[144:147]\n"
MF"v[152:155],a[176:179],a[20:23],v[152:155]\n"
RT"a[134:135],v237 offset:33280\n"
MF"v[160:163],a[176:179],a[24:27],v[160:163]\n"
BL"a[108:111],v235,s[8:11],s59 offen offset:2048 sc1\n"
MF"v[168:171],a[176:179],a[28:31],v[168:171]\n"
RT"a[136:137],v228 offset:32768\n"
MF"v[148:151],a[180:183],a[16:19],v[148:151]\n"
BL"v240,s[20:23],s48 offen offset:2048 lds\n"
MF"v[156:159],a[180:183],a[20:23],v[156:159]\n"
RT"a[138:139],v228 offset:33280\n"
MF"v[164:167],a[180:183],a[24:27],v[164:167]\n"
MF"v[172:175],a[180:183],a[28:31],v[172:175]\n"
RT"a[140:141],v229 offset:32768\n"
MF"v[176:179],a[184:187],a[16:19],v[176:179]\n"
BL"a[112:115],v235,s[8:11],s56 offen offset:3072 sc1\n"
MF"v[184:187],a[184:187],a[20:23],v[184:187]\n"
RT"a[142:143],v229 offset:33280\n"
MF"v[192:195],a[184:187],a[24:27],v[192:195]\n"
MF"v[200:203],a[184:187],a[28:31],v[200:203]\n"
WT"lgkmcnt(3)\n"
RT"a[144:145],v236 offset:36864\n"
MF"v[180:183],a[188:191],a[16:19],v[180:183]\n"
SA"s61,s61,524288\n"
BS"v[12:15],v250,s[8:11],s49 offen offset:1024 sc1\n"
MF"v[188:191],a[188:191],a[20:23],v[188:191]\n"
"s_cmp_eq_u32 s61,5242880\n"
BL"v241,s[20:23],s48 offen offset:3072 lds\n"
MF"v[196:199],a[188:191],a[24:27],v[196:199]\n"
"s_cselect_b32 s61,0,s61\n"
RT"a[146:147],v236 offset:37376\n"
MF"v[204:207],a[188:191],a[28:31],v[204:207]\n"
WT"vmcnt(32)\n"
RT"a[148:149],v237 offset:36864\n"
MF"v[80:83],a[128:131],a[32:35],v[80:83]\n"
WT"vmcnt(31)\n"
BL"a[116:119],v235,s[8:11],s57 offen offset:3072 sc1\n"
MF"v[88:91],a[128:131],a[36:39],v[88:91]\n"
WT"vmcnt(30)\n"
RT"a[150:151],v237 offset:37376\n"
MF"v[96:99],a[128:131],a[40:43],v[96:99]\n"
WT"vmcnt(29)\n"
RT"a[152:153],v228 offset:36864\n"
MF"v[104:107],a[128:131],a[44:47],v[104:107]\n"
MF"v[84:87],a[132:135],a[32:35],v[84:87]\n"
RT"a[154:155],v228 offset:37376\n"
MF"v[92:95],a[132:135],a[36:39],v[92:95]\n"
MF"v[100:103],a[132:135],a[40:43],v[100:103]\n"
RT"a[156:157],v229 offset:36864\n"
MF"v[108:111],a[132:135],a[44:47],v[108:111]\n"
WT"lgkmcnt(4)\n"
BL"a[120:123],v235,s[8:11],s58 offen offset:3072 sc1\n"
MF"v[112:115],a[136:139],a[32:35],v[112:115]\n"
RT"a[158:159],v229 offset:37376\n"
MF"v[120:123],a[136:139],a[36:39],v[120:123]\n"
MF"v[128:131],a[136:139],a[40:43],v[128:131]\n"
RT"a[160:161],v236 offset:40960\n"
MF"v[136:139],a[136:139],a[44:47],v[136:139]\n"
MF"v[116:119],a[140:143],a[32:35],v[116:119]\n"
SA"s99,s99,4096\n"
RT"a[162:163],v236 offset:41472\n"
MF"v[124:127],a[140:143],a[36:39],v[124:127]\n"
"s_cmp_eq_u32 s99,20480\n"
BL"a[124:127],v235,s[8:11],s59 offen offset:3072 sc1\n"
MF"v[132:135],a[140:143],a[40:43],v[132:135]\n"
"s_cselect_b32 s99,0,s99\n"
RT"a[164:165],v237 offset:40960\n"
MF"v[140:143],a[140:143],a[44:47],v[140:143]\n"
MF"v[144:147],a[144:147],a[32:35],v[144:147]\n"
RT"a[166:167],v237 offset:41472\n"
MF"v[152:155],a[144:147],a[36:39],v[152:155]\n"
MF"v[160:163],a[144:147],a[40:43],v[160:163]\n"
RT"a[168:169],v228 offset:40960\n"
MF"v[168:171],a[144:147],a[44:47],v[168:171]\n"
WT"lgkmcnt(3)\n"
"v_add_u32 v235,s99,v224\n"
MF"v[148:151],a[148:151],a[32:35],v[148:151]\n"
RT"a[170:171],v228 offset:41472\n"
MF"v[156:159],a[148:151],a[36:39],v[156:159]\n"
BL"a[0:3],v235,s[8:11],s56 offen offset:0 sc1\n"
MF"v[164:167],a[148:151],a[40:43],v[164:167]\n"
RT"a[172:173],v229 offset:40960\n"
MF"v[172:175],a[148:151],a[44:47],v[172:175]\n"
RT"a[174:175],v229 offset:41472\n"
MF"v[176:179],a[152:155],a[32:35],v[176:179]\n"
MF"v[184:187],a[152:155],a[36:39],v[184:187]\n"
RT"a[176:177],v236 offset:45056\n"
MF"v[192:195],a[152:155],a[40:43],v[192:195]\n"
MF"v[200:203],a[152:155],a[44:47],v[200:203]\n"
RT"a[178:179],v236 offset:45568\n"
MF"v[180:183],a[156:159],a[32:35],v[180:183]\n"
BL"a[4:7],v235,s[8:11],s57 offen offset:0 sc1\n"
MF"v[188:191],a[156:159],a[36:39],v[188:191]\n"
RT"a[180:181],v237 offset:45056\n"
MF"v[196:199],a[156:159],a[40:43],v[196:199]\n"
BS"v[20:23],v250,s[8:11],s49 offen offset:2048 sc1\n"
MF"v[204:207],a[156:159],a[44:47],v[204:207]\n"
RT"a[182:183],v237 offset:45568\n"
"LQ_%=:\n"
WT"vmcnt(33)\n"
MF"v[80:83],a[160:163],a[48:51],v[80:83]\n"
WT"vmcnt(32)\n"
MF"v[88:91],a[160:163],a[52:55],v[88:91]\n"
WT"vmcnt(31)\n"
RT"a[184:185],v228 offset:45056\n"
MF"v[96:99],a[160:163],a[56:59],v[96:99]\n"
WT"vmcnt(30)\n"
BL"a[8:11],v235,s[8:11],s58 offen offset:0 sc1\n"
MF"v[104:107],a[160:163],a[60:63],v[104:107]\n"
WT"lgkmcnt(3)\n"
RT"a[186:187],v228 offset:45568\n"
MF"v[84:87],a[164:167],a[48:51],v[84:87]\n"
MF"v[92:95],a[164:167],a[52:55],v[92:95]\n"
RT"a[188:189],v229 offset:45056\n"
MF"v[100:103],a[164:167],a[56:59],v[100:103]\n"
MF"v[108:111],a[164:167],a[60:63],v[108:111]\n"
RT"a[190:191],v229 offset:45568\n"
MF"v[112:115],a[168:171],a[48:51],v[112:115]\n"
"s_lshl_b32 s50,s35,3\n"
SA"s50,s50,s34\n"
"s_lshl_b32 s50,s50,3\n"
MF"v[120:123],a[168:171],a[52:55],v[120:123]\n"
SA"s50,s50,2304\n"
BL"a[12:15],v235,s[8:11],s59 offen offset:0 sc1\n"
MF"v[128:131],a[168:171],a[56:59],v[128:131]\n"
SA"s51,s50,s45\n"
MF"v[136:139],a[168:171],a[60:63],v[136:139]\n"
MF"v[116:119],a[172:175],a[48:51],v[116:119]\n"
MF"v[124:127],a[172:175],a[52:55],v[124:127]\n"
WT"vmcnt(29) lgkmcnt(0)\n"
"s_barrier\n"
MF"v[132:135],a[172:175],a[56:59],v[132:135]\n"
RT"a[128:129],v236 offset:0\n"
MF"v[140:143],a[172:175],a[60:63],v[140:143]\n"
"buffer_store_dwordx2 v[232:233],v230,s[8:11],s51 offen sc1\n"
MF"v[144:147],a[176:179],a[48:51],v[144:147]\n"
RT"a[130:131],v236 offset:512\n"
MF"v[152:155],a[176:179],a[52:55],v[152:155]\n"
BL"a[16:19],v235,s[8:11],s56 offen offset:1024 sc1\n"
MF"v[160:163],a[176:179],a[56:59],v[160:163]\n"
SA"m0,s38,81920\n"
RT"a[132:133],v237 offset:0\n"
SA"s48,s60,s61\n"
MF"v[168:171],a[176:179],a[60:63],v[168:171]\n"
MF"v[148:151],a[180:183],a[48:51],v[148:151]\n"
RT"a[134:135],v237 offset:512\n"
MF"v[156:159],a[180:183],a[52:55],v[156:159]\n"
BL"v238,s[20:23],s48 offen offset:0 lds\n"
MF"v[164:167],a[180:183],a[56:59],v[164:167]\n"
RT"a[136:137],v228 offset:0\n"
MF"v[172:175],a[180:183],a[60:63],v[172:175]\n"
BL"a[20:23],v235,s[8:11],s57 offen offset:1024 sc1\n"
MF"v[176:179],a[184:187],a[48:51],v[176:179]\n"
RT"a[138:139],v228 offset:512\n"
MF"v[184:187],a[184:187],a[52:55],v[184:187]\n"
MF"v[192:195],a[184:187],a[56:59],v[192:195]\n"
RT"a[140:141],v229 offset:0\n"
MF"v[200:203],a[184:187],a[60:63],v[200:203]\n"
MF"v[180:183],a[188:191],a[48:51],v[180:183]\n"
RT"a[142:143],v229 offset:512\n"
MF"v[188:191],a[188:191],a[52:55],v[188:191]\n"
BL"v239,s[20:23],s48 offen offset:1024 lds\n"
MF"v[196:199],a[188:191],a[56:59],v[196:199]\n"
SA"s49,s88,262144\n"
RT"a[144:145],v236 offset:4096\n"
MF"v[204:207],a[188:191],a[60:63],v[204:207]\n"
WT"lgkmcnt(3)\n"
BL"a[24:27],v235,s[8:11],s58 offen offset:1024 sc1\n"
MF"v[80:83],a[128:131],a[64:67],v[80:83]\n"
RT"a[146:147],v236 offset:4608\n"
MF"v[88:91],a[128:131],a[68:71],v[88:91]\n"
WT"vmcnt(33)\n"
BS"v[28:31],v250,s[8:11],s49 offen offset:1024 sc1\n"
MF"v[96:99],a[128:131],a[72:75],v[96:99]\n"
WT"vmcnt(32)\n"
RT"a[148:149],v237 offset:4096\n"
MF"v[104:107],a[128:131],a[76:79],v[104:107]\n"
MF"v[84:87],a[132:135],a[64:67],v[84:87]\n"
RT"a[150:151],v237 offset:4608\n"
MF"v[92:95],a[132:135],a[68:71],v[92:95]\n"
BL"v240,s[20:23],s48 offen offset:2048 lds\n"
MF"v[100:103],a[132:135],a[72:75],v[100:103]\n"
RT"a[152:153],v228 offset:4096\n"
MF"v[108:111],a[132:135],a[76:79],v[108:111]\n"
BL"a[28:31],v235,s[8:11],s59 offen offset:1024 sc1\n"
MF"v[112:115],a[136:139],a[64:67],v[112:115]\n"
RT"a[154:155],v228 offset:4608\n"
MF"v[120:123],a[136:139],a[68:71],v[120:123]\n"
RT"a[156:157],v229 offset:4096\n"
MF"v[128:131],a[136:139],a[72:75],v[128:131]\n"
MF"v[136:139],a[136:139],a[76:79],v[136:139]\n"
WT"lgkmcnt(3)\n"
RT"a[158:159],v229 offset:4608\n"
MF"v[116:119],a[140:143],a[64:67],v[116:119]\n"
SA"s61,s61,524288\n"
BL"v241,s[20:23],s48 offen offset:3072 lds\n"
MF"v[124:127],a[140:143],a[68:71],v[124:127]\n"
BL"a[32:35],v235,s[8:11],s56 offen offset:2048 sc1\n"
MF"v[132:135],a[140:143],a[72:75],v[132:135]\n"
RT"a[160:161],v236 offset:8192\n"
MF"v[140:143],a[140:143],a[76:79],v[140:143]\n"
RT"a[162:163],v236 offset:8704\n"
MF"v[144:147],a[144:147],a[64:67],v[144:147]\n"
MF"v[152:155],a[144:147],a[68:71],v[152:155]\n"
RT"a[164:165],v237 offset:8192\n"
MF"v[160:163],a[144:147],a[72:75],v[160:163]\n"
MF"v[168:171],a[144:147],a[76:79],v[168:171]\n"
RT"a[166:167],v237 offset:8704\n"
MF"v[148:151],a[148:151],a[64:67],v[148:151]\n"
BL"a[36:39],v235,s[8:11],s57 offen offset:2048 sc1\n"
MF"v[156:159],a[148:151],a[68:71],v[156:159]\n"
RT"a[168:169],v228 offset:8192\n"
MF"v[164:167],a[148:151],a[72:75],v[164:167]\n"
MF"v[172:175],a[148:151],a[76:79],v[172:175]\n"
WT"lgkmcnt(3)\n"
RT"a[170:171],v228 offset:8704\n"
MF"v[176:179],a[152:155],a[64:67],v[176:179]\n"
RT"a[172:173],v229 offset:8192\n"
MF"v[184:187],a[152:155],a[68:71],v[184:187]\n"
MF"v[192:195],a[152:155],a[72:75],v[192:195]\n"
RT"a[174:175],v229 offset:8704\n"
MF"v[200:203],a[152:155],a[76:79],v[200:203]\n"
BL"a[40:43],v235,s[8:11],s58 offen offset:2048 sc1\n"
MF"v[180:183],a[156:159],a[64:67],v[180:183]\n"
RT"a[176:177],v236 offset:12288\n"
MF"v[188:191],a[156:159],a[68:71],v[188:191]\n"
MF"v[196:199],a[156:159],a[72:75],v[196:199]\n"
RT"a[178:179],v236 offset:12800\n"
MF"v[204:207],a[156:159],a[76:79],v[204:207]\n"
WT"vmcnt(37)\n"
MF"v[80:83],a[160:163],a[80:83],v[80:83]\n"
WT"vmcnt(35)\n"
RT"a[180:181],v237 offset:12288\n"
"s_mov_b32 s49,286720\n"
MF"v[88:91],a[160:163],a[84:87],v[88:91]\n"
WT"vmcnt(33)\n"
BL"a[44:47],v235,s[8:11],s59 offen offset:2048 sc1\n"
MF"v[96:99],a[160:163],a[88:91],v[96:99]\n"
WT"vmcnt(33)\n"
RT"a[182:183],v237 offset:12800\n"
MF"v[104:107],a[160:163],a[92:95],v[104:107]\n"
WT"lgkmcnt(3)\n"
BS"v[0:3],v250,s[8:11],s49 offen offset:0 sc1\n"
MF"v[84:87],a[164:167],a[80:83],v[84:87]\n"
RT"a[184:185],v228 offset:12288\n"
MF"v[92:95],a[164:167],a[84:87],v[92:95]\n"
MF"v[100:103],a[164:167],a[88:91],v[100:103]\n"
RT"a[186:187],v228 offset:12800\n"
MF"v[108:111],a[164:167],a[92:95],v[108:111]\n"
MF"v[112:115],a[168:171],a[80:83],v[112:115]\n"
RT"a[188:189],v229 offset:12288\n"
MF"v[120:123],a[168:171],a[84:87],v[120:123]\n"
BL"a[48:51],v235,s[8:11],s56 offen offset:3072 sc1\n"
MF"v[128:131],a[168:171],a[88:91],v[128:131]\n"
RT"a[190:191],v229 offset:12800\n"
MF"v[136:139],a[168:171],a[92:95],v[136:139]\n"
RT"a[128:129],v246 offset:0\n"
MF"v[116:119],a[172:175],a[80:83],v[116:119]\n"
MF"v[124:127],a[172:175],a[84:87],v[124:127]\n"
RT"a[130:131],v246 offset:512\n"
MF"v[132:135],a[172:175],a[88:91],v[132:135]\n"
SA"m0,s38,131072\n"
SA"s48,s60,s61\n"
MF"v[140:143],a[172:175],a[92:95],v[140:143]\n"
WT"lgkmcnt(3)\n"
RT"a[132:133],v247 offset:0\n"
MF"v[144:147],a[176:179],a[80:83],v[144:147]\n"
BL"a[52:55],v235,s[8:11],s57 offen offset:3072 sc1\n"
MF"v[152:155],a[176:179],a[84:87],v[152:155]\n"
BL"v238,s[20:23],s48 offen offset:0 lds\n"
MF"v[160:163],a[176:179],a[88:91],v[160:163]\n"
RT"a[134:135],v247 offset:512\n"
MF"v[168:171],a[176:179],a[92:95],v[168:171]\n"
RT"a[136:137],v248 offset:0\n"
MF"v[148:151],a[180:183],a[80:83],v[148:151]\n"
MF"v[156:159],a[180:183],a[84:87],v[156:159]\n"
RT"a[138:139],v248 offset:512\n"
MF"v[164:167],a[180:183],a[88:91],v[164:167]\n"
BL"a[56:59],v235,s[8:11],s58 offen offset:3072 sc1\n"
MF"v[172:175],a[180:183],a[92:95],v[172:175]\n"
RT"a[140:141],v249 offset:0\n"
MF"v[176:179],a[184:187],a[80:83],v[176:179]\n"
BL"v239,s[20:23],s48 offen offset:1024 lds\n"
MF"v[184:187],a[184:187],a[84:87],v[184:187]\n"
RT"a[142:143],v249 offset:512\n"
MF"v[192:195],a[184:187],a[88:91],v[192:195]\n"
MF"v[200:203],a[184:187],a[92:95],v[200:203]\n"
WT"lgkmcnt(3)\n"
RT"a[144:145],v246 offset:4096\n"
MF"v[180:183],a[188:191],a[80:83],v[180:183]\n"
SA"s99,s99,4096\n"
MF"v[188:191],a[188:191],a[84:87],v[188:191]\n"
"s_cmp_eq_u32 s99,20480\n"
RT"a[146:147],v246 offset:4608\n"
"s_cselect_b32 s99,0,s99\n"
MF"v[196:199],a[188:191],a[88:91],v[196:199]\n"
BL"a[60:63],v235,s[8:11],s59 offen offset:3072 sc1\n"
MF"v[204:207],a[188:191],a[92:95],v[204:207]\n"
WT"vmcnt(38)\n"
RT"a[148:149],v247 offset:4096\n"
MF"v[80:83],a[128:131],a[96:99],v[80:83]\n"
WT"vmcnt(37)\n"
BL"v240,s[20:23],s48 offen offset:2048 lds\n"
MF"v[88:91],a[128:131],a[100:103],v[88:91]\n"
WT"vmcnt(36)\n"
RT"a[150:151],v247 offset:4608\n"
MF"v[96:99],a[128:131],a[104:107],v[96:99]\n"
WT"vmcnt(34)\n"
RT"a[152:153],v248 offset:4096\n"
MF"v[104:107],a[128:131],a[108:111],v[104:107]\n"
"v_add_u32 v235,s99,v224\n"
MF"v[84:87],a[132:135],a[96:99],v[84:87]\n"
RT"a[154:155],v248 offset:4608\n"
MF"v[92:95],a[132:135],a[100:103],v[92:95]\n"
BS"v[16:19],v250,s[8:11],s49 offen offset:1024 sc1\n"
MF"v[100:103],a[132:135],a[104:107],v[100:103]\n"
BL"a[64:67],v235,s[8:11],s56 offen offset:0 sc1\n"
MF"v[108:111],a[132:135],a[108:111],v[108:111]\n"
WT"lgkmcnt(3)\n"
RT"a[156:157],v249 offset:4096\n"
MF"v[112:115],a[136:139],a[96:99],v[112:115]\n"
SA"s61,s61,524288\n"
RT"a[158:159],v249 offset:4608\n"
MF"v[120:123],a[136:139],a[100:103],v[120:123]\n"
"s_cmp_eq_u32 s61,5242880\n"
BL"v241,s[20:23],s48 offen offset:3072 lds\n"
MF"v[128:131],a[136:139],a[104:107],v[128:131]\n"
"s_cselect_b32 s61,0,s61\n"
RT"a[160:161],v246 offset:24576\n"
MF"v[136:139],a[136:139],a[108:111],v[136:139]\n"
BL"a[68:71],v235,s[8:11],s57 offen offset:0 sc1\n"
MF"v[116:119],a[140:143],a[96:99],v[116:119]\n"
RT"a[162:163],v246 offset:25088\n"
MF"v[124:127],a[140:143],a[100:103],v[124:127]\n"
MF"v[132:135],a[140:143],a[104:107],v[132:135]\n"
RT"a[164:165],v247 offset:24576\n"
MF"v[140:143],a[140:143],a[108:111],v[140:143]\n"
MF"v[144:147],a[144:147],a[96:99],v[144:147]\n"
"s_lshl_b32 s39,s35,20\n"
RT"a[166:167],v247 offset:25088\n"
"v_add_u32 v225,s36,v224\n"
MF"v[152:155],a[144:147],a[100:103],v[152:155]\n"
SA"s39,s39,9175040\n"
MF"v[160:163],a[144:147],a[104:107],v[160:163]\n"
RT"a[168:169],v248 offset:24576\n"
MF"v[168:171],a[144:147],a[108:111],v[168:171]\n"
WT"lgkmcnt(3)\n"
BL"a[72:75],v235,s[8:11],s58 offen offset:0 sc1\n"
MF"v[148:151],a[148:151],a[96:99],v[148:151]\n"
RT"a[170:171],v248 offset:25088\n"
MF"v[156:159],a[148:151],a[100:103],v[156:159]\n"
RT"a[172:173],v249 offset:24576\n"
MF"v[164:167],a[148:151],a[104:107],v[164:167]\n"
MF"v[172:175],a[148:151],a[108:111],v[172:175]\n"
RT"a[174:175],v249 offset:25088\n"
MF"v[176:179],a[152:155],a[96:99],v[176:179]\n"
MF"v[184:187],a[152:155],a[100:103],v[184:187]\n"
RT"a[176:177],v246 offset:28672\n"
MF"v[192:195],a[152:155],a[104:107],v[192:195]\n"
BL"a[76:79],v235,s[8:11],s59 offen offset:0 sc1\n"
MF"v[200:203],a[152:155],a[108:111],v[200:203]\n"
RT"a[178:179],v246 offset:29184\n"
MF"v[180:183],a[156:159],a[96:99],v[180:183]\n"
MF"v[188:191],a[156:159],a[100:103],v[188:191]\n"
RT"a[180:181],v247 offset:28672\n"
MF"v[196:199],a[156:159],a[104:107],v[196:199]\n"
MF"v[204:207],a[156:159],a[108:111],v[204:207]\n"
WT"vmcnt(38)\n"
RT"a[182:183],v247 offset:29184\n"
MF"v[80:83],a[160:163],a[112:115],v[80:83]\n"
WT"vmcnt(35)\n"
BL"a[80:83],v235,s[8:11],s56 offen offset:1024 sc1\n"
MF"v[88:91],a[160:163],a[116:119],v[88:91]\n"
WT"vmcnt(35)\n"
RT"a[184:185],v248 offset:28672\n"
MF"v[96:99],a[160:163],a[120:123],v[96:99]\n"
WT"vmcnt(34)\n"
MF"v[104:107],a[160:163],a[124:127],v[104:107]\n"
WT"lgkmcnt(3)\n"
RT"a[186:187],v248 offset:29184\n"
MF"v[84:87],a[164:167],a[112:115],v[84:87]\n"
SA"s49,s88,286720\n"
MF"v[92:95],a[164:167],a[116:119],v[92:95]\n"
RT"a[188:189],v249 offset:28672\n"
MF"v[100:103],a[164:167],a[120:123],v[100:103]\n"
BS"v[32:35],v250,s[8:11],s49 offen offset:0 sc1\n"
MF"v[108:111],a[164:167],a[124:127],v[108:111]\n"
RT"a[190:191],v249 offset:29184\n"
MF"v[112:115],a[168:171],a[112:115],v[112:115]\n"
BL"a[84:87],v235,s[8:11],s57 offen offset:1024 sc1\n"
MF"v[120:123],a[168:171],a[116:119],v[120:123]\n"
MF"v[128:131],a[168:171],a[120:123],v[128:131]\n"
MF"v[136:139],a[168:171],a[124:127],v[136:139]\n"
MF"v[116:119],a[172:175],a[112:115],v[116:119]\n"
MF"v[124:127],a[172:175],a[116:119],v[124:127]\n"
"s_barrier\n"
MF"v[132:135],a[172:175],a[120:123],v[132:135]\n"
RT"a[128:129],v242 offset:0\n"
MF"v[140:143],a[172:175],a[124:127],v[140:143]\n"
BL"a[88:91],v235,s[8:11],s58 offen offset:1024 sc1\n"
MF"v[144:147],a[176:179],a[112:115],v[144:147]\n"
RT"a[130:131],v242 offset:512\n"
MF"v[152:155],a[176:179],a[116:119],v[152:155]\n"
MF"v[160:163],a[176:179],a[120:123],v[160:163]\n"
RT"a[132:133],v243 offset:0\n"
MF"v[168:171],a[176:179],a[124:127],v[168:171]\n"
WT"lgkmcnt(3)\n"
MF"v[148:151],a[180:183],a[112:115],v[148:151]\n"
RT"a[134:135],v243 offset:512\n"
MF"v[156:159],a[180:183],a[116:119],v[156:159]\n"
BL"a[92:95],v235,s[8:11],s59 offen offset:1024 sc1\n"
MF"v[164:167],a[180:183],a[120:123],v[164:167]\n"
RT"a[136:137],v244 offset:0\n"
MF"v[172:175],a[180:183],a[124:127],v[172:175]\n"
MF"v[176:179],a[184:187],a[112:115],v[176:179]\n"
RT"a[138:139],v244 offset:512\n"
MF"v[184:187],a[184:187],a[116:119],v[184:187]\n"
MF"v[192:195],a[184:187],a[120:123],v[192:195]\n"
RT"a[140:141],v245 offset:0\n"
MF"v[200:203],a[184:187],a[124:127],v[200:203]\n"
MF"v[180:183],a[188:191],a[112:115],v[180:183]\n"
RT"a[142:143],v245 offset:512\n"
MF"v[188:191],a[188:191],a[116:119],v[188:191]\n"
BL"a[96:99],v235,s[8:11],s56 offen offset:2048 sc1\n"
MF"v[196:199],a[188:191],a[120:123],v[196:199]\n"
RT"a[144:145],v242 offset:4096\n"
MF"v[204:207],a[188:191],a[124:127],v[204:207]\n"
WT"vmcnt(38) lgkmcnt(3)\n"
MF"v[80:83],a[128:131],a[0:3],v[80:83]\n"
WT"vmcnt(37)\n"
RT"a[146:147],v242 offset:4608\n"
MF"v[88:91],a[128:131],a[4:7],v[88:91]\n"
WT"vmcnt(35)\n"
MF"v[96:99],a[128:131],a[8:11],v[96:99]\n"
WT"vmcnt(34)\n"
RT"a[148:149],v243 offset:4096\n"
MF"v[104:107],a[128:131],a[12:15],v[104:107]\n"
BL"a[100:103],v235,s[8:11],s57 offen offset:2048 sc1\n"
MF"v[84:87],a[132:135],a[0:3],v[84:87]\n"
RT"a[150:151],v243 offset:4608\n"
MF"v[92:95],a[132:135],a[4:7],v[92:95]\n"
RT"a[152:153],v244 offset:4096\n"
MF"v[100:103],a[132:135],a[8:11],v[100:103]\n"
MF"v[108:111],a[132:135],a[12:15],v[108:111]\n"
RT"a[154:155],v244 offset:4608\n"
MF"v[112:115],a[136:139],a[0:3],v[112:115]\n"
BS"v[48:51],v250,s[8:11],s49 offen offset:1024 sc1\n"
MF"v[120:123],a[136:139],a[4:7],v[120:123]\n"
RT"a[156:157],v245 offset:4096\n"
MF"v[128:131],a[136:139],a[8:11],v[128:131]\n"
BL"a[104:107],v235,s[8:11],s58 offen offset:2048 sc1\n"
MF"v[136:139],a[136:139],a[12:15],v[136:139]\n"
WT"lgkmcnt(3)\n"
RT"a[158:159],v245 offset:4608\n"
MF"v[116:119],a[140:143],a[0:3],v[116:119]\n"
MF"v[124:127],a[140:143],a[4:7],v[124:127]\n"
RT"a[160:161],v242 offset:24576\n"
MF"v[132:135],a[140:143],a[8:11],v[132:135]\n"
MF"v[140:143],a[140:143],a[12:15],v[140:143]\n"
RT"a[162:163],v242 offset:25088\n"
MF"v[144:147],a[144:147],a[0:3],v[144:147]\n"
MF"v[152:155],a[144:147],a[4:7],v[152:155]\n"
RT"a[164:165],v243 offset:24576\n"
MF"v[160:163],a[144:147],a[8:11],v[160:163]\n"
BL"a[108:111],v235,s[8:11],s59 offen offset:2048 sc1\n"
MF"v[168:171],a[144:147],a[12:15],v[168:171]\n"
RT"a[166:167],v243 offset:25088\n"
MF"v[148:151],a[148:151],a[0:3],v[148:151]\n"
MF"v[156:159],a[148:151],a[4:7],v[156:159]\n"
RT"a[168:169],v244 offset:24576\n"
MF"v[164:167],a[148:151],a[8:11],v[164:167]\n"
MF"v[172:175],a[148:151],a[12:15],v[172:175]\n"
WT"lgkmcnt(3)\n"
RT"a[170:171],v244 offset:25088\n"
MF"v[176:179],a[152:155],a[0:3],v[176:179]\n"
RT"a[172:173],v245 offset:24576\n"
MF"v[184:187],a[152:155],a[4:7],v[184:187]\n"
BL"a[112:115],v235,s[8:11],s56 offen offset:3072 sc1\n"
MF"v[192:195],a[152:155],a[8:11],v[192:195]\n"
RT"a[174:175],v245 offset:25088\n"
MF"v[200:203],a[152:155],a[12:15],v[200:203]\n"
MF"v[180:183],a[156:159],a[0:3],v[180:183]\n"
RT"a[176:177],v242 offset:28672\n"
MF"v[188:191],a[156:159],a[4:7],v[188:191]\n"
MF"v[196:199],a[156:159],a[8:11],v[196:199]\n"
RT"a[178:179],v242 offset:29184\n"
MF"v[204:207],a[156:159],a[12:15],v[204:207]\n"
WT"vmcnt(37)\n"
BL"a[116:119],v235,s[8:11],s57 offen offset:3072 sc1\n"
MF"v[80:83],a[160:163],a[16:19],v[80:83]\n"
WT"vmcnt(36)\n"
RT"a[180:181],v243 offset:28672\n"
MF"v[88:91],a[160:163],a[20:23],v[88:91]\n"
WT"vmcnt(34)\n"
MF"v[96:99],a[160:163],a[24:27],v[96:99]\n"
WT"vmcnt(31)\n"
RT"a[182:183],v243 offset:29184\n"
MF"v[104:107],a[160:163],a[28:31],v[104:107]\n"
WT"lgkmcnt(3)\n"
MF"v[84:87],a[164:167],a[16:19],v[84:87]\n"
RT"a[184:185],v244 offset:28672\n"
MF"v[92:95],a[164:167],a[20:23],v[92:95]\n"
MF"v[100:103],a[164:167],a[24:27],v[100:103]\n"
RT"a[186:187],v244 offset:29184\n"
MF"v[108:111],a[164:167],a[28:31],v[108:111]\n"
BL"a[120:123],v235,s[8:11],s58 offen offset:3072 sc1\n"
MF"v[112:115],a[168:171],a[16:19],v[112:115]\n"
"s_mov_b32 s49,270336\n"
RT"a[188:189],v245 offset:28672\n"
MF"v[120:123],a[168:171],a[20:23],v[120:123]\n"
MF"v[128:131],a[168:171],a[24:27],v[128:131]\n"
RT"a[190:191],v245 offset:29184\n"
MF"v[136:139],a[168:171],a[28:31],v[136:139]\n"
BS"v[36:39],v250,s[8:11],s49 offen offset:0 sc1\n"
MF"v[116:119],a[172:175],a[16:19],v[116:119]\n"
SA"s99,s99,4096\n"
RT"a[128:129],v236 offset:16384\n"
MF"v[124:127],a[172:175],a[20:23],v[124:127]\n"
"s_cmp_eq_u32 s99,20480\n"
RT"a[130:131],v236 offset:16896\n"
"s_cselect_b32 s99,0,s99\n"
MF"v[132:135],a[172:175],a[24:27],v[132:135]\n"
BL"a[124:127],v235,s[8:11],s59 offen offset:3072 sc1\n"
MF"v[140:143],a[172:175],a[28:31],v[140:143]\n"
WT"lgkmcnt(3)\n"
RT"a[132:133],v237 offset:16384\n"
MF"v[144:147],a[176:179],a[16:19],v[144:147]\n"
MF"v[152:155],a[176:179],a[20:23],v[152:155]\n"
RT"a[134:135],v237 offset:16896\n"
MF"v[160:163],a[176:179],a[24:27],v[160:163]\n"
MF"v[168:171],a[176:179],a[28:31],v[168:171]\n"
RT"a[136:137],v228 offset:16384\n"
"v_add_u32 v235,s99,v224\n"
MF"v[148:151],a[180:183],a[16:19],v[148:151]\n"
BL"a[192:195],v235,s[8:11],s56 offen offset:0 sc1\n"
MF"v[156:159],a[180:183],a[20:23],v[156:159]\n"
RT"a[138:139],v228 offset:16896\n"
MF"v[164:167],a[180:183],a[24:27],v[164:167]\n"
MF"v[172:175],a[180:183],a[28:31],v[172:175]\n"
RT"a[140:141],v229 offset:16384\n"
MF"v[176:179],a[184:187],a[16:19],v[176:179]\n"
"v_lshrrev_b32 v252,1,v224\n"
MF"v[184:187],a[184:187],a[20:23],v[184:187]\n"
RT"a[142:143],v229 offset:16896\n"
"v_and_b32 v252,56,v252\n"
MF"v[192:195],a[184:187],a[24:27],v[192:195]\n"
"s_lshl_b32 s50,s35,6\n"
MF"v[200:203],a[184:187],a[28:31],v[200:203]\n"
WT"lgkmcnt(3)\n"
RT"a[144:145],v236 offset:20480\n"
SA"s50,s50,2304\n"
MF"v[180:183],a[188:191],a[16:19],v[180:183]\n"
BL"a[196:199],v235,s[8:11],s57 offen offset:0 sc1\n"
MF"v[188:191],a[188:191],a[20:23],v[188:191]\n"
"buffer_load_dwordx2 v[208:209],v252,s[8:11],s50 offen sc1\n"
MF"v[196:199],a[188:191],a[24:27],v[196:199]\n"
RT"a[146:147],v236 offset:20992\n"
MF"v[204:207],a[188:191],a[28:31],v[204:207]\n"
WT"vmcnt(35)\n"
RT"a[148:149],v237 offset:20480\n"
MF"v[80:83],a[128:131],a[32:35],v[80:83]\n"
WT"vmcnt(34)\n"
MF"v[88:91],a[128:131],a[36:39],v[88:91]\n"
WT"vmcnt(33)\n"
RT"a[150:151],v237 offset:20992\n"
MF"v[96:99],a[128:131],a[40:43],v[96:99]\n"
WT"vmcnt(32)\n"
RT"a[152:153],v228 offset:20480\n"
MF"v[104:107],a[128:131],a[44:47],v[104:107]\n"
BL"a[200:203],v235,s[8:11],s58 offen offset:0 sc1\n"
MF"v[84:87],a[132:135],a[32:35],v[84:87]\n"
RT"a[154:155],v228 offset:20992\n"
MF"v[92:95],a[132:135],a[36:39],v[92:95]\n"
MF"v[100:103],a[132:135],a[40:43],v[100:103]\n"
RT"a[156:157],v229 offset:20480\n"
MF"v[108:111],a[132:135],a[44:47],v[108:111]\n"
WT"lgkmcnt(4)\n"
MF"v[112:115],a[136:139],a[32:35],v[112:115]\n"
RT"a[158:159],v229 offset:20992\n"
MF"v[120:123],a[136:139],a[36:39],v[120:123]\n"
BL"a[204:207],v235,s[8:11],s59 offen offset:0 sc1\n"
MF"v[128:131],a[136:139],a[40:43],v[128:131]\n"
RT"a[160:161],v236 offset:24576\n"
MF"v[136:139],a[136:139],a[44:47],v[136:139]\n"
MF"v[116:119],a[140:143],a[32:35],v[116:119]\n"
RT"a[162:163],v236 offset:25088\n"
MF"v[124:127],a[140:143],a[36:39],v[124:127]\n"
BS"v[44:47],v250,s[8:11],s49 offen offset:1024 sc1\n"
MF"v[132:135],a[140:143],a[40:43],v[132:135]\n"
RT"a[164:165],v237 offset:24576\n"
MF"v[140:143],a[140:143],a[44:47],v[140:143]\n"
MF"v[144:147],a[144:147],a[32:35],v[144:147]\n"
RT"a[166:167],v237 offset:25088\n"
MF"v[152:155],a[144:147],a[36:39],v[152:155]\n"
BL"a[208:211],v235,s[8:11],s56 offen offset:1024 sc1\n"
MF"v[160:163],a[144:147],a[40:43],v[160:163]\n"
RT"a[168:169],v228 offset:24576\n"
MF"v[168:171],a[144:147],a[44:47],v[168:171]\n"
WT"lgkmcnt(3)\n"
MF"v[148:151],a[148:151],a[32:35],v[148:151]\n"
RT"a[170:171],v228 offset:25088\n"
MF"v[156:159],a[148:151],a[36:39],v[156:159]\n"
RT"a[172:173],v229 offset:24576\n"
MF"v[164:167],a[148:151],a[40:43],v[164:167]\n"
MF"v[172:175],a[148:151],a[44:47],v[172:175]\n"
RT"a[174:175],v229 offset:25088\n"
MF"v[176:179],a[152:155],a[32:35],v[176:179]\n"
BL"a[212:215],v235,s[8:11],s57 offen offset:1024 sc1\n"
MF"v[184:187],a[152:155],a[36:39],v[184:187]\n"
RT"a[176:177],v236 offset:28672\n"
MF"v[192:195],a[152:155],a[40:43],v[192:195]\n"
MF"v[200:203],a[152:155],a[44:47],v[200:203]\n"
RT"a[178:179],v236 offset:29184\n"
MF"v[180:183],a[156:159],a[32:35],v[180:183]\n"
MF"v[188:191],a[156:159],a[36:39],v[188:191]\n"
RT"a[180:181],v237 offset:28672\n"
MF"v[196:199],a[156:159],a[40:43],v[196:199]\n"
BL"a[216:219],v235,s[8:11],s58 offen offset:1024 sc1\n"
MF"v[204:207],a[156:159],a[44:47],v[204:207]\n"
WT"vmcnt(36)\n"
RT"a[182:183],v237 offset:29184\n"
MF"v[80:83],a[160:163],a[48:51],v[80:83]\n"
WT"vmcnt(35)\n"
MF"v[88:91],a[160:163],a[52:55],v[88:91]\n"
WT"vmcnt(33)\n"
RT"a[184:185],v228 offset:28672\n"
MF"v[96:99],a[160:163],a[56:59],v[96:99]\n"
WT"vmcnt(31)\n"
MF"v[104:107],a[160:163],a[60:63],v[104:107]\n"
WT"lgkmcnt(3)\n"
RT"a[186:187],v228 offset:29184\n"
MF"v[84:87],a[164:167],a[48:51],v[84:87]\n"
MF"v[92:95],a[164:167],a[52:55],v[92:95]\n"
RT"a[188:189],v229 offset:28672\n"
MF"v[100:103],a[164:167],a[56:59],v[100:103]\n"
BL"a[220:223],v235,s[8:11],s59 offen offset:1024 sc1\n"
MF"v[108:111],a[164:167],a[60:63],v[108:111]\n"
RT"a[190:191],v229 offset:29184\n"
MF"v[112:115],a[168:171],a[48:51],v[112:115]\n"
MF"v[120:123],a[168:171],a[52:55],v[120:123]\n"
WT"vmcnt(7)\n"
"v_cmp_eq_u64 vcc,s[28:29],v[208:209]\n"
"s_cmp_eq_u64 vcc,exec\n"
"s_cbranch_scc0 L1010_%=\n"
"L1011_%=:\n"
MF"v[128:131],a[168:171],a[56:59],v[128:131]\n"
MF"v[136:139],a[168:171],a[60:63],v[136:139]\n"
MF"v[116:119],a[172:175],a[48:51],v[116:119]\n"
SA"s49,s88,270336\n"
BL"a[224:227],v235,s[8:11],s56 offen offset:2048 sc1\n"
MF"v[124:127],a[172:175],a[52:55],v[124:127]\n"
WT"lgkmcnt(0)\n"
"s_barrier\n"
MF"v[132:135],a[172:175],a[56:59],v[132:135]\n"
SA"m0,s36,0\n"
RT"a[128:129],v246 offset:8192\n"
SA"s48,s39,0\n"
MF"v[140:143],a[172:175],a[60:63],v[140:143]\n"
BS"v[52:55],v250,s[8:11],s49 offen offset:0 sc1\n"
MF"v[144:147],a[176:179],a[48:51],v[144:147]\n"
RT"a[130:131],v246 offset:8704\n"
MF"v[152:155],a[176:179],a[52:55],v[152:155]\n"
BL"v225,s[8:11],s48 offen offset:0 sc1 lds\n"
MF"v[160:163],a[176:179],a[56:59],v[160:163]\n"
RT"a[132:133],v247 offset:8192\n"
"v_lshlrev_b32 v254,10,v224\n"
MF"v[168:171],a[176:179],a[60:63],v[168:171]\n"
BL"a[228:231],v235,s[8:11],s57 offen offset:2048 sc1\n"
MF"v[148:151],a[180:183],a[48:51],v[148:151]\n"
"s_lshl_b32 s94,s34,1\n"
RT"a[134:135],v247 offset:8704\n"
"v_lshrrev_b32 v255,4,v224\n"
MF"v[156:159],a[180:183],a[52:55],v[156:159]\n"
SA"s95,s94,2\n"
MF"v[164:167],a[180:183],a[56:59],v[164:167]\n"
"s_sub_u32 s94,32,s94\n"
RT"a[136:137],v248 offset:8192\n"
MF"v[172:175],a[180:183],a[60:63],v[172:175]\n"
"v_add_u32 v228,s39,v254\n"
MF"v[176:179],a[184:187],a[48:51],v[176:179]\n"
RT"a[138:139],v248 offset:8704\n"
"v_and_b32 v254,1,v255\n"
MF"v[184:187],a[184:187],a[52:55],v[184:187]\n"
BL"v225,s[8:11],s48 offen offset:1024 sc1 lds\n"
MF"v[192:195],a[184:187],a[56:59],v[192:195]\n"
RT"a[140:141],v249 offset:8192\n"
"v_mul_u32_u24 v254,491520,v254\n"
MF"v[200:203],a[184:187],a[60:63],v[200:203]\n"
BL"a[232:235],v235,s[8:11],s58 offen offset:2048 sc1\n"
MF"v[180:183],a[188:191],a[48:51],v[180:183]\n"
RT"a[142:143],v249 offset:8704\n"
"v_cmp_gt_u32 vcc,s95,v255\n"
MF"v[188:191],a[188:191],a[52:55],v[188:191]\n"
"v_mov_b32 v229,16384\n"
"v_mov_b32 v253,516096\n"
MF"v[196:199],a[188:191],a[56:59],v[196:199]\n"
RT"a[144:145],v246 offset:12288\n"
"v_cndmask_b32_e64 v254,v254,0,vcc\n"
MF"v[204:207],a[188:191],a[60:63],v[204:207]\n"
WT"lgkmcnt(3)\n"
"v_cndmask_b32 v229,v229,v253,vcc\n"
"v_cmp_eq_u32 vcc,s94,v255\n"
MF"v[80:83],a[128:131],a[64:67],v[80:83]\n"
RT"a[146:147],v246 offset:12800\n"
"v_mov_b32 v253,507904\n"
MF"v[88:91],a[128:131],a[68:71],v[88:91]\n"
BL"v225,s[8:11],s48 offen offset:2048 sc1 lds\n"
MF"v[96:99],a[128:131],a[72:75],v[96:99]\n"
BL"a[236:239],v235,s[8:11],s59 offen offset:2048 sc1\n"
MF"v[104:107],a[128:131],a[76:79],v[104:107]\n"
RT"a[148:149],v247 offset:12288\n"
"v_cndmask_b32 v254,v254,v253,vcc\n"
MF"v[84:87],a[132:135],a[64:67],v[84:87]\n"
RT"a[150:151],v247 offset:12800\n"
"v_mov_b32 v253,1862270976\n"
MF"v[92:95],a[132:135],a[68:71],v[92:95]\n"
"s_mul_i32 s95,s35,163840\n"
RT"a[152:153],v248 offset:12288\n"
"v_cndmask_b32 v229,v229,v253,vcc\n"
MF"v[100:103],a[132:135],a[72:75],v[100:103]\n"
SA"s95,s95,3395584\n"
"v_add_u32 v228,v228,v254\n"
MF"v[108:111],a[132:135],a[76:79],v[108:111]\n"
RT"a[154:155],v248 offset:12800\n"
"v_subrev_u32 v254,s94,v255\n"
MF"v[112:115],a[136:139],a[64:67],v[112:115]\n"
BL"a[240:243],v235,s[8:11],s56 offen offset:3072 sc1\n"
MF"v[120:123],a[136:139],a[68:71],v[120:123]\n"
BL"v225,s[8:11],s48 offen offset:3072 sc1 lds\n"
MF"v[128:131],a[136:139],a[72:75],v[128:131]\n"
RT"a[156:157],v249 offset:12288\n"
"v_lshlrev_b32 v254,15,v254\n"
MF"v[136:139],a[136:139],a[76:79],v[136:139]\n"
WT"lgkmcnt(3)\n"
RT"a[158:159],v249 offset:12800\n"
"v_add_u32 v254,s95,v254\n"
MF"v[116:119],a[140:143],a[64:67],v[116:119]\n"
"v_cmp_lt_u32 vcc,s94,v255\n"
MF"v[124:127],a[140:143],a[68:71],v[124:127]\n"
"s_mov_b32 s47,s36\n"
RT"a[160:161],v246 offset:32768\n"
"v_cndmask_b32 v228,v228,v254,vcc\n"
MF"v[132:135],a[140:143],a[72:75],v[132:135]\n"
SA"m0,s36,16384\n"
"v_add_u32 v229,v229,v228\n"
SA"s48,s39,516096\n"
MF"v[140:143],a[140:143],a[76:79],v[140:143]\n"
SA"s46,s36,163840\n"
RT"a[162:163],v246 offset:33280\n"
SA"s40,s36,65536\n"
MF"v[144:147],a[144:147],a[64:67],v[144:147]\n"
"s_mov_b32 s41,0\n"
BL"a[244:247],v235,s[8:11],s57 offen offset:3072 sc1\n"
MF"v[152:155],a[144:147],a[68:71],v[152:155]\n"
"s_mov_b32 s42,2\n"
BS"v[60:63],v250,s[8:11],s49 offen offset:1024 sc1\n"
MF"v[160:163],a[144:147],a[72:75],v[160:163]\n"
BL"v225,s[8:11],s48 offen offset:0 sc1 lds\n"
MF"v[168:171],a[144:147],a[76:79],v[168:171]\n"
RT"a[164:165],v247 offset:32768\n"
"v_mov_b32 v226,v224\n"
MF"v[148:151],a[148:151],a[64:67],v[148:151]\n"
RT"a[166:167],v247 offset:33280\n"
"v_bfe_u32 v254,v224,4,4\n"
MF"v[156:159],a[148:151],a[68:71],v[156:159]\n"
RT"a[168:169],v248 offset:32768\n"
"v_lshrrev_b32 v255,8,v224\n"
MF"v[164:167],a[148:151],a[72:75],v[164:167]\n"
"s_lshl_b32 s94,s30,4\n"
BL"a[248:251],v235,s[8:11],s58 offen offset:3072 sc1\n"
MF"v[172:175],a[148:151],a[76:79],v[172:175]\n"
WT"lgkmcnt(3)\n"
RT"a[170:171],v248 offset:33280\n"
"v_lshlrev_b32 v255,2,v255\n"
MF"v[176:179],a[152:155],a[64:67],v[176:179]\n"
RT"a[172:173],v249 offset:32768\n"
"v_sub_u32 v254,v254,v255\n"
MF"v[184:187],a[152:155],a[68:71],v[184:187]\n"
BL"v225,s[8:11],s48 offen offset:1024 sc1 lds\n"
MF"v[192:195],a[152:155],a[72:75],v[192:195]\n"
RT"a[174:175],v249 offset:33280\n"
"v_add_u32 v254,s94,v254\n"
MF"v[200:203],a[152:155],a[76:79],v[200:203]\n"
"v_mov_b32 v255,0xff800000\n"
"v_cmp_gt_i32_e64 vcc,0,v254\n"
MF"v[180:183],a[156:159],a[64:67],v[180:183]\n"
RT"a[176:177],v246 offset:36864\n"
"v_cndmask_b32_e64 v208,0,v255,vcc\n"
MF"v[188:191],a[156:159],a[68:71],v[188:191]\n"
BL"a[252:255],v235,s[8:11],s59 offen offset:3072 sc1\n"
MF"v[196:199],a[156:159],a[72:75],v[196:199]\n"
RT"a[178:179],v246 offset:37376\n"
"v_cmp_gt_i32_e64 s[52:53],1,v254\n"
MF"v[204:207],a[156:159],a[76:79],v[204:207]\n"
"v_cmp_gt_i32_e64 s[62:63],2,v254\n"
"v_cndmask_b32_e64 v209,0,v255,s[52:53]\n"
MF"v[80:83],a[160:163],a[80:83],v[80:83]\n"
RT"a[180:181],v247 offset:36864\n"
"v_cndmask_b32_e64 v210,0,v255,s[62:63]\n"
MF"v[88:91],a[160:163],a[84:87],v[88:91]\n"
BL"v225,s[8:11],s48 offen offset:2048 sc1 lds\n"
MF"v[96:99],a[160:163],a[88:91],v[96:99]\n"
RT"a[182:183],v247 offset:37376\n"
"v_cmp_gt_i32_e64 vcc,3,v254\n"
MF"v[104:107],a[160:163],a[92:95],v[104:107]\n"
WT"lgkmcnt(3)\n"
"v_cndmask_b32_e64 v211,0,v255,vcc\n"
"v_cmp_gt_i32_e64 s[52:53],16,v254\n"
MF"v[84:87],a[164:167],a[80:83],v[84:87]\n"
RT"a[184:185],v248 offset:36864\n"
"v_cndmask_b32_e64 v212,0,v255,s[52:53]\n"
MF"v[92:95],a[164:167],a[84:87],v[92:95]\n"
"v_cmp_gt_i32_e64 s[62:63],17,v254\n"
"v_cmp_gt_i32_e64 vcc,18,v254\n"
MF"v[100:103],a[164:167],a[88:91],v[100:103]\n"
RT"a[186:187],v248 offset:37376\n"
"v_cndmask_b32_e64 v213,0,v255,s[62:63]\n"
MF"v[108:111],a[164:167],a[92:95],v[108:111]\n"
"v_cndmask_b32_e64 v214,0,v255,vcc\n"
"v_cmp_gt_i32_e64 s[52:53],19,v254\n"
MF"v[112:115],a[168:171],a[80:83],v[112:115]\n"
RT"a[188:189],v249 offset:36864\n"
"v_cndmask_b32_e64 v215,0,v255,s[52:53]\n"
MF"v[120:123],a[168:171],a[84:87],v[120:123]\n"
BL"v225,s[8:11],s48 offen offset:3072 sc1 lds\n"
MF"v[128:131],a[168:171],a[88:91],v[128:131]\n"
RT"a[190:191],v249 offset:37376\n"
"v_cmp_gt_i32_e64 s[62:63],32,v254\n"
MF"v[136:139],a[168:171],a[92:95],v[136:139]\n"
RT"a[128:129],v242 offset:8192\n"
"v_cndmask_b32_e64 v216,0,v255,s[62:63]\n"
MF"v[116:119],a[172:175],a[80:83],v[116:119]\n"
"v_cmp_gt_i32_e64 vcc,33,v254\n"
"v_cmp_gt_i32_e64 s[52:53],34,v254\n"
MF"v[124:127],a[172:175],a[84:87],v[124:127]\n"
RT"a[130:131],v242 offset:8704\n"
"v_cndmask_b32_e64 v217,0,v255,vcc\n"
MF"v[132:135],a[172:175],a[88:91],v[132:135]\n"
SA"m0,s36,32768\n"
"v_cndmask_b32_e64 v218,0,v255,s[52:53]\n"
"v_cmp_gt_i32_e64 s[62:63],35,v254\n"
MF"v[140:143],a[172:175],a[92:95],v[140:143]\n"
WT"lgkmcnt(3)\n"
RT"a[132:133],v243 offset:8192\n"
"v_cndmask_b32_e64 v219,0,v255,s[62:63]\n"
MF"v[144:147],a[176:179],a[80:83],v[144:147]\n"
SA"s48,s39,16384\n"
"v_cmp_gt_i32_e64 vcc,48,v254\n"
"v_cmp_gt_i32_e64 s[52:53],49,v254\n"
MF"v[152:155],a[176:179],a[84:87],v[152:155]\n"
"s_mov_b32 s49,278528\n"
BL"v225,s[8:11],s48 offen offset:0 sc1 lds\n"
MF"v[160:163],a[176:179],a[88:91],v[160:163]\n"
RT"a[134:135],v243 offset:8704\n"
"v_cndmask_b32_e64 v220,0,v255,vcc\n"
MF"v[168:171],a[176:179],a[92:95],v[168:171]\n"
BS"v[64:67],v250,s[8:11],s49 offen offset:0 sc1\n"
MF"v[148:151],a[180:183],a[80:83],v[148:151]\n"
"s_lshl_b32 s59,s34,5\n"
RT"a[136:137],v244 offset:8192\n"
"v_cndmask_b32_e64 v221,0,v255,s[52:53]\n"
MF"v[156:159],a[180:183],a[84:87],v[156:159]\n"
"s_sub_u32 s60,480,s59\n"
RT"a[138:139],v244 offset:8704\n"
"v_cmp_gt_i32_e64 s[62:63],50,v254\n"
MF"v[164:167],a[180:183],a[88:91],v[164:167]\n"
SA"s59,s59,s35\n"
"v_cndmask_b32_e64 v222,0,v255,s[62:63]\n"
"v_cmp_gt_i32_e64 vcc,51,v254\n"
MF"v[172:175],a[180:183],a[92:95],v[172:175]\n"
SA"s60,s60,s35\n"
RT"a[140:141],v245 offset:8192\n"
"v_cndmask_b32_e64 v223,0,v255,vcc\n"
MF"v[176:179],a[184:187],a[80:83],v[176:179]\n"
"s_lshl_b32 s59,s59,15\n"
BL"v225,s[8:11],s48 offen offset:1024 sc1 lds\n"
MF"v[184:187],a[184:187],a[84:87],v[184:187]\n"
"s_lshl_b32 s60,s60,15\n"
RT"a[142:143],v245 offset:8704\n"
SA"s50,s36,42729472\n"
MF"v[192:195],a[184:187],a[88:91],v[192:195]\n"
SA"s59,s59,s50\n"
SA"s60,s60,s50\n"
"s_lshl_b32 s61,s34,3\n"
MF"v[200:203],a[184:187],a[92:95],v[200:203]\n"
WT"lgkmcnt(3)\n"
RT"a[144:145],v242 offset:12288\n"
SA"s61,s61,s32\n"
MF"v[180:183],a[188:191],a[80:83],v[180:183]\n"
"s_lshl_b32 s61,s61,2\n"
SA"s61,s61,s30\n"
MF"v[188:191],a[188:191],a[84:87],v[188:191]\n"
"s_lshl_b32 s61,s61,2\n"
RT"a[146:147],v242 offset:12800\n"
SA"s61,s61,s33\n"
MF"v[196:199],a[188:191],a[88:91],v[196:199]\n"
"s_lshl_b32 s61,s61,6\n"
MF"v[204:207],a[188:191],a[92:95],v[204:207]\n"
SA"s61,s61,81002496\n"
RT"a[148:149],v243 offset:12288\n"
"s_lshl_b32 s98,s35,3\n"
MF"v[80:83],a[128:131],a[96:99],v[80:83]\n"
SA"s98,s98,s34\n"
BL"v225,s[8:11],s48 offen offset:2048 sc1 lds\n"
MF"v[88:91],a[128:131],a[100:103],v[88:91]\n"
"s_lshl_b32 s98,s98,3\n"
RT"a[150:151],v243 offset:12800\n"
MF"v[96:99],a[128:131],a[104:107],v[96:99]\n"
SA"s98,s98,4352\n"
RT"a[152:153],v244 offset:12288\n"
SA"s98,s98,s45\n"
MF"v[104:107],a[128:131],a[108:111],v[104:107]\n"
"s_lshl_b32 s50,s34,2\n"
MF"v[84:87],a[132:135],a[96:99],v[84:87]\n"
"s_mul_i32 s51,s33,0x1011100\n"
RT"a[154:155],v244 offset:12800\n"
SA"s51,s51,0xf48c00\n"
MF"v[92:95],a[132:135],a[100:103],v[92:95]\n"
"s_lshr_b32 s51,s51,s50\n"
MF"v[100:103],a[132:135],a[104:107],v[100:103]\n"
"s_and_b32 s51,s51,15\n"
RT"a[156:157],v245 offset:12288\n"
"s_lshr_b32 s50,s31,3\n"
MF"v[108:111],a[132:135],a[108:111],v[108:111]\n"
WT"lgkmcnt(4)\n"
"s_cmp_eq_u32 s50,6\n"
"s_cselect_b32 s51,15,s51\n"
MF"v[112:115],a[136:139],a[96:99],v[112:115]\n"
"s_cmp_eq_u32 s50,26\n"
RT"a[158:159],v245 offset:12800\n"
"s_cselect_b32 s51,0,s51\n"
MF"v[120:123],a[136:139],a[100:103],v[120:123]\n"
"s_mul_i32 s58,s32,14\n"
BL"v225,s[8:11],s48 offen offset:3072 sc1 lds\n"
MF"v[128:131],a[136:139],a[104:107],v[128:131]\n"
SA"s58,s58,s51\n"
RT"a[160:161],v242 offset:32768\n"
"s_sub_u32 s58,s58,1\n"
MF"v[136:139],a[136:139],a[108:111],v[136:139]\n"
"s_cmp_lt_u32 s58,108\n"
"s_cselect_b32 s90,s51,0\n"
"s_cmp_lg_u32 s90,0\n"
MF"v[116:119],a[140:143],a[96:99],v[116:119]\n"
"s_cselect_b32 s57,1,0\n"
RT"a[162:163],v242 offset:33280\n"
"s_cmp_eq_u32 s51,15\n"
MF"v[124:127],a[140:143],a[100:103],v[124:127]\n"
"s_cselect_b32 s57,2,s57\n"
"s_cmp_eq_u32 s50,6\n"
"s_cselect_b32 s90,4,s33\n"
MF"v[132:135],a[140:143],a[104:107],v[132:135]\n"
"s_mul_i32 s50,s32,5\n"
RT"a[164:165],v243 offset:32768\n"
SA"m0,s36,49152\n"
MF"v[140:143],a[140:143],a[108:111],v[140:143]\n"
SA"s48,s39,532480\n"
SA"s90,s90,s50\n"
"s_cmp_eq_u32 s57,2\n"
MF"v[144:147],a[144:147],a[96:99],v[144:147]\n"
"s_cselect_b32 s58,s90,s58\n"
RT"a[166:167],v243 offset:33280\n"
"s_lshl_b32 s56,s34,1\n"
MF"v[152:155],a[144:147],a[100:103],v[152:155]\n"
"s_sub_u32 s56,s56,2\n"
BL"v225,s[8:11],s48 offen offset:0 sc1 lds\n"
MF"v[160:163],a[144:147],a[104:107],v[160:163]\n"
"s_cmp_eq_u32 s34,7\n"
RT"a[168:169],v244 offset:32768\n"
"s_cselect_b32 s56,3,s56\n"
MF"v[168:171],a[144:147],a[108:111],v[168:171]\n"
WT"lgkmcnt(3)\n"
"s_cmp_eq_u32 s57,2\n"
"s_cselect_b32 s56,5,s56\n"
MF"v[148:151],a[148:151],a[96:99],v[148:151]\n"
"s_cmp_lt_u32 s30,2\n"
RT"a[170:171],v244 offset:33280\n"
"s_cselect_b64 s[24:25],s[78:79],s[80:81]\n"
MF"v[156:159],a[148:151],a[100:103],v[156:159]\n"
"s_cmp_eq_u32 s57,2\n"
BS"v[68:71],v250,s[8:11],s49 offen offset:1024 sc1\n"
MF"v[164:167],a[148:151],a[104:107],v[164:167]\n"
"s_cselect_b64 s[24:25],s[82:83],s[24:25]\n"
RT"a[172:173],v245 offset:32768\n"
"s_and_b32 s50,s30,1\n"
MF"v[172:175],a[148:151],a[108:111],v[172:175]\n"
"s_mul_i32 s50,s50,10\n"
RT"a[174:175],v245 offset:33280\n"
"s_mul_i32 s51,s30,14\n"
MF"v[176:179],a[152:155],a[96:99],v[176:179]\n"
"s_min_u32 s51,s51,41\n"
BL"v225,s[8:11],s48 offen offset:1024 sc1 lds\n"
MF"v[184:187],a[152:155],a[100:103],v[184:187]\n"
"s_cmp_eq_u32 s57,2\n"
RT"a[176:177],v242 offset:36864\n"
"s_cselect_b32 s50,s51,s50\n"
MF"v[192:195],a[152:155],a[104:107],v[192:195]\n"
"s_cmp_lt_u32 s30,2\n"
"s_cselect_b32 s51,14,13\n"
"s_cmp_lt_u32 s58,108\n"
MF"v[200:203],a[152:155],a[108:111],v[200:203]\n"
"s_cselect_b32 s92,10,0\n"
RT"a[178:179],v242 offset:37376\n"
"s_cmp_eq_u32 s57,2\n"
MF"v[180:183],a[156:159],a[96:99],v[180:183]\n"
"s_cselect_b32 s92,s51,s92\n"
"s_mov_b32 s51,3456\n"
"s_cmp_eq_u32 s57,2\n"
MF"v[188:191],a[156:159],a[100:103],v[188:191]\n"
"s_cselect_b32 s51,1280,s51\n"
RT"a[180:181],v243 offset:36864\n"
"s_lshl_b32 s62,s51,5\n"
MF"v[196:199],a[156:159],a[104:107],v[196:199]\n"
"s_mul_i32 s26,s62,s92\n"
"s_mul_i32 s90,s62,s50\n"
"s_lshl_b32 s91,s58,5\n"
MF"v[204:207],a[156:159],a[108:111],v[204:207]\n"
SA"s90,s90,s91\n"
RT"a[182:183],v243 offset:37376\n"
SA"s24,s24,s90\n"
MF"v[80:83],a[160:163],a[112:115],v[80:83]\n"
"s_addc_u32 s25,s25,0\n"
BL"v225,s[8:11],s48 offen offset:2048 sc1 lds\n"
MF"v[88:91],a[160:163],a[116:119],v[88:91]\n"
"s_and_b32 s25,s25,0xffff\n"
RT"a[184:185],v244 offset:36864\n"
"s_sub_u32 s90,s51,32\n"
MF"v[96:99],a[160:163],a[120:123],v[96:99]\n"
"s_sub_u32 s26,s26,s90\n"
"v_lshrrev_b32 v235,5,v224\n"
"v_mul_u32_u24 v235,s51,v235\n"
MF"v[104:107],a[160:163],a[124:127],v[104:107]\n"
WT"lgkmcnt(3)\n"
RT"a[186:187],v244 offset:37376\n"
"v_and_b32 v239,16,v224\n"
MF"v[84:87],a[164:167],a[112:115],v[84:87]\n"
"s_cselect_b32 s26,0,s26\n"
"v_add_u32 v235,v235,v239\n"
"s_mov_b32 s27,0x20000\n"
MF"v[92:95],a[164:167],a[116:119],v[92:95]\n"
"s_mul_i32 s63,s35,20480\n"
RT"a[188:189],v245 offset:36864\n"
SA"s63,s63,2770944\n"
MF"v[100:103],a[164:167],a[120:123],v[100:103]\n"
"s_cmp_eq_u32 s57,2\n"
"s_cselect_b32 s63,2695168,s63\n"
MF"v[108:111],a[164:167],a[124:127],v[108:111]\n"
"s_lshl_b32 s50,s50,10\n"
RT"a[190:191],v245 offset:37376\n"
SA"s63,s63,s50\n"
MF"v[112:115],a[168:171],a[112:115],v[112:115]\n"
"s_mov_b32 s50,1065353216\n"
BL"v225,s[8:11],s48 offen offset:3072 sc1 lds\n"
MF"v[120:123],a[168:171],a[116:119],v[120:123]\n"
"s_cmp_lg_u32 s92,0\n"
"s_cselect_b32 s50,s45,s50\n"
"s_mov_b32 s93,2695168\n"
MF"v[128:131],a[168:171],a[120:123],v[128:131]\n"
"s_cmp_eq_u32 s57,2\n"
"s_cselect_b32 s93,2750464,s93\n"
MF"v[136:139],a[168:171],a[124:127],v[136:139]\n"
"s_lshl_b32 s51,s58,9\n"
MF"v[116:119],a[172:175],a[112:115],v[116:119]\n"
SA"s93,s93,s51\n"
SA"s93,s93,s50\n"
MF"v[124:127],a[172:175],a[116:119],v[124:127]\n"
"s_mov_b32 s94,6912\n"
"s_barrier\n"
MF"v[132:135],a[172:175],a[120:123],v[132:135]\n"
"s_cmp_eq_u32 s57,2\n"
RT"a[128:129],v246 offset:16384\n"
"s_cselect_b32 s94,6400,s94\n"
MF"v[140:143],a[172:175],a[124:127],v[140:143]\n"
"s_lshl_b32 s51,s58,3\n"
SA"s94,s94,s51\n"
MF"v[144:147],a[176:179],a[112:115],v[144:147]\n"
SA"s94,s94,s50\n"
RT"a[130:131],v246 offset:16896\n"
"s_mov_b32 s52,0\n"
MF"v[152:155],a[176:179],a[116:119],v[152:155]\n"
"s_lshl_b32 s50,s35,6\n"
RT"a[132:133],v247 offset:16384\n"
MF"v[160:163],a[176:179],a[120:123],v[160:163]\n"
SA"s50,s50,2304\n"
MF"v[168:171],a[176:179],a[124:127],v[168:171]\n"
WT"lgkmcnt(3)\n"
RT"a[134:135],v247 offset:16896\n"
"s_cmp_eq_u32 s57,2\n"
MF"v[148:151],a[180:183],a[112:115],v[148:151]\n"
"s_cselect_b32 s50,6912,s50\n"
RT"a[136:137],v246 offset:40960\n"
"s_mov_b32 s51,48\n"
MF"v[156:159],a[180:183],a[116:119],v[156:159]\n"
SA"s49,s88,278528\n"
"s_cmp_eq_u32 s57,2\n"
"s_cselect_b32 s51,848,s51\n"
MF"v[164:167],a[180:183],a[120:123],v[164:167]\n"
RT"a[138:139],v246 offset:41472\n"
"v_min_u32 v238,s51,v224\n"
MF"v[172:175],a[180:183],a[124:127],v[172:175]\n"
"s_mov_b32 s16,0\n"
BS"v[72:75],v250,s[8:11],s49 offen offset:0 sc1\n"
MF"v[176:179],a[184:187],a[112:115],v[176:179]\n"
"s_mov_b32 s20,8\n"
RT"a[140:141],v247 offset:40960\n"
"v_add_u32 v238,s50,v238\n"
MF"v[184:187],a[184:187],a[116:119],v[184:187]\n"
"s_mov_b32 s18,2130706432\n"
RT"a[142:143],v247 offset:41472\n"
"v_and_b32 v239,0x80,v224\n"
MF"v[192:195],a[184:187],a[120:123],v[192:195]\n"
"s_lshl_b32 s21,s32,4\n"
"v_lshl_add_u32 v236,v239,23,v224\n"
"s_lshr_b32 s22,s30,1\n"
MF"v[200:203],a[184:187],a[124:127],v[200:203]\n"
SA"s21,s21,s22\n"
RT"a[144:145],v242 offset:16384\n"
MF"v[180:183],a[188:191],a[112:115],v[180:183]\n"
"s_mul_i32 s21,s21,40960\n"
"s_lshl_b32 s22,s33,1\n"
MF"v[188:191],a[188:191],a[116:119],v[188:191]\n"
"s_and_b32 s23,s30,1\n"
RT"a[146:147],v242 offset:16896\n"
MF"v[196:199],a[188:191],a[120:123],v[196:199]\n"
SA"s22,s22,s23\n"
RT"a[148:149],v243 offset:16384\n"
"s_mul_i32 s22,s22,160\n"
MF"v[204:207],a[188:191],a[124:127],v[204:207]\n"
WT"lgkmcnt(4)\n"
SA"s17,s21,s22\n"
MF"v[80:83],a[128:131],a[192:195],v[80:83]\n"
RT"a[150:151],v243 offset:16896\n"
"v_lshrrev_b32 v240,4,v224\n"
MF"v[88:91],a[128:131],a[196:199],v[88:91]\n"
WT"vmcnt(35)\n"
RT"a[152:153],v242 offset:40960\n"
"v_add_u32 v240,0,v240\n"
MF"v[96:99],a[128:131],a[200:203],v[96:99]\n"
WT"vmcnt(34)\n"
"v_mul_u32_u24 v240,205,v240\n"
"v_lshrrev_b32 v240,11,v240\n"
MF"v[104:107],a[128:131],a[204:207],v[104:107]\n"
RT"a[154:155],v242 offset:41472\n"
"v_mul_u32_u24 v240,1120,v240\n"
MF"v[84:87],a[132:135],a[192:195],v[84:87]\n"
RT"a[156:157],v243 offset:40960\n"
"v_add_u32 v240,v240,v224\n"
MF"v[92:95],a[132:135],a[196:199],v[92:95]\n"
"v_lshrrev_b32 v241,4,v224\n"
"v_add_u32 v241,64,v241\n"
MF"v[100:103],a[132:135],a[200:203],v[100:103]\n"
RT"a[158:159],v243 offset:41472\n"
"v_mul_u32_u24 v241,205,v241\n"
MF"v[108:111],a[132:135],a[204:207],v[108:111]\n"
WT"vmcnt(32)\n"
"v_lshrrev_b32 v241,11,v241\n"
"v_mul_u32_u24 v241,1120,v241\n"
MF"v[80:83],a[136:139],a[208:211],v[80:83]\n"
WT"vmcnt(31)\n"
RT"a[160:161],v248 offset:16384\n"
"v_add_u32 v241,v241,v224\n"
MF"v[88:91],a[136:139],a[212:215],v[88:91]\n"
WT"vmcnt(30)\n"
"v_lshrrev_b32 v251,4,v224\n"
"v_add_u32 v251,128,v251\n"
MF"v[96:99],a[136:139],a[216:219],v[96:99]\n"
WT"vmcnt(29)\n"
RT"a[162:163],v248 offset:16896\n"
"v_mul_u32_u24 v251,205,v251\n"
MF"v[104:107],a[136:139],a[220:223],v[104:107]\n"
WT"lgkmcnt(3)\n"
RT"a[164:165],v249 offset:16384\n"
"v_lshrrev_b32 v251,11,v251\n"
MF"v[84:87],a[140:143],a[208:211],v[84:87]\n"
"v_mul_u32_u24 v251,1120,v251\n"
"v_add_u32 v251,v251,v224\n"
MF"v[92:95],a[140:143],a[212:215],v[92:95]\n"
RT"a[166:167],v249 offset:16896\n"
"v_and_b32 v237,512,v224\n"
MF"v[100:103],a[140:143],a[216:219],v[100:103]\n"
RT"a[168:169],v248 offset:40960\n"
"v_lshl_add_u32 v251,v237,20,v251\n"
MF"v[108:111],a[140:143],a[220:223],v[108:111]\n"
WT"vmcnt(28)\n"
"v_lshrrev_b32 v252,6,v224\n"
"v_mul_u32_u24 v252,160,v252\n"
MF"v[80:83],a[144:147],a[224:227],v[80:83]\n"
WT"vmcnt(25)\n"
RT"a[170:171],v248 offset:41472\n"
"v_bfe_u32 v237,v224,4,2\n"
MF"v[88:91],a[144:147],a[228:231],v[88:91]\n"
WT"vmcnt(23)\n"
RT"a[172:173],v249 offset:40960\n"
"v_lshl_add_u32 v252,v237,3,v252\n"
MF"v[96:99],a[144:147],a[232:235],v[96:99]\n"
WT"vmcnt(21)\n"
"v_lshrrev_b32 v254,4,v224\n"
"v_min_u32 v254,49,v254\n"
MF"v[104:107],a[144:147],a[236:239],v[104:107]\n"
RT"a[174:175],v249 offset:41472\n"
"v_mul_u32_u24 v253,52,v254\n"
MF"v[84:87],a[148:151],a[224:227],v[84:87]\n"
"v_lshrrev_b32 v253,8,v253\n"
MF"v[92:95],a[148:151],a[228:231],v[92:95]\n"
RT"a[176:177],v244 offset:16384\n"
"v_mul_u32_u24 v253,2901,v253\n"
MF"v[100:103],a[148:151],a[232:235],v[100:103]\n"
"v_mul_u32_u24 v254,111,v254\n"
"v_add_u32 v254,v254,v253\n"
MF"v[108:111],a[148:151],a[236:239],v[108:111]\n"
WT"vmcnt(20)\n"
RT"a[178:179],v244 offset:16896\n"
"v_and_b32 v254,-4,v254\n"
MF"v[80:83],a[152:155],a[240:243],v[80:83]\n"
WT"vmcnt(18)\n"
RT"a[180:181],v245 offset:16384\n"
"v_lshrrev_b32 v253,5,v224\n"
MF"v[88:91],a[152:155],a[244:247],v[88:91]\n"
WT"vmcnt(15)\n"
BS"v[76:79],v250,s[8:11],s49 offen offset:1024 sc1\n"
MF"v[96:99],a[152:155],a[248:251],v[96:99]\n"
WT"vmcnt(14)\n"
RT"a[182:183],v245 offset:16896\n"
"v_min_u32 v253,13,v253\n"
MF"v[104:107],a[152:155],a[252:255],v[104:107]\n"
WT"lgkmcnt(4)\n"
RT"a[184:185],v244 offset:40960\n"
"v_mul_u32_u24 v253,1280,v253\n"
MF"v[84:87],a[156:159],a[240:243],v[84:87]\n"
"v_bfe_u32 v237,v224,4,1\n"
"v_mul_u32_u24 v237,156,v237\n"
MF"v[92:95],a[156:159],a[244:247],v[92:95]\n"
"s_cmp_lt_u32 s30,2\n"
RT"a[186:187],v244 offset:41472\n"
"v_add_u32 v253,v253,v237\n"
MF"v[100:103],a[156:159],a[248:251],v[100:103]\n"
"s_cselect_b64 s[12:13],s[78:79],s[80:81]\n"
RT"a[188:189],v245 offset:40960\n"
"s_and_b32 s13,s13,0xffff\n"
MF"v[108:111],a[156:159],a[252:255],v[108:111]\n"
"s_mov_b32 s14,2211840\n"
"s_mov_b32 s15,0x20000\n"
MF"v[112:115],a[160:163],a[192:195],v[112:115]\n"
"s_lshr_b32 s90,s31,3\n"
RT"a[190:191],v245 offset:41472\n"
MF"v[120:123],a[160:163],a[196:199],v[120:123]\n"
"s_lshl_b32 s91,s90,1\n"
MF"v[128:131],a[160:163],a[200:203],v[128:131]\n"
"s_and_b32 s50,s30,1\n"
RT"a[128:129],v246 offset:20480\n"
PK"v80,v80,v81\n"
MF"v[136:139],a[160:163],a[204:207],v[136:139]\n"
SA"s91,s91,s50\n"
PK"v81,v82,v83\n"
PK"v82,v84,v85\n"
MF"v[116:119],a[164:167],a[192:195],v[116:119]\n"
"s_mul_i32 s91,s91,34560\n"
RT"a[130:131],v246 offset:20992\n"
PK"v83,v86,v87\n"
MF"v[124:127],a[164:167],a[196:199],v[124:127]\n"
"s_mul_i32 s50,s32,448\n"
RT"a[132:133],v247 offset:20480\n"
"v_accvgpr_write_b32 a0,v80\n"
MF"v[132:135],a[164:167],a[200:203],v[132:135]\n"
SA"s91,s91,s50\n"
"v_accvgpr_write_b32 a1,v81\n"
"v_accvgpr_write_b32 a2,v82\n"
MF"v[140:143],a[164:167],a[204:207],v[140:143]\n"
"buffer_load_dword v255,v254,s[12:15],s91 offen\n"
MF"v[112:115],a[168:171],a[208:211],v[112:115]\n"
"s_mov_b32 s12,s82\n"
RT"a[134:135],v247 offset:20992\n"
"v_accvgpr_write_b32 a3,v83\n"
MF"v[120:123],a[168:171],a[212:215],v[120:123]\n"
"s_and_b32 s13,s83,0xffff\n"
RT"a[136:137],v246 offset:45056\n"
PK"v88,v88,v89\n"
MF"v[128:131],a[168:171],a[216:219],v[128:131]\n"
"s_lshl_b32 s90,s90,2\n"
RT"a[138:139],v246 offset:45568\n"
PK"v89,v90,v91\n"
MF"v[136:139],a[168:171],a[220:223],v[136:139]\n"
SA"s90,s90,s30\n"
RT"a[140:141],v247 offset:45056\n"
PK"v90,v92,v93\n"
MF"v[116:119],a[172:175],a[208:211],v[116:119]\n"
"s_mul_i32 s90,s90,17920\n"
PK"v91,v94,v95\n"
"v_accvgpr_write_b32 a16,v88\n"
MF"v[124:127],a[172:175],a[212:215],v[124:127]\n"
"s_mul_i32 s50,s32,160\n"
RT"a[142:143],v247 offset:45568\n"
"v_accvgpr_write_b32 a17,v89\n"
MF"v[132:135],a[172:175],a[216:219],v[132:135]\n"
SA"s90,s90,s50\n"
"v_accvgpr_write_b32 a18,v90\n"
"v_accvgpr_write_b32 a19,v91\n"
MF"v[140:143],a[172:175],a[220:223],v[140:143]\n"
WT"lgkmcnt(4)\n"
"buffer_load_dword v255,v253,s[12:15],s90 offen\n"
MF"v[112:115],a[176:179],a[224:227],v[112:115]\n"
RT"a[144:145],v242 offset:20480\n"
"v_min_u32 v253,624,v224\n"
MF"v[120:123],a[176:179],a[228:231],v[120:123]\n"
RT"a[146:147],v242 offset:20992\n"
"v_lshlrev_b32 v253,3,v253\n"
MF"v[128:131],a[176:179],a[232:235],v[128:131]\n"
RT"a[148:149],v243 offset:20480\n"
PK"v96,v96,v97\n"
MF"v[136:139],a[176:179],a[236:239],v[136:139]\n"
PK"v97,v98,v99\n"
PK"v98,v100,v101\n"
MF"v[116:119],a[180:183],a[224:227],v[116:119]\n"
RT"a[150:151],v243 offset:20992\n"
PK"v99,v102,v103\n"
MF"v[124:127],a[180:183],a[228:231],v[124:127]\n"
RT"a[152:153],v242 offset:45056\n"
"v_accvgpr_write_b32 a32,v96\n"
MF"v[132:135],a[180:183],a[232:235],v[132:135]\n"
"v_accvgpr_write_b32 a33,v97\n"
"v_accvgpr_write_b32 a34,v98\n"
MF"v[140:143],a[180:183],a[236:239],v[140:143]\n"
RT"a[154:155],v242 offset:45568\n"
"v_accvgpr_write_b32 a35,v99\n"
MF"v[112:115],a[184:187],a[240:243],v[112:115]\n"
RT"a[156:157],v243 offset:45056\n"
PK"v104,v104,v105\n"
MF"v[120:123],a[184:187],a[244:247],v[120:123]\n"
PK"v105,v106,v107\n"
PK"v106,v108,v109\n"
MF"v[128:131],a[184:187],a[248:251],v[128:131]\n"
RT"a[158:159],v243 offset:45568\n"
PK"v107,v110,v111\n"
MF"v[136:139],a[184:187],a[252:255],v[136:139]\n"
"v_accvgpr_write_b32 a48,v104\n"
"v_accvgpr_write_b32 a49,v105\n"
MF"v[116:119],a[188:191],a[240:243],v[116:119]\n"
RT"a[160:161],v248 offset:20480\n"
"v_accvgpr_write_b32 a50,v106\n"
MF"v[124:127],a[188:191],a[244:247],v[124:127]\n"
"v_accvgpr_write_b32 a51,v107\n"
MF"v[132:135],a[188:191],a[248:251],v[132:135]\n"
RT"a[162:163],v248 offset:20992\n"
MF"v[140:143],a[188:191],a[252:255],v[140:143]\n"
RT"a[164:165],v249 offset:20480\n"
MF"v[144:147],a[128:131],a[192:195],v[144:147]\n"
MF"v[152:155],a[128:131],a[196:199],v[152:155]\n"
RT"a[166:167],v249 offset:20992\n"
MF"v[160:163],a[128:131],a[200:203],v[160:163]\n"
RT"a[168:169],v248 offset:45056\n"
PK"v112,v112,v113\n"
MF"v[168:171],a[128:131],a[204:207],v[168:171]\n"
PK"v113,v114,v115\n"
PK"v114,v116,v117\n"
MF"v[148:151],a[132:135],a[192:195],v[148:151]\n"
RT"a[170:171],v248 offset:45568\n"
PK"v115,v118,v119\n"
MF"v[156:159],a[132:135],a[196:199],v[156:159]\n"
RT"a[172:173],v249 offset:45056\n"
"v_accvgpr_write_b32 a4,v112\n"
MF"v[164:167],a[132:135],a[200:203],v[164:167]\n"
"v_accvgpr_write_b32 a5,v113\n"
"v_accvgpr_write_b32 a6,v114\n"
MF"v[172:175],a[132:135],a[204:207],v[172:175]\n"
WT"lgkmcnt(4)\n"
RT"a[174:175],v249 offset:45568\n"
"v_accvgpr_write_b32 a7,v115\n"
MF"v[144:147],a[136:139],a[208:211],v[144:147]\n"
PK"v120,v120,v121\n"
PK"v121,v122,v123\n"
MF"v[152:155],a[136:139],a[212:215],v[152:155]\n"
RT"a[176:177],v244 offset:20480\n"
PK"v122,v124,v125\n"
MF"v[160:163],a[136:139],a[216:219],v[160:163]\n"
PK"v123,v126,v127\n"
"v_accvgpr_write_b32 a20,v120\n"
MF"v[168:171],a[136:139],a[220:223],v[168:171]\n"
RT"a[178:179],v244 offset:20992\n"
"v_accvgpr_write_b32 a21,v121\n"
MF"v[148:151],a[140:143],a[208:211],v[148:151]\n"
RT"a[180:181],v245 offset:20480\n"
"v_accvgpr_write_b32 a22,v122\n"
MF"v[156:159],a[140:143],a[212:215],v[156:159]\n"
"v_accvgpr_write_b32 a23,v123\n"
MF"v[164:167],a[140:143],a[216:219],v[164:167]\n"
RT"a[182:183],v245 offset:20992\n"
PK"v128,v128,v129\n"
MF"v[172:175],a[140:143],a[220:223],v[172:175]\n"
RT"a[184:185],v244 offset:45056\n"
PK"v129,v130,v131\n"
MF"v[144:147],a[144:147],a[224:227],v[144:147]\n"
PK"v130,v132,v133\n"
PK"v131,v134,v135\n"
MF"v[152:155],a[144:147],a[228:231],v[152:155]\n"
RT"a[186:187],v244 offset:45568\n"
"v_accvgpr_write_b32 a36,v128\n"
MF"v[160:163],a[144:147],a[232:235],v[160:163]\n"
RT"a[188:189],v245 offset:45056\n"
"v_accvgpr_write_b32 a37,v129\n"
MF"v[168:171],a[144:147],a[236:239],v[168:171]\n"
"v_accvgpr_write_b32 a38,v130\n"
"v_accvgpr_write_b32 a39,v131\n"
MF"v[148:151],a[148:151],a[224:227],v[148:151]\n"
RT"a[190:191],v245 offset:45568\n"
PK"v136,v136,v137\n"
MF"v[156:159],a[148:151],a[228:231],v[156:159]\n"
PK"v137,v138,v139\n"
PK"v138,v140,v141\n"
MF"v[164:167],a[148:151],a[232:235],v[164:167]\n"
PK"v139,v142,v143\n"
"v_accvgpr_write_b32 a52,v136\n"
MF"v[172:175],a[148:151],a[236:239],v[172:175]\n"
"v_accvgpr_write_b32 a53,v137\n"
"v_accvgpr_write_b32 a54,v138\n"
MF"v[144:147],a[152:155],a[240:243],v[144:147]\n"
"v_accvgpr_write_b32 a55,v139\n"
MF"v[152:155],a[152:155],a[244:247],v[152:155]\n"
MF"v[160:163],a[152:155],a[248:251],v[160:163]\n"
MF"v[168:171],a[152:155],a[252:255],v[168:171]\n"
MF"v[148:151],a[156:159],a[240:243],v[148:151]\n"
MF"v[156:159],a[156:159],a[244:247],v[156:159]\n"
MF"v[164:167],a[156:159],a[248:251],v[164:167]\n"
MF"v[172:175],a[156:159],a[252:255],v[172:175]\n"
MF"v[176:179],a[160:163],a[192:195],v[176:179]\n"
WT"lgkmcnt(0)\n"
MF"v[180:183],a[164:167],a[192:195],v[180:183]\n"
MF"v[176:179],a[168:171],a[208:211],v[176:179]\n"
PK"v144,v144,v145\n"
PK"v145,v146,v147\n"
MF"v[180:183],a[172:175],a[208:211],v[180:183]\n"
PK"v146,v148,v149\n"
PK"v147,v150,v151\n"
MF"v[176:179],a[176:179],a[224:227],v[176:179]\n"
"v_mov_b32 v254,0x3f803f80\n"
"v_accvgpr_write_b32 a8,v144\n"
MF"v[180:183],a[180:183],a[224:227],v[180:183]\n"
"v_accvgpr_write_b32 a9,v145\n"
"v_accvgpr_write_b32 a128,v254\n"
MF"v[176:179],a[184:187],a[240:243],v[176:179]\n"
"v_accvgpr_write_b32 a129,v254\n"
"v_accvgpr_write_b32 a130,v254\n"
MF"v[180:183],a[188:191],a[240:243],v[180:183]\n"
"v_accvgpr_write_b32 a131,v254\n"
"v_accvgpr_write_b32 a10,v146\n"
MF"v[184:187],a[160:163],a[196:199],v[184:187]\n"
"v_accvgpr_write_b32 a11,v147\n"
PK"v152,v152,v153\n"
MF"v[188:191],a[164:167],a[196:199],v[188:191]\n"
PK"v153,v154,v155\n"
PK"v154,v156,v157\n"
MF"v[184:187],a[168:171],a[212:215],v[184:187]\n"
PK"v155,v158,v159\n"
"v_accvgpr_write_b32 a24,v152\n"
MF"v[188:191],a[172:175],a[212:215],v[188:191]\n"
"v_accvgpr_write_b32 a25,v153\n"
PK"v176,v176,v177\n"
MF"v[184:187],a[176:179],a[228:231],v[184:187]\n"
PK"v177,v178,v179\n"
"v_accvgpr_write_b32 a26,v154\n"
MF"v[188:191],a[180:183],a[228:231],v[188:191]\n"
"v_accvgpr_write_b32 a27,v155\n"
PK"v178,v180,v181\n"
MF"v[184:187],a[184:187],a[244:247],v[184:187]\n"
PK"v179,v182,v183\n"
"v_accvgpr_write_b32 a12,v176\n"
MF"v[188:191],a[188:191],a[244:247],v[188:191]\n"
"v_accvgpr_write_b32 a13,v177\n"
"v_accvgpr_write_b32 a14,v178\n"
MF"v[192:195],a[160:163],a[200:203],v[192:195]\n"
"v_accvgpr_write_b32 a15,v179\n"
PK"v160,v160,v161\n"
MF"v[196:199],a[164:167],a[200:203],v[196:199]\n"
WT"vmcnt(14)\n"
"s_barrier\n"
MF"v[192:195],a[168:171],a[216:219],v[192:195]\n"
RD"a[64:67],v226 offset:0\n"
PK"v161,v162,v163\n"
MF"v[196:199],a[172:175],a[216:219],v[196:199]\n"
PK"v162,v164,v165\n"
PK"v163,v166,v167\n"
MF"v[192:195],a[176:179],a[232:235],v[192:195]\n"
RD"a[68:71],v226 offset:1024\n"
"v_accvgpr_write_b32 a40,v160\n"
MF"v[196:199],a[180:183],a[232:235],v[196:199]\n"
"v_accvgpr_write_b32 a41,v161\n"
"v_accvgpr_write_b32 a42,v162\n"
MF"v[192:195],a[184:187],a[248:251],v[192:195]\n"
RD"a[72:75],v226 offset:2048\n"
"v_accvgpr_write_b32 a43,v163\n"
MF"v[196:199],a[188:191],a[248:251],v[196:199]\n"
PK"v184,v184,v185\n"
PK"v185,v186,v187\n"
MF"v[200:203],a[160:163],a[204:207],v[200:203]\n"
RD"a[76:79],v226 offset:3072\n"
PK"v186,v188,v189\n"
MF"v[204:207],a[164:167],a[204:207],v[204:207]\n"
PK"v187,v190,v191\n"
PK"v168,v168,v169\n"
MF"v[200:203],a[168:171],a[220:223],v[200:203]\n"
RD"a[80:83],v226 offset:4096\n"
PK"v169,v170,v171\n"
MF"v[204:207],a[172:175],a[220:223],v[204:207]\n"
"v_accvgpr_write_b32 a28,v184\n"
"v_accvgpr_write_b32 a29,v185\n"
MF"v[200:203],a[176:179],a[236:239],v[200:203]\n"
RD"a[84:87],v226 offset:5120\n"
PK"v170,v172,v173\n"
MF"v[204:207],a[180:183],a[236:239],v[204:207]\n"
PK"v171,v174,v175\n"
"v_accvgpr_write_b32 a30,v186\n"
MF"v[200:203],a[184:187],a[252:255],v[200:203]\n"
"v_accvgpr_write_b32 a31,v187\n"
"v_accvgpr_write_b32 a56,v168\n"
MF"v[204:207],a[188:191],a[252:255],v[204:207]\n"
"v_accvgpr_write_b32 a57,v169\n"
"v_accvgpr_write_b32 a58,v170\n"
"v_accvgpr_write_b32 a59,v171\n"
"s_cmp_eq_u32 s34,0\n"
"s_cbranch_scc1 LFA_%=\n"
WT"lgkmcnt(5)\n"
MF"v[0:3],a[64:67],a[0:3],0\n"
"s_mov_b32 m0,s40\n"
PK"v192,v192,v193\n"
PK"v193,v194,v195\n"
MF"v[8:11],a[64:67],a[16:19],0\n"
"v_readlane_b32 s43,v228,s42\n"
PK"v194,v196,v197\n"
MF"v[16:19],a[64:67],a[32:35],0\n"
PK"v195,v198,v199\n"
"v_readlane_b32 s44,v229,s42\n"
MF"v[24:27],a[64:67],a[48:51],0\n"
WT"lgkmcnt(0)\n"
BL"v225,s[8:11],s43 offen offset:0 sc1 lds\n"
MF"v[0:3],a[68:71],a[4:7],v[0:3]\n"
SA"s42,s42,1\n"
"v_accvgpr_write_b32 a44,v192\n"
"v_accvgpr_write_b32 a45,v193\n"
MF"v[8:11],a[68:71],a[20:23],v[8:11]\n"
"v_accvgpr_write_b32 a46,v194\n"
"v_accvgpr_write_b32 a47,v195\n"
MF"v[16:19],a[68:71],a[36:39],v[16:19]\n"
PK"v200,v200,v201\n"
PK"v201,v202,v203\n"
MF"v[24:27],a[68:71],a[52:55],v[24:27]\n"
PK"v202,v204,v205\n"
PK"v203,v206,v207\n"
"v_mov_b32 v227,v226\n"
MF"v[0:3],a[72:75],a[8:11],v[0:3]\n"
BL"v225,s[8:11],s43 offen offset:1024 sc1 lds\n"
MF"v[8:11],a[72:75],a[24:27],v[8:11]\n"
RD"a[88:91],v226 offset:6144\n"
"v_accvgpr_write_b32 a60,v200\n"
"v_accvgpr_write_b32 a61,v201\n"
MF"v[16:19],a[72:75],a[40:43],v[16:19]\n"
"s_mul_i32 s48,s32,655360\n"
"v_accvgpr_write_b32 a62,v202\n"
"v_accvgpr_write_b32 a63,v203\n"
MF"v[24:27],a[72:75],a[56:59],v[24:27]\n"
SA"s12,s74,s48\n"
"s_addc_u32 s13,s75,0\n"
MF"v[0:3],a[76:79],a[12:15],v[0:3]\n"
"s_and_b32 s13,s13,0xffff\n"
MF"v[8:11],a[76:79],a[28:31],v[8:11]\n"
"s_mov_b32 s14,655360\n"
RD"a[92:95],v226 offset:7168\n"
MF"v[16:19],a[76:79],a[44:47],v[16:19]\n"
"s_lshl_b32 s48,s33,3\n"
BL"v225,s[8:11],s43 offen offset:2048 sc1 lds\n"
MF"v[24:27],a[76:79],a[60:63],v[24:27]\n"
"s_and_b32 s49,s34,2\n"
MF"v[4:7],a[80:83],a[0:3],0\n"
"s_lshl_b32 s49,s49,1\n"
EX"v0,v0\n"
MF"v[12:15],a[80:83],a[16:19],0\n"
SA"s48,s48,s49\n"
RD"a[96:99],v226 offset:8192\n"
MF"v[20:23],a[80:83],a[32:35],0\n"
SA"s48,s48,s30\n"
EX"v1,v1\n"
MF"v[28:31],a[80:83],a[48:51],0\n"
"s_mul_i32 s48,s48,5120\n"
EX"v2,v2\n"
MF"v[4:7],a[84:87],a[4:7],v[4:7]\n"
"s_or_b32 s49,s34,2\n"
BL"v225,s[8:11],s43 offen offset:3072 sc1 lds\n"
MF"v[12:15],a[84:87],a[20:23],v[12:15]\n"
"s_cmp_eq_u32 s49,7\n"
EX"v3,v3\n"
MF"v[20:23],a[84:87],a[36:39],v[20:23]\n"
"s_cselect_b32 s55,s48,2130706432\n"
RD"a[100:103],v226 offset:9216\n"
PK"v0,v0,v1\n"
MF"v[28:31],a[84:87],a[52:55],v[28:31]\n"
WT"lgkmcnt(2)\n"
EX"v8,v8\n"
MF"v[4:7],a[88:91],a[8:11],v[4:7]\n"
SA"m0,s40,16384\n"
EX"v9,v9\n"
PK"v1,v2,v3\n"
MF"v[12:15],a[88:91],a[24:27],v[12:15]\n"
EX"v10,v10\n"
MF"v[20:23],a[88:91],a[40:43],v[20:23]\n"
RD"a[104:107],v226 offset:10240\n"
PK"v8,v8,v9\n"
MF"v[28:31],a[88:91],a[56:59],v[28:31]\n"
SA"s40,s40,32768\n"
BL"v225,s[8:11],s44 offen offset:0 sc1 lds\n"
MF"v[4:7],a[92:95],a[12:15],v[4:7]\n"
"s_cmp_eq_u32 s40,s46\n"
EX"v11,v11\n"
MF"v[12:15],a[92:95],a[28:31],v[12:15]\n"
"s_cselect_b32 s40,s47,s40\n"
EX"v16,v16\n"
MF"v[20:23],a[92:95],a[44:47],v[20:23]\n"
EX"v17,v17\n"
PK"v9,v10,v11\n"
MF"v[28:31],a[92:95],a[60:63],v[28:31]\n"
WT"lgkmcnt(1)\n"
RD"a[108:111],v226 offset:11264\n"
PK"v16,v16,v17\n"
MF"v[32:35],a[96:99],a[0:3],0\n"
EX"v18,v18\n"
MF"v[40:43],a[96:99],a[16:19],0\n"
EX"v19,v19\n"
MF"v[48:51],a[96:99],a[32:35],0\n"
BL"v225,s[8:11],s44 offen offset:1024 sc1 lds\n"
EX"v24,v24\n"
MF"v[56:59],a[96:99],a[48:51],0\n"
RD"a[112:115],v226 offset:12288\n"
PK"v17,v18,v19\n"
MF"v[32:35],a[100:103],a[4:7],v[32:35]\n"
EX"v25,v25\n"
MF"v[40:43],a[100:103],a[20:23],v[40:43]\n"
EX"v26,v26\n"
MF"v[48:51],a[100:103],a[36:39],v[48:51]\n"
EX"v27,v27\n"
PK"v24,v24,v25\n"
MF"v[56:59],a[100:103],a[52:55],v[56:59]\n"
WT"lgkmcnt(1)\n"
RD"a[116:119],v226 offset:13312\n"
PK"v25,v26,v27\n"
MF"v[32:35],a[104:107],a[8:11],v[32:35]\n"
BL"v225,s[8:11],s44 offen offset:2048 sc1 lds\n"
EX"v4,v4\n"
MF"v[40:43],a[104:107],a[24:27],v[40:43]\n"
EX"v5,v5\n"
MF"v[48:51],a[104:107],a[40:43],v[48:51]\n"
EX"v6,v6\n"
MF"v[56:59],a[104:107],a[56:59],v[56:59]\n"
RD"a[120:123],v226 offset:14336\n"
"v_readlane_b32 s43,v228,s42\n"
PK"v2,v4,v5\n"
MF"v[32:35],a[108:111],a[12:15],v[32:35]\n"
EX"v7,v7\n"
MF"v[40:43],a[108:111],a[28:31],v[40:43]\n"
EX"v12,v12\n"
MF"v[48:51],a[108:111],a[44:47],v[48:51]\n"
BL"v225,s[8:11],s44 offen offset:3072 sc1 lds\n"
EX"v13,v13\n"
MF"v[56:59],a[108:111],a[60:63],v[56:59]\n"
WT"lgkmcnt(1)\n"
RD"a[124:127],v226 offset:15360\n"
PK"v3,v6,v7\n"
MF"v[36:39],a[112:115],a[0:3],0\n"
"s_mov_b32 m0,s40\n"
EX"v14,v14\n"
PK"v10,v12,v13\n"
MF"v[44:47],a[112:115],a[16:19],0\n"
EX"v15,v15\n"
MF"v[52:55],a[112:115],a[32:35],0\n"
EX"v20,v20\n"
MF"v[60:63],a[112:115],a[48:51],0\n"
RD"a[96:99],v227 offset:24576\n"
"v_readlane_b32 s44,v229,s42\n"
PK"v11,v14,v15\n"
MF"v[36:39],a[116:119],a[4:7],v[36:39]\n"
SA"s42,s42,1\n"
BL"v225,s[8:11],s43 offen offset:0 sc1 lds\n"
MF"v[44:47],a[116:119],a[20:23],v[44:47]\n"
EX"v21,v21\n"
MF"v[52:55],a[116:119],a[36:39],v[52:55]\n"
EX"v22,v22\n"
MF"v[60:63],a[116:119],a[52:55],v[60:63]\n"
WT"lgkmcnt(1)\n"
RD"a[100:103],v227 offset:25600\n"
PK"v18,v20,v21\n"
MF"v[36:39],a[120:123],a[8:11],v[36:39]\n"
EX"v23,v23\n"
MF"v[44:47],a[120:123],a[24:27],v[44:47]\n"
BL"v225,s[8:11],s43 offen offset:1024 sc1 lds\n"
MF"v[52:55],a[120:123],a[40:43],v[52:55]\n"
EX"v28,v28\n"
PK"v19,v22,v23\n"
MF"v[60:63],a[120:123],a[56:59],v[60:63]\n"
RD"a[104:107],v227 offset:26624\n"
MF"v[36:39],a[124:127],a[12:15],v[36:39]\n"
EX"v29,v29\n"
MF"v[44:47],a[124:127],a[28:31],v[44:47]\n"
EX"v30,v30\n"
MF"v[52:55],a[124:127],a[44:47],v[52:55]\n"
EX"v31,v31\n"
PK"v26,v28,v29\n"
MF"v[60:63],a[124:127],a[60:63],v[60:63]\n"
WT"lgkmcnt(1)\n"
BL"v225,s[8:11],s43 offen offset:2048 sc1 lds\n"
RD"a[108:111],v227 offset:27648\n"
MF"v[80:83],a[96:99],v[0:3],0\n"
SA"s41,s41,32768\n"
PK"v27,v30,v31\n"
EX"v32,v32\n"
MF"v[112:115],a[96:99],v[8:11],0\n"
"s_cmp_eq_u32 s41,163840\n"
EX"v33,v33\n"
MF"v[144:147],a[96:99],v[16:19],0\n"
"s_cselect_b32 s41,0,s41\n"
EX"v34,v34\n"
"v_add_u32 v226,s41,v224\n"
MF"v[176:179],a[96:99],v[24:27],0\n"
RD"a[112:115],v227 offset:28672\n"
PK"v64,v32,v33\n"
MF"v[84:87],a[100:103],v[0:3],0\n"
EX"v35,v35\n"
MF"v[116:119],a[100:103],v[8:11],0\n"
BL"v225,s[8:11],s43 offen offset:3072 sc1 lds\n"
MF"v[148:151],a[100:103],v[16:19],0\n"
SA"m0,s40,16384\n"
EX"v40,v40\n"
PK"v65,v34,v35\n"
MF"v[180:183],a[100:103],v[24:27],0\n"
WT"lgkmcnt(1)\n"
RD"a[116:119],v227 offset:29696\n"
MF"v[88:91],a[104:107],v[0:3],0\n"
EX"v41,v41\n"
MF"v[120:123],a[104:107],v[8:11],0\n"
EX"v42,v42\n"
MF"v[152:155],a[104:107],v[16:19],0\n"
SA"s40,s40,32768\n"
EX"v43,v43\n"
PK"v68,v40,v41\n"
MF"v[184:187],a[104:107],v[24:27],0\n"
"s_cmp_eq_u32 s40,s46\n"
BL"v225,s[8:11],s44 offen offset:0 sc1 lds\n"
RD"a[120:123],v227 offset:30720\n"
MF"v[92:95],a[108:111],v[0:3],0\n"
"s_cselect_b32 s40,s47,s40\n"
EX"v48,v48\n"
PK"v69,v42,v43\n"
MF"v[124:127],a[108:111],v[8:11],0\n"
EX"v49,v49\n"
MF"v[156:159],a[108:111],v[16:19],0\n"
EX"v50,v50\n"
MF"v[188:191],a[108:111],v[24:27],0\n"
WT"lgkmcnt(1)\n"
RD"a[124:127],v227 offset:31744\n"
PK"v72,v48,v49\n"
MF"v[96:99],a[112:115],v[0:3],0\n"
EX"v51,v51\n"
MF"v[128:131],a[112:115],v[8:11],0\n"
BL"v225,s[8:11],s44 offen offset:1024 sc1 lds\n"
MF"v[160:163],a[112:115],v[16:19],0\n"
EX"v36,v36\n"
PK"v73,v50,v51\n"
MF"v[192:195],a[112:115],v[24:27],0\n"
EX"v37,v37\n"
MF"v[100:103],a[116:119],v[0:3],0\n"
EX"v38,v38\n"
MF"v[132:135],a[116:119],v[8:11],0\n"
EX"v39,v39\n"
PK"v66,v36,v37\n"
MF"v[164:167],a[116:119],v[16:19],0\n"
WT"vmcnt(16) lgkmcnt(0)\n"
"s_barrier\n"
MF"v[196:199],a[116:119],v[24:27],0\n"
BL"v225,s[8:11],s44 offen offset:2048 sc1 lds\n"
RD"a[64:67],v226 offset:0\n"
MF"v[104:107],a[120:123],v[0:3],0\n"
"buffer_store_dwordx2 v[232:233],v230,s[8:11],s98 offen sc1\n"
EX"v44,v44\n"
MF"v[136:139],a[120:123],v[8:11],0\n"
EX"v45,v45\n"
PK"v67,v38,v39\n"
MF"v[168:171],a[120:123],v[16:19],0\n"
EX"v46,v46\n"
MF"v[200:203],a[120:123],v[24:27],0\n"
RD"a[68:71],v226 offset:1024\n"
RD"a[72:75],v226 offset:2048\n"
PK"v70,v44,v45\n"
MF"v[108:111],a[124:127],v[0:3],0\n"
EX"v47,v47\n"
MF"v[140:143],a[124:127],v[8:11],0\n"
BL"v225,s[8:11],s44 offen offset:3072 sc1 lds\n"
MF"v[172:175],a[124:127],v[16:19],0\n"
EX"v52,v52\n"
PK"v71,v46,v47\n"
MF"v[204:207],a[124:127],v[24:27],0\n"
RD"a[76:79],v226 offset:3072\n"
MF"a[132:135],a[128:131],v[0:3],0\n"
EX"v53,v53\n"
MF"a[136:139],a[128:131],v[8:11],0\n"
EX"v54,v54\n"
MF"a[140:143],a[128:131],v[16:19],0\n"
EX"v55,v55\n"
PK"v74,v52,v53\n"
MF"a[144:147],a[128:131],v[24:27],0\n"
PK"v75,v54,v55\n"
RD"a[80:83],v226 offset:4096\n"
RD"a[84:87],v226 offset:5120\n"
"s_cmp_eq_u32 s57,1\n"
"s_cbranch_scc1 LEV_%=\n"
".p2align 6\n"
"LP1_%=:\n"
WT"lgkmcnt(5)\n"
MF"v[0:3],a[64:67],a[0:3],0\n"
EX"v56,v56\n"
MF"v[8:11],a[64:67],a[16:19],0\n"
"v_readlane_b32 s43,v228,s42\n"
MF"v[16:19],a[64:67],a[32:35],0\n"
EX"v57,v57\n"
MF"v[24:27],a[64:67],a[48:51],0\n"
WT"lgkmcnt(4)\n"
PK"v76,v56,v57\n"
"s_mov_b32 m0,s40\n"
MF"v[0:3],a[68:71],a[4:7],v[0:3]\n"
EX"v58,v58\n"
MF"v[8:11],a[68:71],a[20:23],v[8:11]\n"
"v_readlane_b32 s44,v229,s42\n"
"v_mov_b32 v227,v226\n"
MF"v[16:19],a[68:71],a[36:39],v[16:19]\n"
EX"v59,v59\n"
MF"v[24:27],a[68:71],a[52:55],v[24:27]\n"
WT"lgkmcnt(0)\n"
BL"v225,s[8:11],s43 offen offset:0 sc1 lds\n"
MF"v[0:3],a[72:75],a[8:11],v[0:3]\n"
SA"s42,s42,1\n"
EX"v60,v60\n"
MF"v[8:11],a[72:75],a[24:27],v[8:11]\n"
RD"a[88:91],v226 offset:6144\n"
PK"v77,v58,v59\n"
MF"v[16:19],a[72:75],a[40:43],v[16:19]\n"
EX"v61,v61\n"
MF"v[24:27],a[72:75],a[56:59],v[24:27]\n"
PK"v78,v60,v61\n"
MF"v[0:3],a[76:79],a[12:15],v[0:3]\n"
EX"v62,v62\n"
MF"v[8:11],a[76:79],a[28:31],v[8:11]\n"
RD"a[92:95],v226 offset:7168\n"
MF"v[16:19],a[76:79],a[44:47],v[16:19]\n"
EX"v63,v63\n"
MF"v[24:27],a[76:79],a[60:63],v[24:27]\n"
BL"v225,s[8:11],s43 offen offset:1024 sc1 lds\n"
MF"v[4:7],a[80:83],a[0:3],0\n"
EX"v0,v0\n"
MF"v[12:15],a[80:83],a[16:19],0\n"
RD"a[96:99],v226 offset:8192\n"
PK"v79,v62,v63\n"
MF"v[20:23],a[80:83],a[32:35],0\n"
EX"v1,v1\n"
MF"v[28:31],a[80:83],a[48:51],0\n"
PK"v0,v0,v1\n"
MF"v[4:7],a[84:87],a[4:7],v[4:7]\n"
EX"v2,v2\n"
MF"v[12:15],a[84:87],a[20:23],v[12:15]\n"
RD"a[100:103],v226 offset:9216\n"
MF"v[20:23],a[84:87],a[36:39],v[20:23]\n"
EX"v3,v3\n"
MF"v[28:31],a[84:87],a[52:55],v[28:31]\n"
WT"lgkmcnt(2)\n"
BL"v225,s[8:11],s43 offen offset:2048 sc1 lds\n"
MF"v[4:7],a[88:91],a[8:11],v[4:7]\n"
EX"v8,v8\n"
MF"v[12:15],a[88:91],a[24:27],v[12:15]\n"
RD"a[104:107],v226 offset:10240\n"
PK"v1,v2,v3\n"
MF"v[20:23],a[88:91],a[40:43],v[20:23]\n"
EX"v9,v9\n"
MF"v[28:31],a[88:91],a[56:59],v[28:31]\n"
PK"v8,v8,v9\n"
MF"v[4:7],a[92:95],a[12:15],v[4:7]\n"
EX"v10,v10\n"
MF"v[12:15],a[92:95],a[28:31],v[12:15]\n"
RD"a[108:111],v226 offset:11264\n"
MF"v[20:23],a[92:95],a[44:47],v[20:23]\n"
EX"v11,v11\n"
MF"v[28:31],a[92:95],a[60:63],v[28:31]\n"
WT"lgkmcnt(2)\n"
BL"v225,s[8:11],s43 offen offset:3072 sc1 lds\n"
MF"v[32:35],a[96:99],a[0:3],0\n"
EX"v16,v16\n"
MF"v[40:43],a[96:99],a[16:19],0\n"
RD"a[112:115],v226 offset:12288\n"
PK"v9,v10,v11\n"
MF"v[48:51],a[96:99],a[32:35],0\n"
EX"v17,v17\n"
MF"v[56:59],a[96:99],a[48:51],0\n"
SA"m0,s40,16384\n"
PK"v16,v16,v17\n"
MF"v[32:35],a[100:103],a[4:7],v[32:35]\n"
EX"v18,v18\n"
MF"v[40:43],a[100:103],a[20:23],v[40:43]\n"
RD"a[116:119],v226 offset:13312\n"
MF"v[48:51],a[100:103],a[36:39],v[48:51]\n"
EX"v19,v19\n"
MF"v[56:59],a[100:103],a[52:55],v[56:59]\n"
WT"lgkmcnt(2)\n"
BL"v225,s[8:11],s44 offen offset:0 sc1 lds\n"
MF"v[32:35],a[104:107],a[8:11],v[32:35]\n"
SA"s40,s40,32768\n"
EX"v24,v24\n"
MF"v[40:43],a[104:107],a[24:27],v[40:43]\n"
"s_cmp_eq_u32 s40,s46\n"
RD"a[120:123],v226 offset:14336\n"
PK"v17,v18,v19\n"
MF"v[48:51],a[104:107],a[40:43],v[48:51]\n"
"s_cselect_b32 s40,s47,s40\n"
EX"v25,v25\n"
MF"v[56:59],a[104:107],a[56:59],v[56:59]\n"
PK"v24,v24,v25\n"
MF"v[32:35],a[108:111],a[12:15],v[32:35]\n"
EX"v26,v26\n"
MF"v[40:43],a[108:111],a[28:31],v[40:43]\n"
RD"a[124:127],v226 offset:15360\n"
MF"v[48:51],a[108:111],a[44:47],v[48:51]\n"
EX"v27,v27\n"
MF"v[56:59],a[108:111],a[60:63],v[56:59]\n"
WT"lgkmcnt(2)\n"
BL"v225,s[8:11],s44 offen offset:1024 sc1 lds\n"
MF"v[36:39],a[112:115],a[0:3],0\n"
EX"v4,v4\n"
MF"v[44:47],a[112:115],a[16:19],0\n"
RD"a[64:67],v227 offset:16384\n"
PK"v25,v26,v27\n"
MF"v[52:55],a[112:115],a[32:35],0\n"
EX"v5,v5\n"
MF"v[60:63],a[112:115],a[48:51],0\n"
PK"v2,v4,v5\n"
MF"v[36:39],a[116:119],a[4:7],v[36:39]\n"
EX"v6,v6\n"
MF"v[44:47],a[116:119],a[20:23],v[44:47]\n"
RD"a[68:71],v227 offset:17408\n"
MF"v[52:55],a[116:119],a[36:39],v[52:55]\n"
EX"v7,v7\n"
MF"v[60:63],a[116:119],a[52:55],v[60:63]\n"
WT"lgkmcnt(2)\n"
BL"v225,s[8:11],s44 offen offset:2048 sc1 lds\n"
MF"v[36:39],a[120:123],a[8:11],v[36:39]\n"
EX"v12,v12\n"
MF"v[44:47],a[120:123],a[24:27],v[44:47]\n"
RD"a[72:75],v227 offset:18432\n"
PK"v3,v6,v7\n"
MF"v[52:55],a[120:123],a[40:43],v[52:55]\n"
EX"v13,v13\n"
MF"v[60:63],a[120:123],a[56:59],v[60:63]\n"
PK"v10,v12,v13\n"
MF"v[36:39],a[124:127],a[12:15],v[36:39]\n"
EX"v14,v14\n"
MF"v[44:47],a[124:127],a[28:31],v[44:47]\n"
RD"a[76:79],v227 offset:19456\n"
MF"v[52:55],a[124:127],a[44:47],v[52:55]\n"
EX"v15,v15\n"
MF"v[60:63],a[124:127],a[60:63],v[60:63]\n"
WT"lgkmcnt(2)\n"
BL"v225,s[8:11],s44 offen offset:3072 sc1 lds\n"
MF"v[80:83],a[64:67],v[64:67],v[80:83]\n"
SA"s41,s41,32768\n"
EX"v20,v20\n"
MF"v[112:115],a[64:67],v[68:71],v[112:115]\n"
"s_cmp_eq_u32 s41,163840\n"
RD"a[80:83],v227 offset:20480\n"
PK"v11,v14,v15\n"
MF"v[144:147],a[64:67],v[72:75],v[144:147]\n"
"s_cselect_b32 s41,0,s41\n"
EX"v21,v21\n"
MF"v[176:179],a[64:67],v[76:79],v[176:179]\n"
"buffer_load_dword v255,v253,s[12:15],s55 offen\n"
MF"v[84:87],a[68:71],v[64:67],v[84:87]\n"
EX"v22,v22\n"
MF"v[116:119],a[68:71],v[68:71],v[116:119]\n"
RD"a[84:87],v227 offset:21504\n"
"v_add_u32 v226,s41,v224\n"
MF"v[148:151],a[68:71],v[72:75],v[148:151]\n"
EX"v23,v23\n"
MF"v[180:183],a[68:71],v[76:79],v[180:183]\n"
WT"lgkmcnt(2)\n"
PK"v18,v20,v21\n"
PK"v19,v22,v23\n"
MF"v[88:91],a[72:75],v[64:67],v[88:91]\n"
EX"v28,v28\n"
MF"v[120:123],a[72:75],v[68:71],v[120:123]\n"
RD"a[88:91],v227 offset:22528\n"
MF"v[152:155],a[72:75],v[72:75],v[152:155]\n"
EX"v29,v29\n"
MF"v[184:187],a[72:75],v[76:79],v[184:187]\n"
PK"v26,v28,v29\n"
MF"v[92:95],a[76:79],v[64:67],v[92:95]\n"
EX"v30,v30\n"
MF"v[124:127],a[76:79],v[68:71],v[124:127]\n"
RD"a[92:95],v227 offset:23552\n"
MF"v[156:159],a[76:79],v[72:75],v[156:159]\n"
EX"v31,v31\n"
MF"v[188:191],a[76:79],v[76:79],v[188:191]\n"
WT"lgkmcnt(2)\n"
PK"v27,v30,v31\n"
MF"v[96:99],a[80:83],v[64:67],v[96:99]\n"
EX"v32,v32\n"
MF"v[128:131],a[80:83],v[68:71],v[128:131]\n"
RD"a[96:99],v227 offset:24576\n"
MF"v[160:163],a[80:83],v[72:75],v[160:163]\n"
EX"v33,v33\n"
MF"v[192:195],a[80:83],v[76:79],v[192:195]\n"
MF"v[100:103],a[84:87],v[64:67],v[100:103]\n"
EX"v34,v34\n"
MF"v[132:135],a[84:87],v[68:71],v[132:135]\n"
RD"a[100:103],v227 offset:25600\n"
MF"v[164:167],a[84:87],v[72:75],v[164:167]\n"
EX"v35,v35\n"
MF"v[196:199],a[84:87],v[76:79],v[196:199]\n"
WT"lgkmcnt(2)\n"
MF"v[104:107],a[88:91],v[64:67],v[104:107]\n"
EX"v40,v40\n"
MF"v[136:139],a[88:91],v[68:71],v[136:139]\n"
RD"a[104:107],v227 offset:26624\n"
MF"v[168:171],a[88:91],v[72:75],v[168:171]\n"
EX"v41,v41\n"
MF"v[200:203],a[88:91],v[76:79],v[200:203]\n"
MF"v[108:111],a[92:95],v[64:67],v[108:111]\n"
EX"v42,v42\n"
MF"v[140:143],a[92:95],v[68:71],v[140:143]\n"
SA"s55,s55,163840\n"
RD"a[108:111],v227 offset:27648\n"
MF"v[172:175],a[92:95],v[72:75],v[172:175]\n"
EX"v43,v43\n"
MF"v[204:207],a[92:95],v[76:79],v[204:207]\n"
MF"a[132:135],a[128:131],v[64:67],a[132:135]\n"
EX"v48,v48\n"
MF"a[136:139],a[128:131],v[68:71],a[136:139]\n"
RD"a[112:115],v227 offset:28672\n"
PK"v64,v32,v33\n"
MF"a[140:143],a[128:131],v[72:75],a[140:143]\n"
EX"v49,v49\n"
MF"a[144:147],a[128:131],v[76:79],a[144:147]\n"
WT"lgkmcnt(2)\n"
PK"v65,v34,v35\n"
PK"v68,v40,v41\n"
MF"v[80:83],a[96:99],v[0:3],v[80:83]\n"
EX"v50,v50\n"
MF"v[112:115],a[96:99],v[8:11],v[112:115]\n"
RD"a[116:119],v227 offset:29696\n"
PK"v69,v42,v43\n"
MF"v[144:147],a[96:99],v[16:19],v[144:147]\n"
EX"v51,v51\n"
MF"v[176:179],a[96:99],v[24:27],v[176:179]\n"
PK"v72,v48,v49\n"
PK"v73,v50,v51\n"
MF"v[84:87],a[100:103],v[0:3],v[84:87]\n"
EX"v36,v36\n"
MF"v[116:119],a[100:103],v[8:11],v[116:119]\n"
RD"a[120:123],v227 offset:30720\n"
MF"v[148:151],a[100:103],v[16:19],v[148:151]\n"
EX"v37,v37\n"
MF"v[180:183],a[100:103],v[24:27],v[180:183]\n"
PK"v66,v36,v37\n"
MF"v[88:91],a[104:107],v[0:3],v[88:91]\n"
EX"v38,v38\n"
MF"v[120:123],a[104:107],v[8:11],v[120:123]\n"
RD"a[124:127],v227 offset:31744\n"
MF"v[152:155],a[104:107],v[16:19],v[152:155]\n"
EX"v39,v39\n"
MF"v[184:187],a[104:107],v[24:27],v[184:187]\n"
WT"lgkmcnt(2)\n"
PK"v67,v38,v39\n"
MF"v[92:95],a[108:111],v[0:3],v[92:95]\n"
EX"v44,v44\n"
MF"v[124:127],a[108:111],v[8:11],v[124:127]\n"
MF"v[156:159],a[108:111],v[16:19],v[156:159]\n"
EX"v45,v45\n"
MF"v[188:191],a[108:111],v[24:27],v[188:191]\n"
PK"v70,v44,v45\n"
MF"v[96:99],a[112:115],v[0:3],v[96:99]\n"
EX"v46,v46\n"
MF"v[128:131],a[112:115],v[8:11],v[128:131]\n"
MF"v[160:163],a[112:115],v[16:19],v[160:163]\n"
EX"v47,v47\n"
MF"v[192:195],a[112:115],v[24:27],v[192:195]\n"
PK"v71,v46,v47\n"
MF"v[100:103],a[116:119],v[0:3],v[100:103]\n"
WT"vmcnt(17) lgkmcnt(0)\n"
"s_barrier\n"
MF"v[132:135],a[116:119],v[8:11],v[132:135]\n"
RD"a[64:67],v226 offset:0\n"
MF"v[164:167],a[116:119],v[16:19],v[164:167]\n"
EX"v52,v52\n"
MF"v[196:199],a[116:119],v[24:27],v[196:199]\n"
RD"a[68:71],v226 offset:1024\n"
MF"v[104:107],a[120:123],v[0:3],v[104:107]\n"
EX"v53,v53\n"
MF"v[136:139],a[120:123],v[8:11],v[136:139]\n"
RD"a[72:75],v226 offset:2048\n"
PK"v74,v52,v53\n"
MF"v[168:171],a[120:123],v[16:19],v[168:171]\n"
EX"v54,v54\n"
MF"v[200:203],a[120:123],v[24:27],v[200:203]\n"
MF"v[108:111],a[124:127],v[0:3],v[108:111]\n"
EX"v55,v55\n"
MF"v[140:143],a[124:127],v[8:11],v[140:143]\n"
RD"a[76:79],v226 offset:3072\n"
PK"v75,v54,v55\n"
MF"v[172:175],a[124:127],v[16:19],v[172:175]\n"
MF"v[204:207],a[124:127],v[24:27],v[204:207]\n"
RD"a[80:83],v226 offset:4096\n"
MF"a[132:135],a[128:131],v[0:3],a[132:135]\n"
MF"a[136:139],a[128:131],v[8:11],a[136:139]\n"
RD"a[84:87],v226 offset:5120\n"
MF"a[140:143],a[128:131],v[16:19],a[140:143]\n"
MF"a[144:147],a[128:131],v[24:27],a[144:147]\n"
"s_sub_u32 s56,s56,1\n"
"s_cbranch_scc0 LP1_%=\n"
"s_cmp_lg_u32 s57,0\n"
"s_cbranch_scc1 LEV_%=\n"
"s_cmp_eq_u32 s34,7\n"
"s_cbranch_scc1 LRS_%=\n"
"LDA_%=:\n"
WT"lgkmcnt(5)\n"
MF"v[0:3],a[64:67],a[0:3],v[208:211]\n"
EX"v56,v56\n"
MF"v[8:11],a[64:67],a[16:19],0\n"
"v_readlane_b32 s43,v228,s42\n"
MF"v[16:19],a[64:67],a[32:35],0\n"
EX"v57,v57\n"
MF"v[24:27],a[64:67],a[48:51],0\n"
WT"lgkmcnt(4)\n"
PK"v76,v56,v57\n"
"s_mov_b32 m0,s40\n"
MF"v[0:3],a[68:71],a[4:7],v[0:3]\n"
EX"v58,v58\n"
MF"v[8:11],a[68:71],a[20:23],v[8:11]\n"
"v_readlane_b32 s44,v229,s42\n"
"v_mov_b32 v227,v226\n"
MF"v[16:19],a[68:71],a[36:39],v[16:19]\n"
EX"v59,v59\n"
MF"v[24:27],a[68:71],a[52:55],v[24:27]\n"
WT"lgkmcnt(0)\n"
BL"v225,s[8:11],s43 offen offset:0 sc1 lds\n"
MF"v[0:3],a[72:75],a[8:11],v[0:3]\n"
SA"s42,s42,1\n"
EX"v60,v60\n"
MF"v[8:11],a[72:75],a[24:27],v[8:11]\n"
RD"a[88:91],v226 offset:6144\n"
PK"v77,v58,v59\n"
MF"v[16:19],a[72:75],a[40:43],v[16:19]\n"
EX"v61,v61\n"
MF"v[24:27],a[72:75],a[56:59],v[24:27]\n"
PK"v78,v60,v61\n"
MF"v[0:3],a[76:79],a[12:15],v[0:3]\n"
EX"v62,v62\n"
MF"v[8:11],a[76:79],a[28:31],v[8:11]\n"
RD"a[92:95],v226 offset:7168\n"
MF"v[16:19],a[76:79],a[44:47],v[16:19]\n"
EX"v63,v63\n"
MF"v[24:27],a[76:79],a[60:63],v[24:27]\n"
BL"v225,s[8:11],s43 offen offset:1024 sc1 lds\n"
MF"v[4:7],a[80:83],a[0:3],v[212:215]\n"
EX"v0,v0\n"
MF"v[12:15],a[80:83],a[16:19],0\n"
RD"a[96:99],v226 offset:8192\n"
PK"v79,v62,v63\n"
MF"v[20:23],a[80:83],a[32:35],0\n"
EX"v1,v1\n"
MF"v[28:31],a[80:83],a[48:51],0\n"
PK"v0,v0,v1\n"
MF"v[4:7],a[84:87],a[4:7],v[4:7]\n"
EX"v2,v2\n"
MF"v[12:15],a[84:87],a[20:23],v[12:15]\n"
RD"a[100:103],v226 offset:9216\n"
MF"v[20:23],a[84:87],a[36:39],v[20:23]\n"
EX"v3,v3\n"
MF"v[28:31],a[84:87],a[52:55],v[28:31]\n"
WT"lgkmcnt(2)\n"
BL"v225,s[8:11],s43 offen offset:2048 sc1 lds\n"
MF"v[4:7],a[88:91],a[8:11],v[4:7]\n"
EX"v8,v8\n"
MF"v[12:15],a[88:91],a[24:27],v[12:15]\n"
RD"a[104:107],v226 offset:10240\n"
PK"v1,v2,v3\n"
MF"v[20:23],a[88:91],a[40:43],v[20:23]\n"
EX"v9,v9\n"
MF"v[28:31],a[88:91],a[56:59],v[28:31]\n"
PK"v8,v8,v9\n"
MF"v[4:7],a[92:95],a[12:15],v[4:7]\n"
EX"v10,v10\n"
MF"v[12:15],a[92:95],a[28:31],v[12:15]\n"
RD"a[108:111],v226 offset:11264\n"
MF"v[20:23],a[92:95],a[44:47],v[20:23]\n"
EX"v11,v11\n"
MF"v[28:31],a[92:95],a[60:63],v[28:31]\n"
WT"lgkmcnt(2)\n"
BL"v225,s[8:11],s43 offen offset:3072 sc1 lds\n"
MF"v[32:35],a[96:99],a[0:3],v[216:219]\n"
EX"v16,v16\n"
MF"v[40:43],a[96:99],a[16:19],0\n"
RD"a[112:115],v226 offset:12288\n"
PK"v9,v10,v11\n"
MF"v[48:51],a[96:99],a[32:35],0\n"
EX"v17,v17\n"
MF"v[56:59],a[96:99],a[48:51],0\n"
SA"m0,s40,16384\n"
PK"v16,v16,v17\n"
MF"v[32:35],a[100:103],a[4:7],v[32:35]\n"
EX"v18,v18\n"
MF"v[40:43],a[100:103],a[20:23],v[40:43]\n"
RD"a[116:119],v226 offset:13312\n"
MF"v[48:51],a[100:103],a[36:39],v[48:51]\n"
EX"v19,v19\n"
MF"v[56:59],a[100:103],a[52:55],v[56:59]\n"
WT"lgkmcnt(2)\n"
BL"v225,s[8:11],s44 offen offset:0 sc1 lds\n"
MF"v[32:35],a[104:107],a[8:11],v[32:35]\n"
SA"s40,s40,32768\n"
EX"v24,v24\n"
MF"v[40:43],a[104:107],a[24:27],v[40:43]\n"
"s_cmp_eq_u32 s40,s46\n"
RD"a[120:123],v226 offset:14336\n"
PK"v17,v18,v19\n"
MF"v[48:51],a[104:107],a[40:43],v[48:51]\n"
"s_cselect_b32 s40,s47,s40\n"
EX"v25,v25\n"
MF"v[56:59],a[104:107],a[56:59],v[56:59]\n"
PK"v24,v24,v25\n"
MF"v[32:35],a[108:111],a[12:15],v[32:35]\n"
EX"v26,v26\n"
MF"v[40:43],a[108:111],a[28:31],v[40:43]\n"
RD"a[124:127],v226 offset:15360\n"
MF"v[48:51],a[108:111],a[44:47],v[48:51]\n"
EX"v27,v27\n"
MF"v[56:59],a[108:111],a[60:63],v[56:59]\n"
WT"lgkmcnt(2)\n"
BL"v225,s[8:11],s44 offen offset:1024 sc1 lds\n"
MF"v[36:39],a[112:115],a[0:3],v[220:223]\n"
EX"v4,v4\n"
MF"v[44:47],a[112:115],a[16:19],0\n"
RD"a[64:67],v227 offset:16384\n"
PK"v25,v26,v27\n"
MF"v[52:55],a[112:115],a[32:35],0\n"
EX"v5,v5\n"
MF"v[60:63],a[112:115],a[48:51],0\n"
PK"v2,v4,v5\n"
MF"v[36:39],a[116:119],a[4:7],v[36:39]\n"
EX"v6,v6\n"
MF"v[44:47],a[116:119],a[20:23],v[44:47]\n"
RD"a[68:71],v227 offset:17408\n"
MF"v[52:55],a[116:119],a[36:39],v[52:55]\n"
EX"v7,v7\n"
MF"v[60:63],a[116:119],a[52:55],v[60:63]\n"
WT"lgkmcnt(2)\n"
BL"v225,s[8:11],s44 offen offset:2048 sc1 lds\n"
MF"v[36:39],a[120:123],a[8:11],v[36:39]\n"
EX"v12,v12\n"
MF"v[44:47],a[120:123],a[24:27],v[44:47]\n"
RD"a[72:75],v227 offset:18432\n"
PK"v3,v6,v7\n"
MF"v[52:55],a[120:123],a[40:43],v[52:55]\n"
EX"v13,v13\n"
MF"v[60:63],a[120:123],a[56:59],v[60:63]\n"
PK"v10,v12,v13\n"
MF"v[36:39],a[124:127],a[12:15],v[36:39]\n"
EX"v14,v14\n"
MF"v[44:47],a[124:127],a[28:31],v[44:47]\n"
RD"a[76:79],v227 offset:19456\n"
MF"v[52:55],a[124:127],a[44:47],v[52:55]\n"
EX"v15,v15\n"
MF"v[60:63],a[124:127],a[60:63],v[60:63]\n"
WT"lgkmcnt(2)\n"
BL"v225,s[8:11],s44 offen offset:3072 sc1 lds\n"
MF"v[80:83],a[64:67],v[64:67],v[80:83]\n"
SA"s41,s41,32768\n"
EX"v20,v20\n"
MF"v[112:115],a[64:67],v[68:71],v[112:115]\n"
"s_cmp_eq_u32 s41,163840\n"
RD"a[80:83],v227 offset:20480\n"
PK"v11,v14,v15\n"
MF"v[144:147],a[64:67],v[72:75],v[144:147]\n"
"s_cselect_b32 s41,0,s41\n"
EX"v21,v21\n"
MF"v[176:179],a[64:67],v[76:79],v[176:179]\n"
"v_add_u32 v226,s41,v224\n"
PK"v18,v20,v21\n"
MF"v[84:87],a[68:71],v[64:67],v[84:87]\n"
EX"v22,v22\n"
MF"v[116:119],a[68:71],v[68:71],v[116:119]\n"
RD"a[84:87],v227 offset:21504\n"
MF"v[148:151],a[68:71],v[72:75],v[148:151]\n"
EX"v23,v23\n"
MF"v[180:183],a[68:71],v[76:79],v[180:183]\n"
WT"lgkmcnt(2)\n"
PK"v19,v22,v23\n"
MF"v[88:91],a[72:75],v[64:67],v[88:91]\n"
EX"v28,v28\n"
MF"v[120:123],a[72:75],v[68:71],v[120:123]\n"
RD"a[88:91],v227 offset:22528\n"
MF"v[152:155],a[72:75],v[72:75],v[152:155]\n"
EX"v29,v29\n"
MF"v[184:187],a[72:75],v[76:79],v[184:187]\n"
PK"v26,v28,v29\n"
MF"v[92:95],a[76:79],v[64:67],v[92:95]\n"
EX"v30,v30\n"
MF"v[124:127],a[76:79],v[68:71],v[124:127]\n"
RD"a[92:95],v227 offset:23552\n"
MF"v[156:159],a[76:79],v[72:75],v[156:159]\n"
EX"v31,v31\n"
MF"v[188:191],a[76:79],v[76:79],v[188:191]\n"
WT"lgkmcnt(2)\n"
PK"v27,v30,v31\n"
MF"v[96:99],a[80:83],v[64:67],v[96:99]\n"
EX"v32,v32\n"
MF"v[128:131],a[80:83],v[68:71],v[128:131]\n"
RD"a[96:99],v227 offset:24576\n"
MF"v[160:163],a[80:83],v[72:75],v[160:163]\n"
EX"v33,v33\n"
MF"v[192:195],a[80:83],v[76:79],v[192:195]\n"
MF"v[100:103],a[84:87],v[64:67],v[100:103]\n"
EX"v34,v34\n"
MF"v[132:135],a[84:87],v[68:71],v[132:135]\n"
RD"a[100:103],v227 offset:25600\n"
MF"v[164:167],a[84:87],v[72:75],v[164:167]\n"
EX"v35,v35\n"
MF"v[196:199],a[84:87],v[76:79],v[196:199]\n"
WT"lgkmcnt(2)\n"
MF"v[104:107],a[88:91],v[64:67],v[104:107]\n"
EX"v40,v40\n"
MF"v[136:139],a[88:91],v[68:71],v[136:139]\n"
RD"a[104:107],v227 offset:26624\n"
MF"v[168:171],a[88:91],v[72:75],v[168:171]\n"
EX"v41,v41\n"
MF"v[200:203],a[88:91],v[76:79],v[200:203]\n"
MF"v[108:111],a[92:95],v[64:67],v[108:111]\n"
EX"v42,v42\n"
MF"v[140:143],a[92:95],v[68:71],v[140:143]\n"
RD"a[108:111],v227 offset:27648\n"
MF"v[172:175],a[92:95],v[72:75],v[172:175]\n"
EX"v43,v43\n"
MF"v[204:207],a[92:95],v[76:79],v[204:207]\n"
MF"a[132:135],a[128:131],v[64:67],a[132:135]\n"
EX"v48,v48\n"
MF"a[136:139],a[128:131],v[68:71],a[136:139]\n"
RD"a[112:115],v227 offset:28672\n"
PK"v64,v32,v33\n"
MF"a[140:143],a[128:131],v[72:75],a[140:143]\n"
EX"v49,v49\n"
MF"a[144:147],a[128:131],v[76:79],a[144:147]\n"
WT"lgkmcnt(2)\n"
PK"v65,v34,v35\n"
PK"v68,v40,v41\n"
MF"v[80:83],a[96:99],v[0:3],v[80:83]\n"
EX"v50,v50\n"
MF"v[112:115],a[96:99],v[8:11],v[112:115]\n"
RD"a[116:119],v227 offset:29696\n"
PK"v69,v42,v43\n"
MF"v[144:147],a[96:99],v[16:19],v[144:147]\n"
EX"v51,v51\n"
MF"v[176:179],a[96:99],v[24:27],v[176:179]\n"
PK"v72,v48,v49\n"
PK"v73,v50,v51\n"
MF"v[84:87],a[100:103],v[0:3],v[84:87]\n"
EX"v36,v36\n"
MF"v[116:119],a[100:103],v[8:11],v[116:119]\n"
RD"a[120:123],v227 offset:30720\n"
MF"v[148:151],a[100:103],v[16:19],v[148:151]\n"
EX"v37,v37\n"
MF"v[180:183],a[100:103],v[24:27],v[180:183]\n"
PK"v66,v36,v37\n"
MF"v[88:91],a[104:107],v[0:3],v[88:91]\n"
EX"v38,v38\n"
MF"v[120:123],a[104:107],v[8:11],v[120:123]\n"
RD"a[124:127],v227 offset:31744\n"
MF"v[152:155],a[104:107],v[16:19],v[152:155]\n"
EX"v39,v39\n"
MF"v[184:187],a[104:107],v[24:27],v[184:187]\n"
WT"lgkmcnt(2)\n"
PK"v67,v38,v39\n"
MF"v[92:95],a[108:111],v[0:3],v[92:95]\n"
EX"v44,v44\n"
MF"v[124:127],a[108:111],v[8:11],v[124:127]\n"
MF"v[156:159],a[108:111],v[16:19],v[156:159]\n"
EX"v45,v45\n"
MF"v[188:191],a[108:111],v[24:27],v[188:191]\n"
PK"v70,v44,v45\n"
MF"v[96:99],a[112:115],v[0:3],v[96:99]\n"
EX"v46,v46\n"
MF"v[128:131],a[112:115],v[8:11],v[128:131]\n"
MF"v[160:163],a[112:115],v[16:19],v[160:163]\n"
EX"v47,v47\n"
MF"v[192:195],a[112:115],v[24:27],v[192:195]\n"
PK"v71,v46,v47\n"
MF"v[100:103],a[116:119],v[0:3],v[100:103]\n"
WT"vmcnt(16) lgkmcnt(0)\n"
"s_barrier\n"
MF"v[132:135],a[116:119],v[8:11],v[132:135]\n"
RD"a[64:67],v226 offset:0\n"
MF"v[164:167],a[116:119],v[16:19],v[164:167]\n"
EX"v52,v52\n"
MF"v[196:199],a[116:119],v[24:27],v[196:199]\n"
RD"a[68:71],v226 offset:1024\n"
MF"v[104:107],a[120:123],v[0:3],v[104:107]\n"
EX"v53,v53\n"
MF"v[136:139],a[120:123],v[8:11],v[136:139]\n"
RD"a[72:75],v226 offset:2048\n"
PK"v74,v52,v53\n"
MF"v[168:171],a[120:123],v[16:19],v[168:171]\n"
EX"v54,v54\n"
MF"v[200:203],a[120:123],v[24:27],v[200:203]\n"
MF"v[108:111],a[124:127],v[0:3],v[108:111]\n"
EX"v55,v55\n"
MF"v[140:143],a[124:127],v[8:11],v[140:143]\n"
RD"a[76:79],v226 offset:3072\n"
PK"v75,v54,v55\n"
MF"v[172:175],a[124:127],v[16:19],v[172:175]\n"
MF"v[204:207],a[124:127],v[24:27],v[204:207]\n"
RD"a[80:83],v226 offset:4096\n"
MF"a[132:135],a[128:131],v[0:3],a[132:135]\n"
MF"a[136:139],a[128:131],v[8:11],a[136:139]\n"
RD"a[84:87],v226 offset:5120\n"
MF"a[140:143],a[128:131],v[16:19],a[140:143]\n"
MF"a[144:147],a[128:131],v[24:27],a[144:147]\n"
"LDB_%=:\n"
WT"lgkmcnt(5)\n"
MF"v[8:11],a[64:67],a[16:19],v[208:211]\n"
EX"v56,v56\n"
MF"v[16:19],a[64:67],a[32:35],0\n"
"v_readlane_b32 s43,v228,s42\n"
MF"v[24:27],a[64:67],a[48:51],0\n"
WT"lgkmcnt(4)\n"
EX"v57,v57\n"
MF"v[8:11],a[68:71],a[20:23],v[8:11]\n"
"s_lshl_b32 s21,s35,8\n"
RD"a[88:91],v226 offset:6144\n"
PK"v76,v56,v57\n"
MF"v[16:19],a[68:71],a[36:39],v[16:19]\n"
SA"s21,s21,81264640\n"
EX"v58,v58\n"
MF"v[24:27],a[68:71],a[52:55],v[24:27]\n"
WT"lgkmcnt(1)\n"
RD"a[92:95],v226 offset:7168\n"
"v_readlane_b32 s44,v229,s42\n"
MF"v[8:11],a[72:75],a[24:27],v[8:11]\n"
"s_mov_b32 m0,s40\n"
EX"v59,v59\n"
MF"v[16:19],a[72:75],a[40:43],v[16:19]\n"
SA"s42,s42,1\n"
BL"v225,s[8:11],s43 offen offset:0 sc1 lds\n"
"v_mov_b32 v227,v226\n"
MF"v[24:27],a[72:75],a[56:59],v[24:27]\n"
EX"v60,v60\n"
MF"v[8:11],a[76:79],a[28:31],v[8:11]\n"
RD"a[96:99],v226 offset:8192\n"
PK"v77,v58,v59\n"
MF"v[16:19],a[76:79],a[44:47],v[16:19]\n"
EX"v61,v61\n"
MF"v[24:27],a[76:79],a[60:63],v[24:27]\n"
RD"a[100:103],v226 offset:9216\n"
PK"v78,v60,v61\n"
MF"v[12:15],a[80:83],a[16:19],v[212:215]\n"
EX"v62,v62\n"
MF"v[20:23],a[80:83],a[32:35],0\n"
"v_lshrrev_b32 v235,7,v224\n"
"v_lshrrev_b32 v230,1,v224\n"
MF"v[28:31],a[80:83],a[48:51],0\n"
EX"v63,v63\n"
MF"v[12:15],a[84:87],a[20:23],v[12:15]\n"
BL"v225,s[8:11],s43 offen offset:1024 sc1 lds\n"
MF"v[20:23],a[84:87],a[36:39],v[20:23]\n"
EX"v8,v8\n"
MF"v[28:31],a[84:87],a[52:55],v[28:31]\n"
WT"lgkmcnt(1)\n"
RD"a[104:107],v226 offset:10240\n"
"v_lshl_add_u32 v230,v235,27,v230\n"
MF"v[12:15],a[88:91],a[24:27],v[12:15]\n"
EX"v9,v9\n"
PK"v79,v62,v63\n"
MF"v[20:23],a[88:91],a[40:43],v[20:23]\n"
RD"a[108:111],v226 offset:11264\n"
PK"v8,v8,v9\n"
MF"v[28:31],a[88:91],a[56:59],v[28:31]\n"
EX"v10,v10\n"
MF"v[12:15],a[92:95],a[28:31],v[12:15]\n"
RD"a[112:115],v226 offset:12288\n"
MF"v[20:23],a[92:95],a[44:47],v[20:23]\n"
EX"v11,v11\n"
MF"v[28:31],a[92:95],a[60:63],v[28:31]\n"
BL"v225,s[8:11],s43 offen offset:2048 sc1 lds\n"
MF"v[40:43],a[96:99],a[16:19],v[216:219]\n"
EX"v16,v16\n"
MF"v[48:51],a[96:99],a[32:35],0\n"
RD"a[116:119],v226 offset:13312\n"
PK"v9,v10,v11\n"
MF"v[56:59],a[96:99],a[48:51],0\n"
WT"lgkmcnt(2)\n"
EX"v17,v17\n"
MF"v[40:43],a[100:103],a[20:23],v[40:43]\n"
RD"a[120:123],v226 offset:14336\n"
PK"v16,v16,v17\n"
MF"v[48:51],a[100:103],a[36:39],v[48:51]\n"
EX"v18,v18\n"
MF"v[56:59],a[100:103],a[52:55],v[56:59]\n"
RD"a[124:127],v226 offset:15360\n"
MF"v[40:43],a[104:107],a[24:27],v[40:43]\n"
EX"v19,v19\n"
MF"v[48:51],a[104:107],a[40:43],v[48:51]\n"
BL"v225,s[8:11],s43 offen offset:3072 sc1 lds\n"
MF"v[56:59],a[104:107],a[56:59],v[56:59]\n"
EX"v24,v24\n"
MF"v[40:43],a[108:111],a[28:31],v[40:43]\n"
RD"a[64:67],v227 offset:16384\n"
PK"v17,v18,v19\n"
MF"v[48:51],a[108:111],a[44:47],v[48:51]\n"
EX"v25,v25\n"
MF"v[56:59],a[108:111],a[60:63],v[56:59]\n"
WT"lgkmcnt(2)\n"
PK"v24,v24,v25\n"
SA"m0,s40,16384\n"
MF"v[44:47],a[112:115],a[16:19],v[220:223]\n"
EX"v26,v26\n"
MF"v[52:55],a[112:115],a[32:35],0\n"
RD"a[68:71],v227 offset:17408\n"
MF"v[60:63],a[112:115],a[48:51],0\n"
EX"v27,v27\n"
MF"v[44:47],a[116:119],a[20:23],v[44:47]\n"
BL"v225,s[8:11],s44 offen offset:0 sc1 lds\n"
MF"v[52:55],a[116:119],a[36:39],v[52:55]\n"
SA"s40,s40,32768\n"
EX"v12,v12\n"
MF"v[60:63],a[116:119],a[52:55],v[60:63]\n"
"s_cmp_eq_u32 s40,s46\n"
RD"a[72:75],v227 offset:18432\n"
PK"v25,v26,v27\n"
MF"v[44:47],a[120:123],a[24:27],v[44:47]\n"
"s_cselect_b32 s40,s47,s40\n"
EX"v13,v13\n"
MF"v[52:55],a[120:123],a[40:43],v[52:55]\n"
PK"v10,v12,v13\n"
MF"v[60:63],a[120:123],a[56:59],v[60:63]\n"
WT"lgkmcnt(1)\n"
EX"v14,v14\n"
MF"v[44:47],a[124:127],a[28:31],v[44:47]\n"
RD"a[76:79],v227 offset:19456\n"
MF"v[52:55],a[124:127],a[44:47],v[52:55]\n"
EX"v15,v15\n"
MF"v[60:63],a[124:127],a[60:63],v[60:63]\n"
SA"s41,s41,32768\n"
BL"v225,s[8:11],s44 offen offset:1024 sc1 lds\n"
MF"v[80:83],a[64:67],v[64:67],v[80:83]\n"
"s_cmp_eq_u32 s41,163840\n"
EX"v20,v20\n"
MF"v[112:115],a[64:67],v[68:71],v[112:115]\n"
"s_cselect_b32 s41,0,s41\n"
RD"a[80:83],v227 offset:20480\n"
"v_add_u32 v226,s41,v224\n"
MF"v[144:147],a[64:67],v[72:75],v[144:147]\n"
EX"v21,v21\n"
PK"v11,v14,v15\n"
MF"v[176:179],a[64:67],v[76:79],v[176:179]\n"
PK"v18,v20,v21\n"
MF"v[84:87],a[68:71],v[64:67],v[84:87]\n"
EX"v22,v22\n"
MF"v[116:119],a[68:71],v[68:71],v[116:119]\n"
RD"a[84:87],v227 offset:21504\n"
MF"v[148:151],a[68:71],v[72:75],v[148:151]\n"
EX"v23,v23\n"
MF"v[180:183],a[68:71],v[76:79],v[180:183]\n"
WT"lgkmcnt(2)\n"
BL"v225,s[8:11],s44 offen offset:2048 sc1 lds\n"
MF"v[88:91],a[72:75],v[64:67],v[88:91]\n"
EX"v28,v28\n"
MF"v[120:123],a[72:75],v[68:71],v[120:123]\n"
RD"a[88:91],v227 offset:22528\n"
PK"v19,v22,v23\n"
MF"v[152:155],a[72:75],v[72:75],v[152:155]\n"
EX"v29,v29\n"
MF"v[184:187],a[72:75],v[76:79],v[184:187]\n"
PK"v26,v28,v29\n"
MF"v[92:95],a[76:79],v[64:67],v[92:95]\n"
EX"v30,v30\n"
MF"v[124:127],a[76:79],v[68:71],v[124:127]\n"
RD"a[92:95],v227 offset:23552\n"
MF"v[156:159],a[76:79],v[72:75],v[156:159]\n"
EX"v31,v31\n"
MF"v[188:191],a[76:79],v[76:79],v[188:191]\n"
WT"lgkmcnt(2)\n"
BL"v225,s[8:11],s44 offen offset:3072 sc1 lds\n"
MF"v[96:99],a[80:83],v[64:67],v[96:99]\n"
EX"v40,v40\n"
MF"v[128:131],a[80:83],v[68:71],v[128:131]\n"
RD"a[96:99],v227 offset:24576\n"
PK"v27,v30,v31\n"
MF"v[160:163],a[80:83],v[72:75],v[160:163]\n"
EX"v41,v41\n"
MF"v[192:195],a[80:83],v[76:79],v[192:195]\n"
RD"a[100:103],v227 offset:25600\n"
MF"v[100:103],a[84:87],v[64:67],v[100:103]\n"
EX"v42,v42\n"
MF"v[132:135],a[84:87],v[68:71],v[132:135]\n"
MF"v[164:167],a[84:87],v[72:75],v[164:167]\n"
EX"v43,v43\n"
MF"v[196:199],a[84:87],v[76:79],v[196:199]\n"
WT"lgkmcnt(2)\n"
MF"v[104:107],a[88:91],v[64:67],v[104:107]\n"
EX"v48,v48\n"
MF"v[136:139],a[88:91],v[68:71],v[136:139]\n"
RD"a[104:107],v227 offset:26624\n"
MF"v[168:171],a[88:91],v[72:75],v[168:171]\n"
EX"v49,v49\n"
MF"v[200:203],a[88:91],v[76:79],v[200:203]\n"
RD"a[108:111],v227 offset:27648\n"
MF"v[108:111],a[92:95],v[64:67],v[108:111]\n"
EX"v50,v50\n"
MF"v[140:143],a[92:95],v[68:71],v[140:143]\n"
RD"a[112:115],v227 offset:28672\n"
MF"v[172:175],a[92:95],v[72:75],v[172:175]\n"
EX"v51,v51\n"
MF"v[204:207],a[92:95],v[76:79],v[204:207]\n"
MF"a[132:135],a[128:131],v[64:67],a[132:135]\n"
EX"v44,v44\n"
MF"a[136:139],a[128:131],v[68:71],a[136:139]\n"
RD"a[116:119],v227 offset:29696\n"
PK"v68,v40,v41\n"
PK"v69,v42,v43\n"
MF"a[140:143],a[128:131],v[72:75],a[140:143]\n"
EX"v45,v45\n"
PK"v72,v48,v49\n"
MF"a[144:147],a[128:131],v[76:79],a[144:147]\n"
WT"lgkmcnt(2)\n"
RD"a[120:123],v227 offset:30720\n"
PK"v73,v50,v51\n"
MF"v[112:115],a[96:99],v[8:11],v[112:115]\n"
EX"v46,v46\n"
MF"v[144:147],a[96:99],v[16:19],v[144:147]\n"
RD"a[124:127],v227 offset:31744\n"
PK"v70,v44,v45\n"
MF"v[176:179],a[96:99],v[24:27],v[176:179]\n"
EX"v47,v47\n"
MF"v[116:119],a[100:103],v[8:11],v[116:119]\n"
"v_accvgpr_read_b32 v251,a132\n"
"v_subrev_u32 v253,0x2b800000,v251\n"
MF"v[148:151],a[100:103],v[16:19],v[148:151]\n"
"v_cmp_le_u32 vcc,0x34000000,v253\n"
"s_cmp_lg_u64 vcc,0\n"
"s_cbranch_scc1 L1014_%=\n"
"L1013_%=:\n"
MF"v[180:183],a[100:103],v[24:27],v[180:183]\n"
EX"v52,v52\n"
MF"v[120:123],a[104:107],v[8:11],v[120:123]\n"
"v_rcp_f32 v251,v251\n"
PK"v71,v46,v47\n"
MF"v[152:155],a[104:107],v[16:19],v[152:155]\n"
EX"v53,v53\n"
MF"v[184:187],a[104:107],v[24:27],v[184:187]\n"
EX"v54,v54\n"
MU"v80,v251,v80\n"
MF"v[124:127],a[108:111],v[8:11],v[124:127]\n"
MU"v81,v251,v81\n"
MU"v82,v251,v82\n"
MU"v83,v251,v83\n"
MF"v[156:159],a[108:111],v[16:19],v[156:159]\n"
EX"v55,v55\n"
PK"v74,v52,v53\n"
MF"v[188:191],a[108:111],v[24:27],v[188:191]\n"
WT"lgkmcnt(0)\n"
MU"v84,v251,v84\n"
MU"v85,v251,v85\n"
MU"v86,v251,v86\n"
MF"v[128:131],a[112:115],v[8:11],v[128:131]\n"
WT"vmcnt(16)\n"
"s_barrier\n"
MF"v[160:163],a[112:115],v[16:19],v[160:163]\n"
RD"a[64:67],v226 offset:0\n"
MU"v87,v251,v87\n"
PK"v80,v80,v81\n"
MF"v[192:195],a[112:115],v[24:27],v[192:195]\n"
PK"v81,v82,v83\n"
PK"v82,v84,v85\n"
PK"v83,v86,v87\n"
MF"v[132:135],a[116:119],v[8:11],v[132:135]\n"
BS"v[80:83],v224,s[8:11],s59 offen offset:0 sc1\n"
PK"v75,v54,v55\n"
MF"v[164:167],a[116:119],v[16:19],v[164:167]\n"
RD"a[68:71],v226 offset:1024\n"
MU"v88,v251,v88\n"
MU"v89,v251,v89\n"
MF"v[196:199],a[116:119],v[24:27],v[196:199]\n"
RD"a[72:75],v226 offset:2048\n"
MU"v90,v251,v90\n"
MU"v91,v251,v91\n"
MF"v[136:139],a[120:123],v[8:11],v[136:139]\n"
MU"v92,v251,v92\n"
MU"v93,v251,v93\n"
MU"v94,v251,v94\n"
MF"v[168:171],a[120:123],v[16:19],v[168:171]\n"
MU"v95,v251,v95\n"
PK"v88,v88,v89\n"
PK"v89,v90,v91\n"
MF"v[200:203],a[120:123],v[24:27],v[200:203]\n"
PK"v90,v92,v93\n"
PK"v91,v94,v95\n"
MU"v96,v251,v96\n"
MF"v[140:143],a[124:127],v[8:11],v[140:143]\n"
BS"v[88:91],v224,s[8:11],s59 offen offset:1024 sc1\n"
MU"v97,v251,v97\n"
MF"v[172:175],a[124:127],v[16:19],v[172:175]\n"
RD"a[76:79],v226 offset:3072\n"
MU"v98,v251,v98\n"
MU"v99,v251,v99\n"
MF"v[204:207],a[124:127],v[24:27],v[204:207]\n"
MU"v104,v251,v104\n"
MU"v105,v251,v105\n"
MU"v100,v251,v100\n"
MF"a[136:139],a[128:131],v[8:11],a[136:139]\n"
PK"v96,v96,v97\n"
MU"v106,v251,v106\n"
MU"v101,v251,v101\n"
MF"a[140:143],a[128:131],v[16:19],a[140:143]\n"
MU"v107,v251,v107\n"
MU"v102,v251,v102\n"
MU"v103,v251,v103\n"
MF"a[144:147],a[128:131],v[24:27],a[144:147]\n"
PK"v97,v98,v99\n"
MU"v108,v251,v108\n"
PK"v98,v100,v101\n"
PK"v99,v102,v103\n"
BS"v[96:99],v224,s[8:11],s59 offen offset:2048 sc1\n"
MU"v109,v251,v109\n"
MU"v110,v251,v110\n"
MU"v111,v251,v111\n"
PK"v104,v104,v105\n"
PK"v105,v106,v107\n"
PK"v106,v108,v109\n"
PK"v107,v110,v111\n"
BS"v[104:107],v224,s[8:11],s59 offen offset:3072 sc1\n"
RD"a[80:83],v226 offset:4096\n"
RD"a[84:87],v226 offset:5120\n"
"s_cmp_eq_u32 s34,7\n"
"s_cbranch_scc1 LD2_%=\n"
WT"lgkmcnt(5)\n"
MF"v[0:3],a[64:67],a[32:35],0\n"
"s_mov_b32 m0,s40\n"
EX"v56,v56\n"
MF"v[8:11],a[64:67],a[48:51],0\n"
WT"lgkmcnt(4)\n"
EX"v57,v57\n"
MF"v[0:3],a[68:71],a[36:39],v[0:3]\n"
"s_lshl_b32 s50,s35,6\n"
RD"a[88:91],v226 offset:6144\n"
"v_readlane_b32 s43,v228,s42\n"
MF"v[8:11],a[68:71],a[52:55],v[8:11]\n"
WT"lgkmcnt(4)\n"
EX"v58,v58\n"
MF"v[0:3],a[72:75],a[40:43],v[0:3]\n"
SA"s50,s50,4352\n"
BL"v225,s[8:11],s43 offen offset:0 sc1 lds\n"
"v_readlane_b32 s44,v229,s42\n"
MF"v[8:11],a[72:75],a[56:59],v[8:11]\n"
WT"lgkmcnt(1)\n"
RD"a[92:95],v226 offset:7168\n"
PK"v76,v56,v57\n"
MF"v[0:3],a[76:79],a[44:47],v[0:3]\n"
SA"s42,s42,1\n"
RD"a[96:99],v226 offset:8192\n"
"v_lshrrev_b32 v237,1,v224\n"
MF"v[8:11],a[76:79],a[60:63],v[8:11]\n"
SA"s41,s41,32768\n"
EX"v59,v59\n"
MF"v[4:7],a[80:83],a[32:35],0\n"
"s_cmp_eq_u32 s41,163840\n"
RD"a[100:103],v226 offset:9216\n"
"v_and_b32 v237,56,v237\n"
MF"v[12:15],a[80:83],a[48:51],0\n"
"s_cselect_b32 s41,0,s41\n"
EX"v60,v60\n"
"v_add_u32 v227,s41,v224\n"
MF"v[4:7],a[84:87],a[36:39],v[4:7]\n"
EX"v61,v61\n"
PK"v77,v58,v59\n"
MF"v[12:15],a[84:87],a[52:55],v[12:15]\n"
WT"lgkmcnt(3)\n"
RD"a[104:107],v226 offset:10240\n"
RD"a[108:111],v226 offset:11264\n"
MF"v[4:7],a[88:91],a[40:43],v[4:7]\n"
BL"v225,s[8:11],s43 offen offset:1024 sc1 lds\n"
MF"v[12:15],a[88:91],a[56:59],v[12:15]\n"
WT"lgkmcnt(3)\n"
EX"v62,v62\n"
PK"v78,v60,v61\n"
MF"v[4:7],a[92:95],a[44:47],v[4:7]\n"
RD"a[112:115],v226 offset:12288\n"
RD"a[116:119],v226 offset:13312\n"
MF"v[12:15],a[92:95],a[60:63],v[12:15]\n"
EX"v63,v63\n"
MF"v[16:19],a[96:99],a[32:35],0\n"
EX"v0,v0\n"
MF"v[24:27],a[96:99],a[48:51],0\n"
WT"lgkmcnt(4)\n"
EX"v1,v1\n"
PK"v79,v62,v63\n"
MF"v[16:19],a[100:103],a[36:39],v[16:19]\n"
RD"a[120:123],v226 offset:14336\n"
PK"v0,v0,v1\n"
MF"v[24:27],a[100:103],a[52:55],v[24:27]\n"
WT"lgkmcnt(3)\n"
BL"v225,s[8:11],s43 offen offset:2048 sc1 lds\n"
MF"v[16:19],a[104:107],a[40:43],v[16:19]\n"
"s_lshl_b32 s56,s34,1\n"
EX"v2,v2\n"
MF"v[24:27],a[104:107],a[56:59],v[24:27]\n"
"s_sub_u32 s56,12,s56\n"
RD"a[124:127],v226 offset:15360\n"
RD"a[64:67],v226 offset:16384\n"
MF"v[16:19],a[108:111],a[44:47],v[16:19]\n"
"s_sub_u32 s22,s56,2\n"
EX"v3,v3\n"
MF"v[24:27],a[108:111],a[60:63],v[24:27]\n"
WT"lgkmcnt(3)\n"
RD"a[68:71],v226 offset:17408\n"
PK"v1,v2,v3\n"
MF"v[20:23],a[112:115],a[32:35],0\n"
"s_min_u32 s56,s56,1\n"
EX"v8,v8\n"
MF"v[28:31],a[112:115],a[48:51],0\n"
"s_mov_b32 s57,s61\n"
EX"v9,v9\n"
MF"v[20:23],a[116:119],a[36:39],v[20:23]\n"
BL"v225,s[8:11],s43 offen offset:3072 sc1 lds\n"
MF"v[28:31],a[116:119],a[52:55],v[28:31]\n"
WT"lgkmcnt(3)\n"
RD"a[72:75],v226 offset:18432\n"
PK"v8,v8,v9\n"
MF"v[20:23],a[120:123],a[40:43],v[20:23]\n"
EX"v10,v10\n"
MF"v[28:31],a[120:123],a[56:59],v[28:31]\n"
WT"lgkmcnt(2)\n"
RD"a[76:79],v226 offset:19456\n"
RD"a[80:83],v226 offset:20480\n"
EX"v11,v11\n"
MF"v[20:23],a[124:127],a[44:47],v[20:23]\n"
SA"m0,s40,16384\n"
RD"a[84:87],v226 offset:21504\n"
PK"v9,v10,v11\n"
MF"v[28:31],a[124:127],a[60:63],v[28:31]\n"
EX"v4,v4\n"
MF"v[32:35],a[64:67],a[32:35],0\n"
"buffer_load_dwordx2 v[238:239],v237,s[8:11],s50 offen sc1\n"
MF"v[40:43],a[64:67],a[48:51],0\n"
WT"lgkmcnt(4)\n"
EX"v5,v5\n"
MF"v[32:35],a[68:71],a[36:39],v[32:35]\n"
SA"s40,s40,32768\n"
BL"v225,s[8:11],s44 offen offset:0 sc1 lds\n"
MF"v[40:43],a[68:71],a[52:55],v[40:43]\n"
WT"lgkmcnt(3)\n"
RD"a[88:91],v226 offset:22528\n"
PK"v2,v4,v5\n"
EX"v6,v6\n"
MF"v[32:35],a[72:75],a[40:43],v[32:35]\n"
"s_cmp_eq_u32 s40,s46\n"
RD"a[92:95],v226 offset:23552\n"
RD"a[96:99],v226 offset:24576\n"
MF"v[40:43],a[72:75],a[56:59],v[40:43]\n"
WT"lgkmcnt(3)\n"
EX"v7,v7\n"
MF"v[32:35],a[76:79],a[44:47],v[32:35]\n"
"s_cselect_b32 s40,s47,s40\n"
RD"a[100:103],v226 offset:25600\n"
PK"v3,v6,v7\n"
MF"v[40:43],a[76:79],a[60:63],v[40:43]\n"
EX"v12,v12\n"
MF"v[36:39],a[80:83],a[32:35],0\n"
EX"v13,v13\n"
MF"v[44:47],a[80:83],a[48:51],0\n"
BL"v225,s[8:11],s44 offen offset:1024 sc1 lds\n"
MF"v[36:39],a[84:87],a[36:39],v[36:39]\n"
EX"v14,v14\n"
MF"v[44:47],a[84:87],a[52:55],v[44:47]\n"
WT"lgkmcnt(1)\n"
RD"a[104:107],v226 offset:26624\n"
RD"a[108:111],v226 offset:27648\n"
PK"v10,v12,v13\n"
MF"v[36:39],a[88:91],a[40:43],v[36:39]\n"
EX"v15,v15\n"
MF"v[44:47],a[88:91],a[56:59],v[44:47]\n"
RD"a[112:115],v226 offset:28672\n"
RD"a[116:119],v226 offset:29696\n"
MF"v[36:39],a[92:95],a[44:47],v[36:39]\n"
EX"v16,v16\n"
PK"v11,v14,v15\n"
MF"v[44:47],a[92:95],a[60:63],v[44:47]\n"
EX"v17,v17\n"
MF"v[48:51],a[96:99],a[32:35],0\n"
BL"v225,s[8:11],s44 offen offset:2048 sc1 lds\n"
MF"v[56:59],a[96:99],a[48:51],0\n"
WT"lgkmcnt(4)\n"
EX"v18,v18\n"
MF"v[48:51],a[100:103],a[36:39],v[48:51]\n"
RD"a[120:123],v226 offset:30720\n"
PK"v16,v16,v17\n"
MF"v[56:59],a[100:103],a[52:55],v[56:59]\n"
WT"lgkmcnt(3)\n"
EX"v19,v19\n"
MF"v[48:51],a[104:107],a[40:43],v[48:51]\n"
RD"a[124:127],v226 offset:31744\n"
PK"v17,v18,v19\n"
MF"v[56:59],a[104:107],a[56:59],v[56:59]\n"
EX"v24,v24\n"
MF"v[48:51],a[108:111],a[44:47],v[48:51]\n"
WT"vmcnt(16)\n"
"s_barrier\n"
MF"v[56:59],a[108:111],a[60:63],v[56:59]\n"
WT"lgkmcnt(2)\n"
EX"v25,v25\n"
MF"v[52:55],a[112:115],a[32:35],0\n"
RD"a[96:99],v227 offset:8192\n"
"v_readlane_b32 s43,v228,s42\n"
MF"v[60:63],a[112:115],a[48:51],0\n"
BL"v225,s[8:11],s44 offen offset:3072 sc1 lds\n"
EX"v26,v26\n"
MF"v[52:55],a[116:119],a[36:39],v[52:55]\n"
EX"v27,v27\n"
PK"v24,v24,v25\n"
MF"v[60:63],a[116:119],a[52:55],v[60:63]\n"
WT"lgkmcnt(1)\n"
RD"a[100:103],v227 offset:9216\n"
RD"a[104:107],v227 offset:10240\n"
MF"v[52:55],a[120:123],a[40:43],v[52:55]\n"
"s_mov_b32 m0,s40\n"
EX"v20,v20\n"
MF"v[60:63],a[120:123],a[56:59],v[60:63]\n"
RD"a[108:111],v227 offset:11264\n"
"v_readlane_b32 s44,v229,s42\n"
PK"v25,v26,v27\n"
MF"v[52:55],a[124:127],a[44:47],v[52:55]\n"
EX"v21,v21\n"
MF"v[60:63],a[124:127],a[60:63],v[60:63]\n"
WT"lgkmcnt(3)\n"
EX"v22,v22\n"
MF"v[112:115],a[96:99],v[68:71],v[112:115]\n"
SA"s41,s41,32768\n"
BL"v225,s[8:11],s43 offen offset:0 sc1 lds\n"
MF"v[144:147],a[96:99],v[72:75],v[144:147]\n"
"s_cmp_eq_u32 s41,163840\n"
EX"v23,v23\n"
PK"v18,v20,v21\n"
MF"v[176:179],a[96:99],v[76:79],v[176:179]\n"
WT"lgkmcnt(1)\n"
RD"a[112:115],v227 offset:12288\n"
PK"v19,v22,v23\n"
MF"v[116:119],a[100:103],v[68:71],v[116:119]\n"
"s_cselect_b32 s41,0,s41\n"
EX"v28,v28\n"
"v_add_u32 v226,s41,v224\n"
MF"v[148:151],a[100:103],v[72:75],v[148:151]\n"
SA"s42,s42,1\n"
RD"a[116:119],v227 offset:13312\n"
MF"v[180:183],a[100:103],v[76:79],v[180:183]\n"
EX"v29,v29\n"
MF"v[120:123],a[104:107],v[68:71],v[120:123]\n"
RD"a[120:123],v227 offset:14336\n"
PK"v26,v28,v29\n"
MF"v[152:155],a[104:107],v[72:75],v[152:155]\n"
EX"v30,v30\n"
MF"v[184:187],a[104:107],v[76:79],v[184:187]\n"
WT"lgkmcnt(3)\n"
BL"v225,s[8:11],s43 offen offset:1024 sc1 lds\n"
MF"v[124:127],a[108:111],v[68:71],v[124:127]\n"
EX"v31,v31\n"
MF"v[156:159],a[108:111],v[72:75],v[156:159]\n"
RD"a[124:127],v227 offset:15360\n"
PK"v27,v30,v31\n"
MF"v[188:191],a[108:111],v[76:79],v[188:191]\n"
WT"lgkmcnt(2)\n"
EX"v32,v32\n"
MF"v[128:131],a[112:115],v[68:71],v[128:131]\n"
EX"v33,v33\n"
MF"v[160:163],a[112:115],v[72:75],v[160:163]\n"
RD"a[64:67],v227 offset:16384\n"
PK"v64,v32,v33\n"
MF"v[192:195],a[112:115],v[76:79],v[192:195]\n"
BL"v225,s[8:11],s43 offen offset:2048 sc1 lds\n"
MF"v[132:135],a[116:119],v[68:71],v[132:135]\n"
EX"v34,v34\n"
MF"v[164:167],a[116:119],v[72:75],v[164:167]\n"
EX"v35,v35\n"
MF"v[196:199],a[116:119],v[76:79],v[196:199]\n"
WT"lgkmcnt(1)\n"
EX"v40,v40\n"
MF"v[136:139],a[120:123],v[68:71],v[136:139]\n"
RD"a[68:71],v227 offset:17408\n"
PK"v65,v34,v35\n"
MF"v[168:171],a[120:123],v[72:75],v[168:171]\n"
EX"v41,v41\n"
MF"v[200:203],a[120:123],v[76:79],v[200:203]\n"
RD"a[72:75],v227 offset:18432\n"
RD"a[76:79],v227 offset:19456\n"
MF"v[140:143],a[124:127],v[68:71],v[140:143]\n"
WT"vmcnt(7)\n"
"v_cmp_eq_u64 vcc,s[28:29],v[238:239]\n"
"s_cmp_eq_u64 vcc,exec\n"
"s_cbranch_scc0 L1019_%=\n"
"L1020_%=:\n"
MF"v[172:175],a[124:127],v[72:75],v[172:175]\n"
EX"v42,v42\n"
MF"v[204:207],a[124:127],v[76:79],v[204:207]\n"
RD"a[80:83],v227 offset:20480\n"
MF"a[136:139],a[128:131],v[68:71],a[136:139]\n"
BL"v225,s[8:11],s43 offen offset:3072 sc1 lds\n"
PK"v68,v40,v41\n"
MF"a[140:143],a[128:131],v[72:75],a[140:143]\n"
SA"m0,s40,16384\n"
EX"v43,v43\n"
MF"a[144:147],a[128:131],v[76:79],a[144:147]\n"
WT"lgkmcnt(3)\n"
EX"v36,v36\n"
MF"v[144:147],a[64:67],v[0:3],v[144:147]\n"
RD"a[84:87],v227 offset:21504\n"
RD"a[88:91],v227 offset:22528\n"
PK"v69,v42,v43\n"
MF"v[176:179],a[64:67],v[8:11],v[176:179]\n"
EX"v37,v37\n"
MF"v[148:151],a[68:71],v[0:3],v[148:151]\n"
SA"s51,s59,16384\n"
RD"a[92:95],v227 offset:23552\n"
"v_accvgpr_read_b32 v252,a136\n"
MF"v[180:183],a[68:71],v[8:11],v[180:183]\n"
WT"lgkmcnt(3)\n"
EX"v38,v38\n"
MF"v[152:155],a[72:75],v[0:3],v[152:155]\n"
SA"s40,s40,32768\n"
BL"v225,s[8:11],s44 offen offset:0 sc1 lds\n"
PK"v66,v36,v37\n"
MF"v[184:187],a[72:75],v[8:11],v[184:187]\n"
"s_cmp_eq_u32 s40,s46\n"
EX"v39,v39\n"
"v_subrev_u32 v253,0x2b800000,v252\n"
MF"v[156:159],a[76:79],v[0:3],v[156:159]\n"
"s_cselect_b32 s40,s47,s40\n"
EX"v44,v44\n"
MF"v[188:191],a[76:79],v[8:11],v[188:191]\n"
"v_cmp_le_u32 vcc,0x34000000,v253\n"
"s_cmp_lg_u64 vcc,0\n"
"s_cbranch_scc1 L1023_%=\n"
"L1022_%=:\n"
MF"v[160:163],a[80:83],v[0:3],v[160:163]\n"
"v_rcp_f32 v252,v252\n"
PK"v67,v38,v39\n"
MF"v[192:195],a[80:83],v[8:11],v[192:195]\n"
WT"lgkmcnt(0)\n"
RD"a[96:99],v227 offset:24576\n"
RD"a[100:103],v227 offset:25600\n"
EX"v45,v45\n"
MF"v[164:167],a[84:87],v[0:3],v[164:167]\n"
RD"a[104:107],v227 offset:26624\n"
RD"a[108:111],v227 offset:27648\n"
MU"v112,v252,v112\n"
MF"v[196:199],a[84:87],v[8:11],v[196:199]\n"
EX"v46,v46\n"
MU"v113,v252,v113\n"
MF"v[168:171],a[88:91],v[0:3],v[168:171]\n"
BL"v225,s[8:11],s44 offen offset:1024 sc1 lds\n"
MU"v114,v252,v114\n"
MF"v[200:203],a[88:91],v[8:11],v[200:203]\n"
EX"v47,v47\n"
MU"v115,v252,v115\n"
MF"v[172:175],a[92:95],v[0:3],v[172:175]\n"
EX"v48,v48\n"
MU"v116,v252,v116\n"
MF"v[204:207],a[92:95],v[8:11],v[204:207]\n"
EX"v49,v49\n"
PK"v70,v44,v45\n"
MF"a[140:143],a[128:131],v[0:3],a[140:143]\n"
RD"a[112:115],v227 offset:28672\n"
RD"a[116:119],v227 offset:29696\n"
MU"v117,v252,v117\n"
MF"a[144:147],a[128:131],v[8:11],a[144:147]\n"
WT"lgkmcnt(2)\n"
EX"v50,v50\n"
MU"v118,v252,v118\n"
MF"v[144:147],a[96:99],v[16:19],v[144:147]\n"
RD"a[120:123],v227 offset:30720\n"
RD"a[124:127],v227 offset:31744\n"
MU"v119,v252,v119\n"
MF"v[176:179],a[96:99],v[24:27],v[176:179]\n"
BL"v225,s[8:11],s44 offen offset:2048 sc1 lds\n"
PK"v112,v112,v113\n"
MF"v[148:151],a[100:103],v[16:19],v[148:151]\n"
EX"v51,v51\n"
PK"v113,v114,v115\n"
MF"v[180:183],a[100:103],v[24:27],v[180:183]\n"
EX"v52,v52\n"
PK"v114,v116,v117\n"
MF"v[152:155],a[104:107],v[16:19],v[152:155]\n"
"s_barrier\n"
MF"v[184:187],a[104:107],v[24:27],v[184:187]\n"
EX"v53,v53\n"
PK"v115,v118,v119\n"
MF"v[156:159],a[108:111],v[16:19],v[156:159]\n"
BS"v[112:115],v224,s[8:11],s51 offen offset:0 sc1\n"
RD"a[64:67],v226 offset:0\n"
MF"v[188:191],a[108:111],v[24:27],v[188:191]\n"
WT"lgkmcnt(1)\n"
EX"v54,v54\n"
PK"v71,v46,v47\n"
MF"v[160:163],a[112:115],v[16:19],v[160:163]\n"
RD"a[68:71],v226 offset:1024\n"
RD"a[72:75],v226 offset:2048\n"
MU"v120,v252,v120\n"
MF"v[192:195],a[112:115],v[24:27],v[192:195]\n"
BL"v225,s[8:11],s44 offen offset:3072 sc1 lds\n"
MU"v121,v252,v121\n"
MF"v[164:167],a[116:119],v[16:19],v[164:167]\n"
EX"v55,v55\n"
MU"v122,v252,v122\n"
MF"v[196:199],a[116:119],v[24:27],v[196:199]\n"
MU"v123,v252,v123\n"
MU"v124,v252,v124\n"
PK"v72,v48,v49\n"
MF"v[168:171],a[120:123],v[16:19],v[168:171]\n"
RD"a[76:79],v226 offset:3072\n"
MU"v125,v252,v125\n"
MU"v126,v252,v126\n"
MF"v[200:203],a[120:123],v[24:27],v[200:203]\n"
MU"v127,v252,v127\n"
PK"v120,v120,v121\n"
PK"v121,v122,v123\n"
MF"v[172:175],a[124:127],v[16:19],v[172:175]\n"
MU"v128,v252,v128\n"
MU"v129,v252,v129\n"
MU"v136,v252,v136\n"
MF"v[204:207],a[124:127],v[24:27],v[204:207]\n"
MU"v137,v252,v137\n"
MU"v130,v252,v130\n"
MU"v131,v252,v131\n"
MF"a[140:143],a[128:131],v[16:19],a[140:143]\n"
PK"v128,v128,v129\n"
MU"v138,v252,v138\n"
MU"v139,v252,v139\n"
MF"a[144:147],a[128:131],v[24:27],a[144:147]\n"
PK"v136,v136,v137\n"
MU"v132,v252,v132\n"
MU"v133,v252,v133\n"
PK"v73,v50,v51\n"
PK"v74,v52,v53\n"
PK"v75,v54,v55\n"
PK"v122,v124,v125\n"
PK"v123,v126,v127\n"
BS"v[120:123],v224,s[8:11],s51 offen offset:1024 sc1\n"
MU"v134,v252,v134\n"
MU"v135,v252,v135\n"
PK"v129,v130,v131\n"
PK"v130,v132,v133\n"
PK"v131,v134,v135\n"
BS"v[128:131],v224,s[8:11],s51 offen offset:2048 sc1\n"
MU"v140,v252,v140\n"
MU"v141,v252,v141\n"
MU"v142,v252,v142\n"
MU"v143,v252,v143\n"
PK"v137,v138,v139\n"
PK"v138,v140,v141\n"
PK"v139,v142,v143\n"
BS"v[136:139],v224,s[8:11],s51 offen offset:3072 sc1\n"
RD"a[80:83],v226 offset:4096\n"
RD"a[84:87],v226 offset:5120\n"
".p2align 6\n"
"LP2_%=:\n"
WT"lgkmcnt(5)\n"
MF"v[0:3],a[64:67],a[32:35],0\n"
EX"v56,v56\n"
MF"v[8:11],a[64:67],a[48:51],0\n"
WT"lgkmcnt(4)\n"
RD"a[88:91],v226 offset:6144\n"
"v_readlane_b32 s43,v228,s42\n"
MF"v[0:3],a[68:71],a[36:39],v[0:3]\n"
EX"v57,v57\n"
MF"v[8:11],a[68:71],a[52:55],v[8:11]\n"
WT"lgkmcnt(4)\n"
RD"a[92:95],v226 offset:7168\n"
RD"a[96:99],v226 offset:8192\n"
MF"v[0:3],a[72:75],a[40:43],v[0:3]\n"
"s_mov_b32 m0,s40\n"
EX"v58,v58\n"
MF"v[8:11],a[72:75],a[56:59],v[8:11]\n"
WT"lgkmcnt(3)\n"
RD"a[100:103],v226 offset:9216\n"
"v_readlane_b32 s44,v229,s42\n"
MF"v[0:3],a[76:79],a[44:47],v[0:3]\n"
SA"s41,s41,32768\n"
EX"v59,v59\n"
MF"v[8:11],a[76:79],a[60:63],v[8:11]\n"
"s_cmp_eq_u32 s41,163840\n"
BL"v225,s[8:11],s43 offen offset:0 sc1 lds\n"
MF"v[4:7],a[80:83],a[32:35],0\n"
"s_cselect_b32 s41,0,s41\n"
EX"v60,v60\n"
MF"v[12:15],a[80:83],a[48:51],0\n"
SA"s42,s42,1\n"
RD"a[104:107],v226 offset:10240\n"
"v_add_u32 v227,s41,v224\n"
MF"v[4:7],a[84:87],a[36:39],v[4:7]\n"
EX"v61,v61\n"
MF"v[12:15],a[84:87],a[52:55],v[12:15]\n"
WT"lgkmcnt(2)\n"
RD"a[108:111],v226 offset:11264\n"
RD"a[112:115],v226 offset:12288\n"
MF"v[4:7],a[88:91],a[40:43],v[4:7]\n"
EX"v62,v62\n"
MF"v[12:15],a[88:91],a[56:59],v[12:15]\n"
RD"a[116:119],v226 offset:13312\n"
PK"v76,v56,v57\n"
MF"v[4:7],a[92:95],a[44:47],v[4:7]\n"
EX"v63,v63\n"
MF"v[12:15],a[92:95],a[60:63],v[12:15]\n"
BL"v225,s[8:11],s43 offen offset:1024 sc1 lds\n"
MF"v[16:19],a[96:99],a[32:35],0\n"
EX"v0,v0\n"
MF"v[24:27],a[96:99],a[48:51],0\n"
WT"lgkmcnt(3)\n"
RD"a[120:123],v226 offset:14336\n"
PK"v77,v58,v59\n"
MF"v[16:19],a[100:103],a[36:39],v[16:19]\n"
EX"v1,v1\n"
MF"v[24:27],a[100:103],a[52:55],v[24:27]\n"
RD"a[124:127],v226 offset:15360\n"
RD"a[64:67],v226 offset:16384\n"
MF"v[16:19],a[104:107],a[40:43],v[16:19]\n"
EX"v2,v2\n"
MF"v[24:27],a[104:107],a[56:59],v[24:27]\n"
WT"lgkmcnt(3)\n"
RD"a[68:71],v226 offset:17408\n"
PK"v78,v60,v61\n"
MF"v[16:19],a[108:111],a[44:47],v[16:19]\n"
EX"v3,v3\n"
MF"v[24:27],a[108:111],a[60:63],v[24:27]\n"
BL"v225,s[8:11],s43 offen offset:2048 sc1 lds\n"
MF"v[20:23],a[112:115],a[32:35],0\n"
EX"v8,v8\n"
MF"v[28:31],a[112:115],a[48:51],0\n"
RD"a[72:75],v226 offset:18432\n"
PK"v79,v62,v63\n"
MF"v[20:23],a[116:119],a[36:39],v[20:23]\n"
EX"v9,v9\n"
MF"v[28:31],a[116:119],a[52:55],v[28:31]\n"
WT"lgkmcnt(2)\n"
RD"a[76:79],v226 offset:19456\n"
RD"a[80:83],v226 offset:20480\n"
MF"v[20:23],a[120:123],a[40:43],v[20:23]\n"
EX"v10,v10\n"
MF"v[28:31],a[120:123],a[56:59],v[28:31]\n"
RD"a[84:87],v226 offset:21504\n"
PK"v0,v0,v1\n"
MF"v[20:23],a[124:127],a[44:47],v[20:23]\n"
EX"v11,v11\n"
MF"v[28:31],a[124:127],a[60:63],v[28:31]\n"
BL"v225,s[8:11],s43 offen offset:3072 sc1 lds\n"
MF"v[32:35],a[64:67],a[32:35],0\n"
EX"v4,v4\n"
MF"v[40:43],a[64:67],a[48:51],0\n"
WT"lgkmcnt(3)\n"
RD"a[88:91],v226 offset:22528\n"
PK"v1,v2,v3\n"
MF"v[32:35],a[68:71],a[36:39],v[32:35]\n"
EX"v5,v5\n"
MF"v[40:43],a[68:71],a[52:55],v[40:43]\n"
SA"m0,s40,16384\n"
RD"a[92:95],v226 offset:23552\n"
RD"a[96:99],v226 offset:24576\n"
MF"v[32:35],a[72:75],a[40:43],v[32:35]\n"
EX"v6,v6\n"
MF"v[40:43],a[72:75],a[56:59],v[40:43]\n"
WT"lgkmcnt(3)\n"
RD"a[100:103],v226 offset:25600\n"
PK"v8,v8,v9\n"
MF"v[32:35],a[76:79],a[44:47],v[32:35]\n"
EX"v7,v7\n"
MF"v[40:43],a[76:79],a[60:63],v[40:43]\n"
BL"v225,s[8:11],s44 offen offset:0 sc1 lds\n"
MF"v[36:39],a[80:83],a[32:35],0\n"
SA"s40,s40,32768\n"
EX"v12,v12\n"
MF"v[44:47],a[80:83],a[48:51],0\n"
"s_cmp_eq_u32 s40,s46\n"
RD"a[104:107],v226 offset:26624\n"
PK"v9,v10,v11\n"
MF"v[36:39],a[84:87],a[36:39],v[36:39]\n"
"s_cselect_b32 s40,s47,s40\n"
EX"v13,v13\n"
MF"v[44:47],a[84:87],a[52:55],v[44:47]\n"
WT"lgkmcnt(2)\n"
RD"a[108:111],v226 offset:27648\n"
RD"a[112:115],v226 offset:28672\n"
MF"v[36:39],a[88:91],a[40:43],v[36:39]\n"
EX"v14,v14\n"
MF"v[44:47],a[88:91],a[56:59],v[44:47]\n"
RD"a[116:119],v226 offset:29696\n"
PK"v2,v4,v5\n"
MF"v[36:39],a[92:95],a[44:47],v[36:39]\n"
EX"v15,v15\n"
MF"v[44:47],a[92:95],a[60:63],v[44:47]\n"
BL"v225,s[8:11],s44 offen offset:1024 sc1 lds\n"
MF"v[48:51],a[96:99],a[32:35],0\n"
EX"v16,v16\n"
MF"v[56:59],a[96:99],a[48:51],0\n"
WT"lgkmcnt(3)\n"
RD"a[120:123],v226 offset:30720\n"
PK"v3,v6,v7\n"
MF"v[48:51],a[100:103],a[36:39],v[48:51]\n"
EX"v17,v17\n"
MF"v[56:59],a[100:103],a[52:55],v[56:59]\n"
RD"a[124:127],v226 offset:31744\n"
PK"v10,v12,v13\n"
MF"v[48:51],a[104:107],a[40:43],v[48:51]\n"
WT"vmcnt(14)\n"
"s_barrier\n"
MF"v[56:59],a[104:107],a[56:59],v[56:59]\n"
WT"lgkmcnt(2)\n"
RD"a[64:67],v227 offset:0\n"
PK"v11,v14,v15\n"
MF"v[48:51],a[108:111],a[44:47],v[48:51]\n"
EX"v18,v18\n"
MF"v[56:59],a[108:111],a[60:63],v[56:59]\n"
BL"v225,s[8:11],s44 offen offset:2048 sc1 lds\n"
MF"v[52:55],a[112:115],a[32:35],0\n"
EX"v19,v19\n"
MF"v[60:63],a[112:115],a[48:51],0\n"
RD"a[68:71],v227 offset:1024\n"
PK"v16,v16,v17\n"
MF"v[52:55],a[116:119],a[36:39],v[52:55]\n"
EX"v24,v24\n"
MF"v[60:63],a[116:119],a[52:55],v[60:63]\n"
WT"lgkmcnt(2)\n"
RD"a[72:75],v227 offset:2048\n"
RD"a[76:79],v227 offset:3072\n"
MF"v[52:55],a[120:123],a[40:43],v[52:55]\n"
EX"v25,v25\n"
MF"v[60:63],a[120:123],a[56:59],v[60:63]\n"
RD"a[80:83],v227 offset:4096\n"
PK"v17,v18,v19\n"
MF"v[52:55],a[124:127],a[44:47],v[52:55]\n"
EX"v26,v26\n"
MF"v[60:63],a[124:127],a[60:63],v[60:63]\n"
WT"lgkmcnt(4)\n"
BL"v225,s[8:11],s44 offen offset:3072 sc1 lds\n"
MF"v[144:147],a[64:67],v[64:67],v[144:147]\n"
SA"s41,s41,32768\n"
EX"v27,v27\n"
MF"v[176:179],a[64:67],v[68:71],v[176:179]\n"
WT"lgkmcnt(3)\n"
RD"a[84:87],v227 offset:5120\n"
"v_readlane_b32 s43,v228,s42\n"
MF"v[148:151],a[68:71],v[64:67],v[148:151]\n"
"s_cmp_eq_u32 s41,163840\n"
EX"v20,v20\n"
MF"v[180:183],a[68:71],v[68:71],v[180:183]\n"
WT"lgkmcnt(2)\n"
RD"a[88:91],v227 offset:6144\n"
RD"a[92:95],v227 offset:7168\n"
MF"v[152:155],a[72:75],v[64:67],v[152:155]\n"
"s_cselect_b32 s41,0,s41\n"
EX"v21,v21\n"
MF"v[184:187],a[72:75],v[68:71],v[184:187]\n"
"s_mov_b32 m0,s40\n"
RD"a[96:99],v227 offset:8192\n"
"v_add_u32 v226,s41,v224\n"
MF"v[156:159],a[76:79],v[64:67],v[156:159]\n"
EX"v22,v22\n"
MF"v[188:191],a[76:79],v[68:71],v[188:191]\n"
WT"lgkmcnt(4)\n"
BL"v225,s[8:11],s43 offen offset:0 sc1 lds\n"
MF"v[160:163],a[80:83],v[64:67],v[160:163]\n"
EX"v23,v23\n"
MF"v[192:195],a[80:83],v[68:71],v[192:195]\n"
WT"lgkmcnt(3)\n"
RD"a[100:103],v227 offset:9216\n"
"v_readlane_b32 s44,v229,s42\n"
MF"v[164:167],a[84:87],v[64:67],v[164:167]\n"
SA"s42,s42,1\n"
EX"v28,v28\n"
MF"v[196:199],a[84:87],v[68:71],v[196:199]\n"
WT"lgkmcnt(2)\n"
RD"a[104:107],v227 offset:10240\n"
RD"a[108:111],v227 offset:11264\n"
MF"v[168:171],a[88:91],v[64:67],v[168:171]\n"
EX"v29,v29\n"
MF"v[200:203],a[88:91],v[68:71],v[200:203]\n"
RD"a[112:115],v227 offset:12288\n"
PK"v24,v24,v25\n"
MF"v[172:175],a[92:95],v[64:67],v[172:175]\n"
EX"v30,v30\n"
MF"v[204:207],a[92:95],v[68:71],v[204:207]\n"
BL"v225,s[8:11],s43 offen offset:1024 sc1 lds\n"
MF"a[140:143],a[128:131],v[64:67],a[140:143]\n"
EX"v31,v31\n"
MF"a[144:147],a[128:131],v[68:71],a[144:147]\n"
WT"lgkmcnt(3)\n"
RD"a[116:119],v227 offset:13312\n"
PK"v25,v26,v27\n"
MF"v[144:147],a[96:99],v[72:75],v[144:147]\n"
EX"v32,v32\n"
MF"v[176:179],a[96:99],v[76:79],v[176:179]\n"
RD"a[120:123],v227 offset:14336\n"
RD"a[124:127],v227 offset:15360\n"
MF"v[148:151],a[100:103],v[72:75],v[148:151]\n"
EX"v33,v33\n"
MF"v[180:183],a[100:103],v[76:79],v[180:183]\n"
WT"lgkmcnt(3)\n"
PK"v18,v20,v21\n"
PK"v19,v22,v23\n"
MF"v[152:155],a[104:107],v[72:75],v[152:155]\n"
EX"v34,v34\n"
MF"v[184:187],a[104:107],v[76:79],v[184:187]\n"
BL"v225,s[8:11],s43 offen offset:2048 sc1 lds\n"
MF"v[156:159],a[108:111],v[72:75],v[156:159]\n"
EX"v35,v35\n"
MF"v[188:191],a[108:111],v[76:79],v[188:191]\n"
RD"a[64:67],v227 offset:16384\n"
PK"v26,v28,v29\n"
MF"v[160:163],a[112:115],v[72:75],v[160:163]\n"
EX"v40,v40\n"
MF"v[192:195],a[112:115],v[76:79],v[192:195]\n"
WT"lgkmcnt(1)\n"
RD"a[68:71],v227 offset:17408\n"
RD"a[72:75],v227 offset:18432\n"
MF"v[164:167],a[116:119],v[72:75],v[164:167]\n"
EX"v41,v41\n"
MF"v[196:199],a[116:119],v[76:79],v[196:199]\n"
RD"a[76:79],v227 offset:19456\n"
PK"v27,v30,v31\n"
MF"v[168:171],a[120:123],v[72:75],v[168:171]\n"
EX"v42,v42\n"
MF"v[200:203],a[120:123],v[76:79],v[200:203]\n"
BL"v225,s[8:11],s43 offen offset:3072 sc1 lds\n"
MF"v[172:175],a[124:127],v[72:75],v[172:175]\n"
EX"v43,v43\n"
MF"v[204:207],a[124:127],v[76:79],v[204:207]\n"
RD"a[80:83],v227 offset:20480\n"
PK"v64,v32,v33\n"
MF"a[140:143],a[128:131],v[72:75],a[140:143]\n"
EX"v36,v36\n"
MF"a[144:147],a[128:131],v[76:79],a[144:147]\n"
WT"lgkmcnt(2)\n"
RD"a[84:87],v227 offset:21504\n"
RD"a[88:91],v227 offset:22528\n"
MF"v[144:147],a[64:67],v[0:3],v[144:147]\n"
SA"m0,s40,16384\n"
EX"v37,v37\n"
MF"v[176:179],a[64:67],v[8:11],v[176:179]\n"
"buffer_load_dwordx2 v[242:243],off,s[8:11],s21 sc1\n"
MF"v[148:151],a[68:71],v[0:3],v[148:151]\n"
RD"a[92:95],v227 offset:23552\n"
PK"v65,v34,v35\n"
MF"v[180:183],a[68:71],v[8:11],v[180:183]\n"
EX"v38,v38\n"
MF"v[152:155],a[72:75],v[0:3],v[152:155]\n"
SA"s40,s40,32768\n"
BL"v225,s[8:11],s44 offen offset:0 sc1 lds\n"
MF"v[184:187],a[72:75],v[8:11],v[184:187]\n"
WT"lgkmcnt(3)\n"
EX"v39,v39\n"
MF"v[156:159],a[76:79],v[0:3],v[156:159]\n"
"s_cmp_eq_u32 s40,s46\n"
RD"a[96:99],v227 offset:24576\n"
PK"v68,v40,v41\n"
MF"v[188:191],a[76:79],v[8:11],v[188:191]\n"
"s_cselect_b32 s40,s47,s40\n"
EX"v44,v44\n"
MF"v[160:163],a[80:83],v[0:3],v[160:163]\n"
RD"a[100:103],v227 offset:25600\n"
PK"v69,v42,v43\n"
MF"v[192:195],a[80:83],v[8:11],v[192:195]\n"
WT"lgkmcnt(2)\n"
EX"v45,v45\n"
MF"v[164:167],a[84:87],v[0:3],v[164:167]\n"
RD"a[104:107],v227 offset:26624\n"
PK"v66,v36,v37\n"
MF"v[196:199],a[84:87],v[8:11],v[196:199]\n"
EX"v46,v46\n"
MF"v[168:171],a[88:91],v[0:3],v[168:171]\n"
BL"v225,s[8:11],s44 offen offset:1024 sc1 lds\n"
MF"v[200:203],a[88:91],v[8:11],v[200:203]\n"
EX"v47,v47\n"
MF"v[172:175],a[92:95],v[0:3],v[172:175]\n"
RD"a[108:111],v227 offset:27648\n"
PK"v67,v38,v39\n"
MF"v[204:207],a[92:95],v[8:11],v[204:207]\n"
EX"v48,v48\n"
MF"a[140:143],a[128:131],v[0:3],a[140:143]\n"
RD"a[112:115],v227 offset:28672\n"
RD"a[116:119],v227 offset:29696\n"
MF"a[144:147],a[128:131],v[8:11],a[144:147]\n"
WT"lgkmcnt(3)\n"
EX"v49,v49\n"
MF"v[144:147],a[96:99],v[16:19],v[144:147]\n"
RD"a[120:123],v227 offset:30720\n"
PK"v70,v44,v45\n"
MF"v[176:179],a[96:99],v[24:27],v[176:179]\n"
EX"v50,v50\n"
MF"v[148:151],a[100:103],v[16:19],v[148:151]\n"
BL"v225,s[8:11],s44 offen offset:2048 sc1 lds\n"
MF"v[180:183],a[100:103],v[24:27],v[180:183]\n"
EX"v51,v51\n"
MF"v[152:155],a[104:107],v[16:19],v[152:155]\n"
RD"a[124:127],v227 offset:31744\n"
PK"v71,v46,v47\n"
MF"v[184:187],a[104:107],v[24:27],v[184:187]\n"
WT"lgkmcnt(2)\n"
WT"vmcnt(16)\n"
"s_barrier\n"
MF"v[156:159],a[108:111],v[16:19],v[156:159]\n"
RD"a[64:67],v226 offset:0\n"
PK"v72,v48,v49\n"
MF"v[188:191],a[108:111],v[24:27],v[188:191]\n"
EX"v52,v52\n"
MF"v[160:163],a[112:115],v[16:19],v[160:163]\n"
RD"a[68:71],v226 offset:1024\n"
PK"v73,v50,v51\n"
MF"v[192:195],a[112:115],v[24:27],v[192:195]\n"
EX"v53,v53\n"
MF"v[164:167],a[116:119],v[16:19],v[164:167]\n"
BL"v225,s[8:11],s44 offen offset:3072 sc1 lds\n"
MF"v[196:199],a[116:119],v[24:27],v[196:199]\n"
WT"lgkmcnt(2)\n"
EX"v54,v54\n"
MF"v[168:171],a[120:123],v[16:19],v[168:171]\n"
RD"a[72:75],v226 offset:2048\n"
PK"v74,v52,v53\n"
MF"v[200:203],a[120:123],v[24:27],v[200:203]\n"
EX"v55,v55\n"
MF"v[172:175],a[124:127],v[16:19],v[172:175]\n"
"buffer_store_dwordx2 v[232:233],v230,s[8:11],s57 offen sc1\n"
MF"v[204:207],a[124:127],v[24:27],v[204:207]\n"
"s_mov_b32 s57,2130706432\n"
RD"a[76:79],v226 offset:3072\n"
PK"v75,v54,v55\n"
MF"a[140:143],a[128:131],v[16:19],a[140:143]\n"
RD"a[80:83],v226 offset:4096\n"
MF"a[144:147],a[128:131],v[24:27],a[144:147]\n"
RD"a[84:87],v226 offset:5120\n"
"s_sub_u32 s56,s56,1\n"
"s_cbranch_scc0 LP2_%=\n"
"s_cmp_lt_i32 s22,0\n"
"s_cbranch_scc1 LE2_%=\n"
WT"lgkmcnt(5)\n"
MF"v[0:3],a[64:67],a[32:35],0\n"
EX"v56,v56\n"
MF"v[8:11],a[64:67],a[48:51],0\n"
WT"lgkmcnt(4)\n"
RD"a[88:91],v226 offset:6144\n"
"v_bfe_u32 v238,v224,4,2\n"
MF"v[0:3],a[68:71],a[36:39],v[0:3]\n"
"s_andn2_b32 s63,s61,255\n"
EX"v57,v57\n"
MF"v[8:11],a[68:71],a[52:55],v[8:11]\n"
WT"lgkmcnt(4)\n"
RD"a[92:95],v226 offset:7168\n"
RD"a[96:99],v226 offset:8192\n"
MF"v[0:3],a[72:75],a[40:43],v[0:3]\n"
"s_mov_b32 m0,s40\n"
EX"v58,v58\n"
MF"v[8:11],a[72:75],a[56:59],v[8:11]\n"
WT"lgkmcnt(3)\n"
RD"a[100:103],v226 offset:9216\n"
"v_readlane_b32 s43,v228,s42\n"
MF"v[0:3],a[76:79],a[44:47],v[0:3]\n"
SA"s41,s41,32768\n"
EX"v59,v59\n"
MF"v[8:11],a[76:79],a[60:63],v[8:11]\n"
"s_cmp_eq_u32 s41,163840\n"
BL"v225,s[8:11],s43 offen offset:0 sc1 lds\n"
MF"v[4:7],a[80:83],a[32:35],0\n"
"s_cselect_b32 s41,0,s41\n"
EX"v60,v60\n"
MF"v[12:15],a[80:83],a[48:51],0\n"
RD"a[104:107],v226 offset:10240\n"
"v_lshlrev_b32 v238,6,v238\n"
MF"v[4:7],a[84:87],a[36:39],v[4:7]\n"
"buffer_load_dwordx2 v[240:241],v238,s[8:11],s63 offen sc1\n"
MF"v[12:15],a[84:87],a[52:55],v[12:15]\n"
WT"lgkmcnt(2)\n"
EX"v61,v61\n"
PK"v76,v56,v57\n"
MF"v[4:7],a[88:91],a[40:43],v[4:7]\n"
RD"a[108:111],v226 offset:11264\n"
RD"a[112:115],v226 offset:12288\n"
"v_add_u32 v227,s41,v224\n"
MF"v[12:15],a[88:91],a[56:59],v[12:15]\n"
EX"v62,v62\n"
"v_readlane_b32 s44,v229,s42\n"
MF"v[4:7],a[92:95],a[44:47],v[4:7]\n"
SA"s42,s42,1\n"
RD"a[116:119],v226 offset:13312\n"
PK"v77,v58,v59\n"
MF"v[12:15],a[92:95],a[60:63],v[12:15]\n"
EX"v63,v63\n"
MF"v[16:19],a[96:99],a[32:35],0\n"
BL"v225,s[8:11],s43 offen offset:1024 sc1 lds\n"
MF"v[24:27],a[96:99],a[48:51],0\n"
WT"lgkmcnt(3)\n"
EX"v0,v0\n"
MF"v[16:19],a[100:103],a[36:39],v[16:19]\n"
RD"a[120:123],v226 offset:14336\n"
PK"v78,v60,v61\n"
MF"v[24:27],a[100:103],a[52:55],v[24:27]\n"
EX"v1,v1\n"
MF"v[16:19],a[104:107],a[40:43],v[16:19]\n"
RD"a[124:127],v226 offset:15360\n"
RD"a[64:67],v226 offset:16384\n"
MF"v[24:27],a[104:107],a[56:59],v[24:27]\n"
WT"lgkmcnt(3)\n"
EX"v2,v2\n"
MF"v[16:19],a[108:111],a[44:47],v[16:19]\n"
RD"a[68:71],v226 offset:17408\n"
PK"v79,v62,v63\n"
MF"v[24:27],a[108:111],a[60:63],v[24:27]\n"
EX"v3,v3\n"
MF"v[20:23],a[112:115],a[32:35],0\n"
BL"v225,s[8:11],s43 offen offset:2048 sc1 lds\n"
MF"v[28:31],a[112:115],a[48:51],0\n"
EX"v8,v8\n"
MF"v[20:23],a[116:119],a[36:39],v[20:23]\n"
RD"a[72:75],v226 offset:18432\n"
PK"v0,v0,v1\n"
MF"v[28:31],a[116:119],a[52:55],v[28:31]\n"
WT"lgkmcnt(2)\n"
EX"v9,v9\n"
MF"v[20:23],a[120:123],a[40:43],v[20:23]\n"
RD"a[76:79],v226 offset:19456\n"
RD"a[80:83],v226 offset:20480\n"
MF"v[28:31],a[120:123],a[56:59],v[28:31]\n"
EX"v10,v10\n"
MF"v[20:23],a[124:127],a[44:47],v[20:23]\n"
RD"a[84:87],v226 offset:21504\n"
PK"v1,v2,v3\n"
MF"v[28:31],a[124:127],a[60:63],v[28:31]\n"
EX"v11,v11\n"
MF"v[32:35],a[64:67],a[32:35],0\n"
BL"v225,s[8:11],s43 offen offset:3072 sc1 lds\n"
MF"v[40:43],a[64:67],a[48:51],0\n"
WT"lgkmcnt(3)\n"
EX"v4,v4\n"
MF"v[32:35],a[68:71],a[36:39],v[32:35]\n"
RD"a[88:91],v226 offset:22528\n"
PK"v8,v8,v9\n"
MF"v[40:43],a[68:71],a[52:55],v[40:43]\n"
SA"m0,s40,16384\n"
EX"v5,v5\n"
MF"v[32:35],a[72:75],a[40:43],v[32:35]\n"
RD"a[92:95],v226 offset:23552\n"
RD"a[96:99],v226 offset:24576\n"
MF"v[40:43],a[72:75],a[56:59],v[40:43]\n"
WT"lgkmcnt(3)\n"
EX"v6,v6\n"
MF"v[32:35],a[76:79],a[44:47],v[32:35]\n"
SA"s48,s59,32768\n"
RD"a[100:103],v226 offset:25600\n"
PK"v9,v10,v11\n"
MF"v[40:43],a[76:79],a[60:63],v[40:43]\n"
"s_and_b32 s48,s48,0x18000\n"
EX"v7,v7\n"
MF"v[36:39],a[80:83],a[32:35],0\n"
"s_andn2_b32 s49,s59,0x18000\n"
BL"v225,s[8:11],s44 offen offset:0 sc1 lds\n"
MF"v[44:47],a[80:83],a[48:51],0\n"
"s_or_b32 s52,s48,s49\n"
WT"vmcnt(4)\n"
"v_cmp_eq_u64 vcc,s[28:29],v[240:241]\n"
"s_cmp_eq_u64 vcc,exec\n"
"s_cbranch_scc0 L1028_%=\n"
"L1029_%=:\n"
MF"v[36:39],a[84:87],a[36:39],v[36:39]\n"
SA"s53,s52,16384\n"
EX"v12,v12\n"
MF"v[44:47],a[84:87],a[52:55],v[44:47]\n"
WT"lgkmcnt(1)\n"
RD"a[104:107],v226 offset:26624\n"
RD"a[108:111],v226 offset:27648\n"
PK"v2,v4,v5\n"
MF"v[36:39],a[88:91],a[40:43],v[36:39]\n"
SA"s40,s40,32768\n"
BL"a[0:3],v224,s[8:11],s52 offen offset:0 sc1\n"
EX"v13,v13\n"
MF"v[44:47],a[88:91],a[56:59],v[44:47]\n"
"s_cmp_eq_u32 s40,s46\n"
RD"a[112:115],v226 offset:28672\n"
RD"a[116:119],v226 offset:29696\n"
MF"v[36:39],a[92:95],a[44:47],v[36:39]\n"
"s_cselect_b32 s40,s47,s40\n"
EX"v14,v14\n"
MF"v[44:47],a[92:95],a[60:63],v[44:47]\n"
BL"a[4:7],v224,s[8:11],s53 offen offset:0 sc1\n"
PK"v3,v6,v7\n"
MF"v[48:51],a[96:99],a[32:35],0\n"
EX"v15,v15\n"
MF"v[56:59],a[96:99],a[48:51],0\n"
WT"lgkmcnt(4)\n"
BL"v225,s[8:11],s44 offen offset:1024 sc1 lds\n"
MF"v[48:51],a[100:103],a[36:39],v[48:51]\n"
EX"v16,v16\n"
MF"v[56:59],a[100:103],a[52:55],v[56:59]\n"
WT"lgkmcnt(2)\n"
BL"a[8:11],v224,s[8:11],s52 offen offset:1024 sc1\n"
RD"a[120:123],v226 offset:30720\n"
MF"v[48:51],a[104:107],a[40:43],v[48:51]\n"
EX"v17,v17\n"
PK"v10,v12,v13\n"
MF"v[56:59],a[104:107],a[56:59],v[56:59]\n"
RD"a[124:127],v226 offset:31744\n"
PK"v11,v14,v15\n"
MF"v[48:51],a[108:111],a[44:47],v[48:51]\n"
"s_barrier\n"
MF"v[56:59],a[108:111],a[60:63],v[56:59]\n"
WT"lgkmcnt(2)\n"
BL"a[12:15],v224,s[8:11],s53 offen offset:1024 sc1\n"
RD"a[64:67],v227 offset:0\n"
MF"v[52:55],a[112:115],a[32:35],0\n"
EX"v18,v18\n"
MF"v[60:63],a[112:115],a[48:51],0\n"
BL"v225,s[8:11],s44 offen offset:2048 sc1 lds\n"
MF"v[52:55],a[116:119],a[36:39],v[52:55]\n"
BL"a[16:19],v224,s[8:11],s52 offen offset:2048 sc1\n"
EX"v19,v19\n"
MF"v[60:63],a[116:119],a[52:55],v[60:63]\n"
WT"lgkmcnt(2)\n"
RD"a[68:71],v227 offset:1024\n"
RD"a[72:75],v227 offset:2048\n"
PK"v16,v16,v17\n"
MF"v[52:55],a[120:123],a[40:43],v[52:55]\n"
EX"v24,v24\n"
MF"v[60:63],a[120:123],a[56:59],v[60:63]\n"
WT"lgkmcnt(3)\n"
RD"a[76:79],v227 offset:3072\n"
RD"a[80:83],v227 offset:4096\n"
MF"v[52:55],a[124:127],a[44:47],v[52:55]\n"
BL"a[20:23],v224,s[8:11],s53 offen offset:2048 sc1\n"
EX"v25,v25\n"
MF"v[60:63],a[124:127],a[60:63],v[60:63]\n"
WT"lgkmcnt(4)\n"
EX"v26,v26\n"
MF"v[144:147],a[64:67],v[64:67],v[144:147]\n"
SA"s41,s41,32768\n"
BL"v225,s[8:11],s44 offen offset:3072 sc1 lds\n"
MF"v[176:179],a[64:67],v[68:71],v[176:179]\n"
WT"lgkmcnt(3)\n"
BL"a[24:27],v224,s[8:11],s52 offen offset:3072 sc1\n"
PK"v17,v18,v19\n"
MF"v[148:151],a[68:71],v[64:67],v[148:151]\n"
"s_cmp_eq_u32 s41,163840\n"
EX"v27,v27\n"
MF"v[180:183],a[68:71],v[68:71],v[180:183]\n"
WT"lgkmcnt(2)\n"
RD"a[84:87],v227 offset:5120\n"
RD"a[88:91],v227 offset:6144\n"
MF"v[152:155],a[72:75],v[64:67],v[152:155]\n"
"s_cselect_b32 s41,0,s41\n"
EX"v20,v20\n"
MF"v[184:187],a[72:75],v[68:71],v[184:187]\n"
WT"lgkmcnt(2)\n"
BL"a[28:31],v224,s[8:11],s53 offen offset:3072 sc1\n"
RD"a[92:95],v227 offset:7168\n"
MF"v[156:159],a[76:79],v[64:67],v[156:159]\n"
SA"s48,s59,65536\n"
EX"v21,v21\n"
PK"v24,v24,v25\n"
MF"v[188:191],a[76:79],v[68:71],v[188:191]\n"
"s_and_b32 s48,s48,0x18000\n"
RD"a[96:99],v227 offset:8192\n"
"v_add_u32 v226,s41,v224\n"
"v_readlane_b32 s43,v228,s42\n"
MF"v[160:163],a[80:83],v[64:67],v[160:163]\n"
"s_mov_b32 m0,s40\n"
EX"v22,v22\n"
MF"v[192:195],a[80:83],v[68:71],v[192:195]\n"
WT"lgkmcnt(3)\n"
BL"v225,s[8:11],s43 offen offset:0 sc1 lds\n"
PK"v25,v26,v27\n"
MF"v[164:167],a[84:87],v[64:67],v[164:167]\n"
"s_andn2_b32 s49,s59,0x18000\n"
EX"v23,v23\n"
MF"v[196:199],a[84:87],v[68:71],v[196:199]\n"
WT"lgkmcnt(2)\n"
RD"a[100:103],v227 offset:9216\n"
RD"a[104:107],v227 offset:10240\n"
MF"v[168:171],a[88:91],v[64:67],v[168:171]\n"
"s_or_b32 s52,s48,s49\n"
EX"v28,v28\n"
MF"v[200:203],a[88:91],v[68:71],v[200:203]\n"
WT"lgkmcnt(3)\n"
BL"a[180:183],v224,s[8:11],s52 offen offset:0 sc1\n"
RD"a[108:111],v227 offset:11264\n"
MF"v[172:175],a[92:95],v[64:67],v[172:175]\n"
SA"s53,s52,16384\n"
EX"v29,v29\n"
"v_readlane_b32 s44,v229,s42\n"
MF"v[204:207],a[92:95],v[68:71],v[204:207]\n"
SA"s42,s42,1\n"
BL"a[184:187],v224,s[8:11],s53 offen offset:0 sc1\n"
RD"a[112:115],v227 offset:12288\n"
MF"a[140:143],a[128:131],v[64:67],a[140:143]\n"
EX"v30,v30\n"
PK"v18,v20,v21\n"
MF"a[144:147],a[128:131],v[68:71],a[144:147]\n"
WT"lgkmcnt(4)\n"
BL"a[188:191],v224,s[8:11],s52 offen offset:1024 sc1\n"
BL"v225,s[8:11],s43 offen offset:1024 sc1 lds\n"
MF"v[144:147],a[96:99],v[72:75],v[144:147]\n"
EX"v31,v31\n"
PK"v19,v22,v23\n"
MF"v[176:179],a[96:99],v[76:79],v[176:179]\n"
WT"lgkmcnt(2)\n"
RD"a[116:119],v227 offset:13312\n"
RD"a[120:123],v227 offset:14336\n"
MF"v[148:151],a[100:103],v[72:75],v[148:151]\n"
BL"a[192:195],v224,s[8:11],s53 offen offset:1024 sc1\n"
EX"v32,v32\n"
MF"v[180:183],a[100:103],v[76:79],v[180:183]\n"
RD"a[124:127],v227 offset:15360\n"
PK"v26,v28,v29\n"
MF"v[152:155],a[104:107],v[72:75],v[152:155]\n"
EX"v33,v33\n"
MF"v[184:187],a[104:107],v[76:79],v[184:187]\n"
WT"lgkmcnt(3)\n"
BL"a[196:199],v224,s[8:11],s52 offen offset:2048 sc1\n"
MF"v[156:159],a[108:111],v[72:75],v[156:159]\n"
EX"v34,v34\n"
MF"v[188:191],a[108:111],v[76:79],v[188:191]\n"
BL"v225,s[8:11],s43 offen offset:2048 sc1 lds\n"
PK"v27,v30,v31\n"
MF"v[160:163],a[112:115],v[72:75],v[160:163]\n"
EX"v35,v35\n"
MF"v[192:195],a[112:115],v[76:79],v[192:195]\n"
WT"lgkmcnt(1)\n"
RD"a[64:67],v227 offset:16384\n"
RD"a[68:71],v227 offset:17408\n"
MF"v[164:167],a[116:119],v[72:75],v[164:167]\n"
BL"a[200:203],v224,s[8:11],s53 offen offset:2048 sc1\n"
EX"v40,v40\n"
MF"v[196:199],a[116:119],v[76:79],v[196:199]\n"
RD"a[72:75],v227 offset:18432\n"
RD"a[76:79],v227 offset:19456\n"
PK"v64,v32,v33\n"
MF"v[168:171],a[120:123],v[72:75],v[168:171]\n"
EX"v41,v41\n"
MF"v[200:203],a[120:123],v[76:79],v[200:203]\n"
WT"lgkmcnt(4)\n"
BL"a[204:207],v224,s[8:11],s52 offen offset:3072 sc1\n"
MF"v[172:175],a[124:127],v[72:75],v[172:175]\n"
SA"s48,s59,98304\n"
EX"v42,v42\n"
MF"v[204:207],a[124:127],v[76:79],v[204:207]\n"
"s_and_b32 s48,s48,0x18000\n"
BL"v225,s[8:11],s43 offen offset:3072 sc1 lds\n"
PK"v65,v34,v35\n"
MF"a[140:143],a[128:131],v[72:75],a[140:143]\n"
"s_andn2_b32 s49,s59,0x18000\n"
EX"v43,v43\n"
MF"a[144:147],a[128:131],v[76:79],a[144:147]\n"
WT"lgkmcnt(2)\n"
BL"a[208:211],v224,s[8:11],s53 offen offset:3072 sc1\n"
RD"a[80:83],v227 offset:20480\n"
MF"v[144:147],a[64:67],v[0:3],v[144:147]\n"
"s_or_b32 s52,s48,s49\n"
EX"v36,v36\n"
MF"v[176:179],a[64:67],v[8:11],v[176:179]\n"
SA"s53,s52,16384\n"
RD"a[84:87],v227 offset:21504\n"
RD"a[88:91],v227 offset:22528\n"
PK"v68,v40,v41\n"
MF"v[148:151],a[68:71],v[0:3],v[148:151]\n"
SA"m0,s40,16384\n"
EX"v37,v37\n"
MF"v[180:183],a[68:71],v[8:11],v[180:183]\n"
WT"lgkmcnt(3)\n"
BL"a[212:215],v224,s[8:11],s52 offen offset:0 sc1\n"
RD"a[92:95],v227 offset:23552\n"
MF"v[152:155],a[72:75],v[0:3],v[152:155]\n"
SA"s40,s40,32768\n"
EX"v38,v38\n"
MF"v[184:187],a[72:75],v[8:11],v[184:187]\n"
"s_cmp_eq_u32 s40,s46\n"
BL"v225,s[8:11],s44 offen offset:0 sc1 lds\n"
PK"v69,v42,v43\n"
MF"v[156:159],a[76:79],v[0:3],v[156:159]\n"
"s_cselect_b32 s40,s47,s40\n"
BL"a[216:219],v224,s[8:11],s53 offen offset:0 sc1\n"
EX"v39,v39\n"
MF"v[188:191],a[76:79],v[8:11],v[188:191]\n"
WT"lgkmcnt(3)\n"
RD"a[96:99],v227 offset:24576\n"
RD"a[100:103],v227 offset:25600\n"
MF"v[160:163],a[80:83],v[0:3],v[160:163]\n"
EX"v44,v44\n"
MF"v[192:195],a[80:83],v[8:11],v[192:195]\n"
WT"lgkmcnt(3)\n"
BL"a[220:223],v224,s[8:11],s52 offen offset:1024 sc1\n"
PK"v66,v36,v37\n"
MF"v[164:167],a[84:87],v[0:3],v[164:167]\n"
EX"v45,v45\n"
MF"v[196:199],a[84:87],v[8:11],v[196:199]\n"
RD"a[104:107],v227 offset:26624\n"
PK"v67,v38,v39\n"
MF"v[168:171],a[88:91],v[0:3],v[168:171]\n"
EX"v46,v46\n"
MF"v[200:203],a[88:91],v[8:11],v[200:203]\n"
WT"lgkmcnt(3)\n"
BL"v225,s[8:11],s44 offen offset:1024 sc1 lds\n"
MF"v[172:175],a[92:95],v[0:3],v[172:175]\n"
BL"a[224:227],v224,s[8:11],s53 offen offset:1024 sc1\n"
EX"v47,v47\n"
MF"v[204:207],a[92:95],v[8:11],v[204:207]\n"
RD"a[108:111],v227 offset:27648\n"
RD"a[112:115],v227 offset:28672\n"
MF"a[140:143],a[128:131],v[0:3],a[140:143]\n"
EX"v48,v48\n"
MF"a[144:147],a[128:131],v[8:11],a[144:147]\n"
WT"lgkmcnt(3)\n"
RD"a[116:119],v227 offset:29696\n"
RD"a[120:123],v227 offset:30720\n"
PK"v70,v44,v45\n"
MF"v[144:147],a[96:99],v[16:19],v[144:147]\n"
BL"a[228:231],v224,s[8:11],s52 offen offset:2048 sc1\n"
EX"v49,v49\n"
MF"v[176:179],a[96:99],v[24:27],v[176:179]\n"
EX"v50,v50\n"
MF"v[148:151],a[100:103],v[16:19],v[148:151]\n"
BL"a[232:235],v224,s[8:11],s53 offen offset:2048 sc1\n"
MF"v[180:183],a[100:103],v[24:27],v[180:183]\n"
WT"lgkmcnt(4)\n"
BL"v225,s[8:11],s44 offen offset:2048 sc1 lds\n"
PK"v71,v46,v47\n"
MF"v[152:155],a[104:107],v[16:19],v[152:155]\n"
EX"v51,v51\n"
MF"v[184:187],a[104:107],v[24:27],v[184:187]\n"
WT"lgkmcnt(2)\n"
RD"a[124:127],v227 offset:31744\n"
PK"v72,v48,v49\n"
MF"v[156:159],a[108:111],v[16:19],v[156:159]\n"
"s_barrier\n"
MF"v[188:191],a[108:111],v[24:27],v[188:191]\n"
BL"a[236:239],v224,s[8:11],s52 offen offset:3072 sc1\n"
RD"a[64:67],v226 offset:0\n"
MF"v[160:163],a[112:115],v[16:19],v[160:163]\n"
EX"v52,v52\n"
MF"v[192:195],a[112:115],v[24:27],v[192:195]\n"
WT"lgkmcnt(2)\n"
RD"a[68:71],v226 offset:1024\n"
PK"v73,v50,v51\n"
MF"v[164:167],a[116:119],v[16:19],v[164:167]\n"
EX"v53,v53\n"
MF"v[196:199],a[116:119],v[24:27],v[196:199]\n"
BL"a[240:243],v224,s[8:11],s53 offen offset:3072 sc1\n"
BL"v225,s[8:11],s44 offen offset:3072 sc1 lds\n"
MF"v[168:171],a[120:123],v[16:19],v[168:171]\n"
EX"v54,v54\n"
MF"v[200:203],a[120:123],v[24:27],v[200:203]\n"
WT"lgkmcnt(2)\n"
RD"a[72:75],v226 offset:2048\n"
RD"a[76:79],v226 offset:3072\n"
MF"v[172:175],a[124:127],v[16:19],v[172:175]\n"
EX"v55,v55\n"
MF"v[204:207],a[124:127],v[24:27],v[204:207]\n"
RD"a[80:83],v226 offset:4096\n"
RD"a[84:87],v226 offset:5120\n"
MF"a[140:143],a[128:131],v[16:19],a[140:143]\n"
PK"v74,v52,v53\n"
PK"v75,v54,v55\n"
MF"a[144:147],a[128:131],v[24:27],a[144:147]\n"
"s_sub_u32 s56,s22,1\n"
"s_mov_b32 s22,-1\n"
"s_cbranch_scc0 LP2_%=\n"
"LE2_%=:\n"
WT"lgkmcnt(5)\n"
MF"v[0:3],a[64:67],a[32:35],v[208:211]\n"
WT"vmcnt(5)\n"
"v_cmp_eq_u64 vcc,s[28:29],v[242:243]\n"
"s_cmp_eq_u64 vcc,exec\n"
"s_cbranch_scc0 L1031_%=\n"
"L1032_%=:\n"
MF"v[8:11],a[64:67],a[48:51],0\n"
WT"lgkmcnt(4)\n"
EX"v56,v56\n"
MF"v[0:3],a[68:71],a[36:39],v[0:3]\n"
"s_mov_b32 m0,s40\n"
EX"v57,v57\n"
MF"v[8:11],a[68:71],a[52:55],v[8:11]\n"
WT"lgkmcnt(3)\n"
RD"a[88:91],v226 offset:6144\n"
RD"a[92:95],v226 offset:7168\n"
"v_readlane_b32 s43,v228,s42\n"
MF"v[0:3],a[72:75],a[40:43],v[0:3]\n"
SA"s41,s41,32768\n"
EX"v58,v58\n"
"v_readlane_b32 s44,v229,s42\n"
MF"v[8:11],a[72:75],a[56:59],v[8:11]\n"
WT"lgkmcnt(2)\n"
BL"v225,s[8:11],s43 offen offset:0 sc1 lds\n"
RD"a[96:99],v226 offset:8192\n"
MF"v[0:3],a[76:79],a[44:47],v[0:3]\n"
SA"s42,s42,1\n"
EX"v59,v59\n"
PK"v76,v56,v57\n"
MF"v[8:11],a[76:79],a[60:63],v[8:11]\n"
"s_cmp_eq_u32 s41,163840\n"
RD"a[100:103],v226 offset:9216\n"
PK"v77,v58,v59\n"
MF"v[4:7],a[80:83],a[32:35],v[212:215]\n"
"s_cselect_b32 s41,0,s41\n"
EX"v60,v60\n"
"v_add_u32 v227,s41,v224\n"
MF"v[12:15],a[80:83],a[48:51],0\n"
BL"v225,s[8:11],s43 offen offset:1024 sc1 lds\n"
MF"v[4:7],a[84:87],a[36:39],v[4:7]\n"
EX"v61,v61\n"
MF"v[12:15],a[84:87],a[52:55],v[12:15]\n"
WT"lgkmcnt(2)\n"
RD"a[104:107],v226 offset:10240\n"
RD"a[108:111],v226 offset:11264\n"
MF"v[4:7],a[88:91],a[40:43],v[4:7]\n"
EX"v62,v62\n"
MF"v[12:15],a[88:91],a[56:59],v[12:15]\n"
RD"a[112:115],v226 offset:12288\n"
RD"a[116:119],v226 offset:13312\n"
PK"v78,v60,v61\n"
MF"v[4:7],a[92:95],a[44:47],v[4:7]\n"
EX"v63,v63\n"
MF"v[12:15],a[92:95],a[60:63],v[12:15]\n"
WT"lgkmcnt(4)\n"
BL"v225,s[8:11],s43 offen offset:2048 sc1 lds\n"
MF"v[16:19],a[96:99],a[32:35],v[216:219]\n"
EX"v0,v0\n"
MF"v[24:27],a[96:99],a[48:51],0\n"
RD"a[120:123],v226 offset:14336\n"
RD"a[124:127],v226 offset:15360\n"
PK"v79,v62,v63\n"
MF"v[16:19],a[100:103],a[36:39],v[16:19]\n"
EX"v1,v1\n"
MF"v[24:27],a[100:103],a[52:55],v[24:27]\n"
WT"lgkmcnt(4)\n"
RD"a[64:67],v226 offset:16384\n"
RD"a[68:71],v226 offset:17408\n"
MF"v[16:19],a[104:107],a[40:43],v[16:19]\n"
BL"v225,s[8:11],s43 offen offset:3072 sc1 lds\n"
MF"v[24:27],a[104:107],a[56:59],v[24:27]\n"
SA"m0,s40,16384\n"
EX"v2,v2\n"
PK"v0,v0,v1\n"
MF"v[16:19],a[108:111],a[44:47],v[16:19]\n"
RD"a[72:75],v226 offset:18432\n"
RD"a[76:79],v226 offset:19456\n"
MF"v[24:27],a[108:111],a[60:63],v[24:27]\n"
WT"lgkmcnt(6)\n"
EX"v3,v3\n"
MF"v[20:23],a[112:115],a[32:35],v[220:223]\n"
RD"a[80:83],v226 offset:20480\n"
RD"a[84:87],v226 offset:21504\n"
MF"v[28:31],a[112:115],a[48:51],0\n"
EX"v8,v8\n"
PK"v1,v2,v3\n"
MF"v[20:23],a[116:119],a[36:39],v[20:23]\n"
SA"s40,s40,32768\n"
BL"v225,s[8:11],s44 offen offset:0 sc1 lds\n"
MF"v[28:31],a[116:119],a[52:55],v[28:31]\n"
WT"lgkmcnt(4)\n"
RD"a[88:91],v226 offset:22528\n"
EX"v9,v9\n"
MF"v[20:23],a[120:123],a[40:43],v[20:23]\n"
"s_cmp_eq_u32 s40,s46\n"
RD"a[92:95],v226 offset:23552\n"
RD"a[96:99],v226 offset:24576\n"
EX"v10,v10\n"
MF"v[28:31],a[120:123],a[56:59],v[28:31]\n"
"s_cselect_b32 s40,s47,s40\n"
RD"a[100:103],v226 offset:25600\n"
RD"a[104:107],v226 offset:26624\n"
PK"v8,v8,v9\n"
MF"v[20:23],a[124:127],a[44:47],v[20:23]\n"
EX"v11,v11\n"
MF"v[28:31],a[124:127],a[60:63],v[28:31]\n"
RD"a[108:111],v226 offset:27648\n"
RD"a[112:115],v226 offset:28672\n"
MF"v[40:43],a[64:67],a[48:51],v[208:211]\n"
BL"v225,s[8:11],s44 offen offset:1024 sc1 lds\n"
EX"v4,v4\n"
MF"v[40:43],a[68:71],a[52:55],v[40:43]\n"
WT"lgkmcnt(7)\n"
RD"a[116:119],v226 offset:29696\n"
RD"a[120:123],v226 offset:30720\n"
PK"v9,v10,v11\n"
MF"v[40:43],a[72:75],a[56:59],v[40:43]\n"
EX"v5,v5\n"
MF"v[40:43],a[76:79],a[60:63],v[40:43]\n"
RD"a[124:127],v226 offset:31744\n"
PK"v2,v4,v5\n"
MF"v[44:47],a[80:83],a[48:51],v[212:215]\n"
EX"v6,v6\n"
MF"v[44:47],a[84:87],a[52:55],v[44:47]\n"
WT"lgkmcnt(5)\n"
BL"v225,s[8:11],s44 offen offset:2048 sc1 lds\n"
MF"v[44:47],a[88:91],a[56:59],v[44:47]\n"
EX"v7,v7\n"
MF"v[44:47],a[92:95],a[60:63],v[44:47]\n"
"s_barrier\n"
MF"v[56:59],a[96:99],a[48:51],v[216:219]\n"
RD"a[64:67],v227 offset:0\n"
"v_readlane_b32 s43,v228,s42\n"
EX"v12,v12\n"
MF"v[56:59],a[100:103],a[52:55],v[56:59]\n"
EX"v13,v13\n"
PK"v3,v6,v7\n"
MF"v[56:59],a[104:107],a[56:59],v[56:59]\n"
WT"lgkmcnt(1)\n"
BL"v225,s[8:11],s44 offen offset:3072 sc1 lds\n"
MF"v[56:59],a[108:111],a[60:63],v[56:59]\n"
"s_mov_b32 m0,s40\n"
RD"a[68:71],v227 offset:1024\n"
PK"v10,v12,v13\n"
MF"v[60:63],a[112:115],a[48:51],v[220:223]\n"
EX"v14,v14\n"
MF"v[60:63],a[116:119],a[52:55],v[60:63]\n"
RD"a[72:75],v227 offset:2048\n"
RD"a[76:79],v227 offset:3072\n"
MF"v[60:63],a[120:123],a[56:59],v[60:63]\n"
EX"v15,v15\n"
MF"v[60:63],a[124:127],a[60:63],v[60:63]\n"
WT"lgkmcnt(3)\n"
RD"a[80:83],v227 offset:4096\n"
"v_readlane_b32 s44,v229,s42\n"
MF"v[144:147],a[64:67],v[64:67],v[144:147]\n"
SA"s41,s41,32768\n"
EX"v16,v16\n"
PK"v11,v14,v15\n"
MF"v[176:179],a[64:67],v[68:71],v[176:179]\n"
WT"lgkmcnt(3)\n"
BL"v225,s[8:11],s43 offen offset:0 sc1 lds\n"
MF"v[148:151],a[68:71],v[64:67],v[148:151]\n"
"s_cmp_eq_u32 s41,163840\n"
EX"v17,v17\n"
MF"v[180:183],a[68:71],v[68:71],v[180:183]\n"
WT"lgkmcnt(2)\n"
RD"a[84:87],v227 offset:5120\n"
RD"a[88:91],v227 offset:6144\n"
MF"v[152:155],a[72:75],v[64:67],v[152:155]\n"
"s_cselect_b32 s41,0,s41\n"
EX"v18,v18\n"
"v_add_u32 v226,s41,v224\n"
MF"v[184:187],a[72:75],v[68:71],v[184:187]\n"
WT"lgkmcnt(3)\n"
RD"a[92:95],v227 offset:7168\n"
RD"a[96:99],v227 offset:8192\n"
PK"v16,v16,v17\n"
MF"v[156:159],a[76:79],v[64:67],v[156:159]\n"
SA"s42,s42,1\n"
EX"v19,v19\n"
MF"v[188:191],a[76:79],v[68:71],v[188:191]\n"
WT"lgkmcnt(4)\n"
BL"v225,s[8:11],s43 offen offset:1024 sc1 lds\n"
MF"v[160:163],a[80:83],v[64:67],v[160:163]\n"
EX"v24,v24\n"
PK"v17,v18,v19\n"
MF"v[192:195],a[80:83],v[68:71],v[192:195]\n"
WT"lgkmcnt(3)\n"
EX"v25,v25\n"
MF"v[164:167],a[84:87],v[64:67],v[164:167]\n"
EX"v26,v26\n"
MF"v[196:199],a[84:87],v[68:71],v[196:199]\n"
WT"lgkmcnt(2)\n"
RD"a[100:103],v227 offset:9216\n"
RD"a[104:107],v227 offset:10240\n"
PK"v24,v24,v25\n"
MF"v[168:171],a[88:91],v[64:67],v[168:171]\n"
BL"v225,s[8:11],s43 offen offset:2048 sc1 lds\n"
MF"v[200:203],a[88:91],v[68:71],v[200:203]\n"
WT"lgkmcnt(2)\n"
EX"v27,v27\n"
MF"v[172:175],a[92:95],v[64:67],v[172:175]\n"
RD"a[108:111],v227 offset:11264\n"
RD"a[112:115],v227 offset:12288\n"
MF"v[204:207],a[92:95],v[68:71],v[204:207]\n"
EX"v20,v20\n"
PK"v25,v26,v27\n"
MF"a[140:143],a[128:131],v[64:67],a[140:143]\n"
EX"v21,v21\n"
MF"a[144:147],a[128:131],v[68:71],a[144:147]\n"
BL"v225,s[8:11],s43 offen offset:3072 sc1 lds\n"
MF"v[144:147],a[96:99],v[72:75],v[144:147]\n"
SA"m0,s40,16384\n"
EX"v22,v22\n"
MF"v[176:179],a[96:99],v[76:79],v[176:179]\n"
WT"lgkmcnt(2)\n"
RD"a[116:119],v227 offset:13312\n"
RD"a[120:123],v227 offset:14336\n"
PK"v18,v20,v21\n"
MF"v[148:151],a[100:103],v[72:75],v[148:151]\n"
EX"v23,v23\n"
MF"v[180:183],a[100:103],v[76:79],v[180:183]\n"
RD"a[124:127],v227 offset:15360\n"
PK"v19,v22,v23\n"
MF"v[152:155],a[104:107],v[72:75],v[152:155]\n"
SA"s40,s40,32768\n"
EX"v28,v28\n"
MF"v[184:187],a[104:107],v[76:79],v[184:187]\n"
WT"lgkmcnt(3)\n"
BL"v225,s[8:11],s44 offen offset:0 sc1 lds\n"
MF"v[156:159],a[108:111],v[72:75],v[156:159]\n"
"s_cmp_eq_u32 s40,s46\n"
EX"v29,v29\n"
MF"v[188:191],a[108:111],v[76:79],v[188:191]\n"
"s_cselect_b32 s40,s47,s40\n"
EX"v30,v30\n"
MF"v[160:163],a[112:115],v[72:75],v[160:163]\n"
RD"a[64:67],v227 offset:16384\n"
PK"v26,v28,v29\n"
MF"v[192:195],a[112:115],v[76:79],v[192:195]\n"
WT"lgkmcnt(2)\n"
BL"v225,s[8:11],s44 offen offset:1024 sc1 lds\n"
MF"v[164:167],a[116:119],v[72:75],v[164:167]\n"
EX"v31,v31\n"
MF"v[196:199],a[116:119],v[76:79],v[196:199]\n"
RD"a[68:71],v227 offset:17408\n"
RD"a[72:75],v227 offset:18432\n"
MF"v[168:171],a[120:123],v[72:75],v[168:171]\n"
EX"v40,v40\n"
MF"v[200:203],a[120:123],v[76:79],v[200:203]\n"
WT"lgkmcnt(3)\n"
RD"a[76:79],v227 offset:19456\n"
PK"v27,v30,v31\n"
MF"v[172:175],a[124:127],v[72:75],v[172:175]\n"
EX"v41,v41\n"
MF"v[204:207],a[124:127],v[76:79],v[204:207]\n"
BL"v225,s[8:11],s44 offen offset:2048 sc1 lds\n"
MF"a[140:143],a[128:131],v[72:75],a[140:143]\n"
EX"v42,v42\n"
MF"a[144:147],a[128:131],v[76:79],a[144:147]\n"
WT"lgkmcnt(3)\n"
RD"a[80:83],v227 offset:20480\n"
RD"a[84:87],v227 offset:21504\n"
PK"v68,v40,v41\n"
MF"v[144:147],a[64:67],v[0:3],v[144:147]\n"
EX"v43,v43\n"
MF"v[176:179],a[64:67],v[8:11],v[176:179]\n"
WT"lgkmcnt(3)\n"
RD"a[88:91],v227 offset:22528\n"
RD"a[92:95],v227 offset:23552\n"
MF"v[148:151],a[68:71],v[0:3],v[148:151]\n"
EX"v44,v44\n"
"v_readlane_b32 s43,v228,s42\n"
MF"v[180:183],a[68:71],v[8:11],v[180:183]\n"
BL"v225,s[8:11],s44 offen offset:3072 sc1 lds\n"
PK"v69,v42,v43\n"
MF"v[152:155],a[72:75],v[0:3],v[152:155]\n"
"s_mov_b32 m0,s40\n"
EX"v45,v45\n"
MF"v[184:187],a[72:75],v[8:11],v[184:187]\n"
WT"lgkmcnt(4)\n"
EX"v46,v46\n"
MF"v[156:159],a[76:79],v[0:3],v[156:159]\n"
RD"a[96:99],v227 offset:24576\n"
"v_readlane_b32 s44,v229,s42\n"
PK"v70,v44,v45\n"
MF"v[188:191],a[76:79],v[8:11],v[188:191]\n"
WT"lgkmcnt(3)\n"
EX"v47,v47\n"
MF"v[160:163],a[80:83],v[0:3],v[160:163]\n"
SA"s42,s42,1\n"
RD"a[100:103],v227 offset:25600\n"
PK"v71,v46,v47\n"
MF"v[192:195],a[80:83],v[8:11],v[192:195]\n"
EX"v56,v56\n"
MF"v[164:167],a[84:87],v[0:3],v[164:167]\n"
RD"a[104:107],v227 offset:26624\n"
MF"v[196:199],a[84:87],v[8:11],v[196:199]\n"
WT"lgkmcnt(3)\n"
EX"v57,v57\n"
MF"v[168:171],a[88:91],v[0:3],v[168:171]\n"
EX"v58,v58\n"
MF"v[200:203],a[88:91],v[8:11],v[200:203]\n"
RD"a[108:111],v227 offset:27648\n"
PK"v76,v56,v57\n"
MF"v[172:175],a[92:95],v[0:3],v[172:175]\n"
EX"v59,v59\n"
MF"v[204:207],a[92:95],v[8:11],v[204:207]\n"
RD"a[112:115],v227 offset:28672\n"
RD"a[116:119],v227 offset:29696\n"
MF"a[140:143],a[128:131],v[0:3],a[140:143]\n"
EX"v60,v60\n"
MF"a[144:147],a[128:131],v[8:11],a[144:147]\n"
WT"lgkmcnt(3)\n"
RD"a[120:123],v227 offset:30720\n"
PK"v77,v58,v59\n"
MF"v[144:147],a[96:99],v[16:19],v[144:147]\n"
EX"v61,v61\n"
MF"v[176:179],a[96:99],v[24:27],v[176:179]\n"
PK"v78,v60,v61\n"
MF"v[148:151],a[100:103],v[16:19],v[148:151]\n"
EX"v62,v62\n"
MF"v[180:183],a[100:103],v[24:27],v[180:183]\n"
RD"a[124:127],v227 offset:31744\n"
MF"v[152:155],a[104:107],v[16:19],v[152:155]\n"
SA"s41,s41,32768\n"
WT"vmcnt(16)\n"
"s_barrier\n"
MF"v[184:187],a[104:107],v[24:27],v[184:187]\n"
WT"lgkmcnt(2)\n"
BL"v225,s[8:11],s43 offen offset:0 sc1 lds\n"
BL"v225,s[8:11],s43 offen offset:1024 sc1 lds\n"
MF"v[156:159],a[108:111],v[16:19],v[156:159]\n"
"s_cmp_eq_u32 s41,163840\n"
BL"v225,s[8:11],s43 offen offset:2048 sc1 lds\n"
EX"v63,v63\n"
MF"v[188:191],a[108:111],v[24:27],v[188:191]\n"
"s_cselect_b32 s41,0,s41\n"
RD"a[64:67],v226 offset:0\n"
RD"a[68:71],v226 offset:1024\n"
BL"v225,s[8:11],s43 offen offset:3072 sc1 lds\n"
MF"v[160:163],a[112:115],v[16:19],v[160:163]\n"
SA"m0,s40,16384\n"
RD"a[72:75],v226 offset:2048\n"
"v_add_u32 v227,s41,v224\n"
PK"v79,v62,v63\n"
MF"v[192:195],a[112:115],v[24:27],v[192:195]\n"
RD"a[76:79],v226 offset:3072\n"
MF"v[164:167],a[116:119],v[16:19],v[164:167]\n"
RD"a[80:83],v226 offset:4096\n"
MF"v[196:199],a[116:119],v[24:27],v[196:199]\n"
WT"lgkmcnt(5)\n"
RD"a[84:87],v226 offset:5120\n"
MF"v[168:171],a[120:123],v[16:19],v[168:171]\n"
SA"s40,s40,32768\n"
BL"v225,s[8:11],s44 offen offset:0 sc1 lds\n"
MF"v[200:203],a[120:123],v[24:27],v[200:203]\n"
"s_cmp_eq_u32 s40,s46\n"
RD"a[88:91],v226 offset:6144\n"
"s_cselect_b32 s40,s47,s40\n"
MF"v[172:175],a[124:127],v[16:19],v[172:175]\n"
RD"a[92:95],v226 offset:7168\n"
MF"v[204:207],a[124:127],v[24:27],v[204:207]\n"
RD"a[96:99],v226 offset:8192\n"
MF"a[140:143],a[128:131],v[16:19],a[140:143]\n"
RD"a[100:103],v226 offset:9216\n"
MF"a[144:147],a[128:131],v[24:27],a[144:147]\n"
WT"lgkmcnt(5)\n"
RD"a[104:107],v226 offset:10240\n"
MF"v[176:179],a[64:67],v[68:71],v[176:179]\n"
BL"v225,s[8:11],s44 offen offset:1024 sc1 lds\n"
RD"a[108:111],v226 offset:11264\n"
MF"v[180:183],a[68:71],v[68:71],v[180:183]\n"
RD"a[112:115],v226 offset:12288\n"
MF"v[184:187],a[72:75],v[68:71],v[184:187]\n"
RD"a[116:119],v226 offset:13312\n"
MF"v[188:191],a[76:79],v[68:71],v[188:191]\n"
RD"a[120:123],v226 offset:14336\n"
MF"v[192:195],a[80:83],v[68:71],v[192:195]\n"
WT"lgkmcnt(6)\n"
RD"a[124:127],v226 offset:15360\n"
MF"v[196:199],a[84:87],v[68:71],v[196:199]\n"
BL"v225,s[8:11],s44 offen offset:2048 sc1 lds\n"
MF"v[200:203],a[88:91],v[68:71],v[200:203]\n"
WT"vmcnt(15)\n"
"s_barrier\n"
MF"v[204:207],a[92:95],v[68:71],v[204:207]\n"
RD"a[64:67],v227 offset:0\n"
MF"a[144:147],a[128:131],v[68:71],a[144:147]\n"
MF"v[176:179],a[96:99],v[76:79],v[176:179]\n"
WT"lgkmcnt(3)\n"
BL"v225,s[8:11],s44 offen offset:3072 sc1 lds\n"
MF"v[180:183],a[100:103],v[76:79],v[180:183]\n"
RD"a[68:71],v227 offset:1024\n"
MF"v[184:187],a[104:107],v[76:79],v[184:187]\n"
RD"a[72:75],v227 offset:2048\n"
MF"v[188:191],a[108:111],v[76:79],v[188:191]\n"
RD"a[76:79],v227 offset:3072\n"
MF"v[192:195],a[112:115],v[76:79],v[192:195]\n"
RD"a[80:83],v227 offset:4096\n"
MF"v[196:199],a[116:119],v[76:79],v[196:199]\n"
WT"lgkmcnt(4)\n"
MF"v[200:203],a[120:123],v[76:79],v[200:203]\n"
MF"v[204:207],a[124:127],v[76:79],v[204:207]\n"
MF"a[144:147],a[128:131],v[76:79],a[144:147]\n"
RD"a[84:87],v227 offset:5120\n"
"LTL_%=:\n"
"s_cmp_ge_u32 s34,6\n"
"s_cbranch_scc1 LTH_%=\n"
RD"a[88:91],v227 offset:6144\n"
MF"v[0:3],a[64:67],v[80:83],0\n"
"v_accvgpr_read_b32 v251,a140\n"
"v_subrev_u32 v253,0x2b800000,v251\n"
MF"v[40:43],a[64:67],v[112:115],0\n"
WT"lgkmcnt(3)\n"
RD"a[92:95],v227 offset:7168\n"
MF"v[4:7],a[68:71],v[80:83],0\n"
"v_cmp_le_u32 vcc,0x34000000,v253\n"
"s_cmp_lg_u64 vcc,0\n"
"s_cbranch_scc1 L1041_%=\n"
"L1040_%=:\n"
MF"v[44:47],a[68:71],v[112:115],0\n"
"v_rcp_f32 v251,v251\n"
MF"v[8:11],a[72:75],v[80:83],0\n"
SA"s41,s41,32768\n"
RD"a[96:99],v227 offset:8192\n"
MU"v144,v251,v144\n"
MF"v[48:51],a[72:75],v[112:115],0\n"
"s_cmp_eq_u32 s41,163840\n"
RD"a[100:103],v227 offset:9216\n"
MU"v145,v251,v145\n"
MF"v[12:15],a[76:79],v[80:83],0\n"
"s_cselect_b32 s41,0,s41\n"
MU"v146,v251,v146\n"
"v_add_u32 v226,s41,v224\n"
MF"v[52:55],a[76:79],v[112:115],0\n"
WT"lgkmcnt(3)\n"
RD"a[104:107],v227 offset:10240\n"
MU"v147,v251,v147\n"
MF"v[16:19],a[80:83],v[80:83],0\n"
SA"s41,s41,32768\n"
MU"v148,v251,v148\n"
MU"v149,v251,v149\n"
MF"v[56:59],a[80:83],v[112:115],0\n"
"s_cmp_eq_u32 s41,163840\n"
RD"a[108:111],v227 offset:11264\n"
MU"v150,v251,v150\n"
MU"v151,v251,v151\n"
MF"v[20:23],a[84:87],v[80:83],0\n"
"s_cselect_b32 s41,0,s41\n"
PK"v144,v144,v145\n"
"v_add_u32 v235,s41,v224\n"
PK"v145,v146,v147\n"
MF"v[60:63],a[84:87],v[112:115],0\n"
RD"a[112:115],v227 offset:12288\n"
PK"v146,v148,v149\n"
MF"v[24:27],a[88:91],v[80:83],0\n"
SA"s41,s41,32768\n"
PK"v147,v150,v151\n"
MU"v152,v251,v152\n"
MF"v[64:67],a[88:91],v[112:115],0\n"
WT"lgkmcnt(3)\n"
BS"v[144:147],v224,s[8:11],s60 offen offset:0 sc1\n"
MU"v153,v251,v153\n"
MF"v[28:31],a[92:95],v[80:83],0\n"
"s_cmp_eq_u32 s41,163840\n"
RD"a[116:119],v227 offset:13312\n"
MU"v154,v251,v154\n"
MU"v155,v251,v155\n"
MF"v[68:71],a[92:95],v[112:115],0\n"
"s_cselect_b32 s41,0,s41\n"
RD"a[120:123],v227 offset:14336\n"
"v_add_u32 v236,s41,v224\n"
MU"v156,v251,v156\n"
MF"v[32:35],a[96:99],v[80:83],0\n"
SA"s41,s41,32768\n"
MU"v157,v251,v157\n"
MU"v158,v251,v158\n"
MU"v159,v251,v159\n"
MF"v[72:75],a[96:99],v[112:115],0\n"
"s_cmp_eq_u32 s41,163840\n"
RD"a[124:127],v227 offset:15360\n"
PK"v152,v152,v153\n"
PK"v153,v154,v155\n"
MF"v[36:39],a[100:103],v[80:83],0\n"
"s_cselect_b32 s41,0,s41\n"
PK"v154,v156,v157\n"
"v_add_u32 v237,s41,v224\n"
PK"v155,v158,v159\n"
MF"v[76:79],a[100:103],v[112:115],0\n"
WT"lgkmcnt(3)\n"
BS"v[152:155],v224,s[8:11],s60 offen offset:1024 sc1\n"
MU"v160,v251,v160\n"
MF"v[0:3],a[104:107],v[88:91],v[0:3]\n"
RD"a[64:67],v227 offset:16384\n"
MU"v161,v251,v161\n"
MU"v162,v251,v162\n"
MF"v[40:43],a[104:107],v[120:123],v[40:43]\n"
RD"a[68:71],v227 offset:17408\n"
MU"v163,v251,v163\n"
MU"v164,v251,v164\n"
MF"v[4:7],a[108:111],v[88:91],v[4:7]\n"
MU"v165,v251,v165\n"
MU"v166,v251,v166\n"
MF"v[44:47],a[108:111],v[120:123],v[44:47]\n"
RD"a[72:75],v227 offset:18432\n"
MU"v167,v251,v167\n"
PK"v160,v160,v161\n"
MF"v[8:11],a[112:115],v[88:91],v[8:11]\n"
SA"s51,s60,16384\n"
"v_accvgpr_read_b32 v252,a144\n"
"v_subrev_u32 v253,0x2b800000,v252\n"
PK"v161,v162,v163\n"
MF"v[48:51],a[112:115],v[120:123],v[48:51]\n"
WT"lgkmcnt(3)\n"
RD"a[76:79],v227 offset:19456\n"
PK"v162,v164,v165\n"
PK"v163,v166,v167\n"
MF"v[12:15],a[116:119],v[88:91],v[12:15]\n"
"s_lshl_b32 s62,s34,14\n"
BS"v[160:163],v224,s[8:11],s60 offen offset:2048 sc1\n"
MU"v168,v251,v168\n"
MF"v[52:55],a[116:119],v[120:123],v[52:55]\n"
"s_sub_u32 s62,s61,s62\n"
"v_cmp_le_u32 vcc,0x34000000,v253\n"
"s_cmp_lg_u64 vcc,0\n"
"s_cbranch_scc1 L1047_%=\n"
"L1046_%=:\n"
MF"v[16:19],a[120:123],v[88:91],v[16:19]\n"
SA"s62,s62,122880\n"
"v_rcp_f32 v252,v252\n"
MU"v169,v251,v169\n"
MF"v[56:59],a[120:123],v[120:123],v[56:59]\n"
RD"a[80:83],v227 offset:20480\n"
RD"a[84:87],v227 offset:21504\n"
MU"v170,v251,v170\n"
MF"v[20:23],a[124:127],v[88:91],v[20:23]\n"
MU"v171,v251,v171\n"
MU"v172,v251,v172\n"
MU"v173,v251,v173\n"
MF"v[60:63],a[124:127],v[120:123],v[60:63]\n"
WT"lgkmcnt(3)\n"
RD"a[88:91],v227 offset:22528\n"
MU"v174,v251,v174\n"
MU"v175,v251,v175\n"
MF"v[24:27],a[64:67],v[88:91],v[24:27]\n"
PK"v168,v168,v169\n"
PK"v169,v170,v171\n"
MU"v176,v252,v176\n"
MF"v[64:67],a[64:67],v[120:123],v[64:67]\n"
RD"a[92:95],v227 offset:23552\n"
MU"v177,v252,v177\n"
PK"v170,v172,v173\n"
MF"v[28:31],a[68:71],v[88:91],v[28:31]\n"
PK"v171,v174,v175\n"
MU"v178,v252,v178\n"
MU"v179,v252,v179\n"
MF"v[68:71],a[68:71],v[120:123],v[68:71]\n"
BS"v[168:171],v224,s[8:11],s60 offen offset:3072 sc1\n"
MU"v180,v252,v180\n"
MF"v[32:35],a[72:75],v[88:91],v[32:35]\n"
RD"a[96:99],v227 offset:24576\n"
MU"v181,v252,v181\n"
MU"v182,v252,v182\n"
MF"v[72:75],a[72:75],v[120:123],v[72:75]\n"
WT"lgkmcnt(3)\n"
RD"a[100:103],v227 offset:25600\n"
MU"v183,v252,v183\n"
PK"v176,v176,v177\n"
MF"v[36:39],a[76:79],v[88:91],v[36:39]\n"
PK"v177,v178,v179\n"
PK"v178,v180,v181\n"
PK"v179,v182,v183\n"
MF"v[76:79],a[76:79],v[120:123],v[76:79]\n"
BS"v[176:179],v224,s[8:11],s51 offen offset:0 sc1\n"
MU"v184,v252,v184\n"
MF"v[0:3],a[80:83],v[96:99],v[0:3]\n"
RD"a[104:107],v227 offset:26624\n"
MU"v185,v252,v185\n"
MU"v186,v252,v186\n"
MF"v[40:43],a[80:83],v[128:131],v[40:43]\n"
RD"a[108:111],v227 offset:27648\n"
MU"v187,v252,v187\n"
MU"v188,v252,v188\n"
MF"v[4:7],a[84:87],v[96:99],v[4:7]\n"
MU"v189,v252,v189\n"
MU"v190,v252,v190\n"
MU"v191,v252,v191\n"
MF"v[44:47],a[84:87],v[128:131],v[44:47]\n"
WT"lgkmcnt(3)\n"
RD"a[112:115],v227 offset:28672\n"
PK"v184,v184,v185\n"
PK"v185,v186,v187\n"
MF"v[8:11],a[88:91],v[96:99],v[8:11]\n"
PK"v186,v188,v189\n"
PK"v187,v190,v191\n"
MU"v192,v252,v192\n"
MF"v[48:51],a[88:91],v[128:131],v[48:51]\n"
BS"v[184:187],v224,s[8:11],s51 offen offset:1024 sc1\n"
MU"v193,v252,v193\n"
MF"v[12:15],a[92:95],v[96:99],v[12:15]\n"
RD"a[116:119],v227 offset:29696\n"
MU"v194,v252,v194\n"
MU"v195,v252,v195\n"
MF"v[52:55],a[92:95],v[128:131],v[52:55]\n"
RD"a[120:123],v227 offset:30720\n"
MU"v196,v252,v196\n"
MU"v197,v252,v197\n"
MF"v[16:19],a[96:99],v[96:99],v[16:19]\n"
MU"v198,v252,v198\n"
MU"v199,v252,v199\n"
PK"v192,v192,v193\n"
MF"v[56:59],a[96:99],v[128:131],v[56:59]\n"
WT"lgkmcnt(3)\n"
RD"a[124:127],v227 offset:31744\n"
PK"v193,v194,v195\n"
PK"v194,v196,v197\n"
MF"v[20:23],a[100:103],v[96:99],v[20:23]\n"
WT"vmcnt(14)\n"
"s_barrier\n"
MF"v[60:63],a[100:103],v[128:131],v[60:63]\n"
RD"a[64:67],v226 offset:0\n"
PK"v195,v198,v199\n"
MU"v200,v252,v200\n"
MF"v[24:27],a[104:107],v[96:99],v[24:27]\n"
BS"v[192:195],v224,s[8:11],s51 offen offset:2048 sc1\n"
MU"v201,v252,v201\n"
MF"v[64:67],a[104:107],v[128:131],v[64:67]\n"
RD"a[68:71],v226 offset:1024\n"
MU"v202,v252,v202\n"
MU"v203,v252,v203\n"
MF"v[28:31],a[108:111],v[96:99],v[28:31]\n"
MU"v204,v252,v204\n"
MU"v205,v252,v205\n"
MU"v206,v252,v206\n"
MF"v[68:71],a[108:111],v[128:131],v[68:71]\n"
WT"lgkmcnt(3)\n"
RD"a[72:75],v226 offset:2048\n"
MU"v207,v252,v207\n"
PK"v200,v200,v201\n"
MF"v[32:35],a[112:115],v[96:99],v[32:35]\n"
PK"v201,v202,v203\n"
PK"v202,v204,v205\n"
PK"v203,v206,v207\n"
MF"v[72:75],a[112:115],v[128:131],v[72:75]\n"
BS"v[200:203],v224,s[8:11],s51 offen offset:3072 sc1\n"
MF"v[36:39],a[116:119],v[96:99],v[36:39]\n"
RD"a[76:79],v226 offset:3072\n"
"v_readlane_b32 s43,v228,s42\n"
MF"v[76:79],a[116:119],v[128:131],v[76:79]\n"
RD"a[80:83],v226 offset:4096\n"
MF"v[0:3],a[120:123],v[104:107],v[0:3]\n"
MF"v[40:43],a[120:123],v[136:139],v[40:43]\n"
WT"lgkmcnt(3)\n"
RD"a[84:87],v226 offset:5120\n"
MF"v[4:7],a[124:127],v[104:107],v[4:7]\n"
"s_mov_b32 m0,s40\n"
MF"v[44:47],a[124:127],v[136:139],v[44:47]\n"
RD"a[88:91],v226 offset:6144\n"
MF"v[8:11],a[64:67],v[104:107],v[8:11]\n"
"v_readlane_b32 s44,v229,s42\n"
MF"v[48:51],a[64:67],v[136:139],v[48:51]\n"
RD"a[92:95],v226 offset:7168\n"
MF"v[12:15],a[68:71],v[104:107],v[12:15]\n"
SA"s42,s42,1\n"
BL"v225,s[8:11],s43 offen offset:0 sc1 lds\n"
MF"v[52:55],a[68:71],v[136:139],v[52:55]\n"
WT"lgkmcnt(3)\n"
RD"a[96:99],v227 offset:0\n"
MF"v[16:19],a[72:75],v[104:107],v[16:19]\n"
MF"v[56:59],a[72:75],v[136:139],v[56:59]\n"
RD"a[100:103],v227 offset:1024\n"
MF"v[20:23],a[76:79],v[104:107],v[20:23]\n"
MF"v[60:63],a[76:79],v[136:139],v[60:63]\n"
RD"a[104:107],v227 offset:2048\n"
MF"v[24:27],a[80:83],v[104:107],v[24:27]\n"
MF"v[64:67],a[80:83],v[136:139],v[64:67]\n"
WT"lgkmcnt(3)\n"
RD"a[108:111],v227 offset:3072\n"
MF"v[28:31],a[84:87],v[104:107],v[28:31]\n"
BL"v225,s[8:11],s43 offen offset:1024 sc1 lds\n"
MF"v[68:71],a[84:87],v[136:139],v[68:71]\n"
RD"a[112:115],v227 offset:4096\n"
MF"v[32:35],a[88:91],v[104:107],v[32:35]\n"
MF"v[72:75],a[88:91],v[136:139],v[72:75]\n"
RD"a[116:119],v227 offset:5120\n"
MF"v[36:39],a[92:95],v[104:107],v[36:39]\n"
MF"v[76:79],a[92:95],v[136:139],v[76:79]\n"
WT"lgkmcnt(3)\n"
RD"a[120:123],v227 offset:6144\n"
MF"v[84:87],a[96:99],v[144:147],0\n"
BL"v225,s[8:11],s43 offen offset:2048 sc1 lds\n"
MF"v[164:167],a[96:99],v[176:179],0\n"
RD"a[124:127],v227 offset:7168\n"
MF"v[92:95],a[100:103],v[144:147],0\n"
MF"v[172:175],a[100:103],v[176:179],0\n"
RD"a[64:67],v227 offset:8192\n"
MF"v[100:103],a[104:107],v[144:147],0\n"
MF"v[180:183],a[104:107],v[176:179],0\n"
WT"lgkmcnt(3)\n"
RD"a[68:71],v227 offset:9216\n"
MF"v[108:111],a[108:111],v[144:147],0\n"
MF"v[188:191],a[108:111],v[176:179],0\n"
RD"a[72:75],v227 offset:10240\n"
MF"v[116:119],a[112:115],v[144:147],0\n"
BL"v225,s[8:11],s43 offen offset:3072 sc1 lds\n"
MF"v[196:199],a[112:115],v[176:179],0\n"
RD"a[76:79],v227 offset:11264\n"
MF"v[124:127],a[116:119],v[144:147],0\n"
SA"m0,s40,16384\n"
MF"v[204:207],a[116:119],v[176:179],0\n"
WT"lgkmcnt(3)\n"
RD"a[80:83],v227 offset:12288\n"
MF"v[132:135],a[120:123],v[144:147],0\n"
MF"v[208:211],a[120:123],v[176:179],0\n"
RD"a[84:87],v227 offset:13312\n"
MF"v[140:143],a[124:127],v[144:147],0\n"
BL"v225,s[8:11],s44 offen offset:0 sc1 lds\n"
MF"v[212:215],a[124:127],v[176:179],0\n"
SA"s40,s40,32768\n"
RD"a[88:91],v227 offset:14336\n"
MF"v[148:151],a[64:67],v[144:147],0\n"
"s_cmp_eq_u32 s40,s46\n"
"s_cselect_b32 s40,s47,s40\n"
MF"v[216:219],a[64:67],v[176:179],0\n"
WT"lgkmcnt(3)\n"
RD"a[92:95],v227 offset:15360\n"
MF"v[156:159],a[68:71],v[144:147],0\n"
MF"v[220:223],a[68:71],v[176:179],0\n"
RD"a[96:99],v227 offset:16384\n"
MF"v[84:87],a[72:75],v[152:155],v[84:87]\n"
MF"v[164:167],a[72:75],v[184:187],v[164:167]\n"
RD"a[100:103],v227 offset:17408\n"
MF"v[92:95],a[76:79],v[152:155],v[92:95]\n"
BL"v225,s[8:11],s44 offen offset:1024 sc1 lds\n"
MF"v[172:175],a[76:79],v[184:187],v[172:175]\n"
WT"lgkmcnt(3)\n"
RD"a[104:107],v227 offset:18432\n"
MF"v[100:103],a[80:83],v[152:155],v[100:103]\n"
MF"v[180:183],a[80:83],v[184:187],v[180:183]\n"
RD"a[108:111],v227 offset:19456\n"
MF"v[108:111],a[84:87],v[152:155],v[108:111]\n"
MF"v[188:191],a[84:87],v[184:187],v[188:191]\n"
RD"a[112:115],v227 offset:20480\n"
MF"v[116:119],a[88:91],v[152:155],v[116:119]\n"
BL"v225,s[8:11],s44 offen offset:2048 sc1 lds\n"
MF"v[196:199],a[88:91],v[184:187],v[196:199]\n"
WT"lgkmcnt(3)\n"
RD"a[116:119],v227 offset:21504\n"
MF"v[124:127],a[92:95],v[152:155],v[124:127]\n"
WT"vmcnt(7)\n"
"buffer_store_dwordx2 v[232:233],v230,s[8:11],s62 offen sc1\n"
MF"v[204:207],a[92:95],v[184:187],v[204:207]\n"
RD"a[120:123],v227 offset:22528\n"
MF"v[132:135],a[96:99],v[152:155],v[132:135]\n"
MF"v[208:211],a[96:99],v[184:187],v[208:211]\n"
RD"a[124:127],v227 offset:23552\n"
MF"v[140:143],a[100:103],v[152:155],v[140:143]\n"
"v_readlane_b32 s43,v228,s42\n"
MF"v[212:215],a[100:103],v[184:187],v[212:215]\n"
WT"lgkmcnt(3)\n"
RD"a[64:67],v227 offset:24576\n"
MF"v[148:151],a[104:107],v[152:155],v[148:151]\n"
BL"v225,s[8:11],s44 offen offset:3072 sc1 lds\n"
MF"v[216:219],a[104:107],v[184:187],v[216:219]\n"
RD"a[68:71],v227 offset:25600\n"
MF"v[156:159],a[108:111],v[152:155],v[156:159]\n"
"s_mov_b32 m0,s40\n"
MF"v[220:223],a[108:111],v[184:187],v[220:223]\n"
RD"a[72:75],v227 offset:26624\n"
MF"v[84:87],a[112:115],v[160:163],v[84:87]\n"
"s_lshr_b32 s89,s31,3\n"
"v_readlane_b32 s44,v229,s42\n"
MF"v[164:167],a[112:115],v[192:195],v[164:167]\n"
WT"lgkmcnt(3)\n"
RD"a[76:79],v227 offset:27648\n"
"s_lshl_b32 s89,s89,4\n"
MF"v[92:95],a[116:119],v[160:163],v[92:95]\n"
SA"s42,s42,1\n"
BL"v225,s[8:11],s43 offen offset:0 sc1 lds\n"
MF"v[172:175],a[116:119],v[192:195],v[172:175]\n"
SA"s89,s89,s30\n"
RD"a[80:83],v227 offset:28672\n"
"s_lshl_b32 s89,s89,3\n"
MF"v[100:103],a[120:123],v[160:163],v[100:103]\n"
"s_xor_b32 s23,s32,s30\n"
MF"v[180:183],a[120:123],v[192:195],v[180:183]\n"
SA"s89,s89,s23\n"
RD"a[84:87],v227 offset:29696\n"
MF"v[108:111],a[124:127],v[160:163],v[108:111]\n"
"s_xor_b32 s90,s89,36\n"
MF"v[188:191],a[124:127],v[192:195],v[188:191]\n"
WT"lgkmcnt(3)\n"
RD"a[88:91],v227 offset:30720\n"
"s_mul_i32 s88,s89,5120\n"
MF"v[116:119],a[64:67],v[160:163],v[116:119]\n"
"s_mul_i32 s22,s90,5120\n"
MF"v[196:199],a[64:67],v[192:195],v[196:199]\n"
"s_sub_u32 s90,s90,s89\n"
RD"a[92:95],v227 offset:31744\n"
MF"v[124:127],a[68:71],v[160:163],v[124:127]\n"
"s_lshl_b32 s90,s90,6\n"
BL"v225,s[8:11],s43 offen offset:1024 sc1 lds\n"
MF"v[204:207],a[68:71],v[192:195],v[204:207]\n"
SA"s88,s88,59506688\n"
RD"a[96:99],v226 offset:0\n"
MF"v[132:135],a[72:75],v[160:163],v[132:135]\n"
SA"s22,s22,59506688\n"
MF"v[208:211],a[72:75],v[192:195],v[208:211]\n"
WT"lgkmcnt(3)\n"
RD"a[100:103],v226 offset:1024\n"
"s_cmp_eq_u32 s23,0\n"
MF"v[140:143],a[76:79],v[160:163],v[140:143]\n"
"s_cselect_b32 s88,2130706432,s88\n"
"s_cmp_eq_u32 s23,4\n"
"s_cselect_b32 s22,2130706432,s22\n"
MF"v[212:215],a[76:79],v[192:195],v[212:215]\n"
"s_lshl_b32 s89,s89,6\n"
RD"a[104:107],v226 offset:2048\n"
MF"v[148:151],a[80:83],v[160:163],v[148:151]\n"
SA"s89,s89,80478208\n"
BL"v225,s[8:11],s43 offen offset:2048 sc1 lds\n"
MF"v[216:219],a[80:83],v[192:195],v[216:219]\n"
RD"a[108:111],v226 offset:3072\n"
"v_bfe_u32 v239,v224,1,6\n"
MF"v[156:159],a[84:87],v[160:163],v[156:159]\n"
"v_bfe_u32 v242,v224,7,1\n"
MF"v[220:223],a[84:87],v[192:195],v[220:223]\n"
WT"lgkmcnt(3)\n"
RD"a[112:115],v226 offset:4096\n"
"v_mad_u32_u24 v239,v242,s90,v239\n"
MF"v[84:87],a[88:91],v[168:171],v[84:87]\n"
"v_lshrrev_b32 v242,8,v224\n"
MF"v[164:167],a[88:91],v[200:203],v[164:167]\n"
RD"a[116:119],v226 offset:5120\n"
"v_lshl_add_u32 v239,v242,27,v239\n"
MF"v[92:95],a[92:95],v[168:171],v[92:95]\n"
MF"v[172:175],a[92:95],v[200:203],v[172:175]\n"
RD"a[120:123],v226 offset:6144\n"
MF"v[100:103],a[96:99],v[168:171],v[100:103]\n"
BL"v225,s[8:11],s43 offen offset:3072 sc1 lds\n"
MF"v[180:183],a[96:99],v[200:203],v[180:183]\n"
WT"lgkmcnt(3)\n"
RD"a[124:127],v226 offset:7168\n"
MF"v[108:111],a[100:103],v[168:171],v[108:111]\n"
SA"m0,s40,16384\n"
MF"v[188:191],a[100:103],v[200:203],v[188:191]\n"
RD"a[64:67],v226 offset:8192\n"
MF"v[116:119],a[104:107],v[168:171],v[116:119]\n"
MF"v[196:199],a[104:107],v[200:203],v[196:199]\n"
RD"a[68:71],v226 offset:9216\n"
MF"v[124:127],a[108:111],v[168:171],v[124:127]\n"
BL"v225,s[8:11],s44 offen offset:0 sc1 lds\n"
MF"v[204:207],a[108:111],v[200:203],v[204:207]\n"
WT"lgkmcnt(3)\n"
RD"a[72:75],v226 offset:10240\n"
SA"s40,s40,32768\n"
MF"v[132:135],a[112:115],v[168:171],v[132:135]\n"
"s_cmp_eq_u32 s40,s46\n"
"s_cselect_b32 s40,s47,s40\n"
MF"v[208:211],a[112:115],v[200:203],v[208:211]\n"
RD"a[76:79],v226 offset:11264\n"
MF"v[140:143],a[116:119],v[168:171],v[140:143]\n"
MF"v[212:215],a[116:119],v[200:203],v[212:215]\n"
RD"a[80:83],v226 offset:12288\n"
MF"v[148:151],a[120:123],v[168:171],v[148:151]\n"
MF"v[216:219],a[120:123],v[200:203],v[216:219]\n"
WT"lgkmcnt(3)\n"
RD"a[84:87],v226 offset:13312\n"
MF"v[156:159],a[124:127],v[168:171],v[156:159]\n"
BL"v225,s[8:11],s44 offen offset:1024 sc1 lds\n"
MF"v[220:223],a[124:127],v[200:203],v[220:223]\n"
RD"a[88:91],v226 offset:14336\n"
MF"v[0:3],a[64:67],a[0:3],v[0:3]\n"
MF"v[40:43],a[64:67],a[4:7],v[40:43]\n"
RD"a[92:95],v226 offset:15360\n"
MF"v[4:7],a[68:71],a[0:3],v[4:7]\n"
MF"v[44:47],a[68:71],a[4:7],v[44:47]\n"
WT"lgkmcnt(3)\n"
RD"a[96:99],v226 offset:16384\n"
MF"v[8:11],a[72:75],a[0:3],v[8:11]\n"
BL"v225,s[8:11],s44 offen offset:2048 sc1 lds\n"
MF"v[48:51],a[72:75],a[4:7],v[48:51]\n"
RD"a[100:103],v226 offset:17408\n"
MF"v[12:15],a[76:79],a[0:3],v[12:15]\n"
MF"v[52:55],a[76:79],a[4:7],v[52:55]\n"
RD"a[104:107],v226 offset:18432\n"
MF"v[16:19],a[80:83],a[0:3],v[16:19]\n"
MF"v[56:59],a[80:83],a[4:7],v[56:59]\n"
WT"lgkmcnt(3)\n"
RD"a[108:111],v226 offset:19456\n"
MF"v[20:23],a[84:87],a[0:3],v[20:23]\n"
MF"v[60:63],a[84:87],a[4:7],v[60:63]\n"
RD"a[112:115],v226 offset:20480\n"
MF"v[24:27],a[88:91],a[0:3],v[24:27]\n"
BL"v225,s[8:11],s44 offen offset:3072 sc1 lds\n"
MF"v[64:67],a[88:91],a[4:7],v[64:67]\n"
RD"a[116:119],v226 offset:21504\n"
MF"v[28:31],a[92:95],a[0:3],v[28:31]\n"
MF"v[68:71],a[92:95],a[4:7],v[68:71]\n"
WT"lgkmcnt(3)\n"
RD"a[120:123],v226 offset:22528\n"
MF"v[32:35],a[96:99],a[0:3],v[32:35]\n"
MF"v[72:75],a[96:99],a[4:7],v[72:75]\n"
RD"a[124:127],v226 offset:23552\n"
MF"v[36:39],a[100:103],a[0:3],v[36:39]\n"
MF"v[76:79],a[100:103],a[4:7],v[76:79]\n"
RD"a[64:67],v226 offset:24576\n"
MF"v[0:3],a[104:107],a[8:11],v[0:3]\n"
MF"v[40:43],a[104:107],a[12:15],v[40:43]\n"
WT"lgkmcnt(3)\n"
RD"a[68:71],v226 offset:25600\n"
MF"v[4:7],a[108:111],a[8:11],v[4:7]\n"
MF"v[44:47],a[108:111],a[12:15],v[44:47]\n"
RD"a[72:75],v226 offset:26624\n"
MF"v[8:11],a[112:115],a[8:11],v[8:11]\n"
MF"v[48:51],a[112:115],a[12:15],v[48:51]\n"
RD"a[76:79],v226 offset:27648\n"
MF"v[12:15],a[116:119],a[8:11],v[12:15]\n"
MF"v[52:55],a[116:119],a[12:15],v[52:55]\n"
WT"lgkmcnt(3)\n"
RD"a[80:83],v226 offset:28672\n"
MF"v[16:19],a[120:123],a[8:11],v[16:19]\n"
MF"v[56:59],a[120:123],a[12:15],v[56:59]\n"
RD"a[84:87],v226 offset:29696\n"
MF"v[20:23],a[124:127],a[8:11],v[20:23]\n"
MF"v[60:63],a[124:127],a[12:15],v[60:63]\n"
RD"a[88:91],v226 offset:30720\n"
MF"v[24:27],a[64:67],a[8:11],v[24:27]\n"
MF"v[64:67],a[64:67],a[12:15],v[64:67]\n"
WT"lgkmcnt(3)\n"
RD"a[92:95],v226 offset:31744\n"
MF"v[28:31],a[68:71],a[8:11],v[28:31]\n"
"s_barrier\n"
MF"v[68:71],a[68:71],a[12:15],v[68:71]\n"
RD"a[96:99],v235 offset:0\n"
MF"v[32:35],a[72:75],a[8:11],v[32:35]\n"
MF"v[72:75],a[72:75],a[12:15],v[72:75]\n"
RD"a[100:103],v235 offset:1024\n"
MF"v[36:39],a[76:79],a[8:11],v[36:39]\n"
MF"v[76:79],a[76:79],a[12:15],v[76:79]\n"
WT"lgkmcnt(3)\n"
RD"a[104:107],v235 offset:2048\n"
MF"v[0:3],a[80:83],a[16:19],v[0:3]\n"
MF"v[40:43],a[80:83],a[20:23],v[40:43]\n"
RD"a[108:111],v235 offset:3072\n"
MF"v[4:7],a[84:87],a[16:19],v[4:7]\n"
MF"v[44:47],a[84:87],a[20:23],v[44:47]\n"
RD"a[112:115],v235 offset:4096\n"
MF"v[8:11],a[88:91],a[16:19],v[8:11]\n"
MF"v[48:51],a[88:91],a[20:23],v[48:51]\n"
WT"lgkmcnt(3)\n"
RD"a[116:119],v235 offset:5120\n"
MF"v[12:15],a[92:95],a[16:19],v[12:15]\n"
MF"v[52:55],a[92:95],a[20:23],v[52:55]\n"
RD"a[120:123],v235 offset:6144\n"
MF"v[16:19],a[96:99],a[16:19],v[16:19]\n"
MF"v[56:59],a[96:99],a[20:23],v[56:59]\n"
RD"a[124:127],v235 offset:7168\n"
MF"v[20:23],a[100:103],a[16:19],v[20:23]\n"
MF"v[60:63],a[100:103],a[20:23],v[60:63]\n"
WT"lgkmcnt(3)\n"
RD"a[64:67],v235 offset:8192\n"
MF"v[24:27],a[104:107],a[16:19],v[24:27]\n"
MF"v[64:67],a[104:107],a[20:23],v[64:67]\n"
RD"a[68:71],v235 offset:9216\n"
"v_bfe_u32 v238,v224,4,2\n"
MF"v[28:31],a[108:111],a[16:19],v[28:31]\n"
"s_andn2_b32 s63,s62,255\n"
"v_lshlrev_b32 v238,6,v238\n"
MF"v[68:71],a[108:111],a[20:23],v[68:71]\n"
RD"a[72:75],v235 offset:10240\n"
MF"v[32:35],a[112:115],a[16:19],v[32:35]\n"
"buffer_load_dwordx2 v[240:241],v238,s[8:11],s63 offen sc1\n"
MF"v[72:75],a[112:115],a[20:23],v[72:75]\n"
WT"lgkmcnt(3)\n"
RD"a[76:79],v235 offset:11264\n"
MF"v[36:39],a[116:119],a[16:19],v[36:39]\n"
MF"v[76:79],a[116:119],a[20:23],v[76:79]\n"
RD"a[80:83],v235 offset:12288\n"
MF"v[0:3],a[120:123],a[24:27],v[0:3]\n"
MF"v[40:43],a[120:123],a[28:31],v[40:43]\n"
RD"a[84:87],v235 offset:13312\n"
MF"v[4:7],a[124:127],a[24:27],v[4:7]\n"
MF"v[44:47],a[124:127],a[28:31],v[44:47]\n"
RD"a[88:91],v235 offset:14336\n"
"LTJ_%=:\n"
WT"lgkmcnt(6)\n"
MF"v[8:11],a[64:67],a[24:27],v[8:11]\n"
MF"v[48:51],a[64:67],a[28:31],v[48:51]\n"
WT"lgkmcnt(3)\n"
RD"a[92:95],v235 offset:15360\n"
MF"v[12:15],a[68:71],a[24:27],v[12:15]\n"
MF"v[52:55],a[68:71],a[28:31],v[52:55]\n"
RD"a[96:99],v235 offset:16384\n"
MF"v[16:19],a[72:75],a[24:27],v[16:19]\n"
MF"v[56:59],a[72:75],a[28:31],v[56:59]\n"
RD"a[100:103],v235 offset:17408\n"
MF"v[20:23],a[76:79],a[24:27],v[20:23]\n"
MF"v[60:63],a[76:79],a[28:31],v[60:63]\n"
WT"lgkmcnt(3)\n"
RD"a[104:107],v235 offset:26624\n"
MF"v[24:27],a[80:83],a[24:27],v[24:27]\n"
MF"v[64:67],a[80:83],a[28:31],v[64:67]\n"
RD"a[108:111],v235 offset:27648\n"
MF"v[28:31],a[84:87],a[24:27],v[28:31]\n"
WT"vmcnt(1)\n"
"s_barrier\n"
MF"v[68:71],a[84:87],a[28:31],v[68:71]\n"
RD"a[112:115],v236 offset:4096\n"
MF"v[32:35],a[88:91],a[24:27],v[32:35]\n"
MF"v[72:75],a[88:91],a[28:31],v[72:75]\n"
WT"lgkmcnt(3)\n"
RD"a[116:119],v236 offset:5120\n"
MF"v[36:39],a[92:95],a[24:27],v[36:39]\n"
MF"v[76:79],a[92:95],a[28:31],v[76:79]\n"
RD"a[120:123],v236 offset:14336\n"
MF"v[0:3],a[96:99],a[180:183],v[0:3]\n"
MF"v[40:43],a[96:99],a[184:187],v[40:43]\n"
RD"a[124:127],v236 offset:15360\n"
MF"v[4:7],a[100:103],a[180:183],v[4:7]\n"
MF"v[44:47],a[100:103],a[184:187],v[44:47]\n"
WT"lgkmcnt(3)\n"
RD"a[64:67],v236 offset:24576\n"
MF"v[0:3],a[104:107],a[188:191],v[0:3]\n"
MF"v[40:43],a[104:107],a[192:195],v[40:43]\n"
RD"a[68:71],v236 offset:25600\n"
MF"v[4:7],a[108:111],a[188:191],v[4:7]\n"
MF"v[44:47],a[108:111],a[192:195],v[44:47]\n"
RD"a[72:75],v237 offset:2048\n"
MF"v[0:3],a[112:115],a[196:199],v[0:3]\n"
MF"v[40:43],a[112:115],a[200:203],v[40:43]\n"
WT"lgkmcnt(3)\n"
RD"a[76:79],v237 offset:3072\n"
MF"v[4:7],a[116:119],a[196:199],v[4:7]\n"
MF"v[44:47],a[116:119],a[200:203],v[44:47]\n"
RD"a[80:83],v237 offset:12288\n"
MF"v[0:3],a[120:123],a[204:207],v[0:3]\n"
MF"v[40:43],a[120:123],a[208:211],v[40:43]\n"
SA"s48,s60,32768\n"
RD"a[84:87],v237 offset:13312\n"
MF"v[4:7],a[124:127],a[204:207],v[4:7]\n"
"s_and_b32 s48,s48,0x18000\n"
MF"v[44:47],a[124:127],a[208:211],v[44:47]\n"
WT"lgkmcnt(3)\n"
RD"a[88:91],v237 offset:22528\n"
"s_andn2_b32 s49,s60,0x18000\n"
MF"v[0:3],a[64:67],a[212:215],v[0:3]\n"
"s_or_b32 s52,s48,s49\n"
WT"vmcnt(0)\n"
"v_cmp_eq_u64 vcc,s[28:29],v[240:241]\n"
"s_cmp_eq_u64 vcc,exec\n"
"s_cbranch_scc0 L1083_%=\n"
"L1084_%=:\n"
MF"v[40:43],a[64:67],a[216:219],v[40:43]\n"
SA"s53,s52,16384\n"
RD"a[92:95],v237 offset:23552\n"
MF"v[4:7],a[68:71],a[212:215],v[4:7]\n"
BL"a[244:247],v224,s[8:11],s52 offen offset:0 sc1\n"
MF"v[44:47],a[68:71],a[216:219],v[44:47]\n"
RD"a[96:99],v235 offset:18432\n"
MF"v[0:3],a[72:75],a[220:223],v[0:3]\n"
MF"v[40:43],a[72:75],a[224:227],v[40:43]\n"
WT"lgkmcnt(3)\n"
RD"a[100:103],v235 offset:19456\n"
MF"v[4:7],a[76:79],a[220:223],v[4:7]\n"
"v_bfe_u32 v243,v224,6,2\n"
MF"v[44:47],a[76:79],a[224:227],v[44:47]\n"
RD"a[104:107],v235 offset:28672\n"
"v_bfe_u32 v244,v224,4,2\n"
MF"v[0:3],a[80:83],a[228:231],v[0:3]\n"
BL"a[248:251],v224,s[8:11],s53 offen offset:0 sc1\n"
MF"v[40:43],a[80:83],a[232:235],v[40:43]\n"
RD"a[108:111],v235 offset:29696\n"
"v_lshrrev_b32 v245,8,v224\n"
MF"v[4:7],a[84:87],a[228:231],v[4:7]\n"
"v_lshlrev_b32 v245,3,v245\n"
"v_sub_u32 v244,v244,v245\n"
MF"v[44:47],a[84:87],a[232:235],v[44:47]\n"
WT"lgkmcnt(3)\n"
RD"a[112:115],v236 offset:6144\n"
"v_and_b32 v245,16,v224\n"
MF"v[0:3],a[88:91],a[236:239],v[0:3]\n"
"v_mov_b32 v246,0x3f80\n"
MF"v[40:43],a[88:91],a[240:243],v[40:43]\n"
RD"a[116:119],v236 offset:7168\n"
"v_lshlrev_b32 v245,v245,v246\n"
MF"v[4:7],a[92:95],a[236:239],v[4:7]\n"
BL"a[252:255],v224,s[8:11],s52 offen offset:1024 sc1\n"
MF"v[44:47],a[92:95],a[240:243],v[44:47]\n"
RD"a[120:123],v236 offset:16384\n"
"v_lshlrev_b32 v246,4,v243\n"
MF"v[8:11],a[96:99],a[180:183],v[8:11]\n"
"v_and_b32 v246,16,v246\n"
"v_and_b32 v247,2,v243\n"
MF"v[48:51],a[96:99],a[184:187],v[48:51]\n"
WT"lgkmcnt(3)\n"
RD"a[124:127],v236 offset:17408\n"
"v_lshl_add_u32 v246,v247,1,v246\n"
MF"v[12:15],a[100:103],a[180:183],v[12:15]\n"
SA"s90,s88,0\n"
BL"a[128:131],v224,s[8:11],s53 offen offset:1024 sc1\n"
MF"v[52:55],a[100:103],a[184:187],v[52:55]\n"
RD"a[64:67],v236 offset:26624\n"
PK"v0,v0,v1\n"
MF"v[8:11],a[104:107],a[188:191],v[8:11]\n"
PK"v1,v2,v3\n"
"v_add3_u32 v246,v246,v244,0\n"
MF"v[48:51],a[104:107],a[192:195],v[48:51]\n"
RD"a[68:71],v236 offset:27648\n"
PK"v2,v4,v5\n"
MF"v[12:15],a[108:111],a[188:191],v[12:15]\n"
PK"v3,v6,v7\n"
"v_ashrrev_i32 v246,1,v246\n"
MF"v[52:55],a[108:111],a[192:195],v[52:55]\n"
WT"lgkmcnt(3)\n"
BS"v[0:3],v224,s[8:11],s90 offen sc1\n"
"v_cmp_eq_u32_e64 vcc,0,v246\n"
MF"v[8:11],a[112:115],a[196:199],v[8:11]\n"
RD"a[72:75],v237 offset:4096\n"
"v_cndmask_b32_e64 v80,0,v245,vcc\n"
MF"v[48:51],a[112:115],a[200:203],v[48:51]\n"
SA"s91,s22,0\n"
BL"a[132:135],v224,s[8:11],s52 offen offset:2048 sc1\n"
"v_cmp_eq_u32_e64 s[92:93],1,v246\n"
MF"v[12:15],a[116:119],a[196:199],v[12:15]\n"
RD"a[76:79],v237 offset:5120\n"
"v_cndmask_b32_e64 v81,0,v245,s[92:93]\n"
MF"v[52:55],a[116:119],a[200:203],v[52:55]\n"
RD"a[80:83],v237 offset:14336\n"
"v_cmp_eq_u32_e64 s[94:95],2,v246\n"
MF"v[8:11],a[120:123],a[204:207],v[8:11]\n"
PK"v40,v40,v41\n"
PK"v41,v42,v43\n"
"v_cndmask_b32_e64 v82,0,v245,s[94:95]\n"
MF"v[48:51],a[120:123],a[208:211],v[48:51]\n"
WT"lgkmcnt(3)\n"
RD"a[84:87],v237 offset:15360\n"
PK"v42,v44,v45\n"
PK"v43,v46,v47\n"
MF"v[12:15],a[124:127],a[204:207],v[12:15]\n"
BS"v[40:43],v224,s[8:11],s91 offen sc1\n"
"v_cmp_eq_u32_e64 vcc,3,v246\n"
MF"v[52:55],a[124:127],a[208:211],v[52:55]\n"
BL"a[136:139],v224,s[8:11],s53 offen offset:2048 sc1\n"
"v_cndmask_b32_e64 v83,0,v245,vcc\n"
MF"v[8:11],a[64:67],a[212:215],v[8:11]\n"
RD"a[88:91],v237 offset:24576\n"
"v_lshlrev_b32 v246,4,v243\n"
"v_and_b32 v246,16,v246\n"
MF"v[48:51],a[64:67],a[216:219],v[48:51]\n"
RD"a[92:95],v237 offset:25600\n"
"v_and_b32 v247,2,v243\n"
MF"v[12:15],a[68:71],a[212:215],v[12:15]\n"
"v_lshl_add_u32 v246,v247,1,v246\n"
"v_add3_u32 v246,v246,v244,8\n"
MF"v[52:55],a[68:71],a[216:219],v[52:55]\n"
WT"lgkmcnt(3)\n"
RD"a[96:99],v235 offset:20480\n"
"v_ashrrev_i32 v246,1,v246\n"
MF"v[8:11],a[72:75],a[220:223],v[8:11]\n"
BL"a[140:143],v224,s[8:11],s52 offen offset:3072 sc1\n"
MF"v[48:51],a[72:75],a[224:227],v[48:51]\n"
RD"a[100:103],v235 offset:21504\n"
"v_cmp_eq_u32_e64 vcc,0,v246\n"
MF"v[12:15],a[76:79],a[220:223],v[12:15]\n"
"v_cmp_eq_u32_e64 s[92:93],1,v246\n"
"v_cndmask_b32_e64 v88,0,v245,vcc\n"
MF"v[52:55],a[76:79],a[224:227],v[52:55]\n"
RD"a[104:107],v235 offset:30720\n"
"v_cndmask_b32_e64 v89,0,v245,s[92:93]\n"
MF"v[8:11],a[80:83],a[228:231],v[8:11]\n"
BL"a[144:147],v224,s[8:11],s53 offen offset:3072 sc1\n"
MF"v[48:51],a[80:83],a[232:235],v[48:51]\n"
WT"lgkmcnt(3)\n"
RD"a[108:111],v235 offset:31744\n"
"v_cmp_eq_u32_e64 s[94:95],2,v246\n"
MF"v[12:15],a[84:87],a[228:231],v[12:15]\n"
SA"s48,s60,65536\n"
"v_cndmask_b32_e64 v90,0,v245,s[94:95]\n"
"v_cmp_eq_u32_e64 vcc,3,v246\n"
MF"v[52:55],a[84:87],a[232:235],v[52:55]\n"
"s_and_b32 s48,s48,0x18000\n"
RD"a[112:115],v236 offset:8192\n"
"v_cndmask_b32_e64 v91,0,v245,vcc\n"
MF"v[8:11],a[88:91],a[236:239],v[8:11]\n"
"s_andn2_b32 s49,s60,0x18000\n"
"v_lshl_add_u32 v246,v243,3,v244\n"
"v_add_u32 v246,0,v246\n"
MF"v[48:51],a[88:91],a[240:243],v[48:51]\n"
"s_or_b32 s52,s48,s49\n"
RD"a[116:119],v236 offset:9216\n"
"v_ashrrev_i32 v246,1,v246\n"
MF"v[12:15],a[92:95],a[236:239],v[12:15]\n"
SA"s53,s52,16384\n"
BL"a[148:151],v224,s[8:11],s52 offen offset:0 sc1\n"
MF"v[52:55],a[92:95],a[240:243],v[52:55]\n"
WT"lgkmcnt(3)\n"
RD"a[120:123],v236 offset:18432\n"
"v_cmp_eq_u32_e64 vcc,0,v246\n"
MF"v[16:19],a[96:99],a[180:183],v[16:19]\n"
"v_cmp_eq_u32_e64 s[92:93],1,v246\n"
"v_cndmask_b32_e64 v96,0,v245,vcc\n"
MF"v[56:59],a[96:99],a[184:187],v[56:59]\n"
RD"a[124:127],v236 offset:19456\n"
"v_cndmask_b32_e64 v97,0,v245,s[92:93]\n"
MF"v[20:23],a[100:103],a[180:183],v[20:23]\n"
SA"s90,s88,1024\n"
"v_cmp_eq_u32_e64 s[94:95],2,v246\n"
PK"v8,v8,v9\n"
MF"v[60:63],a[100:103],a[184:187],v[60:63]\n"
RD"a[64:67],v236 offset:28672\n"
PK"v9,v10,v11\n"
MF"v[16:19],a[104:107],a[188:191],v[16:19]\n"
BL"a[152:155],v224,s[8:11],s53 offen offset:0 sc1\n"
MF"v[56:59],a[104:107],a[192:195],v[56:59]\n"
WT"lgkmcnt(3)\n"
RD"a[68:71],v236 offset:29696\n"
"v_cndmask_b32_e64 v98,0,v245,s[94:95]\n"
MF"v[20:23],a[108:111],a[188:191],v[20:23]\n"
PK"v10,v12,v13\n"
PK"v11,v14,v15\n"
"v_cmp_eq_u32_e64 vcc,3,v246\n"
MF"v[60:63],a[108:111],a[192:195],v[60:63]\n"
BS"v[8:11],v224,s[8:11],s90 offen sc1\n"
"v_cndmask_b32_e64 v99,0,v245,vcc\n"
MF"v[16:19],a[112:115],a[196:199],v[16:19]\n"
RD"a[72:75],v237 offset:6144\n"
"v_lshl_add_u32 v246,v243,3,v244\n"
MF"v[56:59],a[112:115],a[200:203],v[56:59]\n"
SA"s91,s22,1024\n"
RD"a[76:79],v237 offset:7168\n"
"v_add_u32 v246,4,v246\n"
MF"v[20:23],a[116:119],a[196:199],v[20:23]\n"
BL"a[156:159],v224,s[8:11],s52 offen offset:1024 sc1\n"
"v_ashrrev_i32 v246,1,v246\n"
MF"v[60:63],a[116:119],a[200:203],v[60:63]\n"
WT"lgkmcnt(3)\n"
RD"a[80:83],v237 offset:16384\n"
"v_cmp_eq_u32_e64 vcc,0,v246\n"
MF"v[16:19],a[120:123],a[204:207],v[16:19]\n"
PK"v48,v48,v49\n"
PK"v49,v50,v51\n"
"v_cndmask_b32_e64 v104,0,v245,vcc\n"
MF"v[56:59],a[120:123],a[208:211],v[56:59]\n"
RD"a[84:87],v237 offset:17408\n"
PK"v50,v52,v53\n"
PK"v51,v54,v55\n"
MF"v[20:23],a[124:127],a[204:207],v[20:23]\n"
BS"v[48:51],v224,s[8:11],s91 offen sc1\n"
"v_cmp_eq_u32_e64 s[92:93],1,v246\n"
MF"v[60:63],a[124:127],a[208:211],v[60:63]\n"
BL"a[160:163],v224,s[8:11],s53 offen offset:1024 sc1\n"
"v_cndmask_b32_e64 v105,0,v245,s[92:93]\n"
MF"v[16:19],a[64:67],a[212:215],v[16:19]\n"
"s_mov_b32 s12,s64\n"
RD"a[88:91],v237 offset:26624\n"
"v_cmp_eq_u32_e64 s[94:95],2,v246\n"
MF"v[56:59],a[64:67],a[216:219],v[56:59]\n"
WT"lgkmcnt(3)\n"
RD"a[92:95],v237 offset:27648\n"
"v_cndmask_b32_e64 v106,0,v245,s[94:95]\n"
"v_cmp_eq_u32_e64 vcc,3,v246\n"
MF"v[20:23],a[68:71],a[212:215],v[20:23]\n"
"s_and_b32 s13,s65,0xffff\n"
"v_cndmask_b32_e64 v107,0,v245,vcc\n"
"s_mov_b32 s14,2621440\n"
MF"v[60:63],a[68:71],a[216:219],v[60:63]\n"
"s_mov_b32 s15,0x20000\n"
RD"a[96:99],v235 offset:22528\n"
MF"v[16:19],a[72:75],a[220:223],v[16:19]\n"
"s_mov_b32 s16,s86\n"
BL"a[164:167],v224,s[8:11],s52 offen offset:2048 sc1\n"
MF"v[56:59],a[72:75],a[224:227],v[56:59]\n"
"s_and_b32 s17,s87,0xffff\n"
RD"a[100:103],v235 offset:23552\n"
MF"v[20:23],a[76:79],a[220:223],v[20:23]\n"
"s_mov_b32 s18,2621440\n"
MF"v[60:63],a[76:79],a[224:227],v[60:63]\n"
WT"lgkmcnt(3)\n"
RD"a[104:107],v236 offset:0\n"
"s_mov_b32 s19,0x20000\n"
MF"v[16:19],a[80:83],a[228:231],v[16:19]\n"
"v_bfe_u32 v243,v224,4,4\n"
MF"v[56:59],a[80:83],a[232:235],v[56:59]\n"
"s_lshl_b32 s92,s34,3\n"
RD"a[108:111],v236 offset:1024\n"
MF"v[20:23],a[84:87],a[228:231],v[20:23]\n"
SA"s93,s92,s32\n"
BL"a[168:171],v224,s[8:11],s53 offen offset:2048 sc1\n"
MF"v[60:63],a[84:87],a[232:235],v[60:63]\n"
"s_sub_u32 s92,s32,s92\n"
RD"a[112:115],v236 offset:10240\n"
MF"v[16:19],a[88:91],a[236:239],v[16:19]\n"
SA"s92,s92,120\n"
MF"v[56:59],a[88:91],a[240:243],v[56:59]\n"
WT"lgkmcnt(3)\n"
RD"a[116:119],v236 offset:11264\n"
"v_lshl_add_u32 v244,s93,4,v243\n"
MF"v[20:23],a[92:95],a[236:239],v[20:23]\n"
"v_lshl_add_u32 v245,s92,4,v243\n"
MF"v[60:63],a[92:95],a[240:243],v[60:63]\n"
"s_mul_i32 s93,s93,20480\n"
RD"a[120:123],v236 offset:20480\n"
MF"v[24:27],a[96:99],a[180:183],v[24:27]\n"
"s_mul_i32 s92,s92,20480\n"
BL"a[172:175],v224,s[8:11],s52 offen offset:3072 sc1\n"
MF"v[64:67],a[96:99],a[184:187],v[64:67]\n"
RD"a[124:127],v236 offset:21504\n"
"v_add_u32 v178,s93,v224\n"
MF"v[28:31],a[100:103],a[180:183],v[28:31]\n"
SA"s90,s88,2048\n"
PK"v16,v16,v17\n"
PK"v17,v18,v19\n"
MF"v[68:71],a[100:103],a[184:187],v[68:71]\n"
WT"lgkmcnt(3)\n"
RD"a[64:67],v236 offset:30720\n"
"v_add_u32 v179,s92,v224\n"
MF"v[24:27],a[104:107],a[188:191],v[24:27]\n"
BL"a[176:179],v224,s[8:11],s53 offen offset:3072 sc1\n"
MF"v[64:67],a[104:107],a[192:195],v[64:67]\n"
SA"s48,s60,98304\n"
RD"a[68:71],v236 offset:31744\n"
PK"v18,v20,v21\n"
MF"v[28:31],a[108:111],a[188:191],v[28:31]\n"
"s_and_b32 s48,s48,0x18000\n"
PK"v19,v22,v23\n"
"v_lshrrev_b32 v243,4,v224\n"
MF"v[68:71],a[108:111],a[192:195],v[68:71]\n"
"s_andn2_b32 s49,s60,0x18000\n"
BS"v[16:19],v224,s[8:11],s90 offen sc1\n"
"v_and_b32 v243,48,v243\n"
MF"v[24:27],a[112:115],a[196:199],v[24:27]\n"
"s_or_b32 s52,s48,s49\n"
RD"a[72:75],v237 offset:8192\n"
"v_mul_u32_u24 v176,1280,v244\n"
MF"v[64:67],a[112:115],a[200:203],v[64:67]\n"
WT"lgkmcnt(3)\n"
RD"a[76:79],v237 offset:9216\n"
"v_mul_u32_u24 v177,1280,v245\n"
MF"v[28:31],a[116:119],a[196:199],v[28:31]\n"
SA"s53,s52,16384\n"
BL"a[32:35],v224,s[8:11],s52 offen offset:0 sc1\n"
"v_add_u32 v176,v176,v243\n"
MF"v[68:71],a[116:119],a[200:203],v[68:71]\n"
SA"s91,s22,2048\n"
RD"a[80:83],v237 offset:18432\n"
"v_add_u32 v177,v177,v243\n"
MF"v[24:27],a[120:123],a[204:207],v[24:27]\n"
PK"v56,v56,v57\n"
PK"v57,v58,v59\n"
"v_and_b32 v243,0x300,v224\n"
MF"v[64:67],a[120:123],a[208:211],v[64:67]\n"
RD"a[84:87],v237 offset:19456\n"
PK"v58,v60,v61\n"
PK"v59,v62,v63\n"
MF"v[28:31],a[124:127],a[204:207],v[28:31]\n"
BS"v[56:59],v224,s[8:11],s91 offen sc1\n"
"v_add_u32 v243,2684928,v243\n"
MF"v[68:71],a[124:127],a[208:211],v[68:71]\n"
WT"lgkmcnt(3)\n"
RD"a[88:91],v237 offset:28672\n"
"v_sub_u32 v244,2047,v244\n"
"v_sub_u32 v245,2047,v245\n"
MF"v[24:27],a[64:67],a[212:215],v[24:27]\n"
BL"a[36:39],v224,s[8:11],s53 offen offset:0 sc1\n"
"v_readlane_b32 s92,v234,0\n"
MF"v[64:67],a[64:67],a[216:219],v[64:67]\n"
"s_and_b32 s92,s92,0xffff\n"
RD"a[92:95],v237 offset:29696\n"
"v_cmp_eq_u32 vcc,s92,v244\n"
MF"v[28:31],a[68:71],a[212:215],v[28:31]\n"
"v_cmp_eq_u32_e64 s[94:95],s92,v245\n"
"v_cndmask_b32 v178,v178,v243,vcc\n"
MF"v[68:71],a[68:71],a[216:219],v[68:71]\n"
RD"a[96:99],v235 offset:24576\n"
"v_cndmask_b32_e64 v179,v179,v243,s[94:95]\n"
MF"v[24:27],a[72:75],a[220:223],v[24:27]\n"
"v_add_u32 v243,16,v243\n"
"v_readlane_b32 s92,v234,1\n"
MF"v[64:67],a[72:75],a[224:227],v[64:67]\n"
WT"lgkmcnt(3)\n"
RD"a[100:103],v235 offset:25600\n"
"s_and_b32 s92,s92,0xffff\n"
MF"v[28:31],a[76:79],a[220:223],v[28:31]\n"
BL"a[40:43],v224,s[8:11],s52 offen offset:1024 sc1\n"
MF"v[68:71],a[76:79],a[224:227],v[68:71]\n"
RD"a[104:107],v236 offset:2048\n"
"v_cmp_eq_u32 vcc,s92,v244\n"
MF"v[24:27],a[80:83],a[228:231],v[24:27]\n"
"v_cmp_eq_u32_e64 s[94:95],s92,v245\n"
"v_cndmask_b32 v178,v178,v243,vcc\n"
MF"v[64:67],a[80:83],a[232:235],v[64:67]\n"
RD"a[108:111],v236 offset:3072\n"
"v_cndmask_b32_e64 v179,v179,v243,s[94:95]\n"
MF"v[28:31],a[84:87],a[228:231],v[28:31]\n"
BL"a[44:47],v224,s[8:11],s53 offen offset:1024 sc1\n"
MF"v[68:71],a[84:87],a[232:235],v[68:71]\n"
WT"lgkmcnt(3)\n"
RD"a[112:115],v236 offset:12288\n"
"v_add_u32 v243,16,v243\n"
MF"v[24:27],a[88:91],a[236:239],v[24:27]\n"
"v_readlane_b32 s92,v234,2\n"
"s_and_b32 s92,s92,0xffff\n"
MF"v[64:67],a[88:91],a[240:243],v[64:67]\n"
RD"a[116:119],v236 offset:13312\n"
"v_cmp_eq_u32 vcc,s92,v244\n"
MF"v[28:31],a[92:95],a[236:239],v[28:31]\n"
"v_cmp_eq_u32_e64 s[94:95],s92,v245\n"
MF"v[68:71],a[92:95],a[240:243],v[68:71]\n"
RD"a[120:123],v236 offset:22528\n"
"v_cndmask_b32 v178,v178,v243,vcc\n"
MF"v[32:35],a[96:99],a[180:183],v[32:35]\n"
BL"a[48:51],v224,s[8:11],s52 offen offset:2048 sc1\n"
MF"v[72:75],a[96:99],a[184:187],v[72:75]\n"
WT"lgkmcnt(3)\n"
RD"a[124:127],v236 offset:23552\n"
"v_cndmask_b32_e64 v179,v179,v243,s[94:95]\n"
MF"v[36:39],a[100:103],a[180:183],v[36:39]\n"
SA"s90,s88,3072\n"
"v_add_u32 v243,16,v243\n"
PK"v24,v24,v25\n"
MF"v[76:79],a[100:103],a[184:187],v[76:79]\n"
RD"a[64:67],v237 offset:0\n"
PK"v25,v26,v27\n"
MF"v[32:35],a[104:107],a[188:191],v[32:35]\n"
"v_readlane_b32 s92,v234,3\n"
PK"v26,v28,v29\n"
MF"v[72:75],a[104:107],a[192:195],v[72:75]\n"
"s_and_b32 s92,s92,0xffff\n"
RD"a[68:71],v237 offset:1024\n"
PK"v27,v30,v31\n"
MF"v[36:39],a[108:111],a[188:191],v[36:39]\n"
BL"a[52:55],v224,s[8:11],s53 offen offset:2048 sc1\n"
MF"v[76:79],a[108:111],a[192:195],v[76:79]\n"
WT"lgkmcnt(3)\n"
BS"v[24:27],v224,s[8:11],s90 offen sc1\n"
"v_cmp_eq_u32 vcc,s92,v244\n"
MF"v[32:35],a[112:115],a[196:199],v[32:35]\n"
RD"a[72:75],v237 offset:10240\n"
"v_cmp_eq_u32_e64 s[94:95],s92,v245\n"
MF"v[72:75],a[112:115],a[200:203],v[72:75]\n"
SA"s91,s22,3072\n"
RD"a[76:79],v237 offset:11264\n"
"v_cndmask_b32 v178,v178,v243,vcc\n"
MF"v[36:39],a[116:119],a[196:199],v[36:39]\n"
"v_cndmask_b32_e64 v179,v179,v243,s[94:95]\n"
"v_add_u32 v243,16,v243\n"
MF"v[76:79],a[116:119],a[200:203],v[76:79]\n"
RD"a[80:83],v237 offset:20480\n"
PK"v64,v64,v65\n"
MF"v[32:35],a[120:123],a[204:207],v[32:35]\n"
BL"a[56:59],v224,s[8:11],s52 offen offset:3072 sc1\n"
PK"v65,v66,v67\n"
MF"v[72:75],a[120:123],a[208:211],v[72:75]\n"
WT"lgkmcnt(3)\n"
RD"a[84:87],v237 offset:21504\n"
"v_readlane_b32 s92,v234,4\n"
PK"v66,v68,v69\n"
MF"v[36:39],a[124:127],a[204:207],v[36:39]\n"
"s_and_b32 s92,s92,0xffff\n"
PK"v67,v70,v71\n"
"v_cmp_eq_u32 vcc,s92,v244\n"
MF"v[76:79],a[124:127],a[208:211],v[76:79]\n"
BS"v[64:67],v224,s[8:11],s91 offen sc1\n"
"v_cmp_eq_u32_e64 s[94:95],s92,v245\n"
MF"v[32:35],a[64:67],a[212:215],v[32:35]\n"
RD"a[88:91],v237 offset:30720\n"
"v_cndmask_b32 v178,v178,v243,vcc\n"
MF"v[72:75],a[64:67],a[216:219],v[72:75]\n"
BL"a[60:63],v224,s[8:11],s53 offen offset:3072 sc1\n"
"v_cndmask_b32_e64 v179,v179,v243,s[94:95]\n"
MF"v[36:39],a[68:71],a[212:215],v[36:39]\n"
RD"a[92:95],v237 offset:31744\n"
"v_add_u32 v243,16,v243\n"
MF"v[76:79],a[68:71],a[216:219],v[76:79]\n"
WT"lgkmcnt(3)\n"
RD"a[96:99],v226 offset:8192\n"
"v_readlane_b32 s92,v234,5\n"
MF"v[32:35],a[72:75],a[220:223],v[32:35]\n"
"s_and_b32 s92,s92,0xffff\n"
"v_cmp_eq_u32 vcc,s92,v244\n"
"v_cmp_eq_u32_e64 s[94:95],s92,v245\n"
MF"v[72:75],a[72:75],a[224:227],v[72:75]\n"
RD"a[100:103],v226 offset:9216\n"
"v_cndmask_b32 v178,v178,v243,vcc\n"
MF"v[36:39],a[76:79],a[220:223],v[36:39]\n"
"v_cndmask_b32_e64 v179,v179,v243,s[94:95]\n"
"v_add_u32 v243,16,v243\n"
MF"v[76:79],a[76:79],a[224:227],v[76:79]\n"
RD"a[104:107],v226 offset:18432\n"
"v_readlane_b32 s92,v234,6\n"
MF"v[32:35],a[80:83],a[228:231],v[32:35]\n"
"s_and_b32 s92,s92,0xffff\n"
MF"v[72:75],a[80:83],a[232:235],v[72:75]\n"
WT"lgkmcnt(3)\n"
RD"a[108:111],v226 offset:19456\n"
"v_cmp_eq_u32 vcc,s92,v244\n"
MF"v[36:39],a[84:87],a[228:231],v[36:39]\n"
"v_cmp_eq_u32_e64 s[94:95],s92,v245\n"
MF"v[76:79],a[84:87],a[232:235],v[76:79]\n"
RD"a[112:115],v226 offset:28672\n"
"v_cndmask_b32 v178,v178,v243,vcc\n"
MF"v[32:35],a[88:91],a[236:239],v[32:35]\n"
"v_cndmask_b32_e64 v179,v179,v243,s[94:95]\n"
MF"v[72:75],a[88:91],a[240:243],v[72:75]\n"
RD"a[116:119],v226 offset:29696\n"
"v_add_u32 v243,16,v243\n"
MF"v[36:39],a[92:95],a[236:239],v[36:39]\n"
"v_readlane_b32 s92,v234,7\n"
MF"v[76:79],a[92:95],a[240:243],v[76:79]\n"
WT"vmcnt(31) lgkmcnt(3)\n"
RD"a[120:123],v235 offset:6144\n"
"s_and_b32 s92,s92,0xffff\n"
MF"v[84:87],a[96:99],a[244:247],v[84:87]\n"
WT"vmcnt(30)\n"
"v_cmp_eq_u32 vcc,s92,v244\n"
MF"v[164:167],a[96:99],a[248:251],v[164:167]\n"
RD"a[124:127],v235 offset:7168\n"
"v_cmp_eq_u32_e64 s[94:95],s92,v245\n"
MF"v[92:95],a[100:103],a[244:247],v[92:95]\n"
SA"s90,s88,4096\n"
PK"v32,v32,v33\n"
PK"v33,v34,v35\n"
MF"v[172:175],a[100:103],a[248:251],v[172:175]\n"
WT"vmcnt(29)\n"
RD"a[64:67],v235 offset:16384\n"
"v_cndmask_b32 v178,v178,v243,vcc\n"
MF"v[84:87],a[104:107],a[252:255],v[84:87]\n"
WT"vmcnt(28)\n"
PK"v34,v36,v37\n"
PK"v35,v38,v39\n"
MF"v[164:167],a[104:107],a[128:131],v[164:167]\n"
WT"lgkmcnt(3)\n"
RD"a[68:71],v235 offset:17408\n"
"v_cndmask_b32_e64 v179,v179,v243,s[94:95]\n"
MF"v[92:95],a[108:111],a[252:255],v[92:95]\n"
"s_mul_i32 s92,s33,5\n"
BS"v[32:35],v224,s[8:11],s90 offen sc1\n"
MF"v[172:175],a[108:111],a[128:131],v[172:175]\n"
WT"vmcnt(27)\n"
RD"a[72:75],v235 offset:26624\n"
SA"s93,s92,s30\n"
MF"v[84:87],a[112:115],a[132:135],v[84:87]\n"
WT"vmcnt(25)\n"
SA"s92,s92,4\n"
"s_lshl_b32 s38,s93,6\n"
MF"v[164:167],a[112:115],a[136:139],v[164:167]\n"
"s_lshl_b32 s55,s92,6\n"
RD"a[76:79],v235 offset:27648\n"
PK"v72,v72,v73\n"
MF"v[92:95],a[116:119],a[132:135],v[92:95]\n"
SA"s91,s22,4096\n"
PK"v73,v74,v75\n"
PK"v74,v76,v77\n"
MF"v[172:175],a[116:119],a[136:139],v[172:175]\n"
WT"vmcnt(24) lgkmcnt(3)\n"
RD"a[80:83],v236 offset:4096\n"
PK"v75,v78,v79\n"
MF"v[84:87],a[120:123],a[140:143],v[84:87]\n"
WT"vmcnt(23)\n"
BS"v[72:75],v224,s[8:11],s91 offen sc1\n"
MF"v[164:167],a[120:123],a[144:147],v[164:167]\n"
SA"s55,s55,s45\n"
RD"a[84:87],v236 offset:5120\n"
"s_lshl_b32 s36,s93,10\n"
MF"v[92:95],a[124:127],a[140:143],v[92:95]\n"
"s_lshl_b32 s37,s92,10\n"
SA"s36,s36,65536\n"
SA"s37,s37,65536\n"
MF"v[172:175],a[124:127],a[144:147],v[172:175]\n"
WT"vmcnt(23)\n"
RD"a[88:91],v236 offset:14336\n"
"v_lshrrev_b32 v243,1,v224\n"
MF"v[84:87],a[64:67],a[148:151],v[84:87]\n"
WT"vmcnt(22)\n"
"v_min_u32 v244,312,v243\n"
SA"s37,s37,s45\n"
MF"v[164:167],a[64:67],a[152:155],v[164:167]\n"
WT"lgkmcnt(3)\n"
RD"a[92:95],v236 offset:15360\n"
"v_and_b32 v243,56,v243\n"
MF"v[92:95],a[68:71],a[148:151],v[92:95]\n"
"v_lshlrev_b32 v243,3,v243\n"
MF"v[172:175],a[68:71],a[152:155],v[172:175]\n"
WT"vmcnt(20)\n"
RD"a[96:99],v236 offset:24576\n"
"s_mov_b32 s92,6400\n"
MF"v[84:87],a[72:75],a[156:159],v[84:87]\n"
WT"vmcnt(18)\n"
MF"v[164:167],a[72:75],a[160:163],v[164:167]\n"
RD"a[100:103],v236 offset:25600\n"
MF"v[92:95],a[76:79],a[156:159],v[92:95]\n"
MF"v[172:175],a[76:79],a[160:163],v[172:175]\n"
WT"vmcnt(17) lgkmcnt(3)\n"
RD"a[104:107],v237 offset:2048\n"
MF"v[84:87],a[80:83],a[164:167],v[84:87]\n"
WT"vmcnt(16)\n"
MF"v[164:167],a[80:83],a[168:171],v[164:167]\n"
RD"a[108:111],v237 offset:3072\n"
MF"v[92:95],a[84:87],a[164:167],v[92:95]\n"
MF"v[172:175],a[84:87],a[168:171],v[172:175]\n"
WT"vmcnt(15)\n"
RD"a[112:115],v237 offset:12288\n"
MF"v[84:87],a[88:91],a[172:175],v[84:87]\n"
WT"vmcnt(14)\n"
MF"v[164:167],a[88:91],a[176:179],v[164:167]\n"
WT"lgkmcnt(3)\n"
RD"a[116:119],v237 offset:13312\n"
MF"v[92:95],a[92:95],a[172:175],v[92:95]\n"
MF"v[172:175],a[92:95],a[176:179],v[172:175]\n"
WT"vmcnt(12)\n"
RD"a[120:123],v237 offset:22528\n"
MF"v[84:87],a[96:99],a[32:35],v[84:87]\n"
WT"vmcnt(10)\n"
MF"v[164:167],a[96:99],a[36:39],v[164:167]\n"
RD"a[124:127],v237 offset:23552\n"
MF"v[92:95],a[100:103],a[32:35],v[92:95]\n"
MF"v[172:175],a[100:103],a[36:39],v[172:175]\n"
WT"vmcnt(9) lgkmcnt(3)\n"
RD"a[64:67],v226 offset:10240\n"
MF"v[84:87],a[104:107],a[40:43],v[84:87]\n"
WT"vmcnt(8)\n"
MF"v[164:167],a[104:107],a[44:47],v[164:167]\n"
RD"a[68:71],v226 offset:11264\n"
MF"v[92:95],a[108:111],a[40:43],v[92:95]\n"
MF"v[172:175],a[108:111],a[44:47],v[172:175]\n"
WT"vmcnt(7)\n"
RD"a[72:75],v226 offset:20480\n"
MF"v[84:87],a[112:115],a[48:51],v[84:87]\n"
WT"vmcnt(6)\n"
"buffer_load_dwordx2 v[240:241],v244,s[8:11],s92 offen sc1\n"
MF"v[164:167],a[112:115],a[52:55],v[164:167]\n"
WT"lgkmcnt(3)\n"
RD"a[76:79],v226 offset:21504\n"
MF"v[92:95],a[116:119],a[48:51],v[92:95]\n"
MF"v[172:175],a[116:119],a[52:55],v[172:175]\n"
WT"vmcnt(5)\n"
RD"a[80:83],v226 offset:30720\n"
MF"v[84:87],a[120:123],a[56:59],v[84:87]\n"
WT"vmcnt(3)\n"
MF"v[164:167],a[120:123],a[60:63],v[164:167]\n"
RD"a[84:87],v226 offset:31744\n"
MF"v[92:95],a[124:127],a[56:59],v[92:95]\n"
MF"v[172:175],a[124:127],a[60:63],v[172:175]\n"
WT"lgkmcnt(3)\n"
RD"a[88:91],v235 offset:8192\n"
MF"v[100:103],a[64:67],a[244:247],v[100:103]\n"
MF"v[180:183],a[64:67],a[248:251],v[180:183]\n"
RD"a[92:95],v235 offset:9216\n"
MF"v[108:111],a[68:71],a[244:247],v[108:111]\n"
SA"s90,s88,327680\n"
PK"v84,v84,v85\n"
PK"v85,v86,v87\n"
MF"v[188:191],a[68:71],a[248:251],v[188:191]\n"
RD"a[96:99],v235 offset:18432\n"
PK"v86,v92,v93\n"
MF"v[100:103],a[72:75],a[252:255],v[100:103]\n"
PK"v87,v94,v95\n"
MF"v[180:183],a[72:75],a[128:131],v[180:183]\n"
WT"lgkmcnt(3)\n"
RD"a[100:103],v235 offset:19456\n"
MF"v[108:111],a[76:79],a[252:255],v[108:111]\n"
BS"v[84:87],v224,s[8:11],s90 offen sc1\n"
MF"v[188:191],a[76:79],a[128:131],v[188:191]\n"
RD"a[104:107],v235 offset:28672\n"
MF"v[100:103],a[80:83],a[132:135],v[100:103]\n"
MF"v[180:183],a[80:83],a[136:139],v[180:183]\n"
SA"s91,s22,327680\n"
RD"a[108:111],v235 offset:29696\n"
PK"v164,v164,v165\n"
MF"v[108:111],a[84:87],a[132:135],v[108:111]\n"
PK"v165,v166,v167\n"
PK"v166,v172,v173\n"
MF"v[188:191],a[84:87],a[136:139],v[188:191]\n"
WT"lgkmcnt(3)\n"
RD"a[112:115],v236 offset:6144\n"
PK"v167,v174,v175\n"
MF"v[100:103],a[88:91],a[140:143],v[100:103]\n"
BS"v[164:167],v224,s[8:11],s91 offen sc1\n"
MF"v[180:183],a[88:91],a[144:147],v[180:183]\n"
RD"a[116:119],v236 offset:7168\n"
MF"v[108:111],a[92:95],a[140:143],v[108:111]\n"
MF"v[188:191],a[92:95],a[144:147],v[188:191]\n"
RD"a[120:123],v236 offset:16384\n"
MF"v[100:103],a[96:99],a[148:151],v[100:103]\n"
MF"v[180:183],a[96:99],a[152:155],v[180:183]\n"
WT"lgkmcnt(3)\n"
RD"a[124:127],v236 offset:17408\n"
MF"v[108:111],a[100:103],a[148:151],v[108:111]\n"
WT"vmcnt(3)\n"
"buffer_store_dwordx2 v[232:233],v239,s[8:11],s89 offen sc1\n"
MF"v[188:191],a[100:103],a[152:155],v[188:191]\n"
RD"a[64:67],v236 offset:26624\n"
MF"v[100:103],a[104:107],a[156:159],v[100:103]\n"
MF"v[180:183],a[104:107],a[160:163],v[180:183]\n"
RD"a[68:71],v236 offset:27648\n"
MF"v[108:111],a[108:111],a[156:159],v[108:111]\n"
MF"v[188:191],a[108:111],a[160:163],v[188:191]\n"
WT"lgkmcnt(3)\n"
RD"a[72:75],v237 offset:4096\n"
MF"v[100:103],a[112:115],a[164:167],v[100:103]\n"
MF"v[180:183],a[112:115],a[168:171],v[180:183]\n"
RD"a[76:79],v237 offset:5120\n"
MF"v[108:111],a[116:119],a[164:167],v[108:111]\n"
MF"v[188:191],a[116:119],a[168:171],v[188:191]\n"
RD"a[80:83],v237 offset:14336\n"
MF"v[100:103],a[120:123],a[172:175],v[100:103]\n"
MF"v[180:183],a[120:123],a[176:179],v[180:183]\n"
WT"lgkmcnt(3)\n"
RD"a[84:87],v237 offset:15360\n"
MF"v[108:111],a[124:127],a[172:175],v[108:111]\n"
MF"v[188:191],a[124:127],a[176:179],v[188:191]\n"
RD"a[88:91],v237 offset:24576\n"
MF"v[100:103],a[64:67],a[32:35],v[100:103]\n"
MF"v[180:183],a[64:67],a[36:39],v[180:183]\n"
RD"a[92:95],v237 offset:25600\n"
MF"v[108:111],a[68:71],a[32:35],v[108:111]\n"
MF"v[188:191],a[68:71],a[36:39],v[188:191]\n"
WT"lgkmcnt(3)\n"
RD"a[96:99],v226 offset:12288\n"
MF"v[100:103],a[72:75],a[40:43],v[100:103]\n"
MF"v[180:183],a[72:75],a[44:47],v[180:183]\n"
RD"a[100:103],v226 offset:13312\n"
MF"v[108:111],a[76:79],a[40:43],v[108:111]\n"
MF"v[188:191],a[76:79],a[44:47],v[188:191]\n"
RD"a[104:107],v226 offset:22528\n"
MF"v[100:103],a[80:83],a[48:51],v[100:103]\n"
MF"v[180:183],a[80:83],a[52:55],v[180:183]\n"
WT"lgkmcnt(3)\n"
RD"a[108:111],v226 offset:23552\n"
MF"v[108:111],a[84:87],a[48:51],v[108:111]\n"
MF"v[188:191],a[84:87],a[52:55],v[188:191]\n"
RD"a[112:115],v235 offset:0\n"
MF"v[100:103],a[88:91],a[56:59],v[100:103]\n"
MF"v[180:183],a[88:91],a[60:63],v[180:183]\n"
RD"a[116:119],v235 offset:1024\n"
MF"v[108:111],a[92:95],a[56:59],v[108:111]\n"
MF"v[188:191],a[92:95],a[60:63],v[188:191]\n"
WT"lgkmcnt(3)\n"
RD"a[120:123],v235 offset:10240\n"
MF"v[116:119],a[96:99],a[244:247],v[116:119]\n"
MF"v[196:199],a[96:99],a[248:251],v[196:199]\n"
RD"a[124:127],v235 offset:11264\n"
MF"v[124:127],a[100:103],a[244:247],v[124:127]\n"
SA"s90,s88,328704\n"
PK"v100,v100,v101\n"
PK"v101,v102,v103\n"
MF"v[204:207],a[100:103],a[248:251],v[204:207]\n"
RD"a[64:67],v235 offset:20480\n"
PK"v102,v108,v109\n"
MF"v[116:119],a[104:107],a[252:255],v[116:119]\n"
WT"vmcnt(3)\n"
"v_cmp_eq_u64 vcc,s[28:29],v[240:241]\n"
"s_cmp_eq_u64 vcc,exec\n"
"s_cbranch_scc0 L1086_%=\n"
"L1087_%=:\n"
MF"v[196:199],a[104:107],a[128:131],v[196:199]\n"
WT"lgkmcnt(3)\n"
RD"a[68:71],v235 offset:21504\n"
PK"v103,v110,v111\n"
MF"v[124:127],a[108:111],a[252:255],v[124:127]\n"
BS"v[100:103],v224,s[8:11],s90 offen sc1\n"
MF"v[204:207],a[108:111],a[128:131],v[204:207]\n"
RD"a[72:75],v235 offset:30720\n"
MF"v[116:119],a[112:115],a[132:135],v[116:119]\n"
BL"v[112:115],v176,s[12:15],s38 offen\n"
MF"v[196:199],a[112:115],a[136:139],v[196:199]\n"
SA"s91,s22,328704\n"
RD"a[76:79],v235 offset:31744\n"
PK"v180,v180,v181\n"
MF"v[124:127],a[116:119],a[132:135],v[124:127]\n"
BL"v[144:147],v178,s[8:11],s36 offen sc1\n"
MF"v[204:207],a[116:119],a[136:139],v[204:207]\n"
WT"lgkmcnt(3)\n"
RD"a[80:83],v236 offset:8192\n"
PK"v181,v182,v183\n"
MF"v[116:119],a[120:123],a[140:143],v[116:119]\n"
PK"v182,v188,v189\n"
PK"v183,v190,v191\n"
MF"v[196:199],a[120:123],a[144:147],v[196:199]\n"
BS"v[180:183],v224,s[8:11],s91 offen sc1\n"
MF"v[124:127],a[124:127],a[140:143],v[124:127]\n"
RD"a[84:87],v236 offset:9216\n"
MF"v[204:207],a[124:127],a[144:147],v[204:207]\n"
BL"v[120:123],v176,s[12:15],s55 offen\n"
MF"v[116:119],a[64:67],a[148:151],v[116:119]\n"
RD"a[88:91],v236 offset:18432\n"
MF"v[196:199],a[64:67],a[152:155],v[196:199]\n"
WT"lgkmcnt(3)\n"
BL"v[152:155],v178,s[8:11],s37 offen sc1\n"
MF"v[124:127],a[68:71],a[148:151],v[124:127]\n"
RD"a[92:95],v236 offset:19456\n"
MF"v[204:207],a[68:71],a[152:155],v[204:207]\n"
RD"a[96:99],v236 offset:28672\n"
MF"v[116:119],a[72:75],a[156:159],v[116:119]\n"
BL"v[128:131],v177,s[12:15],s38 offen\n"
MF"v[196:199],a[72:75],a[160:163],v[196:199]\n"
RD"a[100:103],v236 offset:29696\n"
MF"v[124:127],a[76:79],a[156:159],v[124:127]\n"
BL"v[160:163],v179,s[8:11],s36 offen sc1\n"
MF"v[204:207],a[76:79],a[160:163],v[204:207]\n"
WT"lgkmcnt(3)\n"
RD"a[104:107],v237 offset:6144\n"
MF"v[116:119],a[80:83],a[164:167],v[116:119]\n"
MF"v[196:199],a[80:83],a[168:171],v[196:199]\n"
RD"a[108:111],v237 offset:7168\n"
MF"v[124:127],a[84:87],a[164:167],v[124:127]\n"
BL"v[136:139],v177,s[12:15],s55 offen\n"
MF"v[204:207],a[84:87],a[168:171],v[204:207]\n"
RD"a[112:115],v237 offset:16384\n"
MF"v[116:119],a[88:91],a[172:175],v[116:119]\n"
BL"v[168:171],v179,s[8:11],s37 offen sc1\n"
MF"v[196:199],a[88:91],a[176:179],v[196:199]\n"
WT"lgkmcnt(3)\n"
RD"a[116:119],v237 offset:17408\n"
MF"v[124:127],a[92:95],a[172:175],v[124:127]\n"
MF"v[204:207],a[92:95],a[176:179],v[204:207]\n"
RD"a[120:123],v237 offset:26624\n"
MF"v[116:119],a[96:99],a[32:35],v[116:119]\n"
MF"v[196:199],a[96:99],a[36:39],v[196:199]\n"
RD"a[124:127],v237 offset:27648\n"
MF"v[124:127],a[100:103],a[32:35],v[124:127]\n"
"s_lshr_b32 s93,s31,3\n"
MF"v[204:207],a[100:103],a[36:39],v[204:207]\n"
WT"lgkmcnt(3)\n"
RD"a[64:67],v226 offset:14336\n"
"s_lshl_b32 s93,s93,4\n"
MF"v[116:119],a[104:107],a[40:43],v[116:119]\n"
SA"s93,s93,s32\n"
MF"v[196:199],a[104:107],a[44:47],v[196:199]\n"
"s_mul_i32 s94,s93,40960\n"
RD"a[68:71],v226 offset:15360\n"
MF"v[124:127],a[108:111],a[40:43],v[124:127]\n"
"s_lshl_b32 s93,s93,9\n"
MF"v[204:207],a[108:111],a[44:47],v[204:207]\n"
SA"s93,s93,80478208\n"
RD"a[72:75],v226 offset:24576\n"
MF"v[116:119],a[112:115],a[48:51],v[116:119]\n"
SA"s94,s94,59506688\n"
MF"v[196:199],a[112:115],a[52:55],v[196:199]\n"
WT"lgkmcnt(3)\n"
RD"a[76:79],v226 offset:25600\n"
"s_lshl_b32 s92,s30,10\n"
MF"v[124:127],a[116:119],a[48:51],v[124:127]\n"
"v_add_u32 v244,s92,v224\n"
MF"v[204:207],a[116:119],a[52:55],v[204:207]\n"
SA"s92,s45,4096\n"
RD"a[80:83],v235 offset:2048\n"
MF"v[116:119],a[120:123],a[56:59],v[116:119]\n"
"v_add_u32 v245,s92,v224\n"
MF"v[196:199],a[120:123],a[60:63],v[196:199]\n"
RD"a[84:87],v235 offset:3072\n"
MF"v[124:127],a[124:127],a[56:59],v[124:127]\n"
"v_and_b32 v246,128,v224\n"
MF"v[204:207],a[124:127],a[60:63],v[204:207]\n"
WT"lgkmcnt(3)\n"
RD"a[88:91],v235 offset:12288\n"
"v_lshl_add_u32 v243,v246,5,v243\n"
MF"v[132:135],a[64:67],a[244:247],v[132:135]\n"
"v_lshrrev_b32 v246,8,v224\n"
MF"v[208:211],a[64:67],a[248:251],v[208:211]\n"
RD"a[92:95],v235 offset:13312\n"
"v_lshl_add_u32 v243,v246,27,v243\n"
MF"v[140:143],a[68:71],a[244:247],v[140:143]\n"
SA"s90,s88,329728\n"
PK"v116,v116,v117\n"
PK"v117,v118,v119\n"
MF"v[212:215],a[68:71],a[248:251],v[212:215]\n"
"s_mov_b32 s95,0xffff0101\n"
RD"a[96:99],v235 offset:22528\n"
PK"v118,v124,v125\n"
MF"v[132:135],a[72:75],a[252:255],v[132:135]\n"
"s_lshl_b32 s92,s30,10\n"
PK"v119,v126,v127\n"
"v_add_u32 v247,s92,v227\n"
MF"v[208:211],a[72:75],a[128:131],v[208:211]\n"
WT"lgkmcnt(3)\n"
RD"a[100:103],v235 offset:23552\n"
MF"v[140:143],a[76:79],a[252:255],v[140:143]\n"
BS"v[116:119],v224,s[8:11],s90 offen sc1\n"
MF"v[212:215],a[76:79],a[128:131],v[212:215]\n"
"buffer_load_dwordx2 v[240:241],v243,s[8:11],s93 offen sc1\n"
MF"v[132:135],a[80:83],a[132:135],v[132:135]\n"
RD"a[104:107],v236 offset:0\n"
MF"v[208:211],a[80:83],a[136:139],v[208:211]\n"
SA"s91,s22,329728\n"
RD"a[108:111],v236 offset:1024\n"
PK"v196,v196,v197\n"
MF"v[140:143],a[84:87],a[132:135],v[140:143]\n"
PK"v197,v198,v199\n"
PK"v198,v204,v205\n"
MF"v[212:215],a[84:87],a[136:139],v[212:215]\n"
WT"lgkmcnt(3)\n"
RD"a[112:115],v236 offset:10240\n"
PK"v199,v206,v207\n"
MF"v[132:135],a[88:91],a[140:143],v[132:135]\n"
BS"v[196:199],v224,s[8:11],s91 offen sc1\n"
MF"v[208:211],a[88:91],a[144:147],v[208:211]\n"
RD"a[116:119],v236 offset:11264\n"
MF"v[140:143],a[92:95],a[140:143],v[140:143]\n"
MF"v[212:215],a[92:95],a[144:147],v[212:215]\n"
RD"a[120:123],v236 offset:20480\n"
MF"v[132:135],a[96:99],a[148:151],v[132:135]\n"
MF"v[208:211],a[96:99],a[152:155],v[208:211]\n"
WT"lgkmcnt(3)\n"
RD"a[124:127],v236 offset:21504\n"
MF"v[140:143],a[100:103],a[148:151],v[140:143]\n"
MF"v[212:215],a[100:103],a[152:155],v[212:215]\n"
RD"a[64:67],v236 offset:30720\n"
MF"v[132:135],a[104:107],a[156:159],v[132:135]\n"
MF"v[208:211],a[104:107],a[160:163],v[208:211]\n"
RD"a[68:71],v236 offset:31744\n"
MF"v[140:143],a[108:111],a[156:159],v[140:143]\n"
MF"v[212:215],a[108:111],a[160:163],v[212:215]\n"
WT"lgkmcnt(3)\n"
RD"a[72:75],v237 offset:8192\n"
MF"v[132:135],a[112:115],a[164:167],v[132:135]\n"
MF"v[208:211],a[112:115],a[168:171],v[208:211]\n"
RD"a[76:79],v237 offset:9216\n"
MF"v[140:143],a[116:119],a[164:167],v[140:143]\n"
MF"v[212:215],a[116:119],a[168:171],v[212:215]\n"
RD"a[80:83],v237 offset:18432\n"
MF"v[132:135],a[120:123],a[172:175],v[132:135]\n"
MF"v[208:211],a[120:123],a[176:179],v[208:211]\n"
WT"lgkmcnt(3)\n"
RD"a[84:87],v237 offset:19456\n"
MF"v[140:143],a[124:127],a[172:175],v[140:143]\n"
MF"v[212:215],a[124:127],a[176:179],v[212:215]\n"
RD"a[88:91],v237 offset:28672\n"
MF"v[132:135],a[64:67],a[32:35],v[132:135]\n"
MF"v[208:211],a[64:67],a[36:39],v[208:211]\n"
RD"a[92:95],v237 offset:29696\n"
MF"v[140:143],a[68:71],a[32:35],v[140:143]\n"
MF"v[212:215],a[68:71],a[36:39],v[212:215]\n"
WT"lgkmcnt(3)\n"
RD"a[96:99],v226 offset:16384\n"
MF"v[132:135],a[72:75],a[40:43],v[132:135]\n"
MF"v[208:211],a[72:75],a[44:47],v[208:211]\n"
RD"a[100:103],v226 offset:17408\n"
MF"v[140:143],a[76:79],a[40:43],v[140:143]\n"
MF"v[212:215],a[76:79],a[44:47],v[212:215]\n"
RD"a[104:107],v226 offset:26624\n"
MF"v[132:135],a[80:83],a[48:51],v[132:135]\n"
MF"v[208:211],a[80:83],a[52:55],v[208:211]\n"
WT"lgkmcnt(3)\n"
RD"a[108:111],v226 offset:27648\n"
MF"v[140:143],a[84:87],a[48:51],v[140:143]\n"
WT"vmcnt(1)\n"
"v_cmp_eq_u64 vcc,s[28:29],v[240:241]\n"
MF"v[212:215],a[84:87],a[52:55],v[212:215]\n"
"s_and_b32 s50,vcc_lo,0xff\n"
RD"a[112:115],v235 offset:4096\n"
"s_or_b32 s95,s95,s50\n"
MF"v[132:135],a[88:91],a[56:59],v[132:135]\n"
"s_bitcmp1_b32 s95,1\n"
"s_cselect_b32 s51,s94,1065353216\n"
MF"v[208:211],a[88:91],a[60:63],v[208:211]\n"
SA"s92,s51,5120\n"
RD"a[116:119],v235 offset:5120\n"
MF"v[140:143],a[92:95],a[56:59],v[140:143]\n"
BL"a[184:187],v244,s[8:11],s92 offen sc1\n"
MF"v[212:215],a[92:95],a[60:63],v[212:215]\n"
WT"lgkmcnt(3)\n"
RD"a[120:123],v235 offset:14336\n"
MF"v[148:151],a[96:99],a[244:247],v[148:151]\n"
BL"a[216:219],v245,s[8:11],s92 offen sc1\n"
MF"v[216:219],a[96:99],a[248:251],v[216:219]\n"
RD"a[124:127],v235 offset:15360\n"
MF"v[156:159],a[100:103],a[244:247],v[156:159]\n"
SA"s90,s88,330752\n"
PK"v132,v132,v133\n"
PK"v133,v134,v135\n"
MF"v[220:223],a[100:103],a[248:251],v[220:223]\n"
"s_bitcmp1_b32 s95,2\n"
RD"a[64:67],v235 offset:24576\n"
PK"v134,v140,v141\n"
MF"v[148:151],a[104:107],a[252:255],v[148:151]\n"
"s_cselect_b32 s51,s94,1065353216\n"
PK"v135,v142,v143\n"
SA"s92,s51,10240\n"
MF"v[216:219],a[104:107],a[128:131],v[216:219]\n"
WT"lgkmcnt(3)\n"
BL"a[188:191],v244,s[8:11],s92 offen sc1\n"
MF"v[156:159],a[108:111],a[252:255],v[156:159]\n"
RD"a[68:71],v235 offset:25600\n"
MF"v[220:223],a[108:111],a[128:131],v[220:223]\n"
BS"v[132:135],v224,s[8:11],s90 offen sc1\n"
MF"v[148:151],a[112:115],a[132:135],v[148:151]\n"
"s_bitcmp1_b32 s95,3\n"
BL"a[220:223],v245,s[8:11],s92 offen sc1\n"
MF"v[216:219],a[112:115],a[136:139],v[216:219]\n"
"s_cselect_b32 s51,s94,1065353216\n"
RD"a[72:75],v236 offset:2048\n"
PK"v208,v208,v209\n"
MF"v[156:159],a[116:119],a[132:135],v[156:159]\n"
SA"s91,s22,330752\n"
RD"a[76:79],v236 offset:3072\n"
PK"v209,v210,v211\n"
MF"v[220:223],a[116:119],a[136:139],v[220:223]\n"
WT"lgkmcnt(3)\n"
RD"a[80:83],v236 offset:12288\n"
PK"v210,v212,v213\n"
MF"v[148:151],a[120:123],a[140:143],v[148:151]\n"
SA"s92,s51,15360\n"
PK"v211,v214,v215\n"
MF"v[216:219],a[120:123],a[144:147],v[216:219]\n"
BL"a[192:195],v244,s[8:11],s92 offen sc1\n"
MF"v[156:159],a[124:127],a[140:143],v[156:159]\n"
"s_bitcmp1_b32 s95,4\n"
BS"v[208:211],v224,s[8:11],s91 offen sc1\n"
BL"a[224:227],v245,s[8:11],s92 offen sc1\n"
MF"v[220:223],a[124:127],a[144:147],v[220:223]\n"
"s_cselect_b32 s51,s94,1065353216\n"
RD"a[84:87],v236 offset:13312\n"
SA"s92,s51,20480\n"
MF"v[148:151],a[64:67],a[148:151],v[148:151]\n"
RD"a[88:91],v236 offset:22528\n"
MF"v[216:219],a[64:67],a[152:155],v[216:219]\n"
WT"lgkmcnt(3)\n"
BL"a[196:199],v244,s[8:11],s92 offen sc1\n"
MF"v[156:159],a[68:71],a[148:151],v[156:159]\n"
RD"a[92:95],v236 offset:23552\n"
MF"v[220:223],a[68:71],a[152:155],v[220:223]\n"
BL"a[228:231],v245,s[8:11],s92 offen sc1\n"
MF"v[148:151],a[72:75],a[156:159],v[148:151]\n"
"s_bitcmp1_b32 s95,5\n"
RD"a[96:99],v237 offset:0\n"
"s_cselect_b32 s51,s94,1065353216\n"
MF"v[216:219],a[72:75],a[160:163],v[216:219]\n"
SA"s92,s51,25600\n"
RD"a[100:103],v237 offset:1024\n"
MF"v[156:159],a[76:79],a[156:159],v[156:159]\n"
BL"a[200:203],v244,s[8:11],s92 offen sc1\n"
MF"v[220:223],a[76:79],a[160:163],v[220:223]\n"
WT"lgkmcnt(3)\n"
RD"a[104:107],v237 offset:10240\n"
MF"v[148:151],a[80:83],a[164:167],v[148:151]\n"
BL"a[232:235],v245,s[8:11],s92 offen sc1\n"
MF"v[216:219],a[80:83],a[168:171],v[216:219]\n"
RD"a[108:111],v237 offset:11264\n"
MF"v[156:159],a[84:87],a[164:167],v[156:159]\n"
"s_bitcmp1_b32 s95,6\n"
"s_cselect_b32 s51,s94,1065353216\n"
MF"v[220:223],a[84:87],a[168:171],v[220:223]\n"
SA"s92,s51,30720\n"
RD"a[112:115],v237 offset:20480\n"
MF"v[148:151],a[88:91],a[172:175],v[148:151]\n"
BL"a[204:207],v244,s[8:11],s92 offen sc1\n"
MF"v[216:219],a[88:91],a[176:179],v[216:219]\n"
WT"lgkmcnt(3)\n"
RD"a[116:119],v237 offset:21504\n"
MF"v[156:159],a[92:95],a[172:175],v[156:159]\n"
BL"a[236:239],v245,s[8:11],s92 offen sc1\n"
MF"v[220:223],a[92:95],a[176:179],v[220:223]\n"
RD"a[120:123],v237 offset:30720\n"
MF"v[148:151],a[96:99],a[32:35],v[148:151]\n"
"s_bitcmp1_b32 s95,7\n"
"s_cselect_b32 s51,s94,1065353216\n"
MF"v[216:219],a[96:99],a[36:39],v[216:219]\n"
SA"s92,s51,35840\n"
RD"a[124:127],v237 offset:31744\n"
MF"v[156:159],a[100:103],a[32:35],v[156:159]\n"
BL"a[208:211],v244,s[8:11],s92 offen sc1\n"
MF"v[220:223],a[100:103],a[36:39],v[220:223]\n"
WT"lgkmcnt(3)\n"
MF"v[148:151],a[104:107],a[40:43],v[148:151]\n"
BL"a[240:243],v245,s[8:11],s92 offen sc1\n"
MF"v[216:219],a[104:107],a[44:47],v[216:219]\n"
MF"v[156:159],a[108:111],a[40:43],v[156:159]\n"
MF"v[220:223],a[108:111],a[44:47],v[220:223]\n"
MF"v[148:151],a[112:115],a[48:51],v[148:151]\n"
MF"v[216:219],a[112:115],a[52:55],v[216:219]\n"
WT"lgkmcnt(0)\n"
MF"v[156:159],a[116:119],a[48:51],v[156:159]\n"
MF"v[220:223],a[116:119],a[52:55],v[220:223]\n"
MF"v[148:151],a[120:123],a[56:59],v[148:151]\n"
MF"v[216:219],a[120:123],a[60:63],v[216:219]\n"
MF"v[156:159],a[124:127],a[56:59],v[156:159]\n"
MF"v[220:223],a[124:127],a[60:63],v[220:223]\n"
"s_nop 5\n"
PK"v148,v148,v149\n"
PK"v149,v150,v151\n"
PK"v150,v156,v157\n"
PK"v151,v158,v159\n"
SA"s90,s88,331776\n"
BS"v[148:151],v224,s[8:11],s90 offen sc1\n"
PK"v216,v216,v217\n"
PK"v217,v218,v219\n"
PK"v218,v220,v221\n"
PK"v219,v222,v223\n"
SA"s91,s22,331776\n"
BS"v[216:219],v224,s[8:11],s91 offen sc1\n"
"s_cmp_eq_u32 s23,0\n"
"s_cbranch_scc0 L1089_%=\n"
"ds_write_b128 v227,v[0:3] offset:0\n"
"ds_write_b128 v227,v[8:11] offset:1024\n"
"ds_write_b128 v227,v[16:19] offset:2048\n"
"ds_write_b128 v227,v[24:27] offset:3072\n"
"ds_write_b128 v227,v[32:35] offset:4096\n"
"ds_write_b128 v227,v[84:87] offset:5120\n"
"ds_write_b128 v227,v[100:103] offset:6144\n"
"ds_write_b128 v227,v[116:119] offset:7168\n"
"ds_write_b128 v227,v[132:135] offset:8192\n"
"ds_write_b128 v227,v[148:151] offset:9216\n"
"L1089_%=:\n"
"s_cmp_eq_u32 s23,4\n"
"s_cbranch_scc0 L1090_%=\n"
"ds_write_b128 v227,v[40:43] offset:0\n"
"ds_write_b128 v227,v[48:51] offset:1024\n"
"ds_write_b128 v227,v[56:59] offset:2048\n"
"ds_write_b128 v227,v[64:67] offset:3072\n"
"ds_write_b128 v227,v[72:75] offset:4096\n"
"ds_write_b128 v227,v[164:167] offset:5120\n"
"ds_write_b128 v227,v[180:183] offset:6144\n"
"ds_write_b128 v227,v[196:199] offset:7168\n"
"ds_write_b128 v227,v[208:211] offset:8192\n"
"ds_write_b128 v227,v[216:219] offset:9216\n"
"L1090_%=:\n"
MF"v[4:7],v[96:99],v[112:115],0\n"
MF"v[12:15],v[104:107],v[112:115],0\n"
MF"v[20:23],v[96:99],v[120:123],0\n"
MF"v[28:31],v[104:107],v[120:123],0\n"
MF"v[4:7],v[96:99],v[144:147],v[4:7]\n"
MF"v[12:15],v[104:107],v[144:147],v[12:15]\n"
MF"v[20:23],v[96:99],v[152:155],v[20:23]\n"
MF"v[28:31],v[104:107],v[152:155],v[28:31]\n"
MF"v[36:39],v[96:99],v[128:131],0\n"
MF"v[44:47],v[104:107],v[128:131],0\n"
MF"v[52:55],v[96:99],v[136:139],0\n"
MF"v[60:63],v[104:107],v[136:139],0\n"
MF"v[36:39],v[96:99],v[160:163],v[36:39]\n"
MF"v[44:47],v[104:107],v[160:163],v[44:47]\n"
MF"v[52:55],v[96:99],v[168:171],v[52:55]\n"
MF"v[60:63],v[104:107],v[168:171],v[60:63]\n"
SA"s89,s89,4096\n"
"s_mov_b32 s54,1048576\n"
WT"vmcnt(0)\n"
"buffer_store_dwordx2 v[232:233],v239,s[8:11],s89 offen sc1\n"
"buffer_load_dwordx2 v[240:241],v243,s[8:11],s93 offen sc1\n"
WT"lgkmcnt(0)\n"
"s_barrier\n"
RD"a[180:183],v247 offset:0\n"
RD"a[212:215],v227 offset:4096\n"
RD"a[64:67],v247 offset:5120\n"
RD"a[96:99],v227 offset:9216\n"
"s_getpc_b64 s[52:53]\n"
"L1097_%=:\n"
SA"s52,s52,L1096_%=-L1097_%=-32\n"
"s_addc_u32 s53,s53,0\n"
"s_mov_b32 s89,0\n"
WT"lgkmcnt(0)\n"
MF"v[4:7],v[80:83],a[180:183],v[4:7]\n"
MF"v[12:15],v[88:91],a[180:183],v[12:15]\n"
MF"v[20:23],v[80:83],a[212:215],v[20:23]\n"
MF"v[28:31],v[88:91],a[212:215],v[28:31]\n"
MF"v[36:39],v[80:83],a[64:67],v[36:39]\n"
MF"v[44:47],v[88:91],a[64:67],v[44:47]\n"
MF"v[52:55],v[80:83],a[96:99],v[52:55]\n"
MF"v[60:63],v[88:91],a[96:99],v[60:63]\n"
"s_branch L1092_%=\n"
"L1094_%=:\n"
"s_sleep 3\n"
"buffer_load_dwordx2 v[240:241],v243,s[8:11],s93 offen sc1\n"
"L1091_%=:\n"
WT"vmcnt(0)\n"
"v_cmp_eq_u64 vcc,s[28:29],v[240:241]\n"
"s_sub_u32 s54,s54,1\n"
"s_cselect_b32 s90,-1,vcc_lo\n"
"s_andn2_b32 s90,s90,s95\n"
"s_cbranch_scc0 L1094_%=\n"
"s_or_b32 s95,s95,s90\n"
"L1098_%=:\n"
"s_ff1_i32_b32 s50,s90\n"
"s_bitset0_b32 s90,s50\n"
"s_lshl_b32 s50,s50,5\n"
SA"s50,s50,s52\n"
"s_addc_u32 s51,s53,0\n"
"s_setpc_b64 s[50:51]\n"
"L1096_%=:\n"
SA"s92,s94,5120\n"
BL"a[184:187],v244,s[8:11],s92 offen sc1\n"
BL"a[216:219],v245,s[8:11],s92 offen sc1\n"
"s_branch L1093_%=\n"
"s_nop 0\n"
SA"s92,s94,10240\n"
BL"a[188:191],v244,s[8:11],s92 offen sc1\n"
BL"a[220:223],v245,s[8:11],s92 offen sc1\n"
"s_branch L1093_%=\n"
"s_nop 0\n"
SA"s92,s94,15360\n"
BL"a[192:195],v244,s[8:11],s92 offen sc1\n"
BL"a[224:227],v245,s[8:11],s92 offen sc1\n"
"s_branch L1093_%=\n"
"s_nop 0\n"
SA"s92,s94,20480\n"
BL"a[196:199],v244,s[8:11],s92 offen sc1\n"
BL"a[228:231],v245,s[8:11],s92 offen sc1\n"
"s_branch L1093_%=\n"
"s_nop 0\n"
SA"s92,s94,25600\n"
BL"a[200:203],v244,s[8:11],s92 offen sc1\n"
BL"a[232:235],v245,s[8:11],s92 offen sc1\n"
"s_branch L1093_%=\n"
"s_nop 0\n"
SA"s92,s94,30720\n"
BL"a[204:207],v244,s[8:11],s92 offen sc1\n"
BL"a[236:239],v245,s[8:11],s92 offen sc1\n"
"s_branch L1093_%=\n"
"s_nop 0\n"
SA"s92,s94,35840\n"
BL"a[208:211],v244,s[8:11],s92 offen sc1\n"
BL"a[240:243],v245,s[8:11],s92 offen sc1\n"
"s_branch L1093_%=\n"
"s_nop 0\n"
"s_nop 0\n"
"s_nop 0\n"
"s_nop 0\n"
"s_nop 0\n"
"s_nop 0\n"
"s_nop 0\n"
"s_nop 0\n"
"s_nop 0\n"
SA"s92,s94,332800\n"
BL"a[68:71],v244,s[8:11],s92 offen sc1\n"
BL"a[100:103],v245,s[8:11],s92 offen sc1\n"
"s_branch L1093_%=\n"
"s_nop 0\n"
SA"s92,s94,337920\n"
BL"a[72:75],v244,s[8:11],s92 offen sc1\n"
BL"a[104:107],v245,s[8:11],s92 offen sc1\n"
"s_branch L1093_%=\n"
"s_nop 0\n"
SA"s92,s94,343040\n"
BL"a[76:79],v244,s[8:11],s92 offen sc1\n"
BL"a[108:111],v245,s[8:11],s92 offen sc1\n"
"s_branch L1093_%=\n"
"s_nop 0\n"
SA"s92,s94,348160\n"
BL"a[80:83],v244,s[8:11],s92 offen sc1\n"
BL"a[112:115],v245,s[8:11],s92 offen sc1\n"
"s_branch L1093_%=\n"
"s_nop 0\n"
SA"s92,s94,353280\n"
BL"a[84:87],v244,s[8:11],s92 offen sc1\n"
BL"a[116:119],v245,s[8:11],s92 offen sc1\n"
"s_branch L1093_%=\n"
"s_nop 0\n"
SA"s92,s94,358400\n"
BL"a[88:91],v244,s[8:11],s92 offen sc1\n"
BL"a[120:123],v245,s[8:11],s92 offen sc1\n"
"s_branch L1093_%=\n"
"s_nop 0\n"
SA"s92,s94,363520\n"
BL"a[92:95],v244,s[8:11],s92 offen sc1\n"
BL"a[124:127],v245,s[8:11],s92 offen sc1\n"
"s_branch L1093_%=\n"
"s_nop 0\n"
"L1093_%=:\n"
"s_cmp_lg_u32 s90,0\n"
"s_cbranch_scc1 L1098_%=\n"
"s_and_b32 s92,s95,s89\n"
"s_cmp_eq_u32 s92,-1\n"
"s_cbranch_scc1 L1095_%=\n"
"buffer_load_dwordx2 v[240:241],v243,s[8:11],s93 offen sc1\n"
WT"vmcnt(1)\n"
"L1092_%=:\n"
"s_or_b32 s92,s95,0xff00\n"
"s_andn2_b32 s92,s92,s89\n"
"s_cmp_eq_u32 s92,-1\n"
"s_cbranch_scc0 L1091_%=\n"
MF"v[4:7],v[80:83],a[184:187],v[4:7]\n"
MF"v[12:15],v[88:91],a[184:187],v[12:15]\n"
MF"v[4:7],v[80:83],a[188:191],v[4:7]\n"
MF"v[12:15],v[88:91],a[188:191],v[12:15]\n"
MF"v[4:7],v[80:83],a[192:195],v[4:7]\n"
MF"v[12:15],v[88:91],a[192:195],v[12:15]\n"
MF"v[4:7],v[80:83],a[196:199],v[4:7]\n"
MF"v[12:15],v[88:91],a[196:199],v[12:15]\n"
MF"v[4:7],v[80:83],a[200:203],v[4:7]\n"
MF"v[12:15],v[88:91],a[200:203],v[12:15]\n"
MF"v[4:7],v[80:83],a[204:207],v[4:7]\n"
MF"v[12:15],v[88:91],a[204:207],v[12:15]\n"
MF"v[4:7],v[80:83],a[208:211],v[4:7]\n"
MF"v[12:15],v[88:91],a[208:211],v[12:15]\n"
MF"v[20:23],v[80:83],a[216:219],v[20:23]\n"
MF"v[28:31],v[88:91],a[216:219],v[28:31]\n"
MF"v[20:23],v[80:83],a[220:223],v[20:23]\n"
MF"v[28:31],v[88:91],a[220:223],v[28:31]\n"
MF"v[20:23],v[80:83],a[224:227],v[20:23]\n"
MF"v[28:31],v[88:91],a[224:227],v[28:31]\n"
MF"v[20:23],v[80:83],a[228:231],v[20:23]\n"
MF"v[28:31],v[88:91],a[228:231],v[28:31]\n"
PK"v4,v4,v5\n"
PK"v5,v6,v7\n"
PK"v6,v12,v13\n"
PK"v7,v14,v15\n"
BS"v[4:7],v176,s[16:19],s38 offen\n"
MF"v[20:23],v[80:83],a[232:235],v[20:23]\n"
MF"v[28:31],v[88:91],a[232:235],v[28:31]\n"
MF"v[20:23],v[80:83],a[236:239],v[20:23]\n"
MF"v[28:31],v[88:91],a[236:239],v[28:31]\n"
MF"v[20:23],v[80:83],a[240:243],v[20:23]\n"
MF"v[28:31],v[88:91],a[240:243],v[28:31]\n"
"s_nop 7\n"
PK"v20,v20,v21\n"
PK"v21,v22,v23\n"
PK"v22,v28,v29\n"
PK"v23,v30,v31\n"
BS"v[20:23],v176,s[16:19],s55 offen\n"
"s_mov_b32 s89,-1\n"
"s_cmp_eq_u32 s95,-1\n"
"s_cbranch_scc0 L1091_%=\n"
"L1095_%=:\n"
WT"vmcnt(0)\n"
MF"v[36:39],v[80:83],a[68:71],v[36:39]\n"
MF"v[44:47],v[88:91],a[68:71],v[44:47]\n"
MF"v[36:39],v[80:83],a[72:75],v[36:39]\n"
MF"v[44:47],v[88:91],a[72:75],v[44:47]\n"
MF"v[36:39],v[80:83],a[76:79],v[36:39]\n"
MF"v[44:47],v[88:91],a[76:79],v[44:47]\n"
MF"v[36:39],v[80:83],a[80:83],v[36:39]\n"
MF"v[44:47],v[88:91],a[80:83],v[44:47]\n"
MF"v[36:39],v[80:83],a[84:87],v[36:39]\n"
MF"v[44:47],v[88:91],a[84:87],v[44:47]\n"
MF"v[36:39],v[80:83],a[88:91],v[36:39]\n"
MF"v[44:47],v[88:91],a[88:91],v[44:47]\n"
MF"v[36:39],v[80:83],a[92:95],v[36:39]\n"
MF"v[44:47],v[88:91],a[92:95],v[44:47]\n"
MF"v[52:55],v[80:83],a[100:103],v[52:55]\n"
MF"v[60:63],v[88:91],a[100:103],v[60:63]\n"
MF"v[52:55],v[80:83],a[104:107],v[52:55]\n"
MF"v[60:63],v[88:91],a[104:107],v[60:63]\n"
MF"v[52:55],v[80:83],a[108:111],v[52:55]\n"
MF"v[60:63],v[88:91],a[108:111],v[60:63]\n"
MF"v[52:55],v[80:83],a[112:115],v[52:55]\n"
MF"v[60:63],v[88:91],a[112:115],v[60:63]\n"
PK"v36,v36,v37\n"
PK"v37,v38,v39\n"
PK"v38,v44,v45\n"
PK"v39,v46,v47\n"
BS"v[36:39],v177,s[16:19],s38 offen\n"
MF"v[52:55],v[80:83],a[116:119],v[52:55]\n"
MF"v[60:63],v[88:91],a[116:119],v[60:63]\n"
MF"v[52:55],v[80:83],a[120:123],v[52:55]\n"
MF"v[60:63],v[88:91],a[120:123],v[60:63]\n"
MF"v[52:55],v[80:83],a[124:127],v[52:55]\n"
MF"v[60:63],v[88:91],a[124:127],v[60:63]\n"
"s_nop 7\n"
PK"v52,v52,v53\n"
PK"v53,v54,v55\n"
PK"v54,v60,v61\n"
PK"v55,v62,v63\n"
BS"v[52:55],v177,s[16:19],s55 offen\n"
WT"vmcnt(0) lgkmcnt(0)\n"
"s_endpgm\n"
"L1004_%=:\n"
"s_mov_b32 s54,0\n"
"L1006_%=:\n"
"s_sleep 2\n"
BL"v[160:163],v224,s[8:11],0 offen offset:256 sc1\n"
BL"v[164:167],v224,s[8:11],0 offen offset:1280 sc1\n"
WT"vmcnt(0)\n"
SA"s54,s54,1\n"
"v_cmp_eq_u64 vcc,s[28:29],v[160:161]\n"
"v_cmp_eq_u64_e64 s[62:63],s[28:29],v[162:163]\n"
"s_and_b64 vcc,vcc,s[62:63]\n"
"v_cmp_eq_u64_e64 s[62:63],s[28:29],v[164:165]\n"
"s_and_b64 vcc,vcc,s[62:63]\n"
"v_cmp_eq_u64_e64 s[62:63],s[28:29],v[166:167]\n"
"s_and_b64 vcc,vcc,s[62:63]\n"
"s_cmp_eq_u64 vcc,exec\n"
"s_cbranch_scc1 L1005_%=\n"
"s_cmp_lt_u32 s54,1048576\n"
"s_cbranch_scc1 L1006_%=\n"
"s_branch L1005_%=\n"
"L1007_%=:\n"
"s_mov_b32 s54,0\n"
"L1009_%=:\n"
"v_max3_u32 v226,v160,v161,v162\n"
"v_max3_u32 v226,v226,v163,v164\n"
"v_max3_u32 v226,v226,v165,v166\n"
"v_max3_u32 v226,v226,v167,v168\n"
"v_max3_u32 v226,v226,v169,v170\n"
"v_max3_u32 v226,v226,v171,v172\n"
"v_max3_u32 v226,v226,v173,v174\n"
"v_max3_u32 v226,v226,v175,v176\n"
"v_max3_u32 v226,v226,v177,v178\n"
"v_max3_u32 v226,v226,v179,v180\n"
"v_max3_u32 v226,v226,v181,v182\n"
"v_max3_u32 v226,v226,v183,v184\n"
"v_max3_u32 v226,v226,v185,v186\n"
"v_max3_u32 v226,v226,v187,v188\n"
"v_max3_u32 v226,v226,v189,v190\n"
"v_max_u32 v226,v226,v191\n"
"v_mov_b32 v229,v226\n"
"s_nop 1\n"
"v_max_u32_dpp v229,v229,v229 row_shr:1 row_mask:0xf bank_mask:0xf\n"
"s_nop 1\n"
"v_max_u32_dpp v229,v229,v229 row_shr:2 row_mask:0xf bank_mask:0xf\n"
"s_nop 1\n"
"v_max_u32_dpp v229,v229,v229 row_shr:4 row_mask:0xf bank_mask:0xf\n"
"s_nop 1\n"
"v_max_u32_dpp v229,v229,v229 row_shr:8 row_mask:0xf bank_mask:0xf\n"
"s_nop 1\n"
"v_max_u32_dpp v229,v229,v229 row_bcast:15 row_mask:0xa bank_mask:0xf\n"
"s_nop 1\n"
"v_max_u32_dpp v229,v229,v229 row_bcast:31 row_mask:0xc bank_mask:0xf\n"
"s_nop 1\n"
"v_readlane_b32 s62,v229,63\n"
"s_nop 1\n"
"v_cmp_eq_u32 vcc,s62,v160\n"
"s_nop 1\n"
"v_cndmask_b32_e64 v160,v160,0,vcc\n"
"v_cmp_eq_u32 vcc,s62,v161\n"
"s_nop 1\n"
"v_cndmask_b32_e64 v161,v161,0,vcc\n"
"v_cmp_eq_u32 vcc,s62,v162\n"
"s_nop 1\n"
"v_cndmask_b32_e64 v162,v162,0,vcc\n"
"v_cmp_eq_u32 vcc,s62,v163\n"
"s_nop 1\n"
"v_cndmask_b32_e64 v163,v163,0,vcc\n"
"v_cmp_eq_u32 vcc,s62,v164\n"
"s_nop 1\n"
"v_cndmask_b32_e64 v164,v164,0,vcc\n"
"v_cmp_eq_u32 vcc,s62,v165\n"
"s_nop 1\n"
"v_cndmask_b32_e64 v165,v165,0,vcc\n"
"v_cmp_eq_u32 vcc,s62,v166\n"
"s_nop 1\n"
"v_cndmask_b32_e64 v166,v166,0,vcc\n"
"v_cmp_eq_u32 vcc,s62,v167\n"
"s_nop 1\n"
"v_cndmask_b32_e64 v167,v167,0,vcc\n"
"v_cmp_eq_u32 vcc,s62,v168\n"
"s_nop 1\n"
"v_cndmask_b32_e64 v168,v168,0,vcc\n"
"v_cmp_eq_u32 vcc,s62,v169\n"
"s_nop 1\n"
"v_cndmask_b32_e64 v169,v169,0,vcc\n"
"v_cmp_eq_u32 vcc,s62,v170\n"
"s_nop 1\n"
"v_cndmask_b32_e64 v170,v170,0,vcc\n"
"v_cmp_eq_u32 vcc,s62,v171\n"
"s_nop 1\n"
"v_cndmask_b32_e64 v171,v171,0,vcc\n"
"v_cmp_eq_u32 vcc,s62,v172\n"
"s_nop 1\n"
"v_cndmask_b32_e64 v172,v172,0,vcc\n"
"v_cmp_eq_u32 vcc,s62,v173\n"
"s_nop 1\n"
"v_cndmask_b32_e64 v173,v173,0,vcc\n"
"v_cmp_eq_u32 vcc,s62,v174\n"
"s_nop 1\n"
"v_cndmask_b32_e64 v174,v174,0,vcc\n"
"v_cmp_eq_u32 vcc,s62,v175\n"
"s_nop 1\n"
"v_cndmask_b32_e64 v175,v175,0,vcc\n"
"v_cmp_eq_u32 vcc,s62,v176\n"
"s_nop 1\n"
"v_cndmask_b32_e64 v176,v176,0,vcc\n"
"v_cmp_eq_u32 vcc,s62,v177\n"
"s_nop 1\n"
"v_cndmask_b32_e64 v177,v177,0,vcc\n"
"v_cmp_eq_u32 vcc,s62,v178\n"
"s_nop 1\n"
"v_cndmask_b32_e64 v178,v178,0,vcc\n"
"v_cmp_eq_u32 vcc,s62,v179\n"
"s_nop 1\n"
"v_cndmask_b32_e64 v179,v179,0,vcc\n"
"v_cmp_eq_u32 vcc,s62,v180\n"
"s_nop 1\n"
"v_cndmask_b32_e64 v180,v180,0,vcc\n"
"v_cmp_eq_u32 vcc,s62,v181\n"
"s_nop 1\n"
"v_cndmask_b32_e64 v181,v181,0,vcc\n"
"v_cmp_eq_u32 vcc,s62,v182\n"
"s_nop 1\n"
"v_cndmask_b32_e64 v182,v182,0,vcc\n"
"v_cmp_eq_u32 vcc,s62,v183\n"
"s_nop 1\n"
"v_cndmask_b32_e64 v183,v183,0,vcc\n"
"v_cmp_eq_u32 vcc,s62,v184\n"
"s_nop 1\n"
"v_cndmask_b32_e64 v184,v184,0,vcc\n"
"v_cmp_eq_u32 vcc,s62,v185\n"
"s_nop 1\n"
"v_cndmask_b32_e64 v185,v185,0,vcc\n"
"v_cmp_eq_u32 vcc,s62,v186\n"
"s_nop 1\n"
"v_cndmask_b32_e64 v186,v186,0,vcc\n"
"v_cmp_eq_u32 vcc,s62,v187\n"
"s_nop 1\n"
"v_cndmask_b32_e64 v187,v187,0,vcc\n"
"v_cmp_eq_u32 vcc,s62,v188\n"
"s_nop 1\n"
"v_cndmask_b32_e64 v188,v188,0,vcc\n"
"v_cmp_eq_u32 vcc,s62,v189\n"
"s_nop 1\n"
"v_cndmask_b32_e64 v189,v189,0,vcc\n"
"v_cmp_eq_u32 vcc,s62,v190\n"
"s_nop 1\n"
"v_cndmask_b32_e64 v190,v190,0,vcc\n"
"v_cmp_eq_u32 vcc,s62,v191\n"
"s_nop 1\n"
"v_cndmask_b32_e64 v191,v191,0,vcc\n"
"v_mov_b32 v229,s62\n"
"s_lshl_b64 exec,1,s54\n"
"v_mov_b32 v225,v229\n"
"s_mov_b64 exec,-1\n"
SA"s54,s54,1\n"
"s_cmp_lt_u32 s54,8\n"
"s_cbranch_scc1 L1009_%=\n"
"s_nop 4\n"
"s_branch L1008_%=\n"
"L1010_%=:\n"
"s_mov_b32 s54,0\n"
"L1012_%=:\n"
"s_sleep 2\n"
"buffer_load_dwordx2 v[208:209],v252,s[8:11],s50 offen sc1\n"
WT"vmcnt(0)\n"
SA"s54,s54,1\n"
"v_cmp_eq_u64 vcc,s[28:29],v[208:209]\n"
"s_cmp_eq_u64 vcc,exec\n"
"s_cbranch_scc1 L1011_%=\n"
"s_cmp_lt_u32 s54,1048576\n"
"s_cbranch_scc1 L1012_%=\n"
"s_branch L1011_%=\n"
"L1014_%=:\n"
"s_nop 15\n"
WT"vmcnt(0)\n"
"s_lshl_b32 s89,s34,3\n"
SA"s89,s89,s30\n"
SA"s89,s89,0\n"
"s_lshl_b32 s91,s89,4\n"
"s_lshr_b32 s89,s89,1\n"
"v_bfe_u32 v249,v224,4,4\n"
"v_lshrrev_b32 v254,8,v224\n"
"v_lshlrev_b32 v254,2,v254\n"
"v_sub_u32 v249,v249,v254\n"
"v_add_u32 v249,s91,v249\n"
"v_mov_b32 v255,0xff800000\n"
"v_mov_b32 v248,v255\n"
"s_mov_b32 s90,0\n"
"s_mov_b32 s88,0\n"
"v_mov_b32 v80,0\n"
"v_mov_b32 v81,0\n"
"v_mov_b32 v82,0\n"
"v_mov_b32 v83,0\n"
"v_mov_b32 v84,0\n"
"v_mov_b32 v85,0\n"
"v_mov_b32 v86,0\n"
"v_mov_b32 v87,0\n"
"v_mov_b32 v88,0\n"
"v_mov_b32 v89,0\n"
"v_mov_b32 v90,0\n"
"v_mov_b32 v91,0\n"
"v_mov_b32 v92,0\n"
"v_mov_b32 v93,0\n"
"v_mov_b32 v94,0\n"
"v_mov_b32 v95,0\n"
"v_mov_b32 v96,0\n"
"v_mov_b32 v97,0\n"
"v_mov_b32 v98,0\n"
"v_mov_b32 v99,0\n"
"v_mov_b32 v100,0\n"
"v_mov_b32 v101,0\n"
"v_mov_b32 v102,0\n"
"v_mov_b32 v103,0\n"
"v_mov_b32 v104,0\n"
"v_mov_b32 v105,0\n"
"v_mov_b32 v106,0\n"
"v_mov_b32 v107,0\n"
"v_mov_b32 v108,0\n"
"v_mov_b32 v109,0\n"
"v_mov_b32 v110,0\n"
"v_mov_b32 v111,0\n"
"v_accvgpr_write_b32 a132,0\n"
"v_accvgpr_write_b32 a133,0\n"
"v_accvgpr_write_b32 a134,0\n"
"v_accvgpr_write_b32 a135,0\n"
"L1015_%=:\n"
"s_lshl_b32 s91,s88,13\n"
SA"s91,s91,s39\n"
SA"s91,s91,0\n"
BL"a[148:151],v224,s[8:11],s91 offen offset:0 sc1\n"
BL"a[152:155],v224,s[8:11],s91 offen offset:1024 sc1\n"
BL"a[156:159],v224,s[8:11],s91 offen offset:2048 sc1\n"
BL"a[160:163],v224,s[8:11],s91 offen offset:3072 sc1\n"
SA"s91,s91,4096\n"
BL"a[164:167],v224,s[8:11],s91 offen offset:0 sc1\n"
BL"a[168:171],v224,s[8:11],s91 offen offset:1024 sc1\n"
BL"a[172:175],v224,s[8:11],s91 offen offset:2048 sc1\n"
BL"a[176:179],v224,s[8:11],s91 offen offset:3072 sc1\n"
WT"vmcnt(0)\n"
MF"v[240:243],a[148:151],a[0:3],0\n"
MF"v[240:243],a[152:155],a[4:7],v[240:243]\n"
MF"v[240:243],a[156:159],a[8:11],v[240:243]\n"
MF"v[240:243],a[160:163],a[12:15],v[240:243]\n"
MF"v[244:247],a[164:167],a[0:3],0\n"
MF"v[244:247],a[168:171],a[4:7],v[244:247]\n"
MF"v[244:247],a[172:175],a[8:11],v[244:247]\n"
MF"v[244:247],a[176:179],a[12:15],v[244:247]\n"
"s_lshl_b32 s91,s88,5\n"
"v_subrev_u32 v250,s91,v249\n"
"s_nop 7\n"
"v_cmp_gt_i32 vcc,0,v250\n"
"s_nop 1\n"
"v_cndmask_b32 v240,v240,v255,vcc\n"
"v_cmp_gt_i32 vcc,1,v250\n"
"s_nop 1\n"
"v_cndmask_b32 v241,v241,v255,vcc\n"
"v_cmp_gt_i32 vcc,2,v250\n"
"s_nop 1\n"
"v_cndmask_b32 v242,v242,v255,vcc\n"
"v_cmp_gt_i32 vcc,3,v250\n"
"s_nop 1\n"
"v_cndmask_b32 v243,v243,v255,vcc\n"
"v_cmp_gt_i32 vcc,16,v250\n"
"s_nop 1\n"
"v_cndmask_b32 v244,v244,v255,vcc\n"
"v_cmp_gt_i32 vcc,17,v250\n"
"s_nop 1\n"
"v_cndmask_b32 v245,v245,v255,vcc\n"
"v_cmp_gt_i32 vcc,18,v250\n"
"s_nop 1\n"
"v_cndmask_b32 v246,v246,v255,vcc\n"
"v_cmp_gt_i32 vcc,19,v250\n"
"s_nop 1\n"
"v_cndmask_b32 v247,v247,v255,vcc\n"
"s_cmp_lg_u32 s90,0\n"
"s_cbranch_scc1 L1016_%=\n"
"v_max3_f32 v248,v248,v240,v241\n"
"v_max3_f32 v248,v248,v242,v243\n"
"v_max3_f32 v248,v248,v244,v245\n"
"v_max3_f32 v248,v248,v246,v247\n"
"s_branch L1017_%=\n"
"L1016_%=:\n"
"s_lshl_b32 s91,s88,13\n"
SA"s91,s91,s39\n"
SA"s91,s91,524288\n"
BL"a[148:151],v224,s[8:11],s91 offen offset:0 sc1\n"
BL"a[152:155],v224,s[8:11],s91 offen offset:1024 sc1\n"
BL"a[156:159],v224,s[8:11],s91 offen offset:2048 sc1\n"
BL"a[160:163],v224,s[8:11],s91 offen offset:3072 sc1\n"
SA"s91,s91,4096\n"
BL"a[164:167],v224,s[8:11],s91 offen offset:0 sc1\n"
BL"a[168:171],v224,s[8:11],s91 offen offset:1024 sc1\n"
BL"a[172:175],v224,s[8:11],s91 offen offset:2048 sc1\n"
BL"a[176:179],v224,s[8:11],s91 offen offset:3072 sc1\n"
WT"vmcnt(0)\n"
"v_sub_f32 v240,v240,v248\n"
"v_sub_f32 v241,v241,v248\n"
"v_sub_f32 v242,v242,v248\n"
"v_sub_f32 v243,v243,v248\n"
"v_sub_f32 v244,v244,v248\n"
"v_sub_f32 v245,v245,v248\n"
"v_sub_f32 v246,v246,v248\n"
"v_sub_f32 v247,v247,v248\n"
EX"v240,v240\n"
EX"v241,v241\n"
EX"v242,v242\n"
EX"v243,v243\n"
EX"v244,v244\n"
EX"v245,v245\n"
EX"v246,v246\n"
EX"v247,v247\n"
"s_nop 0\n"
PK"v240,v240,v241\n"
PK"v241,v242,v243\n"
PK"v242,v244,v245\n"
PK"v243,v246,v247\n"
"s_nop 1\n"
MF"v[80:83],a[148:151],v[240:243],v[80:83]\n"
MF"v[84:87],a[152:155],v[240:243],v[84:87]\n"
MF"v[88:91],a[156:159],v[240:243],v[88:91]\n"
MF"v[92:95],a[160:163],v[240:243],v[92:95]\n"
MF"v[96:99],a[164:167],v[240:243],v[96:99]\n"
MF"v[100:103],a[168:171],v[240:243],v[100:103]\n"
MF"v[104:107],a[172:175],v[240:243],v[104:107]\n"
MF"v[108:111],a[176:179],v[240:243],v[108:111]\n"
MF"a[132:135],a[128:131],v[240:243],a[132:135]\n"
"s_nop 7\n"
"L1017_%=:\n"
SA"s88,s88,1\n"
"s_cmp_le_u32 s88,s89\n"
"s_cbranch_scc1 L1015_%=\n"
"s_cmp_lg_u32 s90,0\n"
"s_cbranch_scc1 L1018_%=\n"
"v_mov_b32 v254,v248\n"
"s_nop 1\n"
"v_permlane16_swap_b32 v254,v248\n"
"s_nop 1\n"
"v_max_f32 v248,v248,v254\n"
"v_mov_b32 v254,v248\n"
"s_nop 1\n"
"v_permlane32_swap_b32 v254,v248\n"
"s_nop 1\n"
"v_max_f32 v248,v248,v254\n"
"s_mov_b32 s90,1\n"
"s_mov_b32 s88,0\n"
"s_branch L1015_%=\n"
"L1018_%=:\n"
"s_nop 7\n"
"v_accvgpr_read_b32 v251,a132\n"
"s_nop 1\n"
"s_branch L1013_%=\n"
"L1019_%=:\n"
"s_mov_b32 s54,0\n"
"L1021_%=:\n"
"s_sleep 2\n"
"buffer_load_dwordx2 v[238:239],v237,s[8:11],s50 offen sc1\n"
WT"vmcnt(0)\n"
SA"s54,s54,1\n"
"v_cmp_eq_u64 vcc,s[28:29],v[238:239]\n"
"s_cmp_eq_u64 vcc,exec\n"
"s_cbranch_scc1 L1020_%=\n"
"s_cmp_lt_u32 s54,1048576\n"
"s_cbranch_scc1 L1021_%=\n"
"s_branch L1020_%=\n"
"L1023_%=:\n"
"s_nop 15\n"
WT"vmcnt(0)\n"
"s_lshl_b32 s89,s34,3\n"
SA"s89,s89,s30\n"
SA"s89,s89,4\n"
"s_lshl_b32 s91,s89,4\n"
"s_lshr_b32 s89,s89,1\n"
"v_bfe_u32 v249,v224,4,4\n"
"v_lshrrev_b32 v254,8,v224\n"
"v_lshlrev_b32 v254,2,v254\n"
"v_sub_u32 v249,v249,v254\n"
"v_add_u32 v249,s91,v249\n"
"v_mov_b32 v255,0xff800000\n"
"v_mov_b32 v248,v255\n"
"s_mov_b32 s90,0\n"
"s_mov_b32 s88,0\n"
"v_mov_b32 v112,0\n"
"v_mov_b32 v113,0\n"
"v_mov_b32 v114,0\n"
"v_mov_b32 v115,0\n"
"v_mov_b32 v116,0\n"
"v_mov_b32 v117,0\n"
"v_mov_b32 v118,0\n"
"v_mov_b32 v119,0\n"
"v_mov_b32 v120,0\n"
"v_mov_b32 v121,0\n"
"v_mov_b32 v122,0\n"
"v_mov_b32 v123,0\n"
"v_mov_b32 v124,0\n"
"v_mov_b32 v125,0\n"
"v_mov_b32 v126,0\n"
"v_mov_b32 v127,0\n"
"v_mov_b32 v128,0\n"
"v_mov_b32 v129,0\n"
"v_mov_b32 v130,0\n"
"v_mov_b32 v131,0\n"
"v_mov_b32 v132,0\n"
"v_mov_b32 v133,0\n"
"v_mov_b32 v134,0\n"
"v_mov_b32 v135,0\n"
"v_mov_b32 v136,0\n"
"v_mov_b32 v137,0\n"
"v_mov_b32 v138,0\n"
"v_mov_b32 v139,0\n"
"v_mov_b32 v140,0\n"
"v_mov_b32 v141,0\n"
"v_mov_b32 v142,0\n"
"v_mov_b32 v143,0\n"
"v_accvgpr_write_b32 a136,0\n"
"v_accvgpr_write_b32 a137,0\n"
"v_accvgpr_write_b32 a138,0\n"
"v_accvgpr_write_b32 a139,0\n"
"L1024_%=:\n"
"s_lshl_b32 s91,s88,13\n"
SA"s91,s91,s39\n"
SA"s91,s91,0\n"
BL"a[148:151],v224,s[8:11],s91 offen offset:0 sc1\n"
BL"a[152:155],v224,s[8:11],s91 offen offset:1024 sc1\n"
BL"a[156:159],v224,s[8:11],s91 offen offset:2048 sc1\n"
BL"a[160:163],v224,s[8:11],s91 offen offset:3072 sc1\n"
SA"s91,s91,4096\n"
BL"a[164:167],v224,s[8:11],s91 offen offset:0 sc1\n"
BL"a[168:171],v224,s[8:11],s91 offen offset:1024 sc1\n"
BL"a[172:175],v224,s[8:11],s91 offen offset:2048 sc1\n"
BL"a[176:179],v224,s[8:11],s91 offen offset:3072 sc1\n"
WT"vmcnt(0)\n"
MF"v[240:243],a[148:151],a[16:19],0\n"
MF"v[240:243],a[152:155],a[20:23],v[240:243]\n"
MF"v[240:243],a[156:159],a[24:27],v[240:243]\n"
MF"v[240:243],a[160:163],a[28:31],v[240:243]\n"
MF"v[244:247],a[164:167],a[16:19],0\n"
MF"v[244:247],a[168:171],a[20:23],v[244:247]\n"
MF"v[244:247],a[172:175],a[24:27],v[244:247]\n"
MF"v[244:247],a[176:179],a[28:31],v[244:247]\n"
"s_lshl_b32 s91,s88,5\n"
"v_subrev_u32 v250,s91,v249\n"
"s_nop 7\n"
"v_cmp_gt_i32 vcc,0,v250\n"
"s_nop 1\n"
"v_cndmask_b32 v240,v240,v255,vcc\n"
"v_cmp_gt_i32 vcc,1,v250\n"
"s_nop 1\n"
"v_cndmask_b32 v241,v241,v255,vcc\n"
"v_cmp_gt_i32 vcc,2,v250\n"
"s_nop 1\n"
"v_cndmask_b32 v242,v242,v255,vcc\n"
"v_cmp_gt_i32 vcc,3,v250\n"
"s_nop 1\n"
"v_cndmask_b32 v243,v243,v255,vcc\n"
"v_cmp_gt_i32 vcc,16,v250\n"
"s_nop 1\n"
"v_cndmask_b32 v244,v244,v255,vcc\n"
"v_cmp_gt_i32 vcc,17,v250\n"
"s_nop 1\n"
"v_cndmask_b32 v245,v245,v255,vcc\n"
"v_cmp_gt_i32 vcc,18,v250\n"
"s_nop 1\n"
"v_cndmask_b32 v246,v246,v255,vcc\n"
"v_cmp_gt_i32 vcc,19,v250\n"
"s_nop 1\n"
"v_cndmask_b32 v247,v247,v255,vcc\n"
"s_cmp_lg_u32 s90,0\n"
"s_cbranch_scc1 L1025_%=\n"
"v_max3_f32 v248,v248,v240,v241\n"
"v_max3_f32 v248,v248,v242,v243\n"
"v_max3_f32 v248,v248,v244,v245\n"
"v_max3_f32 v248,v248,v246,v247\n"
"s_branch L1026_%=\n"
"L1025_%=:\n"
"s_lshl_b32 s91,s88,13\n"
SA"s91,s91,s39\n"
SA"s91,s91,524288\n"
BL"a[148:151],v224,s[8:11],s91 offen offset:0 sc1\n"
BL"a[152:155],v224,s[8:11],s91 offen offset:1024 sc1\n"
BL"a[156:159],v224,s[8:11],s91 offen offset:2048 sc1\n"
BL"a[160:163],v224,s[8:11],s91 offen offset:3072 sc1\n"
SA"s91,s91,4096\n"
BL"a[164:167],v224,s[8:11],s91 offen offset:0 sc1\n"
BL"a[168:171],v224,s[8:11],s91 offen offset:1024 sc1\n"
BL"a[172:175],v224,s[8:11],s91 offen offset:2048 sc1\n"
BL"a[176:179],v224,s[8:11],s91 offen offset:3072 sc1\n"
WT"vmcnt(0)\n"
"v_sub_f32 v240,v240,v248\n"
"v_sub_f32 v241,v241,v248\n"
"v_sub_f32 v242,v242,v248\n"
"v_sub_f32 v243,v243,v248\n"
"v_sub_f32 v244,v244,v248\n"
"v_sub_f32 v245,v245,v248\n"
"v_sub_f32 v246,v246,v248\n"
"v_sub_f32 v247,v247,v248\n"
EX"v240,v240\n"
EX"v241,v241\n"
EX"v242,v242\n"
EX"v243,v243\n"
EX"v244,v244\n"
EX"v245,v245\n"
EX"v246,v246\n"
EX"v247,v247\n"
"s_nop 0\n"
PK"v240,v240,v241\n"
PK"v241,v242,v243\n"
PK"v242,v244,v245\n"
PK"v243,v246,v247\n"
"s_nop 1\n"
MF"v[112:115],a[148:151],v[240:243],v[112:115]\n"
MF"v[116:119],a[152:155],v[240:243],v[116:119]\n"
MF"v[120:123],a[156:159],v[240:243],v[120:123]\n"
MF"v[124:127],a[160:163],v[240:243],v[124:127]\n"
MF"v[128:131],a[164:167],v[240:243],v[128:131]\n"
MF"v[132:135],a[168:171],v[240:243],v[132:135]\n"
MF"v[136:139],a[172:175],v[240:243],v[136:139]\n"
MF"v[140:143],a[176:179],v[240:243],v[140:143]\n"
MF"a[136:139],a[128:131],v[240:243],a[136:139]\n"
"s_nop 7\n"
"L1026_%=:\n"
SA"s88,s88,1\n"
"s_cmp_le_u32 s88,s89\n"
"s_cbranch_scc1 L1024_%=\n"
"s_cmp_lg_u32 s90,0\n"
"s_cbranch_scc1 L1027_%=\n"
"v_mov_b32 v254,v248\n"
"s_nop 1\n"
"v_permlane16_swap_b32 v254,v248\n"
"s_nop 1\n"
"v_max_f32 v248,v248,v254\n"
"v_mov_b32 v254,v248\n"
"s_nop 1\n"
"v_permlane32_swap_b32 v254,v248\n"
"s_nop 1\n"
"v_max_f32 v248,v248,v254\n"
"s_mov_b32 s90,1\n"
"s_mov_b32 s88,0\n"
"s_branch L1024_%=\n"
"L1027_%=:\n"
"s_nop 7\n"
"v_accvgpr_read_b32 v252,a136\n"
"s_nop 1\n"
"s_branch L1022_%=\n"
"L1028_%=:\n"
"s_mov_b32 s54,0\n"
"L1030_%=:\n"
"s_sleep 2\n"
"buffer_load_dwordx2 v[240:241],v238,s[8:11],s63 offen sc1\n"
WT"vmcnt(0)\n"
SA"s54,s54,1\n"
"v_cmp_eq_u64 vcc,s[28:29],v[240:241]\n"
"s_cmp_eq_u64 vcc,exec\n"
"s_cbranch_scc1 L1029_%=\n"
"s_cmp_lt_u32 s54,1048576\n"
"s_cbranch_scc1 L1030_%=\n"
"s_branch L1029_%=\n"
"L1031_%=:\n"
"s_mov_b32 s54,0\n"
"L1033_%=:\n"
"s_sleep 2\n"
"buffer_load_dwordx2 v[242:243],off,s[8:11],s21 sc1\n"
WT"vmcnt(0)\n"
SA"s54,s54,1\n"
"v_cmp_eq_u64 vcc,s[28:29],v[242:243]\n"
"s_cmp_eq_u64 vcc,exec\n"
"s_cbranch_scc1 L1032_%=\n"
"s_cmp_lt_u32 s54,1048576\n"
"s_cbranch_scc1 L1033_%=\n"
"s_branch L1032_%=\n"
"L1035_%=:\n"
"s_nop 15\n"
WT"vmcnt(0)\n"
"s_lshl_b32 s89,s34,3\n"
SA"s89,s89,s30\n"
SA"s89,s89,4\n"
"s_lshl_b32 s91,s89,4\n"
"s_lshr_b32 s89,s89,1\n"
"v_bfe_u32 v249,v224,4,4\n"
"v_lshrrev_b32 v254,8,v224\n"
"v_lshlrev_b32 v254,2,v254\n"
"v_sub_u32 v249,v249,v254\n"
"v_add_u32 v249,s91,v249\n"
"v_mov_b32 v255,0xff800000\n"
"v_mov_b32 v248,v255\n"
"s_mov_b32 s90,0\n"
"s_mov_b32 s88,0\n"
"v_mov_b32 v112,0\n"
"v_mov_b32 v113,0\n"
"v_mov_b32 v114,0\n"
"v_mov_b32 v115,0\n"
"v_mov_b32 v116,0\n"
"v_mov_b32 v117,0\n"
"v_mov_b32 v118,0\n"
"v_mov_b32 v119,0\n"
"v_mov_b32 v120,0\n"
"v_mov_b32 v121,0\n"
"v_mov_b32 v122,0\n"
"v_mov_b32 v123,0\n"
"v_mov_b32 v124,0\n"
"v_mov_b32 v125,0\n"
"v_mov_b32 v126,0\n"
"v_mov_b32 v127,0\n"
"v_mov_b32 v128,0\n"
"v_mov_b32 v129,0\n"
"v_mov_b32 v130,0\n"
"v_mov_b32 v131,0\n"
"v_mov_b32 v132,0\n"
"v_mov_b32 v133,0\n"
"v_mov_b32 v134,0\n"
"v_mov_b32 v135,0\n"
"v_mov_b32 v136,0\n"
"v_mov_b32 v137,0\n"
"v_mov_b32 v138,0\n"
"v_mov_b32 v139,0\n"
"v_mov_b32 v140,0\n"
"v_mov_b32 v141,0\n"
"v_mov_b32 v142,0\n"
"v_mov_b32 v143,0\n"
"v_accvgpr_write_b32 a136,0\n"
"v_accvgpr_write_b32 a137,0\n"
"v_accvgpr_write_b32 a138,0\n"
"v_accvgpr_write_b32 a139,0\n"
"L1036_%=:\n"
"s_lshl_b32 s91,s88,13\n"
SA"s91,s91,s39\n"
SA"s91,s91,0\n"
BL"a[148:151],v224,s[8:11],s91 offen offset:0 sc1\n"
BL"a[152:155],v224,s[8:11],s91 offen offset:1024 sc1\n"
BL"a[156:159],v224,s[8:11],s91 offen offset:2048 sc1\n"
BL"a[160:163],v224,s[8:11],s91 offen offset:3072 sc1\n"
SA"s91,s91,4096\n"
BL"a[164:167],v224,s[8:11],s91 offen offset:0 sc1\n"
BL"a[168:171],v224,s[8:11],s91 offen offset:1024 sc1\n"
BL"a[172:175],v224,s[8:11],s91 offen offset:2048 sc1\n"
BL"a[176:179],v224,s[8:11],s91 offen offset:3072 sc1\n"
WT"vmcnt(0)\n"
MF"v[240:243],a[148:151],a[16:19],0\n"
MF"v[240:243],a[152:155],a[20:23],v[240:243]\n"
MF"v[240:243],a[156:159],a[24:27],v[240:243]\n"
MF"v[240:243],a[160:163],a[28:31],v[240:243]\n"
MF"v[244:247],a[164:167],a[16:19],0\n"
MF"v[244:247],a[168:171],a[20:23],v[244:247]\n"
MF"v[244:247],a[172:175],a[24:27],v[244:247]\n"
MF"v[244:247],a[176:179],a[28:31],v[244:247]\n"
"s_lshl_b32 s91,s88,5\n"
"v_subrev_u32 v250,s91,v249\n"
"s_nop 7\n"
"v_cmp_gt_i32 vcc,0,v250\n"
"s_nop 1\n"
"v_cndmask_b32 v240,v240,v255,vcc\n"
"v_cmp_gt_i32 vcc,1,v250\n"
"s_nop 1\n"
"v_cndmask_b32 v241,v241,v255,vcc\n"
"v_cmp_gt_i32 vcc,2,v250\n"
"s_nop 1\n"
"v_cndmask_b32 v242,v242,v255,vcc\n"
"v_cmp_gt_i32 vcc,3,v250\n"
"s_nop 1\n"
"v_cndmask_b32 v243,v243,v255,vcc\n"
"v_cmp_gt_i32 vcc,16,v250\n"
"s_nop 1\n"
"v_cndmask_b32 v244,v244,v255,vcc\n"
"v_cmp_gt_i32 vcc,17,v250\n"
"s_nop 1\n"
"v_cndmask_b32 v245,v245,v255,vcc\n"
"v_cmp_gt_i32 vcc,18,v250\n"
"s_nop 1\n"
"v_cndmask_b32 v246,v246,v255,vcc\n"
"v_cmp_gt_i32 vcc,19,v250\n"
"s_nop 1\n"
"v_cndmask_b32 v247,v247,v255,vcc\n"
"s_cmp_lg_u32 s90,0\n"
"s_cbranch_scc1 L1037_%=\n"
"v_max3_f32 v248,v248,v240,v241\n"
"v_max3_f32 v248,v248,v242,v243\n"
"v_max3_f32 v248,v248,v244,v245\n"
"v_max3_f32 v248,v248,v246,v247\n"
"s_branch L1038_%=\n"
"L1037_%=:\n"
"s_lshl_b32 s91,s88,13\n"
SA"s91,s91,s39\n"
SA"s91,s91,524288\n"
BL"a[148:151],v224,s[8:11],s91 offen offset:0 sc1\n"
BL"a[152:155],v224,s[8:11],s91 offen offset:1024 sc1\n"
BL"a[156:159],v224,s[8:11],s91 offen offset:2048 sc1\n"
BL"a[160:163],v224,s[8:11],s91 offen offset:3072 sc1\n"
SA"s91,s91,4096\n"
BL"a[164:167],v224,s[8:11],s91 offen offset:0 sc1\n"
BL"a[168:171],v224,s[8:11],s91 offen offset:1024 sc1\n"
BL"a[172:175],v224,s[8:11],s91 offen offset:2048 sc1\n"
BL"a[176:179],v224,s[8:11],s91 offen offset:3072 sc1\n"
WT"vmcnt(0)\n"
"v_sub_f32 v240,v240,v248\n"
"v_sub_f32 v241,v241,v248\n"
"v_sub_f32 v242,v242,v248\n"
"v_sub_f32 v243,v243,v248\n"
"v_sub_f32 v244,v244,v248\n"
"v_sub_f32 v245,v245,v248\n"
"v_sub_f32 v246,v246,v248\n"
"v_sub_f32 v247,v247,v248\n"
EX"v240,v240\n"
EX"v241,v241\n"
EX"v242,v242\n"
EX"v243,v243\n"
EX"v244,v244\n"
EX"v245,v245\n"
EX"v246,v246\n"
EX"v247,v247\n"
"s_nop 0\n"
PK"v240,v240,v241\n"
PK"v241,v242,v243\n"
PK"v242,v244,v245\n"
PK"v243,v246,v247\n"
"s_nop 1\n"
MF"v[112:115],a[148:151],v[240:243],v[112:115]\n"
MF"v[116:119],a[152:155],v[240:243],v[116:119]\n"
MF"v[120:123],a[156:159],v[240:243],v[120:123]\n"
MF"v[124:127],a[160:163],v[240:243],v[124:127]\n"
MF"v[128:131],a[164:167],v[240:243],v[128:131]\n"
MF"v[132:135],a[168:171],v[240:243],v[132:135]\n"
MF"v[136:139],a[172:175],v[240:243],v[136:139]\n"
MF"v[140:143],a[176:179],v[240:243],v[140:143]\n"
MF"a[136:139],a[128:131],v[240:243],a[136:139]\n"
"s_nop 7\n"
"L1038_%=:\n"
SA"s88,s88,1\n"
"s_cmp_le_u32 s88,s89\n"
"s_cbranch_scc1 L1036_%=\n"
"s_cmp_lg_u32 s90,0\n"
"s_cbranch_scc1 L1039_%=\n"
"v_mov_b32 v254,v248\n"
"s_nop 1\n"
"v_permlane16_swap_b32 v254,v248\n"
"s_nop 1\n"
"v_max_f32 v248,v248,v254\n"
"v_mov_b32 v254,v248\n"
"s_nop 1\n"
"v_permlane32_swap_b32 v254,v248\n"
"s_nop 1\n"
"v_max_f32 v248,v248,v254\n"
"s_mov_b32 s90,1\n"
"s_mov_b32 s88,0\n"
"s_branch L1036_%=\n"
"L1039_%=:\n"
"s_nop 7\n"
"v_accvgpr_read_b32 v252,a136\n"
"s_nop 1\n"
"s_branch L1034_%=\n"
"L1041_%=:\n"
"s_nop 15\n"
WT"vmcnt(0)\n"
"s_lshl_b32 s89,s34,3\n"
"s_sub_u32 s89,120,s89\n"
SA"s89,s89,s30\n"
SA"s89,s89,0\n"
"s_lshl_b32 s91,s89,4\n"
"s_lshr_b32 s89,s89,1\n"
"v_bfe_u32 v249,v224,4,4\n"
"v_lshrrev_b32 v254,8,v224\n"
"v_lshlrev_b32 v254,2,v254\n"
"v_sub_u32 v249,v249,v254\n"
"v_add_u32 v249,s91,v249\n"
"v_mov_b32 v255,0xff800000\n"
"v_mov_b32 v248,v255\n"
"s_mov_b32 s90,0\n"
"s_mov_b32 s88,0\n"
"v_mov_b32 v144,0\n"
"v_mov_b32 v145,0\n"
"v_mov_b32 v146,0\n"
"v_mov_b32 v147,0\n"
"v_mov_b32 v148,0\n"
"v_mov_b32 v149,0\n"
"v_mov_b32 v150,0\n"
"v_mov_b32 v151,0\n"
"v_mov_b32 v152,0\n"
"v_mov_b32 v153,0\n"
"v_mov_b32 v154,0\n"
"v_mov_b32 v155,0\n"
"v_mov_b32 v156,0\n"
"v_mov_b32 v157,0\n"
"v_mov_b32 v158,0\n"
"v_mov_b32 v159,0\n"
"v_mov_b32 v160,0\n"
"v_mov_b32 v161,0\n"
"v_mov_b32 v162,0\n"
"v_mov_b32 v163,0\n"
"v_mov_b32 v164,0\n"
"v_mov_b32 v165,0\n"
"v_mov_b32 v166,0\n"
"v_mov_b32 v167,0\n"
"v_mov_b32 v168,0\n"
"v_mov_b32 v169,0\n"
"v_mov_b32 v170,0\n"
"v_mov_b32 v171,0\n"
"v_mov_b32 v172,0\n"
"v_mov_b32 v173,0\n"
"v_mov_b32 v174,0\n"
"v_mov_b32 v175,0\n"
"v_accvgpr_write_b32 a140,0\n"
"v_accvgpr_write_b32 a141,0\n"
"v_accvgpr_write_b32 a142,0\n"
"v_accvgpr_write_b32 a143,0\n"
"L1042_%=:\n"
"s_lshl_b32 s91,s88,13\n"
SA"s91,s91,s39\n"
SA"s91,s91,0\n"
BL"a[148:151],v224,s[8:11],s91 offen offset:0 sc1\n"
BL"a[152:155],v224,s[8:11],s91 offen offset:1024 sc1\n"
BL"a[156:159],v224,s[8:11],s91 offen offset:2048 sc1\n"
BL"a[160:163],v224,s[8:11],s91 offen offset:3072 sc1\n"
SA"s91,s91,4096\n"
BL"a[164:167],v224,s[8:11],s91 offen offset:0 sc1\n"
BL"a[168:171],v224,s[8:11],s91 offen offset:1024 sc1\n"
BL"a[172:175],v224,s[8:11],s91 offen offset:2048 sc1\n"
BL"a[176:179],v224,s[8:11],s91 offen offset:3072 sc1\n"
WT"vmcnt(0)\n"
MF"v[240:243],a[148:151],a[32:35],0\n"
MF"v[240:243],a[152:155],a[36:39],v[240:243]\n"
MF"v[240:243],a[156:159],a[40:43],v[240:243]\n"
MF"v[240:243],a[160:163],a[44:47],v[240:243]\n"
MF"v[244:247],a[164:167],a[32:35],0\n"
MF"v[244:247],a[168:171],a[36:39],v[244:247]\n"
MF"v[244:247],a[172:175],a[40:43],v[244:247]\n"
MF"v[244:247],a[176:179],a[44:47],v[244:247]\n"
"s_lshl_b32 s91,s88,5\n"
"v_subrev_u32 v250,s91,v249\n"
"s_nop 7\n"
"v_cmp_gt_i32 vcc,0,v250\n"
"s_nop 1\n"
"v_cndmask_b32 v240,v240,v255,vcc\n"
"v_cmp_gt_i32 vcc,1,v250\n"
"s_nop 1\n"
"v_cndmask_b32 v241,v241,v255,vcc\n"
"v_cmp_gt_i32 vcc,2,v250\n"
"s_nop 1\n"
"v_cndmask_b32 v242,v242,v255,vcc\n"
"v_cmp_gt_i32 vcc,3,v250\n"
"s_nop 1\n"
"v_cndmask_b32 v243,v243,v255,vcc\n"
"v_cmp_gt_i32 vcc,16,v250\n"
"s_nop 1\n"
"v_cndmask_b32 v244,v244,v255,vcc\n"
"v_cmp_gt_i32 vcc,17,v250\n"
"s_nop 1\n"
"v_cndmask_b32 v245,v245,v255,vcc\n"
"v_cmp_gt_i32 vcc,18,v250\n"
"s_nop 1\n"
"v_cndmask_b32 v246,v246,v255,vcc\n"
"v_cmp_gt_i32 vcc,19,v250\n"
"s_nop 1\n"
"v_cndmask_b32 v247,v247,v255,vcc\n"
"s_cmp_lg_u32 s90,0\n"
"s_cbranch_scc1 L1043_%=\n"
"v_max3_f32 v248,v248,v240,v241\n"
"v_max3_f32 v248,v248,v242,v243\n"
"v_max3_f32 v248,v248,v244,v245\n"
"v_max3_f32 v248,v248,v246,v247\n"
"s_branch L1044_%=\n"
"L1043_%=:\n"
"s_lshl_b32 s91,s88,13\n"
SA"s91,s91,s39\n"
SA"s91,s91,524288\n"
BL"a[148:151],v224,s[8:11],s91 offen offset:0 sc1\n"
BL"a[152:155],v224,s[8:11],s91 offen offset:1024 sc1\n"
BL"a[156:159],v224,s[8:11],s91 offen offset:2048 sc1\n"
BL"a[160:163],v224,s[8:11],s91 offen offset:3072 sc1\n"
SA"s91,s91,4096\n"
BL"a[164:167],v224,s[8:11],s91 offen offset:0 sc1\n"
BL"a[168:171],v224,s[8:11],s91 offen offset:1024 sc1\n"
BL"a[172:175],v224,s[8:11],s91 offen offset:2048 sc1\n"
BL"a[176:179],v224,s[8:11],s91 offen offset:3072 sc1\n"
WT"vmcnt(0)\n"
"v_sub_f32 v240,v240,v248\n"
"v_sub_f32 v241,v241,v248\n"
"v_sub_f32 v242,v242,v248\n"
"v_sub_f32 v243,v243,v248\n"
"v_sub_f32 v244,v244,v248\n"
"v_sub_f32 v245,v245,v248\n"
"v_sub_f32 v246,v246,v248\n"
"v_sub_f32 v247,v247,v248\n"
EX"v240,v240\n"
EX"v241,v241\n"
EX"v242,v242\n"
EX"v243,v243\n"
EX"v244,v244\n"
EX"v245,v245\n"
EX"v246,v246\n"
EX"v247,v247\n"
"s_nop 0\n"
PK"v240,v240,v241\n"
PK"v241,v242,v243\n"
PK"v242,v244,v245\n"
PK"v243,v246,v247\n"
"s_nop 1\n"
MF"v[144:147],a[148:151],v[240:243],v[144:147]\n"
MF"v[148:151],a[152:155],v[240:243],v[148:151]\n"
MF"v[152:155],a[156:159],v[240:243],v[152:155]\n"
MF"v[156:159],a[160:163],v[240:243],v[156:159]\n"
MF"v[160:163],a[164:167],v[240:243],v[160:163]\n"
MF"v[164:167],a[168:171],v[240:243],v[164:167]\n"
MF"v[168:171],a[172:175],v[240:243],v[168:171]\n"
MF"v[172:175],a[176:179],v[240:243],v[172:175]\n"
MF"a[140:143],a[128:131],v[240:243],a[140:143]\n"
"s_nop 7\n"
"L1044_%=:\n"
SA"s88,s88,1\n"
"s_cmp_le_u32 s88,s89\n"
"s_cbranch_scc1 L1042_%=\n"
"s_cmp_lg_u32 s90,0\n"
"s_cbranch_scc1 L1045_%=\n"
"v_mov_b32 v254,v248\n"
"s_nop 1\n"
"v_permlane16_swap_b32 v254,v248\n"
"s_nop 1\n"
"v_max_f32 v248,v248,v254\n"
"v_mov_b32 v254,v248\n"
"s_nop 1\n"
"v_permlane32_swap_b32 v254,v248\n"
"s_nop 1\n"
"v_max_f32 v248,v248,v254\n"
"s_mov_b32 s90,1\n"
"s_mov_b32 s88,0\n"
"s_branch L1042_%=\n"
"L1045_%=:\n"
"s_nop 7\n"
"v_accvgpr_read_b32 v251,a140\n"
"s_nop 1\n"
"s_branch L1040_%=\n"
"L1047_%=:\n"
"s_nop 15\n"
WT"vmcnt(0)\n"
"s_lshl_b32 s89,s34,3\n"
"s_sub_u32 s89,120,s89\n"
SA"s89,s89,s30\n"
SA"s89,s89,4\n"
"s_lshl_b32 s91,s89,4\n"
"s_lshr_b32 s89,s89,1\n"
"v_bfe_u32 v249,v224,4,4\n"
"v_lshrrev_b32 v254,8,v224\n"
"v_lshlrev_b32 v254,2,v254\n"
"v_sub_u32 v249,v249,v254\n"
"v_add_u32 v249,s91,v249\n"
"v_mov_b32 v255,0xff800000\n"
"v_mov_b32 v248,v255\n"
"s_mov_b32 s90,0\n"
"s_mov_b32 s88,0\n"
"v_mov_b32 v176,0\n"
"v_mov_b32 v177,0\n"
"v_mov_b32 v178,0\n"
"v_mov_b32 v179,0\n"
"v_mov_b32 v180,0\n"
"v_mov_b32 v181,0\n"
"v_mov_b32 v182,0\n"
"v_mov_b32 v183,0\n"
"v_mov_b32 v184,0\n"
"v_mov_b32 v185,0\n"
"v_mov_b32 v186,0\n"
"v_mov_b32 v187,0\n"
"v_mov_b32 v188,0\n"
"v_mov_b32 v189,0\n"
"v_mov_b32 v190,0\n"
"v_mov_b32 v191,0\n"
"v_mov_b32 v192,0\n"
"v_mov_b32 v193,0\n"
"v_mov_b32 v194,0\n"
"v_mov_b32 v195,0\n"
"v_mov_b32 v196,0\n"
"v_mov_b32 v197,0\n"
"v_mov_b32 v198,0\n"
"v_mov_b32 v199,0\n"
"v_mov_b32 v200,0\n"
"v_mov_b32 v201,0\n"
"v_mov_b32 v202,0\n"
"v_mov_b32 v203,0\n"
"v_mov_b32 v204,0\n"
"v_mov_b32 v205,0\n"
"v_mov_b32 v206,0\n"
"v_mov_b32 v207,0\n"
"v_accvgpr_write_b32 a144,0\n"
"v_accvgpr_write_b32 a145,0\n"
"v_accvgpr_write_b32 a146,0\n"
"v_accvgpr_write_b32 a147,0\n"
"L1048_%=:\n"
"s_lshl_b32 s91,s88,13\n"
SA"s91,s91,s39\n"
SA"s91,s91,0\n"
BL"a[148:151],v224,s[8:11],s91 offen offset:0 sc1\n"
BL"a[152:155],v224,s[8:11],s91 offen offset:1024 sc1\n"
BL"a[156:159],v224,s[8:11],s91 offen offset:2048 sc1\n"
BL"a[160:163],v224,s[8:11],s91 offen offset:3072 sc1\n"
SA"s91,s91,4096\n"
BL"a[164:167],v224,s[8:11],s91 offen offset:0 sc1\n"
BL"a[168:171],v224,s[8:11],s91 offen offset:1024 sc1\n"
BL"a[172:175],v224,s[8:11],s91 offen offset:2048 sc1\n"
BL"a[176:179],v224,s[8:11],s91 offen offset:3072 sc1\n"
WT"vmcnt(0)\n"
MF"v[240:243],a[148:151],a[48:51],0\n"
MF"v[240:243],a[152:155],a[52:55],v[240:243]\n"
MF"v[240:243],a[156:159],a[56:59],v[240:243]\n"
MF"v[240:243],a[160:163],a[60:63],v[240:243]\n"
MF"v[244:247],a[164:167],a[48:51],0\n"
MF"v[244:247],a[168:171],a[52:55],v[244:247]\n"
MF"v[244:247],a[172:175],a[56:59],v[244:247]\n"
MF"v[244:247],a[176:179],a[60:63],v[244:247]\n"
"s_lshl_b32 s91,s88,5\n"
"v_subrev_u32 v250,s91,v249\n"
"s_nop 7\n"
"v_cmp_gt_i32 vcc,0,v250\n"
"s_nop 1\n"
"v_cndmask_b32 v240,v240,v255,vcc\n"
"v_cmp_gt_i32 vcc,1,v250\n"
"s_nop 1\n"
"v_cndmask_b32 v241,v241,v255,vcc\n"
"v_cmp_gt_i32 vcc,2,v250\n"
"s_nop 1\n"
"v_cndmask_b32 v242,v242,v255,vcc\n"
"v_cmp_gt_i32 vcc,3,v250\n"
"s_nop 1\n"
"v_cndmask_b32 v243,v243,v255,vcc\n"
"v_cmp_gt_i32 vcc,16,v250\n"
"s_nop 1\n"
"v_cndmask_b32 v244,v244,v255,vcc\n"
"v_cmp_gt_i32 vcc,17,v250\n"
"s_nop 1\n"
"v_cndmask_b32 v245,v245,v255,vcc\n"
"v_cmp_gt_i32 vcc,18,v250\n"
"s_nop 1\n"
"v_cndmask_b32 v246,v246,v255,vcc\n"
"v_cmp_gt_i32 vcc,19,v250\n"
"s_nop 1\n"
"v_cndmask_b32 v247,v247,v255,vcc\n"
"s_cmp_lg_u32 s90,0\n"
"s_cbranch_scc1 L1049_%=\n"
"v_max3_f32 v248,v248,v240,v241\n"
"v_max3_f32 v248,v248,v242,v243\n"
"v_max3_f32 v248,v248,v244,v245\n"
"v_max3_f32 v248,v248,v246,v247\n"
"s_branch L1050_%=\n"
"L1049_%=:\n"
"s_lshl_b32 s91,s88,13\n"
SA"s91,s91,s39\n"
SA"s91,s91,524288\n"
BL"a[148:151],v224,s[8:11],s91 offen offset:0 sc1\n"
BL"a[152:155],v224,s[8:11],s91 offen offset:1024 sc1\n"
BL"a[156:159],v224,s[8:11],s91 offen offset:2048 sc1\n"
BL"a[160:163],v224,s[8:11],s91 offen offset:3072 sc1\n"
SA"s91,s91,4096\n"
BL"a[164:167],v224,s[8:11],s91 offen offset:0 sc1\n"
BL"a[168:171],v224,s[8:11],s91 offen offset:1024 sc1\n"
BL"a[172:175],v224,s[8:11],s91 offen offset:2048 sc1\n"
BL"a[176:179],v224,s[8:11],s91 offen offset:3072 sc1\n"
WT"vmcnt(0)\n"
"v_sub_f32 v240,v240,v248\n"
"v_sub_f32 v241,v241,v248\n"
"v_sub_f32 v242,v242,v248\n"
"v_sub_f32 v243,v243,v248\n"
"v_sub_f32 v244,v244,v248\n"
"v_sub_f32 v245,v245,v248\n"
"v_sub_f32 v246,v246,v248\n"
"v_sub_f32 v247,v247,v248\n"
EX"v240,v240\n"
EX"v241,v241\n"
EX"v242,v242\n"
EX"v243,v243\n"
EX"v244,v244\n"
EX"v245,v245\n"
EX"v246,v246\n"
EX"v247,v247\n"
"s_nop 0\n"
PK"v240,v240,v241\n"
PK"v241,v242,v243\n"
PK"v242,v244,v245\n"
PK"v243,v246,v247\n"
"s_nop 1\n"
MF"v[176:179],a[148:151],v[240:243],v[176:179]\n"
MF"v[180:183],a[152:155],v[240:243],v[180:183]\n"
MF"v[184:187],a[156:159],v[240:243],v[184:187]\n"
MF"v[188:191],a[160:163],v[240:243],v[188:191]\n"
MF"v[192:195],a[164:167],v[240:243],v[192:195]\n"
MF"v[196:199],a[168:171],v[240:243],v[196:199]\n"
MF"v[200:203],a[172:175],v[240:243],v[200:203]\n"
MF"v[204:207],a[176:179],v[240:243],v[204:207]\n"
MF"a[144:147],a[128:131],v[240:243],a[144:147]\n"
"s_nop 7\n"
"L1050_%=:\n"
SA"s88,s88,1\n"
"s_cmp_le_u32 s88,s89\n"
"s_cbranch_scc1 L1048_%=\n"
"s_cmp_lg_u32 s90,0\n"
"s_cbranch_scc1 L1051_%=\n"
"v_mov_b32 v254,v248\n"
"s_nop 1\n"
"v_permlane16_swap_b32 v254,v248\n"
"s_nop 1\n"
"v_max_f32 v248,v248,v254\n"
"v_mov_b32 v254,v248\n"
"s_nop 1\n"
"v_permlane32_swap_b32 v254,v248\n"
"s_nop 1\n"
"v_max_f32 v248,v248,v254\n"
"s_mov_b32 s90,1\n"
"s_mov_b32 s88,0\n"
"s_branch L1048_%=\n"
"L1051_%=:\n"
"s_nop 7\n"
"v_accvgpr_read_b32 v252,a144\n"
"s_nop 1\n"
"s_branch L1046_%=\n"
"L1069_%=:\n"
"s_nop 15\n"
WT"vmcnt(0)\n"
"s_lshl_b32 s89,s34,3\n"
"s_sub_u32 s89,120,s89\n"
SA"s89,s89,s30\n"
SA"s89,s89,0\n"
"s_lshl_b32 s91,s89,4\n"
"s_lshr_b32 s89,s89,1\n"
"v_bfe_u32 v249,v224,4,4\n"
"v_lshrrev_b32 v254,8,v224\n"
"v_lshlrev_b32 v254,2,v254\n"
"v_sub_u32 v249,v249,v254\n"
"v_add_u32 v249,s91,v249\n"
"v_mov_b32 v255,0xff800000\n"
"v_mov_b32 v248,v255\n"
"s_mov_b32 s90,0\n"
"s_mov_b32 s88,0\n"
"v_mov_b32 v144,0\n"
"v_mov_b32 v145,0\n"
"v_mov_b32 v146,0\n"
"v_mov_b32 v147,0\n"
"v_mov_b32 v148,0\n"
"v_mov_b32 v149,0\n"
"v_mov_b32 v150,0\n"
"v_mov_b32 v151,0\n"
"v_mov_b32 v152,0\n"
"v_mov_b32 v153,0\n"
"v_mov_b32 v154,0\n"
"v_mov_b32 v155,0\n"
"v_mov_b32 v156,0\n"
"v_mov_b32 v157,0\n"
"v_mov_b32 v158,0\n"
"v_mov_b32 v159,0\n"
"v_mov_b32 v160,0\n"
"v_mov_b32 v161,0\n"
"v_mov_b32 v162,0\n"
"v_mov_b32 v163,0\n"
"v_mov_b32 v164,0\n"
"v_mov_b32 v165,0\n"
"v_mov_b32 v166,0\n"
"v_mov_b32 v167,0\n"
"v_mov_b32 v168,0\n"
"v_mov_b32 v169,0\n"
"v_mov_b32 v170,0\n"
"v_mov_b32 v171,0\n"
"v_mov_b32 v172,0\n"
"v_mov_b32 v173,0\n"
"v_mov_b32 v174,0\n"
"v_mov_b32 v175,0\n"
"v_accvgpr_write_b32 a140,0\n"
"v_accvgpr_write_b32 a141,0\n"
"v_accvgpr_write_b32 a142,0\n"
"v_accvgpr_write_b32 a143,0\n"
"L1070_%=:\n"
"s_lshl_b32 s91,s88,13\n"
SA"s91,s91,s39\n"
SA"s91,s91,0\n"
BL"a[148:151],v224,s[8:11],s91 offen offset:0 sc1\n"
BL"a[152:155],v224,s[8:11],s91 offen offset:1024 sc1\n"
BL"a[156:159],v224,s[8:11],s91 offen offset:2048 sc1\n"
BL"a[160:163],v224,s[8:11],s91 offen offset:3072 sc1\n"
SA"s91,s91,4096\n"
BL"a[164:167],v224,s[8:11],s91 offen offset:0 sc1\n"
BL"a[168:171],v224,s[8:11],s91 offen offset:1024 sc1\n"
BL"a[172:175],v224,s[8:11],s91 offen offset:2048 sc1\n"
BL"a[176:179],v224,s[8:11],s91 offen offset:3072 sc1\n"
WT"vmcnt(0)\n"
MF"v[240:243],a[148:151],a[32:35],0\n"
MF"v[240:243],a[152:155],a[36:39],v[240:243]\n"
MF"v[240:243],a[156:159],a[40:43],v[240:243]\n"
MF"v[240:243],a[160:163],a[44:47],v[240:243]\n"
MF"v[244:247],a[164:167],a[32:35],0\n"
MF"v[244:247],a[168:171],a[36:39],v[244:247]\n"
MF"v[244:247],a[172:175],a[40:43],v[244:247]\n"
MF"v[244:247],a[176:179],a[44:47],v[244:247]\n"
"s_lshl_b32 s91,s88,5\n"
"v_subrev_u32 v250,s91,v249\n"
"s_nop 7\n"
"v_cmp_gt_i32 vcc,0,v250\n"
"s_nop 1\n"
"v_cndmask_b32 v240,v240,v255,vcc\n"
"v_cmp_gt_i32 vcc,1,v250\n"
"s_nop 1\n"
"v_cndmask_b32 v241,v241,v255,vcc\n"
"v_cmp_gt_i32 vcc,2,v250\n"
"s_nop 1\n"
"v_cndmask_b32 v242,v242,v255,vcc\n"
"v_cmp_gt_i32 vcc,3,v250\n"
"s_nop 1\n"
"v_cndmask_b32 v243,v243,v255,vcc\n"
"v_cmp_gt_i32 vcc,16,v250\n"
"s_nop 1\n"
"v_cndmask_b32 v244,v244,v255,vcc\n"
"v_cmp_gt_i32 vcc,17,v250\n"
"s_nop 1\n"
"v_cndmask_b32 v245,v245,v255,vcc\n"
"v_cmp_gt_i32 vcc,18,v250\n"
"s_nop 1\n"
"v_cndmask_b32 v246,v246,v255,vcc\n"
"v_cmp_gt_i32 vcc,19,v250\n"
"s_nop 1\n"
"v_cndmask_b32 v247,v247,v255,vcc\n"
"s_cmp_lg_u32 s90,0\n"
"s_cbranch_scc1 L1071_%=\n"
"v_max3_f32 v248,v248,v240,v241\n"
"v_max3_f32 v248,v248,v242,v243\n"
"v_max3_f32 v248,v248,v244,v245\n"
"v_max3_f32 v248,v248,v246,v247\n"
"s_branch L1072_%=\n"
"L1071_%=:\n"
"s_lshl_b32 s91,s88,13\n"
SA"s91,s91,s39\n"
SA"s91,s91,524288\n"
BL"a[148:151],v224,s[8:11],s91 offen offset:0 sc1\n"
BL"a[152:155],v224,s[8:11],s91 offen offset:1024 sc1\n"
BL"a[156:159],v224,s[8:11],s91 offen offset:2048 sc1\n"
BL"a[160:163],v224,s[8:11],s91 offen offset:3072 sc1\n"
SA"s91,s91,4096\n"
BL"a[164:167],v224,s[8:11],s91 offen offset:0 sc1\n"
BL"a[168:171],v224,s[8:11],s91 offen offset:1024 sc1\n"
BL"a[172:175],v224,s[8:11],s91 offen offset:2048 sc1\n"
BL"a[176:179],v224,s[8:11],s91 offen offset:3072 sc1\n"
WT"vmcnt(0)\n"
"v_sub_f32 v240,v240,v248\n"
"v_sub_f32 v241,v241,v248\n"
"v_sub_f32 v242,v242,v248\n"
"v_sub_f32 v243,v243,v248\n"
"v_sub_f32 v244,v244,v248\n"
"v_sub_f32 v245,v245,v248\n"
"v_sub_f32 v246,v246,v248\n"
"v_sub_f32 v247,v247,v248\n"
EX"v240,v240\n"
EX"v241,v241\n"
EX"v242,v242\n"
EX"v243,v243\n"
EX"v244,v244\n"
EX"v245,v245\n"
EX"v246,v246\n"
EX"v247,v247\n"
"s_nop 0\n"
PK"v240,v240,v241\n"
PK"v241,v242,v243\n"
PK"v242,v244,v245\n"
PK"v243,v246,v247\n"
"s_nop 1\n"
MF"v[144:147],a[148:151],v[240:243],v[144:147]\n"
MF"v[148:151],a[152:155],v[240:243],v[148:151]\n"
MF"v[152:155],a[156:159],v[240:243],v[152:155]\n"
MF"v[156:159],a[160:163],v[240:243],v[156:159]\n"
MF"v[160:163],a[164:167],v[240:243],v[160:163]\n"
MF"v[164:167],a[168:171],v[240:243],v[164:167]\n"
MF"v[168:171],a[172:175],v[240:243],v[168:171]\n"
MF"v[172:175],a[176:179],v[240:243],v[172:175]\n"
MF"a[140:143],a[128:131],v[240:243],a[140:143]\n"
"s_nop 7\n"
"L1072_%=:\n"
SA"s88,s88,1\n"
"s_cmp_le_u32 s88,s89\n"
"s_cbranch_scc1 L1070_%=\n"
"s_cmp_lg_u32 s90,0\n"
"s_cbranch_scc1 L1073_%=\n"
"v_mov_b32 v254,v248\n"
"s_nop 1\n"
"v_permlane16_swap_b32 v254,v248\n"
"s_nop 1\n"
"v_max_f32 v248,v248,v254\n"
"v_mov_b32 v254,v248\n"
"s_nop 1\n"
"v_permlane32_swap_b32 v254,v248\n"
"s_nop 1\n"
"v_max_f32 v248,v248,v254\n"
"s_mov_b32 s90,1\n"
"s_mov_b32 s88,0\n"
"s_branch L1070_%=\n"
"L1073_%=:\n"
"s_nop 7\n"
"v_accvgpr_read_b32 v251,a140\n"
"s_nop 1\n"
"s_branch L1068_%=\n"
"L1075_%=:\n"
"s_nop 15\n"
WT"vmcnt(0)\n"
"s_lshl_b32 s89,s34,3\n"
"s_sub_u32 s89,120,s89\n"
SA"s89,s89,s30\n"
SA"s89,s89,4\n"
"s_lshl_b32 s91,s89,4\n"
"s_lshr_b32 s89,s89,1\n"
"v_bfe_u32 v249,v224,4,4\n"
"v_lshrrev_b32 v254,8,v224\n"
"v_lshlrev_b32 v254,2,v254\n"
"v_sub_u32 v249,v249,v254\n"
"v_add_u32 v249,s91,v249\n"
"v_mov_b32 v255,0xff800000\n"
"v_mov_b32 v248,v255\n"
"s_mov_b32 s90,0\n"
"s_mov_b32 s88,0\n"
"v_mov_b32 v176,0\n"
"v_mov_b32 v177,0\n"
"v_mov_b32 v178,0\n"
"v_mov_b32 v179,0\n"
"v_mov_b32 v180,0\n"
"v_mov_b32 v181,0\n"
"v_mov_b32 v182,0\n"
"v_mov_b32 v183,0\n"
"v_mov_b32 v184,0\n"
"v_mov_b32 v185,0\n"
"v_mov_b32 v186,0\n"
"v_mov_b32 v187,0\n"
"v_mov_b32 v188,0\n"
"v_mov_b32 v189,0\n"
"v_mov_b32 v190,0\n"
"v_mov_b32 v191,0\n"
"v_mov_b32 v192,0\n"
"v_mov_b32 v193,0\n"
"v_mov_b32 v194,0\n"
"v_mov_b32 v195,0\n"
"v_mov_b32 v196,0\n"
"v_mov_b32 v197,0\n"
"v_mov_b32 v198,0\n"
"v_mov_b32 v199,0\n"
"v_mov_b32 v200,0\n"
"v_mov_b32 v201,0\n"
"v_mov_b32 v202,0\n"
"v_mov_b32 v203,0\n"
"v_mov_b32 v204,0\n"
"v_mov_b32 v205,0\n"
"v_mov_b32 v206,0\n"
"v_mov_b32 v207,0\n"
"v_accvgpr_write_b32 a144,0\n"
"v_accvgpr_write_b32 a145,0\n"
"v_accvgpr_write_b32 a146,0\n"
"v_accvgpr_write_b32 a147,0\n"
"L1076_%=:\n"
"s_lshl_b32 s91,s88,13\n"
SA"s91,s91,s39\n"
SA"s91,s91,0\n"
BL"a[148:151],v224,s[8:11],s91 offen offset:0 sc1\n"
BL"a[152:155],v224,s[8:11],s91 offen offset:1024 sc1\n"
BL"a[156:159],v224,s[8:11],s91 offen offset:2048 sc1\n"
BL"a[160:163],v224,s[8:11],s91 offen offset:3072 sc1\n"
SA"s91,s91,4096\n"
BL"a[164:167],v224,s[8:11],s91 offen offset:0 sc1\n"
BL"a[168:171],v224,s[8:11],s91 offen offset:1024 sc1\n"
BL"a[172:175],v224,s[8:11],s91 offen offset:2048 sc1\n"
BL"a[176:179],v224,s[8:11],s91 offen offset:3072 sc1\n"
WT"vmcnt(0)\n"
MF"v[240:243],a[148:151],a[48:51],0\n"
MF"v[240:243],a[152:155],a[52:55],v[240:243]\n"
MF"v[240:243],a[156:159],a[56:59],v[240:243]\n"
MF"v[240:243],a[160:163],a[60:63],v[240:243]\n"
MF"v[244:247],a[164:167],a[48:51],0\n"
MF"v[244:247],a[168:171],a[52:55],v[244:247]\n"
MF"v[244:247],a[172:175],a[56:59],v[244:247]\n"
MF"v[244:247],a[176:179],a[60:63],v[244:247]\n"
"s_lshl_b32 s91,s88,5\n"
"v_subrev_u32 v250,s91,v249\n"
"s_nop 7\n"
"v_cmp_gt_i32 vcc,0,v250\n"
"s_nop 1\n"
"v_cndmask_b32 v240,v240,v255,vcc\n"
"v_cmp_gt_i32 vcc,1,v250\n"
"s_nop 1\n"
"v_cndmask_b32 v241,v241,v255,vcc\n"
"v_cmp_gt_i32 vcc,2,v250\n"
"s_nop 1\n"
"v_cndmask_b32 v242,v242,v255,vcc\n"
"v_cmp_gt_i32 vcc,3,v250\n"
"s_nop 1\n"
"v_cndmask_b32 v243,v243,v255,vcc\n"
"v_cmp_gt_i32 vcc,16,v250\n"
"s_nop 1\n"
"v_cndmask_b32 v244,v244,v255,vcc\n"
"v_cmp_gt_i32 vcc,17,v250\n"
"s_nop 1\n"
"v_cndmask_b32 v245,v245,v255,vcc\n"
"v_cmp_gt_i32 vcc,18,v250\n"
"s_nop 1\n"
"v_cndmask_b32 v246,v246,v255,vcc\n"
"v_cmp_gt_i32 vcc,19,v250\n"
"s_nop 1\n"
"v_cndmask_b32 v247,v247,v255,vcc\n"
"s_cmp_lg_u32 s90,0\n"
"s_cbranch_scc1 L1077_%=\n"
"v_max3_f32 v248,v248,v240,v241\n"
"v_max3_f32 v248,v248,v242,v243\n"
"v_max3_f32 v248,v248,v244,v245\n"
"v_max3_f32 v248,v248,v246,v247\n"
"s_branch L1078_%=\n"
"L1077_%=:\n"
"s_lshl_b32 s91,s88,13\n"
SA"s91,s91,s39\n"
SA"s91,s91,524288\n"
BL"a[148:151],v224,s[8:11],s91 offen offset:0 sc1\n"
BL"a[152:155],v224,s[8:11],s91 offen offset:1024 sc1\n"
BL"a[156:159],v224,s[8:11],s91 offen offset:2048 sc1\n"
BL"a[160:163],v224,s[8:11],s91 offen offset:3072 sc1\n"
SA"s91,s91,4096\n"
BL"a[164:167],v224,s[8:11],s91 offen offset:0 sc1\n"
BL"a[168:171],v224,s[8:11],s91 offen offset:1024 sc1\n"
BL"a[172:175],v224,s[8:11],s91 offen offset:2048 sc1\n"
BL"a[176:179],v224,s[8:11],s91 offen offset:3072 sc1\n"
WT"vmcnt(0)\n"
"v_sub_f32 v240,v240,v248\n"
"v_sub_f32 v241,v241,v248\n"
"v_sub_f32 v242,v242,v248\n"
"v_sub_f32 v243,v243,v248\n"
"v_sub_f32 v244,v244,v248\n"
"v_sub_f32 v245,v245,v248\n"
"v_sub_f32 v246,v246,v248\n"
"v_sub_f32 v247,v247,v248\n"
EX"v240,v240\n"
EX"v241,v241\n"
EX"v242,v242\n"
EX"v243,v243\n"
EX"v244,v244\n"
EX"v245,v245\n"
EX"v246,v246\n"
EX"v247,v247\n"
"s_nop 0\n"
PK"v240,v240,v241\n"
PK"v241,v242,v243\n"
PK"v242,v244,v245\n"
PK"v243,v246,v247\n"
"s_nop 1\n"
MF"v[176:179],a[148:151],v[240:243],v[176:179]\n"
MF"v[180:183],a[152:155],v[240:243],v[180:183]\n"
MF"v[184:187],a[156:159],v[240:243],v[184:187]\n"
MF"v[188:191],a[160:163],v[240:243],v[188:191]\n"
MF"v[192:195],a[164:167],v[240:243],v[192:195]\n"
MF"v[196:199],a[168:171],v[240:243],v[196:199]\n"
MF"v[200:203],a[172:175],v[240:243],v[200:203]\n"
MF"v[204:207],a[176:179],v[240:243],v[204:207]\n"
MF"a[144:147],a[128:131],v[240:243],a[144:147]\n"
"s_nop 7\n"
"L1078_%=:\n"
SA"s88,s88,1\n"
"s_cmp_le_u32 s88,s89\n"
"s_cbranch_scc1 L1076_%=\n"
"s_cmp_lg_u32 s90,0\n"
"s_cbranch_scc1 L1079_%=\n"
"v_mov_b32 v254,v248\n"
"s_nop 1\n"
"v_permlane16_swap_b32 v254,v248\n"
"s_nop 1\n"
"v_max_f32 v248,v248,v254\n"
"v_mov_b32 v254,v248\n"
"s_nop 1\n"
"v_permlane32_swap_b32 v254,v248\n"
"s_nop 1\n"
"v_max_f32 v248,v248,v254\n"
"s_mov_b32 s90,1\n"
"s_mov_b32 s88,0\n"
"s_branch L1076_%=\n"
"L1079_%=:\n"
"s_nop 7\n"
"v_accvgpr_read_b32 v252,a144\n"
"s_nop 1\n"
"s_branch L1074_%=\n"
"L1080_%=:\n"
"s_mov_b32 s54,0\n"
"L1082_%=:\n"
"s_sleep 2\n"
"buffer_load_dwordx2 v[240:241],v238,s[8:11],s63 offen sc1\n"
WT"vmcnt(0)\n"
SA"s54,s54,1\n"
"v_cmp_eq_u64 vcc,s[28:29],v[240:241]\n"
"s_cmp_eq_u64 vcc,exec\n"
"s_cbranch_scc1 L1081_%=\n"
"s_cmp_lt_u32 s54,1048576\n"
"s_cbranch_scc1 L1082_%=\n"
"s_branch L1081_%=\n"
"L1083_%=:\n"
"s_mov_b32 s54,0\n"
"L1085_%=:\n"
"s_sleep 2\n"
"buffer_load_dwordx2 v[240:241],v238,s[8:11],s63 offen sc1\n"
WT"vmcnt(0)\n"
SA"s54,s54,1\n"
"v_cmp_eq_u64 vcc,s[28:29],v[240:241]\n"
"s_cmp_eq_u64 vcc,exec\n"
"s_cbranch_scc1 L1084_%=\n"
"s_cmp_lt_u32 s54,1048576\n"
"s_cbranch_scc1 L1085_%=\n"
"s_branch L1084_%=\n"
"L1086_%=:\n"
"s_mov_b32 s54,0\n"
"L1088_%=:\n"
"s_sleep 2\n"
"buffer_load_dwordx2 v[240:241],v244,s[8:11],s92 offen sc1\n"
WT"vmcnt(0)\n"
SA"s54,s54,1\n"
"v_cmp_eq_u64 vcc,s[28:29],v[240:241]\n"
"s_cmp_eq_u64 vcc,exec\n"
"s_cbranch_scc1 L1087_%=\n"
"s_cmp_lt_u32 s54,1048576\n"
"s_cbranch_scc1 L1088_%=\n"
"s_branch L1087_%=\n"
"LTH_%=:\n"
RD"a[88:91],v227 offset:6144\n"
WT"lgkmcnt(6)\n"
MF"v[0:3],a[64:67],v[80:83],0\n"
"v_accvgpr_read_b32 v251,a140\n"
"v_subrev_u32 v253,0x2b800000,v251\n"
MF"v[40:43],a[64:67],v[112:115],0\n"
WT"lgkmcnt(4)\n"
RD"a[92:95],v227 offset:7168\n"
MF"v[4:7],a[68:71],v[80:83],0\n"
"v_cmp_le_u32 vcc,0x34000000,v253\n"
"s_cmp_lg_u64 vcc,0\n"
"s_cbranch_scc1 L1069_%=\n"
"L1068_%=:\n"
MF"v[44:47],a[68:71],v[112:115],0\n"
"v_rcp_f32 v251,v251\n"
MF"v[8:11],a[72:75],v[80:83],0\n"
SA"s41,s41,32768\n"
RD"a[96:99],v227 offset:8192\n"
MU"v144,v251,v144\n"
MF"v[48:51],a[72:75],v[112:115],0\n"
WT"lgkmcnt(4)\n"
RD"a[100:103],v227 offset:9216\n"
MU"v145,v251,v145\n"
MF"v[12:15],a[76:79],v[80:83],0\n"
"s_cmp_eq_u32 s41,163840\n"
MU"v146,v251,v146\n"
MU"v147,v251,v147\n"
MF"v[52:55],a[76:79],v[112:115],0\n"
"s_cselect_b32 s41,0,s41\n"
RD"a[104:107],v227 offset:10240\n"
"v_add_u32 v226,s41,v224\n"
MF"v[16:19],a[80:83],v[80:83],0\n"
SA"s41,s41,32768\n"
MU"v148,v251,v148\n"
MU"v149,v251,v149\n"
MF"v[56:59],a[80:83],v[112:115],0\n"
WT"lgkmcnt(3)\n"
RD"a[108:111],v227 offset:11264\n"
MU"v150,v251,v150\n"
MU"v151,v251,v151\n"
MF"v[20:23],a[84:87],v[80:83],0\n"
"s_cmp_eq_u32 s41,163840\n"
PK"v144,v144,v145\n"
PK"v145,v146,v147\n"
MF"v[60:63],a[84:87],v[112:115],0\n"
"s_cselect_b32 s41,0,s41\n"
RD"a[112:115],v227 offset:12288\n"
"v_add_u32 v235,s41,v224\n"
PK"v146,v148,v149\n"
MF"v[24:27],a[88:91],v[80:83],0\n"
SA"s41,s41,32768\n"
PK"v147,v150,v151\n"
MU"v152,v251,v152\n"
MF"v[64:67],a[88:91],v[112:115],0\n"
"s_cmp_eq_u32 s41,163840\n"
BS"v[144:147],v224,s[8:11],s60 offen offset:0 sc1\n"
MU"v153,v251,v153\n"
MF"v[28:31],a[92:95],v[80:83],0\n"
"s_cselect_b32 s41,0,s41\n"
RD"a[116:119],v227 offset:13312\n"
"v_add_u32 v236,s41,v224\n"
MU"v154,v251,v154\n"
MF"v[68:71],a[92:95],v[112:115],0\n"
WT"lgkmcnt(3)\n"
RD"a[120:123],v227 offset:14336\n"
MU"v155,v251,v155\n"
MU"v156,v251,v156\n"
MF"v[32:35],a[96:99],v[80:83],0\n"
SA"s41,s41,32768\n"
MU"v157,v251,v157\n"
MU"v158,v251,v158\n"
MU"v159,v251,v159\n"
MF"v[72:75],a[96:99],v[112:115],0\n"
"s_cmp_eq_u32 s41,163840\n"
RD"a[124:127],v227 offset:15360\n"
PK"v152,v152,v153\n"
PK"v153,v154,v155\n"
MF"v[36:39],a[100:103],v[80:83],0\n"
"s_cselect_b32 s41,0,s41\n"
PK"v154,v156,v157\n"
"v_add_u32 v237,s41,v224\n"
PK"v155,v158,v159\n"
MF"v[76:79],a[100:103],v[112:115],0\n"
BS"v[152:155],v224,s[8:11],s60 offen offset:1024 sc1\n"
MU"v160,v251,v160\n"
MF"v[0:3],a[104:107],v[88:91],v[0:3]\n"
RD"a[64:67],v227 offset:16384\n"
MU"v161,v251,v161\n"
MU"v162,v251,v162\n"
MF"v[40:43],a[104:107],v[120:123],v[40:43]\n"
WT"lgkmcnt(3)\n"
RD"a[68:71],v227 offset:17408\n"
MU"v163,v251,v163\n"
MU"v164,v251,v164\n"
MF"v[4:7],a[108:111],v[88:91],v[4:7]\n"
WT"vmcnt(2)\n"
"buffer_store_dwordx2 v[232:233],v230,s[8:11],s61 offen sc1\n"
MU"v165,v251,v165\n"
MF"v[44:47],a[108:111],v[120:123],v[44:47]\n"
RD"a[72:75],v227 offset:18432\n"
MU"v166,v251,v166\n"
MU"v167,v251,v167\n"
MF"v[8:11],a[112:115],v[88:91],v[8:11]\n"
SA"s51,s60,16384\n"
"v_accvgpr_read_b32 v252,a144\n"
"v_subrev_u32 v253,0x2b800000,v252\n"
PK"v160,v160,v161\n"
MF"v[48:51],a[112:115],v[120:123],v[48:51]\n"
"s_lshl_b32 s62,s34,14\n"
RD"a[76:79],v227 offset:19456\n"
PK"v161,v162,v163\n"
PK"v162,v164,v165\n"
MF"v[12:15],a[116:119],v[88:91],v[12:15]\n"
"s_sub_u32 s62,s61,s62\n"
"v_cmp_le_u32 vcc,0x34000000,v253\n"
"s_cmp_lg_u64 vcc,0\n"
"s_cbranch_scc1 L1075_%=\n"
"L1074_%=:\n"
MF"v[52:55],a[116:119],v[120:123],v[52:55]\n"
WT"lgkmcnt(3)\n"
"v_rcp_f32 v252,v252\n"
PK"v163,v166,v167\n"
MF"v[16:19],a[120:123],v[88:91],v[16:19]\n"
SA"s62,s62,122880\n"
BS"v[160:163],v224,s[8:11],s60 offen offset:2048 sc1\n"
MU"v168,v251,v168\n"
MF"v[56:59],a[120:123],v[120:123],v[56:59]\n"
RD"a[80:83],v227 offset:20480\n"
MU"v169,v251,v169\n"
MU"v170,v251,v170\n"
MF"v[20:23],a[124:127],v[88:91],v[20:23]\n"
RD"a[84:87],v227 offset:21504\n"
MU"v171,v251,v171\n"
MU"v172,v251,v172\n"
MF"v[60:63],a[124:127],v[120:123],v[60:63]\n"
RD"a[88:91],v227 offset:22528\n"
MU"v173,v251,v173\n"
MU"v174,v251,v174\n"
MF"v[24:27],a[64:67],v[88:91],v[24:27]\n"
MU"v175,v251,v175\n"
PK"v168,v168,v169\n"
PK"v169,v170,v171\n"
MF"v[64:67],a[64:67],v[120:123],v[64:67]\n"
WT"lgkmcnt(3)\n"
RD"a[92:95],v227 offset:23552\n"
MU"v176,v252,v176\n"
MU"v177,v252,v177\n"
MF"v[28:31],a[68:71],v[88:91],v[28:31]\n"
PK"v170,v172,v173\n"
PK"v171,v174,v175\n"
MU"v178,v252,v178\n"
MF"v[68:71],a[68:71],v[120:123],v[68:71]\n"
BS"v[168:171],v224,s[8:11],s60 offen offset:3072 sc1\n"
MU"v179,v252,v179\n"
MF"v[32:35],a[72:75],v[88:91],v[32:35]\n"
RD"a[96:99],v227 offset:24576\n"
MU"v180,v252,v180\n"
MU"v181,v252,v181\n"
MF"v[72:75],a[72:75],v[120:123],v[72:75]\n"
RD"a[100:103],v227 offset:25600\n"
MU"v182,v252,v182\n"
MU"v183,v252,v183\n"
MF"v[36:39],a[76:79],v[88:91],v[36:39]\n"
PK"v176,v176,v177\n"
PK"v177,v178,v179\n"
PK"v178,v180,v181\n"
MF"v[76:79],a[76:79],v[120:123],v[76:79]\n"
WT"lgkmcnt(3)\n"
RD"a[104:107],v227 offset:26624\n"
PK"v179,v182,v183\n"
MU"v184,v252,v184\n"
MF"v[0:3],a[80:83],v[96:99],v[0:3]\n"
BS"v[176:179],v224,s[8:11],s51 offen offset:0 sc1\n"
MU"v185,v252,v185\n"
MF"v[40:43],a[80:83],v[128:131],v[40:43]\n"
RD"a[108:111],v227 offset:27648\n"
MU"v186,v252,v186\n"
MU"v187,v252,v187\n"
MF"v[4:7],a[84:87],v[96:99],v[4:7]\n"
MU"v188,v252,v188\n"
MU"v189,v252,v189\n"
MU"v190,v252,v190\n"
MF"v[44:47],a[84:87],v[128:131],v[44:47]\n"
RD"a[112:115],v227 offset:28672\n"
MU"v191,v252,v191\n"
PK"v184,v184,v185\n"
MF"v[8:11],a[88:91],v[96:99],v[8:11]\n"
PK"v185,v186,v187\n"
PK"v186,v188,v189\n"
PK"v187,v190,v191\n"
MF"v[48:51],a[88:91],v[128:131],v[48:51]\n"
WT"lgkmcnt(3)\n"
BS"v[184:187],v224,s[8:11],s51 offen offset:1024 sc1\n"
MU"v192,v252,v192\n"
MF"v[12:15],a[92:95],v[96:99],v[12:15]\n"
RD"a[116:119],v227 offset:29696\n"
MU"v193,v252,v193\n"
MU"v194,v252,v194\n"
MF"v[52:55],a[92:95],v[128:131],v[52:55]\n"
RD"a[120:123],v227 offset:30720\n"
"v_bfe_u32 v238,v224,4,2\n"
MU"v195,v252,v195\n"
MF"v[16:19],a[96:99],v[96:99],v[16:19]\n"
"s_andn2_b32 s63,s61,255\n"
"v_lshlrev_b32 v238,6,v238\n"
MU"v196,v252,v196\n"
MU"v197,v252,v197\n"
MF"v[56:59],a[96:99],v[128:131],v[56:59]\n"
RD"a[124:127],v227 offset:31744\n"
MU"v198,v252,v198\n"
MU"v199,v252,v199\n"
MF"v[20:23],a[100:103],v[96:99],v[20:23]\n"
"s_barrier\n"
MF"v[60:63],a[100:103],v[128:131],v[60:63]\n"
WT"lgkmcnt(3)\n"
"buffer_load_dwordx2 v[240:241],v238,s[8:11],s63 offen sc1\n"
PK"v192,v192,v193\n"
MF"v[24:27],a[104:107],v[96:99],v[24:27]\n"
RD"a[64:67],v226 offset:0\n"
PK"v193,v194,v195\n"
PK"v194,v196,v197\n"
MF"v[64:67],a[104:107],v[128:131],v[64:67]\n"
RD"a[68:71],v226 offset:1024\n"
PK"v195,v198,v199\n"
MU"v200,v252,v200\n"
MF"v[28:31],a[108:111],v[96:99],v[28:31]\n"
BS"v[192:195],v224,s[8:11],s51 offen offset:2048 sc1\n"
MU"v201,v252,v201\n"
MF"v[68:71],a[108:111],v[128:131],v[68:71]\n"
RD"a[72:75],v226 offset:2048\n"
MU"v202,v252,v202\n"
MU"v203,v252,v203\n"
MF"v[32:35],a[112:115],v[96:99],v[32:35]\n"
MU"v204,v252,v204\n"
MU"v205,v252,v205\n"
MU"v206,v252,v206\n"
MF"v[72:75],a[112:115],v[128:131],v[72:75]\n"
WT"lgkmcnt(3)\n"
RD"a[76:79],v226 offset:3072\n"
MU"v207,v252,v207\n"
PK"v200,v200,v201\n"
MF"v[36:39],a[116:119],v[96:99],v[36:39]\n"
"v_readlane_b32 s43,v228,s42\n"
PK"v201,v202,v203\n"
PK"v202,v204,v205\n"
MF"v[76:79],a[116:119],v[128:131],v[76:79]\n"
RD"a[80:83],v226 offset:4096\n"
PK"v203,v206,v207\n"
MF"v[0:3],a[120:123],v[104:107],v[0:3]\n"
BS"v[200:203],v224,s[8:11],s51 offen offset:3072 sc1\n"
MF"v[40:43],a[120:123],v[136:139],v[40:43]\n"
RD"a[84:87],v226 offset:5120\n"
MF"v[4:7],a[124:127],v[104:107],v[4:7]\n"
"s_mov_b32 m0,s40\n"
MF"v[44:47],a[124:127],v[136:139],v[44:47]\n"
WT"lgkmcnt(3)\n"
RD"a[88:91],v226 offset:6144\n"
MF"v[8:11],a[64:67],v[104:107],v[8:11]\n"
"v_readlane_b32 s44,v229,s42\n"
MF"v[48:51],a[64:67],v[136:139],v[48:51]\n"
RD"a[92:95],v226 offset:7168\n"
MF"v[12:15],a[68:71],v[104:107],v[12:15]\n"
SA"s42,s42,1\n"
BL"v225,s[8:11],s43 offen offset:0 sc1 lds\n"
MF"v[52:55],a[68:71],v[136:139],v[52:55]\n"
RD"a[96:99],v227 offset:0\n"
MF"v[16:19],a[72:75],v[104:107],v[16:19]\n"
MF"v[56:59],a[72:75],v[136:139],v[56:59]\n"
WT"lgkmcnt(3)\n"
RD"a[100:103],v227 offset:1024\n"
MF"v[20:23],a[76:79],v[104:107],v[20:23]\n"
MF"v[60:63],a[76:79],v[136:139],v[60:63]\n"
RD"a[104:107],v227 offset:2048\n"
MF"v[24:27],a[80:83],v[104:107],v[24:27]\n"
BL"v225,s[8:11],s43 offen offset:1024 sc1 lds\n"
MF"v[64:67],a[80:83],v[136:139],v[64:67]\n"
RD"a[108:111],v227 offset:3072\n"
MF"v[28:31],a[84:87],v[104:107],v[28:31]\n"
MF"v[68:71],a[84:87],v[136:139],v[68:71]\n"
WT"lgkmcnt(3)\n"
RD"a[112:115],v227 offset:4096\n"
MF"v[32:35],a[88:91],v[104:107],v[32:35]\n"
MF"v[72:75],a[88:91],v[136:139],v[72:75]\n"
RD"a[116:119],v227 offset:5120\n"
MF"v[36:39],a[92:95],v[104:107],v[36:39]\n"
BL"v225,s[8:11],s43 offen offset:2048 sc1 lds\n"
MF"v[76:79],a[92:95],v[136:139],v[76:79]\n"
RD"a[120:123],v227 offset:6144\n"
MF"v[84:87],a[96:99],v[144:147],0\n"
MF"v[164:167],a[96:99],v[176:179],0\n"
WT"lgkmcnt(3)\n"
RD"a[124:127],v227 offset:7168\n"
MF"v[92:95],a[100:103],v[144:147],0\n"
MF"v[172:175],a[100:103],v[176:179],0\n"
RD"a[64:67],v227 offset:8192\n"
MF"v[100:103],a[104:107],v[144:147],0\n"
BL"v225,s[8:11],s43 offen offset:3072 sc1 lds\n"
MF"v[180:183],a[104:107],v[176:179],0\n"
RD"a[68:71],v227 offset:9216\n"
MF"v[108:111],a[108:111],v[144:147],0\n"
SA"m0,s40,16384\n"
MF"v[188:191],a[108:111],v[176:179],0\n"
WT"lgkmcnt(3)\n"
RD"a[72:75],v227 offset:10240\n"
MF"v[116:119],a[112:115],v[144:147],0\n"
MF"v[196:199],a[112:115],v[176:179],0\n"
SA"s48,s59,32768\n"
RD"a[76:79],v227 offset:11264\n"
MF"v[124:127],a[116:119],v[144:147],0\n"
"s_and_b32 s48,s48,0x18000\n"
BL"v225,s[8:11],s44 offen offset:0 sc1 lds\n"
MF"v[204:207],a[116:119],v[176:179],0\n"
SA"s40,s40,32768\n"
RD"a[80:83],v227 offset:12288\n"
"s_andn2_b32 s49,s59,0x18000\n"
MF"v[132:135],a[120:123],v[144:147],0\n"
"s_cmp_eq_u32 s40,s46\n"
"s_cselect_b32 s40,s47,s40\n"
WT"vmcnt(7)\n"
"v_cmp_eq_u64 vcc,s[28:29],v[240:241]\n"
"s_cmp_eq_u64 vcc,exec\n"
"s_cbranch_scc0 L1080_%=\n"
"L1081_%=:\n"
MF"v[208:211],a[120:123],v[176:179],0\n"
WT"lgkmcnt(3)\n"
RD"a[84:87],v227 offset:13312\n"
"s_or_b32 s52,s48,s49\n"
MF"v[140:143],a[124:127],v[144:147],0\n"
SA"s53,s52,16384\n"
BL"a[0:3],v224,s[8:11],s52 offen offset:0 sc1\n"
MF"v[212:215],a[124:127],v[176:179],0\n"
RD"a[88:91],v227 offset:14336\n"
MF"v[148:151],a[64:67],v[144:147],0\n"
BL"v225,s[8:11],s44 offen offset:1024 sc1 lds\n"
MF"v[216:219],a[64:67],v[176:179],0\n"
RD"a[92:95],v227 offset:15360\n"
MF"v[156:159],a[68:71],v[144:147],0\n"
BL"a[4:7],v224,s[8:11],s53 offen offset:0 sc1\n"
MF"v[220:223],a[68:71],v[176:179],0\n"
WT"lgkmcnt(3)\n"
RD"a[96:99],v227 offset:16384\n"
MF"v[84:87],a[72:75],v[152:155],v[84:87]\n"
MF"v[164:167],a[72:75],v[184:187],v[164:167]\n"
RD"a[100:103],v227 offset:17408\n"
MF"v[92:95],a[76:79],v[152:155],v[92:95]\n"
BL"v225,s[8:11],s44 offen offset:2048 sc1 lds\n"
MF"v[172:175],a[76:79],v[184:187],v[172:175]\n"
RD"a[104:107],v227 offset:18432\n"
MF"v[100:103],a[80:83],v[152:155],v[100:103]\n"
BL"a[8:11],v224,s[8:11],s52 offen offset:1024 sc1\n"
MF"v[180:183],a[80:83],v[184:187],v[180:183]\n"
WT"lgkmcnt(3)\n"
RD"a[108:111],v227 offset:19456\n"
MF"v[108:111],a[84:87],v[152:155],v[108:111]\n"
"v_readlane_b32 s43,v228,s42\n"
MF"v[188:191],a[84:87],v[184:187],v[188:191]\n"
RD"a[112:115],v227 offset:20480\n"
MF"v[116:119],a[88:91],v[152:155],v[116:119]\n"
BL"a[12:15],v224,s[8:11],s53 offen offset:1024 sc1\n"
MF"v[196:199],a[88:91],v[184:187],v[196:199]\n"
BL"v225,s[8:11],s44 offen offset:3072 sc1 lds\n"
MF"v[124:127],a[92:95],v[152:155],v[124:127]\n"
"s_mov_b32 m0,s40\n"
RD"a[116:119],v227 offset:21504\n"
MF"v[204:207],a[92:95],v[184:187],v[204:207]\n"
WT"lgkmcnt(3)\n"
RD"a[120:123],v227 offset:22528\n"
MF"v[132:135],a[96:99],v[152:155],v[132:135]\n"
WT"vmcnt(12)\n"
"buffer_store_dwordx2 v[232:233],v230,s[8:11],s62 offen sc1\n"
MF"v[208:211],a[96:99],v[184:187],v[208:211]\n"
BL"a[16:19],v224,s[8:11],s52 offen offset:2048 sc1\n"
MF"v[140:143],a[100:103],v[152:155],v[140:143]\n"
RD"a[124:127],v227 offset:23552\n"
"v_readlane_b32 s44,v229,s42\n"
MF"v[212:215],a[100:103],v[184:187],v[212:215]\n"
SA"s42,s42,1\n"
BL"v225,s[8:11],s43 offen offset:0 sc1 lds\n"
MF"v[148:151],a[104:107],v[152:155],v[148:151]\n"
RD"a[64:67],v227 offset:24576\n"
MF"v[216:219],a[104:107],v[184:187],v[216:219]\n"
WT"lgkmcnt(3)\n"
BL"a[20:23],v224,s[8:11],s53 offen offset:2048 sc1\n"
MF"v[156:159],a[108:111],v[152:155],v[156:159]\n"
RD"a[68:71],v227 offset:25600\n"
MF"v[220:223],a[108:111],v[184:187],v[220:223]\n"
RD"a[72:75],v227 offset:26624\n"
MF"v[84:87],a[112:115],v[160:163],v[84:87]\n"
"s_lshr_b32 s89,s31,3\n"
BL"v225,s[8:11],s43 offen offset:1024 sc1 lds\n"
MF"v[164:167],a[112:115],v[192:195],v[164:167]\n"
"s_lshl_b32 s89,s89,4\n"
RD"a[76:79],v227 offset:27648\n"
MF"v[92:95],a[116:119],v[160:163],v[92:95]\n"
SA"s89,s89,s30\n"
BL"a[24:27],v224,s[8:11],s52 offen offset:3072 sc1\n"
MF"v[172:175],a[116:119],v[192:195],v[172:175]\n"
WT"lgkmcnt(3)\n"
RD"a[80:83],v227 offset:28672\n"
"s_lshl_b32 s89,s89,3\n"
MF"v[100:103],a[120:123],v[160:163],v[100:103]\n"
"s_xor_b32 s23,s32,s30\n"
MF"v[180:183],a[120:123],v[192:195],v[180:183]\n"
SA"s48,s59,65536\n"
RD"a[84:87],v227 offset:29696\n"
SA"s89,s89,s23\n"
MF"v[108:111],a[124:127],v[160:163],v[108:111]\n"
"s_and_b32 s48,s48,0x18000\n"
BL"a[28:31],v224,s[8:11],s53 offen offset:3072 sc1\n"
MF"v[188:191],a[124:127],v[192:195],v[188:191]\n"
"s_xor_b32 s90,s89,36\n"
BL"v225,s[8:11],s43 offen offset:2048 sc1 lds\n"
MF"v[116:119],a[64:67],v[160:163],v[116:119]\n"
"s_andn2_b32 s49,s59,0x18000\n"
RD"a[88:91],v227 offset:30720\n"
"s_mul_i32 s88,s89,5120\n"
MF"v[196:199],a[64:67],v[192:195],v[196:199]\n"
WT"lgkmcnt(3)\n"
RD"a[92:95],v227 offset:31744\n"
"s_or_b32 s52,s48,s49\n"
MF"v[124:127],a[68:71],v[160:163],v[124:127]\n"
SA"s53,s52,16384\n"
BL"a[180:183],v224,s[8:11],s52 offen offset:0 sc1\n"
MF"v[204:207],a[68:71],v[192:195],v[204:207]\n"
"s_mul_i32 s22,s90,5120\n"
RD"a[96:99],v226 offset:0\n"
"s_sub_u32 s90,s90,s89\n"
MF"v[132:135],a[72:75],v[160:163],v[132:135]\n"
"s_lshl_b32 s90,s90,6\n"
BL"v225,s[8:11],s43 offen offset:3072 sc1 lds\n"
MF"v[208:211],a[72:75],v[192:195],v[208:211]\n"
SA"s88,s88,59506688\n"
RD"a[100:103],v226 offset:1024\n"
SA"s22,s22,59506688\n"
MF"v[140:143],a[76:79],v[160:163],v[140:143]\n"
"s_cmp_eq_u32 s23,0\n"
BL"a[184:187],v224,s[8:11],s53 offen offset:0 sc1\n"
MF"v[212:215],a[76:79],v[192:195],v[212:215]\n"
WT"lgkmcnt(3)\n"
RD"a[104:107],v226 offset:2048\n"
"s_cselect_b32 s88,2130706432,s88\n"
MF"v[148:151],a[80:83],v[160:163],v[148:151]\n"
SA"m0,s40,16384\n"
"s_cmp_eq_u32 s23,4\n"
"s_cselect_b32 s22,2130706432,s22\n"
MF"v[216:219],a[80:83],v[192:195],v[216:219]\n"
"s_lshl_b32 s89,s89,6\n"
RD"a[108:111],v226 offset:3072\n"
"v_bfe_u32 v239,v224,1,6\n"
MF"v[156:159],a[84:87],v[160:163],v[156:159]\n"
SA"s89,s89,80478208\n"
BL"v225,s[8:11],s44 offen offset:0 sc1 lds\n"
MF"v[220:223],a[84:87],v[192:195],v[220:223]\n"
SA"s40,s40,32768\n"
RD"a[112:115],v226 offset:4096\n"
"v_bfe_u32 v242,v224,7,1\n"
MF"v[84:87],a[88:91],v[168:171],v[84:87]\n"
"s_cmp_eq_u32 s40,s46\n"
BL"a[188:191],v224,s[8:11],s52 offen offset:1024 sc1\n"
MF"v[164:167],a[88:91],v[200:203],v[164:167]\n"
WT"lgkmcnt(3)\n"
RD"a[116:119],v226 offset:5120\n"
"v_mad_u32_u24 v239,v242,s90,v239\n"
MF"v[92:95],a[92:95],v[168:171],v[92:95]\n"
"s_cselect_b32 s40,s47,s40\n"
"v_lshrrev_b32 v242,8,v224\n"
"v_lshl_add_u32 v239,v242,27,v239\n"
MF"v[172:175],a[92:95],v[200:203],v[172:175]\n"
RD"a[120:123],v226 offset:6144\n"
MF"v[100:103],a[96:99],v[168:171],v[100:103]\n"
BL"a[192:195],v224,s[8:11],s53 offen offset:1024 sc1\n"
MF"v[180:183],a[96:99],v[200:203],v[180:183]\n"
BL"v225,s[8:11],s44 offen offset:1024 sc1 lds\n"
MF"v[108:111],a[100:103],v[168:171],v[108:111]\n"
RD"a[124:127],v226 offset:7168\n"
MF"v[188:191],a[100:103],v[200:203],v[188:191]\n"
WT"lgkmcnt(3)\n"
RD"a[64:67],v226 offset:8192\n"
MF"v[116:119],a[104:107],v[168:171],v[116:119]\n"
BL"a[196:199],v224,s[8:11],s52 offen offset:2048 sc1\n"
MF"v[196:199],a[104:107],v[200:203],v[196:199]\n"
RD"a[68:71],v226 offset:9216\n"
MF"v[124:127],a[108:111],v[168:171],v[124:127]\n"
BL"v225,s[8:11],s44 offen offset:2048 sc1 lds\n"
MF"v[204:207],a[108:111],v[200:203],v[204:207]\n"
RD"a[72:75],v226 offset:10240\n"
MF"v[132:135],a[112:115],v[168:171],v[132:135]\n"
BL"a[200:203],v224,s[8:11],s53 offen offset:2048 sc1\n"
MF"v[208:211],a[112:115],v[200:203],v[208:211]\n"
WT"lgkmcnt(3)\n"
RD"a[76:79],v226 offset:11264\n"
MF"v[140:143],a[116:119],v[168:171],v[140:143]\n"
MF"v[212:215],a[116:119],v[200:203],v[212:215]\n"
RD"a[80:83],v226 offset:12288\n"
MF"v[148:151],a[120:123],v[168:171],v[148:151]\n"
BL"v225,s[8:11],s44 offen offset:3072 sc1 lds\n"
MF"v[216:219],a[120:123],v[200:203],v[216:219]\n"
RD"a[84:87],v226 offset:13312\n"
MF"v[156:159],a[124:127],v[168:171],v[156:159]\n"
BL"a[204:207],v224,s[8:11],s52 offen offset:3072 sc1\n"
MF"v[220:223],a[124:127],v[200:203],v[220:223]\n"
WT"vmcnt(26) lgkmcnt(3)\n"
RD"a[88:91],v226 offset:14336\n"
MF"v[0:3],a[64:67],a[0:3],v[0:3]\n"
WT"vmcnt(24)\n"
MF"v[40:43],a[64:67],a[4:7],v[40:43]\n"
SA"s48,s59,98304\n"
RD"a[92:95],v226 offset:15360\n"
MF"v[4:7],a[68:71],a[0:3],v[4:7]\n"
"s_and_b32 s48,s48,0x18000\n"
BL"a[208:211],v224,s[8:11],s53 offen offset:3072 sc1\n"
MF"v[44:47],a[68:71],a[4:7],v[44:47]\n"
"s_andn2_b32 s49,s59,0x18000\n"
RD"a[96:99],v226 offset:16384\n"
MF"v[8:11],a[72:75],a[0:3],v[8:11]\n"
"s_or_b32 s52,s48,s49\n"
SA"s53,s52,16384\n"
MF"v[48:51],a[72:75],a[4:7],v[48:51]\n"
WT"lgkmcnt(3)\n"
RD"a[100:103],v226 offset:17408\n"
MF"v[12:15],a[76:79],a[0:3],v[12:15]\n"
BL"a[212:215],v224,s[8:11],s52 offen offset:0 sc1\n"
MF"v[52:55],a[76:79],a[4:7],v[52:55]\n"
RD"a[104:107],v226 offset:18432\n"
MF"v[16:19],a[80:83],a[0:3],v[16:19]\n"
MF"v[56:59],a[80:83],a[4:7],v[56:59]\n"
RD"a[108:111],v226 offset:19456\n"
MF"v[20:23],a[84:87],a[0:3],v[20:23]\n"
BL"a[216:219],v224,s[8:11],s53 offen offset:0 sc1\n"
MF"v[60:63],a[84:87],a[4:7],v[60:63]\n"
WT"lgkmcnt(3)\n"
RD"a[112:115],v226 offset:20480\n"
MF"v[24:27],a[88:91],a[0:3],v[24:27]\n"
MF"v[64:67],a[88:91],a[4:7],v[64:67]\n"
RD"a[116:119],v226 offset:21504\n"
MF"v[28:31],a[92:95],a[0:3],v[28:31]\n"
MF"v[68:71],a[92:95],a[4:7],v[68:71]\n"
RD"a[120:123],v226 offset:22528\n"
MF"v[32:35],a[96:99],a[0:3],v[32:35]\n"
BL"a[220:223],v224,s[8:11],s52 offen offset:1024 sc1\n"
MF"v[72:75],a[96:99],a[4:7],v[72:75]\n"
WT"lgkmcnt(3)\n"
RD"a[124:127],v226 offset:23552\n"
MF"v[36:39],a[100:103],a[0:3],v[36:39]\n"
MF"v[76:79],a[100:103],a[4:7],v[76:79]\n"
WT"vmcnt(26)\n"
RD"a[64:67],v226 offset:24576\n"
MF"v[0:3],a[104:107],a[8:11],v[0:3]\n"
WT"vmcnt(25)\n"
BL"a[224:227],v224,s[8:11],s53 offen offset:1024 sc1\n"
MF"v[40:43],a[104:107],a[12:15],v[40:43]\n"
RD"a[68:71],v226 offset:25600\n"
MF"v[4:7],a[108:111],a[8:11],v[4:7]\n"
MF"v[44:47],a[108:111],a[12:15],v[44:47]\n"
WT"lgkmcnt(3)\n"
RD"a[72:75],v226 offset:26624\n"
MF"v[8:11],a[112:115],a[8:11],v[8:11]\n"
BL"a[228:231],v224,s[8:11],s52 offen offset:2048 sc1\n"
MF"v[48:51],a[112:115],a[12:15],v[48:51]\n"
RD"a[76:79],v226 offset:27648\n"
MF"v[12:15],a[116:119],a[8:11],v[12:15]\n"
MF"v[52:55],a[116:119],a[12:15],v[52:55]\n"
RD"a[80:83],v226 offset:28672\n"
MF"v[16:19],a[120:123],a[8:11],v[16:19]\n"
BL"a[232:235],v224,s[8:11],s53 offen offset:2048 sc1\n"
MF"v[56:59],a[120:123],a[12:15],v[56:59]\n"
WT"lgkmcnt(3)\n"
RD"a[84:87],v226 offset:29696\n"
MF"v[20:23],a[124:127],a[8:11],v[20:23]\n"
MF"v[60:63],a[124:127],a[12:15],v[60:63]\n"
RD"a[88:91],v226 offset:30720\n"
MF"v[24:27],a[64:67],a[8:11],v[24:27]\n"
MF"v[64:67],a[64:67],a[12:15],v[64:67]\n"
RD"a[92:95],v226 offset:31744\n"
MF"v[28:31],a[68:71],a[8:11],v[28:31]\n"
"s_barrier\n"
MF"v[68:71],a[68:71],a[12:15],v[68:71]\n"
WT"lgkmcnt(3)\n"
BL"a[236:239],v224,s[8:11],s52 offen offset:3072 sc1\n"
MF"v[32:35],a[72:75],a[8:11],v[32:35]\n"
RD"a[96:99],v235 offset:0\n"
MF"v[72:75],a[72:75],a[12:15],v[72:75]\n"
RD"a[100:103],v235 offset:1024\n"
MF"v[36:39],a[76:79],a[8:11],v[36:39]\n"
BL"a[240:243],v224,s[8:11],s53 offen offset:3072 sc1\n"
MF"v[76:79],a[76:79],a[12:15],v[76:79]\n"
WT"vmcnt(27)\n"
RD"a[104:107],v235 offset:2048\n"
MF"v[0:3],a[80:83],a[16:19],v[0:3]\n"
WT"vmcnt(25)\n"
MF"v[40:43],a[80:83],a[20:23],v[40:43]\n"
WT"lgkmcnt(3)\n"
RD"a[108:111],v235 offset:3072\n"
MF"v[4:7],a[84:87],a[16:19],v[4:7]\n"
MF"v[44:47],a[84:87],a[20:23],v[44:47]\n"
RD"a[112:115],v235 offset:4096\n"
MF"v[8:11],a[88:91],a[16:19],v[8:11]\n"
MF"v[48:51],a[88:91],a[20:23],v[48:51]\n"
RD"a[116:119],v235 offset:5120\n"
MF"v[12:15],a[92:95],a[16:19],v[12:15]\n"
MF"v[52:55],a[92:95],a[20:23],v[52:55]\n"
WT"lgkmcnt(3)\n"
RD"a[120:123],v235 offset:6144\n"
MF"v[16:19],a[96:99],a[16:19],v[16:19]\n"
MF"v[56:59],a[96:99],a[20:23],v[56:59]\n"
RD"a[124:127],v235 offset:7168\n"
MF"v[20:23],a[100:103],a[16:19],v[20:23]\n"
MF"v[60:63],a[100:103],a[20:23],v[60:63]\n"
RD"a[64:67],v235 offset:8192\n"
MF"v[24:27],a[104:107],a[16:19],v[24:27]\n"
MF"v[64:67],a[104:107],a[20:23],v[64:67]\n"
WT"lgkmcnt(3)\n"
RD"a[68:71],v235 offset:9216\n"
"v_bfe_u32 v238,v224,4,2\n"
MF"v[28:31],a[108:111],a[16:19],v[28:31]\n"
"s_andn2_b32 s63,s62,255\n"
"v_lshlrev_b32 v238,6,v238\n"
MF"v[68:71],a[108:111],a[20:23],v[68:71]\n"
RD"a[72:75],v235 offset:10240\n"
MF"v[32:35],a[112:115],a[16:19],v[32:35]\n"
"buffer_load_dwordx2 v[240:241],v238,s[8:11],s63 offen sc1\n"
MF"v[72:75],a[112:115],a[20:23],v[72:75]\n"
RD"a[76:79],v235 offset:11264\n"
MF"v[36:39],a[116:119],a[16:19],v[36:39]\n"
MF"v[76:79],a[116:119],a[20:23],v[76:79]\n"
WT"vmcnt(24) lgkmcnt(3)\n"
RD"a[80:83],v235 offset:12288\n"
MF"v[0:3],a[120:123],a[24:27],v[0:3]\n"
WT"vmcnt(23)\n"
MF"v[40:43],a[120:123],a[28:31],v[40:43]\n"
RD"a[84:87],v235 offset:13312\n"
MF"v[4:7],a[124:127],a[24:27],v[4:7]\n"
MF"v[44:47],a[124:127],a[28:31],v[44:47]\n"
RD"a[88:91],v235 offset:14336\n"
"s_branch LTJ_%=\n"
"L1099_%=:\n"
"s_mov_b32 s54,0\n"
"L1101_%=:\n"
"s_sleep 2\n"
BL"v[240:243],v238,s[8:11],0 offen sc1\n"
WT"vmcnt(0)\n"
SA"s54,s54,1\n"
"v_cmp_eq_u64 vcc,s[28:29],v[240:241]\n"
"v_cmp_eq_u64_e64 s[50:51],s[28:29],v[242:243]\n"
"s_and_b64 vcc,vcc,s[50:51]\n"
"s_cmp_eq_u64 vcc,exec\n"
"s_cbranch_scc1 L1100_%=\n"
"s_cmp_lt_u32 s54,1048576\n"
"s_cbranch_scc1 L1101_%=\n"
"s_branch L1100_%=\n"
"L1102_%=:\n"
"s_mov_b32 s54,0\n"
"L1104_%=:\n"
"s_sleep 2\n"
"buffer_load_dwordx2 v[240:241],v238,s[8:11],s50 offen sc1\n"
WT"vmcnt(0)\n"
SA"s54,s54,1\n"
"v_cmp_eq_u64 vcc,s[28:29],v[240:241]\n"
"s_cmp_eq_u64 vcc,exec\n"
"s_cbranch_scc1 L1103_%=\n"
"s_cmp_lt_u32 s54,1048576\n"
"s_cbranch_scc1 L1104_%=\n"
"s_branch L1103_%=\n"
"L1106_%=:\n"
"s_mov_b32 s54,0\n"
"L1108_%=:\n"
"s_sleep 2\n"
"buffer_load_dwordx2 v[244:245],v242,s[8:11],s23 offen sc1\n"
WT"vmcnt(0)\n"
SA"s54,s54,1\n"
"v_cmp_eq_u64 vcc,s[28:29],v[244:245]\n"
"s_cmp_eq_u64 vcc,exec\n"
"s_cbranch_scc1 L1107_%=\n"
"s_cmp_lt_u32 s54,1048576\n"
"s_cbranch_scc1 L1108_%=\n"
"s_branch L1107_%=\n"
"LFA_%=:\n"
WT"lgkmcnt(5)\n"
MF"v[0:3],a[64:67],a[0:3],v[208:211]\n"
"s_mov_b32 m0,s40\n"
PK"v192,v192,v193\n"
PK"v193,v194,v195\n"
MF"v[8:11],a[64:67],a[16:19],0\n"
"v_readlane_b32 s43,v228,s42\n"
PK"v194,v196,v197\n"
MF"v[16:19],a[64:67],a[32:35],0\n"
PK"v195,v198,v199\n"
"v_readlane_b32 s44,v229,s42\n"
MF"v[24:27],a[64:67],a[48:51],0\n"
WT"lgkmcnt(0)\n"
BL"v225,s[8:11],s43 offen offset:0 sc1 lds\n"
MF"v[0:3],a[68:71],a[4:7],v[0:3]\n"
SA"s42,s42,1\n"
"v_accvgpr_write_b32 a44,v192\n"
"v_accvgpr_write_b32 a45,v193\n"
MF"v[8:11],a[68:71],a[20:23],v[8:11]\n"
"v_accvgpr_write_b32 a46,v194\n"
"v_accvgpr_write_b32 a47,v195\n"
MF"v[16:19],a[68:71],a[36:39],v[16:19]\n"
PK"v200,v200,v201\n"
PK"v201,v202,v203\n"
MF"v[24:27],a[68:71],a[52:55],v[24:27]\n"
PK"v202,v204,v205\n"
PK"v203,v206,v207\n"
"v_mov_b32 v227,v226\n"
MF"v[0:3],a[72:75],a[8:11],v[0:3]\n"
BL"v225,s[8:11],s43 offen offset:1024 sc1 lds\n"
MF"v[8:11],a[72:75],a[24:27],v[8:11]\n"
RD"a[88:91],v226 offset:6144\n"
"v_accvgpr_write_b32 a60,v200\n"
"v_accvgpr_write_b32 a61,v201\n"
MF"v[16:19],a[72:75],a[40:43],v[16:19]\n"
"s_mul_i32 s48,s32,655360\n"
"v_accvgpr_write_b32 a62,v202\n"
"v_accvgpr_write_b32 a63,v203\n"
MF"v[24:27],a[72:75],a[56:59],v[24:27]\n"
SA"s12,s74,s48\n"
"s_addc_u32 s13,s75,0\n"
MF"v[0:3],a[76:79],a[12:15],v[0:3]\n"
"s_and_b32 s13,s13,0xffff\n"
MF"v[8:11],a[76:79],a[28:31],v[8:11]\n"
"s_mov_b32 s14,655360\n"
RD"a[92:95],v226 offset:7168\n"
MF"v[16:19],a[76:79],a[44:47],v[16:19]\n"
"s_lshl_b32 s48,s33,3\n"
BL"v225,s[8:11],s43 offen offset:2048 sc1 lds\n"
MF"v[24:27],a[76:79],a[60:63],v[24:27]\n"
"s_and_b32 s49,s34,2\n"
MF"v[4:7],a[80:83],a[0:3],v[212:215]\n"
"s_lshl_b32 s49,s49,1\n"
EX"v0,v0\n"
MF"v[12:15],a[80:83],a[16:19],0\n"
SA"s48,s48,s49\n"
RD"a[96:99],v226 offset:8192\n"
MF"v[20:23],a[80:83],a[32:35],0\n"
SA"s48,s48,s30\n"
EX"v1,v1\n"
MF"v[28:31],a[80:83],a[48:51],0\n"
"s_mul_i32 s48,s48,5120\n"
EX"v2,v2\n"
MF"v[4:7],a[84:87],a[4:7],v[4:7]\n"
"s_or_b32 s49,s34,2\n"
BL"v225,s[8:11],s43 offen offset:3072 sc1 lds\n"
MF"v[12:15],a[84:87],a[20:23],v[12:15]\n"
"s_cmp_eq_u32 s49,7\n"
EX"v3,v3\n"
MF"v[20:23],a[84:87],a[36:39],v[20:23]\n"
"s_cselect_b32 s55,s48,2130706432\n"
RD"a[100:103],v226 offset:9216\n"
PK"v0,v0,v1\n"
MF"v[28:31],a[84:87],a[52:55],v[28:31]\n"
WT"lgkmcnt(2)\n"
EX"v8,v8\n"
MF"v[4:7],a[88:91],a[8:11],v[4:7]\n"
SA"m0,s40,16384\n"
EX"v9,v9\n"
PK"v1,v2,v3\n"
MF"v[12:15],a[88:91],a[24:27],v[12:15]\n"
EX"v10,v10\n"
MF"v[20:23],a[88:91],a[40:43],v[20:23]\n"
RD"a[104:107],v226 offset:10240\n"
PK"v8,v8,v9\n"
MF"v[28:31],a[88:91],a[56:59],v[28:31]\n"
SA"s40,s40,32768\n"
BL"v225,s[8:11],s44 offen offset:0 sc1 lds\n"
MF"v[4:7],a[92:95],a[12:15],v[4:7]\n"
"s_cmp_eq_u32 s40,s46\n"
EX"v11,v11\n"
MF"v[12:15],a[92:95],a[28:31],v[12:15]\n"
"s_cselect_b32 s40,s47,s40\n"
EX"v16,v16\n"
MF"v[20:23],a[92:95],a[44:47],v[20:23]\n"
EX"v17,v17\n"
PK"v9,v10,v11\n"
MF"v[28:31],a[92:95],a[60:63],v[28:31]\n"
WT"lgkmcnt(1)\n"
RD"a[108:111],v226 offset:11264\n"
PK"v16,v16,v17\n"
MF"v[32:35],a[96:99],a[0:3],v[216:219]\n"
EX"v18,v18\n"
MF"v[40:43],a[96:99],a[16:19],0\n"
EX"v19,v19\n"
MF"v[48:51],a[96:99],a[32:35],0\n"
BL"v225,s[8:11],s44 offen offset:1024 sc1 lds\n"
EX"v24,v24\n"
MF"v[56:59],a[96:99],a[48:51],0\n"
RD"a[112:115],v226 offset:12288\n"
PK"v17,v18,v19\n"
MF"v[32:35],a[100:103],a[4:7],v[32:35]\n"
EX"v25,v25\n"
MF"v[40:43],a[100:103],a[20:23],v[40:43]\n"
EX"v26,v26\n"
MF"v[48:51],a[100:103],a[36:39],v[48:51]\n"
EX"v27,v27\n"
PK"v24,v24,v25\n"
MF"v[56:59],a[100:103],a[52:55],v[56:59]\n"
WT"lgkmcnt(1)\n"
RD"a[116:119],v226 offset:13312\n"
PK"v25,v26,v27\n"
MF"v[32:35],a[104:107],a[8:11],v[32:35]\n"
BL"v225,s[8:11],s44 offen offset:2048 sc1 lds\n"
EX"v4,v4\n"
MF"v[40:43],a[104:107],a[24:27],v[40:43]\n"
EX"v5,v5\n"
MF"v[48:51],a[104:107],a[40:43],v[48:51]\n"
EX"v6,v6\n"
MF"v[56:59],a[104:107],a[56:59],v[56:59]\n"
RD"a[120:123],v226 offset:14336\n"
"v_readlane_b32 s43,v228,s42\n"
PK"v2,v4,v5\n"
MF"v[32:35],a[108:111],a[12:15],v[32:35]\n"
EX"v7,v7\n"
MF"v[40:43],a[108:111],a[28:31],v[40:43]\n"
EX"v12,v12\n"
MF"v[48:51],a[108:111],a[44:47],v[48:51]\n"
BL"v225,s[8:11],s44 offen offset:3072 sc1 lds\n"
EX"v13,v13\n"
MF"v[56:59],a[108:111],a[60:63],v[56:59]\n"
WT"lgkmcnt(1)\n"
RD"a[124:127],v226 offset:15360\n"
PK"v3,v6,v7\n"
MF"v[36:39],a[112:115],a[0:3],v[220:223]\n"
"s_mov_b32 m0,s40\n"
EX"v14,v14\n"
PK"v10,v12,v13\n"
MF"v[44:47],a[112:115],a[16:19],0\n"
EX"v15,v15\n"
MF"v[52:55],a[112:115],a[32:35],0\n"
EX"v20,v20\n"
MF"v[60:63],a[112:115],a[48:51],0\n"
RD"a[96:99],v227 offset:24576\n"
"v_readlane_b32 s44,v229,s42\n"
PK"v11,v14,v15\n"
MF"v[36:39],a[116:119],a[4:7],v[36:39]\n"
SA"s42,s42,1\n"
BL"v225,s[8:11],s43 offen offset:0 sc1 lds\n"
MF"v[44:47],a[116:119],a[20:23],v[44:47]\n"
EX"v21,v21\n"
MF"v[52:55],a[116:119],a[36:39],v[52:55]\n"
EX"v22,v22\n"
MF"v[60:63],a[116:119],a[52:55],v[60:63]\n"
WT"lgkmcnt(1)\n"
RD"a[100:103],v227 offset:25600\n"
PK"v18,v20,v21\n"
MF"v[36:39],a[120:123],a[8:11],v[36:39]\n"
EX"v23,v23\n"
MF"v[44:47],a[120:123],a[24:27],v[44:47]\n"
BL"v225,s[8:11],s43 offen offset:1024 sc1 lds\n"
MF"v[52:55],a[120:123],a[40:43],v[52:55]\n"
EX"v28,v28\n"
PK"v19,v22,v23\n"
MF"v[60:63],a[120:123],a[56:59],v[60:63]\n"
RD"a[104:107],v227 offset:26624\n"
MF"v[36:39],a[124:127],a[12:15],v[36:39]\n"
EX"v29,v29\n"
MF"v[44:47],a[124:127],a[28:31],v[44:47]\n"
EX"v30,v30\n"
MF"v[52:55],a[124:127],a[44:47],v[52:55]\n"
EX"v31,v31\n"
PK"v26,v28,v29\n"
MF"v[60:63],a[124:127],a[60:63],v[60:63]\n"
WT"lgkmcnt(1)\n"
BL"v225,s[8:11],s43 offen offset:2048 sc1 lds\n"
RD"a[108:111],v227 offset:27648\n"
MF"v[80:83],a[96:99],v[0:3],0\n"
SA"s41,s41,32768\n"
PK"v27,v30,v31\n"
EX"v32,v32\n"
MF"v[112:115],a[96:99],v[8:11],0\n"
"s_cmp_eq_u32 s41,163840\n"
EX"v33,v33\n"
MF"v[144:147],a[96:99],v[16:19],0\n"
"s_cselect_b32 s41,0,s41\n"
EX"v34,v34\n"
"v_add_u32 v226,s41,v224\n"
MF"v[176:179],a[96:99],v[24:27],0\n"
RD"a[112:115],v227 offset:28672\n"
PK"v64,v32,v33\n"
MF"v[84:87],a[100:103],v[0:3],0\n"
EX"v35,v35\n"
MF"v[116:119],a[100:103],v[8:11],0\n"
BL"v225,s[8:11],s43 offen offset:3072 sc1 lds\n"
MF"v[148:151],a[100:103],v[16:19],0\n"
SA"m0,s40,16384\n"
EX"v40,v40\n"
PK"v65,v34,v35\n"
MF"v[180:183],a[100:103],v[24:27],0\n"
WT"lgkmcnt(1)\n"
RD"a[116:119],v227 offset:29696\n"
MF"v[88:91],a[104:107],v[0:3],0\n"
EX"v41,v41\n"
MF"v[120:123],a[104:107],v[8:11],0\n"
EX"v42,v42\n"
MF"v[152:155],a[104:107],v[16:19],0\n"
SA"s40,s40,32768\n"
EX"v43,v43\n"
PK"v68,v40,v41\n"
MF"v[184:187],a[104:107],v[24:27],0\n"
"s_cmp_eq_u32 s40,s46\n"
BL"v225,s[8:11],s44 offen offset:0 sc1 lds\n"
RD"a[120:123],v227 offset:30720\n"
MF"v[92:95],a[108:111],v[0:3],0\n"
"s_cselect_b32 s40,s47,s40\n"
EX"v48,v48\n"
PK"v69,v42,v43\n"
MF"v[124:127],a[108:111],v[8:11],0\n"
EX"v49,v49\n"
MF"v[156:159],a[108:111],v[16:19],0\n"
EX"v50,v50\n"
MF"v[188:191],a[108:111],v[24:27],0\n"
WT"lgkmcnt(1)\n"
RD"a[124:127],v227 offset:31744\n"
PK"v72,v48,v49\n"
MF"v[96:99],a[112:115],v[0:3],0\n"
EX"v51,v51\n"
MF"v[128:131],a[112:115],v[8:11],0\n"
BL"v225,s[8:11],s44 offen offset:1024 sc1 lds\n"
MF"v[160:163],a[112:115],v[16:19],0\n"
EX"v36,v36\n"
PK"v73,v50,v51\n"
MF"v[192:195],a[112:115],v[24:27],0\n"
EX"v37,v37\n"
MF"v[100:103],a[116:119],v[0:3],0\n"
EX"v38,v38\n"
MF"v[132:135],a[116:119],v[8:11],0\n"
EX"v39,v39\n"
PK"v66,v36,v37\n"
MF"v[164:167],a[116:119],v[16:19],0\n"
WT"vmcnt(16) lgkmcnt(0)\n"
"s_barrier\n"
MF"v[196:199],a[116:119],v[24:27],0\n"
BL"v225,s[8:11],s44 offen offset:2048 sc1 lds\n"
RD"a[64:67],v226 offset:0\n"
MF"v[104:107],a[120:123],v[0:3],0\n"
"buffer_store_dwordx2 v[232:233],v230,s[8:11],s98 offen sc1\n"
EX"v44,v44\n"
MF"v[136:139],a[120:123],v[8:11],0\n"
EX"v45,v45\n"
PK"v67,v38,v39\n"
MF"v[168:171],a[120:123],v[16:19],0\n"
EX"v46,v46\n"
MF"v[200:203],a[120:123],v[24:27],0\n"
RD"a[68:71],v226 offset:1024\n"
RD"a[72:75],v226 offset:2048\n"
PK"v70,v44,v45\n"
MF"v[108:111],a[124:127],v[0:3],0\n"
EX"v47,v47\n"
MF"v[140:143],a[124:127],v[8:11],0\n"
BL"v225,s[8:11],s44 offen offset:3072 sc1 lds\n"
MF"v[172:175],a[124:127],v[16:19],0\n"
EX"v52,v52\n"
PK"v71,v46,v47\n"
MF"v[204:207],a[124:127],v[24:27],0\n"
RD"a[76:79],v226 offset:3072\n"
MF"a[132:135],a[128:131],v[0:3],0\n"
EX"v53,v53\n"
MF"a[136:139],a[128:131],v[8:11],0\n"
EX"v54,v54\n"
MF"a[140:143],a[128:131],v[16:19],0\n"
EX"v55,v55\n"
PK"v74,v52,v53\n"
MF"a[144:147],a[128:131],v[24:27],0\n"
PK"v75,v54,v55\n"
RD"a[80:83],v226 offset:4096\n"
RD"a[84:87],v226 offset:5120\n"
"s_branch LDB_%=\n"
"LD2_%=:\n"
WT"lgkmcnt(5)\n"
MF"v[0:3],a[64:67],a[32:35],v[208:211]\n"
"s_mov_b32 m0,s40\n"
EX"v56,v56\n"
MF"v[8:11],a[64:67],a[48:51],0\n"
WT"lgkmcnt(3)\n"
EX"v57,v57\n"
MF"v[0:3],a[68:71],a[36:39],v[0:3]\n"
RD"a[88:91],v226 offset:6144\n"
"v_readlane_b32 s43,v228,s42\n"
MF"v[8:11],a[68:71],a[52:55],v[8:11]\n"
EX"v58,v58\n"
MF"v[0:3],a[72:75],a[40:43],v[0:3]\n"
SA"s41,s41,32768\n"
RD"a[92:95],v226 offset:7168\n"
RD"a[96:99],v226 offset:8192\n"
"v_readlane_b32 s44,v229,s42\n"
MF"v[8:11],a[72:75],a[56:59],v[8:11]\n"
WT"lgkmcnt(5)\n"
BL"v225,s[8:11],s43 offen offset:0 sc1 lds\n"
PK"v76,v56,v57\n"
MF"v[0:3],a[76:79],a[44:47],v[0:3]\n"
SA"s42,s42,1\n"
EX"v59,v59\n"
MF"v[8:11],a[76:79],a[60:63],v[8:11]\n"
WT"lgkmcnt(3)\n"
RD"a[100:103],v226 offset:9216\n"
PK"v77,v58,v59\n"
MF"v[4:7],a[80:83],a[32:35],v[212:215]\n"
"s_cmp_eq_u32 s41,163840\n"
EX"v60,v60\n"
MF"v[12:15],a[80:83],a[48:51],0\n"
"s_cselect_b32 s41,0,s41\n"
EX"v61,v61\n"
"v_add_u32 v227,s41,v224\n"
MF"v[4:7],a[84:87],a[36:39],v[4:7]\n"
BL"v225,s[8:11],s43 offen offset:1024 sc1 lds\n"
MF"v[12:15],a[84:87],a[52:55],v[12:15]\n"
WT"lgkmcnt(1)\n"
RD"a[104:107],v226 offset:10240\n"
PK"v78,v60,v61\n"
MF"v[4:7],a[88:91],a[40:43],v[4:7]\n"
EX"v62,v62\n"
MF"v[12:15],a[88:91],a[56:59],v[12:15]\n"
RD"a[108:111],v226 offset:11264\n"
RD"a[112:115],v226 offset:12288\n"
MF"v[4:7],a[92:95],a[44:47],v[4:7]\n"
EX"v63,v63\n"
MF"v[12:15],a[92:95],a[60:63],v[12:15]\n"
BL"v225,s[8:11],s43 offen offset:2048 sc1 lds\n"
RD"a[116:119],v226 offset:13312\n"
MF"v[16:19],a[96:99],a[32:35],v[216:219]\n"
EX"v0,v0\n"
PK"v79,v62,v63\n"
MF"v[24:27],a[96:99],a[48:51],0\n"
WT"lgkmcnt(4)\n"
EX"v1,v1\n"
MF"v[16:19],a[100:103],a[36:39],v[16:19]\n"
RD"a[120:123],v226 offset:14336\n"
RD"a[124:127],v226 offset:15360\n"
MF"v[24:27],a[100:103],a[52:55],v[24:27]\n"
WT"lgkmcnt(5)\n"
EX"v2,v2\n"
PK"v0,v0,v1\n"
MF"v[16:19],a[104:107],a[40:43],v[16:19]\n"
BL"v225,s[8:11],s43 offen offset:3072 sc1 lds\n"
RD"a[64:67],v226 offset:16384\n"
MF"v[24:27],a[104:107],a[56:59],v[24:27]\n"
WT"lgkmcnt(4)\n"
RD"a[68:71],v226 offset:17408\n"
EX"v3,v3\n"
MF"v[16:19],a[108:111],a[44:47],v[16:19]\n"
SA"m0,s40,16384\n"
RD"a[72:75],v226 offset:18432\n"
RD"a[76:79],v226 offset:19456\n"
MF"v[24:27],a[108:111],a[60:63],v[24:27]\n"
EX"v8,v8\n"
PK"v1,v2,v3\n"
MF"v[20:23],a[112:115],a[32:35],v[220:223]\n"
SA"s40,s40,32768\n"
RD"a[80:83],v226 offset:20480\n"
RD"a[84:87],v226 offset:21504\n"
EX"v9,v9\n"
MF"v[28:31],a[112:115],a[48:51],0\n"
WT"lgkmcnt(6)\n"
BL"v225,s[8:11],s44 offen offset:0 sc1 lds\n"
MF"v[20:23],a[116:119],a[36:39],v[20:23]\n"
"s_cmp_eq_u32 s40,s46\n"
EX"v10,v10\n"
PK"v8,v8,v9\n"
MF"v[28:31],a[116:119],a[52:55],v[28:31]\n"
"s_cselect_b32 s40,s47,s40\n"
RD"a[88:91],v226 offset:22528\n"
RD"a[92:95],v226 offset:23552\n"
MF"v[20:23],a[120:123],a[40:43],v[20:23]\n"
EX"v11,v11\n"
MF"v[28:31],a[120:123],a[56:59],v[28:31]\n"
RD"a[96:99],v226 offset:24576\n"
RD"a[100:103],v226 offset:25600\n"
MF"v[20:23],a[124:127],a[44:47],v[20:23]\n"
BL"v225,s[8:11],s44 offen offset:1024 sc1 lds\n"
RD"a[104:107],v226 offset:26624\n"
MF"v[28:31],a[124:127],a[60:63],v[28:31]\n"
WT"lgkmcnt(5)\n"
EX"v4,v4\n"
PK"v9,v10,v11\n"
MF"v[40:43],a[64:67],a[48:51],v[208:211]\n"
RD"a[108:111],v226 offset:27648\n"
RD"a[112:115],v226 offset:28672\n"
EX"v5,v5\n"
MF"v[40:43],a[68:71],a[52:55],v[40:43]\n"
EX"v6,v6\n"
MF"v[40:43],a[72:75],a[56:59],v[40:43]\n"
RD"a[116:119],v226 offset:29696\n"
RD"a[120:123],v226 offset:30720\n"
PK"v2,v4,v5\n"
MF"v[40:43],a[76:79],a[60:63],v[40:43]\n"
BL"v225,s[8:11],s44 offen offset:2048 sc1 lds\n"
EX"v7,v7\n"
MF"v[44:47],a[80:83],a[48:51],v[212:215]\n"
RD"a[124:127],v226 offset:31744\n"
"v_readlane_b32 s43,v228,s42\n"
MF"v[44:47],a[84:87],a[52:55],v[44:47]\n"
WT"lgkmcnt(5)\n"
EX"v12,v12\n"
PK"v3,v6,v7\n"
MF"v[44:47],a[88:91],a[56:59],v[44:47]\n"
WT"vmcnt(19)\n"
"s_barrier\n"
MF"v[44:47],a[92:95],a[60:63],v[44:47]\n"
EX"v13,v13\n"
MF"v[56:59],a[96:99],a[48:51],v[216:219]\n"
RD"a[96:99],v227 offset:8192\n"
PK"v10,v12,v13\n"
EX"v14,v14\n"
MF"v[56:59],a[100:103],a[52:55],v[56:59]\n"
BL"v225,s[8:11],s44 offen offset:3072 sc1 lds\n"
MF"v[56:59],a[104:107],a[56:59],v[56:59]\n"
WT"lgkmcnt(2)\n"
EX"v15,v15\n"
MF"v[56:59],a[108:111],a[60:63],v[56:59]\n"
"s_mov_b32 m0,s40\n"
RD"a[100:103],v227 offset:9216\n"
RD"a[104:107],v227 offset:10240\n"
MF"v[60:63],a[112:115],a[48:51],v[220:223]\n"
EX"v16,v16\n"
PK"v11,v14,v15\n"
MF"v[60:63],a[116:119],a[52:55],v[60:63]\n"
RD"a[108:111],v227 offset:11264\n"
"v_readlane_b32 s44,v229,s42\n"
MF"v[60:63],a[120:123],a[56:59],v[60:63]\n"
WT"lgkmcnt(4)\n"
EX"v17,v17\n"
MF"v[60:63],a[124:127],a[60:63],v[60:63]\n"
WT"lgkmcnt(3)\n"
BL"v225,s[8:11],s43 offen offset:0 sc1 lds\n"
EX"v18,v18\n"
MF"v[112:115],a[96:99],v[68:71],v[112:115]\n"
SA"s42,s42,1\n"
EX"v19,v19\n"
PK"v16,v16,v17\n"
MF"v[144:147],a[96:99],v[72:75],v[144:147]\n"
SA"s41,s41,32768\n"
EX"v24,v24\n"
MF"v[176:179],a[96:99],v[76:79],v[176:179]\n"
WT"lgkmcnt(1)\n"
BL"v225,s[8:11],s43 offen offset:1024 sc1 lds\n"
MF"v[116:119],a[100:103],v[68:71],v[116:119]\n"
"s_cmp_eq_u32 s41,163840\n"
RD"a[112:115],v227 offset:12288\n"
PK"v17,v18,v19\n"
MF"v[148:151],a[100:103],v[72:75],v[148:151]\n"
"s_cselect_b32 s41,0,s41\n"
EX"v25,v25\n"
"v_add_u32 v226,s41,v224\n"
MF"v[180:183],a[100:103],v[76:79],v[180:183]\n"
RD"a[116:119],v227 offset:13312\n"
RD"a[120:123],v227 offset:14336\n"
MF"v[120:123],a[104:107],v[68:71],v[120:123]\n"
EX"v26,v26\n"
MF"v[152:155],a[104:107],v[72:75],v[152:155]\n"
EX"v27,v27\n"
PK"v24,v24,v25\n"
MF"v[184:187],a[104:107],v[76:79],v[184:187]\n"
WT"lgkmcnt(3)\n"
BL"v225,s[8:11],s43 offen offset:2048 sc1 lds\n"
MF"v[124:127],a[108:111],v[68:71],v[124:127]\n"
EX"v20,v20\n"
MF"v[156:159],a[108:111],v[72:75],v[156:159]\n"
RD"a[124:127],v227 offset:15360\n"
PK"v25,v26,v27\n"
MF"v[188:191],a[108:111],v[76:79],v[188:191]\n"
WT"lgkmcnt(3)\n"
EX"v21,v21\n"
MF"v[128:131],a[112:115],v[68:71],v[128:131]\n"
BL"v225,s[8:11],s43 offen offset:3072 sc1 lds\n"
MF"v[160:163],a[112:115],v[72:75],v[160:163]\n"
SA"m0,s40,16384\n"
EX"v22,v22\n"
MF"v[192:195],a[112:115],v[76:79],v[192:195]\n"
WT"lgkmcnt(1)\n"
EX"v23,v23\n"
PK"v18,v20,v21\n"
MF"v[132:135],a[116:119],v[68:71],v[132:135]\n"
RD"a[64:67],v227 offset:16384\n"
PK"v19,v22,v23\n"
MF"v[164:167],a[116:119],v[72:75],v[164:167]\n"
EX"v28,v28\n"
MF"v[196:199],a[116:119],v[76:79],v[196:199]\n"
SA"s40,s40,32768\n"
BL"v225,s[8:11],s44 offen offset:0 sc1 lds\n"
MF"v[136:139],a[120:123],v[68:71],v[136:139]\n"
"s_cmp_eq_u32 s40,s46\n"
EX"v29,v29\n"
MF"v[168:171],a[120:123],v[72:75],v[168:171]\n"
"s_cselect_b32 s40,s47,s40\n"
RD"a[68:71],v227 offset:17408\n"
RD"a[72:75],v227 offset:18432\n"
MF"v[200:203],a[120:123],v[76:79],v[200:203]\n"
WT"lgkmcnt(3)\n"
EX"v30,v30\n"
MF"v[140:143],a[124:127],v[68:71],v[140:143]\n"
RD"a[76:79],v227 offset:19456\n"
RD"a[80:83],v227 offset:20480\n"
PK"v26,v28,v29\n"
MF"v[172:175],a[124:127],v[72:75],v[172:175]\n"
EX"v31,v31\n"
MF"v[204:207],a[124:127],v[76:79],v[204:207]\n"
BL"v225,s[8:11],s44 offen offset:1024 sc1 lds\n"
MF"a[136:139],a[128:131],v[68:71],a[136:139]\n"
EX"v40,v40\n"
PK"v27,v30,v31\n"
MF"a[140:143],a[128:131],v[72:75],a[140:143]\n"
EX"v41,v41\n"
MF"a[144:147],a[128:131],v[76:79],a[144:147]\n"
WT"lgkmcnt(2)\n"
RD"a[84:87],v227 offset:21504\n"
PK"v68,v40,v41\n"
MF"v[144:147],a[64:67],v[0:3],v[144:147]\n"
BL"v225,s[8:11],s44 offen offset:2048 sc1 lds\n"
MF"v[176:179],a[64:67],v[8:11],v[176:179]\n"
EX"v42,v42\n"
MF"v[148:151],a[68:71],v[0:3],v[148:151]\n"
SA"s51,s59,16384\n"
RD"a[88:91],v227 offset:22528\n"
RD"a[92:95],v227 offset:23552\n"
MF"v[180:183],a[68:71],v[8:11],v[180:183]\n"
EX"v43,v43\n"
MF"v[152:155],a[72:75],v[0:3],v[152:155]\n"
EX"v44,v44\n"
MF"v[184:187],a[72:75],v[8:11],v[184:187]\n"
WT"lgkmcnt(3)\n"
BL"v225,s[8:11],s44 offen offset:3072 sc1 lds\n"
"v_accvgpr_read_b32 v252,a136\n"
MF"v[156:159],a[76:79],v[0:3],v[156:159]\n"
"s_mov_b32 m0,s40\n"
EX"v45,v45\n"
"v_subrev_u32 v253,0x2b800000,v252\n"
MF"v[188:191],a[76:79],v[8:11],v[188:191]\n"
"v_cmp_le_u32 vcc,0x34000000,v253\n"
"s_cmp_lg_u64 vcc,0\n"
"s_cbranch_scc1 L1035_%=\n"
"L1034_%=:\n"
MF"v[160:163],a[80:83],v[0:3],v[160:163]\n"
"v_rcp_f32 v252,v252\n"
EX"v46,v46\n"
MF"v[192:195],a[80:83],v[8:11],v[192:195]\n"
WT"lgkmcnt(0)\n"
RD"a[96:99],v227 offset:24576\n"
RD"a[100:103],v227 offset:25600\n"
"v_readlane_b32 s43,v228,s42\n"
MF"v[164:167],a[84:87],v[0:3],v[164:167]\n"
EX"v47,v47\n"
PK"v69,v42,v43\n"
MF"v[196:199],a[84:87],v[8:11],v[196:199]\n"
RD"a[104:107],v227 offset:26624\n"
RD"a[108:111],v227 offset:27648\n"
EX"v56,v56\n"
MF"v[168:171],a[88:91],v[0:3],v[168:171]\n"
EX"v57,v57\n"
"v_readlane_b32 s44,v229,s42\n"
MF"v[200:203],a[88:91],v[8:11],v[200:203]\n"
SA"s42,s42,1\n"
EX"v58,v58\n"
PK"v70,v44,v45\n"
MF"v[172:175],a[92:95],v[0:3],v[172:175]\n"
RD"a[112:115],v227 offset:28672\n"
MU"v112,v252,v112\n"
MU"v113,v252,v113\n"
MF"v[204:207],a[92:95],v[8:11],v[204:207]\n"
EX"v59,v59\n"
MU"v114,v252,v114\n"
MF"a[140:143],a[128:131],v[0:3],a[140:143]\n"
EX"v60,v60\n"
MU"v115,v252,v115\n"
MF"a[144:147],a[128:131],v[8:11],a[144:147]\n"
WT"lgkmcnt(3)\n"
RD"a[116:119],v227 offset:29696\n"
RD"a[120:123],v227 offset:30720\n"
MU"v116,v252,v116\n"
MF"v[144:147],a[96:99],v[16:19],v[144:147]\n"
EX"v61,v61\n"
MU"v117,v252,v117\n"
MF"v[176:179],a[96:99],v[24:27],v[176:179]\n"
RD"a[124:127],v227 offset:31744\n"
MU"v118,v252,v118\n"
MU"v119,v252,v119\n"
MF"v[148:151],a[100:103],v[16:19],v[148:151]\n"
EX"v62,v62\n"
PK"v71,v46,v47\n"
MF"v[180:183],a[100:103],v[24:27],v[180:183]\n"
WT"lgkmcnt(3)\n"
WT"vmcnt(20)\n"
"s_barrier\n"
MF"v[152:155],a[104:107],v[16:19],v[152:155]\n"
SA"s41,s41,32768\n"
BL"v225,s[8:11],s43 offen offset:0 sc1 lds\n"
BL"v225,s[8:11],s43 offen offset:1024 sc1 lds\n"
MF"v[184:187],a[104:107],v[24:27],v[184:187]\n"
"s_cmp_eq_u32 s41,163840\n"
BL"v225,s[8:11],s43 offen offset:2048 sc1 lds\n"
PK"v112,v112,v113\n"
MF"v[156:159],a[108:111],v[16:19],v[156:159]\n"
"s_cselect_b32 s41,0,s41\n"
EX"v63,v63\n"
PK"v113,v114,v115\n"
MF"v[188:191],a[108:111],v[24:27],v[188:191]\n"
RD"a[64:67],v226 offset:0\n"
RD"a[68:71],v226 offset:1024\n"
"v_add_u32 v227,s41,v224\n"
MF"v[160:163],a[112:115],v[16:19],v[160:163]\n"
RD"a[72:75],v226 offset:2048\n"
PK"v114,v116,v117\n"
PK"v115,v118,v119\n"
MF"v[192:195],a[112:115],v[24:27],v[192:195]\n"
WT"lgkmcnt(3)\n"
BS"v[112:115],v224,s[8:11],s51 offen offset:0 sc1\n"
BL"v225,s[8:11],s43 offen offset:3072 sc1 lds\n"
MF"v[164:167],a[116:119],v[16:19],v[164:167]\n"
SA"m0,s40,16384\n"
RD"a[76:79],v226 offset:3072\n"
MU"v120,v252,v120\n"
MU"v121,v252,v121\n"
MF"v[196:199],a[116:119],v[24:27],v[196:199]\n"
RD"a[80:83],v226 offset:4096\n"
PK"v76,v56,v57\n"
MU"v122,v252,v122\n"
MF"v[168:171],a[120:123],v[16:19],v[168:171]\n"
RD"a[84:87],v226 offset:5120\n"
MU"v123,v252,v123\n"
MU"v124,v252,v124\n"
MF"v[200:203],a[120:123],v[24:27],v[200:203]\n"
SA"s40,s40,32768\n"
RD"a[88:91],v226 offset:6144\n"
MU"v125,v252,v125\n"
MU"v126,v252,v126\n"
MF"v[172:175],a[124:127],v[16:19],v[172:175]\n"
"s_cmp_eq_u32 s40,s46\n"
BL"v225,s[8:11],s44 offen offset:0 sc1 lds\n"
RD"a[92:95],v226 offset:7168\n"
MF"v[204:207],a[124:127],v[24:27],v[204:207]\n"
"s_cselect_b32 s40,s47,s40\n"
RD"a[96:99],v226 offset:8192\n"
MU"v127,v252,v127\n"
PK"v120,v120,v121\n"
MF"a[140:143],a[128:131],v[16:19],a[140:143]\n"
RD"a[100:103],v226 offset:9216\n"
PK"v121,v122,v123\n"
PK"v77,v58,v59\n"
MF"a[144:147],a[128:131],v[24:27],a[144:147]\n"
WT"lgkmcnt(6)\n"
RD"a[104:107],v226 offset:10240\n"
PK"v122,v124,v125\n"
PK"v123,v126,v127\n"
MF"v[176:179],a[64:67],v[68:71],v[176:179]\n"
BS"v[120:123],v224,s[8:11],s51 offen offset:1024 sc1\n"
RD"a[108:111],v226 offset:11264\n"
MF"v[180:183],a[68:71],v[68:71],v[180:183]\n"
BL"v225,s[8:11],s44 offen offset:1024 sc1 lds\n"
RD"a[112:115],v226 offset:12288\n"
MF"v[184:187],a[72:75],v[68:71],v[184:187]\n"
RD"a[116:119],v226 offset:13312\n"
MU"v128,v252,v128\n"
MU"v129,v252,v129\n"
MF"v[188:191],a[76:79],v[68:71],v[188:191]\n"
WT"lgkmcnt(6)\n"
RD"a[120:123],v226 offset:14336\n"
MU"v130,v252,v130\n"
MU"v131,v252,v131\n"
MF"v[192:195],a[80:83],v[68:71],v[192:195]\n"
RD"a[124:127],v226 offset:15360\n"
PK"v78,v60,v61\n"
MU"v132,v252,v132\n"
MF"v[196:199],a[84:87],v[68:71],v[196:199]\n"
BL"v225,s[8:11],s44 offen offset:2048 sc1 lds\n"
MU"v133,v252,v133\n"
MF"v[200:203],a[88:91],v[68:71],v[200:203]\n"
PK"v79,v62,v63\n"
MU"v134,v252,v134\n"
MU"v135,v252,v135\n"
MF"v[204:207],a[92:95],v[68:71],v[204:207]\n"
WT"vmcnt(17)\n"
"s_barrier\n"
MF"a[144:147],a[128:131],v[68:71],a[144:147]\n"
WT"lgkmcnt(3)\n"
RD"a[64:67],v227 offset:0\n"
PK"v128,v128,v129\n"
PK"v129,v130,v131\n"
MF"v[176:179],a[96:99],v[76:79],v[176:179]\n"
RD"a[68:71],v227 offset:1024\n"
PK"v130,v132,v133\n"
PK"v131,v134,v135\n"
MF"v[180:183],a[100:103],v[76:79],v[180:183]\n"
BS"v[128:131],v224,s[8:11],s51 offen offset:2048 sc1\n"
MU"v136,v252,v136\n"
MF"v[184:187],a[104:107],v[76:79],v[184:187]\n"
BL"v225,s[8:11],s44 offen offset:3072 sc1 lds\n"
MU"v137,v252,v137\n"
MF"v[188:191],a[108:111],v[76:79],v[188:191]\n"
RD"a[72:75],v227 offset:2048\n"
MU"v138,v252,v138\n"
MU"v139,v252,v139\n"
MF"v[192:195],a[112:115],v[76:79],v[192:195]\n"
WT"lgkmcnt(3)\n"
RD"a[76:79],v227 offset:3072\n"
MU"v140,v252,v140\n"
MU"v141,v252,v141\n"
MF"v[196:199],a[116:119],v[76:79],v[196:199]\n"
RD"a[80:83],v227 offset:4096\n"
MU"v142,v252,v142\n"
MU"v143,v252,v143\n"
MF"v[200:203],a[120:123],v[76:79],v[200:203]\n"
PK"v136,v136,v137\n"
PK"v137,v138,v139\n"
PK"v138,v140,v141\n"
MF"v[204:207],a[124:127],v[76:79],v[204:207]\n"
PK"v139,v142,v143\n"
MF"a[144:147],a[128:131],v[76:79],a[144:147]\n"
BS"v[136:139],v224,s[8:11],s51 offen offset:3072 sc1\n"
RD"a[84:87],v227 offset:5120\n"
"s_branch LTL_%=\n"
"LEV_%=:\n"
"s_mov_b32 s14,0\n"
WT"lgkmcnt(5)\n"
MF"v[0:3],a[64:67],a[0:3],0\n"
SA"s95,s40,32768\n"
EX"v56,v56\n"
MF"v[8:11],a[64:67],a[16:19],0\n"
"s_cmp_eq_u32 s95,s46\n"
"s_cselect_b32 s95,s47,s95\n"
MF"v[16:19],a[64:67],a[32:35],0\n"
EX"v57,v57\n"
MF"v[24:27],a[64:67],a[48:51],0\n"
WT"lgkmcnt(4)\n"
PK"v76,v56,v57\n"
MF"v[0:3],a[68:71],a[4:7],v[0:3]\n"
SA"m0,s95,0\n"
EX"v58,v58\n"
MF"v[8:11],a[68:71],a[20:23],v[8:11]\n"
"v_mov_b32 v227,v226\n"
MF"v[16:19],a[68:71],a[36:39],v[16:19]\n"
EX"v59,v59\n"
MF"v[24:27],a[68:71],a[52:55],v[24:27]\n"
WT"lgkmcnt(0)\n"
BL"v235,s[24:27],s52 offen lds\n"
MF"v[0:3],a[72:75],a[8:11],v[0:3]\n"
SA"s52,s52,s62\n"
EX"v60,v60\n"
MF"v[8:11],a[72:75],a[24:27],v[8:11]\n"
RD"a[88:91],v226 offset:6144\n"
PK"v77,v58,v59\n"
MF"v[16:19],a[72:75],a[40:43],v[16:19]\n"
EX"v61,v61\n"
MF"v[24:27],a[72:75],a[56:59],v[24:27]\n"
PK"v78,v60,v61\n"
MF"v[0:3],a[76:79],a[12:15],v[0:3]\n"
SA"m0,s95,1024\n"
EX"v62,v62\n"
MF"v[8:11],a[76:79],a[28:31],v[8:11]\n"
RD"a[92:95],v226 offset:7168\n"
MF"v[16:19],a[76:79],a[44:47],v[16:19]\n"
EX"v63,v63\n"
MF"v[24:27],a[76:79],a[60:63],v[24:27]\n"
BL"v235,s[24:27],s52 offen lds\n"
MF"v[4:7],a[80:83],a[0:3],0\n"
SA"s52,s52,s62\n"
EX"v0,v0\n"
MF"v[12:15],a[80:83],a[16:19],0\n"
RD"a[96:99],v226 offset:8192\n"
PK"v79,v62,v63\n"
MF"v[20:23],a[80:83],a[32:35],0\n"
EX"v1,v1\n"
MF"v[28:31],a[80:83],a[48:51],0\n"
PK"v0,v0,v1\n"
MF"v[4:7],a[84:87],a[4:7],v[4:7]\n"
SA"m0,s95,2048\n"
EX"v2,v2\n"
MF"v[12:15],a[84:87],a[20:23],v[12:15]\n"
RD"a[100:103],v226 offset:9216\n"
MF"v[20:23],a[84:87],a[36:39],v[20:23]\n"
EX"v3,v3\n"
MF"v[28:31],a[84:87],a[52:55],v[28:31]\n"
WT"lgkmcnt(2)\n"
BL"v235,s[24:27],s52 offen lds\n"
MF"v[4:7],a[88:91],a[8:11],v[4:7]\n"
SA"s52,s52,s62\n"
EX"v8,v8\n"
MF"v[12:15],a[88:91],a[24:27],v[12:15]\n"
RD"a[104:107],v226 offset:10240\n"
PK"v1,v2,v3\n"
MF"v[20:23],a[88:91],a[40:43],v[20:23]\n"
EX"v9,v9\n"
MF"v[28:31],a[88:91],a[56:59],v[28:31]\n"
PK"v8,v8,v9\n"
MF"v[4:7],a[92:95],a[12:15],v[4:7]\n"
SA"m0,s95,3072\n"
EX"v10,v10\n"
MF"v[12:15],a[92:95],a[28:31],v[12:15]\n"
RD"a[108:111],v226 offset:11264\n"
MF"v[20:23],a[92:95],a[44:47],v[20:23]\n"
EX"v11,v11\n"
MF"v[28:31],a[92:95],a[60:63],v[28:31]\n"
WT"lgkmcnt(2)\n"
BL"v235,s[24:27],s52 offen lds\n"
MF"v[32:35],a[96:99],a[0:3],0\n"
SA"s52,s52,s62\n"
EX"v16,v16\n"
MF"v[40:43],a[96:99],a[16:19],0\n"
RD"a[112:115],v226 offset:12288\n"
PK"v9,v10,v11\n"
MF"v[48:51],a[96:99],a[32:35],0\n"
EX"v17,v17\n"
MF"v[56:59],a[96:99],a[48:51],0\n"
PK"v16,v16,v17\n"
MF"v[32:35],a[100:103],a[4:7],v[32:35]\n"
SA"m0,s95,16384\n"
EX"v18,v18\n"
MF"v[40:43],a[100:103],a[20:23],v[40:43]\n"
RD"a[116:119],v226 offset:13312\n"
MF"v[48:51],a[100:103],a[36:39],v[48:51]\n"
EX"v19,v19\n"
MF"v[56:59],a[100:103],a[52:55],v[56:59]\n"
WT"lgkmcnt(2)\n"
BL"v235,s[24:27],s52 offen lds\n"
MF"v[32:35],a[104:107],a[8:11],v[32:35]\n"
SA"s52,s52,s62\n"
EX"v24,v24\n"
MF"v[40:43],a[104:107],a[24:27],v[40:43]\n"
RD"a[120:123],v226 offset:14336\n"
PK"v17,v18,v19\n"
MF"v[48:51],a[104:107],a[40:43],v[48:51]\n"
EX"v25,v25\n"
MF"v[56:59],a[104:107],a[56:59],v[56:59]\n"
PK"v24,v24,v25\n"
MF"v[32:35],a[108:111],a[12:15],v[32:35]\n"
SA"m0,s95,17408\n"
EX"v26,v26\n"
MF"v[40:43],a[108:111],a[28:31],v[40:43]\n"
RD"a[124:127],v226 offset:15360\n"
MF"v[48:51],a[108:111],a[44:47],v[48:51]\n"
EX"v27,v27\n"
MF"v[56:59],a[108:111],a[60:63],v[56:59]\n"
WT"lgkmcnt(2)\n"
BL"v235,s[24:27],s52 offen lds\n"
MF"v[36:39],a[112:115],a[0:3],0\n"
SA"s52,s52,s62\n"
EX"v4,v4\n"
MF"v[44:47],a[112:115],a[16:19],0\n"
RD"a[64:67],v227 offset:16384\n"
PK"v25,v26,v27\n"
MF"v[52:55],a[112:115],a[32:35],0\n"
EX"v5,v5\n"
MF"v[60:63],a[112:115],a[48:51],0\n"
PK"v2,v4,v5\n"
MF"v[36:39],a[116:119],a[4:7],v[36:39]\n"
SA"m0,s95,18432\n"
EX"v6,v6\n"
MF"v[44:47],a[116:119],a[20:23],v[44:47]\n"
RD"a[68:71],v227 offset:17408\n"
MF"v[52:55],a[116:119],a[36:39],v[52:55]\n"
EX"v7,v7\n"
MF"v[60:63],a[116:119],a[52:55],v[60:63]\n"
WT"lgkmcnt(2)\n"
BL"v235,s[24:27],s52 offen lds\n"
MF"v[36:39],a[120:123],a[8:11],v[36:39]\n"
SA"s52,s52,s62\n"
EX"v12,v12\n"
MF"v[44:47],a[120:123],a[24:27],v[44:47]\n"
RD"a[72:75],v227 offset:18432\n"
"v_readlane_b32 s43,v228,s42\n"
MF"v[52:55],a[120:123],a[40:43],v[52:55]\n"
EX"v13,v13\n"
MF"v[60:63],a[120:123],a[56:59],v[60:63]\n"
"s_mov_b32 m0,s40\n"
PK"v3,v6,v7\n"
PK"v10,v12,v13\n"
MF"v[36:39],a[124:127],a[12:15],v[36:39]\n"
EX"v14,v14\n"
MF"v[44:47],a[124:127],a[28:31],v[44:47]\n"
RD"a[76:79],v227 offset:19456\n"
"v_readlane_b32 s44,v229,s42\n"
MF"v[52:55],a[124:127],a[44:47],v[52:55]\n"
EX"v15,v15\n"
MF"v[60:63],a[124:127],a[60:63],v[60:63]\n"
WT"lgkmcnt(2)\n"
BL"v225,s[8:11],s43 offen offset:0 sc1 lds\n"
MF"v[80:83],a[64:67],v[64:67],v[80:83]\n"
SA"s42,s42,1\n"
EX"v20,v20\n"
MF"v[112:115],a[64:67],v[68:71],v[112:115]\n"
SA"s41,s41,32768\n"
RD"a[80:83],v227 offset:20480\n"
PK"v11,v14,v15\n"
MF"v[144:147],a[64:67],v[72:75],v[144:147]\n"
"s_cmp_eq_u32 s41,163840\n"
EX"v21,v21\n"
MF"v[176:179],a[64:67],v[76:79],v[176:179]\n"
"s_cselect_b32 s41,0,s41\n"
PK"v18,v20,v21\n"
"v_add_u32 v226,s41,v224\n"
MF"v[84:87],a[68:71],v[64:67],v[84:87]\n"
EX"v22,v22\n"
MF"v[116:119],a[68:71],v[68:71],v[116:119]\n"
RD"a[84:87],v227 offset:21504\n"
MF"v[148:151],a[68:71],v[72:75],v[148:151]\n"
EX"v23,v23\n"
MF"v[180:183],a[68:71],v[76:79],v[180:183]\n"
WT"lgkmcnt(2)\n"
BL"v225,s[8:11],s43 offen offset:1024 sc1 lds\n"
MF"v[88:91],a[72:75],v[64:67],v[88:91]\n"
EX"v28,v28\n"
MF"v[120:123],a[72:75],v[68:71],v[120:123]\n"
RD"a[88:91],v227 offset:22528\n"
PK"v19,v22,v23\n"
MF"v[152:155],a[72:75],v[72:75],v[152:155]\n"
EX"v29,v29\n"
MF"v[184:187],a[72:75],v[76:79],v[184:187]\n"
PK"v26,v28,v29\n"
MF"v[92:95],a[76:79],v[64:67],v[92:95]\n"
EX"v30,v30\n"
MF"v[124:127],a[76:79],v[68:71],v[124:127]\n"
RD"a[92:95],v227 offset:23552\n"
MF"v[156:159],a[76:79],v[72:75],v[156:159]\n"
EX"v31,v31\n"
MF"v[188:191],a[76:79],v[76:79],v[188:191]\n"
WT"lgkmcnt(2)\n"
BL"v225,s[8:11],s43 offen offset:2048 sc1 lds\n"
MF"v[96:99],a[80:83],v[64:67],v[96:99]\n"
EX"v32,v32\n"
MF"v[128:131],a[80:83],v[68:71],v[128:131]\n"
RD"a[96:99],v227 offset:24576\n"
PK"v27,v30,v31\n"
MF"v[160:163],a[80:83],v[72:75],v[160:163]\n"
EX"v33,v33\n"
MF"v[192:195],a[80:83],v[76:79],v[192:195]\n"
MF"v[100:103],a[84:87],v[64:67],v[100:103]\n"
EX"v34,v34\n"
MF"v[132:135],a[84:87],v[68:71],v[132:135]\n"
RD"a[100:103],v227 offset:25600\n"
MF"v[164:167],a[84:87],v[72:75],v[164:167]\n"
EX"v35,v35\n"
MF"v[196:199],a[84:87],v[76:79],v[196:199]\n"
WT"lgkmcnt(2)\n"
BL"v225,s[8:11],s43 offen offset:3072 sc1 lds\n"
MF"v[104:107],a[88:91],v[64:67],v[104:107]\n"
EX"v40,v40\n"
MF"v[136:139],a[88:91],v[68:71],v[136:139]\n"
RD"a[104:107],v227 offset:26624\n"
MF"v[168:171],a[88:91],v[72:75],v[168:171]\n"
EX"v41,v41\n"
MF"v[200:203],a[88:91],v[76:79],v[200:203]\n"
SA"m0,s40,16384\n"
MF"v[108:111],a[92:95],v[64:67],v[108:111]\n"
EX"v42,v42\n"
MF"v[140:143],a[92:95],v[68:71],v[140:143]\n"
RD"a[108:111],v227 offset:27648\n"
MF"v[172:175],a[92:95],v[72:75],v[172:175]\n"
EX"v43,v43\n"
MF"v[204:207],a[92:95],v[76:79],v[204:207]\n"
BL"v225,s[8:11],s44 offen offset:0 sc1 lds\n"
MF"a[132:135],a[128:131],v[64:67],a[132:135]\n"
SA"s40,s40,32768\n"
EX"v48,v48\n"
PK"v64,v32,v33\n"
MF"a[136:139],a[128:131],v[68:71],a[136:139]\n"
"s_cmp_eq_u32 s40,s46\n"
RD"a[112:115],v227 offset:28672\n"
PK"v65,v34,v35\n"
MF"a[140:143],a[128:131],v[72:75],a[140:143]\n"
"s_cselect_b32 s40,s47,s40\n"
EX"v49,v49\n"
MF"a[144:147],a[128:131],v[76:79],a[144:147]\n"
WT"lgkmcnt(2)\n"
BL"v[240:243],v238,s[8:11],0 offen sc1\n"
PK"v68,v40,v41\n"
MF"v[80:83],a[96:99],v[0:3],v[80:83]\n"
EX"v50,v50\n"
MF"v[112:115],a[96:99],v[8:11],v[112:115]\n"
RD"a[116:119],v227 offset:29696\n"
PK"v69,v42,v43\n"
MF"v[144:147],a[96:99],v[16:19],v[144:147]\n"
EX"v51,v51\n"
MF"v[176:179],a[96:99],v[24:27],v[176:179]\n"
BL"v225,s[8:11],s44 offen offset:1024 sc1 lds\n"
MF"v[84:87],a[100:103],v[0:3],v[84:87]\n"
EX"v36,v36\n"
MF"v[116:119],a[100:103],v[8:11],v[116:119]\n"
RD"a[120:123],v227 offset:30720\n"
PK"v72,v48,v49\n"
MF"v[148:151],a[100:103],v[16:19],v[148:151]\n"
EX"v37,v37\n"
MF"v[180:183],a[100:103],v[24:27],v[180:183]\n"
PK"v73,v50,v51\n"
PK"v66,v36,v37\n"
MF"v[88:91],a[104:107],v[0:3],v[88:91]\n"
EX"v38,v38\n"
MF"v[120:123],a[104:107],v[8:11],v[120:123]\n"
RD"a[124:127],v227 offset:31744\n"
MF"v[152:155],a[104:107],v[16:19],v[152:155]\n"
EX"v39,v39\n"
MF"v[184:187],a[104:107],v[24:27],v[184:187]\n"
WT"lgkmcnt(2)\n"
BL"v225,s[8:11],s44 offen offset:2048 sc1 lds\n"
MF"v[92:95],a[108:111],v[0:3],v[92:95]\n"
EX"v44,v44\n"
MF"v[124:127],a[108:111],v[8:11],v[124:127]\n"
PK"v67,v38,v39\n"
MF"v[156:159],a[108:111],v[16:19],v[156:159]\n"
EX"v45,v45\n"
MF"v[188:191],a[108:111],v[24:27],v[188:191]\n"
PK"v70,v44,v45\n"
MF"v[96:99],a[112:115],v[0:3],v[96:99]\n"
EX"v46,v46\n"
MF"v[128:131],a[112:115],v[8:11],v[128:131]\n"
MF"v[160:163],a[112:115],v[16:19],v[160:163]\n"
EX"v47,v47\n"
MF"v[192:195],a[112:115],v[24:27],v[192:195]\n"
BL"v225,s[8:11],s44 offen offset:3072 sc1 lds\n"
MF"v[100:103],a[116:119],v[0:3],v[100:103]\n"
WT"vmcnt(24) lgkmcnt(0)\n"
"s_barrier\n"
MF"v[132:135],a[116:119],v[8:11],v[132:135]\n"
RD"a[64:67],v226 offset:0\n"
PK"v71,v46,v47\n"
MF"v[164:167],a[116:119],v[16:19],v[164:167]\n"
EX"v52,v52\n"
MF"v[196:199],a[116:119],v[24:27],v[196:199]\n"
RD"a[68:71],v226 offset:1024\n"
MF"v[104:107],a[120:123],v[0:3],v[104:107]\n"
EX"v53,v53\n"
MF"v[136:139],a[120:123],v[8:11],v[136:139]\n"
RD"a[72:75],v226 offset:2048\n"
PK"v74,v52,v53\n"
MF"v[168:171],a[120:123],v[16:19],v[168:171]\n"
EX"v54,v54\n"
MF"v[200:203],a[120:123],v[24:27],v[200:203]\n"
MF"v[108:111],a[124:127],v[0:3],v[108:111]\n"
EX"v55,v55\n"
MF"v[140:143],a[124:127],v[8:11],v[140:143]\n"
RD"a[76:79],v226 offset:3072\n"
PK"v75,v54,v55\n"
MF"v[172:175],a[124:127],v[16:19],v[172:175]\n"
MF"v[204:207],a[124:127],v[24:27],v[204:207]\n"
RD"a[80:83],v226 offset:4096\n"
MF"a[132:135],a[128:131],v[0:3],a[132:135]\n"
MF"a[136:139],a[128:131],v[8:11],a[136:139]\n"
RD"a[84:87],v226 offset:5120\n"
MF"a[140:143],a[128:131],v[16:19],a[140:143]\n"
MF"a[144:147],a[128:131],v[24:27],a[144:147]\n"
WT"lgkmcnt(5)\n"
MF"v[0:3],a[64:67],a[0:3],0\n"
SA"s95,s40,32768\n"
EX"v56,v56\n"
MF"v[8:11],a[64:67],a[16:19],0\n"
"s_cmp_eq_u32 s95,s46\n"
"v_lshrrev_b32 v237,1,v224\n"
"v_and_b32 v237,0x78,v237\n"
MF"v[16:19],a[64:67],a[32:35],0\n"
"s_cselect_b32 s95,s47,s95\n"
EX"v57,v57\n"
MF"v[24:27],a[64:67],a[48:51],0\n"
WT"lgkmcnt(1)\n"
"v_and_b32 v239,0x300,v224\n"
"v_add3_u32 v237,v237,v239,s40\n"
MF"v[0:3],a[68:71],a[4:7],v[0:3]\n"
SA"m0,s95,0\n"
EX"v58,v58\n"
MF"v[8:11],a[68:71],a[20:23],v[8:11]\n"
PK"v76,v56,v57\n"
"v_mov_b32 v227,v226\n"
MF"v[16:19],a[68:71],a[36:39],v[16:19]\n"
EX"v59,v59\n"
MF"v[24:27],a[68:71],a[52:55],v[24:27]\n"
BL"v235,s[24:27],s52 offen lds\n"
MF"v[0:3],a[72:75],a[8:11],v[0:3]\n"
SA"s52,s52,s62\n"
EX"v60,v60\n"
MF"v[8:11],a[72:75],a[24:27],v[8:11]\n"
RD"a[88:91],v226 offset:6144\n"
PK"v77,v58,v59\n"
MF"v[16:19],a[72:75],a[40:43],v[16:19]\n"
EX"v61,v61\n"
MF"v[24:27],a[72:75],a[56:59],v[24:27]\n"
WT"vmcnt(4)\n"
"v_cmp_eq_u64 vcc,s[28:29],v[240:241]\n"
"v_cmp_eq_u64_e64 s[50:51],s[28:29],v[242:243]\n"
"s_and_b64 vcc,vcc,s[50:51]\n"
"s_cmp_eq_u64 vcc,exec\n"
"s_cbranch_scc0 L1099_%=\n"
"L1100_%=:\n"
MF"v[0:3],a[76:79],a[12:15],v[0:3]\n"
SA"m0,s95,1024\n"
EX"v62,v62\n"
MF"v[8:11],a[76:79],a[28:31],v[8:11]\n"
RD"a[92:95],v226 offset:7168\n"
PK"v78,v60,v61\n"
MF"v[16:19],a[76:79],a[44:47],v[16:19]\n"
EX"v63,v63\n"
MF"v[24:27],a[76:79],a[60:63],v[24:27]\n"
BL"v235,s[24:27],s52 offen lds\n"
MF"v[4:7],a[80:83],a[0:3],0\n"
SA"s52,s52,s62\n"
EX"v0,v0\n"
MF"v[12:15],a[80:83],a[16:19],0\n"
RD"a[96:99],v226 offset:8192\n"
PK"v79,v62,v63\n"
MF"v[20:23],a[80:83],a[32:35],0\n"
EX"v1,v1\n"
MF"v[28:31],a[80:83],a[48:51],0\n"
WT"lgkmcnt(2)\n"
PK"v0,v0,v1\n"
MF"v[4:7],a[84:87],a[4:7],v[4:7]\n"
SA"m0,s95,2048\n"
EX"v2,v2\n"
MF"v[12:15],a[84:87],a[20:23],v[12:15]\n"
RD"a[100:103],v226 offset:9216\n"
MF"v[20:23],a[84:87],a[36:39],v[20:23]\n"
EX"v3,v3\n"
MF"v[28:31],a[84:87],a[52:55],v[28:31]\n"
BL"v235,s[24:27],s52 offen lds\n"
MF"v[4:7],a[88:91],a[8:11],v[4:7]\n"
SA"s52,s52,s62\n"
EX"v8,v8\n"
MF"v[12:15],a[88:91],a[24:27],v[12:15]\n"
RD"a[104:107],v226 offset:10240\n"
PK"v1,v2,v3\n"
MF"v[20:23],a[88:91],a[40:43],v[20:23]\n"
EX"v9,v9\n"
MF"v[28:31],a[88:91],a[56:59],v[28:31]\n"
WT"lgkmcnt(2)\n"
RT"a[148:149],v237 offset:0\n"
RT"a[150:151],v237 offset:128\n"
MF"v[4:7],a[92:95],a[12:15],v[4:7]\n"
SA"m0,s95,3072\n"
EX"v10,v10\n"
MF"v[12:15],a[92:95],a[28:31],v[12:15]\n"
RD"a[108:111],v226 offset:11264\n"
PK"v8,v8,v9\n"
MF"v[20:23],a[92:95],a[44:47],v[20:23]\n"
EX"v11,v11\n"
MF"v[28:31],a[92:95],a[60:63],v[28:31]\n"
RT"a[152:153],v237 offset:1024\n"
RT"a[154:155],v237 offset:1152\n"
MF"v[32:35],a[96:99],a[0:3],0\n"
BL"v235,s[24:27],s52 offen lds\n"
MF"v[40:43],a[96:99],a[16:19],0\n"
SA"s52,s52,s62\n"
EX"v16,v16\n"
MF"v[48:51],a[96:99],a[32:35],0\n"
RD"a[112:115],v226 offset:12288\n"
PK"v9,v10,v11\n"
MF"v[56:59],a[96:99],a[48:51],0\n"
WT"lgkmcnt(4)\n"
EX"v17,v17\n"
MF"v[32:35],a[100:103],a[4:7],v[32:35]\n"
SA"m0,s95,16384\n"
RT"a[156:157],v237 offset:2048\n"
RT"a[158:159],v237 offset:2176\n"
MF"v[40:43],a[100:103],a[20:23],v[40:43]\n"
EX"v18,v18\n"
MF"v[48:51],a[100:103],a[36:39],v[48:51]\n"
RD"a[116:119],v226 offset:13312\n"
PK"v16,v16,v17\n"
MF"v[56:59],a[100:103],a[52:55],v[56:59]\n"
EX"v19,v19\n"
MF"v[32:35],a[104:107],a[8:11],v[32:35]\n"
RT"a[160:161],v237 offset:3072\n"
RT"a[162:163],v237 offset:3200\n"
MF"v[40:43],a[104:107],a[24:27],v[40:43]\n"
BL"v235,s[24:27],s52 offen lds\n"
MF"v[48:51],a[104:107],a[40:43],v[48:51]\n"
SA"s52,s52,s62\n"
EX"v24,v24\n"
MF"v[56:59],a[104:107],a[56:59],v[56:59]\n"
WT"lgkmcnt(3)\n"
RD"a[120:123],v226 offset:14336\n"
RT"a[164:165],v237 offset:16384\n"
MF"v[32:35],a[108:111],a[12:15],v[32:35]\n"
SA"m0,s95,17408\n"
EX"v25,v25\n"
MF"v[40:43],a[108:111],a[28:31],v[40:43]\n"
RT"a[166:167],v237 offset:16512\n"
RD"a[124:127],v226 offset:15360\n"
MF"v[48:51],a[108:111],a[44:47],v[48:51]\n"
EX"v26,v26\n"
MF"v[56:59],a[108:111],a[60:63],v[56:59]\n"
EX"v27,v27\n"
PK"v17,v18,v19\n"
MF"v[36:39],a[112:115],a[0:3],0\n"
RT"a[168:169],v237 offset:17408\n"
RT"a[170:171],v237 offset:17536\n"
MF"v[44:47],a[112:115],a[16:19],0\n"
BL"v235,s[24:27],s52 offen lds\n"
MF"v[52:55],a[112:115],a[32:35],0\n"
SA"s52,s52,s62\n"
EX"v4,v4\n"
MF"v[60:63],a[112:115],a[48:51],0\n"
WT"lgkmcnt(4)\n"
RD"a[64:67],v227 offset:16384\n"
RT"a[172:173],v237 offset:18432\n"
PK"v24,v24,v25\n"
MF"v[36:39],a[116:119],a[4:7],v[36:39]\n"
SA"m0,s95,18432\n"
EX"v5,v5\n"
MF"v[44:47],a[116:119],a[20:23],v[44:47]\n"
RT"a[174:175],v237 offset:18560\n"
RD"a[68:71],v227 offset:17408\n"
MF"v[52:55],a[116:119],a[36:39],v[52:55]\n"
EX"v6,v6\n"
MF"v[60:63],a[116:119],a[52:55],v[60:63]\n"
EX"v7,v7\n"
PK"v25,v26,v27\n"
MF"v[36:39],a[120:123],a[8:11],v[36:39]\n"
"s_cmp_gt_u32 s92,0\n"
BL"v235,s[24:27],s52 offen lds\n"
MF"v[44:47],a[120:123],a[24:27],v[44:47]\n"
"s_cselect_b32 s63,s63,1065353216\n"
EX"v12,v12\n"
MF"v[52:55],a[120:123],a[40:43],v[52:55]\n"
SA"s52,s52,s62\n"
RD"a[72:75],v227 offset:18432\n"
"v_readlane_b32 s43,v228,s42\n"
MF"v[60:63],a[120:123],a[56:59],v[60:63]\n"
WT"lgkmcnt(3)\n"
EX"v13,v13\n"
PK"v2,v4,v5\n"
MF"v[36:39],a[124:127],a[12:15],v[36:39]\n"
"s_mov_b32 m0,s40\n"
BL"a[204:207],v236,s[8:11],s63 offen offset:0 sc1\n"
MF"v[44:47],a[124:127],a[28:31],v[44:47]\n"
EX"v14,v14\n"
MF"v[52:55],a[124:127],a[44:47],v[52:55]\n"
RD"a[76:79],v227 offset:19456\n"
"v_readlane_b32 s44,v229,s42\n"
MF"v[60:63],a[124:127],a[60:63],v[60:63]\n"
SA"s42,s42,1\n"
EX"v15,v15\n"
PK"v3,v6,v7\n"
MF"v[80:83],a[64:67],v[64:67],v[80:83]\n"
WT"lgkmcnt(3)\n"
BL"v225,s[8:11],s43 offen offset:0 sc1 lds\n"
MF"v[112:115],a[64:67],v[68:71],v[112:115]\n"
SA"s41,s41,32768\n"
EX"v20,v20\n"
MF"v[144:147],a[64:67],v[72:75],v[144:147]\n"
"s_cmp_eq_u32 s41,163840\n"
RD"a[80:83],v227 offset:20480\n"
PK"v10,v12,v13\n"
MF"v[176:179],a[64:67],v[76:79],v[176:179]\n"
WT"lgkmcnt(2)\n"
EX"v21,v21\n"
MF"v[84:87],a[68:71],v[64:67],v[84:87]\n"
"s_cselect_b32 s41,0,s41\n"
BL"a[208:211],v236,s[8:11],s63 offen offset:1024 sc1\n"
MF"v[116:119],a[68:71],v[68:71],v[116:119]\n"
EX"v22,v22\n"
MF"v[148:151],a[68:71],v[72:75],v[148:151]\n"
RD"a[84:87],v227 offset:21504\n"
"v_add_u32 v226,s41,v224\n"
MF"v[180:183],a[68:71],v[76:79],v[180:183]\n"
EX"v23,v23\n"
PK"v11,v14,v15\n"
MF"v[88:91],a[72:75],v[64:67],v[88:91]\n"
BL"v225,s[8:11],s43 offen offset:1024 sc1 lds\n"
MF"v[120:123],a[72:75],v[68:71],v[120:123]\n"
EX"v28,v28\n"
MF"v[152:155],a[72:75],v[72:75],v[152:155]\n"
RD"a[88:91],v227 offset:22528\n"
PK"v18,v20,v21\n"
MF"v[184:187],a[72:75],v[76:79],v[184:187]\n"
WT"lgkmcnt(2)\n"
EX"v29,v29\n"
MF"v[92:95],a[76:79],v[64:67],v[92:95]\n"
BL"a[212:215],v236,s[8:11],s63 offen offset:2048 sc1\n"
MF"v[124:127],a[76:79],v[68:71],v[124:127]\n"
EX"v30,v30\n"
MF"v[156:159],a[76:79],v[72:75],v[156:159]\n"
RD"a[92:95],v227 offset:23552\n"
PK"v19,v22,v23\n"
MF"v[188:191],a[76:79],v[76:79],v[188:191]\n"
EX"v31,v31\n"
MF"v[96:99],a[80:83],v[64:67],v[96:99]\n"
BL"v225,s[8:11],s43 offen offset:2048 sc1 lds\n"
MF"v[128:131],a[80:83],v[68:71],v[128:131]\n"
EX"v32,v32\n"
MF"v[160:163],a[80:83],v[72:75],v[160:163]\n"
RD"a[96:99],v227 offset:24576\n"
PK"v26,v28,v29\n"
MF"v[192:195],a[80:83],v[76:79],v[192:195]\n"
WT"lgkmcnt(2)\n"
EX"v33,v33\n"
MF"v[100:103],a[84:87],v[64:67],v[100:103]\n"
BL"a[216:219],v236,s[8:11],s63 offen offset:3072 sc1\n"
MF"v[132:135],a[84:87],v[68:71],v[132:135]\n"
EX"v34,v34\n"
MF"v[164:167],a[84:87],v[72:75],v[164:167]\n"
RD"a[100:103],v227 offset:25600\n"
PK"v27,v30,v31\n"
MF"v[196:199],a[84:87],v[76:79],v[196:199]\n"
SA"s63,s63,4096\n"
EX"v35,v35\n"
MF"v[104:107],a[88:91],v[64:67],v[104:107]\n"
BL"v225,s[8:11],s43 offen offset:3072 sc1 lds\n"
MF"v[136:139],a[88:91],v[68:71],v[136:139]\n"
EX"v40,v40\n"
MF"v[168:171],a[88:91],v[72:75],v[168:171]\n"
RD"a[104:107],v227 offset:26624\n"
MF"v[200:203],a[88:91],v[76:79],v[200:203]\n"
WT"lgkmcnt(2)\n"
EX"v41,v41\n"
MF"v[108:111],a[92:95],v[64:67],v[108:111]\n"
SA"m0,s40,16384\n"
BL"a[220:223],v236,s[8:11],s63 offen offset:0 sc1\n"
MF"v[140:143],a[92:95],v[68:71],v[140:143]\n"
EX"v42,v42\n"
MF"v[172:175],a[92:95],v[72:75],v[172:175]\n"
RD"a[108:111],v227 offset:27648\n"
MF"v[204:207],a[92:95],v[76:79],v[204:207]\n"
EX"v43,v43\n"
MF"a[132:135],a[128:131],v[64:67],a[132:135]\n"
SA"s40,s40,32768\n"
BL"v225,s[8:11],s44 offen offset:0 sc1 lds\n"
PK"v64,v32,v33\n"
MF"a[136:139],a[128:131],v[68:71],a[136:139]\n"
"s_cmp_eq_u32 s40,s46\n"
EX"v48,v48\n"
PK"v65,v34,v35\n"
MF"a[140:143],a[128:131],v[72:75],a[140:143]\n"
"s_cselect_b32 s40,s47,s40\n"
RD"a[112:115],v227 offset:28672\n"
PK"v68,v40,v41\n"
MF"a[144:147],a[128:131],v[76:79],a[144:147]\n"
EX"v49,v49\n"
MF"v[80:83],a[96:99],v[0:3],v[80:83]\n"
BL"a[224:227],v236,s[8:11],s63 offen offset:1024 sc1\n"
MF"v[112:115],a[96:99],v[8:11],v[112:115]\n"
EX"v50,v50\n"
MF"v[144:147],a[96:99],v[16:19],v[144:147]\n"
RD"a[116:119],v227 offset:29696\n"
PK"v69,v42,v43\n"
MF"v[176:179],a[96:99],v[24:27],v[176:179]\n"
WT"lgkmcnt(2)\n"
EX"v51,v51\n"
MF"v[84:87],a[100:103],v[0:3],v[84:87]\n"
BL"v225,s[8:11],s44 offen offset:1024 sc1 lds\n"
MF"v[116:119],a[100:103],v[8:11],v[116:119]\n"
EX"v36,v36\n"
MF"v[148:151],a[100:103],v[16:19],v[148:151]\n"
RD"a[120:123],v227 offset:30720\n"
PK"v72,v48,v49\n"
MF"v[180:183],a[100:103],v[24:27],v[180:183]\n"
EX"v37,v37\n"
MF"v[88:91],a[104:107],v[0:3],v[88:91]\n"
BL"a[228:231],v236,s[8:11],s63 offen offset:2048 sc1\n"
MF"v[120:123],a[104:107],v[8:11],v[120:123]\n"
EX"v38,v38\n"
MF"v[152:155],a[104:107],v[16:19],v[152:155]\n"
RD"a[124:127],v227 offset:31744\n"
PK"v73,v50,v51\n"
MF"v[184:187],a[104:107],v[24:27],v[184:187]\n"
EX"v39,v39\n"
MF"v[92:95],a[108:111],v[0:3],v[92:95]\n"
BL"v225,s[8:11],s44 offen offset:2048 sc1 lds\n"
MF"v[124:127],a[108:111],v[8:11],v[124:127]\n"
EX"v44,v44\n"
MF"v[156:159],a[108:111],v[16:19],v[156:159]\n"
EX"v45,v45\n"
MF"v[188:191],a[108:111],v[24:27],v[188:191]\n"
WT"lgkmcnt(1)\n"
BL"a[232:235],v236,s[8:11],s63 offen offset:3072 sc1\n"
PK"v66,v36,v37\n"
MF"v[96:99],a[112:115],v[0:3],v[96:99]\n"
EX"v46,v46\n"
MF"v[128:131],a[112:115],v[8:11],v[128:131]\n"
PK"v67,v38,v39\n"
PK"v70,v44,v45\n"
MF"v[160:163],a[112:115],v[16:19],v[160:163]\n"
EX"v47,v47\n"
MF"v[192:195],a[112:115],v[24:27],v[192:195]\n"
SA"s63,s63,4096\n"
BL"v225,s[8:11],s44 offen offset:3072 sc1 lds\n"
MF"v[100:103],a[116:119],v[0:3],v[100:103]\n"
WT"lgkmcnt(0)\n"
"s_barrier\n"
MF"v[132:135],a[116:119],v[8:11],v[132:135]\n"
RD"a[64:67],v226 offset:0\n"
PK"v71,v46,v47\n"
MF"v[164:167],a[116:119],v[16:19],v[164:167]\n"
EX"v52,v52\n"
MF"v[196:199],a[116:119],v[24:27],v[196:199]\n"
BL"a[236:239],v236,s[8:11],s63 offen offset:0 sc1\n"
MF"v[104:107],a[120:123],v[0:3],v[104:107]\n"
RD"a[68:71],v226 offset:1024\n"
MF"v[136:139],a[120:123],v[8:11],v[136:139]\n"
EX"v53,v53\n"
MF"v[168:171],a[120:123],v[16:19],v[168:171]\n"
RD"a[72:75],v226 offset:2048\n"
PK"v74,v52,v53\n"
MF"v[200:203],a[120:123],v[24:27],v[200:203]\n"
EX"v54,v54\n"
MF"v[108:111],a[124:127],v[0:3],v[108:111]\n"
EX"v55,v55\n"
MF"v[140:143],a[124:127],v[8:11],v[140:143]\n"
RD"a[76:79],v226 offset:3072\n"
PK"v75,v54,v55\n"
MF"v[172:175],a[124:127],v[16:19],v[172:175]\n"
MF"v[204:207],a[124:127],v[24:27],v[204:207]\n"
RD"a[80:83],v226 offset:4096\n"
MF"a[132:135],a[128:131],v[0:3],a[132:135]\n"
MF"a[136:139],a[128:131],v[8:11],a[136:139]\n"
RD"a[84:87],v226 offset:5120\n"
MF"a[140:143],a[128:131],v[16:19],a[140:143]\n"
MF"a[144:147],a[128:131],v[24:27],a[144:147]\n"
WT"lgkmcnt(5)\n"
MF"v[0:3],a[64:67],a[0:3],0\n"
SA"s95,s40,32768\n"
EX"v56,v56\n"
MF"v[8:11],a[64:67],a[16:19],0\n"
"s_cmp_eq_u32 s95,s46\n"
"v_lshrrev_b32 v237,1,v224\n"
"v_and_b32 v237,0x78,v237\n"
MF"v[16:19],a[64:67],a[32:35],0\n"
"s_cselect_b32 s95,s47,s95\n"
EX"v57,v57\n"
MF"v[24:27],a[64:67],a[48:51],0\n"
WT"lgkmcnt(1)\n"
BL"a[240:243],v236,s[8:11],s63 offen offset:1024 sc1\n"
MF"v[0:3],a[68:71],a[4:7],v[0:3]\n"
"s_sub_u32 s95,s95,s47\n"
EX"v58,v58\n"
MF"v[8:11],a[68:71],a[20:23],v[8:11]\n"
"s_lshr_b32 s53,s47,2\n"
"v_and_b32 v239,0x300,v224\n"
"v_add3_u32 v237,v237,v239,s40\n"
MF"v[16:19],a[68:71],a[36:39],v[16:19]\n"
"s_cmp_gt_u32 s92,10\n"
EX"v59,v59\n"
MF"v[24:27],a[68:71],a[52:55],v[24:27]\n"
"s_cselect_b32 s63,s63,1065353216\n"
PK"v76,v56,v57\n"
"v_add_u32 v235,s95,v224\n"
MF"v[0:3],a[72:75],a[8:11],v[0:3]\n"
"s_lshl_b32 s50,s35,6\n"
BL"a[244:247],v236,s[8:11],s63 offen offset:2048 sc1\n"
MF"v[8:11],a[72:75],a[24:27],v[8:11]\n"
SA"s50,s50,4352\n"
EX"v60,v60\n"
MF"v[16:19],a[72:75],a[40:43],v[16:19]\n"
RD"a[88:91],v226 offset:6144\n"
"v_mov_b32 v227,v226\n"
MF"v[24:27],a[72:75],a[56:59],v[24:27]\n"
EX"v61,v61\n"
MF"v[0:3],a[76:79],a[12:15],v[0:3]\n"
BL"a[248:251],v236,s[8:11],s63 offen offset:3072 sc1\n"
MF"v[8:11],a[76:79],a[28:31],v[8:11]\n"
SA"s63,s63,4096\n"
EX"v62,v62\n"
MF"v[16:19],a[76:79],a[44:47],v[16:19]\n"
RD"a[92:95],v226 offset:7168\n"
PK"v77,v58,v59\n"
MF"v[24:27],a[76:79],a[60:63],v[24:27]\n"
EX"v63,v63\n"
MF"v[4:7],a[80:83],a[0:3],0\n"
BL"a[252:255],v236,s[8:11],s63 offen offset:0 sc1\n"
"v_lshrrev_b32 v238,1,v224\n"
MF"v[12:15],a[80:83],a[16:19],0\n"
"s_cmp_gt_u32 s92,13\n"
EX"v0,v0\n"
"v_and_b32 v238,56,v238\n"
MF"v[20:23],a[80:83],a[32:35],0\n"
"s_cselect_b32 s63,s63,1065353216\n"
RD"a[96:99],v226 offset:8192\n"
PK"v78,v60,v61\n"
MF"v[28:31],a[80:83],a[48:51],0\n"
WT"lgkmcnt(2)\n"
EX"v1,v1\n"
MF"v[4:7],a[84:87],a[4:7],v[4:7]\n"
BL"v[244:247],v236,s[8:11],s63 offen offset:1024 sc1\n"
"v_add_u32 v236,s53,v235\n"
MF"v[12:15],a[84:87],a[20:23],v[12:15]\n"
EX"v2,v2\n"
MF"v[20:23],a[84:87],a[36:39],v[20:23]\n"
RD"a[100:103],v226 offset:9216\n"
PK"v79,v62,v63\n"
MF"v[28:31],a[84:87],a[52:55],v[28:31]\n"
EX"v3,v3\n"
MF"v[4:7],a[88:91],a[8:11],v[4:7]\n"
"buffer_load_dwordx2 v[240:241],v238,s[8:11],s50 offen sc1\n"
MF"v[12:15],a[88:91],a[24:27],v[12:15]\n"
EX"v8,v8\n"
MF"v[20:23],a[88:91],a[40:43],v[20:23]\n"
RD"a[104:107],v226 offset:10240\n"
PK"v0,v0,v1\n"
MF"v[28:31],a[88:91],a[56:59],v[28:31]\n"
WT"lgkmcnt(2)\n"
EX"v9,v9\n"
MF"v[4:7],a[92:95],a[12:15],v[4:7]\n"
WT"vmcnt(23)\n"
RT"a[176:177],v237 offset:0\n"
RT"a[178:179],v237 offset:128\n"
MF"v[12:15],a[92:95],a[28:31],v[12:15]\n"
EX"v10,v10\n"
MF"v[20:23],a[92:95],a[44:47],v[20:23]\n"
RD"a[108:111],v226 offset:11264\n"
PK"v1,v2,v3\n"
MF"v[28:31],a[92:95],a[60:63],v[28:31]\n"
EX"v11,v11\n"
MF"v[32:35],a[96:99],a[0:3],0\n"
RT"a[180:181],v237 offset:1024\n"
RT"a[182:183],v237 offset:1152\n"
MF"v[40:43],a[96:99],a[16:19],0\n"
EX"v16,v16\n"
MF"v[48:51],a[96:99],a[32:35],0\n"
RD"a[112:115],v226 offset:12288\n"
PK"v8,v8,v9\n"
MF"v[56:59],a[96:99],a[48:51],0\n"
WT"lgkmcnt(4)\n"
EX"v17,v17\n"
MF"v[32:35],a[100:103],a[4:7],v[32:35]\n"
RT"a[184:185],v237 offset:2048\n"
RT"a[186:187],v237 offset:2176\n"
MF"v[40:43],a[100:103],a[20:23],v[40:43]\n"
EX"v18,v18\n"
MF"v[48:51],a[100:103],a[36:39],v[48:51]\n"
RD"a[116:119],v226 offset:13312\n"
PK"v9,v10,v11\n"
MF"v[56:59],a[100:103],a[52:55],v[56:59]\n"
EX"v19,v19\n"
MF"v[32:35],a[104:107],a[8:11],v[32:35]\n"
RT"a[188:189],v237 offset:3072\n"
RT"a[190:191],v237 offset:3200\n"
MF"v[40:43],a[104:107],a[24:27],v[40:43]\n"
EX"v24,v24\n"
MF"v[48:51],a[104:107],a[40:43],v[48:51]\n"
RD"a[120:123],v226 offset:14336\n"
PK"v16,v16,v17\n"
MF"v[56:59],a[104:107],a[56:59],v[56:59]\n"
WT"lgkmcnt(4)\n"
EX"v25,v25\n"
MF"v[32:35],a[108:111],a[12:15],v[32:35]\n"
RT"a[192:193],v237 offset:16384\n"
RT"a[194:195],v237 offset:16512\n"
MF"v[40:43],a[108:111],a[28:31],v[40:43]\n"
EX"v26,v26\n"
MF"v[48:51],a[108:111],a[44:47],v[48:51]\n"
RD"a[124:127],v226 offset:15360\n"
PK"v17,v18,v19\n"
MF"v[56:59],a[108:111],a[60:63],v[56:59]\n"
EX"v27,v27\n"
MF"v[36:39],a[112:115],a[0:3],0\n"
RT"a[196:197],v237 offset:17408\n"
RT"a[198:199],v237 offset:17536\n"
MF"v[44:47],a[112:115],a[16:19],0\n"
EX"v4,v4\n"
MF"v[52:55],a[112:115],a[32:35],0\n"
RD"a[64:67],v227 offset:16384\n"
PK"v24,v24,v25\n"
MF"v[60:63],a[112:115],a[48:51],0\n"
WT"lgkmcnt(4)\n"
EX"v5,v5\n"
MF"v[36:39],a[116:119],a[4:7],v[36:39]\n"
RT"a[200:201],v237 offset:18432\n"
RT"a[202:203],v237 offset:18560\n"
MF"v[44:47],a[116:119],a[20:23],v[44:47]\n"
EX"v6,v6\n"
MF"v[52:55],a[116:119],a[36:39],v[52:55]\n"
RD"a[68:71],v227 offset:17408\n"
PK"v25,v26,v27\n"
MF"v[60:63],a[116:119],a[52:55],v[60:63]\n"
EX"v7,v7\n"
MF"v[36:39],a[120:123],a[8:11],v[36:39]\n"
EX"v12,v12\n"
MF"v[44:47],a[120:123],a[24:27],v[44:47]\n"
RD"a[72:75],v227 offset:18432\n"
"v_readlane_b32 s43,v228,s42\n"
MF"v[52:55],a[120:123],a[40:43],v[52:55]\n"
EX"v13,v13\n"
MF"v[60:63],a[120:123],a[56:59],v[60:63]\n"
WT"lgkmcnt(2)\n"
PK"v2,v4,v5\n"
PK"v3,v6,v7\n"
MF"v[36:39],a[124:127],a[12:15],v[36:39]\n"
"s_mov_b32 m0,s40\n"
EX"v14,v14\n"
MF"v[44:47],a[124:127],a[28:31],v[44:47]\n"
RD"a[76:79],v227 offset:19456\n"
"v_readlane_b32 s44,v229,s42\n"
MF"v[52:55],a[124:127],a[44:47],v[52:55]\n"
EX"v15,v15\n"
MF"v[60:63],a[124:127],a[60:63],v[60:63]\n"
SA"s42,s42,1\n"
BL"v225,s[8:11],s43 offen offset:0 sc1 lds\n"
MF"v[80:83],a[64:67],v[64:67],v[80:83]\n"
SA"s41,s41,32768\n"
EX"v20,v20\n"
MF"v[112:115],a[64:67],v[68:71],v[112:115]\n"
"s_cmp_eq_u32 s41,163840\n"
RD"a[80:83],v227 offset:20480\n"
PK"v10,v12,v13\n"
MF"v[144:147],a[64:67],v[72:75],v[144:147]\n"
"s_cselect_b32 s41,0,s41\n"
EX"v21,v21\n"
MF"v[176:179],a[64:67],v[76:79],v[176:179]\n"
WT"lgkmcnt(2)\n"
"v_add_u32 v226,s41,v224\n"
PK"v11,v14,v15\n"
MF"v[84:87],a[68:71],v[64:67],v[84:87]\n"
EX"v22,v22\n"
MF"v[116:119],a[68:71],v[68:71],v[116:119]\n"
RD"a[84:87],v227 offset:21504\n"
PK"v18,v20,v21\n"
MF"v[148:151],a[68:71],v[72:75],v[148:151]\n"
WT"vmcnt(1)\n"
"v_cmp_eq_u64 vcc,s[28:29],v[240:241]\n"
"s_cmp_eq_u64 vcc,exec\n"
"s_cbranch_scc0 L1102_%=\n"
"L1103_%=:\n"
MF"v[180:183],a[68:71],v[76:79],v[180:183]\n"
EX"v23,v23\n"
MF"v[88:91],a[72:75],v[64:67],v[88:91]\n"
BL"v225,s[8:11],s43 offen offset:1024 sc1 lds\n"
MF"v[120:123],a[72:75],v[68:71],v[120:123]\n"
EX"v28,v28\n"
MF"v[152:155],a[72:75],v[72:75],v[152:155]\n"
RD"a[88:91],v227 offset:22528\n"
PK"v19,v22,v23\n"
MF"v[184:187],a[72:75],v[76:79],v[184:187]\n"
WT"lgkmcnt(2)\n"
EX"v29,v29\n"
MF"v[92:95],a[76:79],v[64:67],v[92:95]\n"
EX"v30,v30\n"
MF"v[124:127],a[76:79],v[68:71],v[124:127]\n"
RD"a[92:95],v227 offset:23552\n"
PK"v26,v28,v29\n"
MF"v[156:159],a[76:79],v[72:75],v[156:159]\n"
EX"v31,v31\n"
MF"v[188:191],a[76:79],v[76:79],v[188:191]\n"
BL"v225,s[8:11],s43 offen offset:2048 sc1 lds\n"
MF"v[96:99],a[80:83],v[64:67],v[96:99]\n"
EX"v32,v32\n"
MF"v[128:131],a[80:83],v[68:71],v[128:131]\n"
RD"a[96:99],v227 offset:24576\n"
PK"v27,v30,v31\n"
MF"v[160:163],a[80:83],v[72:75],v[160:163]\n"
EX"v33,v33\n"
MF"v[192:195],a[80:83],v[76:79],v[192:195]\n"
WT"lgkmcnt(2)\n"
MF"v[100:103],a[84:87],v[64:67],v[100:103]\n"
EX"v34,v34\n"
MF"v[132:135],a[84:87],v[68:71],v[132:135]\n"
RD"a[100:103],v227 offset:25600\n"
MF"v[164:167],a[84:87],v[72:75],v[164:167]\n"
EX"v35,v35\n"
MF"v[196:199],a[84:87],v[76:79],v[196:199]\n"
BL"v225,s[8:11],s43 offen offset:3072 sc1 lds\n"
MF"v[104:107],a[88:91],v[64:67],v[104:107]\n"
EX"v40,v40\n"
MF"v[136:139],a[88:91],v[68:71],v[136:139]\n"
RD"a[104:107],v227 offset:26624\n"
MF"v[168:171],a[88:91],v[72:75],v[168:171]\n"
EX"v41,v41\n"
MF"v[200:203],a[88:91],v[76:79],v[200:203]\n"
WT"lgkmcnt(2)\n"
SA"m0,s40,16384\n"
MF"v[108:111],a[92:95],v[64:67],v[108:111]\n"
EX"v42,v42\n"
MF"v[140:143],a[92:95],v[68:71],v[140:143]\n"
RD"a[108:111],v227 offset:27648\n"
MF"v[172:175],a[92:95],v[72:75],v[172:175]\n"
EX"v43,v43\n"
MF"v[204:207],a[92:95],v[76:79],v[204:207]\n"
BL"v225,s[8:11],s44 offen offset:0 sc1 lds\n"
MF"a[132:135],a[128:131],v[64:67],a[132:135]\n"
SA"s40,s40,32768\n"
EX"v48,v48\n"
PK"v64,v32,v33\n"
MF"a[136:139],a[128:131],v[68:71],a[136:139]\n"
"s_cmp_eq_u32 s40,s46\n"
RD"a[112:115],v227 offset:28672\n"
PK"v65,v34,v35\n"
MF"a[140:143],a[128:131],v[72:75],a[140:143]\n"
"s_cselect_b32 s40,s47,s40\n"
EX"v49,v49\n"
MF"a[144:147],a[128:131],v[76:79],a[144:147]\n"
PK"v68,v40,v41\n"
PK"v69,v42,v43\n"
MF"v[240:243],a[148:151],a[204:207],0\n"
EX"v50,v50\n"
MF"v[240:243],a[152:155],a[208:211],v[240:243]\n"
RD"a[116:119],v227 offset:29696\n"
PK"v72,v48,v49\n"
MF"v[240:243],a[156:159],a[212:215],v[240:243]\n"
EX"v51,v51\n"
MF"v[240:243],a[160:163],a[216:219],v[240:243]\n"
BL"v225,s[8:11],s44 offen offset:1024 sc1 lds\n"
MF"v[240:243],a[164:167],a[220:223],v[240:243]\n"
EX"v36,v36\n"
MF"v[240:243],a[168:171],a[224:227],v[240:243]\n"
RD"a[120:123],v227 offset:30720\n"
PK"v73,v50,v51\n"
MF"v[240:243],a[172:175],a[228:231],v[240:243]\n"
EX"v37,v37\n"
MF"v[240:243],a[176:179],a[232:235],v[240:243]\n"
PK"v66,v36,v37\n"
MF"v[240:243],a[180:183],a[236:239],v[240:243]\n"
EX"v38,v38\n"
MF"v[240:243],a[184:187],a[240:243],v[240:243]\n"
RD"a[124:127],v227 offset:31744\n"
MF"v[240:243],a[188:191],a[244:247],v[240:243]\n"
EX"v39,v39\n"
MF"v[240:243],a[192:195],a[248:251],v[240:243]\n"
BL"v225,s[8:11],s44 offen offset:2048 sc1 lds\n"
MF"v[240:243],a[196:199],a[252:255],v[240:243]\n"
EX"v44,v44\n"
MF"v[240:243],a[200:203],v[244:247],v[240:243]\n"
PK"v67,v38,v39\n"
MF"v[80:83],a[96:99],v[0:3],v[80:83]\n"
EX"v45,v45\n"
MF"v[112:115],a[96:99],v[8:11],v[112:115]\n"
PK"v70,v44,v45\n"
MF"v[144:147],a[96:99],v[16:19],v[144:147]\n"
EX"v46,v46\n"
MF"v[176:179],a[96:99],v[24:27],v[176:179]\n"
WT"lgkmcnt(0)\n"
MF"v[84:87],a[100:103],v[0:3],v[84:87]\n"
EX"v47,v47\n"
MF"v[116:119],a[100:103],v[8:11],v[116:119]\n"
"ds_write_b128 v236,v[240:243]\n"
MF"v[148:151],a[100:103],v[16:19],v[148:151]\n"
BL"v225,s[8:11],s44 offen offset:3072 sc1 lds\n"
MF"v[180:183],a[100:103],v[24:27],v[180:183]\n"
PK"v71,v46,v47\n"
MF"v[88:91],a[104:107],v[0:3],v[88:91]\n"
EX"v52,v52\n"
MF"v[120:123],a[104:107],v[8:11],v[120:123]\n"
MF"v[152:155],a[104:107],v[16:19],v[152:155]\n"
EX"v53,v53\n"
MF"v[184:187],a[104:107],v[24:27],v[184:187]\n"
PK"v74,v52,v53\n"
MF"v[92:95],a[108:111],v[0:3],v[92:95]\n"
EX"v54,v54\n"
MF"v[124:127],a[108:111],v[8:11],v[124:127]\n"
MF"v[156:159],a[108:111],v[16:19],v[156:159]\n"
EX"v55,v55\n"
MF"v[188:191],a[108:111],v[24:27],v[188:191]\n"
PK"v75,v54,v55\n"
MF"v[96:99],a[112:115],v[0:3],v[96:99]\n"
MF"v[128:131],a[112:115],v[8:11],v[128:131]\n"
MF"v[160:163],a[112:115],v[16:19],v[160:163]\n"
MF"v[192:195],a[112:115],v[24:27],v[192:195]\n"
MF"v[100:103],a[116:119],v[0:3],v[100:103]\n"
WT"lgkmcnt(0)\n"
"s_barrier\n"
MF"v[132:135],a[116:119],v[8:11],v[132:135]\n"
RD"a[64:67],v226 offset:0\n"
MF"v[164:167],a[116:119],v[16:19],v[164:167]\n"
RD"v[240:243],v235 offset:0\n"
MF"v[196:199],a[116:119],v[24:27],v[196:199]\n"
RD"a[68:71],v226 offset:1024\n"
MF"v[104:107],a[120:123],v[0:3],v[104:107]\n"
RD"v[244:247],v235 offset:1024\n"
MF"v[136:139],a[120:123],v[8:11],v[136:139]\n"
RD"a[72:75],v226 offset:2048\n"
MF"v[168:171],a[120:123],v[16:19],v[168:171]\n"
RD"v[248:251],v235 offset:2048\n"
MF"v[200:203],a[120:123],v[24:27],v[200:203]\n"
MF"v[108:111],a[124:127],v[0:3],v[108:111]\n"
RD"v[252:255],v235 offset:3072\n"
MF"v[140:143],a[124:127],v[8:11],v[140:143]\n"
RD"a[76:79],v226 offset:3072\n"
MF"v[172:175],a[124:127],v[16:19],v[172:175]\n"
MF"v[204:207],a[124:127],v[24:27],v[204:207]\n"
RD"a[80:83],v226 offset:4096\n"
MF"a[132:135],a[128:131],v[0:3],a[132:135]\n"
MF"a[136:139],a[128:131],v[8:11],a[136:139]\n"
RD"a[84:87],v226 offset:5120\n"
MF"a[140:143],a[128:131],v[16:19],a[140:143]\n"
MF"a[144:147],a[128:131],v[24:27],a[144:147]\n"
"s_cmp_eq_u32 s57,2\n"
"s_cbranch_scc1 LMD_%=\n"
"s_cmp_eq_u32 s34,2\n"
"s_cbranch_scc1 LMA_%=\n"
WT"lgkmcnt(9)\n"
MF"v[0:3],a[64:67],a[0:3],0\n"
EX"v56,v56\n"
MF"v[8:11],a[64:67],a[16:19],0\n"
WT"lgkmcnt(6)\n"
"v_add_f32 v240,v240,v244\n"
MF"v[16:19],a[64:67],a[32:35],0\n"
EX"v57,v57\n"
MF"v[24:27],a[64:67],a[48:51],0\n"
WT"lgkmcnt(3)\n"
"v_add_f32 v248,v248,v252\n"
"v_add_f32 v241,v241,v245\n"
MF"v[0:3],a[68:71],a[4:7],v[0:3]\n"
EX"v58,v58\n"
MF"v[8:11],a[68:71],a[20:23],v[8:11]\n"
"v_add_f32 v249,v249,v253\n"
PK"v76,v56,v57\n"
MF"v[16:19],a[68:71],a[36:39],v[16:19]\n"
EX"v59,v59\n"
MF"v[24:27],a[68:71],a[52:55],v[24:27]\n"
"v_add_f32 v242,v242,v246\n"
"v_mov_b32 v227,v226\n"
MF"v[0:3],a[72:75],a[8:11],v[0:3]\n"
EX"v60,v60\n"
MF"v[8:11],a[72:75],a[24:27],v[8:11]\n"
RD"a[88:91],v226 offset:6144\n"
"v_add_f32 v250,v250,v254\n"
MF"v[16:19],a[72:75],a[40:43],v[16:19]\n"
EX"v61,v61\n"
MF"v[24:27],a[72:75],a[56:59],v[24:27]\n"
WT"lgkmcnt(1)\n"
"v_add_f32 v243,v243,v247\n"
"v_add_f32 v251,v251,v255\n"
MF"v[0:3],a[76:79],a[12:15],v[0:3]\n"
EX"v62,v62\n"
MF"v[8:11],a[76:79],a[28:31],v[8:11]\n"
RD"a[92:95],v226 offset:7168\n"
PK"v77,v58,v59\n"
MF"v[16:19],a[76:79],a[44:47],v[16:19]\n"
EX"v63,v63\n"
MF"v[24:27],a[76:79],a[60:63],v[24:27]\n"
PK"v240,v240,v240\n"
"v_lshlrev_b32 v240,16,v240\n"
MF"v[4:7],a[80:83],a[0:3],0\n"
EX"v0,v0\n"
MF"v[12:15],a[80:83],a[16:19],0\n"
RD"a[96:99],v226 offset:8192\n"
PK"v241,v241,v241\n"
MF"v[20:23],a[80:83],a[32:35],0\n"
EX"v1,v1\n"
MF"v[28:31],a[80:83],a[48:51],0\n"
"v_lshlrev_b32 v241,16,v241\n"
PK"v78,v60,v61\n"
MF"v[4:7],a[84:87],a[4:7],v[4:7]\n"
EX"v2,v2\n"
PK"v242,v242,v242\n"
MF"v[12:15],a[84:87],a[20:23],v[12:15]\n"
RD"a[100:103],v226 offset:9216\n"
"v_lshlrev_b32 v242,16,v242\n"
MF"v[20:23],a[84:87],a[36:39],v[20:23]\n"
EX"v3,v3\n"
PK"v243,v243,v243\n"
MF"v[28:31],a[84:87],a[52:55],v[28:31]\n"
WT"lgkmcnt(2)\n"
"v_lshlrev_b32 v243,16,v243\n"
PK"v79,v62,v63\n"
MF"v[4:7],a[88:91],a[8:11],v[4:7]\n"
EX"v8,v8\n"
MU"v244,0xbfb8aa3b,v240\n"
MF"v[12:15],a[88:91],a[24:27],v[12:15]\n"
EX"v244,v244\n"
MU"v245,0xbfb8aa3b,v241\n"
MF"v[20:23],a[88:91],a[40:43],v[20:23]\n"
EX"v245,v245\n"
MU"v246,0xbfb8aa3b,v242\n"
MF"v[28:31],a[88:91],a[56:59],v[28:31]\n"
RD"a[104:107],v226 offset:10240\n"
MU"v247,0xbfb8aa3b,v243\n"
PK"v0,v0,v1\n"
MF"v[4:7],a[92:95],a[12:15],v[4:7]\n"
EX"v246,v246\n"
EX"v9,v9\n"
MF"v[12:15],a[92:95],a[28:31],v[12:15]\n"
EX"v10,v10\n"
PK"v248,v248,v248\n"
MF"v[20:23],a[92:95],a[44:47],v[20:23]\n"
EX"v247,v247\n"
"v_lshlrev_b32 v248,16,v248\n"
MF"v[28:31],a[92:95],a[60:63],v[28:31]\n"
WT"lgkmcnt(1)\n"
RD"a[108:111],v226 offset:11264\n"
PK"v1,v2,v3\n"
MF"v[32:35],a[96:99],a[0:3],0\n"
EX"v11,v11\n"
PK"v249,v249,v249\n"
MF"v[40:43],a[96:99],a[16:19],0\n"
EX"v16,v16\n"
"v_lshlrev_b32 v249,16,v249\n"
MF"v[48:51],a[96:99],a[32:35],0\n"
RD"a[112:115],v226 offset:12288\n"
PK"v250,v250,v250\n"
MF"v[56:59],a[96:99],a[48:51],0\n"
EX"v17,v17\n"
PK"v8,v8,v9\n"
MF"v[32:35],a[100:103],a[4:7],v[32:35]\n"
EX"v18,v18\n"
"v_lshlrev_b32 v250,16,v250\n"
MF"v[40:43],a[100:103],a[20:23],v[40:43]\n"
RD"a[116:119],v226 offset:13312\n"
PK"v251,v251,v251\n"
MF"v[48:51],a[100:103],a[36:39],v[48:51]\n"
EX"v19,v19\n"
MF"v[56:59],a[100:103],a[52:55],v[56:59]\n"
WT"lgkmcnt(2)\n"
"v_lshlrev_b32 v251,16,v251\n"
PK"v9,v10,v11\n"
MF"v[32:35],a[104:107],a[8:11],v[32:35]\n"
EX"v24,v24\n"
"v_add_f32 v244,1.0,v244\n"
MF"v[40:43],a[104:107],a[24:27],v[40:43]\n"
"v_rcp_f32 v244,v244\n"
"v_add_f32 v245,1.0,v245\n"
MF"v[48:51],a[104:107],a[40:43],v[48:51]\n"
"v_rcp_f32 v245,v245\n"
"v_add_f32 v246,1.0,v246\n"
MF"v[56:59],a[104:107],a[56:59],v[56:59]\n"
"v_rcp_f32 v246,v246\n"
"v_add_f32 v247,1.0,v247\n"
MF"v[32:35],a[108:111],a[12:15],v[32:35]\n"
"v_rcp_f32 v247,v247\n"
EX"v25,v25\n"
MF"v[40:43],a[108:111],a[28:31],v[40:43]\n"
RD"a[120:123],v226 offset:14336\n"
RD"a[124:127],v226 offset:15360\n"
PK"v16,v16,v17\n"
MF"v[48:51],a[108:111],a[44:47],v[48:51]\n"
EX"v26,v26\n"
MF"v[56:59],a[108:111],a[60:63],v[56:59]\n"
WT"lgkmcnt(2)\n"
EX"v27,v27\n"
PK"v17,v18,v19\n"
MF"v[36:39],a[112:115],a[0:3],0\n"
EX"v4,v4\n"
MU"v240,v240,v244\n"
MF"v[44:47],a[112:115],a[16:19],0\n"
RD"a[64:67],v227 offset:16384\n"
MU"v241,v241,v245\n"
MF"v[52:55],a[112:115],a[32:35],0\n"
EX"v5,v5\n"
MU"v242,v242,v246\n"
MF"v[60:63],a[112:115],a[48:51],0\n"
MU"v243,v243,v247\n"
PK"v24,v24,v25\n"
MF"v[36:39],a[116:119],a[4:7],v[36:39]\n"
EX"v6,v6\n"
PK"v240,v240,v240\n"
MF"v[44:47],a[116:119],a[20:23],v[44:47]\n"
RD"a[68:71],v227 offset:17408\n"
"v_lshlrev_b32 v240,16,v240\n"
MF"v[52:55],a[116:119],a[36:39],v[52:55]\n"
EX"v7,v7\n"
PK"v241,v241,v241\n"
MF"v[60:63],a[116:119],a[52:55],v[60:63]\n"
WT"lgkmcnt(2)\n"
"v_readlane_b32 s43,v228,s42\n"
"v_lshlrev_b32 v241,16,v241\n"
PK"v25,v26,v27\n"
MF"v[36:39],a[120:123],a[8:11],v[36:39]\n"
EX"v12,v12\n"
PK"v242,v242,v242\n"
MF"v[44:47],a[120:123],a[24:27],v[44:47]\n"
RD"a[72:75],v227 offset:18432\n"
"v_lshlrev_b32 v242,16,v242\n"
MF"v[52:55],a[120:123],a[40:43],v[52:55]\n"
EX"v13,v13\n"
PK"v243,v243,v243\n"
MF"v[60:63],a[120:123],a[56:59],v[60:63]\n"
"s_mov_b32 m0,s40\n"
"v_lshlrev_b32 v243,16,v243\n"
PK"v2,v4,v5\n"
MF"v[36:39],a[124:127],a[12:15],v[36:39]\n"
EX"v14,v14\n"
MU"v240,v240,v248\n"
MF"v[44:47],a[124:127],a[28:31],v[44:47]\n"
RD"a[76:79],v227 offset:19456\n"
"v_readlane_b32 s44,v229,s42\n"
MU"v241,v241,v249\n"
MF"v[52:55],a[124:127],a[44:47],v[52:55]\n"
EX"v15,v15\n"
MU"v242,v242,v250\n"
MF"v[60:63],a[124:127],a[60:63],v[60:63]\n"
WT"lgkmcnt(2)\n"
BL"v225,s[8:11],s43 offen offset:0 sc1 lds\n"
MU"v243,v243,v251\n"
MF"v[80:83],a[64:67],v[64:67],v[80:83]\n"
SA"s42,s42,1\n"
EX"v20,v20\n"
PK"v3,v6,v7\n"
MF"v[112:115],a[64:67],v[68:71],v[112:115]\n"
SA"s41,s41,32768\n"
RD"a[80:83],v227 offset:20480\n"
PK"v240,v240,v241\n"
PK"v241,v242,v243\n"
MF"v[144:147],a[64:67],v[72:75],v[144:147]\n"
"s_cmp_eq_u32 s41,163840\n"
EX"v21,v21\n"
"v_lshrrev_b32 v239,9,v224\n"
MF"v[176:179],a[64:67],v[76:79],v[176:179]\n"
"s_cselect_b32 s41,0,s41\n"
"v_lshlrev_b32 v239,8,v239\n"
"v_add_u32 v226,s41,v224\n"
PK"v10,v12,v13\n"
MF"v[84:87],a[68:71],v[64:67],v[84:87]\n"
EX"v22,v22\n"
"v_bfe_u32 v237,v224,8,1\n"
MF"v[116:119],a[68:71],v[68:71],v[116:119]\n"
RD"a[84:87],v227 offset:21504\n"
"v_lshl_add_u32 v239,v237,3,v239\n"
MF"v[148:151],a[68:71],v[72:75],v[148:151]\n"
EX"v23,v23\n"
"v_and_b32 v237,0x80,v224\n"
MF"v[180:183],a[68:71],v[76:79],v[180:183]\n"
WT"lgkmcnt(2)\n"
BL"v225,s[8:11],s43 offen offset:1024 sc1 lds\n"
"v_lshl_add_u32 v239,v237,23,v239\n"
MF"v[88:91],a[72:75],v[64:67],v[88:91]\n"
EX"v28,v28\n"
PK"v11,v14,v15\n"
MF"v[120:123],a[72:75],v[68:71],v[120:123]\n"
RD"a[88:91],v227 offset:22528\n"
"v_and_b32 v237,0x70,v224\n"
"v_add_u32 v239,v239,v237\n"
MF"v[152:155],a[72:75],v[72:75],v[152:155]\n"
"buffer_store_dwordx2 v[240:241],v239,s[8:11],s93 offen sc1\n"
MF"v[184:187],a[72:75],v[76:79],v[184:187]\n"
EX"v29,v29\n"
PK"v18,v20,v21\n"
MF"v[92:95],a[76:79],v[64:67],v[92:95]\n"
EX"v30,v30\n"
MF"v[124:127],a[76:79],v[68:71],v[124:127]\n"
RD"a[92:95],v227 offset:23552\n"
PK"v19,v22,v23\n"
MF"v[156:159],a[76:79],v[72:75],v[156:159]\n"
EX"v31,v31\n"
MF"v[188:191],a[76:79],v[76:79],v[188:191]\n"
WT"lgkmcnt(2)\n"
BL"v225,s[8:11],s43 offen offset:2048 sc1 lds\n"
MF"v[96:99],a[80:83],v[64:67],v[96:99]\n"
EX"v32,v32\n"
MF"v[128:131],a[80:83],v[68:71],v[128:131]\n"
RD"a[96:99],v227 offset:24576\n"
PK"v26,v28,v29\n"
MF"v[160:163],a[80:83],v[72:75],v[160:163]\n"
EX"v33,v33\n"
MF"v[192:195],a[80:83],v[76:79],v[192:195]\n"
PK"v27,v30,v31\n"
MF"v[100:103],a[84:87],v[64:67],v[100:103]\n"
EX"v34,v34\n"
MF"v[132:135],a[84:87],v[68:71],v[132:135]\n"
RD"a[100:103],v227 offset:25600\n"
MF"v[164:167],a[84:87],v[72:75],v[164:167]\n"
EX"v35,v35\n"
MF"v[196:199],a[84:87],v[76:79],v[196:199]\n"
WT"lgkmcnt(2)\n"
BL"v225,s[8:11],s43 offen offset:3072 sc1 lds\n"
MF"v[104:107],a[88:91],v[64:67],v[104:107]\n"
EX"v40,v40\n"
MF"v[136:139],a[88:91],v[68:71],v[136:139]\n"
RD"a[104:107],v227 offset:26624\n"
MF"v[168:171],a[88:91],v[72:75],v[168:171]\n"
EX"v41,v41\n"
MF"v[200:203],a[88:91],v[76:79],v[200:203]\n"
SA"m0,s40,16384\n"
MF"v[108:111],a[92:95],v[64:67],v[108:111]\n"
EX"v42,v42\n"
MF"v[140:143],a[92:95],v[68:71],v[140:143]\n"
RD"a[108:111],v227 offset:27648\n"
MF"v[172:175],a[92:95],v[72:75],v[172:175]\n"
EX"v43,v43\n"
MF"v[204:207],a[92:95],v[76:79],v[204:207]\n"
BL"v225,s[8:11],s44 offen offset:0 sc1 lds\n"
MF"a[132:135],a[128:131],v[64:67],a[132:135]\n"
SA"s40,s40,32768\n"
EX"v48,v48\n"
PK"v64,v32,v33\n"
MF"a[136:139],a[128:131],v[68:71],a[136:139]\n"
"s_cmp_eq_u32 s40,s46\n"
RD"a[112:115],v227 offset:28672\n"
PK"v65,v34,v35\n"
MF"a[140:143],a[128:131],v[72:75],a[140:143]\n"
"s_cselect_b32 s40,s47,s40\n"
EX"v49,v49\n"
MF"a[144:147],a[128:131],v[76:79],a[144:147]\n"
WT"lgkmcnt(2)\n"
PK"v68,v40,v41\n"
PK"v69,v42,v43\n"
MF"v[80:83],a[96:99],v[0:3],v[80:83]\n"
EX"v50,v50\n"
MF"v[112:115],a[96:99],v[8:11],v[112:115]\n"
RD"a[116:119],v227 offset:29696\n"
PK"v72,v48,v49\n"
MF"v[144:147],a[96:99],v[16:19],v[144:147]\n"
EX"v51,v51\n"
MF"v[176:179],a[96:99],v[24:27],v[176:179]\n"
BL"v225,s[8:11],s44 offen offset:1024 sc1 lds\n"
MF"v[84:87],a[100:103],v[0:3],v[84:87]\n"
EX"v36,v36\n"
MF"v[116:119],a[100:103],v[8:11],v[116:119]\n"
RD"a[120:123],v227 offset:30720\n"
PK"v73,v50,v51\n"
MF"v[148:151],a[100:103],v[16:19],v[148:151]\n"
EX"v37,v37\n"
MF"v[180:183],a[100:103],v[24:27],v[180:183]\n"
PK"v66,v36,v37\n"
MF"v[88:91],a[104:107],v[0:3],v[88:91]\n"
EX"v38,v38\n"
MF"v[120:123],a[104:107],v[8:11],v[120:123]\n"
RD"a[124:127],v227 offset:31744\n"
MF"v[152:155],a[104:107],v[16:19],v[152:155]\n"
EX"v39,v39\n"
MF"v[184:187],a[104:107],v[24:27],v[184:187]\n"
WT"lgkmcnt(2)\n"
BL"v225,s[8:11],s44 offen offset:2048 sc1 lds\n"
MF"v[92:95],a[108:111],v[0:3],v[92:95]\n"
EX"v44,v44\n"
MF"v[124:127],a[108:111],v[8:11],v[124:127]\n"
PK"v67,v38,v39\n"
MF"v[156:159],a[108:111],v[16:19],v[156:159]\n"
EX"v45,v45\n"
MF"v[188:191],a[108:111],v[24:27],v[188:191]\n"
PK"v70,v44,v45\n"
MF"v[96:99],a[112:115],v[0:3],v[96:99]\n"
EX"v46,v46\n"
MF"v[128:131],a[112:115],v[8:11],v[128:131]\n"
MF"v[160:163],a[112:115],v[16:19],v[160:163]\n"
EX"v47,v47\n"
MF"v[192:195],a[112:115],v[24:27],v[192:195]\n"
BL"v225,s[8:11],s44 offen offset:3072 sc1 lds\n"
MF"v[100:103],a[116:119],v[0:3],v[100:103]\n"
WT"lgkmcnt(0)\n"
"s_barrier\n"
MF"v[132:135],a[116:119],v[8:11],v[132:135]\n"
RD"a[64:67],v226 offset:0\n"
PK"v71,v46,v47\n"
MF"v[164:167],a[116:119],v[16:19],v[164:167]\n"
EX"v52,v52\n"
MF"v[196:199],a[116:119],v[24:27],v[196:199]\n"
RD"a[68:71],v226 offset:1024\n"
MF"v[104:107],a[120:123],v[0:3],v[104:107]\n"
EX"v53,v53\n"
MF"v[136:139],a[120:123],v[8:11],v[136:139]\n"
RD"a[72:75],v226 offset:2048\n"
PK"v74,v52,v53\n"
MF"v[168:171],a[120:123],v[16:19],v[168:171]\n"
EX"v54,v54\n"
MF"v[200:203],a[120:123],v[24:27],v[200:203]\n"
MF"v[108:111],a[124:127],v[0:3],v[108:111]\n"
WT"vmcnt(6)\n"
"buffer_store_dwordx2 v[232:233],v230,s[8:11],s94 offen sc1\n"
MF"v[140:143],a[124:127],v[8:11],v[140:143]\n"
EX"v55,v55\n"
MF"v[172:175],a[124:127],v[16:19],v[172:175]\n"
RD"a[76:79],v226 offset:3072\n"
PK"v75,v54,v55\n"
MF"v[204:207],a[124:127],v[24:27],v[204:207]\n"
RD"a[80:83],v226 offset:4096\n"
MF"a[132:135],a[128:131],v[0:3],a[132:135]\n"
MF"a[136:139],a[128:131],v[8:11],a[136:139]\n"
RD"a[84:87],v226 offset:5120\n"
MF"a[140:143],a[128:131],v[16:19],a[140:143]\n"
MF"a[144:147],a[128:131],v[24:27],a[144:147]\n"
"s_mov_b32 s57,0\n"
"s_lshl_b32 s56,s34,1\n"
"s_sub_u32 s56,s56,6\n"
"s_branch LP1_%=\n"
"LMA_%=:\n"
WT"lgkmcnt(9)\n"
MF"v[0:3],a[64:67],a[0:3],v[208:211]\n"
EX"v56,v56\n"
MF"v[8:11],a[64:67],a[16:19],0\n"
WT"lgkmcnt(6)\n"
"v_add_f32 v240,v240,v244\n"
MF"v[16:19],a[64:67],a[32:35],0\n"
EX"v57,v57\n"
MF"v[24:27],a[64:67],a[48:51],0\n"
WT"lgkmcnt(3)\n"
"v_add_f32 v248,v248,v252\n"
"v_add_f32 v241,v241,v245\n"
MF"v[0:3],a[68:71],a[4:7],v[0:3]\n"
EX"v58,v58\n"
MF"v[8:11],a[68:71],a[20:23],v[8:11]\n"
"v_add_f32 v249,v249,v253\n"
PK"v76,v56,v57\n"
MF"v[16:19],a[68:71],a[36:39],v[16:19]\n"
EX"v59,v59\n"
MF"v[24:27],a[68:71],a[52:55],v[24:27]\n"
"v_add_f32 v242,v242,v246\n"
"v_mov_b32 v227,v226\n"
MF"v[0:3],a[72:75],a[8:11],v[0:3]\n"
EX"v60,v60\n"
MF"v[8:11],a[72:75],a[24:27],v[8:11]\n"
RD"a[88:91],v226 offset:6144\n"
"v_add_f32 v250,v250,v254\n"
MF"v[16:19],a[72:75],a[40:43],v[16:19]\n"
EX"v61,v61\n"
MF"v[24:27],a[72:75],a[56:59],v[24:27]\n"
WT"lgkmcnt(1)\n"
"v_add_f32 v243,v243,v247\n"
"v_add_f32 v251,v251,v255\n"
MF"v[0:3],a[76:79],a[12:15],v[0:3]\n"
EX"v62,v62\n"
MF"v[8:11],a[76:79],a[28:31],v[8:11]\n"
RD"a[92:95],v226 offset:7168\n"
PK"v77,v58,v59\n"
MF"v[16:19],a[76:79],a[44:47],v[16:19]\n"
EX"v63,v63\n"
MF"v[24:27],a[76:79],a[60:63],v[24:27]\n"
PK"v240,v240,v240\n"
"v_lshlrev_b32 v240,16,v240\n"
MF"v[4:7],a[80:83],a[0:3],v[212:215]\n"
EX"v0,v0\n"
MF"v[12:15],a[80:83],a[16:19],0\n"
RD"a[96:99],v226 offset:8192\n"
PK"v241,v241,v241\n"
MF"v[20:23],a[80:83],a[32:35],0\n"
EX"v1,v1\n"
MF"v[28:31],a[80:83],a[48:51],0\n"
"v_lshlrev_b32 v241,16,v241\n"
PK"v78,v60,v61\n"
MF"v[4:7],a[84:87],a[4:7],v[4:7]\n"
EX"v2,v2\n"
PK"v242,v242,v242\n"
MF"v[12:15],a[84:87],a[20:23],v[12:15]\n"
RD"a[100:103],v226 offset:9216\n"
"v_lshlrev_b32 v242,16,v242\n"
MF"v[20:23],a[84:87],a[36:39],v[20:23]\n"
EX"v3,v3\n"
PK"v243,v243,v243\n"
MF"v[28:31],a[84:87],a[52:55],v[28:31]\n"
WT"lgkmcnt(2)\n"
"v_lshlrev_b32 v243,16,v243\n"
PK"v79,v62,v63\n"
MF"v[4:7],a[88:91],a[8:11],v[4:7]\n"
EX"v8,v8\n"
MU"v244,0xbfb8aa3b,v240\n"
MF"v[12:15],a[88:91],a[24:27],v[12:15]\n"
EX"v244,v244\n"
MU"v245,0xbfb8aa3b,v241\n"
MF"v[20:23],a[88:91],a[40:43],v[20:23]\n"
EX"v245,v245\n"
MU"v246,0xbfb8aa3b,v242\n"
MF"v[28:31],a[88:91],a[56:59],v[28:31]\n"
RD"a[104:107],v226 offset:10240\n"
MU"v247,0xbfb8aa3b,v243\n"
PK"v0,v0,v1\n"
MF"v[4:7],a[92:95],a[12:15],v[4:7]\n"
EX"v246,v246\n"
EX"v9,v9\n"
MF"v[12:15],a[92:95],a[28:31],v[12:15]\n"
EX"v10,v10\n"
PK"v248,v248,v248\n"
MF"v[20:23],a[92:95],a[44:47],v[20:23]\n"
EX"v247,v247\n"
"v_lshlrev_b32 v248,16,v248\n"
MF"v[28:31],a[92:95],a[60:63],v[28:31]\n"
WT"lgkmcnt(1)\n"
RD"a[108:111],v226 offset:11264\n"
PK"v1,v2,v3\n"
MF"v[32:35],a[96:99],a[0:3],v[216:219]\n"
EX"v11,v11\n"
PK"v249,v249,v249\n"
MF"v[40:43],a[96:99],a[16:19],0\n"
EX"v16,v16\n"
"v_lshlrev_b32 v249,16,v249\n"
MF"v[48:51],a[96:99],a[32:35],0\n"
RD"a[112:115],v226 offset:12288\n"
PK"v250,v250,v250\n"
MF"v[56:59],a[96:99],a[48:51],0\n"
EX"v17,v17\n"
PK"v8,v8,v9\n"
MF"v[32:35],a[100:103],a[4:7],v[32:35]\n"
EX"v18,v18\n"
"v_lshlrev_b32 v250,16,v250\n"
MF"v[40:43],a[100:103],a[20:23],v[40:43]\n"
RD"a[116:119],v226 offset:13312\n"
PK"v251,v251,v251\n"
MF"v[48:51],a[100:103],a[36:39],v[48:51]\n"
EX"v19,v19\n"
MF"v[56:59],a[100:103],a[52:55],v[56:59]\n"
WT"lgkmcnt(2)\n"
"v_lshlrev_b32 v251,16,v251\n"
PK"v9,v10,v11\n"
MF"v[32:35],a[104:107],a[8:11],v[32:35]\n"
EX"v24,v24\n"
"v_add_f32 v244,1.0,v244\n"
MF"v[40:43],a[104:107],a[24:27],v[40:43]\n"
"v_rcp_f32 v244,v244\n"
"v_add_f32 v245,1.0,v245\n"
MF"v[48:51],a[104:107],a[40:43],v[48:51]\n"
"v_rcp_f32 v245,v245\n"
"v_add_f32 v246,1.0,v246\n"
MF"v[56:59],a[104:107],a[56:59],v[56:59]\n"
"v_rcp_f32 v246,v246\n"
"v_add_f32 v247,1.0,v247\n"
MF"v[32:35],a[108:111],a[12:15],v[32:35]\n"
"v_rcp_f32 v247,v247\n"
EX"v25,v25\n"
MF"v[40:43],a[108:111],a[28:31],v[40:43]\n"
RD"a[120:123],v226 offset:14336\n"
RD"a[124:127],v226 offset:15360\n"
PK"v16,v16,v17\n"
MF"v[48:51],a[108:111],a[44:47],v[48:51]\n"
EX"v26,v26\n"
MF"v[56:59],a[108:111],a[60:63],v[56:59]\n"
WT"lgkmcnt(2)\n"
EX"v27,v27\n"
PK"v17,v18,v19\n"
MF"v[36:39],a[112:115],a[0:3],v[220:223]\n"
EX"v4,v4\n"
MU"v240,v240,v244\n"
MF"v[44:47],a[112:115],a[16:19],0\n"
RD"a[64:67],v227 offset:16384\n"
MU"v241,v241,v245\n"
MF"v[52:55],a[112:115],a[32:35],0\n"
EX"v5,v5\n"
MU"v242,v242,v246\n"
MF"v[60:63],a[112:115],a[48:51],0\n"
MU"v243,v243,v247\n"
PK"v24,v24,v25\n"
MF"v[36:39],a[116:119],a[4:7],v[36:39]\n"
EX"v6,v6\n"
PK"v240,v240,v240\n"
MF"v[44:47],a[116:119],a[20:23],v[44:47]\n"
RD"a[68:71],v227 offset:17408\n"
"v_lshlrev_b32 v240,16,v240\n"
MF"v[52:55],a[116:119],a[36:39],v[52:55]\n"
EX"v7,v7\n"
PK"v241,v241,v241\n"
MF"v[60:63],a[116:119],a[52:55],v[60:63]\n"
WT"lgkmcnt(2)\n"
"v_readlane_b32 s43,v228,s42\n"
"v_lshlrev_b32 v241,16,v241\n"
PK"v25,v26,v27\n"
MF"v[36:39],a[120:123],a[8:11],v[36:39]\n"
EX"v12,v12\n"
PK"v242,v242,v242\n"
MF"v[44:47],a[120:123],a[24:27],v[44:47]\n"
RD"a[72:75],v227 offset:18432\n"
"v_lshlrev_b32 v242,16,v242\n"
MF"v[52:55],a[120:123],a[40:43],v[52:55]\n"
EX"v13,v13\n"
PK"v243,v243,v243\n"
MF"v[60:63],a[120:123],a[56:59],v[60:63]\n"
"s_mov_b32 m0,s40\n"
"v_lshlrev_b32 v243,16,v243\n"
PK"v2,v4,v5\n"
MF"v[36:39],a[124:127],a[12:15],v[36:39]\n"
EX"v14,v14\n"
MU"v240,v240,v248\n"
MF"v[44:47],a[124:127],a[28:31],v[44:47]\n"
RD"a[76:79],v227 offset:19456\n"
"v_readlane_b32 s44,v229,s42\n"
MU"v241,v241,v249\n"
MF"v[52:55],a[124:127],a[44:47],v[52:55]\n"
EX"v15,v15\n"
MU"v242,v242,v250\n"
MF"v[60:63],a[124:127],a[60:63],v[60:63]\n"
WT"lgkmcnt(2)\n"
BL"v225,s[8:11],s43 offen offset:0 sc1 lds\n"
MU"v243,v243,v251\n"
MF"v[80:83],a[64:67],v[64:67],v[80:83]\n"
SA"s42,s42,1\n"
EX"v20,v20\n"
PK"v3,v6,v7\n"
MF"v[112:115],a[64:67],v[68:71],v[112:115]\n"
SA"s41,s41,32768\n"
RD"a[80:83],v227 offset:20480\n"
PK"v240,v240,v241\n"
PK"v241,v242,v243\n"
MF"v[144:147],a[64:67],v[72:75],v[144:147]\n"
"s_cmp_eq_u32 s41,163840\n"
EX"v21,v21\n"
"v_lshrrev_b32 v239,9,v224\n"
MF"v[176:179],a[64:67],v[76:79],v[176:179]\n"
"s_cselect_b32 s41,0,s41\n"
"v_lshlrev_b32 v239,8,v239\n"
"v_add_u32 v226,s41,v224\n"
PK"v10,v12,v13\n"
MF"v[84:87],a[68:71],v[64:67],v[84:87]\n"
EX"v22,v22\n"
"v_bfe_u32 v237,v224,8,1\n"
MF"v[116:119],a[68:71],v[68:71],v[116:119]\n"
RD"a[84:87],v227 offset:21504\n"
"v_lshl_add_u32 v239,v237,3,v239\n"
MF"v[148:151],a[68:71],v[72:75],v[148:151]\n"
EX"v23,v23\n"
"v_and_b32 v237,0x80,v224\n"
MF"v[180:183],a[68:71],v[76:79],v[180:183]\n"
WT"lgkmcnt(2)\n"
BL"v225,s[8:11],s43 offen offset:1024 sc1 lds\n"
"v_lshl_add_u32 v239,v237,23,v239\n"
MF"v[88:91],a[72:75],v[64:67],v[88:91]\n"
EX"v28,v28\n"
PK"v11,v14,v15\n"
MF"v[120:123],a[72:75],v[68:71],v[120:123]\n"
RD"a[88:91],v227 offset:22528\n"
"v_and_b32 v237,0x70,v224\n"
"v_add_u32 v239,v239,v237\n"
MF"v[152:155],a[72:75],v[72:75],v[152:155]\n"
"buffer_store_dwordx2 v[240:241],v239,s[8:11],s93 offen sc1\n"
MF"v[184:187],a[72:75],v[76:79],v[184:187]\n"
EX"v29,v29\n"
PK"v18,v20,v21\n"
MF"v[92:95],a[76:79],v[64:67],v[92:95]\n"
EX"v30,v30\n"
MF"v[124:127],a[76:79],v[68:71],v[124:127]\n"
RD"a[92:95],v227 offset:23552\n"
PK"v19,v22,v23\n"
MF"v[156:159],a[76:79],v[72:75],v[156:159]\n"
EX"v31,v31\n"
MF"v[188:191],a[76:79],v[76:79],v[188:191]\n"
WT"lgkmcnt(2)\n"
BL"v225,s[8:11],s43 offen offset:2048 sc1 lds\n"
MF"v[96:99],a[80:83],v[64:67],v[96:99]\n"
EX"v32,v32\n"
MF"v[128:131],a[80:83],v[68:71],v[128:131]\n"
RD"a[96:99],v227 offset:24576\n"
PK"v26,v28,v29\n"
MF"v[160:163],a[80:83],v[72:75],v[160:163]\n"
EX"v33,v33\n"
MF"v[192:195],a[80:83],v[76:79],v[192:195]\n"
PK"v27,v30,v31\n"
MF"v[100:103],a[84:87],v[64:67],v[100:103]\n"
EX"v34,v34\n"
MF"v[132:135],a[84:87],v[68:71],v[132:135]\n"
RD"a[100:103],v227 offset:25600\n"
MF"v[164:167],a[84:87],v[72:75],v[164:167]\n"
EX"v35,v35\n"
MF"v[196:199],a[84:87],v[76:79],v[196:199]\n"
WT"lgkmcnt(2)\n"
BL"v225,s[8:11],s43 offen offset:3072 sc1 lds\n"
MF"v[104:107],a[88:91],v[64:67],v[104:107]\n"
EX"v40,v40\n"
MF"v[136:139],a[88:91],v[68:71],v[136:139]\n"
RD"a[104:107],v227 offset:26624\n"
MF"v[168:171],a[88:91],v[72:75],v[168:171]\n"
EX"v41,v41\n"
MF"v[200:203],a[88:91],v[76:79],v[200:203]\n"
SA"m0,s40,16384\n"
MF"v[108:111],a[92:95],v[64:67],v[108:111]\n"
EX"v42,v42\n"
MF"v[140:143],a[92:95],v[68:71],v[140:143]\n"
RD"a[108:111],v227 offset:27648\n"
MF"v[172:175],a[92:95],v[72:75],v[172:175]\n"
EX"v43,v43\n"
MF"v[204:207],a[92:95],v[76:79],v[204:207]\n"
BL"v225,s[8:11],s44 offen offset:0 sc1 lds\n"
MF"a[132:135],a[128:131],v[64:67],a[132:135]\n"
SA"s40,s40,32768\n"
EX"v48,v48\n"
PK"v64,v32,v33\n"
MF"a[136:139],a[128:131],v[68:71],a[136:139]\n"
"s_cmp_eq_u32 s40,s46\n"
RD"a[112:115],v227 offset:28672\n"
PK"v65,v34,v35\n"
MF"a[140:143],a[128:131],v[72:75],a[140:143]\n"
"s_cselect_b32 s40,s47,s40\n"
EX"v49,v49\n"
MF"a[144:147],a[128:131],v[76:79],a[144:147]\n"
WT"lgkmcnt(2)\n"
PK"v68,v40,v41\n"
PK"v69,v42,v43\n"
MF"v[80:83],a[96:99],v[0:3],v[80:83]\n"
EX"v50,v50\n"
MF"v[112:115],a[96:99],v[8:11],v[112:115]\n"
RD"a[116:119],v227 offset:29696\n"
PK"v72,v48,v49\n"
MF"v[144:147],a[96:99],v[16:19],v[144:147]\n"
EX"v51,v51\n"
MF"v[176:179],a[96:99],v[24:27],v[176:179]\n"
BL"v225,s[8:11],s44 offen offset:1024 sc1 lds\n"
MF"v[84:87],a[100:103],v[0:3],v[84:87]\n"
EX"v36,v36\n"
MF"v[116:119],a[100:103],v[8:11],v[116:119]\n"
RD"a[120:123],v227 offset:30720\n"
PK"v73,v50,v51\n"
MF"v[148:151],a[100:103],v[16:19],v[148:151]\n"
EX"v37,v37\n"
MF"v[180:183],a[100:103],v[24:27],v[180:183]\n"
PK"v66,v36,v37\n"
MF"v[88:91],a[104:107],v[0:3],v[88:91]\n"
EX"v38,v38\n"
MF"v[120:123],a[104:107],v[8:11],v[120:123]\n"
RD"a[124:127],v227 offset:31744\n"
MF"v[152:155],a[104:107],v[16:19],v[152:155]\n"
EX"v39,v39\n"
MF"v[184:187],a[104:107],v[24:27],v[184:187]\n"
WT"lgkmcnt(2)\n"
BL"v225,s[8:11],s44 offen offset:2048 sc1 lds\n"
MF"v[92:95],a[108:111],v[0:3],v[92:95]\n"
EX"v44,v44\n"
MF"v[124:127],a[108:111],v[8:11],v[124:127]\n"
PK"v67,v38,v39\n"
MF"v[156:159],a[108:111],v[16:19],v[156:159]\n"
EX"v45,v45\n"
MF"v[188:191],a[108:111],v[24:27],v[188:191]\n"
PK"v70,v44,v45\n"
MF"v[96:99],a[112:115],v[0:3],v[96:99]\n"
EX"v46,v46\n"
MF"v[128:131],a[112:115],v[8:11],v[128:131]\n"
MF"v[160:163],a[112:115],v[16:19],v[160:163]\n"
EX"v47,v47\n"
MF"v[192:195],a[112:115],v[24:27],v[192:195]\n"
BL"v225,s[8:11],s44 offen offset:3072 sc1 lds\n"
MF"v[100:103],a[116:119],v[0:3],v[100:103]\n"
WT"lgkmcnt(0)\n"
"s_barrier\n"
MF"v[132:135],a[116:119],v[8:11],v[132:135]\n"
RD"a[64:67],v226 offset:0\n"
PK"v71,v46,v47\n"
MF"v[164:167],a[116:119],v[16:19],v[164:167]\n"
EX"v52,v52\n"
MF"v[196:199],a[116:119],v[24:27],v[196:199]\n"
RD"a[68:71],v226 offset:1024\n"
MF"v[104:107],a[120:123],v[0:3],v[104:107]\n"
EX"v53,v53\n"
MF"v[136:139],a[120:123],v[8:11],v[136:139]\n"
RD"a[72:75],v226 offset:2048\n"
PK"v74,v52,v53\n"
MF"v[168:171],a[120:123],v[16:19],v[168:171]\n"
EX"v54,v54\n"
MF"v[200:203],a[120:123],v[24:27],v[200:203]\n"
MF"v[108:111],a[124:127],v[0:3],v[108:111]\n"
WT"vmcnt(6)\n"
"buffer_store_dwordx2 v[232:233],v230,s[8:11],s94 offen sc1\n"
MF"v[140:143],a[124:127],v[8:11],v[140:143]\n"
EX"v55,v55\n"
MF"v[172:175],a[124:127],v[16:19],v[172:175]\n"
RD"a[76:79],v226 offset:3072\n"
PK"v75,v54,v55\n"
MF"v[204:207],a[124:127],v[24:27],v[204:207]\n"
RD"a[80:83],v226 offset:4096\n"
MF"a[132:135],a[128:131],v[0:3],a[132:135]\n"
MF"a[136:139],a[128:131],v[8:11],a[136:139]\n"
RD"a[84:87],v226 offset:5120\n"
MF"a[140:143],a[128:131],v[16:19],a[140:143]\n"
MF"a[144:147],a[128:131],v[24:27],a[144:147]\n"
"s_branch LDB_%=\n"
"LMD_%=:\n"
"s_cmp_eq_u32 s34,6\n"
"s_cbranch_scc1 LME_%=\n"
WT"lgkmcnt(9)\n"
MF"v[0:3],a[64:67],a[0:3],v[208:211]\n"
EX"v56,v56\n"
MF"v[8:11],a[64:67],a[16:19],0\n"
"v_lshrrev_b32 v239,9,v224\n"
"v_lshlrev_b32 v239,8,v239\n"
MF"v[16:19],a[64:67],a[32:35],0\n"
EX"v57,v57\n"
MF"v[24:27],a[64:67],a[48:51],0\n"
WT"lgkmcnt(1)\n"
"v_bfe_u32 v237,v224,8,1\n"
"v_lshl_add_u32 v239,v237,3,v239\n"
MF"v[0:3],a[68:71],a[4:7],v[0:3]\n"
EX"v58,v58\n"
MF"v[8:11],a[68:71],a[20:23],v[8:11]\n"
"v_and_b32 v237,0x80,v224\n"
PK"v76,v56,v57\n"
MF"v[16:19],a[68:71],a[36:39],v[16:19]\n"
EX"v59,v59\n"
MF"v[24:27],a[68:71],a[52:55],v[24:27]\n"
"v_lshl_add_u32 v239,v237,23,v239\n"
"v_not_b32 v236,v234\n"
MF"v[0:3],a[72:75],a[8:11],v[0:3]\n"
EX"v60,v60\n"
MF"v[8:11],a[72:75],a[24:27],v[8:11]\n"
SA"s53,s93,4292282368\n"
RD"a[88:91],v226 offset:6144\n"
"v_mov_b32 v227,v226\n"
MF"v[16:19],a[72:75],a[40:43],v[16:19]\n"
EX"v61,v61\n"
MF"v[24:27],a[72:75],a[56:59],v[24:27]\n"
"v_and_b32 v237,15,v236\n"
"v_bfe_u32 v236,v236,4,7\n"
MF"v[0:3],a[76:79],a[12:15],v[0:3]\n"
EX"v62,v62\n"
MF"v[8:11],a[76:79],a[28:31],v[8:11]\n"
RD"a[92:95],v226 offset:7168\n"
"v_mul_u32_u24 v236,20480,v236\n"
MF"v[16:19],a[76:79],a[44:47],v[16:19]\n"
EX"v63,v63\n"
MF"v[24:27],a[76:79],a[60:63],v[24:27]\n"
PK"v77,v58,v59\n"
"v_lshl_add_u32 v236,v237,4,v236\n"
MF"v[4:7],a[80:83],a[0:3],v[212:215]\n"
EX"v0,v0\n"
"v_add_u32 v236,v236,v239\n"
MF"v[12:15],a[80:83],a[16:19],0\n"
RD"a[96:99],v226 offset:8192\n"
"v_and_b32 v237,0x70,v224\n"
MF"v[20:23],a[80:83],a[32:35],0\n"
EX"v1,v1\n"
MF"v[28:31],a[80:83],a[48:51],0\n"
WT"lgkmcnt(2)\n"
"v_add_u32 v239,v239,v237\n"
PK"v78,v60,v61\n"
MF"v[4:7],a[84:87],a[4:7],v[4:7]\n"
EX"v2,v2\n"
"v_add_f32 v240,v240,v244\n"
MF"v[12:15],a[84:87],a[20:23],v[12:15]\n"
RD"a[100:103],v226 offset:9216\n"
"v_add_f32 v248,v248,v252\n"
"v_add_f32 v241,v241,v245\n"
MF"v[20:23],a[84:87],a[36:39],v[20:23]\n"
EX"v3,v3\n"
"v_add_f32 v249,v249,v253\n"
MF"v[28:31],a[84:87],a[52:55],v[28:31]\n"
"v_add_f32 v242,v242,v246\n"
PK"v79,v62,v63\n"
MF"v[4:7],a[88:91],a[8:11],v[4:7]\n"
EX"v8,v8\n"
"v_add_f32 v250,v250,v254\n"
MF"v[12:15],a[88:91],a[24:27],v[12:15]\n"
RD"a[104:107],v226 offset:10240\n"
"v_add_f32 v243,v243,v247\n"
"v_add_f32 v251,v251,v255\n"
MF"v[20:23],a[88:91],a[40:43],v[20:23]\n"
"buffer_load_dwordx2 v[254:255],v236,s[8:11],s53 offen sc1\n"
"v_ashrrev_i32 v235,31,v234\n"
MF"v[28:31],a[88:91],a[56:59],v[28:31]\n"
WT"lgkmcnt(2)\n"
EX"v9,v9\n"
"v_not_b32 v235,v235\n"
MF"v[4:7],a[92:95],a[12:15],v[4:7]\n"
EX"v10,v10\n"
PK"v0,v0,v1\n"
MF"v[12:15],a[92:95],a[28:31],v[12:15]\n"
RD"a[108:111],v226 offset:11264\n"
"v_or_b32 v235,0x80000000,v235\n"
"v_xor_b32 v235,v235,v234\n"
MF"v[20:23],a[92:95],a[44:47],v[20:23]\n"
EX"v11,v11\n"
"v_and_b32 v235,0xffff0000,v235\n"
MF"v[28:31],a[92:95],a[60:63],v[28:31]\n"
MU"v235,0xbfb8aa3b,v235\n"
PK"v1,v2,v3\n"
EX"v235,v235\n"
MF"v[32:35],a[96:99],a[0:3],v[216:219]\n"
EX"v16,v16\n"
MF"v[40:43],a[96:99],a[16:19],0\n"
RD"a[112:115],v226 offset:12288\n"
"v_add_f32 v235,1.0,v235\n"
"v_rcp_f32 v235,v235\n"
MF"v[48:51],a[96:99],a[32:35],0\n"
EX"v17,v17\n"
MF"v[56:59],a[96:99],a[48:51],0\n"
WT"lgkmcnt(2)\n"
PK"v235,v235,v235\n"
PK"v8,v8,v9\n"
MF"v[32:35],a[100:103],a[4:7],v[32:35]\n"
EX"v18,v18\n"
"v_lshlrev_b32 v235,16,v235\n"
MF"v[40:43],a[100:103],a[20:23],v[40:43]\n"
RD"a[116:119],v226 offset:13312\n"
"v_add_f32 v240,v240,v248\n"
MF"v[48:51],a[100:103],a[36:39],v[48:51]\n"
EX"v19,v19\n"
"v_add_f32 v241,v241,v249\n"
MF"v[56:59],a[100:103],a[52:55],v[56:59]\n"
"v_add_f32 v242,v242,v250\n"
PK"v9,v10,v11\n"
MF"v[32:35],a[104:107],a[8:11],v[32:35]\n"
EX"v24,v24\n"
"v_add_f32 v243,v243,v251\n"
MF"v[40:43],a[104:107],a[24:27],v[40:43]\n"
RD"a[120:123],v226 offset:14336\n"
PK"v240,v240,v240\n"
MF"v[48:51],a[104:107],a[40:43],v[48:51]\n"
EX"v25,v25\n"
"v_lshlrev_b32 v240,16,v240\n"
MF"v[56:59],a[104:107],a[56:59],v[56:59]\n"
WT"lgkmcnt(2)\n"
MU"v240,v240,v235\n"
PK"v16,v16,v17\n"
MF"v[32:35],a[108:111],a[12:15],v[32:35]\n"
EX"v26,v26\n"
PK"v240,v240,v240\n"
MF"v[40:43],a[108:111],a[28:31],v[40:43]\n"
RD"a[124:127],v226 offset:15360\n"
"v_lshlrev_b32 v240,16,v240\n"
MF"v[48:51],a[108:111],a[44:47],v[48:51]\n"
EX"v27,v27\n"
PK"v241,v241,v241\n"
MF"v[56:59],a[108:111],a[60:63],v[56:59]\n"
"v_lshlrev_b32 v241,16,v241\n"
PK"v17,v18,v19\n"
MF"v[36:39],a[112:115],a[0:3],v[220:223]\n"
EX"v4,v4\n"
MU"v241,v241,v235\n"
MF"v[44:47],a[112:115],a[16:19],0\n"
RD"a[64:67],v227 offset:16384\n"
PK"v241,v241,v241\n"
MF"v[52:55],a[112:115],a[32:35],0\n"
EX"v5,v5\n"
"v_lshlrev_b32 v241,16,v241\n"
MF"v[60:63],a[112:115],a[48:51],0\n"
WT"lgkmcnt(2)\n"
PK"v242,v242,v242\n"
PK"v24,v24,v25\n"
MF"v[36:39],a[116:119],a[4:7],v[36:39]\n"
EX"v6,v6\n"
"v_lshlrev_b32 v242,16,v242\n"
MF"v[44:47],a[116:119],a[20:23],v[44:47]\n"
RD"a[68:71],v227 offset:17408\n"
MU"v242,v242,v235\n"
MF"v[52:55],a[116:119],a[36:39],v[52:55]\n"
EX"v7,v7\n"
PK"v242,v242,v242\n"
MF"v[60:63],a[116:119],a[52:55],v[60:63]\n"
"v_readlane_b32 s43,v228,s42\n"
"v_lshlrev_b32 v242,16,v242\n"
PK"v25,v26,v27\n"
MF"v[36:39],a[120:123],a[8:11],v[36:39]\n"
EX"v12,v12\n"
PK"v243,v243,v243\n"
MF"v[44:47],a[120:123],a[24:27],v[44:47]\n"
RD"a[72:75],v227 offset:18432\n"
"v_lshlrev_b32 v243,16,v243\n"
MF"v[52:55],a[120:123],a[40:43],v[52:55]\n"
EX"v13,v13\n"
MU"v243,v243,v235\n"
MF"v[60:63],a[120:123],a[56:59],v[60:63]\n"
WT"lgkmcnt(2)\n"
PK"v243,v243,v243\n"
PK"v2,v4,v5\n"
MF"v[36:39],a[124:127],a[12:15],v[36:39]\n"
"s_mov_b32 m0,s40\n"
EX"v14,v14\n"
"v_lshlrev_b32 v243,16,v243\n"
MF"v[44:47],a[124:127],a[28:31],v[44:47]\n"
RD"a[76:79],v227 offset:19456\n"
"v_readlane_b32 s44,v229,s42\n"
MF"v[52:55],a[124:127],a[44:47],v[52:55]\n"
EX"v15,v15\n"
MF"v[60:63],a[124:127],a[60:63],v[60:63]\n"
SA"s42,s42,1\n"
BL"v225,s[8:11],s43 offen offset:0 sc1 lds\n"
PK"v3,v6,v7\n"
MF"v[80:83],a[64:67],v[64:67],v[80:83]\n"
SA"s41,s41,32768\n"
EX"v20,v20\n"
MF"v[112:115],a[64:67],v[68:71],v[112:115]\n"
"s_cmp_eq_u32 s41,163840\n"
RD"a[80:83],v227 offset:20480\n"
PK"v10,v12,v13\n"
MF"v[144:147],a[64:67],v[72:75],v[144:147]\n"
"s_cselect_b32 s41,0,s41\n"
EX"v21,v21\n"
MF"v[176:179],a[64:67],v[76:79],v[176:179]\n"
WT"lgkmcnt(2)\n"
"v_add_u32 v226,s41,v224\n"
PK"v11,v14,v15\n"
MF"v[84:87],a[68:71],v[64:67],v[84:87]\n"
EX"v22,v22\n"
MF"v[116:119],a[68:71],v[68:71],v[116:119]\n"
RD"a[84:87],v227 offset:21504\n"
PK"v18,v20,v21\n"
MF"v[148:151],a[68:71],v[72:75],v[148:151]\n"
EX"v23,v23\n"
MF"v[180:183],a[68:71],v[76:79],v[180:183]\n"
BL"v225,s[8:11],s43 offen offset:1024 sc1 lds\n"
MF"v[88:91],a[72:75],v[64:67],v[88:91]\n"
EX"v28,v28\n"
MF"v[120:123],a[72:75],v[68:71],v[120:123]\n"
RD"a[88:91],v227 offset:22528\n"
PK"v19,v22,v23\n"
MF"v[152:155],a[72:75],v[72:75],v[152:155]\n"
EX"v29,v29\n"
MF"v[184:187],a[72:75],v[76:79],v[184:187]\n"
WT"lgkmcnt(2)\n"
WT"vmcnt(2)\n"
"v_lshlrev_b32 v244,16,v254\n"
"v_and_b32 v245,0xffff0000,v254\n"
MF"v[92:95],a[76:79],v[64:67],v[92:95]\n"
EX"v30,v30\n"
"v_lshlrev_b32 v246,16,v255\n"
MF"v[124:127],a[76:79],v[68:71],v[124:127]\n"
RD"a[92:95],v227 offset:23552\n"
"v_and_b32 v247,0xffff0000,v255\n"
"v_add_f32 v240,v240,v244\n"
MF"v[156:159],a[76:79],v[72:75],v[156:159]\n"
EX"v31,v31\n"
"v_add_f32 v241,v241,v245\n"
MF"v[188:191],a[76:79],v[76:79],v[188:191]\n"
BL"v225,s[8:11],s43 offen offset:2048 sc1 lds\n"
"v_add_f32 v242,v242,v246\n"
MF"v[96:99],a[80:83],v[64:67],v[96:99]\n"
EX"v32,v32\n"
"v_add_f32 v243,v243,v247\n"
MF"v[128:131],a[80:83],v[68:71],v[128:131]\n"
RD"a[96:99],v227 offset:24576\n"
PK"v240,v240,v241\n"
PK"v241,v242,v243\n"
MF"v[160:163],a[80:83],v[72:75],v[160:163]\n"
"buffer_store_dwordx2 v[240:241],v239,s[8:11],s93 offen sc1\n"
MF"v[192:195],a[80:83],v[76:79],v[192:195]\n"
WT"lgkmcnt(2)\n"
EX"v33,v33\n"
PK"v26,v28,v29\n"
MF"v[100:103],a[84:87],v[64:67],v[100:103]\n"
EX"v34,v34\n"
MF"v[132:135],a[84:87],v[68:71],v[132:135]\n"
RD"a[100:103],v227 offset:25600\n"
PK"v27,v30,v31\n"
MF"v[164:167],a[84:87],v[72:75],v[164:167]\n"
EX"v35,v35\n"
MF"v[196:199],a[84:87],v[76:79],v[196:199]\n"
BL"v225,s[8:11],s43 offen offset:3072 sc1 lds\n"
MF"v[104:107],a[88:91],v[64:67],v[104:107]\n"
EX"v40,v40\n"
MF"v[136:139],a[88:91],v[68:71],v[136:139]\n"
RD"a[104:107],v227 offset:26624\n"
MF"v[168:171],a[88:91],v[72:75],v[168:171]\n"
EX"v41,v41\n"
MF"v[200:203],a[88:91],v[76:79],v[200:203]\n"
WT"lgkmcnt(2)\n"
SA"m0,s40,16384\n"
MF"v[108:111],a[92:95],v[64:67],v[108:111]\n"
EX"v42,v42\n"
MF"v[140:143],a[92:95],v[68:71],v[140:143]\n"
RD"a[108:111],v227 offset:27648\n"
MF"v[172:175],a[92:95],v[72:75],v[172:175]\n"
EX"v43,v43\n"
MF"v[204:207],a[92:95],v[76:79],v[204:207]\n"
BL"v225,s[8:11],s44 offen offset:0 sc1 lds\n"
MF"a[132:135],a[128:131],v[64:67],a[132:135]\n"
SA"s40,s40,32768\n"
EX"v48,v48\n"
PK"v64,v32,v33\n"
MF"a[136:139],a[128:131],v[68:71],a[136:139]\n"
"s_cmp_eq_u32 s40,s46\n"
RD"a[112:115],v227 offset:28672\n"
PK"v65,v34,v35\n"
MF"a[140:143],a[128:131],v[72:75],a[140:143]\n"
"s_cselect_b32 s40,s47,s40\n"
EX"v49,v49\n"
MF"a[144:147],a[128:131],v[76:79],a[144:147]\n"
PK"v68,v40,v41\n"
PK"v69,v42,v43\n"
MF"v[80:83],a[96:99],v[0:3],v[80:83]\n"
EX"v50,v50\n"
MF"v[112:115],a[96:99],v[8:11],v[112:115]\n"
RD"a[116:119],v227 offset:29696\n"
PK"v72,v48,v49\n"
MF"v[144:147],a[96:99],v[16:19],v[144:147]\n"
EX"v51,v51\n"
MF"v[176:179],a[96:99],v[24:27],v[176:179]\n"
WT"lgkmcnt(2)\n"
BL"v225,s[8:11],s44 offen offset:1024 sc1 lds\n"
MF"v[84:87],a[100:103],v[0:3],v[84:87]\n"
EX"v36,v36\n"
MF"v[116:119],a[100:103],v[8:11],v[116:119]\n"
RD"a[120:123],v227 offset:30720\n"
PK"v73,v50,v51\n"
MF"v[148:151],a[100:103],v[16:19],v[148:151]\n"
EX"v37,v37\n"
MF"v[180:183],a[100:103],v[24:27],v[180:183]\n"
PK"v66,v36,v37\n"
MF"v[88:91],a[104:107],v[0:3],v[88:91]\n"
EX"v38,v38\n"
MF"v[120:123],a[104:107],v[8:11],v[120:123]\n"
RD"a[124:127],v227 offset:31744\n"
MF"v[152:155],a[104:107],v[16:19],v[152:155]\n"
EX"v39,v39\n"
MF"v[184:187],a[104:107],v[24:27],v[184:187]\n"
BL"v225,s[8:11],s44 offen offset:2048 sc1 lds\n"
MF"v[92:95],a[108:111],v[0:3],v[92:95]\n"
EX"v44,v44\n"
MF"v[124:127],a[108:111],v[8:11],v[124:127]\n"
PK"v67,v38,v39\n"
MF"v[156:159],a[108:111],v[16:19],v[156:159]\n"
EX"v45,v45\n"
MF"v[188:191],a[108:111],v[24:27],v[188:191]\n"
WT"lgkmcnt(1)\n"
PK"v70,v44,v45\n"
MF"v[96:99],a[112:115],v[0:3],v[96:99]\n"
EX"v46,v46\n"
MF"v[128:131],a[112:115],v[8:11],v[128:131]\n"
MF"v[160:163],a[112:115],v[16:19],v[160:163]\n"
EX"v47,v47\n"
MF"v[192:195],a[112:115],v[24:27],v[192:195]\n"
BL"v225,s[8:11],s44 offen offset:3072 sc1 lds\n"
MF"v[100:103],a[116:119],v[0:3],v[100:103]\n"
WT"lgkmcnt(0)\n"
"s_barrier\n"
MF"v[132:135],a[116:119],v[8:11],v[132:135]\n"
RD"a[64:67],v226 offset:0\n"
PK"v71,v46,v47\n"
MF"v[164:167],a[116:119],v[16:19],v[164:167]\n"
EX"v52,v52\n"
MF"v[196:199],a[116:119],v[24:27],v[196:199]\n"
RD"a[68:71],v226 offset:1024\n"
MF"v[104:107],a[120:123],v[0:3],v[104:107]\n"
EX"v53,v53\n"
MF"v[136:139],a[120:123],v[8:11],v[136:139]\n"
RD"a[72:75],v226 offset:2048\n"
PK"v74,v52,v53\n"
MF"v[168:171],a[120:123],v[16:19],v[168:171]\n"
EX"v54,v54\n"
MF"v[200:203],a[120:123],v[24:27],v[200:203]\n"
MF"v[108:111],a[124:127],v[0:3],v[108:111]\n"
WT"vmcnt(5)\n"
"buffer_store_dwordx2 v[232:233],v230,s[8:11],s94 offen sc1\n"
MF"v[140:143],a[124:127],v[8:11],v[140:143]\n"
EX"v55,v55\n"
MF"v[172:175],a[124:127],v[16:19],v[172:175]\n"
RD"a[76:79],v226 offset:3072\n"
PK"v75,v54,v55\n"
MF"v[204:207],a[124:127],v[24:27],v[204:207]\n"
RD"a[80:83],v226 offset:4096\n"
MF"a[132:135],a[128:131],v[0:3],a[132:135]\n"
MF"a[136:139],a[128:131],v[8:11],a[136:139]\n"
RD"a[84:87],v226 offset:5120\n"
MF"a[140:143],a[128:131],v[16:19],a[140:143]\n"
MF"a[144:147],a[128:131],v[24:27],a[144:147]\n"
"s_branch LDB_%=\n"
"LME_%=:\n"
WT"lgkmcnt(9)\n"
MF"v[0:3],a[64:67],a[0:3],0\n"
EX"v56,v56\n"
MF"v[8:11],a[64:67],a[16:19],0\n"
"v_lshrrev_b32 v239,9,v224\n"
"v_lshlrev_b32 v239,8,v239\n"
MF"v[16:19],a[64:67],a[32:35],0\n"
EX"v57,v57\n"
MF"v[24:27],a[64:67],a[48:51],0\n"
WT"lgkmcnt(1)\n"
"v_bfe_u32 v237,v224,8,1\n"
"v_lshl_add_u32 v239,v237,3,v239\n"
MF"v[0:3],a[68:71],a[4:7],v[0:3]\n"
EX"v58,v58\n"
MF"v[8:11],a[68:71],a[20:23],v[8:11]\n"
"v_and_b32 v237,0x80,v224\n"
PK"v76,v56,v57\n"
MF"v[16:19],a[68:71],a[36:39],v[16:19]\n"
EX"v59,v59\n"
MF"v[24:27],a[68:71],a[52:55],v[24:27]\n"
"v_lshl_add_u32 v239,v237,23,v239\n"
"v_not_b32 v236,v234\n"
MF"v[0:3],a[72:75],a[8:11],v[0:3]\n"
EX"v60,v60\n"
MF"v[8:11],a[72:75],a[24:27],v[8:11]\n"
SA"s53,s93,4292282368\n"
RD"a[88:91],v226 offset:6144\n"
"v_mov_b32 v227,v226\n"
MF"v[16:19],a[72:75],a[40:43],v[16:19]\n"
EX"v61,v61\n"
MF"v[24:27],a[72:75],a[56:59],v[24:27]\n"
"v_and_b32 v237,15,v236\n"
"v_bfe_u32 v236,v236,4,7\n"
MF"v[0:3],a[76:79],a[12:15],v[0:3]\n"
EX"v62,v62\n"
MF"v[8:11],a[76:79],a[28:31],v[8:11]\n"
RD"a[92:95],v226 offset:7168\n"
"v_mul_u32_u24 v236,20480,v236\n"
MF"v[16:19],a[76:79],a[44:47],v[16:19]\n"
EX"v63,v63\n"
MF"v[24:27],a[76:79],a[60:63],v[24:27]\n"
PK"v77,v58,v59\n"
"v_lshl_add_u32 v236,v237,4,v236\n"
MF"v[4:7],a[80:83],a[0:3],0\n"
EX"v0,v0\n"
"v_add_u32 v236,v236,v239\n"
MF"v[12:15],a[80:83],a[16:19],0\n"
RD"a[96:99],v226 offset:8192\n"
"v_and_b32 v237,0x70,v224\n"
MF"v[20:23],a[80:83],a[32:35],0\n"
EX"v1,v1\n"
MF"v[28:31],a[80:83],a[48:51],0\n"
WT"lgkmcnt(2)\n"
"v_add_u32 v239,v239,v237\n"
PK"v78,v60,v61\n"
MF"v[4:7],a[84:87],a[4:7],v[4:7]\n"
EX"v2,v2\n"
"v_add_f32 v240,v240,v244\n"
MF"v[12:15],a[84:87],a[20:23],v[12:15]\n"
RD"a[100:103],v226 offset:9216\n"
"v_add_f32 v248,v248,v252\n"
"v_add_f32 v241,v241,v245\n"
MF"v[20:23],a[84:87],a[36:39],v[20:23]\n"
EX"v3,v3\n"
"v_add_f32 v249,v249,v253\n"
MF"v[28:31],a[84:87],a[52:55],v[28:31]\n"
"v_add_f32 v242,v242,v246\n"
PK"v79,v62,v63\n"
MF"v[4:7],a[88:91],a[8:11],v[4:7]\n"
EX"v8,v8\n"
"v_add_f32 v250,v250,v254\n"
MF"v[12:15],a[88:91],a[24:27],v[12:15]\n"
RD"a[104:107],v226 offset:10240\n"
"v_add_f32 v243,v243,v247\n"
"v_add_f32 v251,v251,v255\n"
MF"v[20:23],a[88:91],a[40:43],v[20:23]\n"
"buffer_load_dwordx2 v[254:255],v236,s[8:11],s53 offen sc1\n"
"v_ashrrev_i32 v235,31,v234\n"
MF"v[28:31],a[88:91],a[56:59],v[28:31]\n"
WT"lgkmcnt(2)\n"
EX"v9,v9\n"
"v_not_b32 v235,v235\n"
MF"v[4:7],a[92:95],a[12:15],v[4:7]\n"
EX"v10,v10\n"
PK"v0,v0,v1\n"
MF"v[12:15],a[92:95],a[28:31],v[12:15]\n"
RD"a[108:111],v226 offset:11264\n"
"v_or_b32 v235,0x80000000,v235\n"
"v_xor_b32 v235,v235,v234\n"
MF"v[20:23],a[92:95],a[44:47],v[20:23]\n"
EX"v11,v11\n"
"v_and_b32 v235,0xffff0000,v235\n"
MF"v[28:31],a[92:95],a[60:63],v[28:31]\n"
MU"v235,0xbfb8aa3b,v235\n"
PK"v1,v2,v3\n"
EX"v235,v235\n"
MF"v[32:35],a[96:99],a[0:3],0\n"
EX"v16,v16\n"
MF"v[40:43],a[96:99],a[16:19],0\n"
RD"a[112:115],v226 offset:12288\n"
"v_add_f32 v235,1.0,v235\n"
"v_rcp_f32 v235,v235\n"
MF"v[48:51],a[96:99],a[32:35],0\n"
EX"v17,v17\n"
MF"v[56:59],a[96:99],a[48:51],0\n"
WT"lgkmcnt(2)\n"
PK"v235,v235,v235\n"
PK"v8,v8,v9\n"
MF"v[32:35],a[100:103],a[4:7],v[32:35]\n"
EX"v18,v18\n"
"v_lshlrev_b32 v235,16,v235\n"
MF"v[40:43],a[100:103],a[20:23],v[40:43]\n"
RD"a[116:119],v226 offset:13312\n"
"v_add_f32 v240,v240,v248\n"
MF"v[48:51],a[100:103],a[36:39],v[48:51]\n"
EX"v19,v19\n"
"v_add_f32 v241,v241,v249\n"
MF"v[56:59],a[100:103],a[52:55],v[56:59]\n"
"v_add_f32 v242,v242,v250\n"
PK"v9,v10,v11\n"
MF"v[32:35],a[104:107],a[8:11],v[32:35]\n"
EX"v24,v24\n"
"v_add_f32 v243,v243,v251\n"
MF"v[40:43],a[104:107],a[24:27],v[40:43]\n"
RD"a[120:123],v226 offset:14336\n"
PK"v240,v240,v240\n"
MF"v[48:51],a[104:107],a[40:43],v[48:51]\n"
EX"v25,v25\n"
"v_lshlrev_b32 v240,16,v240\n"
MF"v[56:59],a[104:107],a[56:59],v[56:59]\n"
WT"lgkmcnt(2)\n"
MU"v240,v240,v235\n"
PK"v16,v16,v17\n"
MF"v[32:35],a[108:111],a[12:15],v[32:35]\n"
EX"v26,v26\n"
PK"v240,v240,v240\n"
MF"v[40:43],a[108:111],a[28:31],v[40:43]\n"
RD"a[124:127],v226 offset:15360\n"
"v_lshlrev_b32 v240,16,v240\n"
MF"v[48:51],a[108:111],a[44:47],v[48:51]\n"
EX"v27,v27\n"
PK"v241,v241,v241\n"
MF"v[56:59],a[108:111],a[60:63],v[56:59]\n"
"v_lshlrev_b32 v241,16,v241\n"
PK"v17,v18,v19\n"
MF"v[36:39],a[112:115],a[0:3],0\n"
EX"v4,v4\n"
MU"v241,v241,v235\n"
MF"v[44:47],a[112:115],a[16:19],0\n"
RD"a[64:67],v227 offset:16384\n"
PK"v241,v241,v241\n"
MF"v[52:55],a[112:115],a[32:35],0\n"
EX"v5,v5\n"
"v_lshlrev_b32 v241,16,v241\n"
MF"v[60:63],a[112:115],a[48:51],0\n"
WT"lgkmcnt(2)\n"
PK"v242,v242,v242\n"
PK"v24,v24,v25\n"
MF"v[36:39],a[116:119],a[4:7],v[36:39]\n"
EX"v6,v6\n"
"v_lshlrev_b32 v242,16,v242\n"
MF"v[44:47],a[116:119],a[20:23],v[44:47]\n"
RD"a[68:71],v227 offset:17408\n"
MU"v242,v242,v235\n"
MF"v[52:55],a[116:119],a[36:39],v[52:55]\n"
EX"v7,v7\n"
PK"v242,v242,v242\n"
MF"v[60:63],a[116:119],a[52:55],v[60:63]\n"
"v_readlane_b32 s43,v228,s42\n"
"v_lshlrev_b32 v242,16,v242\n"
PK"v25,v26,v27\n"
MF"v[36:39],a[120:123],a[8:11],v[36:39]\n"
EX"v12,v12\n"
PK"v243,v243,v243\n"
MF"v[44:47],a[120:123],a[24:27],v[44:47]\n"
RD"a[72:75],v227 offset:18432\n"
"v_lshlrev_b32 v243,16,v243\n"
MF"v[52:55],a[120:123],a[40:43],v[52:55]\n"
EX"v13,v13\n"
MU"v243,v243,v235\n"
MF"v[60:63],a[120:123],a[56:59],v[60:63]\n"
WT"lgkmcnt(2)\n"
PK"v243,v243,v243\n"
PK"v2,v4,v5\n"
MF"v[36:39],a[124:127],a[12:15],v[36:39]\n"
"s_mov_b32 m0,s40\n"
EX"v14,v14\n"
"v_lshlrev_b32 v243,16,v243\n"
MF"v[44:47],a[124:127],a[28:31],v[44:47]\n"
RD"a[76:79],v227 offset:19456\n"
"v_readlane_b32 s44,v229,s42\n"
MF"v[52:55],a[124:127],a[44:47],v[52:55]\n"
EX"v15,v15\n"
MF"v[60:63],a[124:127],a[60:63],v[60:63]\n"
SA"s42,s42,1\n"
BL"v225,s[8:11],s43 offen offset:0 sc1 lds\n"
PK"v3,v6,v7\n"
MF"v[80:83],a[64:67],v[64:67],v[80:83]\n"
SA"s41,s41,32768\n"
EX"v20,v20\n"
MF"v[112:115],a[64:67],v[68:71],v[112:115]\n"
"s_cmp_eq_u32 s41,163840\n"
RD"a[80:83],v227 offset:20480\n"
PK"v10,v12,v13\n"
MF"v[144:147],a[64:67],v[72:75],v[144:147]\n"
"s_cselect_b32 s41,0,s41\n"
EX"v21,v21\n"
MF"v[176:179],a[64:67],v[76:79],v[176:179]\n"
WT"lgkmcnt(2)\n"
"v_add_u32 v226,s41,v224\n"
PK"v11,v14,v15\n"
MF"v[84:87],a[68:71],v[64:67],v[84:87]\n"
EX"v22,v22\n"
MF"v[116:119],a[68:71],v[68:71],v[116:119]\n"
RD"a[84:87],v227 offset:21504\n"
PK"v18,v20,v21\n"
MF"v[148:151],a[68:71],v[72:75],v[148:151]\n"
EX"v23,v23\n"
MF"v[180:183],a[68:71],v[76:79],v[180:183]\n"
BL"v225,s[8:11],s43 offen offset:1024 sc1 lds\n"
MF"v[88:91],a[72:75],v[64:67],v[88:91]\n"
EX"v28,v28\n"
MF"v[120:123],a[72:75],v[68:71],v[120:123]\n"
RD"a[88:91],v227 offset:22528\n"
PK"v19,v22,v23\n"
MF"v[152:155],a[72:75],v[72:75],v[152:155]\n"
EX"v29,v29\n"
MF"v[184:187],a[72:75],v[76:79],v[184:187]\n"
WT"lgkmcnt(2)\n"
WT"vmcnt(2)\n"
"v_lshlrev_b32 v244,16,v254\n"
"v_and_b32 v245,0xffff0000,v254\n"
MF"v[92:95],a[76:79],v[64:67],v[92:95]\n"
EX"v30,v30\n"
"v_lshlrev_b32 v246,16,v255\n"
MF"v[124:127],a[76:79],v[68:71],v[124:127]\n"
RD"a[92:95],v227 offset:23552\n"
"v_and_b32 v247,0xffff0000,v255\n"
"v_add_f32 v240,v240,v244\n"
MF"v[156:159],a[76:79],v[72:75],v[156:159]\n"
EX"v31,v31\n"
"v_add_f32 v241,v241,v245\n"
MF"v[188:191],a[76:79],v[76:79],v[188:191]\n"
BL"v225,s[8:11],s43 offen offset:2048 sc1 lds\n"
"v_add_f32 v242,v242,v246\n"
MF"v[96:99],a[80:83],v[64:67],v[96:99]\n"
EX"v32,v32\n"
"v_add_f32 v243,v243,v247\n"
MF"v[128:131],a[80:83],v[68:71],v[128:131]\n"
RD"a[96:99],v227 offset:24576\n"
PK"v240,v240,v241\n"
PK"v241,v242,v243\n"
MF"v[160:163],a[80:83],v[72:75],v[160:163]\n"
"buffer_store_dwordx2 v[240:241],v239,s[8:11],s93 offen sc1\n"
MF"v[192:195],a[80:83],v[76:79],v[192:195]\n"
WT"lgkmcnt(2)\n"
EX"v33,v33\n"
PK"v26,v28,v29\n"
MF"v[100:103],a[84:87],v[64:67],v[100:103]\n"
EX"v34,v34\n"
MF"v[132:135],a[84:87],v[68:71],v[132:135]\n"
RD"a[100:103],v227 offset:25600\n"
PK"v27,v30,v31\n"
MF"v[164:167],a[84:87],v[72:75],v[164:167]\n"
EX"v35,v35\n"
MF"v[196:199],a[84:87],v[76:79],v[196:199]\n"
BL"v225,s[8:11],s43 offen offset:3072 sc1 lds\n"
MF"v[104:107],a[88:91],v[64:67],v[104:107]\n"
EX"v40,v40\n"
MF"v[136:139],a[88:91],v[68:71],v[136:139]\n"
RD"a[104:107],v227 offset:26624\n"
MF"v[168:171],a[88:91],v[72:75],v[168:171]\n"
EX"v41,v41\n"
MF"v[200:203],a[88:91],v[76:79],v[200:203]\n"
WT"lgkmcnt(2)\n"
SA"m0,s40,16384\n"
MF"v[108:111],a[92:95],v[64:67],v[108:111]\n"
EX"v42,v42\n"
MF"v[140:143],a[92:95],v[68:71],v[140:143]\n"
RD"a[108:111],v227 offset:27648\n"
MF"v[172:175],a[92:95],v[72:75],v[172:175]\n"
EX"v43,v43\n"
MF"v[204:207],a[92:95],v[76:79],v[204:207]\n"
BL"v225,s[8:11],s44 offen offset:0 sc1 lds\n"
MF"a[132:135],a[128:131],v[64:67],a[132:135]\n"
SA"s40,s40,32768\n"
EX"v48,v48\n"
PK"v64,v32,v33\n"
MF"a[136:139],a[128:131],v[68:71],a[136:139]\n"
"s_cmp_eq_u32 s40,s46\n"
RD"a[112:115],v227 offset:28672\n"
PK"v65,v34,v35\n"
MF"a[140:143],a[128:131],v[72:75],a[140:143]\n"
"s_cselect_b32 s40,s47,s40\n"
EX"v49,v49\n"
MF"a[144:147],a[128:131],v[76:79],a[144:147]\n"
PK"v68,v40,v41\n"
PK"v69,v42,v43\n"
MF"v[80:83],a[96:99],v[0:3],v[80:83]\n"
EX"v50,v50\n"
MF"v[112:115],a[96:99],v[8:11],v[112:115]\n"
RD"a[116:119],v227 offset:29696\n"
PK"v72,v48,v49\n"
MF"v[144:147],a[96:99],v[16:19],v[144:147]\n"
EX"v51,v51\n"
MF"v[176:179],a[96:99],v[24:27],v[176:179]\n"
WT"lgkmcnt(2)\n"
BL"v225,s[8:11],s44 offen offset:1024 sc1 lds\n"
MF"v[84:87],a[100:103],v[0:3],v[84:87]\n"
EX"v36,v36\n"
MF"v[116:119],a[100:103],v[8:11],v[116:119]\n"
RD"a[120:123],v227 offset:30720\n"
PK"v73,v50,v51\n"
MF"v[148:151],a[100:103],v[16:19],v[148:151]\n"
EX"v37,v37\n"
MF"v[180:183],a[100:103],v[24:27],v[180:183]\n"
PK"v66,v36,v37\n"
MF"v[88:91],a[104:107],v[0:3],v[88:91]\n"
EX"v38,v38\n"
MF"v[120:123],a[104:107],v[8:11],v[120:123]\n"
RD"a[124:127],v227 offset:31744\n"
MF"v[152:155],a[104:107],v[16:19],v[152:155]\n"
EX"v39,v39\n"
MF"v[184:187],a[104:107],v[24:27],v[184:187]\n"
BL"v225,s[8:11],s44 offen offset:2048 sc1 lds\n"
MF"v[92:95],a[108:111],v[0:3],v[92:95]\n"
EX"v44,v44\n"
MF"v[124:127],a[108:111],v[8:11],v[124:127]\n"
PK"v67,v38,v39\n"
MF"v[156:159],a[108:111],v[16:19],v[156:159]\n"
EX"v45,v45\n"
MF"v[188:191],a[108:111],v[24:27],v[188:191]\n"
WT"lgkmcnt(1)\n"
PK"v70,v44,v45\n"
MF"v[96:99],a[112:115],v[0:3],v[96:99]\n"
EX"v46,v46\n"
MF"v[128:131],a[112:115],v[8:11],v[128:131]\n"
MF"v[160:163],a[112:115],v[16:19],v[160:163]\n"
EX"v47,v47\n"
MF"v[192:195],a[112:115],v[24:27],v[192:195]\n"
BL"v225,s[8:11],s44 offen offset:3072 sc1 lds\n"
MF"v[100:103],a[116:119],v[0:3],v[100:103]\n"
WT"lgkmcnt(0)\n"
"s_barrier\n"
MF"v[132:135],a[116:119],v[8:11],v[132:135]\n"
RD"a[64:67],v226 offset:0\n"
PK"v71,v46,v47\n"
MF"v[164:167],a[116:119],v[16:19],v[164:167]\n"
EX"v52,v52\n"
MF"v[196:199],a[116:119],v[24:27],v[196:199]\n"
RD"a[68:71],v226 offset:1024\n"
MF"v[104:107],a[120:123],v[0:3],v[104:107]\n"
EX"v53,v53\n"
MF"v[136:139],a[120:123],v[8:11],v[136:139]\n"
RD"a[72:75],v226 offset:2048\n"
PK"v74,v52,v53\n"
MF"v[168:171],a[120:123],v[16:19],v[168:171]\n"
EX"v54,v54\n"
MF"v[200:203],a[120:123],v[24:27],v[200:203]\n"
MF"v[108:111],a[124:127],v[0:3],v[108:111]\n"
WT"vmcnt(5)\n"
"buffer_store_dwordx2 v[232:233],v230,s[8:11],s94 offen sc1\n"
MF"v[140:143],a[124:127],v[8:11],v[140:143]\n"
EX"v55,v55\n"
MF"v[172:175],a[124:127],v[16:19],v[172:175]\n"
RD"a[76:79],v226 offset:3072\n"
PK"v75,v54,v55\n"
MF"v[204:207],a[124:127],v[24:27],v[204:207]\n"
RD"a[80:83],v226 offset:4096\n"
MF"a[132:135],a[128:131],v[0:3],a[132:135]\n"
MF"a[136:139],a[128:131],v[8:11],a[136:139]\n"
RD"a[84:87],v226 offset:5120\n"
MF"a[140:143],a[128:131],v[16:19],a[140:143]\n"
MF"a[144:147],a[128:131],v[24:27],a[144:147]\n"
"s_mov_b32 s57,0\n"
"s_lshl_b32 s56,s34,1\n"
"s_sub_u32 s56,s56,12\n"
"s_branch LP1_%=\n"
"LRS_%=:\n"
"LRT_%=:\n"
WT"lgkmcnt(5)\n"
MF"v[0:3],a[64:67],a[0:3],0\n"
"s_mov_b32 s12,s74\n"
EX"v56,v56\n"
MF"v[8:11],a[64:67],a[16:19],0\n"
"s_and_b32 s13,s75,0xffff\n"
"v_lshrrev_b32 v242,1,v224\n"
"v_and_b32 v242,56,v242\n"
MF"v[16:19],a[64:67],a[32:35],0\n"
"s_mov_b32 s14,5242880\n"
EX"v57,v57\n"
MF"v[24:27],a[64:67],a[48:51],0\n"
WT"lgkmcnt(4)\n"
PK"v76,v56,v57\n"
"s_lshl_b32 s23,s35,6\n"
MF"v[0:3],a[68:71],a[4:7],v[0:3]\n"
"s_mov_b32 s15,0x20000\n"
EX"v58,v58\n"
MF"v[8:11],a[68:71],a[20:23],v[8:11]\n"
SA"s23,s23,4352\n"
"v_add_u32 v243,s40,v252\n"
"v_mov_b32 v227,v226\n"
MF"v[16:19],a[68:71],a[36:39],v[16:19]\n"
SA"s19,s40,32768\n"
EX"v59,v59\n"
MF"v[24:27],a[68:71],a[52:55],v[24:27]\n"
WT"lgkmcnt(0)\n"
"buffer_load_dwordx2 v[244:245],v242,s[8:11],s23 offen sc1\n"
MF"v[0:3],a[72:75],a[8:11],v[0:3]\n"
"s_cmp_eq_u32 s19,s46\n"
EX"v60,v60\n"
MF"v[8:11],a[72:75],a[24:27],v[8:11]\n"
"s_cselect_b32 s19,s47,s19\n"
RD"a[88:91],v226 offset:6144\n"
PK"v77,v58,v59\n"
MF"v[16:19],a[72:75],a[40:43],v[16:19]\n"
"s_cmp_eq_u32 s20,0\n"
EX"v61,v61\n"
MF"v[24:27],a[72:75],a[56:59],v[24:27]\n"
"s_cselect_b32 s17,2130706432,s17\n"
PK"v78,v60,v61\n"
SA"m0,s19,0\n"
MF"v[0:3],a[76:79],a[12:15],v[0:3]\n"
BL"v240,s[12:15],s17 offen offset:0 lds\n"
MF"v[8:11],a[76:79],a[28:31],v[8:11]\n"
EX"v62,v62\n"
MF"v[16:19],a[76:79],a[44:47],v[16:19]\n"
RD"a[92:95],v226 offset:7168\n"
MF"v[24:27],a[76:79],a[60:63],v[24:27]\n"
EX"v63,v63\n"
MF"v[4:7],a[80:83],a[0:3],0\n"
BL"v241,s[12:15],s17 offen offset:1024 lds\n"
MF"v[12:15],a[80:83],a[16:19],0\n"
EX"v0,v0\n"
MF"v[20:23],a[80:83],a[32:35],0\n"
RD"a[96:99],v226 offset:8192\n"
PK"v79,v62,v63\n"
MF"v[28:31],a[80:83],a[48:51],0\n"
EX"v1,v1\n"
MF"v[4:7],a[84:87],a[4:7],v[4:7]\n"
BL"v251,s[12:15],s17 offen offset:2048 lds\n"
MF"v[12:15],a[84:87],a[20:23],v[12:15]\n"
SA"m0,s19,16384\n"
EX"v2,v2\n"
MF"v[20:23],a[84:87],a[36:39],v[20:23]\n"
SA"s17,s17,20480\n"
RD"a[100:103],v226 offset:9216\n"
PK"v0,v0,v1\n"
MF"v[28:31],a[84:87],a[52:55],v[28:31]\n"
WT"lgkmcnt(2)\n"
EX"v3,v3\n"
MF"v[4:7],a[88:91],a[8:11],v[4:7]\n"
BL"v240,s[12:15],s17 offen offset:0 lds\n"
MF"v[12:15],a[88:91],a[24:27],v[12:15]\n"
EX"v8,v8\n"
MF"v[20:23],a[88:91],a[40:43],v[20:23]\n"
RD"a[104:107],v226 offset:10240\n"
PK"v1,v2,v3\n"
MF"v[28:31],a[88:91],a[56:59],v[28:31]\n"
EX"v9,v9\n"
MF"v[4:7],a[92:95],a[12:15],v[4:7]\n"
WT"vmcnt(13)\n"
RT"a[148:149],v243 offset:0\n"
RT"a[150:151],v243 offset:16384\n"
MF"v[12:15],a[92:95],a[28:31],v[12:15]\n"
EX"v10,v10\n"
MF"v[20:23],a[92:95],a[44:47],v[20:23]\n"
RD"a[108:111],v226 offset:11264\n"
PK"v8,v8,v9\n"
MF"v[28:31],a[92:95],a[60:63],v[28:31]\n"
WT"lgkmcnt(4)\n"
EX"v11,v11\n"
MF"v[32:35],a[96:99],a[0:3],0\n"
BL"v241,s[12:15],s17 offen offset:1024 lds\n"
MF"v[40:43],a[96:99],a[16:19],0\n"
EX"v16,v16\n"
MF"v[48:51],a[96:99],a[32:35],0\n"
RD"a[112:115],v226 offset:12288\n"
PK"v9,v10,v11\n"
MF"v[56:59],a[96:99],a[48:51],0\n"
EX"v17,v17\n"
MF"v[32:35],a[100:103],a[4:7],v[32:35]\n"
RT"a[152:153],v243 offset:32\n"
RT"a[154:155],v243 offset:16416\n"
MF"v[40:43],a[100:103],a[20:23],v[40:43]\n"
EX"v18,v18\n"
MF"v[48:51],a[100:103],a[36:39],v[48:51]\n"
RD"a[116:119],v226 offset:13312\n"
PK"v16,v16,v17\n"
MF"v[56:59],a[100:103],a[52:55],v[56:59]\n"
WT"lgkmcnt(4)\n"
EX"v19,v19\n"
MF"v[32:35],a[104:107],a[8:11],v[32:35]\n"
BL"v251,s[12:15],s17 offen offset:2048 lds\n"
MF"v[40:43],a[104:107],a[24:27],v[40:43]\n"
EX"v24,v24\n"
MF"v[48:51],a[104:107],a[40:43],v[48:51]\n"
RD"a[120:123],v226 offset:14336\n"
PK"v17,v18,v19\n"
MF"v[56:59],a[104:107],a[56:59],v[56:59]\n"
EX"v25,v25\n"
MF"v[32:35],a[108:111],a[12:15],v[32:35]\n"
RT"a[156:157],v243 offset:64\n"
RT"a[158:159],v243 offset:16448\n"
MF"v[40:43],a[108:111],a[28:31],v[40:43]\n"
EX"v26,v26\n"
MF"v[48:51],a[108:111],a[44:47],v[48:51]\n"
RD"a[124:127],v226 offset:15360\n"
PK"v24,v24,v25\n"
MF"v[56:59],a[108:111],a[60:63],v[56:59]\n"
WT"lgkmcnt(4)\n"
EX"v27,v27\n"
MF"v[36:39],a[112:115],a[0:3],0\n"
EX"v4,v4\n"
MF"v[44:47],a[112:115],a[16:19],0\n"
RD"a[64:67],v227 offset:16384\n"
PK"v25,v26,v27\n"
MF"v[52:55],a[112:115],a[32:35],0\n"
EX"v5,v5\n"
MF"v[60:63],a[112:115],a[48:51],0\n"
RT"a[160:161],v243 offset:96\n"
RT"a[162:163],v243 offset:16480\n"
MF"v[36:39],a[116:119],a[4:7],v[36:39]\n"
EX"v6,v6\n"
MF"v[44:47],a[116:119],a[20:23],v[44:47]\n"
RD"a[68:71],v227 offset:17408\n"
PK"v2,v4,v5\n"
MF"v[52:55],a[116:119],a[36:39],v[52:55]\n"
EX"v7,v7\n"
MF"v[60:63],a[116:119],a[52:55],v[60:63]\n"
WT"lgkmcnt(4)\n"
"v_readlane_b32 s43,v228,s42\n"
PK"v3,v6,v7\n"
MF"v[36:39],a[120:123],a[8:11],v[36:39]\n"
EX"v12,v12\n"
MF"v[44:47],a[120:123],a[24:27],v[44:47]\n"
RD"a[72:75],v227 offset:18432\n"
MF"v[52:55],a[120:123],a[40:43],v[52:55]\n"
EX"v13,v13\n"
MF"v[60:63],a[120:123],a[56:59],v[60:63]\n"
"s_mov_b32 m0,s40\n"
RT"a[164:165],v243 offset:128\n"
RT"a[166:167],v243 offset:16512\n"
MF"v[36:39],a[124:127],a[12:15],v[36:39]\n"
SA"s17,s17,61440\n"
EX"v14,v14\n"
MF"v[44:47],a[124:127],a[28:31],v[44:47]\n"
RD"a[76:79],v227 offset:19456\n"
"v_readlane_b32 s44,v229,s42\n"
MF"v[52:55],a[124:127],a[44:47],v[52:55]\n"
EX"v15,v15\n"
MF"v[60:63],a[124:127],a[60:63],v[60:63]\n"
WT"lgkmcnt(4)\n"
PK"v10,v12,v13\n"
PK"v11,v14,v15\n"
MF"v[80:83],a[64:67],v[64:67],v[80:83]\n"
WT"lgkmcnt(1)\n"
BL"v225,s[8:11],s43 offen offset:0 sc1 lds\n"
MF"v[112:115],a[64:67],v[68:71],v[112:115]\n"
SA"s42,s42,1\n"
EX"v20,v20\n"
MF"v[144:147],a[64:67],v[72:75],v[144:147]\n"
SA"s41,s41,32768\n"
RD"a[80:83],v227 offset:20480\n"
"s_cmp_eq_u32 s41,163840\n"
MF"v[176:179],a[64:67],v[76:79],v[176:179]\n"
"s_cselect_b32 s41,0,s41\n"
EX"v21,v21\n"
MF"v[84:87],a[68:71],v[64:67],v[84:87]\n"
BS"a[148:151],v224,s[8:11],s18 offen offset:0 sc1\n"
MF"v[116:119],a[68:71],v[68:71],v[116:119]\n"
EX"v22,v22\n"
MF"v[148:151],a[68:71],v[72:75],v[148:151]\n"
RD"a[84:87],v227 offset:21504\n"
"v_add_u32 v226,s41,v224\n"
MF"v[180:183],a[68:71],v[76:79],v[180:183]\n"
EX"v23,v23\n"
MF"v[88:91],a[72:75],v[64:67],v[88:91]\n"
BL"v225,s[8:11],s43 offen offset:1024 sc1 lds\n"
MF"v[120:123],a[72:75],v[68:71],v[120:123]\n"
EX"v28,v28\n"
MF"v[152:155],a[72:75],v[72:75],v[152:155]\n"
RD"a[88:91],v227 offset:22528\n"
PK"v18,v20,v21\n"
MF"v[184:187],a[72:75],v[76:79],v[184:187]\n"
WT"lgkmcnt(2)\n"
EX"v29,v29\n"
MF"v[92:95],a[76:79],v[64:67],v[92:95]\n"
BS"a[152:155],v224,s[8:11],s18 offen offset:1024 sc1\n"
MF"v[124:127],a[76:79],v[68:71],v[124:127]\n"
EX"v30,v30\n"
MF"v[156:159],a[76:79],v[72:75],v[156:159]\n"
RD"a[92:95],v227 offset:23552\n"
PK"v19,v22,v23\n"
MF"v[188:191],a[76:79],v[76:79],v[188:191]\n"
EX"v31,v31\n"
MF"v[96:99],a[80:83],v[64:67],v[96:99]\n"
BL"v225,s[8:11],s43 offen offset:2048 sc1 lds\n"
MF"v[128:131],a[80:83],v[68:71],v[128:131]\n"
EX"v32,v32\n"
MF"v[160:163],a[80:83],v[72:75],v[160:163]\n"
RD"a[96:99],v227 offset:24576\n"
PK"v26,v28,v29\n"
MF"v[192:195],a[80:83],v[76:79],v[192:195]\n"
WT"lgkmcnt(2)\n"
EX"v33,v33\n"
MF"v[100:103],a[84:87],v[64:67],v[100:103]\n"
BS"a[156:159],v224,s[8:11],s18 offen offset:2048 sc1\n"
MF"v[132:135],a[84:87],v[68:71],v[132:135]\n"
EX"v34,v34\n"
MF"v[164:167],a[84:87],v[72:75],v[164:167]\n"
RD"a[100:103],v227 offset:25600\n"
PK"v27,v30,v31\n"
MF"v[196:199],a[84:87],v[76:79],v[196:199]\n"
EX"v35,v35\n"
MF"v[104:107],a[88:91],v[64:67],v[104:107]\n"
BL"v225,s[8:11],s43 offen offset:3072 sc1 lds\n"
MF"v[136:139],a[88:91],v[68:71],v[136:139]\n"
EX"v40,v40\n"
MF"v[168:171],a[88:91],v[72:75],v[168:171]\n"
RD"a[104:107],v227 offset:26624\n"
MF"v[200:203],a[88:91],v[76:79],v[200:203]\n"
WT"lgkmcnt(2)\n"
EX"v41,v41\n"
MF"v[108:111],a[92:95],v[64:67],v[108:111]\n"
SA"m0,s40,16384\n"
BS"a[160:163],v224,s[8:11],s18 offen offset:3072 sc1\n"
MF"v[140:143],a[92:95],v[68:71],v[140:143]\n"
EX"v42,v42\n"
MF"v[172:175],a[92:95],v[72:75],v[172:175]\n"
SA"s18,s18,4096\n"
RD"a[108:111],v227 offset:27648\n"
MF"v[204:207],a[92:95],v[76:79],v[204:207]\n"
EX"v43,v43\n"
MF"a[132:135],a[128:131],v[64:67],a[132:135]\n"
SA"s40,s40,32768\n"
BL"v225,s[8:11],s44 offen offset:0 sc1 lds\n"
PK"v64,v32,v33\n"
MF"a[136:139],a[128:131],v[68:71],a[136:139]\n"
"s_cmp_eq_u32 s40,s46\n"
EX"v48,v48\n"
PK"v65,v34,v35\n"
MF"a[140:143],a[128:131],v[72:75],a[140:143]\n"
"s_cselect_b32 s40,s47,s40\n"
RD"a[112:115],v227 offset:28672\n"
PK"v68,v40,v41\n"
MF"a[144:147],a[128:131],v[76:79],a[144:147]\n"
EX"v49,v49\n"
MF"v[80:83],a[96:99],v[0:3],v[80:83]\n"
"s_lshr_b32 s21,s16,1\n"
BS"a[164:167],v224,s[8:11],s18 offen offset:0 sc1\n"
MF"v[112:115],a[96:99],v[8:11],v[112:115]\n"
"s_sub_u32 s21,s21,s33\n"
EX"v50,v50\n"
MF"v[144:147],a[96:99],v[16:19],v[144:147]\n"
"s_and_b32 s21,s21,3\n"
RD"a[116:119],v227 offset:29696\n"
PK"v69,v42,v43\n"
MF"v[176:179],a[96:99],v[24:27],v[176:179]\n"
WT"lgkmcnt(2)\n"
EX"v51,v51\n"
MF"v[84:87],a[100:103],v[0:3],v[84:87]\n"
"s_lshl_b32 s21,s21,1\n"
BL"v225,s[8:11],s44 offen offset:1024 sc1 lds\n"
MF"v[116:119],a[100:103],v[8:11],v[116:119]\n"
"s_and_b32 s22,s16,1\n"
EX"v36,v36\n"
MF"v[148:151],a[100:103],v[16:19],v[148:151]\n"
SA"s21,s21,s22\n"
RD"a[120:123],v227 offset:30720\n"
PK"v72,v48,v49\n"
MF"v[180:183],a[100:103],v[24:27],v[180:183]\n"
"s_lshl_b32 s21,s21,2\n"
EX"v37,v37\n"
MF"v[88:91],a[104:107],v[0:3],v[88:91]\n"
SA"s21,s21,s30\n"
EX"v38,v38\n"
MF"v[120:123],a[104:107],v[8:11],v[120:123]\n"
"s_mul_i32 s21,s21,5120\n"
RD"a[124:127],v227 offset:31744\n"
PK"v73,v50,v51\n"
MF"v[152:155],a[104:107],v[16:19],v[152:155]\n"
"s_mul_i32 s22,s35,163840\n"
EX"v39,v39\n"
MF"v[184:187],a[104:107],v[24:27],v[184:187]\n"
SA"s21,s21,s22\n"
BL"v225,s[8:11],s44 offen offset:2048 sc1 lds\n"
MF"v[92:95],a[108:111],v[0:3],v[92:95]\n"
SA"s18,s21,3428352\n"
EX"v44,v44\n"
MF"v[124:127],a[108:111],v[8:11],v[124:127]\n"
SA"s16,s16,1\n"
PK"v66,v36,v37\n"
PK"v67,v38,v39\n"
MF"v[156:159],a[108:111],v[16:19],v[156:159]\n"
EX"v45,v45\n"
MF"v[188:191],a[108:111],v[24:27],v[188:191]\n"
WT"lgkmcnt(1)\n"
PK"v70,v44,v45\n"
MF"v[96:99],a[112:115],v[0:3],v[96:99]\n"
EX"v46,v46\n"
MF"v[128:131],a[112:115],v[8:11],v[128:131]\n"
MF"v[160:163],a[112:115],v[16:19],v[160:163]\n"
EX"v47,v47\n"
MF"v[192:195],a[112:115],v[24:27],v[192:195]\n"
BL"v225,s[8:11],s44 offen offset:3072 sc1 lds\n"
MF"v[100:103],a[116:119],v[0:3],v[100:103]\n"
WT"lgkmcnt(0)\n"
"s_barrier\n"
MF"v[132:135],a[116:119],v[8:11],v[132:135]\n"
RD"a[64:67],v226 offset:0\n"
PK"v71,v46,v47\n"
MF"v[164:167],a[116:119],v[16:19],v[164:167]\n"
EX"v52,v52\n"
MF"v[196:199],a[116:119],v[24:27],v[196:199]\n"
RD"a[68:71],v226 offset:1024\n"
MF"v[104:107],a[120:123],v[0:3],v[104:107]\n"
EX"v53,v53\n"
MF"v[136:139],a[120:123],v[8:11],v[136:139]\n"
RD"a[72:75],v226 offset:2048\n"
PK"v74,v52,v53\n"
MF"v[168:171],a[120:123],v[16:19],v[168:171]\n"
EX"v54,v54\n"
MF"v[200:203],a[120:123],v[24:27],v[200:203]\n"
MF"v[108:111],a[124:127],v[0:3],v[108:111]\n"
EX"v55,v55\n"
MF"v[140:143],a[124:127],v[8:11],v[140:143]\n"
RD"a[76:79],v226 offset:3072\n"
PK"v75,v54,v55\n"
MF"v[172:175],a[124:127],v[16:19],v[172:175]\n"
MF"v[204:207],a[124:127],v[24:27],v[204:207]\n"
RD"a[80:83],v226 offset:4096\n"
MF"a[132:135],a[128:131],v[0:3],a[132:135]\n"
MF"a[136:139],a[128:131],v[8:11],a[136:139]\n"
RD"a[84:87],v226 offset:5120\n"
MF"a[140:143],a[128:131],v[16:19],a[140:143]\n"
MF"a[144:147],a[128:131],v[24:27],a[144:147]\n"
"s_cmp_eq_u32 s20,7\n"
"s_cbranch_scc0 L1105_%=\n"
WT"vmcnt(8)\n"
"v_cmp_eq_u64 vcc,s[28:29],v[244:245]\n"
"s_cmp_eq_u64 vcc,exec\n"
"s_cbranch_scc0 L1106_%=\n"
"L1107_%=:\n"
"L1105_%=:\n"
"s_sub_u32 s20,s20,1\n"
"s_cbranch_scc0 LRT_%=\n"
"s_lshl_b32 s21,s35,8\n"
SA"s21,s21,81264640\n"
SA"s21,s21,s45\n"
WT"vmcnt(3)\n"
"s_barrier\n"
"buffer_store_dwordx2 v[232:233],v230,s[8:11],s21 offen sc1\n"
"s_branch LDA_%=\n"
"LEND_%=:\n"
      : : [ka] "s"(__builtin_amdgcn_kernarg_segment_ptr()), [b] "s"(blockIdx.x), [t] "v"(threadIdx.x)
      : "v1", "v2", "v3", "v4", "v5", "v6", "v7", "v8", "v9", "v10", "v11", "v12", "v13", "v14", "v15", "v16", "v17", "v18", "v19", "v20", "v21", "v22", "v23", "v24", "v25", "v26", "v27", "v28", "v29", "v30", "v31", "v32", "v33", "v34", "v35", "v36", "v37", "v38", "v39", "v40", "v41", "v42", "v43", "v44", "v45", "v46", "v47", "v48", "v49", "v50", "v51", "v52", "v53", "v54", "v55", "v56", "v57", "v58", "v59", "v60", "v61", "v62", "v63", "v64", "v65", "v66", "v67", "v68", "v69", "v70", "v71", "v72", "v73", "v74", "v75", "v76", "v77", "v78", "v79", "v80", "v81", "v82", "v83", "v84", "v85", "v86", "v87", "v88", "v89", "v90", "v91", "v92", "v93", "v94", "v95", "v96", "v97", "v98", "v99", "v100", "v101", "v102", "v103", "v104", "v105", "v106", "v107", "v108", "v109", "v110", "v111", "v112", "v113", "v114", "v115", "v116", "v117", "v118", "v119", "v120", "v121", "v122", "v123", "v124", "v125", "v126", "v127", "v128", "v129", "v130", "v131", "v132", "v133", "v134", "v135", "v136", "v137", "v138", "v139", "v140", "v141", "v142", "v143", "v144", "v145", "v146", "v147", "v148", "v149", "v150", "v151", "v152", "v153", "v154", "v155", "v156", "v157", "v158", "v159", "v160", "v161", "v162", "v163", "v164", "v165", "v166", "v167", "v168", "v169", "v170", "v171", "v172", "v173", "v174", "v175", "v176", "v177", "v178", "v179", "v180", "v181", "v182", "v183", "v184", "v185", "v186", "v187", "v188", "v189", "v190", "v191", "v192", "v193", "v194", "v195", "v196", "v197", "v198", "v199", "v200", "v201", "v202", "v203", "v204", "v205", "v206", "v207", "v208", "v209", "v210", "v211", "v212", "v213", "v214", "v215", "v216", "v217", "v218", "v219", "v220", "v221", "v222", "v223", "v224", "v225", "v226", "v227", "v228", "v229", "v230", "v231", "v232", "v233", "v234", "v235", "v236", "v237", "v238", "v239", "v240", "v241", "v242", "v243", "v244", "v245", "v246", "v247", "v248", "v249", "v250", "v251", "v252", "v253", "v254", "v255", "a0", "a1", "a2", "a3", "a4", "a5", "a6", "a7", "a8", "a9", "a10", "a11", "a12", "a13", "a14", "a15", "a16", "a17", "a18", "a19", "a20", "a21", "a22", "a23", "a24", "a25", "a26", "a27", "a28", "a29", "a30", "a31", "a32", "a33", "a34", "a35", "a36", "a37", "a38", "a39", "a40", "a41", "a42", "a43", "a44", "a45", "a46", "a47", "a48", "a49", "a50", "a51", "a52", "a53", "a54", "a55", "a56", "a57", "a58", "a59", "a60", "a61", "a62", "a63", "a64", "a65", "a66", "a67", "a68", "a69", "a70", "a71", "a72", "a73", "a74", "a75", "a76", "a77", "a78", "a79", "a80", "a81", "a82", "a83", "a84", "a85", "a86", "a87", "a88", "a89", "a90", "a91", "a92", "a93", "a94", "a95", "a96", "a97", "a98", "a99", "a100", "a101", "a102", "a103", "a104", "a105", "a106", "a107", "a108", "a109", "a110", "a111", "a112", "a113", "a114", "a115", "a116", "a117", "a118", "a119", "a120", "a121", "a122", "a123", "a124", "a125", "a126", "a127", "a128", "a129", "a130", "a131", "a132", "a133", "a134", "a135", "a136", "a137", "a138", "a139", "a140", "a141", "a142", "a143", "a144", "a145", "a146", "a147", "a148", "a149", "a150", "a151", "a152", "a153", "a154", "a155", "a156", "a157", "a158", "a159", "a160", "a161", "a162", "a163", "a164", "a165", "a166", "a167", "a168", "a169", "a170", "a171", "a172", "a173", "a174", "a175", "a176", "a177", "a178", "a179", "a180", "a181", "a182", "a183", "a184", "a185", "a186", "a187", "a188", "a189", "a190", "a191", "a192", "a193", "a194", "a195", "a196", "a197", "a198", "a199", "a200", "a201", "a202", "a203", "a204", "a205", "a206", "a207", "a208", "a209", "a210", "a211", "a212", "a213", "a214", "a215", "a216", "a217", "a218", "a219", "a220", "a221", "a222", "a223", "a224", "a225", "a226", "a227", "a228", "a229", "a230", "a231", "a232", "a233", "a234", "a235", "a236", "a237", "a238", "a239", "a240", "a241", "a242", "a243", "a244", "a245", "a246", "a247", "a248", "a249", "a250", "a251", "a252", "a253", "a254", "a255", "s8", "s9", "s10", "s11", "s12", "s13", "s14", "s15", "s16", "s17", "s18", "s19", "s20", "s21", "s22", "s23", "s24", "s25", "s26", "s27", "s28", "s29", "s30", "s31", "s32", "s33", "s34", "s35", "s36", "s37", "s38", "s39", "s40", "s41", "s42", "s43", "s44", "s45", "s46", "s47", "s48", "s49", "s50", "s51", "s52", "s53", "s54", "s55", "s56", "s57", "s58", "s59", "s60", "s61", "s62", "s63", "s64", "s65", "s66", "s67", "s68", "s69", "s70", "s71", "s72", "s73", "s74", "s75", "s76", "s77", "s78", "s79", "s80", "s81", "s82", "s83", "s84", "s85", "s86", "s87", "s88", "s89", "s90", "s91", "s92", "s93", "s94", "s95", "s96", "s97", "s98", "s99", "scc", "vcc", "memory");
}

extern "C" void kernel_launch(void* const* d_in, const int* in_sizes, int n_in, void* d_out, int out_size, void* d_ws, size_t ws_size, hipStream_t stream) {
  (void)hipFuncSetAttribute((const void*)mega_kernel, hipFuncAttributeMaxDynamicSharedMemorySize, 163840);
  mega_kernel<<<256, 256, 163840, stream>>>((const bf16*)d_in[0], (const bf16*)d_in[1], (const bf16*)d_in[2], (const bf16*)d_in[3], (const bf16*)d_in[4], (const bf16*)d_in[5],
      (const bf16*)d_in[7], (const bf16*)d_in[8], (const bf16*)d_in[9], (const bf16*)d_in[10], (char*)d_ws + (size_t)0 * 1048576, (bf16*)d_out);
}
```

```cpp
#include <hip/hip_runtime.h>
#include <hip/hip_bf16.h>
typedef __hip_bfloat16 bf16;
#define MF "v_mfma_f32_16x16x32_bf16 "
#define RD "ds_read_b128 "
#define RT "ds_read_b64_tr_b16 "
#define BL "buffer_load_dwordx4 "
#define BS "buffer_store_dwordx4 "
#define PK "v_cvt_pk_bf16_f32 "
#define EX "v_exp_f32 "
#define WT "s_waitcnt "
#define MU "v_mul_f32 "
#define SA "s_add_u32 "

__global__ __launch_bounds__(256, 1) void mega_kernel(const bf16* x, const bf16* ckv, const bf16* wq, const bf16* wuk, const bf16* wuv, const bf16* wo, const bf16* w_r,
                                                      const bf16* wg, const bf16* wu, const bf16* wd, char* ws, bf16* out) {
  asm volatile(
"s_load_dwordx16 s[64:79],%[ka],0\n"
"s_load_dwordx8 s[80:87],%[ka],64\n"
"s_mov_b32 s31,%[b]\n"
"v_lshrrev_b32 v1,6,%[t]\n"
"v_and_b32 v2,63,%[t]\n"
"v_readfirstlane_b32 s30,v1\n"
"v_lshlrev_b32 v224,4,v2\n"
"s_and_b32 s32,s31,7\n"
"s_lshr_b32 s33,s31,6\n"
"s_bfe_u32 s34,s31,0x30003\n"
"s_lshl_b32 s35,s32,2\n"
SA"s35,s35,s33\n"
SA"s46,s34,s32\n"
"s_and_b32 s46,s46,6\n"
"s_lshl_b32 s47,s46,19\n"
"s_lshl_b32 s46,s46,7\n"
"s_lshl_b32 s36,s30,12\n"
"s_mul_i32 s37,s30,24576\n"
SA"s37,s37,65536\n"
"v_lshrrev_b32 v3,3,v2\n"
"v_lshrrev_b32 v4,4,v2\n"
"v_and_b32 v5,7,v2\n"
"v_xor_b32 v6,v5,v4\n"
"v_lshlrev_b32 v6,4,v6\n"
"v_lshl_add_u32 v6,v3,10,v6\n"
"s_lshl_b32 s50,s30,15\n"
"v_add_u32 v234,s50,v6\n"
"v_xor_b32 v235,64,v234\n"
"v_add_u32 v235,7168,v235\n"
"v_add_u32 v236,14336,v234\n"
"v_add_u32 v237,14336,v235\n"
"v_and_b32 v7,1,v4\n"
"v_lshlrev_b32 v7,1,v7\n"
"v_xor_b32 v7,v7,v5\n"
"v_lshlrev_b32 v7,4,v7\n"
"v_lshl_add_u32 v238,v3,13,v7\n"
"v_xor_b32 v239,64,v238\n"
"v_add_u32 v239,64512,v239\n"
"v_add_u32 v240,129024,v238\n"
"v_add_u32 v241,129024,v239\n"
"v_and_b32 v8,31,v2\n"
"v_lshlrev_b32 v8,3,v8\n"
"v_lshrrev_b32 v9,5,v2\n"
"s_lshl_b32 s50,s31,2\n"
SA"s50,s50,s30\n"
"s_mul_i32 s50,s50,2560\n"
"v_mul_u32_u24 v9,1280,v9\n"
"v_add3_u32 v9,v9,v8,s50\n"
"s_lshl_b32 s50,s34,17\n"
"s_lshl_b32 s51,s35,8\n"
"s_and_b32 s52,s30,1\n"
"s_lshl_b32 s52,s52,7\n"
SA"s52,s52,s51\n"
"s_mov_b32 s14,0x120000\n"
"s_mov_b32 s15,0x20000\n"
"s_sub_u32 s18,4194304,s52\n"
"s_mov_b32 s19,0x20000\n"
"s_getpc_b64 s[24:25]\n"
"LPC_%=:\n"
"s_and_b32 s25,s25,0xffff\n"
"s_mov_b32 s26,LEND_%=-LPC_%=\n"
"s_mov_b32 s27,0x20000\n"
"s_lshr_b32 s49,s31,3\n"
"s_lshl_b32 s49,s49,2\n"
SA"s49,s49,s30\n"
"s_lshl_b32 s49,s49,7\n"
"v_lshlrev_b32 v10,14,v2\n"
"buffer_load_dword v255,v10,s[24:27],s49 offen\n"
WT"lgkmcnt(0)\n"
"s_load_dwordx2 s[28:29],s[84:85],0\n"
SA"s12,s66,s50\n"
"s_addc_u32 s13,s67,0\n"
"s_and_b32 s13,s13,0xffff\n"
"s_cmp_lt_u32 s30,2\n"
"s_cselect_b64 s[16:17],s[70:71],s[72:73]\n"
SA"s16,s16,s52\n"
"s_addc_u32 s17,s17,0\n"
"s_and_b32 s17,s17,0xffff\n"
"global_load_dwordx2 v[128:129],v9,s[64:65] offset:0\n"
"global_load_dwordx2 v[130:131],v9,s[64:65] offset:256\n"
"global_load_dwordx2 v[132:133],v9,s[64:65] offset:512\n"
"global_load_dwordx2 v[134:135],v9,s[64:65] offset:768\n"
"global_load_dwordx2 v[136:137],v9,s[64:65] offset:1024\n"
"global_load_dwordx2 v[138:139],v8,s[76:77] offset:0\n"
"global_load_dwordx2 v[140:141],v8,s[76:77] offset:256\n"
"global_load_dwordx2 v[142:143],v8,s[76:77] offset:512\n"
"global_load_dwordx2 v[144:145],v8,s[76:77] offset:768\n"
"global_load_dwordx2 v[146:147],v8,s[76:77] offset:1024\n"
SA"m0,s37,0\n"
SA"s48,s47,0\n"
"s_and_b32 s48,s48,4194303\n"
BL"v238,s[16:19],s48 offen offset:0 lds\n"
BL"v239,s[16:19],s48 offen offset:1024 lds\n"
BL"v240,s[16:19],s48 offen offset:2048 lds\n"
BL"v241,s[16:19],s48 offen offset:3072 lds\n"
SA"m0,s37,4096\n"
SA"s48,s47,262144\n"
"s_and_b32 s48,s48,4194303\n"
BL"v238,s[16:19],s48 offen offset:0 lds\n"
BL"v239,s[16:19],s48 offen offset:1024 lds\n"
BL"v240,s[16:19],s48 offen offset:2048 lds\n"
BL"v241,s[16:19],s48 offen offset:3072 lds\n"
"v_lshlrev_b32 v253,7,%[t]\n"
"s_mov_b32 s8,s84\n"
"s_and_b32 s9,s85,0xffff\n"
"s_mov_b32 s10,81272832\n"
"s_mov_b32 s11,0x20000\n"
SA"s20,s68,s51\n"
"s_addc_u32 s21,s69,0\n"
"s_and_b32 s21,s21,0xffff\n"
"s_sub_u32 s22,5242880,s51\n"
"s_mov_b32 s23,0x20000\n"
"v_and_b32 v11,15,v2\n"
"v_lshrrev_b32 v12,1,v11\n"
"v_xor_b32 v12,v12,v4\n"
"v_lshlrev_b32 v12,4,v12\n"
"v_lshl_add_u32 v242,v11,7,v12\n"
"v_xor_b32 v243,64,v242\n"
"v_lshrrev_b32 v12,2,v11\n"
"v_lshl_add_u32 v12,v4,3,v12\n"
"v_lshlrev_b32 v12,7,v12\n"
"v_and_b32 v13,3,v2\n"
"v_lshl_add_u32 v12,v13,3,v12\n"
"v_and_b32 v13,3,v3\n"
"v_lshl_add_u32 v12,v13,5,v12\n"
"v_add_u32 v246,s37,v12\n"
"v_xor_b32 v247,32,v246\n"
"v_xor_b32 v248,64,v246\n"
"v_xor_b32 v249,96,v246\n"
"v_mov_b32 v13,2130706432\n"
"v_cmp_eq_u32 vcc,0,v2\n"
"s_cmp_eq_u32 s30,0\n"
"s_cselect_b32 s45,0,2130706432\n"
"v_cndmask_b32_e64 v230,v13,0,vcc\n"
"s_lshl_b32 s39,s30,8\n"
SA"s39,s39,s46\n"
SA"s98,s39,128\n"
"s_and_b32 s39,s39,1023\n"
"s_and_b32 s98,s98,1023\n"
"s_lshl_b32 s50,s30,15\n"
"s_sub_u32 s50,1048576,s50\n"
SA"s39,s39,s50\n"
SA"s98,s98,s50\n"
"s_mov_b32 s55,0x3e0293ee\n"
"s_cmp_lt_u32 s30,2\n"
"s_cbranch_scc0 LV_%=\n"
WT"vmcnt(17)\n"
"v_lshlrev_b32 v148,16,v128\n"
"v_and_b32 v149,0xffff0000,v128\n"
"v_lshlrev_b32 v150,16,v129\n"
"v_and_b32 v151,0xffff0000,v129\n"
WT"vmcnt(16)\n"
"v_lshlrev_b32 v152,16,v130\n"
"v_and_b32 v153,0xffff0000,v130\n"
"v_lshlrev_b32 v154,16,v131\n"
"v_and_b32 v155,0xffff0000,v131\n"
WT"vmcnt(15)\n"
"v_lshlrev_b32 v156,16,v132\n"
"v_and_b32 v157,0xffff0000,v132\n"
"v_lshlrev_b32 v158,16,v133\n"
"v_and_b32 v159,0xffff0000,v133\n"
WT"vmcnt(14)\n"
"v_lshlrev_b32 v160,16,v134\n"
"v_and_b32 v161,0xffff0000,v134\n"
"v_lshlrev_b32 v162,16,v135\n"
"v_and_b32 v163,0xffff0000,v135\n"
WT"vmcnt(13)\n"
"v_lshlrev_b32 v164,16,v136\n"
"v_and_b32 v165,0xffff0000,v136\n"
"v_lshlrev_b32 v166,16,v137\n"
"v_and_b32 v167,0xffff0000,v137\n"
"v_add_f32 v188,v148,v149\n"
"v_add_f32 v189,v150,v151\n"
"v_add_f32 v188,v188,v152\n"
"v_add_f32 v189,v189,v153\n"
"v_add_f32 v188,v188,v154\n"
"v_add_f32 v189,v189,v155\n"
"v_add_f32 v188,v188,v156\n"
"v_add_f32 v189,v189,v157\n"
"v_add_f32 v188,v188,v158\n"
"v_add_f32 v189,v189,v159\n"
"v_add_f32 v188,v188,v160\n"
"v_add_f32 v189,v189,v161\n"
"v_add_f32 v188,v188,v162\n"
"v_add_f32 v189,v189,v163\n"
"v_add_f32 v188,v188,v164\n"
"v_add_f32 v189,v189,v165\n"
"v_add_f32 v188,v188,v166\n"
"v_add_f32 v189,v189,v167\n"
"v_add_f32 v188,v188,v189\n"
"s_nop 1\n"
"v_add_f32_dpp v188,v188,v188 quad_perm:[1,0,3,2] row_mask:0xf bank_mask:0xf\n"
WT"vmcnt(12)\n"
"v_lshlrev_b32 v168,16,v138\n"
"v_and_b32 v169,0xffff0000,v138\n"
"v_add_f32_dpp v188,v188,v188 quad_perm:[2,3,0,1] row_mask:0xf bank_mask:0xf\n"
"v_lshlrev_b32 v170,16,v139\n"
"v_and_b32 v171,0xffff0000,v139\n"
"v_add_f32_dpp v188,v188,v188 row_half_mirror row_mask:0xf bank_mask:0xf\n"
WT"vmcnt(11)\n"
"v_lshlrev_b32 v172,16,v140\n"
SA"m0,s36,0\n"
SA"s48,s46,0\n"
"s_and_b32 s48,s48,1023\n"
BL"v234,s[12:15],s48 offen offset:0 lds\n"
BL"v235,s[12:15],s48 offen offset:1024 lds\n"
BL"v236,s[12:15],s48 offen offset:2048 lds\n"
BL"v237,s[12:15],s48 offen offset:3072 lds\n"
"v_and_b32 v173,0xffff0000,v140\n"
"v_add_f32_dpp v188,v188,v188 row_mirror row_mask:0xf bank_mask:0xf\n"
"v_lshlrev_b32 v174,16,v141\n"
"v_and_b32 v175,0xffff0000,v141\n"
"v_mov_b32 v189,v188\n"
WT"vmcnt(14)\n"
"v_lshlrev_b32 v176,16,v142\n"
"v_and_b32 v177,0xffff0000,v142\n"
"v_permlane16_swap_b32 v189,v188\n"
"v_lshlrev_b32 v178,16,v143\n"
"v_and_b32 v179,0xffff0000,v143\n"
"v_add_f32 v188,v188,v189\n"
WT"vmcnt(13)\n"
"v_lshlrev_b32 v180,16,v144\n"
"v_and_b32 v181,0xffff0000,v144\n"
MU"v191,0x3acccccd,v188\n"
"v_lshlrev_b32 v182,16,v145\n"
"v_and_b32 v183,0xffff0000,v145\n"
PK"v128,v191,v191\n"
WT"vmcnt(12)\n"
"v_lshlrev_b32 v184,16,v146\n"
"v_and_b32 v185,0xffff0000,v146\n"
"v_lshlrev_b32 v128,16,v128\n"
"v_lshlrev_b32 v186,16,v147\n"
"v_and_b32 v187,0xffff0000,v147\n"
"v_sub_f32 v148,v148,v128\n"
"v_sub_f32 v149,v149,v128\n"
"v_sub_f32 v150,v150,v128\n"
"v_sub_f32 v151,v151,v128\n"
"v_sub_f32 v152,v152,v128\n"
"v_sub_f32 v153,v153,v128\n"
"v_sub_f32 v154,v154,v128\n"
"v_sub_f32 v155,v155,v128\n"
"v_sub_f32 v156,v156,v128\n"
"v_sub_f32 v157,v157,v128\n"
"v_sub_f32 v158,v158,v128\n"
"v_sub_f32 v159,v159,v128\n"
"v_sub_f32 v160,v160,v128\n"
"v_sub_f32 v161,v161,v128\n"
"v_sub_f32 v162,v162,v128\n"
"v_sub_f32 v163,v163,v128\n"
"v_sub_f32 v164,v164,v128\n"
"v_sub_f32 v165,v165,v128\n"
"v_sub_f32 v166,v166,v128\n"
"v_sub_f32 v167,v167,v128\n"
MU"v188,v148,v148\n"
MU"v189,v149,v149\n"
"v_fmac_f32 v188,v150,v150\n"
"v_fmac_f32 v189,v151,v151\n"
"v_fmac_f32 v188,v152,v152\n"
SA"m0,s37,8192\n"
SA"s48,s47,524288\n"
"s_and_b32 s48,s48,4194303\n"
BL"v238,s[16:19],s48 offen offset:0 lds\n"
BL"v239,s[16:19],s48 offen offset:1024 lds\n"
BL"v240,s[16:19],s48 offen offset:2048 lds\n"
BL"v241,s[16:19],s48 offen offset:3072 lds\n"
"v_fmac_f32 v189,v153,v153\n"
"v_fmac_f32 v188,v154,v154\n"
"v_fmac_f32 v189,v155,v155\n"
"v_fmac_f32 v188,v156,v156\n"
"v_fmac_f32 v189,v157,v157\n"
"v_fmac_f32 v188,v158,v158\n"
"v_fmac_f32 v189,v159,v159\n"
"v_fmac_f32 v188,v160,v160\n"
"v_fmac_f32 v189,v161,v161\n"
"v_fmac_f32 v188,v162,v162\n"
"v_fmac_f32 v189,v163,v163\n"
"v_fmac_f32 v188,v164,v164\n"
"v_fmac_f32 v189,v165,v165\n"
"v_fmac_f32 v188,v166,v166\n"
"v_fmac_f32 v189,v167,v167\n"
"v_add_f32 v188,v188,v189\n"
PK"v149,v148,v149\n"
"v_lshlrev_b32 v148,16,v149\n"
"v_add_f32_dpp v188,v188,v188 quad_perm:[1,0,3,2] row_mask:0xf bank_mask:0xf\n"
"v_and_b32 v149,0xffff0000,v149\n"
PK"v151,v150,v151\n"
"v_add_f32_dpp v188,v188,v188 quad_perm:[2,3,0,1] row_mask:0xf bank_mask:0xf\n"
"v_lshlrev_b32 v150,16,v151\n"
"v_and_b32 v151,0xffff0000,v151\n"
"v_add_f32_dpp v188,v188,v188 row_half_mirror row_mask:0xf bank_mask:0xf\n"
PK"v153,v152,v153\n"
"v_lshlrev_b32 v152,16,v153\n"
"v_add_f32_dpp v188,v188,v188 row_mirror row_mask:0xf bank_mask:0xf\n"
"v_and_b32 v153,0xffff0000,v153\n"
PK"v155,v154,v155\n"
"v_mov_b32 v189,v188\n"
"v_lshlrev_b32 v154,16,v155\n"
"v_and_b32 v155,0xffff0000,v155\n"
"v_permlane16_swap_b32 v189,v188\n"
PK"v157,v156,v157\n"
"v_lshlrev_b32 v156,16,v157\n"
"v_add_f32 v188,v188,v189\n"
"v_and_b32 v157,0xffff0000,v157\n"
PK"v159,v158,v159\n"
"v_sub_f32 v191,v191,v128\n"
"v_lshlrev_b32 v158,16,v159\n"
"v_and_b32 v159,0xffff0000,v159\n"
MU"v188,0x3acccccd,v188\n"
PK"v161,v160,v161\n"
"v_lshlrev_b32 v160,16,v161\n"
"v_fma_f32 v188,-v191,v191,v188\n"
"v_and_b32 v161,0xffff0000,v161\n"
PK"v163,v162,v163\n"
SA"m0,s37,12288\n"
SA"s48,s47,786432\n"
"s_and_b32 s48,s48,4194303\n"
BL"v238,s[16:19],s48 offen offset:0 lds\n"
BL"v239,s[16:19],s48 offen offset:1024 lds\n"
BL"v240,s[16:19],s48 offen offset:2048 lds\n"
BL"v241,s[16:19],s48 offen offset:3072 lds\n"
"v_max_f32 v188,0,v188\n"
"v_lshlrev_b32 v162,16,v163\n"
"v_and_b32 v163,0xffff0000,v163\n"
PK"v188,v188,v188\n"
PK"v165,v164,v165\n"
"v_lshlrev_b32 v164,16,v165\n"
"v_lshlrev_b32 v188,16,v188\n"
"v_and_b32 v165,0xffff0000,v165\n"
PK"v167,v166,v167\n"
"v_add_f32 v188,0x3727c5ac,v188\n"
"v_lshlrev_b32 v166,16,v167\n"
"v_and_b32 v167,0xffff0000,v167\n"
PK"v188,v188,v188\n"
"v_bfe_u32 v130,v224,5,4\n"
"v_mul_u32_u24 v130,144,v130\n"
"v_lshlrev_b32 v188,16,v188\n"
"v_bfe_u32 v131,v224,4,1\n"
"v_lshl_add_u32 v130,v131,3,v130\n"
"v_sqrt_f32 v189,v188\n"
"v_lshrrev_b32 v131,9,v224\n"
"v_lshl_add_u32 v130,v131,4,v130\n"
"v_rcp_f32 v190,v189\n"
"s_lshl_b32 s52,s30,5\n"
"v_add_u32 v130,s52,v130\n"
"v_fma_f32 v188,-v189,v189,v188\n"
MU"v190,0.5,v190\n"
"v_fma_f32 v189,v188,v190,v189\n"
PK"v189,v189,v189\n"
"v_lshlrev_b32 v189,16,v189\n"
"v_rcp_f32 v129,v189\n"
"s_nop 0\n"
MU"v148,v148,v129\n"
MU"v149,v149,v129\n"
PK"v138,v148,v149\n"
MU"v150,v150,v129\n"
MU"v151,v151,v129\n"
PK"v139,v150,v151\n"
"ds_write_b64 v130,v[138:139] offset:49152\n"
MU"v152,v152,v129\n"
MU"v153,v153,v129\n"
PK"v140,v152,v153\n"
MU"v154,v154,v129\n"
MU"v155,v155,v129\n"
PK"v141,v154,v155\n"
"ds_write_b64 v130,v[140:141] offset:51456\n"
MU"v156,v156,v129\n"
MU"v157,v157,v129\n"
PK"v142,v156,v157\n"
SA"m0,s36,16384\n"
SA"s48,s46,128\n"
"s_and_b32 s48,s48,1023\n"
BL"v234,s[12:15],s48 offen offset:0 lds\n"
BL"v235,s[12:15],s48 offen offset:1024 lds\n"
BL"v236,s[12:15],s48 offen offset:2048 lds\n"
BL"v237,s[12:15],s48 offen offset:3072 lds\n"
MU"v158,v158,v129\n"
MU"v159,v159,v129\n"
PK"v143,v158,v159\n"
"ds_write_b64 v130,v[142:143] offset:53760\n"
MU"v160,v160,v129\n"
MU"v161,v161,v129\n"
PK"v144,v160,v161\n"
MU"v162,v162,v129\n"
MU"v163,v163,v129\n"
PK"v145,v162,v163\n"
"ds_write_b64 v130,v[144:145] offset:56064\n"
MU"v164,v164,v129\n"
MU"v165,v165,v129\n"
PK"v146,v164,v165\n"
MU"v166,v166,v129\n"
MU"v167,v167,v129\n"
PK"v147,v166,v167\n"
"ds_write_b64 v130,v[146:147] offset:58368\n"
"v_lshlrev_b32 v148,16,v138\n"
"v_and_b32 v149,0xffff0000,v138\n"
MU"v188,v148,v168\n"
MU"v189,v149,v169\n"
"v_lshlrev_b32 v150,16,v139\n"
"v_and_b32 v151,0xffff0000,v139\n"
"v_fmac_f32 v188,v150,v170\n"
"v_fmac_f32 v189,v151,v171\n"
"v_lshlrev_b32 v152,16,v140\n"
"v_and_b32 v153,0xffff0000,v140\n"
"v_fmac_f32 v188,v152,v172\n"
"v_fmac_f32 v189,v153,v173\n"
"v_lshlrev_b32 v154,16,v141\n"
"v_and_b32 v155,0xffff0000,v141\n"
"v_fmac_f32 v188,v154,v174\n"
"v_fmac_f32 v189,v155,v175\n"
"v_lshlrev_b32 v156,16,v142\n"
"v_and_b32 v157,0xffff0000,v142\n"
"v_fmac_f32 v188,v156,v176\n"
"v_fmac_f32 v189,v157,v177\n"
"v_lshlrev_b32 v158,16,v143\n"
"v_and_b32 v159,0xffff0000,v143\n"
"v_fmac_f32 v188,v158,v178\n"
"v_fmac_f32 v189,v159,v179\n"
"v_lshlrev_b32 v160,16,v144\n"
"v_and_b32 v161,0xffff0000,v144\n"
"v_fmac_f32 v188,v160,v180\n"
"v_fmac_f32 v189,v161,v181\n"
"v_lshlrev_b32 v162,16,v145\n"
"v_and_b32 v163,0xffff0000,v145\n"
WT"lgkmcnt(0)\n"
SA"s28,s28,0x7f4a7c15\n"
"s_addc_u32 s29,s29,0x9e3779b9\n"
"v_mov_b32 v232,s28\n"
"v_mov_b32 v233,s29\n"
WT"vmcnt(20)\n"
RT"a[0:1],v246 offset:0\n"
RT"a[2:3],v246 offset:512\n"
RT"a[4:5],v247 offset:0\n"
RT"a[6:7],v247 offset:512\n"
RT"a[8:9],v248 offset:0\n"
RT"a[10:11],v248 offset:512\n"
RT"a[12:13],v249 offset:0\n"
RT"a[14:15],v249 offset:512\n"
WT"vmcnt(16)\n"
RT"a[16:17],v246 offset:4096\n"
RT"a[18:19],v246 offset:4608\n"
RT"a[20:21],v247 offset:4096\n"
RT"a[22:23],v247 offset:4608\n"
RT"a[24:25],v248 offset:4096\n"
RT"a[26:27],v248 offset:4608\n"
RT"a[28:29],v249 offset:4096\n"
RT"a[30:31],v249 offset:4608\n"
WT"vmcnt(12) lgkmcnt(15)\n"
"s_barrier\n"
RD"v[192:195],v242 offset:0\n"
RD"v[196:199],v243 offset:0\n"
RD"v[200:203],v242 offset:2048\n"
RD"v[204:207],v243 offset:2048\n"
RD"v[208:211],v242 offset:4096\n"
"v_lshrrev_b32 v131,7,v224\n"
WT"lgkmcnt(4)\n"
MF"v[0:3],a[0:3],v[192:195],0\n"
WT"lgkmcnt(3)\n"
RD"v[212:215],v243 offset:4096\n"
"v_lshl_add_u32 v131,v131,4,v224\n"
MF"v[0:3],a[16:19],v[196:199],v[0:3]\n"
SA"m0,s37,16384\n"
SA"s48,s47,1048576\n"
"s_and_b32 s48,s48,4194303\n"
MF"v[4:7],a[20:23],v[196:199],0\n"
"s_mul_i32 s52,s30,1152\n"
BL"v238,s[16:19],s48 offen offset:0 lds\n"
MF"v[4:7],a[4:7],v[192:195],v[4:7]\n"
"v_add_u32 v128,s52,v131\n"
"s_and_b32 s52,s30,1\n"
MF"v[8:11],a[8:11],v[192:195],0\n"
"s_mul_i32 s51,s52,1152\n"
RD"v[216:219],v242 offset:6144\n"
"v_add_u32 v131,s51,v131\n"
MF"v[8:11],a[24:27],v[196:199],v[8:11]\n"
"s_lshl_b32 s52,s52,10\n"
"v_fmac_f32 v188,v162,v182\n"
"v_fmac_f32 v189,v163,v183\n"
MF"v[12:15],a[28:31],v[196:199],0\n"
BL"v239,s[16:19],s48 offen offset:1024 lds\n"
MF"v[12:15],a[12:15],v[192:195],v[12:15]\n"
WT"lgkmcnt(1)\n"
RD"v[220:223],v243 offset:6144\n"
RD"v[132:135],v128 offset:49152\n"
MF"v[16:19],a[0:3],v[200:203],0\n"
WT"vmcnt(10)\n"
RT"a[32:33],v246 offset:8192\n"
RD"a[240:243],v128 offset:53760\n"
MF"v[16:19],a[16:19],v[204:207],v[16:19]\n"
"s_lshr_b32 s50,s31,1\n"
RD"a[244:247],v131 offset:58368\n"
"v_lshlrev_b32 v164,16,v146\n"
MF"v[20:23],a[20:23],v[204:207],0\n"
"s_mul_i32 s50,s50,20480\n"
"v_and_b32 v129,0x70,v224\n"
"v_and_b32 v165,0xffff0000,v146\n"
MF"v[20:23],a[4:7],v[200:203],v[20:23]\n"
"s_and_b32 s51,s31,1\n"
RD"v[192:195],v242 offset:8192\n"
RT"a[34:35],v246 offset:8704\n"
MF"v[24:27],a[8:11],v[200:203],0\n"
"s_lshl_b32 s51,s51,7\n"
BL"v240,s[16:19],s48 offen offset:2048 lds\n"
MF"v[24:27],a[24:27],v[204:207],v[24:27]\n"
SA"s50,s50,s51\n"
"v_and_b32 v130,0x380,v224\n"
"v_fmac_f32 v188,v164,v184\n"
MF"v[28:31],a[28:31],v[204:207],0\n"
SA"s50,s50,65536\n"
RT"a[36:37],v247 offset:8192\n"
"v_lshl_add_u32 v129,v130,1,v129\n"
MF"v[28:31],a[12:15],v[200:203],v[28:31]\n"
"s_lshl_b32 s52,s52,1\n"
RD"v[196:199],v243 offset:8192\n"
"v_fmac_f32 v189,v165,v185\n"
MF"v[32:35],a[0:3],v[208:211],0\n"
SA"s52,s52,s50\n"
BL"v241,s[16:19],s48 offen offset:3072 lds\n"
MF"v[32:35],a[16:19],v[212:215],v[32:35]\n"
SA"s52,s52,16384\n"
RT"a[38:39],v247 offset:8704\n"
"v_lshlrev_b32 v166,16,v147\n"
MF"v[36:39],a[20:23],v[212:215],0\n"
SA"m0,s37,20480\n"
RD"v[200:203],v242 offset:10240\n"
"v_and_b32 v167,0xffff0000,v147\n"
MF"v[36:39],a[4:7],v[208:211],v[36:39]\n"
SA"s48,s47,1310720\n"
"v_fmac_f32 v188,v166,v186\n"
"v_fmac_f32 v189,v167,v187\n"
MF"v[40:43],a[8:11],v[208:211],0\n"
"s_and_b32 s48,s48,4194303\n"
RT"a[40:41],v248 offset:8192\n"
"v_add_f32 v188,v188,v189\n"
MF"v[40:43],a[24:27],v[212:215],v[40:43]\n"
"s_lshl_b32 s51,s30,11\n"
BL"v238,s[16:19],s48 offen offset:0 lds\n"
MF"v[44:47],a[28:31],v[212:215],0\n"
SA"s50,s50,s51\n"
RD"v[204:207],v243 offset:10240\n"
"v_add_f32_dpp v188,v188,v188 quad_perm:[1,0,3,2] row_mask:0xf bank_mask:0xf\n"
MF"v[44:47],a[12:15],v[208:211],v[44:47]\n"
WT"lgkmcnt(4)\n"
BS"v[132:135],v129,s[8:11],s50 offen sc1\n"
MF"v[48:51],a[0:3],v[216:219],0\n"
SA"s51,s50,8192\n"
RT"a[42:43],v248 offset:8704\n"
"v_add_f32_dpp v188,v188,v188 quad_perm:[2,3,0,1] row_mask:0xf bank_mask:0xf\n"
MF"v[48:51],a[16:19],v[220:223],v[48:51]\n"
"s_lshl_b32 s90,s31,2\n"
BL"v239,s[16:19],s48 offen offset:1024 lds\n"
MF"v[52:55],a[20:23],v[220:223],0\n"
SA"s90,s90,s30\n"
BS"a[240:243],v129,s[8:11],s51 offen sc1\n"
MF"v[52:55],a[4:7],v[216:219],v[52:55]\n"
"s_lshl_b32 s90,s90,1\n"
RD"v[208:211],v242 offset:12288\n"
RT"a[44:45],v249 offset:8192\n"
MF"v[56:59],a[8:11],v[216:219],0\n"
"s_sub_u32 s91,2047,s90\n"
BS"a[244:247],v129,s[8:11],s52 offen sc1\n"
MF"v[56:59],a[24:27],v[220:223],v[56:59]\n"
RD"v[212:215],v243 offset:12288\n"
RT"a[46:47],v249 offset:8704\n"
MF"v[60:63],a[28:31],v[220:223],0\n"
BL"v240,s[16:19],s48 offen offset:2048 lds\n"
MF"v[60:63],a[12:15],v[216:219],v[60:63]\n"
"v_add_f32_dpp v188,v188,v188 row_half_mirror row_mask:0xf bank_mask:0xf\n"
MF"v[64:67],a[0:3],v[192:195],0\n"
RD"v[216:219],v242 offset:14336\n"
"v_add_f32_dpp v188,v188,v188 row_mirror row_mask:0xf bank_mask:0xf\n"
MF"v[64:67],a[16:19],v[196:199],v[64:67]\n"
WT"vmcnt(14)\n"
RT"a[48:49],v246 offset:12288\n"
"v_mov_b32 v189,v188\n"
MF"v[68:71],a[20:23],v[196:199],0\n"
"s_lshl_b32 s90,s90,2\n"
BL"v241,s[16:19],s48 offen offset:3072 lds\n"
MF"v[68:71],a[4:7],v[192:195],v[68:71]\n"
SA"s90,s90,2686976\n"
RT"a[50:51],v246 offset:12800\n"
"v_permlane16_swap_b32 v189,v188\n"
MF"v[72:75],a[8:11],v[192:195],0\n"
SA"m0,s36,32768\n"
RD"v[220:223],v243 offset:14336\n"
"v_add_f32 v188,v188,v189\n"
MF"v[72:75],a[24:27],v[196:199],v[72:75]\n"
SA"s48,s46,256\n"
PK"v188,v188,v188\n"
"v_lshlrev_b32 v188,16,v188\n"
MF"v[76:79],a[28:31],v[196:199],0\n"
"s_and_b32 s48,s48,1023\n"
RT"a[52:53],v247 offset:12288\n"
"v_ashrrev_i32 v189,31,v188\n"
MF"v[76:79],a[12:15],v[192:195],v[76:79]\n"
WT"lgkmcnt(4)\n"
BL"v234,s[12:15],s48 offen offset:0 lds\n"
MF"v[80:83],a[0:3],v[200:203],0\n"
"v_or_b32 v189,0x80000000,v189\n"
"v_xor_b32 v188,v188,v189\n"
MF"v[80:83],a[16:19],v[204:207],v[80:83]\n"
RT"a[54:55],v247 offset:12800\n"
"v_and_b32 v188,0xffff0000,v188\n"
MF"v[84:87],a[20:23],v[204:207],0\n"
BL"v235,s[12:15],s48 offen offset:1024 lds\n"
MF"v[84:87],a[4:7],v[200:203],v[84:87]\n"
"v_lshrrev_b32 v189,9,v224\n"
"v_sub_u32 v190,s91,v189\n"
MF"v[88:91],a[8:11],v[200:203],0\n"
RT"a[56:57],v248 offset:12288\n"
"v_or_b32 v188,v188,v190\n"
MF"v[88:91],a[24:27],v[204:207],v[88:91]\n"
WT"vmcnt(13) lgkmcnt(3)\n"
"s_barrier\n"
MF"v[92:95],a[28:31],v[204:207],0\n"
RD"v[192:195],v242 offset:16384\n"
"v_and_b32 v190,0x1f0,v224\n"
MF"v[92:95],a[12:15],v[200:203],v[92:95]\n"
RT"a[58:59],v248 offset:12800\n"
"v_lshlrev_b32 v190,23,v190\n"
MF"v[96:99],a[0:3],v[208:211],0\n"
BL"v236,s[12:15],s48 offen offset:2048 lds\n"
MF"v[96:99],a[16:19],v[212:215],v[96:99]\n"
"v_lshl_add_u32 v190,v189,2,v190\n"
MF"v[100:103],a[20:23],v[212:215],0\n"
"buffer_store_dword v188,v190,s[8:11],s90 offen sc1\n"
MF"v[100:103],a[4:7],v[208:211],v[100:103]\n"
RD"v[196:199],v243 offset:16384\n"
RT"a[60:61],v249 offset:12288\n"
MF"v[104:107],a[8:11],v[208:211],0\n"
BL"v237,s[12:15],s48 offen offset:3072 lds\n"
MF"v[104:107],a[24:27],v[212:215],v[104:107]\n"
RT"a[62:63],v249 offset:12800\n"
MF"v[108:111],a[28:31],v[212:215],0\n"
SA"m0,s37,0\n"
RD"v[200:203],v242 offset:18432\n"
SA"s48,s47,1572864\n"
MF"v[108:111],a[12:15],v[208:211],v[108:111]\n"
"s_and_b32 s48,s48,4194303\n"
MF"v[112:115],a[0:3],v[216:219],0\n"
BL"v238,s[16:19],s48 offen offset:0 lds\n"
MF"v[112:115],a[16:19],v[220:223],v[112:115]\n"
RD"v[204:207],v243 offset:18432\n"
MF"v[116:119],a[20:23],v[220:223],0\n"
MF"v[116:119],a[4:7],v[216:219],v[116:119]\n"
MF"v[120:123],a[8:11],v[216:219],0\n"
BL"v239,s[16:19],s48 offen offset:1024 lds\n"
MF"v[120:123],a[24:27],v[220:223],v[120:123]\n"
RD"v[208:211],v242 offset:20480\n"
MF"v[124:127],a[28:31],v[220:223],0\n"
MF"v[124:127],a[12:15],v[216:219],v[124:127]\n"
WT"lgkmcnt(2)\n"
MF"v[0:3],a[32:35],v[192:195],v[0:3]\n"
RD"v[212:215],v243 offset:20480\n"
MF"v[0:3],a[48:51],v[196:199],v[0:3]\n"
BL"v240,s[16:19],s48 offen offset:2048 lds\n"
MF"v[4:7],a[52:55],v[196:199],v[4:7]\n"
MF"v[4:7],a[36:39],v[192:195],v[4:7]\n"
MF"v[8:11],a[40:43],v[192:195],v[8:11]\n"
RD"v[216:219],v242 offset:22528\n"
MF"v[8:11],a[56:59],v[196:199],v[8:11]\n"
BL"v241,s[16:19],s48 offen offset:3072 lds\n"
MF"v[12:15],a[60:63],v[196:199],v[12:15]\n"
MF"v[12:15],a[44:47],v[192:195],v[12:15]\n"
RD"v[220:223],v243 offset:22528\n"
MF"v[16:19],a[32:35],v[200:203],v[16:19]\n"
WT"lgkmcnt(2)\n"
WT"vmcnt(16)\n"
RT"a[64:65],v246 offset:16384\n"
MF"v[16:19],a[48:51],v[204:207],v[16:19]\n"
SA"m0,s37,4096\n"
SA"s48,s47,1835008\n"
"s_and_b32 s48,s48,4194303\n"
MF"v[20:23],a[52:55],v[204:207],v[20:23]\n"
BL"v238,s[16:19],s48 offen offset:0 lds\n"
MF"v[20:23],a[36:39],v[200:203],v[20:23]\n"
RD"v[192:195],v242 offset:24576\n"
RT"a[66:67],v246 offset:16896\n"
MF"v[24:27],a[40:43],v[200:203],v[24:27]\n"
MF"v[24:27],a[56:59],v[204:207],v[24:27]\n"
MF"v[28:31],a[60:63],v[204:207],v[28:31]\n"
RT"a[68:69],v247 offset:16384\n"
MF"v[28:31],a[44:47],v[200:203],v[28:31]\n"
BL"v239,s[16:19],s48 offen offset:1024 lds\n"
MF"v[32:35],a[32:35],v[208:211],v[32:35]\n"
RD"v[196:199],v243 offset:24576\n"
MF"v[32:35],a[48:51],v[212:215],v[32:35]\n"
RT"a[70:71],v247 offset:16896\n"
MF"v[36:39],a[52:55],v[212:215],v[36:39]\n"
RD"v[200:203],v242 offset:26624\n"
MF"v[36:39],a[36:39],v[208:211],v[36:39]\n"
BL"v240,s[16:19],s48 offen offset:2048 lds\n"
MF"v[40:43],a[40:43],v[208:211],v[40:43]\n"
RT"a[72:73],v248 offset:16384\n"
MF"v[40:43],a[56:59],v[212:215],v[40:43]\n"
MF"v[44:47],a[60:63],v[212:215],v[44:47]\n"
RD"v[204:207],v243 offset:26624\n"
MF"v[44:47],a[44:47],v[208:211],v[44:47]\n"
WT"lgkmcnt(4)\n"
RT"a[74:75],v248 offset:16896\n"
MF"v[48:51],a[32:35],v[216:219],v[48:51]\n"
BL"v241,s[16:19],s48 offen offset:3072 lds\n"
MF"v[48:51],a[48:51],v[220:223],v[48:51]\n"
RD"v[208:211],v242 offset:28672\n"
MF"v[52:55],a[52:55],v[220:223],v[52:55]\n"
SA"m0,s36,0\n"
RT"a[76:77],v249 offset:16384\n"
SA"s48,s46,384\n"
MF"v[52:55],a[36:39],v[216:219],v[52:55]\n"
"s_and_b32 s48,s48,1023\n"
MF"v[56:59],a[40:43],v[216:219],v[56:59]\n"
BL"v234,s[12:15],s48 offen offset:0 lds\n"
MF"v[56:59],a[56:59],v[220:223],v[56:59]\n"
RD"v[212:215],v243 offset:28672\n"
RT"a[78:79],v249 offset:16896\n"
MF"v[60:63],a[60:63],v[220:223],v[60:63]\n"
MF"v[60:63],a[44:47],v[216:219],v[60:63]\n"
MF"v[64:67],a[32:35],v[192:195],v[64:67]\n"
RD"v[216:219],v242 offset:30720\n"
MF"v[64:67],a[48:51],v[196:199],v[64:67]\n"
WT"vmcnt(14)\n"
RT"a[80:81],v246 offset:20480\n"
MF"v[68:71],a[52:55],v[196:199],v[68:71]\n"
BL"v235,s[12:15],s48 offen offset:1024 lds\n"
MF"v[68:71],a[36:39],v[192:195],v[68:71]\n"
RT"a[82:83],v246 offset:20992\n"
MF"v[72:75],a[40:43],v[192:195],v[72:75]\n"
RD"v[220:223],v243 offset:30720\n"
MF"v[72:75],a[56:59],v[196:199],v[72:75]\n"
BL"v236,s[12:15],s48 offen offset:2048 lds\n"
MF"v[76:79],a[60:63],v[196:199],v[76:79]\n"
RT"a[84:85],v247 offset:20480\n"
MF"v[76:79],a[44:47],v[192:195],v[76:79]\n"
WT"lgkmcnt(4)\n"
MF"v[80:83],a[32:35],v[200:203],v[80:83]\n"
"s_lshl_b32 s51,s32,8\n"
"s_andn2_b32 s50,s31,7\n"
SA"s50,s50,s51\n"
MF"v[80:83],a[48:51],v[204:207],v[80:83]\n"
SA"s50,s50,256\n"
RT"a[86:87],v247 offset:20992\n"
SA"s50,s50,s45\n"
MF"v[84:87],a[52:55],v[204:207],v[84:87]\n"
BL"v237,s[12:15],s48 offen offset:3072 lds\n"
MF"v[84:87],a[36:39],v[200:203],v[84:87]\n"
MF"v[88:91],a[40:43],v[200:203],v[88:91]\n"
SA"m0,s37,8192\n"
RT"a[88:89],v248 offset:20480\n"
SA"s48,s47,2097152\n"
MF"v[88:91],a[56:59],v[204:207],v[88:91]\n"
"s_and_b32 s48,s48,4194303\n"
WT"vmcnt(12) lgkmcnt(3)\n"
"s_barrier\n"
MF"v[92:95],a[60:63],v[204:207],v[92:95]\n"
RD"v[192:195],v242 offset:32768\n"
MF"v[92:95],a[44:47],v[200:203],v[92:95]\n"
BL"v238,s[16:19],s48 offen offset:0 lds\n"
MF"v[96:99],a[32:35],v[208:211],v[96:99]\n"
"buffer_store_dwordx2 v[232:233],v230,s[8:11],s50 offen sc1\n"
MF"v[96:99],a[48:51],v[212:215],v[96:99]\n"
RT"a[90:91],v248 offset:20992\n"
MF"v[100:103],a[52:55],v[212:215],v[100:103]\n"
RD"v[196:199],v243 offset:32768\n"
RT"a[92:93],v249 offset:20480\n"
MF"v[100:103],a[36:39],v[208:211],v[100:103]\n"
BL"v239,s[16:19],s48 offen offset:1024 lds\n"
MF"v[104:107],a[40:43],v[208:211],v[104:107]\n"
MF"v[104:107],a[56:59],v[212:215],v[104:107]\n"
RT"a[94:95],v249 offset:20992\n"
MF"v[108:111],a[60:63],v[212:215],v[108:111]\n"
RD"v[200:203],v242 offset:34816\n"
MF"v[108:111],a[44:47],v[208:211],v[108:111]\n"
BL"v240,s[16:19],s48 offen offset:2048 lds\n"
MF"v[112:115],a[32:35],v[216:219],v[112:115]\n"
MF"v[112:115],a[48:51],v[220:223],v[112:115]\n"
RD"v[204:207],v243 offset:34816\n"
MF"v[116:119],a[52:55],v[220:223],v[116:119]\n"
MF"v[116:119],a[36:39],v[216:219],v[116:119]\n"
BL"v241,s[16:19],s48 offen offset:3072 lds\n"
MF"v[120:123],a[40:43],v[216:219],v[120:123]\n"
MF"v[120:123],a[56:59],v[220:223],v[120:123]\n"
RD"v[208:211],v242 offset:36864\n"
MF"v[124:127],a[60:63],v[220:223],v[124:127]\n"
SA"m0,s37,12288\n"
SA"s48,s47,2359296\n"
"s_and_b32 s48,s48,4194303\n"
MF"v[124:127],a[44:47],v[216:219],v[124:127]\n"
WT"lgkmcnt(2)\n"
MF"v[0:3],a[64:67],v[192:195],v[0:3]\n"
RD"v[212:215],v243 offset:36864\n"
MF"v[0:3],a[80:83],v[196:199],v[0:3]\n"
BL"v238,s[16:19],s48 offen offset:0 lds\n"
MF"v[4:7],a[84:87],v[196:199],v[4:7]\n"
MF"v[4:7],a[68:71],v[192:195],v[4:7]\n"
MF"v[8:11],a[72:75],v[192:195],v[8:11]\n"
RD"v[216:219],v242 offset:38912\n"
MF"v[8:11],a[88:91],v[196:199],v[8:11]\n"
BL"v239,s[16:19],s48 offen offset:1024 lds\n"
MF"v[12:15],a[92:95],v[196:199],v[12:15]\n"
MF"v[12:15],a[76:79],v[192:195],v[12:15]\n"
RD"v[220:223],v243 offset:38912\n"
MF"v[16:19],a[64:67],v[200:203],v[16:19]\n"
WT"lgkmcnt(2)\n"
WT"vmcnt(15)\n"
RT"a[96:97],v246 offset:0\n"
MF"v[16:19],a[80:83],v[204:207],v[16:19]\n"
BL"v240,s[16:19],s48 offen offset:2048 lds\n"
MF"v[20:23],a[84:87],v[204:207],v[20:23]\n"
MF"v[20:23],a[68:71],v[200:203],v[20:23]\n"
RD"v[192:195],v242 offset:40960\n"
MF"v[24:27],a[72:75],v[200:203],v[24:27]\n"
RT"a[98:99],v246 offset:512\n"
MF"v[24:27],a[88:91],v[204:207],v[24:27]\n"
BL"v241,s[16:19],s48 offen offset:3072 lds\n"
MF"v[28:31],a[92:95],v[204:207],v[28:31]\n"
RT"a[100:101],v247 offset:0\n"
MF"v[28:31],a[76:79],v[200:203],v[28:31]\n"
RD"v[196:199],v243 offset:40960\n"
MF"v[32:35],a[64:67],v[208:211],v[32:35]\n"
SA"m0,s36,16384\n"
SA"s48,s46,512\n"
"s_and_b32 s48,s48,1023\n"
MF"v[32:35],a[80:83],v[212:215],v[32:35]\n"
RT"a[102:103],v247 offset:512\n"
MF"v[36:39],a[84:87],v[212:215],v[36:39]\n"
RD"v[200:203],v242 offset:43008\n"
MF"v[36:39],a[68:71],v[208:211],v[36:39]\n"
BL"v234,s[12:15],s48 offen offset:0 lds\n"
MF"v[40:43],a[72:75],v[208:211],v[40:43]\n"
RT"a[104:105],v248 offset:0\n"
MF"v[40:43],a[88:91],v[212:215],v[40:43]\n"
MF"v[44:47],a[92:95],v[212:215],v[44:47]\n"
RD"v[204:207],v243 offset:43008\n"
MF"v[44:47],a[76:79],v[208:211],v[44:47]\n"
WT"lgkmcnt(4)\n"
BL"v235,s[12:15],s48 offen offset:1024 lds\n"
MF"v[48:51],a[64:67],v[216:219],v[48:51]\n"
RT"a[106:107],v248 offset:512\n"
MF"v[48:51],a[80:83],v[220:223],v[48:51]\n"
RD"v[208:211],v242 offset:45056\n"
MF"v[52:55],a[84:87],v[220:223],v[52:55]\n"
RT"a[108:109],v249 offset:0\n"
MF"v[52:55],a[68:71],v[216:219],v[52:55]\n"
BL"v236,s[12:15],s48 offen offset:2048 lds\n"
MF"v[56:59],a[72:75],v[216:219],v[56:59]\n"
MF"v[56:59],a[88:91],v[220:223],v[56:59]\n"
RD"v[212:215],v243 offset:45056\n"
MF"v[60:63],a[92:95],v[220:223],v[60:63]\n"
RT"a[110:111],v249 offset:512\n"
MF"v[60:63],a[76:79],v[216:219],v[60:63]\n"
BL"v237,s[12:15],s48 offen offset:3072 lds\n"
MF"v[64:67],a[64:67],v[192:195],v[64:67]\n"
RD"v[216:219],v242 offset:47104\n"
MF"v[64:67],a[80:83],v[196:199],v[64:67]\n"
WT"vmcnt(17)\n"
RT"a[112:113],v246 offset:4096\n"
MF"v[68:71],a[84:87],v[196:199],v[68:71]\n"
SA"m0,s37,16384\n"
SA"s48,s47,2621440\n"
"s_and_b32 s48,s48,4194303\n"
MF"v[68:71],a[68:71],v[192:195],v[68:71]\n"
RT"a[114:115],v246 offset:4608\n"
MF"v[72:75],a[72:75],v[192:195],v[72:75]\n"
RD"v[220:223],v243 offset:47104\n"
MF"v[72:75],a[88:91],v[196:199],v[72:75]\n"
BL"v238,s[16:19],s48 offen offset:0 lds\n"
MF"v[76:79],a[92:95],v[196:199],v[76:79]\n"
RT"a[116:117],v247 offset:4096\n"
MF"v[76:79],a[76:79],v[192:195],v[76:79]\n"
WT"lgkmcnt(4)\n"
MF"v[80:83],a[64:67],v[200:203],v[80:83]\n"
BL"v239,s[16:19],s48 offen offset:1024 lds\n"
MF"v[80:83],a[80:83],v[204:207],v[80:83]\n"
RT"a[118:119],v247 offset:4608\n"
MF"v[84:87],a[84:87],v[204:207],v[84:87]\n"
MF"v[84:87],a[68:71],v[200:203],v[84:87]\n"
MF"v[88:91],a[72:75],v[200:203],v[88:91]\n"
RT"a[120:121],v248 offset:4096\n"
MF"v[88:91],a[88:91],v[204:207],v[88:91]\n"
WT"vmcnt(15) lgkmcnt(3)\n"
"s_barrier\n"
MF"v[92:95],a[92:95],v[204:207],v[92:95]\n"
BL"v240,s[16:19],s48 offen offset:2048 lds\n"
MF"v[92:95],a[76:79],v[200:203],v[92:95]\n"
RD"v[192:195],v242 offset:0\n"
RT"a[122:123],v248 offset:4608\n"
MF"v[96:99],a[64:67],v[208:211],v[96:99]\n"
MF"v[96:99],a[80:83],v[212:215],v[96:99]\n"
BL"v241,s[16:19],s48 offen offset:3072 lds\n"
MF"v[100:103],a[84:87],v[212:215],v[100:103]\n"
RD"v[196:199],v243 offset:0\n"
RT"a[124:125],v249 offset:4096\n"
MF"v[100:103],a[68:71],v[208:211],v[100:103]\n"
MF"v[104:107],a[72:75],v[208:211],v[104:107]\n"
SA"m0,s37,20480\n"
SA"s48,s47,2883584\n"
"s_and_b32 s48,s48,4194303\n"
MF"v[104:107],a[88:91],v[212:215],v[104:107]\n"
RT"a[126:127],v249 offset:4608\n"
MF"v[108:111],a[92:95],v[212:215],v[108:111]\n"
RD"v[200:203],v242 offset:2048\n"
MF"v[108:111],a[76:79],v[208:211],v[108:111]\n"
BL"v238,s[16:19],s48 offen offset:0 lds\n"
MF"v[112:115],a[64:67],v[216:219],v[112:115]\n"
MF"v[112:115],a[80:83],v[220:223],v[112:115]\n"
RD"v[204:207],v243 offset:2048\n"
MF"v[116:119],a[84:87],v[220:223],v[116:119]\n"
BL"v239,s[16:19],s48 offen offset:1024 lds\n"
MF"v[116:119],a[68:71],v[216:219],v[116:119]\n"
MF"v[120:123],a[72:75],v[216:219],v[120:123]\n"
MF"v[120:123],a[88:91],v[220:223],v[120:123]\n"
RD"v[208:211],v242 offset:4096\n"
MF"v[124:127],a[92:95],v[220:223],v[124:127]\n"
MF"v[124:127],a[76:79],v[216:219],v[124:127]\n"
WT"lgkmcnt(2)\n"
BL"v240,s[16:19],s48 offen offset:2048 lds\n"
MF"v[0:3],a[96:99],v[192:195],v[0:3]\n"
RD"v[212:215],v243 offset:4096\n"
MF"v[0:3],a[112:115],v[196:199],v[0:3]\n"
MF"v[4:7],a[116:119],v[196:199],v[4:7]\n"
MF"v[4:7],a[100:103],v[192:195],v[4:7]\n"
BL"v241,s[16:19],s48 offen offset:3072 lds\n"
MF"v[8:11],a[104:107],v[192:195],v[8:11]\n"
RD"v[216:219],v242 offset:6144\n"
MF"v[8:11],a[120:123],v[196:199],v[8:11]\n"
MF"v[12:15],a[124:127],v[196:199],v[12:15]\n"
SA"m0,s36,32768\n"
SA"s48,s46,640\n"
"s_and_b32 s48,s48,1023\n"
MF"v[12:15],a[108:111],v[192:195],v[12:15]\n"
RD"v[220:223],v243 offset:6144\n"
MF"v[16:19],a[96:99],v[200:203],v[16:19]\n"
WT"lgkmcnt(2)\n"
WT"vmcnt(16)\n"
RT"a[128:129],v246 offset:8192\n"
MF"v[16:19],a[112:115],v[204:207],v[16:19]\n"
BL"v234,s[12:15],s48 offen offset:0 lds\n"
MF"v[20:23],a[116:119],v[204:207],v[20:23]\n"
MF"v[20:23],a[100:103],v[200:203],v[20:23]\n"
RD"v[192:195],v242 offset:8192\n"
MF"v[24:27],a[104:107],v[200:203],v[24:27]\n"
RT"a[130:131],v246 offset:8704\n"
MF"v[24:27],a[120:123],v[204:207],v[24:27]\n"
BL"v235,s[12:15],s48 offen offset:1024 lds\n"
MF"v[28:31],a[124:127],v[204:207],v[28:31]\n"
RT"a[132:133],v247 offset:8192\n"
MF"v[28:31],a[108:111],v[200:203],v[28:31]\n"
RD"v[196:199],v243 offset:8192\n"
MF"v[32:35],a[96:99],v[208:211],v[32:35]\n"
MF"v[32:35],a[112:115],v[212:215],v[32:35]\n"
RT"a[134:135],v247 offset:8704\n"
MF"v[36:39],a[116:119],v[212:215],v[36:39]\n"
BL"v236,s[12:15],s48 offen offset:2048 lds\n"
MF"v[36:39],a[100:103],v[208:211],v[36:39]\n"
RD"v[200:203],v242 offset:10240\n"
MF"v[40:43],a[104:107],v[208:211],v[40:43]\n"
RT"a[136:137],v248 offset:8192\n"
MF"v[40:43],a[120:123],v[212:215],v[40:43]\n"
BL"v237,s[12:15],s48 offen offset:3072 lds\n"
MF"v[44:47],a[124:127],v[212:215],v[44:47]\n"
RD"v[204:207],v243 offset:10240\n"
MF"v[44:47],a[108:111],v[208:211],v[44:47]\n"
WT"lgkmcnt(4)\n"
RT"a[138:139],v248 offset:8704\n"
MF"v[48:51],a[96:99],v[216:219],v[48:51]\n"
SA"m0,s37,0\n"
SA"s48,s47,3145728\n"
"s_and_b32 s48,s48,4194303\n"
MF"v[48:51],a[112:115],v[220:223],v[48:51]\n"
RD"v[208:211],v242 offset:12288\n"
MF"v[52:55],a[116:119],v[220:223],v[52:55]\n"
RT"a[140:141],v249 offset:8192\n"
MF"v[52:55],a[100:103],v[216:219],v[52:55]\n"
BL"v238,s[16:19],s48 offen offset:0 lds\n"
MF"v[56:59],a[104:107],v[216:219],v[56:59]\n"
MF"v[56:59],a[120:123],v[220:223],v[56:59]\n"
RD"v[212:215],v243 offset:12288\n"
MF"v[60:63],a[124:127],v[220:223],v[60:63]\n"
RT"a[142:143],v249 offset:8704\n"
MF"v[60:63],a[108:111],v[216:219],v[60:63]\n"
BL"v239,s[16:19],s48 offen offset:1024 lds\n"
MF"v[64:67],a[96:99],v[192:195],v[64:67]\n"
RD"v[216:219],v242 offset:14336\n"
MF"v[64:67],a[112:115],v[196:199],v[64:67]\n"
WT"vmcnt(18)\n"
RT"a[144:145],v246 offset:12288\n"
MF"v[68:71],a[116:119],v[196:199],v[68:71]\n"
MF"v[68:71],a[100:103],v[192:195],v[68:71]\n"
RT"a[146:147],v246 offset:12800\n"
MF"v[72:75],a[104:107],v[192:195],v[72:75]\n"
BL"v240,s[16:19],s48 offen offset:2048 lds\n"
MF"v[72:75],a[120:123],v[196:199],v[72:75]\n"
RD"v[220:223],v243 offset:14336\n"
MF"v[76:79],a[124:127],v[196:199],v[76:79]\n"
RT"a[148:149],v247 offset:12288\n"
MF"v[76:79],a[108:111],v[192:195],v[76:79]\n"
WT"lgkmcnt(4)\n"
BL"v241,s[16:19],s48 offen offset:3072 lds\n"
MF"v[80:83],a[96:99],v[200:203],v[80:83]\n"
MF"v[80:83],a[112:115],v[204:207],v[80:83]\n"
RT"a[150:151],v247 offset:12800\n"
MF"v[84:87],a[116:119],v[204:207],v[84:87]\n"
SA"m0,s37,4096\n"
SA"s48,s47,3407872\n"
"s_and_b32 s48,s48,4194303\n"
MF"v[84:87],a[100:103],v[200:203],v[84:87]\n"
MF"v[88:91],a[104:107],v[200:203],v[88:91]\n"
RT"a[152:153],v248 offset:12288\n"
MF"v[88:91],a[120:123],v[204:207],v[88:91]\n"
BL"v238,s[16:19],s48 offen offset:0 lds\n"
MF"v[92:95],a[124:127],v[204:207],v[92:95]\n"
WT"vmcnt(17) lgkmcnt(3)\n"
"s_barrier\n"
MF"v[92:95],a[108:111],v[200:203],v[92:95]\n"
RD"v[192:195],v242 offset:16384\n"
RT"a[154:155],v248 offset:12800\n"
MF"v[96:99],a[96:99],v[208:211],v[96:99]\n"
BL"v239,s[16:19],s48 offen offset:1024 lds\n"
MF"v[96:99],a[112:115],v[212:215],v[96:99]\n"
MF"v[100:103],a[116:119],v[212:215],v[100:103]\n"
RD"v[196:199],v243 offset:16384\n"
MF"v[100:103],a[100:103],v[208:211],v[100:103]\n"
RT"a[156:157],v249 offset:12288\n"
MF"v[104:107],a[104:107],v[208:211],v[104:107]\n"
MF"v[104:107],a[120:123],v[212:215],v[104:107]\n"
RT"a[158:159],v249 offset:12800\n"
MF"v[108:111],a[124:127],v[212:215],v[108:111]\n"
BL"v240,s[16:19],s48 offen offset:2048 lds\n"
MF"v[108:111],a[108:111],v[208:211],v[108:111]\n"
RD"v[200:203],v242 offset:18432\n"
MF"v[112:115],a[96:99],v[216:219],v[112:115]\n"
MF"v[112:115],a[112:115],v[220:223],v[112:115]\n"
RD"v[204:207],v243 offset:18432\n"
MF"v[116:119],a[116:119],v[220:223],v[116:119]\n"
BL"v241,s[16:19],s48 offen offset:3072 lds\n"
MF"v[116:119],a[100:103],v[216:219],v[116:119]\n"
MF"v[120:123],a[104:107],v[216:219],v[120:123]\n"
SA"m0,s36,0\n"
SA"s48,s46,768\n"
"s_and_b32 s48,s48,1023\n"
MF"v[120:123],a[120:123],v[220:223],v[120:123]\n"
RD"v[208:211],v242 offset:20480\n"
MF"v[124:127],a[124:127],v[220:223],v[124:127]\n"
BL"v234,s[12:15],s48 offen offset:0 lds\n"
MF"v[124:127],a[108:111],v[216:219],v[124:127]\n"
WT"lgkmcnt(2)\n"
MF"v[0:3],a[128:131],v[192:195],v[0:3]\n"
RD"v[212:215],v243 offset:20480\n"
MF"v[0:3],a[144:147],v[196:199],v[0:3]\n"
MF"v[4:7],a[148:151],v[196:199],v[4:7]\n"
BL"v235,s[12:15],s48 offen offset:1024 lds\n"
MF"v[4:7],a[132:135],v[192:195],v[4:7]\n"
MF"v[8:11],a[136:139],v[192:195],v[8:11]\n"
RD"v[216:219],v242 offset:22528\n"
MF"v[8:11],a[152:155],v[196:199],v[8:11]\n"
MF"v[12:15],a[156:159],v[196:199],v[12:15]\n"
MF"v[12:15],a[140:143],v[192:195],v[12:15]\n"
RD"v[220:223],v243 offset:22528\n"
MF"v[16:19],a[128:131],v[200:203],v[16:19]\n"
WT"lgkmcnt(2)\n"
BL"v236,s[12:15],s48 offen offset:2048 lds\n"
MF"v[16:19],a[144:147],v[204:207],v[16:19]\n"
WT"vmcnt(19)\n"
RT"a[160:161],v246 offset:16384\n"
MF"v[20:23],a[148:151],v[204:207],v[20:23]\n"
MF"v[20:23],a[132:135],v[200:203],v[20:23]\n"
RD"v[192:195],v242 offset:24576\n"
MF"v[24:27],a[136:139],v[200:203],v[24:27]\n"
RT"a[162:163],v246 offset:16896\n"
MF"v[24:27],a[152:155],v[204:207],v[24:27]\n"
BL"v237,s[12:15],s48 offen offset:3072 lds\n"
MF"v[28:31],a[156:159],v[204:207],v[28:31]\n"
SA"m0,s37,8192\n"
RT"a[164:165],v247 offset:16384\n"
SA"s48,s47,3670016\n"
MF"v[28:31],a[140:143],v[200:203],v[28:31]\n"
"s_and_b32 s48,s48,4194303\n"
RD"v[196:199],v243 offset:24576\n"
MF"v[32:35],a[128:131],v[208:211],v[32:35]\n"
BL"v238,s[16:19],s48 offen offset:0 lds\n"
MF"v[32:35],a[144:147],v[212:215],v[32:35]\n"
RT"a[166:167],v247 offset:16896\n"
MF"v[36:39],a[148:151],v[212:215],v[36:39]\n"
RD"v[200:203],v242 offset:26624\n"
MF"v[36:39],a[132:135],v[208:211],v[36:39]\n"
MF"v[40:43],a[136:139],v[208:211],v[40:43]\n"
RT"a[168:169],v248 offset:16384\n"
MF"v[40:43],a[152:155],v[212:215],v[40:43]\n"
BL"v239,s[16:19],s48 offen offset:1024 lds\n"
MF"v[44:47],a[156:159],v[212:215],v[44:47]\n"
RD"v[204:207],v243 offset:26624\n"
MF"v[44:47],a[140:143],v[208:211],v[44:47]\n"
WT"lgkmcnt(4)\n"
RT"a[170:171],v248 offset:16896\n"
MF"v[48:51],a[128:131],v[216:219],v[48:51]\n"
MF"v[48:51],a[144:147],v[220:223],v[48:51]\n"
RD"v[208:211],v242 offset:28672\n"
MF"v[52:55],a[148:151],v[220:223],v[52:55]\n"
BL"v240,s[16:19],s48 offen offset:2048 lds\n"
MF"v[52:55],a[132:135],v[216:219],v[52:55]\n"
RT"a[172:173],v249 offset:16384\n"
MF"v[56:59],a[136:139],v[216:219],v[56:59]\n"
MF"v[56:59],a[152:155],v[220:223],v[56:59]\n"
RD"v[212:215],v243 offset:28672\n"
MF"v[60:63],a[156:159],v[220:223],v[60:63]\n"
RT"a[174:175],v249 offset:16896\n"
MF"v[60:63],a[140:143],v[216:219],v[60:63]\n"
BL"v241,s[16:19],s48 offen offset:3072 lds\n"
MF"v[64:67],a[128:131],v[192:195],v[64:67]\n"
SA"m0,s37,12288\n"
RD"v[216:219],v242 offset:30720\n"
SA"s48,s47,3932160\n"
MF"v[64:67],a[144:147],v[196:199],v[64:67]\n"
"s_and_b32 s48,s48,4194303\n"
WT"vmcnt(20)\n"
RT"a[176:177],v246 offset:20480\n"
MF"v[68:71],a[148:151],v[196:199],v[68:71]\n"
BL"v238,s[16:19],s48 offen offset:0 lds\n"
MF"v[68:71],a[132:135],v[192:195],v[68:71]\n"
RT"a[178:179],v246 offset:20992\n"
MF"v[72:75],a[136:139],v[192:195],v[72:75]\n"
RD"v[220:223],v243 offset:30720\n"
MF"v[72:75],a[152:155],v[196:199],v[72:75]\n"
MF"v[76:79],a[156:159],v[196:199],v[76:79]\n"
RT"a[180:181],v247 offset:20480\n"
MF"v[76:79],a[140:143],v[192:195],v[76:79]\n"
WT"lgkmcnt(4)\n"
BL"v239,s[16:19],s48 offen offset:1024 lds\n"
MF"v[80:83],a[128:131],v[200:203],v[80:83]\n"
MF"v[80:83],a[144:147],v[204:207],v[80:83]\n"
RT"a[182:183],v247 offset:20992\n"
MF"v[84:87],a[148:151],v[204:207],v[84:87]\n"
MF"v[84:87],a[132:135],v[200:203],v[84:87]\n"
BL"v240,s[16:19],s48 offen offset:2048 lds\n"
MF"v[88:91],a[136:139],v[200:203],v[88:91]\n"
RT"a[184:185],v248 offset:20480\n"
MF"v[88:91],a[152:155],v[204:207],v[88:91]\n"
WT"vmcnt(19) lgkmcnt(3)\n"
"s_barrier\n"
MF"v[92:95],a[156:159],v[204:207],v[92:95]\n"
RD"v[192:195],v242 offset:32768\n"
MF"v[92:95],a[140:143],v[200:203],v[92:95]\n"
RT"a[186:187],v248 offset:20992\n"
MF"v[96:99],a[128:131],v[208:211],v[96:99]\n"
BL"v241,s[16:19],s48 offen offset:3072 lds\n"
MF"v[96:99],a[144:147],v[212:215],v[96:99]\n"
MF"v[100:103],a[148:151],v[212:215],v[100:103]\n"
SA"m0,s36,16384\n"
RD"v[196:199],v243 offset:32768\n"
SA"s48,s46,896\n"
MF"v[100:103],a[132:135],v[208:211],v[100:103]\n"
"s_and_b32 s48,s48,1023\n"
RT"a[188:189],v249 offset:20480\n"
MF"v[104:107],a[136:139],v[208:211],v[104:107]\n"
BL"v234,s[12:15],s48 offen offset:0 lds\n"
MF"v[104:107],a[152:155],v[212:215],v[104:107]\n"
RT"a[190:191],v249 offset:20992\n"
MF"v[108:111],a[156:159],v[212:215],v[108:111]\n"
RD"v[200:203],v242 offset:34816\n"
MF"v[108:111],a[140:143],v[208:211],v[108:111]\n"
MF"v[112:115],a[128:131],v[216:219],v[112:115]\n"
BL"v235,s[12:15],s48 offen offset:1024 lds\n"
MF"v[112:115],a[144:147],v[220:223],v[112:115]\n"
RD"v[204:207],v243 offset:34816\n"
MF"v[116:119],a[148:151],v[220:223],v[116:119]\n"
MF"v[116:119],a[132:135],v[216:219],v[116:119]\n"
MF"v[120:123],a[136:139],v[216:219],v[120:123]\n"
MF"v[120:123],a[152:155],v[220:223],v[120:123]\n"
RD"v[208:211],v242 offset:36864\n"
MF"v[124:127],a[156:159],v[220:223],v[124:127]\n"
BL"v236,s[12:15],s48 offen offset:2048 lds\n"
MF"v[124:127],a[140:143],v[216:219],v[124:127]\n"
WT"lgkmcnt(2)\n"
MF"v[0:3],a[160:163],v[192:195],v[0:3]\n"
RD"v[212:215],v243 offset:36864\n"
MF"v[0:3],a[176:179],v[196:199],v[0:3]\n"
BL"v237,s[12:15],s48 offen offset:3072 lds\n"
MF"v[4:7],a[180:183],v[196:199],v[4:7]\n"
MF"v[4:7],a[164:167],v[192:195],v[4:7]\n"
MF"v[8:11],a[168:171],v[192:195],v[8:11]\n"
SA"m0,s37,16384\n"
RD"v[216:219],v242 offset:38912\n"
SA"s48,s39,0\n"
MF"v[8:11],a[184:187],v[196:199],v[8:11]\n"
MF"v[12:15],a[188:191],v[196:199],v[12:15]\n"
BL"v234,s[12:15],s48 offen offset:0 lds\n"
MF"v[12:15],a[172:175],v[192:195],v[12:15]\n"
RD"v[220:223],v243 offset:38912\n"
MF"v[16:19],a[160:163],v[200:203],v[16:19]\n"
WT"lgkmcnt(2)\n"
WT"vmcnt(21)\n"
RT"a[192:193],v246 offset:0\n"
MF"v[16:19],a[176:179],v[204:207],v[16:19]\n"
MF"v[20:23],a[180:183],v[204:207],v[20:23]\n"
BL"v235,s[12:15],s48 offen offset:1024 lds\n"
MF"v[20:23],a[164:167],v[200:203],v[20:23]\n"
RD"v[192:195],v242 offset:40960\n"
RT"a[194:195],v246 offset:512\n"
MF"v[24:27],a[168:171],v[200:203],v[24:27]\n"
MF"v[24:27],a[184:187],v[204:207],v[24:27]\n"
MF"v[28:31],a[188:191],v[204:207],v[28:31]\n"
RT"a[196:197],v247 offset:0\n"
MF"v[28:31],a[172:175],v[200:203],v[28:31]\n"
RD"v[196:199],v243 offset:40960\n"
MF"v[32:35],a[160:163],v[208:211],v[32:35]\n"
BL"v236,s[12:15],s48 offen offset:2048 lds\n"
MF"v[32:35],a[176:179],v[212:215],v[32:35]\n"
RT"a[198:199],v247 offset:512\n"
MF"v[36:39],a[180:183],v[212:215],v[36:39]\n"
RD"v[200:203],v242 offset:43008\n"
MF"v[36:39],a[164:167],v[208:211],v[36:39]\n"
BL"v237,s[12:15],s48 offen offset:3072 lds\n"
MF"v[40:43],a[168:171],v[208:211],v[40:43]\n"
RT"a[200:201],v248 offset:0\n"
MF"v[40:43],a[184:187],v[212:215],v[40:43]\n"
MF"v[44:47],a[188:191],v[212:215],v[44:47]\n"
SA"m0,s37,20480\n"
RD"v[204:207],v243 offset:43008\n"
SA"s48,s98,0\n"
MF"v[44:47],a[172:175],v[208:211],v[44:47]\n"
WT"lgkmcnt(4)\n"
RT"a[202:203],v248 offset:512\n"
"s_lshr_b32 s50,s30,1\n"
MF"v[48:51],a[160:163],v[216:219],v[48:51]\n"
"s_and_b32 s51,s30,1\n"
BL"v234,s[12:15],s48 offen offset:0 lds\n"
MF"v[48:51],a[176:179],v[220:223],v[48:51]\n"
"s_mul_i32 s38,s50,24576\n"
RD"v[208:211],v242 offset:45056\n"
MF"v[52:55],a[180:183],v[220:223],v[52:55]\n"
"s_lshl_b32 s60,s50,18\n"
RT"a[204:205],v249 offset:0\n"
MF"v[52:55],a[164:167],v[216:219],v[52:55]\n"
"s_lshl_b32 s50,s51,12\n"
MF"v[56:59],a[168:171],v[216:219],v[56:59]\n"
SA"s38,s38,s50\n"
BL"v235,s[12:15],s48 offen offset:1024 lds\n"
MF"v[56:59],a[184:187],v[220:223],v[56:59]\n"
"s_lshl_b32 s50,s51,7\n"
RD"v[212:215],v243 offset:45056\n"
RT"a[206:207],v249 offset:512\n"
MF"v[60:63],a[188:191],v[220:223],v[60:63]\n"
SA"s60,s60,s50\n"
MF"v[60:63],a[172:175],v[216:219],v[60:63]\n"
MF"v[64:67],a[160:163],v[192:195],v[64:67]\n"
RD"v[216:219],v242 offset:47104\n"
MF"v[64:67],a[176:179],v[196:199],v[64:67]\n"
"s_sub_u32 s99,s34,5\n"
WT"vmcnt(22)\n"
RT"a[208:209],v246 offset:4096\n"
MF"v[68:71],a[180:183],v[196:199],v[68:71]\n"
"s_cmp_lt_u32 s34,5\n"
BL"v236,s[12:15],s48 offen offset:2048 lds\n"
MF"v[68:71],a[164:167],v[192:195],v[68:71]\n"
"s_cselect_b32 s99,s34,s99\n"
RT"a[210:211],v246 offset:4608\n"
"s_mul_i32 s99,s99,1\n"
MF"v[72:75],a[168:171],v[192:195],v[72:75]\n"
"s_lshl_b32 s61,s99,20\n"
RD"v[220:223],v243 offset:47104\n"
MF"v[72:75],a[184:187],v[196:199],v[72:75]\n"
BL"v237,s[12:15],s48 offen offset:3072 lds\n"
MF"v[76:79],a[188:191],v[196:199],v[76:79]\n"
RT"a[212:213],v247 offset:4096\n"
MF"v[76:79],a[172:175],v[192:195],v[76:79]\n"
WT"lgkmcnt(4)\n"
MF"v[80:83],a[160:163],v[200:203],v[80:83]\n"
SA"m0,s37,0\n"
SA"s48,s39,32768\n"
MF"v[80:83],a[176:179],v[204:207],v[80:83]\n"
RT"a[214:215],v247 offset:4608\n"
MF"v[84:87],a[180:183],v[204:207],v[84:87]\n"
BL"v234,s[12:15],s48 offen offset:0 lds\n"
MF"v[84:87],a[164:167],v[200:203],v[84:87]\n"
MF"v[88:91],a[168:171],v[200:203],v[88:91]\n"
RT"a[216:217],v248 offset:4096\n"
MF"v[88:91],a[184:187],v[204:207],v[88:91]\n"
WT"vmcnt(21) lgkmcnt(3)\n"
"s_barrier\n"
MF"v[92:95],a[188:191],v[204:207],v[92:95]\n"
RD"v[192:195],v242 offset:0\n"
MF"v[92:95],a[172:175],v[200:203],v[92:95]\n"
BL"v235,s[12:15],s48 offen offset:1024 lds\n"
MF"v[96:99],a[160:163],v[208:211],v[96:99]\n"
RT"a[218:219],v248 offset:4608\n"
MF"v[96:99],a[176:179],v[212:215],v[96:99]\n"
MF"v[100:103],a[180:183],v[212:215],v[100:103]\n"
RD"v[196:199],v243 offset:0\n"
MF"v[100:103],a[164:167],v[208:211],v[100:103]\n"
RT"a[220:221],v249 offset:4096\n"
MF"v[104:107],a[168:171],v[208:211],v[104:107]\n"
BL"v236,s[12:15],s48 offen offset:2048 lds\n"
MF"v[104:107],a[184:187],v[212:215],v[104:107]\n"
RT"a[222:223],v249 offset:4608\n"
MF"v[108:111],a[188:191],v[212:215],v[108:111]\n"
RD"v[200:203],v242 offset:2048\n"
MF"v[108:111],a[172:175],v[208:211],v[108:111]\n"
BL"v237,s[12:15],s48 offen offset:3072 lds\n"
MF"v[112:115],a[160:163],v[216:219],v[112:115]\n"
MF"v[112:115],a[176:179],v[220:223],v[112:115]\n"
RD"v[204:207],v243 offset:2048\n"
MF"v[116:119],a[180:183],v[220:223],v[116:119]\n"
SA"m0,s37,4096\n"
SA"s48,s98,32768\n"
MF"v[116:119],a[164:167],v[216:219],v[116:119]\n"
MF"v[120:123],a[168:171],v[216:219],v[120:123]\n"
WT"lgkmcnt(2)\n"
BL"v234,s[12:15],s48 offen offset:0 lds\n"
MF"v[120:123],a[184:187],v[220:223],v[120:123]\n"
RD"v[208:211],v242 offset:4096\n"
MF"v[124:127],a[188:191],v[220:223],v[124:127]\n"
MF"v[124:127],a[172:175],v[216:219],v[124:127]\n"
MF"v[0:3],a[192:195],v[192:195],v[0:3]\n"
RD"v[212:215],v243 offset:4096\n"
MF"v[0:3],a[208:211],v[196:199],v[0:3]\n"
BL"v235,s[12:15],s48 offen offset:1024 lds\n"
MF"v[4:7],a[212:215],v[196:199],v[4:7]\n"
MF"v[4:7],a[196:199],v[192:195],v[4:7]\n"
MF"v[8:11],a[200:203],v[192:195],v[8:11]\n"
RD"v[216:219],v242 offset:6144\n"
MF"v[8:11],a[216:219],v[196:199],v[8:11]\n"
BL"v236,s[12:15],s48 offen offset:2048 lds\n"
MF"v[12:15],a[220:223],v[196:199],v[12:15]\n"
MF"v[12:15],a[204:207],v[192:195],v[12:15]\n"
WT"lgkmcnt(1)\n"
RD"v[220:223],v243 offset:6144\n"
MF"v[16:19],a[192:195],v[200:203],v[16:19]\n"
WT"vmcnt(23)\n"
RT"a[224:225],v246 offset:8192\n"
MF"v[16:19],a[208:211],v[204:207],v[16:19]\n"
BL"v237,s[12:15],s48 offen offset:3072 lds\n"
MF"v[20:23],a[212:215],v[204:207],v[20:23]\n"
MF"v[20:23],a[196:199],v[200:203],v[20:23]\n"
RD"v[192:195],v242 offset:8192\n"
MF"v[24:27],a[200:203],v[200:203],v[24:27]\n"
RT"a[226:227],v246 offset:8704\n"
MF"v[24:27],a[216:219],v[204:207],v[24:27]\n"
MF"v[28:31],a[220:223],v[204:207],v[28:31]\n"
RT"a[228:229],v247 offset:8192\n"
MF"v[28:31],a[204:207],v[200:203],v[28:31]\n"
RD"v[196:199],v243 offset:8192\n"
MF"v[32:35],a[192:195],v[208:211],v[32:35]\n"
MF"v[32:35],a[208:211],v[212:215],v[32:35]\n"
RT"a[230:231],v247 offset:8704\n"
MF"v[36:39],a[212:215],v[212:215],v[36:39]\n"
RD"v[200:203],v242 offset:10240\n"
MF"v[36:39],a[196:199],v[208:211],v[36:39]\n"
MF"v[40:43],a[200:203],v[208:211],v[40:43]\n"
RT"a[232:233],v248 offset:8192\n"
MF"v[40:43],a[216:219],v[212:215],v[40:43]\n"
MF"v[44:47],a[220:223],v[212:215],v[44:47]\n"
RD"v[204:207],v243 offset:10240\n"
MF"v[44:47],a[204:207],v[208:211],v[44:47]\n"
WT"lgkmcnt(4)\n"
RT"a[234:235],v248 offset:8704\n"
MF"v[48:51],a[192:195],v[216:219],v[48:51]\n"
MF"v[48:51],a[208:211],v[220:223],v[48:51]\n"
RD"v[208:211],v242 offset:12288\n"
MF"v[52:55],a[212:215],v[220:223],v[52:55]\n"
RT"a[236:237],v249 offset:8192\n"
MF"v[52:55],a[196:199],v[216:219],v[52:55]\n"
MF"v[56:59],a[200:203],v[216:219],v[56:59]\n"
MF"v[56:59],a[216:219],v[220:223],v[56:59]\n"
RD"v[212:215],v243 offset:12288\n"
MF"v[60:63],a[220:223],v[220:223],v[60:63]\n"
RT"a[238:239],v249 offset:8704\n"
MF"v[60:63],a[204:207],v[216:219],v[60:63]\n"
MF"v[64:67],a[192:195],v[192:195],v[64:67]\n"
RD"v[216:219],v242 offset:14336\n"
MF"v[64:67],a[208:211],v[196:199],v[64:67]\n"
SA"m0,s37,8192\n"
WT"vmcnt(20)\n"
RT"a[240:241],v246 offset:12288\n"
MF"v[68:71],a[212:215],v[196:199],v[68:71]\n"
SA"s48,s39,65536\n"
MF"v[68:71],a[196:199],v[192:195],v[68:71]\n"
RT"a[242:243],v246 offset:12800\n"
MF"v[72:75],a[200:203],v[192:195],v[72:75]\n"
WT"lgkmcnt(3)\n"
BL"v234,s[12:15],s48 offen offset:0 lds\n"
MF"v[72:75],a[216:219],v[196:199],v[72:75]\n"
RD"v[220:223],v243 offset:14336\n"
MF"v[76:79],a[220:223],v[196:199],v[76:79]\n"
RT"a[244:245],v247 offset:12288\n"
MF"v[76:79],a[204:207],v[192:195],v[76:79]\n"
BL"v235,s[12:15],s48 offen offset:1024 lds\n"
MF"v[80:83],a[192:195],v[200:203],v[80:83]\n"
MF"v[80:83],a[208:211],v[204:207],v[80:83]\n"
RT"a[246:247],v247 offset:12800\n"
MF"v[84:87],a[212:215],v[204:207],v[84:87]\n"
MF"v[84:87],a[196:199],v[200:203],v[84:87]\n"
MF"v[88:91],a[200:203],v[200:203],v[88:91]\n"
RT"a[248:249],v248 offset:12288\n"
MF"v[88:91],a[216:219],v[204:207],v[88:91]\n"
BL"v236,s[12:15],s48 offen offset:2048 lds\n"
MF"v[92:95],a[220:223],v[204:207],v[92:95]\n"
WT"vmcnt(19) lgkmcnt(3)\n"
"s_barrier\n"
MF"v[92:95],a[204:207],v[200:203],v[92:95]\n"
RD"v[192:195],v242 offset:16384\n"
RT"a[250:251],v248 offset:12800\n"
MF"v[96:99],a[192:195],v[208:211],v[96:99]\n"
BL"v237,s[12:15],s48 offen offset:3072 lds\n"
MF"v[96:99],a[208:211],v[212:215],v[96:99]\n"
MF"v[100:103],a[212:215],v[212:215],v[100:103]\n"
RD"v[196:199],v243 offset:16384\n"
MF"v[100:103],a[196:199],v[208:211],v[100:103]\n"
RT"a[252:253],v249 offset:12288\n"
MF"v[104:107],a[200:203],v[208:211],v[104:107]\n"
MF"v[104:107],a[216:219],v[212:215],v[104:107]\n"
RT"a[254:255],v249 offset:12800\n"
MF"v[108:111],a[220:223],v[212:215],v[108:111]\n"
RD"v[200:203],v242 offset:18432\n"
MF"v[108:111],a[204:207],v[208:211],v[108:111]\n"
MF"v[112:115],a[192:195],v[216:219],v[112:115]\n"
"s_lshl_b32 s50,s35,20\n"
MF"v[112:115],a[208:211],v[220:223],v[112:115]\n"
"s_lshl_b32 s51,s34,15\n"
RD"v[204:207],v243 offset:18432\n"
SA"m0,s37,12288\n"
MF"v[116:119],a[212:215],v[220:223],v[116:119]\n"
SA"s48,s98,65536\n"
SA"s50,s50,s51\n"
MF"v[116:119],a[196:199],v[216:219],v[116:119]\n"
WT"lgkmcnt(2)\n"
BL"v234,s[12:15],s48 offen offset:0 lds\n"
MF"v[120:123],a[200:203],v[216:219],v[120:123]\n"
"s_and_b32 s51,s30,1\n"
"s_lshl_b32 s51,s51,11\n"
MF"v[120:123],a[216:219],v[220:223],v[120:123]\n"
SA"s50,s50,s51\n"
RD"v[208:211],v242 offset:20480\n"
MF"v[124:127],a[220:223],v[220:223],v[124:127]\n"
SA"s50,s50,9175040\n"
MF"v[124:127],a[204:207],v[216:219],v[124:127]\n"
BL"v235,s[12:15],s48 offen offset:1024 lds\n"
MF"v[0:3],a[224:227],v[192:195],v[0:3]\n"
RD"v[212:215],v243 offset:20480\n"
"v_add_u32 v250,s50,v224\n"
MF"v[0:3],a[240:243],v[196:199],v[0:3]\n"
MF"v[4:7],a[244:247],v[196:199],v[4:7]\n"
MF"v[4:7],a[228:231],v[192:195],v[4:7]\n"
MF"v[8:11],a[232:235],v[192:195],v[8:11]\n"
RD"v[216:219],v242 offset:22528\n"
MF"v[8:11],a[248:251],v[196:199],v[8:11]\n"
BL"v236,s[12:15],s48 offen offset:2048 lds\n"
MF"v[12:15],a[252:255],v[196:199],v[12:15]\n"
MF"v[12:15],a[236:239],v[192:195],v[12:15]\n"
WT"lgkmcnt(1)\n"
RD"v[220:223],v243 offset:22528\n"
MF"v[16:19],a[224:227],v[200:203],v[16:19]\n"
BL"v237,s[12:15],s48 offen offset:3072 lds\n"
MF"v[16:19],a[240:243],v[204:207],v[16:19]\n"
MF"v[20:23],a[244:247],v[204:207],v[20:23]\n"
MF"v[20:23],a[228:231],v[200:203],v[20:23]\n"
RD"v[192:195],v242 offset:24576\n"
MF"v[24:27],a[232:235],v[200:203],v[24:27]\n"
MF"v[24:27],a[248:251],v[204:207],v[24:27]\n"
MF"v[28:31],a[252:255],v[204:207],v[28:31]\n"
MF"v[28:31],a[236:239],v[200:203],v[28:31]\n"
RD"v[196:199],v243 offset:24576\n"
MF"v[32:35],a[224:227],v[208:211],v[32:35]\n"
MF"v[32:35],a[240:243],v[212:215],v[32:35]\n"
MF"v[36:39],a[244:247],v[212:215],v[36:39]\n"
RD"v[200:203],v242 offset:26624\n"
MF"v[36:39],a[228:231],v[208:211],v[36:39]\n"
MF"v[40:43],a[232:235],v[208:211],v[40:43]\n"
MF"v[40:43],a[248:251],v[212:215],v[40:43]\n"
MF"v[44:47],a[252:255],v[212:215],v[44:47]\n"
RD"v[204:207],v243 offset:26624\n"
MF"v[44:47],a[236:239],v[208:211],v[44:47]\n"
WT"lgkmcnt(2)\n"
MF"v[48:51],a[224:227],v[216:219],v[48:51]\n"
MF"v[48:51],a[240:243],v[220:223],v[48:51]\n"
RD"v[208:211],v242 offset:28672\n"
MF"v[52:55],a[244:247],v[220:223],v[52:55]\n"
MF"v[52:55],a[228:231],v[216:219],v[52:55]\n"
MF"v[56:59],a[232:235],v[216:219],v[56:59]\n"
MF"v[56:59],a[248:251],v[220:223],v[56:59]\n"
RD"v[212:215],v243 offset:28672\n"
MF"v[60:63],a[252:255],v[220:223],v[60:63]\n"
MF"v[60:63],a[236:239],v[216:219],v[60:63]\n"
MF"v[64:67],a[224:227],v[192:195],v[64:67]\n"
RD"v[216:219],v242 offset:30720\n"
MF"v[64:67],a[240:243],v[196:199],v[64:67]\n"
MF"v[68:71],a[244:247],v[196:199],v[68:71]\n"
MF"v[68:71],a[228:231],v[192:195],v[68:71]\n"
MF"v[72:75],a[232:235],v[192:195],v[72:75]\n"
RD"v[220:223],v243 offset:30720\n"
MF"v[72:75],a[248:251],v[196:199],v[72:75]\n"
MF"v[76:79],a[252:255],v[196:199],v[76:79]\n"
MF"v[76:79],a[236:239],v[192:195],v[76:79]\n"
WT"lgkmcnt(1)\n"
MF"v[80:83],a[224:227],v[200:203],v[80:83]\n"
MF"v[80:83],a[240:243],v[204:207],v[80:83]\n"
MF"v[84:87],a[244:247],v[204:207],v[84:87]\n"
"v_add_u32 v244,114688,v242\n"
"v_add_u32 v245,114688,v243\n"
MF"v[84:87],a[228:231],v[200:203],v[84:87]\n"
"v_add_u32 v242,65536,v242\n"
"v_add_u32 v243,65536,v243\n"
MF"v[88:91],a[232:235],v[200:203],v[88:91]\n"
MF"v[88:91],a[248:251],v[204:207],v[88:91]\n"
WT"vmcnt(16) lgkmcnt(0)\n"
"s_barrier\n"
MF"v[92:95],a[252:255],v[204:207],v[92:95]\n"
RD"v[192:195],v242 offset:16384\n"
MF"v[92:95],a[236:239],v[200:203],v[92:95]\n"
MF"v[96:99],a[224:227],v[208:211],v[96:99]\n"
MF"v[96:99],a[240:243],v[212:215],v[96:99]\n"
MF"v[100:103],a[244:247],v[212:215],v[100:103]\n"
SA"m0,s36,49152\n"
RD"v[196:199],v243 offset:16384\n"
SA"s48,s60,s61\n"
MF"v[100:103],a[228:231],v[208:211],v[100:103]\n"
MF"v[104:107],a[232:235],v[208:211],v[104:107]\n"
BL"v238,s[20:23],s48 offen offset:0 lds\n"
MF"v[104:107],a[248:251],v[212:215],v[104:107]\n"
MF"v[108:111],a[252:255],v[212:215],v[108:111]\n"
RD"v[200:203],v242 offset:18432\n"
MF"v[108:111],a[236:239],v[208:211],v[108:111]\n"
MF"v[112:115],a[224:227],v[216:219],v[112:115]\n"
BL"v239,s[20:23],s48 offen offset:1024 lds\n"
MF"v[112:115],a[240:243],v[220:223],v[112:115]\n"
MF"v[116:119],a[244:247],v[220:223],v[116:119]\n"
RD"v[204:207],v243 offset:18432\n"
MF"v[116:119],a[228:231],v[216:219],v[116:119]\n"
MF"v[120:123],a[232:235],v[216:219],v[120:123]\n"
MF"v[120:123],a[248:251],v[220:223],v[120:123]\n"
BL"v240,s[20:23],s48 offen offset:2048 lds\n"
MF"v[124:127],a[252:255],v[220:223],v[124:127]\n"
SA"s61,s61,524288\n"
RD"v[208:211],v242 offset:20480\n"
MF"v[124:127],a[236:239],v[216:219],v[124:127]\n"
WT"lgkmcnt(2)\n"
MF"v[128:131],a[0:3],v[192:195],0\n"
MF"v[128:131],a[16:19],v[196:199],v[128:131]\n"
RD"v[212:215],v243 offset:20480\n"
MF"v[132:135],a[20:23],v[196:199],0\n"
BL"v241,s[20:23],s48 offen offset:3072 lds\n"
MF"v[132:135],a[4:7],v[192:195],v[132:135]\n"
MU"v0,s55,v0\n"
MU"v1,s55,v1\n"
MF"v[136:139],a[8:11],v[192:195],0\n"
MU"v2,s55,v2\n"
MU"v3,s55,v3\n"
MF"v[136:139],a[24:27],v[196:199],v[136:139]\n"
RD"v[216:219],v242 offset:22528\n"
MU"v4,s55,v4\n"
MF"v[140:143],a[28:31],v[196:199],0\n"
SA"m0,s36,32768\n"
MU"v5,s55,v5\n"
MU"v6,s55,v6\n"
MF"v[140:143],a[12:15],v[192:195],v[140:143]\n"
SA"s48,s60,s61\n"
MU"v7,s55,v7\n"
PK"v0,v0,v1\n"
MF"v[144:147],a[0:3],v[200:203],0\n"
WT"lgkmcnt(1)\n"
BL"v238,s[20:23],s48 offen offset:0 lds\n"
MF"v[144:147],a[16:19],v[204:207],v[144:147]\n"
RD"v[220:223],v243 offset:22528\n"
PK"v1,v2,v3\n"
MF"v[148:151],a[20:23],v[204:207],0\n"
PK"v2,v4,v5\n"
PK"v3,v6,v7\n"
MF"v[148:151],a[4:7],v[200:203],v[148:151]\n"
MF"v[152:155],a[8:11],v[200:203],0\n"
BL"v239,s[20:23],s48 offen offset:1024 lds\n"
MF"v[152:155],a[24:27],v[204:207],v[152:155]\n"
RD"v[192:195],v242 offset:40960\n"
MU"v8,s55,v8\n"
MF"v[156:159],a[28:31],v[204:207],0\n"
MU"v9,s55,v9\n"
MU"v10,s55,v10\n"
MF"v[156:159],a[12:15],v[200:203],v[156:159]\n"
MU"v11,s55,v11\n"
MU"v12,s55,v12\n"
MF"v[128:131],a[32:35],v[208:211],v[128:131]\n"
MU"v13,s55,v13\n"
MU"v14,s55,v14\n"
MF"v[128:131],a[48:51],v[212:215],v[128:131]\n"
RD"v[196:199],v243 offset:40960\n"
MU"v15,s55,v15\n"
MF"v[132:135],a[52:55],v[212:215],v[132:135]\n"
SA"s61,s61,524288\n"
BL"v240,s[20:23],s48 offen offset:2048 lds\n"
MF"v[132:135],a[36:39],v[208:211],v[132:135]\n"
"s_cmp_eq_u32 s61,5242880\n"
PK"v8,v8,v9\n"
PK"v9,v10,v11\n"
MF"v[136:139],a[40:43],v[208:211],v[136:139]\n"
"s_cselect_b32 s61,0,s61\n"
RD"v[200:203],v242 offset:43008\n"
PK"v10,v12,v13\n"
MF"v[136:139],a[56:59],v[212:215],v[136:139]\n"
BL"v241,s[20:23],s48 offen offset:3072 lds\n"
MF"v[140:143],a[60:63],v[212:215],v[140:143]\n"
PK"v11,v14,v15\n"
MU"v16,s55,v16\n"
MF"v[140:143],a[44:47],v[208:211],v[140:143]\n"
WT"lgkmcnt(2)\n"
MU"v17,s55,v17\n"
MU"v18,s55,v18\n"
MF"v[144:147],a[32:35],v[216:219],v[144:147]\n"
RD"v[204:207],v243 offset:43008\n"
MU"v19,s55,v19\n"
MF"v[144:147],a[48:51],v[220:223],v[144:147]\n"
MU"v20,s55,v20\n"
MU"v21,s55,v21\n"
MF"v[148:151],a[52:55],v[220:223],v[148:151]\n"
MU"v22,s55,v22\n"
MU"v23,s55,v23\n"
MF"v[148:151],a[36:39],v[216:219],v[148:151]\n"
PK"v16,v16,v17\n"
PK"v17,v18,v19\n"
MF"v[152:155],a[40:43],v[216:219],v[152:155]\n"
RD"v[208:211],v242 offset:45056\n"
PK"v18,v20,v21\n"
MF"v[152:155],a[56:59],v[220:223],v[152:155]\n"
PK"v19,v22,v23\n"
MF"v[156:159],a[60:63],v[220:223],v[156:159]\n"
MF"v[156:159],a[44:47],v[216:219],v[156:159]\n"
"s_mov_b32 s49,0\n"
MF"v[128:131],a[64:67],v[192:195],v[128:131]\n"
WT"lgkmcnt(1)\n"
RD"v[212:215],v243 offset:45056\n"
MU"v24,s55,v24\n"
MF"v[128:131],a[80:83],v[196:199],v[128:131]\n"
BS"v[0:3],v250,s[8:11],s49 offen offset:0 sc1\n"
MF"v[132:135],a[84:87],v[196:199],v[132:135]\n"
MU"v25,s55,v25\n"
MU"v26,s55,v26\n"
MF"v[132:135],a[68:71],v[192:195],v[132:135]\n"
RD"v[216:219],v242 offset:47104\n"
MU"v27,s55,v27\n"
MF"v[136:139],a[72:75],v[192:195],v[136:139]\n"
MU"v28,s55,v28\n"
MU"v29,s55,v29\n"
MF"v[136:139],a[88:91],v[196:199],v[136:139]\n"
MU"v30,s55,v30\n"
MU"v31,s55,v31\n"
MF"v[140:143],a[92:95],v[196:199],v[140:143]\n"
PK"v24,v24,v25\n"
PK"v25,v26,v27\n"
MF"v[140:143],a[76:79],v[192:195],v[140:143]\n"
RD"v[220:223],v243 offset:47104\n"
PK"v26,v28,v29\n"
MF"v[144:147],a[64:67],v[200:203],v[144:147]\n"
PK"v27,v30,v31\n"
MF"v[144:147],a[80:83],v[204:207],v[144:147]\n"
MF"v[148:151],a[84:87],v[204:207],v[148:151]\n"
MU"v32,s55,v32\n"
MU"v33,s55,v33\n"
MF"v[148:151],a[68:71],v[200:203],v[148:151]\n"
RD"v[192:195],v244 offset:16384\n"
MU"v34,s55,v34\n"
MF"v[152:155],a[72:75],v[200:203],v[152:155]\n"
MU"v35,s55,v35\n"
MU"v36,s55,v36\n"
MF"v[152:155],a[88:91],v[204:207],v[152:155]\n"
MU"v37,s55,v37\n"
MU"v38,s55,v38\n"
MF"v[156:159],a[92:95],v[204:207],v[156:159]\n"
MU"v39,s55,v39\n"
PK"v32,v32,v33\n"
MF"v[156:159],a[76:79],v[200:203],v[156:159]\n"
WT"lgkmcnt(1)\n"
RD"v[196:199],v245 offset:16384\n"
PK"v33,v34,v35\n"
MF"v[128:131],a[96:99],v[208:211],v[128:131]\n"
PK"v34,v36,v37\n"
PK"v35,v38,v39\n"
MF"v[128:131],a[112:115],v[212:215],v[128:131]\n"
MF"v[132:135],a[116:119],v[212:215],v[132:135]\n"
MF"v[132:135],a[100:103],v[208:211],v[132:135]\n"
RD"v[200:203],v244 offset:18432\n"
MF"v[136:139],a[104:107],v[208:211],v[136:139]\n"
MU"v40,s55,v40\n"
MU"v41,s55,v41\n"
MF"v[136:139],a[120:123],v[212:215],v[136:139]\n"
MU"v42,s55,v42\n"
MU"v43,s55,v43\n"
MF"v[140:143],a[124:127],v[212:215],v[140:143]\n"
RD"v[204:207],v245 offset:18432\n"
MU"v44,s55,v44\n"
MF"v[140:143],a[108:111],v[208:211],v[140:143]\n"
MU"v45,s55,v45\n"
MU"v46,s55,v46\n"
MF"v[144:147],a[96:99],v[216:219],v[144:147]\n"
MU"v47,s55,v47\n"
PK"v40,v40,v41\n"
MF"v[144:147],a[112:115],v[220:223],v[144:147]\n"
PK"v41,v42,v43\n"
PK"v42,v44,v45\n"
MF"v[148:151],a[116:119],v[220:223],v[148:151]\n"
RD"v[208:211],v244 offset:20480\n"
PK"v43,v46,v47\n"
MF"v[148:151],a[100:103],v[216:219],v[148:151]\n"
MF"v[152:155],a[104:107],v[216:219],v[152:155]\n"
MF"v[152:155],a[120:123],v[220:223],v[152:155]\n"
MF"v[156:159],a[124:127],v[220:223],v[156:159]\n"
RD"v[212:215],v245 offset:20480\n"
MU"v48,s55,v48\n"
MF"v[156:159],a[108:111],v[216:219],v[156:159]\n"
WT"lgkmcnt(2)\n"
MU"v49,s55,v49\n"
MU"v50,s55,v50\n"
MF"v[128:131],a[128:131],v[192:195],v[128:131]\n"
BS"v[8:11],v250,s[8:11],s49 offen offset:1024 sc1\n"
MF"v[128:131],a[144:147],v[196:199],v[128:131]\n"
MU"v51,s55,v51\n"
MU"v52,s55,v52\n"
MF"v[132:135],a[148:151],v[196:199],v[132:135]\n"
RD"v[216:219],v244 offset:22528\n"
MU"v53,s55,v53\n"
MF"v[132:135],a[132:135],v[192:195],v[132:135]\n"
MU"v54,s55,v54\n"
MU"v55,s55,v55\n"
MF"v[136:139],a[136:139],v[192:195],v[136:139]\n"
PK"v48,v48,v49\n"
PK"v49,v50,v51\n"
MF"v[136:139],a[152:155],v[196:199],v[136:139]\n"
RD"v[220:223],v245 offset:22528\n"
PK"v50,v52,v53\n"
MF"v[140:143],a[156:159],v[196:199],v[140:143]\n"
PK"v51,v54,v55\n"
MF"v[140:143],a[140:143],v[192:195],v[140:143]\n"
MF"v[144:147],a[128:131],v[200:203],v[144:147]\n"
MU"v56,s55,v56\n"
MU"v57,s55,v57\n"
MF"v[144:147],a[144:147],v[204:207],v[144:147]\n"
RD"v[192:195],v244 offset:40960\n"
MU"v58,s55,v58\n"
MF"v[148:151],a[148:151],v[204:207],v[148:151]\n"
MU"v59,s55,v59\n"
MU"v60,s55,v60\n"
MF"v[148:151],a[132:135],v[200:203],v[148:151]\n"
MU"v61,s55,v61\n"
MU"v62,s55,v62\n"
MF"v[152:155],a[136:139],v[200:203],v[152:155]\n"
MU"v63,s55,v63\n"
PK"v56,v56,v57\n"
MF"v[152:155],a[152:155],v[204:207],v[152:155]\n"
RD"v[196:199],v245 offset:40960\n"
PK"v57,v58,v59\n"
MF"v[156:159],a[156:159],v[204:207],v[156:159]\n"
PK"v58,v60,v61\n"
PK"v59,v62,v63\n"
MF"v[156:159],a[140:143],v[200:203],v[156:159]\n"
WT"lgkmcnt(2)\n"
MF"v[128:131],a[160:163],v[208:211],v[128:131]\n"
MF"v[128:131],a[176:179],v[212:215],v[128:131]\n"
RD"v[200:203],v244 offset:43008\n"
MF"v[132:135],a[180:183],v[212:215],v[132:135]\n"
MU"v64,s55,v64\n"
MU"v65,s55,v65\n"
MF"v[132:135],a[164:167],v[208:211],v[132:135]\n"
MU"v66,s55,v66\n"
MU"v67,s55,v67\n"
MF"v[136:139],a[168:171],v[208:211],v[136:139]\n"
MU"v68,s55,v68\n"
MU"v69,s55,v69\n"
MF"v[136:139],a[184:187],v[212:215],v[136:139]\n"
RD"v[204:207],v245 offset:43008\n"
MU"v70,s55,v70\n"
MF"v[140:143],a[188:191],v[212:215],v[140:143]\n"
MU"v71,s55,v71\n"
PK"v64,v64,v65\n"
MF"v[140:143],a[172:175],v[208:211],v[140:143]\n"
PK"v65,v66,v67\n"
PK"v66,v68,v69\n"
MF"v[144:147],a[160:163],v[216:219],v[144:147]\n"
RD"v[208:211],v244 offset:45056\n"
PK"v67,v70,v71\n"
MF"v[144:147],a[176:179],v[220:223],v[144:147]\n"
MF"v[148:151],a[180:183],v[220:223],v[148:151]\n"
MF"v[148:151],a[164:167],v[216:219],v[148:151]\n"
MF"v[152:155],a[168:171],v[216:219],v[152:155]\n"
RD"v[212:215],v245 offset:45056\n"
MU"v72,s55,v72\n"
MF"v[152:155],a[184:187],v[220:223],v[152:155]\n"
"s_mov_b32 s49,4096\n"
MU"v73,s55,v73\n"
MU"v74,s55,v74\n"
MF"v[156:159],a[188:191],v[220:223],v[156:159]\n"
MU"v75,s55,v75\n"
MU"v76,s55,v76\n"
MF"v[156:159],a[172:175],v[216:219],v[156:159]\n"
WT"lgkmcnt(1)\n"
BS"v[16:19],v250,s[8:11],s49 offen offset:0 sc1\n"
MF"v[128:131],a[192:195],v[192:195],v[128:131]\n"
RD"v[216:219],v244 offset:47104\n"
MU"v77,s55,v77\n"
MF"v[128:131],a[208:211],v[196:199],v[128:131]\n"
MU"v78,s55,v78\n"
MU"v79,s55,v79\n"
MF"v[132:135],a[212:215],v[196:199],v[132:135]\n"
PK"v72,v72,v73\n"
PK"v73,v74,v75\n"
MF"v[132:135],a[196:199],v[192:195],v[132:135]\n"
PK"v74,v76,v77\n"
PK"v75,v78,v79\n"
MF"v[136:139],a[200:203],v[192:195],v[136:139]\n"
RD"v[220:223],v245 offset:47104\n"
MF"v[136:139],a[216:219],v[196:199],v[136:139]\n"
MF"v[140:143],a[220:223],v[196:199],v[140:143]\n"
MU"v80,s55,v80\n"
MU"v81,s55,v81\n"
MF"v[140:143],a[204:207],v[192:195],v[140:143]\n"
MU"v82,s55,v82\n"
MU"v83,s55,v83\n"
MF"v[144:147],a[192:195],v[200:203],v[144:147]\n"
MU"v84,s55,v84\n"
MU"v85,s55,v85\n"
MF"v[144:147],a[208:211],v[204:207],v[144:147]\n"
MU"v86,s55,v86\n"
MU"v87,s55,v87\n"
MF"v[148:151],a[212:215],v[204:207],v[148:151]\n"
PK"v80,v80,v81\n"
PK"v81,v82,v83\n"
MF"v[148:151],a[196:199],v[200:203],v[148:151]\n"
PK"v82,v84,v85\n"
PK"v83,v86,v87\n"
MF"v[152:155],a[200:203],v[200:203],v[152:155]\n"
MF"v[152:155],a[216:219],v[204:207],v[152:155]\n"
WT"vmcnt(19) lgkmcnt(0)\n"
"s_barrier\n"
MF"v[156:159],a[220:223],v[204:207],v[156:159]\n"
RD"v[192:195],v242 offset:0\n"
MF"v[156:159],a[204:207],v[200:203],v[156:159]\n"
MF"v[128:131],a[224:227],v[208:211],v[128:131]\n"
MU"v88,s55,v88\n"
MU"v89,s55,v89\n"
MF"v[128:131],a[240:243],v[212:215],v[128:131]\n"
MU"v90,s55,v90\n"
MU"v91,s55,v91\n"
MF"v[132:135],a[244:247],v[212:215],v[132:135]\n"
RD"v[196:199],v243 offset:0\n"
MU"v92,s55,v92\n"
MF"v[132:135],a[228:231],v[208:211],v[132:135]\n"
MU"v93,s55,v93\n"
MU"v94,s55,v94\n"
MF"v[136:139],a[232:235],v[208:211],v[136:139]\n"
MU"v95,s55,v95\n"
PK"v88,v88,v89\n"
MF"v[136:139],a[248:251],v[212:215],v[136:139]\n"
PK"v89,v90,v91\n"
PK"v90,v92,v93\n"
MF"v[140:143],a[252:255],v[212:215],v[140:143]\n"
RD"v[200:203],v242 offset:2048\n"
PK"v91,v94,v95\n"
MF"v[140:143],a[236:239],v[208:211],v[140:143]\n"
MF"v[144:147],a[224:227],v[216:219],v[144:147]\n"
MF"v[144:147],a[240:243],v[220:223],v[144:147]\n"
MF"v[148:151],a[244:247],v[220:223],v[148:151]\n"
RD"v[204:207],v243 offset:2048\n"
MF"v[148:151],a[228:231],v[216:219],v[148:151]\n"
MF"v[152:155],a[232:235],v[216:219],v[152:155]\n"
MF"v[152:155],a[248:251],v[220:223],v[152:155]\n"
MF"v[156:159],a[252:255],v[220:223],v[156:159]\n"
RD"v[208:211],v242 offset:4096\n"
MF"v[156:159],a[236:239],v[216:219],v[156:159]\n"
WT"lgkmcnt(2)\n"
BS"v[24:27],v250,s[8:11],s49 offen offset:1024 sc1\n"
MF"v[36:39],a[0:3],v[192:195],0\n"
MF"v[36:39],a[16:19],v[196:199],v[36:39]\n"
RD"v[212:215],v243 offset:4096\n"
MF"v[68:71],a[20:23],v[196:199],0\n"
MF"v[68:71],a[4:7],v[192:195],v[68:71]\n"
MU"v128,s55,v128\n"
MU"v129,s55,v129\n"
MF"v[44:47],a[8:11],v[192:195],0\n"
MU"v130,s55,v130\n"
MU"v131,s55,v131\n"
MF"v[44:47],a[24:27],v[196:199],v[44:47]\n"
RD"v[216:219],v242 offset:6144\n"
MU"v132,s55,v132\n"
MF"v[76:79],a[28:31],v[196:199],0\n"
MU"v133,s55,v133\n"
MU"v134,s55,v134\n"
MF"v[76:79],a[12:15],v[192:195],v[76:79]\n"
MU"v135,s55,v135\n"
PK"v4,v128,v129\n"
MF"v[52:55],a[0:3],v[200:203],0\n"
WT"lgkmcnt(1)\n"
PK"v5,v130,v131\n"
PK"v6,v132,v133\n"
MF"v[52:55],a[16:19],v[204:207],v[52:55]\n"
RD"v[220:223],v243 offset:6144\n"
PK"v7,v134,v135\n"
MF"v[84:87],a[20:23],v[204:207],0\n"
MF"v[84:87],a[4:7],v[200:203],v[84:87]\n"
MF"v[60:63],a[8:11],v[200:203],0\n"
MF"v[60:63],a[24:27],v[204:207],v[60:63]\n"
RD"v[192:195],v242 offset:24576\n"
MU"v136,s55,v136\n"
MF"v[92:95],a[28:31],v[204:207],0\n"
MU"v137,s55,v137\n"
MU"v138,s55,v138\n"
MF"v[92:95],a[12:15],v[200:203],v[92:95]\n"
MU"v139,s55,v139\n"
MU"v140,s55,v140\n"
MF"v[36:39],a[32:35],v[208:211],v[36:39]\n"
MU"v141,s55,v141\n"
MU"v142,s55,v142\n"
MF"v[36:39],a[48:51],v[212:215],v[36:39]\n"
RD"v[196:199],v243 offset:24576\n"
MU"v143,s55,v143\n"
MF"v[68:71],a[52:55],v[212:215],v[68:71]\n"
PK"v12,v136,v137\n"
PK"v13,v138,v139\n"
MF"v[68:71],a[36:39],v[208:211],v[68:71]\n"
PK"v14,v140,v141\n"
PK"v15,v142,v143\n"
MF"v[44:47],a[40:43],v[208:211],v[44:47]\n"
RD"v[200:203],v242 offset:26624\n"
MF"v[44:47],a[56:59],v[212:215],v[44:47]\n"
MF"v[76:79],a[60:63],v[212:215],v[76:79]\n"
MF"v[76:79],a[44:47],v[208:211],v[76:79]\n"
WT"lgkmcnt(2)\n"
MU"v144,s55,v144\n"
MU"v145,s55,v145\n"
MF"v[52:55],a[32:35],v[216:219],v[52:55]\n"
RD"v[204:207],v243 offset:26624\n"
MU"v146,s55,v146\n"
MF"v[52:55],a[48:51],v[220:223],v[52:55]\n"
MU"v147,s55,v147\n"
MU"v148,s55,v148\n"
MF"v[84:87],a[52:55],v[220:223],v[84:87]\n"
MU"v149,s55,v149\n"
MU"v150,s55,v150\n"
MF"v[84:87],a[36:39],v[216:219],v[84:87]\n"
"s_mov_b32 s49,8192\n"
MU"v151,s55,v151\n"
PK"v20,v144,v145\n"
MF"v[60:63],a[40:43],v[216:219],v[60:63]\n"
RD"v[208:211],v242 offset:28672\n"
PK"v21,v146,v147\n"
MF"v[60:63],a[56:59],v[220:223],v[60:63]\n"
BS"v[32:35],v250,s[8:11],s49 offen offset:0 sc1\n"
MF"v[92:95],a[60:63],v[220:223],v[92:95]\n"
PK"v22,v148,v149\n"
PK"v23,v150,v151\n"
MF"v[92:95],a[44:47],v[216:219],v[92:95]\n"
MF"v[36:39],a[64:67],v[192:195],v[36:39]\n"
WT"lgkmcnt(1)\n"
RD"v[212:215],v243 offset:28672\n"
MF"v[36:39],a[80:83],v[196:199],v[36:39]\n"
MU"v152,s55,v152\n"
MU"v153,s55,v153\n"
MF"v[68:71],a[84:87],v[196:199],v[68:71]\n"
MU"v154,s55,v154\n"
MU"v155,s55,v155\n"
MF"v[68:71],a[68:71],v[192:195],v[68:71]\n"
RD"v[216:219],v242 offset:30720\n"
MU"v156,s55,v156\n"
MF"v[44:47],a[72:75],v[192:195],v[44:47]\n"
MU"v157,s55,v157\n"
MU"v158,s55,v158\n"
MF"v[44:47],a[88:91],v[196:199],v[44:47]\n"
MU"v159,s55,v159\n"
PK"v28,v152,v153\n"
MF"v[76:79],a[92:95],v[196:199],v[76:79]\n"
PK"v29,v154,v155\n"
PK"v30,v156,v157\n"
MF"v[76:79],a[76:79],v[192:195],v[76:79]\n"
RD"v[220:223],v243 offset:30720\n"
PK"v31,v158,v159\n"
MF"v[52:55],a[64:67],v[200:203],v[52:55]\n"
MF"v[52:55],a[80:83],v[204:207],v[52:55]\n"
MF"v[84:87],a[84:87],v[204:207],v[84:87]\n"
MF"v[84:87],a[68:71],v[200:203],v[84:87]\n"
RD"v[192:195],v244 offset:0\n"
MF"v[60:63],a[72:75],v[200:203],v[60:63]\n"
MF"v[60:63],a[88:91],v[204:207],v[60:63]\n"
MF"v[92:95],a[92:95],v[204:207],v[92:95]\n"
MF"v[92:95],a[76:79],v[200:203],v[92:95]\n"
WT"lgkmcnt(1)\n"
RD"v[196:199],v245 offset:0\n"
MF"v[36:39],a[96:99],v[208:211],v[36:39]\n"
MF"v[36:39],a[112:115],v[212:215],v[36:39]\n"
MF"v[68:71],a[116:119],v[212:215],v[68:71]\n"
MF"v[68:71],a[100:103],v[208:211],v[68:71]\n"
RD"v[200:203],v244 offset:2048\n"
MF"v[44:47],a[104:107],v[208:211],v[44:47]\n"
MF"v[44:47],a[120:123],v[212:215],v[44:47]\n"
MF"v[76:79],a[124:127],v[212:215],v[76:79]\n"
RD"v[204:207],v245 offset:2048\n"
MF"v[76:79],a[108:111],v[208:211],v[76:79]\n"
MF"v[52:55],a[96:99],v[216:219],v[52:55]\n"
MF"v[52:55],a[112:115],v[220:223],v[52:55]\n"
MF"v[84:87],a[116:119],v[220:223],v[84:87]\n"
SA"m0,s37,16384\n"
RD"v[208:211],v244 offset:4096\n"
SA"s48,s39,98304\n"
MF"v[84:87],a[100:103],v[216:219],v[84:87]\n"
MF"v[60:63],a[104:107],v[216:219],v[60:63]\n"
BS"v[40:43],v250,s[8:11],s49 offen offset:1024 sc1\n"
MF"v[60:63],a[120:123],v[220:223],v[60:63]\n"
BL"v234,s[12:15],s48 offen offset:0 lds\n"
MF"v[92:95],a[124:127],v[220:223],v[92:95]\n"
RD"v[212:215],v245 offset:4096\n"
MU"v96,s55,v96\n"
MF"v[92:95],a[108:111],v[216:219],v[92:95]\n"
WT"lgkmcnt(2)\n"
MU"v97,s55,v97\n"
MU"v98,s55,v98\n"
MF"v[36:39],a[128:131],v[192:195],v[36:39]\n"
MU"v99,s55,v99\n"
MU"v100,s55,v100\n"
MF"v[36:39],a[144:147],v[196:199],v[36:39]\n"
MU"v101,s55,v101\n"
MU"v102,s55,v102\n"
MF"v[68:71],a[148:151],v[196:199],v[68:71]\n"
RD"v[216:219],v244 offset:6144\n"
MU"v103,s55,v103\n"
MF"v[68:71],a[132:135],v[192:195],v[68:71]\n"
BL"v235,s[12:15],s48 offen offset:1024 lds\n"
MF"v[44:47],a[136:139],v[192:195],v[44:47]\n"
PK"v96,v96,v97\n"
PK"v97,v98,v99\n"
MF"v[44:47],a[152:155],v[196:199],v[44:47]\n"
RD"v[220:223],v245 offset:6144\n"
PK"v98,v100,v101\n"
MF"v[76:79],a[156:159],v[196:199],v[76:79]\n"
PK"v99,v102,v103\n"
MU"v104,s55,v104\n"
MF"v[76:79],a[140:143],v[192:195],v[76:79]\n"
MU"v105,s55,v105\n"
MU"v106,s55,v106\n"
MF"v[52:55],a[128:131],v[200:203],v[52:55]\n"
BL"v236,s[12:15],s48 offen offset:2048 lds\n"
MF"v[52:55],a[144:147],v[204:207],v[52:55]\n"
RD"v[192:195],v244 offset:24576\n"
MU"v107,s55,v107\n"
MF"v[84:87],a[148:151],v[204:207],v[84:87]\n"
MU"v108,s55,v108\n"
MU"v109,s55,v109\n"
MF"v[84:87],a[132:135],v[200:203],v[84:87]\n"
MU"v110,s55,v110\n"
MU"v111,s55,v111\n"
MF"v[60:63],a[136:139],v[200:203],v[60:63]\n"
PK"v104,v104,v105\n"
PK"v105,v106,v107\n"
MF"v[60:63],a[152:155],v[204:207],v[60:63]\n"
RD"v[196:199],v245 offset:24576\n"
PK"v106,v108,v109\n"
MF"v[92:95],a[156:159],v[204:207],v[92:95]\n"
BL"v237,s[12:15],s48 offen offset:3072 lds\n"
MF"v[92:95],a[140:143],v[200:203],v[92:95]\n"
WT"lgkmcnt(2)\n"
PK"v107,v110,v111\n"
MF"v[36:39],a[160:163],v[208:211],v[36:39]\n"
MU"v112,s55,v112\n"
MU"v113,s55,v113\n"
MF"v[36:39],a[176:179],v[212:215],v[36:39]\n"
RD"v[200:203],v244 offset:26624\n"
MU"v114,s55,v114\n"
MF"v[68:71],a[180:183],v[212:215],v[68:71]\n"
SA"m0,s37,20480\n"
MU"v115,s55,v115\n"
MU"v116,s55,v116\n"
MF"v[68:71],a[164:167],v[208:211],v[68:71]\n"
SA"s48,s98,98304\n"
MU"v117,s55,v117\n"
MU"v118,s55,v118\n"
MF"v[44:47],a[168:171],v[208:211],v[44:47]\n"
BL"v234,s[12:15],s48 offen offset:0 lds\n"
MF"v[44:47],a[184:187],v[212:215],v[44:47]\n"
RD"v[204:207],v245 offset:26624\n"
MU"v119,s55,v119\n"
MF"v[76:79],a[188:191],v[212:215],v[76:79]\n"
PK"v112,v112,v113\n"
PK"v113,v114,v115\n"
MF"v[76:79],a[172:175],v[208:211],v[76:79]\n"
PK"v114,v116,v117\n"
PK"v115,v118,v119\n"
MF"v[52:55],a[160:163],v[216:219],v[52:55]\n"
RD"v[208:211],v244 offset:28672\n"
MF"v[52:55],a[176:179],v[220:223],v[52:55]\n"
"s_mov_b32 s49,12288\n"
MF"v[84:87],a[180:183],v[220:223],v[84:87]\n"
BL"v235,s[12:15],s48 offen offset:1024 lds\n"
MF"v[84:87],a[164:167],v[216:219],v[84:87]\n"
BS"v[48:51],v250,s[8:11],s49 offen offset:0 sc1\n"
MF"v[60:63],a[168:171],v[216:219],v[60:63]\n"
RD"v[212:215],v245 offset:28672\n"
MU"v120,s55,v120\n"
MF"v[60:63],a[184:187],v[220:223],v[60:63]\n"
MU"v121,s55,v121\n"
MU"v122,s55,v122\n"
MF"v[92:95],a[188:191],v[220:223],v[92:95]\n"
MU"v123,s55,v123\n"
MU"v124,s55,v124\n"
MF"v[92:95],a[172:175],v[216:219],v[92:95]\n"
WT"lgkmcnt(1)\n"
MU"v125,s55,v125\n"
MU"v126,s55,v126\n"
MF"v[36:39],a[192:195],v[192:195],v[36:39]\n"
RD"v[216:219],v244 offset:30720\n"
MU"v127,s55,v127\n"
MF"v[36:39],a[208:211],v[196:199],v[36:39]\n"
BL"v236,s[12:15],s48 offen offset:2048 lds\n"
MF"v[68:71],a[212:215],v[196:199],v[68:71]\n"
PK"v120,v120,v121\n"
PK"v121,v122,v123\n"
MF"v[68:71],a[196:199],v[192:195],v[68:71]\n"
PK"v122,v124,v125\n"
PK"v123,v126,v127\n"
MF"v[44:47],a[200:203],v[192:195],v[44:47]\n"
RD"v[220:223],v245 offset:30720\n"
MF"v[44:47],a[216:219],v[196:199],v[44:47]\n"
MF"v[76:79],a[220:223],v[196:199],v[76:79]\n"
BL"v237,s[12:15],s48 offen offset:3072 lds\n"
MF"v[76:79],a[204:207],v[192:195],v[76:79]\n"
MF"v[52:55],a[192:195],v[200:203],v[52:55]\n"
MF"v[52:55],a[208:211],v[204:207],v[52:55]\n"
MF"v[84:87],a[212:215],v[204:207],v[84:87]\n"
MF"v[84:87],a[196:199],v[200:203],v[84:87]\n"
"v_lshrrev_b32 v251,2,v224\n"
MF"v[60:63],a[200:203],v[200:203],v[60:63]\n"
"v_and_b32 v251,28,v251\n"
MF"v[60:63],a[216:219],v[204:207],v[60:63]\n"
WT"vmcnt(23) lgkmcnt(0)\n"
"s_barrier\n"
MF"v[92:95],a[220:223],v[204:207],v[92:95]\n"
RD"v[192:195],v242 offset:8192\n"
"ds_read_b32 v234,v251 offset:16384\n"
MF"v[92:95],a[204:207],v[200:203],v[92:95]\n"
MF"v[36:39],a[224:227],v[208:211],v[36:39]\n"
MF"v[36:39],a[240:243],v[212:215],v[36:39]\n"
MF"v[68:71],a[244:247],v[212:215],v[68:71]\n"
RD"v[196:199],v243 offset:8192\n"
MF"v[68:71],a[228:231],v[208:211],v[68:71]\n"
MF"v[44:47],a[232:235],v[208:211],v[44:47]\n"
MF"v[44:47],a[248:251],v[212:215],v[44:47]\n"
MF"v[76:79],a[252:255],v[212:215],v[76:79]\n"
RD"v[200:203],v242 offset:10240\n"
MF"v[76:79],a[236:239],v[208:211],v[76:79]\n"
WT"lgkmcnt(2)\n"
"v_not_b32 v251,v234\n"
"v_bfe_u32 v252,v251,4,7\n"
MF"v[52:55],a[224:227],v[216:219],v[52:55]\n"
"v_mul_u32_u24 v252,20480,v252\n"
MF"v[52:55],a[240:243],v[220:223],v[52:55]\n"
"v_and_b32 v251,15,v251\n"
MF"v[84:87],a[244:247],v[220:223],v[84:87]\n"
RD"v[204:207],v243 offset:10240\n"
"v_lshl_add_u32 v251,v251,4,v252\n"
MF"v[84:87],a[228:231],v[216:219],v[84:87]\n"
BS"v[56:59],v250,s[8:11],s49 offen offset:1024 sc1\n"
MF"v[60:63],a[232:235],v[216:219],v[60:63]\n"
"v_and_b32 v252,0x380,v224\n"
"v_lshl_add_u32 v251,v252,1,v251\n"
MF"v[60:63],a[248:251],v[220:223],v[60:63]\n"
"v_add_u32 v252,v252,v224\n"
MF"v[92:95],a[252:255],v[220:223],v[92:95]\n"
"s_lshl_b32 s92,s34,2\n"
RD"v[208:211],v242 offset:12288\n"
MF"v[92:95],a[236:239],v[216:219],v[92:95]\n"
SA"s92,s92,s30\n"
MF"v[128:131],a[0:3],v[192:195],0\n"
WT"lgkmcnt(2)\n"
"s_lshl_b32 s93,s92,11\n"
MF"v[128:131],a[16:19],v[196:199],v[128:131]\n"
"s_cmp_lt_u32 s92,10\n"
RD"v[212:215],v243 offset:12288\n"
"s_cselect_b32 s93,s93,2130706432\n"
MF"v[132:135],a[20:23],v[196:199],0\n"
SA"s92,s93,65536\n"
MF"v[132:135],a[4:7],v[192:195],v[132:135]\n"
BL"v[100:103],v251,s[8:11],s92 offen sc1\n"
MF"v[136:139],a[8:11],v[192:195],0\n"
"s_mul_i32 s92,s35,20480\n"
MU"v36,s55,v36\n"
MU"v37,s55,v37\n"
MF"v[136:139],a[24:27],v[196:199],v[136:139]\n"
SA"s92,s92,s93\n"
RD"v[216:219],v242 offset:14336\n"
MU"v38,s55,v38\n"
MF"v[140:143],a[28:31],v[196:199],0\n"
SA"s92,s92,2770944\n"
MU"v39,s55,v39\n"
MU"v68,s55,v68\n"
MF"v[140:143],a[12:15],v[192:195],v[140:143]\n"
MU"v69,s55,v69\n"
MU"v70,s55,v70\n"
MF"v[144:147],a[0:3],v[200:203],0\n"
WT"lgkmcnt(1)\n"
MU"v71,s55,v71\n"
PK"v36,v36,v37\n"
MF"v[144:147],a[16:19],v[204:207],v[144:147]\n"
RD"v[220:223],v243 offset:14336\n"
PK"v37,v38,v39\n"
MF"v[148:151],a[20:23],v[204:207],0\n"
PK"v38,v68,v69\n"
PK"v39,v70,v71\n"
MF"v[148:151],a[4:7],v[200:203],v[148:151]\n"
MF"v[152:155],a[8:11],v[200:203],0\n"
MF"v[152:155],a[24:27],v[204:207],v[152:155]\n"
RD"v[192:195],v242 offset:32768\n"
MU"v44,s55,v44\n"
MF"v[156:159],a[28:31],v[204:207],0\n"
MU"v45,s55,v45\n"
MU"v46,s55,v46\n"
MF"v[156:159],a[12:15],v[200:203],v[156:159]\n"
MU"v47,s55,v47\n"
MU"v76,s55,v76\n"
MF"v[128:131],a[32:35],v[208:211],v[128:131]\n"
MU"v77,s55,v77\n"
MU"v78,s55,v78\n"
MF"v[128:131],a[48:51],v[212:215],v[128:131]\n"
RD"v[196:199],v243 offset:32768\n"
MU"v79,s55,v79\n"
MF"v[132:135],a[52:55],v[212:215],v[132:135]\n"
PK"v44,v44,v45\n"
PK"v45,v46,v47\n"
MF"v[132:135],a[36:39],v[208:211],v[132:135]\n"
PK"v46,v76,v77\n"
PK"v47,v78,v79\n"
MF"v[136:139],a[40:43],v[208:211],v[136:139]\n"
SA"m0,s36,0\n"
RD"v[200:203],v242 offset:34816\n"
SA"s48,s60,s61\n"
MF"v[136:139],a[56:59],v[212:215],v[136:139]\n"
MF"v[140:143],a[60:63],v[212:215],v[140:143]\n"
BL"v238,s[20:23],s48 offen offset:0 lds\n"
MF"v[140:143],a[44:47],v[208:211],v[140:143]\n"
WT"lgkmcnt(2)\n"
MU"v52,s55,v52\n"
MU"v53,s55,v53\n"
MF"v[144:147],a[32:35],v[216:219],v[144:147]\n"
"s_mov_b32 s49,16384\n"
RD"v[204:207],v243 offset:34816\n"
MU"v54,s55,v54\n"
MF"v[144:147],a[48:51],v[220:223],v[144:147]\n"
BS"v[64:67],v250,s[8:11],s49 offen offset:0 sc1\n"
MF"v[148:151],a[52:55],v[220:223],v[148:151]\n"
MU"v55,s55,v55\n"
MU"v84,s55,v84\n"
MF"v[148:151],a[36:39],v[216:219],v[148:151]\n"
MU"v85,s55,v85\n"
MU"v86,s55,v86\n"
MF"v[152:155],a[40:43],v[216:219],v[152:155]\n"
RD"v[208:211],v242 offset:36864\n"
MU"v87,s55,v87\n"
MF"v[152:155],a[56:59],v[220:223],v[152:155]\n"
PK"v52,v52,v53\n"
PK"v53,v54,v55\n"
MF"v[156:159],a[60:63],v[220:223],v[156:159]\n"
BL"v239,s[20:23],s48 offen offset:1024 lds\n"
MF"v[156:159],a[44:47],v[216:219],v[156:159]\n"
PK"v54,v84,v85\n"
PK"v55,v86,v87\n"
MF"v[128:131],a[64:67],v[192:195],v[128:131]\n"
WT"lgkmcnt(1)\n"
RD"v[212:215],v243 offset:36864\n"
MF"v[128:131],a[80:83],v[196:199],v[128:131]\n"
MU"v60,s55,v60\n"
MU"v61,s55,v61\n"
MF"v[132:135],a[84:87],v[196:199],v[132:135]\n"
MU"v62,s55,v62\n"
MU"v63,s55,v63\n"
MF"v[132:135],a[68:71],v[192:195],v[132:135]\n"
RD"v[216:219],v242 offset:38912\n"
MU"v92,s55,v92\n"
MF"v[136:139],a[72:75],v[192:195],v[136:139]\n"
MU"v93,s55,v93\n"
MU"v94,s55,v94\n"
MF"v[136:139],a[88:91],v[196:199],v[136:139]\n"
MU"v95,s55,v95\n"
PK"v60,v60,v61\n"
MF"v[140:143],a[92:95],v[196:199],v[140:143]\n"
BL"v240,s[20:23],s48 offen offset:2048 lds\n"
MF"v[140:143],a[76:79],v[192:195],v[140:143]\n"
RD"v[220:223],v243 offset:38912\n"
PK"v61,v62,v63\n"
MF"v[144:147],a[64:67],v[200:203],v[144:147]\n"
PK"v62,v92,v93\n"
PK"v63,v94,v95\n"
MF"v[144:147],a[80:83],v[204:207],v[144:147]\n"
MF"v[148:151],a[84:87],v[204:207],v[148:151]\n"
MF"v[148:151],a[68:71],v[200:203],v[148:151]\n"
RD"v[192:195],v244 offset:8192\n"
MF"v[152:155],a[72:75],v[200:203],v[152:155]\n"
MF"v[152:155],a[88:91],v[204:207],v[152:155]\n"
MF"v[156:159],a[92:95],v[204:207],v[156:159]\n"
SA"s61,s61,524288\n"
BL"v241,s[20:23],s48 offen offset:3072 lds\n"
MF"v[156:159],a[76:79],v[200:203],v[156:159]\n"
WT"lgkmcnt(1)\n"
RD"v[196:199],v245 offset:8192\n"
MF"v[128:131],a[96:99],v[208:211],v[128:131]\n"
MF"v[128:131],a[112:115],v[212:215],v[128:131]\n"
MF"v[132:135],a[116:119],v[212:215],v[132:135]\n"
MF"v[132:135],a[100:103],v[208:211],v[132:135]\n"
RD"v[200:203],v244 offset:10240\n"
MF"v[136:139],a[104:107],v[208:211],v[136:139]\n"
MF"v[136:139],a[120:123],v[212:215],v[136:139]\n"
MF"v[140:143],a[124:127],v[212:215],v[140:143]\n"
RD"v[204:207],v245 offset:10240\n"
MF"v[140:143],a[108:111],v[208:211],v[140:143]\n"
MF"v[144:147],a[96:99],v[216:219],v[144:147]\n"
BS"v[72:75],v250,s[8:11],s49 offen offset:1024 sc1\n"
MF"v[144:147],a[112:115],v[220:223],v[144:147]\n"
MF"v[148:151],a[116:119],v[220:223],v[148:151]\n"
RD"v[208:211],v244 offset:12288\n"
MF"v[148:151],a[100:103],v[216:219],v[148:151]\n"
MF"v[152:155],a[104:107],v[216:219],v[152:155]\n"
SA"m0,s38,65536\n"
SA"s48,s60,s61\n"
MF"v[152:155],a[120:123],v[220:223],v[152:155]\n"
MF"v[156:159],a[124:127],v[220:223],v[156:159]\n"
RD"v[212:215],v245 offset:12288\n"
MF"v[156:159],a[108:111],v[216:219],v[156:159]\n"
WT"lgkmcnt(2)\n"
BL"v238,s[20:23],s48 offen offset:0 lds\n"
MF"v[128:131],a[128:131],v[192:195],v[128:131]\n"
MF"v[128:131],a[144:147],v[196:199],v[128:131]\n"
MF"v[132:135],a[148:151],v[196:199],v[132:135]\n"
RD"v[216:219],v244 offset:14336\n"
MF"v[132:135],a[132:135],v[192:195],v[132:135]\n"
MF"v[136:139],a[136:139],v[192:195],v[136:139]\n"
MF"v[136:139],a[152:155],v[196:199],v[136:139]\n"
RD"v[220:223],v245 offset:14336\n"
MF"v[140:143],a[156:159],v[196:199],v[140:143]\n"
BL"v239,s[20:23],s48 offen offset:1024 lds\n"
MF"v[140:143],a[140:143],v[192:195],v[140:143]\n"
MF"v[144:147],a[128:131],v[200:203],v[144:147]\n"
MF"v[144:147],a[144:147],v[204:207],v[144:147]\n"
RD"v[192:195],v244 offset:32768\n"
MF"v[148:151],a[148:151],v[204:207],v[148:151]\n"
MF"v[148:151],a[132:135],v[200:203],v[148:151]\n"
MF"v[152:155],a[136:139],v[200:203],v[152:155]\n"
MF"v[152:155],a[152:155],v[204:207],v[152:155]\n"
RD"v[196:199],v245 offset:32768\n"
MF"v[156:159],a[156:159],v[204:207],v[156:159]\n"
BL"v240,s[20:23],s48 offen offset:2048 lds\n"
MF"v[156:159],a[140:143],v[200:203],v[156:159]\n"
WT"lgkmcnt(2)\n"
MF"v[128:131],a[160:163],v[208:211],v[128:131]\n"
MF"v[128:131],a[176:179],v[212:215],v[128:131]\n"
RD"v[200:203],v244 offset:34816\n"
MF"v[132:135],a[180:183],v[212:215],v[132:135]\n"
MF"v[132:135],a[164:167],v[208:211],v[132:135]\n"
MF"v[136:139],a[168:171],v[208:211],v[136:139]\n"
MF"v[136:139],a[184:187],v[212:215],v[136:139]\n"
"s_mov_b32 s49,20480\n"
RD"v[204:207],v245 offset:34816\n"
MF"v[140:143],a[188:191],v[212:215],v[140:143]\n"
SA"s61,s61,524288\n"
BL"v241,s[20:23],s48 offen offset:3072 lds\n"
MF"v[140:143],a[172:175],v[208:211],v[140:143]\n"
"s_cmp_eq_u32 s61,5242880\n"
BS"v[80:83],v250,s[8:11],s49 offen offset:0 sc1\n"
MF"v[144:147],a[160:163],v[216:219],v[144:147]\n"
"s_cselect_b32 s61,0,s61\n"
RD"v[208:211],v244 offset:36864\n"
MF"v[144:147],a[176:179],v[220:223],v[144:147]\n"
MF"v[148:151],a[180:183],v[220:223],v[148:151]\n"
MF"v[148:151],a[164:167],v[216:219],v[148:151]\n"
MF"v[152:155],a[168:171],v[216:219],v[152:155]\n"
SA"m0,s38,114688\n"
RD"v[212:215],v245 offset:36864\n"
SA"s48,s60,s61\n"
MF"v[152:155],a[184:187],v[220:223],v[152:155]\n"
MF"v[156:159],a[188:191],v[220:223],v[156:159]\n"
BL"v238,s[20:23],s48 offen offset:0 lds\n"
MF"v[156:159],a[172:175],v[216:219],v[156:159]\n"
WT"lgkmcnt(1)\n"
MF"v[128:131],a[192:195],v[192:195],v[128:131]\n"
RD"v[216:219],v244 offset:38912\n"
MF"v[128:131],a[208:211],v[196:199],v[128:131]\n"
MF"v[132:135],a[212:215],v[196:199],v[132:135]\n"
MF"v[132:135],a[196:199],v[192:195],v[132:135]\n"
MF"v[136:139],a[200:203],v[192:195],v[136:139]\n"
RD"v[220:223],v245 offset:38912\n"
MF"v[136:139],a[216:219],v[196:199],v[136:139]\n"
MF"v[140:143],a[220:223],v[196:199],v[140:143]\n"
BL"v239,s[20:23],s48 offen offset:1024 lds\n"
MF"v[140:143],a[204:207],v[192:195],v[140:143]\n"
WT"vmcnt(13)\n"
BS"v[100:103],v252,s[8:11],s92 offen sc1\n"
MF"v[144:147],a[192:195],v[200:203],v[144:147]\n"
MF"v[144:147],a[208:211],v[204:207],v[144:147]\n"
MF"v[148:151],a[212:215],v[204:207],v[148:151]\n"
MF"v[148:151],a[196:199],v[200:203],v[148:151]\n"
MF"v[152:155],a[200:203],v[200:203],v[152:155]\n"
MF"v[152:155],a[216:219],v[204:207],v[152:155]\n"
WT"lgkmcnt(0)\n"
"s_barrier\n"
MF"v[156:159],a[220:223],v[204:207],v[156:159]\n"
RD"v[192:195],v242 offset:16384\n"
MF"v[156:159],a[204:207],v[200:203],v[156:159]\n"
BL"v240,s[20:23],s48 offen offset:2048 lds\n"
MF"v[128:131],a[224:227],v[208:211],v[128:131]\n"
MF"v[128:131],a[240:243],v[212:215],v[128:131]\n"
MF"v[132:135],a[244:247],v[212:215],v[132:135]\n"
RD"v[196:199],v243 offset:16384\n"
MF"v[132:135],a[228:231],v[208:211],v[132:135]\n"
MF"v[136:139],a[232:235],v[208:211],v[136:139]\n"
MF"v[136:139],a[248:251],v[212:215],v[136:139]\n"
MF"v[140:143],a[252:255],v[212:215],v[140:143]\n"
SA"s61,s61,524288\n"
RD"v[200:203],v242 offset:18432\n"
MF"v[140:143],a[236:239],v[208:211],v[140:143]\n"
BS"v[88:91],v250,s[8:11],s49 offen offset:1024 sc1\n"
MF"v[144:147],a[224:227],v[216:219],v[144:147]\n"
"s_lshl_b32 s50,s34,3\n"
BL"v241,s[20:23],s48 offen offset:3072 lds\n"
MF"v[144:147],a[240:243],v[220:223],v[144:147]\n"
SA"s51,s50,s30\n"
MF"v[148:151],a[244:247],v[220:223],v[148:151]\n"
"s_mul_i32 s51,s51,20480\n"
RD"v[204:207],v243 offset:18432\n"
MF"v[148:151],a[228:231],v[216:219],v[148:151]\n"
SA"s56,s51,65536\n"
MF"v[152:155],a[232:235],v[216:219],v[152:155]\n"
SA"m0,s36,16384\n"
SA"s48,s60,s61\n"
SA"s57,s56,81920\n"
MF"v[152:155],a[248:251],v[220:223],v[152:155]\n"
"s_sub_u32 s51,s30,s50\n"
MF"v[156:159],a[252:255],v[220:223],v[156:159]\n"
SA"s51,s51,120\n"
RD"v[208:211],v242 offset:20480\n"
MF"v[156:159],a[236:239],v[216:219],v[156:159]\n"
WT"lgkmcnt(2)\n"
BL"v238,s[20:23],s48 offen offset:0 lds\n"
MF"v[0:3],a[0:3],v[192:195],0\n"
"s_mul_i32 s51,s51,20480\n"
SA"s58,s51,65536\n"
"s_lshl_b32 s99,s99,12\n"
MF"v[0:3],a[16:19],v[196:199],v[0:3]\n"
SA"s59,s58,81920\n"
RD"v[212:215],v243 offset:20480\n"
MF"v[8:11],a[20:23],v[196:199],0\n"
"v_add_u32 v235,s99,v224\n"
MF"v[8:11],a[4:7],v[192:195],v[8:11]\n"
MU"v128,s55,v128\n"
MU"v129,s55,v129\n"
MF"v[16:19],a[8:11],v[192:195],0\n"
MU"v130,s55,v130\n"
MU"v131,s55,v131\n"
MF"v[16:19],a[24:27],v[196:199],v[16:19]\n"
RD"v[216:219],v242 offset:22528\n"
MU"v132,s55,v132\n"
MF"v[24:27],a[28:31],v[196:199],0\n"
BL"v239,s[20:23],s48 offen offset:1024 lds\n"
MF"v[24:27],a[12:15],v[192:195],v[24:27]\n"
MU"v133,s55,v133\n"
MU"v134,s55,v134\n"
MF"v[32:35],a[0:3],v[200:203],0\n"
WT"lgkmcnt(1)\n"
MU"v135,s55,v135\n"
PK"v64,v128,v129\n"
MF"v[32:35],a[16:19],v[204:207],v[32:35]\n"
RD"v[220:223],v243 offset:22528\n"
PK"v65,v130,v131\n"
MF"v[40:43],a[20:23],v[204:207],0\n"
PK"v66,v132,v133\n"
PK"v67,v134,v135\n"
MF"v[40:43],a[4:7],v[200:203],v[40:43]\n"
MF"v[48:51],a[8:11],v[200:203],0\n"
MF"v[48:51],a[24:27],v[204:207],v[48:51]\n"
RD"v[192:195],v242 offset:40960\n"
MU"v136,s55,v136\n"
MF"v[56:59],a[28:31],v[204:207],0\n"
BL"v240,s[20:23],s48 offen offset:2048 lds\n"
MF"v[56:59],a[12:15],v[200:203],v[56:59]\n"
MU"v137,s55,v137\n"
MU"v138,s55,v138\n"
MF"v[0:3],a[32:35],v[208:211],v[0:3]\n"
BL"a[0:3],v235,s[8:11],s56 offen offset:0 sc1\n"
MF"v[0:3],a[48:51],v[212:215],v[0:3]\n"
RD"v[196:199],v243 offset:40960\n"
MU"v139,s55,v139\n"
MF"v[8:11],a[52:55],v[212:215],v[8:11]\n"
MU"v140,s55,v140\n"
MU"v141,s55,v141\n"
MF"v[8:11],a[36:39],v[208:211],v[8:11]\n"
"s_mov_b32 s49,24576\n"
MU"v142,s55,v142\n"
MU"v143,s55,v143\n"
MF"v[16:19],a[40:43],v[208:211],v[16:19]\n"
RD"v[200:203],v242 offset:43008\n"
PK"v68,v136,v137\n"
MF"v[16:19],a[56:59],v[212:215],v[16:19]\n"
BS"v[96:99],v250,s[8:11],s49 offen offset:0 sc1\n"
MF"v[24:27],a[60:63],v[212:215],v[24:27]\n"
SA"s61,s61,524288\n"
BL"v241,s[20:23],s48 offen offset:3072 lds\n"
MF"v[24:27],a[44:47],v[208:211],v[24:27]\n"
WT"lgkmcnt(2)\n"
BL"a[4:7],v235,s[8:11],s57 offen offset:0 sc1\n"
MF"v[32:35],a[32:35],v[216:219],v[32:35]\n"
"s_cmp_eq_u32 s61,5242880\n"
RD"v[204:207],v243 offset:43008\n"
PK"v69,v138,v139\n"
MF"v[32:35],a[48:51],v[220:223],v[32:35]\n"
"s_cselect_b32 s61,0,s61\n"
PK"v70,v140,v141\n"
PK"v71,v142,v143\n"
MF"v[40:43],a[52:55],v[220:223],v[40:43]\n"
MU"v144,s55,v144\n"
MU"v145,s55,v145\n"
MF"v[40:43],a[36:39],v[216:219],v[40:43]\n"
MU"v146,s55,v146\n"
MU"v147,s55,v147\n"
MF"v[48:51],a[40:43],v[216:219],v[48:51]\n"
RD"v[208:211],v242 offset:45056\n"
MU"v148,s55,v148\n"
MF"v[48:51],a[56:59],v[220:223],v[48:51]\n"
BL"a[8:11],v235,s[8:11],s58 offen offset:0 sc1\n"
MF"v[56:59],a[60:63],v[220:223],v[56:59]\n"
MU"v149,s55,v149\n"
MU"v150,s55,v150\n"
MF"v[56:59],a[44:47],v[216:219],v[56:59]\n"
MU"v151,s55,v151\n"
PK"v72,v144,v145\n"
MF"v[0:3],a[64:67],v[192:195],v[0:3]\n"
WT"lgkmcnt(1)\n"
RD"v[212:215],v243 offset:45056\n"
PK"v73,v146,v147\n"
MF"v[0:3],a[80:83],v[196:199],v[0:3]\n"
PK"v74,v148,v149\n"
PK"v75,v150,v151\n"
MF"v[8:11],a[84:87],v[196:199],v[8:11]\n"
BL"a[12:15],v235,s[8:11],s59 offen offset:0 sc1\n"
MF"v[8:11],a[68:71],v[192:195],v[8:11]\n"
RD"v[216:219],v242 offset:47104\n"
MU"v152,s55,v152\n"
MF"v[16:19],a[72:75],v[192:195],v[16:19]\n"
MU"v153,s55,v153\n"
MU"v154,s55,v154\n"
MF"v[16:19],a[88:91],v[196:199],v[16:19]\n"
MU"v155,s55,v155\n"
MU"v156,s55,v156\n"
MF"v[24:27],a[92:95],v[196:199],v[24:27]\n"
MU"v157,s55,v157\n"
MU"v158,s55,v158\n"
MF"v[24:27],a[76:79],v[192:195],v[24:27]\n"
RD"v[220:223],v243 offset:47104\n"
MU"v159,s55,v159\n"
MF"v[32:35],a[64:67],v[200:203],v[32:35]\n"
BL"a[16:19],v235,s[8:11],s56 offen offset:1024 sc1\n"
MF"v[32:35],a[80:83],v[204:207],v[32:35]\n"
PK"v76,v152,v153\n"
PK"v77,v154,v155\n"
MF"v[40:43],a[84:87],v[204:207],v[40:43]\n"
PK"v78,v156,v157\n"
PK"v79,v158,v159\n"
MF"v[40:43],a[68:71],v[200:203],v[40:43]\n"
RD"v[192:195],v244 offset:16384\n"
MF"v[48:51],a[72:75],v[200:203],v[48:51]\n"
MF"v[48:51],a[88:91],v[204:207],v[48:51]\n"
MF"v[56:59],a[92:95],v[204:207],v[56:59]\n"
BL"a[20:23],v235,s[8:11],s57 offen offset:1024 sc1\n"
MF"v[56:59],a[76:79],v[200:203],v[56:59]\n"
WT"lgkmcnt(1)\n"
RD"v[196:199],v245 offset:16384\n"
MF"v[0:3],a[96:99],v[208:211],v[0:3]\n"
MF"v[0:3],a[112:115],v[212:215],v[0:3]\n"
MF"v[8:11],a[116:119],v[212:215],v[8:11]\n"
MF"v[8:11],a[100:103],v[208:211],v[8:11]\n"
RD"v[200:203],v244 offset:18432\n"
MF"v[16:19],a[104:107],v[208:211],v[16:19]\n"
BS"v[104:107],v250,s[8:11],s49 offen offset:1024 sc1\n"
MF"v[16:19],a[120:123],v[212:215],v[16:19]\n"
BL"a[24:27],v235,s[8:11],s58 offen offset:1024 sc1\n"
MF"v[24:27],a[124:127],v[212:215],v[24:27]\n"
RD"v[204:207],v245 offset:18432\n"
MF"v[24:27],a[108:111],v[208:211],v[24:27]\n"
MF"v[32:35],a[96:99],v[216:219],v[32:35]\n"
MF"v[32:35],a[112:115],v[220:223],v[32:35]\n"
MF"v[40:43],a[116:119],v[220:223],v[40:43]\n"
RD"v[208:211],v244 offset:20480\n"
MF"v[40:43],a[100:103],v[216:219],v[40:43]\n"
BL"a[28:31],v235,s[8:11],s59 offen offset:1024 sc1\n"
MF"v[48:51],a[104:107],v[216:219],v[48:51]\n"
MF"v[48:51],a[120:123],v[220:223],v[48:51]\n"
MF"v[56:59],a[124:127],v[220:223],v[56:59]\n"
RD"v[212:215],v245 offset:20480\n"
MF"v[56:59],a[108:111],v[216:219],v[56:59]\n"
WT"lgkmcnt(2)\n"
MF"v[0:3],a[128:131],v[192:195],v[0:3]\n"
BL"a[32:35],v235,s[8:11],s56 offen offset:2048 sc1\n"
MF"v[0:3],a[144:147],v[196:199],v[0:3]\n"
MF"v[8:11],a[148:151],v[196:199],v[8:11]\n"
RD"v[216:219],v244 offset:22528\n"
MF"v[8:11],a[132:135],v[192:195],v[8:11]\n"
MF"v[16:19],a[136:139],v[192:195],v[16:19]\n"
MF"v[16:19],a[152:155],v[196:199],v[16:19]\n"
RD"v[220:223],v245 offset:22528\n"
MF"v[24:27],a[156:159],v[196:199],v[24:27]\n"
BL"a[36:39],v235,s[8:11],s57 offen offset:2048 sc1\n"
MF"v[24:27],a[140:143],v[192:195],v[24:27]\n"
MF"v[32:35],a[128:131],v[200:203],v[32:35]\n"
MF"v[32:35],a[144:147],v[204:207],v[32:35]\n"
RD"v[192:195],v244 offset:40960\n"
MF"v[40:43],a[148:151],v[204:207],v[40:43]\n"
MF"v[40:43],a[132:135],v[200:203],v[40:43]\n"
MF"v[48:51],a[136:139],v[200:203],v[48:51]\n"
BL"a[40:43],v235,s[8:11],s58 offen offset:2048 sc1\n"
MF"v[48:51],a[152:155],v[204:207],v[48:51]\n"
RD"v[196:199],v245 offset:40960\n"
MF"v[56:59],a[156:159],v[204:207],v[56:59]\n"
MF"v[56:59],a[140:143],v[200:203],v[56:59]\n"
WT"lgkmcnt(2)\n"
MF"v[0:3],a[160:163],v[208:211],v[0:3]\n"
"s_sub_u32 s53,s37,65536\n"
MF"v[0:3],a[176:179],v[212:215],v[0:3]\n"
"s_mov_b32 s49,28672\n"
RD"v[200:203],v244 offset:43008\n"
MF"v[8:11],a[180:183],v[212:215],v[8:11]\n"
BL"a[44:47],v235,s[8:11],s59 offen offset:2048 sc1\n"
MF"v[8:11],a[164:167],v[208:211],v[8:11]\n"
BS"v[112:115],v250,s[8:11],s49 offen offset:0 sc1\n"
MF"v[16:19],a[168:171],v[208:211],v[16:19]\n"
"v_subrev_u32 v246,s53,v246\n"
"v_subrev_u32 v247,s53,v247\n"
MF"v[16:19],a[184:187],v[212:215],v[16:19]\n"
RD"v[204:207],v245 offset:43008\n"
"v_subrev_u32 v248,s53,v248\n"
MF"v[24:27],a[188:191],v[212:215],v[24:27]\n"
"v_subrev_u32 v249,s53,v249\n"
"v_subrev_u32 v236,65536,v246\n"
MF"v[24:27],a[172:175],v[208:211],v[24:27]\n"
"v_subrev_u32 v237,65536,v247\n"
"v_subrev_u32 v228,65536,v248\n"
MF"v[32:35],a[160:163],v[216:219],v[32:35]\n"
RD"v[208:211],v244 offset:45056\n"
"v_subrev_u32 v229,65536,v249\n"
MF"v[32:35],a[176:179],v[220:223],v[32:35]\n"
BL"a[48:51],v235,s[8:11],s56 offen offset:3072 sc1\n"
MF"v[40:43],a[180:183],v[220:223],v[40:43]\n"
MF"v[40:43],a[164:167],v[216:219],v[40:43]\n"
MF"v[48:51],a[168:171],v[216:219],v[48:51]\n"
RD"v[212:215],v245 offset:45056\n"
MF"v[48:51],a[184:187],v[220:223],v[48:51]\n"
MF"v[56:59],a[188:191],v[220:223],v[56:59]\n"
BL"a[52:55],v235,s[8:11],s57 offen offset:3072 sc1\n"
MF"v[56:59],a[172:175],v[216:219],v[56:59]\n"
WT"lgkmcnt(1)\n"
MF"v[0:3],a[192:195],v[192:195],v[0:3]\n"
RD"v[216:219],v244 offset:47104\n"
MF"v[0:3],a[208:211],v[196:199],v[0:3]\n"
MF"v[8:11],a[212:215],v[196:199],v[8:11]\n"
MF"v[8:11],a[196:199],v[192:195],v[8:11]\n"
MF"v[16:19],a[200:203],v[192:195],v[16:19]\n"
RD"v[220:223],v245 offset:47104\n"
MF"v[16:19],a[216:219],v[196:199],v[16:19]\n"
BL"a[56:59],v235,s[8:11],s58 offen offset:3072 sc1\n"
MF"v[24:27],a[220:223],v[196:199],v[24:27]\n"
MF"v[24:27],a[204:207],v[192:195],v[24:27]\n"
MF"v[32:35],a[192:195],v[200:203],v[32:35]\n"
SA"s99,s99,4096\n"
MF"v[32:35],a[208:211],v[204:207],v[32:35]\n"
"s_cmp_eq_u32 s99,20480\n"
"s_cselect_b32 s99,0,s99\n"
MF"v[40:43],a[212:215],v[204:207],v[40:43]\n"
BL"a[60:63],v235,s[8:11],s59 offen offset:3072 sc1\n"
MF"v[40:43],a[196:199],v[200:203],v[40:43]\n"
MF"v[48:51],a[200:203],v[200:203],v[48:51]\n"
MF"v[48:51],a[216:219],v[204:207],v[48:51]\n"
WT"lgkmcnt(0)\n"
"s_barrier\n"
MF"v[56:59],a[220:223],v[204:207],v[56:59]\n"
RT"a[128:129],v236 offset:49152\n"
MF"v[56:59],a[204:207],v[200:203],v[56:59]\n"
"v_add_u32 v235,s99,v224\n"
MF"v[0:3],a[224:227],v[208:211],v[0:3]\n"
SA"m0,s38,73728\n"
RT"a[130:131],v236 offset:49664\n"
SA"s48,s60,s61\n"
MF"v[0:3],a[240:243],v[212:215],v[0:3]\n"
BL"a[64:67],v235,s[8:11],s56 offen offset:0 sc1\n"
MF"v[8:11],a[244:247],v[212:215],v[8:11]\n"
RT"a[132:133],v236 offset:57344\n"
"v_add_u32 v242,49152,v246\n"
MF"v[8:11],a[228:231],v[208:211],v[8:11]\n"
BS"v[120:123],v250,s[8:11],s49 offen offset:1024 sc1\n"
MF"v[16:19],a[232:235],v[208:211],v[16:19]\n"
BL"v238,s[20:23],s48 offen offset:0 lds\n"
MF"v[16:19],a[248:251],v[212:215],v[16:19]\n"
RT"a[134:135],v236 offset:57856\n"
"v_add_u32 v243,49152,v247\n"
MF"v[24:27],a[252:255],v[212:215],v[24:27]\n"
RT"a[136:137],v237 offset:49152\n"
"v_add_u32 v244,49152,v248\n"
MF"v[24:27],a[236:239],v[208:211],v[24:27]\n"
BL"a[68:71],v235,s[8:11],s57 offen offset:0 sc1\n"
MF"v[32:35],a[224:227],v[216:219],v[32:35]\n"
RT"a[138:139],v237 offset:49664\n"
"v_add_u32 v245,49152,v249\n"
MF"v[32:35],a[240:243],v[220:223],v[32:35]\n"
MF"v[40:43],a[244:247],v[220:223],v[40:43]\n"
RT"a[140:141],v237 offset:57344\n"
MF"v[40:43],a[228:231],v[216:219],v[40:43]\n"
BL"v239,s[20:23],s48 offen offset:1024 lds\n"
MF"v[48:51],a[232:235],v[216:219],v[48:51]\n"
RT"a[142:143],v237 offset:57856\n"
MF"v[48:51],a[248:251],v[220:223],v[48:51]\n"
BL"a[72:75],v235,s[8:11],s58 offen offset:0 sc1\n"
MF"v[56:59],a[252:255],v[220:223],v[56:59]\n"
RT"a[144:145],v228 offset:49152\n"
MF"v[56:59],a[236:239],v[216:219],v[56:59]\n"
WT"vmcnt(25) lgkmcnt(3)\n"
MF"v[80:83],a[128:131],a[0:3],0\n"
WT"vmcnt(19)\n"
RT"a[146:147],v228 offset:49664\n"
MF"v[80:83],a[132:135],a[16:19],v[80:83]\n"
WT"vmcnt(18)\n"
MF"v[88:91],a[132:135],a[20:23],0\n"
RT"a[148:149],v228 offset:57344\n"
MF"v[88:91],a[128:131],a[4:7],v[88:91]\n"
BL"v240,s[20:23],s48 offen offset:2048 lds\n"
MF"v[96:99],a[128:131],a[8:11],0\n"
WT"vmcnt(17)\n"
BL"a[76:79],v235,s[8:11],s59 offen offset:0 sc1\n"
MF"v[96:99],a[132:135],a[24:27],v[96:99]\n"
WT"vmcnt(17)\n"
RT"a[150:151],v228 offset:57856\n"
RT"a[152:153],v229 offset:49152\n"
MF"v[104:107],a[132:135],a[28:31],0\n"
MU"v0,s55,v0\n"
MU"v1,s55,v1\n"
MF"v[104:107],a[128:131],a[12:15],v[104:107]\n"
RT"a[154:155],v229 offset:49664\n"
MU"v2,s55,v2\n"
MF"v[84:87],a[136:139],a[0:3],0\n"
WT"lgkmcnt(4)\n"
MU"v3,s55,v3\n"
MU"v8,s55,v8\n"
MF"v[84:87],a[140:143],a[16:19],v[84:87]\n"
RT"a[156:157],v229 offset:57344\n"
MU"v9,s55,v9\n"
MF"v[92:95],a[140:143],a[20:23],0\n"
SA"s61,s61,524288\n"
BL"a[80:83],v235,s[8:11],s56 offen offset:1024 sc1\n"
MF"v[92:95],a[136:139],a[4:7],v[92:95]\n"
BL"v241,s[20:23],s48 offen offset:3072 lds\n"
MF"v[100:103],a[136:139],a[8:11],0\n"
RT"a[158:159],v229 offset:57856\n"
MU"v10,s55,v10\n"
MF"v[100:103],a[140:143],a[24:27],v[100:103]\n"
RT"a[160:161],v236 offset:53248\n"
MU"v11,s55,v11\n"
MF"v[108:111],a[140:143],a[28:31],0\n"
PK"v0,v0,v1\n"
PK"v1,v2,v3\n"
MF"v[108:111],a[136:139],a[12:15],v[108:111]\n"
RT"a[162:163],v236 offset:53760\n"
PK"v2,v8,v9\n"
MF"v[112:115],a[144:147],a[0:3],0\n"
WT"lgkmcnt(3)\n"
BL"a[84:87],v235,s[8:11],s57 offen offset:1024 sc1\n"
MF"v[112:115],a[148:151],a[16:19],v[112:115]\n"
RT"a[164:165],v236 offset:61440\n"
PK"v3,v10,v11\n"
MF"v[120:123],a[148:151],a[20:23],0\n"
MU"v16,s55,v16\n"
MU"v17,s55,v17\n"
MF"v[120:123],a[144:147],a[4:7],v[120:123]\n"
RT"a[166:167],v236 offset:61952\n"
MU"v18,s55,v18\n"
MF"v[128:131],a[144:147],a[8:11],0\n"
MU"v19,s55,v19\n"
MU"v24,s55,v24\n"
MF"v[128:131],a[148:151],a[24:27],v[128:131]\n"
RT"a[168:169],v237 offset:53248\n"
MU"v25,s55,v25\n"
MF"v[136:139],a[148:151],a[28:31],0\n"
MU"v26,s55,v26\n"
MU"v27,s55,v27\n"
MF"v[136:139],a[144:147],a[12:15],v[136:139]\n"
RT"a[170:171],v237 offset:53760\n"
PK"v16,v16,v17\n"
MF"v[116:119],a[152:155],a[0:3],0\n"
WT"lgkmcnt(3)\n"
BL"a[88:91],v235,s[8:11],s58 offen offset:1024 sc1\n"
MF"v[116:119],a[156:159],a[16:19],v[116:119]\n"
"s_mov_b32 s49,262144\n"
RT"a[172:173],v237 offset:61440\n"
PK"v17,v18,v19\n"
MF"v[124:127],a[156:159],a[20:23],0\n"
RT"a[174:175],v237 offset:61952\n"
PK"v18,v24,v25\n"
MF"v[124:127],a[152:155],a[4:7],v[124:127]\n"
BS"v[4:7],v250,s[8:11],s49 offen offset:0 sc1\n"
MF"v[132:135],a[152:155],a[8:11],0\n"
RT"a[176:177],v228 offset:53248\n"
PK"v19,v26,v27\n"
MF"v[132:135],a[156:159],a[24:27],v[132:135]\n"
MU"v32,s55,v32\n"
MU"v33,s55,v33\n"
MF"v[140:143],a[156:159],a[28:31],0\n"
RT"a[178:179],v228 offset:53760\n"
MU"v34,s55,v34\n"
MF"v[140:143],a[152:155],a[12:15],v[140:143]\n"
BL"a[92:95],v235,s[8:11],s59 offen offset:1024 sc1\n"
MF"v[144:147],a[160:163],a[0:3],0\n"
WT"lgkmcnt(3)\n"
RT"a[180:181],v228 offset:61440\n"
MU"v35,s55,v35\n"
MF"v[144:147],a[164:167],a[16:19],v[144:147]\n"
MU"v40,s55,v40\n"
MU"v41,s55,v41\n"
MF"v[152:155],a[164:167],a[20:23],0\n"
SA"m0,s38,122880\n"
RT"a[182:183],v228 offset:61952\n"
MU"v42,s55,v42\n"
MF"v[152:155],a[160:163],a[4:7],v[152:155]\n"
SA"s48,s60,s61\n"
MU"v43,s55,v43\n"
PK"v32,v32,v33\n"
MF"v[160:163],a[160:163],a[8:11],0\n"
"s_mov_b32 s88,4096\n"
RT"a[184:185],v229 offset:53248\n"
PK"v33,v34,v35\n"
MF"v[160:163],a[164:167],a[24:27],v[160:163]\n"
BL"v238,s[20:23],s48 offen offset:0 lds\n"
MF"v[168:171],a[164:167],a[28:31],0\n"
BL"a[96:99],v235,s[8:11],s56 offen offset:2048 sc1\n"
MF"v[168:171],a[160:163],a[12:15],v[168:171]\n"
RT"a[186:187],v229 offset:53760\n"
PK"v34,v40,v41\n"
MF"v[148:151],a[168:171],a[0:3],0\n"
WT"lgkmcnt(3)\n"
RT"a[188:189],v229 offset:61440\n"
PK"v35,v42,v43\n"
MF"v[148:151],a[172:175],a[16:19],v[148:151]\n"
MU"v48,s55,v48\n"
MU"v49,s55,v49\n"
MF"v[156:159],a[172:175],a[20:23],0\n"
RT"a[190:191],v229 offset:61952\n"
MU"v50,s55,v50\n"
MF"v[156:159],a[168:171],a[4:7],v[156:159]\n"
RT"a[128:129],v236 offset:32768\n"
MU"v51,s55,v51\n"
MF"v[164:167],a[168:171],a[8:11],0\n"
BL"v239,s[20:23],s48 offen offset:1024 lds\n"
MF"v[164:167],a[172:175],a[24:27],v[164:167]\n"
BL"a[100:103],v235,s[8:11],s57 offen offset:2048 sc1\n"
MF"v[172:175],a[172:175],a[28:31],0\n"
RT"a[130:131],v236 offset:33280\n"
MU"v56,s55,v56\n"
MF"v[172:175],a[168:171],a[12:15],v[172:175]\n"
RT"a[132:133],v236 offset:40960\n"
MU"v57,s55,v57\n"
MF"v[176:179],a[176:179],a[0:3],0\n"
WT"lgkmcnt(4)\n"
MU"v58,s55,v58\n"
MU"v59,s55,v59\n"
MF"v[176:179],a[180:183],a[16:19],v[176:179]\n"
RT"a[134:135],v236 offset:41472\n"
PK"v48,v48,v49\n"
MF"v[184:187],a[180:183],a[20:23],0\n"
PK"v49,v50,v51\n"
PK"v50,v56,v57\n"
MF"v[184:187],a[176:179],a[4:7],v[184:187]\n"
RT"a[136:137],v237 offset:32768\n"
PK"v51,v58,v59\n"
MF"v[192:195],a[176:179],a[8:11],0\n"
BL"a[104:107],v235,s[8:11],s58 offen offset:2048 sc1\n"
MF"v[192:195],a[180:183],a[24:27],v[192:195]\n"
BL"v240,s[20:23],s48 offen offset:2048 lds\n"
MF"v[200:203],a[180:183],a[28:31],0\n"
RT"a[138:139],v237 offset:33280\n"
MF"v[200:203],a[176:179],a[12:15],v[200:203]\n"
RT"a[140:141],v237 offset:40960\n"
MF"v[180:183],a[184:187],a[0:3],0\n"
WT"lgkmcnt(3)\n"
MF"v[180:183],a[188:191],a[16:19],v[180:183]\n"
RT"a[142:143],v237 offset:41472\n"
MF"v[188:191],a[188:191],a[20:23],0\n"
BL"a[108:111],v235,s[8:11],s59 offen offset:2048 sc1\n"
MF"v[188:191],a[184:187],a[4:7],v[188:191]\n"
RT"a[144:145],v228 offset:32768\n"
MF"v[196:199],a[184:187],a[8:11],0\n"
SA"s61,s61,524288\n"
BS"v[12:15],v250,s[8:11],s49 offen offset:1024 sc1\n"
MF"v[196:199],a[188:191],a[24:27],v[196:199]\n"
"s_cmp_eq_u32 s61,5242880\n"
BL"v241,s[20:23],s48 offen offset:3072 lds\n"
MF"v[204:207],a[188:191],a[28:31],0\n"
"s_cselect_b32 s61,0,s61\n"
RT"a[146:147],v228 offset:33280\n"
MF"v[204:207],a[184:187],a[12:15],v[204:207]\n"
WT"vmcnt(31)\n"
RT"a[148:149],v228 offset:40960\n"
MF"v[80:83],a[128:131],a[32:35],v[80:83]\n"
WT"vmcnt(26)\n"
MF"v[80:83],a[132:135],a[48:51],v[80:83]\n"
WT"vmcnt(25)\n"
RT"a[150:151],v228 offset:41472\n"
MF"v[88:91],a[132:135],a[52:55],v[88:91]\n"
BL"a[112:115],v235,s[8:11],s56 offen offset:3072 sc1\n"
MF"v[88:91],a[128:131],a[36:39],v[88:91]\n"
RT"a[152:153],v229 offset:32768\n"
MF"v[96:99],a[128:131],a[40:43],v[96:99]\n"
WT"vmcnt(25)\n"
RT"a[154:155],v229 offset:33280\n"
MF"v[96:99],a[132:135],a[56:59],v[96:99]\n"
WT"vmcnt(24)\n"
MF"v[104:107],a[132:135],a[60:63],v[104:107]\n"
RT"a[156:157],v229 offset:40960\n"
MF"v[104:107],a[128:131],a[44:47],v[104:107]\n"
WT"lgkmcnt(4)\n"
MF"v[84:87],a[136:139],a[32:35],v[84:87]\n"
RT"a[158:159],v229 offset:41472\n"
MF"v[84:87],a[140:143],a[48:51],v[84:87]\n"
BL"a[116:119],v235,s[8:11],s57 offen offset:3072 sc1\n"
MF"v[92:95],a[140:143],a[52:55],v[92:95]\n"
RT"a[160:161],v236 offset:36864\n"
MF"v[92:95],a[136:139],a[36:39],v[92:95]\n"
MF"v[100:103],a[136:139],a[40:43],v[100:103]\n"
RT"a[162:163],v236 offset:37376\n"
MF"v[100:103],a[140:143],a[56:59],v[100:103]\n"
MF"v[108:111],a[140:143],a[60:63],v[108:111]\n"
RT"a[164:165],v236 offset:45056\n"
MF"v[108:111],a[136:139],a[44:47],v[108:111]\n"
BL"a[120:123],v235,s[8:11],s58 offen offset:3072 sc1\n"
MF"v[112:115],a[144:147],a[32:35],v[112:115]\n"
WT"lgkmcnt(3)\n"
RT"a[166:167],v236 offset:45568\n"
MF"v[112:115],a[148:151],a[48:51],v[112:115]\n"
MF"v[120:123],a[148:151],a[52:55],v[120:123]\n"
RT"a[168:169],v237 offset:36864\n"
MF"v[120:123],a[144:147],a[36:39],v[120:123]\n"
MF"v[128:131],a[144:147],a[40:43],v[128:131]\n"
RT"a[170:171],v237 offset:37376\n"
MF"v[128:131],a[148:151],a[56:59],v[128:131]\n"
SA"s99,s99,4096\n"
RT"a[172:173],v237 offset:45056\n"
MF"v[136:139],a[148:151],a[60:63],v[136:139]\n"
"s_cmp_eq_u32 s99,20480\n"
BL"a[124:127],v235,s[8:11],s59 offen offset:3072 sc1\n"
MF"v[136:139],a[144:147],a[44:47],v[136:139]\n"
"s_cselect_b32 s99,0,s99\n"
RT"a[174:175],v237 offset:45568\n"
MF"v[116:119],a[152:155],a[32:35],v[116:119]\n"
MF"v[116:119],a[156:159],a[48:51],v[116:119]\n"
RT"a[176:177],v228 offset:36864\n"
MF"v[124:127],a[156:159],a[52:55],v[124:127]\n"
MF"v[124:127],a[152:155],a[36:39],v[124:127]\n"
RT"a[178:179],v228 offset:37376\n"
MF"v[132:135],a[152:155],a[40:43],v[132:135]\n"
"s_mov_b32 s49,266240\n"
"v_add_u32 v235,s99,v224\n"
MF"v[132:135],a[156:159],a[56:59],v[132:135]\n"
RT"a[180:181],v228 offset:45056\n"
MF"v[140:143],a[156:159],a[60:63],v[140:143]\n"
BL"a[0:3],v235,s[8:11],s56 offen offset:0 sc1\n"
MF"v[140:143],a[152:155],a[44:47],v[140:143]\n"
WT"lgkmcnt(3)\n"
BS"v[20:23],v250,s[8:11],s49 offen offset:0 sc1\n"
MF"v[144:147],a[160:163],a[32:35],v[144:147]\n"
RT"a[182:183],v228 offset:45568\n"
MF"v[144:147],a[164:167],a[48:51],v[144:147]\n"
RT"a[184:185],v229 offset:36864\n"
MF"v[152:155],a[164:167],a[52:55],v[152:155]\n"
MF"v[152:155],a[160:163],a[36:39],v[152:155]\n"
RT"a[186:187],v229 offset:37376\n"
MF"v[160:163],a[160:163],a[40:43],v[160:163]\n"
BL"a[4:7],v235,s[8:11],s57 offen offset:0 sc1\n"
MF"v[160:163],a[164:167],a[56:59],v[160:163]\n"
RT"a[188:189],v229 offset:45056\n"
MF"v[168:171],a[164:167],a[60:63],v[168:171]\n"
MF"v[168:171],a[160:163],a[44:47],v[168:171]\n"
RT"a[190:191],v229 offset:45568\n"
MF"v[148:151],a[168:171],a[32:35],v[148:151]\n"
"s_lshl_b32 s50,s35,3\n"
SA"s50,s50,s34\n"
"s_lshl_b32 s50,s50,3\n"
MF"v[148:151],a[172:175],a[48:51],v[148:151]\n"
SA"s50,s50,2304\n"
SA"s51,s50,s45\n"
MF"v[156:159],a[172:175],a[52:55],v[156:159]\n"
MF"v[156:159],a[168:171],a[36:39],v[156:159]\n"
BL"a[8:11],v235,s[8:11],s58 offen offset:0 sc1\n"
MF"v[164:167],a[168:171],a[40:43],v[164:167]\n"
MF"v[164:167],a[172:175],a[56:59],v[164:167]\n"
WT"vmcnt(29) lgkmcnt(0)\n"
"s_barrier\n"
MF"v[172:175],a[172:175],a[60:63],v[172:175]\n"
RT"a[128:129],v236 offset:0\n"
MF"v[172:175],a[168:171],a[44:47],v[172:175]\n"
"buffer_store_dwordx2 v[232:233],v230,s[8:11],s51 offen sc1\n"
MF"v[176:179],a[176:179],a[32:35],v[176:179]\n"
RT"a[130:131],v236 offset:512\n"
MF"v[176:179],a[180:183],a[48:51],v[176:179]\n"
MF"v[184:187],a[180:183],a[52:55],v[184:187]\n"
SA"m0,s38,81920\n"
RT"a[132:133],v236 offset:8192\n"
SA"s48,s60,s61\n"
MF"v[184:187],a[176:179],a[36:39],v[184:187]\n"
BL"a[12:15],v235,s[8:11],s59 offen offset:0 sc1\n"
MF"v[192:195],a[176:179],a[40:43],v[192:195]\n"
RT"a[134:135],v236 offset:8704\n"
MF"v[192:195],a[180:183],a[56:59],v[192:195]\n"
BL"v238,s[20:23],s48 offen offset:0 lds\n"
MF"v[200:203],a[180:183],a[60:63],v[200:203]\n"
RT"a[136:137],v237 offset:0\n"
MF"v[200:203],a[176:179],a[44:47],v[200:203]\n"
MF"v[180:183],a[184:187],a[32:35],v[180:183]\n"
RT"a[138:139],v237 offset:512\n"
MF"v[180:183],a[188:191],a[48:51],v[180:183]\n"
BL"a[16:19],v235,s[8:11],s56 offen offset:1024 sc1\n"
MF"v[188:191],a[188:191],a[52:55],v[188:191]\n"
RT"a[140:141],v237 offset:8192\n"
MF"v[188:191],a[184:187],a[36:39],v[188:191]\n"
MF"v[196:199],a[184:187],a[40:43],v[196:199]\n"
RT"a[142:143],v237 offset:8704\n"
MF"v[196:199],a[188:191],a[56:59],v[196:199]\n"
BL"v239,s[20:23],s48 offen offset:1024 lds\n"
MF"v[204:207],a[188:191],a[60:63],v[204:207]\n"
SA"s49,s88,262144\n"
RT"a[144:145],v228 offset:0\n"
MF"v[204:207],a[184:187],a[44:47],v[204:207]\n"
"s_branch LQ_%=\n"
"LV_%=:\n"
WT"vmcnt(17)\n"
"v_lshlrev_b32 v148,16,v128\n"
"v_and_b32 v149,0xffff0000,v128\n"
"v_lshlrev_b32 v150,16,v129\n"
"v_and_b32 v151,0xffff0000,v129\n"
WT"vmcnt(16)\n"
"v_lshlrev_b32 v152,16,v130\n"
"v_and_b32 v153,0xffff0000,v130\n"
"v_lshlrev_b32 v154,16,v131\n"
"v_and_b32 v155,0xffff0000,v131\n"
WT"vmcnt(15)\n"
"v_lshlrev_b32 v156,16,v132\n"
"v_and_b32 v157,0xffff0000,v132\n"
"v_lshlrev_b32 v158,16,v133\n"
"v_and_b32 v159,0xffff0000,v133\n"
WT"vmcnt(14)\n"
"v_lshlrev_b32 v160,16,v134\n"
"v_and_b32 v161,0xffff0000,v134\n"
"v_lshlrev_b32 v162,16,v135\n"
"v_and_b32 v163,0xffff0000,v135\n"
WT"vmcnt(13)\n"
"v_lshlrev_b32 v164,16,v136\n"
"v_and_b32 v165,0xffff0000,v136\n"
"v_lshlrev_b32 v166,16,v137\n"
"v_and_b32 v167,0xffff0000,v137\n"
"v_add_f32 v188,v148,v149\n"
"v_add_f32 v189,v150,v151\n"
"v_add_f32 v188,v188,v152\n"
"v_add_f32 v189,v189,v153\n"
"v_add_f32 v188,v188,v154\n"
"v_add_f32 v189,v189,v155\n"
"v_add_f32 v188,v188,v156\n"
"v_add_f32 v189,v189,v157\n"
"v_add_f32 v188,v188,v158\n"
"v_add_f32 v189,v189,v159\n"
"v_add_f32 v188,v188,v160\n"
"v_add_f32 v189,v189,v161\n"
"v_add_f32 v188,v188,v162\n"
"v_add_f32 v189,v189,v163\n"
"v_add_f32 v188,v188,v164\n"
"v_add_f32 v189,v189,v165\n"
"v_add_f32 v188,v188,v166\n"
"v_add_f32 v189,v189,v167\n"
"v_add_f32 v188,v188,v189\n"
"s_nop 1\n"
"v_add_f32_dpp v188,v188,v188 quad_perm:[1,0,3,2] row_mask:0xf bank_mask:0xf\n"
WT"vmcnt(12)\n"
"v_lshlrev_b32 v168,16,v138\n"
"v_and_b32 v169,0xffff0000,v138\n"
"v_add_f32_dpp v188,v188,v188 quad_perm:[2,3,0,1] row_mask:0xf bank_mask:0xf\n"
"v_lshlrev_b32 v170,16,v139\n"
"v_and_b32 v171,0xffff0000,v139\n"
"v_add_f32_dpp v188,v188,v188 row_half_mirror row_mask:0xf bank_mask:0xf\n"
WT"vmcnt(11)\n"
"v_lshlrev_b32 v172,16,v140\n"
SA"m0,s36,0\n"
SA"s48,s46,0\n"
"s_and_b32 s48,s48,1023\n"
BL"v234,s[12:15],s48 offen offset:0 lds\n"
BL"v235,s[12:15],s48 offen offset:1024 lds\n"
BL"v236,s[12:15],s48 offen offset:2048 lds\n"
BL"v237,s[12:15],s48 offen offset:3072 lds\n"
"v_and_b32 v173,0xffff0000,v140\n"
"v_add_f32_dpp v188,v188,v188 row_mirror row_mask:0xf bank_mask:0xf\n"
"v_lshlrev_b32 v174,16,v141\n"
"v_and_b32 v175,0xffff0000,v141\n"
"v_mov_b32 v189,v188\n"
WT"vmcnt(14)\n"
"v_lshlrev_b32 v176,16,v142\n"
"v_and_b32 v177,0xffff0000,v142\n"
"v_permlane16_swap_b32 v189,v188\n"
"v_lshlrev_b32 v178,16,v143\n"
"v_and_b32 v179,0xffff0000,v143\n"
"v_add_f32 v188,v188,v189\n"
WT"vmcnt(13)\n"
"v_lshlrev_b32 v180,16,v144\n"
"v_and_b32 v181,0xffff0000,v144\n"
MU"v191,0x3acccccd,v188\n"
"v_lshlrev_b32 v182,16,v145\n"
"v_and_b32 v183,0xffff0000,v145\n"
PK"v128,v191,v191\n"
WT"vmcnt(12)\n"
"v_lshlrev_b32 v184,16,v146\n"
"v_and_b32 v185,0xffff0000,v146\n"
"v_lshlrev_b32 v128,16,v128\n"
"v_lshlrev_b32 v186,16,v147\n"
"v_and_b32 v187,0xffff0000,v147\n"
"v_sub_f32 v148,v148,v128\n"
"v_sub_f32 v149,v149,v128\n"
"v_sub_f32 v150,v150,v128\n"
"v_sub_f32 v151,v151,v128\n"
"v_sub_f32 v152,v152,v128\n"
"v_sub_f32 v153,v153,v128\n"
"v_sub_f32 v154,v154,v128\n"
"v_sub_f32 v155,v155,v128\n"
"v_sub_f32 v156,v156,v128\n"
"v_sub_f32 v157,v157,v128\n"
"v_sub_f32 v158,v158,v128\n"
"v_sub_f32 v159,v159,v128\n"
"v_sub_f32 v160,v160,v128\n"
"v_sub_f32 v161,v161,v128\n"
"v_sub_f32 v162,v162,v128\n"
"v_sub_f32 v163,v163,v128\n"
"v_sub_f32 v164,v164,v128\n"
"v_sub_f32 v165,v165,v128\n"
"v_sub_f32 v166,v166,v128\n"
"v_sub_f32 v167,v167,v128\n"
MU"v188,v148,v148\n"
MU"v189,v149,v149\n"
"v_fmac_f32 v188,v150,v150\n"
"v_fmac_f32 v189,v151,v151\n"
"v_fmac_f32 v188,v152,v152\n"
SA"m0,s37,8192\n"
SA"s48,s47,524288\n"
"s_and_b32 s48,s48,4194303\n"
BL"v238,s[16:19],s48 offen offset:0 lds\n"
BL"v239,s[16:19],s48 offen offset:1024 lds\n"
BL"v240,s[16:19],s48 offen offset:2048 lds\n"
BL"v241,s[16:19],s48 offen offset:3072 lds\n"
"v_fmac_f32 v189,v153,v153\n"
"v_fmac_f32 v188,v154,v154\n"
"v_fmac_f32 v189,v155,v155\n"
"v_fmac_f32 v188,v156,v156\n"
"v_fmac_f32 v189,v157,v157\n"
"v_fmac_f32 v188,v158,v158\n"
"v_fmac_f32 v189,v159,v159\n"
"v_fmac_f32 v188,v160,v160\n"
"v_fmac_f32 v189,v161,v161\n"
"v_fmac_f32 v188,v162,v162\n"
"v_fmac_f32 v189,v163,v163\n"
"v_fmac_f32 v188,v164,v164\n"
"v_fmac_f32 v189,v165,v165\n"
"v_fmac_f32 v188,v166,v166\n"
"v_fmac_f32 v189,v167,v167\n"
"v_add_f32 v188,v188,v189\n"
PK"v149,v148,v149\n"
"v_lshlrev_b32 v148,16,v149\n"
"v_add_f32_dpp v188,v188,v188 quad_perm:[1,0,3,2] row_mask:0xf bank_mask:0xf\n"
"v_and_b32 v149,0xffff0000,v149\n"
PK"v151,v150,v151\n"
"v_add_f32_dpp v188,v188,v188 quad_perm:[2,3,0,1] row_mask:0xf bank_mask:0xf\n"
"v_lshlrev_b32 v150,16,v151\n"
"v_and_b32 v151,0xffff0000,v151\n"
"v_add_f32_dpp v188,v188,v188 row_half_mirror row_mask:0xf bank_mask:0xf\n"
PK"v153,v152,v153\n"
"v_lshlrev_b32 v152,16,v153\n"
"v_add_f32_dpp v188,v188,v188 row_mirror row_mask:0xf bank_mask:0xf\n"
"v_and_b32 v153,0xffff0000,v153\n"
PK"v155,v154,v155\n"
"v_mov_b32 v189,v188\n"
"v_lshlrev_b32 v154,16,v155\n"
"v_and_b32 v155,0xffff0000,v155\n"
"v_permlane16_swap_b32 v189,v188\n"
PK"v157,v156,v157\n"
"v_lshlrev_b32 v156,16,v157\n"
"v_add_f32 v188,v188,v189\n"
"v_and_b32 v157,0xffff0000,v157\n"
PK"v159,v158,v159\n"
"v_sub_f32 v191,v191,v128\n"
"v_lshlrev_b32 v158,16,v159\n"
"v_and_b32 v159,0xffff0000,v159\n"
MU"v188,0x3acccccd,v188\n"
PK"v161,v160,v161\n"
"v_lshlrev_b32 v160,16,v161\n"
"v_fma_f32 v188,-v191,v191,v188\n"
"v_and_b32 v161,0xffff0000,v161\n"
PK"v163,v162,v163\n"
SA"m0,s37,12288\n"
SA"s48,s47,786432\n"
"s_and_b32 s48,s48,4194303\n"
BL"v238,s[16:19],s48 offen offset:0 lds\n"
BL"v239,s[16:19],s48 offen offset:1024 lds\n"
BL"v240,s[16:19],s48 offen offset:2048 lds\n"
BL"v241,s[16:19],s48 offen offset:3072 lds\n"
"v_max_f32 v188,0,v188\n"
"v_lshlrev_b32 v162,16,v163\n"
"v_and_b32 v163,0xffff0000,v163\n"
PK"v188,v188,v188\n"
PK"v165,v164,v165\n"
"v_lshlrev_b32 v164,16,v165\n"
"v_lshlrev_b32 v188,16,v188\n"
"v_and_b32 v165,0xffff0000,v165\n"
PK"v167,v166,v167\n"
"v_add_f32 v188,0x3727c5ac,v188\n"
"v_lshlrev_b32 v166,16,v167\n"
"v_and_b32 v167,0xffff0000,v167\n"
PK"v188,v188,v188\n"
"v_bfe_u32 v130,v224,5,4\n"
"v_mul_u32_u24 v130,144,v130\n"
"v_lshlrev_b32 v188,16,v188\n"
"v_bfe_u32 v131,v224,4,1\n"
"v_lshl_add_u32 v130,v131,3,v130\n"
"v_sqrt_f32 v189,v188\n"
"v_lshrrev_b32 v131,9,v224\n"
"v_lshl_add_u32 v130,v131,4,v130\n"
"v_rcp_f32 v190,v189\n"
"s_lshl_b32 s52,s30,5\n"
"v_add_u32 v130,s52,v130\n"
"v_fma_f32 v188,-v189,v189,v188\n"
MU"v190,0.5,v190\n"
"v_fma_f32 v189,v188,v190,v189\n"
PK"v189,v189,v189\n"
"v_lshlrev_b32 v189,16,v189\n"
"v_rcp_f32 v129,v189\n"
"s_nop 0\n"
MU"v148,v148,v129\n"
MU"v149,v149,v129\n"
PK"v138,v148,v149\n"
MU"v150,v150,v129\n"
MU"v151,v151,v129\n"
PK"v139,v150,v151\n"
"ds_write_b64 v130,v[138:139] offset:49152\n"
MU"v152,v152,v129\n"
MU"v153,v153,v129\n"
PK"v140,v152,v153\n"
MU"v154,v154,v129\n"
MU"v155,v155,v129\n"
PK"v141,v154,v155\n"
"ds_write_b64 v130,v[140:141] offset:51456\n"
MU"v156,v156,v129\n"
MU"v157,v157,v129\n"
PK"v142,v156,v157\n"
SA"m0,s36,16384\n"
SA"s48,s46,128\n"
"s_and_b32 s48,s48,1023\n"
BL"v234,s[12:15],s48 offen offset:0 lds\n"
BL"v235,s[12:15],s48 offen offset:1024 lds\n"
BL"v236,s[12:15],s48 offen offset:2048 lds\n"
BL"v237,s[12:15],s48 offen offset:3072 lds\n"
MU"v158,v158,v129\n"
MU"v159,v159,v129\n"
PK"v143,v158,v159\n"
"ds_write_b64 v130,v[142:143] offset:53760\n"
MU"v160,v160,v129\n"
MU"v161,v161,v129\n"
PK"v144,v160,v161\n"
MU"v162,v162,v129\n"
MU"v163,v163,v129\n"
PK"v145,v162,v163\n"
"ds_write_b64 v130,v[144:145] offset:56064\n"
MU"v164,v164,v129\n"
MU"v165,v165,v129\n"
PK"v146,v164,v165\n"
MU"v166,v166,v129\n"
MU"v167,v167,v129\n"
PK"v147,v166,v167\n"
"ds_write_b64 v130,v[146:147] offset:58368\n"
"v_lshlrev_b32 v148,16,v138\n"
"v_and_b32 v149,0xffff0000,v138\n"
MU"v188,v148,v168\n"
MU"v189,v149,v169\n"
"v_lshlrev_b32 v150,16,v139\n"
"v_and_b32 v151,0xffff0000,v139\n"
"v_fmac_f32 v188,v150,v170\n"
"v_fmac_f32 v189,v151,v171\n"
"v_lshlrev_b32 v152,16,v140\n"
"v_and_b32 v153,0xffff0000,v140\n"
"v_fmac_f32 v188,v152,v172\n"
"v_fmac_f32 v189,v153,v173\n"
"v_lshlrev_b32 v154,16,v141\n"
"v_and_b32 v155,0xffff0000,v141\n"
"v_fmac_f32 v188,v154,v174\n"
"v_fmac_f32 v189,v155,v175\n"
"v_lshlrev_b32 v156,16,v142\n"
"v_and_b32 v157,0xffff0000,v142\n"
"v_fmac_f32 v188,v156,v176\n"
"v_fmac_f32 v189,v157,v177\n"
"v_lshlrev_b32 v158,16,v143\n"
"v_and_b32 v159,0xffff0000,v143\n"
"v_fmac_f32 v188,v158,v178\n"
"v_fmac_f32 v189,v159,v179\n"
"v_lshlrev_b32 v160,16,v144\n"
"v_and_b32 v161,0xffff0000,v144\n"
"v_fmac_f32 v188,v160,v180\n"
"v_fmac_f32 v189,v161,v181\n"
"v_lshlrev_b32 v162,16,v145\n"
"v_and_b32 v163,0xffff0000,v145\n"
WT"lgkmcnt(0)\n"
SA"s28,s28,0x7f4a7c15\n"
"s_addc_u32 s29,s29,0x9e3779b9\n"
"v_mov_b32 v232,s28\n"
"v_mov_b32 v233,s29\n"
WT"vmcnt(20)\n"
RT"a[0:1],v246 offset:0\n"
RT"a[2:3],v246 offset:512\n"
RT"a[4:5],v247 offset:0\n"
RT"a[6:7],v247 offset:512\n"
RT"a[8:9],v248 offset:0\n"
RT"a[10:11],v248 offset:512\n"
RT"a[12:13],v249 offset:0\n"
RT"a[14:15],v249 offset:512\n"
WT"vmcnt(16)\n"
RT"a[16:17],v246 offset:4096\n"
RT"a[18:19],v246 offset:4608\n"
RT"a[20:21],v247 offset:4096\n"
RT"a[22:23],v247 offset:4608\n"
RT"a[24:25],v248 offset:4096\n"
RT"a[26:27],v248 offset:4608\n"
RT"a[28:29],v249 offset:4096\n"
RT"a[30:31],v249 offset:4608\n"
WT"vmcnt(12) lgkmcnt(15)\n"
"s_barrier\n"
RD"v[192:195],v242 offset:0\n"
RD"v[196:199],v243 offset:0\n"
RD"v[200:203],v242 offset:2048\n"
RD"v[204:207],v243 offset:2048\n"
RD"v[208:211],v242 offset:4096\n"
"v_lshrrev_b32 v131,7,v224\n"
WT"lgkmcnt(4)\n"
MF"v[0:3],v[192:195],a[0:3],0\n"
WT"lgkmcnt(3)\n"
RD"v[212:215],v243 offset:4096\n"
"v_lshl_add_u32 v131,v131,4,v224\n"
MF"v[0:3],v[196:199],a[16:19],v[0:3]\n"
SA"m0,s37,16384\n"
SA"s48,s47,1048576\n"
"s_and_b32 s48,s48,4194303\n"
MF"v[8:11],v[196:199],a[20:23],0\n"
"s_mul_i32 s52,s30,1152\n"
BL"v238,s[16:19],s48 offen offset:0 lds\n"
MF"v[8:11],v[192:195],a[4:7],v[8:11]\n"
"v_add_u32 v128,s52,v131\n"
"s_and_b32 s52,s30,1\n"
MF"v[16:19],v[192:195],a[8:11],0\n"
"s_mul_i32 s51,s52,1152\n"
RD"v[216:219],v242 offset:6144\n"
"v_add_u32 v131,s51,v131\n"
MF"v[16:19],v[196:199],a[24:27],v[16:19]\n"
"s_lshl_b32 s52,s52,10\n"
"v_fmac_f32 v188,v162,v182\n"
"v_fmac_f32 v189,v163,v183\n"
MF"v[24:27],v[196:199],a[28:31],0\n"
BL"v239,s[16:19],s48 offen offset:1024 lds\n"
MF"v[24:27],v[192:195],a[12:15],v[24:27]\n"
WT"lgkmcnt(1)\n"
RD"v[220:223],v243 offset:6144\n"
RD"v[132:135],v128 offset:49152\n"
MF"v[4:7],v[200:203],a[0:3],0\n"
WT"vmcnt(10)\n"
RT"a[32:33],v246 offset:8192\n"
RD"a[240:243],v128 offset:53760\n"
MF"v[4:7],v[204:207],a[16:19],v[4:7]\n"
"s_lshr_b32 s50,s31,1\n"
RD"a[244:247],v131 offset:58368\n"
"v_lshlrev_b32 v164,16,v146\n"
MF"v[12:15],v[204:207],a[20:23],0\n"
"s_mul_i32 s50,s50,20480\n"
"v_and_b32 v129,0x70,v224\n"
"v_and_b32 v165,0xffff0000,v146\n"
MF"v[12:15],v[200:203],a[4:7],v[12:15]\n"
"s_and_b32 s51,s31,1\n"
RD"v[192:195],v242 offset:8192\n"
RT"a[34:35],v246 offset:8704\n"
MF"v[20:23],v[200:203],a[8:11],0\n"
"s_lshl_b32 s51,s51,7\n"
BL"v240,s[16:19],s48 offen offset:2048 lds\n"
MF"v[20:23],v[204:207],a[24:27],v[20:23]\n"
SA"s50,s50,s51\n"
"v_and_b32 v130,0x380,v224\n"
"v_fmac_f32 v188,v164,v184\n"
MF"v[28:31],v[204:207],a[28:31],0\n"
SA"s50,s50,65536\n"
RT"a[36:37],v247 offset:8192\n"
"v_lshl_add_u32 v129,v130,1,v129\n"
MF"v[28:31],v[200:203],a[12:15],v[28:31]\n"
"s_lshl_b32 s52,s52,1\n"
RD"v[196:199],v243 offset:8192\n"
"v_fmac_f32 v189,v165,v185\n"
MF"v[32:35],v[208:211],a[0:3],0\n"
SA"s52,s52,s50\n"
BL"v241,s[16:19],s48 offen offset:3072 lds\n"
MF"v[32:35],v[212:215],a[16:19],v[32:35]\n"
SA"s52,s52,16384\n"
RT"a[38:39],v247 offset:8704\n"
"v_lshlrev_b32 v166,16,v147\n"
MF"v[40:43],v[212:215],a[20:23],0\n"
SA"m0,s37,20480\n"
RD"v[200:203],v242 offset:10240\n"
"v_and_b32 v167,0xffff0000,v147\n"
MF"v[40:43],v[208:211],a[4:7],v[40:43]\n"
SA"s48,s47,1310720\n"
"v_fmac_f32 v188,v166,v186\n"
"v_fmac_f32 v189,v167,v187\n"
MF"v[48:51],v[208:211],a[8:11],0\n"
"s_and_b32 s48,s48,4194303\n"
RT"a[40:41],v248 offset:8192\n"
"v_add_f32 v188,v188,v189\n"
MF"v[48:51],v[212:215],a[24:27],v[48:51]\n"
"s_lshl_b32 s51,s30,11\n"
BL"v238,s[16:19],s48 offen offset:0 lds\n"
MF"v[56:59],v[212:215],a[28:31],0\n"
SA"s50,s50,s51\n"
RD"v[204:207],v243 offset:10240\n"
"v_add_f32_dpp v188,v188,v188 quad_perm:[1,0,3,2] row_mask:0xf bank_mask:0xf\n"
MF"v[56:59],v[208:211],a[12:15],v[56:59]\n"
WT"lgkmcnt(4)\n"
BS"v[132:135],v129,s[8:11],s50 offen sc1\n"
MF"v[36:39],v[216:219],a[0:3],0\n"
SA"s51,s50,8192\n"
RT"a[42:43],v248 offset:8704\n"
"v_add_f32_dpp v188,v188,v188 quad_perm:[2,3,0,1] row_mask:0xf bank_mask:0xf\n"
MF"v[36:39],v[220:223],a[16:19],v[36:39]\n"
"s_lshl_b32 s90,s31,2\n"
BL"v239,s[16:19],s48 offen offset:1024 lds\n"
MF"v[44:47],v[220:223],a[20:23],0\n"
SA"s90,s90,s30\n"
BS"a[240:243],v129,s[8:11],s51 offen sc1\n"
MF"v[44:47],v[216:219],a[4:7],v[44:47]\n"
"s_lshl_b32 s90,s90,1\n"
RD"v[208:211],v242 offset:12288\n"
RT"a[44:45],v249 offset:8192\n"
MF"v[52:55],v[216:219],a[8:11],0\n"
"s_sub_u32 s91,2047,s90\n"
BS"a[244:247],v129,s[8:11],s52 offen sc1\n"
MF"v[52:55],v[220:223],a[24:27],v[52:55]\n"
RD"v[212:215],v243 offset:12288\n"
RT"a[46:47],v249 offset:8704\n"
MF"v[60:63],v[220:223],a[28:31],0\n"
BL"v240,s[16:19],s48 offen offset:2048 lds\n"
MF"v[60:63],v[216:219],a[12:15],v[60:63]\n"
"v_add_f32_dpp v188,v188,v188 row_half_mirror row_mask:0xf bank_mask:0xf\n"
MF"v[64:67],v[192:195],a[0:3],0\n"
RD"v[216:219],v242 offset:14336\n"
"v_add_f32_dpp v188,v188,v188 row_mirror row_mask:0xf bank_mask:0xf\n"
MF"v[64:67],v[196:199],a[16:19],v[64:67]\n"
WT"vmcnt(14)\n"
RT"a[48:49],v246 offset:12288\n"
"v_mov_b32 v189,v188\n"
MF"v[72:75],v[196:199],a[20:23],0\n"
"s_lshl_b32 s90,s90,2\n"
BL"v241,s[16:19],s48 offen offset:3072 lds\n"
MF"v[72:75],v[192:195],a[4:7],v[72:75]\n"
SA"s90,s90,2686976\n"
RT"a[50:51],v246 offset:12800\n"
"v_permlane16_swap_b32 v189,v188\n"
MF"v[80:83],v[192:195],a[8:11],0\n"
SA"m0,s36,32768\n"
RD"v[220:223],v243 offset:14336\n"
"v_add_f32 v188,v188,v189\n"
MF"v[80:83],v[196:199],a[24:27],v[80:83]\n"
SA"s48,s46,256\n"
PK"v188,v188,v188\n"
"v_lshlrev_b32 v188,16,v188\n"
MF"v[88:91],v[196:199],a[28:31],0\n"
"s_and_b32 s48,s48,1023\n"
RT"a[52:53],v247 offset:12288\n"
"v_ashrrev_i32 v189,31,v188\n"
MF"v[88:91],v[192:195],a[12:15],v[88:91]\n"
WT"lgkmcnt(4)\n"
BL"v234,s[12:15],s48 offen offset:0 lds\n"
MF"v[68:71],v[200:203],a[0:3],0\n"
"v_or_b32 v189,0x80000000,v189\n"
"v_xor_b32 v188,v188,v189\n"
MF"v[68:71],v[204:207],a[16:19],v[68:71]\n"
RT"a[54:55],v247 offset:12800\n"
"v_and_b32 v188,0xffff0000,v188\n"
MF"v[76:79],v[204:207],a[20:23],0\n"
BL"v235,s[12:15],s48 offen offset:1024 lds\n"
MF"v[76:79],v[200:203],a[4:7],v[76:79]\n"
"v_lshrrev_b32 v189,9,v224\n"
"v_sub_u32 v190,s91,v189\n"
MF"v[84:87],v[200:203],a[8:11],0\n"
RT"a[56:57],v248 offset:12288\n"
"v_or_b32 v188,v188,v190\n"
MF"v[84:87],v[204:207],a[24:27],v[84:87]\n"
WT"vmcnt(13) lgkmcnt(3)\n"
"s_barrier\n"
MF"v[92:95],v[204:207],a[28:31],0\n"
RD"v[192:195],v242 offset:16384\n"
"v_and_b32 v190,0x1f0,v224\n"
MF"v[92:95],v[200:203],a[12:15],v[92:95]\n"
RT"a[58:59],v248 offset:12800\n"
"v_lshlrev_b32 v190,23,v190\n"
MF"v[96:99],v[208:211],a[0:3],0\n"
BL"v236,s[12:15],s48 offen offset:2048 lds\n"
MF"v[96:99],v[212:215],a[16:19],v[96:99]\n"
"v_lshl_add_u32 v190,v189,2,v190\n"
MF"v[104:107],v[212:215],a[20:23],0\n"
"buffer_store_dword v188,v190,s[8:11],s90 offen sc1\n"
MF"v[104:107],v[208:211],a[4:7],v[104:107]\n"
RD"v[196:199],v243 offset:16384\n"
RT"a[60:61],v249 offset:12288\n"
MF"v[112:115],v[208:211],a[8:11],0\n"
BL"v237,s[12:15],s48 offen offset:3072 lds\n"
MF"v[112:115],v[212:215],a[24:27],v[112:115]\n"
RT"a[62:63],v249 offset:12800\n"
MF"v[120:123],v[212:215],a[28:31],0\n"
SA"m0,s37,0\n"
RD"v[200:203],v242 offset:18432\n"
SA"s48,s47,1572864\n"
MF"v[120:123],v[208:211],a[12:15],v[120:123]\n"
"s_and_b32 s48,s48,4194303\n"
MF"v[100:103],v[216:219],a[0:3],0\n"
BL"v238,s[16:19],s48 offen offset:0 lds\n"
MF"v[100:103],v[220:223],a[16:19],v[100:103]\n"
RD"v[204:207],v243 offset:18432\n"
MF"v[108:111],v[220:223],a[20:23],0\n"
MF"v[108:111],v[216:219],a[4:7],v[108:111]\n"
MF"v[116:119],v[216:219],a[8:11],0\n"
BL"v239,s[16:19],s48 offen offset:1024 lds\n"
MF"v[116:119],v[220:223],a[24:27],v[116:119]\n"
RD"v[208:211],v242 offset:20480\n"
MF"v[124:127],v[220:223],a[28:31],0\n"
MF"v[124:127],v[216:219],a[12:15],v[124:127]\n"
WT"lgkmcnt(2)\n"
MF"v[0:3],v[192:195],a[32:35],v[0:3]\n"
RD"v[212:215],v243 offset:20480\n"
MF"v[0:3],v[196:199],a[48:51],v[0:3]\n"
BL"v240,s[16:19],s48 offen offset:2048 lds\n"
MF"v[8:11],v[196:199],a[52:55],v[8:11]\n"
MF"v[8:11],v[192:195],a[36:39],v[8:11]\n"
MF"v[16:19],v[192:195],a[40:43],v[16:19]\n"
RD"v[216:219],v242 offset:22528\n"
MF"v[16:19],v[196:199],a[56:59],v[16:19]\n"
BL"v241,s[16:19],s48 offen offset:3072 lds\n"
MF"v[24:27],v[196:199],a[60:63],v[24:27]\n"
MF"v[24:27],v[192:195],a[44:47],v[24:27]\n"
RD"v[220:223],v243 offset:22528\n"
MF"v[4:7],v[200:203],a[32:35],v[4:7]\n"
WT"lgkmcnt(2)\n"
WT"vmcnt(16)\n"
RT"a[64:65],v246 offset:16384\n"
MF"v[4:7],v[204:207],a[48:51],v[4:7]\n"
SA"m0,s37,4096\n"
SA"s48,s47,1835008\n"
"s_and_b32 s48,s48,4194303\n"
MF"v[12:15],v[204:207],a[52:55],v[12:15]\n"
BL"v238,s[16:19],s48 offen offset:0 lds\n"
MF"v[12:15],v[200:203],a[36:39],v[12:15]\n"
RD"v[192:195],v242 offset:24576\n"
RT"a[66:67],v246 offset:16896\n"
MF"v[20:23],v[200:203],a[40:43],v[20:23]\n"
MF"v[20:23],v[204:207],a[56:59],v[20:23]\n"
MF"v[28:31],v[204:207],a[60:63],v[28:31]\n"
RT"a[68:69],v247 offset:16384\n"
MF"v[28:31],v[200:203],a[44:47],v[28:31]\n"
BL"v239,s[16:19],s48 offen offset:1024 lds\n"
MF"v[32:35],v[208:211],a[32:35],v[32:35]\n"
RD"v[196:199],v243 offset:24576\n"
MF"v[32:35],v[212:215],a[48:51],v[32:35]\n"
RT"a[70:71],v247 offset:16896\n"
MF"v[40:43],v[212:215],a[52:55],v[40:43]\n"
RD"v[200:203],v242 offset:26624\n"
MF"v[40:43],v[208:211],a[36:39],v[40:43]\n"
BL"v240,s[16:19],s48 offen offset:2048 lds\n"
MF"v[48:51],v[208:211],a[40:43],v[48:51]\n"
RT"a[72:73],v248 offset:16384\n"
MF"v[48:51],v[212:215],a[56:59],v[48:51]\n"
MF"v[56:59],v[212:215],a[60:63],v[56:59]\n"
RD"v[204:207],v243 offset:26624\n"
MF"v[56:59],v[208:211],a[44:47],v[56:59]\n"
WT"lgkmcnt(4)\n"
RT"a[74:75],v248 offset:16896\n"
MF"v[36:39],v[216:219],a[32:35],v[36:39]\n"
BL"v241,s[16:19],s48 offen offset:3072 lds\n"
MF"v[36:39],v[220:223],a[48:51],v[36:39]\n"
RD"v[208:211],v242 offset:28672\n"
MF"v[44:47],v[220:223],a[52:55],v[44:47]\n"
SA"m0,s36,0\n"
RT"a[76:77],v249 offset:16384\n"
SA"s48,s46,384\n"
MF"v[44:47],v[216:219],a[36:39],v[44:47]\n"
"s_and_b32 s48,s48,1023\n"
MF"v[52:55],v[216:219],a[40:43],v[52:55]\n"
BL"v234,s[12:15],s48 offen offset:0 lds\n"
MF"v[52:55],v[220:223],a[56:59],v[52:55]\n"
RD"v[212:215],v243 offset:28672\n"
RT"a[78:79],v249 offset:16896\n"
MF"v[60:63],v[220:223],a[60:63],v[60:63]\n"
MF"v[60:63],v[216:219],a[44:47],v[60:63]\n"
MF"v[64:67],v[192:195],a[32:35],v[64:67]\n"
RD"v[216:219],v242 offset:30720\n"
MF"v[64:67],v[196:199],a[48:51],v[64:67]\n"
WT"vmcnt(14)\n"
RT"a[80:81],v246 offset:20480\n"
MF"v[72:75],v[196:199],a[52:55],v[72:75]\n"
BL"v235,s[12:15],s48 offen offset:1024 lds\n"
MF"v[72:75],v[192:195],a[36:39],v[72:75]\n"
RT"a[82:83],v246 offset:20992\n"
MF"v[80:83],v[192:195],a[40:43],v[80:83]\n"
RD"v[220:223],v243 offset:30720\n"
MF"v[80:83],v[196:199],a[56:59],v[80:83]\n"
BL"v236,s[12:15],s48 offen offset:2048 lds\n"
MF"v[88:91],v[196:199],a[60:63],v[88:91]\n"
RT"a[84:85],v247 offset:20480\n"
MF"v[88:91],v[192:195],a[44:47],v[88:91]\n"
WT"lgkmcnt(4)\n"
MF"v[68:71],v[200:203],a[32:35],v[68:71]\n"
"s_lshl_b32 s51,s32,8\n"
"s_andn2_b32 s50,s31,7\n"
SA"s50,s50,s51\n"
MF"v[68:71],v[204:207],a[48:51],v[68:71]\n"
SA"s50,s50,256\n"
RT"a[86:87],v247 offset:20992\n"
SA"s50,s50,s45\n"
MF"v[76:79],v[204:207],a[52:55],v[76:79]\n"
BL"v237,s[12:15],s48 offen offset:3072 lds\n"
MF"v[76:79],v[200:203],a[36:39],v[76:79]\n"
MF"v[84:87],v[200:203],a[40:43],v[84:87]\n"
SA"m0,s37,8192\n"
RT"a[88:89],v248 offset:20480\n"
SA"s48,s47,2097152\n"
MF"v[84:87],v[204:207],a[56:59],v[84:87]\n"
"s_and_b32 s48,s48,4194303\n"
WT"vmcnt(12) lgkmcnt(3)\n"
"s_barrier\n"
MF"v[92:95],v[204:207],a[60:63],v[92:95]\n"
RD"v[192:195],v242 offset:32768\n"
MF"v[92:95],v[200:203],a[44:47],v[92:95]\n"
BL"v238,s[16:19],s48 offen offset:0 lds\n"
MF"v[96:99],v[208:211],a[32:35],v[96:99]\n"
"buffer_store_dwordx2 v[232:233],v230,s[8:11],s50 offen sc1\n"
MF"v[96:99],v[212:215],a[48:51],v[96:99]\n"
RT"a[90:91],v248 offset:20992\n"
MF"v[104:107],v[212:215],a[52:55],v[104:107]\n"
RD"v[196:199],v243 offset:32768\n"
RT"a[92:93],v249 offset:20480\n"
MF"v[104:107],v[208:211],a[36:39],v[104:107]\n"
BL"v239,s[16:19],s48 offen offset:1024 lds\n"
MF"v[112:115],v[208:211],a[40:43],v[112:115]\n"
MF"v[112:115],v[212:215],a[56:59],v[112:115]\n"
RT"a[94:95],v249 offset:20992\n"
MF"v[120:123],v[212:215],a[60:63],v[120:123]\n"
RD"v[200:203],v242 offset:34816\n"
MF"v[120:123],v[208:211],a[44:47],v[120:123]\n"
BL"v240,s[16:19],s48 offen offset:2048 lds\n"
MF"v[100:103],v[216:219],a[32:35],v[100:103]\n"
MF"v[100:103],v[220:223],a[48:51],v[100:103]\n"
RD"v[204:207],v243 offset:34816\n"
MF"v[108:111],v[220:223],a[52:55],v[108:111]\n"
MF"v[108:111],v[216:219],a[36:39],v[108:111]\n"
BL"v241,s[16:19],s48 offen offset:3072 lds\n"
MF"v[116:119],v[216:219],a[40:43],v[116:119]\n"
MF"v[116:119],v[220:223],a[56:59],v[116:119]\n"
RD"v[208:211],v242 offset:36864\n"
MF"v[124:127],v[220:223],a[60:63],v[124:127]\n"
SA"m0,s37,12288\n"
SA"s48,s47,2359296\n"
"s_and_b32 s48,s48,4194303\n"
MF"v[124:127],v[216:219],a[44:47],v[124:127]\n"
WT"lgkmcnt(2)\n"
MF"v[0:3],v[192:195],a[64:67],v[0:3]\n"
RD"v[212:215],v243 offset:36864\n"
MF"v[0:3],v[196:199],a[80:83],v[0:3]\n"
BL"v238,s[16:19],s48 offen offset:0 lds\n"
MF"v[8:11],v[196:199],a[84:87],v[8:11]\n"
MF"v[8:11],v[192:195],a[68:71],v[8:11]\n"
MF"v[16:19],v[192:195],a[72:75],v[16:19]\n"
RD"v[216:219],v242 offset:38912\n"
MF"v[16:19],v[196:199],a[88:91],v[16:19]\n"
BL"v239,s[16:19],s48 offen offset:1024 lds\n"
MF"v[24:27],v[196:199],a[92:95],v[24:27]\n"
MF"v[24:27],v[192:195],a[76:79],v[24:27]\n"
RD"v[220:223],v243 offset:38912\n"
MF"v[4:7],v[200:203],a[64:67],v[4:7]\n"
WT"lgkmcnt(2)\n"
WT"vmcnt(15)\n"
RT"a[96:97],v246 offset:0\n"
MF"v[4:7],v[204:207],a[80:83],v[4:7]\n"
BL"v240,s[16:19],s48 offen offset:2048 lds\n"
MF"v[12:15],v[204:207],a[84:87],v[12:15]\n"
MF"v[12:15],v[200:203],a[68:71],v[12:15]\n"
RD"v[192:195],v242 offset:40960\n"
MF"v[20:23],v[200:203],a[72:75],v[20:23]\n"
RT"a[98:99],v246 offset:512\n"
MF"v[20:23],v[204:207],a[88:91],v[20:23]\n"
BL"v241,s[16:19],s48 offen offset:3072 lds\n"
MF"v[28:31],v[204:207],a[92:95],v[28:31]\n"
RT"a[100:101],v247 offset:0\n"
MF"v[28:31],v[200:203],a[76:79],v[28:31]\n"
RD"v[196:199],v243 offset:40960\n"
MF"v[32:35],v[208:211],a[64:67],v[32:35]\n"
SA"m0,s36,16384\n"
SA"s48,s46,512\n"
"s_and_b32 s48,s48,1023\n"
MF"v[32:35],v[212:215],a[80:83],v[32:35]\n"
RT"a[102:103],v247 offset:512\n"
MF"v[40:43],v[212:215],a[84:87],v[40:43]\n"
RD"v[200:203],v242 offset:43008\n"
MF"v[40:43],v[208:211],a[68:71],v[40:43]\n"
BL"v234,s[12:15],s48 offen offset:0 lds\n"
MF"v[48:51],v[208:211],a[72:75],v[48:51]\n"
RT"a[104:105],v248 offset:0\n"
MF"v[48:51],v[212:215],a[88:91],v[48:51]\n"
MF"v[56:59],v[212:215],a[92:95],v[56:59]\n"
RD"v[204:207],v243 offset:43008\n"
MF"v[56:59],v[208:211],a[76:79],v[56:59]\n"
WT"lgkmcnt(4)\n"
BL"v235,s[12:15],s48 offen offset:1024 lds\n"
MF"v[36:39],v[216:219],a[64:67],v[36:39]\n"
RT"a[106:107],v248 offset:512\n"
MF"v[36:39],v[220:223],a[80:83],v[36:39]\n"
RD"v[208:211],v242 offset:45056\n"
MF"v[44:47],v[220:223],a[84:87],v[44:47]\n"
RT"a[108:109],v249 offset:0\n"
MF"v[44:47],v[216:219],a[68:71],v[44:47]\n"
BL"v236,s[12:15],s48 offen offset:2048 lds\n"
MF"v[52:55],v[216:219],a[72:75],v[52:55]\n"
MF"v[52:55],v[220:223],a[88:91],v[52:55]\n"
RD"v[212:215],v243 offset:45056\n"
MF"v[60:63],v[220:223],a[92:95],v[60:63]\n"
RT"a[110:111],v249 offset:512\n"
MF"v[60:63],v[216:219],a[76:79],v[60:63]\n"
BL"v237,s[12:15],s48 offen offset:3072 lds\n"
MF"v[64:67],v[192:195],a[64:67],v[64:67]\n"
RD"v[216:219],v242 offset:47104\n"
MF"v[64:67],v[196:199],a[80:83],v[64:67]\n"
WT"vmcnt(17)\n"
RT"a[112:113],v246 offset:4096\n"
MF"v[72:75],v[196:199],a[84:87],v[72:75]\n"
SA"m0,s37,16384\n"
SA"s48,s47,2621440\n"
"s_and_b32 s48,s48,4194303\n"
MF"v[72:75],v[192:195],a[68:71],v[72:75]\n"
RT"a[114:115],v246 offset:4608\n"
MF"v[80:83],v[192:195],a[72:75],v[80:83]\n"
RD"v[220:223],v243 offset:47104\n"
MF"v[80:83],v[196:199],a[88:91],v[80:83]\n"
BL"v238,s[16:19],s48 offen offset:0 lds\n"
MF"v[88:91],v[196:199],a[92:95],v[88:91]\n"
RT"a[116:117],v247 offset:4096\n"
MF"v[88:91],v[192:195],a[76:79],v[88:91]\n"
WT"lgkmcnt(4)\n"
MF"v[68:71],v[200:203],a[64:67],v[68:71]\n"
BL"v239,s[16:19],s48 offen offset:1024 lds\n"
MF"v[68:71],v[204:207],a[80:83],v[68:71]\n"
RT"a[118:119],v247 offset:4608\n"
MF"v[76:79],v[204:207],a[84:87],v[76:79]\n"
MF"v[76:79],v[200:203],a[68:71],v[76:79]\n"
MF"v[84:87],v[200:203],a[72:75],v[84:87]\n"
RT"a[120:121],v248 offset:4096\n"
MF"v[84:87],v[204:207],a[88:91],v[84:87]\n"
WT"vmcnt(15) lgkmcnt(3)\n"
"s_barrier\n"
MF"v[92:95],v[204:207],a[92:95],v[92:95]\n"
BL"v240,s[16:19],s48 offen offset:2048 lds\n"
MF"v[92:95],v[200:203],a[76:79],v[92:95]\n"
RD"v[192:195],v242 offset:0\n"
RT"a[122:123],v248 offset:4608\n"
MF"v[96:99],v[208:211],a[64:67],v[96:99]\n"
MF"v[96:99],v[212:215],a[80:83],v[96:99]\n"
BL"v241,s[16:19],s48 offen offset:3072 lds\n"
MF"v[104:107],v[212:215],a[84:87],v[104:107]\n"
RD"v[196:199],v243 offset:0\n"
RT"a[124:125],v249 offset:4096\n"
MF"v[104:107],v[208:211],a[68:71],v[104:107]\n"
MF"v[112:115],v[208:211],a[72:75],v[112:115]\n"
SA"m0,s37,20480\n"
SA"s48,s47,2883584\n"
"s_and_b32 s48,s48,4194303\n"
MF"v[112:115],v[212:215],a[88:91],v[112:115]\n"
RT"a[126:127],v249 offset:4608\n"
MF"v[120:123],v[212:215],a[92:95],v[120:123]\n"
RD"v[200:203],v242 offset:2048\n"
MF"v[120:123],v[208:211],a[76:79],v[120:123]\n"
BL"v238,s[16:19],s48 offen offset:0 lds\n"
MF"v[100:103],v[216:219],a[64:67],v[100:103]\n"
MF"v[100:103],v[220:223],a[80:83],v[100:103]\n"
RD"v[204:207],v243 offset:2048\n"
MF"v[108:111],v[220:223],a[84:87],v[108:111]\n"
BL"v239,s[16:19],s48 offen offset:1024 lds\n"
MF"v[108:111],v[216:219],a[68:71],v[108:111]\n"
MF"v[116:119],v[216:219],a[72:75],v[116:119]\n"
MF"v[116:119],v[220:223],a[88:91],v[116:119]\n"
RD"v[208:211],v242 offset:4096\n"
MF"v[124:127],v[220:223],a[92:95],v[124:127]\n"
MF"v[124:127],v[216:219],a[76:79],v[124:127]\n"
WT"lgkmcnt(2)\n"
BL"v240,s[16:19],s48 offen offset:2048 lds\n"
MF"v[0:3],v[192:195],a[96:99],v[0:3]\n"
RD"v[212:215],v243 offset:4096\n"
MF"v[0:3],v[196:199],a[112:115],v[0:3]\n"
MF"v[8:11],v[196:199],a[116:119],v[8:11]\n"
MF"v[8:11],v[192:195],a[100:103],v[8:11]\n"
BL"v241,s[16:19],s48 offen offset:3072 lds\n"
MF"v[16:19],v[192:195],a[104:107],v[16:19]\n"
RD"v[216:219],v242 offset:6144\n"
MF"v[16:19],v[196:199],a[120:123],v[16:19]\n"
MF"v[24:27],v[196:199],a[124:127],v[24:27]\n"
SA"m0,s36,32768\n"
SA"s48,s46,640\n"
"s_and_b32 s48,s48,1023\n"
MF"v[24:27],v[192:195],a[108:111],v[24:27]\n"
RD"v[220:223],v243 offset:6144\n"
MF"v[4:7],v[200:203],a[96:99],v[4:7]\n"
WT"lgkmcnt(2)\n"
WT"vmcnt(16)\n"
RT"a[128:129],v246 offset:8192\n"
MF"v[4:7],v[204:207],a[112:115],v[4:7]\n"
BL"v234,s[12:15],s48 offen offset:0 lds\n"
MF"v[12:15],v[204:207],a[116:119],v[12:15]\n"
MF"v[12:15],v[200:203],a[100:103],v[12:15]\n"
RD"v[192:195],v242 offset:8192\n"
MF"v[20:23],v[200:203],a[104:107],v[20:23]\n"
RT"a[130:131],v246 offset:8704\n"
MF"v[20:23],v[204:207],a[120:123],v[20:23]\n"
BL"v235,s[12:15],s48 offen offset:1024 lds\n"
MF"v[28:31],v[204:207],a[124:127],v[28:31]\n"
RT"a[132:133],v247 offset:8192\n"
MF"v[28:31],v[200:203],a[108:111],v[28:31]\n"
RD"v[196:199],v243 offset:8192\n"
MF"v[32:35],v[208:211],a[96:99],v[32:35]\n"
MF"v[32:35],v[212:215],a[112:115],v[32:35]\n"
RT"a[134:135],v247 offset:8704\n"
MF"v[40:43],v[212:215],a[116:119],v[40:43]\n"
BL"v236,s[12:15],s48 offen offset:2048 lds\n"
MF"v[40:43],v[208:211],a[100:103],v[40:43]\n"
RD"v[200:203],v242 offset:10240\n"
MF"v[48:51],v[208:211],a[104:107],v[48:51]\n"
RT"a[136:137],v248 offset:8192\n"
MF"v[48:51],v[212:215],a[120:123],v[48:51]\n"
BL"v237,s[12:15],s48 offen offset:3072 lds\n"
MF"v[56:59],v[212:215],a[124:127],v[56:59]\n"
RD"v[204:207],v243 offset:10240\n"
MF"v[56:59],v[208:211],a[108:111],v[56:59]\n"
WT"lgkmcnt(4)\n"
RT"a[138:139],v248 offset:8704\n"
MF"v[36:39],v[216:219],a[96:99],v[36:39]\n"
SA"m0,s37,0\n"
SA"s48,s47,3145728\n"
"s_and_b32 s48,s48,4194303\n"
MF"v[36:39],v[220:223],a[112:115],v[36:39]\n"
RD"v[208:211],v242 offset:12288\n"
MF"v[44:47],v[220:223],a[116:119],v[44:47]\n"
RT"a[140:141],v249 offset:8192\n"
MF"v[44:47],v[216:219],a[100:103],v[44:47]\n"
BL"v238,s[16:19],s48 offen offset:0 lds\n"
MF"v[52:55],v[216:219],a[104:107],v[52:55]\n"
MF"v[52:55],v[220:223],a[120:123],v[52:55]\n"
RD"v[212:215],v243 offset:12288\n"
MF"v[60:63],v[220:223],a[124:127],v[60:63]\n"
RT"a[142:143],v249 offset:8704\n"
MF"v[60:63],v[216:219],a[108:111],v[60:63]\n"
BL"v239,s[16:19],s48 offen offset:1024 lds\n"
MF"v[64:67],v[192:195],a[96:99],v[64:67]\n"
RD"v[216:219],v242 offset:14336\n"
MF"v[64:67],v[196:199],a[112:115],v[64:67]\n"
WT"vmcnt(18)\n"
RT"a[144:145],v246 offset:12288\n"
MF"v[72:75],v[196:199],a[116:119],v[72:75]\n"
MF"v[72:75],v[192:195],a[100:103],v[72:75]\n"
RT"a[146:147],v246 offset:12800\n"
MF"v[80:83],v[192:195],a[104:107],v[80:83]\n"
BL"v240,s[16:19],s48 offen offset:2048 lds\n"
MF"v[80:83],v[196:199],a[120:123],v[80:83]\n"
RD"v[220:223],v243 offset:14336\n"
MF"v[88:91],v[196:199],a[124:127],v[88:91]\n"
RT"a[148:149],v247 offset:12288\n"
MF"v[88:91],v[192:195],a[108:111],v[88:91]\n"
WT"lgkmcnt(4)\n"
BL"v241,s[16:19],s48 offen offset:3072 lds\n"
MF"v[68:71],v[200:203],a[96:99],v[68:71]\n"
MF"v[68:71],v[204:207],a[112:115],v[68:71]\n"
RT"a[150:151],v247 offset:12800\n"
MF"v[76:79],v[204:207],a[116:119],v[76:79]\n"
SA"m0,s37,4096\n"
SA"s48,s47,3407872\n"
"s_and_b32 s48,s48,4194303\n"
MF"v[76:79],v[200:203],a[100:103],v[76:79]\n"
MF"v[84:87],v[200:203],a[104:107],v[84:87]\n"
RT"a[152:153],v248 offset:12288\n"
MF"v[84:87],v[204:207],a[120:123],v[84:87]\n"
BL"v238,s[16:19],s48 offen offset:0 lds\n"
MF"v[92:95],v[204:207],a[124:127],v[92:95]\n"
WT"vmcnt(17) lgkmcnt(3)\n"
"s_barrier\n"
MF"v[92:95],v[200:203],a[108:111],v[92:95]\n"
RD"v[192:195],v242 offset:16384\n"
RT"a[154:155],v248 offset:12800\n"
MF"v[96:99],v[208:211],a[96:99],v[96:99]\n"
BL"v239,s[16:19],s48 offen offset:1024 lds\n"
MF"v[96:99],v[212:215],a[112:115],v[96:99]\n"
MF"v[104:107],v[212:215],a[116:119],v[104:107]\n"
RD"v[196:199],v243 offset:16384\n"
MF"v[104:107],v[208:211],a[100:103],v[104:107]\n"
RT"a[156:157],v249 offset:12288\n"
MF"v[112:115],v[208:211],a[104:107],v[112:115]\n"
MF"v[112:115],v[212:215],a[120:123],v[112:115]\n"
RT"a[158:159],v249 offset:12800\n"
MF"v[120:123],v[212:215],a[124:127],v[120:123]\n"
BL"v240,s[16:19],s48 offen offset:2048 lds\n"
MF"v[120:123],v[208:211],a[108:111],v[120:123]\n"
RD"v[200:203],v242 offset:18432\n"
MF"v[100:103],v[216:219],a[96:99],v[100:103]\n"
MF"v[100:103],v[220:223],a[112:115],v[100:103]\n"
RD"v[204:207],v243 offset:18432\n"
MF"v[108:111],v[220:223],a[116:119],v[108:111]\n"
BL"v241,s[16:19],s48 offen offset:3072 lds\n"
MF"v[108:111],v[216:219],a[100:103],v[108:111]\n"
MF"v[116:119],v[216:219],a[104:107],v[116:119]\n"
SA"m0,s36,0\n"
SA"s48,s46,768\n"
"s_and_b32 s48,s48,1023\n"
MF"v[116:119],v[220:223],a[120:123],v[116:119]\n"
RD"v[208:211],v242 offset:20480\n"
MF"v[124:127],v[220:223],a[124:127],v[124:127]\n"
BL"v234,s[12:15],s48 offen offset:0 lds\n"
MF"v[124:127],v[216:219],a[108:111],v[124:127]\n"
WT"lgkmcnt(2)\n"
MF"v[0:3],v[192:195],a[128:131],v[0:3]\n"
RD"v[212:215],v243 offset:20480\n"
MF"v[0:3],v[196:199],a[144:147],v[0:3]\n"
MF"v[8:11],v[196:199],a[148:151],v[8:11]\n"
BL"v235,s[12:15],s48 offen offset:1024 lds\n"
MF"v[8:11],v[192:195],a[132:135],v[8:11]\n"
MF"v[16:19],v[192:195],a[136:139],v[16:19]\n"
RD"v[216:219],v242 offset:22528\n"
MF"v[16:19],v[196:199],a[152:155],v[16:19]\n"
MF"v[24:27],v[196:199],a[156:159],v[24:27]\n"
MF"v[24:27],v[192:195],a[140:143],v[24:27]\n"
RD"v[220:223],v243 offset:22528\n"
MF"v[4:7],v[200:203],a[128:131],v[4:7]\n"
WT"lgkmcnt(2)\n"
BL"v236,s[12:15],s48 offen offset:2048 lds\n"
MF"v[4:7],v[204:207],a[144:147],v[4:7]\n"
WT"vmcnt(19)\n"
RT"a[160:161],v246 offset:16384\n"
MF"v[12:15],v[204:207],a[148:151],v[12:15]\n"
MF"v[12:15],v[200:203],a[132:135],v[12:15]\n"
RD"v[192:195],v242 offset:24576\n"
MF"v[20:23],v[200:203],a[136:139],v[20:23]\n"
RT"a[162:163],v246 offset:16896\n"
MF"v[20:23],v[204:207],a[152:155],v[20:23]\n"
BL"v237,s[12:15],s48 offen offset:3072 lds\n"
MF"v[28:31],v[204:207],a[156:159],v[28:31]\n"
SA"m0,s37,8192\n"
RT"a[164:165],v247 offset:16384\n"
SA"s48,s47,3670016\n"
MF"v[28:31],v[200:203],a[140:143],v[28:31]\n"
"s_and_b32 s48,s48,4194303\n"
RD"v[196:199],v243 offset:24576\n"
MF"v[32:35],v[208:211],a[128:131],v[32:35]\n"
BL"v238,s[16:19],s48 offen offset:0 lds\n"
MF"v[32:35],v[212:215],a[144:147],v[32:35]\n"
RT"a[166:167],v247 offset:16896\n"
MF"v[40:43],v[212:215],a[148:151],v[40:43]\n"
RD"v[200:203],v242 offset:26624\n"
MF"v[40:43],v[208:211],a[132:135],v[40:43]\n"
MF"v[48:51],v[208:211],a[136:139],v[48:51]\n"
RT"a[168:169],v248 offset:16384\n"
MF"v[48:51],v[212:215],a[152:155],v[48:51]\n"
BL"v239,s[16:19],s48 offen offset:1024 lds\n"
MF"v[56:59],v[212:215],a[156:159],v[56:59]\n"
RD"v[204:207],v243 offset:26624\n"
MF"v[56:59],v[208:211],a[140:143],v[56:59]\n"
WT"lgkmcnt(4)\n"
RT"a[170:171],v248 offset:16896\n"
MF"v[36:39],v[216:219],a[128:131],v[36:39]\n"
MF"v[36:39],v[220:223],a[144:147],v[36:39]\n"
RD"v[208:211],v242 offset:28672\n"
MF"v[44:47],v[220:223],a[148:151],v[44:47]\n"
BL"v240,s[16:19],s48 offen offset:2048 lds\n"
MF"v[44:47],v[216:219],a[132:135],v[44:47]\n"
RT"a[172:173],v249 offset:16384\n"
MF"v[52:55],v[216:219],a[136:139],v[52:55]\n"
MF"v[52:55],v[220:223],a[152:155],v[52:55]\n"
RD"v[212:215],v243 offset:28672\n"
MF"v[60:63],v[220:223],a[156:159],v[60:63]\n"
RT"a[174:175],v249 offset:16896\n"
MF"v[60:63],v[216:219],a[140:143],v[60:63]\n"
BL"v241,s[16:19],s48 offen offset:3072 lds\n"
MF"v[64:67],v[192:195],a[128:131],v[64:67]\n"
SA"m0,s37,12288\n"
RD"v[216:219],v242 offset:30720\n"
SA"s48,s47,3932160\n"
MF"v[64:67],v[196:199],a[144:147],v[64:67]\n"
"s_and_b32 s48,s48,4194303\n"
WT"vmcnt(20)\n"
RT"a[176:177],v246 offset:20480\n"
MF"v[72:75],v[196:199],a[148:151],v[72:75]\n"
BL"v238,s[16:19],s48 offen offset:0 lds\n"
MF"v[72:75],v[192:195],a[132:135],v[72:75]\n"
RT"a[178:179],v246 offset:20992\n"
MF"v[80:83],v[192:195],a[136:139],v[80:83]\n"
RD"v[220:223],v243 offset:30720\n"
MF"v[80:83],v[196:199],a[152:155],v[80:83]\n"
MF"v[88:91],v[196:199],a[156:159],v[88:91]\n"
RT"a[180:181],v247 offset:20480\n"
MF"v[88:91],v[192:195],a[140:143],v[88:91]\n"
WT"lgkmcnt(4)\n"
BL"v239,s[16:19],s48 offen offset:1024 lds\n"
MF"v[68:71],v[200:203],a[128:131],v[68:71]\n"
MF"v[68:71],v[204:207],a[144:147],v[68:71]\n"
RT"a[182:183],v247 offset:20992\n"
MF"v[76:79],v[204:207],a[148:151],v[76:79]\n"
MF"v[76:79],v[200:203],a[132:135],v[76:79]\n"
BL"v240,s[16:19],s48 offen offset:2048 lds\n"
MF"v[84:87],v[200:203],a[136:139],v[84:87]\n"
RT"a[184:185],v248 offset:20480\n"
MF"v[84:87],v[204:207],a[152:155],v[84:87]\n"
WT"vmcnt(19) lgkmcnt(3)\n"
"s_barrier\n"
MF"v[92:95],v[204:207],a[156:159],v[92:95]\n"
RD"v[192:195],v242 offset:32768\n"
MF"v[92:95],v[200:203],a[140:143],v[92:95]\n"
RT"a[186:187],v248 offset:20992\n"
MF"v[96:99],v[208:211],a[128:131],v[96:99]\n"
BL"v241,s[16:19],s48 offen offset:3072 lds\n"
MF"v[96:99],v[212:215],a[144:147],v[96:99]\n"
MF"v[104:107],v[212:215],a[148:151],v[104:107]\n"
SA"m0,s36,16384\n"
RD"v[196:199],v243 offset:32768\n"
SA"s48,s46,896\n"
MF"v[104:107],v[208:211],a[132:135],v[104:107]\n"
"s_and_b32 s48,s48,1023\n"
RT"a[188:189],v249 offset:20480\n"
MF"v[112:115],v[208:211],a[136:139],v[112:115]\n"
BL"v234,s[12:15],s48 offen offset:0 lds\n"
MF"v[112:115],v[212:215],a[152:155],v[112:115]\n"
RT"a[190:191],v249 offset:20992\n"
MF"v[120:123],v[212:215],a[156:159],v[120:123]\n"
RD"v[200:203],v242 offset:34816\n"
MF"v[120:123],v[208:211],a[140:143],v[120:123]\n"
MF"v[100:103],v[216:219],a[128:131],v[100:103]\n"
BL"v235,s[12:15],s48 offen offset:1024 lds\n"
MF"v[100:103],v[220:223],a[144:147],v[100:103]\n"
RD"v[204:207],v243 offset:34816\n"
MF"v[108:111],v[220:223],a[148:151],v[108:111]\n"
MF"v[108:111],v[216:219],a[132:135],v[108:111]\n"
MF"v[116:119],v[216:219],a[136:139],v[116:119]\n"
MF"v[116:119],v[220:223],a[152:155],v[116:119]\n"
RD"v[208:211],v242 offset:36864\n"
MF"v[124:127],v[220:223],a[156:159],v[124:127]\n"
BL"v236,s[12:15],s48 offen offset:2048 lds\n"
MF"v[124:127],v[216:219],a[140:143],v[124:127]\n"
WT"lgkmcnt(2)\n"
MF"v[0:3],v[192:195],a[160:163],v[0:3]\n"
RD"v[212:215],v243 offset:36864\n"
MF"v[0:3],v[196:199],a[176:179],v[0:3]\n"
BL"v237,s[12:15],s48 offen offset:3072 lds\n"
MF"v[8:11],v[196:199],a[180:183],v[8:11]\n"
MF"v[8:11],v[192:195],a[164:167],v[8:11]\n"
MF"v[16:19],v[192:195],a[168:171],v[16:19]\n"
SA"m0,s37,16384\n"
RD"v[216:219],v242 offset:38912\n"
SA"s48,s39,0\n"
MF"v[16:19],v[196:199],a[184:187],v[16:19]\n"
MF"v[24:27],v[196:199],a[188:191],v[24:27]\n"
BL"v234,s[12:15],s48 offen offset:0 lds\n"
MF"v[24:27],v[192:195],a[172:175],v[24:27]\n"
RD"v[220:223],v243 offset:38912\n"
MF"v[4:7],v[200:203],a[160:163],v[4:7]\n"
WT"lgkmcnt(2)\n"
WT"vmcnt(21)\n"
RT"a[192:193],v246 offset:0\n"
MF"v[4:7],v[204:207],a[176:179],v[4:7]\n"
MF"v[12:15],v[204:207],a[180:183],v[12:15]\n"
BL"v235,s[12:15],s48 offen offset:1024 lds\n"
MF"v[12:15],v[200:203],a[164:167],v[12:15]\n"
RD"v[192:195],v242 offset:40960\n"
RT"a[194:195],v246 offset:512\n"
MF"v[20:23],v[200:203],a[168:171],v[20:23]\n"
MF"v[20:23],v[204:207],a[184:187],v[20:23]\n"
MF"v[28:31],v[204:207],a[188:191],v[28:31]\n"
RT"a[196:197],v247 offset:0\n"
MF"v[28:31],v[200:203],a[172:175],v[28:31]\n"
RD"v[196:199],v243 offset:40960\n"
MF"v[32:35],v[208:211],a[160:163],v[32:35]\n"
BL"v236,s[12:15],s48 offen offset:2048 lds\n"
MF"v[32:35],v[212:215],a[176:179],v[32:35]\n"
RT"a[198:199],v247 offset:512\n"
MF"v[40:43],v[212:215],a[180:183],v[40:43]\n"
RD"v[200:203],v242 offset:43008\n"
MF"v[40:43],v[208:211],a[164:167],v[40:43]\n"
BL"v237,s[12:15],s48 offen offset:3072 lds\n"
MF"v[48:51],v[208:211],a[168:171],v[48:51]\n"
RT"a[200:201],v248 offset:0\n"
MF"v[48:51],v[212:215],a[184:187],v[48:51]\n"
MF"v[56:59],v[212:215],a[188:191],v[56:59]\n"
SA"m0,s37,20480\n"
RD"v[204:207],v243 offset:43008\n"
SA"s48,s98,0\n"
MF"v[56:59],v[208:211],a[172:175],v[56:59]\n"
WT"lgkmcnt(4)\n"
RT"a[202:203],v248 offset:512\n"
"s_lshr_b32 s50,s30,1\n"
MF"v[36:39],v[216:219],a[160:163],v[36:39]\n"
"s_and_b32 s51,s30,1\n"
BL"v234,s[12:15],s48 offen offset:0 lds\n"
MF"v[36:39],v[220:223],a[176:179],v[36:39]\n"
"s_mul_i32 s38,s50,24576\n"
RD"v[208:211],v242 offset:45056\n"
MF"v[44:47],v[220:223],a[180:183],v[44:47]\n"
"s_lshl_b32 s60,s50,18\n"
RT"a[204:205],v249 offset:0\n"
MF"v[44:47],v[216:219],a[164:167],v[44:47]\n"
"s_lshl_b32 s50,s51,12\n"
MF"v[52:55],v[216:219],a[168:171],v[52:55]\n"
SA"s38,s38,s50\n"
BL"v235,s[12:15],s48 offen offset:1024 lds\n"
MF"v[52:55],v[220:223],a[184:187],v[52:55]\n"
"s_lshl_b32 s50,s51,7\n"
RD"v[212:215],v243 offset:45056\n"
RT"a[206:207],v249 offset:512\n"
MF"v[60:63],v[220:223],a[188:191],v[60:63]\n"
SA"s60,s60,s50\n"
MF"v[60:63],v[216:219],a[172:175],v[60:63]\n"
MF"v[64:67],v[192:195],a[160:163],v[64:67]\n"
RD"v[216:219],v242 offset:47104\n"
MF"v[64:67],v[196:199],a[176:179],v[64:67]\n"
"s_sub_u32 s99,s34,5\n"
WT"vmcnt(22)\n"
RT"a[208:209],v246 offset:4096\n"
MF"v[72:75],v[196:199],a[180:183],v[72:75]\n"
"s_cmp_lt_u32 s34,5\n"
BL"v236,s[12:15],s48 offen offset:2048 lds\n"
MF"v[72:75],v[192:195],a[164:167],v[72:75]\n"
"s_cselect_b32 s99,s34,s99\n"
RT"a[210:211],v246 offset:4608\n"
"s_mul_i32 s99,s99,1\n"
MF"v[80:83],v[192:195],a[168:171],v[80:83]\n"
"s_lshl_b32 s61,s99,20\n"
RD"v[220:223],v243 offset:47104\n"
MF"v[80:83],v[196:199],a[184:187],v[80:83]\n"
BL"v237,s[12:15],s48 offen offset:3072 lds\n"
MF"v[88:91],v[196:199],a[188:191],v[88:91]\n"
RT"a[212:213],v247 offset:4096\n"
MF"v[88:91],v[192:195],a[172:175],v[88:91]\n"
WT"lgkmcnt(4)\n"
MF"v[68:71],v[200:203],a[160:163],v[68:71]\n"
SA"m0,s37,0\n"
SA"s48,s39,32768\n"
MF"v[68:71],v[204:207],a[176:179],v[68:71]\n"
RT"a[214:215],v247 offset:4608\n"
MF"v[76:79],v[204:207],a[180:183],v[76:79]\n"
BL"v234,s[12:15],s48 offen offset:0 lds\n"
MF"v[76:79],v[200:203],a[164:167],v[76:79]\n"
MF"v[84:87],v[200:203],a[168:171],v[84:87]\n"
RT"a[216:217],v248 offset:4096\n"
MF"v[84:87],v[204:207],a[184:187],v[84:87]\n"
WT"vmcnt(21) lgkmcnt(3)\n"
"s_barrier\n"
MF"v[92:95],v[204:207],a[188:191],v[92:95]\n"
RD"v[192:195],v242 offset:0\n"
MF"v[92:95],v[200:203],a[172:175],v[92:95]\n"
BL"v235,s[12:15],s48 offen offset:1024 lds\n"
MF"v[96:99],v[208:211],a[160:163],v[96:99]\n"
RT"a[218:219],v248 offset:4608\n"
MF"v[96:99],v[212:215],a[176:179],v[96:99]\n"
MF"v[104:107],v[212:215],a[180:183],v[104:107]\n"
RD"v[196:199],v243 offset:0\n"
MF"v[104:107],v[208:211],a[164:167],v[104:107]\n"
RT"a[220:221],v249 offset:4096\n"
MF"v[112:115],v[208:211],a[168:171],v[112:115]\n"
BL"v236,s[12:15],s48 offen offset:2048 lds\n"
MF"v[112:115],v[212:215],a[184:187],v[112:115]\n"
RT"a[222:223],v249 offset:4608\n"
MF"v[120:123],v[212:215],a[188:191],v[120:123]\n"
RD"v[200:203],v242 offset:2048\n"
MF"v[120:123],v[208:211],a[172:175],v[120:123]\n"
BL"v237,s[12:15],s48 offen offset:3072 lds\n"
MF"v[100:103],v[216:219],a[160:163],v[100:103]\n"
MF"v[100:103],v[220:223],a[176:179],v[100:103]\n"
RD"v[204:207],v243 offset:2048\n"
MF"v[108:111],v[220:223],a[180:183],v[108:111]\n"
SA"m0,s37,4096\n"
SA"s48,s98,32768\n"
MF"v[108:111],v[216:219],a[164:167],v[108:111]\n"
MF"v[116:119],v[216:219],a[168:171],v[116:119]\n"
WT"lgkmcnt(2)\n"
BL"v234,s[12:15],s48 offen offset:0 lds\n"
MF"v[116:119],v[220:223],a[184:187],v[116:119]\n"
RD"v[208:211],v242 offset:4096\n"
MF"v[124:127],v[220:223],a[188:191],v[124:127]\n"
MF"v[124:127],v[216:219],a[172:175],v[124:127]\n"
MF"v[0:3],v[192:195],a[192:195],v[0:3]\n"
RD"v[212:215],v243 offset:4096\n"
MF"v[0:3],v[196:199],a[208:211],v[0:3]\n"
BL"v235,s[12:15],s48 offen offset:1024 lds\n"
MF"v[8:11],v[196:199],a[212:215],v[8:11]\n"
MF"v[8:11],v[192:195],a[196:199],v[8:11]\n"
MF"v[16:19],v[192:195],a[200:203],v[16:19]\n"
RD"v[216:219],v242 offset:6144\n"
MF"v[16:19],v[196:199],a[216:219],v[16:19]\n"
BL"v236,s[12:15],s48 offen offset:2048 lds\n"
MF"v[24:27],v[196:199],a[220:223],v[24:27]\n"
BL"v[160:163],v224,s[8:11],0 offen offset:256 sc1\n"
MF"v[24:27],v[192:195],a[204:207],v[24:27]\n"
WT"lgkmcnt(1)\n"
RD"v[220:223],v243 offset:6144\n"
MF"v[4:7],v[200:203],a[192:195],v[4:7]\n"
WT"vmcnt(24)\n"
RT"a[224:225],v246 offset:8192\n"
MF"v[4:7],v[204:207],a[208:211],v[4:7]\n"
BL"v[164:167],v224,s[8:11],0 offen offset:1280 sc1\n"
MF"v[12:15],v[204:207],a[212:215],v[12:15]\n"
BL"v237,s[12:15],s48 offen offset:3072 lds\n"
MF"v[12:15],v[200:203],a[196:199],v[12:15]\n"
RD"v[192:195],v242 offset:8192\n"
RT"a[226:227],v246 offset:8704\n"
MF"v[20:23],v[200:203],a[200:203],v[20:23]\n"
MF"v[20:23],v[204:207],a[216:219],v[20:23]\n"
MF"v[28:31],v[204:207],a[220:223],v[28:31]\n"
RT"a[228:229],v247 offset:8192\n"
MF"v[28:31],v[200:203],a[204:207],v[28:31]\n"
RD"v[196:199],v243 offset:8192\n"
MF"v[32:35],v[208:211],a[192:195],v[32:35]\n"
MF"v[32:35],v[212:215],a[208:211],v[32:35]\n"
RT"a[230:231],v247 offset:8704\n"
MF"v[40:43],v[212:215],a[212:215],v[40:43]\n"
RD"v[200:203],v242 offset:10240\n"
MF"v[40:43],v[208:211],a[196:199],v[40:43]\n"
MF"v[48:51],v[208:211],a[200:203],v[48:51]\n"
RT"a[232:233],v248 offset:8192\n"
MF"v[48:51],v[212:215],a[216:219],v[48:51]\n"
MF"v[56:59],v[212:215],a[220:223],v[56:59]\n"
RD"v[204:207],v243 offset:10240\n"
MF"v[56:59],v[208:211],a[204:207],v[56:59]\n"
WT"lgkmcnt(4)\n"
RT"a[234:235],v248 offset:8704\n"
MF"v[36:39],v[216:219],a[192:195],v[36:39]\n"
MF"v[36:39],v[220:223],a[208:211],v[36:39]\n"
RD"v[208:211],v242 offset:12288\n"
MF"v[44:47],v[220:223],a[212:215],v[44:47]\n"
RT"a[236:237],v249 offset:8192\n"
MF"v[44:47],v[216:219],a[196:199],v[44:47]\n"
MF"v[52:55],v[216:219],a[200:203],v[52:55]\n"
MF"v[52:55],v[220:223],a[216:219],v[52:55]\n"
RD"v[212:215],v243 offset:12288\n"
MF"v[60:63],v[220:223],a[220:223],v[60:63]\n"
RT"a[238:239],v249 offset:8704\n"
MF"v[60:63],v[216:219],a[204:207],v[60:63]\n"
MF"v[64:67],v[192:195],a[192:195],v[64:67]\n"
RD"v[216:219],v242 offset:14336\n"
MF"v[64:67],v[196:199],a[208:211],v[64:67]\n"
SA"m0,s37,8192\n"
WT"vmcnt(22)\n"
RT"a[240:241],v246 offset:12288\n"
MF"v[72:75],v[196:199],a[212:215],v[72:75]\n"
SA"s48,s39,65536\n"
MF"v[72:75],v[192:195],a[196:199],v[72:75]\n"
RT"a[242:243],v246 offset:12800\n"
MF"v[80:83],v[192:195],a[200:203],v[80:83]\n"
WT"lgkmcnt(3)\n"
BL"v234,s[12:15],s48 offen offset:0 lds\n"
MF"v[80:83],v[196:199],a[216:219],v[80:83]\n"
RD"v[220:223],v243 offset:14336\n"
MF"v[88:91],v[196:199],a[220:223],v[88:91]\n"
RT"a[244:245],v247 offset:12288\n"
MF"v[88:91],v[192:195],a[204:207],v[88:91]\n"
BL"v235,s[12:15],s48 offen offset:1024 lds\n"
MF"v[68:71],v[200:203],a[192:195],v[68:71]\n"
MF"v[68:71],v[204:207],a[208:211],v[68:71]\n"
RT"a[246:247],v247 offset:12800\n"
MF"v[76:79],v[204:207],a[212:215],v[76:79]\n"
MF"v[76:79],v[200:203],a[196:199],v[76:79]\n"
MF"v[84:87],v[200:203],a[200:203],v[84:87]\n"
RT"a[248:249],v248 offset:12288\n"
MF"v[84:87],v[204:207],a[216:219],v[84:87]\n"
BL"v236,s[12:15],s48 offen offset:2048 lds\n"
MF"v[92:95],v[204:207],a[220:223],v[92:95]\n"
WT"vmcnt(21) lgkmcnt(3)\n"
"s_barrier\n"
MF"v[92:95],v[200:203],a[204:207],v[92:95]\n"
RD"v[192:195],v242 offset:16384\n"
RT"a[250:251],v248 offset:12800\n"
MF"v[96:99],v[208:211],a[192:195],v[96:99]\n"
BL"v237,s[12:15],s48 offen offset:3072 lds\n"
MF"v[96:99],v[212:215],a[208:211],v[96:99]\n"
MF"v[104:107],v[212:215],a[212:215],v[104:107]\n"
RD"v[196:199],v243 offset:16384\n"
MF"v[104:107],v[208:211],a[196:199],v[104:107]\n"
RT"a[252:253],v249 offset:12288\n"
MF"v[112:115],v[208:211],a[200:203],v[112:115]\n"
MF"v[112:115],v[212:215],a[216:219],v[112:115]\n"
RT"a[254:255],v249 offset:12800\n"
MF"v[120:123],v[212:215],a[220:223],v[120:123]\n"
RD"v[200:203],v242 offset:18432\n"
MF"v[120:123],v[208:211],a[204:207],v[120:123]\n"
MF"v[100:103],v[216:219],a[192:195],v[100:103]\n"
"s_lshl_b32 s50,s35,20\n"
MF"v[100:103],v[220:223],a[208:211],v[100:103]\n"
"s_lshl_b32 s51,s34,15\n"
RD"v[204:207],v243 offset:18432\n"
SA"m0,s37,12288\n"
MF"v[108:111],v[220:223],a[212:215],v[108:111]\n"
SA"s48,s98,65536\n"
SA"s50,s50,s51\n"
MF"v[108:111],v[216:219],a[196:199],v[108:111]\n"
WT"lgkmcnt(2)\n"
BL"v234,s[12:15],s48 offen offset:0 lds\n"
MF"v[116:119],v[216:219],a[200:203],v[116:119]\n"
"s_and_b32 s51,s30,1\n"
"s_lshl_b32 s51,s51,12\n"
MF"v[116:119],v[220:223],a[216:219],v[116:119]\n"
SA"s50,s50,s51\n"
RD"v[208:211],v242 offset:20480\n"
MF"v[124:127],v[220:223],a[220:223],v[124:127]\n"
SA"s50,s50,9699328\n"
WT"vmcnt(6)\n"
"v_cmp_eq_u64 vcc,s[28:29],v[160:161]\n"
"v_cmp_eq_u64_e64 s[62:63],s[28:29],v[162:163]\n"
"s_and_b64 vcc,vcc,s[62:63]\n"
"v_cmp_eq_u64_e64 s[62:63],s[28:29],v[164:165]\n"
"s_and_b64 vcc,vcc,s[62:63]\n"
"v_cmp_eq_u64_e64 s[62:63],s[28:29],v[166:167]\n"
"s_and_b64 vcc,vcc,s[62:63]\n"
"s_cmp_eq_u64 vcc,exec\n"
"s_cbranch_scc0 L1004_%=\n"
"L1005_%=:\n"
MF"v[124:127],v[216:219],a[204:207],v[124:127]\n"
"s_mov_b32 s94,2686976\n"
BL"v235,s[12:15],s48 offen offset:1024 lds\n"
MF"v[0:3],v[192:195],a[224:227],v[0:3]\n"
"s_mov_b32 s95,2691072\n"
RD"v[212:215],v243 offset:20480\n"
"v_add_u32 v250,s50,v224\n"
MF"v[0:3],v[196:199],a[240:243],v[0:3]\n"
BL"v[160:163],v224,s[8:11],s94 offen offset:0 sc1\n"
MF"v[8:11],v[196:199],a[244:247],v[8:11]\n"
BL"v[164:167],v224,s[8:11],s94 offen offset:1024 sc1\n"
MF"v[8:11],v[192:195],a[228:231],v[8:11]\n"
MF"v[16:19],v[192:195],a[232:235],v[16:19]\n"
RD"v[216:219],v242 offset:22528\n"
MF"v[16:19],v[196:199],a[248:251],v[16:19]\n"
BL"v236,s[12:15],s48 offen offset:2048 lds\n"
MF"v[24:27],v[196:199],a[252:255],v[24:27]\n"
BL"v[168:171],v224,s[8:11],s94 offen offset:2048 sc1\n"
MF"v[24:27],v[192:195],a[236:239],v[24:27]\n"
WT"lgkmcnt(1)\n"
BL"v[172:175],v224,s[8:11],s94 offen offset:3072 sc1\n"
MF"v[4:7],v[200:203],a[224:227],v[4:7]\n"
RD"v[220:223],v243 offset:22528\n"
MF"v[4:7],v[204:207],a[240:243],v[4:7]\n"
BL"v237,s[12:15],s48 offen offset:3072 lds\n"
MF"v[12:15],v[204:207],a[244:247],v[12:15]\n"
BL"v[176:179],v224,s[8:11],s95 offen offset:0 sc1\n"
MF"v[12:15],v[200:203],a[228:231],v[12:15]\n"
BL"v[180:183],v224,s[8:11],s95 offen offset:1024 sc1\n"
MF"v[20:23],v[200:203],a[232:235],v[20:23]\n"
RD"v[192:195],v242 offset:24576\n"
MF"v[20:23],v[204:207],a[248:251],v[20:23]\n"
BL"v[184:187],v224,s[8:11],s95 offen offset:2048 sc1\n"
MF"v[28:31],v[204:207],a[252:255],v[28:31]\n"
BL"v[188:191],v224,s[8:11],s95 offen offset:3072 sc1\n"
MF"v[28:31],v[200:203],a[236:239],v[28:31]\n"
RD"v[196:199],v243 offset:24576\n"
MF"v[32:35],v[208:211],a[224:227],v[32:35]\n"
MF"v[32:35],v[212:215],a[240:243],v[32:35]\n"
MF"v[40:43],v[212:215],a[244:247],v[40:43]\n"
RD"v[200:203],v242 offset:26624\n"
MF"v[40:43],v[208:211],a[228:231],v[40:43]\n"
MF"v[48:51],v[208:211],a[232:235],v[48:51]\n"
MF"v[48:51],v[212:215],a[248:251],v[48:51]\n"
MF"v[56:59],v[212:215],a[252:255],v[56:59]\n"
RD"v[204:207],v243 offset:26624\n"
MF"v[56:59],v[208:211],a[236:239],v[56:59]\n"
WT"lgkmcnt(2)\n"
MF"v[36:39],v[216:219],a[224:227],v[36:39]\n"
MF"v[36:39],v[220:223],a[240:243],v[36:39]\n"
RD"v[208:211],v242 offset:28672\n"
MF"v[44:47],v[220:223],a[244:247],v[44:47]\n"
MF"v[44:47],v[216:219],a[228:231],v[44:47]\n"
MF"v[52:55],v[216:219],a[232:235],v[52:55]\n"
MF"v[52:55],v[220:223],a[248:251],v[52:55]\n"
RD"v[212:215],v243 offset:28672\n"
MF"v[60:63],v[220:223],a[252:255],v[60:63]\n"
MF"v[60:63],v[216:219],a[236:239],v[60:63]\n"
MF"v[64:67],v[192:195],a[224:227],v[64:67]\n"
RD"v[216:219],v242 offset:30720\n"
MF"v[64:67],v[196:199],a[240:243],v[64:67]\n"
MF"v[72:75],v[196:199],a[244:247],v[72:75]\n"
MF"v[72:75],v[192:195],a[228:231],v[72:75]\n"
MF"v[80:83],v[192:195],a[232:235],v[80:83]\n"
RD"v[220:223],v243 offset:30720\n"
MF"v[80:83],v[196:199],a[248:251],v[80:83]\n"
MF"v[88:91],v[196:199],a[252:255],v[88:91]\n"
MF"v[88:91],v[192:195],a[236:239],v[88:91]\n"
WT"lgkmcnt(1)\n"
MF"v[68:71],v[200:203],a[224:227],v[68:71]\n"
MF"v[68:71],v[204:207],a[240:243],v[68:71]\n"
MF"v[76:79],v[204:207],a[244:247],v[76:79]\n"
"v_add_u32 v244,114688,v242\n"
"v_add_u32 v245,114688,v243\n"
MF"v[76:79],v[200:203],a[228:231],v[76:79]\n"
"v_add_u32 v242,65536,v242\n"
"v_add_u32 v243,65536,v243\n"
MF"v[84:87],v[200:203],a[232:235],v[84:87]\n"
MF"v[84:87],v[204:207],a[248:251],v[84:87]\n"
WT"lgkmcnt(0)\n"
"s_barrier\n"
MF"v[92:95],v[204:207],a[252:255],v[92:95]\n"
RD"v[192:195],v242 offset:16384\n"
MF"v[92:95],v[200:203],a[236:239],v[92:95]\n"
MF"v[96:99],v[208:211],a[224:227],v[96:99]\n"
MF"v[96:99],v[212:215],a[240:243],v[96:99]\n"
MF"v[104:107],v[212:215],a[244:247],v[104:107]\n"
SA"m0,s36,49152\n"
RD"v[196:199],v243 offset:16384\n"
SA"s48,s60,s61\n"
MF"v[104:107],v[208:211],a[228:231],v[104:107]\n"
MF"v[112:115],v[208:211],a[232:235],v[112:115]\n"
BL"v238,s[20:23],s48 offen offset:0 lds\n"
MF"v[112:115],v[212:215],a[248:251],v[112:115]\n"
MF"v[120:123],v[212:215],a[252:255],v[120:123]\n"
RD"v[200:203],v242 offset:18432\n"
MF"v[120:123],v[208:211],a[236:239],v[120:123]\n"
MF"v[100:103],v[216:219],a[224:227],v[100:103]\n"
BL"v239,s[20:23],s48 offen offset:1024 lds\n"
MF"v[100:103],v[220:223],a[240:243],v[100:103]\n"
"s_cmp_eq_u32 s31,1\n"
"s_cselect_b32 s94,0,2130706432\n"
MF"v[108:111],v[220:223],a[244:247],v[108:111]\n"
"buffer_store_dwordx2 v[232:233],v230,s[8:11],s94 offen sc1\n"
MF"v[108:111],v[216:219],a[228:231],v[108:111]\n"
RD"v[204:207],v243 offset:18432\n"
"v_mov_b32 v227,0\n"
MF"v[116:119],v[216:219],a[232:235],v[116:119]\n"
WT"vmcnt(12)\n"
"v_mov_b32 v226,v160\n"
"v_mov_b32 v228,0\n"
MF"v[116:119],v[220:223],a[248:251],v[116:119]\n"
BL"v240,s[20:23],s48 offen offset:2048 lds\n"
MF"v[124:127],v[220:223],a[252:255],v[124:127]\n"
SA"s61,s61,524288\n"
RD"v[208:211],v242 offset:20480\n"
"v_med3_u32 v228,v227,v228,v161\n"
MF"v[124:127],v[216:219],a[236:239],v[124:127]\n"
WT"lgkmcnt(2)\n"
"v_med3_u32 v227,v226,v227,v161\n"
"v_max_u32 v226,v226,v161\n"
MF"v[128:131],v[192:195],a[0:3],0\n"
"v_med3_u32 v228,v227,v228,v162\n"
"v_med3_u32 v227,v226,v227,v162\n"
MF"v[128:131],v[196:199],a[16:19],v[128:131]\n"
RD"v[212:215],v243 offset:20480\n"
"v_max_u32 v226,v226,v162\n"
MF"v[136:139],v[196:199],a[20:23],0\n"
BL"v241,s[20:23],s48 offen offset:3072 lds\n"
MF"v[136:139],v[192:195],a[4:7],v[136:139]\n"
"v_med3_u32 v228,v227,v228,v163\n"
"v_med3_u32 v227,v226,v227,v163\n"
MF"v[144:147],v[192:195],a[8:11],0\n"
"v_max_u32 v226,v226,v163\n"
PK"v0,v0,v1\n"
MF"v[144:147],v[196:199],a[24:27],v[144:147]\n"
RD"v[216:219],v242 offset:22528\n"
PK"v1,v2,v3\n"
MF"v[152:155],v[196:199],a[28:31],0\n"
SA"m0,s36,32768\n"
WT"vmcnt(13)\n"
"v_med3_u32 v228,v227,v228,v164\n"
MF"v[152:155],v[192:195],a[12:15],v[152:155]\n"
SA"s48,s60,s61\n"
PK"v2,v4,v5\n"
PK"v3,v6,v7\n"
MF"v[132:135],v[200:203],a[0:3],0\n"
WT"lgkmcnt(1)\n"
BL"v238,s[20:23],s48 offen offset:0 lds\n"
MF"v[132:135],v[204:207],a[16:19],v[132:135]\n"
RD"v[220:223],v243 offset:22528\n"
"v_med3_u32 v227,v226,v227,v164\n"
MF"v[140:143],v[204:207],a[20:23],0\n"
"v_max_u32 v226,v226,v164\n"
"v_med3_u32 v228,v227,v228,v165\n"
MF"v[140:143],v[200:203],a[4:7],v[140:143]\n"
"v_med3_u32 v227,v226,v227,v165\n"
"v_max_u32 v226,v226,v165\n"
MF"v[148:151],v[200:203],a[8:11],0\n"
BL"v239,s[20:23],s48 offen offset:1024 lds\n"
MF"v[148:151],v[204:207],a[24:27],v[148:151]\n"
RD"v[192:195],v242 offset:40960\n"
"v_med3_u32 v228,v227,v228,v166\n"
MF"v[156:159],v[204:207],a[28:31],0\n"
PK"v8,v8,v9\n"
PK"v9,v10,v11\n"
MF"v[156:159],v[200:203],a[12:15],v[156:159]\n"
"v_med3_u32 v227,v226,v227,v166\n"
PK"v10,v12,v13\n"
MF"v[128:131],v[208:211],a[32:35],v[128:131]\n"
"s_mov_b32 s49,0\n"
PK"v11,v14,v15\n"
"v_max_u32 v226,v226,v166\n"
MF"v[128:131],v[212:215],a[48:51],v[128:131]\n"
RD"v[196:199],v243 offset:40960\n"
"v_med3_u32 v228,v227,v228,v167\n"
MF"v[136:139],v[212:215],a[52:55],v[136:139]\n"
SA"s61,s61,524288\n"
BL"v240,s[20:23],s48 offen offset:2048 lds\n"
MF"v[136:139],v[208:211],a[36:39],v[136:139]\n"
"s_cmp_eq_u32 s61,5242880\n"
BS"v[0:3],v250,s[8:11],s49 offen offset:0 sc1\n"
MF"v[144:147],v[208:211],a[40:43],v[144:147]\n"
"s_cselect_b32 s61,0,s61\n"
RD"v[200:203],v242 offset:43008\n"
"v_med3_u32 v227,v226,v227,v167\n"
MF"v[144:147],v[212:215],a[56:59],v[144:147]\n"
BL"v241,s[20:23],s48 offen offset:3072 lds\n"
MF"v[152:155],v[212:215],a[60:63],v[152:155]\n"
"v_max_u32 v226,v226,v167\n"
PK"v16,v16,v17\n"
MF"v[152:155],v[208:211],a[44:47],v[152:155]\n"
WT"lgkmcnt(2)\n"
WT"vmcnt(16)\n"
"v_med3_u32 v228,v227,v228,v168\n"
MF"v[132:135],v[216:219],a[32:35],v[132:135]\n"
RD"v[204:207],v243 offset:43008\n"
"v_med3_u32 v227,v226,v227,v168\n"
MF"v[132:135],v[220:223],a[48:51],v[132:135]\n"
PK"v17,v18,v19\n"
"v_max_u32 v226,v226,v168\n"
MF"v[140:143],v[220:223],a[52:55],v[140:143]\n"
"v_med3_u32 v228,v227,v228,v169\n"
PK"v18,v20,v21\n"
MF"v[140:143],v[216:219],a[36:39],v[140:143]\n"
PK"v19,v22,v23\n"
"v_med3_u32 v227,v226,v227,v169\n"
MF"v[148:151],v[216:219],a[40:43],v[148:151]\n"
RD"v[208:211],v242 offset:45056\n"
"v_max_u32 v226,v226,v169\n"
MF"v[148:151],v[220:223],a[56:59],v[148:151]\n"
"v_med3_u32 v228,v227,v228,v170\n"
"v_med3_u32 v227,v226,v227,v170\n"
MF"v[156:159],v[220:223],a[60:63],v[156:159]\n"
"v_max_u32 v226,v226,v170\n"
PK"v24,v24,v25\n"
MF"v[156:159],v[216:219],a[44:47],v[156:159]\n"
PK"v25,v26,v27\n"
"v_med3_u32 v228,v227,v228,v171\n"
MF"v[128:131],v[192:195],a[64:67],v[128:131]\n"
WT"lgkmcnt(1)\n"
RD"v[212:215],v243 offset:45056\n"
"v_med3_u32 v227,v226,v227,v171\n"
MF"v[128:131],v[196:199],a[80:83],v[128:131]\n"
PK"v26,v28,v29\n"
PK"v27,v30,v31\n"
MF"v[136:139],v[196:199],a[84:87],v[136:139]\n"
"v_max_u32 v226,v226,v171\n"
PK"v32,v32,v33\n"
MF"v[136:139],v[192:195],a[68:71],v[136:139]\n"
RD"v[216:219],v242 offset:47104\n"
PK"v33,v34,v35\n"
MF"v[144:147],v[192:195],a[72:75],v[144:147]\n"
WT"vmcnt(15)\n"
"v_med3_u32 v228,v227,v228,v172\n"
"v_med3_u32 v227,v226,v227,v172\n"
MF"v[144:147],v[196:199],a[88:91],v[144:147]\n"
"v_max_u32 v226,v226,v172\n"
"v_med3_u32 v228,v227,v228,v173\n"
MF"v[152:155],v[196:199],a[92:95],v[152:155]\n"
"v_med3_u32 v227,v226,v227,v173\n"
"v_max_u32 v226,v226,v173\n"
MF"v[152:155],v[192:195],a[76:79],v[152:155]\n"
RD"v[220:223],v243 offset:47104\n"
PK"v34,v36,v37\n"
MF"v[132:135],v[200:203],a[64:67],v[132:135]\n"
PK"v35,v38,v39\n"
"v_med3_u32 v228,v227,v228,v174\n"
MF"v[132:135],v[204:207],a[80:83],v[132:135]\n"
"v_med3_u32 v227,v226,v227,v174\n"
"v_max_u32 v226,v226,v174\n"
MF"v[140:143],v[204:207],a[84:87],v[140:143]\n"
"v_med3_u32 v228,v227,v228,v175\n"
"v_med3_u32 v227,v226,v227,v175\n"
MF"v[140:143],v[200:203],a[68:71],v[140:143]\n"
RD"v[192:195],v244 offset:16384\n"
"v_max_u32 v226,v226,v175\n"
MF"v[148:151],v[200:203],a[72:75],v[148:151]\n"
PK"v40,v40,v41\n"
PK"v41,v42,v43\n"
MF"v[148:151],v[204:207],a[88:91],v[148:151]\n"
WT"vmcnt(13)\n"
"v_med3_u32 v228,v227,v228,v176\n"
PK"v42,v44,v45\n"
MF"v[156:159],v[204:207],a[92:95],v[156:159]\n"
PK"v43,v46,v47\n"
"v_med3_u32 v227,v226,v227,v176\n"
MF"v[156:159],v[200:203],a[76:79],v[156:159]\n"
WT"lgkmcnt(1)\n"
RD"v[196:199],v245 offset:16384\n"
"v_max_u32 v226,v226,v176\n"
MF"v[128:131],v[208:211],a[96:99],v[128:131]\n"
"v_med3_u32 v228,v227,v228,v177\n"
"v_med3_u32 v227,v226,v227,v177\n"
MF"v[128:131],v[212:215],a[112:115],v[128:131]\n"
BS"v[8:11],v250,s[8:11],s49 offen offset:1024 sc1\n"
MF"v[136:139],v[212:215],a[116:119],v[136:139]\n"
"v_max_u32 v226,v226,v177\n"
"v_med3_u32 v228,v227,v228,v178\n"
MF"v[136:139],v[208:211],a[100:103],v[136:139]\n"
RD"v[200:203],v244 offset:18432\n"
PK"v48,v48,v49\n"
MF"v[144:147],v[208:211],a[104:107],v[144:147]\n"
PK"v49,v50,v51\n"
"v_med3_u32 v227,v226,v227,v178\n"
MF"v[144:147],v[212:215],a[120:123],v[144:147]\n"
PK"v50,v52,v53\n"
PK"v51,v54,v55\n"
MF"v[152:155],v[212:215],a[124:127],v[152:155]\n"
RD"v[204:207],v245 offset:18432\n"
"v_max_u32 v226,v226,v178\n"
MF"v[152:155],v[208:211],a[108:111],v[152:155]\n"
"v_med3_u32 v228,v227,v228,v179\n"
"v_med3_u32 v227,v226,v227,v179\n"
MF"v[132:135],v[216:219],a[96:99],v[132:135]\n"
"v_max_u32 v226,v226,v179\n"
PK"v56,v56,v57\n"
MF"v[132:135],v[220:223],a[112:115],v[132:135]\n"
WT"vmcnt(13)\n"
"v_med3_u32 v228,v227,v228,v180\n"
"v_med3_u32 v227,v226,v227,v180\n"
MF"v[140:143],v[220:223],a[116:119],v[140:143]\n"
RD"v[208:211],v244 offset:20480\n"
PK"v57,v58,v59\n"
MF"v[140:143],v[216:219],a[100:103],v[140:143]\n"
"v_max_u32 v226,v226,v180\n"
PK"v58,v60,v61\n"
MF"v[148:151],v[216:219],a[104:107],v[148:151]\n"
PK"v59,v62,v63\n"
"v_med3_u32 v228,v227,v228,v181\n"
MF"v[148:151],v[220:223],a[120:123],v[148:151]\n"
"v_med3_u32 v227,v226,v227,v181\n"
"v_max_u32 v226,v226,v181\n"
MF"v[156:159],v[220:223],a[124:127],v[156:159]\n"
RD"v[212:215],v245 offset:20480\n"
"v_med3_u32 v228,v227,v228,v182\n"
MF"v[156:159],v[216:219],a[108:111],v[156:159]\n"
WT"lgkmcnt(2)\n"
"v_med3_u32 v227,v226,v227,v182\n"
"v_max_u32 v226,v226,v182\n"
MF"v[128:131],v[192:195],a[128:131],v[128:131]\n"
PK"v64,v64,v65\n"
PK"v65,v66,v67\n"
MF"v[128:131],v[196:199],a[144:147],v[128:131]\n"
"v_med3_u32 v228,v227,v228,v183\n"
PK"v66,v68,v69\n"
MF"v[136:139],v[196:199],a[148:151],v[136:139]\n"
RD"v[216:219],v244 offset:22528\n"
PK"v67,v70,v71\n"
MF"v[136:139],v[192:195],a[132:135],v[136:139]\n"
"v_med3_u32 v227,v226,v227,v183\n"
"v_max_u32 v226,v226,v183\n"
MF"v[144:147],v[192:195],a[136:139],v[144:147]\n"
WT"vmcnt(12)\n"
"v_med3_u32 v228,v227,v228,v184\n"
"v_med3_u32 v227,v226,v227,v184\n"
MF"v[144:147],v[196:199],a[152:155],v[144:147]\n"
RD"v[220:223],v245 offset:22528\n"
"v_max_u32 v226,v226,v184\n"
MF"v[152:155],v[196:199],a[156:159],v[152:155]\n"
"v_med3_u32 v228,v227,v228,v185\n"
PK"v72,v72,v73\n"
MF"v[152:155],v[192:195],a[140:143],v[152:155]\n"
PK"v73,v74,v75\n"
"v_med3_u32 v227,v226,v227,v185\n"
MF"v[132:135],v[200:203],a[128:131],v[132:135]\n"
"v_max_u32 v226,v226,v185\n"
PK"v74,v76,v77\n"
MF"v[132:135],v[204:207],a[144:147],v[132:135]\n"
RD"v[192:195],v244 offset:40960\n"
PK"v75,v78,v79\n"
MF"v[140:143],v[204:207],a[148:151],v[140:143]\n"
"v_med3_u32 v228,v227,v228,v186\n"
"v_med3_u32 v227,v226,v227,v186\n"
MF"v[140:143],v[200:203],a[132:135],v[140:143]\n"
"v_max_u32 v226,v226,v186\n"
"v_med3_u32 v228,v227,v228,v187\n"
MF"v[148:151],v[200:203],a[136:139],v[148:151]\n"
"v_med3_u32 v227,v226,v227,v187\n"
PK"v80,v80,v81\n"
MF"v[148:151],v[204:207],a[152:155],v[148:151]\n"
RD"v[196:199],v245 offset:40960\n"
PK"v81,v82,v83\n"
MF"v[156:159],v[204:207],a[156:159],v[156:159]\n"
"v_max_u32 v226,v226,v187\n"
PK"v82,v84,v85\n"
MF"v[156:159],v[200:203],a[140:143],v[156:159]\n"
WT"lgkmcnt(2)\n"
WT"vmcnt(11)\n"
"v_med3_u32 v228,v227,v228,v188\n"
MF"v[128:131],v[208:211],a[160:163],v[128:131]\n"
BS"v[16:19],v250,s[8:11],s49 offen offset:2048 sc1\n"
MF"v[128:131],v[212:215],a[176:179],v[128:131]\n"
RD"v[200:203],v244 offset:43008\n"
PK"v83,v86,v87\n"
MF"v[136:139],v[212:215],a[180:183],v[136:139]\n"
"v_med3_u32 v227,v226,v227,v188\n"
"v_max_u32 v226,v226,v188\n"
MF"v[136:139],v[208:211],a[164:167],v[136:139]\n"
"v_med3_u32 v228,v227,v228,v189\n"
"v_med3_u32 v227,v226,v227,v189\n"
MF"v[144:147],v[208:211],a[168:171],v[144:147]\n"
"v_max_u32 v226,v226,v189\n"
PK"v88,v88,v89\n"
MF"v[144:147],v[212:215],a[184:187],v[144:147]\n"
RD"v[204:207],v245 offset:43008\n"
PK"v89,v90,v91\n"
MF"v[152:155],v[212:215],a[188:191],v[152:155]\n"
"v_med3_u32 v228,v227,v228,v190\n"
"v_med3_u32 v227,v226,v227,v190\n"
MF"v[152:155],v[208:211],a[172:175],v[152:155]\n"
PK"v90,v92,v93\n"
PK"v91,v94,v95\n"
MF"v[132:135],v[216:219],a[160:163],v[132:135]\n"
RD"v[208:211],v244 offset:45056\n"
"v_max_u32 v226,v226,v190\n"
MF"v[132:135],v[220:223],a[176:179],v[132:135]\n"
"v_med3_u32 v228,v227,v228,v191\n"
"v_med3_u32 v227,v226,v227,v191\n"
MF"v[140:143],v[220:223],a[180:183],v[140:143]\n"
"v_max_u32 v226,v226,v191\n"
"v_mov_b32 v229,v226\n"
MF"v[140:143],v[216:219],a[164:167],v[140:143]\n"
MF"v[148:151],v[216:219],a[168:171],v[148:151]\n"
RD"v[212:215],v245 offset:45056\n"
"v_max_u32_dpp v229,v229,v229 row_shr:1 row_mask:0xf bank_mask:0xf\n"
MF"v[148:151],v[220:223],a[184:187],v[148:151]\n"
MF"v[156:159],v[220:223],a[188:191],v[156:159]\n"
"v_max_u32_dpp v229,v229,v229 row_shr:2 row_mask:0xf bank_mask:0xf\n"
MF"v[156:159],v[216:219],a[172:175],v[156:159]\n"
WT"lgkmcnt(1)\n"
"v_max_u32_dpp v229,v229,v229 row_shr:4 row_mask:0xf bank_mask:0xf\n"
MF"v[128:131],v[192:195],a[192:195],v[128:131]\n"
RD"v[216:219],v244 offset:47104\n"
"v_max_u32_dpp v229,v229,v229 row_shr:8 row_mask:0xf bank_mask:0xf\n"
MF"v[128:131],v[196:199],a[208:211],v[128:131]\n"
MF"v[136:139],v[196:199],a[212:215],v[136:139]\n"
"v_max_u32_dpp v229,v229,v229 row_bcast:15 row_mask:0xa bank_mask:0xf\n"
MF"v[136:139],v[192:195],a[196:199],v[136:139]\n"
MF"v[144:147],v[192:195],a[200:203],v[144:147]\n"
RD"v[220:223],v245 offset:47104\n"
"v_max_u32_dpp v229,v229,v229 row_bcast:31 row_mask:0xc bank_mask:0xf\n"
MF"v[144:147],v[196:199],a[216:219],v[144:147]\n"
"v_readlane_b32 s62,v229,63\n"
MF"v[152:155],v[196:199],a[220:223],v[152:155]\n"
MF"v[152:155],v[192:195],a[204:207],v[152:155]\n"
"v_cmp_eq_u32 vcc,s62,v226\n"
"v_writelane_b32 v225,s62,0\n"
MF"v[132:135],v[200:203],a[192:195],v[132:135]\n"
"v_cndmask_b32 v226,v226,v227,vcc\n"
"v_cndmask_b32_e64 v227,v227,0,vcc\n"
MF"v[132:135],v[204:207],a[208:211],v[132:135]\n"
"v_mov_b32 v229,v226\n"
MF"v[140:143],v[204:207],a[212:215],v[140:143]\n"
MF"v[140:143],v[200:203],a[196:199],v[140:143]\n"
"v_max_u32_dpp v229,v229,v229 row_shr:1 row_mask:0xf bank_mask:0xf\n"
MF"v[148:151],v[200:203],a[200:203],v[148:151]\n"
MF"v[148:151],v[204:207],a[216:219],v[148:151]\n"
WT"lgkmcnt(0)\n"
"s_barrier\n"
MF"v[156:159],v[204:207],a[220:223],v[156:159]\n"
RD"v[192:195],v242 offset:0\n"
"v_max_u32_dpp v229,v229,v229 row_shr:2 row_mask:0xf bank_mask:0xf\n"
MF"v[156:159],v[200:203],a[204:207],v[156:159]\n"
BS"v[24:27],v250,s[8:11],s49 offen offset:3072 sc1\n"
MF"v[128:131],v[208:211],a[224:227],v[128:131]\n"
"v_max_u32_dpp v229,v229,v229 row_shr:4 row_mask:0xf bank_mask:0xf\n"
MF"v[128:131],v[212:215],a[240:243],v[128:131]\n"
MF"v[136:139],v[212:215],a[244:247],v[136:139]\n"
RD"v[196:199],v243 offset:0\n"
"v_max_u32_dpp v229,v229,v229 row_shr:8 row_mask:0xf bank_mask:0xf\n"
MF"v[136:139],v[208:211],a[228:231],v[136:139]\n"
MF"v[144:147],v[208:211],a[232:235],v[144:147]\n"
"v_max_u32_dpp v229,v229,v229 row_bcast:15 row_mask:0xa bank_mask:0xf\n"
MF"v[144:147],v[212:215],a[248:251],v[144:147]\n"
MF"v[152:155],v[212:215],a[252:255],v[152:155]\n"
RD"v[200:203],v242 offset:2048\n"
"v_max_u32_dpp v229,v229,v229 row_bcast:31 row_mask:0xc bank_mask:0xf\n"
MF"v[152:155],v[208:211],a[236:239],v[152:155]\n"
"v_readlane_b32 s62,v229,63\n"
MF"v[132:135],v[216:219],a[224:227],v[132:135]\n"
MF"v[132:135],v[220:223],a[240:243],v[132:135]\n"
"v_cmp_eq_u32 vcc,s62,v226\n"
"v_writelane_b32 v225,s62,1\n"
MF"v[140:143],v[220:223],a[244:247],v[140:143]\n"
RD"v[204:207],v243 offset:2048\n"
"v_cndmask_b32 v226,v226,v227,vcc\n"
MF"v[140:143],v[216:219],a[228:231],v[140:143]\n"
"v_cndmask_b32_e64 v227,v227,0,vcc\n"
"v_mov_b32 v229,v226\n"
MF"v[148:151],v[216:219],a[232:235],v[148:151]\n"
MF"v[148:151],v[220:223],a[248:251],v[148:151]\n"
"v_max_u32_dpp v229,v229,v229 row_shr:1 row_mask:0xf bank_mask:0xf\n"
MF"v[156:159],v[220:223],a[252:255],v[156:159]\n"
RD"v[208:211],v242 offset:4096\n"
"v_max_u32_dpp v229,v229,v229 row_shr:2 row_mask:0xf bank_mask:0xf\n"
MF"v[156:159],v[216:219],a[236:239],v[156:159]\n"
WT"lgkmcnt(2)\n"
"v_max_u32_dpp v229,v229,v229 row_shr:4 row_mask:0xf bank_mask:0xf\n"
MF"v[36:39],v[192:195],a[0:3],0\n"
MF"v[36:39],v[196:199],a[16:19],v[36:39]\n"
RD"v[212:215],v243 offset:4096\n"
"v_max_u32_dpp v229,v229,v229 row_shr:8 row_mask:0xf bank_mask:0xf\n"
MF"v[44:47],v[196:199],a[20:23],0\n"
MF"v[44:47],v[192:195],a[4:7],v[44:47]\n"
"v_max_u32_dpp v229,v229,v229 row_bcast:15 row_mask:0xa bank_mask:0xf\n"
PK"v4,v128,v129\n"
MF"v[52:55],v[192:195],a[8:11],0\n"
"v_max_u32_dpp v229,v229,v229 row_bcast:31 row_mask:0xc bank_mask:0xf\n"
PK"v5,v130,v131\n"
MF"v[52:55],v[196:199],a[24:27],v[52:55]\n"
RD"v[216:219],v242 offset:6144\n"
"v_readlane_b32 s62,v229,63\n"
MF"v[60:63],v[196:199],a[28:31],0\n"
PK"v6,v132,v133\n"
"v_cmp_eq_u32 vcc,s62,v226\n"
MF"v[60:63],v[192:195],a[12:15],v[60:63]\n"
"v_writelane_b32 v225,s62,2\n"
"v_cndmask_b32 v226,v226,v227,vcc\n"
MF"v[68:71],v[200:203],a[0:3],0\n"
WT"lgkmcnt(1)\n"
"v_cndmask_b32_e64 v227,v227,0,vcc\n"
"v_mov_b32 v229,v226\n"
MF"v[68:71],v[204:207],a[16:19],v[68:71]\n"
RD"v[220:223],v243 offset:6144\n"
"v_max_u32_dpp v229,v229,v229 row_shr:1 row_mask:0xf bank_mask:0xf\n"
MF"v[76:79],v[204:207],a[20:23],0\n"
PK"v7,v134,v135\n"
"v_max_u32_dpp v229,v229,v229 row_shr:2 row_mask:0xf bank_mask:0xf\n"
MF"v[76:79],v[200:203],a[4:7],v[76:79]\n"
PK"v12,v136,v137\n"
"v_max_u32_dpp v229,v229,v229 row_shr:4 row_mask:0xf bank_mask:0xf\n"
MF"v[84:87],v[200:203],a[8:11],0\n"
"s_mov_b32 s49,8192\n"
"v_max_u32_dpp v229,v229,v229 row_shr:8 row_mask:0xf bank_mask:0xf\n"
PK"v13,v138,v139\n"
MF"v[84:87],v[204:207],a[24:27],v[84:87]\n"
RD"v[192:195],v242 offset:24576\n"
"v_max_u32_dpp v229,v229,v229 row_bcast:15 row_mask:0xa bank_mask:0xf\n"
MF"v[92:95],v[204:207],a[28:31],0\n"
BS"v[32:35],v250,s[8:11],s49 offen offset:0 sc1\n"
MF"v[92:95],v[200:203],a[12:15],v[92:95]\n"
"v_max_u32_dpp v229,v229,v229 row_bcast:31 row_mask:0xc bank_mask:0xf\n"
PK"v14,v140,v141\n"
MF"v[36:39],v[208:211],a[32:35],v[36:39]\n"
"v_readlane_b32 s62,v229,63\n"
PK"v15,v142,v143\n"
MF"v[36:39],v[212:215],a[48:51],v[36:39]\n"
RD"v[196:199],v243 offset:24576\n"
"v_cmp_eq_u32 vcc,s62,v226\n"
MF"v[44:47],v[212:215],a[52:55],v[44:47]\n"
"v_writelane_b32 v225,s62,3\n"
"v_cndmask_b32 v226,v226,v227,vcc\n"
MF"v[44:47],v[208:211],a[36:39],v[44:47]\n"
"v_cndmask_b32_e64 v227,v227,0,vcc\n"
"v_mov_b32 v229,v226\n"
MF"v[52:55],v[208:211],a[40:43],v[52:55]\n"
RD"v[200:203],v242 offset:26624\n"
"v_max_u32_dpp v229,v229,v229 row_shr:1 row_mask:0xf bank_mask:0xf\n"
MF"v[52:55],v[212:215],a[56:59],v[52:55]\n"
PK"v20,v144,v145\n"
"v_max_u32_dpp v229,v229,v229 row_shr:2 row_mask:0xf bank_mask:0xf\n"
MF"v[60:63],v[212:215],a[60:63],v[60:63]\n"
PK"v21,v146,v147\n"
"v_max_u32_dpp v229,v229,v229 row_shr:4 row_mask:0xf bank_mask:0xf\n"
MF"v[60:63],v[208:211],a[44:47],v[60:63]\n"
WT"lgkmcnt(2)\n"
"v_max_u32_dpp v229,v229,v229 row_shr:8 row_mask:0xf bank_mask:0xf\n"
PK"v22,v148,v149\n"
MF"v[68:71],v[216:219],a[32:35],v[68:71]\n"
RD"v[204:207],v243 offset:26624\n"
"v_max_u32_dpp v229,v229,v229 row_bcast:15 row_mask:0xa bank_mask:0xf\n"
MF"v[68:71],v[220:223],a[48:51],v[68:71]\n"
PK"v23,v150,v151\n"
"v_max_u32_dpp v229,v229,v229 row_bcast:31 row_mask:0xc bank_mask:0xf\n"
MF"v[76:79],v[220:223],a[52:55],v[76:79]\n"
"v_readlane_b32 s62,v229,63\n"
PK"v28,v152,v153\n"
MF"v[76:79],v[216:219],a[36:39],v[76:79]\n"
"v_cmp_eq_u32 vcc,s62,v226\n"
"v_writelane_b32 v225,s62,4\n"
MF"v[84:87],v[216:219],a[40:43],v[84:87]\n"
RD"v[208:211],v242 offset:28672\n"
"v_cndmask_b32 v226,v226,v227,vcc\n"
MF"v[84:87],v[220:223],a[56:59],v[84:87]\n"
"v_cndmask_b32_e64 v227,v227,0,vcc\n"
"v_mov_b32 v229,v226\n"
MF"v[92:95],v[220:223],a[60:63],v[92:95]\n"
PK"v29,v154,v155\n"
"v_max_u32_dpp v229,v229,v229 row_shr:1 row_mask:0xf bank_mask:0xf\n"
MF"v[92:95],v[216:219],a[44:47],v[92:95]\n"
PK"v30,v156,v157\n"
"v_max_u32_dpp v229,v229,v229 row_shr:2 row_mask:0xf bank_mask:0xf\n"
MF"v[36:39],v[192:195],a[64:67],v[36:39]\n"
WT"lgkmcnt(1)\n"
RD"v[212:215],v243 offset:28672\n"
"v_max_u32_dpp v229,v229,v229 row_shr:4 row_mask:0xf bank_mask:0xf\n"
MF"v[36:39],v[196:199],a[80:83],v[36:39]\n"
PK"v31,v158,v159\n"
"v_max_u32_dpp v229,v229,v229 row_shr:8 row_mask:0xf bank_mask:0xf\n"
MF"v[44:47],v[196:199],a[84:87],v[44:47]\n"
MF"v[44:47],v[192:195],a[68:71],v[44:47]\n"
RD"v[216:219],v242 offset:30720\n"
"v_max_u32_dpp v229,v229,v229 row_bcast:15 row_mask:0xa bank_mask:0xf\n"
MF"v[52:55],v[192:195],a[72:75],v[52:55]\n"
MF"v[52:55],v[196:199],a[88:91],v[52:55]\n"
"v_max_u32_dpp v229,v229,v229 row_bcast:31 row_mask:0xc bank_mask:0xf\n"
MF"v[60:63],v[196:199],a[92:95],v[60:63]\n"
"v_readlane_b32 s62,v229,63\n"
MF"v[60:63],v[192:195],a[76:79],v[60:63]\n"
RD"v[220:223],v243 offset:30720\n"
"v_cmp_eq_u32 vcc,s62,v226\n"
MF"v[68:71],v[200:203],a[64:67],v[68:71]\n"
"v_writelane_b32 v225,s62,5\n"
"v_cndmask_b32 v226,v226,v227,vcc\n"
MF"v[68:71],v[204:207],a[80:83],v[68:71]\n"
"v_cndmask_b32_e64 v227,v227,0,vcc\n"
"v_mov_b32 v229,v226\n"
MF"v[76:79],v[204:207],a[84:87],v[76:79]\n"
MF"v[76:79],v[200:203],a[68:71],v[76:79]\n"
RD"v[192:195],v244 offset:0\n"
"v_max_u32_dpp v229,v229,v229 row_shr:1 row_mask:0xf bank_mask:0xf\n"
MF"v[84:87],v[200:203],a[72:75],v[84:87]\n"
MF"v[84:87],v[204:207],a[88:91],v[84:87]\n"
BS"v[40:43],v250,s[8:11],s49 offen offset:1024 sc1\n"
MF"v[92:95],v[204:207],a[92:95],v[92:95]\n"
"v_max_u32_dpp v229,v229,v229 row_shr:2 row_mask:0xf bank_mask:0xf\n"
MF"v[92:95],v[200:203],a[76:79],v[92:95]\n"
WT"lgkmcnt(1)\n"
RD"v[196:199],v245 offset:0\n"
"v_max_u32_dpp v229,v229,v229 row_shr:4 row_mask:0xf bank_mask:0xf\n"
MF"v[36:39],v[208:211],a[96:99],v[36:39]\n"
MF"v[36:39],v[212:215],a[112:115],v[36:39]\n"
"v_max_u32_dpp v229,v229,v229 row_shr:8 row_mask:0xf bank_mask:0xf\n"
MF"v[44:47],v[212:215],a[116:119],v[44:47]\n"
MF"v[44:47],v[208:211],a[100:103],v[44:47]\n"
RD"v[200:203],v244 offset:2048\n"
"v_max_u32_dpp v229,v229,v229 row_bcast:15 row_mask:0xa bank_mask:0xf\n"
MF"v[52:55],v[208:211],a[104:107],v[52:55]\n"
MF"v[52:55],v[212:215],a[120:123],v[52:55]\n"
"v_max_u32_dpp v229,v229,v229 row_bcast:31 row_mask:0xc bank_mask:0xf\n"
MF"v[60:63],v[212:215],a[124:127],v[60:63]\n"
RD"v[204:207],v245 offset:2048\n"
"v_readlane_b32 s62,v229,63\n"
MF"v[60:63],v[208:211],a[108:111],v[60:63]\n"
MF"v[68:71],v[216:219],a[96:99],v[68:71]\n"
"v_cmp_eq_u32 vcc,s62,v226\n"
"v_writelane_b32 v225,s62,6\n"
MF"v[68:71],v[220:223],a[112:115],v[68:71]\n"
"v_cndmask_b32 v226,v226,v227,vcc\n"
"v_cndmask_b32_e64 v227,v227,0,vcc\n"
MF"v[76:79],v[220:223],a[116:119],v[76:79]\n"
SA"m0,s37,16384\n"
RD"v[208:211],v244 offset:4096\n"
"v_mov_b32 v229,v226\n"
MF"v[76:79],v[216:219],a[100:103],v[76:79]\n"
SA"s48,s39,98304\n"
"v_max_u32_dpp v229,v229,v229 row_shr:1 row_mask:0xf bank_mask:0xf\n"
MF"v[84:87],v[216:219],a[104:107],v[84:87]\n"
BL"v234,s[12:15],s48 offen offset:0 lds\n"
MF"v[84:87],v[220:223],a[120:123],v[84:87]\n"
"v_max_u32_dpp v229,v229,v229 row_shr:2 row_mask:0xf bank_mask:0xf\n"
PK"v96,v96,v97\n"
MF"v[92:95],v[220:223],a[124:127],v[92:95]\n"
RD"v[212:215],v245 offset:4096\n"
"v_max_u32_dpp v229,v229,v229 row_shr:4 row_mask:0xf bank_mask:0xf\n"
MF"v[92:95],v[216:219],a[108:111],v[92:95]\n"
WT"lgkmcnt(2)\n"
"v_max_u32_dpp v229,v229,v229 row_shr:8 row_mask:0xf bank_mask:0xf\n"
PK"v97,v98,v99\n"
MF"v[36:39],v[192:195],a[128:131],v[36:39]\n"
"v_max_u32_dpp v229,v229,v229 row_bcast:15 row_mask:0xa bank_mask:0xf\n"
PK"v98,v100,v101\n"
MF"v[36:39],v[196:199],a[144:147],v[36:39]\n"
"v_max_u32_dpp v229,v229,v229 row_bcast:31 row_mask:0xc bank_mask:0xf\n"
PK"v99,v102,v103\n"
MF"v[44:47],v[196:199],a[148:151],v[44:47]\n"
RD"v[216:219],v244 offset:6144\n"
"v_readlane_b32 s62,v229,63\n"
MF"v[44:47],v[192:195],a[132:135],v[44:47]\n"
BL"v235,s[12:15],s48 offen offset:1024 lds\n"
MF"v[52:55],v[192:195],a[136:139],v[52:55]\n"
"v_cmp_eq_u32 vcc,s62,v226\n"
"v_writelane_b32 v225,s62,7\n"
MF"v[52:55],v[196:199],a[152:155],v[52:55]\n"
RD"v[220:223],v245 offset:6144\n"
"v_cndmask_b32 v226,v226,v227,vcc\n"
MF"v[60:63],v[196:199],a[156:159],v[60:63]\n"
"v_cndmask_b32_e64 v227,v227,0,vcc\n"
"v_mov_b32 v229,v228\n"
MF"v[60:63],v[192:195],a[140:143],v[60:63]\n"
PK"v104,v104,v105\n"
"v_max_u32_dpp v229,v229,v229 row_shr:1 row_mask:0xf bank_mask:0xf\n"
MF"v[68:71],v[200:203],a[128:131],v[68:71]\n"
BL"v236,s[12:15],s48 offen offset:2048 lds\n"
MF"v[68:71],v[204:207],a[144:147],v[68:71]\n"
RD"v[192:195],v244 offset:24576\n"
"v_max_u32_dpp v229,v229,v229 row_shr:2 row_mask:0xf bank_mask:0xf\n"
MF"v[76:79],v[204:207],a[148:151],v[76:79]\n"
PK"v105,v106,v107\n"
"v_max_u32_dpp v229,v229,v229 row_shr:4 row_mask:0xf bank_mask:0xf\n"
MF"v[76:79],v[200:203],a[132:135],v[76:79]\n"
PK"v106,v108,v109\n"
"v_max_u32_dpp v229,v229,v229 row_shr:8 row_mask:0xf bank_mask:0xf\n"
MF"v[84:87],v[200:203],a[136:139],v[84:87]\n"
BS"v[48:51],v250,s[8:11],s49 offen offset:2048 sc1\n"
MF"v[84:87],v[204:207],a[152:155],v[84:87]\n"
RD"v[196:199],v245 offset:24576\n"
"v_max_u32_dpp v229,v229,v229 row_bcast:15 row_mask:0xa bank_mask:0xf\n"
MF"v[92:95],v[204:207],a[156:159],v[92:95]\n"
BL"v237,s[12:15],s48 offen offset:3072 lds\n"
MF"v[92:95],v[200:203],a[140:143],v[92:95]\n"
WT"lgkmcnt(2)\n"
"v_max_u32_dpp v229,v229,v229 row_bcast:31 row_mask:0xc bank_mask:0xf\n"
PK"v107,v110,v111\n"
MF"v[36:39],v[208:211],a[160:163],v[36:39]\n"
"v_readlane_b32 s63,v229,63\n"
PK"v112,v112,v113\n"
MF"v[36:39],v[212:215],a[176:179],v[36:39]\n"
"s_cmp_gt_u32 s63,s62\n"
"s_cbranch_scc1 L1007_%=\n"
"L1008_%=:\n"
MF"v[44:47],v[212:215],a[180:183],v[44:47]\n"
SA"m0,s37,20480\n"
RD"v[200:203],v244 offset:26624\n"
"v_lshrrev_b32 v229,2,v224\n"
MF"v[44:47],v[208:211],a[164:167],v[44:47]\n"
SA"s48,s98,98304\n"
"ds_write_b32 v229,v225 offset:16384\n"
MF"v[52:55],v[208:211],a[168:171],v[52:55]\n"
BL"v234,s[12:15],s48 offen offset:0 lds\n"
MF"v[52:55],v[212:215],a[184:187],v[52:55]\n"
RD"v[204:207],v245 offset:26624\n"
PK"v113,v114,v115\n"
MF"v[60:63],v[212:215],a[188:191],v[60:63]\n"
PK"v114,v116,v117\n"
PK"v115,v118,v119\n"
MF"v[60:63],v[208:211],a[172:175],v[60:63]\n"
PK"v120,v120,v121\n"
PK"v121,v122,v123\n"
MF"v[68:71],v[216:219],a[160:163],v[68:71]\n"
RD"v[208:211],v244 offset:28672\n"
PK"v122,v124,v125\n"
MF"v[68:71],v[220:223],a[176:179],v[68:71]\n"
PK"v123,v126,v127\n"
MF"v[76:79],v[220:223],a[180:183],v[76:79]\n"
BL"v235,s[12:15],s48 offen offset:1024 lds\n"
MF"v[76:79],v[216:219],a[164:167],v[76:79]\n"
MF"v[84:87],v[216:219],a[168:171],v[84:87]\n"
RD"v[212:215],v245 offset:28672\n"
MF"v[84:87],v[220:223],a[184:187],v[84:87]\n"
MF"v[92:95],v[220:223],a[188:191],v[92:95]\n"
MF"v[92:95],v[216:219],a[172:175],v[92:95]\n"
WT"lgkmcnt(1)\n"
MF"v[36:39],v[192:195],a[192:195],v[36:39]\n"
RD"v[216:219],v244 offset:30720\n"
MF"v[36:39],v[196:199],a[208:211],v[36:39]\n"
BL"v236,s[12:15],s48 offen offset:2048 lds\n"
MF"v[44:47],v[196:199],a[212:215],v[44:47]\n"
MF"v[44:47],v[192:195],a[196:199],v[44:47]\n"
MF"v[52:55],v[192:195],a[200:203],v[52:55]\n"
RD"v[220:223],v245 offset:30720\n"
MF"v[52:55],v[196:199],a[216:219],v[52:55]\n"
MF"v[60:63],v[196:199],a[220:223],v[60:63]\n"
BL"v237,s[12:15],s48 offen offset:3072 lds\n"
MF"v[60:63],v[192:195],a[204:207],v[60:63]\n"
MF"v[68:71],v[200:203],a[192:195],v[68:71]\n"
MF"v[68:71],v[204:207],a[208:211],v[68:71]\n"
MF"v[76:79],v[204:207],a[212:215],v[76:79]\n"
MF"v[76:79],v[200:203],a[196:199],v[76:79]\n"
BS"v[56:59],v250,s[8:11],s49 offen offset:3072 sc1\n"
MF"v[84:87],v[200:203],a[200:203],v[84:87]\n"
"v_lshrrev_b32 v251,2,v224\n"
"v_and_b32 v251,28,v251\n"
MF"v[84:87],v[204:207],a[216:219],v[84:87]\n"
WT"lgkmcnt(0)\n"
"s_barrier\n"
MF"v[92:95],v[204:207],a[220:223],v[92:95]\n"
RD"v[192:195],v242 offset:8192\n"
"ds_read_b32 v234,v251 offset:16384\n"
MF"v[92:95],v[200:203],a[204:207],v[92:95]\n"
MF"v[36:39],v[208:211],a[224:227],v[36:39]\n"
MF"v[36:39],v[212:215],a[240:243],v[36:39]\n"
MF"v[44:47],v[212:215],a[244:247],v[44:47]\n"
RD"v[196:199],v243 offset:8192\n"
MF"v[44:47],v[208:211],a[228:231],v[44:47]\n"
MF"v[52:55],v[208:211],a[232:235],v[52:55]\n"
MF"v[52:55],v[212:215],a[248:251],v[52:55]\n"
MF"v[60:63],v[212:215],a[252:255],v[60:63]\n"
RD"v[200:203],v242 offset:10240\n"
MF"v[60:63],v[208:211],a[236:239],v[60:63]\n"
WT"lgkmcnt(2)\n"
"v_not_b32 v251,v234\n"
"v_bfe_u32 v252,v251,4,7\n"
MF"v[68:71],v[216:219],a[224:227],v[68:71]\n"
"v_mul_u32_u24 v252,20480,v252\n"
MF"v[68:71],v[220:223],a[240:243],v[68:71]\n"
"v_and_b32 v251,15,v251\n"
MF"v[76:79],v[220:223],a[244:247],v[76:79]\n"
RD"v[204:207],v243 offset:10240\n"
"v_lshl_add_u32 v251,v251,4,v252\n"
MF"v[76:79],v[216:219],a[228:231],v[76:79]\n"
"v_and_b32 v252,0x380,v224\n"
MF"v[84:87],v[216:219],a[232:235],v[84:87]\n"
"v_lshl_add_u32 v251,v252,1,v251\n"
MF"v[84:87],v[220:223],a[248:251],v[84:87]\n"
"v_add_u32 v252,v252,v224\n"
MF"v[92:95],v[220:223],a[252:255],v[92:95]\n"
"s_lshl_b32 s92,s34,2\n"
RD"v[208:211],v242 offset:12288\n"
MF"v[92:95],v[216:219],a[236:239],v[92:95]\n"
SA"s92,s92,s30\n"
MF"v[128:131],v[192:195],a[0:3],0\n"
WT"lgkmcnt(2)\n"
"s_lshl_b32 s93,s92,11\n"
MF"v[128:131],v[196:199],a[16:19],v[128:131]\n"
"s_cmp_lt_u32 s92,10\n"
RD"v[212:215],v243 offset:12288\n"
"s_cselect_b32 s93,s93,2130706432\n"
MF"v[136:139],v[196:199],a[20:23],0\n"
SA"s92,s93,65536\n"
MF"v[136:139],v[192:195],a[4:7],v[136:139]\n"
BL"v[100:103],v251,s[8:11],s92 offen sc1\n"
MF"v[144:147],v[192:195],a[8:11],0\n"
"s_mul_i32 s92,s35,20480\n"
PK"v36,v36,v37\n"
PK"v37,v38,v39\n"
MF"v[144:147],v[196:199],a[24:27],v[144:147]\n"
SA"s92,s92,s93\n"
RD"v[216:219],v242 offset:14336\n"
PK"v38,v68,v69\n"
MF"v[152:155],v[196:199],a[28:31],0\n"
SA"s92,s92,2770944\n"
PK"v39,v70,v71\n"
MF"v[152:155],v[192:195],a[12:15],v[152:155]\n"
MF"v[132:135],v[200:203],a[0:3],0\n"
WT"lgkmcnt(1)\n"
PK"v44,v44,v45\n"
PK"v45,v46,v47\n"
MF"v[132:135],v[204:207],a[16:19],v[132:135]\n"
"s_mov_b32 s49,16384\n"
RD"v[220:223],v243 offset:14336\n"
PK"v46,v76,v77\n"
MF"v[140:143],v[204:207],a[20:23],0\n"
BS"v[64:67],v250,s[8:11],s49 offen offset:0 sc1\n"
MF"v[140:143],v[200:203],a[4:7],v[140:143]\n"
PK"v47,v78,v79\n"
MF"v[148:151],v[200:203],a[8:11],0\n"
MF"v[148:151],v[204:207],a[24:27],v[148:151]\n"
RD"v[192:195],v242 offset:32768\n"
PK"v52,v52,v53\n"
MF"v[156:159],v[204:207],a[28:31],0\n"
PK"v53,v54,v55\n"
PK"v54,v84,v85\n"
MF"v[156:159],v[200:203],a[12:15],v[156:159]\n"
PK"v55,v86,v87\n"
MF"v[128:131],v[208:211],a[32:35],v[128:131]\n"
MF"v[128:131],v[212:215],a[48:51],v[128:131]\n"
RD"v[196:199],v243 offset:32768\n"
MF"v[136:139],v[212:215],a[52:55],v[136:139]\n"
PK"v60,v60,v61\n"
PK"v61,v62,v63\n"
MF"v[136:139],v[208:211],a[36:39],v[136:139]\n"
PK"v62,v92,v93\n"
PK"v63,v94,v95\n"
MF"v[144:147],v[208:211],a[40:43],v[144:147]\n"
SA"m0,s36,0\n"
RD"v[200:203],v242 offset:34816\n"
SA"s48,s60,s61\n"
MF"v[144:147],v[212:215],a[56:59],v[144:147]\n"
MF"v[152:155],v[212:215],a[60:63],v[152:155]\n"
BL"v238,s[20:23],s48 offen offset:0 lds\n"
MF"v[152:155],v[208:211],a[44:47],v[152:155]\n"
WT"lgkmcnt(2)\n"
MF"v[132:135],v[216:219],a[32:35],v[132:135]\n"
RD"v[204:207],v243 offset:34816\n"
MF"v[132:135],v[220:223],a[48:51],v[132:135]\n"
MF"v[140:143],v[220:223],a[52:55],v[140:143]\n"
MF"v[140:143],v[216:219],a[36:39],v[140:143]\n"
MF"v[148:151],v[216:219],a[40:43],v[148:151]\n"
RD"v[208:211],v242 offset:36864\n"
MF"v[148:151],v[220:223],a[56:59],v[148:151]\n"
MF"v[156:159],v[220:223],a[60:63],v[156:159]\n"
BL"v239,s[20:23],s48 offen offset:1024 lds\n"
MF"v[156:159],v[216:219],a[44:47],v[156:159]\n"
MF"v[128:131],v[192:195],a[64:67],v[128:131]\n"
WT"lgkmcnt(1)\n"
RD"v[212:215],v243 offset:36864\n"
MF"v[128:131],v[196:199],a[80:83],v[128:131]\n"
MF"v[136:139],v[196:199],a[84:87],v[136:139]\n"
MF"v[136:139],v[192:195],a[68:71],v[136:139]\n"
RD"v[216:219],v242 offset:38912\n"
MF"v[144:147],v[192:195],a[72:75],v[144:147]\n"
MF"v[144:147],v[196:199],a[88:91],v[144:147]\n"
MF"v[152:155],v[196:199],a[92:95],v[152:155]\n"
BL"v240,s[20:23],s48 offen offset:2048 lds\n"
MF"v[152:155],v[192:195],a[76:79],v[152:155]\n"
RD"v[220:223],v243 offset:38912\n"
MF"v[132:135],v[200:203],a[64:67],v[132:135]\n"
MF"v[132:135],v[204:207],a[80:83],v[132:135]\n"
BS"v[72:75],v250,s[8:11],s49 offen offset:1024 sc1\n"
MF"v[140:143],v[204:207],a[84:87],v[140:143]\n"
MF"v[140:143],v[200:203],a[68:71],v[140:143]\n"
RD"v[192:195],v244 offset:8192\n"
MF"v[148:151],v[200:203],a[72:75],v[148:151]\n"
MF"v[148:151],v[204:207],a[88:91],v[148:151]\n"
MF"v[156:159],v[204:207],a[92:95],v[156:159]\n"
SA"s61,s61,524288\n"
BL"v241,s[20:23],s48 offen offset:3072 lds\n"
MF"v[156:159],v[200:203],a[76:79],v[156:159]\n"
WT"lgkmcnt(1)\n"
RD"v[196:199],v245 offset:8192\n"
MF"v[128:131],v[208:211],a[96:99],v[128:131]\n"
MF"v[128:131],v[212:215],a[112:115],v[128:131]\n"
MF"v[136:139],v[212:215],a[116:119],v[136:139]\n"
MF"v[136:139],v[208:211],a[100:103],v[136:139]\n"
RD"v[200:203],v244 offset:10240\n"
MF"v[144:147],v[208:211],a[104:107],v[144:147]\n"
MF"v[144:147],v[212:215],a[120:123],v[144:147]\n"
MF"v[152:155],v[212:215],a[124:127],v[152:155]\n"
RD"v[204:207],v245 offset:10240\n"
MF"v[152:155],v[208:211],a[108:111],v[152:155]\n"
MF"v[132:135],v[216:219],a[96:99],v[132:135]\n"
MF"v[132:135],v[220:223],a[112:115],v[132:135]\n"
MF"v[140:143],v[220:223],a[116:119],v[140:143]\n"
RD"v[208:211],v244 offset:12288\n"
MF"v[140:143],v[216:219],a[100:103],v[140:143]\n"
MF"v[148:151],v[216:219],a[104:107],v[148:151]\n"
SA"m0,s38,65536\n"
SA"s48,s60,s61\n"
MF"v[148:151],v[220:223],a[120:123],v[148:151]\n"
MF"v[156:159],v[220:223],a[124:127],v[156:159]\n"
RD"v[212:215],v245 offset:12288\n"
MF"v[156:159],v[216:219],a[108:111],v[156:159]\n"
WT"lgkmcnt(2)\n"
BL"v238,s[20:23],s48 offen offset:0 lds\n"
MF"v[128:131],v[192:195],a[128:131],v[128:131]\n"
MF"v[128:131],v[196:199],a[144:147],v[128:131]\n"
MF"v[136:139],v[196:199],a[148:151],v[136:139]\n"
RD"v[216:219],v244 offset:14336\n"
MF"v[136:139],v[192:195],a[132:135],v[136:139]\n"
MF"v[144:147],v[192:195],a[136:139],v[144:147]\n"
MF"v[144:147],v[196:199],a[152:155],v[144:147]\n"
RD"v[220:223],v245 offset:14336\n"
MF"v[152:155],v[196:199],a[156:159],v[152:155]\n"
BL"v239,s[20:23],s48 offen offset:1024 lds\n"
MF"v[152:155],v[192:195],a[140:143],v[152:155]\n"
MF"v[132:135],v[200:203],a[128:131],v[132:135]\n"
BS"v[80:83],v250,s[8:11],s49 offen offset:2048 sc1\n"
MF"v[132:135],v[204:207],a[144:147],v[132:135]\n"
RD"v[192:195],v244 offset:32768\n"
MF"v[140:143],v[204:207],a[148:151],v[140:143]\n"
MF"v[140:143],v[200:203],a[132:135],v[140:143]\n"
MF"v[148:151],v[200:203],a[136:139],v[148:151]\n"
MF"v[148:151],v[204:207],a[152:155],v[148:151]\n"
RD"v[196:199],v245 offset:32768\n"
MF"v[156:159],v[204:207],a[156:159],v[156:159]\n"
BL"v240,s[20:23],s48 offen offset:2048 lds\n"
MF"v[156:159],v[200:203],a[140:143],v[156:159]\n"
WT"lgkmcnt(2)\n"
MF"v[128:131],v[208:211],a[160:163],v[128:131]\n"
MF"v[128:131],v[212:215],a[176:179],v[128:131]\n"
RD"v[200:203],v244 offset:34816\n"
MF"v[136:139],v[212:215],a[180:183],v[136:139]\n"
MF"v[136:139],v[208:211],a[164:167],v[136:139]\n"
MF"v[144:147],v[208:211],a[168:171],v[144:147]\n"
MF"v[144:147],v[212:215],a[184:187],v[144:147]\n"
RD"v[204:207],v245 offset:34816\n"
MF"v[152:155],v[212:215],a[188:191],v[152:155]\n"
SA"s61,s61,524288\n"
BL"v241,s[20:23],s48 offen offset:3072 lds\n"
MF"v[152:155],v[208:211],a[172:175],v[152:155]\n"
"s_cmp_eq_u32 s61,5242880\n"
"s_cselect_b32 s61,0,s61\n"
MF"v[132:135],v[216:219],a[160:163],v[132:135]\n"
RD"v[208:211],v244 offset:36864\n"
MF"v[132:135],v[220:223],a[176:179],v[132:135]\n"
MF"v[140:143],v[220:223],a[180:183],v[140:143]\n"
MF"v[140:143],v[216:219],a[164:167],v[140:143]\n"
MF"v[148:151],v[216:219],a[168:171],v[148:151]\n"
SA"m0,s38,114688\n"
RD"v[212:215],v245 offset:36864\n"
SA"s48,s60,s61\n"
MF"v[148:151],v[220:223],a[184:187],v[148:151]\n"
MF"v[156:159],v[220:223],a[188:191],v[156:159]\n"
BL"v238,s[20:23],s48 offen offset:0 lds\n"
MF"v[156:159],v[216:219],a[172:175],v[156:159]\n"
WT"lgkmcnt(1)\n"
MF"v[128:131],v[192:195],a[192:195],v[128:131]\n"
RD"v[216:219],v244 offset:38912\n"
MF"v[128:131],v[196:199],a[208:211],v[128:131]\n"
MF"v[136:139],v[196:199],a[212:215],v[136:139]\n"
MF"v[136:139],v[192:195],a[196:199],v[136:139]\n"
MF"v[144:147],v[192:195],a[200:203],v[144:147]\n"
RD"v[220:223],v245 offset:38912\n"
MF"v[144:147],v[196:199],a[216:219],v[144:147]\n"
MF"v[152:155],v[196:199],a[220:223],v[152:155]\n"
BL"v239,s[20:23],s48 offen offset:1024 lds\n"
MF"v[152:155],v[192:195],a[204:207],v[152:155]\n"
BS"v[88:91],v250,s[8:11],s49 offen offset:3072 sc1\n"
MF"v[132:135],v[200:203],a[192:195],v[132:135]\n"
WT"vmcnt(14)\n"
BS"v[100:103],v252,s[8:11],s92 offen sc1\n"
MF"v[132:135],v[204:207],a[208:211],v[132:135]\n"
MF"v[140:143],v[204:207],a[212:215],v[140:143]\n"
MF"v[140:143],v[200:203],a[196:199],v[140:143]\n"
MF"v[148:151],v[200:203],a[200:203],v[148:151]\n"
MF"v[148:151],v[204:207],a[216:219],v[148:151]\n"
WT"lgkmcnt(0)\n"
"s_barrier\n"
MF"v[156:159],v[204:207],a[220:223],v[156:159]\n"
RD"v[192:195],v242 offset:16384\n"
MF"v[156:159],v[200:203],a[204:207],v[156:159]\n"
BL"v240,s[20:23],s48 offen offset:2048 lds\n"
MF"v[128:131],v[208:211],a[224:227],v[128:131]\n"
MF"v[128:131],v[212:215],a[240:243],v[128:131]\n"
MF"v[136:139],v[212:215],a[244:247],v[136:139]\n"
RD"v[196:199],v243 offset:16384\n"
MF"v[136:139],v[208:211],a[228:231],v[136:139]\n"
MF"v[144:147],v[208:211],a[232:235],v[144:147]\n"
MF"v[144:147],v[212:215],a[248:251],v[144:147]\n"
MF"v[152:155],v[212:215],a[252:255],v[152:155]\n"
SA"s61,s61,524288\n"
RD"v[200:203],v242 offset:18432\n"
MF"v[152:155],v[208:211],a[236:239],v[152:155]\n"
BL"v241,s[20:23],s48 offen offset:3072 lds\n"
MF"v[132:135],v[216:219],a[224:227],v[132:135]\n"
"s_lshl_b32 s50,s34,3\n"
MF"v[132:135],v[220:223],a[240:243],v[132:135]\n"
SA"s51,s50,s30\n"
MF"v[140:143],v[220:223],a[244:247],v[140:143]\n"
"s_mul_i32 s51,s51,20480\n"
RD"v[204:207],v243 offset:18432\n"
MF"v[140:143],v[216:219],a[228:231],v[140:143]\n"
SA"s56,s51,65536\n"
MF"v[148:151],v[216:219],a[232:235],v[148:151]\n"
SA"m0,s36,16384\n"
SA"s48,s60,s61\n"
SA"s57,s56,81920\n"
MF"v[148:151],v[220:223],a[248:251],v[148:151]\n"
"s_sub_u32 s51,s30,s50\n"
MF"v[156:159],v[220:223],a[252:255],v[156:159]\n"
SA"s51,s51,120\n"
RD"v[208:211],v242 offset:20480\n"
MF"v[156:159],v[216:219],a[236:239],v[156:159]\n"
WT"lgkmcnt(2)\n"
BL"v238,s[20:23],s48 offen offset:0 lds\n"
MF"v[0:3],v[192:195],a[0:3],0\n"
"s_mul_i32 s51,s51,20480\n"
SA"s58,s51,65536\n"
"s_lshl_b32 s99,s99,12\n"
MF"v[0:3],v[196:199],a[16:19],v[0:3]\n"
SA"s59,s58,81920\n"
RD"v[212:215],v243 offset:20480\n"
MF"v[16:19],v[196:199],a[20:23],0\n"
"v_add_u32 v235,s99,v224\n"
MF"v[16:19],v[192:195],a[4:7],v[16:19]\n"
PK"v64,v128,v129\n"
PK"v65,v130,v131\n"
MF"v[32:35],v[192:195],a[8:11],0\n"
"s_mov_b32 s49,24576\n"
PK"v66,v132,v133\n"
PK"v67,v134,v135\n"
MF"v[32:35],v[196:199],a[24:27],v[32:35]\n"
RD"v[216:219],v242 offset:22528\n"
MF"v[48:51],v[196:199],a[28:31],0\n"
BS"v[96:99],v250,s[8:11],s49 offen offset:0 sc1\n"
MF"v[48:51],v[192:195],a[12:15],v[48:51]\n"
BL"v239,s[20:23],s48 offen offset:1024 lds\n"
MF"v[8:11],v[200:203],a[0:3],0\n"
WT"lgkmcnt(1)\n"
PK"v68,v136,v137\n"
PK"v69,v138,v139\n"
MF"v[8:11],v[204:207],a[16:19],v[8:11]\n"
RD"v[220:223],v243 offset:22528\n"
PK"v70,v140,v141\n"
MF"v[24:27],v[204:207],a[20:23],0\n"
PK"v71,v142,v143\n"
MF"v[24:27],v[200:203],a[4:7],v[24:27]\n"
MF"v[40:43],v[200:203],a[8:11],0\n"
MF"v[40:43],v[204:207],a[24:27],v[40:43]\n"
RD"v[192:195],v242 offset:40960\n"
PK"v72,v144,v145\n"
MF"v[56:59],v[204:207],a[28:31],0\n"
BL"v240,s[20:23],s48 offen offset:2048 lds\n"
MF"v[56:59],v[200:203],a[12:15],v[56:59]\n"
PK"v73,v146,v147\n"
PK"v74,v148,v149\n"
MF"v[0:3],v[208:211],a[32:35],v[0:3]\n"
BL"a[0:3],v235,s[8:11],s56 offen offset:0 sc1\n"
MF"v[0:3],v[212:215],a[48:51],v[0:3]\n"
RD"v[196:199],v243 offset:40960\n"
PK"v75,v150,v151\n"
MF"v[16:19],v[212:215],a[52:55],v[16:19]\n"
PK"v76,v152,v153\n"
PK"v77,v154,v155\n"
MF"v[16:19],v[208:211],a[36:39],v[16:19]\n"
PK"v78,v156,v157\n"
PK"v79,v158,v159\n"
MF"v[32:35],v[208:211],a[40:43],v[32:35]\n"
RD"v[200:203],v242 offset:43008\n"
MF"v[32:35],v[212:215],a[56:59],v[32:35]\n"
MF"v[48:51],v[212:215],a[60:63],v[48:51]\n"
SA"s61,s61,524288\n"
BL"v241,s[20:23],s48 offen offset:3072 lds\n"
MF"v[48:51],v[208:211],a[44:47],v[48:51]\n"
WT"lgkmcnt(2)\n"
BL"a[4:7],v235,s[8:11],s57 offen offset:0 sc1\n"
MF"v[8:11],v[216:219],a[32:35],v[8:11]\n"
"s_cmp_eq_u32 s61,5242880\n"
RD"v[204:207],v243 offset:43008\n"
"s_cselect_b32 s61,0,s61\n"
MF"v[8:11],v[220:223],a[48:51],v[8:11]\n"
MF"v[24:27],v[220:223],a[52:55],v[24:27]\n"
MF"v[24:27],v[216:219],a[36:39],v[24:27]\n"
MF"v[40:43],v[216:219],a[40:43],v[40:43]\n"
RD"v[208:211],v242 offset:45056\n"
MF"v[40:43],v[220:223],a[56:59],v[40:43]\n"
BL"a[8:11],v235,s[8:11],s58 offen offset:0 sc1\n"
MF"v[56:59],v[220:223],a[60:63],v[56:59]\n"
MF"v[56:59],v[216:219],a[44:47],v[56:59]\n"
MF"v[0:3],v[192:195],a[64:67],v[0:3]\n"
WT"lgkmcnt(1)\n"
RD"v[212:215],v243 offset:45056\n"
MF"v[0:3],v[196:199],a[80:83],v[0:3]\n"
MF"v[16:19],v[196:199],a[84:87],v[16:19]\n"
BL"a[12:15],v235,s[8:11],s59 offen offset:0 sc1\n"
MF"v[16:19],v[192:195],a[68:71],v[16:19]\n"
RD"v[216:219],v242 offset:47104\n"
MF"v[32:35],v[192:195],a[72:75],v[32:35]\n"
MF"v[32:35],v[196:199],a[88:91],v[32:35]\n"
BS"v[104:107],v250,s[8:11],s49 offen offset:1024 sc1\n"
MF"v[48:51],v[196:199],a[92:95],v[48:51]\n"
MF"v[48:51],v[192:195],a[76:79],v[48:51]\n"
RD"v[220:223],v243 offset:47104\n"
MF"v[8:11],v[200:203],a[64:67],v[8:11]\n"
BL"a[16:19],v235,s[8:11],s56 offen offset:1024 sc1\n"
MF"v[8:11],v[204:207],a[80:83],v[8:11]\n"
MF"v[24:27],v[204:207],a[84:87],v[24:27]\n"
MF"v[24:27],v[200:203],a[68:71],v[24:27]\n"
RD"v[192:195],v244 offset:16384\n"
MF"v[40:43],v[200:203],a[72:75],v[40:43]\n"
MF"v[40:43],v[204:207],a[88:91],v[40:43]\n"
MF"v[56:59],v[204:207],a[92:95],v[56:59]\n"
BL"a[20:23],v235,s[8:11],s57 offen offset:1024 sc1\n"
MF"v[56:59],v[200:203],a[76:79],v[56:59]\n"
WT"lgkmcnt(1)\n"
RD"v[196:199],v245 offset:16384\n"
MF"v[0:3],v[208:211],a[96:99],v[0:3]\n"
MF"v[0:3],v[212:215],a[112:115],v[0:3]\n"
MF"v[16:19],v[212:215],a[116:119],v[16:19]\n"
MF"v[16:19],v[208:211],a[100:103],v[16:19]\n"
RD"v[200:203],v244 offset:18432\n"
MF"v[32:35],v[208:211],a[104:107],v[32:35]\n"
BL"a[24:27],v235,s[8:11],s58 offen offset:1024 sc1\n"
MF"v[32:35],v[212:215],a[120:123],v[32:35]\n"
MF"v[48:51],v[212:215],a[124:127],v[48:51]\n"
RD"v[204:207],v245 offset:18432\n"
MF"v[48:51],v[208:211],a[108:111],v[48:51]\n"
MF"v[8:11],v[216:219],a[96:99],v[8:11]\n"
MF"v[8:11],v[220:223],a[112:115],v[8:11]\n"
MF"v[24:27],v[220:223],a[116:119],v[24:27]\n"
RD"v[208:211],v244 offset:20480\n"
MF"v[24:27],v[216:219],a[100:103],v[24:27]\n"
BL"a[28:31],v235,s[8:11],s59 offen offset:1024 sc1\n"
MF"v[40:43],v[216:219],a[104:107],v[40:43]\n"
MF"v[40:43],v[220:223],a[120:123],v[40:43]\n"
MF"v[56:59],v[220:223],a[124:127],v[56:59]\n"
RD"v[212:215],v245 offset:20480\n"
MF"v[56:59],v[216:219],a[108:111],v[56:59]\n"
WT"lgkmcnt(2)\n"
MF"v[0:3],v[192:195],a[128:131],v[0:3]\n"
BL"a[32:35],v235,s[8:11],s56 offen offset:2048 sc1\n"
MF"v[0:3],v[196:199],a[144:147],v[0:3]\n"
MF"v[16:19],v[196:199],a[148:151],v[16:19]\n"
RD"v[216:219],v244 offset:22528\n"
MF"v[16:19],v[192:195],a[132:135],v[16:19]\n"
MF"v[32:35],v[192:195],a[136:139],v[32:35]\n"
BS"v[112:115],v250,s[8:11],s49 offen offset:2048 sc1\n"
MF"v[32:35],v[196:199],a[152:155],v[32:35]\n"
RD"v[220:223],v245 offset:22528\n"
MF"v[48:51],v[196:199],a[156:159],v[48:51]\n"
BL"a[36:39],v235,s[8:11],s57 offen offset:2048 sc1\n"
MF"v[48:51],v[192:195],a[140:143],v[48:51]\n"
MF"v[8:11],v[200:203],a[128:131],v[8:11]\n"
MF"v[8:11],v[204:207],a[144:147],v[8:11]\n"
RD"v[192:195],v244 offset:40960\n"
MF"v[24:27],v[204:207],a[148:151],v[24:27]\n"
MF"v[24:27],v[200:203],a[132:135],v[24:27]\n"
MF"v[40:43],v[200:203],a[136:139],v[40:43]\n"
BL"a[40:43],v235,s[8:11],s58 offen offset:2048 sc1\n"
MF"v[40:43],v[204:207],a[152:155],v[40:43]\n"
RD"v[196:199],v245 offset:40960\n"
MF"v[56:59],v[204:207],a[156:159],v[56:59]\n"
MF"v[56:59],v[200:203],a[140:143],v[56:59]\n"
WT"lgkmcnt(2)\n"
MF"v[0:3],v[208:211],a[160:163],v[0:3]\n"
"s_sub_u32 s53,s37,65536\n"
MF"v[0:3],v[212:215],a[176:179],v[0:3]\n"
RD"v[200:203],v244 offset:43008\n"
MF"v[16:19],v[212:215],a[180:183],v[16:19]\n"
BL"a[44:47],v235,s[8:11],s59 offen offset:2048 sc1\n"
MF"v[16:19],v[208:211],a[164:167],v[16:19]\n"
"v_subrev_u32 v246,s53,v246\n"
"v_subrev_u32 v247,s53,v247\n"
MF"v[32:35],v[208:211],a[168:171],v[32:35]\n"
"v_subrev_u32 v248,s53,v248\n"
"v_subrev_u32 v249,s53,v249\n"
MF"v[32:35],v[212:215],a[184:187],v[32:35]\n"
RD"v[204:207],v245 offset:43008\n"
"v_subrev_u32 v236,65536,v246\n"
MF"v[48:51],v[212:215],a[188:191],v[48:51]\n"
"v_subrev_u32 v237,65536,v247\n"
"v_subrev_u32 v228,65536,v248\n"
MF"v[48:51],v[208:211],a[172:175],v[48:51]\n"
"v_subrev_u32 v229,65536,v249\n"
MF"v[8:11],v[216:219],a[160:163],v[8:11]\n"
RD"v[208:211],v244 offset:45056\n"
MF"v[8:11],v[220:223],a[176:179],v[8:11]\n"
BL"a[48:51],v235,s[8:11],s56 offen offset:3072 sc1\n"
MF"v[24:27],v[220:223],a[180:183],v[24:27]\n"
MF"v[24:27],v[216:219],a[164:167],v[24:27]\n"
MF"v[40:43],v[216:219],a[168:171],v[40:43]\n"
RD"v[212:215],v245 offset:45056\n"
MF"v[40:43],v[220:223],a[184:187],v[40:43]\n"
MF"v[56:59],v[220:223],a[188:191],v[56:59]\n"
BL"a[52:55],v235,s[8:11],s57 offen offset:3072 sc1\n"
MF"v[56:59],v[216:219],a[172:175],v[56:59]\n"
WT"lgkmcnt(1)\n"
MF"v[0:3],v[192:195],a[192:195],v[0:3]\n"
RD"v[216:219],v244 offset:47104\n"
MF"v[0:3],v[196:199],a[208:211],v[0:3]\n"
MF"v[16:19],v[196:199],a[212:215],v[16:19]\n"
MF"v[16:19],v[192:195],a[196:199],v[16:19]\n"
BS"v[120:123],v250,s[8:11],s49 offen offset:3072 sc1\n"
MF"v[32:35],v[192:195],a[200:203],v[32:35]\n"
RD"v[220:223],v245 offset:47104\n"
MF"v[32:35],v[196:199],a[216:219],v[32:35]\n"
BL"a[56:59],v235,s[8:11],s58 offen offset:3072 sc1\n"
MF"v[48:51],v[196:199],a[220:223],v[48:51]\n"
MF"v[48:51],v[192:195],a[204:207],v[48:51]\n"
MF"v[8:11],v[200:203],a[192:195],v[8:11]\n"
SA"s99,s99,4096\n"
MF"v[8:11],v[204:207],a[208:211],v[8:11]\n"
"s_cmp_eq_u32 s99,20480\n"
"s_cselect_b32 s99,0,s99\n"
MF"v[24:27],v[204:207],a[212:215],v[24:27]\n"
BL"a[60:63],v235,s[8:11],s59 offen offset:3072 sc1\n"
MF"v[24:27],v[200:203],a[196:199],v[24:27]\n"
MF"v[40:43],v[200:203],a[200:203],v[40:43]\n"
MF"v[40:43],v[204:207],a[216:219],v[40:43]\n"
WT"lgkmcnt(0)\n"
"s_barrier\n"
MF"v[56:59],v[204:207],a[220:223],v[56:59]\n"
RT"a[128:129],v236 offset:49152\n"
MF"v[56:59],v[200:203],a[204:207],v[56:59]\n"
"v_add_u32 v235,s99,v224\n"
MF"v[0:3],v[208:211],a[224:227],v[0:3]\n"
SA"m0,s38,73728\n"
RT"a[130:131],v236 offset:49664\n"
SA"s48,s60,s61\n"
MF"v[0:3],v[212:215],a[240:243],v[0:3]\n"
BL"a[64:67],v235,s[8:11],s56 offen offset:0 sc1\n"
MF"v[16:19],v[212:215],a[244:247],v[16:19]\n"
RT"a[132:133],v236 offset:57344\n"
"v_add_u32 v242,49152,v246\n"
MF"v[16:19],v[208:211],a[228:231],v[16:19]\n"
BL"v238,s[20:23],s48 offen offset:0 lds\n"
MF"v[32:35],v[208:211],a[232:235],v[32:35]\n"
RT"a[134:135],v236 offset:57856\n"
"v_add_u32 v243,49152,v247\n"
MF"v[32:35],v[212:215],a[248:251],v[32:35]\n"
"v_add_u32 v244,49152,v248\n"
"v_add_u32 v245,49152,v249\n"
MF"v[48:51],v[212:215],a[252:255],v[48:51]\n"
RT"a[136:137],v237 offset:49152\n"
MF"v[48:51],v[208:211],a[236:239],v[48:51]\n"
BL"a[68:71],v235,s[8:11],s57 offen offset:0 sc1\n"
MF"v[8:11],v[216:219],a[224:227],v[8:11]\n"
RT"a[138:139],v237 offset:49664\n"
MF"v[8:11],v[220:223],a[240:243],v[8:11]\n"
MF"v[24:27],v[220:223],a[244:247],v[24:27]\n"
RT"a[140:141],v237 offset:57344\n"
MF"v[24:27],v[216:219],a[228:231],v[24:27]\n"
BL"v239,s[20:23],s48 offen offset:1024 lds\n"
MF"v[40:43],v[216:219],a[232:235],v[40:43]\n"
RT"a[142:143],v237 offset:57856\n"
MF"v[40:43],v[220:223],a[248:251],v[40:43]\n"
BL"a[72:75],v235,s[8:11],s58 offen offset:0 sc1\n"
MF"v[56:59],v[220:223],a[252:255],v[56:59]\n"
RT"a[144:145],v228 offset:49152\n"
MF"v[56:59],v[216:219],a[236:239],v[56:59]\n"
WT"vmcnt(24) lgkmcnt(3)\n"
MF"v[80:83],a[128:131],a[0:3],0\n"
WT"vmcnt(18)\n"
RT"a[146:147],v228 offset:49664\n"
MF"v[80:83],a[132:135],a[16:19],v[80:83]\n"
WT"vmcnt(17)\n"
MF"v[88:91],a[132:135],a[20:23],0\n"
RT"a[148:149],v228 offset:57344\n"
MF"v[88:91],a[128:131],a[4:7],v[88:91]\n"
BL"v240,s[20:23],s48 offen offset:2048 lds\n"
MF"v[96:99],a[128:131],a[8:11],0\n"
WT"vmcnt(17)\n"
BL"a[76:79],v235,s[8:11],s59 offen offset:0 sc1\n"
MF"v[96:99],a[132:135],a[24:27],v[96:99]\n"
WT"vmcnt(17)\n"
RT"a[150:151],v228 offset:57856\n"
RT"a[152:153],v229 offset:49152\n"
MF"v[104:107],a[132:135],a[28:31],0\n"
PK"v0,v0,v1\n"
PK"v1,v2,v3\n"
MF"v[104:107],a[128:131],a[12:15],v[104:107]\n"
RT"a[154:155],v229 offset:49664\n"
PK"v2,v8,v9\n"
MF"v[84:87],a[136:139],a[0:3],0\n"
WT"lgkmcnt(4)\n"
PK"v3,v10,v11\n"
PK"v16,v16,v17\n"
MF"v[84:87],a[140:143],a[16:19],v[84:87]\n"
RT"a[156:157],v229 offset:57344\n"
PK"v17,v18,v19\n"
MF"v[92:95],a[140:143],a[20:23],0\n"
SA"s61,s61,524288\n"
BL"a[80:83],v235,s[8:11],s56 offen offset:1024 sc1\n"
MF"v[92:95],a[136:139],a[4:7],v[92:95]\n"
BL"v241,s[20:23],s48 offen offset:3072 lds\n"
MF"v[100:103],a[136:139],a[8:11],0\n"
RT"a[158:159],v229 offset:57856\n"
PK"v18,v24,v25\n"
MF"v[100:103],a[140:143],a[24:27],v[100:103]\n"
RT"a[160:161],v236 offset:53248\n"
PK"v19,v26,v27\n"
MF"v[108:111],a[140:143],a[28:31],0\n"
PK"v32,v32,v33\n"
PK"v33,v34,v35\n"
MF"v[108:111],a[136:139],a[12:15],v[108:111]\n"
RT"a[162:163],v236 offset:53760\n"
PK"v34,v40,v41\n"
MF"v[112:115],a[144:147],a[0:3],0\n"
WT"lgkmcnt(3)\n"
BL"a[84:87],v235,s[8:11],s57 offen offset:1024 sc1\n"
MF"v[112:115],a[148:151],a[16:19],v[112:115]\n"
RT"a[164:165],v236 offset:61440\n"
PK"v35,v42,v43\n"
MF"v[120:123],a[148:151],a[20:23],0\n"
PK"v48,v48,v49\n"
PK"v49,v50,v51\n"
MF"v[120:123],a[144:147],a[4:7],v[120:123]\n"
RT"a[166:167],v236 offset:61952\n"
PK"v50,v56,v57\n"
MF"v[128:131],a[144:147],a[8:11],0\n"
PK"v51,v58,v59\n"
MF"v[128:131],a[148:151],a[24:27],v[128:131]\n"
RT"a[168:169],v237 offset:53248\n"
MF"v[136:139],a[148:151],a[28:31],0\n"
MF"v[136:139],a[144:147],a[12:15],v[136:139]\n"
RT"a[170:171],v237 offset:53760\n"
MF"v[116:119],a[152:155],a[0:3],0\n"
WT"lgkmcnt(3)\n"
BL"a[88:91],v235,s[8:11],s58 offen offset:1024 sc1\n"
MF"v[116:119],a[156:159],a[16:19],v[116:119]\n"
"s_mov_b32 s49,262144\n"
RT"a[172:173],v237 offset:61440\n"
MF"v[124:127],a[156:159],a[20:23],0\n"
RT"a[174:175],v237 offset:61952\n"
MF"v[124:127],a[152:155],a[4:7],v[124:127]\n"
BS"v[4:7],v250,s[8:11],s49 offen offset:0 sc1\n"
MF"v[132:135],a[152:155],a[8:11],0\n"
RT"a[176:177],v228 offset:53248\n"
MF"v[132:135],a[156:159],a[24:27],v[132:135]\n"
MF"v[140:143],a[156:159],a[28:31],0\n"
RT"a[178:179],v228 offset:53760\n"
MF"v[140:143],a[152:155],a[12:15],v[140:143]\n"
BL"a[92:95],v235,s[8:11],s59 offen offset:1024 sc1\n"
MF"v[144:147],a[160:163],a[0:3],0\n"
WT"lgkmcnt(3)\n"
RT"a[180:181],v228 offset:61440\n"
MF"v[144:147],a[164:167],a[16:19],v[144:147]\n"
MF"v[152:155],a[164:167],a[20:23],0\n"
SA"m0,s38,122880\n"
RT"a[182:183],v228 offset:61952\n"
SA"s48,s60,s61\n"
MF"v[152:155],a[160:163],a[4:7],v[152:155]\n"
MF"v[160:163],a[160:163],a[8:11],0\n"
"s_mov_b32 s88,2048\n"
RT"a[184:185],v229 offset:53248\n"
MF"v[160:163],a[164:167],a[24:27],v[160:163]\n"
BL"v238,s[20:23],s48 offen offset:0 lds\n"
MF"v[168:171],a[164:167],a[28:31],0\n"
BL"a[96:99],v235,s[8:11],s56 offen offset:2048 sc1\n"
MF"v[168:171],a[160:163],a[12:15],v[168:171]\n"
RT"a[186:187],v229 offset:53760\n"
MF"v[148:151],a[168:171],a[0:3],0\n"
WT"lgkmcnt(3)\n"
RT"a[188:189],v229 offset:61440\n"
MF"v[148:151],a[172:175],a[16:19],v[148:151]\n"
MF"v[156:159],a[172:175],a[20:23],0\n"
RT"a[190:191],v229 offset:61952\n"
MF"v[156:159],a[168:171],a[4:7],v[156:159]\n"
RT"a[128:129],v236 offset:32768\n"
MF"v[164:167],a[168:171],a[8:11],0\n"
BL"v239,s[20:23],s48 offen offset:1024 lds\n"
MF"v[164:167],a[172:175],a[24:27],v[164:167]\n"
BL"a[100:103],v235,s[8:11],s57 offen offset:2048 sc1\n"
MF"v[172:175],a[172:175],a[28:31],0\n"
RT"a[130:131],v236 offset:33280\n"
MF"v[172:175],a[168:171],a[12:15],v[172:175]\n"
RT"a[132:133],v236 offset:40960\n"
MF"v[176:179],a[176:179],a[0:3],0\n"
WT"lgkmcnt(4)\n"
MF"v[176:179],a[180:183],a[16:19],v[176:179]\n"
RT"a[134:135],v236 offset:41472\n"
MF"v[184:187],a[180:183],a[20:23],0\n"
MF"v[184:187],a[176:179],a[4:7],v[184:187]\n"
RT"a[136:137],v237 offset:32768\n"
MF"v[192:195],a[176:179],a[8:11],0\n"
BL"a[104:107],v235,s[8:11],s58 offen offset:2048 sc1\n"
MF"v[192:195],a[180:183],a[24:27],v[192:195]\n"
BL"v240,s[20:23],s48 offen offset:2048 lds\n"
MF"v[200:203],a[180:183],a[28:31],0\n"
RT"a[138:139],v237 offset:33280\n"
MF"v[200:203],a[176:179],a[12:15],v[200:203]\n"
RT"a[140:141],v237 offset:40960\n"
MF"v[180:183],a[184:187],a[0:3],0\n"
WT"lgkmcnt(3)\n"
MF"v[180:183],a[188:191],a[16:19],v[180:183]\n"
RT"a[142:143],v237 offset:41472\n"
MF"v[188:191],a[188:191],a[20:23],0\n"
BL"a[108:111],v235,s[8:11],s59 offen offset:2048 sc1\n"
MF"v[188:191],a[184:187],a[4:7],v[188:191]\n"
RT"a[144:145],v228 offset:32768\n"
MF"v[196:199],a[184:187],a[8:11],0\n"
SA"s61,s61,524288\n"
BS"v[12:15],v250,s[8:11],s49 offen offset:1024 sc1\n"
MF"v[196:199],a[188:191],a[24:27],v[196:199]\n"
"s_cmp_eq_u32 s61,5242880\n"
BL"v241,s[20:23],s48 offen offset:3072 lds\n"
MF"v[204:207],a[188:191],a[28:31],0\n"
"s_cselect_b32 s61,0,s61\n"
RT"a[146:147],v228 offset:33280\n"
MF"v[204:207],a[184:187],a[12:15],v[204:207]\n"
WT"vmcnt(31)\n"
RT"a[148:149],v228 offset:40960\n"
MF"v[80:83],a[128:131],a[32:35],v[80:83]\n"
WT"vmcnt(26)\n"
MF"v[80:83],a[132:135],a[48:51],v[80:83]\n"
WT"vmcnt(25)\n"
RT"a[150:151],v228 offset:41472\n"
MF"v[88:91],a[132:135],a[52:55],v[88:91]\n"
BL"a[112:115],v235,s[8:11],s56 offen offset:3072 sc1\n"
MF"v[88:91],a[128:131],a[36:39],v[88:91]\n"
RT"a[152:153],v229 offset:32768\n"
MF"v[96:99],a[128:131],a[40:43],v[96:99]\n"
WT"vmcnt(24)\n"
RT"a[154:155],v229 offset:33280\n"
MF"v[96:99],a[132:135],a[56:59],v[96:99]\n"
WT"vmcnt(23)\n"
MF"v[104:107],a[132:135],a[60:63],v[104:107]\n"
RT"a[156:157],v229 offset:40960\n"
MF"v[104:107],a[128:131],a[44:47],v[104:107]\n"
WT"lgkmcnt(4)\n"
MF"v[84:87],a[136:139],a[32:35],v[84:87]\n"
RT"a[158:159],v229 offset:41472\n"
MF"v[84:87],a[140:143],a[48:51],v[84:87]\n"
BL"a[116:119],v235,s[8:11],s57 offen offset:3072 sc1\n"
MF"v[92:95],a[140:143],a[52:55],v[92:95]\n"
RT"a[160:161],v236 offset:36864\n"
MF"v[92:95],a[136:139],a[36:39],v[92:95]\n"
MF"v[100:103],a[136:139],a[40:43],v[100:103]\n"
RT"a[162:163],v236 offset:37376\n"
MF"v[100:103],a[140:143],a[56:59],v[100:103]\n"
MF"v[108:111],a[140:143],a[60:63],v[108:111]\n"
RT"a[164:165],v236 offset:45056\n"
MF"v[108:111],a[136:139],a[44:47],v[108:111]\n"
BL"a[120:123],v235,s[8:11],s58 offen offset:3072 sc1\n"
MF"v[112:115],a[144:147],a[32:35],v[112:115]\n"
WT"lgkmcnt(3)\n"
RT"a[166:167],v236 offset:45568\n"
MF"v[112:115],a[148:151],a[48:51],v[112:115]\n"
MF"v[120:123],a[148:151],a[52:55],v[120:123]\n"
RT"a[168:169],v237 offset:36864\n"
MF"v[120:123],a[144:147],a[36:39],v[120:123]\n"
MF"v[128:131],a[144:147],a[40:43],v[128:131]\n"
RT"a[170:171],v237 offset:37376\n"
MF"v[128:131],a[148:151],a[56:59],v[128:131]\n"
SA"s99,s99,4096\n"
RT"a[172:173],v237 offset:45056\n"
MF"v[136:139],a[148:151],a[60:63],v[136:139]\n"
"s_cmp_eq_u32 s99,20480\n"
BL"a[124:127],v235,s[8:11],s59 offen offset:3072 sc1\n"
MF"v[136:139],a[144:147],a[44:47],v[136:139]\n"
"s_cselect_b32 s99,0,s99\n"
RT"a[174:175],v237 offset:45568\n"
MF"v[116:119],a[152:155],a[32:35],v[116:119]\n"
MF"v[116:119],a[156:159],a[48:51],v[116:119]\n"
RT"a[176:177],v228 offset:36864\n"
MF"v[124:127],a[156:159],a[52:55],v[124:127]\n"
MF"v[124:127],a[152:155],a[36:39],v[124:127]\n"
RT"a[178:179],v228 offset:37376\n"
MF"v[132:135],a[152:155],a[40:43],v[132:135]\n"
"v_add_u32 v235,s99,v224\n"
MF"v[132:135],a[156:159],a[56:59],v[132:135]\n"
RT"a[180:181],v228 offset:45056\n"
MF"v[140:143],a[156:159],a[60:63],v[140:143]\n"
BL"a[0:3],v235,s[8:11],s56 offen offset:0 sc1\n"
MF"v[140:143],a[152:155],a[44:47],v[140:143]\n"
WT"lgkmcnt(3)\n"
BS"v[20:23],v250,s[8:11],s49 offen offset:2048 sc1\n"
MF"v[144:147],a[160:163],a[32:35],v[144:147]\n"
RT"a[182:183],v228 offset:45568\n"
MF"v[144:147],a[164:167],a[48:51],v[144:147]\n"
RT"a[184:185],v229 offset:36864\n"
MF"v[152:155],a[164:167],a[52:55],v[152:155]\n"
MF"v[152:155],a[160:163],a[36:39],v[152:155]\n"
RT"a[186:187],v229 offset:37376\n"
MF"v[160:163],a[160:163],a[40:43],v[160:163]\n"
BL"a[4:7],v235,s[8:11],s57 offen offset:0 sc1\n"
MF"v[160:163],a[164:167],a[56:59],v[160:163]\n"
RT"a[188:189],v229 offset:45056\n"
MF"v[168:171],a[164:167],a[60:63],v[168:171]\n"
MF"v[168:171],a[160:163],a[44:47],v[168:171]\n"
RT"a[190:191],v229 offset:45568\n"
MF"v[148:151],a[168:171],a[32:35],v[148:151]\n"
"s_lshl_b32 s50,s35,3\n"
SA"s50,s50,s34\n"
"s_lshl_b32 s50,s50,3\n"
MF"v[148:151],a[172:175],a[48:51],v[148:151]\n"
SA"s50,s50,2304\n"
SA"s51,s50,s45\n"
MF"v[156:159],a[172:175],a[52:55],v[156:159]\n"
MF"v[156:159],a[168:171],a[36:39],v[156:159]\n"
BL"a[8:11],v235,s[8:11],s58 offen offset:0 sc1\n"
MF"v[164:167],a[168:171],a[40:43],v[164:167]\n"
MF"v[164:167],a[172:175],a[56:59],v[164:167]\n"
WT"lgkmcnt(0)\n"
"s_barrier\n"
MF"v[172:175],a[172:175],a[60:63],v[172:175]\n"
RT"a[128:129],v236 offset:0\n"
MF"v[172:175],a[168:171],a[44:47],v[172:175]\n"
"buffer_store_dwordx2 v[232:233],v230,s[8:11],s51 offen sc1\n"
MF"v[176:179],a[176:179],a[32:35],v[176:179]\n"
RT"a[130:131],v236 offset:512\n"
MF"v[176:179],a[180:183],a[48:51],v[176:179]\n"
MF"v[184:187],a[180:183],a[52:55],v[184:187]\n"
SA"m0,s38,81920\n"
RT"a[132:133],v236 offset:8192\n"
SA"s48,s60,s61\n"
MF"v[184:187],a[176:179],a[36:39],v[184:187]\n"
BL"a[12:15],v235,s[8:11],s59 offen offset:0 sc1\n"
MF"v[192:195],a[176:179],a[40:43],v[192:195]\n"
RT"a[134:135],v236 offset:8704\n"
MF"v[192:195],a[180:183],a[56:59],v[192:195]\n"
BL"v238,s[20:23],s48 offen offset:0 lds\n"
MF"v[200:203],a[180:183],a[60:63],v[200:203]\n"
RT"a[136:137],v237 offset:0\n"
MF"v[200:203],a[176:179],a[44:47],v[200:203]\n"
MF"v[180:183],a[184:187],a[32:35],v[180:183]\n"
RT"a[138:139],v237 offset:512\n"
MF"v[180:183],a[188:191],a[48:51],v[180:183]\n"
BL"a[16:19],v235,s[8:11],s56 offen offset:1024 sc1\n"
MF"v[188:191],a[188:191],a[52:55],v[188:191]\n"
RT"a[140:141],v237 offset:8192\n"
MF"v[188:191],a[184:187],a[36:39],v[188:191]\n"
MF"v[196:199],a[184:187],a[40:43],v[196:199]\n"
RT"a[142:143],v237 offset:8704\n"
MF"v[196:199],a[188:191],a[56:59],v[196:199]\n"
BL"v239,s[20:23],s48 offen offset:1024 lds\n"
MF"v[204:207],a[188:191],a[60:63],v[204:207]\n"
SA"s49,s88,262144\n"
RT"a[144:145],v228 offset:0\n"
MF"v[204:207],a[184:187],a[44:47],v[204:207]\n"
"LQ_%=:\n"
WT"vmcnt(34) lgkmcnt(7)\n"
MF"v[80:83],a[128:131],a[64:67],v[80:83]\n"
WT"vmcnt(27) lgkmcnt(3)\n"
RT"a[146:147],v228 offset:512\n"
MF"v[80:83],a[132:135],a[80:83],v[80:83]\n"
WT"vmcnt(25)\n"
BS"v[28:31],v250,s[8:11],s49 offen offset:1024 sc1\n"
MF"v[88:91],a[132:135],a[84:87],v[88:91]\n"
BL"a[20:23],v235,s[8:11],s57 offen offset:1024 sc1\n"
MF"v[88:91],a[128:131],a[68:71],v[88:91]\n"
RT"a[148:149],v228 offset:8192\n"
MF"v[96:99],a[128:131],a[72:75],v[96:99]\n"
WT"vmcnt(26)\n"
RT"a[150:151],v228 offset:8704\n"
MF"v[96:99],a[132:135],a[88:91],v[96:99]\n"
WT"vmcnt(24)\n"
BL"v240,s[20:23],s48 offen offset:2048 lds\n"
MF"v[104:107],a[132:135],a[92:95],v[104:107]\n"
RT"a[152:153],v229 offset:0\n"
MF"v[104:107],a[128:131],a[76:79],v[104:107]\n"
RT"a[154:155],v229 offset:512\n"
MF"v[84:87],a[136:139],a[64:67],v[84:87]\n"
WT"lgkmcnt(4)\n"
BL"a[24:27],v235,s[8:11],s58 offen offset:1024 sc1\n"
MF"v[84:87],a[140:143],a[80:83],v[84:87]\n"
RT"a[156:157],v229 offset:8192\n"
MF"v[92:95],a[140:143],a[84:87],v[92:95]\n"
MF"v[92:95],a[136:139],a[68:71],v[92:95]\n"
RT"a[158:159],v229 offset:8704\n"
MF"v[100:103],a[136:139],a[72:75],v[100:103]\n"
SA"s61,s61,524288\n"
BL"v241,s[20:23],s48 offen offset:3072 lds\n"
MF"v[100:103],a[140:143],a[88:91],v[100:103]\n"
RT"a[160:161],v236 offset:4096\n"
MF"v[108:111],a[140:143],a[92:95],v[108:111]\n"
BL"a[28:31],v235,s[8:11],s59 offen offset:1024 sc1\n"
MF"v[108:111],a[136:139],a[76:79],v[108:111]\n"
RT"a[162:163],v236 offset:4608\n"
MF"v[112:115],a[144:147],a[64:67],v[112:115]\n"
WT"lgkmcnt(3)\n"
MF"v[112:115],a[148:151],a[80:83],v[112:115]\n"
RT"a[164:165],v236 offset:12288\n"
MF"v[120:123],a[148:151],a[84:87],v[120:123]\n"
MF"v[120:123],a[144:147],a[68:71],v[120:123]\n"
RT"a[166:167],v236 offset:12800\n"
MF"v[128:131],a[144:147],a[72:75],v[128:131]\n"
MF"v[128:131],a[148:151],a[88:91],v[128:131]\n"
RT"a[168:169],v237 offset:4096\n"
MF"v[136:139],a[148:151],a[92:95],v[136:139]\n"
BL"a[32:35],v235,s[8:11],s56 offen offset:2048 sc1\n"
MF"v[136:139],a[144:147],a[76:79],v[136:139]\n"
RT"a[170:171],v237 offset:4608\n"
MF"v[116:119],a[152:155],a[64:67],v[116:119]\n"
WT"lgkmcnt(3)\n"
RT"a[172:173],v237 offset:12288\n"
MF"v[116:119],a[156:159],a[80:83],v[116:119]\n"
MF"v[124:127],a[156:159],a[84:87],v[124:127]\n"
RT"a[174:175],v237 offset:12800\n"
MF"v[124:127],a[152:155],a[68:71],v[124:127]\n"
MF"v[132:135],a[152:155],a[72:75],v[132:135]\n"
RT"a[176:177],v228 offset:4096\n"
MF"v[132:135],a[156:159],a[88:91],v[132:135]\n"
BL"a[36:39],v235,s[8:11],s57 offen offset:2048 sc1\n"
MF"v[140:143],a[156:159],a[92:95],v[140:143]\n"
RT"a[178:179],v228 offset:4608\n"
MF"v[140:143],a[152:155],a[76:79],v[140:143]\n"
MF"v[144:147],a[160:163],a[64:67],v[144:147]\n"
WT"lgkmcnt(3)\n"
RT"a[180:181],v228 offset:12288\n"
"s_mov_b32 s49,286720\n"
MF"v[144:147],a[164:167],a[80:83],v[144:147]\n"
MF"v[152:155],a[164:167],a[84:87],v[152:155]\n"
RT"a[182:183],v228 offset:12800\n"
MF"v[152:155],a[160:163],a[68:71],v[152:155]\n"
BS"v[0:3],v250,s[8:11],s49 offen offset:0 sc1\n"
MF"v[160:163],a[160:163],a[72:75],v[160:163]\n"
BL"a[40:43],v235,s[8:11],s58 offen offset:2048 sc1\n"
MF"v[160:163],a[164:167],a[88:91],v[160:163]\n"
RT"a[184:185],v229 offset:4096\n"
MF"v[168:171],a[164:167],a[92:95],v[168:171]\n"
RT"a[186:187],v229 offset:4608\n"
MF"v[168:171],a[160:163],a[76:79],v[168:171]\n"
MF"v[148:151],a[168:171],a[64:67],v[148:151]\n"
WT"lgkmcnt(3)\n"
RT"a[188:189],v229 offset:12288\n"
MF"v[148:151],a[172:175],a[80:83],v[148:151]\n"
MF"v[156:159],a[172:175],a[84:87],v[156:159]\n"
RT"a[190:191],v229 offset:12800\n"
MF"v[156:159],a[168:171],a[68:71],v[156:159]\n"
BL"a[44:47],v235,s[8:11],s59 offen offset:2048 sc1\n"
MF"v[164:167],a[168:171],a[72:75],v[164:167]\n"
RT"a[128:129],v246 offset:0\n"
MF"v[164:167],a[172:175],a[88:91],v[164:167]\n"
RT"a[130:131],v246 offset:512\n"
MF"v[172:175],a[172:175],a[92:95],v[172:175]\n"
SA"m0,s38,131072\n"
SA"s48,s60,s61\n"
MF"v[172:175],a[168:171],a[76:79],v[172:175]\n"
RT"a[132:133],v246 offset:24576\n"
MF"v[176:179],a[176:179],a[64:67],v[176:179]\n"
WT"lgkmcnt(4)\n"
BL"v238,s[20:23],s48 offen offset:0 lds\n"
MF"v[176:179],a[180:183],a[80:83],v[176:179]\n"
RT"a[134:135],v246 offset:25088\n"
MF"v[184:187],a[180:183],a[84:87],v[184:187]\n"
BL"a[48:51],v235,s[8:11],s56 offen offset:3072 sc1\n"
MF"v[184:187],a[176:179],a[68:71],v[184:187]\n"
RT"a[136:137],v247 offset:0\n"
MF"v[192:195],a[176:179],a[72:75],v[192:195]\n"
MF"v[192:195],a[180:183],a[88:91],v[192:195]\n"
RT"a[138:139],v247 offset:512\n"
MF"v[200:203],a[180:183],a[92:95],v[200:203]\n"
MF"v[200:203],a[176:179],a[76:79],v[200:203]\n"
RT"a[140:141],v247 offset:24576\n"
MF"v[180:183],a[184:187],a[64:67],v[180:183]\n"
WT"lgkmcnt(3)\n"
BL"v239,s[20:23],s48 offen offset:1024 lds\n"
MF"v[180:183],a[188:191],a[80:83],v[180:183]\n"
BL"a[52:55],v235,s[8:11],s57 offen offset:3072 sc1\n"
MF"v[188:191],a[188:191],a[84:87],v[188:191]\n"
RT"a[142:143],v247 offset:25088\n"
MF"v[188:191],a[184:187],a[68:71],v[188:191]\n"
RT"a[144:145],v248 offset:0\n"
MF"v[196:199],a[184:187],a[72:75],v[196:199]\n"
MF"v[196:199],a[188:191],a[88:91],v[196:199]\n"
RT"a[146:147],v248 offset:512\n"
MF"v[204:207],a[188:191],a[92:95],v[204:207]\n"
MF"v[204:207],a[184:187],a[76:79],v[204:207]\n"
WT"vmcnt(35)\n"
RT"a[148:149],v248 offset:24576\n"
MF"v[80:83],a[128:131],a[96:99],v[80:83]\n"
WT"vmcnt(27)\n"
BL"a[56:59],v235,s[8:11],s58 offen offset:3072 sc1\n"
MF"v[80:83],a[132:135],a[112:115],v[80:83]\n"
WT"vmcnt(27)\n"
BL"v240,s[20:23],s48 offen offset:2048 lds\n"
MF"v[88:91],a[132:135],a[116:119],v[88:91]\n"
RT"a[150:151],v248 offset:25088\n"
RT"a[152:153],v249 offset:0\n"
MF"v[88:91],a[128:131],a[100:103],v[88:91]\n"
MF"v[96:99],a[128:131],a[104:107],v[96:99]\n"
WT"vmcnt(27)\n"
RT"a[154:155],v249 offset:512\n"
MF"v[96:99],a[132:135],a[120:123],v[96:99]\n"
WT"vmcnt(26)\n"
BS"v[16:19],v250,s[8:11],s49 offen offset:1024 sc1\n"
MF"v[104:107],a[132:135],a[124:127],v[104:107]\n"
SA"s99,s99,4096\n"
RT"a[156:157],v249 offset:24576\n"
"s_cmp_eq_u32 s99,20480\n"
MF"v[104:107],a[128:131],a[108:111],v[104:107]\n"
WT"lgkmcnt(4)\n"
BL"a[60:63],v235,s[8:11],s59 offen offset:3072 sc1\n"
MF"v[84:87],a[136:139],a[96:99],v[84:87]\n"
"s_cselect_b32 s99,0,s99\n"
RT"a[158:159],v249 offset:25088\n"
SA"s61,s61,524288\n"
MF"v[84:87],a[140:143],a[112:115],v[84:87]\n"
"s_cmp_eq_u32 s61,5242880\n"
BL"v241,s[20:23],s48 offen offset:3072 lds\n"
MF"v[92:95],a[140:143],a[116:119],v[92:95]\n"
"s_cselect_b32 s61,0,s61\n"
RT"a[160:161],v246 offset:4096\n"
MF"v[92:95],a[136:139],a[100:103],v[92:95]\n"
MF"v[100:103],a[136:139],a[104:107],v[100:103]\n"
RT"a[162:163],v246 offset:4608\n"
"v_add_u32 v235,s99,v224\n"
MF"v[100:103],a[140:143],a[120:123],v[100:103]\n"
BL"a[64:67],v235,s[8:11],s56 offen offset:0 sc1\n"
MF"v[108:111],a[140:143],a[124:127],v[108:111]\n"
RT"a[164:165],v246 offset:28672\n"
MF"v[108:111],a[136:139],a[108:111],v[108:111]\n"
MF"v[112:115],a[144:147],a[96:99],v[112:115]\n"
WT"lgkmcnt(3)\n"
RT"a[166:167],v246 offset:29184\n"
"v_add_u32 v225,s36,v224\n"
MF"v[112:115],a[148:151],a[112:115],v[112:115]\n"
"s_lshl_b32 s39,s35,20\n"
SA"s39,s39,9175040\n"
MF"v[120:123],a[148:151],a[116:119],v[120:123]\n"
RT"a[168:169],v247 offset:4096\n"
MF"v[120:123],a[144:147],a[100:103],v[120:123]\n"
MF"v[128:131],a[144:147],a[104:107],v[128:131]\n"
RT"a[170:171],v247 offset:4608\n"
MF"v[128:131],a[148:151],a[120:123],v[128:131]\n"
BL"a[68:71],v235,s[8:11],s57 offen offset:0 sc1\n"
MF"v[136:139],a[148:151],a[124:127],v[136:139]\n"
RT"a[172:173],v247 offset:28672\n"
MF"v[136:139],a[144:147],a[108:111],v[136:139]\n"
RT"a[174:175],v247 offset:29184\n"
MF"v[116:119],a[152:155],a[96:99],v[116:119]\n"
MF"v[116:119],a[156:159],a[112:115],v[116:119]\n"
RT"a[176:177],v248 offset:4096\n"
MF"v[124:127],a[156:159],a[116:119],v[124:127]\n"
MF"v[124:127],a[152:155],a[100:103],v[124:127]\n"
RT"a[178:179],v248 offset:4608\n"
MF"v[132:135],a[152:155],a[104:107],v[132:135]\n"
BL"a[72:75],v235,s[8:11],s58 offen offset:0 sc1\n"
MF"v[132:135],a[156:159],a[120:123],v[132:135]\n"
RT"a[180:181],v248 offset:28672\n"
MF"v[140:143],a[156:159],a[124:127],v[140:143]\n"
MF"v[140:143],a[152:155],a[108:111],v[140:143]\n"
WT"lgkmcnt(3)\n"
RT"a[182:183],v248 offset:29184\n"
MF"v[144:147],a[160:163],a[96:99],v[144:147]\n"
MF"v[144:147],a[164:167],a[112:115],v[144:147]\n"
RT"a[184:185],v249 offset:4096\n"
MF"v[152:155],a[164:167],a[116:119],v[152:155]\n"
BL"a[76:79],v235,s[8:11],s59 offen offset:0 sc1\n"
MF"v[152:155],a[160:163],a[100:103],v[152:155]\n"
RT"a[186:187],v249 offset:4608\n"
MF"v[160:163],a[160:163],a[104:107],v[160:163]\n"
SA"s49,s88,286720\n"
MF"v[160:163],a[164:167],a[120:123],v[160:163]\n"
RT"a[188:189],v249 offset:28672\n"
MF"v[168:171],a[164:167],a[124:127],v[168:171]\n"
BS"v[32:35],v250,s[8:11],s49 offen offset:0 sc1\n"
MF"v[168:171],a[160:163],a[108:111],v[168:171]\n"
RT"a[190:191],v249 offset:29184\n"
MF"v[148:151],a[168:171],a[96:99],v[148:151]\n"
MF"v[148:151],a[172:175],a[112:115],v[148:151]\n"
BL"a[80:83],v235,s[8:11],s56 offen offset:1024 sc1\n"
MF"v[156:159],a[172:175],a[116:119],v[156:159]\n"
MF"v[156:159],a[168:171],a[100:103],v[156:159]\n"
MF"v[164:167],a[168:171],a[104:107],v[164:167]\n"
MF"v[164:167],a[172:175],a[120:123],v[164:167]\n"
"s_barrier\n"
MF"v[172:175],a[172:175],a[124:127],v[172:175]\n"
RT"a[128:129],v242 offset:0\n"
MF"v[172:175],a[168:171],a[108:111],v[172:175]\n"
WT"lgkmcnt(1)\n"
MF"v[176:179],a[176:179],a[96:99],v[176:179]\n"
RT"a[130:131],v242 offset:512\n"
MF"v[176:179],a[180:183],a[112:115],v[176:179]\n"
BL"a[84:87],v235,s[8:11],s57 offen offset:1024 sc1\n"
MF"v[184:187],a[180:183],a[116:119],v[184:187]\n"
RT"a[132:133],v242 offset:24576\n"
MF"v[184:187],a[176:179],a[100:103],v[184:187]\n"
MF"v[192:195],a[176:179],a[104:107],v[192:195]\n"
RT"a[134:135],v242 offset:25088\n"
MF"v[192:195],a[180:183],a[120:123],v[192:195]\n"
MF"v[200:203],a[180:183],a[124:127],v[200:203]\n"
RT"a[136:137],v243 offset:0\n"
MF"v[200:203],a[176:179],a[108:111],v[200:203]\n"
BL"a[88:91],v235,s[8:11],s58 offen offset:1024 sc1\n"
MF"v[180:183],a[184:187],a[96:99],v[180:183]\n"
RT"a[138:139],v243 offset:512\n"
MF"v[180:183],a[188:191],a[112:115],v[180:183]\n"
MF"v[188:191],a[188:191],a[116:119],v[188:191]\n"
RT"a[140:141],v243 offset:24576\n"
MF"v[188:191],a[184:187],a[100:103],v[188:191]\n"
MF"v[196:199],a[184:187],a[104:107],v[196:199]\n"
RT"a[142:143],v243 offset:25088\n"
MF"v[196:199],a[188:191],a[120:123],v[196:199]\n"
MF"v[204:207],a[188:191],a[124:127],v[204:207]\n"
RT"a[144:145],v244 offset:0\n"
MF"v[204:207],a[184:187],a[108:111],v[204:207]\n"
WT"vmcnt(36) lgkmcnt(3)\n"
BL"a[92:95],v235,s[8:11],s59 offen offset:1024 sc1\n"
MF"v[80:83],a[128:131],a[0:3],v[80:83]\n"
WT"vmcnt(30)\n"
RT"a[146:147],v244 offset:512\n"
MF"v[80:83],a[132:135],a[16:19],v[80:83]\n"
WT"vmcnt(27)\n"
MF"v[88:91],a[132:135],a[20:23],v[88:91]\n"
RT"a[148:149],v244 offset:24576\n"
MF"v[88:91],a[128:131],a[4:7],v[88:91]\n"
MF"v[96:99],a[128:131],a[8:11],v[96:99]\n"
WT"vmcnt(25)\n"
RT"a[150:151],v244 offset:25088\n"
MF"v[96:99],a[132:135],a[24:27],v[96:99]\n"
WT"vmcnt(23)\n"
RT"a[152:153],v245 offset:0\n"
MF"v[104:107],a[132:135],a[28:31],v[104:107]\n"
BL"a[96:99],v235,s[8:11],s56 offen offset:2048 sc1\n"
MF"v[104:107],a[128:131],a[12:15],v[104:107]\n"
RT"a[154:155],v245 offset:512\n"
MF"v[84:87],a[136:139],a[0:3],v[84:87]\n"
WT"lgkmcnt(4)\n"
BS"v[48:51],v250,s[8:11],s49 offen offset:1024 sc1\n"
MF"v[84:87],a[140:143],a[16:19],v[84:87]\n"
RT"a[156:157],v245 offset:24576\n"
MF"v[92:95],a[140:143],a[20:23],v[92:95]\n"
MF"v[92:95],a[136:139],a[4:7],v[92:95]\n"
RT"a[158:159],v245 offset:25088\n"
MF"v[100:103],a[136:139],a[8:11],v[100:103]\n"
BL"a[100:103],v235,s[8:11],s57 offen offset:2048 sc1\n"
MF"v[100:103],a[140:143],a[24:27],v[100:103]\n"
RT"a[160:161],v242 offset:4096\n"
MF"v[108:111],a[140:143],a[28:31],v[108:111]\n"
MF"v[108:111],a[136:139],a[12:15],v[108:111]\n"
RT"a[162:163],v242 offset:4608\n"
MF"v[112:115],a[144:147],a[0:3],v[112:115]\n"
WT"lgkmcnt(3)\n"
MF"v[112:115],a[148:151],a[16:19],v[112:115]\n"
RT"a[164:165],v242 offset:28672\n"
MF"v[120:123],a[148:151],a[20:23],v[120:123]\n"
MF"v[120:123],a[144:147],a[4:7],v[120:123]\n"
RT"a[166:167],v242 offset:29184\n"
MF"v[128:131],a[144:147],a[8:11],v[128:131]\n"
BL"a[104:107],v235,s[8:11],s58 offen offset:2048 sc1\n"
MF"v[128:131],a[148:151],a[24:27],v[128:131]\n"
RT"a[168:169],v243 offset:4096\n"
MF"v[136:139],a[148:151],a[28:31],v[136:139]\n"
MF"v[136:139],a[144:147],a[12:15],v[136:139]\n"
RT"a[170:171],v243 offset:4608\n"
MF"v[116:119],a[152:155],a[0:3],v[116:119]\n"
WT"lgkmcnt(3)\n"
RT"a[172:173],v243 offset:28672\n"
MF"v[116:119],a[156:159],a[16:19],v[116:119]\n"
MF"v[124:127],a[156:159],a[20:23],v[124:127]\n"
RT"a[174:175],v243 offset:29184\n"
MF"v[124:127],a[152:155],a[4:7],v[124:127]\n"
BL"a[108:111],v235,s[8:11],s59 offen offset:2048 sc1\n"
MF"v[132:135],a[152:155],a[8:11],v[132:135]\n"
RT"a[176:177],v244 offset:4096\n"
MF"v[132:135],a[156:159],a[24:27],v[132:135]\n"
MF"v[140:143],a[156:159],a[28:31],v[140:143]\n"
RT"a[178:179],v244 offset:4608\n"
MF"v[140:143],a[152:155],a[12:15],v[140:143]\n"
MF"v[144:147],a[160:163],a[0:3],v[144:147]\n"
WT"lgkmcnt(3)\n"
RT"a[180:181],v244 offset:28672\n"
MF"v[144:147],a[164:167],a[16:19],v[144:147]\n"
BL"a[112:115],v235,s[8:11],s56 offen offset:3072 sc1\n"
MF"v[152:155],a[164:167],a[20:23],v[152:155]\n"
RT"a[182:183],v244 offset:29184\n"
MF"v[152:155],a[160:163],a[4:7],v[152:155]\n"
MF"v[160:163],a[160:163],a[8:11],v[160:163]\n"
RT"a[184:185],v245 offset:4096\n"
MF"v[160:163],a[164:167],a[24:27],v[160:163]\n"
MF"v[168:171],a[164:167],a[28:31],v[168:171]\n"
RT"a[186:187],v245 offset:4608\n"
MF"v[168:171],a[160:163],a[12:15],v[168:171]\n"
MF"v[148:151],a[168:171],a[0:3],v[148:151]\n"
WT"lgkmcnt(3)\n"
RT"a[188:189],v245 offset:28672\n"
"s_mov_b32 s49,270336\n"
MF"v[148:151],a[172:175],a[16:19],v[148:151]\n"
BL"a[116:119],v235,s[8:11],s57 offen offset:3072 sc1\n"
MF"v[156:159],a[172:175],a[20:23],v[156:159]\n"
RT"a[190:191],v245 offset:29184\n"
MF"v[156:159],a[168:171],a[4:7],v[156:159]\n"
BS"v[36:39],v250,s[8:11],s49 offen offset:0 sc1\n"
MF"v[164:167],a[168:171],a[8:11],v[164:167]\n"
RT"a[128:129],v236 offset:16384\n"
MF"v[164:167],a[172:175],a[24:27],v[164:167]\n"
RT"a[130:131],v236 offset:16896\n"
MF"v[172:175],a[172:175],a[28:31],v[172:175]\n"
MF"v[172:175],a[168:171],a[12:15],v[172:175]\n"
RT"a[132:133],v236 offset:24576\n"
MF"v[176:179],a[176:179],a[0:3],v[176:179]\n"
WT"lgkmcnt(4)\n"
BL"a[120:123],v235,s[8:11],s58 offen offset:3072 sc1\n"
MF"v[176:179],a[180:183],a[16:19],v[176:179]\n"
RT"a[134:135],v236 offset:25088\n"
MF"v[184:187],a[180:183],a[20:23],v[184:187]\n"
MF"v[184:187],a[176:179],a[4:7],v[184:187]\n"
RT"a[136:137],v237 offset:16384\n"
MF"v[192:195],a[176:179],a[8:11],v[192:195]\n"
MF"v[192:195],a[180:183],a[24:27],v[192:195]\n"
SA"s99,s99,4096\n"
RT"a[138:139],v237 offset:16896\n"
MF"v[200:203],a[180:183],a[28:31],v[200:203]\n"
"s_cmp_eq_u32 s99,20480\n"
BL"a[124:127],v235,s[8:11],s59 offen offset:3072 sc1\n"
MF"v[200:203],a[176:179],a[12:15],v[200:203]\n"
"s_cselect_b32 s99,0,s99\n"
RT"a[140:141],v237 offset:24576\n"
MF"v[180:183],a[184:187],a[0:3],v[180:183]\n"
WT"lgkmcnt(3)\n"
"v_lshrrev_b32 v252,1,v224\n"
MF"v[180:183],a[188:191],a[16:19],v[180:183]\n"
RT"a[142:143],v237 offset:25088\n"
"v_and_b32 v252,56,v252\n"
MF"v[188:191],a[188:191],a[20:23],v[188:191]\n"
"s_lshl_b32 s50,s35,6\n"
MF"v[188:191],a[184:187],a[4:7],v[188:191]\n"
SA"s50,s50,2304\n"
RT"a[144:145],v228 offset:16384\n"
MF"v[196:199],a[184:187],a[8:11],v[196:199]\n"
"buffer_load_dwordx2 v[208:209],v252,s[8:11],s50 offen sc1\n"
MF"v[196:199],a[188:191],a[24:27],v[196:199]\n"
RT"a[146:147],v228 offset:16896\n"
"v_add_u32 v235,s99,v224\n"
MF"v[204:207],a[188:191],a[28:31],v[204:207]\n"
BL"a[192:195],v235,s[8:11],s56 offen offset:0 sc1\n"
MF"v[204:207],a[184:187],a[12:15],v[204:207]\n"
WT"vmcnt(34)\n"
RT"a[148:149],v228 offset:24576\n"
MF"v[80:83],a[128:131],a[32:35],v[80:83]\n"
WT"vmcnt(28)\n"
MF"v[80:83],a[132:135],a[48:51],v[80:83]\n"
WT"vmcnt(26)\n"
RT"a[150:151],v228 offset:25088\n"
MF"v[88:91],a[132:135],a[52:55],v[88:91]\n"
RT"a[152:153],v229 offset:16384\n"
MF"v[88:91],a[128:131],a[36:39],v[88:91]\n"
MF"v[96:99],a[128:131],a[40:43],v[96:99]\n"
WT"vmcnt(25)\n"
RT"a[154:155],v229 offset:16896\n"
MF"v[96:99],a[132:135],a[56:59],v[96:99]\n"
WT"vmcnt(22)\n"
BL"a[196:199],v235,s[8:11],s57 offen offset:0 sc1\n"
MF"v[104:107],a[132:135],a[60:63],v[104:107]\n"
RT"a[156:157],v229 offset:24576\n"
MF"v[104:107],a[128:131],a[44:47],v[104:107]\n"
WT"lgkmcnt(4)\n"
MF"v[84:87],a[136:139],a[32:35],v[84:87]\n"
RT"a[158:159],v229 offset:25088\n"
MF"v[84:87],a[140:143],a[48:51],v[84:87]\n"
MF"v[92:95],a[140:143],a[52:55],v[92:95]\n"
RT"a[160:161],v236 offset:20480\n"
MF"v[92:95],a[136:139],a[36:39],v[92:95]\n"
BL"a[200:203],v235,s[8:11],s58 offen offset:0 sc1\n"
MF"v[100:103],a[136:139],a[40:43],v[100:103]\n"
RT"a[162:163],v236 offset:20992\n"
MF"v[100:103],a[140:143],a[56:59],v[100:103]\n"
BS"v[44:47],v250,s[8:11],s49 offen offset:1024 sc1\n"
MF"v[108:111],a[140:143],a[60:63],v[108:111]\n"
RT"a[164:165],v236 offset:28672\n"
MF"v[108:111],a[136:139],a[44:47],v[108:111]\n"
MF"v[112:115],a[144:147],a[32:35],v[112:115]\n"
WT"lgkmcnt(3)\n"
RT"a[166:167],v236 offset:29184\n"
MF"v[112:115],a[148:151],a[48:51],v[112:115]\n"
MF"v[120:123],a[148:151],a[52:55],v[120:123]\n"
RT"a[168:169],v237 offset:20480\n"
MF"v[120:123],a[144:147],a[36:39],v[120:123]\n"
BL"a[204:207],v235,s[8:11],s59 offen offset:0 sc1\n"
MF"v[128:131],a[144:147],a[40:43],v[128:131]\n"
RT"a[170:171],v237 offset:20992\n"
MF"v[128:131],a[148:151],a[56:59],v[128:131]\n"
RT"a[172:173],v237 offset:28672\n"
MF"v[136:139],a[148:151],a[60:63],v[136:139]\n"
MF"v[136:139],a[144:147],a[44:47],v[136:139]\n"
RT"a[174:175],v237 offset:29184\n"
MF"v[116:119],a[152:155],a[32:35],v[116:119]\n"
MF"v[116:119],a[156:159],a[48:51],v[116:119]\n"
RT"a[176:177],v228 offset:20480\n"
MF"v[124:127],a[156:159],a[52:55],v[124:127]\n"
BL"a[208:211],v235,s[8:11],s56 offen offset:1024 sc1\n"
MF"v[124:127],a[152:155],a[36:39],v[124:127]\n"
RT"a[178:179],v228 offset:20992\n"
MF"v[132:135],a[152:155],a[40:43],v[132:135]\n"
MF"v[132:135],a[156:159],a[56:59],v[132:135]\n"
RT"a[180:181],v228 offset:28672\n"
MF"v[140:143],a[156:159],a[60:63],v[140:143]\n"
MF"v[140:143],a[152:155],a[44:47],v[140:143]\n"
WT"lgkmcnt(3)\n"
RT"a[182:183],v228 offset:29184\n"
MF"v[144:147],a[160:163],a[32:35],v[144:147]\n"
BL"a[212:215],v235,s[8:11],s57 offen offset:1024 sc1\n"
MF"v[144:147],a[164:167],a[48:51],v[144:147]\n"
RT"a[184:185],v229 offset:20480\n"
MF"v[152:155],a[164:167],a[52:55],v[152:155]\n"
MF"v[152:155],a[160:163],a[36:39],v[152:155]\n"
RT"a[186:187],v229 offset:20992\n"
MF"v[160:163],a[160:163],a[40:43],v[160:163]\n"
MF"v[160:163],a[164:167],a[56:59],v[160:163]\n"
RT"a[188:189],v229 offset:28672\n"
MF"v[168:171],a[164:167],a[60:63],v[168:171]\n"
MF"v[168:171],a[160:163],a[44:47],v[168:171]\n"
RT"a[190:191],v229 offset:29184\n"
MF"v[148:151],a[168:171],a[32:35],v[148:151]\n"
BL"a[216:219],v235,s[8:11],s58 offen offset:1024 sc1\n"
MF"v[148:151],a[172:175],a[48:51],v[148:151]\n"
WT"vmcnt(8)\n"
"v_cmp_eq_u64 vcc,s[28:29],v[208:209]\n"
"s_cmp_eq_u64 vcc,exec\n"
"s_cbranch_scc0 L1010_%=\n"
"L1011_%=:\n"
MF"v[156:159],a[172:175],a[52:55],v[156:159]\n"
MF"v[156:159],a[168:171],a[36:39],v[156:159]\n"
MF"v[164:167],a[168:171],a[40:43],v[164:167]\n"
SA"s49,s88,270336\n"
MF"v[164:167],a[172:175],a[56:59],v[164:167]\n"
WT"lgkmcnt(0)\n"
"s_barrier\n"
MF"v[172:175],a[172:175],a[60:63],v[172:175]\n"
SA"m0,s36,0\n"
RT"a[128:129],v246 offset:8192\n"
SA"s48,s39,0\n"
MF"v[172:175],a[168:171],a[44:47],v[172:175]\n"
BS"v[52:55],v250,s[8:11],s49 offen offset:0 sc1\n"
MF"v[176:179],a[176:179],a[32:35],v[176:179]\n"
BL"a[220:223],v235,s[8:11],s59 offen offset:1024 sc1\n"
MF"v[176:179],a[180:183],a[48:51],v[176:179]\n"
RT"a[130:131],v246 offset:8704\n"
MF"v[184:187],a[180:183],a[52:55],v[184:187]\n"
BL"v225,s[8:11],s48 offen offset:0 sc1 lds\n"
MF"v[184:187],a[176:179],a[36:39],v[184:187]\n"
RT"a[132:133],v246 offset:32768\n"
"v_lshlrev_b32 v254,10,v224\n"
MF"v[192:195],a[176:179],a[40:43],v[192:195]\n"
"s_lshl_b32 s94,s34,1\n"
RT"a[134:135],v246 offset:33280\n"
"v_lshrrev_b32 v255,4,v224\n"
MF"v[192:195],a[180:183],a[56:59],v[192:195]\n"
SA"s95,s94,2\n"
BL"a[224:227],v235,s[8:11],s56 offen offset:2048 sc1\n"
MF"v[200:203],a[180:183],a[60:63],v[200:203]\n"
"s_sub_u32 s94,32,s94\n"
RT"a[136:137],v247 offset:8192\n"
MF"v[200:203],a[176:179],a[44:47],v[200:203]\n"
"v_add_u32 v228,s39,v254\n"
MF"v[180:183],a[184:187],a[32:35],v[180:183]\n"
RT"a[138:139],v247 offset:8704\n"
"v_and_b32 v254,1,v255\n"
MF"v[180:183],a[188:191],a[48:51],v[180:183]\n"
BL"v225,s[8:11],s48 offen offset:1024 sc1 lds\n"
MF"v[188:191],a[188:191],a[52:55],v[188:191]\n"
RT"a[140:141],v247 offset:32768\n"
"v_mul_u32_u24 v254,491520,v254\n"
MF"v[188:191],a[184:187],a[36:39],v[188:191]\n"
"v_cmp_gt_u32 vcc,s95,v255\n"
"v_mov_b32 v229,16384\n"
MF"v[196:199],a[184:187],a[40:43],v[196:199]\n"
RT"a[142:143],v247 offset:33280\n"
"v_mov_b32 v253,516096\n"
MF"v[196:199],a[188:191],a[56:59],v[196:199]\n"
BL"a[228:231],v235,s[8:11],s57 offen offset:2048 sc1\n"
MF"v[204:207],a[188:191],a[60:63],v[204:207]\n"
RT"a[144:145],v248 offset:8192\n"
"v_cndmask_b32_e64 v254,v254,0,vcc\n"
MF"v[204:207],a[184:187],a[44:47],v[204:207]\n"
WT"lgkmcnt(3)\n"
"v_cndmask_b32 v229,v229,v253,vcc\n"
"v_cmp_eq_u32 vcc,s94,v255\n"
MF"v[80:83],a[128:131],a[64:67],v[80:83]\n"
RT"a[146:147],v248 offset:8704\n"
"v_mov_b32 v253,507904\n"
MF"v[80:83],a[132:135],a[80:83],v[80:83]\n"
BL"v225,s[8:11],s48 offen offset:2048 sc1 lds\n"
MF"v[88:91],a[132:135],a[84:87],v[88:91]\n"
RT"a[148:149],v248 offset:32768\n"
"v_cndmask_b32 v254,v254,v253,vcc\n"
MF"v[88:91],a[128:131],a[68:71],v[88:91]\n"
BL"a[232:235],v235,s[8:11],s58 offen offset:2048 sc1\n"
MF"v[96:99],a[128:131],a[72:75],v[96:99]\n"
RT"a[150:151],v248 offset:33280\n"
"v_mov_b32 v253,1862270976\n"
MF"v[96:99],a[132:135],a[88:91],v[96:99]\n"
"s_mul_i32 s95,s35,163840\n"
RT"a[152:153],v249 offset:8192\n"
"v_cndmask_b32 v229,v229,v253,vcc\n"
MF"v[104:107],a[132:135],a[92:95],v[104:107]\n"
SA"s95,s95,3395584\n"
"v_add_u32 v228,v228,v254\n"
MF"v[104:107],a[128:131],a[76:79],v[104:107]\n"
RT"a[154:155],v249 offset:8704\n"
"v_subrev_u32 v254,s94,v255\n"
MF"v[84:87],a[136:139],a[64:67],v[84:87]\n"
WT"lgkmcnt(4)\n"
BL"v225,s[8:11],s48 offen offset:3072 sc1 lds\n"
MF"v[84:87],a[140:143],a[80:83],v[84:87]\n"
RT"a[156:157],v249 offset:32768\n"
"v_lshlrev_b32 v254,15,v254\n"
MF"v[92:95],a[140:143],a[84:87],v[92:95]\n"
BL"a[236:239],v235,s[8:11],s59 offen offset:2048 sc1\n"
MF"v[92:95],a[136:139],a[68:71],v[92:95]\n"
RT"a[158:159],v249 offset:33280\n"
"v_add_u32 v254,s95,v254\n"
MF"v[100:103],a[136:139],a[72:75],v[100:103]\n"
"v_cmp_lt_u32 vcc,s94,v255\n"
MF"v[100:103],a[140:143],a[88:91],v[100:103]\n"
"s_mov_b32 s47,s36\n"
RT"a[160:161],v246 offset:12288\n"
"v_cndmask_b32 v228,v228,v254,vcc\n"
MF"v[108:111],a[140:143],a[92:95],v[108:111]\n"
SA"m0,s36,16384\n"
"v_add_u32 v229,v229,v228\n"
SA"s48,s39,516096\n"
MF"v[108:111],a[136:139],a[76:79],v[108:111]\n"
SA"s46,s36,163840\n"
RT"a[162:163],v246 offset:12800\n"
SA"s40,s36,65536\n"
MF"v[112:115],a[144:147],a[64:67],v[112:115]\n"
WT"lgkmcnt(3)\n"
BS"v[60:63],v250,s[8:11],s49 offen offset:1024 sc1\n"
MF"v[112:115],a[148:151],a[80:83],v[112:115]\n"
"s_mov_b32 s41,0\n"
BL"v225,s[8:11],s48 offen offset:0 sc1 lds\n"
MF"v[120:123],a[148:151],a[84:87],v[120:123]\n"
"s_mov_b32 s42,2\n"
RT"a[164:165],v246 offset:36864\n"
"v_mov_b32 v226,v224\n"
MF"v[120:123],a[144:147],a[68:71],v[120:123]\n"
BL"a[240:243],v235,s[8:11],s56 offen offset:3072 sc1\n"
MF"v[128:131],a[144:147],a[72:75],v[128:131]\n"
RT"a[166:167],v246 offset:37376\n"
"v_bfe_u32 v254,v224,4,4\n"
MF"v[128:131],a[148:151],a[88:91],v[128:131]\n"
RT"a[168:169],v247 offset:12288\n"
"v_lshrrev_b32 v255,8,v224\n"
MF"v[136:139],a[148:151],a[92:95],v[136:139]\n"
"s_lshl_b32 s94,s30,4\n"
"v_lshlrev_b32 v255,2,v255\n"
"v_sub_u32 v254,v254,v255\n"
MF"v[136:139],a[144:147],a[76:79],v[136:139]\n"
RT"a[170:171],v247 offset:12800\n"
"v_add_u32 v254,s94,v254\n"
MF"v[116:119],a[152:155],a[64:67],v[116:119]\n"
WT"lgkmcnt(4)\n"
RT"a[172:173],v247 offset:36864\n"
"v_mov_b32 v255,0xff800000\n"
MF"v[116:119],a[156:159],a[80:83],v[116:119]\n"
BL"a[244:247],v235,s[8:11],s57 offen offset:3072 sc1\n"
MF"v[124:127],a[156:159],a[84:87],v[124:127]\n"
BL"v225,s[8:11],s48 offen offset:1024 sc1 lds\n"
MF"v[124:127],a[152:155],a[68:71],v[124:127]\n"
RT"a[174:175],v247 offset:37376\n"
"v_cmp_gt_i32_e64 vcc,0,v254\n"
MF"v[132:135],a[152:155],a[72:75],v[132:135]\n"
RT"a[176:177],v248 offset:12288\n"
"v_cndmask_b32_e64 v208,0,v255,vcc\n"
MF"v[132:135],a[156:159],a[88:91],v[132:135]\n"
"v_cmp_gt_i32_e64 s[52:53],1,v254\n"
"v_cmp_gt_i32_e64 s[62:63],2,v254\n"
MF"v[140:143],a[156:159],a[92:95],v[140:143]\n"
RT"a[178:179],v248 offset:12800\n"
"v_cndmask_b32_e64 v209,0,v255,s[52:53]\n"
MF"v[140:143],a[152:155],a[76:79],v[140:143]\n"
BL"a[248:251],v235,s[8:11],s58 offen offset:3072 sc1\n"
MF"v[144:147],a[160:163],a[64:67],v[144:147]\n"
WT"lgkmcnt(3)\n"
RT"a[180:181],v248 offset:36864\n"
"v_cndmask_b32_e64 v210,0,v255,s[62:63]\n"
MF"v[144:147],a[164:167],a[80:83],v[144:147]\n"
BL"v225,s[8:11],s48 offen offset:2048 sc1 lds\n"
MF"v[152:155],a[164:167],a[84:87],v[152:155]\n"
RT"a[182:183],v248 offset:37376\n"
"v_cmp_gt_i32_e64 vcc,3,v254\n"
MF"v[152:155],a[160:163],a[68:71],v[152:155]\n"
"v_cmp_gt_i32_e64 s[52:53],16,v254\n"
"v_cndmask_b32_e64 v211,0,v255,vcc\n"
MF"v[160:163],a[160:163],a[72:75],v[160:163]\n"
RT"a[184:185],v249 offset:12288\n"
"v_cndmask_b32_e64 v212,0,v255,s[52:53]\n"
MF"v[160:163],a[164:167],a[88:91],v[160:163]\n"
"v_cmp_gt_i32_e64 s[62:63],17,v254\n"
"v_cmp_gt_i32_e64 vcc,18,v254\n"
MF"v[168:171],a[164:167],a[92:95],v[168:171]\n"
RT"a[186:187],v249 offset:12800\n"
"v_cndmask_b32_e64 v213,0,v255,s[62:63]\n"
MF"v[168:171],a[160:163],a[76:79],v[168:171]\n"
BL"a[252:255],v235,s[8:11],s59 offen offset:3072 sc1\n"
MF"v[148:151],a[168:171],a[64:67],v[148:151]\n"
WT"lgkmcnt(3)\n"
RT"a[188:189],v249 offset:36864\n"
"v_cndmask_b32_e64 v214,0,v255,vcc\n"
MF"v[148:151],a[172:175],a[80:83],v[148:151]\n"
BL"v225,s[8:11],s48 offen offset:3072 sc1 lds\n"
MF"v[156:159],a[172:175],a[84:87],v[156:159]\n"
RT"a[190:191],v249 offset:37376\n"
"v_cmp_gt_i32_e64 s[52:53],19,v254\n"
MF"v[156:159],a[168:171],a[68:71],v[156:159]\n"
RT"a[128:129],v242 offset:8192\n"
"v_cndmask_b32_e64 v215,0,v255,s[52:53]\n"
MF"v[164:167],a[168:171],a[72:75],v[164:167]\n"
"v_cmp_gt_i32_e64 s[62:63],32,v254\n"
"v_cmp_gt_i32_e64 vcc,33,v254\n"
MF"v[164:167],a[172:175],a[88:91],v[164:167]\n"
RT"a[130:131],v242 offset:8704\n"
"v_cndmask_b32_e64 v216,0,v255,s[62:63]\n"
MF"v[172:175],a[172:175],a[92:95],v[172:175]\n"
SA"m0,s36,32768\n"
"v_cndmask_b32_e64 v217,0,v255,vcc\n"
"v_cmp_gt_i32_e64 s[52:53],34,v254\n"
MF"v[172:175],a[168:171],a[76:79],v[172:175]\n"
SA"s48,s39,16384\n"
RT"a[132:133],v242 offset:32768\n"
"v_cndmask_b32_e64 v218,0,v255,s[52:53]\n"
MF"v[176:179],a[176:179],a[64:67],v[176:179]\n"
WT"lgkmcnt(4)\n"
BL"v225,s[8:11],s48 offen offset:0 sc1 lds\n"
MF"v[176:179],a[180:183],a[80:83],v[176:179]\n"
"s_mov_b32 s49,278528\n"
RT"a[134:135],v242 offset:33280\n"
"v_cmp_gt_i32_e64 s[62:63],35,v254\n"
MF"v[184:187],a[180:183],a[84:87],v[184:187]\n"
BS"v[64:67],v250,s[8:11],s49 offen offset:0 sc1\n"
MF"v[184:187],a[176:179],a[68:71],v[184:187]\n"
RT"a[136:137],v243 offset:8192\n"
"v_cndmask_b32_e64 v219,0,v255,s[62:63]\n"
MF"v[192:195],a[176:179],a[72:75],v[192:195]\n"
"s_lshl_b32 s59,s34,5\n"
"v_cmp_gt_i32_e64 vcc,48,v254\n"
"v_cmp_gt_i32_e64 s[52:53],49,v254\n"
MF"v[192:195],a[180:183],a[88:91],v[192:195]\n"
"s_sub_u32 s60,480,s59\n"
RT"a[138:139],v243 offset:8704\n"
"v_cndmask_b32_e64 v220,0,v255,vcc\n"
MF"v[200:203],a[180:183],a[92:95],v[200:203]\n"
SA"s59,s59,s35\n"
"v_cndmask_b32_e64 v221,0,v255,s[52:53]\n"
"v_cmp_gt_i32_e64 s[62:63],50,v254\n"
MF"v[200:203],a[176:179],a[76:79],v[200:203]\n"
SA"s60,s60,s35\n"
RT"a[140:141],v243 offset:32768\n"
"v_cndmask_b32_e64 v222,0,v255,s[62:63]\n"
MF"v[180:183],a[184:187],a[64:67],v[180:183]\n"
WT"lgkmcnt(3)\n"
BL"v225,s[8:11],s48 offen offset:1024 sc1 lds\n"
MF"v[180:183],a[188:191],a[80:83],v[180:183]\n"
"s_lshl_b32 s59,s59,15\n"
RT"a[142:143],v243 offset:33280\n"
"v_cmp_gt_i32_e64 vcc,51,v254\n"
MF"v[188:191],a[188:191],a[84:87],v[188:191]\n"
"s_lshl_b32 s60,s60,15\n"
"v_cndmask_b32_e64 v223,0,v255,vcc\n"
SA"s50,s36,42729472\n"
MF"v[188:191],a[184:187],a[68:71],v[188:191]\n"
SA"s59,s59,s50\n"
RT"a[144:145],v244 offset:8192\n"
SA"s60,s60,s50\n"
MF"v[196:199],a[184:187],a[72:75],v[196:199]\n"
"s_lshl_b32 s61,s34,3\n"
SA"s61,s61,s32\n"
"s_lshl_b32 s61,s61,2\n"
MF"v[196:199],a[188:191],a[88:91],v[196:199]\n"
SA"s61,s61,s30\n"
RT"a[146:147],v244 offset:8704\n"
"s_lshl_b32 s61,s61,2\n"
MF"v[204:207],a[188:191],a[92:95],v[204:207]\n"
SA"s61,s61,s33\n"
"s_lshl_b32 s61,s61,6\n"
MF"v[204:207],a[184:187],a[76:79],v[204:207]\n"
SA"s61,s61,81002496\n"
RT"a[148:149],v244 offset:32768\n"
"s_lshl_b32 s98,s35,3\n"
MF"v[80:83],a[128:131],a[96:99],v[80:83]\n"
SA"s98,s98,s34\n"
BL"v225,s[8:11],s48 offen offset:2048 sc1 lds\n"
MF"v[80:83],a[132:135],a[112:115],v[80:83]\n"
"s_lshl_b32 s98,s98,3\n"
RT"a[150:151],v244 offset:33280\n"
MF"v[88:91],a[132:135],a[116:119],v[88:91]\n"
SA"s98,s98,4352\n"
RT"a[152:153],v245 offset:8192\n"
SA"s98,s98,s45\n"
MF"v[88:91],a[128:131],a[100:103],v[88:91]\n"
"s_lshl_b32 s50,s34,2\n"
MF"v[96:99],a[128:131],a[104:107],v[96:99]\n"
"s_mul_i32 s51,s33,0x1011100\n"
RT"a[154:155],v245 offset:8704\n"
SA"s51,s51,0xf48c00\n"
MF"v[96:99],a[132:135],a[120:123],v[96:99]\n"
"s_lshr_b32 s51,s51,s50\n"
MF"v[104:107],a[132:135],a[124:127],v[104:107]\n"
"s_and_b32 s51,s51,15\n"
RT"a[156:157],v245 offset:32768\n"
"s_lshr_b32 s50,s31,3\n"
MF"v[104:107],a[128:131],a[108:111],v[104:107]\n"
WT"lgkmcnt(4)\n"
"s_cmp_eq_u32 s50,6\n"
"s_cselect_b32 s51,15,s51\n"
MF"v[84:87],a[136:139],a[96:99],v[84:87]\n"
"s_cmp_eq_u32 s50,26\n"
RT"a[158:159],v245 offset:33280\n"
"s_cselect_b32 s51,0,s51\n"
MF"v[84:87],a[140:143],a[112:115],v[84:87]\n"
"s_mul_i32 s58,s32,14\n"
BL"v225,s[8:11],s48 offen offset:3072 sc1 lds\n"
MF"v[92:95],a[140:143],a[116:119],v[92:95]\n"
SA"s58,s58,s51\n"
RT"a[160:161],v242 offset:12288\n"
"s_sub_u32 s58,s58,1\n"
MF"v[92:95],a[136:139],a[100:103],v[92:95]\n"
"s_cmp_lt_u32 s58,108\n"
"s_cselect_b32 s90,s51,0\n"
"s_cmp_lg_u32 s90,0\n"
MF"v[100:103],a[136:139],a[104:107],v[100:103]\n"
"s_cselect_b32 s57,1,0\n"
RT"a[162:163],v242 offset:12800\n"
"s_cmp_eq_u32 s51,15\n"
MF"v[100:103],a[140:143],a[120:123],v[100:103]\n"
"s_cselect_b32 s57,2,s57\n"
"s_cmp_eq_u32 s50,6\n"
"s_cselect_b32 s90,4,s33\n"
MF"v[108:111],a[140:143],a[124:127],v[108:111]\n"
"s_mul_i32 s50,s32,5\n"
RT"a[164:165],v242 offset:36864\n"
SA"m0,s36,49152\n"
MF"v[108:111],a[136:139],a[108:111],v[108:111]\n"
SA"s48,s39,532480\n"
SA"s90,s90,s50\n"
"s_cmp_eq_u32 s57,2\n"
MF"v[112:115],a[144:147],a[96:99],v[112:115]\n"
WT"lgkmcnt(3)\n"
RT"a[166:167],v242 offset:37376\n"
"s_cselect_b32 s58,s90,s58\n"
MF"v[112:115],a[148:151],a[112:115],v[112:115]\n"
"s_lshl_b32 s56,s34,1\n"
BL"v225,s[8:11],s48 offen offset:0 sc1 lds\n"
MF"v[120:123],a[148:151],a[116:119],v[120:123]\n"
"s_sub_u32 s56,s56,2\n"
RT"a[168:169],v243 offset:12288\n"
"s_cmp_eq_u32 s34,7\n"
MF"v[120:123],a[144:147],a[100:103],v[120:123]\n"
"s_cselect_b32 s56,2,s56\n"
"s_cmp_eq_u32 s57,2\n"
"s_cselect_b32 s56,5,s56\n"
MF"v[128:131],a[144:147],a[104:107],v[128:131]\n"
"s_cmp_lt_u32 s30,2\n"
RT"a[170:171],v243 offset:12800\n"
"s_cselect_b64 s[24:25],s[78:79],s[80:81]\n"
MF"v[128:131],a[148:151],a[120:123],v[128:131]\n"
"s_cmp_eq_u32 s57,2\n"
BS"v[68:71],v250,s[8:11],s49 offen offset:1024 sc1\n"
MF"v[136:139],a[148:151],a[124:127],v[136:139]\n"
"s_cselect_b64 s[24:25],s[82:83],s[24:25]\n"
RT"a[172:173],v243 offset:36864\n"
"s_and_b32 s50,s30,1\n"
MF"v[136:139],a[144:147],a[108:111],v[136:139]\n"
"s_mul_i32 s50,s50,10\n"
RT"a[174:175],v243 offset:37376\n"
"s_mul_i32 s51,s30,14\n"
MF"v[116:119],a[152:155],a[96:99],v[116:119]\n"
"s_min_u32 s51,s51,41\n"
BL"v225,s[8:11],s48 offen offset:1024 sc1 lds\n"
MF"v[116:119],a[156:159],a[112:115],v[116:119]\n"
"s_cmp_eq_u32 s57,2\n"
RT"a[176:177],v244 offset:12288\n"
"s_cselect_b32 s50,s51,s50\n"
MF"v[124:127],a[156:159],a[116:119],v[124:127]\n"
"s_cmp_lt_u32 s30,2\n"
"s_cselect_b32 s51,14,13\n"
"s_cmp_lt_u32 s58,108\n"
MF"v[124:127],a[152:155],a[100:103],v[124:127]\n"
"s_cselect_b32 s92,10,0\n"
RT"a[178:179],v244 offset:12800\n"
"s_cmp_eq_u32 s57,2\n"
MF"v[132:135],a[152:155],a[104:107],v[132:135]\n"
"s_cselect_b32 s92,s51,s92\n"
"s_mov_b32 s51,3456\n"
"s_cmp_eq_u32 s57,2\n"
MF"v[132:135],a[156:159],a[120:123],v[132:135]\n"
"s_cselect_b32 s51,1280,s51\n"
RT"a[180:181],v244 offset:36864\n"
"s_lshl_b32 s62,s51,5\n"
MF"v[140:143],a[156:159],a[124:127],v[140:143]\n"
"s_mul_i32 s26,s62,s92\n"
"s_mul_i32 s90,s62,s50\n"
"s_lshl_b32 s91,s58,5\n"
MF"v[140:143],a[152:155],a[108:111],v[140:143]\n"
WT"lgkmcnt(3)\n"
RT"a[182:183],v244 offset:37376\n"
SA"s90,s90,s91\n"
MF"v[144:147],a[160:163],a[96:99],v[144:147]\n"
SA"s24,s24,s90\n"
BL"v225,s[8:11],s48 offen offset:2048 sc1 lds\n"
MF"v[144:147],a[164:167],a[112:115],v[144:147]\n"
"s_addc_u32 s25,s25,0\n"
RT"a[184:185],v245 offset:12288\n"
"s_and_b32 s25,s25,0xffff\n"
MF"v[152:155],a[164:167],a[116:119],v[152:155]\n"
"s_sub_u32 s90,s51,32\n"
"v_lshrrev_b32 v235,5,v224\n"
"v_mul_u32_u24 v235,s51,v235\n"
MF"v[152:155],a[160:163],a[100:103],v[152:155]\n"
"s_sub_u32 s26,s26,s90\n"
RT"a[186:187],v245 offset:12800\n"
"v_and_b32 v239,16,v224\n"
MF"v[160:163],a[160:163],a[104:107],v[160:163]\n"
"s_cselect_b32 s26,0,s26\n"
"v_add_u32 v235,v235,v239\n"
"s_mov_b32 s27,0x20000\n"
MF"v[160:163],a[164:167],a[120:123],v[160:163]\n"
"s_mul_i32 s63,s35,20480\n"
RT"a[188:189],v245 offset:36864\n"
SA"s63,s63,2770944\n"
MF"v[168:171],a[164:167],a[124:127],v[168:171]\n"
"s_cmp_eq_u32 s57,2\n"
"s_cselect_b32 s63,2695168,s63\n"
MF"v[168:171],a[160:163],a[108:111],v[168:171]\n"
"s_lshl_b32 s50,s50,10\n"
RT"a[190:191],v245 offset:37376\n"
SA"s63,s63,s50\n"
MF"v[148:151],a[168:171],a[96:99],v[148:151]\n"
"s_mov_b32 s50,1065353216\n"
BL"v225,s[8:11],s48 offen offset:3072 sc1 lds\n"
MF"v[148:151],a[172:175],a[112:115],v[148:151]\n"
"s_cmp_lg_u32 s92,0\n"
"s_cselect_b32 s50,s45,s50\n"
"s_mov_b32 s93,2695168\n"
MF"v[156:159],a[172:175],a[116:119],v[156:159]\n"
"s_cmp_eq_u32 s57,2\n"
"s_cselect_b32 s93,2750464,s93\n"
MF"v[156:159],a[168:171],a[100:103],v[156:159]\n"
"s_lshl_b32 s51,s58,9\n"
MF"v[164:167],a[168:171],a[104:107],v[164:167]\n"
SA"s93,s93,s51\n"
SA"s93,s93,s50\n"
MF"v[164:167],a[172:175],a[120:123],v[164:167]\n"
"s_mov_b32 s94,6912\n"
"s_barrier\n"
MF"v[172:175],a[172:175],a[124:127],v[172:175]\n"
"s_cmp_eq_u32 s57,2\n"
RT"a[128:129],v246 offset:16384\n"
"s_cselect_b32 s94,6400,s94\n"
MF"v[172:175],a[168:171],a[108:111],v[172:175]\n"
WT"lgkmcnt(1)\n"
"s_lshl_b32 s51,s58,3\n"
SA"s94,s94,s51\n"
MF"v[176:179],a[176:179],a[96:99],v[176:179]\n"
SA"s94,s94,s50\n"
RT"a[130:131],v246 offset:16896\n"
"s_mov_b32 s52,0\n"
MF"v[176:179],a[180:183],a[112:115],v[176:179]\n"
"s_lshl_b32 s50,s35,6\n"
RT"a[132:133],v246 offset:40960\n"
MF"v[184:187],a[180:183],a[116:119],v[184:187]\n"
SA"s50,s50,2304\n"
MF"v[184:187],a[176:179],a[100:103],v[184:187]\n"
"s_cmp_eq_u32 s57,2\n"
RT"a[134:135],v246 offset:41472\n"
"s_cselect_b32 s50,6912,s50\n"
MF"v[192:195],a[176:179],a[104:107],v[192:195]\n"
"s_mov_b32 s51,48\n"
RT"a[136:137],v242 offset:16384\n"
SA"s49,s88,278528\n"
MF"v[192:195],a[180:183],a[120:123],v[192:195]\n"
"s_cmp_eq_u32 s57,2\n"
"s_cselect_b32 s51,848,s51\n"
"v_min_u32 v238,s51,v224\n"
MF"v[200:203],a[180:183],a[124:127],v[200:203]\n"
RT"a[138:139],v242 offset:16896\n"
"v_add_u32 v238,s50,v238\n"
MF"v[200:203],a[176:179],a[108:111],v[200:203]\n"
"s_mov_b32 s16,0\n"
BS"v[72:75],v250,s[8:11],s49 offen offset:0 sc1\n"
MF"v[180:183],a[184:187],a[96:99],v[180:183]\n"
"s_mov_b32 s20,8\n"
RT"a[140:141],v242 offset:40960\n"
"v_and_b32 v239,0x80,v224\n"
MF"v[180:183],a[188:191],a[112:115],v[180:183]\n"
"s_mov_b32 s18,2130706432\n"
RT"a[142:143],v242 offset:41472\n"
"v_lshl_add_u32 v236,v239,23,v224\n"
MF"v[188:191],a[188:191],a[116:119],v[188:191]\n"
"s_lshl_b32 s21,s32,4\n"
"s_lshr_b32 s22,s30,1\n"
MF"v[188:191],a[184:187],a[100:103],v[188:191]\n"
SA"s21,s21,s22\n"
RT"a[144:145],v247 offset:16384\n"
MF"v[196:199],a[184:187],a[104:107],v[196:199]\n"
"s_mul_i32 s21,s21,40960\n"
"s_lshl_b32 s22,s33,1\n"
MF"v[196:199],a[188:191],a[120:123],v[196:199]\n"
"s_and_b32 s23,s30,1\n"
RT"a[146:147],v247 offset:16896\n"
MF"v[204:207],a[188:191],a[124:127],v[204:207]\n"
SA"s22,s22,s23\n"
RT"a[148:149],v247 offset:40960\n"
"s_mul_i32 s22,s22,160\n"
MF"v[204:207],a[184:187],a[108:111],v[204:207]\n"
WT"vmcnt(37) lgkmcnt(4)\n"
SA"s17,s21,s22\n"
MF"v[80:83],a[128:131],a[192:195],v[80:83]\n"
WT"vmcnt(32)\n"
RT"a[150:151],v247 offset:41472\n"
"v_lshrrev_b32 v240,4,v224\n"
MF"v[80:83],a[132:135],a[208:211],v[80:83]\n"
WT"vmcnt(31)\n"
RT"a[152:153],v243 offset:16384\n"
"v_add_u32 v240,0,v240\n"
MF"v[88:91],a[132:135],a[212:215],v[88:91]\n"
"v_mul_u32_u24 v240,205,v240\n"
"v_lshrrev_b32 v240,11,v240\n"
MF"v[88:91],a[128:131],a[196:199],v[88:91]\n"
RT"a[154:155],v243 offset:16896\n"
"v_mul_u32_u24 v240,1120,v240\n"
MF"v[96:99],a[128:131],a[200:203],v[96:99]\n"
WT"vmcnt(30)\n"
RT"a[156:157],v243 offset:40960\n"
"v_add_u32 v240,v240,v224\n"
MF"v[96:99],a[132:135],a[216:219],v[96:99]\n"
WT"vmcnt(28)\n"
"v_lshrrev_b32 v241,4,v224\n"
"v_add_u32 v241,64,v241\n"
MF"v[104:107],a[132:135],a[220:223],v[104:107]\n"
RT"a[158:159],v243 offset:41472\n"
"v_mul_u32_u24 v241,205,v241\n"
MF"v[104:107],a[128:131],a[204:207],v[104:107]\n"
WT"vmcnt(26)\n"
"v_lshrrev_b32 v241,11,v241\n"
"v_mul_u32_u24 v241,1120,v241\n"
MF"v[80:83],a[136:139],a[224:227],v[80:83]\n"
WT"vmcnt(17) lgkmcnt(3)\n"
RT"a[160:161],v248 offset:16384\n"
"v_add_u32 v241,v241,v224\n"
MF"v[80:83],a[140:143],a[240:243],v[80:83]\n"
WT"vmcnt(16)\n"
"v_lshrrev_b32 v251,4,v224\n"
"v_add_u32 v251,128,v251\n"
MF"v[88:91],a[140:143],a[244:247],v[88:91]\n"
RT"a[162:163],v248 offset:16896\n"
"v_mul_u32_u24 v251,205,v251\n"
MF"v[88:91],a[136:139],a[228:231],v[88:91]\n"
RT"a[164:165],v248 offset:40960\n"
"v_lshrrev_b32 v251,11,v251\n"
MF"v[96:99],a[136:139],a[232:235],v[96:99]\n"
WT"vmcnt(14)\n"
"v_mul_u32_u24 v251,1120,v251\n"
"v_add_u32 v251,v251,v224\n"
MF"v[96:99],a[140:143],a[248:251],v[96:99]\n"
WT"vmcnt(12)\n"
RT"a[166:167],v248 offset:41472\n"
"v_and_b32 v237,512,v224\n"
MF"v[104:107],a[140:143],a[252:255],v[104:107]\n"
RT"a[168:169],v244 offset:16384\n"
"v_lshl_add_u32 v251,v237,20,v251\n"
MF"v[104:107],a[136:139],a[236:239],v[104:107]\n"
"v_lshrrev_b32 v252,6,v224\n"
"v_mul_u32_u24 v252,160,v252\n"
MF"v[84:87],a[144:147],a[192:195],v[84:87]\n"
RT"a[170:171],v244 offset:16896\n"
"v_bfe_u32 v237,v224,4,2\n"
MF"v[84:87],a[148:151],a[208:211],v[84:87]\n"
"s_cmp_eq_u32 s34,7\n"
RT"a[172:173],v244 offset:40960\n"
"v_lshl_add_u32 v252,v237,3,v252\n"
MF"v[92:95],a[148:151],a[212:215],v[92:95]\n"
"s_cselect_b32 s57,3,s57\n"
"v_lshrrev_b32 v254,4,v224\n"
"v_min_u32 v254,49,v254\n"
MF"v[92:95],a[144:147],a[196:199],v[92:95]\n"
RT"a[174:175],v244 offset:41472\n"
"v_mul_u32_u24 v253,52,v254\n"
MF"v[100:103],a[144:147],a[200:203],v[100:103]\n"
"v_lshrrev_b32 v253,8,v253\n"
MF"v[100:103],a[148:151],a[216:219],v[100:103]\n"
RT"a[176:177],v249 offset:16384\n"
"v_mul_u32_u24 v253,2901,v253\n"
MF"v[108:111],a[148:151],a[220:223],v[108:111]\n"
"v_mul_u32_u24 v254,111,v254\n"
"v_add_u32 v254,v254,v253\n"
MF"v[108:111],a[144:147],a[204:207],v[108:111]\n"
WT"lgkmcnt(3)\n"
RT"a[178:179],v249 offset:16896\n"
"v_and_b32 v254,-4,v254\n"
MF"v[84:87],a[152:155],a[224:227],v[84:87]\n"
RT"a[180:181],v249 offset:40960\n"
"v_lshrrev_b32 v253,5,v224\n"
MF"v[84:87],a[156:159],a[240:243],v[84:87]\n"
BS"v[76:79],v250,s[8:11],s49 offen offset:1024 sc1\n"
MF"v[92:95],a[156:159],a[244:247],v[92:95]\n"
RT"a[182:183],v249 offset:41472\n"
"v_min_u32 v253,13,v253\n"
MF"v[92:95],a[152:155],a[228:231],v[92:95]\n"
RT"a[184:185],v245 offset:16384\n"
"v_mul_u32_u24 v253,1280,v253\n"
MF"v[100:103],a[152:155],a[232:235],v[100:103]\n"
"v_bfe_u32 v237,v224,4,1\n"
"v_mul_u32_u24 v237,156,v237\n"
MF"v[100:103],a[156:159],a[248:251],v[100:103]\n"
"s_cmp_lt_u32 s30,2\n"
RT"a[186:187],v245 offset:16896\n"
"v_add_u32 v253,v253,v237\n"
MF"v[108:111],a[156:159],a[252:255],v[108:111]\n"
"s_cselect_b64 s[12:13],s[78:79],s[80:81]\n"
RT"a[188:189],v245 offset:40960\n"
"s_and_b32 s13,s13,0xffff\n"
MF"v[108:111],a[152:155],a[236:239],v[108:111]\n"
"s_mov_b32 s14,2211840\n"
"s_mov_b32 s15,0x20000\n"
MF"v[112:115],a[160:163],a[192:195],v[112:115]\n"
"s_lshr_b32 s90,s31,3\n"
RT"a[190:191],v245 offset:41472\n"
MF"v[112:115],a[164:167],a[208:211],v[112:115]\n"
"s_lshl_b32 s91,s90,1\n"
MF"v[120:123],a[164:167],a[212:215],v[120:123]\n"
"s_and_b32 s50,s30,1\n"
RT"a[128:129],v246 offset:20480\n"
PK"v80,v80,v81\n"
MF"v[120:123],a[160:163],a[196:199],v[120:123]\n"
SA"s91,s91,s50\n"
PK"v81,v82,v83\n"
PK"v82,v84,v85\n"
MF"v[128:131],a[160:163],a[200:203],v[128:131]\n"
"s_mul_i32 s91,s91,34560\n"
RT"a[130:131],v246 offset:20992\n"
PK"v83,v86,v87\n"
MF"v[128:131],a[164:167],a[216:219],v[128:131]\n"
"s_mul_i32 s50,s32,448\n"
RT"a[132:133],v246 offset:45056\n"
"v_accvgpr_write_b32 a0,v80\n"
MF"v[136:139],a[164:167],a[220:223],v[136:139]\n"
SA"s91,s91,s50\n"
"v_accvgpr_write_b32 a1,v81\n"
"v_accvgpr_write_b32 a2,v82\n"
MF"v[136:139],a[160:163],a[204:207],v[136:139]\n"
"buffer_load_dword v255,v254,s[12:15],s91 offen\n"
MF"v[112:115],a[168:171],a[224:227],v[112:115]\n"
WT"lgkmcnt(3)\n"
RT"a[134:135],v246 offset:45568\n"
RT"a[136:137],v242 offset:20480\n"
MF"v[112:115],a[172:175],a[240:243],v[112:115]\n"
"s_mov_b32 s12,s82\n"
"v_accvgpr_write_b32 a3,v83\n"
PK"v88,v88,v89\n"
MF"v[120:123],a[172:175],a[244:247],v[120:123]\n"
"s_and_b32 s13,s83,0xffff\n"
RT"a[138:139],v242 offset:20992\n"
PK"v89,v90,v91\n"
MF"v[120:123],a[168:171],a[228:231],v[120:123]\n"
"s_lshl_b32 s90,s90,2\n"
RT"a[140:141],v242 offset:45056\n"
PK"v90,v92,v93\n"
MF"v[128:131],a[168:171],a[232:235],v[128:131]\n"
SA"s90,s90,s30\n"
PK"v91,v94,v95\n"
"v_accvgpr_write_b32 a16,v88\n"
MF"v[128:131],a[172:175],a[248:251],v[128:131]\n"
"s_mul_i32 s90,s90,17920\n"
RT"a[142:143],v242 offset:45568\n"
"v_accvgpr_write_b32 a17,v89\n"
MF"v[136:139],a[172:175],a[252:255],v[136:139]\n"
"s_mul_i32 s50,s32,160\n"
"v_accvgpr_write_b32 a18,v90\n"
"v_accvgpr_write_b32 a19,v91\n"
MF"v[136:139],a[168:171],a[236:239],v[136:139]\n"
SA"s90,s90,s50\n"
RT"a[144:145],v247 offset:20480\n"
PK"v96,v96,v97\n"
MF"v[116:119],a[176:179],a[192:195],v[116:119]\n"
"buffer_load_dword v255,v253,s[12:15],s90 offen\n"
MF"v[116:119],a[180:183],a[208:211],v[116:119]\n"
RT"a[146:147],v247 offset:20992\n"
"v_min_u32 v253,624,v224\n"
MF"v[124:127],a[180:183],a[212:215],v[124:127]\n"
RT"a[148:149],v247 offset:45056\n"
"v_lshlrev_b32 v253,3,v253\n"
MF"v[124:127],a[176:179],a[196:199],v[124:127]\n"
PK"v97,v98,v99\n"
PK"v98,v100,v101\n"
MF"v[132:135],a[176:179],a[200:203],v[132:135]\n"
RT"a[150:151],v247 offset:45568\n"
PK"v99,v102,v103\n"
MF"v[132:135],a[180:183],a[216:219],v[132:135]\n"
RT"a[152:153],v243 offset:20480\n"
"v_accvgpr_write_b32 a32,v96\n"
MF"v[140:143],a[180:183],a[220:223],v[140:143]\n"
"v_accvgpr_write_b32 a33,v97\n"
"v_accvgpr_write_b32 a34,v98\n"
MF"v[140:143],a[176:179],a[204:207],v[140:143]\n"
RT"a[154:155],v243 offset:20992\n"
"v_accvgpr_write_b32 a35,v99\n"
MF"v[116:119],a[184:187],a[224:227],v[116:119]\n"
RT"a[156:157],v243 offset:45056\n"
PK"v104,v104,v105\n"
MF"v[116:119],a[188:191],a[240:243],v[116:119]\n"
PK"v105,v106,v107\n"
PK"v106,v108,v109\n"
MF"v[124:127],a[188:191],a[244:247],v[124:127]\n"
RT"a[158:159],v243 offset:45568\n"
PK"v107,v110,v111\n"
MF"v[124:127],a[184:187],a[228:231],v[124:127]\n"
"v_accvgpr_write_b32 a48,v104\n"
"v_accvgpr_write_b32 a49,v105\n"
MF"v[132:135],a[184:187],a[232:235],v[132:135]\n"
RT"a[160:161],v248 offset:20480\n"
"v_accvgpr_write_b32 a50,v106\n"
MF"v[132:135],a[188:191],a[248:251],v[132:135]\n"
"v_accvgpr_write_b32 a51,v107\n"
MF"v[140:143],a[188:191],a[252:255],v[140:143]\n"
RT"a[162:163],v248 offset:20992\n"
MF"v[140:143],a[184:187],a[236:239],v[140:143]\n"
WT"lgkmcnt(3)\n"
RT"a[164:165],v248 offset:45056\n"
MF"v[144:147],a[128:131],a[192:195],v[144:147]\n"
MF"v[144:147],a[132:135],a[208:211],v[144:147]\n"
RT"a[166:167],v248 offset:45568\n"
MF"v[152:155],a[132:135],a[212:215],v[152:155]\n"
RT"a[168:169],v244 offset:20480\n"
PK"v112,v112,v113\n"
MF"v[152:155],a[128:131],a[196:199],v[152:155]\n"
PK"v113,v114,v115\n"
PK"v114,v116,v117\n"
MF"v[160:163],a[128:131],a[200:203],v[160:163]\n"
RT"a[170:171],v244 offset:20992\n"
PK"v115,v118,v119\n"
MF"v[160:163],a[132:135],a[216:219],v[160:163]\n"
RT"a[172:173],v244 offset:45056\n"
"v_accvgpr_write_b32 a4,v112\n"
MF"v[168:171],a[132:135],a[220:223],v[168:171]\n"
"v_accvgpr_write_b32 a5,v113\n"
"v_accvgpr_write_b32 a6,v114\n"
MF"v[168:171],a[128:131],a[204:207],v[168:171]\n"
RT"a[174:175],v244 offset:45568\n"
"v_accvgpr_write_b32 a7,v115\n"
MF"v[144:147],a[136:139],a[224:227],v[144:147]\n"
PK"v120,v120,v121\n"
PK"v121,v122,v123\n"
MF"v[144:147],a[140:143],a[240:243],v[144:147]\n"
RT"a[176:177],v249 offset:20480\n"
PK"v122,v124,v125\n"
MF"v[152:155],a[140:143],a[244:247],v[152:155]\n"
PK"v123,v126,v127\n"
"v_accvgpr_write_b32 a20,v120\n"
MF"v[152:155],a[136:139],a[228:231],v[152:155]\n"
RT"a[178:179],v249 offset:20992\n"
"v_accvgpr_write_b32 a21,v121\n"
MF"v[160:163],a[136:139],a[232:235],v[160:163]\n"
RT"a[180:181],v249 offset:45056\n"
"v_accvgpr_write_b32 a22,v122\n"
MF"v[160:163],a[140:143],a[248:251],v[160:163]\n"
"v_accvgpr_write_b32 a23,v123\n"
MF"v[168:171],a[140:143],a[252:255],v[168:171]\n"
RT"a[182:183],v249 offset:45568\n"
PK"v128,v128,v129\n"
MF"v[168:171],a[136:139],a[236:239],v[168:171]\n"
RT"a[184:185],v245 offset:20480\n"
PK"v129,v130,v131\n"
MF"v[148:151],a[144:147],a[192:195],v[148:151]\n"
PK"v130,v132,v133\n"
PK"v131,v134,v135\n"
MF"v[148:151],a[148:151],a[208:211],v[148:151]\n"
RT"a[186:187],v245 offset:20992\n"
"v_accvgpr_write_b32 a36,v128\n"
MF"v[156:159],a[148:151],a[212:215],v[156:159]\n"
RT"a[188:189],v245 offset:45056\n"
"v_accvgpr_write_b32 a37,v129\n"
MF"v[156:159],a[144:147],a[196:199],v[156:159]\n"
"v_accvgpr_write_b32 a38,v130\n"
"v_accvgpr_write_b32 a39,v131\n"
MF"v[164:167],a[144:147],a[200:203],v[164:167]\n"
RT"a[190:191],v245 offset:45568\n"
PK"v136,v136,v137\n"
MF"v[164:167],a[148:151],a[216:219],v[164:167]\n"
PK"v137,v138,v139\n"
PK"v138,v140,v141\n"
MF"v[172:175],a[148:151],a[220:223],v[172:175]\n"
PK"v139,v142,v143\n"
"v_accvgpr_write_b32 a52,v136\n"
MF"v[172:175],a[144:147],a[204:207],v[172:175]\n"
"v_accvgpr_write_b32 a53,v137\n"
"v_accvgpr_write_b32 a54,v138\n"
MF"v[148:151],a[152:155],a[224:227],v[148:151]\n"
WT"lgkmcnt(2)\n"
"v_accvgpr_write_b32 a55,v139\n"
MF"v[148:151],a[156:159],a[240:243],v[148:151]\n"
MF"v[156:159],a[156:159],a[244:247],v[156:159]\n"
MF"v[156:159],a[152:155],a[228:231],v[156:159]\n"
MF"v[164:167],a[152:155],a[232:235],v[164:167]\n"
MF"v[164:167],a[156:159],a[248:251],v[164:167]\n"
MF"v[172:175],a[156:159],a[252:255],v[172:175]\n"
MF"v[172:175],a[152:155],a[236:239],v[172:175]\n"
MF"v[176:179],a[160:163],a[192:195],v[176:179]\n"
MF"v[176:179],a[164:167],a[208:211],v[176:179]\n"
MF"v[176:179],a[168:171],a[224:227],v[176:179]\n"
PK"v144,v144,v145\n"
PK"v145,v146,v147\n"
MF"v[176:179],a[172:175],a[240:243],v[176:179]\n"
PK"v146,v148,v149\n"
PK"v147,v150,v151\n"
MF"v[180:183],a[176:179],a[192:195],v[180:183]\n"
"v_mov_b32 v254,0x3f803f80\n"
"v_accvgpr_write_b32 a8,v144\n"
MF"v[180:183],a[180:183],a[208:211],v[180:183]\n"
"v_accvgpr_write_b32 a9,v145\n"
"v_accvgpr_write_b32 a128,v254\n"
MF"v[180:183],a[184:187],a[224:227],v[180:183]\n"
WT"lgkmcnt(0)\n"
"v_accvgpr_write_b32 a129,v254\n"
"v_accvgpr_write_b32 a130,v254\n"
MF"v[180:183],a[188:191],a[240:243],v[180:183]\n"
"v_accvgpr_write_b32 a131,v254\n"
"v_accvgpr_write_b32 a10,v146\n"
MF"v[184:187],a[160:163],a[196:199],v[184:187]\n"
"v_accvgpr_write_b32 a11,v147\n"
PK"v152,v152,v153\n"
MF"v[184:187],a[164:167],a[212:215],v[184:187]\n"
PK"v153,v154,v155\n"
PK"v154,v156,v157\n"
MF"v[184:187],a[168:171],a[228:231],v[184:187]\n"
PK"v155,v158,v159\n"
"v_accvgpr_write_b32 a24,v152\n"
MF"v[184:187],a[172:175],a[244:247],v[184:187]\n"
"v_accvgpr_write_b32 a25,v153\n"
PK"v176,v176,v177\n"
MF"v[188:191],a[176:179],a[196:199],v[188:191]\n"
PK"v177,v178,v179\n"
"v_accvgpr_write_b32 a26,v154\n"
MF"v[188:191],a[180:183],a[212:215],v[188:191]\n"
"v_accvgpr_write_b32 a27,v155\n"
PK"v178,v180,v181\n"
MF"v[188:191],a[184:187],a[228:231],v[188:191]\n"
PK"v179,v182,v183\n"
"v_accvgpr_write_b32 a12,v176\n"
MF"v[188:191],a[188:191],a[244:247],v[188:191]\n"
"v_accvgpr_write_b32 a13,v177\n"
"v_accvgpr_write_b32 a14,v178\n"
MF"v[192:195],a[160:163],a[200:203],v[192:195]\n"
"v_accvgpr_write_b32 a15,v179\n"
PK"v160,v160,v161\n"
MF"v[192:195],a[164:167],a[216:219],v[192:195]\n"
WT"vmcnt(14)\n"
"s_barrier\n"
MF"v[192:195],a[168:171],a[232:235],v[192:195]\n"
RD"a[64:67],v226 offset:0\n"
PK"v161,v162,v163\n"
MF"v[192:195],a[172:175],a[248:251],v[192:195]\n"
PK"v162,v164,v165\n"
PK"v163,v166,v167\n"
MF"v[196:199],a[176:179],a[200:203],v[196:199]\n"
RD"a[68:71],v226 offset:1024\n"
"v_accvgpr_write_b32 a40,v160\n"
MF"v[196:199],a[180:183],a[216:219],v[196:199]\n"
"v_accvgpr_write_b32 a41,v161\n"
"v_accvgpr_write_b32 a42,v162\n"
MF"v[196:199],a[184:187],a[232:235],v[196:199]\n"
RD"a[72:75],v226 offset:2048\n"
"v_accvgpr_write_b32 a43,v163\n"
MF"v[196:199],a[188:191],a[248:251],v[196:199]\n"
PK"v184,v184,v185\n"
PK"v185,v186,v187\n"
MF"v[200:203],a[160:163],a[204:207],v[200:203]\n"
RD"a[76:79],v226 offset:3072\n"
PK"v186,v188,v189\n"
MF"v[200:203],a[164:167],a[220:223],v[200:203]\n"
PK"v187,v190,v191\n"
PK"v168,v168,v169\n"
MF"v[200:203],a[168:171],a[236:239],v[200:203]\n"
RD"a[80:83],v226 offset:4096\n"
PK"v169,v170,v171\n"
MF"v[200:203],a[172:175],a[252:255],v[200:203]\n"
"v_accvgpr_write_b32 a28,v184\n"
"v_accvgpr_write_b32 a29,v185\n"
MF"v[204:207],a[176:179],a[204:207],v[204:207]\n"
RD"a[84:87],v226 offset:5120\n"
PK"v170,v172,v173\n"
MF"v[204:207],a[180:183],a[220:223],v[204:207]\n"
PK"v171,v174,v175\n"
"v_accvgpr_write_b32 a30,v186\n"
MF"v[204:207],a[184:187],a[236:239],v[204:207]\n"
"v_accvgpr_write_b32 a31,v187\n"
"v_accvgpr_write_b32 a56,v168\n"
MF"v[204:207],a[188:191],a[252:255],v[204:207]\n"
"v_accvgpr_write_b32 a57,v169\n"
"v_accvgpr_write_b32 a58,v170\n"
"v_accvgpr_write_b32 a59,v171\n"
RD"a[88:91],v226 offset:6144\n"
"s_cmp_eq_u32 s34,0\n"
"s_cbranch_scc1 LFA_%=\n"
WT"lgkmcnt(6)\n"
MF"v[0:3],a[64:67],a[0:3],0\n"
WT"lgkmcnt(3)\n"
PK"v192,v192,v193\n"
PK"v193,v194,v195\n"
MF"v[0:3],a[68:71],a[4:7],v[0:3]\n"
"s_mov_b32 m0,s40\n"
"v_readlane_b32 s43,v228,s42\n"
PK"v194,v196,v197\n"
MF"v[8:11],a[68:71],a[20:23],0\n"
PK"v195,v198,v199\n"
"v_readlane_b32 s44,v229,s42\n"
MF"v[8:11],a[64:67],a[16:19],v[8:11]\n"
SA"s42,s42,1\n"
BL"v225,s[8:11],s43 offen offset:0 sc1 lds\n"
MF"v[16:19],a[64:67],a[32:35],0\n"
"v_accvgpr_write_b32 a44,v192\n"
"v_accvgpr_write_b32 a45,v193\n"
MF"v[16:19],a[68:71],a[36:39],v[16:19]\n"
RD"a[92:95],v226 offset:7168\n"
"v_accvgpr_write_b32 a46,v194\n"
MF"v[24:27],a[68:71],a[52:55],0\n"
"v_accvgpr_write_b32 a47,v195\n"
PK"v200,v200,v201\n"
MF"v[24:27],a[64:67],a[48:51],v[24:27]\n"
PK"v201,v202,v203\n"
PK"v202,v204,v205\n"
"v_mov_b32 v227,v226\n"
MF"v[0:3],a[72:75],a[8:11],v[0:3]\n"
BL"v225,s[8:11],s43 offen offset:1024 sc1 lds\n"
PK"v203,v206,v207\n"
MF"v[0:3],a[76:79],a[12:15],v[0:3]\n"
"v_accvgpr_write_b32 a60,v200\n"
"v_accvgpr_write_b32 a61,v201\n"
MF"v[8:11],a[76:79],a[28:31],v[8:11]\n"
"s_mul_i32 s48,s32,655360\n"
"v_accvgpr_write_b32 a62,v202\n"
"v_accvgpr_write_b32 a63,v203\n"
MF"v[8:11],a[72:75],a[24:27],v[8:11]\n"
SA"s12,s74,s48\n"
"s_addc_u32 s13,s75,0\n"
MF"v[16:19],a[72:75],a[40:43],v[16:19]\n"
"s_and_b32 s13,s13,0xffff\n"
MF"v[16:19],a[76:79],a[44:47],v[16:19]\n"
"s_mov_b32 s14,655360\n"
RD"a[96:99],v226 offset:8192\n"
MF"v[24:27],a[76:79],a[60:63],v[24:27]\n"
"s_lshl_b32 s48,s33,3\n"
BL"v225,s[8:11],s43 offen offset:2048 sc1 lds\n"
MF"v[24:27],a[72:75],a[56:59],v[24:27]\n"
WT"lgkmcnt(1)\n"
EX"v0,v0\n"
MF"v[4:7],a[80:83],a[0:3],0\n"
"s_and_b32 s49,s34,2\n"
EX"v1,v1\n"
MF"v[4:7],a[84:87],a[4:7],v[4:7]\n"
"s_lshl_b32 s49,s49,1\n"
EX"v2,v2\n"
MF"v[12:15],a[84:87],a[20:23],0\n"
SA"s48,s48,s49\n"
RD"a[100:103],v226 offset:9216\n"
PK"v0,v0,v1\n"
MF"v[12:15],a[80:83],a[16:19],v[12:15]\n"
SA"s48,s48,s30\n"
EX"v3,v3\n"
MF"v[20:23],a[80:83],a[32:35],0\n"
"s_mul_i32 s48,s48,5120\n"
EX"v8,v8\n"
MF"v[20:23],a[84:87],a[36:39],v[20:23]\n"
"s_or_b32 s49,s34,2\n"
BL"v225,s[8:11],s43 offen offset:3072 sc1 lds\n"
MF"v[28:31],a[84:87],a[52:55],0\n"
"s_cmp_eq_u32 s49,7\n"
EX"v9,v9\n"
PK"v1,v2,v3\n"
MF"v[28:31],a[80:83],a[48:51],v[28:31]\n"
"s_cselect_b32 s55,s48,2130706432\n"
RD"a[104:107],v226 offset:10240\n"
PK"v8,v8,v9\n"
MF"v[4:7],a[88:91],a[8:11],v[4:7]\n"
SA"m0,s40,16384\n"
EX"v10,v10\n"
MF"v[4:7],a[92:95],a[12:15],v[4:7]\n"
EX"v11,v11\n"
MF"v[12:15],a[92:95],a[28:31],v[12:15]\n"
EX"v16,v16\n"
MF"v[12:15],a[88:91],a[24:27],v[12:15]\n"
SA"s40,s40,32768\n"
RD"a[108:111],v226 offset:11264\n"
PK"v9,v10,v11\n"
MF"v[20:23],a[88:91],a[40:43],v[20:23]\n"
"s_cmp_eq_u32 s40,s46\n"
BL"v225,s[8:11],s44 offen offset:0 sc1 lds\n"
EX"v17,v17\n"
MF"v[20:23],a[92:95],a[44:47],v[20:23]\n"
"s_cselect_b32 s40,s47,s40\n"
EX"v18,v18\n"
MF"v[28:31],a[92:95],a[60:63],v[28:31]\n"
EX"v19,v19\n"
PK"v16,v16,v17\n"
MF"v[28:31],a[88:91],a[56:59],v[28:31]\n"
WT"lgkmcnt(1)\n"
RD"a[112:115],v226 offset:12288\n"
PK"v17,v18,v19\n"
MF"v[32:35],a[96:99],a[0:3],0\n"
EX"v24,v24\n"
MF"v[32:35],a[100:103],a[4:7],v[32:35]\n"
EX"v25,v25\n"
MF"v[40:43],a[100:103],a[20:23],0\n"
BL"v225,s[8:11],s44 offen offset:1024 sc1 lds\n"
EX"v26,v26\n"
MF"v[40:43],a[96:99],a[16:19],v[40:43]\n"
RD"a[116:119],v226 offset:13312\n"
PK"v24,v24,v25\n"
MF"v[48:51],a[96:99],a[32:35],0\n"
EX"v27,v27\n"
MF"v[48:51],a[100:103],a[36:39],v[48:51]\n"
EX"v4,v4\n"
MF"v[56:59],a[100:103],a[52:55],0\n"
EX"v5,v5\n"
PK"v25,v26,v27\n"
MF"v[56:59],a[96:99],a[48:51],v[56:59]\n"
RD"a[120:123],v226 offset:14336\n"
PK"v2,v4,v5\n"
MF"v[32:35],a[104:107],a[8:11],v[32:35]\n"
WT"lgkmcnt(2)\n"
BL"v225,s[8:11],s44 offen offset:2048 sc1 lds\n"
EX"v6,v6\n"
MF"v[32:35],a[108:111],a[12:15],v[32:35]\n"
EX"v7,v7\n"
MF"v[40:43],a[108:111],a[28:31],v[40:43]\n"
EX"v12,v12\n"
MF"v[40:43],a[104:107],a[24:27],v[40:43]\n"
RD"a[124:127],v226 offset:15360\n"
"v_readlane_b32 s43,v228,s42\n"
PK"v3,v6,v7\n"
MF"v[48:51],a[104:107],a[40:43],v[48:51]\n"
EX"v13,v13\n"
MF"v[48:51],a[108:111],a[44:47],v[48:51]\n"
EX"v14,v14\n"
MF"v[56:59],a[108:111],a[60:63],v[56:59]\n"
BL"v225,s[8:11],s44 offen offset:3072 sc1 lds\n"
EX"v15,v15\n"
MF"v[56:59],a[104:107],a[56:59],v[56:59]\n"
"s_mov_b32 m0,s40\n"
RD"a[96:99],v227 offset:24576\n"
PK"v10,v12,v13\n"
MF"v[36:39],a[112:115],a[0:3],0\n"
WT"lgkmcnt(2)\n"
EX"v20,v20\n"
PK"v11,v14,v15\n"
MF"v[36:39],a[116:119],a[4:7],v[36:39]\n"
EX"v21,v21\n"
MF"v[44:47],a[116:119],a[20:23],0\n"
EX"v22,v22\n"
MF"v[44:47],a[112:115],a[16:19],v[44:47]\n"
RD"a[100:103],v227 offset:25600\n"
"v_readlane_b32 s44,v229,s42\n"
PK"v18,v20,v21\n"
MF"v[52:55],a[112:115],a[32:35],0\n"
SA"s42,s42,1\n"
BL"v225,s[8:11],s43 offen offset:0 sc1 lds\n"
MF"v[52:55],a[116:119],a[36:39],v[52:55]\n"
EX"v23,v23\n"
MF"v[60:63],a[116:119],a[52:55],0\n"
EX"v28,v28\n"
MF"v[60:63],a[112:115],a[48:51],v[60:63]\n"
RD"a[104:107],v227 offset:26624\n"
PK"v19,v22,v23\n"
MF"v[36:39],a[120:123],a[8:11],v[36:39]\n"
WT"lgkmcnt(2)\n"
EX"v29,v29\n"
MF"v[36:39],a[124:127],a[12:15],v[36:39]\n"
BL"v225,s[8:11],s43 offen offset:1024 sc1 lds\n"
MF"v[44:47],a[124:127],a[28:31],v[44:47]\n"
EX"v30,v30\n"
PK"v26,v28,v29\n"
MF"v[44:47],a[120:123],a[24:27],v[44:47]\n"
RD"a[108:111],v227 offset:27648\n"
MF"v[52:55],a[120:123],a[40:43],v[52:55]\n"
EX"v31,v31\n"
MF"v[52:55],a[124:127],a[44:47],v[52:55]\n"
EX"v32,v32\n"
MF"v[60:63],a[124:127],a[60:63],v[60:63]\n"
EX"v33,v33\n"
PK"v27,v30,v31\n"
MF"v[60:63],a[120:123],a[56:59],v[60:63]\n"
SA"s41,s41,32768\n"
BL"v225,s[8:11],s43 offen offset:2048 sc1 lds\n"
RD"a[112:115],v227 offset:28672\n"
MF"v[80:83],a[96:99],v[0:3],0\n"
"s_cmp_eq_u32 s41,163840\n"
EX"v34,v34\n"
MF"v[112:115],a[96:99],v[8:11],0\n"
"s_cselect_b32 s41,0,s41\n"
EX"v35,v35\n"
MF"v[144:147],a[96:99],v[16:19],0\n"
EX"v40,v40\n"
"v_add_u32 v226,s41,v224\n"
MF"v[176:179],a[96:99],v[24:27],0\n"
WT"lgkmcnt(1)\n"
RD"a[116:119],v227 offset:29696\n"
MF"v[84:87],a[100:103],v[0:3],0\n"
EX"v41,v41\n"
MF"v[116:119],a[100:103],v[8:11],0\n"
BL"v225,s[8:11],s43 offen offset:3072 sc1 lds\n"
MF"v[148:151],a[100:103],v[16:19],0\n"
SA"m0,s40,16384\n"
EX"v42,v42\n"
MF"v[180:183],a[100:103],v[24:27],0\n"
RD"a[120:123],v227 offset:30720\n"
MF"v[88:91],a[104:107],v[0:3],0\n"
EX"v43,v43\n"
MF"v[120:123],a[104:107],v[8:11],0\n"
EX"v48,v48\n"
MF"v[152:155],a[104:107],v[16:19],0\n"
SA"s40,s40,32768\n"
EX"v49,v49\n"
MF"v[184:187],a[104:107],v[24:27],0\n"
"s_cmp_eq_u32 s40,s46\n"
BL"v225,s[8:11],s44 offen offset:0 sc1 lds\n"
RD"a[124:127],v227 offset:31744\n"
MF"v[92:95],a[108:111],v[0:3],0\n"
"s_cselect_b32 s40,s47,s40\n"
EX"v50,v50\n"
MF"v[124:127],a[108:111],v[8:11],0\n"
EX"v51,v51\n"
MF"v[156:159],a[108:111],v[16:19],0\n"
EX"v56,v56\n"
MF"v[188:191],a[108:111],v[24:27],0\n"
WT"lgkmcnt(1)\n"
EX"v57,v57\n"
MF"v[96:99],a[112:115],v[0:3],0\n"
EX"v58,v58\n"
MF"v[128:131],a[112:115],v[8:11],0\n"
BL"v225,s[8:11],s44 offen offset:1024 sc1 lds\n"
MF"v[160:163],a[112:115],v[16:19],0\n"
EX"v59,v59\n"
MF"v[192:195],a[112:115],v[24:27],0\n"
EX"v36,v36\n"
MF"v[100:103],a[116:119],v[0:3],0\n"
WT"vmcnt(16) lgkmcnt(0)\n"
"s_barrier\n"
MF"v[132:135],a[116:119],v[8:11],0\n"
RD"a[64:67],v226 offset:0\n"
RD"a[68:71],v226 offset:1024\n"
"buffer_store_dwordx2 v[232:233],v230,s[8:11],s98 offen sc1\n"
MF"v[164:167],a[116:119],v[16:19],0\n"
EX"v37,v37\n"
MF"v[196:199],a[116:119],v[24:27],0\n"
BL"v225,s[8:11],s44 offen offset:2048 sc1 lds\n"
MF"v[104:107],a[120:123],v[0:3],0\n"
EX"v38,v38\n"
MF"v[136:139],a[120:123],v[8:11],0\n"
EX"v39,v39\n"
MF"v[168:171],a[120:123],v[16:19],0\n"
EX"v44,v44\n"
MF"v[200:203],a[120:123],v[24:27],0\n"
RD"a[72:75],v226 offset:2048\n"
RD"a[76:79],v226 offset:3072\n"
MF"v[108:111],a[124:127],v[0:3],0\n"
EX"v45,v45\n"
MF"v[140:143],a[124:127],v[8:11],0\n"
BL"v225,s[8:11],s44 offen offset:3072 sc1 lds\n"
MF"v[172:175],a[124:127],v[16:19],0\n"
EX"v46,v46\n"
MF"v[204:207],a[124:127],v[24:27],0\n"
RD"a[80:83],v226 offset:4096\n"
MF"a[132:135],a[128:131],v[0:3],0\n"
EX"v47,v47\n"
MF"a[136:139],a[128:131],v[8:11],0\n"
"buffer_load_dword v255,v253,s[12:15],s55 offen\n"
EX"v52,v52\n"
MF"a[140:143],a[128:131],v[16:19],0\n"
SA"s55,s55,163840\n"
EX"v53,v53\n"
MF"a[144:147],a[128:131],v[24:27],0\n"
RD"a[84:87],v226 offset:5120\n"
RD"a[88:91],v226 offset:6144\n"
EX"v54,v54\n"
EX"v55,v55\n"
EX"v60,v60\n"
EX"v61,v61\n"
EX"v62,v62\n"
EX"v63,v63\n"
"s_cmp_eq_u32 s57,1\n"
"s_cbranch_scc1 LEV_%=\n"
".p2align 6\n"
"LP1_%=:\n"
WT"lgkmcnt(6)\n"
MF"v[0:3],a[64:67],a[0:3],0\n"
WT"lgkmcnt(5)\n"
"v_readlane_b32 s43,v228,s42\n"
PK"v64,v32,v33\n"
MF"v[0:3],a[68:71],a[4:7],v[0:3]\n"
"s_mov_b32 m0,s40\n"
"v_readlane_b32 s44,v229,s42\n"
PK"v65,v34,v35\n"
MF"v[8:11],a[68:71],a[20:23],0\n"
PK"v66,v36,v37\n"
PK"v67,v38,v39\n"
MF"v[8:11],a[64:67],a[16:19],v[8:11]\n"
SA"s42,s42,1\n"
BL"v225,s[8:11],s43 offen offset:0 sc1 lds\n"
MF"v[16:19],a[64:67],a[32:35],0\n"
PK"v68,v40,v41\n"
PK"v69,v42,v43\n"
MF"v[16:19],a[68:71],a[36:39],v[16:19]\n"
RD"a[92:95],v226 offset:7168\n"
PK"v70,v44,v45\n"
MF"v[24:27],a[68:71],a[52:55],0\n"
PK"v71,v46,v47\n"
PK"v72,v48,v49\n"
MF"v[24:27],a[64:67],a[48:51],v[24:27]\n"
WT"lgkmcnt(1)\n"
BL"v225,s[8:11],s43 offen offset:1024 sc1 lds\n"
MF"v[0:3],a[72:75],a[8:11],v[0:3]\n"
PK"v73,v50,v51\n"
"v_mov_b32 v227,v226\n"
MF"v[0:3],a[76:79],a[12:15],v[0:3]\n"
PK"v74,v52,v53\n"
PK"v75,v54,v55\n"
MF"v[8:11],a[76:79],a[28:31],v[8:11]\n"
PK"v76,v56,v57\n"
PK"v77,v58,v59\n"
MF"v[8:11],a[72:75],a[24:27],v[8:11]\n"
PK"v78,v60,v61\n"
PK"v79,v62,v63\n"
MF"v[16:19],a[72:75],a[40:43],v[16:19]\n"
MF"v[16:19],a[76:79],a[44:47],v[16:19]\n"
RD"a[96:99],v226 offset:8192\n"
MF"v[24:27],a[76:79],a[60:63],v[24:27]\n"
EX"v0,v0\n"
MF"v[24:27],a[72:75],a[56:59],v[24:27]\n"
EX"v1,v1\n"
MF"v[4:7],a[80:83],a[0:3],0\n"
EX"v2,v2\n"
MF"v[4:7],a[84:87],a[4:7],v[4:7]\n"
RD"a[100:103],v226 offset:9216\n"
PK"v0,v0,v1\n"
MF"v[12:15],a[84:87],a[20:23],0\n"
EX"v3,v3\n"
MF"v[12:15],a[80:83],a[16:19],v[12:15]\n"
EX"v8,v8\n"
MF"v[20:23],a[80:83],a[32:35],0\n"
EX"v9,v9\n"
MF"v[20:23],a[84:87],a[36:39],v[20:23]\n"
RD"a[104:107],v226 offset:10240\n"
PK"v1,v2,v3\n"
MF"v[28:31],a[84:87],a[52:55],0\n"
EX"v10,v10\n"
MF"v[28:31],a[80:83],a[48:51],v[28:31]\n"
EX"v11,v11\n"
MF"v[4:7],a[88:91],a[8:11],v[4:7]\n"
WT"lgkmcnt(1)\n"
EX"v16,v16\n"
MF"v[4:7],a[92:95],a[12:15],v[4:7]\n"
RD"a[108:111],v226 offset:11264\n"
PK"v8,v8,v9\n"
MF"v[12:15],a[92:95],a[28:31],v[12:15]\n"
EX"v17,v17\n"
MF"v[12:15],a[88:91],a[24:27],v[12:15]\n"
EX"v18,v18\n"
MF"v[20:23],a[88:91],a[40:43],v[20:23]\n"
EX"v19,v19\n"
MF"v[20:23],a[92:95],a[44:47],v[20:23]\n"
RD"a[112:115],v226 offset:12288\n"
PK"v9,v10,v11\n"
MF"v[28:31],a[92:95],a[60:63],v[28:31]\n"
EX"v24,v24\n"
MF"v[28:31],a[88:91],a[56:59],v[28:31]\n"
EX"v25,v25\n"
MF"v[32:35],a[96:99],a[0:3],0\n"
EX"v26,v26\n"
MF"v[32:35],a[100:103],a[4:7],v[32:35]\n"
RD"a[116:119],v226 offset:13312\n"
PK"v16,v16,v17\n"
MF"v[40:43],a[100:103],a[20:23],0\n"
EX"v27,v27\n"
MF"v[40:43],a[96:99],a[16:19],v[40:43]\n"
EX"v4,v4\n"
MF"v[48:51],a[96:99],a[32:35],0\n"
EX"v5,v5\n"
MF"v[48:51],a[100:103],a[36:39],v[48:51]\n"
RD"a[120:123],v226 offset:14336\n"
PK"v17,v18,v19\n"
MF"v[56:59],a[100:103],a[52:55],0\n"
EX"v6,v6\n"
MF"v[56:59],a[96:99],a[48:51],v[56:59]\n"
WT"lgkmcnt(2)\n"
EX"v7,v7\n"
MF"v[32:35],a[104:107],a[8:11],v[32:35]\n"
EX"v12,v12\n"
MF"v[32:35],a[108:111],a[12:15],v[32:35]\n"
RD"a[124:127],v226 offset:15360\n"
PK"v24,v24,v25\n"
MF"v[40:43],a[108:111],a[28:31],v[40:43]\n"
EX"v13,v13\n"
MF"v[40:43],a[104:107],a[24:27],v[40:43]\n"
EX"v14,v14\n"
MF"v[48:51],a[104:107],a[40:43],v[48:51]\n"
EX"v15,v15\n"
MF"v[48:51],a[108:111],a[44:47],v[48:51]\n"
RD"a[64:67],v227 offset:16384\n"
PK"v25,v26,v27\n"
MF"v[56:59],a[108:111],a[60:63],v[56:59]\n"
EX"v20,v20\n"
MF"v[56:59],a[104:107],a[56:59],v[56:59]\n"
EX"v21,v21\n"
MF"v[36:39],a[112:115],a[0:3],0\n"
WT"lgkmcnt(1)\n"
EX"v22,v22\n"
MF"v[36:39],a[116:119],a[4:7],v[36:39]\n"
RD"a[68:71],v227 offset:24576\n"
PK"v2,v4,v5\n"
MF"v[44:47],a[116:119],a[20:23],0\n"
EX"v23,v23\n"
MF"v[44:47],a[112:115],a[16:19],v[44:47]\n"
EX"v28,v28\n"
MF"v[52:55],a[112:115],a[32:35],0\n"
EX"v29,v29\n"
MF"v[52:55],a[116:119],a[36:39],v[52:55]\n"
RD"a[72:75],v227 offset:17408\n"
PK"v3,v6,v7\n"
MF"v[60:63],a[116:119],a[52:55],0\n"
EX"v30,v30\n"
MF"v[60:63],a[112:115],a[48:51],v[60:63]\n"
EX"v31,v31\n"
MF"v[36:39],a[120:123],a[8:11],v[36:39]\n"
PK"v10,v12,v13\n"
PK"v11,v14,v15\n"
MF"v[36:39],a[124:127],a[12:15],v[36:39]\n"
RD"a[76:79],v227 offset:25600\n"
PK"v18,v20,v21\n"
MF"v[44:47],a[124:127],a[28:31],v[44:47]\n"
PK"v19,v22,v23\n"
PK"v26,v28,v29\n"
MF"v[44:47],a[120:123],a[24:27],v[44:47]\n"
PK"v27,v30,v31\n"
MF"v[52:55],a[120:123],a[40:43],v[52:55]\n"
MF"v[52:55],a[124:127],a[44:47],v[52:55]\n"
RD"a[80:83],v227 offset:18432\n"
MF"v[60:63],a[124:127],a[60:63],v[60:63]\n"
EX"v32,v32\n"
MF"v[60:63],a[120:123],a[56:59],v[60:63]\n"
WT"lgkmcnt(2)\n"
SA"s41,s41,32768\n"
"s_cmp_eq_u32 s41,163840\n"
MF"v[80:83],a[64:67],v[64:67],v[80:83]\n"
"s_cselect_b32 s41,0,s41\n"
EX"v33,v33\n"
MF"v[80:83],a[68:71],v[0:3],v[80:83]\n"
RD"a[84:87],v227 offset:26624\n"
"v_add_u32 v226,s41,v224\n"
MF"v[112:115],a[68:71],v[8:11],v[112:115]\n"
EX"v34,v34\n"
MF"v[112:115],a[64:67],v[68:71],v[112:115]\n"
MF"v[144:147],a[64:67],v[72:75],v[144:147]\n"
EX"v35,v35\n"
MF"v[144:147],a[68:71],v[16:19],v[144:147]\n"
RD"a[88:91],v227 offset:19456\n"
MF"v[176:179],a[68:71],v[24:27],v[176:179]\n"
EX"v40,v40\n"
MF"v[176:179],a[64:67],v[76:79],v[176:179]\n"
BL"v225,s[8:11],s43 offen offset:2048 sc1 lds\n"
MF"v[84:87],a[72:75],v[64:67],v[84:87]\n"
WT"lgkmcnt(1)\n"
EX"v41,v41\n"
MF"v[84:87],a[76:79],v[0:3],v[84:87]\n"
RD"a[92:95],v227 offset:27648\n"
MF"v[116:119],a[76:79],v[8:11],v[116:119]\n"
EX"v42,v42\n"
MF"v[116:119],a[72:75],v[68:71],v[116:119]\n"
MF"v[148:151],a[72:75],v[72:75],v[148:151]\n"
EX"v43,v43\n"
MF"v[148:151],a[76:79],v[16:19],v[148:151]\n"
RD"a[96:99],v227 offset:20480\n"
MF"v[180:183],a[76:79],v[24:27],v[180:183]\n"
EX"v48,v48\n"
MF"v[180:183],a[72:75],v[76:79],v[180:183]\n"
BL"v225,s[8:11],s43 offen offset:3072 sc1 lds\n"
MF"v[88:91],a[80:83],v[64:67],v[88:91]\n"
EX"v49,v49\n"
MF"v[88:91],a[84:87],v[0:3],v[88:91]\n"
RD"a[100:103],v227 offset:28672\n"
MF"v[120:123],a[84:87],v[8:11],v[120:123]\n"
EX"v50,v50\n"
MF"v[120:123],a[80:83],v[68:71],v[120:123]\n"
SA"m0,s40,16384\n"
MF"v[152:155],a[80:83],v[72:75],v[152:155]\n"
EX"v51,v51\n"
MF"v[152:155],a[84:87],v[16:19],v[152:155]\n"
RD"a[104:107],v227 offset:21504\n"
MF"v[184:187],a[84:87],v[24:27],v[184:187]\n"
EX"v56,v56\n"
MF"v[184:187],a[80:83],v[76:79],v[184:187]\n"
WT"lgkmcnt(2)\n"
BL"v225,s[8:11],s44 offen offset:0 sc1 lds\n"
MF"v[92:95],a[88:91],v[64:67],v[92:95]\n"
SA"s40,s40,32768\n"
EX"v57,v57\n"
MF"v[92:95],a[92:95],v[0:3],v[92:95]\n"
"s_cmp_eq_u32 s40,s46\n"
RD"a[108:111],v227 offset:29696\n"
"s_cselect_b32 s40,s47,s40\n"
MF"v[124:127],a[92:95],v[8:11],v[124:127]\n"
EX"v58,v58\n"
MF"v[124:127],a[88:91],v[68:71],v[124:127]\n"
MF"v[156:159],a[88:91],v[72:75],v[156:159]\n"
EX"v59,v59\n"
MF"v[156:159],a[92:95],v[16:19],v[156:159]\n"
RD"a[112:115],v227 offset:22528\n"
MF"v[188:191],a[92:95],v[24:27],v[188:191]\n"
EX"v36,v36\n"
MF"v[188:191],a[88:91],v[76:79],v[188:191]\n"
BL"v225,s[8:11],s44 offen offset:1024 sc1 lds\n"
MF"v[96:99],a[96:99],v[64:67],v[96:99]\n"
WT"lgkmcnt(1)\n"
EX"v37,v37\n"
MF"v[96:99],a[100:103],v[0:3],v[96:99]\n"
RD"a[116:119],v227 offset:30720\n"
MF"v[128:131],a[100:103],v[8:11],v[128:131]\n"
EX"v38,v38\n"
MF"v[128:131],a[96:99],v[68:71],v[128:131]\n"
MF"v[160:163],a[96:99],v[72:75],v[160:163]\n"
EX"v39,v39\n"
MF"v[160:163],a[100:103],v[16:19],v[160:163]\n"
RD"a[120:123],v227 offset:23552\n"
MF"v[192:195],a[100:103],v[24:27],v[192:195]\n"
EX"v44,v44\n"
MF"v[192:195],a[96:99],v[76:79],v[192:195]\n"
BL"v225,s[8:11],s44 offen offset:2048 sc1 lds\n"
MF"v[100:103],a[104:107],v[64:67],v[100:103]\n"
EX"v45,v45\n"
MF"v[100:103],a[108:111],v[0:3],v[100:103]\n"
RD"a[124:127],v227 offset:31744\n"
MF"v[132:135],a[108:111],v[8:11],v[132:135]\n"
EX"v46,v46\n"
MF"v[132:135],a[104:107],v[68:71],v[132:135]\n"
MF"v[164:167],a[104:107],v[72:75],v[164:167]\n"
EX"v47,v47\n"
MF"v[164:167],a[108:111],v[16:19],v[164:167]\n"
MF"v[196:199],a[108:111],v[24:27],v[196:199]\n"
EX"v52,v52\n"
MF"v[196:199],a[104:107],v[76:79],v[196:199]\n"
WT"lgkmcnt(1)\n"
BL"v225,s[8:11],s44 offen offset:3072 sc1 lds\n"
MF"v[104:107],a[112:115],v[64:67],v[104:107]\n"
EX"v53,v53\n"
MF"v[104:107],a[116:119],v[0:3],v[104:107]\n"
"buffer_load_dword v255,v253,s[12:15],s55 offen\n"
MF"v[136:139],a[116:119],v[8:11],v[136:139]\n"
EX"v54,v54\n"
MF"v[136:139],a[112:115],v[68:71],v[136:139]\n"
SA"s55,s55,163840\n"
MF"v[168:171],a[112:115],v[72:75],v[168:171]\n"
WT"vmcnt(17) lgkmcnt(0)\n"
"s_barrier\n"
MF"v[168:171],a[116:119],v[16:19],v[168:171]\n"
RD"a[64:67],v226 offset:0\n"
MF"v[200:203],a[116:119],v[24:27],v[200:203]\n"
EX"v55,v55\n"
MF"v[200:203],a[112:115],v[76:79],v[200:203]\n"
MF"v[108:111],a[120:123],v[64:67],v[108:111]\n"
EX"v60,v60\n"
MF"v[108:111],a[124:127],v[0:3],v[108:111]\n"
RD"a[68:71],v226 offset:1024\n"
MF"v[140:143],a[124:127],v[8:11],v[140:143]\n"
EX"v61,v61\n"
MF"v[140:143],a[120:123],v[68:71],v[140:143]\n"
RD"a[72:75],v226 offset:2048\n"
MF"v[172:175],a[120:123],v[72:75],v[172:175]\n"
EX"v62,v62\n"
MF"v[172:175],a[124:127],v[16:19],v[172:175]\n"
RD"a[76:79],v226 offset:3072\n"
MF"v[204:207],a[124:127],v[24:27],v[204:207]\n"
EX"v63,v63\n"
MF"v[204:207],a[120:123],v[76:79],v[204:207]\n"
MF"a[132:135],a[128:131],v[64:67],a[132:135]\n"
MF"a[132:135],a[128:131],v[0:3],a[132:135]\n"
RD"a[80:83],v226 offset:4096\n"
MF"a[136:139],a[128:131],v[8:11],a[136:139]\n"
MF"a[136:139],a[128:131],v[68:71],a[136:139]\n"
RD"a[84:87],v226 offset:5120\n"
MF"a[140:143],a[128:131],v[72:75],a[140:143]\n"
MF"a[140:143],a[128:131],v[16:19],a[140:143]\n"
RD"a[88:91],v226 offset:6144\n"
MF"a[144:147],a[128:131],v[24:27],a[144:147]\n"
MF"a[144:147],a[128:131],v[76:79],a[144:147]\n"
"s_sub_u32 s56,s56,1\n"
"s_cbranch_scc0 LP1_%=\n"
"s_cmp_eq_u32 s57,2\n"
"s_cbranch_scc1 LEV_%=\n"
"s_cmp_eq_u32 s57,3\n"
"s_cbranch_scc1 LRT_%=\n"
"LDA_%=:\n"
WT"lgkmcnt(6)\n"
MF"v[0:3],a[64:67],a[0:3],v[208:211]\n"
WT"lgkmcnt(5)\n"
"v_readlane_b32 s43,v228,s42\n"
PK"v64,v32,v33\n"
MF"v[0:3],a[68:71],a[4:7],v[0:3]\n"
"s_mov_b32 m0,s40\n"
"v_readlane_b32 s44,v229,s42\n"
PK"v65,v34,v35\n"
MF"v[8:11],a[68:71],a[20:23],0\n"
PK"v66,v36,v37\n"
PK"v67,v38,v39\n"
MF"v[8:11],a[64:67],a[16:19],v[8:11]\n"
SA"s42,s42,1\n"
BL"v225,s[8:11],s43 offen offset:0 sc1 lds\n"
MF"v[16:19],a[64:67],a[32:35],0\n"
PK"v68,v40,v41\n"
PK"v69,v42,v43\n"
MF"v[16:19],a[68:71],a[36:39],v[16:19]\n"
RD"a[92:95],v226 offset:7168\n"
PK"v70,v44,v45\n"
MF"v[24:27],a[68:71],a[52:55],0\n"
PK"v71,v46,v47\n"
PK"v72,v48,v49\n"
MF"v[24:27],a[64:67],a[48:51],v[24:27]\n"
WT"lgkmcnt(1)\n"
BL"v225,s[8:11],s43 offen offset:1024 sc1 lds\n"
MF"v[0:3],a[72:75],a[8:11],v[0:3]\n"
PK"v73,v50,v51\n"
"v_mov_b32 v227,v226\n"
MF"v[0:3],a[76:79],a[12:15],v[0:3]\n"
PK"v74,v52,v53\n"
PK"v75,v54,v55\n"
MF"v[8:11],a[76:79],a[28:31],v[8:11]\n"
PK"v76,v56,v57\n"
PK"v77,v58,v59\n"
MF"v[8:11],a[72:75],a[24:27],v[8:11]\n"
PK"v78,v60,v61\n"
PK"v79,v62,v63\n"
MF"v[16:19],a[72:75],a[40:43],v[16:19]\n"
MF"v[16:19],a[76:79],a[44:47],v[16:19]\n"
RD"a[96:99],v226 offset:8192\n"
MF"v[24:27],a[76:79],a[60:63],v[24:27]\n"
EX"v0,v0\n"
MF"v[24:27],a[72:75],a[56:59],v[24:27]\n"
EX"v1,v1\n"
MF"v[4:7],a[80:83],a[0:3],v[212:215]\n"
EX"v2,v2\n"
MF"v[4:7],a[84:87],a[4:7],v[4:7]\n"
RD"a[100:103],v226 offset:9216\n"
PK"v0,v0,v1\n"
MF"v[12:15],a[84:87],a[20:23],0\n"
EX"v3,v3\n"
MF"v[12:15],a[80:83],a[16:19],v[12:15]\n"
EX"v8,v8\n"
MF"v[20:23],a[80:83],a[32:35],0\n"
EX"v9,v9\n"
MF"v[20:23],a[84:87],a[36:39],v[20:23]\n"
RD"a[104:107],v226 offset:10240\n"
PK"v1,v2,v3\n"
MF"v[28:31],a[84:87],a[52:55],0\n"
EX"v10,v10\n"
MF"v[28:31],a[80:83],a[48:51],v[28:31]\n"
EX"v11,v11\n"
MF"v[4:7],a[88:91],a[8:11],v[4:7]\n"
WT"lgkmcnt(1)\n"
EX"v16,v16\n"
MF"v[4:7],a[92:95],a[12:15],v[4:7]\n"
RD"a[108:111],v226 offset:11264\n"
PK"v8,v8,v9\n"
MF"v[12:15],a[92:95],a[28:31],v[12:15]\n"
EX"v17,v17\n"
MF"v[12:15],a[88:91],a[24:27],v[12:15]\n"
EX"v18,v18\n"
MF"v[20:23],a[88:91],a[40:43],v[20:23]\n"
EX"v19,v19\n"
MF"v[20:23],a[92:95],a[44:47],v[20:23]\n"
RD"a[112:115],v226 offset:12288\n"
PK"v9,v10,v11\n"
MF"v[28:31],a[92:95],a[60:63],v[28:31]\n"
EX"v24,v24\n"
MF"v[28:31],a[88:91],a[56:59],v[28:31]\n"
EX"v25,v25\n"
MF"v[32:35],a[96:99],a[0:3],v[216:219]\n"
EX"v26,v26\n"
MF"v[32:35],a[100:103],a[4:7],v[32:35]\n"
RD"a[116:119],v226 offset:13312\n"
PK"v16,v16,v17\n"
MF"v[40:43],a[100:103],a[20:23],0\n"
EX"v27,v27\n"
MF"v[40:43],a[96:99],a[16:19],v[40:43]\n"
EX"v4,v4\n"
MF"v[48:51],a[96:99],a[32:35],0\n"
EX"v5,v5\n"
MF"v[48:51],a[100:103],a[36:39],v[48:51]\n"
RD"a[120:123],v226 offset:14336\n"
PK"v17,v18,v19\n"
MF"v[56:59],a[100:103],a[52:55],0\n"
EX"v6,v6\n"
MF"v[56:59],a[96:99],a[48:51],v[56:59]\n"
WT"lgkmcnt(2)\n"
EX"v7,v7\n"
MF"v[32:35],a[104:107],a[8:11],v[32:35]\n"
EX"v12,v12\n"
MF"v[32:35],a[108:111],a[12:15],v[32:35]\n"
RD"a[124:127],v226 offset:15360\n"
PK"v24,v24,v25\n"
MF"v[40:43],a[108:111],a[28:31],v[40:43]\n"
EX"v13,v13\n"
MF"v[40:43],a[104:107],a[24:27],v[40:43]\n"
EX"v14,v14\n"
MF"v[48:51],a[104:107],a[40:43],v[48:51]\n"
EX"v15,v15\n"
MF"v[48:51],a[108:111],a[44:47],v[48:51]\n"
RD"a[64:67],v227 offset:16384\n"
PK"v25,v26,v27\n"
MF"v[56:59],a[108:111],a[60:63],v[56:59]\n"
EX"v20,v20\n"
MF"v[56:59],a[104:107],a[56:59],v[56:59]\n"
EX"v21,v21\n"
MF"v[36:39],a[112:115],a[0:3],v[220:223]\n"
WT"lgkmcnt(1)\n"
EX"v22,v22\n"
MF"v[36:39],a[116:119],a[4:7],v[36:39]\n"
RD"a[68:71],v227 offset:24576\n"
PK"v2,v4,v5\n"
MF"v[44:47],a[116:119],a[20:23],0\n"
EX"v23,v23\n"
MF"v[44:47],a[112:115],a[16:19],v[44:47]\n"
EX"v28,v28\n"
MF"v[52:55],a[112:115],a[32:35],0\n"
EX"v29,v29\n"
MF"v[52:55],a[116:119],a[36:39],v[52:55]\n"
RD"a[72:75],v227 offset:17408\n"
PK"v3,v6,v7\n"
MF"v[60:63],a[116:119],a[52:55],0\n"
EX"v30,v30\n"
MF"v[60:63],a[112:115],a[48:51],v[60:63]\n"
EX"v31,v31\n"
MF"v[36:39],a[120:123],a[8:11],v[36:39]\n"
PK"v10,v12,v13\n"
PK"v11,v14,v15\n"
MF"v[36:39],a[124:127],a[12:15],v[36:39]\n"
RD"a[76:79],v227 offset:25600\n"
PK"v18,v20,v21\n"
MF"v[44:47],a[124:127],a[28:31],v[44:47]\n"
PK"v19,v22,v23\n"
PK"v26,v28,v29\n"
MF"v[44:47],a[120:123],a[24:27],v[44:47]\n"
PK"v27,v30,v31\n"
MF"v[52:55],a[120:123],a[40:43],v[52:55]\n"
MF"v[52:55],a[124:127],a[44:47],v[52:55]\n"
RD"a[80:83],v227 offset:18432\n"
MF"v[60:63],a[124:127],a[60:63],v[60:63]\n"
EX"v32,v32\n"
MF"v[60:63],a[120:123],a[56:59],v[60:63]\n"
WT"lgkmcnt(2)\n"
SA"s41,s41,32768\n"
"s_cmp_eq_u32 s41,163840\n"
MF"v[80:83],a[64:67],v[64:67],v[80:83]\n"
"s_cselect_b32 s41,0,s41\n"
EX"v33,v33\n"
MF"v[80:83],a[68:71],v[0:3],v[80:83]\n"
RD"a[84:87],v227 offset:26624\n"
"v_add_u32 v226,s41,v224\n"
MF"v[112:115],a[68:71],v[8:11],v[112:115]\n"
EX"v34,v34\n"
MF"v[112:115],a[64:67],v[68:71],v[112:115]\n"
MF"v[144:147],a[64:67],v[72:75],v[144:147]\n"
EX"v35,v35\n"
MF"v[144:147],a[68:71],v[16:19],v[144:147]\n"
RD"a[88:91],v227 offset:19456\n"
MF"v[176:179],a[68:71],v[24:27],v[176:179]\n"
EX"v40,v40\n"
MF"v[176:179],a[64:67],v[76:79],v[176:179]\n"
BL"v225,s[8:11],s43 offen offset:2048 sc1 lds\n"
MF"v[84:87],a[72:75],v[64:67],v[84:87]\n"
WT"lgkmcnt(1)\n"
EX"v41,v41\n"
MF"v[84:87],a[76:79],v[0:3],v[84:87]\n"
RD"a[92:95],v227 offset:27648\n"
MF"v[116:119],a[76:79],v[8:11],v[116:119]\n"
EX"v42,v42\n"
MF"v[116:119],a[72:75],v[68:71],v[116:119]\n"
MF"v[148:151],a[72:75],v[72:75],v[148:151]\n"
EX"v43,v43\n"
MF"v[148:151],a[76:79],v[16:19],v[148:151]\n"
RD"a[96:99],v227 offset:20480\n"
MF"v[180:183],a[76:79],v[24:27],v[180:183]\n"
EX"v48,v48\n"
MF"v[180:183],a[72:75],v[76:79],v[180:183]\n"
BL"v225,s[8:11],s43 offen offset:3072 sc1 lds\n"
MF"v[88:91],a[80:83],v[64:67],v[88:91]\n"
EX"v49,v49\n"
MF"v[88:91],a[84:87],v[0:3],v[88:91]\n"
RD"a[100:103],v227 offset:28672\n"
MF"v[120:123],a[84:87],v[8:11],v[120:123]\n"
EX"v50,v50\n"
MF"v[120:123],a[80:83],v[68:71],v[120:123]\n"
SA"m0,s40,16384\n"
MF"v[152:155],a[80:83],v[72:75],v[152:155]\n"
EX"v51,v51\n"
MF"v[152:155],a[84:87],v[16:19],v[152:155]\n"
RD"a[104:107],v227 offset:21504\n"
MF"v[184:187],a[84:87],v[24:27],v[184:187]\n"
EX"v56,v56\n"
MF"v[184:187],a[80:83],v[76:79],v[184:187]\n"
WT"lgkmcnt(2)\n"
BL"v225,s[8:11],s44 offen offset:0 sc1 lds\n"
MF"v[92:95],a[88:91],v[64:67],v[92:95]\n"
SA"s40,s40,32768\n"
EX"v57,v57\n"
MF"v[92:95],a[92:95],v[0:3],v[92:95]\n"
"s_cmp_eq_u32 s40,s46\n"
RD"a[108:111],v227 offset:29696\n"
"s_cselect_b32 s40,s47,s40\n"
MF"v[124:127],a[92:95],v[8:11],v[124:127]\n"
EX"v58,v58\n"
MF"v[124:127],a[88:91],v[68:71],v[124:127]\n"
MF"v[156:159],a[88:91],v[72:75],v[156:159]\n"
EX"v59,v59\n"
MF"v[156:159],a[92:95],v[16:19],v[156:159]\n"
RD"a[112:115],v227 offset:22528\n"
MF"v[188:191],a[92:95],v[24:27],v[188:191]\n"
EX"v36,v36\n"
MF"v[188:191],a[88:91],v[76:79],v[188:191]\n"
BL"v225,s[8:11],s44 offen offset:1024 sc1 lds\n"
MF"v[96:99],a[96:99],v[64:67],v[96:99]\n"
WT"lgkmcnt(1)\n"
EX"v37,v37\n"
MF"v[96:99],a[100:103],v[0:3],v[96:99]\n"
RD"a[116:119],v227 offset:30720\n"
MF"v[128:131],a[100:103],v[8:11],v[128:131]\n"
EX"v38,v38\n"
MF"v[128:131],a[96:99],v[68:71],v[128:131]\n"
MF"v[160:163],a[96:99],v[72:75],v[160:163]\n"
EX"v39,v39\n"
MF"v[160:163],a[100:103],v[16:19],v[160:163]\n"
RD"a[120:123],v227 offset:23552\n"
MF"v[192:195],a[100:103],v[24:27],v[192:195]\n"
EX"v44,v44\n"
MF"v[192:195],a[96:99],v[76:79],v[192:195]\n"
BL"v225,s[8:11],s44 offen offset:2048 sc1 lds\n"
MF"v[100:103],a[104:107],v[64:67],v[100:103]\n"
EX"v45,v45\n"
MF"v[100:103],a[108:111],v[0:3],v[100:103]\n"
RD"a[124:127],v227 offset:31744\n"
MF"v[132:135],a[108:111],v[8:11],v[132:135]\n"
EX"v46,v46\n"
MF"v[132:135],a[104:107],v[68:71],v[132:135]\n"
MF"v[164:167],a[104:107],v[72:75],v[164:167]\n"
EX"v47,v47\n"
MF"v[164:167],a[108:111],v[16:19],v[164:167]\n"
MF"v[196:199],a[108:111],v[24:27],v[196:199]\n"
EX"v52,v52\n"
MF"v[196:199],a[104:107],v[76:79],v[196:199]\n"
WT"lgkmcnt(1)\n"
BL"v225,s[8:11],s44 offen offset:3072 sc1 lds\n"
MF"v[104:107],a[112:115],v[64:67],v[104:107]\n"
EX"v53,v53\n"
MF"v[104:107],a[116:119],v[0:3],v[104:107]\n"
MF"v[136:139],a[116:119],v[8:11],v[136:139]\n"
EX"v54,v54\n"
MF"v[136:139],a[112:115],v[68:71],v[136:139]\n"
MF"v[168:171],a[112:115],v[72:75],v[168:171]\n"
WT"vmcnt(16) lgkmcnt(0)\n"
"s_barrier\n"
MF"v[168:171],a[116:119],v[16:19],v[168:171]\n"
RD"a[64:67],v226 offset:0\n"
MF"v[200:203],a[116:119],v[24:27],v[200:203]\n"
EX"v55,v55\n"
MF"v[200:203],a[112:115],v[76:79],v[200:203]\n"
MF"v[108:111],a[120:123],v[64:67],v[108:111]\n"
EX"v60,v60\n"
MF"v[108:111],a[124:127],v[0:3],v[108:111]\n"
RD"a[68:71],v226 offset:1024\n"
MF"v[140:143],a[124:127],v[8:11],v[140:143]\n"
EX"v61,v61\n"
MF"v[140:143],a[120:123],v[68:71],v[140:143]\n"
RD"a[72:75],v226 offset:2048\n"
MF"v[172:175],a[120:123],v[72:75],v[172:175]\n"
EX"v62,v62\n"
MF"v[172:175],a[124:127],v[16:19],v[172:175]\n"
RD"a[76:79],v226 offset:3072\n"
MF"v[204:207],a[124:127],v[24:27],v[204:207]\n"
EX"v63,v63\n"
MF"v[204:207],a[120:123],v[76:79],v[204:207]\n"
MF"a[132:135],a[128:131],v[64:67],a[132:135]\n"
MF"a[132:135],a[128:131],v[0:3],a[132:135]\n"
RD"a[80:83],v226 offset:4096\n"
MF"a[136:139],a[128:131],v[8:11],a[136:139]\n"
MF"a[136:139],a[128:131],v[68:71],a[136:139]\n"
RD"a[84:87],v226 offset:5120\n"
MF"a[140:143],a[128:131],v[72:75],a[140:143]\n"
MF"a[140:143],a[128:131],v[16:19],a[140:143]\n"
RD"a[88:91],v226 offset:6144\n"
MF"a[144:147],a[128:131],v[24:27],a[144:147]\n"
MF"a[144:147],a[128:131],v[76:79],a[144:147]\n"
"LDB_%=:\n"
WT"lgkmcnt(6)\n"
MF"v[8:11],a[64:67],a[16:19],v[208:211]\n"
WT"lgkmcnt(5)\n"
"v_readlane_b32 s43,v228,s42\n"
PK"v64,v32,v33\n"
MF"v[8:11],a[68:71],a[20:23],v[8:11]\n"
RD"a[92:95],v226 offset:7168\n"
PK"v65,v34,v35\n"
MF"v[16:19],a[68:71],a[36:39],0\n"
PK"v66,v36,v37\n"
PK"v67,v38,v39\n"
MF"v[16:19],a[64:67],a[32:35],v[16:19]\n"
"s_mov_b32 m0,s40\n"
PK"v68,v40,v41\n"
PK"v69,v42,v43\n"
MF"v[24:27],a[64:67],a[48:51],0\n"
PK"v70,v44,v45\n"
PK"v71,v46,v47\n"
MF"v[24:27],a[68:71],a[52:55],v[24:27]\n"
WT"lgkmcnt(1)\n"
RD"a[96:99],v226 offset:8192\n"
"v_readlane_b32 s44,v229,s42\n"
MF"v[8:11],a[72:75],a[24:27],v[8:11]\n"
PK"v72,v48,v49\n"
PK"v73,v50,v51\n"
MF"v[8:11],a[76:79],a[28:31],v[8:11]\n"
SA"s42,s42,1\n"
BL"v225,s[8:11],s43 offen offset:0 sc1 lds\n"
"v_mov_b32 v227,v226\n"
MF"v[16:19],a[76:79],a[44:47],v[16:19]\n"
PK"v74,v52,v53\n"
PK"v75,v54,v55\n"
MF"v[16:19],a[72:75],a[40:43],v[16:19]\n"
RD"a[100:103],v226 offset:9216\n"
PK"v76,v56,v57\n"
PK"v77,v58,v59\n"
MF"v[24:27],a[72:75],a[56:59],v[24:27]\n"
PK"v78,v60,v61\n"
PK"v79,v62,v63\n"
MF"v[24:27],a[76:79],a[60:63],v[24:27]\n"
RD"a[104:107],v226 offset:10240\n"
MF"v[12:15],a[80:83],a[16:19],v[212:215]\n"
EX"v8,v8\n"
MF"v[12:15],a[84:87],a[20:23],v[12:15]\n"
RD"a[108:111],v226 offset:11264\n"
"v_lshrrev_b32 v235,7,v224\n"
MF"v[20:23],a[84:87],a[36:39],0\n"
EX"v9,v9\n"
MF"v[20:23],a[80:83],a[32:35],v[20:23]\n"
BL"v225,s[8:11],s43 offen offset:1024 sc1 lds\n"
MF"v[28:31],a[80:83],a[48:51],0\n"
EX"v10,v10\n"
"v_lshrrev_b32 v230,1,v224\n"
MF"v[28:31],a[84:87],a[52:55],v[28:31]\n"
RD"a[112:115],v226 offset:12288\n"
"v_lshl_add_u32 v230,v235,27,v230\n"
MF"v[12:15],a[88:91],a[24:27],v[12:15]\n"
WT"lgkmcnt(2)\n"
EX"v11,v11\n"
PK"v8,v8,v9\n"
MF"v[12:15],a[92:95],a[28:31],v[12:15]\n"
RD"a[116:119],v226 offset:13312\n"
PK"v9,v10,v11\n"
MF"v[20:23],a[92:95],a[44:47],v[20:23]\n"
"s_lshl_b32 s48,s35,8\n"
EX"v16,v16\n"
MF"v[20:23],a[88:91],a[40:43],v[20:23]\n"
MF"v[28:31],a[88:91],a[56:59],v[28:31]\n"
SA"s48,s48,81264640\n"
EX"v17,v17\n"
MF"v[28:31],a[92:95],a[60:63],v[28:31]\n"
BL"v225,s[8:11],s43 offen offset:2048 sc1 lds\n"
MF"v[40:43],a[96:99],a[16:19],v[216:219]\n"
"s_andn2_b32 s49,s61,255\n"
EX"v18,v18\n"
MF"v[40:43],a[100:103],a[20:23],v[40:43]\n"
RD"a[120:123],v226 offset:14336\n"
PK"v16,v16,v17\n"
MF"v[48:51],a[100:103],a[36:39],0\n"
EX"v19,v19\n"
MF"v[48:51],a[96:99],a[32:35],v[48:51]\n"
RD"a[124:127],v226 offset:15360\n"
"v_bfe_u32 v236,v224,4,2\n"
MF"v[56:59],a[96:99],a[48:51],0\n"
EX"v24,v24\n"
MF"v[56:59],a[100:103],a[52:55],v[56:59]\n"
PK"v17,v18,v19\n"
"v_lshl_add_u32 v236,v236,6,s49\n"
MF"v[40:43],a[104:107],a[24:27],v[40:43]\n"
WT"lgkmcnt(2)\n"
EX"v25,v25\n"
MF"v[40:43],a[108:111],a[28:31],v[40:43]\n"
BL"v225,s[8:11],s43 offen offset:3072 sc1 lds\n"
MF"v[48:51],a[108:111],a[44:47],v[48:51]\n"
EX"v26,v26\n"
MF"v[48:51],a[104:107],a[40:43],v[48:51]\n"
RD"a[64:67],v227 offset:16384\n"
"v_mov_b32 v237,s48\n"
MF"v[56:59],a[104:107],a[56:59],v[56:59]\n"
EX"v27,v27\n"
PK"v24,v24,v25\n"
MF"v[56:59],a[108:111],a[60:63],v[56:59]\n"
SA"m0,s40,16384\n"
"v_cmp_lt_u32 vcc,511,v224\n"
PK"v25,v26,v27\n"
MF"v[44:47],a[112:115],a[16:19],v[220:223]\n"
EX"v12,v12\n"
MF"v[44:47],a[116:119],a[20:23],v[44:47]\n"
RD"a[68:71],v227 offset:17408\n"
"v_cndmask_b32 v236,v236,v237,vcc\n"
MF"v[52:55],a[116:119],a[36:39],0\n"
EX"v13,v13\n"
MF"v[52:55],a[112:115],a[32:35],v[52:55]\n"
BL"v225,s[8:11],s44 offen offset:0 sc1 lds\n"
MF"v[60:63],a[112:115],a[48:51],0\n"
SA"s40,s40,32768\n"
EX"v14,v14\n"
MF"v[60:63],a[116:119],a[52:55],v[60:63]\n"
WT"lgkmcnt(1)\n"
RD"a[72:75],v227 offset:18432\n"
PK"v10,v12,v13\n"
MF"v[44:47],a[120:123],a[24:27],v[44:47]\n"
"s_cmp_eq_u32 s40,s46\n"
EX"v15,v15\n"
MF"v[44:47],a[124:127],a[28:31],v[44:47]\n"
"s_cselect_b32 s40,s47,s40\n"
PK"v11,v14,v15\n"
MF"v[52:55],a[124:127],a[44:47],v[52:55]\n"
EX"v20,v20\n"
MF"v[52:55],a[120:123],a[40:43],v[52:55]\n"
RD"a[76:79],v227 offset:19456\n"
MF"v[60:63],a[120:123],a[56:59],v[60:63]\n"
EX"v21,v21\n"
MF"v[60:63],a[124:127],a[60:63],v[60:63]\n"
SA"s41,s41,32768\n"
BL"v225,s[8:11],s44 offen offset:1024 sc1 lds\n"
MF"v[80:83],a[64:67],v[64:67],v[80:83]\n"
"s_cmp_eq_u32 s41,163840\n"
EX"v22,v22\n"
MF"v[112:115],a[64:67],v[68:71],v[112:115]\n"
"s_cselect_b32 s41,0,s41\n"
RD"a[80:83],v227 offset:20480\n"
"v_add_u32 v226,s41,v224\n"
MF"v[144:147],a[64:67],v[72:75],v[144:147]\n"
EX"v23,v23\n"
PK"v18,v20,v21\n"
MF"v[176:179],a[64:67],v[76:79],v[176:179]\n"
WT"lgkmcnt(2)\n"
PK"v19,v22,v23\n"
MF"v[84:87],a[68:71],v[64:67],v[84:87]\n"
EX"v28,v28\n"
MF"v[116:119],a[68:71],v[68:71],v[116:119]\n"
RD"a[84:87],v227 offset:21504\n"
MF"v[148:151],a[68:71],v[72:75],v[148:151]\n"
EX"v29,v29\n"
MF"v[180:183],a[68:71],v[76:79],v[180:183]\n"
BL"v225,s[8:11],s44 offen offset:2048 sc1 lds\n"
MF"v[88:91],a[72:75],v[64:67],v[88:91]\n"
EX"v30,v30\n"
MF"v[120:123],a[72:75],v[68:71],v[120:123]\n"
RD"a[88:91],v227 offset:22528\n"
PK"v26,v28,v29\n"
MF"v[152:155],a[72:75],v[72:75],v[152:155]\n"
EX"v31,v31\n"
MF"v[184:187],a[72:75],v[76:79],v[184:187]\n"
WT"lgkmcnt(2)\n"
PK"v27,v30,v31\n"
MF"v[92:95],a[76:79],v[64:67],v[92:95]\n"
EX"v40,v40\n"
MF"v[124:127],a[76:79],v[68:71],v[124:127]\n"
RD"a[92:95],v227 offset:23552\n"
MF"v[156:159],a[76:79],v[72:75],v[156:159]\n"
EX"v41,v41\n"
MF"v[188:191],a[76:79],v[76:79],v[188:191]\n"
BL"v225,s[8:11],s44 offen offset:3072 sc1 lds\n"
MF"v[96:99],a[80:83],v[64:67],v[96:99]\n"
EX"v42,v42\n"
MF"v[128:131],a[80:83],v[68:71],v[128:131]\n"
RD"a[96:99],v227 offset:24576\n"
MF"v[160:163],a[80:83],v[72:75],v[160:163]\n"
EX"v43,v43\n"
MF"v[192:195],a[80:83],v[76:79],v[192:195]\n"
WT"lgkmcnt(2)\n"
RD"a[100:103],v227 offset:25600\n"
MF"v[100:103],a[84:87],v[64:67],v[100:103]\n"
EX"v48,v48\n"
MF"v[132:135],a[84:87],v[68:71],v[132:135]\n"
MF"v[164:167],a[84:87],v[72:75],v[164:167]\n"
EX"v49,v49\n"
MF"v[196:199],a[84:87],v[76:79],v[196:199]\n"
MF"v[104:107],a[88:91],v[64:67],v[104:107]\n"
EX"v50,v50\n"
MF"v[136:139],a[88:91],v[68:71],v[136:139]\n"
RD"a[104:107],v227 offset:26624\n"
MF"v[168:171],a[88:91],v[72:75],v[168:171]\n"
EX"v51,v51\n"
MF"v[200:203],a[88:91],v[76:79],v[200:203]\n"
WT"lgkmcnt(1)\n"
RD"a[108:111],v227 offset:27648\n"
MF"v[108:111],a[92:95],v[64:67],v[108:111]\n"
EX"v44,v44\n"
MF"v[140:143],a[92:95],v[68:71],v[140:143]\n"
RD"a[112:115],v227 offset:28672\n"
MF"v[172:175],a[92:95],v[72:75],v[172:175]\n"
EX"v45,v45\n"
MF"v[204:207],a[92:95],v[76:79],v[204:207]\n"
MF"a[132:135],a[128:131],v[64:67],a[132:135]\n"
EX"v46,v46\n"
MF"a[136:139],a[128:131],v[68:71],a[136:139]\n"
RD"a[116:119],v227 offset:29696\n"
PK"v68,v40,v41\n"
PK"v69,v42,v43\n"
MF"a[140:143],a[128:131],v[72:75],a[140:143]\n"
EX"v47,v47\n"
PK"v72,v48,v49\n"
MF"a[144:147],a[128:131],v[76:79],a[144:147]\n"
RD"a[120:123],v227 offset:30720\n"
PK"v73,v50,v51\n"
PK"v70,v44,v45\n"
MF"v[112:115],a[96:99],v[8:11],v[112:115]\n"
EX"v52,v52\n"
MF"v[144:147],a[96:99],v[16:19],v[144:147]\n"
RD"a[124:127],v227 offset:31744\n"
PK"v71,v46,v47\n"
MF"v[176:179],a[96:99],v[24:27],v[176:179]\n"
EX"v53,v53\n"
MF"v[116:119],a[100:103],v[8:11],v[116:119]\n"
"v_accvgpr_read_b32 v251,a132\n"
"v_subrev_u32 v253,0x2b800000,v251\n"
MF"v[148:151],a[100:103],v[16:19],v[148:151]\n"
"v_cmp_le_u32 vcc,0x34000000,v253\n"
"s_cmp_lg_u64 vcc,0\n"
"s_cbranch_scc1 L1014_%=\n"
"L1013_%=:\n"
MF"v[180:183],a[100:103],v[24:27],v[180:183]\n"
WT"lgkmcnt(2)\n"
EX"v54,v54\n"
MF"v[120:123],a[104:107],v[8:11],v[120:123]\n"
"v_rcp_f32 v251,v251\n"
PK"v74,v52,v53\n"
MF"v[152:155],a[104:107],v[16:19],v[152:155]\n"
EX"v55,v55\n"
MF"v[184:187],a[104:107],v[24:27],v[184:187]\n"
WT"vmcnt(16) lgkmcnt(0)\n"
"s_barrier\n"
MF"v[124:127],a[108:111],v[8:11],v[124:127]\n"
RD"a[64:67],v226 offset:0\n"
MU"v80,v251,v80\n"
MU"v81,v251,v81\n"
MF"v[156:159],a[108:111],v[16:19],v[156:159]\n"
MU"v82,v251,v82\n"
MU"v83,v251,v83\n"
PK"v75,v54,v55\n"
MF"v[188:191],a[108:111],v[24:27],v[188:191]\n"
MU"v84,v251,v84\n"
MU"v85,v251,v85\n"
MU"v86,v251,v86\n"
MF"v[128:131],a[112:115],v[8:11],v[128:131]\n"
MU"v87,v251,v87\n"
PK"v80,v80,v81\n"
PK"v81,v82,v83\n"
MF"v[160:163],a[112:115],v[16:19],v[160:163]\n"
RD"a[68:71],v226 offset:1024\n"
PK"v82,v84,v85\n"
PK"v83,v86,v87\n"
MF"v[192:195],a[112:115],v[24:27],v[192:195]\n"
BS"v[80:83],v224,s[8:11],s59 offen offset:0 sc1\n"
MU"v88,v251,v88\n"
MF"v[132:135],a[116:119],v[8:11],v[132:135]\n"
RD"a[72:75],v226 offset:2048\n"
MU"v89,v251,v89\n"
MU"v90,v251,v90\n"
MF"v[164:167],a[116:119],v[16:19],v[164:167]\n"
MU"v91,v251,v91\n"
MU"v92,v251,v92\n"
MU"v93,v251,v93\n"
MF"v[196:199],a[116:119],v[24:27],v[196:199]\n"
RD"a[76:79],v226 offset:3072\n"
MU"v94,v251,v94\n"
MU"v95,v251,v95\n"
MF"v[136:139],a[120:123],v[8:11],v[136:139]\n"
PK"v88,v88,v89\n"
PK"v89,v90,v91\n"
MF"v[168:171],a[120:123],v[16:19],v[168:171]\n"
PK"v90,v92,v93\n"
PK"v91,v94,v95\n"
MF"v[200:203],a[120:123],v[24:27],v[200:203]\n"
BS"v[88:91],v224,s[8:11],s59 offen offset:1024 sc1\n"
MU"v96,v251,v96\n"
MF"v[140:143],a[124:127],v[8:11],v[140:143]\n"
RD"a[80:83],v226 offset:4096\n"
MU"v97,v251,v97\n"
MU"v98,v251,v98\n"
MF"v[172:175],a[124:127],v[16:19],v[172:175]\n"
MU"v99,v251,v99\n"
MU"v100,v251,v100\n"
MU"v101,v251,v101\n"
MF"v[204:207],a[124:127],v[24:27],v[204:207]\n"
MU"v104,v251,v104\n"
MU"v105,v251,v105\n"
MU"v102,v251,v102\n"
MF"a[136:139],a[128:131],v[8:11],a[136:139]\n"
PK"v96,v96,v97\n"
MU"v106,v251,v106\n"
MU"v103,v251,v103\n"
MF"a[140:143],a[128:131],v[16:19],a[140:143]\n"
MU"v107,v251,v107\n"
PK"v97,v98,v99\n"
PK"v98,v100,v101\n"
MF"a[144:147],a[128:131],v[24:27],a[144:147]\n"
MU"v108,v251,v108\n"
MU"v109,v251,v109\n"
PK"v99,v102,v103\n"
BS"v[96:99],v224,s[8:11],s59 offen offset:2048 sc1\n"
MU"v110,v251,v110\n"
MU"v111,v251,v111\n"
PK"v104,v104,v105\n"
PK"v105,v106,v107\n"
PK"v106,v108,v109\n"
PK"v107,v110,v111\n"
BS"v[104:107],v224,s[8:11],s59 offen offset:3072 sc1\n"
RD"a[84:87],v226 offset:5120\n"
RD"a[88:91],v226 offset:6144\n"
"s_cmp_eq_u32 s34,7\n"
"s_cbranch_scc1 LD2_%=\n"
WT"lgkmcnt(6)\n"
MF"v[0:3],a[64:67],a[32:35],0\n"
WT"lgkmcnt(5)\n"
EX"v56,v56\n"
MF"v[0:3],a[68:71],a[36:39],v[0:3]\n"
"s_mov_b32 m0,s40\n"
EX"v57,v57\n"
MF"v[8:11],a[68:71],a[52:55],0\n"
"s_lshl_b32 s50,s35,6\n"
RD"a[92:95],v226 offset:7168\n"
"v_readlane_b32 s43,v228,s42\n"
MF"v[8:11],a[64:67],a[48:51],v[8:11]\n"
WT"lgkmcnt(5)\n"
EX"v58,v58\n"
MF"v[0:3],a[72:75],a[40:43],v[0:3]\n"
WT"lgkmcnt(1)\n"
BL"v225,s[8:11],s43 offen offset:0 sc1 lds\n"
"v_readlane_b32 s44,v229,s42\n"
MF"v[0:3],a[76:79],a[44:47],v[0:3]\n"
SA"s50,s50,4352\n"
RD"a[96:99],v226 offset:8192\n"
RD"a[100:103],v226 offset:9216\n"
PK"v76,v56,v57\n"
MF"v[8:11],a[76:79],a[60:63],v[8:11]\n"
SA"s42,s42,1\n"
EX"v59,v59\n"
MF"v[8:11],a[72:75],a[56:59],v[8:11]\n"
SA"s41,s41,32768\n"
RD"a[104:107],v226 offset:10240\n"
"v_lshrrev_b32 v237,1,v224\n"
MF"v[4:7],a[80:83],a[32:35],0\n"
"s_cmp_eq_u32 s41,163840\n"
EX"v60,v60\n"
"v_and_b32 v237,56,v237\n"
MF"v[4:7],a[84:87],a[36:39],v[4:7]\n"
"s_cselect_b32 s41,0,s41\n"
EX"v61,v61\n"
"v_add_u32 v227,s41,v224\n"
MF"v[12:15],a[84:87],a[52:55],0\n"
RD"a[108:111],v226 offset:11264\n"
PK"v77,v58,v59\n"
MF"v[12:15],a[80:83],a[48:51],v[12:15]\n"
BL"v225,s[8:11],s43 offen offset:1024 sc1 lds\n"
MF"v[4:7],a[88:91],a[40:43],v[4:7]\n"
WT"lgkmcnt(2)\n"
EX"v62,v62\n"
MF"v[4:7],a[92:95],a[44:47],v[4:7]\n"
RD"a[112:115],v226 offset:12288\n"
RD"a[116:119],v226 offset:13312\n"
PK"v78,v60,v61\n"
MF"v[12:15],a[92:95],a[60:63],v[12:15]\n"
EX"v63,v63\n"
MF"v[12:15],a[88:91],a[56:59],v[12:15]\n"
RD"a[120:123],v226 offset:14336\n"
PK"v79,v62,v63\n"
MF"v[16:19],a[96:99],a[32:35],0\n"
EX"v0,v0\n"
MF"v[16:19],a[100:103],a[36:39],v[16:19]\n"
EX"v1,v1\n"
MF"v[24:27],a[100:103],a[52:55],0\n"
RD"a[124:127],v226 offset:15360\n"
PK"v0,v0,v1\n"
MF"v[24:27],a[96:99],a[48:51],v[24:27]\n"
WT"lgkmcnt(4)\n"
BL"v225,s[8:11],s43 offen offset:2048 sc1 lds\n"
MF"v[16:19],a[104:107],a[40:43],v[16:19]\n"
"s_lshl_b32 s56,s34,1\n"
EX"v2,v2\n"
MF"v[16:19],a[108:111],a[44:47],v[16:19]\n"
"s_sub_u32 s56,12,s56\n"
RD"a[64:67],v226 offset:16384\n"
RD"a[68:71],v226 offset:17408\n"
MF"v[24:27],a[108:111],a[60:63],v[24:27]\n"
"s_sub_u32 s22,s56,2\n"
EX"v3,v3\n"
MF"v[24:27],a[104:107],a[56:59],v[24:27]\n"
WT"lgkmcnt(3)\n"
RD"a[72:75],v226 offset:18432\n"
PK"v1,v2,v3\n"
MF"v[20:23],a[112:115],a[32:35],0\n"
"s_min_u32 s56,s56,1\n"
EX"v8,v8\n"
MF"v[20:23],a[116:119],a[36:39],v[20:23]\n"
"s_mov_b32 s57,s61\n"
EX"v9,v9\n"
MF"v[28:31],a[116:119],a[52:55],0\n"
BL"v225,s[8:11],s43 offen offset:3072 sc1 lds\n"
MF"v[28:31],a[112:115],a[48:51],v[28:31]\n"
RD"a[76:79],v226 offset:19456\n"
RD"a[80:83],v226 offset:20480\n"
MF"v[20:23],a[120:123],a[40:43],v[20:23]\n"
WT"lgkmcnt(3)\n"
EX"v10,v10\n"
PK"v8,v8,v9\n"
MF"v[20:23],a[124:127],a[44:47],v[20:23]\n"
SA"m0,s40,16384\n"
RD"a[84:87],v226 offset:21504\n"
RD"a[88:91],v226 offset:22528\n"
EX"v11,v11\n"
MF"v[28:31],a[124:127],a[60:63],v[28:31]\n"
EX"v4,v4\n"
MF"v[28:31],a[120:123],a[56:59],v[28:31]\n"
"buffer_load_dwordx2 v[238:239],v237,s[8:11],s50 offen sc1\n"
PK"v9,v10,v11\n"
MF"v[32:35],a[64:67],a[32:35],0\n"
EX"v5,v5\n"
MF"v[32:35],a[68:71],a[36:39],v[32:35]\n"
BL"v225,s[8:11],s44 offen offset:0 sc1 lds\n"
MF"v[40:43],a[68:71],a[52:55],0\n"
SA"s40,s40,32768\n"
RD"a[92:95],v226 offset:23552\n"
PK"v2,v4,v5\n"
MF"v[40:43],a[64:67],a[48:51],v[40:43]\n"
WT"lgkmcnt(3)\n"
EX"v6,v6\n"
MF"v[32:35],a[72:75],a[40:43],v[32:35]\n"
"s_cmp_eq_u32 s40,s46\n"
RD"a[96:99],v226 offset:24576\n"
RD"a[100:103],v226 offset:25600\n"
MF"v[32:35],a[76:79],a[44:47],v[32:35]\n"
"s_cselect_b32 s40,s47,s40\n"
EX"v7,v7\n"
MF"v[40:43],a[76:79],a[60:63],v[40:43]\n"
RD"a[104:107],v226 offset:26624\n"
PK"v3,v6,v7\n"
MF"v[40:43],a[72:75],a[56:59],v[40:43]\n"
EX"v12,v12\n"
MF"v[36:39],a[80:83],a[32:35],0\n"
WT"lgkmcnt(4)\n"
EX"v13,v13\n"
MF"v[36:39],a[84:87],a[36:39],v[36:39]\n"
BL"v225,s[8:11],s44 offen offset:1024 sc1 lds\n"
MF"v[44:47],a[84:87],a[52:55],0\n"
EX"v14,v14\n"
MF"v[44:47],a[80:83],a[48:51],v[44:47]\n"
RD"a[108:111],v226 offset:27648\n"
RD"a[112:115],v226 offset:28672\n"
PK"v10,v12,v13\n"
MF"v[36:39],a[88:91],a[40:43],v[36:39]\n"
WT"lgkmcnt(3)\n"
EX"v15,v15\n"
MF"v[36:39],a[92:95],a[44:47],v[36:39]\n"
RD"a[116:119],v226 offset:29696\n"
RD"a[120:123],v226 offset:30720\n"
MF"v[44:47],a[92:95],a[60:63],v[44:47]\n"
EX"v16,v16\n"
PK"v11,v14,v15\n"
MF"v[44:47],a[88:91],a[56:59],v[44:47]\n"
EX"v17,v17\n"
MF"v[48:51],a[96:99],a[32:35],0\n"
BL"v225,s[8:11],s44 offen offset:2048 sc1 lds\n"
MF"v[48:51],a[100:103],a[36:39],v[48:51]\n"
EX"v18,v18\n"
MF"v[56:59],a[100:103],a[52:55],0\n"
RD"a[124:127],v226 offset:31744\n"
PK"v16,v16,v17\n"
MF"v[56:59],a[96:99],a[48:51],v[56:59]\n"
WT"lgkmcnt(3)\n"
EX"v19,v19\n"
MF"v[48:51],a[104:107],a[40:43],v[48:51]\n"
WT"vmcnt(16)\n"
"s_barrier\n"
MF"v[48:51],a[108:111],a[44:47],v[48:51]\n"
RD"a[96:99],v227 offset:8192\n"
RD"a[100:103],v227 offset:9216\n"
MF"v[56:59],a[108:111],a[60:63],v[56:59]\n"
EX"v24,v24\n"
PK"v17,v18,v19\n"
MF"v[56:59],a[104:107],a[56:59],v[56:59]\n"
EX"v25,v25\n"
MF"v[52:55],a[112:115],a[32:35],0\n"
WT"lgkmcnt(3)\n"
BL"v225,s[8:11],s44 offen offset:3072 sc1 lds\n"
MF"v[52:55],a[116:119],a[36:39],v[52:55]\n"
EX"v26,v26\n"
PK"v24,v24,v25\n"
MF"v[60:63],a[116:119],a[52:55],0\n"
EX"v27,v27\n"
"v_readlane_b32 s43,v228,s42\n"
MF"v[60:63],a[112:115],a[48:51],v[60:63]\n"
"s_mov_b32 m0,s40\n"
RD"a[104:107],v227 offset:10240\n"
RD"a[108:111],v227 offset:11264\n"
MF"v[52:55],a[120:123],a[40:43],v[52:55]\n"
WT"lgkmcnt(2)\n"
EX"v20,v20\n"
MF"v[52:55],a[124:127],a[44:47],v[52:55]\n"
EX"v21,v21\n"
PK"v25,v26,v27\n"
MF"v[60:63],a[124:127],a[60:63],v[60:63]\n"
RD"a[112:115],v227 offset:12288\n"
"v_readlane_b32 s44,v229,s42\n"
MF"v[60:63],a[120:123],a[56:59],v[60:63]\n"
SA"s41,s41,32768\n"
EX"v22,v22\n"
MF"v[112:115],a[96:99],v[68:71],v[112:115]\n"
"s_cmp_eq_u32 s41,163840\n"
BL"v225,s[8:11],s43 offen offset:0 sc1 lds\n"
MF"v[144:147],a[96:99],v[72:75],v[144:147]\n"
"s_cselect_b32 s41,0,s41\n"
EX"v23,v23\n"
PK"v18,v20,v21\n"
MF"v[176:179],a[96:99],v[76:79],v[176:179]\n"
SA"s42,s42,1\n"
RD"a[116:119],v227 offset:13312\n"
"v_add_u32 v226,s41,v224\n"
MF"v[116:119],a[100:103],v[68:71],v[116:119]\n"
EX"v28,v28\n"
MF"v[148:151],a[100:103],v[72:75],v[148:151]\n"
RD"a[120:123],v227 offset:14336\n"
PK"v19,v22,v23\n"
MF"v[180:183],a[100:103],v[76:79],v[180:183]\n"
WT"lgkmcnt(2)\n"
EX"v29,v29\n"
MF"v[120:123],a[104:107],v[68:71],v[120:123]\n"
EX"v30,v30\n"
MF"v[152:155],a[104:107],v[72:75],v[152:155]\n"
BL"v225,s[8:11],s43 offen offset:1024 sc1 lds\n"
MF"v[184:187],a[104:107],v[76:79],v[184:187]\n"
EX"v31,v31\n"
PK"v26,v28,v29\n"
MF"v[124:127],a[108:111],v[68:71],v[124:127]\n"
RD"a[124:127],v227 offset:15360\n"
PK"v27,v30,v31\n"
MF"v[156:159],a[108:111],v[72:75],v[156:159]\n"
EX"v32,v32\n"
MF"v[188:191],a[108:111],v[76:79],v[188:191]\n"
MF"v[128:131],a[112:115],v[68:71],v[128:131]\n"
EX"v33,v33\n"
MF"v[160:163],a[112:115],v[72:75],v[160:163]\n"
RD"a[64:67],v227 offset:16384\n"
PK"v64,v32,v33\n"
MF"v[192:195],a[112:115],v[76:79],v[192:195]\n"
WT"lgkmcnt(2)\n"
BL"v225,s[8:11],s43 offen offset:2048 sc1 lds\n"
MF"v[132:135],a[116:119],v[68:71],v[132:135]\n"
EX"v34,v34\n"
MF"v[164:167],a[116:119],v[72:75],v[164:167]\n"
EX"v35,v35\n"
MF"v[196:199],a[116:119],v[76:79],v[196:199]\n"
EX"v40,v40\n"
MF"v[136:139],a[120:123],v[68:71],v[136:139]\n"
RD"a[68:71],v227 offset:24576\n"
PK"v65,v34,v35\n"
MF"v[168:171],a[120:123],v[72:75],v[168:171]\n"
EX"v41,v41\n"
MF"v[200:203],a[120:123],v[76:79],v[200:203]\n"
WT"lgkmcnt(1)\n"
RD"a[72:75],v227 offset:17408\n"
RD"a[76:79],v227 offset:25600\n"
MF"v[140:143],a[124:127],v[68:71],v[140:143]\n"
WT"vmcnt(7)\n"
"v_cmp_eq_u64 vcc,s[28:29],v[238:239]\n"
"s_cmp_eq_u64 vcc,exec\n"
"s_cbranch_scc0 L1019_%=\n"
"L1020_%=:\n"
MF"v[172:175],a[124:127],v[72:75],v[172:175]\n"
EX"v42,v42\n"
MF"v[204:207],a[124:127],v[76:79],v[204:207]\n"
RD"a[80:83],v227 offset:18432\n"
MF"a[136:139],a[128:131],v[68:71],a[136:139]\n"
BL"v225,s[8:11],s43 offen offset:3072 sc1 lds\n"
PK"v68,v40,v41\n"
MF"a[140:143],a[128:131],v[72:75],a[140:143]\n"
SA"m0,s40,16384\n"
EX"v43,v43\n"
MF"a[144:147],a[128:131],v[76:79],a[144:147]\n"
EX"v36,v36\n"
MF"v[144:147],a[64:67],v[0:3],v[144:147]\n"
WT"lgkmcnt(1)\n"
RD"a[84:87],v227 offset:26624\n"
RD"a[88:91],v227 offset:19456\n"
PK"v69,v42,v43\n"
MF"v[144:147],a[68:71],v[16:19],v[144:147]\n"
EX"v37,v37\n"
MF"v[176:179],a[68:71],v[24:27],v[176:179]\n"
SA"s51,s59,16384\n"
RD"a[92:95],v227 offset:27648\n"
RD"a[96:99],v227 offset:20480\n"
MF"v[176:179],a[64:67],v[8:11],v[176:179]\n"
SA"s40,s40,32768\n"
EX"v38,v38\n"
MF"v[148:151],a[72:75],v[0:3],v[148:151]\n"
"s_cmp_eq_u32 s40,s46\n"
BL"v225,s[8:11],s44 offen offset:0 sc1 lds\n"
PK"v66,v36,v37\n"
MF"v[148:151],a[76:79],v[16:19],v[148:151]\n"
"s_cselect_b32 s40,s47,s40\n"
EX"v39,v39\n"
"v_accvgpr_read_b32 v252,a136\n"
MF"v[180:183],a[76:79],v[24:27],v[180:183]\n"
EX"v44,v44\n"
"v_subrev_u32 v253,0x2b800000,v252\n"
MF"v[180:183],a[72:75],v[8:11],v[180:183]\n"
WT"lgkmcnt(2)\n"
"v_cmp_le_u32 vcc,0x34000000,v253\n"
"s_cmp_lg_u64 vcc,0\n"
"s_cbranch_scc1 L1023_%=\n"
"L1022_%=:\n"
MF"v[152:155],a[80:83],v[0:3],v[152:155]\n"
"v_rcp_f32 v252,v252\n"
PK"v67,v38,v39\n"
MF"v[152:155],a[84:87],v[16:19],v[152:155]\n"
RD"a[100:103],v227 offset:28672\n"
RD"a[104:107],v227 offset:21504\n"
EX"v45,v45\n"
MF"v[184:187],a[84:87],v[24:27],v[184:187]\n"
RD"a[108:111],v227 offset:29696\n"
RD"a[112:115],v227 offset:22528\n"
MU"v112,v252,v112\n"
MF"v[184:187],a[80:83],v[8:11],v[184:187]\n"
EX"v46,v46\n"
MU"v113,v252,v113\n"
MF"v[156:159],a[88:91],v[0:3],v[156:159]\n"
WT"lgkmcnt(4)\n"
BL"v225,s[8:11],s44 offen offset:1024 sc1 lds\n"
MU"v114,v252,v114\n"
MF"v[156:159],a[92:95],v[16:19],v[156:159]\n"
EX"v47,v47\n"
MU"v115,v252,v115\n"
MF"v[188:191],a[92:95],v[24:27],v[188:191]\n"
EX"v48,v48\n"
MU"v116,v252,v116\n"
MF"v[188:191],a[88:91],v[8:11],v[188:191]\n"
EX"v49,v49\n"
PK"v70,v44,v45\n"
MF"v[160:163],a[96:99],v[0:3],v[160:163]\n"
WT"lgkmcnt(2)\n"
RD"a[116:119],v227 offset:30720\n"
RD"a[120:123],v227 offset:23552\n"
MU"v117,v252,v117\n"
MF"v[160:163],a[100:103],v[16:19],v[160:163]\n"
EX"v50,v50\n"
MU"v118,v252,v118\n"
MF"v[192:195],a[100:103],v[24:27],v[192:195]\n"
RD"a[124:127],v227 offset:31744\n"
MU"v119,v252,v119\n"
PK"v112,v112,v113\n"
MF"v[192:195],a[96:99],v[8:11],v[192:195]\n"
BL"v225,s[8:11],s44 offen offset:2048 sc1 lds\n"
PK"v113,v114,v115\n"
MF"v[164:167],a[104:107],v[0:3],v[164:167]\n"
WT"lgkmcnt(3)\n"
EX"v51,v51\n"
PK"v114,v116,v117\n"
MF"v[164:167],a[108:111],v[16:19],v[164:167]\n"
EX"v52,v52\n"
PK"v115,v118,v119\n"
MF"v[196:199],a[108:111],v[24:27],v[196:199]\n"
BS"v[112:115],v224,s[8:11],s51 offen offset:0 sc1\n"
PK"v71,v46,v47\n"
MF"v[196:199],a[104:107],v[8:11],v[196:199]\n"
"s_barrier\n"
MF"v[168:171],a[112:115],v[0:3],v[168:171]\n"
WT"lgkmcnt(1)\n"
EX"v53,v53\n"
MU"v120,v252,v120\n"
MF"v[168:171],a[116:119],v[16:19],v[168:171]\n"
RD"a[64:67],v226 offset:0\n"
RD"a[68:71],v226 offset:1024\n"
MU"v121,v252,v121\n"
MF"v[200:203],a[116:119],v[24:27],v[200:203]\n"
EX"v54,v54\n"
MU"v122,v252,v122\n"
MF"v[200:203],a[112:115],v[8:11],v[200:203]\n"
BL"v225,s[8:11],s44 offen offset:3072 sc1 lds\n"
MU"v123,v252,v123\n"
MF"v[172:175],a[120:123],v[0:3],v[172:175]\n"
WT"lgkmcnt(2)\n"
EX"v55,v55\n"
MU"v124,v252,v124\n"
MF"v[172:175],a[124:127],v[16:19],v[172:175]\n"
RD"a[72:75],v226 offset:2048\n"
PK"v72,v48,v49\n"
MU"v125,v252,v125\n"
MF"v[204:207],a[124:127],v[24:27],v[204:207]\n"
RD"a[76:79],v226 offset:3072\n"
MU"v126,v252,v126\n"
MU"v127,v252,v127\n"
MF"v[204:207],a[120:123],v[8:11],v[204:207]\n"
PK"v120,v120,v121\n"
PK"v121,v122,v123\n"
PK"v122,v124,v125\n"
MF"a[140:143],a[128:131],v[0:3],a[140:143]\n"
MU"v128,v252,v128\n"
MU"v129,v252,v129\n"
MU"v136,v252,v136\n"
MF"a[140:143],a[128:131],v[16:19],a[140:143]\n"
MU"v137,v252,v137\n"
MU"v130,v252,v130\n"
MU"v131,v252,v131\n"
MF"a[144:147],a[128:131],v[24:27],a[144:147]\n"
PK"v128,v128,v129\n"
MU"v138,v252,v138\n"
MU"v139,v252,v139\n"
MF"a[144:147],a[128:131],v[8:11],a[144:147]\n"
PK"v136,v136,v137\n"
MU"v132,v252,v132\n"
MU"v133,v252,v133\n"
PK"v73,v50,v51\n"
PK"v74,v52,v53\n"
PK"v75,v54,v55\n"
PK"v123,v126,v127\n"
BS"v[120:123],v224,s[8:11],s51 offen offset:1024 sc1\n"
MU"v134,v252,v134\n"
MU"v135,v252,v135\n"
PK"v129,v130,v131\n"
PK"v130,v132,v133\n"
PK"v131,v134,v135\n"
BS"v[128:131],v224,s[8:11],s51 offen offset:2048 sc1\n"
MU"v140,v252,v140\n"
MU"v141,v252,v141\n"
MU"v142,v252,v142\n"
MU"v143,v252,v143\n"
PK"v137,v138,v139\n"
PK"v138,v140,v141\n"
PK"v139,v142,v143\n"
BS"v[136:139],v224,s[8:11],s51 offen offset:3072 sc1\n"
RD"a[80:83],v226 offset:4096\n"
RD"a[84:87],v226 offset:5120\n"
RD"a[88:91],v226 offset:6144\n"
".p2align 6\n"
"LP2_%=:\n"
WT"lgkmcnt(6)\n"
MF"v[0:3],a[64:67],a[32:35],0\n"
WT"lgkmcnt(5)\n"
EX"v56,v56\n"
MF"v[0:3],a[68:71],a[36:39],v[0:3]\n"
RD"a[92:95],v226 offset:7168\n"
"v_readlane_b32 s43,v228,s42\n"
MF"v[8:11],a[68:71],a[52:55],0\n"
EX"v57,v57\n"
MF"v[8:11],a[64:67],a[48:51],v[8:11]\n"
WT"lgkmcnt(5)\n"
RD"a[96:99],v226 offset:8192\n"
RD"a[100:103],v226 offset:9216\n"
MF"v[0:3],a[72:75],a[40:43],v[0:3]\n"
WT"lgkmcnt(3)\n"
EX"v58,v58\n"
MF"v[0:3],a[76:79],a[44:47],v[0:3]\n"
"s_mov_b32 m0,s40\n"
RD"a[104:107],v226 offset:10240\n"
"v_readlane_b32 s44,v229,s42\n"
MF"v[8:11],a[76:79],a[60:63],v[8:11]\n"
SA"s41,s41,32768\n"
EX"v59,v59\n"
MF"v[8:11],a[72:75],a[56:59],v[8:11]\n"
"s_cmp_eq_u32 s41,163840\n"
BL"v225,s[8:11],s43 offen offset:0 sc1 lds\n"
MF"v[4:7],a[80:83],a[32:35],0\n"
"s_cselect_b32 s41,0,s41\n"
EX"v60,v60\n"
MF"v[4:7],a[84:87],a[36:39],v[4:7]\n"
SA"s42,s42,1\n"
RD"a[108:111],v226 offset:11264\n"
"v_add_u32 v227,s41,v224\n"
MF"v[12:15],a[84:87],a[52:55],0\n"
EX"v61,v61\n"
MF"v[12:15],a[80:83],a[48:51],v[12:15]\n"
RD"a[112:115],v226 offset:12288\n"
RD"a[116:119],v226 offset:13312\n"
MF"v[4:7],a[88:91],a[40:43],v[4:7]\n"
WT"lgkmcnt(3)\n"
EX"v62,v62\n"
MF"v[4:7],a[92:95],a[44:47],v[4:7]\n"
RD"a[120:123],v226 offset:14336\n"
PK"v76,v56,v57\n"
MF"v[12:15],a[92:95],a[60:63],v[12:15]\n"
EX"v63,v63\n"
MF"v[12:15],a[88:91],a[56:59],v[12:15]\n"
BL"v225,s[8:11],s43 offen offset:1024 sc1 lds\n"
MF"v[16:19],a[96:99],a[32:35],0\n"
EX"v0,v0\n"
MF"v[16:19],a[100:103],a[36:39],v[16:19]\n"
RD"a[124:127],v226 offset:15360\n"
PK"v77,v58,v59\n"
MF"v[24:27],a[100:103],a[52:55],0\n"
EX"v1,v1\n"
MF"v[24:27],a[96:99],a[48:51],v[24:27]\n"
RD"a[64:67],v226 offset:16384\n"
RD"a[68:71],v226 offset:17408\n"
MF"v[16:19],a[104:107],a[40:43],v[16:19]\n"
WT"lgkmcnt(3)\n"
EX"v2,v2\n"
MF"v[16:19],a[108:111],a[44:47],v[16:19]\n"
RD"a[72:75],v226 offset:18432\n"
PK"v78,v60,v61\n"
MF"v[24:27],a[108:111],a[60:63],v[24:27]\n"
EX"v3,v3\n"
MF"v[24:27],a[104:107],a[56:59],v[24:27]\n"
BL"v225,s[8:11],s43 offen offset:2048 sc1 lds\n"
MF"v[20:23],a[112:115],a[32:35],0\n"
EX"v8,v8\n"
MF"v[20:23],a[116:119],a[36:39],v[20:23]\n"
RD"a[76:79],v226 offset:19456\n"
PK"v79,v62,v63\n"
MF"v[28:31],a[116:119],a[52:55],0\n"
EX"v9,v9\n"
MF"v[28:31],a[112:115],a[48:51],v[28:31]\n"
RD"a[80:83],v226 offset:20480\n"
RD"a[84:87],v226 offset:21504\n"
MF"v[20:23],a[120:123],a[40:43],v[20:23]\n"
WT"lgkmcnt(3)\n"
EX"v10,v10\n"
MF"v[20:23],a[124:127],a[44:47],v[20:23]\n"
RD"a[88:91],v226 offset:22528\n"
PK"v0,v0,v1\n"
MF"v[28:31],a[124:127],a[60:63],v[28:31]\n"
EX"v11,v11\n"
MF"v[28:31],a[120:123],a[56:59],v[28:31]\n"
BL"v225,s[8:11],s43 offen offset:3072 sc1 lds\n"
MF"v[32:35],a[64:67],a[32:35],0\n"
EX"v4,v4\n"
MF"v[32:35],a[68:71],a[36:39],v[32:35]\n"
RD"a[92:95],v226 offset:23552\n"
PK"v1,v2,v3\n"
MF"v[40:43],a[68:71],a[52:55],0\n"
EX"v5,v5\n"
MF"v[40:43],a[64:67],a[48:51],v[40:43]\n"
SA"m0,s40,16384\n"
RD"a[96:99],v226 offset:24576\n"
RD"a[100:103],v226 offset:25600\n"
MF"v[32:35],a[72:75],a[40:43],v[32:35]\n"
WT"lgkmcnt(3)\n"
EX"v6,v6\n"
MF"v[32:35],a[76:79],a[44:47],v[32:35]\n"
RD"a[104:107],v226 offset:26624\n"
PK"v8,v8,v9\n"
MF"v[40:43],a[76:79],a[60:63],v[40:43]\n"
EX"v7,v7\n"
MF"v[40:43],a[72:75],a[56:59],v[40:43]\n"
BL"v225,s[8:11],s44 offen offset:0 sc1 lds\n"
MF"v[36:39],a[80:83],a[32:35],0\n"
SA"s40,s40,32768\n"
EX"v12,v12\n"
MF"v[36:39],a[84:87],a[36:39],v[36:39]\n"
"s_cmp_eq_u32 s40,s46\n"
RD"a[108:111],v226 offset:27648\n"
PK"v9,v10,v11\n"
MF"v[44:47],a[84:87],a[52:55],0\n"
"s_cselect_b32 s40,s47,s40\n"
EX"v13,v13\n"
MF"v[44:47],a[80:83],a[48:51],v[44:47]\n"
RD"a[112:115],v226 offset:28672\n"
RD"a[116:119],v226 offset:29696\n"
MF"v[36:39],a[88:91],a[40:43],v[36:39]\n"
WT"lgkmcnt(3)\n"
EX"v14,v14\n"
MF"v[36:39],a[92:95],a[44:47],v[36:39]\n"
RD"a[120:123],v226 offset:30720\n"
PK"v2,v4,v5\n"
MF"v[44:47],a[92:95],a[60:63],v[44:47]\n"
EX"v15,v15\n"
MF"v[44:47],a[88:91],a[56:59],v[44:47]\n"
BL"v225,s[8:11],s44 offen offset:1024 sc1 lds\n"
MF"v[48:51],a[96:99],a[32:35],0\n"
EX"v16,v16\n"
MF"v[48:51],a[100:103],a[36:39],v[48:51]\n"
RD"a[124:127],v226 offset:31744\n"
PK"v3,v6,v7\n"
MF"v[56:59],a[100:103],a[52:55],0\n"
WT"vmcnt(14)\n"
"s_barrier\n"
MF"v[56:59],a[96:99],a[48:51],v[56:59]\n"
RD"a[64:67],v227 offset:0\n"
RD"a[68:71],v227 offset:8192\n"
MF"v[48:51],a[104:107],a[40:43],v[48:51]\n"
WT"lgkmcnt(3)\n"
EX"v17,v17\n"
MF"v[48:51],a[108:111],a[44:47],v[48:51]\n"
RD"a[72:75],v227 offset:1024\n"
PK"v10,v12,v13\n"
MF"v[56:59],a[108:111],a[60:63],v[56:59]\n"
EX"v18,v18\n"
MF"v[56:59],a[104:107],a[56:59],v[56:59]\n"
BL"v225,s[8:11],s44 offen offset:2048 sc1 lds\n"
MF"v[52:55],a[112:115],a[32:35],0\n"
EX"v19,v19\n"
MF"v[52:55],a[116:119],a[36:39],v[52:55]\n"
RD"a[76:79],v227 offset:9216\n"
PK"v11,v14,v15\n"
MF"v[60:63],a[116:119],a[52:55],0\n"
EX"v24,v24\n"
MF"v[60:63],a[112:115],a[48:51],v[60:63]\n"
RD"a[80:83],v227 offset:2048\n"
RD"a[84:87],v227 offset:10240\n"
MF"v[52:55],a[120:123],a[40:43],v[52:55]\n"
WT"lgkmcnt(3)\n"
EX"v25,v25\n"
MF"v[52:55],a[124:127],a[44:47],v[52:55]\n"
RD"a[88:91],v227 offset:3072\n"
PK"v16,v16,v17\n"
MF"v[60:63],a[124:127],a[60:63],v[60:63]\n"
EX"v26,v26\n"
MF"v[60:63],a[120:123],a[56:59],v[60:63]\n"
SA"s41,s41,32768\n"
BL"v225,s[8:11],s44 offen offset:3072 sc1 lds\n"
MF"v[144:147],a[64:67],v[64:67],v[144:147]\n"
"s_cmp_eq_u32 s41,163840\n"
EX"v27,v27\n"
MF"v[144:147],a[68:71],v[72:75],v[144:147]\n"
"s_cselect_b32 s41,0,s41\n"
RD"a[92:95],v227 offset:11264\n"
"v_add_u32 v226,s41,v224\n"
MF"v[176:179],a[68:71],v[76:79],v[176:179]\n"
EX"v20,v20\n"
MF"v[176:179],a[64:67],v[68:71],v[176:179]\n"
"s_mov_b32 m0,s40\n"
RD"a[96:99],v227 offset:4096\n"
RD"a[100:103],v227 offset:12288\n"
MF"v[148:151],a[72:75],v[64:67],v[148:151]\n"
WT"lgkmcnt(3)\n"
EX"v21,v21\n"
MF"v[148:151],a[76:79],v[72:75],v[148:151]\n"
RD"a[104:107],v227 offset:5120\n"
"v_readlane_b32 s43,v228,s42\n"
MF"v[180:183],a[76:79],v[76:79],v[180:183]\n"
EX"v22,v22\n"
MF"v[180:183],a[72:75],v[68:71],v[180:183]\n"
"v_readlane_b32 s44,v229,s42\n"
PK"v17,v18,v19\n"
MF"v[152:155],a[80:83],v[64:67],v[152:155]\n"
SA"s42,s42,1\n"
BL"v225,s[8:11],s43 offen offset:0 sc1 lds\n"
MF"v[152:155],a[84:87],v[72:75],v[152:155]\n"
EX"v23,v23\n"
MF"v[184:187],a[84:87],v[76:79],v[184:187]\n"
RD"a[108:111],v227 offset:13312\n"
PK"v24,v24,v25\n"
MF"v[184:187],a[80:83],v[68:71],v[184:187]\n"
EX"v28,v28\n"
MF"v[156:159],a[88:91],v[64:67],v[156:159]\n"
WT"lgkmcnt(1)\n"
RD"a[112:115],v227 offset:6144\n"
RD"a[116:119],v227 offset:14336\n"
MF"v[156:159],a[92:95],v[72:75],v[156:159]\n"
EX"v29,v29\n"
MF"v[188:191],a[92:95],v[76:79],v[188:191]\n"
RD"a[120:123],v227 offset:7168\n"
PK"v25,v26,v27\n"
MF"v[188:191],a[88:91],v[68:71],v[188:191]\n"
EX"v30,v30\n"
MF"v[160:163],a[96:99],v[64:67],v[160:163]\n"
BL"v225,s[8:11],s43 offen offset:1024 sc1 lds\n"
MF"v[160:163],a[100:103],v[72:75],v[160:163]\n"
EX"v31,v31\n"
MF"v[192:195],a[100:103],v[76:79],v[192:195]\n"
RD"a[124:127],v227 offset:15360\n"
PK"v18,v20,v21\n"
MF"v[192:195],a[96:99],v[68:71],v[192:195]\n"
EX"v32,v32\n"
MF"v[164:167],a[104:107],v[64:67],v[164:167]\n"
WT"lgkmcnt(2)\n"
EX"v33,v33\n"
MF"v[164:167],a[108:111],v[72:75],v[164:167]\n"
RD"a[64:67],v227 offset:16384\n"
PK"v19,v22,v23\n"
MF"v[196:199],a[108:111],v[76:79],v[196:199]\n"
EX"v34,v34\n"
MF"v[196:199],a[104:107],v[68:71],v[196:199]\n"
BL"v225,s[8:11],s43 offen offset:2048 sc1 lds\n"
MF"v[168:171],a[112:115],v[64:67],v[168:171]\n"
EX"v35,v35\n"
MF"v[168:171],a[116:119],v[72:75],v[168:171]\n"
RD"a[68:71],v227 offset:24576\n"
PK"v26,v28,v29\n"
MF"v[200:203],a[116:119],v[76:79],v[200:203]\n"
EX"v40,v40\n"
MF"v[200:203],a[112:115],v[68:71],v[200:203]\n"
WT"lgkmcnt(2)\n"
RD"a[72:75],v227 offset:17408\n"
RD"a[76:79],v227 offset:25600\n"
MF"v[172:175],a[120:123],v[64:67],v[172:175]\n"
EX"v41,v41\n"
MF"v[172:175],a[124:127],v[72:75],v[172:175]\n"
RD"a[80:83],v227 offset:18432\n"
PK"v27,v30,v31\n"
MF"v[204:207],a[124:127],v[76:79],v[204:207]\n"
EX"v42,v42\n"
MF"v[204:207],a[120:123],v[68:71],v[204:207]\n"
BL"v225,s[8:11],s43 offen offset:3072 sc1 lds\n"
MF"a[140:143],a[128:131],v[64:67],a[140:143]\n"
EX"v43,v43\n"
MF"a[140:143],a[128:131],v[72:75],a[140:143]\n"
RD"a[84:87],v227 offset:26624\n"
PK"v64,v32,v33\n"
MF"a[144:147],a[128:131],v[76:79],a[144:147]\n"
EX"v36,v36\n"
MF"a[144:147],a[128:131],v[68:71],a[144:147]\n"
WT"lgkmcnt(2)\n"
RD"a[88:91],v227 offset:19456\n"
RD"a[92:95],v227 offset:27648\n"
MF"v[144:147],a[64:67],v[0:3],v[144:147]\n"
SA"m0,s40,16384\n"
EX"v37,v37\n"
MF"v[144:147],a[68:71],v[16:19],v[144:147]\n"
"buffer_load_dwordx2 v[242:243],v236,s[8:11],0 offen sc1\n"
MF"v[176:179],a[68:71],v[24:27],v[176:179]\n"
RD"a[96:99],v227 offset:20480\n"
PK"v65,v34,v35\n"
MF"v[176:179],a[64:67],v[8:11],v[176:179]\n"
EX"v38,v38\n"
MF"v[148:151],a[72:75],v[0:3],v[148:151]\n"
SA"s40,s40,32768\n"
BL"v225,s[8:11],s44 offen offset:0 sc1 lds\n"
MF"v[148:151],a[76:79],v[16:19],v[148:151]\n"
"s_cmp_eq_u32 s40,s46\n"
EX"v39,v39\n"
MF"v[180:183],a[76:79],v[24:27],v[180:183]\n"
"s_cselect_b32 s40,s47,s40\n"
RD"a[100:103],v227 offset:28672\n"
PK"v68,v40,v41\n"
MF"v[180:183],a[72:75],v[8:11],v[180:183]\n"
WT"lgkmcnt(2)\n"
EX"v44,v44\n"
MF"v[152:155],a[80:83],v[0:3],v[152:155]\n"
RD"a[104:107],v227 offset:21504\n"
RD"a[108:111],v227 offset:29696\n"
MF"v[152:155],a[84:87],v[16:19],v[152:155]\n"
EX"v45,v45\n"
MF"v[184:187],a[84:87],v[24:27],v[184:187]\n"
RD"a[112:115],v227 offset:22528\n"
PK"v69,v42,v43\n"
MF"v[184:187],a[80:83],v[8:11],v[184:187]\n"
EX"v46,v46\n"
MF"v[156:159],a[88:91],v[0:3],v[156:159]\n"
BL"v225,s[8:11],s44 offen offset:1024 sc1 lds\n"
MF"v[156:159],a[92:95],v[16:19],v[156:159]\n"
EX"v47,v47\n"
MF"v[188:191],a[92:95],v[24:27],v[188:191]\n"
RD"a[116:119],v227 offset:30720\n"
PK"v66,v36,v37\n"
MF"v[188:191],a[88:91],v[8:11],v[188:191]\n"
WT"lgkmcnt(2)\n"
EX"v48,v48\n"
MF"v[160:163],a[96:99],v[0:3],v[160:163]\n"
RD"a[120:123],v227 offset:23552\n"
RD"a[124:127],v227 offset:31744\n"
MF"v[160:163],a[100:103],v[16:19],v[160:163]\n"
EX"v49,v49\n"
MF"v[192:195],a[100:103],v[24:27],v[192:195]\n"
EX"v50,v50\n"
MF"v[192:195],a[96:99],v[8:11],v[192:195]\n"
BL"v225,s[8:11],s44 offen offset:2048 sc1 lds\n"
MF"v[164:167],a[104:107],v[0:3],v[164:167]\n"
WT"vmcnt(16)\n"
"s_barrier\n"
MF"v[164:167],a[108:111],v[16:19],v[164:167]\n"
RD"a[64:67],v226 offset:0\n"
PK"v67,v38,v39\n"
MF"v[196:199],a[108:111],v[24:27],v[196:199]\n"
EX"v51,v51\n"
MF"v[196:199],a[104:107],v[8:11],v[196:199]\n"
WT"lgkmcnt(1)\n"
RD"a[68:71],v226 offset:1024\n"
PK"v70,v44,v45\n"
MF"v[168:171],a[112:115],v[0:3],v[168:171]\n"
EX"v52,v52\n"
MF"v[168:171],a[116:119],v[16:19],v[168:171]\n"
RD"a[72:75],v226 offset:2048\n"
PK"v71,v46,v47\n"
MF"v[200:203],a[116:119],v[24:27],v[200:203]\n"
EX"v53,v53\n"
MF"v[200:203],a[112:115],v[8:11],v[200:203]\n"
BL"v225,s[8:11],s44 offen offset:3072 sc1 lds\n"
MF"v[172:175],a[120:123],v[0:3],v[172:175]\n"
EX"v54,v54\n"
MF"v[172:175],a[124:127],v[16:19],v[172:175]\n"
"buffer_store_dwordx2 v[232:233],v230,s[8:11],s57 offen sc1\n"
MF"v[204:207],a[124:127],v[24:27],v[204:207]\n"
"s_mov_b32 s57,2130706432\n"
RD"a[76:79],v226 offset:3072\n"
PK"v72,v48,v49\n"
MF"v[204:207],a[120:123],v[8:11],v[204:207]\n"
EX"v55,v55\n"
MF"a[140:143],a[128:131],v[0:3],a[140:143]\n"
RD"a[80:83],v226 offset:4096\n"
RD"a[84:87],v226 offset:5120\n"
MF"a[140:143],a[128:131],v[16:19],a[140:143]\n"
RD"a[88:91],v226 offset:6144\n"
PK"v73,v50,v51\n"
MF"a[144:147],a[128:131],v[24:27],a[144:147]\n"
PK"v74,v52,v53\n"
PK"v75,v54,v55\n"
MF"a[144:147],a[128:131],v[8:11],a[144:147]\n"
"s_sub_u32 s56,s56,1\n"
"s_cbranch_scc0 LP2_%=\n"
"s_cmp_lt_i32 s22,0\n"
"s_cbranch_scc1 LE2_%=\n"
WT"lgkmcnt(6)\n"
MF"v[0:3],a[64:67],a[32:35],0\n"
WT"lgkmcnt(5)\n"
WT"vmcnt(5)\n"
"v_cmp_eq_u64 vcc,s[28:29],v[242:243]\n"
"s_mov_b32 vcc_hi,-1\n"
"s_cmp_eq_u64 vcc,exec\n"
"s_cbranch_scc0 L1028_%=\n"
"L1029_%=:\n"
MF"v[0:3],a[68:71],a[36:39],v[0:3]\n"
SA"s48,s59,32768\n"
EX"v56,v56\n"
MF"v[8:11],a[68:71],a[52:55],0\n"
"s_and_b32 s48,s48,0x18000\n"
RD"a[92:95],v226 offset:7168\n"
"v_readlane_b32 s43,v228,s42\n"
MF"v[8:11],a[64:67],a[48:51],v[8:11]\n"
WT"lgkmcnt(5)\n"
EX"v57,v57\n"
MF"v[0:3],a[72:75],a[40:43],v[0:3]\n"
WT"lgkmcnt(1)\n"
RD"a[96:99],v226 offset:8192\n"
RD"a[100:103],v226 offset:9216\n"
MF"v[0:3],a[76:79],a[44:47],v[0:3]\n"
"s_andn2_b32 s49,s59,0x18000\n"
EX"v58,v58\n"
MF"v[8:11],a[76:79],a[60:63],v[8:11]\n"
"s_or_b32 s52,s48,s49\n"
RD"a[104:107],v226 offset:10240\n"
"v_readlane_b32 s44,v229,s42\n"
MF"v[8:11],a[72:75],a[56:59],v[8:11]\n"
SA"s53,s52,16384\n"
BL"a[0:3],v224,s[8:11],s52 offen offset:0 sc1\n"
MF"v[4:7],a[80:83],a[32:35],0\n"
"s_mov_b32 m0,s40\n"
EX"v59,v59\n"
MF"v[4:7],a[84:87],a[36:39],v[4:7]\n"
SA"s41,s41,32768\n"
BL"v225,s[8:11],s43 offen offset:0 sc1 lds\n"
MF"v[12:15],a[84:87],a[52:55],0\n"
"s_cmp_eq_u32 s41,163840\n"
EX"v60,v60\n"
MF"v[12:15],a[80:83],a[48:51],v[12:15]\n"
"s_cselect_b32 s41,0,s41\n"
RD"a[108:111],v226 offset:11264\n"
RD"a[112:115],v226 offset:12288\n"
PK"v76,v56,v57\n"
MF"v[4:7],a[88:91],a[40:43],v[4:7]\n"
WT"lgkmcnt(3)\n"
EX"v61,v61\n"
"v_add_u32 v227,s41,v224\n"
MF"v[4:7],a[92:95],a[44:47],v[4:7]\n"
SA"s42,s42,1\n"
BL"a[4:7],v224,s[8:11],s53 offen offset:0 sc1\n"
RD"a[116:119],v226 offset:13312\n"
MF"v[12:15],a[92:95],a[60:63],v[12:15]\n"
EX"v62,v62\n"
MF"v[12:15],a[88:91],a[56:59],v[12:15]\n"
RD"a[120:123],v226 offset:14336\n"
PK"v77,v58,v59\n"
MF"v[16:19],a[96:99],a[32:35],0\n"
EX"v63,v63\n"
MF"v[16:19],a[100:103],a[36:39],v[16:19]\n"
BL"v225,s[8:11],s43 offen offset:1024 sc1 lds\n"
MF"v[24:27],a[100:103],a[52:55],0\n"
EX"v0,v0\n"
MF"v[24:27],a[96:99],a[48:51],v[24:27]\n"
WT"lgkmcnt(2)\n"
BL"a[8:11],v224,s[8:11],s52 offen offset:1024 sc1\n"
RD"a[124:127],v226 offset:15360\n"
MF"v[16:19],a[104:107],a[40:43],v[16:19]\n"
EX"v1,v1\n"
PK"v78,v60,v61\n"
MF"v[16:19],a[108:111],a[44:47],v[16:19]\n"
RD"a[64:67],v226 offset:16384\n"
RD"a[68:71],v226 offset:17408\n"
MF"v[24:27],a[108:111],a[60:63],v[24:27]\n"
EX"v2,v2\n"
MF"v[24:27],a[104:107],a[56:59],v[24:27]\n"
RD"a[72:75],v226 offset:18432\n"
PK"v79,v62,v63\n"
MF"v[20:23],a[112:115],a[32:35],0\n"
WT"lgkmcnt(4)\n"
EX"v3,v3\n"
MF"v[20:23],a[116:119],a[36:39],v[20:23]\n"
BL"a[12:15],v224,s[8:11],s53 offen offset:1024 sc1\n"
BL"v225,s[8:11],s43 offen offset:2048 sc1 lds\n"
MF"v[28:31],a[116:119],a[52:55],0\n"
EX"v8,v8\n"
MF"v[28:31],a[112:115],a[48:51],v[28:31]\n"
RD"a[76:79],v226 offset:19456\n"
RD"a[80:83],v226 offset:20480\n"
PK"v0,v0,v1\n"
MF"v[20:23],a[120:123],a[40:43],v[20:23]\n"
WT"lgkmcnt(3)\n"
EX"v9,v9\n"
MF"v[20:23],a[124:127],a[44:47],v[20:23]\n"
RD"a[84:87],v226 offset:21504\n"
RD"a[88:91],v226 offset:22528\n"
MF"v[28:31],a[124:127],a[60:63],v[28:31]\n"
EX"v10,v10\n"
MF"v[28:31],a[120:123],a[56:59],v[28:31]\n"
BL"a[16:19],v224,s[8:11],s52 offen offset:2048 sc1\n"
PK"v1,v2,v3\n"
MF"v[32:35],a[64:67],a[32:35],0\n"
EX"v11,v11\n"
MF"v[32:35],a[68:71],a[36:39],v[32:35]\n"
BL"v225,s[8:11],s43 offen offset:3072 sc1 lds\n"
MF"v[40:43],a[68:71],a[52:55],0\n"
EX"v4,v4\n"
MF"v[40:43],a[64:67],a[48:51],v[40:43]\n"
WT"lgkmcnt(2)\n"
RD"a[92:95],v226 offset:23552\n"
RD"a[96:99],v226 offset:24576\n"
PK"v8,v8,v9\n"
MF"v[32:35],a[72:75],a[40:43],v[32:35]\n"
SA"m0,s40,16384\n"
EX"v5,v5\n"
MF"v[32:35],a[76:79],a[44:47],v[32:35]\n"
RD"a[100:103],v226 offset:25600\n"
RD"a[104:107],v226 offset:26624\n"
MF"v[40:43],a[76:79],a[60:63],v[40:43]\n"
BL"a[20:23],v224,s[8:11],s53 offen offset:2048 sc1\n"
EX"v6,v6\n"
MF"v[40:43],a[72:75],a[56:59],v[40:43]\n"
EX"v7,v7\n"
PK"v9,v10,v11\n"
MF"v[36:39],a[80:83],a[32:35],0\n"
WT"lgkmcnt(4)\n"
BL"v225,s[8:11],s44 offen offset:0 sc1 lds\n"
MF"v[36:39],a[84:87],a[36:39],v[36:39]\n"
SA"s40,s40,32768\n"
EX"v12,v12\n"
MF"v[44:47],a[84:87],a[52:55],0\n"
"s_cmp_eq_u32 s40,s46\n"
RD"a[108:111],v226 offset:27648\n"
PK"v2,v4,v5\n"
MF"v[44:47],a[80:83],a[48:51],v[44:47]\n"
"s_cselect_b32 s40,s47,s40\n"
BL"a[24:27],v224,s[8:11],s52 offen offset:3072 sc1\n"
MF"v[36:39],a[88:91],a[40:43],v[36:39]\n"
WT"lgkmcnt(1)\n"
EX"v13,v13\n"
MF"v[36:39],a[92:95],a[44:47],v[36:39]\n"
RD"a[112:115],v226 offset:28672\n"
RD"a[116:119],v226 offset:29696\n"
MF"v[44:47],a[92:95],a[60:63],v[44:47]\n"
EX"v14,v14\n"
MF"v[44:47],a[88:91],a[56:59],v[44:47]\n"
RD"a[120:123],v226 offset:30720\n"
PK"v3,v6,v7\n"
MF"v[48:51],a[96:99],a[32:35],0\n"
EX"v15,v15\n"
MF"v[48:51],a[100:103],a[36:39],v[48:51]\n"
BL"v225,s[8:11],s44 offen offset:1024 sc1 lds\n"
MF"v[56:59],a[100:103],a[52:55],0\n"
SA"s48,s59,65536\n"
EX"v16,v16\n"
MF"v[56:59],a[96:99],a[48:51],v[56:59]\n"
"s_and_b32 s48,s48,0x18000\n"
BL"a[28:31],v224,s[8:11],s53 offen offset:3072 sc1\n"
RD"a[124:127],v226 offset:31744\n"
MF"v[48:51],a[104:107],a[40:43],v[48:51]\n"
WT"lgkmcnt(2)\n"
"s_barrier\n"
MF"v[48:51],a[108:111],a[44:47],v[48:51]\n"
"s_andn2_b32 s49,s59,0x18000\n"
RD"a[64:67],v227 offset:0\n"
RD"a[68:71],v227 offset:8192\n"
PK"v10,v12,v13\n"
MF"v[56:59],a[108:111],a[60:63],v[56:59]\n"
"s_or_b32 s52,s48,s49\n"
EX"v17,v17\n"
MF"v[56:59],a[104:107],a[56:59],v[56:59]\n"
SA"s53,s52,16384\n"
RD"a[72:75],v227 offset:1024\n"
PK"v11,v14,v15\n"
MF"v[52:55],a[112:115],a[32:35],0\n"
EX"v18,v18\n"
MF"v[52:55],a[116:119],a[36:39],v[52:55]\n"
BL"a[180:183],v224,s[8:11],s52 offen offset:0 sc1\n"
BL"v225,s[8:11],s44 offen offset:2048 sc1 lds\n"
MF"v[60:63],a[116:119],a[52:55],0\n"
EX"v19,v19\n"
MF"v[60:63],a[112:115],a[48:51],v[60:63]\n"
WT"lgkmcnt(3)\n"
RD"a[76:79],v227 offset:9216\n"
RD"a[80:83],v227 offset:2048\n"
MF"v[52:55],a[120:123],a[40:43],v[52:55]\n"
EX"v24,v24\n"
MF"v[52:55],a[124:127],a[44:47],v[52:55]\n"
RD"a[84:87],v227 offset:10240\n"
RD"a[88:91],v227 offset:3072\n"
PK"v16,v16,v17\n"
MF"v[60:63],a[124:127],a[60:63],v[60:63]\n"
EX"v25,v25\n"
MF"v[60:63],a[120:123],a[56:59],v[60:63]\n"
WT"lgkmcnt(4)\n"
BL"a[184:187],v224,s[8:11],s53 offen offset:0 sc1\n"
MF"v[144:147],a[64:67],v[64:67],v[144:147]\n"
SA"s41,s41,32768\n"
EX"v26,v26\n"
MF"v[144:147],a[68:71],v[72:75],v[144:147]\n"
"s_cmp_eq_u32 s41,163840\n"
BL"v225,s[8:11],s44 offen offset:3072 sc1 lds\n"
PK"v17,v18,v19\n"
MF"v[176:179],a[68:71],v[76:79],v[176:179]\n"
"s_cselect_b32 s41,0,s41\n"
EX"v27,v27\n"
MF"v[176:179],a[64:67],v[68:71],v[176:179]\n"
"s_mov_b32 m0,s40\n"
RD"a[92:95],v227 offset:11264\n"
RD"a[96:99],v227 offset:4096\n"
MF"v[148:151],a[72:75],v[64:67],v[148:151]\n"
WT"lgkmcnt(2)\n"
EX"v20,v20\n"
MF"v[148:151],a[76:79],v[72:75],v[148:151]\n"
BL"a[188:191],v224,s[8:11],s52 offen offset:1024 sc1\n"
RD"a[100:103],v227 offset:12288\n"
MF"v[180:183],a[76:79],v[76:79],v[180:183]\n"
EX"v21,v21\n"
PK"v24,v24,v25\n"
MF"v[180:183],a[72:75],v[68:71],v[180:183]\n"
RD"a[104:107],v227 offset:5120\n"
"v_add_u32 v226,s41,v224\n"
"v_readlane_b32 s43,v228,s42\n"
MF"v[152:155],a[80:83],v[64:67],v[152:155]\n"
EX"v22,v22\n"
MF"v[152:155],a[84:87],v[72:75],v[152:155]\n"
EX"v23,v23\n"
PK"v25,v26,v27\n"
MF"v[184:187],a[84:87],v[76:79],v[184:187]\n"
BL"v225,s[8:11],s43 offen offset:0 sc1 lds\n"
MF"v[184:187],a[80:83],v[68:71],v[184:187]\n"
RD"a[108:111],v227 offset:13312\n"
RD"a[112:115],v227 offset:6144\n"
MF"v[156:159],a[88:91],v[64:67],v[156:159]\n"
WT"lgkmcnt(3)\n"
BL"a[192:195],v224,s[8:11],s53 offen offset:1024 sc1\n"
EX"v28,v28\n"
MF"v[156:159],a[92:95],v[72:75],v[156:159]\n"
RD"a[116:119],v227 offset:14336\n"
RD"a[120:123],v227 offset:7168\n"
"v_readlane_b32 s44,v229,s42\n"
MF"v[188:191],a[92:95],v[76:79],v[188:191]\n"
SA"s42,s42,1\n"
EX"v29,v29\n"
PK"v18,v20,v21\n"
MF"v[188:191],a[88:91],v[68:71],v[188:191]\n"
EX"v30,v30\n"
MF"v[160:163],a[96:99],v[64:67],v[160:163]\n"
BL"v225,s[8:11],s43 offen offset:1024 sc1 lds\n"
MF"v[160:163],a[100:103],v[72:75],v[160:163]\n"
BL"a[196:199],v224,s[8:11],s52 offen offset:2048 sc1\n"
PK"v19,v22,v23\n"
MF"v[192:195],a[100:103],v[76:79],v[192:195]\n"
EX"v31,v31\n"
MF"v[192:195],a[96:99],v[68:71],v[192:195]\n"
WT"lgkmcnt(2)\n"
RD"a[124:127],v227 offset:15360\n"
PK"v26,v28,v29\n"
MF"v[164:167],a[104:107],v[64:67],v[164:167]\n"
EX"v32,v32\n"
MF"v[164:167],a[108:111],v[72:75],v[164:167]\n"
EX"v33,v33\n"
MF"v[196:199],a[108:111],v[76:79],v[196:199]\n"
RD"a[64:67],v227 offset:16384\n"
PK"v27,v30,v31\n"
MF"v[196:199],a[104:107],v[68:71],v[196:199]\n"
BL"a[200:203],v224,s[8:11],s53 offen offset:2048 sc1\n"
MF"v[168:171],a[112:115],v[64:67],v[168:171]\n"
WT"lgkmcnt(2)\n"
EX"v34,v34\n"
MF"v[168:171],a[116:119],v[72:75],v[168:171]\n"
BL"v225,s[8:11],s43 offen offset:2048 sc1 lds\n"
MF"v[200:203],a[116:119],v[76:79],v[200:203]\n"
EX"v35,v35\n"
MF"v[200:203],a[112:115],v[68:71],v[200:203]\n"
RD"a[68:71],v227 offset:24576\n"
RD"a[72:75],v227 offset:17408\n"
MF"v[172:175],a[120:123],v[64:67],v[172:175]\n"
WT"lgkmcnt(3)\n"
EX"v40,v40\n"
MF"v[172:175],a[124:127],v[72:75],v[172:175]\n"
RD"a[76:79],v227 offset:25600\n"
RD"a[80:83],v227 offset:18432\n"
MF"v[204:207],a[124:127],v[76:79],v[204:207]\n"
EX"v41,v41\n"
MF"v[204:207],a[120:123],v[68:71],v[204:207]\n"
BL"a[204:207],v224,s[8:11],s52 offen offset:3072 sc1\n"
MF"a[140:143],a[128:131],v[64:67],a[140:143]\n"
EX"v42,v42\n"
PK"v64,v32,v33\n"
MF"a[140:143],a[128:131],v[72:75],a[140:143]\n"
BL"v225,s[8:11],s43 offen offset:3072 sc1 lds\n"
PK"v65,v34,v35\n"
MF"a[144:147],a[128:131],v[76:79],a[144:147]\n"
EX"v43,v43\n"
MF"a[144:147],a[128:131],v[68:71],a[144:147]\n"
WT"lgkmcnt(2)\n"
RD"a[84:87],v227 offset:26624\n"
RD"a[88:91],v227 offset:19456\n"
MF"v[144:147],a[64:67],v[0:3],v[144:147]\n"
SA"m0,s40,16384\n"
EX"v36,v36\n"
MF"v[144:147],a[68:71],v[16:19],v[144:147]\n"
SA"s48,s59,98304\n"
BL"a[208:211],v224,s[8:11],s53 offen offset:3072 sc1\n"
RD"a[92:95],v227 offset:27648\n"
MF"v[176:179],a[68:71],v[24:27],v[176:179]\n"
"s_and_b32 s48,s48,0x18000\n"
"buffer_load_dwordx2 v[242:243],v236,s[8:11],0 offen sc1\n"
EX"v37,v37\n"
MF"v[176:179],a[64:67],v[8:11],v[176:179]\n"
"s_andn2_b32 s49,s59,0x18000\n"
RD"a[96:99],v227 offset:20480\n"
PK"v68,v40,v41\n"
MF"v[148:151],a[72:75],v[0:3],v[148:151]\n"
WT"lgkmcnt(4)\n"
EX"v38,v38\n"
MF"v[148:151],a[76:79],v[16:19],v[148:151]\n"
"s_or_b32 s52,s48,s49\n"
BL"v225,s[8:11],s44 offen offset:0 sc1 lds\n"
PK"v69,v42,v43\n"
MF"v[180:183],a[76:79],v[24:27],v[180:183]\n"
SA"s53,s52,16384\n"
EX"v39,v39\n"
MF"v[180:183],a[72:75],v[8:11],v[180:183]\n"
SA"s40,s40,32768\n"
BL"a[212:215],v224,s[8:11],s52 offen offset:0 sc1\n"
RD"a[100:103],v227 offset:28672\n"
MF"v[152:155],a[80:83],v[0:3],v[152:155]\n"
WT"lgkmcnt(2)\n"
EX"v44,v44\n"
MF"v[152:155],a[84:87],v[16:19],v[152:155]\n"
"s_cmp_eq_u32 s40,s46\n"
RD"a[104:107],v227 offset:21504\n"
RD"a[108:111],v227 offset:29696\n"
PK"v66,v36,v37\n"
MF"v[184:187],a[84:87],v[24:27],v[184:187]\n"
"s_cselect_b32 s40,s47,s40\n"
EX"v45,v45\n"
MF"v[184:187],a[80:83],v[8:11],v[184:187]\n"
RD"a[112:115],v227 offset:22528\n"
PK"v67,v38,v39\n"
MF"v[156:159],a[88:91],v[0:3],v[156:159]\n"
EX"v46,v46\n"
MF"v[156:159],a[92:95],v[16:19],v[156:159]\n"
BL"v225,s[8:11],s44 offen offset:1024 sc1 lds\n"
MF"v[188:191],a[92:95],v[24:27],v[188:191]\n"
BL"a[216:219],v224,s[8:11],s53 offen offset:0 sc1\n"
EX"v47,v47\n"
MF"v[188:191],a[88:91],v[8:11],v[188:191]\n"
WT"lgkmcnt(3)\n"
RD"a[116:119],v227 offset:30720\n"
RD"a[120:123],v227 offset:23552\n"
MF"v[160:163],a[96:99],v[0:3],v[160:163]\n"
EX"v48,v48\n"
MF"v[160:163],a[100:103],v[16:19],v[160:163]\n"
RD"a[124:127],v227 offset:31744\n"
PK"v70,v44,v45\n"
MF"v[192:195],a[100:103],v[24:27],v[192:195]\n"
EX"v49,v49\n"
MF"v[192:195],a[96:99],v[8:11],v[192:195]\n"
WT"lgkmcnt(3)\n"
BL"a[220:223],v224,s[8:11],s52 offen offset:1024 sc1\n"
MF"v[164:167],a[104:107],v[0:3],v[164:167]\n"
EX"v50,v50\n"
MF"v[164:167],a[108:111],v[16:19],v[164:167]\n"
BL"v225,s[8:11],s44 offen offset:2048 sc1 lds\n"
PK"v71,v46,v47\n"
MF"v[196:199],a[108:111],v[24:27],v[196:199]\n"
WT"vmcnt(36)\n"
"s_barrier\n"
MF"v[196:199],a[104:107],v[8:11],v[196:199]\n"
RD"a[64:67],v226 offset:0\n"
RD"a[68:71],v226 offset:1024\n"
MF"v[168:171],a[112:115],v[0:3],v[168:171]\n"
WT"lgkmcnt(2)\n"
EX"v51,v51\n"
MF"v[168:171],a[116:119],v[16:19],v[168:171]\n"
BL"a[224:227],v224,s[8:11],s53 offen offset:1024 sc1\n"
PK"v72,v48,v49\n"
MF"v[200:203],a[116:119],v[24:27],v[200:203]\n"
EX"v52,v52\n"
MF"v[200:203],a[112:115],v[8:11],v[200:203]\n"
RD"a[72:75],v226 offset:2048\n"
PK"v73,v50,v51\n"
MF"v[172:175],a[120:123],v[0:3],v[172:175]\n"
EX"v53,v53\n"
MF"v[172:175],a[124:127],v[16:19],v[172:175]\n"
BL"v225,s[8:11],s44 offen offset:3072 sc1 lds\n"
MF"v[204:207],a[124:127],v[24:27],v[204:207]\n"
EX"v54,v54\n"
MF"v[204:207],a[120:123],v[8:11],v[204:207]\n"
RD"a[76:79],v226 offset:3072\n"
RD"a[80:83],v226 offset:4096\n"
MF"a[140:143],a[128:131],v[0:3],a[140:143]\n"
EX"v55,v55\n"
MF"a[140:143],a[128:131],v[16:19],a[140:143]\n"
RD"a[84:87],v226 offset:5120\n"
RD"a[88:91],v226 offset:6144\n"
MF"a[144:147],a[128:131],v[24:27],a[144:147]\n"
PK"v74,v52,v53\n"
PK"v75,v54,v55\n"
MF"a[144:147],a[128:131],v[8:11],a[144:147]\n"
"s_sub_u32 s56,s22,1\n"
"s_mov_b32 s22,-1\n"
"s_cbranch_scc0 LP2_%=\n"
"LE2_%=:\n"
WT"lgkmcnt(6)\n"
MF"v[0:3],a[64:67],a[32:35],v[208:211]\n"
WT"lgkmcnt(5)\n"
WT"vmcnt(5)\n"
"v_cmp_eq_u64 vcc,s[28:29],v[242:243]\n"
"s_mov_b32 vcc_lo,-1\n"
"s_cmp_eq_u64 vcc,exec\n"
"s_cbranch_scc0 L1031_%=\n"
"L1032_%=:\n"
MF"v[0:3],a[68:71],a[36:39],v[0:3]\n"
"s_mov_b32 m0,s40\n"
EX"v56,v56\n"
MF"v[8:11],a[68:71],a[52:55],0\n"
EX"v57,v57\n"
MF"v[8:11],a[64:67],a[48:51],v[8:11]\n"
WT"lgkmcnt(4)\n"
RD"a[92:95],v226 offset:7168\n"
RD"a[96:99],v226 offset:8192\n"
"v_readlane_b32 s43,v228,s42\n"
MF"v[0:3],a[72:75],a[40:43],v[0:3]\n"
WT"lgkmcnt(2)\n"
EX"v58,v58\n"
"v_readlane_b32 s44,v229,s42\n"
MF"v[0:3],a[76:79],a[44:47],v[0:3]\n"
SA"s42,s42,1\n"
BL"v225,s[8:11],s43 offen offset:0 sc1 lds\n"
RD"a[100:103],v226 offset:9216\n"
MF"v[8:11],a[76:79],a[60:63],v[8:11]\n"
SA"s41,s41,32768\n"
EX"v59,v59\n"
PK"v76,v56,v57\n"
MF"v[8:11],a[72:75],a[56:59],v[8:11]\n"
"s_cmp_eq_u32 s41,163840\n"
RD"a[104:107],v226 offset:10240\n"
PK"v77,v58,v59\n"
MF"v[4:7],a[80:83],a[32:35],v[212:215]\n"
"s_cselect_b32 s41,0,s41\n"
EX"v60,v60\n"
"v_add_u32 v227,s41,v224\n"
MF"v[4:7],a[84:87],a[36:39],v[4:7]\n"
BL"v225,s[8:11],s43 offen offset:1024 sc1 lds\n"
MF"v[12:15],a[84:87],a[52:55],0\n"
EX"v61,v61\n"
MF"v[12:15],a[80:83],a[48:51],v[12:15]\n"
RD"a[108:111],v226 offset:11264\n"
RD"a[112:115],v226 offset:12288\n"
MF"v[4:7],a[88:91],a[40:43],v[4:7]\n"
WT"lgkmcnt(3)\n"
EX"v62,v62\n"
MF"v[4:7],a[92:95],a[44:47],v[4:7]\n"
RD"a[116:119],v226 offset:13312\n"
RD"a[120:123],v226 offset:14336\n"
PK"v78,v60,v61\n"
MF"v[12:15],a[92:95],a[60:63],v[12:15]\n"
EX"v63,v63\n"
MF"v[12:15],a[88:91],a[56:59],v[12:15]\n"
BL"v225,s[8:11],s43 offen offset:2048 sc1 lds\n"
MF"v[16:19],a[96:99],a[32:35],v[216:219]\n"
EX"v0,v0\n"
MF"v[16:19],a[100:103],a[36:39],v[16:19]\n"
RD"a[124:127],v226 offset:15360\n"
RD"a[64:67],v226 offset:16384\n"
PK"v79,v62,v63\n"
MF"v[24:27],a[100:103],a[52:55],0\n"
EX"v1,v1\n"
MF"v[24:27],a[96:99],a[48:51],v[24:27]\n"
WT"lgkmcnt(4)\n"
RD"a[68:71],v226 offset:17408\n"
RD"a[72:75],v226 offset:18432\n"
MF"v[16:19],a[104:107],a[40:43],v[16:19]\n"
BL"v225,s[8:11],s43 offen offset:3072 sc1 lds\n"
MF"v[16:19],a[108:111],a[44:47],v[16:19]\n"
SA"m0,s40,16384\n"
EX"v2,v2\n"
PK"v0,v0,v1\n"
MF"v[24:27],a[108:111],a[60:63],v[24:27]\n"
RD"a[76:79],v226 offset:19456\n"
RD"a[80:83],v226 offset:20480\n"
MF"v[24:27],a[104:107],a[56:59],v[24:27]\n"
EX"v3,v3\n"
MF"v[20:23],a[112:115],a[32:35],v[220:223]\n"
WT"lgkmcnt(4)\n"
RD"a[84:87],v226 offset:21504\n"
RD"a[88:91],v226 offset:22528\n"
MF"v[20:23],a[116:119],a[36:39],v[20:23]\n"
EX"v8,v8\n"
PK"v1,v2,v3\n"
MF"v[28:31],a[116:119],a[52:55],0\n"
SA"s40,s40,32768\n"
BL"v225,s[8:11],s44 offen offset:0 sc1 lds\n"
MF"v[28:31],a[112:115],a[48:51],v[28:31]\n"
"s_cmp_eq_u32 s40,s46\n"
RD"a[92:95],v226 offset:23552\n"
RD"a[96:99],v226 offset:24576\n"
EX"v9,v9\n"
MF"v[20:23],a[120:123],a[40:43],v[20:23]\n"
"s_cselect_b32 s40,s47,s40\n"
EX"v10,v10\n"
MF"v[20:23],a[124:127],a[44:47],v[20:23]\n"
RD"a[100:103],v226 offset:25600\n"
RD"a[104:107],v226 offset:26624\n"
PK"v8,v8,v9\n"
MF"v[28:31],a[124:127],a[60:63],v[28:31]\n"
RD"a[108:111],v226 offset:27648\n"
RD"a[112:115],v226 offset:28672\n"
EX"v11,v11\n"
MF"v[28:31],a[120:123],a[56:59],v[28:31]\n"
BL"v225,s[8:11],s44 offen offset:1024 sc1 lds\n"
MF"v[40:43],a[64:67],a[48:51],v[208:211]\n"
WT"lgkmcnt(6)\n"
RD"a[116:119],v226 offset:29696\n"
RD"a[120:123],v226 offset:30720\n"
EX"v4,v4\n"
MF"v[40:43],a[68:71],a[52:55],v[40:43]\n"
EX"v5,v5\n"
PK"v9,v10,v11\n"
MF"v[40:43],a[72:75],a[56:59],v[40:43]\n"
RD"a[124:127],v226 offset:31744\n"
PK"v2,v4,v5\n"
MF"v[40:43],a[76:79],a[60:63],v[40:43]\n"
EX"v6,v6\n"
MF"v[44:47],a[80:83],a[48:51],v[212:215]\n"
"s_barrier\n"
MF"v[44:47],a[84:87],a[52:55],v[44:47]\n"
RD"a[64:67],v227 offset:0\n"
RD"a[68:71],v227 offset:8192\n"
MF"v[44:47],a[88:91],a[56:59],v[44:47]\n"
WT"lgkmcnt(5)\n"
BL"v225,s[8:11],s44 offen offset:2048 sc1 lds\n"
EX"v7,v7\n"
MF"v[44:47],a[92:95],a[60:63],v[44:47]\n"
RD"a[72:75],v227 offset:1024\n"
"v_readlane_b32 s43,v228,s42\n"
MF"v[56:59],a[96:99],a[48:51],v[216:219]\n"
EX"v12,v12\n"
PK"v3,v6,v7\n"
MF"v[56:59],a[100:103],a[52:55],v[56:59]\n"
EX"v13,v13\n"
MF"v[56:59],a[104:107],a[56:59],v[56:59]\n"
BL"v225,s[8:11],s44 offen offset:3072 sc1 lds\n"
MF"v[56:59],a[108:111],a[60:63],v[56:59]\n"
"s_mov_b32 m0,s40\n"
RD"a[76:79],v227 offset:9216\n"
RD"a[80:83],v227 offset:2048\n"
MF"v[60:63],a[112:115],a[48:51],v[220:223]\n"
WT"lgkmcnt(3)\n"
EX"v14,v14\n"
PK"v10,v12,v13\n"
MF"v[60:63],a[116:119],a[52:55],v[60:63]\n"
RD"a[84:87],v227 offset:10240\n"
RD"a[88:91],v227 offset:3072\n"
MF"v[60:63],a[120:123],a[56:59],v[60:63]\n"
EX"v15,v15\n"
MF"v[60:63],a[124:127],a[60:63],v[60:63]\n"
SA"s41,s41,32768\n"
EX"v16,v16\n"
MF"v[144:147],a[64:67],v[64:67],v[144:147]\n"
"s_cmp_eq_u32 s41,163840\n"
BL"v225,s[8:11],s43 offen offset:0 sc1 lds\n"
"v_readlane_b32 s44,v229,s42\n"
MF"v[144:147],a[68:71],v[72:75],v[144:147]\n"
"s_cselect_b32 s41,0,s41\n"
EX"v17,v17\n"
PK"v11,v14,v15\n"
MF"v[176:179],a[68:71],v[76:79],v[176:179]\n"
SA"s42,s42,1\n"
RD"a[92:95],v227 offset:11264\n"
"v_add_u32 v226,s41,v224\n"
MF"v[176:179],a[64:67],v[68:71],v[176:179]\n"
WT"lgkmcnt(3)\n"
EX"v18,v18\n"
MF"v[148:151],a[72:75],v[64:67],v[148:151]\n"
RD"a[96:99],v227 offset:4096\n"
RD"a[100:103],v227 offset:12288\n"
PK"v16,v16,v17\n"
MF"v[148:151],a[76:79],v[72:75],v[148:151]\n"
EX"v19,v19\n"
MF"v[180:183],a[76:79],v[76:79],v[180:183]\n"
BL"v225,s[8:11],s43 offen offset:1024 sc1 lds\n"
MF"v[180:183],a[72:75],v[68:71],v[180:183]\n"
RD"a[104:107],v227 offset:5120\n"
PK"v17,v18,v19\n"
MF"v[152:155],a[80:83],v[64:67],v[152:155]\n"
WT"lgkmcnt(4)\n"
EX"v24,v24\n"
MF"v[152:155],a[84:87],v[72:75],v[152:155]\n"
EX"v25,v25\n"
MF"v[184:187],a[84:87],v[76:79],v[184:187]\n"
EX"v26,v26\n"
MF"v[184:187],a[80:83],v[68:71],v[184:187]\n"
RD"a[108:111],v227 offset:13312\n"
RD"a[112:115],v227 offset:6144\n"
PK"v24,v24,v25\n"
MF"v[156:159],a[88:91],v[64:67],v[156:159]\n"
WT"lgkmcnt(3)\n"
BL"v225,s[8:11],s43 offen offset:2048 sc1 lds\n"
MF"v[156:159],a[92:95],v[72:75],v[156:159]\n"
EX"v27,v27\n"
MF"v[188:191],a[92:95],v[76:79],v[188:191]\n"
RD"a[116:119],v227 offset:14336\n"
RD"a[120:123],v227 offset:7168\n"
MF"v[188:191],a[88:91],v[68:71],v[188:191]\n"
EX"v20,v20\n"
PK"v25,v26,v27\n"
MF"v[160:163],a[96:99],v[64:67],v[160:163]\n"
EX"v21,v21\n"
MF"v[160:163],a[100:103],v[72:75],v[160:163]\n"
BL"v225,s[8:11],s43 offen offset:3072 sc1 lds\n"
MF"v[192:195],a[100:103],v[76:79],v[192:195]\n"
SA"m0,s40,16384\n"
EX"v22,v22\n"
MF"v[192:195],a[96:99],v[68:71],v[192:195]\n"
WT"lgkmcnt(2)\n"
RD"a[124:127],v227 offset:15360\n"
PK"v18,v20,v21\n"
MF"v[164:167],a[104:107],v[64:67],v[164:167]\n"
EX"v23,v23\n"
MF"v[164:167],a[108:111],v[72:75],v[164:167]\n"
EX"v28,v28\n"
MF"v[196:199],a[108:111],v[76:79],v[196:199]\n"
SA"s40,s40,32768\n"
BL"v225,s[8:11],s44 offen offset:0 sc1 lds\n"
MF"v[196:199],a[104:107],v[68:71],v[196:199]\n"
"s_cmp_eq_u32 s40,s46\n"
RD"a[64:67],v227 offset:16384\n"
PK"v19,v22,v23\n"
MF"v[168:171],a[112:115],v[64:67],v[168:171]\n"
WT"lgkmcnt(2)\n"
EX"v29,v29\n"
MF"v[168:171],a[116:119],v[72:75],v[168:171]\n"
"s_cselect_b32 s40,s47,s40\n"
EX"v30,v30\n"
MF"v[200:203],a[116:119],v[76:79],v[200:203]\n"
RD"a[68:71],v227 offset:24576\n"
PK"v26,v28,v29\n"
MF"v[200:203],a[112:115],v[68:71],v[200:203]\n"
BL"v225,s[8:11],s44 offen offset:1024 sc1 lds\n"
MF"v[172:175],a[120:123],v[64:67],v[172:175]\n"
WT"lgkmcnt(2)\n"
EX"v31,v31\n"
MF"v[172:175],a[124:127],v[72:75],v[172:175]\n"
RD"a[72:75],v227 offset:17408\n"
RD"a[76:79],v227 offset:25600\n"
MF"v[204:207],a[124:127],v[76:79],v[204:207]\n"
EX"v40,v40\n"
MF"v[204:207],a[120:123],v[68:71],v[204:207]\n"
RD"a[80:83],v227 offset:18432\n"
PK"v27,v30,v31\n"
MF"a[140:143],a[128:131],v[64:67],a[140:143]\n"
EX"v41,v41\n"
MF"a[140:143],a[128:131],v[72:75],a[140:143]\n"
BL"v225,s[8:11],s44 offen offset:2048 sc1 lds\n"
MF"a[144:147],a[128:131],v[76:79],a[144:147]\n"
EX"v42,v42\n"
MF"a[144:147],a[128:131],v[68:71],a[144:147]\n"
WT"lgkmcnt(3)\n"
RD"a[84:87],v227 offset:26624\n"
RD"a[88:91],v227 offset:19456\n"
PK"v68,v40,v41\n"
MF"v[144:147],a[64:67],v[0:3],v[144:147]\n"
EX"v43,v43\n"
MF"v[144:147],a[68:71],v[16:19],v[144:147]\n"
RD"a[92:95],v227 offset:27648\n"
RD"a[96:99],v227 offset:20480\n"
MF"v[176:179],a[68:71],v[24:27],v[176:179]\n"
EX"v44,v44\n"
"v_readlane_b32 s43,v228,s42\n"
MF"v[176:179],a[64:67],v[8:11],v[176:179]\n"
WT"lgkmcnt(4)\n"
BL"v225,s[8:11],s44 offen offset:3072 sc1 lds\n"
PK"v69,v42,v43\n"
MF"v[148:151],a[72:75],v[0:3],v[148:151]\n"
"s_mov_b32 m0,s40\n"
EX"v45,v45\n"
MF"v[148:151],a[76:79],v[16:19],v[148:151]\n"
EX"v46,v46\n"
MF"v[180:183],a[76:79],v[24:27],v[180:183]\n"
RD"a[100:103],v227 offset:28672\n"
"v_readlane_b32 s44,v229,s42\n"
PK"v70,v44,v45\n"
MF"v[180:183],a[72:75],v[8:11],v[180:183]\n"
SA"s42,s42,1\n"
EX"v47,v47\n"
MF"v[152:155],a[80:83],v[0:3],v[152:155]\n"
WT"lgkmcnt(1)\n"
RD"a[104:107],v227 offset:21504\n"
RD"a[108:111],v227 offset:29696\n"
MF"v[152:155],a[84:87],v[16:19],v[152:155]\n"
EX"v56,v56\n"
MF"v[184:187],a[84:87],v[24:27],v[184:187]\n"
RD"a[112:115],v227 offset:22528\n"
PK"v71,v46,v47\n"
MF"v[184:187],a[80:83],v[8:11],v[184:187]\n"
EX"v57,v57\n"
MF"v[156:159],a[88:91],v[0:3],v[156:159]\n"
EX"v58,v58\n"
MF"v[156:159],a[92:95],v[16:19],v[156:159]\n"
RD"a[116:119],v227 offset:30720\n"
PK"v76,v56,v57\n"
MF"v[188:191],a[92:95],v[24:27],v[188:191]\n"
EX"v59,v59\n"
MF"v[188:191],a[88:91],v[8:11],v[188:191]\n"
RD"a[120:123],v227 offset:23552\n"
RD"a[124:127],v227 offset:31744\n"
MF"v[160:163],a[96:99],v[0:3],v[160:163]\n"
WT"lgkmcnt(4)\n"
EX"v60,v60\n"
MF"v[160:163],a[100:103],v[16:19],v[160:163]\n"
PK"v77,v58,v59\n"
MF"v[192:195],a[100:103],v[24:27],v[192:195]\n"
EX"v61,v61\n"
MF"v[192:195],a[96:99],v[8:11],v[192:195]\n"
PK"v78,v60,v61\n"
MF"v[164:167],a[104:107],v[0:3],v[164:167]\n"
EX"v62,v62\n"
MF"v[164:167],a[108:111],v[16:19],v[164:167]\n"
SA"s41,s41,32768\n"
WT"vmcnt(16)\n"
"s_barrier\n"
MF"v[196:199],a[108:111],v[24:27],v[196:199]\n"
"s_cmp_eq_u32 s41,163840\n"
BL"v225,s[8:11],s43 offen offset:0 sc1 lds\n"
BL"v225,s[8:11],s43 offen offset:1024 sc1 lds\n"
MF"v[196:199],a[104:107],v[8:11],v[196:199]\n"
WT"lgkmcnt(0)\n"
BL"v225,s[8:11],s43 offen offset:2048 sc1 lds\n"
RD"a[64:67],v226 offset:0\n"
MF"v[168:171],a[112:115],v[0:3],v[168:171]\n"
"s_cselect_b32 s41,0,s41\n"
EX"v63,v63\n"
"v_add_u32 v227,s41,v224\n"
MF"v[168:171],a[116:119],v[16:19],v[168:171]\n"
RD"a[68:71],v226 offset:8192\n"
RD"a[72:75],v226 offset:1024\n"
BL"v225,s[8:11],s43 offen offset:3072 sc1 lds\n"
MF"v[200:203],a[116:119],v[24:27],v[200:203]\n"
SA"m0,s40,16384\n"
RD"a[76:79],v226 offset:9216\n"
RD"a[80:83],v226 offset:2048\n"
PK"v79,v62,v63\n"
MF"v[200:203],a[112:115],v[8:11],v[200:203]\n"
RD"a[84:87],v226 offset:10240\n"
MF"v[172:175],a[120:123],v[0:3],v[172:175]\n"
RD"a[88:91],v226 offset:3072\n"
MF"v[172:175],a[124:127],v[16:19],v[172:175]\n"
BL"v225,s[8:11],s44 offen offset:0 sc1 lds\n"
MF"v[204:207],a[124:127],v[24:27],v[204:207]\n"
SA"s40,s40,32768\n"
RD"a[92:95],v226 offset:11264\n"
RD"a[96:99],v226 offset:4096\n"
MF"v[204:207],a[120:123],v[8:11],v[204:207]\n"
"s_cmp_eq_u32 s40,s46\n"
RD"a[100:103],v226 offset:12288\n"
RD"a[104:107],v226 offset:5120\n"
MF"a[140:143],a[128:131],v[0:3],a[140:143]\n"
"s_cselect_b32 s40,s47,s40\n"
RD"a[108:111],v226 offset:13312\n"
MF"a[140:143],a[128:131],v[16:19],a[140:143]\n"
RD"a[112:115],v226 offset:6144\n"
MF"a[144:147],a[128:131],v[24:27],a[144:147]\n"
BL"v225,s[8:11],s44 offen offset:1024 sc1 lds\n"
MF"a[144:147],a[128:131],v[8:11],a[144:147]\n"
WT"lgkmcnt(6)\n"
RD"a[116:119],v226 offset:14336\n"
MF"v[176:179],a[64:67],v[68:71],v[176:179]\n"
RD"a[120:123],v226 offset:7168\n"
MF"v[176:179],a[68:71],v[76:79],v[176:179]\n"
RD"a[124:127],v226 offset:15360\n"
MF"v[180:183],a[72:75],v[68:71],v[180:183]\n"
WT"vmcnt(14)\n"
"s_barrier\n"
MF"v[180:183],a[76:79],v[76:79],v[180:183]\n"
RD"a[64:67],v227 offset:0\n"
MF"v[184:187],a[80:83],v[68:71],v[184:187]\n"
BL"v225,s[8:11],s44 offen offset:2048 sc1 lds\n"
MF"v[184:187],a[84:87],v[76:79],v[184:187]\n"
RD"a[68:71],v227 offset:1024\n"
MF"v[188:191],a[88:91],v[68:71],v[188:191]\n"
WT"lgkmcnt(4)\n"
MF"v[188:191],a[92:95],v[76:79],v[188:191]\n"
MF"v[192:195],a[96:99],v[68:71],v[192:195]\n"
MF"v[192:195],a[100:103],v[76:79],v[192:195]\n"
BL"v225,s[8:11],s44 offen offset:3072 sc1 lds\n"
MF"v[196:199],a[104:107],v[68:71],v[196:199]\n"
RD"a[72:75],v227 offset:2048\n"
MF"v[196:199],a[108:111],v[76:79],v[196:199]\n"
RD"a[76:79],v227 offset:3072\n"
RD"a[80:83],v227 offset:4096\n"
MF"v[200:203],a[112:115],v[68:71],v[200:203]\n"
MF"v[200:203],a[116:119],v[76:79],v[200:203]\n"
WT"lgkmcnt(3)\n"
RD"a[84:87],v227 offset:5120\n"
MF"v[204:207],a[120:123],v[68:71],v[204:207]\n"
MF"v[204:207],a[124:127],v[76:79],v[204:207]\n"
MF"a[144:147],a[128:131],v[68:71],a[144:147]\n"
MF"a[144:147],a[128:131],v[76:79],a[144:147]\n"
RD"a[88:91],v227 offset:6144\n"
"LTL_%=:\n"
"s_cmp_ge_u32 s34,6\n"
"s_cbranch_scc1 LTH_%=\n"
RD"a[92:95],v227 offset:7168\n"
MF"v[0:3],a[64:67],v[80:83],0\n"
"v_accvgpr_read_b32 v251,a140\n"
"v_subrev_u32 v253,0x2b800000,v251\n"
MF"v[0:3],a[68:71],v[88:91],v[0:3]\n"
RD"a[96:99],v227 offset:8192\n"
MF"v[40:43],a[68:71],v[120:123],0\n"
"v_cmp_le_u32 vcc,0x34000000,v253\n"
"s_cmp_lg_u64 vcc,0\n"
"s_cbranch_scc1 L1041_%=\n"
"L1040_%=:\n"
MF"v[40:43],a[64:67],v[112:115],v[40:43]\n"
WT"lgkmcnt(3)\n"
"v_rcp_f32 v251,v251\n"
MF"v[4:7],a[72:75],v[80:83],0\n"
SA"s41,s41,32768\n"
RD"a[100:103],v227 offset:9216\n"
MU"v144,v251,v144\n"
MF"v[4:7],a[76:79],v[88:91],v[4:7]\n"
"s_cmp_eq_u32 s41,163840\n"
RD"a[104:107],v227 offset:10240\n"
MU"v145,v251,v145\n"
MF"v[44:47],a[76:79],v[120:123],0\n"
"s_cselect_b32 s41,0,s41\n"
MU"v146,v251,v146\n"
"v_add_u32 v226,s41,v224\n"
MF"v[44:47],a[72:75],v[112:115],v[44:47]\n"
RD"a[108:111],v227 offset:11264\n"
MU"v147,v251,v147\n"
MF"v[8:11],a[80:83],v[80:83],0\n"
SA"s41,s41,32768\n"
MU"v148,v251,v148\n"
MU"v149,v251,v149\n"
MF"v[8:11],a[84:87],v[88:91],v[8:11]\n"
"s_cmp_eq_u32 s41,163840\n"
RD"a[112:115],v227 offset:12288\n"
MU"v150,v251,v150\n"
MU"v151,v251,v151\n"
MF"v[48:51],a[84:87],v[120:123],0\n"
"s_cselect_b32 s41,0,s41\n"
PK"v144,v144,v145\n"
"v_add_u32 v235,s41,v224\n"
PK"v145,v146,v147\n"
MF"v[48:51],a[80:83],v[112:115],v[48:51]\n"
WT"lgkmcnt(3)\n"
RD"a[116:119],v227 offset:13312\n"
PK"v146,v148,v149\n"
MF"v[12:15],a[88:91],v[80:83],0\n"
SA"s41,s41,32768\n"
PK"v147,v150,v151\n"
MU"v152,v251,v152\n"
MF"v[12:15],a[92:95],v[88:91],v[12:15]\n"
"s_cmp_eq_u32 s41,163840\n"
BS"v[144:147],v224,s[8:11],s60 offen offset:0 sc1\n"
MU"v153,v251,v153\n"
MF"v[52:55],a[92:95],v[120:123],0\n"
"s_cselect_b32 s41,0,s41\n"
RD"a[120:123],v227 offset:14336\n"
"v_add_u32 v236,s41,v224\n"
MU"v154,v251,v154\n"
MF"v[52:55],a[88:91],v[112:115],v[52:55]\n"
RD"a[124:127],v227 offset:15360\n"
MU"v155,v251,v155\n"
MU"v156,v251,v156\n"
MF"v[16:19],a[96:99],v[80:83],0\n"
SA"s41,s41,32768\n"
MU"v157,v251,v157\n"
MU"v158,v251,v158\n"
MU"v159,v251,v159\n"
MF"v[16:19],a[100:103],v[88:91],v[16:19]\n"
"s_cmp_eq_u32 s41,163840\n"
RD"a[64:67],v227 offset:16384\n"
PK"v152,v152,v153\n"
PK"v153,v154,v155\n"
MF"v[56:59],a[100:103],v[120:123],0\n"
"s_cselect_b32 s41,0,s41\n"
PK"v154,v156,v157\n"
"v_add_u32 v237,s41,v224\n"
PK"v155,v158,v159\n"
MF"v[56:59],a[96:99],v[112:115],v[56:59]\n"
WT"lgkmcnt(3)\n"
BS"v[152:155],v224,s[8:11],s60 offen offset:1024 sc1\n"
MU"v160,v251,v160\n"
MF"v[20:23],a[104:107],v[80:83],0\n"
RD"a[68:71],v227 offset:17408\n"
MU"v161,v251,v161\n"
MU"v162,v251,v162\n"
MF"v[20:23],a[108:111],v[88:91],v[20:23]\n"
RD"a[72:75],v227 offset:18432\n"
MU"v163,v251,v163\n"
MU"v164,v251,v164\n"
MF"v[60:63],a[108:111],v[120:123],0\n"
MU"v165,v251,v165\n"
MU"v166,v251,v166\n"
MF"v[60:63],a[104:107],v[112:115],v[60:63]\n"
RD"a[76:79],v227 offset:19456\n"
MU"v167,v251,v167\n"
PK"v160,v160,v161\n"
MF"v[24:27],a[112:115],v[80:83],0\n"
SA"s51,s60,16384\n"
"v_accvgpr_read_b32 v252,a144\n"
"v_subrev_u32 v253,0x2b800000,v252\n"
PK"v161,v162,v163\n"
MF"v[24:27],a[116:119],v[88:91],v[24:27]\n"
"s_lshl_b32 s62,s34,14\n"
RD"a[80:83],v227 offset:20480\n"
PK"v162,v164,v165\n"
PK"v163,v166,v167\n"
MF"v[64:67],a[116:119],v[120:123],0\n"
"s_sub_u32 s62,s61,s62\n"
BS"v[160:163],v224,s[8:11],s60 offen offset:2048 sc1\n"
MU"v168,v251,v168\n"
MF"v[64:67],a[112:115],v[112:115],v[64:67]\n"
WT"lgkmcnt(3)\n"
"v_cmp_le_u32 vcc,0x34000000,v253\n"
"s_cmp_lg_u64 vcc,0\n"
"s_cbranch_scc1 L1047_%=\n"
"L1046_%=:\n"
MF"v[28:31],a[120:123],v[80:83],0\n"
SA"s62,s62,122880\n"
"v_rcp_f32 v252,v252\n"
MU"v169,v251,v169\n"
MF"v[28:31],a[124:127],v[88:91],v[28:31]\n"
RD"a[84:87],v227 offset:21504\n"
RD"a[88:91],v227 offset:22528\n"
MU"v170,v251,v170\n"
MF"v[68:71],a[124:127],v[120:123],0\n"
MU"v171,v251,v171\n"
MU"v172,v251,v172\n"
MU"v173,v251,v173\n"
MF"v[68:71],a[120:123],v[112:115],v[68:71]\n"
RD"a[92:95],v227 offset:23552\n"
MU"v174,v251,v174\n"
MU"v175,v251,v175\n"
MF"v[32:35],a[64:67],v[80:83],0\n"
PK"v168,v168,v169\n"
PK"v169,v170,v171\n"
MU"v176,v252,v176\n"
MF"v[32:35],a[68:71],v[88:91],v[32:35]\n"
RD"a[96:99],v227 offset:24576\n"
MU"v177,v252,v177\n"
PK"v170,v172,v173\n"
MF"v[72:75],a[68:71],v[120:123],0\n"
PK"v171,v174,v175\n"
MU"v178,v252,v178\n"
MU"v179,v252,v179\n"
MF"v[72:75],a[64:67],v[112:115],v[72:75]\n"
WT"lgkmcnt(4)\n"
BS"v[168:171],v224,s[8:11],s60 offen offset:3072 sc1\n"
MU"v180,v252,v180\n"
MF"v[36:39],a[72:75],v[80:83],0\n"
RD"a[100:103],v227 offset:25600\n"
MU"v181,v252,v181\n"
MU"v182,v252,v182\n"
MF"v[36:39],a[76:79],v[88:91],v[36:39]\n"
RD"a[104:107],v227 offset:26624\n"
MU"v183,v252,v183\n"
PK"v176,v176,v177\n"
MF"v[76:79],a[76:79],v[120:123],0\n"
PK"v177,v178,v179\n"
PK"v178,v180,v181\n"
PK"v179,v182,v183\n"
MF"v[76:79],a[72:75],v[112:115],v[76:79]\n"
BS"v[176:179],v224,s[8:11],s51 offen offset:0 sc1\n"
MU"v184,v252,v184\n"
MF"v[0:3],a[80:83],v[96:99],v[0:3]\n"
WT"lgkmcnt(2)\n"
RD"a[108:111],v227 offset:27648\n"
MU"v185,v252,v185\n"
MU"v186,v252,v186\n"
MF"v[0:3],a[84:87],v[104:107],v[0:3]\n"
RD"a[112:115],v227 offset:28672\n"
MU"v187,v252,v187\n"
MU"v188,v252,v188\n"
MF"v[40:43],a[84:87],v[136:139],v[40:43]\n"
MU"v189,v252,v189\n"
MU"v190,v252,v190\n"
MU"v191,v252,v191\n"
MF"v[40:43],a[80:83],v[128:131],v[40:43]\n"
RD"a[116:119],v227 offset:29696\n"
PK"v184,v184,v185\n"
PK"v185,v186,v187\n"
MF"v[4:7],a[88:91],v[96:99],v[4:7]\n"
PK"v186,v188,v189\n"
PK"v187,v190,v191\n"
MU"v192,v252,v192\n"
MF"v[4:7],a[92:95],v[104:107],v[4:7]\n"
BS"v[184:187],v224,s[8:11],s51 offen offset:1024 sc1\n"
MU"v193,v252,v193\n"
MF"v[44:47],a[92:95],v[136:139],v[44:47]\n"
RD"a[120:123],v227 offset:30720\n"
MU"v194,v252,v194\n"
MU"v195,v252,v195\n"
MF"v[44:47],a[88:91],v[128:131],v[44:47]\n"
RD"a[124:127],v227 offset:31744\n"
MU"v196,v252,v196\n"
MU"v197,v252,v197\n"
MF"v[8:11],a[96:99],v[96:99],v[8:11]\n"
WT"lgkmcnt(3)\n"
WT"vmcnt(14)\n"
"s_barrier\n"
MF"v[8:11],a[100:103],v[104:107],v[8:11]\n"
RD"a[64:67],v226 offset:0\n"
MU"v198,v252,v198\n"
MU"v199,v252,v199\n"
MF"v[48:51],a[100:103],v[136:139],v[48:51]\n"
PK"v192,v192,v193\n"
PK"v193,v194,v195\n"
PK"v194,v196,v197\n"
MF"v[48:51],a[96:99],v[128:131],v[48:51]\n"
RD"a[68:71],v226 offset:1024\n"
PK"v195,v198,v199\n"
MU"v200,v252,v200\n"
MF"v[12:15],a[104:107],v[96:99],v[12:15]\n"
BS"v[192:195],v224,s[8:11],s51 offen offset:2048 sc1\n"
MU"v201,v252,v201\n"
MF"v[12:15],a[108:111],v[104:107],v[12:15]\n"
RD"a[72:75],v226 offset:2048\n"
MU"v202,v252,v202\n"
MU"v203,v252,v203\n"
MF"v[52:55],a[108:111],v[136:139],v[52:55]\n"
MU"v204,v252,v204\n"
MU"v205,v252,v205\n"
MU"v206,v252,v206\n"
MF"v[52:55],a[104:107],v[128:131],v[52:55]\n"
RD"a[76:79],v226 offset:3072\n"
MU"v207,v252,v207\n"
PK"v200,v200,v201\n"
MF"v[16:19],a[112:115],v[96:99],v[16:19]\n"
WT"lgkmcnt(3)\n"
PK"v201,v202,v203\n"
PK"v202,v204,v205\n"
PK"v203,v206,v207\n"
MF"v[16:19],a[116:119],v[104:107],v[16:19]\n"
BS"v[200:203],v224,s[8:11],s51 offen offset:3072 sc1\n"
MF"v[56:59],a[116:119],v[136:139],v[56:59]\n"
RD"a[80:83],v226 offset:4096\n"
"v_readlane_b32 s43,v228,s42\n"
MF"v[56:59],a[112:115],v[128:131],v[56:59]\n"
RD"a[84:87],v226 offset:5120\n"
MF"v[20:23],a[120:123],v[96:99],v[20:23]\n"
MF"v[20:23],a[124:127],v[104:107],v[20:23]\n"
RD"a[88:91],v226 offset:6144\n"
MF"v[60:63],a[124:127],v[136:139],v[60:63]\n"
"s_mov_b32 m0,s40\n"
MF"v[60:63],a[120:123],v[128:131],v[60:63]\n"
RD"a[92:95],v226 offset:7168\n"
MF"v[24:27],a[64:67],v[96:99],v[24:27]\n"
WT"lgkmcnt(4)\n"
"v_readlane_b32 s44,v229,s42\n"
MF"v[24:27],a[68:71],v[104:107],v[24:27]\n"
RD"a[96:99],v227 offset:0\n"
MF"v[64:67],a[68:71],v[136:139],v[64:67]\n"
SA"s42,s42,1\n"
BL"v225,s[8:11],s43 offen offset:0 sc1 lds\n"
MF"v[64:67],a[64:67],v[128:131],v[64:67]\n"
RD"a[100:103],v227 offset:1024\n"
MF"v[28:31],a[72:75],v[96:99],v[28:31]\n"
MF"v[28:31],a[76:79],v[104:107],v[28:31]\n"
RD"a[104:107],v227 offset:2048\n"
MF"v[68:71],a[76:79],v[136:139],v[68:71]\n"
MF"v[68:71],a[72:75],v[128:131],v[68:71]\n"
WT"lgkmcnt(3)\n"
RD"a[108:111],v227 offset:3072\n"
MF"v[32:35],a[80:83],v[96:99],v[32:35]\n"
MF"v[32:35],a[84:87],v[104:107],v[32:35]\n"
RD"a[112:115],v227 offset:4096\n"
MF"v[72:75],a[84:87],v[136:139],v[72:75]\n"
BL"v225,s[8:11],s43 offen offset:1024 sc1 lds\n"
MF"v[72:75],a[80:83],v[128:131],v[72:75]\n"
RD"a[116:119],v227 offset:5120\n"
MF"v[36:39],a[88:91],v[96:99],v[36:39]\n"
MF"v[36:39],a[92:95],v[104:107],v[36:39]\n"
RD"a[120:123],v227 offset:6144\n"
MF"v[76:79],a[92:95],v[136:139],v[76:79]\n"
MF"v[76:79],a[88:91],v[128:131],v[76:79]\n"
WT"lgkmcnt(3)\n"
RD"a[124:127],v227 offset:7168\n"
MF"v[84:87],a[96:99],v[144:147],0\n"
BL"v225,s[8:11],s43 offen offset:2048 sc1 lds\n"
MF"v[84:87],a[100:103],v[152:155],v[84:87]\n"
SA"s48,s59,98304\n"
RD"a[64:67],v227 offset:8192\n"
MF"v[164:167],a[100:103],v[184:187],0\n"
"s_and_b32 s48,s48,0x18000\n"
MF"v[164:167],a[96:99],v[176:179],v[164:167]\n"
"s_andn2_b32 s49,s59,0x18000\n"
RD"a[68:71],v227 offset:9216\n"
MF"v[92:95],a[104:107],v[144:147],0\n"
"s_or_b32 s52,s48,s49\n"
SA"s53,s52,16384\n"
MF"v[92:95],a[108:111],v[152:155],v[92:95]\n"
RD"a[72:75],v227 offset:10240\n"
MF"v[172:175],a[108:111],v[184:187],0\n"
BL"a[228:231],v224,s[8:11],s52 offen offset:2048 sc1\n"
MF"v[172:175],a[104:107],v[176:179],v[172:175]\n"
WT"lgkmcnt(3)\n"
RD"a[76:79],v227 offset:11264\n"
MF"v[100:103],a[112:115],v[144:147],0\n"
BL"v225,s[8:11],s43 offen offset:3072 sc1 lds\n"
MF"v[100:103],a[116:119],v[152:155],v[100:103]\n"
RD"a[80:83],v227 offset:12288\n"
MF"v[180:183],a[116:119],v[184:187],0\n"
SA"m0,s40,16384\n"
MF"v[180:183],a[112:115],v[176:179],v[180:183]\n"
RD"a[84:87],v227 offset:13312\n"
MF"v[108:111],a[120:123],v[144:147],0\n"
MF"v[108:111],a[124:127],v[152:155],v[108:111]\n"
RD"a[88:91],v227 offset:14336\n"
MF"v[188:191],a[124:127],v[184:187],0\n"
BL"v225,s[8:11],s44 offen offset:0 sc1 lds\n"
MF"v[188:191],a[120:123],v[176:179],v[188:191]\n"
WT"lgkmcnt(3)\n"
RD"a[92:95],v227 offset:15360\n"
SA"s40,s40,32768\n"
MF"v[116:119],a[64:67],v[144:147],0\n"
"s_cmp_eq_u32 s40,s46\n"
"s_cselect_b32 s40,s47,s40\n"
MF"v[116:119],a[68:71],v[152:155],v[116:119]\n"
RD"a[96:99],v227 offset:16384\n"
MF"v[196:199],a[68:71],v[184:187],0\n"
BL"a[232:235],v224,s[8:11],s53 offen offset:2048 sc1\n"
MF"v[196:199],a[64:67],v[176:179],v[196:199]\n"
RD"a[100:103],v227 offset:17408\n"
MF"v[124:127],a[72:75],v[144:147],0\n"
MF"v[124:127],a[76:79],v[152:155],v[124:127]\n"
RD"a[104:107],v227 offset:18432\n"
MF"v[204:207],a[76:79],v[184:187],0\n"
BL"v225,s[8:11],s44 offen offset:1024 sc1 lds\n"
MF"v[204:207],a[72:75],v[176:179],v[204:207]\n"
WT"lgkmcnt(3)\n"
RD"a[108:111],v227 offset:19456\n"
MF"v[132:135],a[80:83],v[144:147],0\n"
MF"v[132:135],a[84:87],v[152:155],v[132:135]\n"
RD"a[112:115],v227 offset:20480\n"
MF"v[208:211],a[84:87],v[184:187],0\n"
MF"v[208:211],a[80:83],v[176:179],v[208:211]\n"
RD"a[116:119],v227 offset:21504\n"
MF"v[140:143],a[88:91],v[144:147],0\n"
BL"v225,s[8:11],s44 offen offset:2048 sc1 lds\n"
MF"v[140:143],a[92:95],v[152:155],v[140:143]\n"
RD"a[120:123],v227 offset:22528\n"
MF"v[212:215],a[92:95],v[184:187],0\n"
WT"vmcnt(9)\n"
"buffer_store_dwordx2 v[232:233],v230,s[8:11],s62 offen sc1\n"
MF"v[212:215],a[88:91],v[176:179],v[212:215]\n"
WT"lgkmcnt(3)\n"
BL"a[236:239],v224,s[8:11],s52 offen offset:3072 sc1\n"
MF"v[148:151],a[96:99],v[144:147],0\n"
RD"a[124:127],v227 offset:23552\n"
MF"v[148:151],a[100:103],v[152:155],v[148:151]\n"
RD"a[64:67],v227 offset:24576\n"
MF"v[216:219],a[100:103],v[184:187],0\n"
"v_readlane_b32 s43,v228,s42\n"
MF"v[216:219],a[96:99],v[176:179],v[216:219]\n"
RD"a[68:71],v227 offset:25600\n"
MF"v[156:159],a[104:107],v[144:147],0\n"
BL"v225,s[8:11],s44 offen offset:3072 sc1 lds\n"
MF"v[156:159],a[108:111],v[152:155],v[156:159]\n"
RD"a[72:75],v227 offset:26624\n"
MF"v[220:223],a[108:111],v[184:187],0\n"
"s_mov_b32 m0,s40\n"
MF"v[220:223],a[104:107],v[176:179],v[220:223]\n"
WT"lgkmcnt(3)\n"
RD"a[76:79],v227 offset:27648\n"
MF"v[84:87],a[112:115],v[160:163],v[84:87]\n"
"s_lshr_b32 s89,s31,3\n"
"v_readlane_b32 s44,v229,s42\n"
MF"v[84:87],a[116:119],v[168:171],v[84:87]\n"
"s_lshl_b32 s89,s89,4\n"
RD"a[80:83],v227 offset:28672\n"
MF"v[164:167],a[116:119],v[200:203],v[164:167]\n"
SA"s42,s42,1\n"
BL"v225,s[8:11],s43 offen offset:0 sc1 lds\n"
MF"v[164:167],a[112:115],v[192:195],v[164:167]\n"
SA"s89,s89,s30\n"
BL"a[240:243],v224,s[8:11],s53 offen offset:3072 sc1\n"
MF"v[92:95],a[120:123],v[160:163],v[92:95]\n"
"s_lshl_b32 s89,s89,3\n"
RD"a[84:87],v227 offset:29696\n"
"s_xor_b32 s23,s32,s30\n"
MF"v[92:95],a[124:127],v[168:171],v[92:95]\n"
SA"s89,s89,s23\n"
RD"a[88:91],v227 offset:30720\n"
MF"v[172:175],a[124:127],v[200:203],v[172:175]\n"
"s_xor_b32 s90,s89,36\n"
MF"v[172:175],a[120:123],v[192:195],v[172:175]\n"
WT"lgkmcnt(3)\n"
RD"a[92:95],v227 offset:31744\n"
"s_mul_i32 s88,s89,5120\n"
MF"v[100:103],a[64:67],v[160:163],v[100:103]\n"
"s_mul_i32 s22,s90,5120\n"
MF"v[100:103],a[68:71],v[168:171],v[100:103]\n"
"s_sub_u32 s90,s90,s89\n"
RD"a[96:99],v226 offset:0\n"
MF"v[180:183],a[68:71],v[200:203],v[180:183]\n"
"s_lshl_b32 s90,s90,6\n"
BL"v225,s[8:11],s43 offen offset:1024 sc1 lds\n"
MF"v[180:183],a[64:67],v[192:195],v[180:183]\n"
SA"s88,s88,59506688\n"
RD"a[100:103],v226 offset:1024\n"
MF"v[108:111],a[72:75],v[160:163],v[108:111]\n"
SA"s22,s22,59506688\n"
MF"v[108:111],a[76:79],v[168:171],v[108:111]\n"
"s_cmp_eq_u32 s23,0\n"
RD"a[104:107],v226 offset:2048\n"
"s_cselect_b32 s88,2130706432,s88\n"
MF"v[188:191],a[76:79],v[200:203],v[188:191]\n"
"s_cmp_eq_u32 s23,4\n"
"s_cselect_b32 s22,2130706432,s22\n"
MF"v[188:191],a[72:75],v[192:195],v[188:191]\n"
WT"lgkmcnt(3)\n"
RD"a[108:111],v226 offset:3072\n"
"s_lshl_b32 s89,s89,6\n"
MF"v[116:119],a[80:83],v[160:163],v[116:119]\n"
SA"s89,s89,80478208\n"
BL"v225,s[8:11],s43 offen offset:2048 sc1 lds\n"
MF"v[116:119],a[84:87],v[168:171],v[116:119]\n"
RD"a[112:115],v226 offset:4096\n"
"v_bfe_u32 v239,v224,1,6\n"
MF"v[196:199],a[84:87],v[200:203],v[196:199]\n"
"v_bfe_u32 v242,v224,7,1\n"
MF"v[196:199],a[80:83],v[192:195],v[196:199]\n"
RD"a[116:119],v226 offset:5120\n"
"v_mad_u32_u24 v239,v242,s90,v239\n"
MF"v[124:127],a[88:91],v[160:163],v[124:127]\n"
"v_lshrrev_b32 v242,8,v224\n"
MF"v[124:127],a[92:95],v[168:171],v[124:127]\n"
RD"a[120:123],v226 offset:6144\n"
"v_lshl_add_u32 v239,v242,27,v239\n"
MF"v[204:207],a[92:95],v[200:203],v[204:207]\n"
MF"v[204:207],a[88:91],v[192:195],v[204:207]\n"
WT"lgkmcnt(3)\n"
RD"a[124:127],v226 offset:7168\n"
MF"v[132:135],a[96:99],v[160:163],v[132:135]\n"
BL"v225,s[8:11],s43 offen offset:3072 sc1 lds\n"
MF"v[132:135],a[100:103],v[168:171],v[132:135]\n"
RD"a[64:67],v226 offset:8192\n"
MF"v[208:211],a[100:103],v[200:203],v[208:211]\n"
SA"m0,s40,16384\n"
MF"v[208:211],a[96:99],v[192:195],v[208:211]\n"
RD"a[68:71],v226 offset:9216\n"
MF"v[140:143],a[104:107],v[160:163],v[140:143]\n"
MF"v[140:143],a[108:111],v[168:171],v[140:143]\n"
RD"a[72:75],v226 offset:10240\n"
MF"v[212:215],a[108:111],v[200:203],v[212:215]\n"
BL"v225,s[8:11],s44 offen offset:0 sc1 lds\n"
MF"v[212:215],a[104:107],v[192:195],v[212:215]\n"
WT"lgkmcnt(3)\n"
RD"a[76:79],v226 offset:11264\n"
SA"s40,s40,32768\n"
MF"v[148:151],a[112:115],v[160:163],v[148:151]\n"
"s_cmp_eq_u32 s40,s46\n"
"s_cselect_b32 s40,s47,s40\n"
MF"v[148:151],a[116:119],v[168:171],v[148:151]\n"
RD"a[80:83],v226 offset:12288\n"
MF"v[216:219],a[116:119],v[200:203],v[216:219]\n"
MF"v[216:219],a[112:115],v[192:195],v[216:219]\n"
RD"a[84:87],v226 offset:13312\n"
MF"v[156:159],a[120:123],v[160:163],v[156:159]\n"
MF"v[156:159],a[124:127],v[168:171],v[156:159]\n"
RD"a[88:91],v226 offset:14336\n"
MF"v[220:223],a[124:127],v[200:203],v[220:223]\n"
BL"v225,s[8:11],s44 offen offset:1024 sc1 lds\n"
MF"v[220:223],a[120:123],v[192:195],v[220:223]\n"
WT"lgkmcnt(3)\n"
RD"a[92:95],v226 offset:15360\n"
MF"v[0:3],a[64:67],a[0:3],v[0:3]\n"
MF"v[0:3],a[68:71],a[8:11],v[0:3]\n"
RD"a[96:99],v226 offset:16384\n"
MF"v[40:43],a[68:71],a[12:15],v[40:43]\n"
MF"v[40:43],a[64:67],a[4:7],v[40:43]\n"
RD"a[100:103],v226 offset:17408\n"
MF"v[4:7],a[72:75],a[0:3],v[4:7]\n"
BL"v225,s[8:11],s44 offen offset:2048 sc1 lds\n"
MF"v[4:7],a[76:79],a[8:11],v[4:7]\n"
RD"a[104:107],v226 offset:18432\n"
MF"v[44:47],a[76:79],a[12:15],v[44:47]\n"
MF"v[44:47],a[72:75],a[4:7],v[44:47]\n"
WT"lgkmcnt(3)\n"
RD"a[108:111],v226 offset:19456\n"
MF"v[8:11],a[80:83],a[0:3],v[8:11]\n"
MF"v[8:11],a[84:87],a[8:11],v[8:11]\n"
RD"a[112:115],v226 offset:20480\n"
MF"v[48:51],a[84:87],a[12:15],v[48:51]\n"
MF"v[48:51],a[80:83],a[4:7],v[48:51]\n"
RD"a[116:119],v226 offset:21504\n"
MF"v[12:15],a[88:91],a[0:3],v[12:15]\n"
BL"v225,s[8:11],s44 offen offset:3072 sc1 lds\n"
MF"v[12:15],a[92:95],a[8:11],v[12:15]\n"
RD"a[120:123],v226 offset:22528\n"
MF"v[52:55],a[92:95],a[12:15],v[52:55]\n"
MF"v[52:55],a[88:91],a[4:7],v[52:55]\n"
WT"lgkmcnt(3)\n"
RD"a[124:127],v226 offset:23552\n"
MF"v[16:19],a[96:99],a[0:3],v[16:19]\n"
MF"v[16:19],a[100:103],a[8:11],v[16:19]\n"
RD"a[64:67],v226 offset:24576\n"
MF"v[56:59],a[100:103],a[12:15],v[56:59]\n"
MF"v[56:59],a[96:99],a[4:7],v[56:59]\n"
RD"a[68:71],v226 offset:25600\n"
MF"v[20:23],a[104:107],a[0:3],v[20:23]\n"
MF"v[20:23],a[108:111],a[8:11],v[20:23]\n"
RD"a[72:75],v226 offset:26624\n"
MF"v[60:63],a[108:111],a[12:15],v[60:63]\n"
MF"v[60:63],a[104:107],a[4:7],v[60:63]\n"
WT"lgkmcnt(3)\n"
RD"a[76:79],v226 offset:27648\n"
MF"v[24:27],a[112:115],a[0:3],v[24:27]\n"
MF"v[24:27],a[116:119],a[8:11],v[24:27]\n"
RD"a[80:83],v226 offset:28672\n"
MF"v[64:67],a[116:119],a[12:15],v[64:67]\n"
MF"v[64:67],a[112:115],a[4:7],v[64:67]\n"
RD"a[84:87],v226 offset:29696\n"
MF"v[28:31],a[120:123],a[0:3],v[28:31]\n"
MF"v[28:31],a[124:127],a[8:11],v[28:31]\n"
RD"a[88:91],v226 offset:30720\n"
MF"v[68:71],a[124:127],a[12:15],v[68:71]\n"
MF"v[68:71],a[120:123],a[4:7],v[68:71]\n"
WT"lgkmcnt(3)\n"
RD"a[92:95],v226 offset:31744\n"
MF"v[32:35],a[64:67],a[0:3],v[32:35]\n"
"s_barrier\n"
MF"v[32:35],a[68:71],a[8:11],v[32:35]\n"
RD"a[96:99],v235 offset:0\n"
MF"v[72:75],a[68:71],a[12:15],v[72:75]\n"
MF"v[72:75],a[64:67],a[4:7],v[72:75]\n"
RD"a[100:103],v235 offset:1024\n"
MF"v[36:39],a[72:75],a[0:3],v[36:39]\n"
MF"v[36:39],a[76:79],a[8:11],v[36:39]\n"
RD"a[104:107],v235 offset:2048\n"
MF"v[76:79],a[76:79],a[12:15],v[76:79]\n"
MF"v[76:79],a[72:75],a[4:7],v[76:79]\n"
WT"lgkmcnt(3)\n"
RD"a[108:111],v235 offset:3072\n"
MF"v[0:3],a[80:83],a[16:19],v[0:3]\n"
MF"v[0:3],a[84:87],a[24:27],v[0:3]\n"
RD"a[112:115],v235 offset:4096\n"
MF"v[40:43],a[84:87],a[28:31],v[40:43]\n"
MF"v[40:43],a[80:83],a[20:23],v[40:43]\n"
RD"a[116:119],v235 offset:5120\n"
MF"v[4:7],a[88:91],a[16:19],v[4:7]\n"
MF"v[4:7],a[92:95],a[24:27],v[4:7]\n"
RD"a[120:123],v235 offset:6144\n"
MF"v[44:47],a[92:95],a[28:31],v[44:47]\n"
MF"v[44:47],a[88:91],a[20:23],v[44:47]\n"
WT"lgkmcnt(3)\n"
RD"a[124:127],v235 offset:7168\n"
MF"v[8:11],a[96:99],a[16:19],v[8:11]\n"
MF"v[8:11],a[100:103],a[24:27],v[8:11]\n"
RD"a[64:67],v235 offset:8192\n"
MF"v[48:51],a[100:103],a[28:31],v[48:51]\n"
MF"v[48:51],a[96:99],a[20:23],v[48:51]\n"
RD"a[68:71],v235 offset:9216\n"
MF"v[12:15],a[104:107],a[16:19],v[12:15]\n"
MF"v[12:15],a[108:111],a[24:27],v[12:15]\n"
RD"a[72:75],v235 offset:10240\n"
"v_bfe_u32 v238,v224,4,2\n"
MF"v[52:55],a[108:111],a[28:31],v[52:55]\n"
"s_andn2_b32 s63,s62,255\n"
"v_lshlrev_b32 v238,6,v238\n"
MF"v[52:55],a[104:107],a[20:23],v[52:55]\n"
WT"lgkmcnt(3)\n"
RD"a[76:79],v235 offset:11264\n"
MF"v[16:19],a[112:115],a[16:19],v[16:19]\n"
"buffer_load_dwordx2 v[240:241],v238,s[8:11],s63 offen sc1\n"
MF"v[16:19],a[116:119],a[24:27],v[16:19]\n"
RD"a[80:83],v235 offset:12288\n"
MF"v[56:59],a[116:119],a[28:31],v[56:59]\n"
MF"v[56:59],a[112:115],a[20:23],v[56:59]\n"
RD"a[84:87],v235 offset:13312\n"
MF"v[20:23],a[120:123],a[16:19],v[20:23]\n"
MF"v[20:23],a[124:127],a[24:27],v[20:23]\n"
RD"a[88:91],v235 offset:14336\n"
MF"v[60:63],a[124:127],a[28:31],v[60:63]\n"
MF"v[60:63],a[120:123],a[20:23],v[60:63]\n"
RD"a[92:95],v235 offset:15360\n"
"LTJ_%=:\n"
WT"lgkmcnt(7)\n"
MF"v[24:27],a[64:67],a[16:19],v[24:27]\n"
WT"lgkmcnt(3)\n"
MF"v[24:27],a[68:71],a[24:27],v[24:27]\n"
RD"a[96:99],v235 offset:16384\n"
MF"v[64:67],a[68:71],a[28:31],v[64:67]\n"
MF"v[64:67],a[64:67],a[20:23],v[64:67]\n"
RD"a[100:103],v235 offset:17408\n"
MF"v[28:31],a[72:75],a[16:19],v[28:31]\n"
MF"v[28:31],a[76:79],a[24:27],v[28:31]\n"
RD"a[104:107],v235 offset:18432\n"
MF"v[68:71],a[76:79],a[28:31],v[68:71]\n"
MF"v[68:71],a[72:75],a[20:23],v[68:71]\n"
RD"a[108:111],v235 offset:19456\n"
MF"v[32:35],a[80:83],a[16:19],v[32:35]\n"
WT"lgkmcnt(3)\n"
WT"vmcnt(1)\n"
"s_barrier\n"
MF"v[32:35],a[84:87],a[24:27],v[32:35]\n"
RD"a[112:115],v236 offset:4096\n"
MF"v[72:75],a[84:87],a[28:31],v[72:75]\n"
MF"v[72:75],a[80:83],a[20:23],v[72:75]\n"
RD"a[116:119],v236 offset:5120\n"
MF"v[36:39],a[88:91],a[16:19],v[36:39]\n"
MF"v[36:39],a[92:95],a[24:27],v[36:39]\n"
RD"a[120:123],v236 offset:6144\n"
MF"v[76:79],a[92:95],a[28:31],v[76:79]\n"
MF"v[76:79],a[88:91],a[20:23],v[76:79]\n"
RD"a[124:127],v236 offset:7168\n"
MF"v[0:3],a[96:99],a[180:183],v[0:3]\n"
WT"lgkmcnt(3)\n"
MF"v[0:3],a[100:103],a[188:191],v[0:3]\n"
RD"a[64:67],v236 offset:24576\n"
MF"v[40:43],a[100:103],a[192:195],v[40:43]\n"
MF"v[40:43],a[96:99],a[184:187],v[40:43]\n"
RD"a[68:71],v236 offset:25600\n"
MF"v[4:7],a[104:107],a[180:183],v[4:7]\n"
MF"v[4:7],a[108:111],a[188:191],v[4:7]\n"
RD"a[72:75],v236 offset:26624\n"
MF"v[44:47],a[108:111],a[192:195],v[44:47]\n"
MF"v[44:47],a[104:107],a[184:187],v[44:47]\n"
RD"a[76:79],v236 offset:27648\n"
MF"v[0:3],a[112:115],a[196:199],v[0:3]\n"
WT"lgkmcnt(3)\n"
MF"v[0:3],a[116:119],a[204:207],v[0:3]\n"
RD"a[80:83],v237 offset:12288\n"
MF"v[40:43],a[116:119],a[208:211],v[40:43]\n"
MF"v[40:43],a[112:115],a[200:203],v[40:43]\n"
RD"a[84:87],v237 offset:13312\n"
MF"v[4:7],a[120:123],a[196:199],v[4:7]\n"
MF"v[4:7],a[124:127],a[204:207],v[4:7]\n"
SA"s48,s60,32768\n"
RD"a[88:91],v237 offset:14336\n"
MF"v[44:47],a[124:127],a[208:211],v[44:47]\n"
"s_and_b32 s48,s48,0x18000\n"
MF"v[44:47],a[120:123],a[200:203],v[44:47]\n"
"s_andn2_b32 s49,s60,0x18000\n"
RD"a[92:95],v237 offset:15360\n"
MF"v[0:3],a[64:67],a[212:215],v[0:3]\n"
WT"lgkmcnt(3)\n"
WT"vmcnt(0)\n"
"v_cmp_eq_u64 vcc,s[28:29],v[240:241]\n"
"s_cmp_eq_u64 vcc,exec\n"
"s_cbranch_scc0 L1083_%=\n"
"L1084_%=:\n"
MF"v[0:3],a[68:71],a[220:223],v[0:3]\n"
"s_or_b32 s52,s48,s49\n"
RD"a[96:99],v235 offset:20480\n"
SA"s53,s52,16384\n"
MF"v[40:43],a[68:71],a[224:227],v[40:43]\n"
BL"a[244:247],v224,s[8:11],s52 offen offset:0 sc1\n"
MF"v[40:43],a[64:67],a[216:219],v[40:43]\n"
RD"a[100:103],v235 offset:21504\n"
MF"v[4:7],a[72:75],a[212:215],v[4:7]\n"
MF"v[4:7],a[76:79],a[220:223],v[4:7]\n"
RD"a[104:107],v235 offset:22528\n"
MF"v[44:47],a[76:79],a[224:227],v[44:47]\n"
"v_bfe_u32 v243,v224,6,2\n"
MF"v[44:47],a[72:75],a[216:219],v[44:47]\n"
RD"a[108:111],v235 offset:23552\n"
"v_bfe_u32 v244,v224,4,2\n"
MF"v[0:3],a[80:83],a[228:231],v[0:3]\n"
WT"lgkmcnt(3)\n"
BL"a[248:251],v224,s[8:11],s53 offen offset:0 sc1\n"
MF"v[0:3],a[84:87],a[236:239],v[0:3]\n"
RD"a[112:115],v236 offset:8192\n"
"v_lshrrev_b32 v245,8,v224\n"
MF"v[40:43],a[84:87],a[240:243],v[40:43]\n"
"v_lshlrev_b32 v245,3,v245\n"
"v_sub_u32 v244,v244,v245\n"
MF"v[40:43],a[80:83],a[232:235],v[40:43]\n"
RD"a[116:119],v236 offset:9216\n"
"v_and_b32 v245,16,v224\n"
MF"v[4:7],a[88:91],a[228:231],v[4:7]\n"
"v_mov_b32 v246,0x3f80\n"
MF"v[4:7],a[92:95],a[236:239],v[4:7]\n"
RD"a[120:123],v236 offset:10240\n"
"v_lshlrev_b32 v245,v245,v246\n"
MF"v[44:47],a[92:95],a[240:243],v[44:47]\n"
BL"a[252:255],v224,s[8:11],s52 offen offset:1024 sc1\n"
MF"v[44:47],a[88:91],a[232:235],v[44:47]\n"
RD"a[124:127],v236 offset:11264\n"
"v_lshlrev_b32 v246,4,v243\n"
MF"v[8:11],a[96:99],a[180:183],v[8:11]\n"
WT"lgkmcnt(3)\n"
"v_and_b32 v246,16,v246\n"
"v_and_b32 v247,2,v243\n"
MF"v[8:11],a[100:103],a[188:191],v[8:11]\n"
SA"s90,s88,0\n"
RD"a[64:67],v236 offset:28672\n"
PK"v0,v0,v1\n"
MF"v[48:51],a[100:103],a[192:195],v[48:51]\n"
BL"a[128:131],v224,s[8:11],s53 offen offset:1024 sc1\n"
MF"v[48:51],a[96:99],a[184:187],v[48:51]\n"
RD"a[68:71],v236 offset:29696\n"
PK"v1,v2,v3\n"
MF"v[12:15],a[104:107],a[180:183],v[12:15]\n"
"v_lshl_add_u32 v246,v247,1,v246\n"
PK"v2,v4,v5\n"
MF"v[12:15],a[108:111],a[188:191],v[12:15]\n"
RD"a[72:75],v236 offset:30720\n"
PK"v3,v6,v7\n"
"v_add3_u32 v246,v246,v244,0\n"
MF"v[52:55],a[108:111],a[192:195],v[52:55]\n"
BS"v[0:3],v224,s[8:11],s90 offen sc1\n"
"v_ashrrev_i32 v246,1,v246\n"
MF"v[52:55],a[104:107],a[184:187],v[52:55]\n"
RD"a[76:79],v236 offset:31744\n"
"v_cmp_eq_u32_e64 vcc,0,v246\n"
MF"v[8:11],a[112:115],a[196:199],v[8:11]\n"
WT"lgkmcnt(3)\n"
BL"a[132:135],v224,s[8:11],s52 offen offset:2048 sc1\n"
"v_cndmask_b32_e64 v80,0,v245,vcc\n"
MF"v[8:11],a[116:119],a[204:207],v[8:11]\n"
SA"s91,s22,0\n"
RD"a[80:83],v237 offset:16384\n"
"v_cmp_eq_u32_e64 s[92:93],1,v246\n"
MF"v[48:51],a[116:119],a[208:211],v[48:51]\n"
"v_cmp_eq_u32_e64 s[94:95],2,v246\n"
"v_cndmask_b32_e64 v81,0,v245,s[92:93]\n"
MF"v[48:51],a[112:115],a[200:203],v[48:51]\n"
RD"a[84:87],v237 offset:17408\n"
PK"v40,v40,v41\n"
MF"v[12:15],a[120:123],a[196:199],v[12:15]\n"
PK"v41,v42,v43\n"
"v_cndmask_b32_e64 v82,0,v245,s[94:95]\n"
MF"v[12:15],a[124:127],a[204:207],v[12:15]\n"
RD"a[88:91],v237 offset:18432\n"
PK"v42,v44,v45\n"
PK"v43,v46,v47\n"
MF"v[52:55],a[124:127],a[208:211],v[52:55]\n"
BS"v[40:43],v224,s[8:11],s91 offen sc1\n"
"v_cmp_eq_u32_e64 vcc,3,v246\n"
MF"v[52:55],a[120:123],a[200:203],v[52:55]\n"
BL"a[136:139],v224,s[8:11],s53 offen offset:2048 sc1\n"
"v_cndmask_b32_e64 v83,0,v245,vcc\n"
MF"v[8:11],a[64:67],a[212:215],v[8:11]\n"
WT"lgkmcnt(2)\n"
RD"a[92:95],v237 offset:19456\n"
"v_lshlrev_b32 v246,4,v243\n"
"v_and_b32 v246,16,v246\n"
MF"v[8:11],a[68:71],a[220:223],v[8:11]\n"
RD"a[96:99],v235 offset:24576\n"
"v_and_b32 v247,2,v243\n"
MF"v[48:51],a[68:71],a[224:227],v[48:51]\n"
"v_lshl_add_u32 v246,v247,1,v246\n"
"v_add3_u32 v246,v246,v244,8\n"
MF"v[48:51],a[64:67],a[216:219],v[48:51]\n"
RD"a[100:103],v235 offset:25600\n"
"v_ashrrev_i32 v246,1,v246\n"
MF"v[12:15],a[72:75],a[212:215],v[12:15]\n"
BL"a[140:143],v224,s[8:11],s52 offen offset:3072 sc1\n"
MF"v[12:15],a[76:79],a[220:223],v[12:15]\n"
RD"a[104:107],v235 offset:26624\n"
"v_cmp_eq_u32_e64 vcc,0,v246\n"
MF"v[52:55],a[76:79],a[224:227],v[52:55]\n"
"v_cmp_eq_u32_e64 s[92:93],1,v246\n"
"v_cndmask_b32_e64 v88,0,v245,vcc\n"
MF"v[52:55],a[72:75],a[216:219],v[52:55]\n"
RD"a[108:111],v235 offset:27648\n"
"v_cndmask_b32_e64 v89,0,v245,s[92:93]\n"
MF"v[8:11],a[80:83],a[228:231],v[8:11]\n"
WT"lgkmcnt(3)\n"
BL"a[144:147],v224,s[8:11],s53 offen offset:3072 sc1\n"
MF"v[8:11],a[84:87],a[236:239],v[8:11]\n"
SA"s48,s60,65536\n"
RD"a[112:115],v236 offset:12288\n"
"v_cmp_eq_u32_e64 s[94:95],2,v246\n"
MF"v[48:51],a[84:87],a[240:243],v[48:51]\n"
"s_and_b32 s48,s48,0x18000\n"
"v_cndmask_b32_e64 v90,0,v245,s[94:95]\n"
"v_cmp_eq_u32_e64 vcc,3,v246\n"
MF"v[48:51],a[80:83],a[232:235],v[48:51]\n"
"s_andn2_b32 s49,s60,0x18000\n"
RD"a[116:119],v236 offset:13312\n"
"v_cndmask_b32_e64 v91,0,v245,vcc\n"
MF"v[12:15],a[88:91],a[228:231],v[12:15]\n"
"s_or_b32 s52,s48,s49\n"
"v_lshl_add_u32 v246,v243,3,v244\n"
"v_add_u32 v246,0,v246\n"
MF"v[12:15],a[92:95],a[236:239],v[12:15]\n"
SA"s53,s52,16384\n"
RD"a[120:123],v236 offset:14336\n"
"v_ashrrev_i32 v246,1,v246\n"
MF"v[52:55],a[92:95],a[240:243],v[52:55]\n"
BL"a[148:151],v224,s[8:11],s52 offen offset:0 sc1\n"
MF"v[52:55],a[88:91],a[232:235],v[52:55]\n"
RD"a[124:127],v236 offset:15360\n"
"v_cmp_eq_u32_e64 vcc,0,v246\n"
MF"v[16:19],a[96:99],a[180:183],v[16:19]\n"
WT"lgkmcnt(3)\n"
"v_cndmask_b32_e64 v96,0,v245,vcc\n"
"v_cmp_eq_u32_e64 s[92:93],1,v246\n"
MF"v[16:19],a[100:103],a[188:191],v[16:19]\n"
SA"s90,s88,1024\n"
RD"a[64:67],v237 offset:0\n"
"v_cndmask_b32_e64 v97,0,v245,s[92:93]\n"
MF"v[56:59],a[100:103],a[192:195],v[56:59]\n"
PK"v8,v8,v9\n"
PK"v9,v10,v11\n"
MF"v[56:59],a[96:99],a[184:187],v[56:59]\n"
RD"a[68:71],v237 offset:1024\n"
"v_cmp_eq_u32_e64 s[94:95],2,v246\n"
MF"v[20:23],a[104:107],a[180:183],v[20:23]\n"
BL"a[152:155],v224,s[8:11],s53 offen offset:0 sc1\n"
MF"v[20:23],a[108:111],a[188:191],v[20:23]\n"
RD"a[72:75],v237 offset:2048\n"
PK"v10,v12,v13\n"
PK"v11,v14,v15\n"
MF"v[60:63],a[108:111],a[192:195],v[60:63]\n"
BS"v[8:11],v224,s[8:11],s90 offen sc1\n"
"v_cndmask_b32_e64 v98,0,v245,s[94:95]\n"
MF"v[60:63],a[104:107],a[184:187],v[60:63]\n"
RD"a[76:79],v237 offset:3072\n"
"v_cmp_eq_u32_e64 vcc,3,v246\n"
MF"v[16:19],a[112:115],a[196:199],v[16:19]\n"
WT"lgkmcnt(3)\n"
"v_cndmask_b32_e64 v99,0,v245,vcc\n"
"v_lshl_add_u32 v246,v243,3,v244\n"
MF"v[16:19],a[116:119],a[204:207],v[16:19]\n"
SA"s91,s22,1024\n"
RD"a[80:83],v237 offset:20480\n"
"v_add_u32 v246,4,v246\n"
MF"v[56:59],a[116:119],a[208:211],v[56:59]\n"
BL"a[156:159],v224,s[8:11],s52 offen offset:1024 sc1\n"
"v_ashrrev_i32 v246,1,v246\n"
MF"v[56:59],a[112:115],a[200:203],v[56:59]\n"
RD"a[84:87],v237 offset:21504\n"
"v_cmp_eq_u32_e64 vcc,0,v246\n"
MF"v[20:23],a[120:123],a[196:199],v[20:23]\n"
PK"v48,v48,v49\n"
PK"v49,v50,v51\n"
"v_cndmask_b32_e64 v104,0,v245,vcc\n"
MF"v[20:23],a[124:127],a[204:207],v[20:23]\n"
RD"a[88:91],v237 offset:22528\n"
PK"v50,v52,v53\n"
PK"v51,v54,v55\n"
MF"v[60:63],a[124:127],a[208:211],v[60:63]\n"
BS"v[48:51],v224,s[8:11],s91 offen sc1\n"
"v_cmp_eq_u32_e64 s[92:93],1,v246\n"
MF"v[60:63],a[120:123],a[200:203],v[60:63]\n"
BL"a[160:163],v224,s[8:11],s53 offen offset:1024 sc1\n"
"v_cndmask_b32_e64 v105,0,v245,s[92:93]\n"
MF"v[16:19],a[64:67],a[212:215],v[16:19]\n"
WT"lgkmcnt(2)\n"
RD"a[92:95],v237 offset:23552\n"
"v_cmp_eq_u32_e64 s[94:95],2,v246\n"
MF"v[16:19],a[68:71],a[220:223],v[16:19]\n"
"s_mov_b32 s12,s64\n"
RD"a[96:99],v235 offset:28672\n"
"v_cndmask_b32_e64 v106,0,v245,s[94:95]\n"
"v_cmp_eq_u32_e64 vcc,3,v246\n"
MF"v[56:59],a[68:71],a[224:227],v[56:59]\n"
"s_and_b32 s13,s65,0xffff\n"
"v_cndmask_b32_e64 v107,0,v245,vcc\n"
"s_mov_b32 s14,2621440\n"
MF"v[56:59],a[64:67],a[216:219],v[56:59]\n"
"s_mov_b32 s15,0x20000\n"
RD"a[100:103],v235 offset:29696\n"
MF"v[20:23],a[72:75],a[212:215],v[20:23]\n"
"s_mov_b32 s16,s86\n"
BL"a[164:167],v224,s[8:11],s52 offen offset:2048 sc1\n"
MF"v[20:23],a[76:79],a[220:223],v[20:23]\n"
"s_and_b32 s17,s87,0xffff\n"
RD"a[104:107],v235 offset:30720\n"
MF"v[60:63],a[76:79],a[224:227],v[60:63]\n"
"s_mov_b32 s18,2621440\n"
MF"v[60:63],a[72:75],a[216:219],v[60:63]\n"
"s_mov_b32 s19,0x20000\n"
RD"a[108:111],v235 offset:31744\n"
MF"v[16:19],a[80:83],a[228:231],v[16:19]\n"
WT"lgkmcnt(3)\n"
"v_bfe_u32 v243,v224,4,4\n"
MF"v[16:19],a[84:87],a[236:239],v[16:19]\n"
"s_lshl_b32 s92,s34,3\n"
RD"a[112:115],v236 offset:16384\n"
MF"v[56:59],a[84:87],a[240:243],v[56:59]\n"
SA"s93,s92,s32\n"
BL"a[168:171],v224,s[8:11],s53 offen offset:2048 sc1\n"
MF"v[56:59],a[80:83],a[232:235],v[56:59]\n"
"s_sub_u32 s92,s32,s92\n"
RD"a[116:119],v236 offset:17408\n"
MF"v[20:23],a[88:91],a[228:231],v[20:23]\n"
SA"s92,s92,120\n"
MF"v[20:23],a[92:95],a[236:239],v[20:23]\n"
RD"a[120:123],v236 offset:18432\n"
"v_lshl_add_u32 v244,s93,4,v243\n"
MF"v[60:63],a[92:95],a[240:243],v[60:63]\n"
"v_lshl_add_u32 v245,s92,4,v243\n"
MF"v[60:63],a[88:91],a[232:235],v[60:63]\n"
"s_mul_i32 s93,s93,20480\n"
RD"a[124:127],v236 offset:19456\n"
MF"v[24:27],a[96:99],a[180:183],v[24:27]\n"
WT"lgkmcnt(3)\n"
BL"a[172:175],v224,s[8:11],s52 offen offset:3072 sc1\n"
MF"v[24:27],a[100:103],a[188:191],v[24:27]\n"
"s_mul_i32 s92,s92,20480\n"
RD"a[64:67],v237 offset:4096\n"
PK"v16,v16,v17\n"
MF"v[64:67],a[100:103],a[192:195],v[64:67]\n"
SA"s90,s88,2048\n"
PK"v17,v18,v19\n"
"v_add_u32 v178,s93,v224\n"
MF"v[64:67],a[96:99],a[184:187],v[64:67]\n"
RD"a[68:71],v237 offset:5120\n"
PK"v18,v20,v21\n"
MF"v[28:31],a[104:107],a[180:183],v[28:31]\n"
BL"a[176:179],v224,s[8:11],s53 offen offset:3072 sc1\n"
MF"v[28:31],a[108:111],a[188:191],v[28:31]\n"
SA"s48,s60,98304\n"
RD"a[72:75],v237 offset:6144\n"
PK"v19,v22,v23\n"
"v_add_u32 v179,s92,v224\n"
MF"v[68:71],a[108:111],a[192:195],v[68:71]\n"
"s_and_b32 s48,s48,0x18000\n"
BS"v[16:19],v224,s[8:11],s90 offen sc1\n"
"v_lshrrev_b32 v243,4,v224\n"
MF"v[68:71],a[104:107],a[184:187],v[68:71]\n"
"s_andn2_b32 s49,s60,0x18000\n"
RD"a[76:79],v237 offset:7168\n"
"v_and_b32 v243,48,v243\n"
MF"v[24:27],a[112:115],a[196:199],v[24:27]\n"
WT"lgkmcnt(3)\n"
"v_mul_u32_u24 v176,1280,v244\n"
"v_mul_u32_u24 v177,1280,v245\n"
MF"v[24:27],a[116:119],a[204:207],v[24:27]\n"
"s_or_b32 s52,s48,s49\n"
RD"a[80:83],v237 offset:24576\n"
"v_add_u32 v176,v176,v243\n"
MF"v[64:67],a[116:119],a[208:211],v[64:67]\n"
SA"s53,s52,16384\n"
BL"a[32:35],v224,s[8:11],s52 offen offset:0 sc1\n"
MF"v[64:67],a[112:115],a[200:203],v[64:67]\n"
SA"s91,s22,2048\n"
RD"a[84:87],v237 offset:25600\n"
"v_add_u32 v177,v177,v243\n"
MF"v[28:31],a[120:123],a[196:199],v[28:31]\n"
PK"v56,v56,v57\n"
PK"v57,v58,v59\n"
"v_and_b32 v243,0x300,v224\n"
MF"v[28:31],a[124:127],a[204:207],v[28:31]\n"
RD"a[88:91],v237 offset:26624\n"
PK"v58,v60,v61\n"
PK"v59,v62,v63\n"
MF"v[68:71],a[124:127],a[208:211],v[68:71]\n"
BS"v[56:59],v224,s[8:11],s91 offen sc1\n"
"v_add_u32 v243,2684928,v243\n"
MF"v[68:71],a[120:123],a[200:203],v[68:71]\n"
RD"a[92:95],v237 offset:27648\n"
"v_sub_u32 v244,2047,v244\n"
"v_sub_u32 v245,2047,v245\n"
MF"v[24:27],a[64:67],a[212:215],v[24:27]\n"
WT"lgkmcnt(3)\n"
BL"a[36:39],v224,s[8:11],s53 offen offset:0 sc1\n"
"v_readlane_b32 s92,v234,0\n"
MF"v[24:27],a[68:71],a[220:223],v[24:27]\n"
"s_and_b32 s92,s92,0xffff\n"
RD"a[96:99],v236 offset:0\n"
"v_cmp_eq_u32 vcc,s92,v244\n"
MF"v[64:67],a[68:71],a[224:227],v[64:67]\n"
"v_cmp_eq_u32_e64 s[94:95],s92,v245\n"
"v_cndmask_b32 v178,v178,v243,vcc\n"
MF"v[64:67],a[64:67],a[216:219],v[64:67]\n"
RD"a[100:103],v236 offset:1024\n"
"v_cndmask_b32_e64 v179,v179,v243,s[94:95]\n"
MF"v[28:31],a[72:75],a[212:215],v[28:31]\n"
"v_add_u32 v243,16,v243\n"
"v_readlane_b32 s92,v234,1\n"
MF"v[28:31],a[76:79],a[220:223],v[28:31]\n"
"s_and_b32 s92,s92,0xffff\n"
RD"a[104:107],v236 offset:2048\n"
MF"v[68:71],a[76:79],a[224:227],v[68:71]\n"
BL"a[40:43],v224,s[8:11],s52 offen offset:1024 sc1\n"
MF"v[68:71],a[72:75],a[216:219],v[68:71]\n"
RD"a[108:111],v236 offset:3072\n"
"v_cmp_eq_u32 vcc,s92,v244\n"
MF"v[24:27],a[80:83],a[228:231],v[24:27]\n"
WT"lgkmcnt(3)\n"
"v_cmp_eq_u32_e64 s[94:95],s92,v245\n"
"v_cndmask_b32 v178,v178,v243,vcc\n"
MF"v[24:27],a[84:87],a[236:239],v[24:27]\n"
RD"a[112:115],v236 offset:20480\n"
"v_cndmask_b32_e64 v179,v179,v243,s[94:95]\n"
MF"v[64:67],a[84:87],a[240:243],v[64:67]\n"
BL"a[44:47],v224,s[8:11],s53 offen offset:1024 sc1\n"
MF"v[64:67],a[80:83],a[232:235],v[64:67]\n"
RD"a[116:119],v236 offset:21504\n"
"v_add_u32 v243,16,v243\n"
MF"v[28:31],a[88:91],a[228:231],v[28:31]\n"
"v_readlane_b32 s92,v234,2\n"
"s_and_b32 s92,s92,0xffff\n"
MF"v[28:31],a[92:95],a[236:239],v[28:31]\n"
RD"a[120:123],v236 offset:22528\n"
"v_cmp_eq_u32 vcc,s92,v244\n"
MF"v[68:71],a[92:95],a[240:243],v[68:71]\n"
"v_cmp_eq_u32_e64 s[94:95],s92,v245\n"
MF"v[68:71],a[88:91],a[232:235],v[68:71]\n"
RD"a[124:127],v236 offset:23552\n"
"v_cndmask_b32 v178,v178,v243,vcc\n"
MF"v[32:35],a[96:99],a[180:183],v[32:35]\n"
WT"lgkmcnt(3)\n"
BL"a[48:51],v224,s[8:11],s52 offen offset:2048 sc1\n"
MF"v[32:35],a[100:103],a[188:191],v[32:35]\n"
SA"s90,s88,3072\n"
RD"a[64:67],v237 offset:8192\n"
"v_cndmask_b32_e64 v179,v179,v243,s[94:95]\n"
MF"v[72:75],a[100:103],a[192:195],v[72:75]\n"
PK"v24,v24,v25\n"
PK"v25,v26,v27\n"
MF"v[72:75],a[96:99],a[184:187],v[72:75]\n"
RD"a[68:71],v237 offset:9216\n"
"v_add_u32 v243,16,v243\n"
MF"v[36:39],a[104:107],a[180:183],v[36:39]\n"
PK"v26,v28,v29\n"
PK"v27,v30,v31\n"
MF"v[36:39],a[108:111],a[188:191],v[36:39]\n"
BS"v[24:27],v224,s[8:11],s90 offen sc1\n"
"v_readlane_b32 s92,v234,3\n"
MF"v[76:79],a[108:111],a[192:195],v[76:79]\n"
"s_and_b32 s92,s92,0xffff\n"
RD"a[72:75],v237 offset:10240\n"
"v_cmp_eq_u32 vcc,s92,v244\n"
MF"v[76:79],a[104:107],a[184:187],v[76:79]\n"
BL"a[52:55],v224,s[8:11],s53 offen offset:2048 sc1\n"
MF"v[32:35],a[112:115],a[196:199],v[32:35]\n"
WT"lgkmcnt(2)\n"
RD"a[76:79],v237 offset:11264\n"
"v_cmp_eq_u32_e64 s[94:95],s92,v245\n"
MF"v[32:35],a[116:119],a[204:207],v[32:35]\n"
SA"s91,s22,3072\n"
RD"a[80:83],v237 offset:28672\n"
"v_cndmask_b32 v178,v178,v243,vcc\n"
MF"v[72:75],a[116:119],a[208:211],v[72:75]\n"
"v_cndmask_b32_e64 v179,v179,v243,s[94:95]\n"
"v_add_u32 v243,16,v243\n"
MF"v[72:75],a[112:115],a[200:203],v[72:75]\n"
RD"a[84:87],v237 offset:29696\n"
PK"v64,v64,v65\n"
MF"v[36:39],a[120:123],a[196:199],v[36:39]\n"
BL"a[56:59],v224,s[8:11],s52 offen offset:3072 sc1\n"
PK"v65,v66,v67\n"
MF"v[36:39],a[124:127],a[204:207],v[36:39]\n"
RD"a[88:91],v237 offset:30720\n"
"v_readlane_b32 s92,v234,4\n"
PK"v66,v68,v69\n"
MF"v[76:79],a[124:127],a[208:211],v[76:79]\n"
"s_and_b32 s92,s92,0xffff\n"
PK"v67,v70,v71\n"
"v_cmp_eq_u32 vcc,s92,v244\n"
MF"v[76:79],a[120:123],a[200:203],v[76:79]\n"
BS"v[64:67],v224,s[8:11],s91 offen sc1\n"
"v_cmp_eq_u32_e64 s[94:95],s92,v245\n"
MF"v[32:35],a[64:67],a[212:215],v[32:35]\n"
WT"lgkmcnt(2)\n"
RD"a[92:95],v237 offset:31744\n"
"v_cndmask_b32 v178,v178,v243,vcc\n"
MF"v[32:35],a[68:71],a[220:223],v[32:35]\n"
BL"a[60:63],v224,s[8:11],s53 offen offset:3072 sc1\n"
"v_cndmask_b32_e64 v179,v179,v243,s[94:95]\n"
MF"v[72:75],a[68:71],a[224:227],v[72:75]\n"
RD"a[96:99],v226 offset:8192\n"
"v_add_u32 v243,16,v243\n"
MF"v[72:75],a[64:67],a[216:219],v[72:75]\n"
RD"a[100:103],v226 offset:9216\n"
"v_readlane_b32 s92,v234,5\n"
MF"v[36:39],a[72:75],a[212:215],v[36:39]\n"
"s_and_b32 s92,s92,0xffff\n"
"v_cmp_eq_u32 vcc,s92,v244\n"
"v_cmp_eq_u32_e64 s[94:95],s92,v245\n"
MF"v[36:39],a[76:79],a[220:223],v[36:39]\n"
RD"a[104:107],v226 offset:10240\n"
"v_cndmask_b32 v178,v178,v243,vcc\n"
MF"v[76:79],a[76:79],a[224:227],v[76:79]\n"
"v_cndmask_b32_e64 v179,v179,v243,s[94:95]\n"
"v_add_u32 v243,16,v243\n"
MF"v[76:79],a[72:75],a[216:219],v[76:79]\n"
RD"a[108:111],v226 offset:11264\n"
"v_readlane_b32 s92,v234,6\n"
MF"v[32:35],a[80:83],a[228:231],v[32:35]\n"
WT"lgkmcnt(4)\n"
"s_and_b32 s92,s92,0xffff\n"
MF"v[32:35],a[84:87],a[236:239],v[32:35]\n"
RD"a[112:115],v226 offset:28672\n"
"v_cmp_eq_u32 vcc,s92,v244\n"
MF"v[72:75],a[84:87],a[240:243],v[72:75]\n"
"v_cmp_eq_u32_e64 s[94:95],s92,v245\n"
MF"v[72:75],a[80:83],a[232:235],v[72:75]\n"
RD"a[116:119],v226 offset:29696\n"
"v_cndmask_b32 v178,v178,v243,vcc\n"
MF"v[36:39],a[88:91],a[228:231],v[36:39]\n"
"v_cndmask_b32_e64 v179,v179,v243,s[94:95]\n"
MF"v[36:39],a[92:95],a[236:239],v[36:39]\n"
RD"a[120:123],v226 offset:30720\n"
"v_add_u32 v243,16,v243\n"
MF"v[76:79],a[92:95],a[240:243],v[76:79]\n"
"v_readlane_b32 s92,v234,7\n"
MF"v[76:79],a[88:91],a[232:235],v[76:79]\n"
WT"vmcnt(31) lgkmcnt(3)\n"
RD"a[124:127],v226 offset:31744\n"
"s_and_b32 s92,s92,0xffff\n"
MF"v[84:87],a[96:99],a[244:247],v[84:87]\n"
WT"vmcnt(29)\n"
"v_cmp_eq_u32 vcc,s92,v244\n"
MF"v[84:87],a[100:103],a[252:255],v[84:87]\n"
WT"vmcnt(28)\n"
RD"a[64:67],v235 offset:16384\n"
PK"v32,v32,v33\n"
MF"v[164:167],a[100:103],a[128:131],v[164:167]\n"
SA"s90,s88,4096\n"
PK"v33,v34,v35\n"
"v_cmp_eq_u32_e64 s[94:95],s92,v245\n"
MF"v[164:167],a[96:99],a[248:251],v[164:167]\n"
RD"a[68:71],v235 offset:17408\n"
PK"v34,v36,v37\n"
MF"v[92:95],a[104:107],a[244:247],v[92:95]\n"
"s_mul_i32 s92,s33,5\n"
PK"v35,v38,v39\n"
"v_cndmask_b32 v178,v178,v243,vcc\n"
MF"v[92:95],a[108:111],a[252:255],v[92:95]\n"
SA"s93,s92,s30\n"
BS"v[32:35],v224,s[8:11],s90 offen sc1\n"
MF"v[172:175],a[108:111],a[128:131],v[172:175]\n"
SA"s92,s92,4\n"
RD"a[72:75],v235 offset:18432\n"
"v_cndmask_b32_e64 v179,v179,v243,s[94:95]\n"
MF"v[172:175],a[104:107],a[248:251],v[172:175]\n"
WT"vmcnt(27) lgkmcnt(3)\n"
RD"a[76:79],v235 offset:19456\n"
"s_lshl_b32 s38,s93,6\n"
MF"v[84:87],a[112:115],a[132:135],v[84:87]\n"
WT"vmcnt(24)\n"
"s_lshl_b32 s55,s92,6\n"
MF"v[84:87],a[116:119],a[140:143],v[84:87]\n"
WT"vmcnt(23)\n"
RD"a[80:83],v236 offset:4096\n"
PK"v72,v72,v73\n"
MF"v[164:167],a[116:119],a[144:147],v[164:167]\n"
SA"s91,s22,4096\n"
PK"v73,v74,v75\n"
PK"v74,v76,v77\n"
MF"v[164:167],a[112:115],a[136:139],v[164:167]\n"
SA"s55,s55,s45\n"
RD"a[84:87],v236 offset:5120\n"
PK"v75,v78,v79\n"
MF"v[92:95],a[120:123],a[132:135],v[92:95]\n"
"s_lshl_b32 s36,s93,10\n"
BS"v[72:75],v224,s[8:11],s91 offen sc1\n"
MF"v[92:95],a[124:127],a[140:143],v[92:95]\n"
"s_lshl_b32 s37,s92,10\n"
RD"a[88:91],v236 offset:6144\n"
SA"s36,s36,65536\n"
MF"v[172:175],a[124:127],a[144:147],v[172:175]\n"
SA"s37,s37,65536\n"
SA"s37,s37,s45\n"
MF"v[172:175],a[120:123],a[136:139],v[172:175]\n"
WT"vmcnt(23) lgkmcnt(3)\n"
RD"a[92:95],v236 offset:7168\n"
"v_lshrrev_b32 v243,1,v224\n"
MF"v[84:87],a[64:67],a[148:151],v[84:87]\n"
WT"vmcnt(20)\n"
"v_min_u32 v244,312,v243\n"
MF"v[84:87],a[68:71],a[156:159],v[84:87]\n"
WT"vmcnt(18)\n"
RD"a[96:99],v236 offset:24576\n"
"v_and_b32 v243,56,v243\n"
MF"v[164:167],a[68:71],a[160:163],v[164:167]\n"
"v_lshlrev_b32 v243,3,v243\n"
MF"v[164:167],a[64:67],a[152:155],v[164:167]\n"
"s_mov_b32 s92,6400\n"
RD"a[100:103],v236 offset:25600\n"
MF"v[92:95],a[72:75],a[148:151],v[92:95]\n"
MF"v[92:95],a[76:79],a[156:159],v[92:95]\n"
RD"a[104:107],v236 offset:26624\n"
MF"v[172:175],a[76:79],a[160:163],v[172:175]\n"
MF"v[172:175],a[72:75],a[152:155],v[172:175]\n"
WT"vmcnt(17) lgkmcnt(3)\n"
RD"a[108:111],v236 offset:27648\n"
MF"v[84:87],a[80:83],a[164:167],v[84:87]\n"
WT"vmcnt(15)\n"
MF"v[84:87],a[84:87],a[172:175],v[84:87]\n"
WT"vmcnt(14)\n"
RD"a[112:115],v237 offset:12288\n"
MF"v[164:167],a[84:87],a[176:179],v[164:167]\n"
MF"v[164:167],a[80:83],a[168:171],v[164:167]\n"
RD"a[116:119],v237 offset:13312\n"
MF"v[92:95],a[88:91],a[164:167],v[92:95]\n"
MF"v[92:95],a[92:95],a[172:175],v[92:95]\n"
RD"a[120:123],v237 offset:14336\n"
MF"v[172:175],a[92:95],a[176:179],v[172:175]\n"
MF"v[172:175],a[88:91],a[168:171],v[172:175]\n"
WT"vmcnt(12) lgkmcnt(3)\n"
RD"a[124:127],v237 offset:15360\n"
MF"v[84:87],a[96:99],a[32:35],v[84:87]\n"
WT"vmcnt(9)\n"
MF"v[84:87],a[100:103],a[40:43],v[84:87]\n"
WT"vmcnt(8)\n"
RD"a[64:67],v226 offset:12288\n"
MF"v[164:167],a[100:103],a[44:47],v[164:167]\n"
MF"v[164:167],a[96:99],a[36:39],v[164:167]\n"
RD"a[68:71],v226 offset:13312\n"
MF"v[92:95],a[104:107],a[32:35],v[92:95]\n"
MF"v[92:95],a[108:111],a[40:43],v[92:95]\n"
RD"a[72:75],v226 offset:14336\n"
MF"v[172:175],a[108:111],a[44:47],v[172:175]\n"
MF"v[172:175],a[104:107],a[36:39],v[172:175]\n"
WT"vmcnt(7) lgkmcnt(3)\n"
RD"a[76:79],v226 offset:15360\n"
MF"v[84:87],a[112:115],a[48:51],v[84:87]\n"
WT"vmcnt(4)\n"
"buffer_load_dwordx2 v[240:241],v244,s[8:11],s92 offen sc1\n"
MF"v[84:87],a[116:119],a[56:59],v[84:87]\n"
WT"vmcnt(3)\n"
RD"a[80:83],v235 offset:0\n"
MF"v[164:167],a[116:119],a[60:63],v[164:167]\n"
MF"v[164:167],a[112:115],a[52:55],v[164:167]\n"
RD"a[84:87],v235 offset:1024\n"
MF"v[92:95],a[120:123],a[48:51],v[92:95]\n"
MF"v[92:95],a[124:127],a[56:59],v[92:95]\n"
RD"a[88:91],v235 offset:2048\n"
MF"v[172:175],a[124:127],a[60:63],v[172:175]\n"
MF"v[172:175],a[120:123],a[52:55],v[172:175]\n"
WT"lgkmcnt(3)\n"
RD"a[92:95],v235 offset:3072\n"
MF"v[100:103],a[64:67],a[244:247],v[100:103]\n"
MF"v[100:103],a[68:71],a[252:255],v[100:103]\n"
SA"s90,s88,327680\n"
RD"a[96:99],v235 offset:20480\n"
PK"v84,v84,v85\n"
MF"v[180:183],a[68:71],a[128:131],v[180:183]\n"
PK"v85,v86,v87\n"
PK"v86,v92,v93\n"
MF"v[180:183],a[64:67],a[248:251],v[180:183]\n"
RD"a[100:103],v235 offset:21504\n"
PK"v87,v94,v95\n"
MF"v[108:111],a[72:75],a[244:247],v[108:111]\n"
BS"v[84:87],v224,s[8:11],s90 offen sc1\n"
MF"v[108:111],a[76:79],a[252:255],v[108:111]\n"
RD"a[104:107],v235 offset:22528\n"
MF"v[188:191],a[76:79],a[128:131],v[188:191]\n"
MF"v[188:191],a[72:75],a[248:251],v[188:191]\n"
WT"lgkmcnt(3)\n"
RD"a[108:111],v235 offset:23552\n"
MF"v[100:103],a[80:83],a[132:135],v[100:103]\n"
MF"v[100:103],a[84:87],a[140:143],v[100:103]\n"
SA"s91,s22,327680\n"
RD"a[112:115],v236 offset:8192\n"
PK"v164,v164,v165\n"
MF"v[180:183],a[84:87],a[144:147],v[180:183]\n"
PK"v165,v166,v167\n"
PK"v166,v172,v173\n"
MF"v[180:183],a[80:83],a[136:139],v[180:183]\n"
RD"a[116:119],v236 offset:9216\n"
PK"v167,v174,v175\n"
MF"v[108:111],a[88:91],a[132:135],v[108:111]\n"
BS"v[164:167],v224,s[8:11],s91 offen sc1\n"
MF"v[108:111],a[92:95],a[140:143],v[108:111]\n"
RD"a[120:123],v236 offset:10240\n"
MF"v[188:191],a[92:95],a[144:147],v[188:191]\n"
MF"v[188:191],a[88:91],a[136:139],v[188:191]\n"
WT"lgkmcnt(3)\n"
RD"a[124:127],v236 offset:11264\n"
MF"v[100:103],a[96:99],a[148:151],v[100:103]\n"
MF"v[100:103],a[100:103],a[156:159],v[100:103]\n"
RD"a[64:67],v236 offset:28672\n"
MF"v[180:183],a[100:103],a[160:163],v[180:183]\n"
WT"vmcnt(3)\n"
"buffer_store_dwordx2 v[232:233],v239,s[8:11],s89 offen sc1\n"
MF"v[180:183],a[96:99],a[152:155],v[180:183]\n"
RD"a[68:71],v236 offset:29696\n"
MF"v[108:111],a[104:107],a[148:151],v[108:111]\n"
MF"v[108:111],a[108:111],a[156:159],v[108:111]\n"
RD"a[72:75],v236 offset:30720\n"
MF"v[188:191],a[108:111],a[160:163],v[188:191]\n"
MF"v[188:191],a[104:107],a[152:155],v[188:191]\n"
WT"lgkmcnt(3)\n"
RD"a[76:79],v236 offset:31744\n"
MF"v[100:103],a[112:115],a[164:167],v[100:103]\n"
MF"v[100:103],a[116:119],a[172:175],v[100:103]\n"
RD"a[80:83],v237 offset:16384\n"
MF"v[180:183],a[116:119],a[176:179],v[180:183]\n"
MF"v[180:183],a[112:115],a[168:171],v[180:183]\n"
RD"a[84:87],v237 offset:17408\n"
MF"v[108:111],a[120:123],a[164:167],v[108:111]\n"
MF"v[108:111],a[124:127],a[172:175],v[108:111]\n"
RD"a[88:91],v237 offset:18432\n"
MF"v[188:191],a[124:127],a[176:179],v[188:191]\n"
MF"v[188:191],a[120:123],a[168:171],v[188:191]\n"
WT"lgkmcnt(3)\n"
RD"a[92:95],v237 offset:19456\n"
MF"v[100:103],a[64:67],a[32:35],v[100:103]\n"
MF"v[100:103],a[68:71],a[40:43],v[100:103]\n"
RD"a[96:99],v226 offset:16384\n"
MF"v[180:183],a[68:71],a[44:47],v[180:183]\n"
MF"v[180:183],a[64:67],a[36:39],v[180:183]\n"
RD"a[100:103],v226 offset:17408\n"
MF"v[108:111],a[72:75],a[32:35],v[108:111]\n"
MF"v[108:111],a[76:79],a[40:43],v[108:111]\n"
RD"a[104:107],v226 offset:18432\n"
MF"v[188:191],a[76:79],a[44:47],v[188:191]\n"
MF"v[188:191],a[72:75],a[36:39],v[188:191]\n"
WT"lgkmcnt(3)\n"
RD"a[108:111],v226 offset:19456\n"
MF"v[100:103],a[80:83],a[48:51],v[100:103]\n"
MF"v[100:103],a[84:87],a[56:59],v[100:103]\n"
RD"a[112:115],v235 offset:4096\n"
MF"v[180:183],a[84:87],a[60:63],v[180:183]\n"
MF"v[180:183],a[80:83],a[52:55],v[180:183]\n"
RD"a[116:119],v235 offset:5120\n"
MF"v[108:111],a[88:91],a[48:51],v[108:111]\n"
MF"v[108:111],a[92:95],a[56:59],v[108:111]\n"
RD"a[120:123],v235 offset:6144\n"
MF"v[188:191],a[92:95],a[60:63],v[188:191]\n"
MF"v[188:191],a[88:91],a[52:55],v[188:191]\n"
WT"lgkmcnt(3)\n"
RD"a[124:127],v235 offset:7168\n"
MF"v[116:119],a[96:99],a[244:247],v[116:119]\n"
MF"v[116:119],a[100:103],a[252:255],v[116:119]\n"
SA"s90,s88,328704\n"
RD"a[64:67],v235 offset:24576\n"
PK"v100,v100,v101\n"
MF"v[196:199],a[100:103],a[128:131],v[196:199]\n"
PK"v101,v102,v103\n"
PK"v102,v108,v109\n"
MF"v[196:199],a[96:99],a[248:251],v[196:199]\n"
RD"a[68:71],v235 offset:25600\n"
PK"v103,v110,v111\n"
MF"v[124:127],a[104:107],a[244:247],v[124:127]\n"
BS"v[100:103],v224,s[8:11],s90 offen sc1\n"
MF"v[124:127],a[108:111],a[252:255],v[124:127]\n"
WT"vmcnt(4)\n"
"v_cmp_eq_u64 vcc,s[28:29],v[240:241]\n"
"s_cmp_eq_u64 vcc,exec\n"
"s_cbranch_scc0 L1086_%=\n"
"L1087_%=:\n"
MF"v[204:207],a[108:111],a[128:131],v[204:207]\n"
RD"a[72:75],v235 offset:26624\n"
MF"v[204:207],a[104:107],a[248:251],v[204:207]\n"
WT"lgkmcnt(3)\n"
RD"a[76:79],v235 offset:27648\n"
MF"v[116:119],a[112:115],a[132:135],v[116:119]\n"
BL"v[112:115],v176,s[12:15],s38 offen\n"
MF"v[116:119],a[116:119],a[140:143],v[116:119]\n"
SA"s91,s22,328704\n"
RD"a[80:83],v236 offset:12288\n"
PK"v180,v180,v181\n"
MF"v[196:199],a[116:119],a[144:147],v[196:199]\n"
BL"v[144:147],v178,s[8:11],s36 offen sc1\n"
MF"v[196:199],a[112:115],a[136:139],v[196:199]\n"
RD"a[84:87],v236 offset:13312\n"
PK"v181,v182,v183\n"
MF"v[124:127],a[120:123],a[132:135],v[124:127]\n"
PK"v182,v188,v189\n"
PK"v183,v190,v191\n"
MF"v[124:127],a[124:127],a[140:143],v[124:127]\n"
BS"v[180:183],v224,s[8:11],s91 offen sc1\n"
MF"v[204:207],a[124:127],a[144:147],v[204:207]\n"
RD"a[88:91],v236 offset:14336\n"
MF"v[204:207],a[120:123],a[136:139],v[204:207]\n"
WT"lgkmcnt(3)\n"
BL"v[120:123],v176,s[12:15],s55 offen\n"
MF"v[116:119],a[64:67],a[148:151],v[116:119]\n"
RD"a[92:95],v236 offset:15360\n"
MF"v[116:119],a[68:71],a[156:159],v[116:119]\n"
BL"v[152:155],v178,s[8:11],s37 offen sc1\n"
MF"v[196:199],a[68:71],a[160:163],v[196:199]\n"
RD"a[96:99],v237 offset:0\n"
MF"v[196:199],a[64:67],a[152:155],v[196:199]\n"
RD"a[100:103],v237 offset:1024\n"
MF"v[124:127],a[72:75],a[148:151],v[124:127]\n"
BL"v[128:131],v177,s[12:15],s38 offen\n"
MF"v[124:127],a[76:79],a[156:159],v[124:127]\n"
RD"a[104:107],v237 offset:2048\n"
MF"v[204:207],a[76:79],a[160:163],v[204:207]\n"
BL"v[160:163],v179,s[8:11],s36 offen sc1\n"
MF"v[204:207],a[72:75],a[152:155],v[204:207]\n"
WT"lgkmcnt(3)\n"
RD"a[108:111],v237 offset:3072\n"
MF"v[116:119],a[80:83],a[164:167],v[116:119]\n"
MF"v[116:119],a[84:87],a[172:175],v[116:119]\n"
RD"a[112:115],v237 offset:20480\n"
MF"v[196:199],a[84:87],a[176:179],v[196:199]\n"
BL"v[136:139],v177,s[12:15],s55 offen\n"
MF"v[196:199],a[80:83],a[168:171],v[196:199]\n"
RD"a[116:119],v237 offset:21504\n"
MF"v[124:127],a[88:91],a[164:167],v[124:127]\n"
BL"v[168:171],v179,s[8:11],s37 offen sc1\n"
MF"v[124:127],a[92:95],a[172:175],v[124:127]\n"
RD"a[120:123],v237 offset:22528\n"
MF"v[204:207],a[92:95],a[176:179],v[204:207]\n"
MF"v[204:207],a[88:91],a[168:171],v[204:207]\n"
WT"lgkmcnt(3)\n"
RD"a[124:127],v237 offset:23552\n"
MF"v[116:119],a[96:99],a[32:35],v[116:119]\n"
MF"v[116:119],a[100:103],a[40:43],v[116:119]\n"
RD"a[64:67],v226 offset:20480\n"
MF"v[196:199],a[100:103],a[44:47],v[196:199]\n"
"s_lshr_b32 s93,s31,3\n"
MF"v[196:199],a[96:99],a[36:39],v[196:199]\n"
"s_lshl_b32 s93,s93,4\n"
RD"a[68:71],v226 offset:21504\n"
MF"v[124:127],a[104:107],a[32:35],v[124:127]\n"
SA"s93,s93,s32\n"
MF"v[124:127],a[108:111],a[40:43],v[124:127]\n"
"s_mul_i32 s94,s93,40960\n"
RD"a[72:75],v226 offset:22528\n"
MF"v[204:207],a[108:111],a[44:47],v[204:207]\n"
"s_lshl_b32 s93,s93,9\n"
MF"v[204:207],a[104:107],a[36:39],v[204:207]\n"
WT"lgkmcnt(3)\n"
RD"a[76:79],v226 offset:23552\n"
SA"s93,s93,80478208\n"
MF"v[116:119],a[112:115],a[48:51],v[116:119]\n"
SA"s94,s94,59506688\n"
MF"v[116:119],a[116:119],a[56:59],v[116:119]\n"
"s_lshl_b32 s92,s30,10\n"
RD"a[80:83],v235 offset:8192\n"
MF"v[196:199],a[116:119],a[60:63],v[196:199]\n"
"v_add_u32 v244,s92,v224\n"
MF"v[196:199],a[112:115],a[52:55],v[196:199]\n"
SA"s92,s45,4096\n"
RD"a[84:87],v235 offset:9216\n"
MF"v[124:127],a[120:123],a[48:51],v[124:127]\n"
"v_add_u32 v245,s92,v224\n"
MF"v[124:127],a[124:127],a[56:59],v[124:127]\n"
RD"a[88:91],v235 offset:10240\n"
MF"v[204:207],a[124:127],a[60:63],v[204:207]\n"
"v_and_b32 v246,128,v224\n"
MF"v[204:207],a[120:123],a[52:55],v[204:207]\n"
WT"lgkmcnt(3)\n"
RD"a[92:95],v235 offset:11264\n"
"v_lshl_add_u32 v243,v246,5,v243\n"
MF"v[132:135],a[64:67],a[244:247],v[132:135]\n"
"v_lshrrev_b32 v246,8,v224\n"
MF"v[132:135],a[68:71],a[252:255],v[132:135]\n"
SA"s90,s88,329728\n"
RD"a[96:99],v235 offset:28672\n"
PK"v116,v116,v117\n"
MF"v[208:211],a[68:71],a[128:131],v[208:211]\n"
"s_mov_b32 s95,0xffff0101\n"
PK"v117,v118,v119\n"
"v_lshl_add_u32 v243,v246,27,v243\n"
MF"v[208:211],a[64:67],a[248:251],v[208:211]\n"
"s_lshl_b32 s92,s30,10\n"
RD"a[100:103],v235 offset:29696\n"
PK"v118,v124,v125\n"
MF"v[140:143],a[72:75],a[244:247],v[140:143]\n"
PK"v119,v126,v127\n"
"v_add_u32 v247,s92,v227\n"
MF"v[140:143],a[76:79],a[252:255],v[140:143]\n"
BS"v[116:119],v224,s[8:11],s90 offen sc1\n"
MF"v[212:215],a[76:79],a[128:131],v[212:215]\n"
RD"a[104:107],v235 offset:30720\n"
MF"v[212:215],a[72:75],a[248:251],v[212:215]\n"
WT"lgkmcnt(3)\n"
"buffer_load_dwordx2 v[240:241],v243,s[8:11],s93 offen sc1\n"
MF"v[132:135],a[80:83],a[132:135],v[132:135]\n"
RD"a[108:111],v235 offset:31744\n"
MF"v[132:135],a[84:87],a[140:143],v[132:135]\n"
SA"s91,s22,329728\n"
RD"a[112:115],v236 offset:16384\n"
PK"v196,v196,v197\n"
MF"v[208:211],a[84:87],a[144:147],v[208:211]\n"
PK"v197,v198,v199\n"
PK"v198,v204,v205\n"
MF"v[208:211],a[80:83],a[136:139],v[208:211]\n"
RD"a[116:119],v236 offset:17408\n"
PK"v199,v206,v207\n"
MF"v[140:143],a[88:91],a[132:135],v[140:143]\n"
BS"v[196:199],v224,s[8:11],s91 offen sc1\n"
MF"v[140:143],a[92:95],a[140:143],v[140:143]\n"
RD"a[120:123],v236 offset:18432\n"
MF"v[212:215],a[92:95],a[144:147],v[212:215]\n"
MF"v[212:215],a[88:91],a[136:139],v[212:215]\n"
WT"lgkmcnt(3)\n"
RD"a[124:127],v236 offset:19456\n"
MF"v[132:135],a[96:99],a[148:151],v[132:135]\n"
MF"v[132:135],a[100:103],a[156:159],v[132:135]\n"
RD"a[64:67],v237 offset:4096\n"
MF"v[208:211],a[100:103],a[160:163],v[208:211]\n"
MF"v[208:211],a[96:99],a[152:155],v[208:211]\n"
RD"a[68:71],v237 offset:5120\n"
MF"v[140:143],a[104:107],a[148:151],v[140:143]\n"
MF"v[140:143],a[108:111],a[156:159],v[140:143]\n"
RD"a[72:75],v237 offset:6144\n"
MF"v[212:215],a[108:111],a[160:163],v[212:215]\n"
MF"v[212:215],a[104:107],a[152:155],v[212:215]\n"
WT"lgkmcnt(3)\n"
RD"a[76:79],v237 offset:7168\n"
MF"v[132:135],a[112:115],a[164:167],v[132:135]\n"
MF"v[132:135],a[116:119],a[172:175],v[132:135]\n"
RD"a[80:83],v237 offset:24576\n"
MF"v[208:211],a[116:119],a[176:179],v[208:211]\n"
MF"v[208:211],a[112:115],a[168:171],v[208:211]\n"
RD"a[84:87],v237 offset:25600\n"
MF"v[140:143],a[120:123],a[164:167],v[140:143]\n"
MF"v[140:143],a[124:127],a[172:175],v[140:143]\n"
RD"a[88:91],v237 offset:26624\n"
MF"v[212:215],a[124:127],a[176:179],v[212:215]\n"
MF"v[212:215],a[120:123],a[168:171],v[212:215]\n"
WT"lgkmcnt(3)\n"
RD"a[92:95],v237 offset:27648\n"
MF"v[132:135],a[64:67],a[32:35],v[132:135]\n"
MF"v[132:135],a[68:71],a[40:43],v[132:135]\n"
RD"a[96:99],v226 offset:24576\n"
MF"v[208:211],a[68:71],a[44:47],v[208:211]\n"
MF"v[208:211],a[64:67],a[36:39],v[208:211]\n"
RD"a[100:103],v226 offset:25600\n"
MF"v[140:143],a[72:75],a[32:35],v[140:143]\n"
MF"v[140:143],a[76:79],a[40:43],v[140:143]\n"
RD"a[104:107],v226 offset:26624\n"
MF"v[212:215],a[76:79],a[44:47],v[212:215]\n"
MF"v[212:215],a[72:75],a[36:39],v[212:215]\n"
WT"lgkmcnt(3)\n"
RD"a[108:111],v226 offset:27648\n"
MF"v[132:135],a[80:83],a[48:51],v[132:135]\n"
MF"v[132:135],a[84:87],a[56:59],v[132:135]\n"
RD"a[112:115],v235 offset:12288\n"
MF"v[208:211],a[84:87],a[60:63],v[208:211]\n"
WT"vmcnt(1)\n"
"v_cmp_eq_u64 vcc,s[28:29],v[240:241]\n"
MF"v[208:211],a[80:83],a[52:55],v[208:211]\n"
"s_and_b32 s50,vcc_lo,0xff\n"
RD"a[116:119],v235 offset:13312\n"
"s_or_b32 s95,s95,s50\n"
MF"v[140:143],a[88:91],a[48:51],v[140:143]\n"
"s_bitcmp1_b32 s95,1\n"
"s_cselect_b32 s51,s94,1065353216\n"
MF"v[140:143],a[92:95],a[56:59],v[140:143]\n"
SA"s92,s51,5120\n"
RD"a[120:123],v235 offset:14336\n"
MF"v[212:215],a[92:95],a[60:63],v[212:215]\n"
BL"a[184:187],v244,s[8:11],s92 offen sc1\n"
MF"v[212:215],a[88:91],a[52:55],v[212:215]\n"
WT"lgkmcnt(3)\n"
RD"a[124:127],v235 offset:15360\n"
MF"v[148:151],a[96:99],a[244:247],v[148:151]\n"
BL"a[216:219],v245,s[8:11],s92 offen sc1\n"
MF"v[148:151],a[100:103],a[252:255],v[148:151]\n"
SA"s90,s88,330752\n"
RD"a[64:67],v236 offset:0\n"
PK"v132,v132,v133\n"
MF"v[216:219],a[100:103],a[128:131],v[216:219]\n"
"s_bitcmp1_b32 s95,2\n"
PK"v133,v134,v135\n"
PK"v134,v140,v141\n"
MF"v[216:219],a[96:99],a[248:251],v[216:219]\n"
"s_cselect_b32 s51,s94,1065353216\n"
RD"a[68:71],v236 offset:1024\n"
PK"v135,v142,v143\n"
MF"v[156:159],a[104:107],a[244:247],v[156:159]\n"
SA"s92,s51,10240\n"
BS"v[132:135],v224,s[8:11],s90 offen sc1\n"
MF"v[156:159],a[108:111],a[252:255],v[156:159]\n"
BL"a[188:191],v244,s[8:11],s92 offen sc1\n"
MF"v[220:223],a[108:111],a[128:131],v[220:223]\n"
RD"a[72:75],v236 offset:2048\n"
MF"v[220:223],a[104:107],a[248:251],v[220:223]\n"
WT"lgkmcnt(3)\n"
BL"a[220:223],v245,s[8:11],s92 offen sc1\n"
MF"v[148:151],a[112:115],a[132:135],v[148:151]\n"
"s_bitcmp1_b32 s95,3\n"
RD"a[76:79],v236 offset:3072\n"
"s_cselect_b32 s51,s94,1065353216\n"
MF"v[148:151],a[116:119],a[140:143],v[148:151]\n"
SA"s91,s22,330752\n"
RD"a[80:83],v236 offset:20480\n"
PK"v208,v208,v209\n"
MF"v[216:219],a[116:119],a[144:147],v[216:219]\n"
SA"s92,s51,15360\n"
PK"v209,v210,v211\n"
PK"v210,v212,v213\n"
MF"v[216:219],a[112:115],a[136:139],v[216:219]\n"
BL"a[192:195],v244,s[8:11],s92 offen sc1\n"
MF"v[156:159],a[120:123],a[132:135],v[156:159]\n"
RD"a[84:87],v236 offset:21504\n"
PK"v211,v214,v215\n"
MF"v[156:159],a[124:127],a[140:143],v[156:159]\n"
BS"v[208:211],v224,s[8:11],s91 offen sc1\n"
MF"v[220:223],a[124:127],a[144:147],v[220:223]\n"
"s_bitcmp1_b32 s95,4\n"
BL"a[224:227],v245,s[8:11],s92 offen sc1\n"
MF"v[220:223],a[120:123],a[136:139],v[220:223]\n"
WT"lgkmcnt(2)\n"
RD"a[88:91],v236 offset:22528\n"
RD"a[92:95],v236 offset:23552\n"
MF"v[148:151],a[64:67],a[148:151],v[148:151]\n"
"s_cselect_b32 s51,s94,1065353216\n"
SA"s92,s51,20480\n"
MF"v[148:151],a[68:71],a[156:159],v[148:151]\n"
BL"a[196:199],v244,s[8:11],s92 offen sc1\n"
MF"v[216:219],a[68:71],a[160:163],v[216:219]\n"
RD"a[96:99],v237 offset:8192\n"
MF"v[216:219],a[64:67],a[152:155],v[216:219]\n"
BL"a[228:231],v245,s[8:11],s92 offen sc1\n"
MF"v[156:159],a[72:75],a[148:151],v[156:159]\n"
"s_bitcmp1_b32 s95,5\n"
RD"a[100:103],v237 offset:9216\n"
"s_cselect_b32 s51,s94,1065353216\n"
MF"v[156:159],a[76:79],a[156:159],v[156:159]\n"
SA"s92,s51,25600\n"
RD"a[104:107],v237 offset:10240\n"
MF"v[220:223],a[76:79],a[160:163],v[220:223]\n"
BL"a[200:203],v244,s[8:11],s92 offen sc1\n"
MF"v[220:223],a[72:75],a[152:155],v[220:223]\n"
WT"lgkmcnt(3)\n"
RD"a[108:111],v237 offset:11264\n"
MF"v[148:151],a[80:83],a[164:167],v[148:151]\n"
BL"a[232:235],v245,s[8:11],s92 offen sc1\n"
MF"v[148:151],a[84:87],a[172:175],v[148:151]\n"
RD"a[112:115],v237 offset:28672\n"
MF"v[216:219],a[84:87],a[176:179],v[216:219]\n"
"s_bitcmp1_b32 s95,6\n"
"s_cselect_b32 s51,s94,1065353216\n"
MF"v[216:219],a[80:83],a[168:171],v[216:219]\n"
SA"s92,s51,30720\n"
RD"a[116:119],v237 offset:29696\n"
MF"v[156:159],a[88:91],a[164:167],v[156:159]\n"
BL"a[204:207],v244,s[8:11],s92 offen sc1\n"
MF"v[156:159],a[92:95],a[172:175],v[156:159]\n"
RD"a[120:123],v237 offset:30720\n"
MF"v[220:223],a[92:95],a[176:179],v[220:223]\n"
BL"a[236:239],v245,s[8:11],s92 offen sc1\n"
MF"v[220:223],a[88:91],a[168:171],v[220:223]\n"
WT"lgkmcnt(3)\n"
RD"a[124:127],v237 offset:31744\n"
MF"v[148:151],a[96:99],a[32:35],v[148:151]\n"
"s_bitcmp1_b32 s95,7\n"
"s_cselect_b32 s51,s94,1065353216\n"
MF"v[148:151],a[100:103],a[40:43],v[148:151]\n"
SA"s92,s51,35840\n"
MF"v[216:219],a[100:103],a[44:47],v[216:219]\n"
BL"a[208:211],v244,s[8:11],s92 offen sc1\n"
MF"v[216:219],a[96:99],a[36:39],v[216:219]\n"
MF"v[156:159],a[104:107],a[32:35],v[156:159]\n"
BL"a[240:243],v245,s[8:11],s92 offen sc1\n"
MF"v[156:159],a[108:111],a[40:43],v[156:159]\n"
MF"v[220:223],a[108:111],a[44:47],v[220:223]\n"
MF"v[220:223],a[104:107],a[36:39],v[220:223]\n"
WT"lgkmcnt(0)\n"
MF"v[148:151],a[112:115],a[48:51],v[148:151]\n"
MF"v[148:151],a[116:119],a[56:59],v[148:151]\n"
MF"v[216:219],a[116:119],a[60:63],v[216:219]\n"
MF"v[216:219],a[112:115],a[52:55],v[216:219]\n"
MF"v[156:159],a[120:123],a[48:51],v[156:159]\n"
MF"v[156:159],a[124:127],a[56:59],v[156:159]\n"
MF"v[220:223],a[124:127],a[60:63],v[220:223]\n"
MF"v[220:223],a[120:123],a[52:55],v[220:223]\n"
"s_nop 5\n"
PK"v148,v148,v149\n"
PK"v149,v150,v151\n"
PK"v150,v156,v157\n"
PK"v151,v158,v159\n"
SA"s90,s88,331776\n"
BS"v[148:151],v224,s[8:11],s90 offen sc1\n"
PK"v216,v216,v217\n"
PK"v217,v218,v219\n"
PK"v218,v220,v221\n"
PK"v219,v222,v223\n"
SA"s91,s22,331776\n"
BS"v[216:219],v224,s[8:11],s91 offen sc1\n"
"s_cmp_eq_u32 s23,0\n"
"s_cbranch_scc0 L1089_%=\n"
"ds_write_b128 v227,v[0:3] offset:0\n"
"ds_write_b128 v227,v[8:11] offset:1024\n"
"ds_write_b128 v227,v[16:19] offset:2048\n"
"ds_write_b128 v227,v[24:27] offset:3072\n"
"ds_write_b128 v227,v[32:35] offset:4096\n"
"ds_write_b128 v227,v[84:87] offset:5120\n"
"ds_write_b128 v227,v[100:103] offset:6144\n"
"ds_write_b128 v227,v[116:119] offset:7168\n"
"ds_write_b128 v227,v[132:135] offset:8192\n"
"ds_write_b128 v227,v[148:151] offset:9216\n"
"L1089_%=:\n"
"s_cmp_eq_u32 s23,4\n"
"s_cbranch_scc0 L1090_%=\n"
"ds_write_b128 v227,v[40:43] offset:0\n"
"ds_write_b128 v227,v[48:51] offset:1024\n"
"ds_write_b128 v227,v[56:59] offset:2048\n"
"ds_write_b128 v227,v[64:67] offset:3072\n"
"ds_write_b128 v227,v[72:75] offset:4096\n"
"ds_write_b128 v227,v[164:167] offset:5120\n"
"ds_write_b128 v227,v[180:183] offset:6144\n"
"ds_write_b128 v227,v[196:199] offset:7168\n"
"ds_write_b128 v227,v[208:211] offset:8192\n"
"ds_write_b128 v227,v[216:219] offset:9216\n"
"L1090_%=:\n"
MF"v[4:7],v[96:99],v[112:115],0\n"
MF"v[12:15],v[104:107],v[112:115],0\n"
MF"v[20:23],v[96:99],v[120:123],0\n"
MF"v[28:31],v[104:107],v[120:123],0\n"
MF"v[4:7],v[96:99],v[144:147],v[4:7]\n"
MF"v[12:15],v[104:107],v[144:147],v[12:15]\n"
MF"v[20:23],v[96:99],v[152:155],v[20:23]\n"
MF"v[28:31],v[104:107],v[152:155],v[28:31]\n"
MF"v[36:39],v[96:99],v[128:131],0\n"
MF"v[44:47],v[104:107],v[128:131],0\n"
MF"v[52:55],v[96:99],v[136:139],0\n"
MF"v[60:63],v[104:107],v[136:139],0\n"
MF"v[36:39],v[96:99],v[160:163],v[36:39]\n"
MF"v[44:47],v[104:107],v[160:163],v[44:47]\n"
MF"v[52:55],v[96:99],v[168:171],v[52:55]\n"
MF"v[60:63],v[104:107],v[168:171],v[60:63]\n"
SA"s89,s89,4096\n"
"s_mov_b32 s54,1048576\n"
WT"vmcnt(0)\n"
"buffer_store_dwordx2 v[232:233],v239,s[8:11],s89 offen sc1\n"
"buffer_load_dwordx2 v[240:241],v243,s[8:11],s93 offen sc1\n"
WT"lgkmcnt(0)\n"
"s_barrier\n"
RD"a[180:183],v247 offset:0\n"
RD"a[212:215],v227 offset:4096\n"
RD"a[64:67],v247 offset:5120\n"
RD"a[96:99],v227 offset:9216\n"
"s_getpc_b64 s[52:53]\n"
"L1097_%=:\n"
SA"s52,s52,L1096_%=-L1097_%=-32\n"
"s_addc_u32 s53,s53,0\n"
"s_mov_b32 s89,0\n"
WT"lgkmcnt(0)\n"
MF"v[4:7],v[80:83],a[180:183],v[4:7]\n"
MF"v[12:15],v[88:91],a[180:183],v[12:15]\n"
MF"v[20:23],v[80:83],a[212:215],v[20:23]\n"
MF"v[28:31],v[88:91],a[212:215],v[28:31]\n"
MF"v[36:39],v[80:83],a[64:67],v[36:39]\n"
MF"v[44:47],v[88:91],a[64:67],v[44:47]\n"
MF"v[52:55],v[80:83],a[96:99],v[52:55]\n"
MF"v[60:63],v[88:91],a[96:99],v[60:63]\n"
"s_branch L1092_%=\n"
"L1094_%=:\n"
"s_sleep 3\n"
"buffer_load_dwordx2 v[240:241],v243,s[8:11],s93 offen sc1\n"
"L1091_%=:\n"
WT"vmcnt(0)\n"
"v_cmp_eq_u64 vcc,s[28:29],v[240:241]\n"
"s_sub_u32 s54,s54,1\n"
"s_cselect_b32 s90,-1,vcc_lo\n"
"s_andn2_b32 s90,s90,s95\n"
"s_cbranch_scc0 L1094_%=\n"
"s_or_b32 s95,s95,s90\n"
"L1098_%=:\n"
"s_ff1_i32_b32 s50,s90\n"
"s_bitset0_b32 s90,s50\n"
"s_lshl_b32 s50,s50,5\n"
SA"s50,s50,s52\n"
"s_addc_u32 s51,s53,0\n"
"s_setpc_b64 s[50:51]\n"
"L1096_%=:\n"
SA"s92,s94,5120\n"
BL"a[184:187],v244,s[8:11],s92 offen sc1\n"
BL"a[216:219],v245,s[8:11],s92 offen sc1\n"
"s_branch L1093_%=\n"
"s_nop 0\n"
SA"s92,s94,10240\n"
BL"a[188:191],v244,s[8:11],s92 offen sc1\n"
BL"a[220:223],v245,s[8:11],s92 offen sc1\n"
"s_branch L1093_%=\n"
"s_nop 0\n"
SA"s92,s94,15360\n"
BL"a[192:195],v244,s[8:11],s92 offen sc1\n"
BL"a[224:227],v245,s[8:11],s92 offen sc1\n"
"s_branch L1093_%=\n"
"s_nop 0\n"
SA"s92,s94,20480\n"
BL"a[196:199],v244,s[8:11],s92 offen sc1\n"
BL"a[228:231],v245,s[8:11],s92 offen sc1\n"
"s_branch L1093_%=\n"
"s_nop 0\n"
SA"s92,s94,25600\n"
BL"a[200:203],v244,s[8:11],s92 offen sc1\n"
BL"a[232:235],v245,s[8:11],s92 offen sc1\n"
"s_branch L1093_%=\n"
"s_nop 0\n"
SA"s92,s94,30720\n"
BL"a[204:207],v244,s[8:11],s92 offen sc1\n"
BL"a[236:239],v245,s[8:11],s92 offen sc1\n"
"s_branch L1093_%=\n"
"s_nop 0\n"
SA"s92,s94,35840\n"
BL"a[208:211],v244,s[8:11],s92 offen sc1\n"
BL"a[240:243],v245,s[8:11],s92 offen sc1\n"
"s_branch L1093_%=\n"
"s_nop 0\n"
"s_nop 0\n"
"s_nop 0\n"
"s_nop 0\n"
"s_nop 0\n"
"s_nop 0\n"
"s_nop 0\n"
"s_nop 0\n"
"s_nop 0\n"
SA"s92,s94,332800\n"
BL"a[68:71],v244,s[8:11],s92 offen sc1\n"
BL"a[100:103],v245,s[8:11],s92 offen sc1\n"
"s_branch L1093_%=\n"
"s_nop 0\n"
SA"s92,s94,337920\n"
BL"a[72:75],v244,s[8:11],s92 offen sc1\n"
BL"a[104:107],v245,s[8:11],s92 offen sc1\n"
"s_branch L1093_%=\n"
"s_nop 0\n"
SA"s92,s94,343040\n"
BL"a[76:79],v244,s[8:11],s92 offen sc1\n"
BL"a[108:111],v245,s[8:11],s92 offen sc1\n"
"s_branch L1093_%=\n"
"s_nop 0\n"
SA"s92,s94,348160\n"
BL"a[80:83],v244,s[8:11],s92 offen sc1\n"
BL"a[112:115],v245,s[8:11],s92 offen sc1\n"
"s_branch L1093_%=\n"
"s_nop 0\n"
SA"s92,s94,353280\n"
BL"a[84:87],v244,s[8:11],s92 offen sc1\n"
BL"a[116:119],v245,s[8:11],s92 offen sc1\n"
"s_branch L1093_%=\n"
"s_nop 0\n"
SA"s92,s94,358400\n"
BL"a[88:91],v244,s[8:11],s92 offen sc1\n"
BL"a[120:123],v245,s[8:11],s92 offen sc1\n"
"s_branch L1093_%=\n"
"s_nop 0\n"
SA"s92,s94,363520\n"
BL"a[92:95],v244,s[8:11],s92 offen sc1\n"
BL"a[124:127],v245,s[8:11],s92 offen sc1\n"
"s_branch L1093_%=\n"
"s_nop 0\n"
"L1093_%=:\n"
"s_cmp_lg_u32 s90,0\n"
"s_cbranch_scc1 L1098_%=\n"
"s_and_b32 s92,s95,s89\n"
"s_cmp_eq_u32 s92,-1\n"
"s_cbranch_scc1 L1095_%=\n"
"buffer_load_dwordx2 v[240:241],v243,s[8:11],s93 offen sc1\n"
WT"vmcnt(1)\n"
"L1092_%=:\n"
"s_or_b32 s92,s95,0xff00\n"
"s_andn2_b32 s92,s92,s89\n"
"s_cmp_eq_u32 s92,-1\n"
"s_cbranch_scc0 L1091_%=\n"
MF"v[4:7],v[80:83],a[184:187],v[4:7]\n"
MF"v[12:15],v[88:91],a[184:187],v[12:15]\n"
MF"v[4:7],v[80:83],a[188:191],v[4:7]\n"
MF"v[12:15],v[88:91],a[188:191],v[12:15]\n"
MF"v[4:7],v[80:83],a[192:195],v[4:7]\n"
MF"v[12:15],v[88:91],a[192:195],v[12:15]\n"
MF"v[4:7],v[80:83],a[196:199],v[4:7]\n"
MF"v[12:15],v[88:91],a[196:199],v[12:15]\n"
MF"v[4:7],v[80:83],a[200:203],v[4:7]\n"
MF"v[12:15],v[88:91],a[200:203],v[12:15]\n"
MF"v[4:7],v[80:83],a[204:207],v[4:7]\n"
MF"v[12:15],v[88:91],a[204:207],v[12:15]\n"
MF"v[4:7],v[80:83],a[208:211],v[4:7]\n"
MF"v[12:15],v[88:91],a[208:211],v[12:15]\n"
MF"v[20:23],v[80:83],a[216:219],v[20:23]\n"
MF"v[28:31],v[88:91],a[216:219],v[28:31]\n"
MF"v[20:23],v[80:83],a[220:223],v[20:23]\n"
MF"v[28:31],v[88:91],a[220:223],v[28:31]\n"
MF"v[20:23],v[80:83],a[224:227],v[20:23]\n"
MF"v[28:31],v[88:91],a[224:227],v[28:31]\n"
MF"v[20:23],v[80:83],a[228:231],v[20:23]\n"
MF"v[28:31],v[88:91],a[228:231],v[28:31]\n"
PK"v4,v4,v5\n"
PK"v5,v6,v7\n"
PK"v6,v12,v13\n"
PK"v7,v14,v15\n"
BS"v[4:7],v176,s[16:19],s38 offen\n"
MF"v[20:23],v[80:83],a[232:235],v[20:23]\n"
MF"v[28:31],v[88:91],a[232:235],v[28:31]\n"
MF"v[20:23],v[80:83],a[236:239],v[20:23]\n"
MF"v[28:31],v[88:91],a[236:239],v[28:31]\n"
MF"v[20:23],v[80:83],a[240:243],v[20:23]\n"
MF"v[28:31],v[88:91],a[240:243],v[28:31]\n"
"s_nop 7\n"
PK"v20,v20,v21\n"
PK"v21,v22,v23\n"
PK"v22,v28,v29\n"
PK"v23,v30,v31\n"
BS"v[20:23],v176,s[16:19],s55 offen\n"
"s_mov_b32 s89,-1\n"
"s_cmp_eq_u32 s95,-1\n"
"s_cbranch_scc0 L1091_%=\n"
"L1095_%=:\n"
WT"vmcnt(0)\n"
MF"v[36:39],v[80:83],a[68:71],v[36:39]\n"
MF"v[44:47],v[88:91],a[68:71],v[44:47]\n"
MF"v[36:39],v[80:83],a[72:75],v[36:39]\n"
MF"v[44:47],v[88:91],a[72:75],v[44:47]\n"
MF"v[36:39],v[80:83],a[76:79],v[36:39]\n"
MF"v[44:47],v[88:91],a[76:79],v[44:47]\n"
MF"v[36:39],v[80:83],a[80:83],v[36:39]\n"
MF"v[44:47],v[88:91],a[80:83],v[44:47]\n"
MF"v[36:39],v[80:83],a[84:87],v[36:39]\n"
MF"v[44:47],v[88:91],a[84:87],v[44:47]\n"
MF"v[36:39],v[80:83],a[88:91],v[36:39]\n"
MF"v[44:47],v[88:91],a[88:91],v[44:47]\n"
MF"v[36:39],v[80:83],a[92:95],v[36:39]\n"
MF"v[44:47],v[88:91],a[92:95],v[44:47]\n"
MF"v[52:55],v[80:83],a[100:103],v[52:55]\n"
MF"v[60:63],v[88:91],a[100:103],v[60:63]\n"
MF"v[52:55],v[80:83],a[104:107],v[52:55]\n"
MF"v[60:63],v[88:91],a[104:107],v[60:63]\n"
MF"v[52:55],v[80:83],a[108:111],v[52:55]\n"
MF"v[60:63],v[88:91],a[108:111],v[60:63]\n"
MF"v[52:55],v[80:83],a[112:115],v[52:55]\n"
MF"v[60:63],v[88:91],a[112:115],v[60:63]\n"
PK"v36,v36,v37\n"
PK"v37,v38,v39\n"
PK"v38,v44,v45\n"
PK"v39,v46,v47\n"
BS"v[36:39],v177,s[16:19],s38 offen\n"
MF"v[52:55],v[80:83],a[116:119],v[52:55]\n"
MF"v[60:63],v[88:91],a[116:119],v[60:63]\n"
MF"v[52:55],v[80:83],a[120:123],v[52:55]\n"
MF"v[60:63],v[88:91],a[120:123],v[60:63]\n"
MF"v[52:55],v[80:83],a[124:127],v[52:55]\n"
MF"v[60:63],v[88:91],a[124:127],v[60:63]\n"
"s_nop 7\n"
PK"v52,v52,v53\n"
PK"v53,v54,v55\n"
PK"v54,v60,v61\n"
PK"v55,v62,v63\n"
BS"v[52:55],v177,s[16:19],s55 offen\n"
WT"vmcnt(0) lgkmcnt(0)\n"
"s_endpgm\n"
"L1004_%=:\n"
"s_mov_b32 s54,0\n"
"L1006_%=:\n"
"s_sleep 2\n"
BL"v[160:163],v224,s[8:11],0 offen offset:256 sc1\n"
BL"v[164:167],v224,s[8:11],0 offen offset:1280 sc1\n"
WT"vmcnt(0)\n"
SA"s54,s54,1\n"
"v_cmp_eq_u64 vcc,s[28:29],v[160:161]\n"
"v_cmp_eq_u64_e64 s[62:63],s[28:29],v[162:163]\n"
"s_and_b64 vcc,vcc,s[62:63]\n"
"v_cmp_eq_u64_e64 s[62:63],s[28:29],v[164:165]\n"
"s_and_b64 vcc,vcc,s[62:63]\n"
"v_cmp_eq_u64_e64 s[62:63],s[28:29],v[166:167]\n"
"s_and_b64 vcc,vcc,s[62:63]\n"
"s_cmp_eq_u64 vcc,exec\n"
"s_cbranch_scc1 L1005_%=\n"
"s_cmp_lt_u32 s54,1048576\n"
"s_cbranch_scc1 L1006_%=\n"
"s_branch L1005_%=\n"
"L1007_%=:\n"
"s_mov_b32 s54,0\n"
"L1009_%=:\n"
"v_max3_u32 v226,v160,v161,v162\n"
"v_max3_u32 v226,v226,v163,v164\n"
"v_max3_u32 v226,v226,v165,v166\n"
"v_max3_u32 v226,v226,v167,v168\n"
"v_max3_u32 v226,v226,v169,v170\n"
"v_max3_u32 v226,v226,v171,v172\n"
"v_max3_u32 v226,v226,v173,v174\n"
"v_max3_u32 v226,v226,v175,v176\n"
"v_max3_u32 v226,v226,v177,v178\n"
"v_max3_u32 v226,v226,v179,v180\n"
"v_max3_u32 v226,v226,v181,v182\n"
"v_max3_u32 v226,v226,v183,v184\n"
"v_max3_u32 v226,v226,v185,v186\n"
"v_max3_u32 v226,v226,v187,v188\n"
"v_max3_u32 v226,v226,v189,v190\n"
"v_max_u32 v226,v226,v191\n"
"v_mov_b32 v229,v226\n"
"s_nop 1\n"
"v_max_u32_dpp v229,v229,v229 row_shr:1 row_mask:0xf bank_mask:0xf\n"
"s_nop 1\n"
"v_max_u32_dpp v229,v229,v229 row_shr:2 row_mask:0xf bank_mask:0xf\n"
"s_nop 1\n"
"v_max_u32_dpp v229,v229,v229 row_shr:4 row_mask:0xf bank_mask:0xf\n"
"s_nop 1\n"
"v_max_u32_dpp v229,v229,v229 row_shr:8 row_mask:0xf bank_mask:0xf\n"
"s_nop 1\n"
"v_max_u32_dpp v229,v229,v229 row_bcast:15 row_mask:0xa bank_mask:0xf\n"
"s_nop 1\n"
"v_max_u32_dpp v229,v229,v229 row_bcast:31 row_mask:0xc bank_mask:0xf\n"
"s_nop 1\n"
"v_readlane_b32 s62,v229,63\n"
"s_nop 1\n"
"v_cmp_eq_u32 vcc,s62,v160\n"
"s_nop 1\n"
"v_cndmask_b32_e64 v160,v160,0,vcc\n"
"v_cmp_eq_u32 vcc,s62,v161\n"
"s_nop 1\n"
"v_cndmask_b32_e64 v161,v161,0,vcc\n"
"v_cmp_eq_u32 vcc,s62,v162\n"
"s_nop 1\n"
"v_cndmask_b32_e64 v162,v162,0,vcc\n"
"v_cmp_eq_u32 vcc,s62,v163\n"
"s_nop 1\n"
"v_cndmask_b32_e64 v163,v163,0,vcc\n"
"v_cmp_eq_u32 vcc,s62,v164\n"
"s_nop 1\n"
"v_cndmask_b32_e64 v164,v164,0,vcc\n"
"v_cmp_eq_u32 vcc,s62,v165\n"
"s_nop 1\n"
"v_cndmask_b32_e64 v165,v165,0,vcc\n"
"v_cmp_eq_u32 vcc,s62,v166\n"
"s_nop 1\n"
"v_cndmask_b32_e64 v166,v166,0,vcc\n"
"v_cmp_eq_u32 vcc,s62,v167\n"
"s_nop 1\n"
"v_cndmask_b32_e64 v167,v167,0,vcc\n"
"v_cmp_eq_u32 vcc,s62,v168\n"
"s_nop 1\n"
"v_cndmask_b32_e64 v168,v168,0,vcc\n"
"v_cmp_eq_u32 vcc,s62,v169\n"
"s_nop 1\n"
"v_cndmask_b32_e64 v169,v169,0,vcc\n"
"v_cmp_eq_u32 vcc,s62,v170\n"
"s_nop 1\n"
"v_cndmask_b32_e64 v170,v170,0,vcc\n"
"v_cmp_eq_u32 vcc,s62,v171\n"
"s_nop 1\n"
"v_cndmask_b32_e64 v171,v171,0,vcc\n"
"v_cmp_eq_u32 vcc,s62,v172\n"
"s_nop 1\n"
"v_cndmask_b32_e64 v172,v172,0,vcc\n"
"v_cmp_eq_u32 vcc,s62,v173\n"
"s_nop 1\n"
"v_cndmask_b32_e64 v173,v173,0,vcc\n"
"v_cmp_eq_u32 vcc,s62,v174\n"
"s_nop 1\n"
"v_cndmask_b32_e64 v174,v174,0,vcc\n"
"v_cmp_eq_u32 vcc,s62,v175\n"
"s_nop 1\n"
"v_cndmask_b32_e64 v175,v175,0,vcc\n"
"v_cmp_eq_u32 vcc,s62,v176\n"
"s_nop 1\n"
"v_cndmask_b32_e64 v176,v176,0,vcc\n"
"v_cmp_eq_u32 vcc,s62,v177\n"
"s_nop 1\n"
"v_cndmask_b32_e64 v177,v177,0,vcc\n"
"v_cmp_eq_u32 vcc,s62,v178\n"
"s_nop 1\n"
"v_cndmask_b32_e64 v178,v178,0,vcc\n"
"v_cmp_eq_u32 vcc,s62,v179\n"
"s_nop 1\n"
"v_cndmask_b32_e64 v179,v179,0,vcc\n"
"v_cmp_eq_u32 vcc,s62,v180\n"
"s_nop 1\n"
"v_cndmask_b32_e64 v180,v180,0,vcc\n"
"v_cmp_eq_u32 vcc,s62,v181\n"
"s_nop 1\n"
"v_cndmask_b32_e64 v181,v181,0,vcc\n"
"v_cmp_eq_u32 vcc,s62,v182\n"
"s_nop 1\n"
"v_cndmask_b32_e64 v182,v182,0,vcc\n"
"v_cmp_eq_u32 vcc,s62,v183\n"
"s_nop 1\n"
"v_cndmask_b32_e64 v183,v183,0,vcc\n"
"v_cmp_eq_u32 vcc,s62,v184\n"
"s_nop 1\n"
"v_cndmask_b32_e64 v184,v184,0,vcc\n"
"v_cmp_eq_u32 vcc,s62,v185\n"
"s_nop 1\n"
"v_cndmask_b32_e64 v185,v185,0,vcc\n"
"v_cmp_eq_u32 vcc,s62,v186\n"
"s_nop 1\n"
"v_cndmask_b32_e64 v186,v186,0,vcc\n"
"v_cmp_eq_u32 vcc,s62,v187\n"
"s_nop 1\n"
"v_cndmask_b32_e64 v187,v187,0,vcc\n"
"v_cmp_eq_u32 vcc,s62,v188\n"
"s_nop 1\n"
"v_cndmask_b32_e64 v188,v188,0,vcc\n"
"v_cmp_eq_u32 vcc,s62,v189\n"
"s_nop 1\n"
"v_cndmask_b32_e64 v189,v189,0,vcc\n"
"v_cmp_eq_u32 vcc,s62,v190\n"
"s_nop 1\n"
"v_cndmask_b32_e64 v190,v190,0,vcc\n"
"v_cmp_eq_u32 vcc,s62,v191\n"
"s_nop 1\n"
"v_cndmask_b32_e64 v191,v191,0,vcc\n"
"v_mov_b32 v229,s62\n"
"s_lshl_b64 exec,1,s54\n"
"v_mov_b32 v225,v229\n"
"s_mov_b64 exec,-1\n"
SA"s54,s54,1\n"
"s_cmp_lt_u32 s54,8\n"
"s_cbranch_scc1 L1009_%=\n"
"s_nop 4\n"
"s_branch L1008_%=\n"
"L1010_%=:\n"
"s_mov_b32 s54,0\n"
"L1012_%=:\n"
"s_sleep 2\n"
"buffer_load_dwordx2 v[208:209],v252,s[8:11],s50 offen sc1\n"
WT"vmcnt(0)\n"
SA"s54,s54,1\n"
"v_cmp_eq_u64 vcc,s[28:29],v[208:209]\n"
"s_cmp_eq_u64 vcc,exec\n"
"s_cbranch_scc1 L1011_%=\n"
"s_cmp_lt_u32 s54,1048576\n"
"s_cbranch_scc1 L1012_%=\n"
"s_branch L1011_%=\n"
"L1014_%=:\n"
"s_nop 15\n"
WT"vmcnt(0)\n"
"s_lshl_b32 s89,s34,3\n"
SA"s89,s89,s30\n"
SA"s89,s89,0\n"
"s_lshl_b32 s91,s89,4\n"
"s_lshr_b32 s89,s89,1\n"
"v_bfe_u32 v249,v224,4,4\n"
"v_lshrrev_b32 v254,8,v224\n"
"v_lshlrev_b32 v254,2,v254\n"
"v_sub_u32 v249,v249,v254\n"
"v_add_u32 v249,s91,v249\n"
"v_mov_b32 v255,0xff800000\n"
"v_mov_b32 v248,v255\n"
"s_mov_b32 s90,0\n"
"s_mov_b32 s88,0\n"
"v_mov_b32 v80,0\n"
"v_mov_b32 v81,0\n"
"v_mov_b32 v82,0\n"
"v_mov_b32 v83,0\n"
"v_mov_b32 v84,0\n"
"v_mov_b32 v85,0\n"
"v_mov_b32 v86,0\n"
"v_mov_b32 v87,0\n"
"v_mov_b32 v88,0\n"
"v_mov_b32 v89,0\n"
"v_mov_b32 v90,0\n"
"v_mov_b32 v91,0\n"
"v_mov_b32 v92,0\n"
"v_mov_b32 v93,0\n"
"v_mov_b32 v94,0\n"
"v_mov_b32 v95,0\n"
"v_mov_b32 v96,0\n"
"v_mov_b32 v97,0\n"
"v_mov_b32 v98,0\n"
"v_mov_b32 v99,0\n"
"v_mov_b32 v100,0\n"
"v_mov_b32 v101,0\n"
"v_mov_b32 v102,0\n"
"v_mov_b32 v103,0\n"
"v_mov_b32 v104,0\n"
"v_mov_b32 v105,0\n"
"v_mov_b32 v106,0\n"
"v_mov_b32 v107,0\n"
"v_mov_b32 v108,0\n"
"v_mov_b32 v109,0\n"
"v_mov_b32 v110,0\n"
"v_mov_b32 v111,0\n"
"v_accvgpr_write_b32 a132,0\n"
"v_accvgpr_write_b32 a133,0\n"
"v_accvgpr_write_b32 a134,0\n"
"v_accvgpr_write_b32 a135,0\n"
"L1015_%=:\n"
"s_lshl_b32 s91,s88,13\n"
SA"s91,s91,s39\n"
SA"s91,s91,0\n"
BL"a[148:151],v224,s[8:11],s91 offen offset:0 sc1\n"
BL"a[152:155],v224,s[8:11],s91 offen offset:1024 sc1\n"
BL"a[156:159],v224,s[8:11],s91 offen offset:2048 sc1\n"
BL"a[160:163],v224,s[8:11],s91 offen offset:3072 sc1\n"
SA"s91,s91,4096\n"
BL"a[164:167],v224,s[8:11],s91 offen offset:0 sc1\n"
BL"a[168:171],v224,s[8:11],s91 offen offset:1024 sc1\n"
BL"a[172:175],v224,s[8:11],s91 offen offset:2048 sc1\n"
BL"a[176:179],v224,s[8:11],s91 offen offset:3072 sc1\n"
WT"vmcnt(0)\n"
MF"v[240:243],a[148:151],a[0:3],0\n"
MF"v[240:243],a[152:155],a[4:7],v[240:243]\n"
MF"v[240:243],a[156:159],a[8:11],v[240:243]\n"
MF"v[240:243],a[160:163],a[12:15],v[240:243]\n"
MF"v[244:247],a[164:167],a[0:3],0\n"
MF"v[244:247],a[168:171],a[4:7],v[244:247]\n"
MF"v[244:247],a[172:175],a[8:11],v[244:247]\n"
MF"v[244:247],a[176:179],a[12:15],v[244:247]\n"
"s_lshl_b32 s91,s88,5\n"
"v_subrev_u32 v250,s91,v249\n"
"s_nop 7\n"
"v_cmp_gt_i32 vcc,0,v250\n"
"s_nop 1\n"
"v_cndmask_b32 v240,v240,v255,vcc\n"
"v_cmp_gt_i32 vcc,1,v250\n"
"s_nop 1\n"
"v_cndmask_b32 v241,v241,v255,vcc\n"
"v_cmp_gt_i32 vcc,2,v250\n"
"s_nop 1\n"
"v_cndmask_b32 v242,v242,v255,vcc\n"
"v_cmp_gt_i32 vcc,3,v250\n"
"s_nop 1\n"
"v_cndmask_b32 v243,v243,v255,vcc\n"
"v_cmp_gt_i32 vcc,16,v250\n"
"s_nop 1\n"
"v_cndmask_b32 v244,v244,v255,vcc\n"
"v_cmp_gt_i32 vcc,17,v250\n"
"s_nop 1\n"
"v_cndmask_b32 v245,v245,v255,vcc\n"
"v_cmp_gt_i32 vcc,18,v250\n"
"s_nop 1\n"
"v_cndmask_b32 v246,v246,v255,vcc\n"
"v_cmp_gt_i32 vcc,19,v250\n"
"s_nop 1\n"
"v_cndmask_b32 v247,v247,v255,vcc\n"
"s_cmp_lg_u32 s90,0\n"
"s_cbranch_scc1 L1016_%=\n"
"v_max3_f32 v248,v248,v240,v241\n"
"v_max3_f32 v248,v248,v242,v243\n"
"v_max3_f32 v248,v248,v244,v245\n"
"v_max3_f32 v248,v248,v246,v247\n"
"s_branch L1017_%=\n"
"L1016_%=:\n"
"s_lshl_b32 s91,s88,13\n"
SA"s91,s91,s39\n"
SA"s91,s91,524288\n"
BL"a[148:151],v224,s[8:11],s91 offen offset:0 sc1\n"
BL"a[152:155],v224,s[8:11],s91 offen offset:1024 sc1\n"
BL"a[156:159],v224,s[8:11],s91 offen offset:2048 sc1\n"
BL"a[160:163],v224,s[8:11],s91 offen offset:3072 sc1\n"
SA"s91,s91,4096\n"
BL"a[164:167],v224,s[8:11],s91 offen offset:0 sc1\n"
BL"a[168:171],v224,s[8:11],s91 offen offset:1024 sc1\n"
BL"a[172:175],v224,s[8:11],s91 offen offset:2048 sc1\n"
BL"a[176:179],v224,s[8:11],s91 offen offset:3072 sc1\n"
WT"vmcnt(0)\n"
"v_sub_f32 v240,v240,v248\n"
"v_sub_f32 v241,v241,v248\n"
"v_sub_f32 v242,v242,v248\n"
"v_sub_f32 v243,v243,v248\n"
"v_sub_f32 v244,v244,v248\n"
"v_sub_f32 v245,v245,v248\n"
"v_sub_f32 v246,v246,v248\n"
"v_sub_f32 v247,v247,v248\n"
EX"v240,v240\n"
EX"v241,v241\n"
EX"v242,v242\n"
EX"v243,v243\n"
EX"v244,v244\n"
EX"v245,v245\n"
EX"v246,v246\n"
EX"v247,v247\n"
"s_nop 0\n"
PK"v240,v240,v241\n"
PK"v241,v242,v243\n"
PK"v242,v244,v245\n"
PK"v243,v246,v247\n"
"s_nop 1\n"
MF"v[80:83],a[148:151],v[240:243],v[80:83]\n"
MF"v[84:87],a[152:155],v[240:243],v[84:87]\n"
MF"v[88:91],a[156:159],v[240:243],v[88:91]\n"
MF"v[92:95],a[160:163],v[240:243],v[92:95]\n"
MF"v[96:99],a[164:167],v[240:243],v[96:99]\n"
MF"v[100:103],a[168:171],v[240:243],v[100:103]\n"
MF"v[104:107],a[172:175],v[240:243],v[104:107]\n"
MF"v[108:111],a[176:179],v[240:243],v[108:111]\n"
MF"a[132:135],a[128:131],v[240:243],a[132:135]\n"
"s_nop 7\n"
"L1017_%=:\n"
SA"s88,s88,1\n"
"s_cmp_le_u32 s88,s89\n"
"s_cbranch_scc1 L1015_%=\n"
"s_cmp_lg_u32 s90,0\n"
"s_cbranch_scc1 L1018_%=\n"
"v_mov_b32 v254,v248\n"
"s_nop 1\n"
"v_permlane16_swap_b32 v254,v248\n"
"s_nop 1\n"
"v_max_f32 v248,v248,v254\n"
"v_mov_b32 v254,v248\n"
"s_nop 1\n"
"v_permlane32_swap_b32 v254,v248\n"
"s_nop 1\n"
"v_max_f32 v248,v248,v254\n"
"s_mov_b32 s90,1\n"
"s_mov_b32 s88,0\n"
"s_branch L1015_%=\n"
"L1018_%=:\n"
"s_nop 7\n"
"v_accvgpr_read_b32 v251,a132\n"
"s_nop 1\n"
"s_branch L1013_%=\n"
"L1019_%=:\n"
"s_mov_b32 s54,0\n"
"L1021_%=:\n"
"s_sleep 2\n"
"buffer_load_dwordx2 v[238:239],v237,s[8:11],s50 offen sc1\n"
WT"vmcnt(0)\n"
SA"s54,s54,1\n"
"v_cmp_eq_u64 vcc,s[28:29],v[238:239]\n"
"s_cmp_eq_u64 vcc,exec\n"
"s_cbranch_scc1 L1020_%=\n"
"s_cmp_lt_u32 s54,1048576\n"
"s_cbranch_scc1 L1021_%=\n"
"s_branch L1020_%=\n"
"L1023_%=:\n"
"s_nop 15\n"
WT"vmcnt(0)\n"
"s_lshl_b32 s89,s34,3\n"
SA"s89,s89,s30\n"
SA"s89,s89,4\n"
"s_lshl_b32 s91,s89,4\n"
"s_lshr_b32 s89,s89,1\n"
"v_bfe_u32 v249,v224,4,4\n"
"v_lshrrev_b32 v254,8,v224\n"
"v_lshlrev_b32 v254,2,v254\n"
"v_sub_u32 v249,v249,v254\n"
"v_add_u32 v249,s91,v249\n"
"v_mov_b32 v255,0xff800000\n"
"v_mov_b32 v248,v255\n"
"s_mov_b32 s90,0\n"
"s_mov_b32 s88,0\n"
"v_mov_b32 v112,0\n"
"v_mov_b32 v113,0\n"
"v_mov_b32 v114,0\n"
"v_mov_b32 v115,0\n"
"v_mov_b32 v116,0\n"
"v_mov_b32 v117,0\n"
"v_mov_b32 v118,0\n"
"v_mov_b32 v119,0\n"
"v_mov_b32 v120,0\n"
"v_mov_b32 v121,0\n"
"v_mov_b32 v122,0\n"
"v_mov_b32 v123,0\n"
"v_mov_b32 v124,0\n"
"v_mov_b32 v125,0\n"
"v_mov_b32 v126,0\n"
"v_mov_b32 v127,0\n"
"v_mov_b32 v128,0\n"
"v_mov_b32 v129,0\n"
"v_mov_b32 v130,0\n"
"v_mov_b32 v131,0\n"
"v_mov_b32 v132,0\n"
"v_mov_b32 v133,0\n"
"v_mov_b32 v134,0\n"
"v_mov_b32 v135,0\n"
"v_mov_b32 v136,0\n"
"v_mov_b32 v137,0\n"
"v_mov_b32 v138,0\n"
"v_mov_b32 v139,0\n"
"v_mov_b32 v140,0\n"
"v_mov_b32 v141,0\n"
"v_mov_b32 v142,0\n"
"v_mov_b32 v143,0\n"
"v_accvgpr_write_b32 a136,0\n"
"v_accvgpr_write_b32 a137,0\n"
"v_accvgpr_write_b32 a138,0\n"
"v_accvgpr_write_b32 a139,0\n"
"L1024_%=:\n"
"s_lshl_b32 s91,s88,13\n"
SA"s91,s91,s39\n"
SA"s91,s91,0\n"
BL"a[148:151],v224,s[8:11],s91 offen offset:0 sc1\n"
BL"a[152:155],v224,s[8:11],s91 offen offset:1024 sc1\n"
BL"a[156:159],v224,s[8:11],s91 offen offset:2048 sc1\n"
BL"a[160:163],v224,s[8:11],s91 offen offset:3072 sc1\n"
SA"s91,s91,4096\n"
BL"a[164:167],v224,s[8:11],s91 offen offset:0 sc1\n"
BL"a[168:171],v224,s[8:11],s91 offen offset:1024 sc1\n"
BL"a[172:175],v224,s[8:11],s91 offen offset:2048 sc1\n"
BL"a[176:179],v224,s[8:11],s91 offen offset:3072 sc1\n"
WT"vmcnt(0)\n"
MF"v[240:243],a[148:151],a[16:19],0\n"
MF"v[240:243],a[152:155],a[20:23],v[240:243]\n"
MF"v[240:243],a[156:159],a[24:27],v[240:243]\n"
MF"v[240:243],a[160:163],a[28:31],v[240:243]\n"
MF"v[244:247],a[164:167],a[16:19],0\n"
MF"v[244:247],a[168:171],a[20:23],v[244:247]\n"
MF"v[244:247],a[172:175],a[24:27],v[244:247]\n"
MF"v[244:247],a[176:179],a[28:31],v[244:247]\n"
"s_lshl_b32 s91,s88,5\n"
"v_subrev_u32 v250,s91,v249\n"
"s_nop 7\n"
"v_cmp_gt_i32 vcc,0,v250\n"
"s_nop 1\n"
"v_cndmask_b32 v240,v240,v255,vcc\n"
"v_cmp_gt_i32 vcc,1,v250\n"
"s_nop 1\n"
"v_cndmask_b32 v241,v241,v255,vcc\n"
"v_cmp_gt_i32 vcc,2,v250\n"
"s_nop 1\n"
"v_cndmask_b32 v242,v242,v255,vcc\n"
"v_cmp_gt_i32 vcc,3,v250\n"
"s_nop 1\n"
"v_cndmask_b32 v243,v243,v255,vcc\n"
"v_cmp_gt_i32 vcc,16,v250\n"
"s_nop 1\n"
"v_cndmask_b32 v244,v244,v255,vcc\n"
"v_cmp_gt_i32 vcc,17,v250\n"
"s_nop 1\n"
"v_cndmask_b32 v245,v245,v255,vcc\n"
"v_cmp_gt_i32 vcc,18,v250\n"
"s_nop 1\n"
"v_cndmask_b32 v246,v246,v255,vcc\n"
"v_cmp_gt_i32 vcc,19,v250\n"
"s_nop 1\n"
"v_cndmask_b32 v247,v247,v255,vcc\n"
"s_cmp_lg_u32 s90,0\n"
"s_cbranch_scc1 L1025_%=\n"
"v_max3_f32 v248,v248,v240,v241\n"
"v_max3_f32 v248,v248,v242,v243\n"
"v_max3_f32 v248,v248,v244,v245\n"
"v_max3_f32 v248,v248,v246,v247\n"
"s_branch L1026_%=\n"
"L1025_%=:\n"
"s_lshl_b32 s91,s88,13\n"
SA"s91,s91,s39\n"
SA"s91,s91,524288\n"
BL"a[148:151],v224,s[8:11],s91 offen offset:0 sc1\n"
BL"a[152:155],v224,s[8:11],s91 offen offset:1024 sc1\n"
BL"a[156:159],v224,s[8:11],s91 offen offset:2048 sc1\n"
BL"a[160:163],v224,s[8:11],s91 offen offset:3072 sc1\n"
SA"s91,s91,4096\n"
BL"a[164:167],v224,s[8:11],s91 offen offset:0 sc1\n"
BL"a[168:171],v224,s[8:11],s91 offen offset:1024 sc1\n"
BL"a[172:175],v224,s[8:11],s91 offen offset:2048 sc1\n"
BL"a[176:179],v224,s[8:11],s91 offen offset:3072 sc1\n"
WT"vmcnt(0)\n"
"v_sub_f32 v240,v240,v248\n"
"v_sub_f32 v241,v241,v248\n"
"v_sub_f32 v242,v242,v248\n"
"v_sub_f32 v243,v243,v248\n"
"v_sub_f32 v244,v244,v248\n"
"v_sub_f32 v245,v245,v248\n"
"v_sub_f32 v246,v246,v248\n"
"v_sub_f32 v247,v247,v248\n"
EX"v240,v240\n"
EX"v241,v241\n"
EX"v242,v242\n"
EX"v243,v243\n"
EX"v244,v244\n"
EX"v245,v245\n"
EX"v246,v246\n"
EX"v247,v247\n"
"s_nop 0\n"
PK"v240,v240,v241\n"
PK"v241,v242,v243\n"
PK"v242,v244,v245\n"
PK"v243,v246,v247\n"
"s_nop 1\n"
MF"v[112:115],a[148:151],v[240:243],v[112:115]\n"
MF"v[116:119],a[152:155],v[240:243],v[116:119]\n"
MF"v[120:123],a[156:159],v[240:243],v[120:123]\n"
MF"v[124:127],a[160:163],v[240:243],v[124:127]\n"
MF"v[128:131],a[164:167],v[240:243],v[128:131]\n"
MF"v[132:135],a[168:171],v[240:243],v[132:135]\n"
MF"v[136:139],a[172:175],v[240:243],v[136:139]\n"
MF"v[140:143],a[176:179],v[240:243],v[140:143]\n"
MF"a[136:139],a[128:131],v[240:243],a[136:139]\n"
"s_nop 7\n"
"L1026_%=:\n"
SA"s88,s88,1\n"
"s_cmp_le_u32 s88,s89\n"
"s_cbranch_scc1 L1024_%=\n"
"s_cmp_lg_u32 s90,0\n"
"s_cbranch_scc1 L1027_%=\n"
"v_mov_b32 v254,v248\n"
"s_nop 1\n"
"v_permlane16_swap_b32 v254,v248\n"
"s_nop 1\n"
"v_max_f32 v248,v248,v254\n"
"v_mov_b32 v254,v248\n"
"s_nop 1\n"
"v_permlane32_swap_b32 v254,v248\n"
"s_nop 1\n"
"v_max_f32 v248,v248,v254\n"
"s_mov_b32 s90,1\n"
"s_mov_b32 s88,0\n"
"s_branch L1024_%=\n"
"L1027_%=:\n"
"s_nop 7\n"
"v_accvgpr_read_b32 v252,a136\n"
"s_nop 1\n"
"s_branch L1022_%=\n"
"L1028_%=:\n"
"s_mov_b32 s54,0\n"
"L1030_%=:\n"
"s_sleep 2\n"
"buffer_load_dwordx2 v[242:243],v236,s[8:11],0 offen sc1\n"
WT"vmcnt(0)\n"
SA"s54,s54,1\n"
"v_cmp_eq_u64 vcc,s[28:29],v[242:243]\n"
"s_mov_b32 vcc_hi,-1\n"
"s_cmp_eq_u64 vcc,exec\n"
"s_cbranch_scc1 L1029_%=\n"
"s_cmp_lt_u32 s54,1048576\n"
"s_cbranch_scc1 L1030_%=\n"
"s_branch L1029_%=\n"
"L1031_%=:\n"
"s_mov_b32 s54,0\n"
"L1033_%=:\n"
"s_sleep 2\n"
"buffer_load_dwordx2 v[242:243],v236,s[8:11],0 offen sc1\n"
WT"vmcnt(0)\n"
SA"s54,s54,1\n"
"v_cmp_eq_u64 vcc,s[28:29],v[242:243]\n"
"s_mov_b32 vcc_lo,-1\n"
"s_cmp_eq_u64 vcc,exec\n"
"s_cbranch_scc1 L1032_%=\n"
"s_cmp_lt_u32 s54,1048576\n"
"s_cbranch_scc1 L1033_%=\n"
"s_branch L1032_%=\n"
"L1035_%=:\n"
"s_nop 15\n"
WT"vmcnt(0)\n"
"s_lshl_b32 s89,s34,3\n"
SA"s89,s89,s30\n"
SA"s89,s89,4\n"
"s_lshl_b32 s91,s89,4\n"
"s_lshr_b32 s89,s89,1\n"
"v_bfe_u32 v249,v224,4,4\n"
"v_lshrrev_b32 v254,8,v224\n"
"v_lshlrev_b32 v254,2,v254\n"
"v_sub_u32 v249,v249,v254\n"
"v_add_u32 v249,s91,v249\n"
"v_mov_b32 v255,0xff800000\n"
"v_mov_b32 v248,v255\n"
"s_mov_b32 s90,0\n"
"s_mov_b32 s88,0\n"
"v_mov_b32 v112,0\n"
"v_mov_b32 v113,0\n"
"v_mov_b32 v114,0\n"
"v_mov_b32 v115,0\n"
"v_mov_b32 v116,0\n"
"v_mov_b32 v117,0\n"
"v_mov_b32 v118,0\n"
"v_mov_b32 v119,0\n"
"v_mov_b32 v120,0\n"
"v_mov_b32 v121,0\n"
"v_mov_b32 v122,0\n"
"v_mov_b32 v123,0\n"
"v_mov_b32 v124,0\n"
"v_mov_b32 v125,0\n"
"v_mov_b32 v126,0\n"
"v_mov_b32 v127,0\n"
"v_mov_b32 v128,0\n"
"v_mov_b32 v129,0\n"
"v_mov_b32 v130,0\n"
"v_mov_b32 v131,0\n"
"v_mov_b32 v132,0\n"
"v_mov_b32 v133,0\n"
"v_mov_b32 v134,0\n"
"v_mov_b32 v135,0\n"
"v_mov_b32 v136,0\n"
"v_mov_b32 v137,0\n"
"v_mov_b32 v138,0\n"
"v_mov_b32 v139,0\n"
"v_mov_b32 v140,0\n"
"v_mov_b32 v141,0\n"
"v_mov_b32 v142,0\n"
"v_mov_b32 v143,0\n"
"v_accvgpr_write_b32 a136,0\n"
"v_accvgpr_write_b32 a137,0\n"
"v_accvgpr_write_b32 a138,0\n"
"v_accvgpr_write_b32 a139,0\n"
"L1036_%=:\n"
"s_lshl_b32 s91,s88,13\n"
SA"s91,s91,s39\n"
SA"s91,s91,0\n"
BL"a[148:151],v224,s[8:11],s91 offen offset:0 sc1\n"
BL"a[152:155],v224,s[8:11],s91 offen offset:1024 sc1\n"
BL"a[156:159],v224,s[8:11],s91 offen offset:2048 sc1\n"
BL"a[160:163],v224,s[8:11],s91 offen offset:3072 sc1\n"
SA"s91,s91,4096\n"
BL"a[164:167],v224,s[8:11],s91 offen offset:0 sc1\n"
BL"a[168:171],v224,s[8:11],s91 offen offset:1024 sc1\n"
BL"a[172:175],v224,s[8:11],s91 offen offset:2048 sc1\n"
BL"a[176:179],v224,s[8:11],s91 offen offset:3072 sc1\n"
WT"vmcnt(0)\n"
MF"v[240:243],a[148:151],a[16:19],0\n"
MF"v[240:243],a[152:155],a[20:23],v[240:243]\n"
MF"v[240:243],a[156:159],a[24:27],v[240:243]\n"
MF"v[240:243],a[160:163],a[28:31],v[240:243]\n"
MF"v[244:247],a[164:167],a[16:19],0\n"
MF"v[244:247],a[168:171],a[20:23],v[244:247]\n"
MF"v[244:247],a[172:175],a[24:27],v[244:247]\n"
MF"v[244:247],a[176:179],a[28:31],v[244:247]\n"
"s_lshl_b32 s91,s88,5\n"
"v_subrev_u32 v250,s91,v249\n"
"s_nop 7\n"
"v_cmp_gt_i32 vcc,0,v250\n"
"s_nop 1\n"
"v_cndmask_b32 v240,v240,v255,vcc\n"
"v_cmp_gt_i32 vcc,1,v250\n"
"s_nop 1\n"
"v_cndmask_b32 v241,v241,v255,vcc\n"
"v_cmp_gt_i32 vcc,2,v250\n"
"s_nop 1\n"
"v_cndmask_b32 v242,v242,v255,vcc\n"
"v_cmp_gt_i32 vcc,3,v250\n"
"s_nop 1\n"
"v_cndmask_b32 v243,v243,v255,vcc\n"
"v_cmp_gt_i32 vcc,16,v250\n"
"s_nop 1\n"
"v_cndmask_b32 v244,v244,v255,vcc\n"
"v_cmp_gt_i32 vcc,17,v250\n"
"s_nop 1\n"
"v_cndmask_b32 v245,v245,v255,vcc\n"
"v_cmp_gt_i32 vcc,18,v250\n"
"s_nop 1\n"
"v_cndmask_b32 v246,v246,v255,vcc\n"
"v_cmp_gt_i32 vcc,19,v250\n"
"s_nop 1\n"
"v_cndmask_b32 v247,v247,v255,vcc\n"
"s_cmp_lg_u32 s90,0\n"
"s_cbranch_scc1 L1037_%=\n"
"v_max3_f32 v248,v248,v240,v241\n"
"v_max3_f32 v248,v248,v242,v243\n"
"v_max3_f32 v248,v248,v244,v245\n"
"v_max3_f32 v248,v248,v246,v247\n"
"s_branch L1038_%=\n"
"L1037_%=:\n"
"s_lshl_b32 s91,s88,13\n"
SA"s91,s91,s39\n"
SA"s91,s91,524288\n"
BL"a[148:151],v224,s[8:11],s91 offen offset:0 sc1\n"
BL"a[152:155],v224,s[8:11],s91 offen offset:1024 sc1\n"
BL"a[156:159],v224,s[8:11],s91 offen offset:2048 sc1\n"
BL"a[160:163],v224,s[8:11],s91 offen offset:3072 sc1\n"
SA"s91,s91,4096\n"
BL"a[164:167],v224,s[8:11],s91 offen offset:0 sc1\n"
BL"a[168:171],v224,s[8:11],s91 offen offset:1024 sc1\n"
BL"a[172:175],v224,s[8:11],s91 offen offset:2048 sc1\n"
BL"a[176:179],v224,s[8:11],s91 offen offset:3072 sc1\n"
WT"vmcnt(0)\n"
"v_sub_f32 v240,v240,v248\n"
"v_sub_f32 v241,v241,v248\n"
"v_sub_f32 v242,v242,v248\n"
"v_sub_f32 v243,v243,v248\n"
"v_sub_f32 v244,v244,v248\n"
"v_sub_f32 v245,v245,v248\n"
"v_sub_f32 v246,v246,v248\n"
"v_sub_f32 v247,v247,v248\n"
EX"v240,v240\n"
EX"v241,v241\n"
EX"v242,v242\n"
EX"v243,v243\n"
EX"v244,v244\n"
EX"v245,v245\n"
EX"v246,v246\n"
EX"v247,v247\n"
"s_nop 0\n"
PK"v240,v240,v241\n"
PK"v241,v242,v243\n"
PK"v242,v244,v245\n"
PK"v243,v246,v247\n"
"s_nop 1\n"
MF"v[112:115],a[148:151],v[240:243],v[112:115]\n"
MF"v[116:119],a[152:155],v[240:243],v[116:119]\n"
MF"v[120:123],a[156:159],v[240:243],v[120:123]\n"
MF"v[124:127],a[160:163],v[240:243],v[124:127]\n"
MF"v[128:131],a[164:167],v[240:243],v[128:131]\n"
MF"v[132:135],a[168:171],v[240:243],v[132:135]\n"
MF"v[136:139],a[172:175],v[240:243],v[136:139]\n"
MF"v[140:143],a[176:179],v[240:243],v[140:143]\n"
MF"a[136:139],a[128:131],v[240:243],a[136:139]\n"
"s_nop 7\n"
"L1038_%=:\n"
SA"s88,s88,1\n"
"s_cmp_le_u32 s88,s89\n"
"s_cbranch_scc1 L1036_%=\n"
"s_cmp_lg_u32 s90,0\n"
"s_cbranch_scc1 L1039_%=\n"
"v_mov_b32 v254,v248\n"
"s_nop 1\n"
"v_permlane16_swap_b32 v254,v248\n"
"s_nop 1\n"
"v_max_f32 v248,v248,v254\n"
"v_mov_b32 v254,v248\n"
"s_nop 1\n"
"v_permlane32_swap_b32 v254,v248\n"
"s_nop 1\n"
"v_max_f32 v248,v248,v254\n"
"s_mov_b32 s90,1\n"
"s_mov_b32 s88,0\n"
"s_branch L1036_%=\n"
"L1039_%=:\n"
"s_nop 7\n"
"v_accvgpr_read_b32 v252,a136\n"
"s_nop 1\n"
"s_branch L1034_%=\n"
"L1041_%=:\n"
"s_nop 15\n"
WT"vmcnt(0)\n"
"s_lshl_b32 s89,s34,3\n"
"s_sub_u32 s89,120,s89\n"
SA"s89,s89,s30\n"
SA"s89,s89,0\n"
"s_lshl_b32 s91,s89,4\n"
"s_lshr_b32 s89,s89,1\n"
"v_bfe_u32 v249,v224,4,4\n"
"v_lshrrev_b32 v254,8,v224\n"
"v_lshlrev_b32 v254,2,v254\n"
"v_sub_u32 v249,v249,v254\n"
"v_add_u32 v249,s91,v249\n"
"v_mov_b32 v255,0xff800000\n"
"v_mov_b32 v248,v255\n"
"s_mov_b32 s90,0\n"
"s_mov_b32 s88,0\n"
"v_mov_b32 v144,0\n"
"v_mov_b32 v145,0\n"
"v_mov_b32 v146,0\n"
"v_mov_b32 v147,0\n"
"v_mov_b32 v148,0\n"
"v_mov_b32 v149,0\n"
"v_mov_b32 v150,0\n"
"v_mov_b32 v151,0\n"
"v_mov_b32 v152,0\n"
"v_mov_b32 v153,0\n"
"v_mov_b32 v154,0\n"
"v_mov_b32 v155,0\n"
"v_mov_b32 v156,0\n"
"v_mov_b32 v157,0\n"
"v_mov_b32 v158,0\n"
"v_mov_b32 v159,0\n"
"v_mov_b32 v160,0\n"
"v_mov_b32 v161,0\n"
"v_mov_b32 v162,0\n"
"v_mov_b32 v163,0\n"
"v_mov_b32 v164,0\n"
"v_mov_b32 v165,0\n"
"v_mov_b32 v166,0\n"
"v_mov_b32 v167,0\n"
"v_mov_b32 v168,0\n"
"v_mov_b32 v169,0\n"
"v_mov_b32 v170,0\n"
"v_mov_b32 v171,0\n"
"v_mov_b32 v172,0\n"
"v_mov_b32 v173,0\n"
"v_mov_b32 v174,0\n"
"v_mov_b32 v175,0\n"
"v_accvgpr_write_b32 a140,0\n"
"v_accvgpr_write_b32 a141,0\n"
"v_accvgpr_write_b32 a142,0\n"
"v_accvgpr_write_b32 a143,0\n"
"L1042_%=:\n"
"s_lshl_b32 s91,s88,13\n"
SA"s91,s91,s39\n"
SA"s91,s91,0\n"
BL"a[148:151],v224,s[8:11],s91 offen offset:0 sc1\n"
BL"a[152:155],v224,s[8:11],s91 offen offset:1024 sc1\n"
BL"a[156:159],v224,s[8:11],s91 offen offset:2048 sc1\n"
BL"a[160:163],v224,s[8:11],s91 offen offset:3072 sc1\n"
SA"s91,s91,4096\n"
BL"a[164:167],v224,s[8:11],s91 offen offset:0 sc1\n"
BL"a[168:171],v224,s[8:11],s91 offen offset:1024 sc1\n"
BL"a[172:175],v224,s[8:11],s91 offen offset:2048 sc1\n"
BL"a[176:179],v224,s[8:11],s91 offen offset:3072 sc1\n"
WT"vmcnt(0)\n"
MF"v[240:243],a[148:151],a[32:35],0\n"
MF"v[240:243],a[152:155],a[36:39],v[240:243]\n"
MF"v[240:243],a[156:159],a[40:43],v[240:243]\n"
MF"v[240:243],a[160:163],a[44:47],v[240:243]\n"
MF"v[244:247],a[164:167],a[32:35],0\n"
MF"v[244:247],a[168:171],a[36:39],v[244:247]\n"
MF"v[244:247],a[172:175],a[40:43],v[244:247]\n"
MF"v[244:247],a[176:179],a[44:47],v[244:247]\n"
"s_lshl_b32 s91,s88,5\n"
"v_subrev_u32 v250,s91,v249\n"
"s_nop 7\n"
"v_cmp_gt_i32 vcc,0,v250\n"
"s_nop 1\n"
"v_cndmask_b32 v240,v240,v255,vcc\n"
"v_cmp_gt_i32 vcc,1,v250\n"
"s_nop 1\n"
"v_cndmask_b32 v241,v241,v255,vcc\n"
"v_cmp_gt_i32 vcc,2,v250\n"
"s_nop 1\n"
"v_cndmask_b32 v242,v242,v255,vcc\n"
"v_cmp_gt_i32 vcc,3,v250\n"
"s_nop 1\n"
"v_cndmask_b32 v243,v243,v255,vcc\n"
"v_cmp_gt_i32 vcc,16,v250\n"
"s_nop 1\n"
"v_cndmask_b32 v244,v244,v255,vcc\n"
"v_cmp_gt_i32 vcc,17,v250\n"
"s_nop 1\n"
"v_cndmask_b32 v245,v245,v255,vcc\n"
"v_cmp_gt_i32 vcc,18,v250\n"
"s_nop 1\n"
"v_cndmask_b32 v246,v246,v255,vcc\n"
"v_cmp_gt_i32 vcc,19,v250\n"
"s_nop 1\n"
"v_cndmask_b32 v247,v247,v255,vcc\n"
"s_cmp_lg_u32 s90,0\n"
"s_cbranch_scc1 L1043_%=\n"
"v_max3_f32 v248,v248,v240,v241\n"
"v_max3_f32 v248,v248,v242,v243\n"
"v_max3_f32 v248,v248,v244,v245\n"
"v_max3_f32 v248,v248,v246,v247\n"
"s_branch L1044_%=\n"
"L1043_%=:\n"
"s_lshl_b32 s91,s88,13\n"
SA"s91,s91,s39\n"
SA"s91,s91,524288\n"
BL"a[148:151],v224,s[8:11],s91 offen offset:0 sc1\n"
BL"a[152:155],v224,s[8:11],s91 offen offset:1024 sc1\n"
BL"a[156:159],v224,s[8:11],s91 offen offset:2048 sc1\n"
BL"a[160:163],v224,s[8:11],s91 offen offset:3072 sc1\n"
SA"s91,s91,4096\n"
BL"a[164:167],v224,s[8:11],s91 offen offset:0 sc1\n"
BL"a[168:171],v224,s[8:11],s91 offen offset:1024 sc1\n"
BL"a[172:175],v224,s[8:11],s91 offen offset:2048 sc1\n"
BL"a[176:179],v224,s[8:11],s91 offen offset:3072 sc1\n"
WT"vmcnt(0)\n"
"v_sub_f32 v240,v240,v248\n"
"v_sub_f32 v241,v241,v248\n"
"v_sub_f32 v242,v242,v248\n"
"v_sub_f32 v243,v243,v248\n"
"v_sub_f32 v244,v244,v248\n"
"v_sub_f32 v245,v245,v248\n"
"v_sub_f32 v246,v246,v248\n"
"v_sub_f32 v247,v247,v248\n"
EX"v240,v240\n"
EX"v241,v241\n"
EX"v242,v242\n"
EX"v243,v243\n"
EX"v244,v244\n"
EX"v245,v245\n"
EX"v246,v246\n"
EX"v247,v247\n"
"s_nop 0\n"
PK"v240,v240,v241\n"
PK"v241,v242,v243\n"
PK"v242,v244,v245\n"
PK"v243,v246,v247\n"
"s_nop 1\n"
MF"v[144:147],a[148:151],v[240:243],v[144:147]\n"
MF"v[148:151],a[152:155],v[240:243],v[148:151]\n"
MF"v[152:155],a[156:159],v[240:243],v[152:155]\n"
MF"v[156:159],a[160:163],v[240:243],v[156:159]\n"
MF"v[160:163],a[164:167],v[240:243],v[160:163]\n"
MF"v[164:167],a[168:171],v[240:243],v[164:167]\n"
MF"v[168:171],a[172:175],v[240:243],v[168:171]\n"
MF"v[172:175],a[176:179],v[240:243],v[172:175]\n"
MF"a[140:143],a[128:131],v[240:243],a[140:143]\n"
"s_nop 7\n"
"L1044_%=:\n"
SA"s88,s88,1\n"
"s_cmp_le_u32 s88,s89\n"
"s_cbranch_scc1 L1042_%=\n"
"s_cmp_lg_u32 s90,0\n"
"s_cbranch_scc1 L1045_%=\n"
"v_mov_b32 v254,v248\n"
"s_nop 1\n"
"v_permlane16_swap_b32 v254,v248\n"
"s_nop 1\n"
"v_max_f32 v248,v248,v254\n"
"v_mov_b32 v254,v248\n"
"s_nop 1\n"
"v_permlane32_swap_b32 v254,v248\n"
"s_nop 1\n"
"v_max_f32 v248,v248,v254\n"
"s_mov_b32 s90,1\n"
"s_mov_b32 s88,0\n"
"s_branch L1042_%=\n"
"L1045_%=:\n"
"s_nop 7\n"
"v_accvgpr_read_b32 v251,a140\n"
"s_nop 1\n"
"s_branch L1040_%=\n"
"L1047_%=:\n"
"s_nop 15\n"
WT"vmcnt(0)\n"
"s_lshl_b32 s89,s34,3\n"
"s_sub_u32 s89,120,s89\n"
SA"s89,s89,s30\n"
SA"s89,s89,4\n"
"s_lshl_b32 s91,s89,4\n"
"s_lshr_b32 s89,s89,1\n"
"v_bfe_u32 v249,v224,4,4\n"
"v_lshrrev_b32 v254,8,v224\n"
"v_lshlrev_b32 v254,2,v254\n"
"v_sub_u32 v249,v249,v254\n"
"v_add_u32 v249,s91,v249\n"
"v_mov_b32 v255,0xff800000\n"
"v_mov_b32 v248,v255\n"
"s_mov_b32 s90,0\n"
"s_mov_b32 s88,0\n"
"v_mov_b32 v176,0\n"
"v_mov_b32 v177,0\n"
"v_mov_b32 v178,0\n"
"v_mov_b32 v179,0\n"
"v_mov_b32 v180,0\n"
"v_mov_b32 v181,0\n"
"v_mov_b32 v182,0\n"
"v_mov_b32 v183,0\n"
"v_mov_b32 v184,0\n"
"v_mov_b32 v185,0\n"
"v_mov_b32 v186,0\n"
"v_mov_b32 v187,0\n"
"v_mov_b32 v188,0\n"
"v_mov_b32 v189,0\n"
"v_mov_b32 v190,0\n"
"v_mov_b32 v191,0\n"
"v_mov_b32 v192,0\n"
"v_mov_b32 v193,0\n"
"v_mov_b32 v194,0\n"
"v_mov_b32 v195,0\n"
"v_mov_b32 v196,0\n"
"v_mov_b32 v197,0\n"
"v_mov_b32 v198,0\n"
"v_mov_b32 v199,0\n"
"v_mov_b32 v200,0\n"
"v_mov_b32 v201,0\n"
"v_mov_b32 v202,0\n"
"v_mov_b32 v203,0\n"
"v_mov_b32 v204,0\n"
"v_mov_b32 v205,0\n"
"v_mov_b32 v206,0\n"
"v_mov_b32 v207,0\n"
"v_accvgpr_write_b32 a144,0\n"
"v_accvgpr_write_b32 a145,0\n"
"v_accvgpr_write_b32 a146,0\n"
"v_accvgpr_write_b32 a147,0\n"
"L1048_%=:\n"
"s_lshl_b32 s91,s88,13\n"
SA"s91,s91,s39\n"
SA"s91,s91,0\n"
BL"a[148:151],v224,s[8:11],s91 offen offset:0 sc1\n"
BL"a[152:155],v224,s[8:11],s91 offen offset:1024 sc1\n"
BL"a[156:159],v224,s[8:11],s91 offen offset:2048 sc1\n"
BL"a[160:163],v224,s[8:11],s91 offen offset:3072 sc1\n"
SA"s91,s91,4096\n"
BL"a[164:167],v224,s[8:11],s91 offen offset:0 sc1\n"
BL"a[168:171],v224,s[8:11],s91 offen offset:1024 sc1\n"
BL"a[172:175],v224,s[8:11],s91 offen offset:2048 sc1\n"
BL"a[176:179],v224,s[8:11],s91 offen offset:3072 sc1\n"
WT"vmcnt(0)\n"
MF"v[240:243],a[148:151],a[48:51],0\n"
MF"v[240:243],a[152:155],a[52:55],v[240:243]\n"
MF"v[240:243],a[156:159],a[56:59],v[240:243]\n"
MF"v[240:243],a[160:163],a[60:63],v[240:243]\n"
MF"v[244:247],a[164:167],a[48:51],0\n"
MF"v[244:247],a[168:171],a[52:55],v[244:247]\n"
MF"v[244:247],a[172:175],a[56:59],v[244:247]\n"
MF"v[244:247],a[176:179],a[60:63],v[244:247]\n"
"s_lshl_b32 s91,s88,5\n"
"v_subrev_u32 v250,s91,v249\n"
"s_nop 7\n"
"v_cmp_gt_i32 vcc,0,v250\n"
"s_nop 1\n"
"v_cndmask_b32 v240,v240,v255,vcc\n"
"v_cmp_gt_i32 vcc,1,v250\n"
"s_nop 1\n"
"v_cndmask_b32 v241,v241,v255,vcc\n"
"v_cmp_gt_i32 vcc,2,v250\n"
"s_nop 1\n"
"v_cndmask_b32 v242,v242,v255,vcc\n"
"v_cmp_gt_i32 vcc,3,v250\n"
"s_nop 1\n"
"v_cndmask_b32 v243,v243,v255,vcc\n"
"v_cmp_gt_i32 vcc,16,v250\n"
"s_nop 1\n"
"v_cndmask_b32 v244,v244,v255,vcc\n"
"v_cmp_gt_i32 vcc,17,v250\n"
"s_nop 1\n"
"v_cndmask_b32 v245,v245,v255,vcc\n"
"v_cmp_gt_i32 vcc,18,v250\n"
"s_nop 1\n"
"v_cndmask_b32 v246,v246,v255,vcc\n"
"v_cmp_gt_i32 vcc,19,v250\n"
"s_nop 1\n"
"v_cndmask_b32 v247,v247,v255,vcc\n"
"s_cmp_lg_u32 s90,0\n"
"s_cbranch_scc1 L1049_%=\n"
"v_max3_f32 v248,v248,v240,v241\n"
"v_max3_f32 v248,v248,v242,v243\n"
"v_max3_f32 v248,v248,v244,v245\n"
"v_max3_f32 v248,v248,v246,v247\n"
"s_branch L1050_%=\n"
"L1049_%=:\n"
"s_lshl_b32 s91,s88,13\n"
SA"s91,s91,s39\n"
SA"s91,s91,524288\n"
BL"a[148:151],v224,s[8:11],s91 offen offset:0 sc1\n"
BL"a[152:155],v224,s[8:11],s91 offen offset:1024 sc1\n"
BL"a[156:159],v224,s[8:11],s91 offen offset:2048 sc1\n"
BL"a[160:163],v224,s[8:11],s91 offen offset:3072 sc1\n"
SA"s91,s91,4096\n"
BL"a[164:167],v224,s[8:11],s91 offen offset:0 sc1\n"
BL"a[168:171],v224,s[8:11],s91 offen offset:1024 sc1\n"
BL"a[172:175],v224,s[8:11],s91 offen offset:2048 sc1\n"
BL"a[176:179],v224,s[8:11],s91 offen offset:3072 sc1\n"
WT"vmcnt(0)\n"
"v_sub_f32 v240,v240,v248\n"
"v_sub_f32 v241,v241,v248\n"
"v_sub_f32 v242,v242,v248\n"
"v_sub_f32 v243,v243,v248\n"
"v_sub_f32 v244,v244,v248\n"
"v_sub_f32 v245,v245,v248\n"
"v_sub_f32 v246,v246,v248\n"
"v_sub_f32 v247,v247,v248\n"
EX"v240,v240\n"
EX"v241,v241\n"
EX"v242,v242\n"
EX"v243,v243\n"
EX"v244,v244\n"
EX"v245,v245\n"
EX"v246,v246\n"
EX"v247,v247\n"
"s_nop 0\n"
PK"v240,v240,v241\n"
PK"v241,v242,v243\n"
PK"v242,v244,v245\n"
PK"v243,v246,v247\n"
"s_nop 1\n"
MF"v[176:179],a[148:151],v[240:243],v[176:179]\n"
MF"v[180:183],a[152:155],v[240:243],v[180:183]\n"
MF"v[184:187],a[156:159],v[240:243],v[184:187]\n"
MF"v[188:191],a[160:163],v[240:243],v[188:191]\n"
MF"v[192:195],a[164:167],v[240:243],v[192:195]\n"
MF"v[196:199],a[168:171],v[240:243],v[196:199]\n"
MF"v[200:203],a[172:175],v[240:243],v[200:203]\n"
MF"v[204:207],a[176:179],v[240:243],v[204:207]\n"
MF"a[144:147],a[128:131],v[240:243],a[144:147]\n"
"s_nop 7\n"
"L1050_%=:\n"
SA"s88,s88,1\n"
"s_cmp_le_u32 s88,s89\n"
"s_cbranch_scc1 L1048_%=\n"
"s_cmp_lg_u32 s90,0\n"
"s_cbranch_scc1 L1051_%=\n"
"v_mov_b32 v254,v248\n"
"s_nop 1\n"
"v_permlane16_swap_b32 v254,v248\n"
"s_nop 1\n"
"v_max_f32 v248,v248,v254\n"
"v_mov_b32 v254,v248\n"
"s_nop 1\n"
"v_permlane32_swap_b32 v254,v248\n"
"s_nop 1\n"
"v_max_f32 v248,v248,v254\n"
"s_mov_b32 s90,1\n"
"s_mov_b32 s88,0\n"
"s_branch L1048_%=\n"
"L1051_%=:\n"
"s_nop 7\n"
"v_accvgpr_read_b32 v252,a144\n"
"s_nop 1\n"
"s_branch L1046_%=\n"
"L1069_%=:\n"
"s_nop 15\n"
WT"vmcnt(0)\n"
"s_lshl_b32 s89,s34,3\n"
"s_sub_u32 s89,120,s89\n"
SA"s89,s89,s30\n"
SA"s89,s89,0\n"
"s_lshl_b32 s91,s89,4\n"
"s_lshr_b32 s89,s89,1\n"
"v_bfe_u32 v249,v224,4,4\n"
"v_lshrrev_b32 v254,8,v224\n"
"v_lshlrev_b32 v254,2,v254\n"
"v_sub_u32 v249,v249,v254\n"
"v_add_u32 v249,s91,v249\n"
"v_mov_b32 v255,0xff800000\n"
"v_mov_b32 v248,v255\n"
"s_mov_b32 s90,0\n"
"s_mov_b32 s88,0\n"
"v_mov_b32 v144,0\n"
"v_mov_b32 v145,0\n"
"v_mov_b32 v146,0\n"
"v_mov_b32 v147,0\n"
"v_mov_b32 v148,0\n"
"v_mov_b32 v149,0\n"
"v_mov_b32 v150,0\n"
"v_mov_b32 v151,0\n"
"v_mov_b32 v152,0\n"
"v_mov_b32 v153,0\n"
"v_mov_b32 v154,0\n"
"v_mov_b32 v155,0\n"
"v_mov_b32 v156,0\n"
"v_mov_b32 v157,0\n"
"v_mov_b32 v158,0\n"
"v_mov_b32 v159,0\n"
"v_mov_b32 v160,0\n"
"v_mov_b32 v161,0\n"
"v_mov_b32 v162,0\n"
"v_mov_b32 v163,0\n"
"v_mov_b32 v164,0\n"
"v_mov_b32 v165,0\n"
"v_mov_b32 v166,0\n"
"v_mov_b32 v167,0\n"
"v_mov_b32 v168,0\n"
"v_mov_b32 v169,0\n"
"v_mov_b32 v170,0\n"
"v_mov_b32 v171,0\n"
"v_mov_b32 v172,0\n"
"v_mov_b32 v173,0\n"
"v_mov_b32 v174,0\n"
"v_mov_b32 v175,0\n"
"v_accvgpr_write_b32 a140,0\n"
"v_accvgpr_write_b32 a141,0\n"
"v_accvgpr_write_b32 a142,0\n"
"v_accvgpr_write_b32 a143,0\n"
"L1070_%=:\n"
"s_lshl_b32 s91,s88,13\n"
SA"s91,s91,s39\n"
SA"s91,s91,0\n"
BL"a[148:151],v224,s[8:11],s91 offen offset:0 sc1\n"
BL"a[152:155],v224,s[8:11],s91 offen offset:1024 sc1\n"
BL"a[156:159],v224,s[8:11],s91 offen offset:2048 sc1\n"
BL"a[160:163],v224,s[8:11],s91 offen offset:3072 sc1\n"
SA"s91,s91,4096\n"
BL"a[164:167],v224,s[8:11],s91 offen offset:0 sc1\n"
BL"a[168:171],v224,s[8:11],s91 offen offset:1024 sc1\n"
BL"a[172:175],v224,s[8:11],s91 offen offset:2048 sc1\n"
BL"a[176:179],v224,s[8:11],s91 offen offset:3072 sc1\n"
WT"vmcnt(0)\n"
MF"v[240:243],a[148:151],a[32:35],0\n"
MF"v[240:243],a[152:155],a[36:39],v[240:243]\n"
MF"v[240:243],a[156:159],a[40:43],v[240:243]\n"
MF"v[240:243],a[160:163],a[44:47],v[240:243]\n"
MF"v[244:247],a[164:167],a[32:35],0\n"
MF"v[244:247],a[168:171],a[36:39],v[244:247]\n"
MF"v[244:247],a[172:175],a[40:43],v[244:247]\n"
MF"v[244:247],a[176:179],a[44:47],v[244:247]\n"
"s_lshl_b32 s91,s88,5\n"
"v_subrev_u32 v250,s91,v249\n"
"s_nop 7\n"
"v_cmp_gt_i32 vcc,0,v250\n"
"s_nop 1\n"
"v_cndmask_b32 v240,v240,v255,vcc\n"
"v_cmp_gt_i32 vcc,1,v250\n"
"s_nop 1\n"
"v_cndmask_b32 v241,v241,v255,vcc\n"
"v_cmp_gt_i32 vcc,2,v250\n"
"s_nop 1\n"
"v_cndmask_b32 v242,v242,v255,vcc\n"
"v_cmp_gt_i32 vcc,3,v250\n"
"s_nop 1\n"
"v_cndmask_b32 v243,v243,v255,vcc\n"
"v_cmp_gt_i32 vcc,16,v250\n"
"s_nop 1\n"
"v_cndmask_b32 v244,v244,v255,vcc\n"
"v_cmp_gt_i32 vcc,17,v250\n"
"s_nop 1\n"
"v_cndmask_b32 v245,v245,v255,vcc\n"
"v_cmp_gt_i32 vcc,18,v250\n"
"s_nop 1\n"
"v_cndmask_b32 v246,v246,v255,vcc\n"
"v_cmp_gt_i32 vcc,19,v250\n"
"s_nop 1\n"
"v_cndmask_b32 v247,v247,v255,vcc\n"
"s_cmp_lg_u32 s90,0\n"
"s_cbranch_scc1 L1071_%=\n"
"v_max3_f32 v248,v248,v240,v241\n"
"v_max3_f32 v248,v248,v242,v243\n"
"v_max3_f32 v248,v248,v244,v245\n"
"v_max3_f32 v248,v248,v246,v247\n"
"s_branch L1072_%=\n"
"L1071_%=:\n"
"s_lshl_b32 s91,s88,13\n"
SA"s91,s91,s39\n"
SA"s91,s91,524288\n"
BL"a[148:151],v224,s[8:11],s91 offen offset:0 sc1\n"
BL"a[152:155],v224,s[8:11],s91 offen offset:1024 sc1\n"
BL"a[156:159],v224,s[8:11],s91 offen offset:2048 sc1\n"
BL"a[160:163],v224,s[8:11],s91 offen offset:3072 sc1\n"
SA"s91,s91,4096\n"
BL"a[164:167],v224,s[8:11],s91 offen offset:0 sc1\n"
BL"a[168:171],v224,s[8:11],s91 offen offset:1024 sc1\n"
BL"a[172:175],v224,s[8:11],s91 offen offset:2048 sc1\n"
BL"a[176:179],v224,s[8:11],s91 offen offset:3072 sc1\n"
WT"vmcnt(0)\n"
"v_sub_f32 v240,v240,v248\n"
"v_sub_f32 v241,v241,v248\n"
"v_sub_f32 v242,v242,v248\n"
"v_sub_f32 v243,v243,v248\n"
"v_sub_f32 v244,v244,v248\n"
"v_sub_f32 v245,v245,v248\n"
"v_sub_f32 v246,v246,v248\n"
"v_sub_f32 v247,v247,v248\n"
EX"v240,v240\n"
EX"v241,v241\n"
EX"v242,v242\n"
EX"v243,v243\n"
EX"v244,v244\n"
EX"v245,v245\n"
EX"v246,v246\n"
EX"v247,v247\n"
"s_nop 0\n"
PK"v240,v240,v241\n"
PK"v241,v242,v243\n"
PK"v242,v244,v245\n"
PK"v243,v246,v247\n"
"s_nop 1\n"
MF"v[144:147],a[148:151],v[240:243],v[144:147]\n"
MF"v[148:151],a[152:155],v[240:243],v[148:151]\n"
MF"v[152:155],a[156:159],v[240:243],v[152:155]\n"
MF"v[156:159],a[160:163],v[240:243],v[156:159]\n"
MF"v[160:163],a[164:167],v[240:243],v[160:163]\n"
MF"v[164:167],a[168:171],v[240:243],v[164:167]\n"
MF"v[168:171],a[172:175],v[240:243],v[168:171]\n"
MF"v[172:175],a[176:179],v[240:243],v[172:175]\n"
MF"a[140:143],a[128:131],v[240:243],a[140:143]\n"
"s_nop 7\n"
"L1072_%=:\n"
SA"s88,s88,1\n"
"s_cmp_le_u32 s88,s89\n"
"s_cbranch_scc1 L1070_%=\n"
"s_cmp_lg_u32 s90,0\n"
"s_cbranch_scc1 L1073_%=\n"
"v_mov_b32 v254,v248\n"
"s_nop 1\n"
"v_permlane16_swap_b32 v254,v248\n"
"s_nop 1\n"
"v_max_f32 v248,v248,v254\n"
"v_mov_b32 v254,v248\n"
"s_nop 1\n"
"v_permlane32_swap_b32 v254,v248\n"
"s_nop 1\n"
"v_max_f32 v248,v248,v254\n"
"s_mov_b32 s90,1\n"
"s_mov_b32 s88,0\n"
"s_branch L1070_%=\n"
"L1073_%=:\n"
"s_nop 7\n"
"v_accvgpr_read_b32 v251,a140\n"
"s_nop 1\n"
"s_branch L1068_%=\n"
"L1075_%=:\n"
"s_nop 15\n"
WT"vmcnt(0)\n"
"s_lshl_b32 s89,s34,3\n"
"s_sub_u32 s89,120,s89\n"
SA"s89,s89,s30\n"
SA"s89,s89,4\n"
"s_lshl_b32 s91,s89,4\n"
"s_lshr_b32 s89,s89,1\n"
"v_bfe_u32 v249,v224,4,4\n"
"v_lshrrev_b32 v254,8,v224\n"
"v_lshlrev_b32 v254,2,v254\n"
"v_sub_u32 v249,v249,v254\n"
"v_add_u32 v249,s91,v249\n"
"v_mov_b32 v255,0xff800000\n"
"v_mov_b32 v248,v255\n"
"s_mov_b32 s90,0\n"
"s_mov_b32 s88,0\n"
"v_mov_b32 v176,0\n"
"v_mov_b32 v177,0\n"
"v_mov_b32 v178,0\n"
"v_mov_b32 v179,0\n"
"v_mov_b32 v180,0\n"
"v_mov_b32 v181,0\n"
"v_mov_b32 v182,0\n"
"v_mov_b32 v183,0\n"
"v_mov_b32 v184,0\n"
"v_mov_b32 v185,0\n"
"v_mov_b32 v186,0\n"
"v_mov_b32 v187,0\n"
"v_mov_b32 v188,0\n"
"v_mov_b32 v189,0\n"
"v_mov_b32 v190,0\n"
"v_mov_b32 v191,0\n"
"v_mov_b32 v192,0\n"
"v_mov_b32 v193,0\n"
"v_mov_b32 v194,0\n"
"v_mov_b32 v195,0\n"
"v_mov_b32 v196,0\n"
"v_mov_b32 v197,0\n"
"v_mov_b32 v198,0\n"
"v_mov_b32 v199,0\n"
"v_mov_b32 v200,0\n"
"v_mov_b32 v201,0\n"
"v_mov_b32 v202,0\n"
"v_mov_b32 v203,0\n"
"v_mov_b32 v204,0\n"
"v_mov_b32 v205,0\n"
"v_mov_b32 v206,0\n"
"v_mov_b32 v207,0\n"
"v_accvgpr_write_b32 a144,0\n"
"v_accvgpr_write_b32 a145,0\n"
"v_accvgpr_write_b32 a146,0\n"
"v_accvgpr_write_b32 a147,0\n"
"L1076_%=:\n"
"s_lshl_b32 s91,s88,13\n"
SA"s91,s91,s39\n"
SA"s91,s91,0\n"
BL"a[148:151],v224,s[8:11],s91 offen offset:0 sc1\n"
BL"a[152:155],v224,s[8:11],s91 offen offset:1024 sc1\n"
BL"a[156:159],v224,s[8:11],s91 offen offset:2048 sc1\n"
BL"a[160:163],v224,s[8:11],s91 offen offset:3072 sc1\n"
SA"s91,s91,4096\n"
BL"a[164:167],v224,s[8:11],s91 offen offset:0 sc1\n"
BL"a[168:171],v224,s[8:11],s91 offen offset:1024 sc1\n"
BL"a[172:175],v224,s[8:11],s91 offen offset:2048 sc1\n"
BL"a[176:179],v224,s[8:11],s91 offen offset:3072 sc1\n"
WT"vmcnt(0)\n"
MF"v[240:243],a[148:151],a[48:51],0\n"
MF"v[240:243],a[152:155],a[52:55],v[240:243]\n"
MF"v[240:243],a[156:159],a[56:59],v[240:243]\n"
MF"v[240:243],a[160:163],a[60:63],v[240:243]\n"
MF"v[244:247],a[164:167],a[48:51],0\n"
MF"v[244:247],a[168:171],a[52:55],v[244:247]\n"
MF"v[244:247],a[172:175],a[56:59],v[244:247]\n"
MF"v[244:247],a[176:179],a[60:63],v[244:247]\n"
"s_lshl_b32 s91,s88,5\n"
"v_subrev_u32 v250,s91,v249\n"
"s_nop 7\n"
"v_cmp_gt_i32 vcc,0,v250\n"
"s_nop 1\n"
"v_cndmask_b32 v240,v240,v255,vcc\n"
"v_cmp_gt_i32 vcc,1,v250\n"
"s_nop 1\n"
"v_cndmask_b32 v241,v241,v255,vcc\n"
"v_cmp_gt_i32 vcc,2,v250\n"
"s_nop 1\n"
"v_cndmask_b32 v242,v242,v255,vcc\n"
"v_cmp_gt_i32 vcc,3,v250\n"
"s_nop 1\n"
"v_cndmask_b32 v243,v243,v255,vcc\n"
"v_cmp_gt_i32 vcc,16,v250\n"
"s_nop 1\n"
"v_cndmask_b32 v244,v244,v255,vcc\n"
"v_cmp_gt_i32 vcc,17,v250\n"
"s_nop 1\n"
"v_cndmask_b32 v245,v245,v255,vcc\n"
"v_cmp_gt_i32 vcc,18,v250\n"
"s_nop 1\n"
"v_cndmask_b32 v246,v246,v255,vcc\n"
"v_cmp_gt_i32 vcc,19,v250\n"
"s_nop 1\n"
"v_cndmask_b32 v247,v247,v255,vcc\n"
"s_cmp_lg_u32 s90,0\n"
"s_cbranch_scc1 L1077_%=\n"
"v_max3_f32 v248,v248,v240,v241\n"
"v_max3_f32 v248,v248,v242,v243\n"
"v_max3_f32 v248,v248,v244,v245\n"
"v_max3_f32 v248,v248,v246,v247\n"
"s_branch L1078_%=\n"
"L1077_%=:\n"
"s_lshl_b32 s91,s88,13\n"
SA"s91,s91,s39\n"
SA"s91,s91,524288\n"
BL"a[148:151],v224,s[8:11],s91 offen offset:0 sc1\n"
BL"a[152:155],v224,s[8:11],s91 offen offset:1024 sc1\n"
BL"a[156:159],v224,s[8:11],s91 offen offset:2048 sc1\n"
BL"a[160:163],v224,s[8:11],s91 offen offset:3072 sc1\n"
SA"s91,s91,4096\n"
BL"a[164:167],v224,s[8:11],s91 offen offset:0 sc1\n"
BL"a[168:171],v224,s[8:11],s91 offen offset:1024 sc1\n"
BL"a[172:175],v224,s[8:11],s91 offen offset:2048 sc1\n"
BL"a[176:179],v224,s[8:11],s91 offen offset:3072 sc1\n"
WT"vmcnt(0)\n"
"v_sub_f32 v240,v240,v248\n"
"v_sub_f32 v241,v241,v248\n"
"v_sub_f32 v242,v242,v248\n"
"v_sub_f32 v243,v243,v248\n"
"v_sub_f32 v244,v244,v248\n"
"v_sub_f32 v245,v245,v248\n"
"v_sub_f32 v246,v246,v248\n"
"v_sub_f32 v247,v247,v248\n"
EX"v240,v240\n"
EX"v241,v241\n"
EX"v242,v242\n"
EX"v243,v243\n"
EX"v244,v244\n"
EX"v245,v245\n"
EX"v246,v246\n"
EX"v247,v247\n"
"s_nop 0\n"
PK"v240,v240,v241\n"
PK"v241,v242,v243\n"
PK"v242,v244,v245\n"
PK"v243,v246,v247\n"
"s_nop 1\n"
MF"v[176:179],a[148:151],v[240:243],v[176:179]\n"
MF"v[180:183],a[152:155],v[240:243],v[180:183]\n"
MF"v[184:187],a[156:159],v[240:243],v[184:187]\n"
MF"v[188:191],a[160:163],v[240:243],v[188:191]\n"
MF"v[192:195],a[164:167],v[240:243],v[192:195]\n"
MF"v[196:199],a[168:171],v[240:243],v[196:199]\n"
MF"v[200:203],a[172:175],v[240:243],v[200:203]\n"
MF"v[204:207],a[176:179],v[240:243],v[204:207]\n"
MF"a[144:147],a[128:131],v[240:243],a[144:147]\n"
"s_nop 7\n"
"L1078_%=:\n"
SA"s88,s88,1\n"
"s_cmp_le_u32 s88,s89\n"
"s_cbranch_scc1 L1076_%=\n"
"s_cmp_lg_u32 s90,0\n"
"s_cbranch_scc1 L1079_%=\n"
"v_mov_b32 v254,v248\n"
"s_nop 1\n"
"v_permlane16_swap_b32 v254,v248\n"
"s_nop 1\n"
"v_max_f32 v248,v248,v254\n"
"v_mov_b32 v254,v248\n"
"s_nop 1\n"
"v_permlane32_swap_b32 v254,v248\n"
"s_nop 1\n"
"v_max_f32 v248,v248,v254\n"
"s_mov_b32 s90,1\n"
"s_mov_b32 s88,0\n"
"s_branch L1076_%=\n"
"L1079_%=:\n"
"s_nop 7\n"
"v_accvgpr_read_b32 v252,a144\n"
"s_nop 1\n"
"s_branch L1074_%=\n"
"L1080_%=:\n"
"s_mov_b32 s54,0\n"
"L1082_%=:\n"
"s_sleep 2\n"
"buffer_load_dwordx2 v[240:241],v238,s[8:11],s63 offen sc1\n"
WT"vmcnt(0)\n"
SA"s54,s54,1\n"
"v_cmp_eq_u64 vcc,s[28:29],v[240:241]\n"
"s_cmp_eq_u64 vcc,exec\n"
"s_cbranch_scc1 L1081_%=\n"
"s_cmp_lt_u32 s54,1048576\n"
"s_cbranch_scc1 L1082_%=\n"
"s_branch L1081_%=\n"
"L1083_%=:\n"
"s_mov_b32 s54,0\n"
"L1085_%=:\n"
"s_sleep 2\n"
"buffer_load_dwordx2 v[240:241],v238,s[8:11],s63 offen sc1\n"
WT"vmcnt(0)\n"
SA"s54,s54,1\n"
"v_cmp_eq_u64 vcc,s[28:29],v[240:241]\n"
"s_cmp_eq_u64 vcc,exec\n"
"s_cbranch_scc1 L1084_%=\n"
"s_cmp_lt_u32 s54,1048576\n"
"s_cbranch_scc1 L1085_%=\n"
"s_branch L1084_%=\n"
"L1086_%=:\n"
"s_mov_b32 s54,0\n"
"L1088_%=:\n"
"s_sleep 2\n"
"buffer_load_dwordx2 v[240:241],v244,s[8:11],s92 offen sc1\n"
WT"vmcnt(0)\n"
SA"s54,s54,1\n"
"v_cmp_eq_u64 vcc,s[28:29],v[240:241]\n"
"s_cmp_eq_u64 vcc,exec\n"
"s_cbranch_scc1 L1087_%=\n"
"s_cmp_lt_u32 s54,1048576\n"
"s_cbranch_scc1 L1088_%=\n"
"s_branch L1087_%=\n"
"LTH_%=:\n"
RD"a[92:95],v227 offset:7168\n"
MF"v[0:3],a[64:67],v[80:83],0\n"
WT"lgkmcnt(4)\n"
"v_accvgpr_read_b32 v251,a140\n"
"v_subrev_u32 v253,0x2b800000,v251\n"
MF"v[0:3],a[68:71],v[88:91],v[0:3]\n"
RD"a[96:99],v227 offset:8192\n"
MF"v[40:43],a[68:71],v[120:123],0\n"
"v_cmp_le_u32 vcc,0x34000000,v253\n"
"s_cmp_lg_u64 vcc,0\n"
"s_cbranch_scc1 L1069_%=\n"
"L1068_%=:\n"
MF"v[40:43],a[64:67],v[112:115],v[40:43]\n"
"v_rcp_f32 v251,v251\n"
MF"v[4:7],a[72:75],v[80:83],0\n"
SA"s41,s41,32768\n"
RD"a[100:103],v227 offset:9216\n"
MU"v144,v251,v144\n"
MF"v[4:7],a[76:79],v[88:91],v[4:7]\n"
"s_cmp_eq_u32 s41,163840\n"
RD"a[104:107],v227 offset:10240\n"
MU"v145,v251,v145\n"
MF"v[44:47],a[76:79],v[120:123],0\n"
"s_cselect_b32 s41,0,s41\n"
MU"v146,v251,v146\n"
"v_add_u32 v226,s41,v224\n"
MF"v[44:47],a[72:75],v[112:115],v[44:47]\n"
WT"lgkmcnt(3)\n"
RD"a[108:111],v227 offset:11264\n"
MU"v147,v251,v147\n"
MF"v[8:11],a[80:83],v[80:83],0\n"
SA"s41,s41,32768\n"
MU"v148,v251,v148\n"
MU"v149,v251,v149\n"
MF"v[8:11],a[84:87],v[88:91],v[8:11]\n"
"s_cmp_eq_u32 s41,163840\n"
RD"a[112:115],v227 offset:12288\n"
MU"v150,v251,v150\n"
MU"v151,v251,v151\n"
MF"v[48:51],a[84:87],v[120:123],0\n"
"s_cselect_b32 s41,0,s41\n"
PK"v144,v144,v145\n"
"v_add_u32 v235,s41,v224\n"
PK"v145,v146,v147\n"
MF"v[48:51],a[80:83],v[112:115],v[48:51]\n"
RD"a[116:119],v227 offset:13312\n"
PK"v146,v148,v149\n"
MF"v[12:15],a[88:91],v[80:83],0\n"
SA"s41,s41,32768\n"
PK"v147,v150,v151\n"
MU"v152,v251,v152\n"
MF"v[12:15],a[92:95],v[88:91],v[12:15]\n"
"s_cmp_eq_u32 s41,163840\n"
BS"v[144:147],v224,s[8:11],s60 offen offset:0 sc1\n"
MU"v153,v251,v153\n"
MF"v[52:55],a[92:95],v[120:123],0\n"
"s_cselect_b32 s41,0,s41\n"
RD"a[120:123],v227 offset:14336\n"
"v_add_u32 v236,s41,v224\n"
MU"v154,v251,v154\n"
MF"v[52:55],a[88:91],v[112:115],v[52:55]\n"
WT"lgkmcnt(3)\n"
RD"a[124:127],v227 offset:15360\n"
MU"v155,v251,v155\n"
MU"v156,v251,v156\n"
MF"v[16:19],a[96:99],v[80:83],0\n"
SA"s41,s41,32768\n"
MU"v157,v251,v157\n"
MU"v158,v251,v158\n"
MU"v159,v251,v159\n"
MF"v[16:19],a[100:103],v[88:91],v[16:19]\n"
"s_cmp_eq_u32 s41,163840\n"
RD"a[64:67],v227 offset:16384\n"
PK"v152,v152,v153\n"
PK"v153,v154,v155\n"
MF"v[56:59],a[100:103],v[120:123],0\n"
"s_cselect_b32 s41,0,s41\n"
PK"v154,v156,v157\n"
"v_add_u32 v237,s41,v224\n"
PK"v155,v158,v159\n"
MF"v[56:59],a[96:99],v[112:115],v[56:59]\n"
BS"v[152:155],v224,s[8:11],s60 offen offset:1024 sc1\n"
MU"v160,v251,v160\n"
MF"v[20:23],a[104:107],v[80:83],0\n"
RD"a[68:71],v227 offset:17408\n"
MU"v161,v251,v161\n"
MU"v162,v251,v162\n"
MF"v[20:23],a[108:111],v[88:91],v[20:23]\n"
WT"vmcnt(2)\n"
"buffer_store_dwordx2 v[232:233],v230,s[8:11],s61 offen sc1\n"
MU"v163,v251,v163\n"
MF"v[60:63],a[108:111],v[120:123],0\n"
RD"a[72:75],v227 offset:18432\n"
MU"v164,v251,v164\n"
MU"v165,v251,v165\n"
MF"v[60:63],a[104:107],v[112:115],v[60:63]\n"
WT"lgkmcnt(3)\n"
RD"a[76:79],v227 offset:19456\n"
MU"v166,v251,v166\n"
MU"v167,v251,v167\n"
MF"v[24:27],a[112:115],v[80:83],0\n"
SA"s51,s60,16384\n"
"v_accvgpr_read_b32 v252,a144\n"
"v_subrev_u32 v253,0x2b800000,v252\n"
PK"v160,v160,v161\n"
MF"v[24:27],a[116:119],v[88:91],v[24:27]\n"
"s_lshl_b32 s62,s34,14\n"
RD"a[80:83],v227 offset:20480\n"
PK"v161,v162,v163\n"
PK"v162,v164,v165\n"
MF"v[64:67],a[116:119],v[120:123],0\n"
"s_sub_u32 s62,s61,s62\n"
"v_cmp_le_u32 vcc,0x34000000,v253\n"
"s_cmp_lg_u64 vcc,0\n"
"s_cbranch_scc1 L1075_%=\n"
"L1074_%=:\n"
MF"v[64:67],a[112:115],v[112:115],v[64:67]\n"
SA"s62,s62,122880\n"
"v_rcp_f32 v252,v252\n"
PK"v163,v166,v167\n"
MF"v[28:31],a[120:123],v[80:83],0\n"
BS"v[160:163],v224,s[8:11],s60 offen offset:2048 sc1\n"
MU"v168,v251,v168\n"
MF"v[28:31],a[124:127],v[88:91],v[28:31]\n"
RD"a[84:87],v227 offset:21504\n"
RD"a[88:91],v227 offset:22528\n"
MU"v169,v251,v169\n"
MF"v[68:71],a[124:127],v[120:123],0\n"
MU"v170,v251,v170\n"
MU"v171,v251,v171\n"
MU"v172,v251,v172\n"
MF"v[68:71],a[120:123],v[112:115],v[68:71]\n"
WT"lgkmcnt(3)\n"
RD"a[92:95],v227 offset:23552\n"
MU"v173,v251,v173\n"
MU"v174,v251,v174\n"
MF"v[32:35],a[64:67],v[80:83],0\n"
MU"v175,v251,v175\n"
PK"v168,v168,v169\n"
PK"v169,v170,v171\n"
MF"v[32:35],a[68:71],v[88:91],v[32:35]\n"
RD"a[96:99],v227 offset:24576\n"
MU"v176,v252,v176\n"
MU"v177,v252,v177\n"
MF"v[72:75],a[68:71],v[120:123],0\n"
PK"v170,v172,v173\n"
PK"v171,v174,v175\n"
MU"v178,v252,v178\n"
MF"v[72:75],a[64:67],v[112:115],v[72:75]\n"
BS"v[168:171],v224,s[8:11],s60 offen offset:3072 sc1\n"
MU"v179,v252,v179\n"
MF"v[36:39],a[72:75],v[80:83],0\n"
RD"a[100:103],v227 offset:25600\n"
MU"v180,v252,v180\n"
MU"v181,v252,v181\n"
MF"v[36:39],a[76:79],v[88:91],v[36:39]\n"
RD"a[104:107],v227 offset:26624\n"
MU"v182,v252,v182\n"
MU"v183,v252,v183\n"
MF"v[76:79],a[76:79],v[120:123],0\n"
PK"v176,v176,v177\n"
PK"v177,v178,v179\n"
PK"v178,v180,v181\n"
MF"v[76:79],a[72:75],v[112:115],v[76:79]\n"
WT"lgkmcnt(3)\n"
RD"a[108:111],v227 offset:27648\n"
PK"v179,v182,v183\n"
MU"v184,v252,v184\n"
MF"v[0:3],a[80:83],v[96:99],v[0:3]\n"
BS"v[176:179],v224,s[8:11],s51 offen offset:0 sc1\n"
MU"v185,v252,v185\n"
MF"v[0:3],a[84:87],v[104:107],v[0:3]\n"
RD"a[112:115],v227 offset:28672\n"
MU"v186,v252,v186\n"
MU"v187,v252,v187\n"
MF"v[40:43],a[84:87],v[136:139],v[40:43]\n"
MU"v188,v252,v188\n"
MU"v189,v252,v189\n"
MU"v190,v252,v190\n"
MF"v[40:43],a[80:83],v[128:131],v[40:43]\n"
RD"a[116:119],v227 offset:29696\n"
MU"v191,v252,v191\n"
PK"v184,v184,v185\n"
MF"v[4:7],a[88:91],v[96:99],v[4:7]\n"
PK"v185,v186,v187\n"
PK"v186,v188,v189\n"
PK"v187,v190,v191\n"
MF"v[4:7],a[92:95],v[104:107],v[4:7]\n"
BS"v[184:187],v224,s[8:11],s51 offen offset:1024 sc1\n"
MU"v192,v252,v192\n"
MF"v[44:47],a[92:95],v[136:139],v[44:47]\n"
RD"a[120:123],v227 offset:30720\n"
MU"v193,v252,v193\n"
MU"v194,v252,v194\n"
MF"v[44:47],a[88:91],v[128:131],v[44:47]\n"
WT"lgkmcnt(3)\n"
RD"a[124:127],v227 offset:31744\n"
"v_bfe_u32 v238,v224,4,2\n"
MU"v195,v252,v195\n"
MF"v[8:11],a[96:99],v[96:99],v[8:11]\n"
"s_andn2_b32 s63,s61,255\n"
"s_barrier\n"
MF"v[8:11],a[100:103],v[104:107],v[8:11]\n"
RD"a[64:67],v226 offset:0\n"
"v_lshlrev_b32 v238,6,v238\n"
MU"v196,v252,v196\n"
MF"v[48:51],a[100:103],v[136:139],v[48:51]\n"
"buffer_load_dwordx2 v[240:241],v238,s[8:11],s63 offen sc1\n"
MU"v197,v252,v197\n"
MF"v[48:51],a[96:99],v[128:131],v[48:51]\n"
RD"a[68:71],v226 offset:1024\n"
MU"v198,v252,v198\n"
MU"v199,v252,v199\n"
MF"v[12:15],a[104:107],v[96:99],v[12:15]\n"
PK"v192,v192,v193\n"
PK"v193,v194,v195\n"
PK"v194,v196,v197\n"
MF"v[12:15],a[108:111],v[104:107],v[12:15]\n"
RD"a[72:75],v226 offset:2048\n"
PK"v195,v198,v199\n"
MU"v200,v252,v200\n"
MF"v[52:55],a[108:111],v[136:139],v[52:55]\n"
BS"v[192:195],v224,s[8:11],s51 offen offset:2048 sc1\n"
MU"v201,v252,v201\n"
MF"v[52:55],a[104:107],v[128:131],v[52:55]\n"
WT"lgkmcnt(3)\n"
RD"a[76:79],v226 offset:3072\n"
MU"v202,v252,v202\n"
MU"v203,v252,v203\n"
MF"v[16:19],a[112:115],v[96:99],v[16:19]\n"
MU"v204,v252,v204\n"
MU"v205,v252,v205\n"
MU"v206,v252,v206\n"
MF"v[16:19],a[116:119],v[104:107],v[16:19]\n"
RD"a[80:83],v226 offset:4096\n"
MU"v207,v252,v207\n"
PK"v200,v200,v201\n"
MF"v[56:59],a[116:119],v[136:139],v[56:59]\n"
"v_readlane_b32 s43,v228,s42\n"
PK"v201,v202,v203\n"
PK"v202,v204,v205\n"
MF"v[56:59],a[112:115],v[128:131],v[56:59]\n"
RD"a[84:87],v226 offset:5120\n"
PK"v203,v206,v207\n"
MF"v[20:23],a[120:123],v[96:99],v[20:23]\n"
BS"v[200:203],v224,s[8:11],s51 offen offset:3072 sc1\n"
MF"v[20:23],a[124:127],v[104:107],v[20:23]\n"
RD"a[88:91],v226 offset:6144\n"
MF"v[60:63],a[124:127],v[136:139],v[60:63]\n"
"s_mov_b32 m0,s40\n"
MF"v[60:63],a[120:123],v[128:131],v[60:63]\n"
WT"lgkmcnt(3)\n"
RD"a[92:95],v226 offset:7168\n"
MF"v[24:27],a[64:67],v[96:99],v[24:27]\n"
"v_readlane_b32 s44,v229,s42\n"
MF"v[24:27],a[68:71],v[104:107],v[24:27]\n"
RD"a[96:99],v227 offset:0\n"
MF"v[64:67],a[68:71],v[136:139],v[64:67]\n"
SA"s42,s42,1\n"
BL"v225,s[8:11],s43 offen offset:0 sc1 lds\n"
MF"v[64:67],a[64:67],v[128:131],v[64:67]\n"
RD"a[100:103],v227 offset:1024\n"
MF"v[28:31],a[72:75],v[96:99],v[28:31]\n"
MF"v[28:31],a[76:79],v[104:107],v[28:31]\n"
RD"a[104:107],v227 offset:2048\n"
MF"v[68:71],a[76:79],v[136:139],v[68:71]\n"
MF"v[68:71],a[72:75],v[128:131],v[68:71]\n"
WT"lgkmcnt(3)\n"
RD"a[108:111],v227 offset:3072\n"
MF"v[32:35],a[80:83],v[96:99],v[32:35]\n"
BL"v225,s[8:11],s43 offen offset:1024 sc1 lds\n"
MF"v[32:35],a[84:87],v[104:107],v[32:35]\n"
RD"a[112:115],v227 offset:4096\n"
MF"v[72:75],a[84:87],v[136:139],v[72:75]\n"
MF"v[72:75],a[80:83],v[128:131],v[72:75]\n"
RD"a[116:119],v227 offset:5120\n"
MF"v[36:39],a[88:91],v[96:99],v[36:39]\n"
MF"v[36:39],a[92:95],v[104:107],v[36:39]\n"
RD"a[120:123],v227 offset:6144\n"
MF"v[76:79],a[92:95],v[136:139],v[76:79]\n"
BL"v225,s[8:11],s43 offen offset:2048 sc1 lds\n"
MF"v[76:79],a[88:91],v[128:131],v[76:79]\n"
WT"lgkmcnt(3)\n"
RD"a[124:127],v227 offset:7168\n"
MF"v[84:87],a[96:99],v[144:147],0\n"
MF"v[84:87],a[100:103],v[152:155],v[84:87]\n"
RD"a[64:67],v227 offset:8192\n"
MF"v[164:167],a[100:103],v[184:187],0\n"
MF"v[164:167],a[96:99],v[176:179],v[164:167]\n"
RD"a[68:71],v227 offset:9216\n"
MF"v[92:95],a[104:107],v[144:147],0\n"
BL"v225,s[8:11],s43 offen offset:3072 sc1 lds\n"
MF"v[92:95],a[108:111],v[152:155],v[92:95]\n"
RD"a[72:75],v227 offset:10240\n"
MF"v[172:175],a[108:111],v[184:187],0\n"
SA"m0,s40,16384\n"
MF"v[172:175],a[104:107],v[176:179],v[172:175]\n"
WT"lgkmcnt(3)\n"
RD"a[76:79],v227 offset:11264\n"
MF"v[100:103],a[112:115],v[144:147],0\n"
MF"v[100:103],a[116:119],v[152:155],v[100:103]\n"
SA"s48,s59,32768\n"
RD"a[80:83],v227 offset:12288\n"
MF"v[180:183],a[116:119],v[184:187],0\n"
"s_and_b32 s48,s48,0x18000\n"
BL"v225,s[8:11],s44 offen offset:0 sc1 lds\n"
MF"v[180:183],a[112:115],v[176:179],v[180:183]\n"
SA"s40,s40,32768\n"
RD"a[84:87],v227 offset:13312\n"
"s_andn2_b32 s49,s59,0x18000\n"
MF"v[108:111],a[120:123],v[144:147],0\n"
"s_cmp_eq_u32 s40,s46\n"
"s_cselect_b32 s40,s47,s40\n"
WT"vmcnt(7)\n"
"v_cmp_eq_u64 vcc,s[28:29],v[240:241]\n"
"s_cmp_eq_u64 vcc,exec\n"
"s_cbranch_scc0 L1080_%=\n"
"L1081_%=:\n"
MF"v[108:111],a[124:127],v[152:155],v[108:111]\n"
"s_or_b32 s52,s48,s49\n"
RD"a[88:91],v227 offset:14336\n"
SA"s53,s52,16384\n"
MF"v[188:191],a[124:127],v[184:187],0\n"
BL"a[0:3],v224,s[8:11],s52 offen offset:0 sc1\n"
MF"v[188:191],a[120:123],v[176:179],v[188:191]\n"
WT"lgkmcnt(3)\n"
RD"a[92:95],v227 offset:15360\n"
MF"v[116:119],a[64:67],v[144:147],0\n"
BL"v225,s[8:11],s44 offen offset:1024 sc1 lds\n"
MF"v[116:119],a[68:71],v[152:155],v[116:119]\n"
RD"a[96:99],v227 offset:16384\n"
MF"v[196:199],a[68:71],v[184:187],0\n"
BL"a[4:7],v224,s[8:11],s53 offen offset:0 sc1\n"
MF"v[196:199],a[64:67],v[176:179],v[196:199]\n"
RD"a[100:103],v227 offset:17408\n"
MF"v[124:127],a[72:75],v[144:147],0\n"
MF"v[124:127],a[76:79],v[152:155],v[124:127]\n"
RD"a[104:107],v227 offset:18432\n"
MF"v[204:207],a[76:79],v[184:187],0\n"
BL"v225,s[8:11],s44 offen offset:2048 sc1 lds\n"
MF"v[204:207],a[72:75],v[176:179],v[204:207]\n"
WT"lgkmcnt(3)\n"
RD"a[108:111],v227 offset:19456\n"
MF"v[132:135],a[80:83],v[144:147],0\n"
BL"a[8:11],v224,s[8:11],s52 offen offset:1024 sc1\n"
MF"v[132:135],a[84:87],v[152:155],v[132:135]\n"
RD"a[112:115],v227 offset:20480\n"
MF"v[208:211],a[84:87],v[184:187],0\n"
"v_readlane_b32 s43,v228,s42\n"
MF"v[208:211],a[80:83],v[176:179],v[208:211]\n"
RD"a[116:119],v227 offset:21504\n"
MF"v[140:143],a[88:91],v[144:147],0\n"
BL"a[12:15],v224,s[8:11],s53 offen offset:1024 sc1\n"
MF"v[140:143],a[92:95],v[152:155],v[140:143]\n"
BL"v225,s[8:11],s44 offen offset:3072 sc1 lds\n"
MF"v[212:215],a[92:95],v[184:187],0\n"
"s_mov_b32 m0,s40\n"
RD"a[120:123],v227 offset:22528\n"
MF"v[212:215],a[88:91],v[176:179],v[212:215]\n"
WT"lgkmcnt(3)\n"
RD"a[124:127],v227 offset:23552\n"
MF"v[148:151],a[96:99],v[144:147],0\n"
WT"vmcnt(12)\n"
"buffer_store_dwordx2 v[232:233],v230,s[8:11],s62 offen sc1\n"
MF"v[148:151],a[100:103],v[152:155],v[148:151]\n"
BL"a[16:19],v224,s[8:11],s52 offen offset:2048 sc1\n"
MF"v[216:219],a[100:103],v[184:187],0\n"
RD"a[64:67],v227 offset:24576\n"
"v_readlane_b32 s44,v229,s42\n"
MF"v[216:219],a[96:99],v[176:179],v[216:219]\n"
SA"s42,s42,1\n"
BL"v225,s[8:11],s43 offen offset:0 sc1 lds\n"
MF"v[156:159],a[104:107],v[144:147],0\n"
RD"a[68:71],v227 offset:25600\n"
MF"v[156:159],a[108:111],v[152:155],v[156:159]\n"
BL"a[20:23],v224,s[8:11],s53 offen offset:2048 sc1\n"
MF"v[220:223],a[108:111],v[184:187],0\n"
RD"a[72:75],v227 offset:26624\n"
MF"v[220:223],a[104:107],v[176:179],v[220:223]\n"
WT"lgkmcnt(3)\n"
RD"a[76:79],v227 offset:27648\n"
MF"v[84:87],a[112:115],v[160:163],v[84:87]\n"
"s_lshr_b32 s89,s31,3\n"
BL"v225,s[8:11],s43 offen offset:1024 sc1 lds\n"
MF"v[84:87],a[116:119],v[168:171],v[84:87]\n"
"s_lshl_b32 s89,s89,4\n"
RD"a[80:83],v227 offset:28672\n"
MF"v[164:167],a[116:119],v[200:203],v[164:167]\n"
SA"s89,s89,s30\n"
BL"a[24:27],v224,s[8:11],s52 offen offset:3072 sc1\n"
MF"v[164:167],a[112:115],v[192:195],v[164:167]\n"
"s_lshl_b32 s89,s89,3\n"
RD"a[84:87],v227 offset:29696\n"
MF"v[92:95],a[120:123],v[160:163],v[92:95]\n"
"s_xor_b32 s23,s32,s30\n"
MF"v[92:95],a[124:127],v[168:171],v[92:95]\n"
SA"s48,s59,65536\n"
RD"a[88:91],v227 offset:30720\n"
SA"s89,s89,s23\n"
MF"v[172:175],a[124:127],v[200:203],v[172:175]\n"
"s_and_b32 s48,s48,0x18000\n"
BL"a[28:31],v224,s[8:11],s53 offen offset:3072 sc1\n"
MF"v[172:175],a[120:123],v[192:195],v[172:175]\n"
WT"lgkmcnt(3)\n"
BL"v225,s[8:11],s43 offen offset:2048 sc1 lds\n"
MF"v[100:103],a[64:67],v[160:163],v[100:103]\n"
"s_xor_b32 s90,s89,36\n"
RD"a[92:95],v227 offset:31744\n"
"s_andn2_b32 s49,s59,0x18000\n"
MF"v[100:103],a[68:71],v[168:171],v[100:103]\n"
"s_mul_i32 s88,s89,5120\n"
RD"a[96:99],v226 offset:0\n"
"s_or_b32 s52,s48,s49\n"
MF"v[180:183],a[68:71],v[200:203],v[180:183]\n"
SA"s53,s52,16384\n"
BL"a[180:183],v224,s[8:11],s52 offen offset:0 sc1\n"
MF"v[180:183],a[64:67],v[192:195],v[180:183]\n"
"s_mul_i32 s22,s90,5120\n"
RD"a[100:103],v226 offset:1024\n"
"s_sub_u32 s90,s90,s89\n"
MF"v[108:111],a[72:75],v[160:163],v[108:111]\n"
"s_lshl_b32 s90,s90,6\n"
BL"v225,s[8:11],s43 offen offset:3072 sc1 lds\n"
MF"v[108:111],a[76:79],v[168:171],v[108:111]\n"
SA"s88,s88,59506688\n"
RD"a[104:107],v226 offset:2048\n"
SA"s22,s22,59506688\n"
MF"v[188:191],a[76:79],v[200:203],v[188:191]\n"
"s_cmp_eq_u32 s23,0\n"
BL"a[184:187],v224,s[8:11],s53 offen offset:0 sc1\n"
MF"v[188:191],a[72:75],v[192:195],v[188:191]\n"
WT"lgkmcnt(3)\n"
RD"a[108:111],v226 offset:3072\n"
"s_cselect_b32 s88,2130706432,s88\n"
MF"v[116:119],a[80:83],v[160:163],v[116:119]\n"
SA"m0,s40,16384\n"
"s_cmp_eq_u32 s23,4\n"
"s_cselect_b32 s22,2130706432,s22\n"
MF"v[116:119],a[84:87],v[168:171],v[116:119]\n"
"s_lshl_b32 s89,s89,6\n"
RD"a[112:115],v226 offset:4096\n"
"v_bfe_u32 v239,v224,1,6\n"
MF"v[196:199],a[84:87],v[200:203],v[196:199]\n"
SA"s89,s89,80478208\n"
BL"v225,s[8:11],s44 offen offset:0 sc1 lds\n"
MF"v[196:199],a[80:83],v[192:195],v[196:199]\n"
SA"s40,s40,32768\n"
RD"a[116:119],v226 offset:5120\n"
"v_bfe_u32 v242,v224,7,1\n"
MF"v[124:127],a[88:91],v[160:163],v[124:127]\n"
"s_cmp_eq_u32 s40,s46\n"
BL"a[188:191],v224,s[8:11],s52 offen offset:1024 sc1\n"
MF"v[124:127],a[92:95],v[168:171],v[124:127]\n"
"s_cselect_b32 s40,s47,s40\n"
RD"a[120:123],v226 offset:6144\n"
"v_mad_u32_u24 v239,v242,s90,v239\n"
MF"v[204:207],a[92:95],v[200:203],v[204:207]\n"
"v_lshrrev_b32 v242,8,v224\n"
"v_lshl_add_u32 v239,v242,27,v239\n"
MF"v[204:207],a[88:91],v[192:195],v[204:207]\n"
WT"lgkmcnt(3)\n"
RD"a[124:127],v226 offset:7168\n"
MF"v[132:135],a[96:99],v[160:163],v[132:135]\n"
BL"a[192:195],v224,s[8:11],s53 offen offset:1024 sc1\n"
MF"v[132:135],a[100:103],v[168:171],v[132:135]\n"
BL"v225,s[8:11],s44 offen offset:1024 sc1 lds\n"
MF"v[208:211],a[100:103],v[200:203],v[208:211]\n"
RD"a[64:67],v226 offset:8192\n"
MF"v[208:211],a[96:99],v[192:195],v[208:211]\n"
RD"a[68:71],v226 offset:9216\n"
MF"v[140:143],a[104:107],v[160:163],v[140:143]\n"
BL"a[196:199],v224,s[8:11],s52 offen offset:2048 sc1\n"
MF"v[140:143],a[108:111],v[168:171],v[140:143]\n"
RD"a[72:75],v226 offset:10240\n"
MF"v[212:215],a[108:111],v[200:203],v[212:215]\n"
BL"v225,s[8:11],s44 offen offset:2048 sc1 lds\n"
MF"v[212:215],a[104:107],v[192:195],v[212:215]\n"
WT"lgkmcnt(3)\n"
RD"a[76:79],v226 offset:11264\n"
MF"v[148:151],a[112:115],v[160:163],v[148:151]\n"
BL"a[200:203],v224,s[8:11],s53 offen offset:2048 sc1\n"
MF"v[148:151],a[116:119],v[168:171],v[148:151]\n"
RD"a[80:83],v226 offset:12288\n"
MF"v[216:219],a[116:119],v[200:203],v[216:219]\n"
MF"v[216:219],a[112:115],v[192:195],v[216:219]\n"
RD"a[84:87],v226 offset:13312\n"
MF"v[156:159],a[120:123],v[160:163],v[156:159]\n"
BL"v225,s[8:11],s44 offen offset:3072 sc1 lds\n"
MF"v[156:159],a[124:127],v[168:171],v[156:159]\n"
RD"a[88:91],v226 offset:14336\n"
MF"v[220:223],a[124:127],v[200:203],v[220:223]\n"
BL"a[204:207],v224,s[8:11],s52 offen offset:3072 sc1\n"
MF"v[220:223],a[120:123],v[192:195],v[220:223]\n"
WT"vmcnt(26) lgkmcnt(3)\n"
RD"a[92:95],v226 offset:15360\n"
MF"v[0:3],a[64:67],a[0:3],v[0:3]\n"
WT"vmcnt(22)\n"
MF"v[0:3],a[68:71],a[8:11],v[0:3]\n"
WT"vmcnt(21)\n"
RD"a[96:99],v226 offset:16384\n"
SA"s48,s59,98304\n"
MF"v[40:43],a[68:71],a[12:15],v[40:43]\n"
"s_and_b32 s48,s48,0x18000\n"
BL"a[208:211],v224,s[8:11],s53 offen offset:3072 sc1\n"
MF"v[40:43],a[64:67],a[4:7],v[40:43]\n"
"s_andn2_b32 s49,s59,0x18000\n"
RD"a[100:103],v226 offset:17408\n"
MF"v[4:7],a[72:75],a[0:3],v[4:7]\n"
"s_or_b32 s52,s48,s49\n"
SA"s53,s52,16384\n"
MF"v[4:7],a[76:79],a[8:11],v[4:7]\n"
RD"a[104:107],v226 offset:18432\n"
MF"v[44:47],a[76:79],a[12:15],v[44:47]\n"
BL"a[212:215],v224,s[8:11],s52 offen offset:0 sc1\n"
MF"v[44:47],a[72:75],a[4:7],v[44:47]\n"
WT"lgkmcnt(3)\n"
RD"a[108:111],v226 offset:19456\n"
MF"v[8:11],a[80:83],a[0:3],v[8:11]\n"
MF"v[8:11],a[84:87],a[8:11],v[8:11]\n"
RD"a[112:115],v226 offset:20480\n"
MF"v[48:51],a[84:87],a[12:15],v[48:51]\n"
BL"a[216:219],v224,s[8:11],s53 offen offset:0 sc1\n"
MF"v[48:51],a[80:83],a[4:7],v[48:51]\n"
RD"a[116:119],v226 offset:21504\n"
MF"v[12:15],a[88:91],a[0:3],v[12:15]\n"
MF"v[12:15],a[92:95],a[8:11],v[12:15]\n"
RD"a[120:123],v226 offset:22528\n"
MF"v[52:55],a[92:95],a[12:15],v[52:55]\n"
MF"v[52:55],a[88:91],a[4:7],v[52:55]\n"
WT"lgkmcnt(3)\n"
RD"a[124:127],v226 offset:23552\n"
MF"v[16:19],a[96:99],a[0:3],v[16:19]\n"
BL"a[220:223],v224,s[8:11],s52 offen offset:1024 sc1\n"
MF"v[16:19],a[100:103],a[8:11],v[16:19]\n"
RD"a[64:67],v226 offset:24576\n"
MF"v[56:59],a[100:103],a[12:15],v[56:59]\n"
MF"v[56:59],a[96:99],a[4:7],v[56:59]\n"
RD"a[68:71],v226 offset:25600\n"
MF"v[20:23],a[104:107],a[0:3],v[20:23]\n"
BL"a[224:227],v224,s[8:11],s53 offen offset:1024 sc1\n"
MF"v[20:23],a[108:111],a[8:11],v[20:23]\n"
RD"a[72:75],v226 offset:26624\n"
MF"v[60:63],a[108:111],a[12:15],v[60:63]\n"
MF"v[60:63],a[104:107],a[4:7],v[60:63]\n"
WT"lgkmcnt(3)\n"
RD"a[76:79],v226 offset:27648\n"
MF"v[24:27],a[112:115],a[0:3],v[24:27]\n"
BL"a[228:231],v224,s[8:11],s52 offen offset:2048 sc1\n"
MF"v[24:27],a[116:119],a[8:11],v[24:27]\n"
RD"a[80:83],v226 offset:28672\n"
MF"v[64:67],a[116:119],a[12:15],v[64:67]\n"
MF"v[64:67],a[112:115],a[4:7],v[64:67]\n"
RD"a[84:87],v226 offset:29696\n"
MF"v[28:31],a[120:123],a[0:3],v[28:31]\n"
BL"a[232:235],v224,s[8:11],s53 offen offset:2048 sc1\n"
MF"v[28:31],a[124:127],a[8:11],v[28:31]\n"
RD"a[88:91],v226 offset:30720\n"
MF"v[68:71],a[124:127],a[12:15],v[68:71]\n"
MF"v[68:71],a[120:123],a[4:7],v[68:71]\n"
WT"lgkmcnt(3)\n"
RD"a[92:95],v226 offset:31744\n"
MF"v[32:35],a[64:67],a[0:3],v[32:35]\n"
"s_barrier\n"
MF"v[32:35],a[68:71],a[8:11],v[32:35]\n"
RD"a[96:99],v235 offset:0\n"
MF"v[72:75],a[68:71],a[12:15],v[72:75]\n"
BL"a[236:239],v224,s[8:11],s52 offen offset:3072 sc1\n"
MF"v[72:75],a[64:67],a[4:7],v[72:75]\n"
RD"a[100:103],v235 offset:1024\n"
MF"v[36:39],a[72:75],a[0:3],v[36:39]\n"
MF"v[36:39],a[76:79],a[8:11],v[36:39]\n"
RD"a[104:107],v235 offset:2048\n"
MF"v[76:79],a[76:79],a[12:15],v[76:79]\n"
BL"a[240:243],v224,s[8:11],s53 offen offset:3072 sc1\n"
MF"v[76:79],a[72:75],a[4:7],v[76:79]\n"
WT"vmcnt(27) lgkmcnt(3)\n"
RD"a[108:111],v235 offset:3072\n"
MF"v[0:3],a[80:83],a[16:19],v[0:3]\n"
WT"vmcnt(23)\n"
MF"v[0:3],a[84:87],a[24:27],v[0:3]\n"
WT"vmcnt(22)\n"
RD"a[112:115],v235 offset:4096\n"
MF"v[40:43],a[84:87],a[28:31],v[40:43]\n"
MF"v[40:43],a[80:83],a[20:23],v[40:43]\n"
RD"a[116:119],v235 offset:5120\n"
MF"v[4:7],a[88:91],a[16:19],v[4:7]\n"
MF"v[4:7],a[92:95],a[24:27],v[4:7]\n"
RD"a[120:123],v235 offset:6144\n"
MF"v[44:47],a[92:95],a[28:31],v[44:47]\n"
MF"v[44:47],a[88:91],a[20:23],v[44:47]\n"
WT"lgkmcnt(3)\n"
RD"a[124:127],v235 offset:7168\n"
MF"v[8:11],a[96:99],a[16:19],v[8:11]\n"
MF"v[8:11],a[100:103],a[24:27],v[8:11]\n"
RD"a[64:67],v235 offset:8192\n"
MF"v[48:51],a[100:103],a[28:31],v[48:51]\n"
MF"v[48:51],a[96:99],a[20:23],v[48:51]\n"
RD"a[68:71],v235 offset:9216\n"
MF"v[12:15],a[104:107],a[16:19],v[12:15]\n"
MF"v[12:15],a[108:111],a[24:27],v[12:15]\n"
RD"a[72:75],v235 offset:10240\n"
"v_bfe_u32 v238,v224,4,2\n"
MF"v[52:55],a[108:111],a[28:31],v[52:55]\n"
"s_andn2_b32 s63,s62,255\n"
"v_lshlrev_b32 v238,6,v238\n"
MF"v[52:55],a[104:107],a[20:23],v[52:55]\n"
WT"lgkmcnt(3)\n"
RD"a[76:79],v235 offset:11264\n"
MF"v[16:19],a[112:115],a[16:19],v[16:19]\n"
"buffer_load_dwordx2 v[240:241],v238,s[8:11],s63 offen sc1\n"
MF"v[16:19],a[116:119],a[24:27],v[16:19]\n"
RD"a[80:83],v235 offset:12288\n"
MF"v[56:59],a[116:119],a[28:31],v[56:59]\n"
MF"v[56:59],a[112:115],a[20:23],v[56:59]\n"
RD"a[84:87],v235 offset:13312\n"
MF"v[20:23],a[120:123],a[16:19],v[20:23]\n"
MF"v[20:23],a[124:127],a[24:27],v[20:23]\n"
RD"a[88:91],v235 offset:14336\n"
MF"v[60:63],a[124:127],a[28:31],v[60:63]\n"
MF"v[60:63],a[120:123],a[20:23],v[60:63]\n"
RD"a[92:95],v235 offset:15360\n"
"s_branch LTJ_%=\n"
"L1099_%=:\n"
"s_mov_b32 s54,0\n"
"L1101_%=:\n"
"s_sleep 2\n"
BL"v[240:243],v238,s[8:11],0 offen sc1\n"
WT"vmcnt(0)\n"
SA"s54,s54,1\n"
"v_cmp_eq_u64 vcc,s[28:29],v[240:241]\n"
"v_cmp_eq_u64_e64 s[50:51],s[28:29],v[242:243]\n"
"s_and_b64 vcc,vcc,s[50:51]\n"
"s_cmp_eq_u64 vcc,exec\n"
"s_cbranch_scc1 L1100_%=\n"
"s_cmp_lt_u32 s54,1048576\n"
"s_cbranch_scc1 L1101_%=\n"
"s_branch L1100_%=\n"
"L1102_%=:\n"
"s_mov_b32 s54,0\n"
"L1104_%=:\n"
"s_sleep 2\n"
"buffer_load_dwordx2 v[240:241],v238,s[8:11],s50 offen sc1\n"
WT"vmcnt(0)\n"
SA"s54,s54,1\n"
"v_cmp_eq_u64 vcc,s[28:29],v[240:241]\n"
"s_cmp_eq_u64 vcc,exec\n"
"s_cbranch_scc1 L1103_%=\n"
"s_cmp_lt_u32 s54,1048576\n"
"s_cbranch_scc1 L1104_%=\n"
"s_branch L1103_%=\n"
"L1105_%=:\n"
"s_mov_b32 s54,0\n"
"L1107_%=:\n"
"s_sleep 2\n"
"buffer_load_dwordx2 v[244:245],v242,s[8:11],s23 offen sc1\n"
WT"vmcnt(0)\n"
SA"s54,s54,1\n"
"v_cmp_eq_u64 vcc,s[28:29],v[244:245]\n"
"s_cmp_eq_u64 vcc,exec\n"
"s_cbranch_scc1 L1106_%=\n"
"s_cmp_lt_u32 s54,1048576\n"
"s_cbranch_scc1 L1107_%=\n"
"s_branch L1106_%=\n"
"LFA_%=:\n"
WT"lgkmcnt(6)\n"
MF"v[0:3],a[64:67],a[0:3],v[208:211]\n"
WT"lgkmcnt(3)\n"
PK"v192,v192,v193\n"
PK"v193,v194,v195\n"
MF"v[0:3],a[68:71],a[4:7],v[0:3]\n"
"s_mov_b32 m0,s40\n"
"v_readlane_b32 s43,v228,s42\n"
PK"v194,v196,v197\n"
MF"v[8:11],a[68:71],a[20:23],0\n"
PK"v195,v198,v199\n"
"v_readlane_b32 s44,v229,s42\n"
MF"v[8:11],a[64:67],a[16:19],v[8:11]\n"
SA"s42,s42,1\n"
BL"v225,s[8:11],s43 offen offset:0 sc1 lds\n"
MF"v[16:19],a[64:67],a[32:35],0\n"
"v_accvgpr_write_b32 a44,v192\n"
"v_accvgpr_write_b32 a45,v193\n"
MF"v[16:19],a[68:71],a[36:39],v[16:19]\n"
RD"a[92:95],v226 offset:7168\n"
"v_accvgpr_write_b32 a46,v194\n"
MF"v[24:27],a[68:71],a[52:55],0\n"
"v_accvgpr_write_b32 a47,v195\n"
PK"v200,v200,v201\n"
MF"v[24:27],a[64:67],a[48:51],v[24:27]\n"
PK"v201,v202,v203\n"
PK"v202,v204,v205\n"
"v_mov_b32 v227,v226\n"
MF"v[0:3],a[72:75],a[8:11],v[0:3]\n"
BL"v225,s[8:11],s43 offen offset:1024 sc1 lds\n"
PK"v203,v206,v207\n"
MF"v[0:3],a[76:79],a[12:15],v[0:3]\n"
"v_accvgpr_write_b32 a60,v200\n"
"v_accvgpr_write_b32 a61,v201\n"
MF"v[8:11],a[76:79],a[28:31],v[8:11]\n"
"s_mul_i32 s48,s32,655360\n"
"v_accvgpr_write_b32 a62,v202\n"
"v_accvgpr_write_b32 a63,v203\n"
MF"v[8:11],a[72:75],a[24:27],v[8:11]\n"
SA"s12,s74,s48\n"
"s_addc_u32 s13,s75,0\n"
MF"v[16:19],a[72:75],a[40:43],v[16:19]\n"
"s_and_b32 s13,s13,0xffff\n"
MF"v[16:19],a[76:79],a[44:47],v[16:19]\n"
"s_mov_b32 s14,655360\n"
RD"a[96:99],v226 offset:8192\n"
MF"v[24:27],a[76:79],a[60:63],v[24:27]\n"
"s_lshl_b32 s48,s33,3\n"
BL"v225,s[8:11],s43 offen offset:2048 sc1 lds\n"
MF"v[24:27],a[72:75],a[56:59],v[24:27]\n"
WT"lgkmcnt(1)\n"
EX"v0,v0\n"
MF"v[4:7],a[80:83],a[0:3],v[212:215]\n"
"s_and_b32 s49,s34,2\n"
EX"v1,v1\n"
MF"v[4:7],a[84:87],a[4:7],v[4:7]\n"
"s_lshl_b32 s49,s49,1\n"
EX"v2,v2\n"
MF"v[12:15],a[84:87],a[20:23],0\n"
SA"s48,s48,s49\n"
RD"a[100:103],v226 offset:9216\n"
PK"v0,v0,v1\n"
MF"v[12:15],a[80:83],a[16:19],v[12:15]\n"
SA"s48,s48,s30\n"
EX"v3,v3\n"
MF"v[20:23],a[80:83],a[32:35],0\n"
"s_mul_i32 s48,s48,5120\n"
EX"v8,v8\n"
MF"v[20:23],a[84:87],a[36:39],v[20:23]\n"
"s_or_b32 s49,s34,2\n"
BL"v225,s[8:11],s43 offen offset:3072 sc1 lds\n"
MF"v[28:31],a[84:87],a[52:55],0\n"
"s_cmp_eq_u32 s49,7\n"
EX"v9,v9\n"
PK"v1,v2,v3\n"
MF"v[28:31],a[80:83],a[48:51],v[28:31]\n"
"s_cselect_b32 s55,s48,2130706432\n"
RD"a[104:107],v226 offset:10240\n"
PK"v8,v8,v9\n"
MF"v[4:7],a[88:91],a[8:11],v[4:7]\n"
SA"m0,s40,16384\n"
EX"v10,v10\n"
MF"v[4:7],a[92:95],a[12:15],v[4:7]\n"
EX"v11,v11\n"
MF"v[12:15],a[92:95],a[28:31],v[12:15]\n"
EX"v16,v16\n"
MF"v[12:15],a[88:91],a[24:27],v[12:15]\n"
SA"s40,s40,32768\n"
RD"a[108:111],v226 offset:11264\n"
PK"v9,v10,v11\n"
MF"v[20:23],a[88:91],a[40:43],v[20:23]\n"
"s_cmp_eq_u32 s40,s46\n"
BL"v225,s[8:11],s44 offen offset:0 sc1 lds\n"
EX"v17,v17\n"
MF"v[20:23],a[92:95],a[44:47],v[20:23]\n"
"s_cselect_b32 s40,s47,s40\n"
EX"v18,v18\n"
MF"v[28:31],a[92:95],a[60:63],v[28:31]\n"
EX"v19,v19\n"
PK"v16,v16,v17\n"
MF"v[28:31],a[88:91],a[56:59],v[28:31]\n"
WT"lgkmcnt(1)\n"
RD"a[112:115],v226 offset:12288\n"
PK"v17,v18,v19\n"
MF"v[32:35],a[96:99],a[0:3],v[216:219]\n"
EX"v24,v24\n"
MF"v[32:35],a[100:103],a[4:7],v[32:35]\n"
EX"v25,v25\n"
MF"v[40:43],a[100:103],a[20:23],0\n"
BL"v225,s[8:11],s44 offen offset:1024 sc1 lds\n"
EX"v26,v26\n"
MF"v[40:43],a[96:99],a[16:19],v[40:43]\n"
RD"a[116:119],v226 offset:13312\n"
PK"v24,v24,v25\n"
MF"v[48:51],a[96:99],a[32:35],0\n"
EX"v27,v27\n"
MF"v[48:51],a[100:103],a[36:39],v[48:51]\n"
EX"v4,v4\n"
MF"v[56:59],a[100:103],a[52:55],0\n"
EX"v5,v5\n"
PK"v25,v26,v27\n"
MF"v[56:59],a[96:99],a[48:51],v[56:59]\n"
RD"a[120:123],v226 offset:14336\n"
PK"v2,v4,v5\n"
MF"v[32:35],a[104:107],a[8:11],v[32:35]\n"
WT"lgkmcnt(2)\n"
BL"v225,s[8:11],s44 offen offset:2048 sc1 lds\n"
EX"v6,v6\n"
MF"v[32:35],a[108:111],a[12:15],v[32:35]\n"
EX"v7,v7\n"
MF"v[40:43],a[108:111],a[28:31],v[40:43]\n"
EX"v12,v12\n"
MF"v[40:43],a[104:107],a[24:27],v[40:43]\n"
RD"a[124:127],v226 offset:15360\n"
"v_readlane_b32 s43,v228,s42\n"
PK"v3,v6,v7\n"
MF"v[48:51],a[104:107],a[40:43],v[48:51]\n"
EX"v13,v13\n"
MF"v[48:51],a[108:111],a[44:47],v[48:51]\n"
EX"v14,v14\n"
MF"v[56:59],a[108:111],a[60:63],v[56:59]\n"
BL"v225,s[8:11],s44 offen offset:3072 sc1 lds\n"
EX"v15,v15\n"
MF"v[56:59],a[104:107],a[56:59],v[56:59]\n"
"s_mov_b32 m0,s40\n"
RD"a[96:99],v227 offset:24576\n"
PK"v10,v12,v13\n"
MF"v[36:39],a[112:115],a[0:3],v[220:223]\n"
WT"lgkmcnt(2)\n"
EX"v20,v20\n"
PK"v11,v14,v15\n"
MF"v[36:39],a[116:119],a[4:7],v[36:39]\n"
EX"v21,v21\n"
MF"v[44:47],a[116:119],a[20:23],0\n"
EX"v22,v22\n"
MF"v[44:47],a[112:115],a[16:19],v[44:47]\n"
RD"a[100:103],v227 offset:25600\n"
"v_readlane_b32 s44,v229,s42\n"
PK"v18,v20,v21\n"
MF"v[52:55],a[112:115],a[32:35],0\n"
SA"s42,s42,1\n"
BL"v225,s[8:11],s43 offen offset:0 sc1 lds\n"
MF"v[52:55],a[116:119],a[36:39],v[52:55]\n"
EX"v23,v23\n"
MF"v[60:63],a[116:119],a[52:55],0\n"
EX"v28,v28\n"
MF"v[60:63],a[112:115],a[48:51],v[60:63]\n"
RD"a[104:107],v227 offset:26624\n"
PK"v19,v22,v23\n"
MF"v[36:39],a[120:123],a[8:11],v[36:39]\n"
WT"lgkmcnt(2)\n"
EX"v29,v29\n"
MF"v[36:39],a[124:127],a[12:15],v[36:39]\n"
BL"v225,s[8:11],s43 offen offset:1024 sc1 lds\n"
MF"v[44:47],a[124:127],a[28:31],v[44:47]\n"
EX"v30,v30\n"
PK"v26,v28,v29\n"
MF"v[44:47],a[120:123],a[24:27],v[44:47]\n"
RD"a[108:111],v227 offset:27648\n"
MF"v[52:55],a[120:123],a[40:43],v[52:55]\n"
EX"v31,v31\n"
MF"v[52:55],a[124:127],a[44:47],v[52:55]\n"
EX"v32,v32\n"
MF"v[60:63],a[124:127],a[60:63],v[60:63]\n"
EX"v33,v33\n"
PK"v27,v30,v31\n"
MF"v[60:63],a[120:123],a[56:59],v[60:63]\n"
SA"s41,s41,32768\n"
BL"v225,s[8:11],s43 offen offset:2048 sc1 lds\n"
RD"a[112:115],v227 offset:28672\n"
MF"v[80:83],a[96:99],v[0:3],0\n"
"s_cmp_eq_u32 s41,163840\n"
EX"v34,v34\n"
MF"v[112:115],a[96:99],v[8:11],0\n"
"s_cselect_b32 s41,0,s41\n"
EX"v35,v35\n"
MF"v[144:147],a[96:99],v[16:19],0\n"
EX"v40,v40\n"
"v_add_u32 v226,s41,v224\n"
MF"v[176:179],a[96:99],v[24:27],0\n"
WT"lgkmcnt(1)\n"
RD"a[116:119],v227 offset:29696\n"
MF"v[84:87],a[100:103],v[0:3],0\n"
EX"v41,v41\n"
MF"v[116:119],a[100:103],v[8:11],0\n"
BL"v225,s[8:11],s43 offen offset:3072 sc1 lds\n"
MF"v[148:151],a[100:103],v[16:19],0\n"
SA"m0,s40,16384\n"
EX"v42,v42\n"
MF"v[180:183],a[100:103],v[24:27],0\n"
RD"a[120:123],v227 offset:30720\n"
MF"v[88:91],a[104:107],v[0:3],0\n"
EX"v43,v43\n"
MF"v[120:123],a[104:107],v[8:11],0\n"
EX"v48,v48\n"
MF"v[152:155],a[104:107],v[16:19],0\n"
SA"s40,s40,32768\n"
EX"v49,v49\n"
MF"v[184:187],a[104:107],v[24:27],0\n"
"s_cmp_eq_u32 s40,s46\n"
BL"v225,s[8:11],s44 offen offset:0 sc1 lds\n"
RD"a[124:127],v227 offset:31744\n"
MF"v[92:95],a[108:111],v[0:3],0\n"
"s_cselect_b32 s40,s47,s40\n"
EX"v50,v50\n"
MF"v[124:127],a[108:111],v[8:11],0\n"
EX"v51,v51\n"
MF"v[156:159],a[108:111],v[16:19],0\n"
EX"v56,v56\n"
MF"v[188:191],a[108:111],v[24:27],0\n"
WT"lgkmcnt(1)\n"
EX"v57,v57\n"
MF"v[96:99],a[112:115],v[0:3],0\n"
EX"v58,v58\n"
MF"v[128:131],a[112:115],v[8:11],0\n"
BL"v225,s[8:11],s44 offen offset:1024 sc1 lds\n"
MF"v[160:163],a[112:115],v[16:19],0\n"
EX"v59,v59\n"
MF"v[192:195],a[112:115],v[24:27],0\n"
EX"v36,v36\n"
MF"v[100:103],a[116:119],v[0:3],0\n"
WT"vmcnt(16) lgkmcnt(0)\n"
"s_barrier\n"
MF"v[132:135],a[116:119],v[8:11],0\n"
RD"a[64:67],v226 offset:0\n"
RD"a[68:71],v226 offset:1024\n"
"buffer_store_dwordx2 v[232:233],v230,s[8:11],s98 offen sc1\n"
MF"v[164:167],a[116:119],v[16:19],0\n"
EX"v37,v37\n"
MF"v[196:199],a[116:119],v[24:27],0\n"
BL"v225,s[8:11],s44 offen offset:2048 sc1 lds\n"
MF"v[104:107],a[120:123],v[0:3],0\n"
EX"v38,v38\n"
MF"v[136:139],a[120:123],v[8:11],0\n"
EX"v39,v39\n"
MF"v[168:171],a[120:123],v[16:19],0\n"
EX"v44,v44\n"
MF"v[200:203],a[120:123],v[24:27],0\n"
RD"a[72:75],v226 offset:2048\n"
RD"a[76:79],v226 offset:3072\n"
MF"v[108:111],a[124:127],v[0:3],0\n"
EX"v45,v45\n"
MF"v[140:143],a[124:127],v[8:11],0\n"
BL"v225,s[8:11],s44 offen offset:3072 sc1 lds\n"
MF"v[172:175],a[124:127],v[16:19],0\n"
EX"v46,v46\n"
MF"v[204:207],a[124:127],v[24:27],0\n"
RD"a[80:83],v226 offset:4096\n"
MF"a[132:135],a[128:131],v[0:3],0\n"
EX"v47,v47\n"
MF"a[136:139],a[128:131],v[8:11],0\n"
"buffer_load_dword v255,v253,s[12:15],s55 offen\n"
EX"v52,v52\n"
MF"a[140:143],a[128:131],v[16:19],0\n"
SA"s55,s55,163840\n"
EX"v53,v53\n"
MF"a[144:147],a[128:131],v[24:27],0\n"
RD"a[84:87],v226 offset:5120\n"
RD"a[88:91],v226 offset:6144\n"
EX"v54,v54\n"
EX"v55,v55\n"
EX"v60,v60\n"
EX"v61,v61\n"
EX"v62,v62\n"
EX"v63,v63\n"
"s_branch LDB_%=\n"
"LD2_%=:\n"
WT"lgkmcnt(6)\n"
MF"v[0:3],a[64:67],a[32:35],v[208:211]\n"
WT"lgkmcnt(3)\n"
EX"v56,v56\n"
MF"v[0:3],a[68:71],a[36:39],v[0:3]\n"
"s_mov_b32 m0,s40\n"
EX"v57,v57\n"
MF"v[8:11],a[68:71],a[52:55],0\n"
RD"a[92:95],v226 offset:7168\n"
"v_readlane_b32 s43,v228,s42\n"
MF"v[8:11],a[64:67],a[48:51],v[8:11]\n"
EX"v58,v58\n"
MF"v[0:3],a[72:75],a[40:43],v[0:3]\n"
SA"s41,s41,32768\n"
RD"a[96:99],v226 offset:8192\n"
RD"a[100:103],v226 offset:9216\n"
"v_readlane_b32 s44,v229,s42\n"
MF"v[0:3],a[76:79],a[44:47],v[0:3]\n"
SA"s42,s42,1\n"
BL"v225,s[8:11],s43 offen offset:0 sc1 lds\n"
PK"v76,v56,v57\n"
MF"v[8:11],a[76:79],a[60:63],v[8:11]\n"
"s_cmp_eq_u32 s41,163840\n"
EX"v59,v59\n"
MF"v[8:11],a[72:75],a[56:59],v[8:11]\n"
WT"lgkmcnt(3)\n"
RD"a[104:107],v226 offset:10240\n"
PK"v77,v58,v59\n"
MF"v[4:7],a[80:83],a[32:35],v[212:215]\n"
"s_cselect_b32 s41,0,s41\n"
EX"v60,v60\n"
"v_add_u32 v227,s41,v224\n"
MF"v[4:7],a[84:87],a[36:39],v[4:7]\n"
EX"v61,v61\n"
MF"v[12:15],a[84:87],a[52:55],0\n"
BL"v225,s[8:11],s43 offen offset:1024 sc1 lds\n"
MF"v[12:15],a[80:83],a[48:51],v[12:15]\n"
RD"a[108:111],v226 offset:11264\n"
RD"a[112:115],v226 offset:12288\n"
MF"v[4:7],a[88:91],a[40:43],v[4:7]\n"
WT"lgkmcnt(3)\n"
EX"v62,v62\n"
PK"v78,v60,v61\n"
MF"v[4:7],a[92:95],a[44:47],v[4:7]\n"
RD"a[116:119],v226 offset:13312\n"
RD"a[120:123],v226 offset:14336\n"
MF"v[12:15],a[92:95],a[60:63],v[12:15]\n"
EX"v63,v63\n"
MF"v[12:15],a[88:91],a[56:59],v[12:15]\n"
BL"v225,s[8:11],s43 offen offset:2048 sc1 lds\n"
MF"v[16:19],a[96:99],a[32:35],v[216:219]\n"
EX"v0,v0\n"
PK"v79,v62,v63\n"
MF"v[16:19],a[100:103],a[36:39],v[16:19]\n"
EX"v1,v1\n"
MF"v[24:27],a[100:103],a[52:55],0\n"
RD"a[124:127],v226 offset:15360\n"
RD"a[64:67],v226 offset:16384\n"
MF"v[24:27],a[96:99],a[48:51],v[24:27]\n"
WT"lgkmcnt(4)\n"
EX"v2,v2\n"
PK"v0,v0,v1\n"
MF"v[16:19],a[104:107],a[40:43],v[16:19]\n"
BL"v225,s[8:11],s43 offen offset:3072 sc1 lds\n"
RD"a[68:71],v226 offset:17408\n"
MF"v[16:19],a[108:111],a[44:47],v[16:19]\n"
SA"m0,s40,16384\n"
RD"a[72:75],v226 offset:18432\n"
RD"a[76:79],v226 offset:19456\n"
EX"v3,v3\n"
MF"v[24:27],a[108:111],a[60:63],v[24:27]\n"
RD"a[80:83],v226 offset:20480\n"
RD"a[84:87],v226 offset:21504\n"
MF"v[24:27],a[104:107],a[56:59],v[24:27]\n"
EX"v8,v8\n"
PK"v1,v2,v3\n"
MF"v[20:23],a[112:115],a[32:35],v[220:223]\n"
WT"lgkmcnt(7)\n"
EX"v9,v9\n"
MF"v[20:23],a[116:119],a[36:39],v[20:23]\n"
SA"s40,s40,32768\n"
BL"v225,s[8:11],s44 offen offset:0 sc1 lds\n"
MF"v[28:31],a[116:119],a[52:55],0\n"
"s_cmp_eq_u32 s40,s46\n"
RD"a[88:91],v226 offset:22528\n"
RD"a[92:95],v226 offset:23552\n"
EX"v10,v10\n"
MF"v[28:31],a[112:115],a[48:51],v[28:31]\n"
"s_cselect_b32 s40,s47,s40\n"
RD"a[96:99],v226 offset:24576\n"
RD"a[100:103],v226 offset:25600\n"
PK"v8,v8,v9\n"
MF"v[20:23],a[120:123],a[40:43],v[20:23]\n"
WT"lgkmcnt(6)\n"
EX"v11,v11\n"
MF"v[20:23],a[124:127],a[44:47],v[20:23]\n"
RD"a[104:107],v226 offset:26624\n"
RD"a[108:111],v226 offset:27648\n"
MF"v[28:31],a[124:127],a[60:63],v[28:31]\n"
BL"v225,s[8:11],s44 offen offset:1024 sc1 lds\n"
EX"v4,v4\n"
MF"v[28:31],a[120:123],a[56:59],v[28:31]\n"
RD"a[112:115],v226 offset:28672\n"
RD"a[116:119],v226 offset:29696\n"
PK"v9,v10,v11\n"
MF"v[40:43],a[64:67],a[48:51],v[208:211]\n"
EX"v5,v5\n"
MF"v[40:43],a[68:71],a[52:55],v[40:43]\n"
EX"v6,v6\n"
MF"v[40:43],a[72:75],a[56:59],v[40:43]\n"
RD"a[120:123],v226 offset:30720\n"
RD"a[124:127],v226 offset:31744\n"
PK"v2,v4,v5\n"
MF"v[40:43],a[76:79],a[60:63],v[40:43]\n"
WT"lgkmcnt(6)\n"
BL"v225,s[8:11],s44 offen offset:2048 sc1 lds\n"
EX"v7,v7\n"
MF"v[44:47],a[80:83],a[48:51],v[212:215]\n"
WT"vmcnt(19)\n"
"s_barrier\n"
MF"v[44:47],a[84:87],a[52:55],v[44:47]\n"
EX"v12,v12\n"
PK"v3,v6,v7\n"
MF"v[44:47],a[88:91],a[56:59],v[44:47]\n"
EX"v13,v13\n"
MF"v[44:47],a[92:95],a[60:63],v[44:47]\n"
BL"v225,s[8:11],s44 offen offset:3072 sc1 lds\n"
"v_readlane_b32 s43,v228,s42\n"
MF"v[56:59],a[96:99],a[48:51],v[216:219]\n"
"s_mov_b32 m0,s40\n"
RD"a[96:99],v227 offset:8192\n"
PK"v10,v12,v13\n"
EX"v14,v14\n"
MF"v[56:59],a[100:103],a[52:55],v[56:59]\n"
WT"lgkmcnt(1)\n"
RD"a[100:103],v227 offset:9216\n"
MF"v[56:59],a[104:107],a[56:59],v[56:59]\n"
EX"v15,v15\n"
MF"v[56:59],a[108:111],a[60:63],v[56:59]\n"
RD"a[104:107],v227 offset:10240\n"
RD"a[108:111],v227 offset:11264\n"
MF"v[60:63],a[112:115],a[48:51],v[220:223]\n"
EX"v16,v16\n"
PK"v11,v14,v15\n"
MF"v[60:63],a[116:119],a[52:55],v[60:63]\n"
EX"v17,v17\n"
"v_readlane_b32 s44,v229,s42\n"
MF"v[60:63],a[120:123],a[56:59],v[60:63]\n"
SA"s42,s42,1\n"
BL"v225,s[8:11],s43 offen offset:0 sc1 lds\n"
MF"v[60:63],a[124:127],a[60:63],v[60:63]\n"
WT"lgkmcnt(3)\n"
EX"v18,v18\n"
MF"v[112:115],a[96:99],v[68:71],v[112:115]\n"
SA"s41,s41,32768\n"
RD"a[112:115],v227 offset:12288\n"
PK"v16,v16,v17\n"
MF"v[144:147],a[96:99],v[72:75],v[144:147]\n"
"s_cmp_eq_u32 s41,163840\n"
EX"v19,v19\n"
MF"v[176:179],a[96:99],v[76:79],v[176:179]\n"
WT"lgkmcnt(1)\n"
EX"v24,v24\n"
MF"v[116:119],a[100:103],v[68:71],v[116:119]\n"
"s_cselect_b32 s41,0,s41\n"
BL"v225,s[8:11],s43 offen offset:1024 sc1 lds\n"
RD"a[116:119],v227 offset:13312\n"
MF"v[148:151],a[100:103],v[72:75],v[148:151]\n"
EX"v25,v25\n"
"v_add_u32 v226,s41,v224\n"
MF"v[180:183],a[100:103],v[76:79],v[180:183]\n"
RD"a[120:123],v227 offset:14336\n"
PK"v17,v18,v19\n"
MF"v[120:123],a[104:107],v[68:71],v[120:123]\n"
EX"v26,v26\n"
MF"v[152:155],a[104:107],v[72:75],v[152:155]\n"
EX"v27,v27\n"
PK"v24,v24,v25\n"
MF"v[184:187],a[104:107],v[76:79],v[184:187]\n"
BL"v225,s[8:11],s43 offen offset:2048 sc1 lds\n"
MF"v[124:127],a[108:111],v[68:71],v[124:127]\n"
EX"v20,v20\n"
MF"v[156:159],a[108:111],v[72:75],v[156:159]\n"
RD"a[124:127],v227 offset:15360\n"
PK"v25,v26,v27\n"
MF"v[188:191],a[108:111],v[76:79],v[188:191]\n"
WT"lgkmcnt(2)\n"
EX"v21,v21\n"
MF"v[128:131],a[112:115],v[68:71],v[128:131]\n"
BL"v225,s[8:11],s43 offen offset:3072 sc1 lds\n"
MF"v[160:163],a[112:115],v[72:75],v[160:163]\n"
SA"m0,s40,16384\n"
EX"v22,v22\n"
MF"v[192:195],a[112:115],v[76:79],v[192:195]\n"
EX"v23,v23\n"
PK"v18,v20,v21\n"
MF"v[132:135],a[116:119],v[68:71],v[132:135]\n"
RD"a[64:67],v227 offset:16384\n"
PK"v19,v22,v23\n"
MF"v[164:167],a[116:119],v[72:75],v[164:167]\n"
EX"v28,v28\n"
MF"v[196:199],a[116:119],v[76:79],v[196:199]\n"
WT"lgkmcnt(1)\n"
BL"v225,s[8:11],s44 offen offset:0 sc1 lds\n"
MF"v[136:139],a[120:123],v[68:71],v[136:139]\n"
SA"s40,s40,32768\n"
EX"v29,v29\n"
MF"v[168:171],a[120:123],v[72:75],v[168:171]\n"
"s_cmp_eq_u32 s40,s46\n"
RD"a[68:71],v227 offset:24576\n"
RD"a[72:75],v227 offset:17408\n"
MF"v[200:203],a[120:123],v[76:79],v[200:203]\n"
"s_cselect_b32 s40,s47,s40\n"
EX"v30,v30\n"
MF"v[140:143],a[124:127],v[68:71],v[140:143]\n"
RD"a[76:79],v227 offset:25600\n"
RD"a[80:83],v227 offset:18432\n"
PK"v26,v28,v29\n"
MF"v[172:175],a[124:127],v[72:75],v[172:175]\n"
EX"v31,v31\n"
MF"v[204:207],a[124:127],v[76:79],v[204:207]\n"
BL"v225,s[8:11],s44 offen offset:1024 sc1 lds\n"
MF"a[136:139],a[128:131],v[68:71],a[136:139]\n"
EX"v40,v40\n"
PK"v27,v30,v31\n"
MF"a[140:143],a[128:131],v[72:75],a[140:143]\n"
EX"v41,v41\n"
MF"a[144:147],a[128:131],v[76:79],a[144:147]\n"
WT"lgkmcnt(2)\n"
RD"a[84:87],v227 offset:26624\n"
PK"v68,v40,v41\n"
MF"v[144:147],a[64:67],v[0:3],v[144:147]\n"
BL"v225,s[8:11],s44 offen offset:2048 sc1 lds\n"
MF"v[144:147],a[68:71],v[16:19],v[144:147]\n"
EX"v42,v42\n"
MF"v[176:179],a[68:71],v[24:27],v[176:179]\n"
SA"s51,s59,16384\n"
RD"a[88:91],v227 offset:19456\n"
RD"a[92:95],v227 offset:27648\n"
MF"v[176:179],a[64:67],v[8:11],v[176:179]\n"
EX"v43,v43\n"
MF"v[148:151],a[72:75],v[0:3],v[148:151]\n"
WT"lgkmcnt(3)\n"
EX"v44,v44\n"
MF"v[148:151],a[76:79],v[16:19],v[148:151]\n"
RD"a[96:99],v227 offset:20480\n"
"v_accvgpr_read_b32 v252,a136\n"
"v_subrev_u32 v253,0x2b800000,v252\n"
MF"v[180:183],a[76:79],v[24:27],v[180:183]\n"
"v_cmp_le_u32 vcc,0x34000000,v253\n"
"s_cmp_lg_u64 vcc,0\n"
"s_cbranch_scc1 L1035_%=\n"
"L1034_%=:\n"
MF"v[180:183],a[72:75],v[8:11],v[180:183]\n"
BL"v225,s[8:11],s44 offen offset:3072 sc1 lds\n"
EX"v45,v45\n"
MF"v[152:155],a[80:83],v[0:3],v[152:155]\n"
WT"lgkmcnt(3)\n"
"v_rcp_f32 v252,v252\n"
EX"v46,v46\n"
MF"v[152:155],a[84:87],v[16:19],v[152:155]\n"
"s_mov_b32 m0,s40\n"
RD"a[100:103],v227 offset:28672\n"
RD"a[104:107],v227 offset:21504\n"
"v_readlane_b32 s43,v228,s42\n"
MF"v[184:187],a[84:87],v[24:27],v[184:187]\n"
EX"v47,v47\n"
PK"v69,v42,v43\n"
MF"v[184:187],a[80:83],v[8:11],v[184:187]\n"
WT"lgkmcnt(3)\n"
RD"a[108:111],v227 offset:29696\n"
RD"a[112:115],v227 offset:22528\n"
EX"v56,v56\n"
MF"v[156:159],a[88:91],v[0:3],v[156:159]\n"
EX"v57,v57\n"
"v_readlane_b32 s44,v229,s42\n"
MF"v[156:159],a[92:95],v[16:19],v[156:159]\n"
SA"s42,s42,1\n"
EX"v58,v58\n"
PK"v70,v44,v45\n"
MF"v[188:191],a[92:95],v[24:27],v[188:191]\n"
RD"a[116:119],v227 offset:30720\n"
MU"v112,v252,v112\n"
MU"v113,v252,v113\n"
MF"v[188:191],a[88:91],v[8:11],v[188:191]\n"
WT"lgkmcnt(5)\n"
EX"v59,v59\n"
MU"v114,v252,v114\n"
MF"v[160:163],a[96:99],v[0:3],v[160:163]\n"
WT"lgkmcnt(3)\n"
EX"v60,v60\n"
MU"v115,v252,v115\n"
MF"v[160:163],a[100:103],v[16:19],v[160:163]\n"
RD"a[120:123],v227 offset:23552\n"
RD"a[124:127],v227 offset:31744\n"
MU"v116,v252,v116\n"
MF"v[192:195],a[100:103],v[24:27],v[192:195]\n"
EX"v61,v61\n"
MU"v117,v252,v117\n"
MF"v[192:195],a[96:99],v[8:11],v[192:195]\n"
MU"v118,v252,v118\n"
MU"v119,v252,v119\n"
PK"v71,v46,v47\n"
MF"v[164:167],a[104:107],v[0:3],v[164:167]\n"
WT"lgkmcnt(3)\n"
EX"v62,v62\n"
PK"v112,v112,v113\n"
MF"v[164:167],a[108:111],v[16:19],v[164:167]\n"
SA"s41,s41,32768\n"
WT"vmcnt(20)\n"
"s_barrier\n"
MF"v[196:199],a[108:111],v[24:27],v[196:199]\n"
"s_cmp_eq_u32 s41,163840\n"
BL"v225,s[8:11],s43 offen offset:0 sc1 lds\n"
BL"v225,s[8:11],s43 offen offset:1024 sc1 lds\n"
MF"v[196:199],a[104:107],v[8:11],v[196:199]\n"
"s_cselect_b32 s41,0,s41\n"
BL"v225,s[8:11],s43 offen offset:2048 sc1 lds\n"
RD"a[64:67],v226 offset:0\n"
MF"v[168:171],a[112:115],v[0:3],v[168:171]\n"
WT"lgkmcnt(1)\n"
EX"v63,v63\n"
"v_add_u32 v227,s41,v224\n"
MF"v[168:171],a[116:119],v[16:19],v[168:171]\n"
RD"a[68:71],v226 offset:8192\n"
RD"a[72:75],v226 offset:1024\n"
PK"v113,v114,v115\n"
MF"v[200:203],a[116:119],v[24:27],v[200:203]\n"
RD"a[76:79],v226 offset:9216\n"
RD"a[80:83],v226 offset:2048\n"
PK"v114,v116,v117\n"
MF"v[200:203],a[112:115],v[8:11],v[200:203]\n"
BL"v225,s[8:11],s43 offen offset:3072 sc1 lds\n"
PK"v115,v118,v119\n"
MF"v[172:175],a[120:123],v[0:3],v[172:175]\n"
SA"m0,s40,16384\n"
BS"v[112:115],v224,s[8:11],s51 offen offset:0 sc1\n"
RD"a[84:87],v226 offset:10240\n"
MF"v[172:175],a[124:127],v[16:19],v[172:175]\n"
RD"a[88:91],v226 offset:3072\n"
RD"a[92:95],v226 offset:11264\n"
MU"v120,v252,v120\n"
MF"v[204:207],a[124:127],v[24:27],v[204:207]\n"
BL"v225,s[8:11],s44 offen offset:0 sc1 lds\n"
MU"v121,v252,v121\n"
MF"v[204:207],a[120:123],v[8:11],v[204:207]\n"
SA"s40,s40,32768\n"
RD"a[96:99],v226 offset:4096\n"
RD"a[100:103],v226 offset:12288\n"
PK"v76,v56,v57\n"
MF"a[140:143],a[128:131],v[0:3],a[140:143]\n"
"s_cmp_eq_u32 s40,s46\n"
RD"a[104:107],v226 offset:5120\n"
RD"a[108:111],v226 offset:13312\n"
MU"v122,v252,v122\n"
MF"a[140:143],a[128:131],v[16:19],a[140:143]\n"
"s_cselect_b32 s40,s47,s40\n"
RD"a[112:115],v226 offset:6144\n"
MU"v123,v252,v123\n"
MU"v124,v252,v124\n"
MF"a[144:147],a[128:131],v[24:27],a[144:147]\n"
PK"v77,v58,v59\n"
PK"v78,v60,v61\n"
PK"v79,v62,v63\n"
MF"a[144:147],a[128:131],v[8:11],a[144:147]\n"
WT"lgkmcnt(7)\n"
RD"a[116:119],v226 offset:14336\n"
RD"a[120:123],v226 offset:7168\n"
MU"v125,v252,v125\n"
MF"v[176:179],a[64:67],v[68:71],v[176:179]\n"
BL"v225,s[8:11],s44 offen offset:1024 sc1 lds\n"
MU"v126,v252,v126\n"
MF"v[176:179],a[68:71],v[76:79],v[176:179]\n"
RD"a[124:127],v226 offset:15360\n"
MU"v127,v252,v127\n"
PK"v120,v120,v121\n"
MF"v[180:183],a[72:75],v[68:71],v[180:183]\n"
WT"vmcnt(15)\n"
"s_barrier\n"
MF"v[180:183],a[76:79],v[76:79],v[180:183]\n"
RD"a[64:67],v227 offset:0\n"
PK"v121,v122,v123\n"
PK"v122,v124,v125\n"
MF"v[184:187],a[80:83],v[68:71],v[184:187]\n"
BL"v225,s[8:11],s44 offen offset:2048 sc1 lds\n"
PK"v123,v126,v127\n"
MF"v[184:187],a[84:87],v[76:79],v[184:187]\n"
WT"lgkmcnt(4)\n"
BS"v[120:123],v224,s[8:11],s51 offen offset:1024 sc1\n"
MU"v128,v252,v128\n"
MF"v[188:191],a[88:91],v[68:71],v[188:191]\n"
RD"a[68:71],v227 offset:1024\n"
RD"a[72:75],v227 offset:2048\n"
MU"v129,v252,v129\n"
MF"v[188:191],a[92:95],v[76:79],v[188:191]\n"
MU"v130,v252,v130\n"
MU"v131,v252,v131\n"
MU"v132,v252,v132\n"
MF"v[192:195],a[96:99],v[68:71],v[192:195]\n"
MU"v133,v252,v133\n"
MU"v134,v252,v134\n"
MU"v135,v252,v135\n"
MF"v[192:195],a[100:103],v[76:79],v[192:195]\n"
BL"v225,s[8:11],s44 offen offset:3072 sc1 lds\n"
PK"v128,v128,v129\n"
MF"v[196:199],a[104:107],v[68:71],v[196:199]\n"
RD"a[76:79],v227 offset:3072\n"
PK"v129,v130,v131\n"
PK"v130,v132,v133\n"
MF"v[196:199],a[108:111],v[76:79],v[196:199]\n"
PK"v131,v134,v135\n"
MU"v136,v252,v136\n"
MU"v137,v252,v137\n"
MF"v[200:203],a[112:115],v[68:71],v[200:203]\n"
WT"lgkmcnt(3)\n"
BS"v[128:131],v224,s[8:11],s51 offen offset:2048 sc1\n"
MU"v138,v252,v138\n"
MF"v[200:203],a[116:119],v[76:79],v[200:203]\n"
RD"a[80:83],v227 offset:4096\n"
RD"a[84:87],v227 offset:5120\n"
MU"v139,v252,v139\n"
MF"v[204:207],a[120:123],v[68:71],v[204:207]\n"
RD"a[88:91],v227 offset:6144\n"
MU"v140,v252,v140\n"
MU"v141,v252,v141\n"
MF"v[204:207],a[124:127],v[76:79],v[204:207]\n"
PK"v136,v136,v137\n"
MU"v142,v252,v142\n"
MU"v143,v252,v143\n"
MF"a[144:147],a[128:131],v[68:71],a[144:147]\n"
PK"v137,v138,v139\n"
PK"v138,v140,v141\n"
PK"v139,v142,v143\n"
MF"a[144:147],a[128:131],v[76:79],a[144:147]\n"
BS"v[136:139],v224,s[8:11],s51 offen offset:3072 sc1\n"
"s_branch LTL_%=\n"
"LEV_%=:\n"
"s_mov_b32 s14,0\n"
WT"lgkmcnt(6)\n"
MF"v[0:3],a[64:67],a[0:3],0\n"
WT"lgkmcnt(5)\n"
PK"v64,v32,v33\n"
PK"v65,v34,v35\n"
MF"v[0:3],a[68:71],a[4:7],v[0:3]\n"
SA"s95,s40,32768\n"
PK"v66,v36,v37\n"
PK"v67,v38,v39\n"
MF"v[8:11],a[68:71],a[20:23],0\n"
"s_cmp_eq_u32 s95,s46\n"
PK"v68,v40,v41\n"
PK"v69,v42,v43\n"
MF"v[8:11],a[64:67],a[16:19],v[8:11]\n"
"s_cselect_b32 s95,s47,s95\n"
PK"v70,v44,v45\n"
PK"v71,v46,v47\n"
MF"v[16:19],a[64:67],a[32:35],0\n"
SA"m0,s95,0\n"
PK"v72,v48,v49\n"
PK"v73,v50,v51\n"
MF"v[16:19],a[68:71],a[36:39],v[16:19]\n"
RD"a[92:95],v226 offset:7168\n"
"v_mov_b32 v227,v226\n"
MF"v[24:27],a[68:71],a[52:55],0\n"
PK"v74,v52,v53\n"
PK"v75,v54,v55\n"
MF"v[24:27],a[64:67],a[48:51],v[24:27]\n"
WT"lgkmcnt(1)\n"
BL"v235,s[24:27],s52 offen lds\n"
MF"v[0:3],a[72:75],a[8:11],v[0:3]\n"
SA"s52,s52,s62\n"
PK"v76,v56,v57\n"
PK"v77,v58,v59\n"
MF"v[0:3],a[76:79],a[12:15],v[0:3]\n"
PK"v78,v60,v61\n"
PK"v79,v62,v63\n"
MF"v[8:11],a[76:79],a[28:31],v[8:11]\n"
MF"v[8:11],a[72:75],a[24:27],v[8:11]\n"
MF"v[16:19],a[72:75],a[40:43],v[16:19]\n"
SA"m0,s95,1024\n"
MF"v[16:19],a[76:79],a[44:47],v[16:19]\n"
RD"a[96:99],v226 offset:8192\n"
MF"v[24:27],a[76:79],a[60:63],v[24:27]\n"
EX"v0,v0\n"
MF"v[24:27],a[72:75],a[56:59],v[24:27]\n"
BL"v235,s[24:27],s52 offen lds\n"
MF"v[4:7],a[80:83],a[0:3],0\n"
SA"s52,s52,s62\n"
EX"v1,v1\n"
MF"v[4:7],a[84:87],a[4:7],v[4:7]\n"
RD"a[100:103],v226 offset:9216\n"
PK"v0,v0,v1\n"
MF"v[12:15],a[84:87],a[20:23],0\n"
EX"v2,v2\n"
MF"v[12:15],a[80:83],a[16:19],v[12:15]\n"
EX"v3,v3\n"
MF"v[20:23],a[80:83],a[32:35],0\n"
SA"m0,s95,2048\n"
EX"v8,v8\n"
MF"v[20:23],a[84:87],a[36:39],v[20:23]\n"
RD"a[104:107],v226 offset:10240\n"
PK"v1,v2,v3\n"
MF"v[28:31],a[84:87],a[52:55],0\n"
EX"v9,v9\n"
MF"v[28:31],a[80:83],a[48:51],v[28:31]\n"
BL"v235,s[24:27],s52 offen lds\n"
MF"v[4:7],a[88:91],a[8:11],v[4:7]\n"
WT"lgkmcnt(1)\n"
EX"v10,v10\n"
MF"v[4:7],a[92:95],a[12:15],v[4:7]\n"
SA"s52,s52,s62\n"
RD"a[108:111],v226 offset:11264\n"
PK"v8,v8,v9\n"
MF"v[12:15],a[92:95],a[28:31],v[12:15]\n"
EX"v11,v11\n"
MF"v[12:15],a[88:91],a[24:27],v[12:15]\n"
EX"v16,v16\n"
MF"v[20:23],a[88:91],a[40:43],v[20:23]\n"
SA"m0,s95,3072\n"
EX"v17,v17\n"
MF"v[20:23],a[92:95],a[44:47],v[20:23]\n"
RD"a[112:115],v226 offset:12288\n"
PK"v9,v10,v11\n"
MF"v[28:31],a[92:95],a[60:63],v[28:31]\n"
EX"v18,v18\n"
MF"v[28:31],a[88:91],a[56:59],v[28:31]\n"
BL"v235,s[24:27],s52 offen lds\n"
MF"v[32:35],a[96:99],a[0:3],0\n"
SA"s52,s52,s62\n"
EX"v19,v19\n"
MF"v[32:35],a[100:103],a[4:7],v[32:35]\n"
RD"a[116:119],v226 offset:13312\n"
PK"v16,v16,v17\n"
MF"v[40:43],a[100:103],a[20:23],0\n"
EX"v24,v24\n"
MF"v[40:43],a[96:99],a[16:19],v[40:43]\n"
EX"v25,v25\n"
MF"v[48:51],a[96:99],a[32:35],0\n"
SA"m0,s95,16384\n"
EX"v26,v26\n"
MF"v[48:51],a[100:103],a[36:39],v[48:51]\n"
RD"a[120:123],v226 offset:14336\n"
PK"v17,v18,v19\n"
MF"v[56:59],a[100:103],a[52:55],0\n"
EX"v27,v27\n"
MF"v[56:59],a[96:99],a[48:51],v[56:59]\n"
WT"lgkmcnt(2)\n"
BL"v235,s[24:27],s52 offen lds\n"
MF"v[32:35],a[104:107],a[8:11],v[32:35]\n"
SA"s52,s52,s62\n"
EX"v4,v4\n"
MF"v[32:35],a[108:111],a[12:15],v[32:35]\n"
RD"a[124:127],v226 offset:15360\n"
PK"v24,v24,v25\n"
MF"v[40:43],a[108:111],a[28:31],v[40:43]\n"
EX"v5,v5\n"
MF"v[40:43],a[104:107],a[24:27],v[40:43]\n"
EX"v6,v6\n"
MF"v[48:51],a[104:107],a[40:43],v[48:51]\n"
SA"m0,s95,17408\n"
EX"v7,v7\n"
MF"v[48:51],a[108:111],a[44:47],v[48:51]\n"
PK"v25,v26,v27\n"
PK"v2,v4,v5\n"
MF"v[56:59],a[108:111],a[60:63],v[56:59]\n"
EX"v12,v12\n"
MF"v[56:59],a[104:107],a[56:59],v[56:59]\n"
BL"v235,s[24:27],s52 offen lds\n"
MF"v[36:39],a[112:115],a[0:3],0\n"
WT"lgkmcnt(0)\n"
EX"v13,v13\n"
MF"v[36:39],a[116:119],a[4:7],v[36:39]\n"
SA"s52,s52,s62\n"
RD"a[64:67],v227 offset:16384\n"
PK"v3,v6,v7\n"
MF"v[44:47],a[116:119],a[20:23],0\n"
EX"v14,v14\n"
MF"v[44:47],a[112:115],a[16:19],v[44:47]\n"
PK"v10,v12,v13\n"
MF"v[52:55],a[112:115],a[32:35],0\n"
SA"m0,s95,18432\n"
EX"v15,v15\n"
MF"v[52:55],a[116:119],a[36:39],v[52:55]\n"
RD"a[68:71],v227 offset:17408\n"
PK"v11,v14,v15\n"
MF"v[60:63],a[116:119],a[52:55],0\n"
EX"v20,v20\n"
MF"v[60:63],a[112:115],a[48:51],v[60:63]\n"
BL"v235,s[24:27],s52 offen lds\n"
MF"v[36:39],a[120:123],a[8:11],v[36:39]\n"
SA"s52,s52,s62\n"
EX"v21,v21\n"
MF"v[36:39],a[124:127],a[12:15],v[36:39]\n"
RD"a[72:75],v227 offset:18432\n"
"v_readlane_b32 s43,v228,s42\n"
MF"v[44:47],a[124:127],a[28:31],v[44:47]\n"
EX"v22,v22\n"
MF"v[44:47],a[120:123],a[24:27],v[44:47]\n"
"s_mov_b32 m0,s40\n"
PK"v18,v20,v21\n"
MF"v[52:55],a[120:123],a[40:43],v[52:55]\n"
EX"v23,v23\n"
MF"v[52:55],a[124:127],a[44:47],v[52:55]\n"
RD"a[76:79],v227 offset:19456\n"
"v_readlane_b32 s44,v229,s42\n"
MF"v[60:63],a[124:127],a[60:63],v[60:63]\n"
EX"v28,v28\n"
MF"v[60:63],a[120:123],a[56:59],v[60:63]\n"
WT"lgkmcnt(2)\n"
BL"v225,s[8:11],s43 offen offset:0 sc1 lds\n"
MF"v[80:83],a[64:67],v[64:67],v[80:83]\n"
SA"s42,s42,1\n"
EX"v29,v29\n"
MF"v[112:115],a[64:67],v[68:71],v[112:115]\n"
SA"s41,s41,32768\n"
RD"a[80:83],v227 offset:20480\n"
PK"v19,v22,v23\n"
MF"v[144:147],a[64:67],v[72:75],v[144:147]\n"
"s_cmp_eq_u32 s41,163840\n"
EX"v30,v30\n"
MF"v[176:179],a[64:67],v[76:79],v[176:179]\n"
"s_cselect_b32 s41,0,s41\n"
PK"v26,v28,v29\n"
"v_add_u32 v226,s41,v224\n"
MF"v[84:87],a[68:71],v[64:67],v[84:87]\n"
EX"v31,v31\n"
MF"v[116:119],a[68:71],v[68:71],v[116:119]\n"
RD"a[84:87],v227 offset:21504\n"
PK"v27,v30,v31\n"
MF"v[148:151],a[68:71],v[72:75],v[148:151]\n"
EX"v32,v32\n"
MF"v[180:183],a[68:71],v[76:79],v[180:183]\n"
WT"lgkmcnt(2)\n"
BL"v225,s[8:11],s43 offen offset:1024 sc1 lds\n"
MF"v[88:91],a[72:75],v[64:67],v[88:91]\n"
EX"v33,v33\n"
MF"v[120:123],a[72:75],v[68:71],v[120:123]\n"
RD"a[88:91],v227 offset:22528\n"
MF"v[152:155],a[72:75],v[72:75],v[152:155]\n"
EX"v34,v34\n"
MF"v[184:187],a[72:75],v[76:79],v[184:187]\n"
MF"v[92:95],a[76:79],v[64:67],v[92:95]\n"
EX"v35,v35\n"
MF"v[124:127],a[76:79],v[68:71],v[124:127]\n"
RD"a[92:95],v227 offset:23552\n"
MF"v[156:159],a[76:79],v[72:75],v[156:159]\n"
EX"v40,v40\n"
MF"v[188:191],a[76:79],v[76:79],v[188:191]\n"
WT"lgkmcnt(2)\n"
BL"v225,s[8:11],s43 offen offset:2048 sc1 lds\n"
MF"v[96:99],a[80:83],v[64:67],v[96:99]\n"
EX"v41,v41\n"
MF"v[128:131],a[80:83],v[68:71],v[128:131]\n"
RD"a[96:99],v227 offset:24576\n"
MF"v[160:163],a[80:83],v[72:75],v[160:163]\n"
EX"v42,v42\n"
MF"v[192:195],a[80:83],v[76:79],v[192:195]\n"
MF"v[100:103],a[84:87],v[64:67],v[100:103]\n"
EX"v43,v43\n"
MF"v[132:135],a[84:87],v[68:71],v[132:135]\n"
RD"a[100:103],v227 offset:25600\n"
MF"v[164:167],a[84:87],v[72:75],v[164:167]\n"
EX"v48,v48\n"
MF"v[196:199],a[84:87],v[76:79],v[196:199]\n"
WT"lgkmcnt(2)\n"
BL"v225,s[8:11],s43 offen offset:3072 sc1 lds\n"
MF"v[104:107],a[88:91],v[64:67],v[104:107]\n"
EX"v49,v49\n"
MF"v[136:139],a[88:91],v[68:71],v[136:139]\n"
RD"a[104:107],v227 offset:26624\n"
MF"v[168:171],a[88:91],v[72:75],v[168:171]\n"
EX"v50,v50\n"
MF"v[200:203],a[88:91],v[76:79],v[200:203]\n"
SA"m0,s40,16384\n"
MF"v[108:111],a[92:95],v[64:67],v[108:111]\n"
EX"v51,v51\n"
MF"v[140:143],a[92:95],v[68:71],v[140:143]\n"
RD"a[108:111],v227 offset:27648\n"
MF"v[172:175],a[92:95],v[72:75],v[172:175]\n"
EX"v56,v56\n"
MF"v[204:207],a[92:95],v[76:79],v[204:207]\n"
BL"v225,s[8:11],s44 offen offset:0 sc1 lds\n"
MF"a[132:135],a[128:131],v[64:67],a[132:135]\n"
SA"s40,s40,32768\n"
EX"v57,v57\n"
MF"a[136:139],a[128:131],v[68:71],a[136:139]\n"
"s_cmp_eq_u32 s40,s46\n"
RD"a[112:115],v227 offset:28672\n"
"s_cselect_b32 s40,s47,s40\n"
MF"a[140:143],a[128:131],v[72:75],a[140:143]\n"
EX"v58,v58\n"
MF"a[144:147],a[128:131],v[76:79],a[144:147]\n"
WT"lgkmcnt(2)\n"
BL"v[240:243],v238,s[8:11],0 offen sc1\n"
MF"v[80:83],a[96:99],v[0:3],v[80:83]\n"
EX"v59,v59\n"
MF"v[112:115],a[96:99],v[8:11],v[112:115]\n"
RD"a[116:119],v227 offset:29696\n"
MF"v[144:147],a[96:99],v[16:19],v[144:147]\n"
EX"v36,v36\n"
MF"v[176:179],a[96:99],v[24:27],v[176:179]\n"
BL"v225,s[8:11],s44 offen offset:1024 sc1 lds\n"
MF"v[84:87],a[100:103],v[0:3],v[84:87]\n"
EX"v37,v37\n"
MF"v[116:119],a[100:103],v[8:11],v[116:119]\n"
RD"a[120:123],v227 offset:30720\n"
MF"v[148:151],a[100:103],v[16:19],v[148:151]\n"
EX"v38,v38\n"
MF"v[180:183],a[100:103],v[24:27],v[180:183]\n"
MF"v[88:91],a[104:107],v[0:3],v[88:91]\n"
EX"v39,v39\n"
MF"v[120:123],a[104:107],v[8:11],v[120:123]\n"
RD"a[124:127],v227 offset:31744\n"
MF"v[152:155],a[104:107],v[16:19],v[152:155]\n"
EX"v44,v44\n"
MF"v[184:187],a[104:107],v[24:27],v[184:187]\n"
WT"lgkmcnt(2)\n"
BL"v225,s[8:11],s44 offen offset:2048 sc1 lds\n"
MF"v[92:95],a[108:111],v[0:3],v[92:95]\n"
EX"v45,v45\n"
MF"v[124:127],a[108:111],v[8:11],v[124:127]\n"
MF"v[156:159],a[108:111],v[16:19],v[156:159]\n"
EX"v46,v46\n"
MF"v[188:191],a[108:111],v[24:27],v[188:191]\n"
MF"v[96:99],a[112:115],v[0:3],v[96:99]\n"
WT"vmcnt(23) lgkmcnt(0)\n"
"s_barrier\n"
MF"v[128:131],a[112:115],v[8:11],v[128:131]\n"
RD"a[64:67],v226 offset:0\n"
MF"v[160:163],a[112:115],v[16:19],v[160:163]\n"
EX"v47,v47\n"
MF"v[192:195],a[112:115],v[24:27],v[192:195]\n"
BL"v225,s[8:11],s44 offen offset:3072 sc1 lds\n"
MF"v[100:103],a[116:119],v[0:3],v[100:103]\n"
EX"v52,v52\n"
MF"v[132:135],a[116:119],v[8:11],v[132:135]\n"
RD"a[68:71],v226 offset:1024\n"
MF"v[164:167],a[116:119],v[16:19],v[164:167]\n"
EX"v53,v53\n"
MF"v[196:199],a[116:119],v[24:27],v[196:199]\n"
RD"a[72:75],v226 offset:2048\n"
MF"v[104:107],a[120:123],v[0:3],v[104:107]\n"
EX"v54,v54\n"
MF"v[136:139],a[120:123],v[8:11],v[136:139]\n"
RD"a[76:79],v226 offset:3072\n"
MF"v[168:171],a[120:123],v[16:19],v[168:171]\n"
EX"v55,v55\n"
MF"v[200:203],a[120:123],v[24:27],v[200:203]\n"
MF"v[108:111],a[124:127],v[0:3],v[108:111]\n"
EX"v60,v60\n"
MF"v[140:143],a[124:127],v[8:11],v[140:143]\n"
RD"a[80:83],v226 offset:4096\n"
MF"v[172:175],a[124:127],v[16:19],v[172:175]\n"
EX"v61,v61\n"
MF"v[204:207],a[124:127],v[24:27],v[204:207]\n"
RD"a[84:87],v226 offset:5120\n"
MF"a[132:135],a[128:131],v[0:3],a[132:135]\n"
EX"v62,v62\n"
MF"a[136:139],a[128:131],v[8:11],a[136:139]\n"
RD"a[88:91],v226 offset:6144\n"
MF"a[140:143],a[128:131],v[16:19],a[140:143]\n"
EX"v63,v63\n"
MF"a[144:147],a[128:131],v[24:27],a[144:147]\n"
WT"lgkmcnt(6)\n"
MF"v[0:3],a[64:67],a[0:3],0\n"
WT"lgkmcnt(2)\n"
"v_lshrrev_b32 v237,1,v224\n"
PK"v64,v32,v33\n"
MF"v[0:3],a[68:71],a[4:7],v[0:3]\n"
SA"s95,s40,32768\n"
"v_and_b32 v237,0x78,v237\n"
PK"v65,v34,v35\n"
MF"v[8:11],a[68:71],a[20:23],0\n"
"s_cmp_eq_u32 s95,s46\n"
"v_and_b32 v239,0x300,v224\n"
PK"v66,v36,v37\n"
MF"v[8:11],a[64:67],a[16:19],v[8:11]\n"
"s_cselect_b32 s95,s47,s95\n"
"v_add3_u32 v237,v237,v239,s40\n"
PK"v67,v38,v39\n"
MF"v[16:19],a[64:67],a[32:35],0\n"
SA"m0,s95,0\n"
PK"v68,v40,v41\n"
PK"v69,v42,v43\n"
MF"v[16:19],a[68:71],a[36:39],v[16:19]\n"
RD"a[92:95],v226 offset:7168\n"
PK"v70,v44,v45\n"
MF"v[24:27],a[68:71],a[52:55],0\n"
PK"v71,v46,v47\n"
PK"v72,v48,v49\n"
MF"v[24:27],a[64:67],a[48:51],v[24:27]\n"
BL"v235,s[24:27],s52 offen lds\n"
MF"v[0:3],a[72:75],a[8:11],v[0:3]\n"
SA"s52,s52,s62\n"
PK"v73,v50,v51\n"
"v_mov_b32 v227,v226\n"
MF"v[0:3],a[76:79],a[12:15],v[0:3]\n"
PK"v74,v52,v53\n"
PK"v75,v54,v55\n"
MF"v[8:11],a[76:79],a[28:31],v[8:11]\n"
PK"v76,v56,v57\n"
PK"v77,v58,v59\n"
MF"v[8:11],a[72:75],a[24:27],v[8:11]\n"
WT"vmcnt(4)\n"
"v_cmp_eq_u64 vcc,s[28:29],v[240:241]\n"
"v_cmp_eq_u64_e64 s[50:51],s[28:29],v[242:243]\n"
"s_and_b64 vcc,vcc,s[50:51]\n"
"s_cmp_eq_u64 vcc,exec\n"
"s_cbranch_scc0 L1099_%=\n"
"L1100_%=:\n"
MF"v[16:19],a[72:75],a[40:43],v[16:19]\n"
SA"m0,s95,1024\n"
PK"v78,v60,v61\n"
PK"v79,v62,v63\n"
MF"v[16:19],a[76:79],a[44:47],v[16:19]\n"
RD"a[96:99],v226 offset:8192\n"
MF"v[24:27],a[76:79],a[60:63],v[24:27]\n"
EX"v0,v0\n"
MF"v[24:27],a[72:75],a[56:59],v[24:27]\n"
BL"v235,s[24:27],s52 offen lds\n"
MF"v[4:7],a[80:83],a[0:3],0\n"
WT"lgkmcnt(1)\n"
EX"v1,v1\n"
MF"v[4:7],a[84:87],a[4:7],v[4:7]\n"
SA"s52,s52,s62\n"
RD"a[100:103],v226 offset:9216\n"
PK"v0,v0,v1\n"
MF"v[12:15],a[84:87],a[20:23],0\n"
EX"v2,v2\n"
MF"v[12:15],a[80:83],a[16:19],v[12:15]\n"
EX"v3,v3\n"
MF"v[20:23],a[80:83],a[32:35],0\n"
SA"m0,s95,2048\n"
EX"v8,v8\n"
MF"v[20:23],a[84:87],a[36:39],v[20:23]\n"
RD"a[104:107],v226 offset:10240\n"
PK"v1,v2,v3\n"
MF"v[28:31],a[84:87],a[52:55],0\n"
EX"v9,v9\n"
MF"v[28:31],a[80:83],a[48:51],v[28:31]\n"
BL"v235,s[24:27],s52 offen lds\n"
MF"v[4:7],a[88:91],a[8:11],v[4:7]\n"
SA"s52,s52,s62\n"
EX"v10,v10\n"
MF"v[4:7],a[92:95],a[12:15],v[4:7]\n"
RD"a[108:111],v226 offset:11264\n"
PK"v8,v8,v9\n"
MF"v[12:15],a[92:95],a[28:31],v[12:15]\n"
EX"v11,v11\n"
MF"v[12:15],a[88:91],a[24:27],v[12:15]\n"
RT"a[148:149],v237 offset:0\n"
RT"a[150:151],v237 offset:128\n"
MF"v[20:23],a[88:91],a[40:43],v[20:23]\n"
SA"m0,s95,3072\n"
EX"v16,v16\n"
MF"v[20:23],a[92:95],a[44:47],v[20:23]\n"
EX"v17,v17\n"
MF"v[28:31],a[92:95],a[60:63],v[28:31]\n"
RD"a[112:115],v226 offset:12288\n"
PK"v9,v10,v11\n"
MF"v[28:31],a[88:91],a[56:59],v[28:31]\n"
WT"lgkmcnt(4)\n"
EX"v18,v18\n"
MF"v[32:35],a[96:99],a[0:3],0\n"
RT"a[152:153],v237 offset:1024\n"
RT"a[154:155],v237 offset:1152\n"
MF"v[32:35],a[100:103],a[4:7],v[32:35]\n"
BL"v235,s[24:27],s52 offen lds\n"
MF"v[40:43],a[100:103],a[20:23],0\n"
SA"s52,s52,s62\n"
EX"v19,v19\n"
MF"v[40:43],a[96:99],a[16:19],v[40:43]\n"
RD"a[116:119],v226 offset:13312\n"
RT"a[156:157],v237 offset:2048\n"
MF"v[48:51],a[96:99],a[32:35],0\n"
SA"m0,s95,16384\n"
EX"v24,v24\n"
MF"v[48:51],a[100:103],a[36:39],v[48:51]\n"
RT"a[158:159],v237 offset:2176\n"
RD"a[120:123],v226 offset:14336\n"
EX"v25,v25\n"
MF"v[56:59],a[100:103],a[52:55],0\n"
EX"v26,v26\n"
PK"v16,v16,v17\n"
MF"v[56:59],a[96:99],a[48:51],v[56:59]\n"
EX"v27,v27\n"
MF"v[32:35],a[104:107],a[8:11],v[32:35]\n"
WT"lgkmcnt(4)\n"
RT"a[160:161],v237 offset:3072\n"
RT"a[162:163],v237 offset:3200\n"
MF"v[32:35],a[108:111],a[12:15],v[32:35]\n"
BL"v235,s[24:27],s52 offen lds\n"
PK"v17,v18,v19\n"
MF"v[40:43],a[108:111],a[28:31],v[40:43]\n"
SA"s52,s52,s62\n"
EX"v4,v4\n"
MF"v[40:43],a[104:107],a[24:27],v[40:43]\n"
RD"a[124:127],v226 offset:15360\n"
RT"a[164:165],v237 offset:16384\n"
MF"v[48:51],a[104:107],a[40:43],v[48:51]\n"
SA"m0,s95,17408\n"
EX"v5,v5\n"
PK"v24,v24,v25\n"
MF"v[48:51],a[108:111],a[44:47],v[48:51]\n"
RT"a[166:167],v237 offset:16512\n"
PK"v25,v26,v27\n"
EX"v6,v6\n"
MF"v[56:59],a[108:111],a[60:63],v[56:59]\n"
EX"v7,v7\n"
MF"v[56:59],a[104:107],a[56:59],v[56:59]\n"
EX"v12,v12\n"
MF"v[36:39],a[112:115],a[0:3],0\n"
WT"lgkmcnt(3)\n"
RT"a[168:169],v237 offset:17408\n"
RT"a[170:171],v237 offset:17536\n"
MF"v[36:39],a[116:119],a[4:7],v[36:39]\n"
BL"v235,s[24:27],s52 offen lds\n"
MF"v[44:47],a[116:119],a[20:23],0\n"
SA"s52,s52,s62\n"
EX"v13,v13\n"
MF"v[44:47],a[112:115],a[16:19],v[44:47]\n"
RD"a[64:67],v227 offset:16384\n"
RT"a[172:173],v237 offset:18432\n"
PK"v2,v4,v5\n"
MF"v[52:55],a[112:115],a[32:35],0\n"
SA"m0,s95,18432\n"
EX"v14,v14\n"
MF"v[52:55],a[116:119],a[36:39],v[52:55]\n"
RT"a[174:175],v237 offset:18560\n"
RD"a[68:71],v227 offset:17408\n"
PK"v3,v6,v7\n"
MF"v[60:63],a[116:119],a[52:55],0\n"
EX"v15,v15\n"
MF"v[60:63],a[112:115],a[48:51],v[60:63]\n"
EX"v20,v20\n"
MF"v[36:39],a[120:123],a[8:11],v[36:39]\n"
WT"lgkmcnt(4)\n"
BL"v235,s[24:27],s52 offen lds\n"
MF"v[36:39],a[124:127],a[12:15],v[36:39]\n"
SA"s52,s52,s62\n"
EX"v21,v21\n"
PK"v10,v12,v13\n"
MF"v[44:47],a[124:127],a[28:31],v[44:47]\n"
"s_cmp_gt_u32 s92,0\n"
RD"a[72:75],v227 offset:18432\n"
"v_readlane_b32 s43,v228,s42\n"
MF"v[44:47],a[120:123],a[24:27],v[44:47]\n"
"s_cselect_b32 s63,s63,1065353216\n"
EX"v22,v22\n"
MF"v[52:55],a[120:123],a[40:43],v[52:55]\n"
"s_mov_b32 m0,s40\n"
BL"a[204:207],v236,s[8:11],s63 offen offset:0 sc1\n"
MF"v[52:55],a[124:127],a[44:47],v[52:55]\n"
EX"v23,v23\n"
PK"v11,v14,v15\n"
MF"v[60:63],a[124:127],a[60:63],v[60:63]\n"
RD"a[76:79],v227 offset:19456\n"
"v_readlane_b32 s44,v229,s42\n"
MF"v[60:63],a[120:123],a[56:59],v[60:63]\n"
WT"lgkmcnt(2)\n"
EX"v28,v28\n"
MF"v[80:83],a[64:67],v[64:67],v[80:83]\n"
SA"s42,s42,1\n"
BL"v225,s[8:11],s43 offen offset:0 sc1 lds\n"
MF"v[112:115],a[64:67],v[68:71],v[112:115]\n"
SA"s41,s41,32768\n"
EX"v29,v29\n"
PK"v18,v20,v21\n"
MF"v[144:147],a[64:67],v[72:75],v[144:147]\n"
"s_cmp_eq_u32 s41,163840\n"
RD"a[80:83],v227 offset:20480\n"
PK"v19,v22,v23\n"
MF"v[176:179],a[64:67],v[76:79],v[176:179]\n"
"s_cselect_b32 s41,0,s41\n"
EX"v30,v30\n"
MF"v[84:87],a[68:71],v[64:67],v[84:87]\n"
BL"a[208:211],v236,s[8:11],s63 offen offset:1024 sc1\n"
MF"v[116:119],a[68:71],v[68:71],v[116:119]\n"
EX"v31,v31\n"
MF"v[148:151],a[68:71],v[72:75],v[148:151]\n"
RD"a[84:87],v227 offset:21504\n"
"v_add_u32 v226,s41,v224\n"
MF"v[180:183],a[68:71],v[76:79],v[180:183]\n"
WT"lgkmcnt(2)\n"
EX"v32,v32\n"
MF"v[88:91],a[72:75],v[64:67],v[88:91]\n"
BL"v225,s[8:11],s43 offen offset:1024 sc1 lds\n"
MF"v[120:123],a[72:75],v[68:71],v[120:123]\n"
EX"v33,v33\n"
PK"v26,v28,v29\n"
MF"v[152:155],a[72:75],v[72:75],v[152:155]\n"
RD"a[88:91],v227 offset:22528\n"
PK"v27,v30,v31\n"
MF"v[184:187],a[72:75],v[76:79],v[184:187]\n"
EX"v34,v34\n"
MF"v[92:95],a[76:79],v[64:67],v[92:95]\n"
BL"a[212:215],v236,s[8:11],s63 offen offset:2048 sc1\n"
MF"v[124:127],a[76:79],v[68:71],v[124:127]\n"
EX"v35,v35\n"
MF"v[156:159],a[76:79],v[72:75],v[156:159]\n"
RD"a[92:95],v227 offset:23552\n"
MF"v[188:191],a[76:79],v[76:79],v[188:191]\n"
WT"lgkmcnt(2)\n"
EX"v40,v40\n"
MF"v[96:99],a[80:83],v[64:67],v[96:99]\n"
BL"v225,s[8:11],s43 offen offset:2048 sc1 lds\n"
MF"v[128:131],a[80:83],v[68:71],v[128:131]\n"
EX"v41,v41\n"
MF"v[160:163],a[80:83],v[72:75],v[160:163]\n"
RD"a[96:99],v227 offset:24576\n"
MF"v[192:195],a[80:83],v[76:79],v[192:195]\n"
EX"v42,v42\n"
MF"v[100:103],a[84:87],v[64:67],v[100:103]\n"
BL"a[216:219],v236,s[8:11],s63 offen offset:3072 sc1\n"
MF"v[132:135],a[84:87],v[68:71],v[132:135]\n"
EX"v43,v43\n"
MF"v[164:167],a[84:87],v[72:75],v[164:167]\n"
RD"a[100:103],v227 offset:25600\n"
MF"v[196:199],a[84:87],v[76:79],v[196:199]\n"
WT"lgkmcnt(2)\n"
EX"v48,v48\n"
MF"v[104:107],a[88:91],v[64:67],v[104:107]\n"
SA"s63,s63,4096\n"
BL"v225,s[8:11],s43 offen offset:3072 sc1 lds\n"
MF"v[136:139],a[88:91],v[68:71],v[136:139]\n"
EX"v49,v49\n"
MF"v[168:171],a[88:91],v[72:75],v[168:171]\n"
RD"a[104:107],v227 offset:26624\n"
MF"v[200:203],a[88:91],v[76:79],v[200:203]\n"
SA"m0,s40,16384\n"
EX"v50,v50\n"
MF"v[108:111],a[92:95],v[64:67],v[108:111]\n"
BL"a[220:223],v236,s[8:11],s63 offen offset:0 sc1\n"
MF"v[140:143],a[92:95],v[68:71],v[140:143]\n"
EX"v51,v51\n"
MF"v[172:175],a[92:95],v[72:75],v[172:175]\n"
RD"a[108:111],v227 offset:27648\n"
MF"v[204:207],a[92:95],v[76:79],v[204:207]\n"
EX"v56,v56\n"
MF"a[132:135],a[128:131],v[64:67],a[132:135]\n"
SA"s40,s40,32768\n"
BL"v225,s[8:11],s44 offen offset:0 sc1 lds\n"
MF"a[136:139],a[128:131],v[68:71],a[136:139]\n"
"s_cmp_eq_u32 s40,s46\n"
EX"v57,v57\n"
MF"a[140:143],a[128:131],v[72:75],a[140:143]\n"
"s_cselect_b32 s40,s47,s40\n"
RD"a[112:115],v227 offset:28672\n"
MF"a[144:147],a[128:131],v[76:79],a[144:147]\n"
WT"lgkmcnt(2)\n"
EX"v58,v58\n"
MF"v[80:83],a[96:99],v[0:3],v[80:83]\n"
BL"a[224:227],v236,s[8:11],s63 offen offset:1024 sc1\n"
MF"v[112:115],a[96:99],v[8:11],v[112:115]\n"
EX"v59,v59\n"
MF"v[144:147],a[96:99],v[16:19],v[144:147]\n"
RD"a[116:119],v227 offset:29696\n"
MF"v[176:179],a[96:99],v[24:27],v[176:179]\n"
EX"v36,v36\n"
MF"v[84:87],a[100:103],v[0:3],v[84:87]\n"
BL"v225,s[8:11],s44 offen offset:1024 sc1 lds\n"
MF"v[116:119],a[100:103],v[8:11],v[116:119]\n"
EX"v37,v37\n"
MF"v[148:151],a[100:103],v[16:19],v[148:151]\n"
RD"a[120:123],v227 offset:30720\n"
MF"v[180:183],a[100:103],v[24:27],v[180:183]\n"
EX"v38,v38\n"
MF"v[88:91],a[104:107],v[0:3],v[88:91]\n"
BL"a[228:231],v236,s[8:11],s63 offen offset:2048 sc1\n"
MF"v[120:123],a[104:107],v[8:11],v[120:123]\n"
EX"v39,v39\n"
MF"v[152:155],a[104:107],v[16:19],v[152:155]\n"
RD"a[124:127],v227 offset:31744\n"
MF"v[184:187],a[104:107],v[24:27],v[184:187]\n"
WT"lgkmcnt(2)\n"
EX"v44,v44\n"
MF"v[92:95],a[108:111],v[0:3],v[92:95]\n"
BL"v225,s[8:11],s44 offen offset:2048 sc1 lds\n"
MF"v[124:127],a[108:111],v[8:11],v[124:127]\n"
EX"v45,v45\n"
MF"v[156:159],a[108:111],v[16:19],v[156:159]\n"
EX"v46,v46\n"
MF"v[188:191],a[108:111],v[24:27],v[188:191]\n"
BL"a[232:235],v236,s[8:11],s63 offen offset:3072 sc1\n"
MF"v[96:99],a[112:115],v[0:3],v[96:99]\n"
WT"lgkmcnt(0)\n"
"s_barrier\n"
MF"v[128:131],a[112:115],v[8:11],v[128:131]\n"
RD"a[64:67],v226 offset:0\n"
MF"v[160:163],a[112:115],v[16:19],v[160:163]\n"
EX"v47,v47\n"
MF"v[192:195],a[112:115],v[24:27],v[192:195]\n"
SA"s63,s63,4096\n"
BL"v225,s[8:11],s44 offen offset:3072 sc1 lds\n"
MF"v[100:103],a[116:119],v[0:3],v[100:103]\n"
EX"v52,v52\n"
MF"v[132:135],a[116:119],v[8:11],v[132:135]\n"
RD"a[68:71],v226 offset:1024\n"
MF"v[164:167],a[116:119],v[16:19],v[164:167]\n"
EX"v53,v53\n"
MF"v[196:199],a[116:119],v[24:27],v[196:199]\n"
BL"a[236:239],v236,s[8:11],s63 offen offset:0 sc1\n"
MF"v[104:107],a[120:123],v[0:3],v[104:107]\n"
RD"a[72:75],v226 offset:2048\n"
MF"v[136:139],a[120:123],v[8:11],v[136:139]\n"
EX"v54,v54\n"
MF"v[168:171],a[120:123],v[16:19],v[168:171]\n"
RD"a[76:79],v226 offset:3072\n"
MF"v[200:203],a[120:123],v[24:27],v[200:203]\n"
EX"v55,v55\n"
MF"v[108:111],a[124:127],v[0:3],v[108:111]\n"
EX"v60,v60\n"
MF"v[140:143],a[124:127],v[8:11],v[140:143]\n"
RD"a[80:83],v226 offset:4096\n"
MF"v[172:175],a[124:127],v[16:19],v[172:175]\n"
EX"v61,v61\n"
MF"v[204:207],a[124:127],v[24:27],v[204:207]\n"
RD"a[84:87],v226 offset:5120\n"
MF"a[132:135],a[128:131],v[0:3],a[132:135]\n"
EX"v62,v62\n"
MF"a[136:139],a[128:131],v[8:11],a[136:139]\n"
RD"a[88:91],v226 offset:6144\n"
MF"a[140:143],a[128:131],v[16:19],a[140:143]\n"
EX"v63,v63\n"
MF"a[144:147],a[128:131],v[24:27],a[144:147]\n"
WT"lgkmcnt(6)\n"
MF"v[0:3],a[64:67],a[0:3],0\n"
WT"lgkmcnt(2)\n"
"v_lshrrev_b32 v237,1,v224\n"
PK"v64,v32,v33\n"
MF"v[0:3],a[68:71],a[4:7],v[0:3]\n"
SA"s95,s40,32768\n"
"v_and_b32 v237,0x78,v237\n"
PK"v65,v34,v35\n"
MF"v[8:11],a[68:71],a[20:23],0\n"
"s_cmp_eq_u32 s95,s46\n"
"v_and_b32 v239,0x300,v224\n"
PK"v66,v36,v37\n"
MF"v[8:11],a[64:67],a[16:19],v[8:11]\n"
"s_cselect_b32 s95,s47,s95\n"
BL"a[240:243],v236,s[8:11],s63 offen offset:1024 sc1\n"
MF"v[16:19],a[64:67],a[32:35],0\n"
"s_sub_u32 s95,s95,s47\n"
"v_add3_u32 v237,v237,v239,s40\n"
PK"v67,v38,v39\n"
MF"v[16:19],a[68:71],a[36:39],v[16:19]\n"
"s_lshr_b32 s53,s47,2\n"
RD"a[92:95],v226 offset:7168\n"
PK"v68,v40,v41\n"
MF"v[24:27],a[68:71],a[52:55],0\n"
"s_cmp_gt_u32 s92,10\n"
PK"v69,v42,v43\n"
PK"v70,v44,v45\n"
MF"v[24:27],a[64:67],a[48:51],v[24:27]\n"
"s_cselect_b32 s63,s63,1065353216\n"
PK"v71,v46,v47\n"
"v_add_u32 v235,s95,v224\n"
MF"v[0:3],a[72:75],a[8:11],v[0:3]\n"
"s_lshl_b32 s50,s35,6\n"
BL"a[244:247],v236,s[8:11],s63 offen offset:2048 sc1\n"
MF"v[0:3],a[76:79],a[12:15],v[0:3]\n"
SA"s50,s50,4352\n"
PK"v72,v48,v49\n"
PK"v73,v50,v51\n"
MF"v[8:11],a[76:79],a[28:31],v[8:11]\n"
"v_mov_b32 v227,v226\n"
PK"v74,v52,v53\n"
MF"v[8:11],a[72:75],a[24:27],v[8:11]\n"
BL"a[248:251],v236,s[8:11],s63 offen offset:3072 sc1\n"
MF"v[16:19],a[72:75],a[40:43],v[16:19]\n"
SA"s63,s63,4096\n"
PK"v75,v54,v55\n"
PK"v76,v56,v57\n"
MF"v[16:19],a[76:79],a[44:47],v[16:19]\n"
RD"a[96:99],v226 offset:8192\n"
PK"v77,v58,v59\n"
MF"v[24:27],a[76:79],a[60:63],v[24:27]\n"
EX"v0,v0\n"
MF"v[24:27],a[72:75],a[56:59],v[24:27]\n"
BL"a[252:255],v236,s[8:11],s63 offen offset:0 sc1\n"
PK"v78,v60,v61\n"
MF"v[4:7],a[80:83],a[0:3],0\n"
WT"lgkmcnt(1)\n"
EX"v1,v1\n"
PK"v79,v62,v63\n"
MF"v[4:7],a[84:87],a[4:7],v[4:7]\n"
"s_cmp_gt_u32 s92,13\n"
RD"a[100:103],v226 offset:9216\n"
"v_lshrrev_b32 v238,1,v224\n"
"v_and_b32 v238,56,v238\n"
MF"v[12:15],a[84:87],a[20:23],0\n"
"s_cselect_b32 s63,s63,1065353216\n"
EX"v2,v2\n"
MF"v[12:15],a[80:83],a[16:19],v[12:15]\n"
BL"v[244:247],v236,s[8:11],s63 offen offset:1024 sc1\n"
"v_add_u32 v236,s53,v235\n"
MF"v[20:23],a[80:83],a[32:35],0\n"
EX"v3,v3\n"
MF"v[20:23],a[84:87],a[36:39],v[20:23]\n"
EX"v8,v8\n"
MF"v[28:31],a[84:87],a[52:55],0\n"
RD"a[104:107],v226 offset:10240\n"
PK"v0,v0,v1\n"
MF"v[28:31],a[80:83],a[48:51],v[28:31]\n"
EX"v9,v9\n"
MF"v[4:7],a[88:91],a[8:11],v[4:7]\n"
"buffer_load_dwordx2 v[240:241],v238,s[8:11],s50 offen sc1\n"
MF"v[4:7],a[92:95],a[12:15],v[4:7]\n"
EX"v10,v10\n"
MF"v[12:15],a[92:95],a[28:31],v[12:15]\n"
RD"a[108:111],v226 offset:11264\n"
PK"v1,v2,v3\n"
MF"v[12:15],a[88:91],a[24:27],v[12:15]\n"
EX"v11,v11\n"
MF"v[20:23],a[88:91],a[40:43],v[20:23]\n"
WT"vmcnt(23)\n"
RT"a[176:177],v237 offset:0\n"
RT"a[178:179],v237 offset:128\n"
MF"v[20:23],a[92:95],a[44:47],v[20:23]\n"
EX"v16,v16\n"
MF"v[28:31],a[92:95],a[60:63],v[28:31]\n"
EX"v17,v17\n"
MF"v[28:31],a[88:91],a[56:59],v[28:31]\n"
WT"lgkmcnt(3)\n"
RD"a[112:115],v226 offset:12288\n"
RT"a[180:181],v237 offset:1024\n"
PK"v8,v8,v9\n"
MF"v[32:35],a[96:99],a[0:3],0\n"
EX"v18,v18\n"
MF"v[32:35],a[100:103],a[4:7],v[32:35]\n"
RT"a[182:183],v237 offset:1152\n"
RD"a[116:119],v226 offset:13312\n"
MF"v[40:43],a[100:103],a[20:23],0\n"
EX"v19,v19\n"
MF"v[40:43],a[96:99],a[16:19],v[40:43]\n"
EX"v24,v24\n"
PK"v9,v10,v11\n"
MF"v[48:51],a[96:99],a[32:35],0\n"
RT"a[184:185],v237 offset:2048\n"
RT"a[186:187],v237 offset:2176\n"
MF"v[48:51],a[100:103],a[36:39],v[48:51]\n"
EX"v25,v25\n"
PK"v16,v16,v17\n"
MF"v[56:59],a[100:103],a[52:55],0\n"
EX"v26,v26\n"
MF"v[56:59],a[96:99],a[48:51],v[56:59]\n"
RD"a[120:123],v226 offset:14336\n"
RT"a[188:189],v237 offset:3072\n"
MF"v[32:35],a[104:107],a[8:11],v[32:35]\n"
WT"lgkmcnt(4)\n"
EX"v27,v27\n"
MF"v[32:35],a[108:111],a[12:15],v[32:35]\n"
RT"a[190:191],v237 offset:3200\n"
RD"a[124:127],v226 offset:15360\n"
PK"v17,v18,v19\n"
MF"v[40:43],a[108:111],a[28:31],v[40:43]\n"
EX"v4,v4\n"
MF"v[40:43],a[104:107],a[24:27],v[40:43]\n"
EX"v5,v5\n"
MF"v[48:51],a[104:107],a[40:43],v[48:51]\n"
RT"a[192:193],v237 offset:16384\n"
RT"a[194:195],v237 offset:16512\n"
PK"v24,v24,v25\n"
MF"v[48:51],a[108:111],a[44:47],v[48:51]\n"
EX"v6,v6\n"
MF"v[56:59],a[108:111],a[60:63],v[56:59]\n"
EX"v7,v7\n"
MF"v[56:59],a[104:107],a[56:59],v[56:59]\n"
EX"v12,v12\n"
PK"v25,v26,v27\n"
MF"v[36:39],a[112:115],a[0:3],0\n"
RT"a[196:197],v237 offset:17408\n"
RT"a[198:199],v237 offset:17536\n"
MF"v[36:39],a[116:119],a[4:7],v[36:39]\n"
EX"v13,v13\n"
MF"v[44:47],a[116:119],a[20:23],0\n"
RD"a[64:67],v227 offset:16384\n"
PK"v2,v4,v5\n"
MF"v[44:47],a[112:115],a[16:19],v[44:47]\n"
EX"v14,v14\n"
MF"v[52:55],a[112:115],a[32:35],0\n"
RT"a[200:201],v237 offset:18432\n"
RT"a[202:203],v237 offset:18560\n"
MF"v[52:55],a[116:119],a[36:39],v[52:55]\n"
EX"v15,v15\n"
PK"v3,v6,v7\n"
MF"v[60:63],a[116:119],a[52:55],0\n"
RD"a[68:71],v227 offset:17408\n"
PK"v10,v12,v13\n"
MF"v[60:63],a[112:115],a[48:51],v[60:63]\n"
WT"lgkmcnt(4)\n"
EX"v20,v20\n"
MF"v[36:39],a[120:123],a[8:11],v[36:39]\n"
EX"v21,v21\n"
MF"v[36:39],a[124:127],a[12:15],v[36:39]\n"
RD"a[72:75],v227 offset:18432\n"
"v_readlane_b32 s43,v228,s42\n"
MF"v[44:47],a[124:127],a[28:31],v[44:47]\n"
EX"v22,v22\n"
MF"v[44:47],a[120:123],a[24:27],v[44:47]\n"
"s_mov_b32 m0,s40\n"
PK"v11,v14,v15\n"
PK"v18,v20,v21\n"
MF"v[52:55],a[120:123],a[40:43],v[52:55]\n"
EX"v23,v23\n"
MF"v[52:55],a[124:127],a[44:47],v[52:55]\n"
RD"a[76:79],v227 offset:19456\n"
"v_readlane_b32 s44,v229,s42\n"
MF"v[60:63],a[124:127],a[60:63],v[60:63]\n"
EX"v28,v28\n"
MF"v[60:63],a[120:123],a[56:59],v[60:63]\n"
WT"lgkmcnt(2)\n"
BL"v225,s[8:11],s43 offen offset:0 sc1 lds\n"
MF"v[80:83],a[64:67],v[64:67],v[80:83]\n"
SA"s42,s42,1\n"
EX"v29,v29\n"
MF"v[112:115],a[64:67],v[68:71],v[112:115]\n"
SA"s41,s41,32768\n"
RD"a[80:83],v227 offset:20480\n"
PK"v19,v22,v23\n"
MF"v[144:147],a[64:67],v[72:75],v[144:147]\n"
"s_cmp_eq_u32 s41,163840\n"
EX"v30,v30\n"
MF"v[176:179],a[64:67],v[76:79],v[176:179]\n"
"s_cselect_b32 s41,0,s41\n"
PK"v26,v28,v29\n"
"v_add_u32 v226,s41,v224\n"
MF"v[84:87],a[68:71],v[64:67],v[84:87]\n"
EX"v31,v31\n"
MF"v[116:119],a[68:71],v[68:71],v[116:119]\n"
RD"a[84:87],v227 offset:21504\n"
PK"v27,v30,v31\n"
MF"v[148:151],a[68:71],v[72:75],v[148:151]\n"
WT"vmcnt(1)\n"
"v_cmp_eq_u64 vcc,s[28:29],v[240:241]\n"
"s_cmp_eq_u64 vcc,exec\n"
"s_cbranch_scc0 L1102_%=\n"
"L1103_%=:\n"
MF"v[180:183],a[68:71],v[76:79],v[180:183]\n"
WT"lgkmcnt(2)\n"
EX"v32,v32\n"
MF"v[88:91],a[72:75],v[64:67],v[88:91]\n"
BL"v225,s[8:11],s43 offen offset:1024 sc1 lds\n"
MF"v[120:123],a[72:75],v[68:71],v[120:123]\n"
EX"v33,v33\n"
MF"v[152:155],a[72:75],v[72:75],v[152:155]\n"
RD"a[88:91],v227 offset:22528\n"
MF"v[184:187],a[72:75],v[76:79],v[184:187]\n"
EX"v34,v34\n"
MF"v[92:95],a[76:79],v[64:67],v[92:95]\n"
EX"v35,v35\n"
MF"v[124:127],a[76:79],v[68:71],v[124:127]\n"
RD"a[92:95],v227 offset:23552\n"
MF"v[156:159],a[76:79],v[72:75],v[156:159]\n"
EX"v40,v40\n"
MF"v[188:191],a[76:79],v[76:79],v[188:191]\n"
WT"lgkmcnt(2)\n"
BL"v225,s[8:11],s43 offen offset:2048 sc1 lds\n"
MF"v[96:99],a[80:83],v[64:67],v[96:99]\n"
EX"v41,v41\n"
MF"v[128:131],a[80:83],v[68:71],v[128:131]\n"
RD"a[96:99],v227 offset:24576\n"
MF"v[160:163],a[80:83],v[72:75],v[160:163]\n"
EX"v42,v42\n"
MF"v[192:195],a[80:83],v[76:79],v[192:195]\n"
MF"v[100:103],a[84:87],v[64:67],v[100:103]\n"
EX"v43,v43\n"
MF"v[132:135],a[84:87],v[68:71],v[132:135]\n"
RD"a[100:103],v227 offset:25600\n"
MF"v[164:167],a[84:87],v[72:75],v[164:167]\n"
EX"v48,v48\n"
MF"v[196:199],a[84:87],v[76:79],v[196:199]\n"
WT"lgkmcnt(2)\n"
BL"v225,s[8:11],s43 offen offset:3072 sc1 lds\n"
MF"v[104:107],a[88:91],v[64:67],v[104:107]\n"
EX"v49,v49\n"
MF"v[136:139],a[88:91],v[68:71],v[136:139]\n"
RD"a[104:107],v227 offset:26624\n"
MF"v[168:171],a[88:91],v[72:75],v[168:171]\n"
EX"v50,v50\n"
MF"v[200:203],a[88:91],v[76:79],v[200:203]\n"
SA"m0,s40,16384\n"
MF"v[108:111],a[92:95],v[64:67],v[108:111]\n"
EX"v51,v51\n"
MF"v[140:143],a[92:95],v[68:71],v[140:143]\n"
RD"a[108:111],v227 offset:27648\n"
MF"v[172:175],a[92:95],v[72:75],v[172:175]\n"
EX"v56,v56\n"
MF"v[204:207],a[92:95],v[76:79],v[204:207]\n"
BL"v225,s[8:11],s44 offen offset:0 sc1 lds\n"
MF"a[132:135],a[128:131],v[64:67],a[132:135]\n"
SA"s40,s40,32768\n"
EX"v57,v57\n"
MF"a[136:139],a[128:131],v[68:71],a[136:139]\n"
"s_cmp_eq_u32 s40,s46\n"
RD"a[112:115],v227 offset:28672\n"
"s_cselect_b32 s40,s47,s40\n"
MF"a[140:143],a[128:131],v[72:75],a[140:143]\n"
EX"v58,v58\n"
MF"a[144:147],a[128:131],v[76:79],a[144:147]\n"
MF"v[240:243],a[148:151],a[204:207],0\n"
EX"v59,v59\n"
MF"v[240:243],a[152:155],a[208:211],v[240:243]\n"
RD"a[116:119],v227 offset:29696\n"
MF"v[240:243],a[156:159],a[212:215],v[240:243]\n"
EX"v36,v36\n"
MF"v[240:243],a[160:163],a[216:219],v[240:243]\n"
BL"v225,s[8:11],s44 offen offset:1024 sc1 lds\n"
MF"v[240:243],a[164:167],a[220:223],v[240:243]\n"
EX"v37,v37\n"
MF"v[240:243],a[168:171],a[224:227],v[240:243]\n"
RD"a[120:123],v227 offset:30720\n"
MF"v[240:243],a[172:175],a[228:231],v[240:243]\n"
EX"v38,v38\n"
MF"v[240:243],a[176:179],a[232:235],v[240:243]\n"
MF"v[240:243],a[180:183],a[236:239],v[240:243]\n"
EX"v39,v39\n"
MF"v[240:243],a[184:187],a[240:243],v[240:243]\n"
RD"a[124:127],v227 offset:31744\n"
MF"v[240:243],a[188:191],a[244:247],v[240:243]\n"
EX"v44,v44\n"
MF"v[240:243],a[192:195],a[248:251],v[240:243]\n"
BL"v225,s[8:11],s44 offen offset:2048 sc1 lds\n"
MF"v[240:243],a[196:199],a[252:255],v[240:243]\n"
EX"v45,v45\n"
MF"v[240:243],a[200:203],v[244:247],v[240:243]\n"
WT"lgkmcnt(1)\n"
MF"v[80:83],a[96:99],v[0:3],v[80:83]\n"
EX"v46,v46\n"
MF"v[112:115],a[96:99],v[8:11],v[112:115]\n"
MF"v[144:147],a[96:99],v[16:19],v[144:147]\n"
MF"v[176:179],a[96:99],v[24:27],v[176:179]\n"
MF"v[84:87],a[100:103],v[0:3],v[84:87]\n"
EX"v47,v47\n"
MF"v[116:119],a[100:103],v[8:11],v[116:119]\n"
"ds_write_b128 v236,v[240:243]\n"
MF"v[148:151],a[100:103],v[16:19],v[148:151]\n"
BL"v225,s[8:11],s44 offen offset:3072 sc1 lds\n"
MF"v[180:183],a[100:103],v[24:27],v[180:183]\n"
EX"v52,v52\n"
MF"v[88:91],a[104:107],v[0:3],v[88:91]\n"
EX"v53,v53\n"
MF"v[120:123],a[104:107],v[8:11],v[120:123]\n"
MF"v[152:155],a[104:107],v[16:19],v[152:155]\n"
EX"v54,v54\n"
MF"v[184:187],a[104:107],v[24:27],v[184:187]\n"
MF"v[92:95],a[108:111],v[0:3],v[92:95]\n"
EX"v55,v55\n"
MF"v[124:127],a[108:111],v[8:11],v[124:127]\n"
MF"v[156:159],a[108:111],v[16:19],v[156:159]\n"
EX"v60,v60\n"
MF"v[188:191],a[108:111],v[24:27],v[188:191]\n"
MF"v[96:99],a[112:115],v[0:3],v[96:99]\n"
EX"v61,v61\n"
MF"v[128:131],a[112:115],v[8:11],v[128:131]\n"
WT"lgkmcnt(0)\n"
"s_barrier\n"
MF"v[160:163],a[112:115],v[16:19],v[160:163]\n"
RD"a[64:67],v226 offset:0\n"
RD"v[240:243],v235 offset:0\n"
MF"v[192:195],a[112:115],v[24:27],v[192:195]\n"
EX"v62,v62\n"
MF"v[100:103],a[116:119],v[0:3],v[100:103]\n"
EX"v63,v63\n"
MF"v[132:135],a[116:119],v[8:11],v[132:135]\n"
RD"v[244:247],v235 offset:1024\n"
RD"a[68:71],v226 offset:1024\n"
MF"v[164:167],a[116:119],v[16:19],v[164:167]\n"
RD"v[248:251],v235 offset:2048\n"
MF"v[196:199],a[116:119],v[24:27],v[196:199]\n"
RD"a[72:75],v226 offset:2048\n"
MF"v[104:107],a[120:123],v[0:3],v[104:107]\n"
RD"v[252:255],v235 offset:3072\n"
MF"v[136:139],a[120:123],v[8:11],v[136:139]\n"
RD"a[76:79],v226 offset:3072\n"
MF"v[168:171],a[120:123],v[16:19],v[168:171]\n"
MF"v[200:203],a[120:123],v[24:27],v[200:203]\n"
MF"v[108:111],a[124:127],v[0:3],v[108:111]\n"
MF"v[140:143],a[124:127],v[8:11],v[140:143]\n"
RD"a[80:83],v226 offset:4096\n"
MF"v[172:175],a[124:127],v[16:19],v[172:175]\n"
MF"v[204:207],a[124:127],v[24:27],v[204:207]\n"
RD"a[84:87],v226 offset:5120\n"
MF"a[132:135],a[128:131],v[0:3],a[132:135]\n"
MF"a[136:139],a[128:131],v[8:11],a[136:139]\n"
RD"a[88:91],v226 offset:6144\n"
MF"a[140:143],a[128:131],v[16:19],a[140:143]\n"
MF"a[144:147],a[128:131],v[24:27],a[144:147]\n"
"s_cmp_eq_u32 s57,2\n"
"s_cbranch_scc1 LMD_%=\n"
"s_cmp_eq_u32 s34,2\n"
"s_cbranch_scc1 LMA_%=\n"
WT"lgkmcnt(10)\n"
MF"v[0:3],a[64:67],a[0:3],0\n"
WT"lgkmcnt(2)\n"
"v_add_f32 v240,v240,v244\n"
PK"v64,v32,v33\n"
MF"v[0:3],a[68:71],a[4:7],v[0:3]\n"
PK"v65,v34,v35\n"
"v_add_f32 v248,v248,v252\n"
MF"v[8:11],a[68:71],a[20:23],0\n"
PK"v66,v36,v37\n"
PK"v67,v38,v39\n"
MF"v[8:11],a[64:67],a[16:19],v[8:11]\n"
"v_add_f32 v241,v241,v245\n"
PK"v68,v40,v41\n"
MF"v[16:19],a[64:67],a[32:35],0\n"
PK"v69,v42,v43\n"
"v_add_f32 v249,v249,v253\n"
MF"v[16:19],a[68:71],a[36:39],v[16:19]\n"
RD"a[92:95],v226 offset:7168\n"
PK"v70,v44,v45\n"
MF"v[24:27],a[68:71],a[52:55],0\n"
PK"v71,v46,v47\n"
"v_add_f32 v242,v242,v246\n"
MF"v[24:27],a[64:67],a[48:51],v[24:27]\n"
PK"v72,v48,v49\n"
PK"v73,v50,v51\n"
MF"v[0:3],a[72:75],a[8:11],v[0:3]\n"
"v_mov_b32 v227,v226\n"
"v_add_f32 v250,v250,v254\n"
MF"v[0:3],a[76:79],a[12:15],v[0:3]\n"
PK"v74,v52,v53\n"
PK"v75,v54,v55\n"
MF"v[8:11],a[76:79],a[28:31],v[8:11]\n"
"v_add_f32 v243,v243,v247\n"
PK"v76,v56,v57\n"
MF"v[8:11],a[72:75],a[24:27],v[8:11]\n"
PK"v77,v58,v59\n"
"v_add_f32 v251,v251,v255\n"
MF"v[16:19],a[72:75],a[40:43],v[16:19]\n"
PK"v78,v60,v61\n"
PK"v79,v62,v63\n"
MF"v[16:19],a[76:79],a[44:47],v[16:19]\n"
RD"a[96:99],v226 offset:8192\n"
PK"v240,v240,v240\n"
MF"v[24:27],a[76:79],a[60:63],v[24:27]\n"
EX"v0,v0\n"
MF"v[24:27],a[72:75],a[56:59],v[24:27]\n"
"v_lshlrev_b32 v240,16,v240\n"
PK"v241,v241,v241\n"
MF"v[4:7],a[80:83],a[0:3],0\n"
WT"lgkmcnt(1)\n"
EX"v1,v1\n"
MF"v[4:7],a[84:87],a[4:7],v[4:7]\n"
RD"a[100:103],v226 offset:9216\n"
"v_lshlrev_b32 v241,16,v241\n"
MF"v[12:15],a[84:87],a[20:23],0\n"
EX"v2,v2\n"
MF"v[12:15],a[80:83],a[16:19],v[12:15]\n"
EX"v3,v3\n"
MF"v[20:23],a[80:83],a[32:35],0\n"
EX"v8,v8\n"
PK"v242,v242,v242\n"
MF"v[20:23],a[84:87],a[36:39],v[20:23]\n"
RD"a[104:107],v226 offset:10240\n"
"v_lshlrev_b32 v242,16,v242\n"
MF"v[28:31],a[84:87],a[52:55],0\n"
EX"v9,v9\n"
PK"v243,v243,v243\n"
MF"v[28:31],a[80:83],a[48:51],v[28:31]\n"
"v_lshlrev_b32 v243,16,v243\n"
MU"v244,0xbfb8aa3b,v240\n"
EX"v244,v244\n"
MF"v[4:7],a[88:91],a[8:11],v[4:7]\n"
EX"v10,v10\n"
MF"v[4:7],a[92:95],a[12:15],v[4:7]\n"
RD"a[108:111],v226 offset:11264\n"
MU"v245,0xbfb8aa3b,v241\n"
EX"v245,v245\n"
MF"v[12:15],a[92:95],a[28:31],v[12:15]\n"
EX"v11,v11\n"
PK"v0,v0,v1\n"
MF"v[12:15],a[88:91],a[24:27],v[12:15]\n"
EX"v16,v16\n"
MU"v246,0xbfb8aa3b,v242\n"
MF"v[20:23],a[88:91],a[40:43],v[20:23]\n"
EX"v246,v246\n"
MU"v247,0xbfb8aa3b,v243\n"
MF"v[20:23],a[92:95],a[44:47],v[20:23]\n"
EX"v17,v17\n"
PK"v1,v2,v3\n"
MF"v[28:31],a[92:95],a[60:63],v[28:31]\n"
EX"v247,v247\n"
PK"v248,v248,v248\n"
MF"v[28:31],a[88:91],a[56:59],v[28:31]\n"
WT"lgkmcnt(1)\n"
RD"a[112:115],v226 offset:12288\n"
"v_lshlrev_b32 v248,16,v248\n"
PK"v8,v8,v9\n"
MF"v[32:35],a[96:99],a[0:3],0\n"
EX"v18,v18\n"
PK"v249,v249,v249\n"
MF"v[32:35],a[100:103],a[4:7],v[32:35]\n"
EX"v19,v19\n"
"v_lshlrev_b32 v249,16,v249\n"
MF"v[40:43],a[100:103],a[20:23],0\n"
RD"a[116:119],v226 offset:13312\n"
PK"v250,v250,v250\n"
MF"v[40:43],a[96:99],a[16:19],v[40:43]\n"
EX"v24,v24\n"
PK"v9,v10,v11\n"
MF"v[48:51],a[96:99],a[32:35],0\n"
EX"v25,v25\n"
"v_lshlrev_b32 v250,16,v250\n"
MF"v[48:51],a[100:103],a[36:39],v[48:51]\n"
EX"v26,v26\n"
PK"v16,v16,v17\n"
MF"v[56:59],a[100:103],a[52:55],0\n"
RD"a[120:123],v226 offset:14336\n"
PK"v251,v251,v251\n"
MF"v[56:59],a[96:99],a[48:51],v[56:59]\n"
EX"v27,v27\n"
"v_lshlrev_b32 v251,16,v251\n"
MF"v[32:35],a[104:107],a[8:11],v[32:35]\n"
WT"lgkmcnt(2)\n"
EX"v4,v4\n"
"v_add_f32 v244,1.0,v244\n"
MF"v[32:35],a[108:111],a[12:15],v[32:35]\n"
"v_rcp_f32 v244,v244\n"
"v_add_f32 v245,1.0,v245\n"
MF"v[40:43],a[108:111],a[28:31],v[40:43]\n"
"v_rcp_f32 v245,v245\n"
PK"v17,v18,v19\n"
MF"v[40:43],a[104:107],a[24:27],v[40:43]\n"
RD"a[124:127],v226 offset:15360\n"
"v_add_f32 v246,1.0,v246\n"
"v_rcp_f32 v246,v246\n"
MF"v[48:51],a[104:107],a[40:43],v[48:51]\n"
EX"v5,v5\n"
"v_add_f32 v247,1.0,v247\n"
MF"v[48:51],a[108:111],a[44:47],v[48:51]\n"
"v_rcp_f32 v247,v247\n"
EX"v6,v6\n"
MF"v[56:59],a[108:111],a[60:63],v[56:59]\n"
EX"v7,v7\n"
PK"v24,v24,v25\n"
MF"v[56:59],a[104:107],a[56:59],v[56:59]\n"
EX"v12,v12\n"
PK"v25,v26,v27\n"
MF"v[36:39],a[112:115],a[0:3],0\n"
WT"lgkmcnt(1)\n"
EX"v13,v13\n"
MU"v240,v240,v244\n"
MF"v[36:39],a[116:119],a[4:7],v[36:39]\n"
RD"a[64:67],v227 offset:16384\n"
MU"v241,v241,v245\n"
MF"v[44:47],a[116:119],a[20:23],0\n"
EX"v14,v14\n"
MU"v242,v242,v246\n"
MF"v[44:47],a[112:115],a[16:19],v[44:47]\n"
MU"v243,v243,v247\n"
PK"v2,v4,v5\n"
MF"v[52:55],a[112:115],a[32:35],0\n"
EX"v15,v15\n"
PK"v240,v240,v240\n"
MF"v[52:55],a[116:119],a[36:39],v[52:55]\n"
RD"a[68:71],v227 offset:17408\n"
"v_lshlrev_b32 v240,16,v240\n"
PK"v3,v6,v7\n"
MF"v[60:63],a[116:119],a[52:55],0\n"
EX"v20,v20\n"
PK"v241,v241,v241\n"
MF"v[60:63],a[112:115],a[48:51],v[60:63]\n"
"v_readlane_b32 s43,v228,s42\n"
"v_lshlrev_b32 v241,16,v241\n"
MF"v[36:39],a[120:123],a[8:11],v[36:39]\n"
WT"lgkmcnt(1)\n"
EX"v21,v21\n"
PK"v242,v242,v242\n"
MF"v[36:39],a[124:127],a[12:15],v[36:39]\n"
RD"a[72:75],v227 offset:18432\n"
"v_lshlrev_b32 v242,16,v242\n"
PK"v10,v12,v13\n"
MF"v[44:47],a[124:127],a[28:31],v[44:47]\n"
EX"v22,v22\n"
PK"v243,v243,v243\n"
MF"v[44:47],a[120:123],a[24:27],v[44:47]\n"
"s_mov_b32 m0,s40\n"
"v_lshlrev_b32 v243,16,v243\n"
MU"v240,v240,v248\n"
MF"v[52:55],a[120:123],a[40:43],v[52:55]\n"
EX"v23,v23\n"
MF"v[52:55],a[124:127],a[44:47],v[52:55]\n"
RD"a[76:79],v227 offset:19456\n"
"v_readlane_b32 s44,v229,s42\n"
MU"v241,v241,v249\n"
MF"v[60:63],a[124:127],a[60:63],v[60:63]\n"
EX"v28,v28\n"
PK"v11,v14,v15\n"
MF"v[60:63],a[120:123],a[56:59],v[60:63]\n"
SA"s42,s42,1\n"
BL"v225,s[8:11],s43 offen offset:0 sc1 lds\n"
MU"v242,v242,v250\n"
MF"v[80:83],a[64:67],v[64:67],v[80:83]\n"
SA"s41,s41,32768\n"
EX"v29,v29\n"
MU"v243,v243,v251\n"
MF"v[112:115],a[64:67],v[68:71],v[112:115]\n"
"s_cmp_eq_u32 s41,163840\n"
RD"a[80:83],v227 offset:20480\n"
PK"v240,v240,v241\n"
PK"v241,v242,v243\n"
MF"v[144:147],a[64:67],v[72:75],v[144:147]\n"
"s_cselect_b32 s41,0,s41\n"
EX"v30,v30\n"
PK"v18,v20,v21\n"
MF"v[176:179],a[64:67],v[76:79],v[176:179]\n"
WT"lgkmcnt(2)\n"
"v_add_u32 v226,s41,v224\n"
"v_lshrrev_b32 v239,9,v224\n"
"v_lshlrev_b32 v239,8,v239\n"
MF"v[84:87],a[68:71],v[64:67],v[84:87]\n"
EX"v31,v31\n"
"v_bfe_u32 v237,v224,8,1\n"
MF"v[116:119],a[68:71],v[68:71],v[116:119]\n"
RD"a[84:87],v227 offset:21504\n"
"v_lshl_add_u32 v239,v237,3,v239\n"
PK"v19,v22,v23\n"
MF"v[148:151],a[68:71],v[72:75],v[148:151]\n"
EX"v32,v32\n"
"v_and_b32 v237,0x80,v224\n"
MF"v[180:183],a[68:71],v[76:79],v[180:183]\n"
BL"v225,s[8:11],s43 offen offset:1024 sc1 lds\n"
"v_lshl_add_u32 v239,v237,23,v239\n"
MF"v[88:91],a[72:75],v[64:67],v[88:91]\n"
EX"v33,v33\n"
"v_and_b32 v237,0x70,v224\n"
MF"v[120:123],a[72:75],v[68:71],v[120:123]\n"
RD"a[88:91],v227 offset:22528\n"
"v_add_u32 v239,v239,v237\n"
PK"v26,v28,v29\n"
MF"v[152:155],a[72:75],v[72:75],v[152:155]\n"
"buffer_store_dwordx2 v[240:241],v239,s[8:11],s93 offen sc1\n"
MF"v[184:187],a[72:75],v[76:79],v[184:187]\n"
WT"lgkmcnt(2)\n"
EX"v34,v34\n"
MF"v[92:95],a[76:79],v[64:67],v[92:95]\n"
EX"v35,v35\n"
MF"v[124:127],a[76:79],v[68:71],v[124:127]\n"
RD"a[92:95],v227 offset:23552\n"
PK"v27,v30,v31\n"
MF"v[156:159],a[76:79],v[72:75],v[156:159]\n"
EX"v40,v40\n"
MF"v[188:191],a[76:79],v[76:79],v[188:191]\n"
BL"v225,s[8:11],s43 offen offset:2048 sc1 lds\n"
MF"v[96:99],a[80:83],v[64:67],v[96:99]\n"
EX"v41,v41\n"
MF"v[128:131],a[80:83],v[68:71],v[128:131]\n"
RD"a[96:99],v227 offset:24576\n"
MF"v[160:163],a[80:83],v[72:75],v[160:163]\n"
EX"v42,v42\n"
MF"v[192:195],a[80:83],v[76:79],v[192:195]\n"
WT"lgkmcnt(2)\n"
MF"v[100:103],a[84:87],v[64:67],v[100:103]\n"
EX"v43,v43\n"
MF"v[132:135],a[84:87],v[68:71],v[132:135]\n"
RD"a[100:103],v227 offset:25600\n"
MF"v[164:167],a[84:87],v[72:75],v[164:167]\n"
EX"v48,v48\n"
MF"v[196:199],a[84:87],v[76:79],v[196:199]\n"
BL"v225,s[8:11],s43 offen offset:3072 sc1 lds\n"
MF"v[104:107],a[88:91],v[64:67],v[104:107]\n"
EX"v49,v49\n"
MF"v[136:139],a[88:91],v[68:71],v[136:139]\n"
RD"a[104:107],v227 offset:26624\n"
MF"v[168:171],a[88:91],v[72:75],v[168:171]\n"
EX"v50,v50\n"
MF"v[200:203],a[88:91],v[76:79],v[200:203]\n"
WT"lgkmcnt(2)\n"
SA"m0,s40,16384\n"
MF"v[108:111],a[92:95],v[64:67],v[108:111]\n"
EX"v51,v51\n"
MF"v[140:143],a[92:95],v[68:71],v[140:143]\n"
RD"a[108:111],v227 offset:27648\n"
MF"v[172:175],a[92:95],v[72:75],v[172:175]\n"
EX"v56,v56\n"
MF"v[204:207],a[92:95],v[76:79],v[204:207]\n"
BL"v225,s[8:11],s44 offen offset:0 sc1 lds\n"
MF"a[132:135],a[128:131],v[64:67],a[132:135]\n"
SA"s40,s40,32768\n"
EX"v57,v57\n"
MF"a[136:139],a[128:131],v[68:71],a[136:139]\n"
"s_cmp_eq_u32 s40,s46\n"
RD"a[112:115],v227 offset:28672\n"
"s_cselect_b32 s40,s47,s40\n"
MF"a[140:143],a[128:131],v[72:75],a[140:143]\n"
EX"v58,v58\n"
MF"a[144:147],a[128:131],v[76:79],a[144:147]\n"
MF"v[80:83],a[96:99],v[0:3],v[80:83]\n"
EX"v59,v59\n"
MF"v[112:115],a[96:99],v[8:11],v[112:115]\n"
RD"a[116:119],v227 offset:29696\n"
MF"v[144:147],a[96:99],v[16:19],v[144:147]\n"
EX"v36,v36\n"
MF"v[176:179],a[96:99],v[24:27],v[176:179]\n"
WT"lgkmcnt(2)\n"
BL"v225,s[8:11],s44 offen offset:1024 sc1 lds\n"
MF"v[84:87],a[100:103],v[0:3],v[84:87]\n"
EX"v37,v37\n"
MF"v[116:119],a[100:103],v[8:11],v[116:119]\n"
RD"a[120:123],v227 offset:30720\n"
MF"v[148:151],a[100:103],v[16:19],v[148:151]\n"
EX"v38,v38\n"
MF"v[180:183],a[100:103],v[24:27],v[180:183]\n"
MF"v[88:91],a[104:107],v[0:3],v[88:91]\n"
EX"v39,v39\n"
MF"v[120:123],a[104:107],v[8:11],v[120:123]\n"
RD"a[124:127],v227 offset:31744\n"
MF"v[152:155],a[104:107],v[16:19],v[152:155]\n"
EX"v44,v44\n"
MF"v[184:187],a[104:107],v[24:27],v[184:187]\n"
BL"v225,s[8:11],s44 offen offset:2048 sc1 lds\n"
MF"v[92:95],a[108:111],v[0:3],v[92:95]\n"
EX"v45,v45\n"
MF"v[124:127],a[108:111],v[8:11],v[124:127]\n"
MF"v[156:159],a[108:111],v[16:19],v[156:159]\n"
EX"v46,v46\n"
MF"v[188:191],a[108:111],v[24:27],v[188:191]\n"
WT"lgkmcnt(1)\n"
MF"v[96:99],a[112:115],v[0:3],v[96:99]\n"
WT"lgkmcnt(0)\n"
"s_barrier\n"
MF"v[128:131],a[112:115],v[8:11],v[128:131]\n"
RD"a[64:67],v226 offset:0\n"
MF"v[160:163],a[112:115],v[16:19],v[160:163]\n"
EX"v47,v47\n"
MF"v[192:195],a[112:115],v[24:27],v[192:195]\n"
BL"v225,s[8:11],s44 offen offset:3072 sc1 lds\n"
MF"v[100:103],a[116:119],v[0:3],v[100:103]\n"
EX"v52,v52\n"
MF"v[132:135],a[116:119],v[8:11],v[132:135]\n"
RD"a[68:71],v226 offset:1024\n"
MF"v[164:167],a[116:119],v[16:19],v[164:167]\n"
EX"v53,v53\n"
MF"v[196:199],a[116:119],v[24:27],v[196:199]\n"
RD"a[72:75],v226 offset:2048\n"
MF"v[104:107],a[120:123],v[0:3],v[104:107]\n"
EX"v54,v54\n"
MF"v[136:139],a[120:123],v[8:11],v[136:139]\n"
RD"a[76:79],v226 offset:3072\n"
MF"v[168:171],a[120:123],v[16:19],v[168:171]\n"
EX"v55,v55\n"
MF"v[200:203],a[120:123],v[24:27],v[200:203]\n"
MF"v[108:111],a[124:127],v[0:3],v[108:111]\n"
WT"vmcnt(6)\n"
"buffer_store_dwordx2 v[232:233],v230,s[8:11],s94 offen sc1\n"
MF"v[140:143],a[124:127],v[8:11],v[140:143]\n"
EX"v60,v60\n"
MF"v[172:175],a[124:127],v[16:19],v[172:175]\n"
RD"a[80:83],v226 offset:4096\n"
MF"v[204:207],a[124:127],v[24:27],v[204:207]\n"
EX"v61,v61\n"
MF"a[132:135],a[128:131],v[0:3],a[132:135]\n"
RD"a[84:87],v226 offset:5120\n"
MF"a[136:139],a[128:131],v[8:11],a[136:139]\n"
EX"v62,v62\n"
MF"a[140:143],a[128:131],v[16:19],a[140:143]\n"
RD"a[88:91],v226 offset:6144\n"
MF"a[144:147],a[128:131],v[24:27],a[144:147]\n"
EX"v63,v63\n"
"s_mov_b32 s57,0\n"
"s_lshl_b32 s56,s34,1\n"
"s_sub_u32 s56,s56,6\n"
"s_branch LP1_%=\n"
"LMA_%=:\n"
WT"lgkmcnt(10)\n"
MF"v[0:3],a[64:67],a[0:3],v[208:211]\n"
WT"lgkmcnt(2)\n"
"v_add_f32 v240,v240,v244\n"
PK"v64,v32,v33\n"
MF"v[0:3],a[68:71],a[4:7],v[0:3]\n"
PK"v65,v34,v35\n"
"v_add_f32 v248,v248,v252\n"
MF"v[8:11],a[68:71],a[20:23],0\n"
PK"v66,v36,v37\n"
PK"v67,v38,v39\n"
MF"v[8:11],a[64:67],a[16:19],v[8:11]\n"
"v_add_f32 v241,v241,v245\n"
PK"v68,v40,v41\n"
MF"v[16:19],a[64:67],a[32:35],0\n"
PK"v69,v42,v43\n"
"v_add_f32 v249,v249,v253\n"
MF"v[16:19],a[68:71],a[36:39],v[16:19]\n"
RD"a[92:95],v226 offset:7168\n"
PK"v70,v44,v45\n"
MF"v[24:27],a[68:71],a[52:55],0\n"
PK"v71,v46,v47\n"
"v_add_f32 v242,v242,v246\n"
MF"v[24:27],a[64:67],a[48:51],v[24:27]\n"
PK"v72,v48,v49\n"
PK"v73,v50,v51\n"
MF"v[0:3],a[72:75],a[8:11],v[0:3]\n"
"v_mov_b32 v227,v226\n"
"v_add_f32 v250,v250,v254\n"
MF"v[0:3],a[76:79],a[12:15],v[0:3]\n"
PK"v74,v52,v53\n"
PK"v75,v54,v55\n"
MF"v[8:11],a[76:79],a[28:31],v[8:11]\n"
"v_add_f32 v243,v243,v247\n"
PK"v76,v56,v57\n"
MF"v[8:11],a[72:75],a[24:27],v[8:11]\n"
PK"v77,v58,v59\n"
"v_add_f32 v251,v251,v255\n"
MF"v[16:19],a[72:75],a[40:43],v[16:19]\n"
PK"v78,v60,v61\n"
PK"v79,v62,v63\n"
MF"v[16:19],a[76:79],a[44:47],v[16:19]\n"
RD"a[96:99],v226 offset:8192\n"
PK"v240,v240,v240\n"
MF"v[24:27],a[76:79],a[60:63],v[24:27]\n"
EX"v0,v0\n"
MF"v[24:27],a[72:75],a[56:59],v[24:27]\n"
"v_lshlrev_b32 v240,16,v240\n"
PK"v241,v241,v241\n"
MF"v[4:7],a[80:83],a[0:3],v[212:215]\n"
WT"lgkmcnt(1)\n"
EX"v1,v1\n"
MF"v[4:7],a[84:87],a[4:7],v[4:7]\n"
RD"a[100:103],v226 offset:9216\n"
"v_lshlrev_b32 v241,16,v241\n"
MF"v[12:15],a[84:87],a[20:23],0\n"
EX"v2,v2\n"
MF"v[12:15],a[80:83],a[16:19],v[12:15]\n"
EX"v3,v3\n"
MF"v[20:23],a[80:83],a[32:35],0\n"
EX"v8,v8\n"
PK"v242,v242,v242\n"
MF"v[20:23],a[84:87],a[36:39],v[20:23]\n"
RD"a[104:107],v226 offset:10240\n"
"v_lshlrev_b32 v242,16,v242\n"
MF"v[28:31],a[84:87],a[52:55],0\n"
EX"v9,v9\n"
PK"v243,v243,v243\n"
MF"v[28:31],a[80:83],a[48:51],v[28:31]\n"
"v_lshlrev_b32 v243,16,v243\n"
MU"v244,0xbfb8aa3b,v240\n"
EX"v244,v244\n"
MF"v[4:7],a[88:91],a[8:11],v[4:7]\n"
EX"v10,v10\n"
MF"v[4:7],a[92:95],a[12:15],v[4:7]\n"
RD"a[108:111],v226 offset:11264\n"
MU"v245,0xbfb8aa3b,v241\n"
EX"v245,v245\n"
MF"v[12:15],a[92:95],a[28:31],v[12:15]\n"
EX"v11,v11\n"
PK"v0,v0,v1\n"
MF"v[12:15],a[88:91],a[24:27],v[12:15]\n"
EX"v16,v16\n"
MU"v246,0xbfb8aa3b,v242\n"
MF"v[20:23],a[88:91],a[40:43],v[20:23]\n"
EX"v246,v246\n"
MU"v247,0xbfb8aa3b,v243\n"
MF"v[20:23],a[92:95],a[44:47],v[20:23]\n"
EX"v17,v17\n"
PK"v1,v2,v3\n"
MF"v[28:31],a[92:95],a[60:63],v[28:31]\n"
EX"v247,v247\n"
PK"v248,v248,v248\n"
MF"v[28:31],a[88:91],a[56:59],v[28:31]\n"
WT"lgkmcnt(1)\n"
RD"a[112:115],v226 offset:12288\n"
"v_lshlrev_b32 v248,16,v248\n"
PK"v8,v8,v9\n"
MF"v[32:35],a[96:99],a[0:3],v[216:219]\n"
EX"v18,v18\n"
PK"v249,v249,v249\n"
MF"v[32:35],a[100:103],a[4:7],v[32:35]\n"
EX"v19,v19\n"
"v_lshlrev_b32 v249,16,v249\n"
MF"v[40:43],a[100:103],a[20:23],0\n"
RD"a[116:119],v226 offset:13312\n"
PK"v250,v250,v250\n"
MF"v[40:43],a[96:99],a[16:19],v[40:43]\n"
EX"v24,v24\n"
PK"v9,v10,v11\n"
MF"v[48:51],a[96:99],a[32:35],0\n"
EX"v25,v25\n"
"v_lshlrev_b32 v250,16,v250\n"
MF"v[48:51],a[100:103],a[36:39],v[48:51]\n"
EX"v26,v26\n"
PK"v16,v16,v17\n"
MF"v[56:59],a[100:103],a[52:55],0\n"
RD"a[120:123],v226 offset:14336\n"
PK"v251,v251,v251\n"
MF"v[56:59],a[96:99],a[48:51],v[56:59]\n"
EX"v27,v27\n"
"v_lshlrev_b32 v251,16,v251\n"
MF"v[32:35],a[104:107],a[8:11],v[32:35]\n"
WT"lgkmcnt(2)\n"
EX"v4,v4\n"
"v_add_f32 v244,1.0,v244\n"
MF"v[32:35],a[108:111],a[12:15],v[32:35]\n"
"v_rcp_f32 v244,v244\n"
"v_add_f32 v245,1.0,v245\n"
MF"v[40:43],a[108:111],a[28:31],v[40:43]\n"
"v_rcp_f32 v245,v245\n"
PK"v17,v18,v19\n"
MF"v[40:43],a[104:107],a[24:27],v[40:43]\n"
RD"a[124:127],v226 offset:15360\n"
"v_add_f32 v246,1.0,v246\n"
"v_rcp_f32 v246,v246\n"
MF"v[48:51],a[104:107],a[40:43],v[48:51]\n"
EX"v5,v5\n"
"v_add_f32 v247,1.0,v247\n"
MF"v[48:51],a[108:111],a[44:47],v[48:51]\n"
"v_rcp_f32 v247,v247\n"
EX"v6,v6\n"
MF"v[56:59],a[108:111],a[60:63],v[56:59]\n"
EX"v7,v7\n"
PK"v24,v24,v25\n"
MF"v[56:59],a[104:107],a[56:59],v[56:59]\n"
EX"v12,v12\n"
PK"v25,v26,v27\n"
MF"v[36:39],a[112:115],a[0:3],v[220:223]\n"
WT"lgkmcnt(1)\n"
EX"v13,v13\n"
MU"v240,v240,v244\n"
MF"v[36:39],a[116:119],a[4:7],v[36:39]\n"
RD"a[64:67],v227 offset:16384\n"
MU"v241,v241,v245\n"
MF"v[44:47],a[116:119],a[20:23],0\n"
EX"v14,v14\n"
MU"v242,v242,v246\n"
MF"v[44:47],a[112:115],a[16:19],v[44:47]\n"
MU"v243,v243,v247\n"
PK"v2,v4,v5\n"
MF"v[52:55],a[112:115],a[32:35],0\n"
EX"v15,v15\n"
PK"v240,v240,v240\n"
MF"v[52:55],a[116:119],a[36:39],v[52:55]\n"
RD"a[68:71],v227 offset:17408\n"
"v_lshlrev_b32 v240,16,v240\n"
PK"v3,v6,v7\n"
MF"v[60:63],a[116:119],a[52:55],0\n"
EX"v20,v20\n"
PK"v241,v241,v241\n"
MF"v[60:63],a[112:115],a[48:51],v[60:63]\n"
"v_readlane_b32 s43,v228,s42\n"
"v_lshlrev_b32 v241,16,v241\n"
MF"v[36:39],a[120:123],a[8:11],v[36:39]\n"
WT"lgkmcnt(1)\n"
EX"v21,v21\n"
PK"v242,v242,v242\n"
MF"v[36:39],a[124:127],a[12:15],v[36:39]\n"
RD"a[72:75],v227 offset:18432\n"
"v_lshlrev_b32 v242,16,v242\n"
PK"v10,v12,v13\n"
MF"v[44:47],a[124:127],a[28:31],v[44:47]\n"
EX"v22,v22\n"
PK"v243,v243,v243\n"
MF"v[44:47],a[120:123],a[24:27],v[44:47]\n"
"s_mov_b32 m0,s40\n"
"v_lshlrev_b32 v243,16,v243\n"
MU"v240,v240,v248\n"
MF"v[52:55],a[120:123],a[40:43],v[52:55]\n"
EX"v23,v23\n"
MF"v[52:55],a[124:127],a[44:47],v[52:55]\n"
RD"a[76:79],v227 offset:19456\n"
"v_readlane_b32 s44,v229,s42\n"
MU"v241,v241,v249\n"
MF"v[60:63],a[124:127],a[60:63],v[60:63]\n"
EX"v28,v28\n"
PK"v11,v14,v15\n"
MF"v[60:63],a[120:123],a[56:59],v[60:63]\n"
SA"s42,s42,1\n"
BL"v225,s[8:11],s43 offen offset:0 sc1 lds\n"
MU"v242,v242,v250\n"
MF"v[80:83],a[64:67],v[64:67],v[80:83]\n"
SA"s41,s41,32768\n"
EX"v29,v29\n"
MU"v243,v243,v251\n"
MF"v[112:115],a[64:67],v[68:71],v[112:115]\n"
"s_cmp_eq_u32 s41,163840\n"
RD"a[80:83],v227 offset:20480\n"
PK"v240,v240,v241\n"
PK"v241,v242,v243\n"
MF"v[144:147],a[64:67],v[72:75],v[144:147]\n"
"s_cselect_b32 s41,0,s41\n"
EX"v30,v30\n"
PK"v18,v20,v21\n"
MF"v[176:179],a[64:67],v[76:79],v[176:179]\n"
WT"lgkmcnt(2)\n"
"v_add_u32 v226,s41,v224\n"
"v_lshrrev_b32 v239,9,v224\n"
"v_lshlrev_b32 v239,8,v239\n"
MF"v[84:87],a[68:71],v[64:67],v[84:87]\n"
EX"v31,v31\n"
"v_bfe_u32 v237,v224,8,1\n"
MF"v[116:119],a[68:71],v[68:71],v[116:119]\n"
RD"a[84:87],v227 offset:21504\n"
"v_lshl_add_u32 v239,v237,3,v239\n"
PK"v19,v22,v23\n"
MF"v[148:151],a[68:71],v[72:75],v[148:151]\n"
EX"v32,v32\n"
"v_and_b32 v237,0x80,v224\n"
MF"v[180:183],a[68:71],v[76:79],v[180:183]\n"
BL"v225,s[8:11],s43 offen offset:1024 sc1 lds\n"
"v_lshl_add_u32 v239,v237,23,v239\n"
MF"v[88:91],a[72:75],v[64:67],v[88:91]\n"
EX"v33,v33\n"
"v_and_b32 v237,0x70,v224\n"
MF"v[120:123],a[72:75],v[68:71],v[120:123]\n"
RD"a[88:91],v227 offset:22528\n"
"v_add_u32 v239,v239,v237\n"
PK"v26,v28,v29\n"
MF"v[152:155],a[72:75],v[72:75],v[152:155]\n"
"buffer_store_dwordx2 v[240:241],v239,s[8:11],s93 offen sc1\n"
MF"v[184:187],a[72:75],v[76:79],v[184:187]\n"
WT"lgkmcnt(2)\n"
EX"v34,v34\n"
MF"v[92:95],a[76:79],v[64:67],v[92:95]\n"
EX"v35,v35\n"
MF"v[124:127],a[76:79],v[68:71],v[124:127]\n"
RD"a[92:95],v227 offset:23552\n"
PK"v27,v30,v31\n"
MF"v[156:159],a[76:79],v[72:75],v[156:159]\n"
EX"v40,v40\n"
MF"v[188:191],a[76:79],v[76:79],v[188:191]\n"
BL"v225,s[8:11],s43 offen offset:2048 sc1 lds\n"
MF"v[96:99],a[80:83],v[64:67],v[96:99]\n"
EX"v41,v41\n"
MF"v[128:131],a[80:83],v[68:71],v[128:131]\n"
RD"a[96:99],v227 offset:24576\n"
MF"v[160:163],a[80:83],v[72:75],v[160:163]\n"
EX"v42,v42\n"
MF"v[192:195],a[80:83],v[76:79],v[192:195]\n"
WT"lgkmcnt(2)\n"
MF"v[100:103],a[84:87],v[64:67],v[100:103]\n"
EX"v43,v43\n"
MF"v[132:135],a[84:87],v[68:71],v[132:135]\n"
RD"a[100:103],v227 offset:25600\n"
MF"v[164:167],a[84:87],v[72:75],v[164:167]\n"
EX"v48,v48\n"
MF"v[196:199],a[84:87],v[76:79],v[196:199]\n"
BL"v225,s[8:11],s43 offen offset:3072 sc1 lds\n"
MF"v[104:107],a[88:91],v[64:67],v[104:107]\n"
EX"v49,v49\n"
MF"v[136:139],a[88:91],v[68:71],v[136:139]\n"
RD"a[104:107],v227 offset:26624\n"
MF"v[168:171],a[88:91],v[72:75],v[168:171]\n"
EX"v50,v50\n"
MF"v[200:203],a[88:91],v[76:79],v[200:203]\n"
WT"lgkmcnt(2)\n"
SA"m0,s40,16384\n"
MF"v[108:111],a[92:95],v[64:67],v[108:111]\n"
EX"v51,v51\n"
MF"v[140:143],a[92:95],v[68:71],v[140:143]\n"
RD"a[108:111],v227 offset:27648\n"
MF"v[172:175],a[92:95],v[72:75],v[172:175]\n"
EX"v56,v56\n"
MF"v[204:207],a[92:95],v[76:79],v[204:207]\n"
BL"v225,s[8:11],s44 offen offset:0 sc1 lds\n"
MF"a[132:135],a[128:131],v[64:67],a[132:135]\n"
SA"s40,s40,32768\n"
EX"v57,v57\n"
MF"a[136:139],a[128:131],v[68:71],a[136:139]\n"
"s_cmp_eq_u32 s40,s46\n"
RD"a[112:115],v227 offset:28672\n"
"s_cselect_b32 s40,s47,s40\n"
MF"a[140:143],a[128:131],v[72:75],a[140:143]\n"
EX"v58,v58\n"
MF"a[144:147],a[128:131],v[76:79],a[144:147]\n"
MF"v[80:83],a[96:99],v[0:3],v[80:83]\n"
EX"v59,v59\n"
MF"v[112:115],a[96:99],v[8:11],v[112:115]\n"
RD"a[116:119],v227 offset:29696\n"
MF"v[144:147],a[96:99],v[16:19],v[144:147]\n"
EX"v36,v36\n"
MF"v[176:179],a[96:99],v[24:27],v[176:179]\n"
WT"lgkmcnt(2)\n"
BL"v225,s[8:11],s44 offen offset:1024 sc1 lds\n"
MF"v[84:87],a[100:103],v[0:3],v[84:87]\n"
EX"v37,v37\n"
MF"v[116:119],a[100:103],v[8:11],v[116:119]\n"
RD"a[120:123],v227 offset:30720\n"
MF"v[148:151],a[100:103],v[16:19],v[148:151]\n"
EX"v38,v38\n"
MF"v[180:183],a[100:103],v[24:27],v[180:183]\n"
MF"v[88:91],a[104:107],v[0:3],v[88:91]\n"
EX"v39,v39\n"
MF"v[120:123],a[104:107],v[8:11],v[120:123]\n"
RD"a[124:127],v227 offset:31744\n"
MF"v[152:155],a[104:107],v[16:19],v[152:155]\n"
EX"v44,v44\n"
MF"v[184:187],a[104:107],v[24:27],v[184:187]\n"
BL"v225,s[8:11],s44 offen offset:2048 sc1 lds\n"
MF"v[92:95],a[108:111],v[0:3],v[92:95]\n"
EX"v45,v45\n"
MF"v[124:127],a[108:111],v[8:11],v[124:127]\n"
MF"v[156:159],a[108:111],v[16:19],v[156:159]\n"
EX"v46,v46\n"
MF"v[188:191],a[108:111],v[24:27],v[188:191]\n"
WT"lgkmcnt(1)\n"
MF"v[96:99],a[112:115],v[0:3],v[96:99]\n"
WT"lgkmcnt(0)\n"
"s_barrier\n"
MF"v[128:131],a[112:115],v[8:11],v[128:131]\n"
RD"a[64:67],v226 offset:0\n"
MF"v[160:163],a[112:115],v[16:19],v[160:163]\n"
EX"v47,v47\n"
MF"v[192:195],a[112:115],v[24:27],v[192:195]\n"
BL"v225,s[8:11],s44 offen offset:3072 sc1 lds\n"
MF"v[100:103],a[116:119],v[0:3],v[100:103]\n"
EX"v52,v52\n"
MF"v[132:135],a[116:119],v[8:11],v[132:135]\n"
RD"a[68:71],v226 offset:1024\n"
MF"v[164:167],a[116:119],v[16:19],v[164:167]\n"
EX"v53,v53\n"
MF"v[196:199],a[116:119],v[24:27],v[196:199]\n"
RD"a[72:75],v226 offset:2048\n"
MF"v[104:107],a[120:123],v[0:3],v[104:107]\n"
EX"v54,v54\n"
MF"v[136:139],a[120:123],v[8:11],v[136:139]\n"
RD"a[76:79],v226 offset:3072\n"
MF"v[168:171],a[120:123],v[16:19],v[168:171]\n"
EX"v55,v55\n"
MF"v[200:203],a[120:123],v[24:27],v[200:203]\n"
MF"v[108:111],a[124:127],v[0:3],v[108:111]\n"
WT"vmcnt(6)\n"
"buffer_store_dwordx2 v[232:233],v230,s[8:11],s94 offen sc1\n"
MF"v[140:143],a[124:127],v[8:11],v[140:143]\n"
EX"v60,v60\n"
MF"v[172:175],a[124:127],v[16:19],v[172:175]\n"
RD"a[80:83],v226 offset:4096\n"
MF"v[204:207],a[124:127],v[24:27],v[204:207]\n"
EX"v61,v61\n"
MF"a[132:135],a[128:131],v[0:3],a[132:135]\n"
RD"a[84:87],v226 offset:5120\n"
MF"a[136:139],a[128:131],v[8:11],a[136:139]\n"
EX"v62,v62\n"
MF"a[140:143],a[128:131],v[16:19],a[140:143]\n"
RD"a[88:91],v226 offset:6144\n"
MF"a[144:147],a[128:131],v[24:27],a[144:147]\n"
EX"v63,v63\n"
"s_branch LDB_%=\n"
"LMD_%=:\n"
"s_cmp_eq_u32 s34,6\n"
"s_cbranch_scc1 LME_%=\n"
WT"lgkmcnt(10)\n"
MF"v[0:3],a[64:67],a[0:3],v[208:211]\n"
WT"lgkmcnt(2)\n"
"v_lshrrev_b32 v239,9,v224\n"
"v_lshlrev_b32 v239,8,v239\n"
MF"v[0:3],a[68:71],a[4:7],v[0:3]\n"
PK"v64,v32,v33\n"
PK"v65,v34,v35\n"
MF"v[8:11],a[68:71],a[20:23],0\n"
"v_bfe_u32 v237,v224,8,1\n"
PK"v66,v36,v37\n"
MF"v[8:11],a[64:67],a[16:19],v[8:11]\n"
PK"v67,v38,v39\n"
"v_lshl_add_u32 v239,v237,3,v239\n"
MF"v[16:19],a[64:67],a[32:35],0\n"
PK"v68,v40,v41\n"
PK"v69,v42,v43\n"
MF"v[16:19],a[68:71],a[36:39],v[16:19]\n"
RD"a[92:95],v226 offset:7168\n"
"v_and_b32 v237,0x80,v224\n"
MF"v[24:27],a[68:71],a[52:55],0\n"
PK"v70,v44,v45\n"
PK"v71,v46,v47\n"
MF"v[24:27],a[64:67],a[48:51],v[24:27]\n"
"v_lshl_add_u32 v239,v237,23,v239\n"
"v_not_b32 v236,v234\n"
MF"v[0:3],a[72:75],a[8:11],v[0:3]\n"
PK"v72,v48,v49\n"
PK"v73,v50,v51\n"
MF"v[0:3],a[76:79],a[12:15],v[0:3]\n"
SA"s53,s93,4292282368\n"
"v_mov_b32 v227,v226\n"
"v_and_b32 v237,15,v236\n"
MF"v[8:11],a[76:79],a[28:31],v[8:11]\n"
PK"v74,v52,v53\n"
PK"v75,v54,v55\n"
MF"v[8:11],a[72:75],a[24:27],v[8:11]\n"
"v_bfe_u32 v236,v236,4,7\n"
PK"v76,v56,v57\n"
MF"v[16:19],a[72:75],a[40:43],v[16:19]\n"
PK"v77,v58,v59\n"
"v_mul_u32_u24 v236,20480,v236\n"
MF"v[16:19],a[76:79],a[44:47],v[16:19]\n"
RD"a[96:99],v226 offset:8192\n"
PK"v78,v60,v61\n"
MF"v[24:27],a[76:79],a[60:63],v[24:27]\n"
EX"v0,v0\n"
MF"v[24:27],a[72:75],a[56:59],v[24:27]\n"
PK"v79,v62,v63\n"
"v_lshl_add_u32 v236,v237,4,v236\n"
MF"v[4:7],a[80:83],a[0:3],v[212:215]\n"
WT"lgkmcnt(1)\n"
EX"v1,v1\n"
"v_add_u32 v236,v236,v239\n"
MF"v[4:7],a[84:87],a[4:7],v[4:7]\n"
RD"a[100:103],v226 offset:9216\n"
"v_and_b32 v237,0x70,v224\n"
MF"v[12:15],a[84:87],a[20:23],0\n"
EX"v2,v2\n"
MF"v[12:15],a[80:83],a[16:19],v[12:15]\n"
EX"v3,v3\n"
"v_add_u32 v239,v239,v237\n"
MF"v[20:23],a[80:83],a[32:35],0\n"
EX"v8,v8\n"
"v_add_f32 v240,v240,v244\n"
MF"v[20:23],a[84:87],a[36:39],v[20:23]\n"
RD"a[104:107],v226 offset:10240\n"
"v_add_f32 v248,v248,v252\n"
"v_add_f32 v241,v241,v245\n"
MF"v[28:31],a[84:87],a[52:55],0\n"
EX"v9,v9\n"
"v_add_f32 v249,v249,v253\n"
MF"v[28:31],a[80:83],a[48:51],v[28:31]\n"
"v_add_f32 v242,v242,v246\n"
"v_add_f32 v250,v250,v254\n"
MF"v[4:7],a[88:91],a[8:11],v[4:7]\n"
EX"v10,v10\n"
"v_add_f32 v243,v243,v247\n"
MF"v[4:7],a[92:95],a[12:15],v[4:7]\n"
RD"a[108:111],v226 offset:11264\n"
"v_add_f32 v251,v251,v255\n"
PK"v0,v0,v1\n"
MF"v[12:15],a[92:95],a[28:31],v[12:15]\n"
"buffer_load_dwordx2 v[254:255],v236,s[8:11],s53 offen sc1\n"
"v_ashrrev_i32 v235,31,v234\n"
MF"v[12:15],a[88:91],a[24:27],v[12:15]\n"
EX"v11,v11\n"
"v_not_b32 v235,v235\n"
MF"v[20:23],a[88:91],a[40:43],v[20:23]\n"
EX"v16,v16\n"
"v_or_b32 v235,0x80000000,v235\n"
MF"v[20:23],a[92:95],a[44:47],v[20:23]\n"
EX"v17,v17\n"
PK"v1,v2,v3\n"
MF"v[28:31],a[92:95],a[60:63],v[28:31]\n"
RD"a[112:115],v226 offset:12288\n"
"v_xor_b32 v235,v235,v234\n"
"v_and_b32 v235,0xffff0000,v235\n"
MF"v[28:31],a[88:91],a[56:59],v[28:31]\n"
WT"lgkmcnt(2)\n"
EX"v18,v18\n"
MU"v235,0xbfb8aa3b,v235\n"
MF"v[32:35],a[96:99],a[0:3],v[216:219]\n"
EX"v235,v235\n"
PK"v8,v8,v9\n"
MF"v[32:35],a[100:103],a[4:7],v[32:35]\n"
EX"v19,v19\n"
"v_add_f32 v235,1.0,v235\n"
MF"v[40:43],a[100:103],a[20:23],0\n"
"v_rcp_f32 v235,v235\n"
MF"v[40:43],a[96:99],a[16:19],v[40:43]\n"
RD"a[116:119],v226 offset:13312\n"
PK"v235,v235,v235\n"
PK"v9,v10,v11\n"
MF"v[48:51],a[96:99],a[32:35],0\n"
EX"v24,v24\n"
"v_lshlrev_b32 v235,16,v235\n"
MF"v[48:51],a[100:103],a[36:39],v[48:51]\n"
EX"v25,v25\n"
"v_add_f32 v240,v240,v248\n"
MF"v[56:59],a[100:103],a[52:55],0\n"
EX"v26,v26\n"
PK"v16,v16,v17\n"
MF"v[56:59],a[96:99],a[48:51],v[56:59]\n"
RD"a[120:123],v226 offset:14336\n"
"v_add_f32 v241,v241,v249\n"
"v_add_f32 v242,v242,v250\n"
MF"v[32:35],a[104:107],a[8:11],v[32:35]\n"
WT"lgkmcnt(2)\n"
EX"v27,v27\n"
"v_add_f32 v243,v243,v251\n"
MF"v[32:35],a[108:111],a[12:15],v[32:35]\n"
EX"v4,v4\n"
PK"v240,v240,v240\n"
MF"v[40:43],a[108:111],a[28:31],v[40:43]\n"
RD"a[124:127],v226 offset:15360\n"
PK"v17,v18,v19\n"
"v_lshlrev_b32 v240,16,v240\n"
MF"v[40:43],a[104:107],a[24:27],v[40:43]\n"
EX"v5,v5\n"
MU"v240,v240,v235\n"
MF"v[48:51],a[104:107],a[40:43],v[48:51]\n"
EX"v6,v6\n"
PK"v240,v240,v240\n"
MF"v[48:51],a[108:111],a[44:47],v[48:51]\n"
EX"v7,v7\n"
PK"v24,v24,v25\n"
MF"v[56:59],a[108:111],a[60:63],v[56:59]\n"
EX"v12,v12\n"
"v_lshlrev_b32 v240,16,v240\n"
MF"v[56:59],a[104:107],a[56:59],v[56:59]\n"
PK"v241,v241,v241\n"
"v_lshlrev_b32 v241,16,v241\n"
PK"v25,v26,v27\n"
MF"v[36:39],a[112:115],a[0:3],v[220:223]\n"
WT"lgkmcnt(1)\n"
EX"v13,v13\n"
MU"v241,v241,v235\n"
MF"v[36:39],a[116:119],a[4:7],v[36:39]\n"
RD"a[64:67],v227 offset:16384\n"
PK"v241,v241,v241\n"
MF"v[44:47],a[116:119],a[20:23],0\n"
EX"v14,v14\n"
"v_lshlrev_b32 v241,16,v241\n"
MF"v[44:47],a[112:115],a[16:19],v[44:47]\n"
PK"v242,v242,v242\n"
PK"v2,v4,v5\n"
MF"v[52:55],a[112:115],a[32:35],0\n"
EX"v15,v15\n"
"v_lshlrev_b32 v242,16,v242\n"
MF"v[52:55],a[116:119],a[36:39],v[52:55]\n"
RD"a[68:71],v227 offset:17408\n"
MU"v242,v242,v235\n"
PK"v3,v6,v7\n"
MF"v[60:63],a[116:119],a[52:55],0\n"
EX"v20,v20\n"
PK"v242,v242,v242\n"
MF"v[60:63],a[112:115],a[48:51],v[60:63]\n"
"v_readlane_b32 s43,v228,s42\n"
"v_lshlrev_b32 v242,16,v242\n"
MF"v[36:39],a[120:123],a[8:11],v[36:39]\n"
WT"lgkmcnt(1)\n"
EX"v21,v21\n"
PK"v243,v243,v243\n"
MF"v[36:39],a[124:127],a[12:15],v[36:39]\n"
RD"a[72:75],v227 offset:18432\n"
"v_lshlrev_b32 v243,16,v243\n"
PK"v10,v12,v13\n"
MF"v[44:47],a[124:127],a[28:31],v[44:47]\n"
EX"v22,v22\n"
MU"v243,v243,v235\n"
MF"v[44:47],a[120:123],a[24:27],v[44:47]\n"
"s_mov_b32 m0,s40\n"
PK"v243,v243,v243\n"
"v_lshlrev_b32 v243,16,v243\n"
MF"v[52:55],a[120:123],a[40:43],v[52:55]\n"
EX"v23,v23\n"
MF"v[52:55],a[124:127],a[44:47],v[52:55]\n"
RD"a[76:79],v227 offset:19456\n"
"v_readlane_b32 s44,v229,s42\n"
PK"v11,v14,v15\n"
MF"v[60:63],a[124:127],a[60:63],v[60:63]\n"
EX"v28,v28\n"
MF"v[60:63],a[120:123],a[56:59],v[60:63]\n"
SA"s42,s42,1\n"
BL"v225,s[8:11],s43 offen offset:0 sc1 lds\n"
MF"v[80:83],a[64:67],v[64:67],v[80:83]\n"
SA"s41,s41,32768\n"
EX"v29,v29\n"
MF"v[112:115],a[64:67],v[68:71],v[112:115]\n"
"s_cmp_eq_u32 s41,163840\n"
RD"a[80:83],v227 offset:20480\n"
PK"v18,v20,v21\n"
MF"v[144:147],a[64:67],v[72:75],v[144:147]\n"
"s_cselect_b32 s41,0,s41\n"
EX"v30,v30\n"
MF"v[176:179],a[64:67],v[76:79],v[176:179]\n"
WT"lgkmcnt(2)\n"
"v_add_u32 v226,s41,v224\n"
PK"v19,v22,v23\n"
MF"v[84:87],a[68:71],v[64:67],v[84:87]\n"
EX"v31,v31\n"
MF"v[116:119],a[68:71],v[68:71],v[116:119]\n"
RD"a[84:87],v227 offset:21504\n"
PK"v26,v28,v29\n"
MF"v[148:151],a[68:71],v[72:75],v[148:151]\n"
EX"v32,v32\n"
MF"v[180:183],a[68:71],v[76:79],v[180:183]\n"
BL"v225,s[8:11],s43 offen offset:1024 sc1 lds\n"
MF"v[88:91],a[72:75],v[64:67],v[88:91]\n"
EX"v33,v33\n"
MF"v[120:123],a[72:75],v[68:71],v[120:123]\n"
RD"a[88:91],v227 offset:22528\n"
PK"v27,v30,v31\n"
MF"v[152:155],a[72:75],v[72:75],v[152:155]\n"
EX"v34,v34\n"
MF"v[184:187],a[72:75],v[76:79],v[184:187]\n"
WT"lgkmcnt(2)\n"
WT"vmcnt(2)\n"
"v_lshlrev_b32 v244,16,v254\n"
"v_and_b32 v245,0xffff0000,v254\n"
MF"v[92:95],a[76:79],v[64:67],v[92:95]\n"
EX"v35,v35\n"
"v_lshlrev_b32 v246,16,v255\n"
MF"v[124:127],a[76:79],v[68:71],v[124:127]\n"
RD"a[92:95],v227 offset:23552\n"
"v_and_b32 v247,0xffff0000,v255\n"
"v_add_f32 v240,v240,v244\n"
MF"v[156:159],a[76:79],v[72:75],v[156:159]\n"
EX"v40,v40\n"
"v_add_f32 v241,v241,v245\n"
MF"v[188:191],a[76:79],v[76:79],v[188:191]\n"
BL"v225,s[8:11],s43 offen offset:2048 sc1 lds\n"
"v_add_f32 v242,v242,v246\n"
MF"v[96:99],a[80:83],v[64:67],v[96:99]\n"
EX"v41,v41\n"
"v_add_f32 v243,v243,v247\n"
MF"v[128:131],a[80:83],v[68:71],v[128:131]\n"
RD"a[96:99],v227 offset:24576\n"
PK"v240,v240,v241\n"
PK"v241,v242,v243\n"
MF"v[160:163],a[80:83],v[72:75],v[160:163]\n"
"buffer_store_dwordx2 v[240:241],v239,s[8:11],s93 offen sc1\n"
MF"v[192:195],a[80:83],v[76:79],v[192:195]\n"
WT"lgkmcnt(2)\n"
EX"v42,v42\n"
MF"v[100:103],a[84:87],v[64:67],v[100:103]\n"
EX"v43,v43\n"
MF"v[132:135],a[84:87],v[68:71],v[132:135]\n"
RD"a[100:103],v227 offset:25600\n"
MF"v[164:167],a[84:87],v[72:75],v[164:167]\n"
EX"v48,v48\n"
MF"v[196:199],a[84:87],v[76:79],v[196:199]\n"
BL"v225,s[8:11],s43 offen offset:3072 sc1 lds\n"
MF"v[104:107],a[88:91],v[64:67],v[104:107]\n"
EX"v49,v49\n"
MF"v[136:139],a[88:91],v[68:71],v[136:139]\n"
RD"a[104:107],v227 offset:26624\n"
MF"v[168:171],a[88:91],v[72:75],v[168:171]\n"
EX"v50,v50\n"
MF"v[200:203],a[88:91],v[76:79],v[200:203]\n"
WT"lgkmcnt(2)\n"
SA"m0,s40,16384\n"
MF"v[108:111],a[92:95],v[64:67],v[108:111]\n"
EX"v51,v51\n"
MF"v[140:143],a[92:95],v[68:71],v[140:143]\n"
RD"a[108:111],v227 offset:27648\n"
MF"v[172:175],a[92:95],v[72:75],v[172:175]\n"
EX"v56,v56\n"
MF"v[204:207],a[92:95],v[76:79],v[204:207]\n"
BL"v225,s[8:11],s44 offen offset:0 sc1 lds\n"
MF"a[132:135],a[128:131],v[64:67],a[132:135]\n"
SA"s40,s40,32768\n"
EX"v57,v57\n"
MF"a[136:139],a[128:131],v[68:71],a[136:139]\n"
"s_cmp_eq_u32 s40,s46\n"
RD"a[112:115],v227 offset:28672\n"
"s_cselect_b32 s40,s47,s40\n"
MF"a[140:143],a[128:131],v[72:75],a[140:143]\n"
EX"v58,v58\n"
MF"a[144:147],a[128:131],v[76:79],a[144:147]\n"
MF"v[80:83],a[96:99],v[0:3],v[80:83]\n"
EX"v59,v59\n"
MF"v[112:115],a[96:99],v[8:11],v[112:115]\n"
RD"a[116:119],v227 offset:29696\n"
MF"v[144:147],a[96:99],v[16:19],v[144:147]\n"
EX"v36,v36\n"
MF"v[176:179],a[96:99],v[24:27],v[176:179]\n"
WT"lgkmcnt(2)\n"
BL"v225,s[8:11],s44 offen offset:1024 sc1 lds\n"
MF"v[84:87],a[100:103],v[0:3],v[84:87]\n"
EX"v37,v37\n"
MF"v[116:119],a[100:103],v[8:11],v[116:119]\n"
RD"a[120:123],v227 offset:30720\n"
MF"v[148:151],a[100:103],v[16:19],v[148:151]\n"
EX"v38,v38\n"
MF"v[180:183],a[100:103],v[24:27],v[180:183]\n"
MF"v[88:91],a[104:107],v[0:3],v[88:91]\n"
EX"v39,v39\n"
MF"v[120:123],a[104:107],v[8:11],v[120:123]\n"
RD"a[124:127],v227 offset:31744\n"
MF"v[152:155],a[104:107],v[16:19],v[152:155]\n"
EX"v44,v44\n"
MF"v[184:187],a[104:107],v[24:27],v[184:187]\n"
BL"v225,s[8:11],s44 offen offset:2048 sc1 lds\n"
MF"v[92:95],a[108:111],v[0:3],v[92:95]\n"
EX"v45,v45\n"
MF"v[124:127],a[108:111],v[8:11],v[124:127]\n"
MF"v[156:159],a[108:111],v[16:19],v[156:159]\n"
EX"v46,v46\n"
MF"v[188:191],a[108:111],v[24:27],v[188:191]\n"
WT"lgkmcnt(1)\n"
MF"v[96:99],a[112:115],v[0:3],v[96:99]\n"
WT"lgkmcnt(0)\n"
"s_barrier\n"
MF"v[128:131],a[112:115],v[8:11],v[128:131]\n"
RD"a[64:67],v226 offset:0\n"
MF"v[160:163],a[112:115],v[16:19],v[160:163]\n"
EX"v47,v47\n"
MF"v[192:195],a[112:115],v[24:27],v[192:195]\n"
BL"v225,s[8:11],s44 offen offset:3072 sc1 lds\n"
MF"v[100:103],a[116:119],v[0:3],v[100:103]\n"
EX"v52,v52\n"
MF"v[132:135],a[116:119],v[8:11],v[132:135]\n"
RD"a[68:71],v226 offset:1024\n"
MF"v[164:167],a[116:119],v[16:19],v[164:167]\n"
EX"v53,v53\n"
MF"v[196:199],a[116:119],v[24:27],v[196:199]\n"
RD"a[72:75],v226 offset:2048\n"
MF"v[104:107],a[120:123],v[0:3],v[104:107]\n"
EX"v54,v54\n"
MF"v[136:139],a[120:123],v[8:11],v[136:139]\n"
RD"a[76:79],v226 offset:3072\n"
MF"v[168:171],a[120:123],v[16:19],v[168:171]\n"
EX"v55,v55\n"
MF"v[200:203],a[120:123],v[24:27],v[200:203]\n"
MF"v[108:111],a[124:127],v[0:3],v[108:111]\n"
WT"vmcnt(5)\n"
"buffer_store_dwordx2 v[232:233],v230,s[8:11],s94 offen sc1\n"
MF"v[140:143],a[124:127],v[8:11],v[140:143]\n"
EX"v60,v60\n"
MF"v[172:175],a[124:127],v[16:19],v[172:175]\n"
RD"a[80:83],v226 offset:4096\n"
MF"v[204:207],a[124:127],v[24:27],v[204:207]\n"
EX"v61,v61\n"
MF"a[132:135],a[128:131],v[0:3],a[132:135]\n"
RD"a[84:87],v226 offset:5120\n"
MF"a[136:139],a[128:131],v[8:11],a[136:139]\n"
EX"v62,v62\n"
MF"a[140:143],a[128:131],v[16:19],a[140:143]\n"
RD"a[88:91],v226 offset:6144\n"
MF"a[144:147],a[128:131],v[24:27],a[144:147]\n"
EX"v63,v63\n"
"s_branch LDB_%=\n"
"LME_%=:\n"
WT"lgkmcnt(10)\n"
MF"v[0:3],a[64:67],a[0:3],0\n"
WT"lgkmcnt(2)\n"
"v_lshrrev_b32 v239,9,v224\n"
"v_lshlrev_b32 v239,8,v239\n"
MF"v[0:3],a[68:71],a[4:7],v[0:3]\n"
PK"v64,v32,v33\n"
PK"v65,v34,v35\n"
MF"v[8:11],a[68:71],a[20:23],0\n"
"v_bfe_u32 v237,v224,8,1\n"
PK"v66,v36,v37\n"
MF"v[8:11],a[64:67],a[16:19],v[8:11]\n"
PK"v67,v38,v39\n"
"v_lshl_add_u32 v239,v237,3,v239\n"
MF"v[16:19],a[64:67],a[32:35],0\n"
PK"v68,v40,v41\n"
PK"v69,v42,v43\n"
MF"v[16:19],a[68:71],a[36:39],v[16:19]\n"
RD"a[92:95],v226 offset:7168\n"
"v_and_b32 v237,0x80,v224\n"
MF"v[24:27],a[68:71],a[52:55],0\n"
PK"v70,v44,v45\n"
PK"v71,v46,v47\n"
MF"v[24:27],a[64:67],a[48:51],v[24:27]\n"
"v_lshl_add_u32 v239,v237,23,v239\n"
"v_not_b32 v236,v234\n"
MF"v[0:3],a[72:75],a[8:11],v[0:3]\n"
PK"v72,v48,v49\n"
PK"v73,v50,v51\n"
MF"v[0:3],a[76:79],a[12:15],v[0:3]\n"
SA"s53,s93,4292282368\n"
"v_mov_b32 v227,v226\n"
"v_and_b32 v237,15,v236\n"
MF"v[8:11],a[76:79],a[28:31],v[8:11]\n"
PK"v74,v52,v53\n"
PK"v75,v54,v55\n"
MF"v[8:11],a[72:75],a[24:27],v[8:11]\n"
"v_bfe_u32 v236,v236,4,7\n"
PK"v76,v56,v57\n"
MF"v[16:19],a[72:75],a[40:43],v[16:19]\n"
PK"v77,v58,v59\n"
"v_mul_u32_u24 v236,20480,v236\n"
MF"v[16:19],a[76:79],a[44:47],v[16:19]\n"
RD"a[96:99],v226 offset:8192\n"
PK"v78,v60,v61\n"
MF"v[24:27],a[76:79],a[60:63],v[24:27]\n"
EX"v0,v0\n"
MF"v[24:27],a[72:75],a[56:59],v[24:27]\n"
PK"v79,v62,v63\n"
"v_lshl_add_u32 v236,v237,4,v236\n"
MF"v[4:7],a[80:83],a[0:3],0\n"
WT"lgkmcnt(1)\n"
EX"v1,v1\n"
"v_add_u32 v236,v236,v239\n"
MF"v[4:7],a[84:87],a[4:7],v[4:7]\n"
RD"a[100:103],v226 offset:9216\n"
"v_and_b32 v237,0x70,v224\n"
MF"v[12:15],a[84:87],a[20:23],0\n"
EX"v2,v2\n"
MF"v[12:15],a[80:83],a[16:19],v[12:15]\n"
EX"v3,v3\n"
"v_add_u32 v239,v239,v237\n"
MF"v[20:23],a[80:83],a[32:35],0\n"
EX"v8,v8\n"
"v_add_f32 v240,v240,v244\n"
MF"v[20:23],a[84:87],a[36:39],v[20:23]\n"
RD"a[104:107],v226 offset:10240\n"
"v_add_f32 v248,v248,v252\n"
"v_add_f32 v241,v241,v245\n"
MF"v[28:31],a[84:87],a[52:55],0\n"
EX"v9,v9\n"
"v_add_f32 v249,v249,v253\n"
MF"v[28:31],a[80:83],a[48:51],v[28:31]\n"
"v_add_f32 v242,v242,v246\n"
"v_add_f32 v250,v250,v254\n"
MF"v[4:7],a[88:91],a[8:11],v[4:7]\n"
EX"v10,v10\n"
"v_add_f32 v243,v243,v247\n"
MF"v[4:7],a[92:95],a[12:15],v[4:7]\n"
RD"a[108:111],v226 offset:11264\n"
"v_add_f32 v251,v251,v255\n"
PK"v0,v0,v1\n"
MF"v[12:15],a[92:95],a[28:31],v[12:15]\n"
"buffer_load_dwordx2 v[254:255],v236,s[8:11],s53 offen sc1\n"
"v_ashrrev_i32 v235,31,v234\n"
MF"v[12:15],a[88:91],a[24:27],v[12:15]\n"
EX"v11,v11\n"
"v_not_b32 v235,v235\n"
MF"v[20:23],a[88:91],a[40:43],v[20:23]\n"
EX"v16,v16\n"
"v_or_b32 v235,0x80000000,v235\n"
MF"v[20:23],a[92:95],a[44:47],v[20:23]\n"
EX"v17,v17\n"
PK"v1,v2,v3\n"
MF"v[28:31],a[92:95],a[60:63],v[28:31]\n"
RD"a[112:115],v226 offset:12288\n"
"v_xor_b32 v235,v235,v234\n"
"v_and_b32 v235,0xffff0000,v235\n"
MF"v[28:31],a[88:91],a[56:59],v[28:31]\n"
WT"lgkmcnt(2)\n"
EX"v18,v18\n"
MU"v235,0xbfb8aa3b,v235\n"
MF"v[32:35],a[96:99],a[0:3],0\n"
EX"v235,v235\n"
PK"v8,v8,v9\n"
MF"v[32:35],a[100:103],a[4:7],v[32:35]\n"
EX"v19,v19\n"
"v_add_f32 v235,1.0,v235\n"
MF"v[40:43],a[100:103],a[20:23],0\n"
"v_rcp_f32 v235,v235\n"
MF"v[40:43],a[96:99],a[16:19],v[40:43]\n"
RD"a[116:119],v226 offset:13312\n"
PK"v235,v235,v235\n"
PK"v9,v10,v11\n"
MF"v[48:51],a[96:99],a[32:35],0\n"
EX"v24,v24\n"
"v_lshlrev_b32 v235,16,v235\n"
MF"v[48:51],a[100:103],a[36:39],v[48:51]\n"
EX"v25,v25\n"
"v_add_f32 v240,v240,v248\n"
MF"v[56:59],a[100:103],a[52:55],0\n"
EX"v26,v26\n"
PK"v16,v16,v17\n"
MF"v[56:59],a[96:99],a[48:51],v[56:59]\n"
RD"a[120:123],v226 offset:14336\n"
"v_add_f32 v241,v241,v249\n"
"v_add_f32 v242,v242,v250\n"
MF"v[32:35],a[104:107],a[8:11],v[32:35]\n"
WT"lgkmcnt(2)\n"
EX"v27,v27\n"
"v_add_f32 v243,v243,v251\n"
MF"v[32:35],a[108:111],a[12:15],v[32:35]\n"
EX"v4,v4\n"
PK"v240,v240,v240\n"
MF"v[40:43],a[108:111],a[28:31],v[40:43]\n"
RD"a[124:127],v226 offset:15360\n"
PK"v17,v18,v19\n"
"v_lshlrev_b32 v240,16,v240\n"
MF"v[40:43],a[104:107],a[24:27],v[40:43]\n"
EX"v5,v5\n"
MU"v240,v240,v235\n"
MF"v[48:51],a[104:107],a[40:43],v[48:51]\n"
EX"v6,v6\n"
PK"v240,v240,v240\n"
MF"v[48:51],a[108:111],a[44:47],v[48:51]\n"
EX"v7,v7\n"
PK"v24,v24,v25\n"
MF"v[56:59],a[108:111],a[60:63],v[56:59]\n"
EX"v12,v12\n"
"v_lshlrev_b32 v240,16,v240\n"
MF"v[56:59],a[104:107],a[56:59],v[56:59]\n"
PK"v241,v241,v241\n"
"v_lshlrev_b32 v241,16,v241\n"
PK"v25,v26,v27\n"
MF"v[36:39],a[112:115],a[0:3],0\n"
WT"lgkmcnt(1)\n"
EX"v13,v13\n"
MU"v241,v241,v235\n"
MF"v[36:39],a[116:119],a[4:7],v[36:39]\n"
RD"a[64:67],v227 offset:16384\n"
PK"v241,v241,v241\n"
MF"v[44:47],a[116:119],a[20:23],0\n"
EX"v14,v14\n"
"v_lshlrev_b32 v241,16,v241\n"
MF"v[44:47],a[112:115],a[16:19],v[44:47]\n"
PK"v242,v242,v242\n"
PK"v2,v4,v5\n"
MF"v[52:55],a[112:115],a[32:35],0\n"
EX"v15,v15\n"
"v_lshlrev_b32 v242,16,v242\n"
MF"v[52:55],a[116:119],a[36:39],v[52:55]\n"
RD"a[68:71],v227 offset:17408\n"
MU"v242,v242,v235\n"
PK"v3,v6,v7\n"
MF"v[60:63],a[116:119],a[52:55],0\n"
EX"v20,v20\n"
PK"v242,v242,v242\n"
MF"v[60:63],a[112:115],a[48:51],v[60:63]\n"
"v_readlane_b32 s43,v228,s42\n"
"v_lshlrev_b32 v242,16,v242\n"
MF"v[36:39],a[120:123],a[8:11],v[36:39]\n"
WT"lgkmcnt(1)\n"
EX"v21,v21\n"
PK"v243,v243,v243\n"
MF"v[36:39],a[124:127],a[12:15],v[36:39]\n"
RD"a[72:75],v227 offset:18432\n"
"v_lshlrev_b32 v243,16,v243\n"
PK"v10,v12,v13\n"
MF"v[44:47],a[124:127],a[28:31],v[44:47]\n"
EX"v22,v22\n"
MU"v243,v243,v235\n"
MF"v[44:47],a[120:123],a[24:27],v[44:47]\n"
"s_mov_b32 m0,s40\n"
PK"v243,v243,v243\n"
"v_lshlrev_b32 v243,16,v243\n"
MF"v[52:55],a[120:123],a[40:43],v[52:55]\n"
EX"v23,v23\n"
MF"v[52:55],a[124:127],a[44:47],v[52:55]\n"
RD"a[76:79],v227 offset:19456\n"
"v_readlane_b32 s44,v229,s42\n"
PK"v11,v14,v15\n"
MF"v[60:63],a[124:127],a[60:63],v[60:63]\n"
EX"v28,v28\n"
MF"v[60:63],a[120:123],a[56:59],v[60:63]\n"
SA"s42,s42,1\n"
BL"v225,s[8:11],s43 offen offset:0 sc1 lds\n"
MF"v[80:83],a[64:67],v[64:67],v[80:83]\n"
SA"s41,s41,32768\n"
EX"v29,v29\n"
MF"v[112:115],a[64:67],v[68:71],v[112:115]\n"
"s_cmp_eq_u32 s41,163840\n"
RD"a[80:83],v227 offset:20480\n"
PK"v18,v20,v21\n"
MF"v[144:147],a[64:67],v[72:75],v[144:147]\n"
"s_cselect_b32 s41,0,s41\n"
EX"v30,v30\n"
MF"v[176:179],a[64:67],v[76:79],v[176:179]\n"
WT"lgkmcnt(2)\n"
"v_add_u32 v226,s41,v224\n"
PK"v19,v22,v23\n"
MF"v[84:87],a[68:71],v[64:67],v[84:87]\n"
EX"v31,v31\n"
MF"v[116:119],a[68:71],v[68:71],v[116:119]\n"
RD"a[84:87],v227 offset:21504\n"
PK"v26,v28,v29\n"
MF"v[148:151],a[68:71],v[72:75],v[148:151]\n"
EX"v32,v32\n"
MF"v[180:183],a[68:71],v[76:79],v[180:183]\n"
BL"v225,s[8:11],s43 offen offset:1024 sc1 lds\n"
MF"v[88:91],a[72:75],v[64:67],v[88:91]\n"
EX"v33,v33\n"
MF"v[120:123],a[72:75],v[68:71],v[120:123]\n"
RD"a[88:91],v227 offset:22528\n"
PK"v27,v30,v31\n"
MF"v[152:155],a[72:75],v[72:75],v[152:155]\n"
EX"v34,v34\n"
MF"v[184:187],a[72:75],v[76:79],v[184:187]\n"
WT"lgkmcnt(2)\n"
WT"vmcnt(2)\n"
"v_lshlrev_b32 v244,16,v254\n"
"v_and_b32 v245,0xffff0000,v254\n"
MF"v[92:95],a[76:79],v[64:67],v[92:95]\n"
EX"v35,v35\n"
"v_lshlrev_b32 v246,16,v255\n"
MF"v[124:127],a[76:79],v[68:71],v[124:127]\n"
RD"a[92:95],v227 offset:23552\n"
"v_and_b32 v247,0xffff0000,v255\n"
"v_add_f32 v240,v240,v244\n"
MF"v[156:159],a[76:79],v[72:75],v[156:159]\n"
EX"v40,v40\n"
"v_add_f32 v241,v241,v245\n"
MF"v[188:191],a[76:79],v[76:79],v[188:191]\n"
BL"v225,s[8:11],s43 offen offset:2048 sc1 lds\n"
"v_add_f32 v242,v242,v246\n"
MF"v[96:99],a[80:83],v[64:67],v[96:99]\n"
EX"v41,v41\n"
"v_add_f32 v243,v243,v247\n"
MF"v[128:131],a[80:83],v[68:71],v[128:131]\n"
RD"a[96:99],v227 offset:24576\n"
PK"v240,v240,v241\n"
PK"v241,v242,v243\n"
MF"v[160:163],a[80:83],v[72:75],v[160:163]\n"
"buffer_store_dwordx2 v[240:241],v239,s[8:11],s93 offen sc1\n"
MF"v[192:195],a[80:83],v[76:79],v[192:195]\n"
WT"lgkmcnt(2)\n"
EX"v42,v42\n"
MF"v[100:103],a[84:87],v[64:67],v[100:103]\n"
EX"v43,v43\n"
MF"v[132:135],a[84:87],v[68:71],v[132:135]\n"
RD"a[100:103],v227 offset:25600\n"
MF"v[164:167],a[84:87],v[72:75],v[164:167]\n"
EX"v48,v48\n"
MF"v[196:199],a[84:87],v[76:79],v[196:199]\n"
BL"v225,s[8:11],s43 offen offset:3072 sc1 lds\n"
MF"v[104:107],a[88:91],v[64:67],v[104:107]\n"
EX"v49,v49\n"
MF"v[136:139],a[88:91],v[68:71],v[136:139]\n"
RD"a[104:107],v227 offset:26624\n"
MF"v[168:171],a[88:91],v[72:75],v[168:171]\n"
EX"v50,v50\n"
MF"v[200:203],a[88:91],v[76:79],v[200:203]\n"
WT"lgkmcnt(2)\n"
SA"m0,s40,16384\n"
MF"v[108:111],a[92:95],v[64:67],v[108:111]\n"
EX"v51,v51\n"
MF"v[140:143],a[92:95],v[68:71],v[140:143]\n"
RD"a[108:111],v227 offset:27648\n"
MF"v[172:175],a[92:95],v[72:75],v[172:175]\n"
EX"v56,v56\n"
MF"v[204:207],a[92:95],v[76:79],v[204:207]\n"
BL"v225,s[8:11],s44 offen offset:0 sc1 lds\n"
MF"a[132:135],a[128:131],v[64:67],a[132:135]\n"
SA"s40,s40,32768\n"
EX"v57,v57\n"
MF"a[136:139],a[128:131],v[68:71],a[136:139]\n"
"s_cmp_eq_u32 s40,s46\n"
RD"a[112:115],v227 offset:28672\n"
"s_cselect_b32 s40,s47,s40\n"
MF"a[140:143],a[128:131],v[72:75],a[140:143]\n"
EX"v58,v58\n"
MF"a[144:147],a[128:131],v[76:79],a[144:147]\n"
MF"v[80:83],a[96:99],v[0:3],v[80:83]\n"
EX"v59,v59\n"
MF"v[112:115],a[96:99],v[8:11],v[112:115]\n"
RD"a[116:119],v227 offset:29696\n"
MF"v[144:147],a[96:99],v[16:19],v[144:147]\n"
EX"v36,v36\n"
MF"v[176:179],a[96:99],v[24:27],v[176:179]\n"
WT"lgkmcnt(2)\n"
BL"v225,s[8:11],s44 offen offset:1024 sc1 lds\n"
MF"v[84:87],a[100:103],v[0:3],v[84:87]\n"
EX"v37,v37\n"
MF"v[116:119],a[100:103],v[8:11],v[116:119]\n"
RD"a[120:123],v227 offset:30720\n"
MF"v[148:151],a[100:103],v[16:19],v[148:151]\n"
EX"v38,v38\n"
MF"v[180:183],a[100:103],v[24:27],v[180:183]\n"
MF"v[88:91],a[104:107],v[0:3],v[88:91]\n"
EX"v39,v39\n"
MF"v[120:123],a[104:107],v[8:11],v[120:123]\n"
RD"a[124:127],v227 offset:31744\n"
MF"v[152:155],a[104:107],v[16:19],v[152:155]\n"
EX"v44,v44\n"
MF"v[184:187],a[104:107],v[24:27],v[184:187]\n"
BL"v225,s[8:11],s44 offen offset:2048 sc1 lds\n"
MF"v[92:95],a[108:111],v[0:3],v[92:95]\n"
EX"v45,v45\n"
MF"v[124:127],a[108:111],v[8:11],v[124:127]\n"
MF"v[156:159],a[108:111],v[16:19],v[156:159]\n"
EX"v46,v46\n"
MF"v[188:191],a[108:111],v[24:27],v[188:191]\n"
WT"lgkmcnt(1)\n"
MF"v[96:99],a[112:115],v[0:3],v[96:99]\n"
WT"lgkmcnt(0)\n"
"s_barrier\n"
MF"v[128:131],a[112:115],v[8:11],v[128:131]\n"
RD"a[64:67],v226 offset:0\n"
MF"v[160:163],a[112:115],v[16:19],v[160:163]\n"
EX"v47,v47\n"
MF"v[192:195],a[112:115],v[24:27],v[192:195]\n"
BL"v225,s[8:11],s44 offen offset:3072 sc1 lds\n"
MF"v[100:103],a[116:119],v[0:3],v[100:103]\n"
EX"v52,v52\n"
MF"v[132:135],a[116:119],v[8:11],v[132:135]\n"
RD"a[68:71],v226 offset:1024\n"
MF"v[164:167],a[116:119],v[16:19],v[164:167]\n"
EX"v53,v53\n"
MF"v[196:199],a[116:119],v[24:27],v[196:199]\n"
RD"a[72:75],v226 offset:2048\n"
MF"v[104:107],a[120:123],v[0:3],v[104:107]\n"
EX"v54,v54\n"
MF"v[136:139],a[120:123],v[8:11],v[136:139]\n"
RD"a[76:79],v226 offset:3072\n"
MF"v[168:171],a[120:123],v[16:19],v[168:171]\n"
EX"v55,v55\n"
MF"v[200:203],a[120:123],v[24:27],v[200:203]\n"
MF"v[108:111],a[124:127],v[0:3],v[108:111]\n"
WT"vmcnt(5)\n"
"buffer_store_dwordx2 v[232:233],v230,s[8:11],s94 offen sc1\n"
MF"v[140:143],a[124:127],v[8:11],v[140:143]\n"
EX"v60,v60\n"
MF"v[172:175],a[124:127],v[16:19],v[172:175]\n"
RD"a[80:83],v226 offset:4096\n"
MF"v[204:207],a[124:127],v[24:27],v[204:207]\n"
EX"v61,v61\n"
MF"a[132:135],a[128:131],v[0:3],a[132:135]\n"
RD"a[84:87],v226 offset:5120\n"
MF"a[136:139],a[128:131],v[8:11],a[136:139]\n"
EX"v62,v62\n"
MF"a[140:143],a[128:131],v[16:19],a[140:143]\n"
RD"a[88:91],v226 offset:6144\n"
MF"a[144:147],a[128:131],v[24:27],a[144:147]\n"
EX"v63,v63\n"
"s_mov_b32 s57,0\n"
"s_lshl_b32 s56,s34,1\n"
"s_sub_u32 s56,s56,12\n"
"s_branch LP1_%=\n"
"LRT_%=:\n"
WT"lgkmcnt(6)\n"
MF"v[0:3],a[64:67],a[0:3],0\n"
WT"lgkmcnt(5)\n"
"v_lshrrev_b32 v242,1,v224\n"
PK"v64,v32,v33\n"
MF"v[0:3],a[68:71],a[4:7],v[0:3]\n"
"s_mov_b32 s12,s74\n"
PK"v65,v34,v35\n"
"v_and_b32 v242,56,v242\n"
MF"v[8:11],a[68:71],a[20:23],0\n"
"s_and_b32 s13,s75,0xffff\n"
PK"v66,v36,v37\n"
PK"v67,v38,v39\n"
MF"v[8:11],a[64:67],a[16:19],v[8:11]\n"
"s_mov_b32 s14,5242880\n"
PK"v68,v40,v41\n"
PK"v69,v42,v43\n"
MF"v[16:19],a[64:67],a[32:35],0\n"
"s_lshl_b32 s23,s35,6\n"
PK"v70,v44,v45\n"
PK"v71,v46,v47\n"
MF"v[16:19],a[68:71],a[36:39],v[16:19]\n"
"s_mov_b32 s15,0x20000\n"
RD"a[92:95],v226 offset:7168\n"
"v_add_u32 v243,s40,v252\n"
MF"v[24:27],a[68:71],a[52:55],0\n"
SA"s23,s23,4352\n"
PK"v72,v48,v49\n"
PK"v73,v50,v51\n"
MF"v[24:27],a[64:67],a[48:51],v[24:27]\n"
WT"lgkmcnt(1)\n"
"buffer_load_dwordx2 v[244:245],v242,s[8:11],s23 offen sc1\n"
MF"v[0:3],a[72:75],a[8:11],v[0:3]\n"
SA"s19,s40,32768\n"
"v_mov_b32 v227,v226\n"
PK"v74,v52,v53\n"
MF"v[0:3],a[76:79],a[12:15],v[0:3]\n"
"s_cmp_eq_u32 s19,s46\n"
PK"v75,v54,v55\n"
PK"v76,v56,v57\n"
MF"v[8:11],a[76:79],a[28:31],v[8:11]\n"
"s_cselect_b32 s19,s47,s19\n"
PK"v77,v58,v59\n"
PK"v78,v60,v61\n"
MF"v[8:11],a[72:75],a[24:27],v[8:11]\n"
"s_cmp_eq_u32 s20,0\n"
PK"v79,v62,v63\n"
"s_cselect_b32 s17,2130706432,s17\n"
MF"v[16:19],a[72:75],a[40:43],v[16:19]\n"
SA"m0,s19,0\n"
MF"v[16:19],a[76:79],a[44:47],v[16:19]\n"
BL"v240,s[12:15],s17 offen offset:0 lds\n"
MF"v[24:27],a[76:79],a[60:63],v[24:27]\n"
RD"a[96:99],v226 offset:8192\n"
MF"v[24:27],a[72:75],a[56:59],v[24:27]\n"
EX"v0,v0\n"
MF"v[4:7],a[80:83],a[0:3],0\n"
BL"v241,s[12:15],s17 offen offset:1024 lds\n"
MF"v[4:7],a[84:87],a[4:7],v[4:7]\n"
EX"v1,v1\n"
MF"v[12:15],a[84:87],a[20:23],0\n"
RD"a[100:103],v226 offset:9216\n"
PK"v0,v0,v1\n"
MF"v[12:15],a[80:83],a[16:19],v[12:15]\n"
EX"v2,v2\n"
MF"v[20:23],a[80:83],a[32:35],0\n"
BL"v251,s[12:15],s17 offen offset:2048 lds\n"
MF"v[20:23],a[84:87],a[36:39],v[20:23]\n"
SA"m0,s19,16384\n"
EX"v3,v3\n"
MF"v[28:31],a[84:87],a[52:55],0\n"
SA"s17,s17,20480\n"
EX"v8,v8\n"
MF"v[28:31],a[80:83],a[48:51],v[28:31]\n"
RD"a[104:107],v226 offset:10240\n"
PK"v1,v2,v3\n"
MF"v[4:7],a[88:91],a[8:11],v[4:7]\n"
WT"lgkmcnt(2)\n"
EX"v9,v9\n"
MF"v[4:7],a[92:95],a[12:15],v[4:7]\n"
BL"v240,s[12:15],s17 offen offset:0 lds\n"
MF"v[12:15],a[92:95],a[28:31],v[12:15]\n"
EX"v10,v10\n"
MF"v[12:15],a[88:91],a[24:27],v[12:15]\n"
RD"a[108:111],v226 offset:11264\n"
PK"v8,v8,v9\n"
MF"v[20:23],a[88:91],a[40:43],v[20:23]\n"
EX"v11,v11\n"
MF"v[20:23],a[92:95],a[44:47],v[20:23]\n"
WT"vmcnt(13)\n"
RT"a[148:149],v243 offset:0\n"
RT"a[150:151],v243 offset:16384\n"
EX"v16,v16\n"
MF"v[28:31],a[92:95],a[60:63],v[28:31]\n"
EX"v17,v17\n"
MF"v[28:31],a[88:91],a[56:59],v[28:31]\n"
RD"a[112:115],v226 offset:12288\n"
PK"v9,v10,v11\n"
MF"v[32:35],a[96:99],a[0:3],0\n"
WT"lgkmcnt(4)\n"
EX"v18,v18\n"
MF"v[32:35],a[100:103],a[4:7],v[32:35]\n"
BL"v241,s[12:15],s17 offen offset:1024 lds\n"
MF"v[40:43],a[100:103],a[20:23],0\n"
EX"v19,v19\n"
MF"v[40:43],a[96:99],a[16:19],v[40:43]\n"
RD"a[116:119],v226 offset:13312\n"
RT"a[152:153],v243 offset:32\n"
MF"v[48:51],a[96:99],a[32:35],0\n"
EX"v24,v24\n"
MF"v[48:51],a[100:103],a[36:39],v[48:51]\n"
RT"a[154:155],v243 offset:16416\n"
RD"a[120:123],v226 offset:14336\n"
EX"v25,v25\n"
MF"v[56:59],a[100:103],a[52:55],0\n"
EX"v26,v26\n"
PK"v16,v16,v17\n"
MF"v[56:59],a[96:99],a[48:51],v[56:59]\n"
EX"v27,v27\n"
MF"v[32:35],a[104:107],a[8:11],v[32:35]\n"
WT"lgkmcnt(4)\n"
BL"v251,s[12:15],s17 offen offset:2048 lds\n"
MF"v[32:35],a[108:111],a[12:15],v[32:35]\n"
EX"v4,v4\n"
PK"v17,v18,v19\n"
MF"v[40:43],a[108:111],a[28:31],v[40:43]\n"
RD"a[124:127],v226 offset:15360\n"
PK"v24,v24,v25\n"
MF"v[40:43],a[104:107],a[24:27],v[40:43]\n"
EX"v5,v5\n"
MF"v[48:51],a[104:107],a[40:43],v[48:51]\n"
RT"a[156:157],v243 offset:64\n"
RT"a[158:159],v243 offset:16448\n"
MF"v[48:51],a[108:111],a[44:47],v[48:51]\n"
EX"v6,v6\n"
MF"v[56:59],a[108:111],a[60:63],v[56:59]\n"
EX"v7,v7\n"
MF"v[56:59],a[104:107],a[56:59],v[56:59]\n"
EX"v12,v12\n"
PK"v25,v26,v27\n"
MF"v[36:39],a[112:115],a[0:3],0\n"
WT"lgkmcnt(3)\n"
EX"v13,v13\n"
MF"v[36:39],a[116:119],a[4:7],v[36:39]\n"
RD"a[64:67],v227 offset:16384\n"
PK"v2,v4,v5\n"
MF"v[44:47],a[116:119],a[20:23],0\n"
EX"v14,v14\n"
MF"v[44:47],a[112:115],a[16:19],v[44:47]\n"
RT"a[160:161],v243 offset:96\n"
RT"a[162:163],v243 offset:16480\n"
MF"v[52:55],a[112:115],a[32:35],0\n"
EX"v15,v15\n"
MF"v[52:55],a[116:119],a[36:39],v[52:55]\n"
RD"a[68:71],v227 offset:17408\n"
PK"v3,v6,v7\n"
MF"v[60:63],a[116:119],a[52:55],0\n"
EX"v20,v20\n"
MF"v[60:63],a[112:115],a[48:51],v[60:63]\n"
"v_readlane_b32 s43,v228,s42\n"
PK"v10,v12,v13\n"
MF"v[36:39],a[120:123],a[8:11],v[36:39]\n"
WT"lgkmcnt(3)\n"
EX"v21,v21\n"
MF"v[36:39],a[124:127],a[12:15],v[36:39]\n"
RD"a[72:75],v227 offset:18432\n"
PK"v11,v14,v15\n"
MF"v[44:47],a[124:127],a[28:31],v[44:47]\n"
EX"v22,v22\n"
MF"v[44:47],a[120:123],a[24:27],v[44:47]\n"
"s_mov_b32 m0,s40\n"
RT"a[164:165],v243 offset:128\n"
RT"a[166:167],v243 offset:16512\n"
MF"v[52:55],a[120:123],a[40:43],v[52:55]\n"
SA"s17,s17,61440\n"
EX"v23,v23\n"
MF"v[52:55],a[124:127],a[44:47],v[52:55]\n"
RD"a[76:79],v227 offset:19456\n"
"v_readlane_b32 s44,v229,s42\n"
MF"v[60:63],a[124:127],a[60:63],v[60:63]\n"
EX"v28,v28\n"
MF"v[60:63],a[120:123],a[56:59],v[60:63]\n"
WT"lgkmcnt(1)\n"
BL"v225,s[8:11],s43 offen offset:0 sc1 lds\n"
MF"v[80:83],a[64:67],v[64:67],v[80:83]\n"
SA"s42,s42,1\n"
EX"v29,v29\n"
MF"v[112:115],a[64:67],v[68:71],v[112:115]\n"
SA"s41,s41,32768\n"
RD"a[80:83],v227 offset:20480\n"
PK"v18,v20,v21\n"
MF"v[144:147],a[64:67],v[72:75],v[144:147]\n"
"s_cmp_eq_u32 s41,163840\n"
EX"v30,v30\n"
MF"v[176:179],a[64:67],v[76:79],v[176:179]\n"
"s_cselect_b32 s41,0,s41\n"
BS"a[148:151],v224,s[8:11],s18 offen offset:0 sc1\n"
MF"v[84:87],a[68:71],v[64:67],v[84:87]\n"
EX"v31,v31\n"
MF"v[116:119],a[68:71],v[68:71],v[116:119]\n"
RD"a[84:87],v227 offset:21504\n"
"v_add_u32 v226,s41,v224\n"
PK"v19,v22,v23\n"
MF"v[148:151],a[68:71],v[72:75],v[148:151]\n"
EX"v32,v32\n"
MF"v[180:183],a[68:71],v[76:79],v[180:183]\n"
BL"v225,s[8:11],s43 offen offset:1024 sc1 lds\n"
MF"v[88:91],a[72:75],v[64:67],v[88:91]\n"
EX"v33,v33\n"
MF"v[120:123],a[72:75],v[68:71],v[120:123]\n"
RD"a[88:91],v227 offset:22528\n"
PK"v26,v28,v29\n"
MF"v[152:155],a[72:75],v[72:75],v[152:155]\n"
EX"v34,v34\n"
MF"v[184:187],a[72:75],v[76:79],v[184:187]\n"
WT"lgkmcnt(2)\n"
BS"a[152:155],v224,s[8:11],s18 offen offset:2048 sc1\n"
MF"v[92:95],a[76:79],v[64:67],v[92:95]\n"
EX"v35,v35\n"
MF"v[124:127],a[76:79],v[68:71],v[124:127]\n"
RD"a[92:95],v227 offset:23552\n"
PK"v27,v30,v31\n"
MF"v[156:159],a[76:79],v[72:75],v[156:159]\n"
SA"s18,s18,4096\n"
EX"v40,v40\n"
MF"v[188:191],a[76:79],v[76:79],v[188:191]\n"
BL"v225,s[8:11],s43 offen offset:2048 sc1 lds\n"
MF"v[96:99],a[80:83],v[64:67],v[96:99]\n"
EX"v41,v41\n"
MF"v[128:131],a[80:83],v[68:71],v[128:131]\n"
RD"a[96:99],v227 offset:24576\n"
MF"v[160:163],a[80:83],v[72:75],v[160:163]\n"
EX"v42,v42\n"
MF"v[192:195],a[80:83],v[76:79],v[192:195]\n"
WT"lgkmcnt(2)\n"
BS"a[156:159],v224,s[8:11],s18 offen offset:0 sc1\n"
MF"v[100:103],a[84:87],v[64:67],v[100:103]\n"
EX"v43,v43\n"
MF"v[132:135],a[84:87],v[68:71],v[132:135]\n"
RD"a[100:103],v227 offset:25600\n"
MF"v[164:167],a[84:87],v[72:75],v[164:167]\n"
EX"v48,v48\n"
MF"v[196:199],a[84:87],v[76:79],v[196:199]\n"
BL"v225,s[8:11],s43 offen offset:3072 sc1 lds\n"
MF"v[104:107],a[88:91],v[64:67],v[104:107]\n"
EX"v49,v49\n"
MF"v[136:139],a[88:91],v[68:71],v[136:139]\n"
RD"a[104:107],v227 offset:26624\n"
MF"v[168:171],a[88:91],v[72:75],v[168:171]\n"
EX"v50,v50\n"
MF"v[200:203],a[88:91],v[76:79],v[200:203]\n"
WT"lgkmcnt(2)\n"
BS"a[160:163],v224,s[8:11],s18 offen offset:2048 sc1\n"
MF"v[108:111],a[92:95],v[64:67],v[108:111]\n"
SA"m0,s40,16384\n"
EX"v51,v51\n"
MF"v[140:143],a[92:95],v[68:71],v[140:143]\n"
RD"a[108:111],v227 offset:27648\n"
MF"v[172:175],a[92:95],v[72:75],v[172:175]\n"
SA"s18,s18,4096\n"
EX"v56,v56\n"
MF"v[204:207],a[92:95],v[76:79],v[204:207]\n"
BL"v225,s[8:11],s44 offen offset:0 sc1 lds\n"
MF"a[132:135],a[128:131],v[64:67],a[132:135]\n"
SA"s40,s40,32768\n"
EX"v57,v57\n"
MF"a[136:139],a[128:131],v[68:71],a[136:139]\n"
"s_cmp_eq_u32 s40,s46\n"
RD"a[112:115],v227 offset:28672\n"
"s_cselect_b32 s40,s47,s40\n"
MF"a[140:143],a[128:131],v[72:75],a[140:143]\n"
EX"v58,v58\n"
MF"a[144:147],a[128:131],v[76:79],a[144:147]\n"
BS"a[164:167],v224,s[8:11],s18 offen offset:0 sc1\n"
MF"v[80:83],a[96:99],v[0:3],v[80:83]\n"
"s_lshr_b32 s21,s16,1\n"
EX"v59,v59\n"
MF"v[112:115],a[96:99],v[8:11],v[112:115]\n"
"s_sub_u32 s21,s21,s33\n"
RD"a[116:119],v227 offset:29696\n"
MF"v[144:147],a[96:99],v[16:19],v[144:147]\n"
"s_and_b32 s21,s21,3\n"
EX"v36,v36\n"
MF"v[176:179],a[96:99],v[24:27],v[176:179]\n"
WT"lgkmcnt(2)\n"
BL"v225,s[8:11],s44 offen offset:1024 sc1 lds\n"
MF"v[84:87],a[100:103],v[0:3],v[84:87]\n"
"s_lshl_b32 s21,s21,1\n"
EX"v37,v37\n"
MF"v[116:119],a[100:103],v[8:11],v[116:119]\n"
"s_and_b32 s22,s16,1\n"
RD"a[120:123],v227 offset:30720\n"
SA"s21,s21,s22\n"
MF"v[148:151],a[100:103],v[16:19],v[148:151]\n"
"s_lshl_b32 s21,s21,1\n"
EX"v38,v38\n"
MF"v[180:183],a[100:103],v[24:27],v[180:183]\n"
"s_and_b32 s22,s30,1\n"
MF"v[88:91],a[104:107],v[0:3],v[88:91]\n"
SA"s21,s21,s22\n"
EX"v39,v39\n"
MF"v[120:123],a[104:107],v[8:11],v[120:123]\n"
"s_mul_i32 s21,s21,10240\n"
RD"a[124:127],v227 offset:31744\n"
MF"v[152:155],a[104:107],v[16:19],v[152:155]\n"
"s_lshr_b32 s22,s30,1\n"
EX"v44,v44\n"
MF"v[184:187],a[104:107],v[24:27],v[184:187]\n"
"s_lshl_b32 s22,s22,10\n"
BL"v225,s[8:11],s44 offen offset:2048 sc1 lds\n"
MF"v[92:95],a[108:111],v[0:3],v[92:95]\n"
SA"s21,s21,s22\n"
EX"v45,v45\n"
MF"v[124:127],a[108:111],v[8:11],v[124:127]\n"
"s_mul_i32 s22,s35,163840\n"
MF"v[156:159],a[108:111],v[16:19],v[156:159]\n"
SA"s21,s21,s22\n"
EX"v46,v46\n"
MF"v[188:191],a[108:111],v[24:27],v[188:191]\n"
WT"lgkmcnt(1)\n"
SA"s18,s21,3428352\n"
MF"v[96:99],a[112:115],v[0:3],v[96:99]\n"
SA"s16,s16,1\n"
WT"lgkmcnt(0)\n"
"s_barrier\n"
MF"v[128:131],a[112:115],v[8:11],v[128:131]\n"
RD"a[64:67],v226 offset:0\n"
MF"v[160:163],a[112:115],v[16:19],v[160:163]\n"
EX"v47,v47\n"
MF"v[192:195],a[112:115],v[24:27],v[192:195]\n"
BL"v225,s[8:11],s44 offen offset:3072 sc1 lds\n"
MF"v[100:103],a[116:119],v[0:3],v[100:103]\n"
EX"v52,v52\n"
MF"v[132:135],a[116:119],v[8:11],v[132:135]\n"
RD"a[68:71],v226 offset:1024\n"
MF"v[164:167],a[116:119],v[16:19],v[164:167]\n"
EX"v53,v53\n"
MF"v[196:199],a[116:119],v[24:27],v[196:199]\n"
RD"a[72:75],v226 offset:2048\n"
MF"v[104:107],a[120:123],v[0:3],v[104:107]\n"
EX"v54,v54\n"
MF"v[136:139],a[120:123],v[8:11],v[136:139]\n"
RD"a[76:79],v226 offset:3072\n"
MF"v[168:171],a[120:123],v[16:19],v[168:171]\n"
EX"v55,v55\n"
MF"v[200:203],a[120:123],v[24:27],v[200:203]\n"
MF"v[108:111],a[124:127],v[0:3],v[108:111]\n"
EX"v60,v60\n"
MF"v[140:143],a[124:127],v[8:11],v[140:143]\n"
RD"a[80:83],v226 offset:4096\n"
MF"v[172:175],a[124:127],v[16:19],v[172:175]\n"
EX"v61,v61\n"
MF"v[204:207],a[124:127],v[24:27],v[204:207]\n"
RD"a[84:87],v226 offset:5120\n"
MF"a[132:135],a[128:131],v[0:3],a[132:135]\n"
EX"v62,v62\n"
MF"a[136:139],a[128:131],v[8:11],a[136:139]\n"
RD"a[88:91],v226 offset:6144\n"
MF"a[140:143],a[128:131],v[16:19],a[140:143]\n"
EX"v63,v63\n"
MF"a[144:147],a[128:131],v[24:27],a[144:147]\n"
"s_sub_u32 s20,s20,1\n"
"s_cbranch_scc0 LRT_%=\n"
"s_lshl_b32 s21,s35,8\n"
SA"s21,s21,81264640\n"
SA"s21,s21,s45\n"
WT"vmcnt(3)\n"
"s_barrier\n"
"buffer_store_dwordx2 v[232:233],v230,s[8:11],s21 offen sc1\n"
"v_cmp_eq_u64 vcc,s[28:29],v[244:245]\n"
"s_cmp_eq_u64 vcc,exec\n"
"s_cbranch_scc0 L1105_%=\n"
"L1106_%=:\n"
"s_mov_b32 s57,0\n"
"s_mov_b32 s56,0\n"
"s_mov_b32 s14,0\n"
"s_branch LP1_%=\n"
"LEND_%=:\n"
      : : [ka] "s"(__builtin_amdgcn_kernarg_segment_ptr()), [b] "s"(blockIdx.x), [t] "v"(threadIdx.x)
      : "v1", "v2", "v3", "v4", "v5", "v6", "v7", "v8", "v9", "v10", "v11", "v12", "v13", "v14", "v15", "v16", "v17", "v18", "v19", "v20", "v21", "v22", "v23", "v24", "v25", "v26", "v27", "v28", "v29", "v30", "v31", "v32", "v33", "v34", "v35", "v36", "v37", "v38", "v39", "v40", "v41", "v42", "v43", "v44", "v45", "v46", "v47", "v48", "v49", "v50", "v51", "v52", "v53", "v54", "v55", "v56", "v57", "v58", "v59", "v60", "v61", "v62", "v63", "v64", "v65", "v66", "v67", "v68", "v69", "v70", "v71", "v72", "v73", "v74", "v75", "v76", "v77", "v78", "v79", "v80", "v81", "v82", "v83", "v84", "v85", "v86", "v87", "v88", "v89", "v90", "v91", "v92", "v93", "v94", "v95", "v96", "v97", "v98", "v99", "v100", "v101", "v102", "v103", "v104", "v105", "v106", "v107", "v108", "v109", "v110", "v111", "v112", "v113", "v114", "v115", "v116", "v117", "v118", "v119", "v120", "v121", "v122", "v123", "v124", "v125", "v126", "v127", "v128", "v129", "v130", "v131", "v132", "v133", "v134", "v135", "v136", "v137", "v138", "v139", "v140", "v141", "v142", "v143", "v144", "v145", "v146", "v147", "v148", "v149", "v150", "v151", "v152", "v153", "v154", "v155", "v156", "v157", "v158", "v159", "v160", "v161", "v162", "v163", "v164", "v165", "v166", "v167", "v168", "v169", "v170", "v171", "v172", "v173", "v174", "v175", "v176", "v177", "v178", "v179", "v180", "v181", "v182", "v183", "v184", "v185", "v186", "v187", "v188", "v189", "v190", "v191", "v192", "v193", "v194", "v195", "v196", "v197", "v198", "v199", "v200", "v201", "v202", "v203", "v204", "v205", "v206", "v207", "v208", "v209", "v210", "v211", "v212", "v213", "v214", "v215", "v216", "v217", "v218", "v219", "v220", "v221", "v222", "v223", "v224", "v225", "v226", "v227", "v228", "v229", "v230", "v231", "v232", "v233", "v234", "v235", "v236", "v237", "v238", "v239", "v240", "v241", "v242", "v243", "v244", "v245", "v246", "v247", "v248", "v249", "v250", "v251", "v252", "v253", "v254", "v255", "a0", "a1", "a2", "a3", "a4", "a5", "a6", "a7", "a8", "a9", "a10", "a11", "a12", "a13", "a14", "a15", "a16", "a17", "a18", "a19", "a20", "a21", "a22", "a23", "a24", "a25", "a26", "a27", "a28", "a29", "a30", "a31", "a32", "a33", "a34", "a35", "a36", "a37", "a38", "a39", "a40", "a41", "a42", "a43", "a44", "a45", "a46", "a47", "a48", "a49", "a50", "a51", "a52", "a53", "a54", "a55", "a56", "a57", "a58", "a59", "a60", "a61", "a62", "a63", "a64", "a65", "a66", "a67", "a68", "a69", "a70", "a71", "a72", "a73", "a74", "a75", "a76", "a77", "a78", "a79", "a80", "a81", "a82", "a83", "a84", "a85", "a86", "a87", "a88", "a89", "a90", "a91", "a92", "a93", "a94", "a95", "a96", "a97", "a98", "a99", "a100", "a101", "a102", "a103", "a104", "a105", "a106", "a107", "a108", "a109", "a110", "a111", "a112", "a113", "a114", "a115", "a116", "a117", "a118", "a119", "a120", "a121", "a122", "a123", "a124", "a125", "a126", "a127", "a128", "a129", "a130", "a131", "a132", "a133", "a134", "a135", "a136", "a137", "a138", "a139", "a140", "a141", "a142", "a143", "a144", "a145", "a146", "a147", "a148", "a149", "a150", "a151", "a152", "a153", "a154", "a155", "a156", "a157", "a158", "a159", "a160", "a161", "a162", "a163", "a164", "a165", "a166", "a167", "a168", "a169", "a170", "a171", "a172", "a173", "a174", "a175", "a176", "a177", "a178", "a179", "a180", "a181", "a182", "a183", "a184", "a185", "a186", "a187", "a188", "a189", "a190", "a191", "a192", "a193", "a194", "a195", "a196", "a197", "a198", "a199", "a200", "a201", "a202", "a203", "a204", "a205", "a206", "a207", "a208", "a209", "a210", "a211", "a212", "a213", "a214", "a215", "a216", "a217", "a218", "a219", "a220", "a221", "a222", "a223", "a224", "a225", "a226", "a227", "a228", "a229", "a230", "a231", "a232", "a233", "a234", "a235", "a236", "a237", "a238", "a239", "a240", "a241", "a242", "a243", "a244", "a245", "a246", "a247", "a248", "a249", "a250", "a251", "a252", "a253", "a254", "a255", "s8", "s9", "s10", "s11", "s12", "s13", "s14", "s15", "s16", "s17", "s18", "s19", "s20", "s21", "s22", "s23", "s24", "s25", "s26", "s27", "s28", "s29", "s30", "s31", "s32", "s33", "s34", "s35", "s36", "s37", "s38", "s39", "s40", "s41", "s42", "s43", "s44", "s45", "s46", "s47", "s48", "s49", "s50", "s51", "s52", "s53", "s54", "s55", "s56", "s57", "s58", "s59", "s60", "s61", "s62", "s63", "s64", "s65", "s66", "s67", "s68", "s69", "s70", "s71", "s72", "s73", "s74", "s75", "s76", "s77", "s78", "s79", "s80", "s81", "s82", "s83", "s84", "s85", "s86", "s87", "s88", "s89", "s90", "s91", "s92", "s93", "s94", "s95", "s96", "s97", "s98", "s99", "scc", "vcc", "memory");
}

extern "C" void kernel_launch(void* const* d_in, const int* in_sizes, int n_in, void* d_out, int out_size, void* d_ws, size_t ws_size, hipStream_t stream) {
  (void)hipFuncSetAttribute((const void*)mega_kernel, hipFuncAttributeMaxDynamicSharedMemorySize, 163840);
  mega_kernel<<<256, 256, 163840, stream>>>((const bf16*)d_in[0], (const bf16*)d_in[1], (const bf16*)d_in[2], (const bf16*)d_in[3], (const bf16*)d_in[4], (const bf16*)d_in[5],
      (const bf16*)d_in[7], (const bf16*)d_in[8], (const bf16*)d_in[9], (const bf16*)d_in[10], (char*)d_ws + (size_t)0 * 1048576, (bf16*)d_out);
}
```

```cpp
#include <hip/hip_runtime.h>
#include <hip/hip_bf16.h>
typedef __hip_bfloat16 bf16;
#define MF "v_mfma_f32_16x16x32_bf16 "
#define RD "ds_read_b128 "
#define RT "ds_read_b64_tr_b16 "
#define BL "buffer_load_dwordx4 "
#define BS "buffer_store_dwordx4 "
#define PK "v_cvt_pk_bf16_f32 "
#define EX "v_exp_f32 "
#define WT "s_waitcnt "
#define MU "v_mul_f32 "
#define SA "s_add_u32 "

__global__ __launch_bounds__(256, 1) void mega_kernel(const bf16* x, const bf16* ckv, const bf16* wq, const bf16* wuk, const bf16* wuv, const bf16* wo, const bf16* w_r,
                                                      const bf16* wg, const bf16* wu, const bf16* wd, char* ws, bf16* out) {
  asm volatile(
"s_load_dwordx16 s[64:79],%[ka],0\n"
"s_load_dwordx8 s[80:87],%[ka],64\n"
"s_mov_b32 s31,%[b]\n"
"v_lshrrev_b32 v1,6,%[t]\n"
"v_and_b32 v2,63,%[t]\n"
"v_readfirstlane_b32 s30,v1\n"
"v_lshlrev_b32 v224,4,v2\n"
"s_and_b32 s32,s31,7\n"
"s_lshr_b32 s33,s31,6\n"
"s_bfe_u32 s34,s31,0x30003\n"
"s_lshl_b32 s35,s32,2\n"
SA"s35,s35,s33\n"
"s_mov_b32 s46,0x76543210\n"
"s_mov_b32 s47,0x43217650\n"
"s_bitcmp1_b32 s34,0\n"
"s_cselect_b32 s46,0x21765430,s46\n"
"s_cselect_b32 s47,0x65432170,s47\n"
"s_bitcmp1_b32 s34,1\n"
"s_cselect_b32 s46,s47,s46\n"
"s_mul_i32 s47,s32,0x11111111\n"
SA"s46,s46,s47\n"
"s_lshl_b32 s36,s30,12\n"
"s_mul_i32 s37,s30,24576\n"
SA"s37,s37,65536\n"
"v_lshrrev_b32 v3,3,v2\n"
"v_lshrrev_b32 v4,4,v2\n"
"v_and_b32 v5,7,v2\n"
"v_xor_b32 v6,v5,v4\n"
"v_lshlrev_b32 v6,4,v6\n"
"v_lshl_add_u32 v6,v3,10,v6\n"
"s_lshl_b32 s50,s30,15\n"
"v_add_u32 v234,s50,v6\n"
"v_xor_b32 v235,64,v234\n"
"v_add_u32 v235,7168,v235\n"
"v_add_u32 v236,14336,v234\n"
"v_add_u32 v237,14336,v235\n"
"v_and_b32 v7,1,v4\n"
"v_lshlrev_b32 v7,1,v7\n"
"v_xor_b32 v7,v7,v5\n"
"v_lshlrev_b32 v7,4,v7\n"
"v_lshl_add_u32 v238,v3,13,v7\n"
"v_xor_b32 v239,64,v238\n"
"v_add_u32 v239,64512,v239\n"
"v_add_u32 v240,129024,v238\n"
"v_add_u32 v241,129024,v239\n"
"v_and_b32 v8,31,v2\n"
"v_lshlrev_b32 v8,3,v8\n"
"v_lshrrev_b32 v9,5,v2\n"
"s_lshl_b32 s50,s31,2\n"
SA"s50,s50,s30\n"
"s_mul_i32 s50,s50,2560\n"
"v_mul_u32_u24 v9,1280,v9\n"
"v_add3_u32 v9,v9,v8,s50\n"
"s_lshl_b32 s50,s34,17\n"
"s_lshl_b32 s51,s35,8\n"
"s_and_b32 s52,s30,1\n"
"s_lshl_b32 s52,s52,7\n"
SA"s52,s52,s51\n"
"s_mov_b32 s14,0x120000\n"
"s_mov_b32 s15,0x20000\n"
"s_sub_u32 s18,4194304,s52\n"
"s_mov_b32 s19,0x20000\n"
"s_getpc_b64 s[24:25]\n"
"LPC_%=:\n"
"s_and_b32 s25,s25,0xffff\n"
"s_mov_b32 s26,LEND_%=-LPC_%=\n"
"s_mov_b32 s27,0x20000\n"
"s_lshr_b32 s49,s31,3\n"
"s_lshl_b32 s49,s49,2\n"
SA"s49,s49,s30\n"
"s_lshl_b32 s49,s49,7\n"
"v_lshlrev_b32 v10,14,v2\n"
"buffer_load_dword v255,v10,s[24:27],s49 offen\n"
WT"lgkmcnt(0)\n"
"s_load_dwordx2 s[28:29],s[84:85],0\n"
SA"s12,s66,s50\n"
"s_addc_u32 s13,s67,0\n"
"s_and_b32 s13,s13,0xffff\n"
"s_cmp_lt_u32 s30,2\n"
"s_cselect_b64 s[16:17],s[70:71],s[72:73]\n"
SA"s16,s16,s52\n"
"s_addc_u32 s17,s17,0\n"
"s_and_b32 s17,s17,0xffff\n"
"global_load_dwordx2 v[128:129],v9,s[64:65] offset:0\n"
"global_load_dwordx2 v[130:131],v9,s[64:65] offset:256\n"
"global_load_dwordx2 v[132:133],v9,s[64:65] offset:512\n"
"global_load_dwordx2 v[134:135],v9,s[64:65] offset:768\n"
"global_load_dwordx2 v[136:137],v9,s[64:65] offset:1024\n"
"global_load_dwordx2 v[138:139],v8,s[76:77] offset:0\n"
"global_load_dwordx2 v[140:141],v8,s[76:77] offset:256\n"
"global_load_dwordx2 v[142:143],v8,s[76:77] offset:512\n"
"global_load_dwordx2 v[144:145],v8,s[76:77] offset:768\n"
"global_load_dwordx2 v[146:147],v8,s[76:77] offset:1024\n"
SA"m0,s37,0\n"
"s_bfe_u32 s48,s46,0x30000\n"
"s_lshl_b32 s48,s48,19\n"
BL"v238,s[16:19],s48 offen offset:0 lds\n"
BL"v239,s[16:19],s48 offen offset:1024 lds\n"
BL"v240,s[16:19],s48 offen offset:2048 lds\n"
BL"v241,s[16:19],s48 offen offset:3072 lds\n"
SA"m0,s37,4096\n"
"s_bfe_u32 s48,s46,0x30000\n"
"s_lshl_b32 s48,s48,19\n"
"s_bitset1_b32 s48,18\n"
BL"v238,s[16:19],s48 offen offset:0 lds\n"
BL"v239,s[16:19],s48 offen offset:1024 lds\n"
BL"v240,s[16:19],s48 offen offset:2048 lds\n"
BL"v241,s[16:19],s48 offen offset:3072 lds\n"
"v_lshlrev_b32 v253,7,%[t]\n"
"s_mov_b32 s8,s84\n"
"s_and_b32 s9,s85,0xffff\n"
"s_mov_b32 s10,81272832\n"
"s_mov_b32 s11,0x20000\n"
SA"s20,s68,s51\n"
"s_addc_u32 s21,s69,0\n"
"s_and_b32 s21,s21,0xffff\n"
"s_sub_u32 s22,5242880,s51\n"
"s_mov_b32 s23,0x20000\n"
"v_and_b32 v11,15,v2\n"
"v_lshrrev_b32 v12,1,v11\n"
"v_xor_b32 v12,v12,v4\n"
"v_lshlrev_b32 v12,4,v12\n"
"v_lshl_add_u32 v242,v11,7,v12\n"
"v_xor_b32 v243,64,v242\n"
"v_lshrrev_b32 v12,2,v11\n"
"v_lshl_add_u32 v12,v4,3,v12\n"
"v_lshlrev_b32 v12,7,v12\n"
"v_and_b32 v13,3,v2\n"
"v_lshl_add_u32 v12,v13,3,v12\n"
"v_and_b32 v13,3,v3\n"
"v_lshl_add_u32 v12,v13,5,v12\n"
"v_add_u32 v246,s37,v12\n"
"v_xor_b32 v247,32,v246\n"
"v_xor_b32 v248,64,v246\n"
"v_xor_b32 v249,96,v246\n"
"v_mov_b32 v13,2130706432\n"
"v_cmp_eq_u32 vcc,0,v2\n"
"s_cmp_eq_u32 s30,0\n"
"s_cselect_b32 s45,0,2130706432\n"
"v_cndmask_b32_e64 v230,v13,0,vcc\n"
"s_lshl_b32 s39,s30,3\n"
"s_lshr_b32 s98,s46,s39\n"
"s_and_b32 s39,s98,7\n"
"s_lshl_b32 s39,s39,7\n"
"s_and_b32 s98,s98,0x70\n"
"s_lshl_b32 s98,s98,3\n"
"s_lshl_b32 s50,s30,15\n"
"s_sub_u32 s50,1048576,s50\n"
SA"s39,s39,s50\n"
SA"s98,s98,s50\n"
"s_mov_b32 s55,0x3e0293ee\n"
"s_cmp_lt_u32 s30,2\n"
"s_cbranch_scc0 LV_%=\n"
WT"vmcnt(17)\n"
"v_lshlrev_b32 v148,16,v128\n"
"v_and_b32 v149,0xffff0000,v128\n"
"v_lshlrev_b32 v150,16,v129\n"
"v_and_b32 v151,0xffff0000,v129\n"
WT"vmcnt(16)\n"
"v_lshlrev_b32 v152,16,v130\n"
"v_and_b32 v153,0xffff0000,v130\n"
"v_lshlrev_b32 v154,16,v131\n"
"v_and_b32 v155,0xffff0000,v131\n"
WT"vmcnt(15)\n"
"v_lshlrev_b32 v156,16,v132\n"
"v_and_b32 v157,0xffff0000,v132\n"
"v_lshlrev_b32 v158,16,v133\n"
"v_and_b32 v159,0xffff0000,v133\n"
WT"vmcnt(14)\n"
"v_lshlrev_b32 v160,16,v134\n"
"v_and_b32 v161,0xffff0000,v134\n"
"v_lshlrev_b32 v162,16,v135\n"
"v_and_b32 v163,0xffff0000,v135\n"
WT"vmcnt(13)\n"
"v_lshlrev_b32 v164,16,v136\n"
"v_and_b32 v165,0xffff0000,v136\n"
"v_lshlrev_b32 v166,16,v137\n"
"v_and_b32 v167,0xffff0000,v137\n"
"v_add_f32 v188,v148,v149\n"
"v_add_f32 v189,v150,v151\n"
"v_add_f32 v188,v188,v152\n"
"v_add_f32 v189,v189,v153\n"
"v_add_f32 v188,v188,v154\n"
"v_add_f32 v189,v189,v155\n"
"v_add_f32 v188,v188,v156\n"
"v_add_f32 v189,v189,v157\n"
"v_add_f32 v188,v188,v158\n"
"v_add_f32 v189,v189,v159\n"
"v_add_f32 v188,v188,v160\n"
"v_add_f32 v189,v189,v161\n"
"v_add_f32 v188,v188,v162\n"
"v_add_f32 v189,v189,v163\n"
"v_add_f32 v188,v188,v164\n"
"v_add_f32 v189,v189,v165\n"
"v_add_f32 v188,v188,v166\n"
"v_add_f32 v189,v189,v167\n"
"v_add_f32 v188,v188,v189\n"
"s_nop 1\n"
"v_add_f32_dpp v188,v188,v188 quad_perm:[1,0,3,2] row_mask:0xf bank_mask:0xf\n"
WT"vmcnt(12)\n"
"v_lshlrev_b32 v168,16,v138\n"
"v_and_b32 v169,0xffff0000,v138\n"
"v_add_f32_dpp v188,v188,v188 quad_perm:[2,3,0,1] row_mask:0xf bank_mask:0xf\n"
"v_lshlrev_b32 v170,16,v139\n"
"v_and_b32 v171,0xffff0000,v139\n"
"v_add_f32_dpp v188,v188,v188 row_half_mirror row_mask:0xf bank_mask:0xf\n"
WT"vmcnt(11)\n"
"v_lshlrev_b32 v172,16,v140\n"
SA"m0,s36,0\n"
"s_bfe_u32 s48,s46,0x30000\n"
"s_lshl_b32 s48,s48,7\n"
BL"v234,s[12:15],s48 offen offset:0 lds\n"
BL"v235,s[12:15],s48 offen offset:1024 lds\n"
BL"v236,s[12:15],s48 offen offset:2048 lds\n"
BL"v237,s[12:15],s48 offen offset:3072 lds\n"
"v_and_b32 v173,0xffff0000,v140\n"
"v_add_f32_dpp v188,v188,v188 row_mirror row_mask:0xf bank_mask:0xf\n"
"v_lshlrev_b32 v174,16,v141\n"
"v_and_b32 v175,0xffff0000,v141\n"
"v_mov_b32 v189,v188\n"
WT"vmcnt(14)\n"
"v_lshlrev_b32 v176,16,v142\n"
"v_and_b32 v177,0xffff0000,v142\n"
"v_permlane16_swap_b32 v189,v188\n"
"v_lshlrev_b32 v178,16,v143\n"
"v_and_b32 v179,0xffff0000,v143\n"
"v_add_f32 v188,v188,v189\n"
WT"vmcnt(13)\n"
"v_lshlrev_b32 v180,16,v144\n"
"v_and_b32 v181,0xffff0000,v144\n"
MU"v191,0x3acccccd,v188\n"
"v_lshlrev_b32 v182,16,v145\n"
"v_and_b32 v183,0xffff0000,v145\n"
PK"v128,v191,v191\n"
WT"vmcnt(12)\n"
"v_lshlrev_b32 v184,16,v146\n"
"v_and_b32 v185,0xffff0000,v146\n"
"v_lshlrev_b32 v128,16,v128\n"
"v_lshlrev_b32 v186,16,v147\n"
"v_and_b32 v187,0xffff0000,v147\n"
"v_sub_f32 v148,v148,v128\n"
"v_sub_f32 v149,v149,v128\n"
"v_sub_f32 v150,v150,v128\n"
"v_sub_f32 v151,v151,v128\n"
"v_sub_f32 v152,v152,v128\n"
"v_sub_f32 v153,v153,v128\n"
"v_sub_f32 v154,v154,v128\n"
"v_sub_f32 v155,v155,v128\n"
"v_sub_f32 v156,v156,v128\n"
"v_sub_f32 v157,v157,v128\n"
"v_sub_f32 v158,v158,v128\n"
"v_sub_f32 v159,v159,v128\n"
"v_sub_f32 v160,v160,v128\n"
"v_sub_f32 v161,v161,v128\n"
"v_sub_f32 v162,v162,v128\n"
"v_sub_f32 v163,v163,v128\n"
"v_sub_f32 v164,v164,v128\n"
"v_sub_f32 v165,v165,v128\n"
"v_sub_f32 v166,v166,v128\n"
"v_sub_f32 v167,v167,v128\n"
MU"v188,v148,v148\n"
MU"v189,v149,v149\n"
"v_fmac_f32 v188,v150,v150\n"
"v_fmac_f32 v189,v151,v151\n"
"v_fmac_f32 v188,v152,v152\n"
SA"m0,s37,8192\n"
"s_bfe_u32 s48,s46,0x30004\n"
"s_lshl_b32 s48,s48,19\n"
BL"v238,s[16:19],s48 offen offset:0 lds\n"
BL"v239,s[16:19],s48 offen offset:1024 lds\n"
BL"v240,s[16:19],s48 offen offset:2048 lds\n"
BL"v241,s[16:19],s48 offen offset:3072 lds\n"
"v_fmac_f32 v189,v153,v153\n"
"v_fmac_f32 v188,v154,v154\n"
"v_fmac_f32 v189,v155,v155\n"
"v_fmac_f32 v188,v156,v156\n"
"v_fmac_f32 v189,v157,v157\n"
"v_fmac_f32 v188,v158,v158\n"
"v_fmac_f32 v189,v159,v159\n"
"v_fmac_f32 v188,v160,v160\n"
"v_fmac_f32 v189,v161,v161\n"
"v_fmac_f32 v188,v162,v162\n"
"v_fmac_f32 v189,v163,v163\n"
"v_fmac_f32 v188,v164,v164\n"
"v_fmac_f32 v189,v165,v165\n"
"v_fmac_f32 v188,v166,v166\n"
"v_fmac_f32 v189,v167,v167\n"
"v_add_f32 v188,v188,v189\n"
PK"v149,v148,v149\n"
"v_lshlrev_b32 v148,16,v149\n"
"v_add_f32_dpp v188,v188,v188 quad_perm:[1,0,3,2] row_mask:0xf bank_mask:0xf\n"
"v_and_b32 v149,0xffff0000,v149\n"
PK"v151,v150,v151\n"
"v_add_f32_dpp v188,v188,v188 quad_perm:[2,3,0,1] row_mask:0xf bank_mask:0xf\n"
"v_lshlrev_b32 v150,16,v151\n"
"v_and_b32 v151,0xffff0000,v151\n"
"v_add_f32_dpp v188,v188,v188 row_half_mirror row_mask:0xf bank_mask:0xf\n"
PK"v153,v152,v153\n"
"v_lshlrev_b32 v152,16,v153\n"
"v_add_f32_dpp v188,v188,v188 row_mirror row_mask:0xf bank_mask:0xf\n"
"v_and_b32 v153,0xffff0000,v153\n"
PK"v155,v154,v155\n"
"v_mov_b32 v189,v188\n"
"v_lshlrev_b32 v154,16,v155\n"
"v_and_b32 v155,0xffff0000,v155\n"
"v_permlane16_swap_b32 v189,v188\n"
PK"v157,v156,v157\n"
"v_lshlrev_b32 v156,16,v157\n"
"v_add_f32 v188,v188,v189\n"
"v_and_b32 v157,0xffff0000,v157\n"
PK"v159,v158,v159\n"
"v_sub_f32 v191,v191,v128\n"
"v_lshlrev_b32 v158,16,v159\n"
"v_and_b32 v159,0xffff0000,v159\n"
MU"v188,0x3acccccd,v188\n"
PK"v161,v160,v161\n"
"v_lshlrev_b32 v160,16,v161\n"
"v_fma_f32 v188,-v191,v191,v188\n"
"v_and_b32 v161,0xffff0000,v161\n"
PK"v163,v162,v163\n"
SA"m0,s37,12288\n"
"s_bfe_u32 s48,s46,0x30004\n"
"s_lshl_b32 s48,s48,19\n"
"s_bitset1_b32 s48,18\n"
BL"v238,s[16:19],s48 offen offset:0 lds\n"
BL"v239,s[16:19],s48 offen offset:1024 lds\n"
BL"v240,s[16:19],s48 offen offset:2048 lds\n"
BL"v241,s[16:19],s48 offen offset:3072 lds\n"
"v_max_f32 v188,0,v188\n"
"v_lshlrev_b32 v162,16,v163\n"
"v_and_b32 v163,0xffff0000,v163\n"
PK"v188,v188,v188\n"
PK"v165,v164,v165\n"
"v_lshlrev_b32 v164,16,v165\n"
"v_lshlrev_b32 v188,16,v188\n"
"v_and_b32 v165,0xffff0000,v165\n"
PK"v167,v166,v167\n"
"v_add_f32 v188,0x3727c5ac,v188\n"
"v_lshlrev_b32 v166,16,v167\n"
"v_and_b32 v167,0xffff0000,v167\n"
PK"v188,v188,v188\n"
"v_bfe_u32 v130,v224,5,4\n"
"v_mul_u32_u24 v130,144,v130\n"
"v_lshlrev_b32 v188,16,v188\n"
"v_bfe_u32 v131,v224,4,1\n"
"v_lshl_add_u32 v130,v131,3,v130\n"
"v_sqrt_f32 v189,v188\n"
"v_lshrrev_b32 v131,9,v224\n"
"v_lshl_add_u32 v130,v131,4,v130\n"
"v_rcp_f32 v190,v189\n"
"s_lshl_b32 s52,s30,5\n"
"v_add_u32 v130,s52,v130\n"
"v_fma_f32 v188,-v189,v189,v188\n"
MU"v190,0.5,v190\n"
"v_fma_f32 v189,v188,v190,v189\n"
PK"v189,v189,v189\n"
"v_lshlrev_b32 v189,16,v189\n"
"v_rcp_f32 v129,v189\n"
"s_nop 0\n"
MU"v148,v148,v129\n"
MU"v149,v149,v129\n"
PK"v138,v148,v149\n"
MU"v150,v150,v129\n"
MU"v151,v151,v129\n"
PK"v139,v150,v151\n"
"ds_write_b64 v130,v[138:139] offset:49152\n"
MU"v152,v152,v129\n"
MU"v153,v153,v129\n"
PK"v140,v152,v153\n"
MU"v154,v154,v129\n"
MU"v155,v155,v129\n"
PK"v141,v154,v155\n"
"ds_write_b64 v130,v[140:141] offset:51456\n"
MU"v156,v156,v129\n"
MU"v157,v157,v129\n"
PK"v142,v156,v157\n"
SA"m0,s36,16384\n"
"s_bfe_u32 s48,s46,0x30004\n"
"s_lshl_b32 s48,s48,7\n"
BL"v234,s[12:15],s48 offen offset:0 lds\n"
BL"v235,s[12:15],s48 offen offset:1024 lds\n"
BL"v236,s[12:15],s48 offen offset:2048 lds\n"
BL"v237,s[12:15],s48 offen offset:3072 lds\n"
MU"v158,v158,v129\n"
MU"v159,v159,v129\n"
PK"v143,v158,v159\n"
"ds_write_b64 v130,v[142:143] offset:53760\n"
MU"v160,v160,v129\n"
MU"v161,v161,v129\n"
PK"v144,v160,v161\n"
MU"v162,v162,v129\n"
MU"v163,v163,v129\n"
PK"v145,v162,v163\n"
"ds_write_b64 v130,v[144:145] offset:56064\n"
MU"v164,v164,v129\n"
MU"v165,v165,v129\n"
PK"v146,v164,v165\n"
MU"v166,v166,v129\n"
MU"v167,v167,v129\n"
PK"v147,v166,v167\n"
"ds_write_b64 v130,v[146:147] offset:58368\n"
"v_lshlrev_b32 v148,16,v138\n"
"v_and_b32 v149,0xffff0000,v138\n"
MU"v188,v148,v168\n"
MU"v189,v149,v169\n"
"v_lshlrev_b32 v150,16,v139\n"
"v_and_b32 v151,0xffff0000,v139\n"
"v_fmac_f32 v188,v150,v170\n"
"v_fmac_f32 v189,v151,v171\n"
"v_lshlrev_b32 v152,16,v140\n"
"v_and_b32 v153,0xffff0000,v140\n"
"v_fmac_f32 v188,v152,v172\n"
"v_fmac_f32 v189,v153,v173\n"
"v_lshlrev_b32 v154,16,v141\n"
"v_and_b32 v155,0xffff0000,v141\n"
"v_fmac_f32 v188,v154,v174\n"
"v_fmac_f32 v189,v155,v175\n"
"v_lshlrev_b32 v156,16,v142\n"
"v_and_b32 v157,0xffff0000,v142\n"
"v_fmac_f32 v188,v156,v176\n"
"v_fmac_f32 v189,v157,v177\n"
"v_lshlrev_b32 v158,16,v143\n"
"v_and_b32 v159,0xffff0000,v143\n"
"v_fmac_f32 v188,v158,v178\n"
"v_fmac_f32 v189,v159,v179\n"
"v_lshlrev_b32 v160,16,v144\n"
"v_and_b32 v161,0xffff0000,v144\n"
"v_fmac_f32 v188,v160,v180\n"
"v_fmac_f32 v189,v161,v181\n"
"v_lshlrev_b32 v162,16,v145\n"
"v_and_b32 v163,0xffff0000,v145\n"
WT"lgkmcnt(0)\n"
SA"s28,s28,0x7f4a7c15\n"
"s_addc_u32 s29,s29,0x9e3779b9\n"
"v_mov_b32 v232,s28\n"
"v_mov_b32 v233,s29\n"
WT"vmcnt(20)\n"
RT"a[0:1],v246 offset:0\n"
RT"a[2:3],v246 offset:512\n"
RT"a[4:5],v247 offset:0\n"
RT"a[6:7],v247 offset:512\n"
RT"a[8:9],v248 offset:0\n"
RT"a[10:11],v248 offset:512\n"
RT"a[12:13],v249 offset:0\n"
RT"a[14:15],v249 offset:512\n"
WT"vmcnt(16)\n"
RT"a[16:17],v246 offset:4096\n"
RT"a[18:19],v246 offset:4608\n"
RT"a[20:21],v247 offset:4096\n"
RT"a[22:23],v247 offset:4608\n"
RT"a[24:25],v248 offset:4096\n"
RT"a[26:27],v248 offset:4608\n"
RT"a[28:29],v249 offset:4096\n"
RT"a[30:31],v249 offset:4608\n"
WT"vmcnt(12) lgkmcnt(15)\n"
"s_barrier\n"
RD"v[192:195],v242 offset:0\n"
RD"v[196:199],v243 offset:0\n"
RD"v[200:203],v242 offset:2048\n"
RD"v[204:207],v243 offset:2048\n"
RD"v[208:211],v242 offset:4096\n"
"v_lshrrev_b32 v131,7,v224\n"
WT"lgkmcnt(4)\n"
MF"v[0:3],a[0:3],v[192:195],0\n"
WT"lgkmcnt(3)\n"
RD"v[212:215],v243 offset:4096\n"
"v_lshl_add_u32 v131,v131,4,v224\n"
MF"v[0:3],a[16:19],v[196:199],v[0:3]\n"
SA"m0,s37,16384\n"
"s_bfe_u32 s48,s46,0x30008\n"
"s_lshl_b32 s48,s48,19\n"
MF"v[4:7],a[20:23],v[196:199],0\n"
"s_mul_i32 s52,s30,1152\n"
BL"v238,s[16:19],s48 offen offset:0 lds\n"
MF"v[4:7],a[4:7],v[192:195],v[4:7]\n"
"v_add_u32 v128,s52,v131\n"
"s_and_b32 s52,s30,1\n"
MF"v[8:11],a[8:11],v[192:195],0\n"
"s_mul_i32 s51,s52,1152\n"
RD"v[216:219],v242 offset:6144\n"
"v_add_u32 v131,s51,v131\n"
MF"v[8:11],a[24:27],v[196:199],v[8:11]\n"
"s_lshl_b32 s52,s52,10\n"
"v_fmac_f32 v188,v162,v182\n"
"v_fmac_f32 v189,v163,v183\n"
MF"v[12:15],a[28:31],v[196:199],0\n"
BL"v239,s[16:19],s48 offen offset:1024 lds\n"
MF"v[12:15],a[12:15],v[192:195],v[12:15]\n"
WT"lgkmcnt(1)\n"
RD"v[220:223],v243 offset:6144\n"
RD"v[132:135],v128 offset:49152\n"
MF"v[16:19],a[0:3],v[200:203],0\n"
WT"vmcnt(10)\n"
RT"a[32:33],v246 offset:8192\n"
RD"a[240:243],v128 offset:53760\n"
MF"v[16:19],a[16:19],v[204:207],v[16:19]\n"
"s_lshr_b32 s50,s31,1\n"
RD"a[244:247],v131 offset:58368\n"
"v_lshlrev_b32 v164,16,v146\n"
MF"v[20:23],a[20:23],v[204:207],0\n"
"s_mul_i32 s50,s50,20480\n"
"v_and_b32 v129,0x70,v224\n"
"v_and_b32 v165,0xffff0000,v146\n"
MF"v[20:23],a[4:7],v[200:203],v[20:23]\n"
"s_and_b32 s51,s31,1\n"
RD"v[192:195],v242 offset:8192\n"
RT"a[34:35],v246 offset:8704\n"
MF"v[24:27],a[8:11],v[200:203],0\n"
"s_lshl_b32 s51,s51,7\n"
BL"v240,s[16:19],s48 offen offset:2048 lds\n"
MF"v[24:27],a[24:27],v[204:207],v[24:27]\n"
SA"s50,s50,s51\n"
"v_and_b32 v130,0x380,v224\n"
"v_fmac_f32 v188,v164,v184\n"
MF"v[28:31],a[28:31],v[204:207],0\n"
SA"s50,s50,65536\n"
RT"a[36:37],v247 offset:8192\n"
"v_lshl_add_u32 v129,v130,1,v129\n"
MF"v[28:31],a[12:15],v[200:203],v[28:31]\n"
"s_lshl_b32 s52,s52,1\n"
RD"v[196:199],v243 offset:8192\n"
"v_fmac_f32 v189,v165,v185\n"
MF"v[32:35],a[0:3],v[208:211],0\n"
SA"s52,s52,s50\n"
BL"v241,s[16:19],s48 offen offset:3072 lds\n"
MF"v[32:35],a[16:19],v[212:215],v[32:35]\n"
SA"s52,s52,16384\n"
RT"a[38:39],v247 offset:8704\n"
"v_lshlrev_b32 v166,16,v147\n"
MF"v[36:39],a[20:23],v[212:215],0\n"
SA"m0,s37,20480\n"
RD"v[200:203],v242 offset:10240\n"
"v_and_b32 v167,0xffff0000,v147\n"
MF"v[36:39],a[4:7],v[208:211],v[36:39]\n"
"s_bfe_u32 s48,s46,0x30008\n"
"v_fmac_f32 v188,v166,v186\n"
"v_fmac_f32 v189,v167,v187\n"
MF"v[40:43],a[8:11],v[208:211],0\n"
"s_lshl_b32 s48,s48,19\n"
RT"a[40:41],v248 offset:8192\n"
"v_add_f32 v188,v188,v189\n"
MF"v[40:43],a[24:27],v[212:215],v[40:43]\n"
"s_bitset1_b32 s48,18\n"
"v_add_f32_dpp v188,v188,v188 quad_perm:[1,0,3,2] row_mask:0xf bank_mask:0xf\n"
"s_lshl_b32 s51,s30,11\n"
MF"v[44:47],a[28:31],v[212:215],0\n"
SA"s50,s50,s51\n"
BL"v238,s[16:19],s48 offen offset:0 lds\n"
MF"v[44:47],a[12:15],v[208:211],v[44:47]\n"
WT"lgkmcnt(3)\n"
RD"v[204:207],v243 offset:10240\n"
RT"a[42:43],v248 offset:8704\n"
MF"v[48:51],a[0:3],v[216:219],0\n"
SA"s51,s50,8192\n"
BS"v[132:135],v129,s[8:11],s50 offen sc1\n"
MF"v[48:51],a[16:19],v[220:223],v[48:51]\n"
"s_lshl_b32 s90,s31,2\n"
BL"v239,s[16:19],s48 offen offset:1024 lds\n"
MF"v[52:55],a[20:23],v[220:223],0\n"
SA"s90,s90,s30\n"
BS"a[240:243],v129,s[8:11],s51 offen sc1\n"
MF"v[52:55],a[4:7],v[216:219],v[52:55]\n"
"s_lshl_b32 s90,s90,1\n"
RD"v[208:211],v242 offset:12288\n"
RT"a[44:45],v249 offset:8192\n"
MF"v[56:59],a[8:11],v[216:219],0\n"
"s_sub_u32 s91,2047,s90\n"
BS"a[244:247],v129,s[8:11],s52 offen sc1\n"
MF"v[56:59],a[24:27],v[220:223],v[56:59]\n"
RD"v[212:215],v243 offset:12288\n"
RT"a[46:47],v249 offset:8704\n"
MF"v[60:63],a[28:31],v[220:223],0\n"
BL"v240,s[16:19],s48 offen offset:2048 lds\n"
MF"v[60:63],a[12:15],v[216:219],v[60:63]\n"
"v_add_f32_dpp v188,v188,v188 quad_perm:[2,3,0,1] row_mask:0xf bank_mask:0xf\n"
MF"v[64:67],a[0:3],v[192:195],0\n"
RD"v[216:219],v242 offset:14336\n"
"v_add_f32_dpp v188,v188,v188 row_half_mirror row_mask:0xf bank_mask:0xf\n"
MF"v[64:67],a[16:19],v[196:199],v[64:67]\n"
WT"vmcnt(14)\n"
RT"a[48:49],v246 offset:12288\n"
"v_add_f32_dpp v188,v188,v188 row_mirror row_mask:0xf bank_mask:0xf\n"
MF"v[68:71],a[20:23],v[196:199],0\n"
"s_lshl_b32 s90,s90,2\n"
BL"v241,s[16:19],s48 offen offset:3072 lds\n"
MF"v[68:71],a[4:7],v[192:195],v[68:71]\n"
SA"s90,s90,2686976\n"
RT"a[50:51],v246 offset:12800\n"
"v_mov_b32 v189,v188\n"
MF"v[72:75],a[8:11],v[192:195],0\n"
SA"m0,s36,32768\n"
RD"v[220:223],v243 offset:14336\n"
"v_permlane16_swap_b32 v189,v188\n"
MF"v[72:75],a[24:27],v[196:199],v[72:75]\n"
"s_bfe_u32 s48,s46,0x30008\n"
"v_add_f32 v188,v188,v189\n"
PK"v188,v188,v188\n"
MF"v[76:79],a[28:31],v[196:199],0\n"
"s_lshl_b32 s48,s48,7\n"
RT"a[52:53],v247 offset:12288\n"
"v_lshlrev_b32 v188,16,v188\n"
MF"v[76:79],a[12:15],v[192:195],v[76:79]\n"
WT"lgkmcnt(4)\n"
BL"v234,s[12:15],s48 offen offset:0 lds\n"
MF"v[80:83],a[0:3],v[200:203],0\n"
"v_ashrrev_i32 v189,31,v188\n"
"v_or_b32 v189,0x80000000,v189\n"
MF"v[80:83],a[16:19],v[204:207],v[80:83]\n"
RT"a[54:55],v247 offset:12800\n"
"v_xor_b32 v188,v188,v189\n"
MF"v[84:87],a[20:23],v[204:207],0\n"
BL"v235,s[12:15],s48 offen offset:1024 lds\n"
MF"v[84:87],a[4:7],v[200:203],v[84:87]\n"
"v_and_b32 v188,0xffff0000,v188\n"
"v_lshrrev_b32 v189,9,v224\n"
MF"v[88:91],a[8:11],v[200:203],0\n"
RT"a[56:57],v248 offset:12288\n"
"v_sub_u32 v190,s91,v189\n"
MF"v[88:91],a[24:27],v[204:207],v[88:91]\n"
WT"vmcnt(13) lgkmcnt(3)\n"
"s_barrier\n"
MF"v[92:95],a[28:31],v[204:207],0\n"
RD"v[192:195],v242 offset:16384\n"
"v_or_b32 v188,v188,v190\n"
MF"v[92:95],a[12:15],v[200:203],v[92:95]\n"
RT"a[58:59],v248 offset:12800\n"
"v_and_b32 v190,0x1f0,v224\n"
MF"v[96:99],a[0:3],v[208:211],0\n"
BL"v236,s[12:15],s48 offen offset:2048 lds\n"
MF"v[96:99],a[16:19],v[212:215],v[96:99]\n"
"v_lshlrev_b32 v190,23,v190\n"
"v_lshl_add_u32 v190,v189,2,v190\n"
MF"v[100:103],a[20:23],v[212:215],0\n"
"buffer_store_dword v188,v190,s[8:11],s90 offen sc1\n"
MF"v[100:103],a[4:7],v[208:211],v[100:103]\n"
RD"v[196:199],v243 offset:16384\n"
RT"a[60:61],v249 offset:12288\n"
MF"v[104:107],a[8:11],v[208:211],0\n"
BL"v237,s[12:15],s48 offen offset:3072 lds\n"
MF"v[104:107],a[24:27],v[212:215],v[104:107]\n"
RT"a[62:63],v249 offset:12800\n"
MF"v[108:111],a[28:31],v[212:215],0\n"
SA"m0,s37,0\n"
RD"v[200:203],v242 offset:18432\n"
"s_bfe_u32 s48,s46,0x3000c\n"
MF"v[108:111],a[12:15],v[208:211],v[108:111]\n"
"s_lshl_b32 s48,s48,19\n"
MF"v[112:115],a[0:3],v[216:219],0\n"
BL"v238,s[16:19],s48 offen offset:0 lds\n"
MF"v[112:115],a[16:19],v[220:223],v[112:115]\n"
RD"v[204:207],v243 offset:18432\n"
MF"v[116:119],a[20:23],v[220:223],0\n"
MF"v[116:119],a[4:7],v[216:219],v[116:119]\n"
MF"v[120:123],a[8:11],v[216:219],0\n"
BL"v239,s[16:19],s48 offen offset:1024 lds\n"
MF"v[120:123],a[24:27],v[220:223],v[120:123]\n"
RD"v[208:211],v242 offset:20480\n"
MF"v[124:127],a[28:31],v[220:223],0\n"
MF"v[124:127],a[12:15],v[216:219],v[124:127]\n"
WT"lgkmcnt(2)\n"
MF"v[0:3],a[32:35],v[192:195],v[0:3]\n"
RD"v[212:215],v243 offset:20480\n"
MF"v[0:3],a[48:51],v[196:199],v[0:3]\n"
BL"v240,s[16:19],s48 offen offset:2048 lds\n"
MF"v[4:7],a[52:55],v[196:199],v[4:7]\n"
MF"v[4:7],a[36:39],v[192:195],v[4:7]\n"
MF"v[8:11],a[40:43],v[192:195],v[8:11]\n"
RD"v[216:219],v242 offset:22528\n"
MF"v[8:11],a[56:59],v[196:199],v[8:11]\n"
BL"v241,s[16:19],s48 offen offset:3072 lds\n"
MF"v[12:15],a[60:63],v[196:199],v[12:15]\n"
MF"v[12:15],a[44:47],v[192:195],v[12:15]\n"
RD"v[220:223],v243 offset:22528\n"
MF"v[16:19],a[32:35],v[200:203],v[16:19]\n"
WT"lgkmcnt(2)\n"
WT"vmcnt(16)\n"
RT"a[64:65],v246 offset:16384\n"
MF"v[16:19],a[48:51],v[204:207],v[16:19]\n"
SA"m0,s37,4096\n"
"s_bfe_u32 s48,s46,0x3000c\n"
"s_lshl_b32 s48,s48,19\n"
MF"v[20:23],a[52:55],v[204:207],v[20:23]\n"
"s_bitset1_b32 s48,18\n"
BL"v238,s[16:19],s48 offen offset:0 lds\n"
MF"v[20:23],a[36:39],v[200:203],v[20:23]\n"
RD"v[192:195],v242 offset:24576\n"
RT"a[66:67],v246 offset:16896\n"
MF"v[24:27],a[40:43],v[200:203],v[24:27]\n"
MF"v[24:27],a[56:59],v[204:207],v[24:27]\n"
MF"v[28:31],a[60:63],v[204:207],v[28:31]\n"
RT"a[68:69],v247 offset:16384\n"
MF"v[28:31],a[44:47],v[200:203],v[28:31]\n"
BL"v239,s[16:19],s48 offen offset:1024 lds\n"
MF"v[32:35],a[32:35],v[208:211],v[32:35]\n"
RD"v[196:199],v243 offset:24576\n"
MF"v[32:35],a[48:51],v[212:215],v[32:35]\n"
RT"a[70:71],v247 offset:16896\n"
MF"v[36:39],a[52:55],v[212:215],v[36:39]\n"
RD"v[200:203],v242 offset:26624\n"
MF"v[36:39],a[36:39],v[208:211],v[36:39]\n"
BL"v240,s[16:19],s48 offen offset:2048 lds\n"
MF"v[40:43],a[40:43],v[208:211],v[40:43]\n"
RT"a[72:73],v248 offset:16384\n"
MF"v[40:43],a[56:59],v[212:215],v[40:43]\n"
MF"v[44:47],a[60:63],v[212:215],v[44:47]\n"
RD"v[204:207],v243 offset:26624\n"
MF"v[44:47],a[44:47],v[208:211],v[44:47]\n"
WT"lgkmcnt(4)\n"
RT"a[74:75],v248 offset:16896\n"
MF"v[48:51],a[32:35],v[216:219],v[48:51]\n"
BL"v241,s[16:19],s48 offen offset:3072 lds\n"
MF"v[48:51],a[48:51],v[220:223],v[48:51]\n"
RD"v[208:211],v242 offset:28672\n"
MF"v[52:55],a[52:55],v[220:223],v[52:55]\n"
SA"m0,s36,0\n"
RT"a[76:77],v249 offset:16384\n"
"s_bfe_u32 s48,s46,0x3000c\n"
MF"v[52:55],a[36:39],v[216:219],v[52:55]\n"
"s_lshl_b32 s48,s48,7\n"
MF"v[56:59],a[40:43],v[216:219],v[56:59]\n"
BL"v234,s[12:15],s48 offen offset:0 lds\n"
MF"v[56:59],a[56:59],v[220:223],v[56:59]\n"
RD"v[212:215],v243 offset:28672\n"
RT"a[78:79],v249 offset:16896\n"
MF"v[60:63],a[60:63],v[220:223],v[60:63]\n"
MF"v[60:63],a[44:47],v[216:219],v[60:63]\n"
MF"v[64:67],a[32:35],v[192:195],v[64:67]\n"
RD"v[216:219],v242 offset:30720\n"
MF"v[64:67],a[48:51],v[196:199],v[64:67]\n"
WT"vmcnt(14)\n"
RT"a[80:81],v246 offset:20480\n"
MF"v[68:71],a[52:55],v[196:199],v[68:71]\n"
BL"v235,s[12:15],s48 offen offset:1024 lds\n"
MF"v[68:71],a[36:39],v[192:195],v[68:71]\n"
RT"a[82:83],v246 offset:20992\n"
MF"v[72:75],a[40:43],v[192:195],v[72:75]\n"
RD"v[220:223],v243 offset:30720\n"
MF"v[72:75],a[56:59],v[196:199],v[72:75]\n"
BL"v236,s[12:15],s48 offen offset:2048 lds\n"
MF"v[76:79],a[60:63],v[196:199],v[76:79]\n"
RT"a[84:85],v247 offset:20480\n"
MF"v[76:79],a[44:47],v[192:195],v[76:79]\n"
WT"lgkmcnt(4)\n"
MF"v[80:83],a[32:35],v[200:203],v[80:83]\n"
"s_lshl_b32 s51,s32,8\n"
"s_andn2_b32 s50,s31,7\n"
SA"s50,s50,s51\n"
MF"v[80:83],a[48:51],v[204:207],v[80:83]\n"
SA"s50,s50,256\n"
RT"a[86:87],v247 offset:20992\n"
SA"s50,s50,s45\n"
MF"v[84:87],a[52:55],v[204:207],v[84:87]\n"
BL"v237,s[12:15],s48 offen offset:3072 lds\n"
MF"v[84:87],a[36:39],v[200:203],v[84:87]\n"
MF"v[88:91],a[40:43],v[200:203],v[88:91]\n"
SA"m0,s37,8192\n"
RT"a[88:89],v248 offset:20480\n"
"s_bfe_u32 s48,s46,0x30010\n"
MF"v[88:91],a[56:59],v[204:207],v[88:91]\n"
"s_lshl_b32 s48,s48,19\n"
WT"vmcnt(12) lgkmcnt(3)\n"
"s_barrier\n"
MF"v[92:95],a[60:63],v[204:207],v[92:95]\n"
RD"v[192:195],v242 offset:32768\n"
MF"v[92:95],a[44:47],v[200:203],v[92:95]\n"
BL"v238,s[16:19],s48 offen offset:0 lds\n"
MF"v[96:99],a[32:35],v[208:211],v[96:99]\n"
"buffer_store_dwordx2 v[232:233],v230,s[8:11],s50 offen sc1\n"
MF"v[96:99],a[48:51],v[212:215],v[96:99]\n"
RT"a[90:91],v248 offset:20992\n"
MF"v[100:103],a[52:55],v[212:215],v[100:103]\n"
RD"v[196:199],v243 offset:32768\n"
RT"a[92:93],v249 offset:20480\n"
MF"v[100:103],a[36:39],v[208:211],v[100:103]\n"
BL"v239,s[16:19],s48 offen offset:1024 lds\n"
MF"v[104:107],a[40:43],v[208:211],v[104:107]\n"
MF"v[104:107],a[56:59],v[212:215],v[104:107]\n"
RT"a[94:95],v249 offset:20992\n"
MF"v[108:111],a[60:63],v[212:215],v[108:111]\n"
RD"v[200:203],v242 offset:34816\n"
MF"v[108:111],a[44:47],v[208:211],v[108:111]\n"
BL"v240,s[16:19],s48 offen offset:2048 lds\n"
MF"v[112:115],a[32:35],v[216:219],v[112:115]\n"
MF"v[112:115],a[48:51],v[220:223],v[112:115]\n"
RD"v[204:207],v243 offset:34816\n"
MF"v[116:119],a[52:55],v[220:223],v[116:119]\n"
MF"v[116:119],a[36:39],v[216:219],v[116:119]\n"
BL"v241,s[16:19],s48 offen offset:3072 lds\n"
MF"v[120:123],a[40:43],v[216:219],v[120:123]\n"
MF"v[120:123],a[56:59],v[220:223],v[120:123]\n"
RD"v[208:211],v242 offset:36864\n"
MF"v[124:127],a[60:63],v[220:223],v[124:127]\n"
SA"m0,s37,12288\n"
"s_bfe_u32 s48,s46,0x30010\n"
"s_lshl_b32 s48,s48,19\n"
MF"v[124:127],a[44:47],v[216:219],v[124:127]\n"
WT"lgkmcnt(2)\n"
"s_bitset1_b32 s48,18\n"
MF"v[0:3],a[64:67],v[192:195],v[0:3]\n"
RD"v[212:215],v243 offset:36864\n"
MF"v[0:3],a[80:83],v[196:199],v[0:3]\n"
BL"v238,s[16:19],s48 offen offset:0 lds\n"
MF"v[4:7],a[84:87],v[196:199],v[4:7]\n"
MF"v[4:7],a[68:71],v[192:195],v[4:7]\n"
MF"v[8:11],a[72:75],v[192:195],v[8:11]\n"
RD"v[216:219],v242 offset:38912\n"
MF"v[8:11],a[88:91],v[196:199],v[8:11]\n"
BL"v239,s[16:19],s48 offen offset:1024 lds\n"
MF"v[12:15],a[92:95],v[196:199],v[12:15]\n"
MF"v[12:15],a[76:79],v[192:195],v[12:15]\n"
RD"v[220:223],v243 offset:38912\n"
MF"v[16:19],a[64:67],v[200:203],v[16:19]\n"
WT"lgkmcnt(2)\n"
WT"vmcnt(15)\n"
RT"a[96:97],v246 offset:0\n"
MF"v[16:19],a[80:83],v[204:207],v[16:19]\n"
BL"v240,s[16:19],s48 offen offset:2048 lds\n"
MF"v[20:23],a[84:87],v[204:207],v[20:23]\n"
MF"v[20:23],a[68:71],v[200:203],v[20:23]\n"
RD"v[192:195],v242 offset:40960\n"
MF"v[24:27],a[72:75],v[200:203],v[24:27]\n"
RT"a[98:99],v246 offset:512\n"
MF"v[24:27],a[88:91],v[204:207],v[24:27]\n"
BL"v241,s[16:19],s48 offen offset:3072 lds\n"
MF"v[28:31],a[92:95],v[204:207],v[28:31]\n"
RT"a[100:101],v247 offset:0\n"
MF"v[28:31],a[76:79],v[200:203],v[28:31]\n"
RD"v[196:199],v243 offset:40960\n"
MF"v[32:35],a[64:67],v[208:211],v[32:35]\n"
SA"m0,s36,16384\n"
"s_bfe_u32 s48,s46,0x30010\n"
"s_lshl_b32 s48,s48,7\n"
MF"v[32:35],a[80:83],v[212:215],v[32:35]\n"
RT"a[102:103],v247 offset:512\n"
MF"v[36:39],a[84:87],v[212:215],v[36:39]\n"
RD"v[200:203],v242 offset:43008\n"
MF"v[36:39],a[68:71],v[208:211],v[36:39]\n"
BL"v234,s[12:15],s48 offen offset:0 lds\n"
MF"v[40:43],a[72:75],v[208:211],v[40:43]\n"
RT"a[104:105],v248 offset:0\n"
MF"v[40:43],a[88:91],v[212:215],v[40:43]\n"
MF"v[44:47],a[92:95],v[212:215],v[44:47]\n"
RD"v[204:207],v243 offset:43008\n"
MF"v[44:47],a[76:79],v[208:211],v[44:47]\n"
WT"lgkmcnt(4)\n"
BL"v235,s[12:15],s48 offen offset:1024 lds\n"
MF"v[48:51],a[64:67],v[216:219],v[48:51]\n"
RT"a[106:107],v248 offset:512\n"
MF"v[48:51],a[80:83],v[220:223],v[48:51]\n"
RD"v[208:211],v242 offset:45056\n"
MF"v[52:55],a[84:87],v[220:223],v[52:55]\n"
RT"a[108:109],v249 offset:0\n"
MF"v[52:55],a[68:71],v[216:219],v[52:55]\n"
BL"v236,s[12:15],s48 offen offset:2048 lds\n"
MF"v[56:59],a[72:75],v[216:219],v[56:59]\n"
MF"v[56:59],a[88:91],v[220:223],v[56:59]\n"
RD"v[212:215],v243 offset:45056\n"
MF"v[60:63],a[92:95],v[220:223],v[60:63]\n"
RT"a[110:111],v249 offset:512\n"
MF"v[60:63],a[76:79],v[216:219],v[60:63]\n"
BL"v237,s[12:15],s48 offen offset:3072 lds\n"
MF"v[64:67],a[64:67],v[192:195],v[64:67]\n"
RD"v[216:219],v242 offset:47104\n"
MF"v[64:67],a[80:83],v[196:199],v[64:67]\n"
WT"vmcnt(17)\n"
RT"a[112:113],v246 offset:4096\n"
MF"v[68:71],a[84:87],v[196:199],v[68:71]\n"
SA"m0,s37,16384\n"
"s_bfe_u32 s48,s46,0x30014\n"
"s_lshl_b32 s48,s48,19\n"
MF"v[68:71],a[68:71],v[192:195],v[68:71]\n"
RT"a[114:115],v246 offset:4608\n"
MF"v[72:75],a[72:75],v[192:195],v[72:75]\n"
RD"v[220:223],v243 offset:47104\n"
MF"v[72:75],a[88:91],v[196:199],v[72:75]\n"
BL"v238,s[16:19],s48 offen offset:0 lds\n"
MF"v[76:79],a[92:95],v[196:199],v[76:79]\n"
RT"a[116:117],v247 offset:4096\n"
MF"v[76:79],a[76:79],v[192:195],v[76:79]\n"
WT"lgkmcnt(4)\n"
MF"v[80:83],a[64:67],v[200:203],v[80:83]\n"
BL"v239,s[16:19],s48 offen offset:1024 lds\n"
MF"v[80:83],a[80:83],v[204:207],v[80:83]\n"
RT"a[118:119],v247 offset:4608\n"
MF"v[84:87],a[84:87],v[204:207],v[84:87]\n"
MF"v[84:87],a[68:71],v[200:203],v[84:87]\n"
MF"v[88:91],a[72:75],v[200:203],v[88:91]\n"
RT"a[120:121],v248 offset:4096\n"
MF"v[88:91],a[88:91],v[204:207],v[88:91]\n"
WT"vmcnt(15) lgkmcnt(3)\n"
"s_barrier\n"
MF"v[92:95],a[92:95],v[204:207],v[92:95]\n"
BL"v240,s[16:19],s48 offen offset:2048 lds\n"
MF"v[92:95],a[76:79],v[200:203],v[92:95]\n"
RD"v[192:195],v242 offset:0\n"
RT"a[122:123],v248 offset:4608\n"
MF"v[96:99],a[64:67],v[208:211],v[96:99]\n"
MF"v[96:99],a[80:83],v[212:215],v[96:99]\n"
BL"v241,s[16:19],s48 offen offset:3072 lds\n"
MF"v[100:103],a[84:87],v[212:215],v[100:103]\n"
RD"v[196:199],v243 offset:0\n"
RT"a[124:125],v249 offset:4096\n"
MF"v[100:103],a[68:71],v[208:211],v[100:103]\n"
MF"v[104:107],a[72:75],v[208:211],v[104:107]\n"
SA"m0,s37,20480\n"
"s_bfe_u32 s48,s46,0x30014\n"
"s_lshl_b32 s48,s48,19\n"
MF"v[104:107],a[88:91],v[212:215],v[104:107]\n"
"s_bitset1_b32 s48,18\n"
RT"a[126:127],v249 offset:4608\n"
MF"v[108:111],a[92:95],v[212:215],v[108:111]\n"
RD"v[200:203],v242 offset:2048\n"
MF"v[108:111],a[76:79],v[208:211],v[108:111]\n"
BL"v238,s[16:19],s48 offen offset:0 lds\n"
MF"v[112:115],a[64:67],v[216:219],v[112:115]\n"
MF"v[112:115],a[80:83],v[220:223],v[112:115]\n"
RD"v[204:207],v243 offset:2048\n"
MF"v[116:119],a[84:87],v[220:223],v[116:119]\n"
BL"v239,s[16:19],s48 offen offset:1024 lds\n"
MF"v[116:119],a[68:71],v[216:219],v[116:119]\n"
MF"v[120:123],a[72:75],v[216:219],v[120:123]\n"
MF"v[120:123],a[88:91],v[220:223],v[120:123]\n"
RD"v[208:211],v242 offset:4096\n"
MF"v[124:127],a[92:95],v[220:223],v[124:127]\n"
MF"v[124:127],a[76:79],v[216:219],v[124:127]\n"
WT"lgkmcnt(2)\n"
BL"v240,s[16:19],s48 offen offset:2048 lds\n"
MF"v[0:3],a[96:99],v[192:195],v[0:3]\n"
RD"v[212:215],v243 offset:4096\n"
MF"v[0:3],a[112:115],v[196:199],v[0:3]\n"
MF"v[4:7],a[116:119],v[196:199],v[4:7]\n"
MF"v[4:7],a[100:103],v[192:195],v[4:7]\n"
BL"v241,s[16:19],s48 offen offset:3072 lds\n"
MF"v[8:11],a[104:107],v[192:195],v[8:11]\n"
RD"v[216:219],v242 offset:6144\n"
MF"v[8:11],a[120:123],v[196:199],v[8:11]\n"
MF"v[12:15],a[124:127],v[196:199],v[12:15]\n"
SA"m0,s36,32768\n"
"s_bfe_u32 s48,s46,0x30014\n"
"s_lshl_b32 s48,s48,7\n"
MF"v[12:15],a[108:111],v[192:195],v[12:15]\n"
RD"v[220:223],v243 offset:6144\n"
MF"v[16:19],a[96:99],v[200:203],v[16:19]\n"
WT"lgkmcnt(2)\n"
WT"vmcnt(16)\n"
RT"a[128:129],v246 offset:8192\n"
MF"v[16:19],a[112:115],v[204:207],v[16:19]\n"
BL"v234,s[12:15],s48 offen offset:0 lds\n"
MF"v[20:23],a[116:119],v[204:207],v[20:23]\n"
MF"v[20:23],a[100:103],v[200:203],v[20:23]\n"
RD"v[192:195],v242 offset:8192\n"
MF"v[24:27],a[104:107],v[200:203],v[24:27]\n"
RT"a[130:131],v246 offset:8704\n"
MF"v[24:27],a[120:123],v[204:207],v[24:27]\n"
BL"v235,s[12:15],s48 offen offset:1024 lds\n"
MF"v[28:31],a[124:127],v[204:207],v[28:31]\n"
RT"a[132:133],v247 offset:8192\n"
MF"v[28:31],a[108:111],v[200:203],v[28:31]\n"
RD"v[196:199],v243 offset:8192\n"
MF"v[32:35],a[96:99],v[208:211],v[32:35]\n"
MF"v[32:35],a[112:115],v[212:215],v[32:35]\n"
RT"a[134:135],v247 offset:8704\n"
MF"v[36:39],a[116:119],v[212:215],v[36:39]\n"
BL"v236,s[12:15],s48 offen offset:2048 lds\n"
MF"v[36:39],a[100:103],v[208:211],v[36:39]\n"
RD"v[200:203],v242 offset:10240\n"
MF"v[40:43],a[104:107],v[208:211],v[40:43]\n"
RT"a[136:137],v248 offset:8192\n"
MF"v[40:43],a[120:123],v[212:215],v[40:43]\n"
BL"v237,s[12:15],s48 offen offset:3072 lds\n"
MF"v[44:47],a[124:127],v[212:215],v[44:47]\n"
RD"v[204:207],v243 offset:10240\n"
MF"v[44:47],a[108:111],v[208:211],v[44:47]\n"
WT"lgkmcnt(4)\n"
RT"a[138:139],v248 offset:8704\n"
MF"v[48:51],a[96:99],v[216:219],v[48:51]\n"
SA"m0,s37,0\n"
"s_bfe_u32 s48,s46,0x30018\n"
"s_lshl_b32 s48,s48,19\n"
MF"v[48:51],a[112:115],v[220:223],v[48:51]\n"
RD"v[208:211],v242 offset:12288\n"
MF"v[52:55],a[116:119],v[220:223],v[52:55]\n"
RT"a[140:141],v249 offset:8192\n"
MF"v[52:55],a[100:103],v[216:219],v[52:55]\n"
BL"v238,s[16:19],s48 offen offset:0 lds\n"
MF"v[56:59],a[104:107],v[216:219],v[56:59]\n"
MF"v[56:59],a[120:123],v[220:223],v[56:59]\n"
RD"v[212:215],v243 offset:12288\n"
MF"v[60:63],a[124:127],v[220:223],v[60:63]\n"
RT"a[142:143],v249 offset:8704\n"
MF"v[60:63],a[108:111],v[216:219],v[60:63]\n"
BL"v239,s[16:19],s48 offen offset:1024 lds\n"
MF"v[64:67],a[96:99],v[192:195],v[64:67]\n"
RD"v[216:219],v242 offset:14336\n"
MF"v[64:67],a[112:115],v[196:199],v[64:67]\n"
WT"vmcnt(18)\n"
RT"a[144:145],v246 offset:12288\n"
MF"v[68:71],a[116:119],v[196:199],v[68:71]\n"
MF"v[68:71],a[100:103],v[192:195],v[68:71]\n"
RT"a[146:147],v246 offset:12800\n"
MF"v[72:75],a[104:107],v[192:195],v[72:75]\n"
BL"v240,s[16:19],s48 offen offset:2048 lds\n"
MF"v[72:75],a[120:123],v[196:199],v[72:75]\n"
RD"v[220:223],v243 offset:14336\n"
MF"v[76:79],a[124:127],v[196:199],v[76:79]\n"
RT"a[148:149],v247 offset:12288\n"
MF"v[76:79],a[108:111],v[192:195],v[76:79]\n"
WT"lgkmcnt(4)\n"
BL"v241,s[16:19],s48 offen offset:3072 lds\n"
MF"v[80:83],a[96:99],v[200:203],v[80:83]\n"
MF"v[80:83],a[112:115],v[204:207],v[80:83]\n"
RT"a[150:151],v247 offset:12800\n"
MF"v[84:87],a[116:119],v[204:207],v[84:87]\n"
SA"m0,s37,4096\n"
"s_bfe_u32 s48,s46,0x30018\n"
"s_lshl_b32 s48,s48,19\n"
MF"v[84:87],a[100:103],v[200:203],v[84:87]\n"
"s_bitset1_b32 s48,18\n"
MF"v[88:91],a[104:107],v[200:203],v[88:91]\n"
RT"a[152:153],v248 offset:12288\n"
MF"v[88:91],a[120:123],v[204:207],v[88:91]\n"
BL"v238,s[16:19],s48 offen offset:0 lds\n"
MF"v[92:95],a[124:127],v[204:207],v[92:95]\n"
WT"vmcnt(17) lgkmcnt(3)\n"
"s_barrier\n"
MF"v[92:95],a[108:111],v[200:203],v[92:95]\n"
RD"v[192:195],v242 offset:16384\n"
RT"a[154:155],v248 offset:12800\n"
MF"v[96:99],a[96:99],v[208:211],v[96:99]\n"
BL"v239,s[16:19],s48 offen offset:1024 lds\n"
MF"v[96:99],a[112:115],v[212:215],v[96:99]\n"
MF"v[100:103],a[116:119],v[212:215],v[100:103]\n"
RD"v[196:199],v243 offset:16384\n"
MF"v[100:103],a[100:103],v[208:211],v[100:103]\n"
RT"a[156:157],v249 offset:12288\n"
MF"v[104:107],a[104:107],v[208:211],v[104:107]\n"
MF"v[104:107],a[120:123],v[212:215],v[104:107]\n"
RT"a[158:159],v249 offset:12800\n"
MF"v[108:111],a[124:127],v[212:215],v[108:111]\n"
BL"v240,s[16:19],s48 offen offset:2048 lds\n"
MF"v[108:111],a[108:111],v[208:211],v[108:111]\n"
RD"v[200:203],v242 offset:18432\n"
MF"v[112:115],a[96:99],v[216:219],v[112:115]\n"
MF"v[112:115],a[112:115],v[220:223],v[112:115]\n"
RD"v[204:207],v243 offset:18432\n"
MF"v[116:119],a[116:119],v[220:223],v[116:119]\n"
BL"v241,s[16:19],s48 offen offset:3072 lds\n"
MF"v[116:119],a[100:103],v[216:219],v[116:119]\n"
MF"v[120:123],a[104:107],v[216:219],v[120:123]\n"
SA"m0,s36,0\n"
"s_bfe_u32 s48,s46,0x30018\n"
"s_lshl_b32 s48,s48,7\n"
MF"v[120:123],a[120:123],v[220:223],v[120:123]\n"
RD"v[208:211],v242 offset:20480\n"
MF"v[124:127],a[124:127],v[220:223],v[124:127]\n"
BL"v234,s[12:15],s48 offen offset:0 lds\n"
MF"v[124:127],a[108:111],v[216:219],v[124:127]\n"
WT"lgkmcnt(2)\n"
MF"v[0:3],a[128:131],v[192:195],v[0:3]\n"
RD"v[212:215],v243 offset:20480\n"
MF"v[0:3],a[144:147],v[196:199],v[0:3]\n"
MF"v[4:7],a[148:151],v[196:199],v[4:7]\n"
BL"v235,s[12:15],s48 offen offset:1024 lds\n"
MF"v[4:7],a[132:135],v[192:195],v[4:7]\n"
MF"v[8:11],a[136:139],v[192:195],v[8:11]\n"
RD"v[216:219],v242 offset:22528\n"
MF"v[8:11],a[152:155],v[196:199],v[8:11]\n"
MF"v[12:15],a[156:159],v[196:199],v[12:15]\n"
MF"v[12:15],a[140:143],v[192:195],v[12:15]\n"
RD"v[220:223],v243 offset:22528\n"
MF"v[16:19],a[128:131],v[200:203],v[16:19]\n"
WT"lgkmcnt(2)\n"
BL"v236,s[12:15],s48 offen offset:2048 lds\n"
MF"v[16:19],a[144:147],v[204:207],v[16:19]\n"
WT"vmcnt(19)\n"
RT"a[160:161],v246 offset:16384\n"
MF"v[20:23],a[148:151],v[204:207],v[20:23]\n"
MF"v[20:23],a[132:135],v[200:203],v[20:23]\n"
RD"v[192:195],v242 offset:24576\n"
MF"v[24:27],a[136:139],v[200:203],v[24:27]\n"
RT"a[162:163],v246 offset:16896\n"
MF"v[24:27],a[152:155],v[204:207],v[24:27]\n"
BL"v237,s[12:15],s48 offen offset:3072 lds\n"
MF"v[28:31],a[156:159],v[204:207],v[28:31]\n"
SA"m0,s37,8192\n"
RT"a[164:165],v247 offset:16384\n"
"s_bfe_u32 s48,s46,0x3001c\n"
MF"v[28:31],a[140:143],v[200:203],v[28:31]\n"
"s_lshl_b32 s48,s48,19\n"
RD"v[196:199],v243 offset:24576\n"
MF"v[32:35],a[128:131],v[208:211],v[32:35]\n"
BL"v238,s[16:19],s48 offen offset:0 lds\n"
MF"v[32:35],a[144:147],v[212:215],v[32:35]\n"
RT"a[166:167],v247 offset:16896\n"
MF"v[36:39],a[148:151],v[212:215],v[36:39]\n"
RD"v[200:203],v242 offset:26624\n"
MF"v[36:39],a[132:135],v[208:211],v[36:39]\n"
MF"v[40:43],a[136:139],v[208:211],v[40:43]\n"
RT"a[168:169],v248 offset:16384\n"
MF"v[40:43],a[152:155],v[212:215],v[40:43]\n"
BL"v239,s[16:19],s48 offen offset:1024 lds\n"
MF"v[44:47],a[156:159],v[212:215],v[44:47]\n"
RD"v[204:207],v243 offset:26624\n"
MF"v[44:47],a[140:143],v[208:211],v[44:47]\n"
WT"lgkmcnt(4)\n"
RT"a[170:171],v248 offset:16896\n"
MF"v[48:51],a[128:131],v[216:219],v[48:51]\n"
MF"v[48:51],a[144:147],v[220:223],v[48:51]\n"
RD"v[208:211],v242 offset:28672\n"
MF"v[52:55],a[148:151],v[220:223],v[52:55]\n"
BL"v240,s[16:19],s48 offen offset:2048 lds\n"
MF"v[52:55],a[132:135],v[216:219],v[52:55]\n"
RT"a[172:173],v249 offset:16384\n"
MF"v[56:59],a[136:139],v[216:219],v[56:59]\n"
MF"v[56:59],a[152:155],v[220:223],v[56:59]\n"
RD"v[212:215],v243 offset:28672\n"
MF"v[60:63],a[156:159],v[220:223],v[60:63]\n"
RT"a[174:175],v249 offset:16896\n"
MF"v[60:63],a[140:143],v[216:219],v[60:63]\n"
BL"v241,s[16:19],s48 offen offset:3072 lds\n"
MF"v[64:67],a[128:131],v[192:195],v[64:67]\n"
SA"m0,s37,12288\n"
RD"v[216:219],v242 offset:30720\n"
"s_bfe_u32 s48,s46,0x3001c\n"
MF"v[64:67],a[144:147],v[196:199],v[64:67]\n"
"s_lshl_b32 s48,s48,19\n"
WT"vmcnt(20)\n"
RT"a[176:177],v246 offset:20480\n"
MF"v[68:71],a[148:151],v[196:199],v[68:71]\n"
"s_bitset1_b32 s48,18\n"
BL"v238,s[16:19],s48 offen offset:0 lds\n"
MF"v[68:71],a[132:135],v[192:195],v[68:71]\n"
RT"a[178:179],v246 offset:20992\n"
MF"v[72:75],a[136:139],v[192:195],v[72:75]\n"
RD"v[220:223],v243 offset:30720\n"
MF"v[72:75],a[152:155],v[196:199],v[72:75]\n"
MF"v[76:79],a[156:159],v[196:199],v[76:79]\n"
RT"a[180:181],v247 offset:20480\n"
MF"v[76:79],a[140:143],v[192:195],v[76:79]\n"
WT"lgkmcnt(4)\n"
BL"v239,s[16:19],s48 offen offset:1024 lds\n"
MF"v[80:83],a[128:131],v[200:203],v[80:83]\n"
MF"v[80:83],a[144:147],v[204:207],v[80:83]\n"
RT"a[182:183],v247 offset:20992\n"
MF"v[84:87],a[148:151],v[204:207],v[84:87]\n"
MF"v[84:87],a[132:135],v[200:203],v[84:87]\n"
BL"v240,s[16:19],s48 offen offset:2048 lds\n"
MF"v[88:91],a[136:139],v[200:203],v[88:91]\n"
RT"a[184:185],v248 offset:20480\n"
MF"v[88:91],a[152:155],v[204:207],v[88:91]\n"
WT"vmcnt(19) lgkmcnt(3)\n"
"s_barrier\n"
MF"v[92:95],a[156:159],v[204:207],v[92:95]\n"
RD"v[192:195],v242 offset:32768\n"
MF"v[92:95],a[140:143],v[200:203],v[92:95]\n"
RT"a[186:187],v248 offset:20992\n"
MF"v[96:99],a[128:131],v[208:211],v[96:99]\n"
BL"v241,s[16:19],s48 offen offset:3072 lds\n"
MF"v[96:99],a[144:147],v[212:215],v[96:99]\n"
MF"v[100:103],a[148:151],v[212:215],v[100:103]\n"
SA"m0,s36,16384\n"
RD"v[196:199],v243 offset:32768\n"
"s_bfe_u32 s48,s46,0x3001c\n"
MF"v[100:103],a[132:135],v[208:211],v[100:103]\n"
"s_lshl_b32 s48,s48,7\n"
RT"a[188:189],v249 offset:20480\n"
MF"v[104:107],a[136:139],v[208:211],v[104:107]\n"
BL"v234,s[12:15],s48 offen offset:0 lds\n"
MF"v[104:107],a[152:155],v[212:215],v[104:107]\n"
RT"a[190:191],v249 offset:20992\n"
MF"v[108:111],a[156:159],v[212:215],v[108:111]\n"
RD"v[200:203],v242 offset:34816\n"
MF"v[108:111],a[140:143],v[208:211],v[108:111]\n"
MF"v[112:115],a[128:131],v[216:219],v[112:115]\n"
BL"v235,s[12:15],s48 offen offset:1024 lds\n"
MF"v[112:115],a[144:147],v[220:223],v[112:115]\n"
RD"v[204:207],v243 offset:34816\n"
MF"v[116:119],a[148:151],v[220:223],v[116:119]\n"
MF"v[116:119],a[132:135],v[216:219],v[116:119]\n"
MF"v[120:123],a[136:139],v[216:219],v[120:123]\n"
MF"v[120:123],a[152:155],v[220:223],v[120:123]\n"
RD"v[208:211],v242 offset:36864\n"
MF"v[124:127],a[156:159],v[220:223],v[124:127]\n"
BL"v236,s[12:15],s48 offen offset:2048 lds\n"
MF"v[124:127],a[140:143],v[216:219],v[124:127]\n"
WT"lgkmcnt(2)\n"
MF"v[0:3],a[160:163],v[192:195],v[0:3]\n"
RD"v[212:215],v243 offset:36864\n"
MF"v[0:3],a[176:179],v[196:199],v[0:3]\n"
BL"v237,s[12:15],s48 offen offset:3072 lds\n"
MF"v[4:7],a[180:183],v[196:199],v[4:7]\n"
MF"v[4:7],a[164:167],v[192:195],v[4:7]\n"
MF"v[8:11],a[168:171],v[192:195],v[8:11]\n"
SA"m0,s37,16384\n"
RD"v[216:219],v242 offset:38912\n"
SA"s48,s39,0\n"
MF"v[8:11],a[184:187],v[196:199],v[8:11]\n"
MF"v[12:15],a[188:191],v[196:199],v[12:15]\n"
BL"v234,s[12:15],s48 offen offset:0 lds\n"
MF"v[12:15],a[172:175],v[192:195],v[12:15]\n"
RD"v[220:223],v243 offset:38912\n"
MF"v[16:19],a[160:163],v[200:203],v[16:19]\n"
WT"lgkmcnt(2)\n"
WT"vmcnt(21)\n"
RT"a[192:193],v246 offset:0\n"
MF"v[16:19],a[176:179],v[204:207],v[16:19]\n"
MF"v[20:23],a[180:183],v[204:207],v[20:23]\n"
BL"v235,s[12:15],s48 offen offset:1024 lds\n"
MF"v[20:23],a[164:167],v[200:203],v[20:23]\n"
RD"v[192:195],v242 offset:40960\n"
RT"a[194:195],v246 offset:512\n"
MF"v[24:27],a[168:171],v[200:203],v[24:27]\n"
MF"v[24:27],a[184:187],v[204:207],v[24:27]\n"
MF"v[28:31],a[188:191],v[204:207],v[28:31]\n"
RT"a[196:197],v247 offset:0\n"
MF"v[28:31],a[172:175],v[200:203],v[28:31]\n"
RD"v[196:199],v243 offset:40960\n"
MF"v[32:35],a[160:163],v[208:211],v[32:35]\n"
BL"v236,s[12:15],s48 offen offset:2048 lds\n"
MF"v[32:35],a[176:179],v[212:215],v[32:35]\n"
RT"a[198:199],v247 offset:512\n"
MF"v[36:39],a[180:183],v[212:215],v[36:39]\n"
RD"v[200:203],v242 offset:43008\n"
MF"v[36:39],a[164:167],v[208:211],v[36:39]\n"
BL"v237,s[12:15],s48 offen offset:3072 lds\n"
MF"v[40:43],a[168:171],v[208:211],v[40:43]\n"
RT"a[200:201],v248 offset:0\n"
MF"v[40:43],a[184:187],v[212:215],v[40:43]\n"
MF"v[44:47],a[188:191],v[212:215],v[44:47]\n"
SA"m0,s37,20480\n"
RD"v[204:207],v243 offset:43008\n"
SA"s48,s98,0\n"
MF"v[44:47],a[172:175],v[208:211],v[44:47]\n"
WT"lgkmcnt(4)\n"
RT"a[202:203],v248 offset:512\n"
"s_lshr_b32 s50,s30,1\n"
MF"v[48:51],a[160:163],v[216:219],v[48:51]\n"
"s_and_b32 s51,s30,1\n"
BL"v234,s[12:15],s48 offen offset:0 lds\n"
MF"v[48:51],a[176:179],v[220:223],v[48:51]\n"
"s_mul_i32 s38,s50,24576\n"
RD"v[208:211],v242 offset:45056\n"
MF"v[52:55],a[180:183],v[220:223],v[52:55]\n"
"s_lshl_b32 s60,s50,18\n"
RT"a[204:205],v249 offset:0\n"
MF"v[52:55],a[164:167],v[216:219],v[52:55]\n"
"s_lshl_b32 s50,s51,12\n"
MF"v[56:59],a[168:171],v[216:219],v[56:59]\n"
SA"s38,s38,s50\n"
BL"v235,s[12:15],s48 offen offset:1024 lds\n"
MF"v[56:59],a[184:187],v[220:223],v[56:59]\n"
"s_lshl_b32 s50,s51,7\n"
RD"v[212:215],v243 offset:45056\n"
RT"a[206:207],v249 offset:512\n"
MF"v[60:63],a[188:191],v[220:223],v[60:63]\n"
SA"s60,s60,s50\n"
MF"v[60:63],a[172:175],v[216:219],v[60:63]\n"
MF"v[64:67],a[160:163],v[192:195],v[64:67]\n"
RD"v[216:219],v242 offset:47104\n"
MF"v[64:67],a[176:179],v[196:199],v[64:67]\n"
"s_sub_u32 s99,s34,5\n"
WT"vmcnt(22)\n"
RT"a[208:209],v246 offset:4096\n"
MF"v[68:71],a[180:183],v[196:199],v[68:71]\n"
"s_cmp_lt_u32 s34,5\n"
BL"v236,s[12:15],s48 offen offset:2048 lds\n"
MF"v[68:71],a[164:167],v[192:195],v[68:71]\n"
"s_cselect_b32 s99,s34,s99\n"
RT"a[210:211],v246 offset:4608\n"
"s_mul_i32 s99,s99,1\n"
MF"v[72:75],a[168:171],v[192:195],v[72:75]\n"
"s_lshl_b32 s61,s99,20\n"
RD"v[220:223],v243 offset:47104\n"
MF"v[72:75],a[184:187],v[196:199],v[72:75]\n"
BL"v237,s[12:15],s48 offen offset:3072 lds\n"
MF"v[76:79],a[188:191],v[196:199],v[76:79]\n"
RT"a[212:213],v247 offset:4096\n"
MF"v[76:79],a[172:175],v[192:195],v[76:79]\n"
WT"lgkmcnt(4)\n"
MF"v[80:83],a[160:163],v[200:203],v[80:83]\n"
SA"m0,s37,0\n"
SA"s48,s39,32768\n"
MF"v[80:83],a[176:179],v[204:207],v[80:83]\n"
RT"a[214:215],v247 offset:4608\n"
MF"v[84:87],a[180:183],v[204:207],v[84:87]\n"
BL"v234,s[12:15],s48 offen offset:0 lds\n"
MF"v[84:87],a[164:167],v[200:203],v[84:87]\n"
MF"v[88:91],a[168:171],v[200:203],v[88:91]\n"
RT"a[216:217],v248 offset:4096\n"
MF"v[88:91],a[184:187],v[204:207],v[88:91]\n"
WT"vmcnt(21) lgkmcnt(3)\n"
"s_barrier\n"
MF"v[92:95],a[188:191],v[204:207],v[92:95]\n"
RD"v[192:195],v242 offset:0\n"
MF"v[92:95],a[172:175],v[200:203],v[92:95]\n"
BL"v235,s[12:15],s48 offen offset:1024 lds\n"
MF"v[96:99],a[160:163],v[208:211],v[96:99]\n"
RT"a[218:219],v248 offset:4608\n"
MF"v[96:99],a[176:179],v[212:215],v[96:99]\n"
MF"v[100:103],a[180:183],v[212:215],v[100:103]\n"
RD"v[196:199],v243 offset:0\n"
MF"v[100:103],a[164:167],v[208:211],v[100:103]\n"
RT"a[220:221],v249 offset:4096\n"
MF"v[104:107],a[168:171],v[208:211],v[104:107]\n"
BL"v236,s[12:15],s48 offen offset:2048 lds\n"
MF"v[104:107],a[184:187],v[212:215],v[104:107]\n"
RT"a[222:223],v249 offset:4608\n"
MF"v[108:111],a[188:191],v[212:215],v[108:111]\n"
RD"v[200:203],v242 offset:2048\n"
MF"v[108:111],a[172:175],v[208:211],v[108:111]\n"
BL"v237,s[12:15],s48 offen offset:3072 lds\n"
MF"v[112:115],a[160:163],v[216:219],v[112:115]\n"
MF"v[112:115],a[176:179],v[220:223],v[112:115]\n"
RD"v[204:207],v243 offset:2048\n"
MF"v[116:119],a[180:183],v[220:223],v[116:119]\n"
SA"m0,s37,4096\n"
SA"s48,s98,32768\n"
MF"v[116:119],a[164:167],v[216:219],v[116:119]\n"
MF"v[120:123],a[168:171],v[216:219],v[120:123]\n"
WT"lgkmcnt(2)\n"
BL"v234,s[12:15],s48 offen offset:0 lds\n"
MF"v[120:123],a[184:187],v[220:223],v[120:123]\n"
RD"v[208:211],v242 offset:4096\n"
MF"v[124:127],a[188:191],v[220:223],v[124:127]\n"
MF"v[124:127],a[172:175],v[216:219],v[124:127]\n"
MF"v[0:3],a[192:195],v[192:195],v[0:3]\n"
RD"v[212:215],v243 offset:4096\n"
MF"v[0:3],a[208:211],v[196:199],v[0:3]\n"
BL"v235,s[12:15],s48 offen offset:1024 lds\n"
MF"v[4:7],a[212:215],v[196:199],v[4:7]\n"
MF"v[4:7],a[196:199],v[192:195],v[4:7]\n"
MF"v[8:11],a[200:203],v[192:195],v[8:11]\n"
RD"v[216:219],v242 offset:6144\n"
MF"v[8:11],a[216:219],v[196:199],v[8:11]\n"
BL"v236,s[12:15],s48 offen offset:2048 lds\n"
MF"v[12:15],a[220:223],v[196:199],v[12:15]\n"
MF"v[12:15],a[204:207],v[192:195],v[12:15]\n"
WT"lgkmcnt(1)\n"
RD"v[220:223],v243 offset:6144\n"
MF"v[16:19],a[192:195],v[200:203],v[16:19]\n"
WT"vmcnt(23)\n"
RT"a[224:225],v246 offset:8192\n"
MF"v[16:19],a[208:211],v[204:207],v[16:19]\n"
BL"v237,s[12:15],s48 offen offset:3072 lds\n"
MF"v[20:23],a[212:215],v[204:207],v[20:23]\n"
MF"v[20:23],a[196:199],v[200:203],v[20:23]\n"
RD"v[192:195],v242 offset:8192\n"
MF"v[24:27],a[200:203],v[200:203],v[24:27]\n"
RT"a[226:227],v246 offset:8704\n"
MF"v[24:27],a[216:219],v[204:207],v[24:27]\n"
MF"v[28:31],a[220:223],v[204:207],v[28:31]\n"
RT"a[228:229],v247 offset:8192\n"
MF"v[28:31],a[204:207],v[200:203],v[28:31]\n"
RD"v[196:199],v243 offset:8192\n"
MF"v[32:35],a[192:195],v[208:211],v[32:35]\n"
MF"v[32:35],a[208:211],v[212:215],v[32:35]\n"
RT"a[230:231],v247 offset:8704\n"
MF"v[36:39],a[212:215],v[212:215],v[36:39]\n"
RD"v[200:203],v242 offset:10240\n"
MF"v[36:39],a[196:199],v[208:211],v[36:39]\n"
MF"v[40:43],a[200:203],v[208:211],v[40:43]\n"
RT"a[232:233],v248 offset:8192\n"
MF"v[40:43],a[216:219],v[212:215],v[40:43]\n"
MF"v[44:47],a[220:223],v[212:215],v[44:47]\n"
RD"v[204:207],v243 offset:10240\n"
MF"v[44:47],a[204:207],v[208:211],v[44:47]\n"
WT"lgkmcnt(4)\n"
RT"a[234:235],v248 offset:8704\n"
MF"v[48:51],a[192:195],v[216:219],v[48:51]\n"
MF"v[48:51],a[208:211],v[220:223],v[48:51]\n"
RD"v[208:211],v242 offset:12288\n"
MF"v[52:55],a[212:215],v[220:223],v[52:55]\n"
RT"a[236:237],v249 offset:8192\n"
MF"v[52:55],a[196:199],v[216:219],v[52:55]\n"
MF"v[56:59],a[200:203],v[216:219],v[56:59]\n"
MF"v[56:59],a[216:219],v[220:223],v[56:59]\n"
RD"v[212:215],v243 offset:12288\n"
MF"v[60:63],a[220:223],v[220:223],v[60:63]\n"
RT"a[238:239],v249 offset:8704\n"
MF"v[60:63],a[204:207],v[216:219],v[60:63]\n"
MF"v[64:67],a[192:195],v[192:195],v[64:67]\n"
RD"v[216:219],v242 offset:14336\n"
MF"v[64:67],a[208:211],v[196:199],v[64:67]\n"
SA"m0,s37,8192\n"
WT"vmcnt(20)\n"
RT"a[240:241],v246 offset:12288\n"
MF"v[68:71],a[212:215],v[196:199],v[68:71]\n"
SA"s48,s39,65536\n"
MF"v[68:71],a[196:199],v[192:195],v[68:71]\n"
RT"a[242:243],v246 offset:12800\n"
MF"v[72:75],a[200:203],v[192:195],v[72:75]\n"
WT"lgkmcnt(3)\n"
BL"v234,s[12:15],s48 offen offset:0 lds\n"
MF"v[72:75],a[216:219],v[196:199],v[72:75]\n"
RD"v[220:223],v243 offset:14336\n"
MF"v[76:79],a[220:223],v[196:199],v[76:79]\n"
RT"a[244:245],v247 offset:12288\n"
MF"v[76:79],a[204:207],v[192:195],v[76:79]\n"
BL"v235,s[12:15],s48 offen offset:1024 lds\n"
MF"v[80:83],a[192:195],v[200:203],v[80:83]\n"
MF"v[80:83],a[208:211],v[204:207],v[80:83]\n"
RT"a[246:247],v247 offset:12800\n"
MF"v[84:87],a[212:215],v[204:207],v[84:87]\n"
MF"v[84:87],a[196:199],v[200:203],v[84:87]\n"
MF"v[88:91],a[200:203],v[200:203],v[88:91]\n"
RT"a[248:249],v248 offset:12288\n"
MF"v[88:91],a[216:219],v[204:207],v[88:91]\n"
BL"v236,s[12:15],s48 offen offset:2048 lds\n"
MF"v[92:95],a[220:223],v[204:207],v[92:95]\n"
WT"vmcnt(19) lgkmcnt(3)\n"
"s_barrier\n"
MF"v[92:95],a[204:207],v[200:203],v[92:95]\n"
RD"v[192:195],v242 offset:16384\n"
RT"a[250:251],v248 offset:12800\n"
MF"v[96:99],a[192:195],v[208:211],v[96:99]\n"
BL"v237,s[12:15],s48 offen offset:3072 lds\n"
MF"v[96:99],a[208:211],v[212:215],v[96:99]\n"
MF"v[100:103],a[212:215],v[212:215],v[100:103]\n"
RD"v[196:199],v243 offset:16384\n"
MF"v[100:103],a[196:199],v[208:211],v[100:103]\n"
RT"a[252:253],v249 offset:12288\n"
MF"v[104:107],a[200:203],v[208:211],v[104:107]\n"
MF"v[104:107],a[216:219],v[212:215],v[104:107]\n"
RT"a[254:255],v249 offset:12800\n"
MF"v[108:111],a[220:223],v[212:215],v[108:111]\n"
RD"v[200:203],v242 offset:18432\n"
MF"v[108:111],a[204:207],v[208:211],v[108:111]\n"
MF"v[112:115],a[192:195],v[216:219],v[112:115]\n"
"s_lshl_b32 s50,s35,20\n"
MF"v[112:115],a[208:211],v[220:223],v[112:115]\n"
"s_lshl_b32 s51,s34,15\n"
RD"v[204:207],v243 offset:18432\n"
SA"m0,s37,12288\n"
MF"v[116:119],a[212:215],v[220:223],v[116:119]\n"
SA"s48,s98,65536\n"
SA"s50,s50,s51\n"
MF"v[116:119],a[196:199],v[216:219],v[116:119]\n"
WT"lgkmcnt(2)\n"
BL"v234,s[12:15],s48 offen offset:0 lds\n"
MF"v[120:123],a[200:203],v[216:219],v[120:123]\n"
"s_and_b32 s51,s30,1\n"
"s_lshl_b32 s51,s51,11\n"
MF"v[120:123],a[216:219],v[220:223],v[120:123]\n"
SA"s50,s50,s51\n"
RD"v[208:211],v242 offset:20480\n"
MF"v[124:127],a[220:223],v[220:223],v[124:127]\n"
SA"s50,s50,9175040\n"
MF"v[124:127],a[204:207],v[216:219],v[124:127]\n"
BL"v235,s[12:15],s48 offen offset:1024 lds\n"
MF"v[0:3],a[224:227],v[192:195],v[0:3]\n"
RD"v[212:215],v243 offset:20480\n"
"v_add_u32 v250,s50,v224\n"
MF"v[0:3],a[240:243],v[196:199],v[0:3]\n"
MF"v[4:7],a[244:247],v[196:199],v[4:7]\n"
MF"v[4:7],a[228:231],v[192:195],v[4:7]\n"
MF"v[8:11],a[232:235],v[192:195],v[8:11]\n"
RD"v[216:219],v242 offset:22528\n"
MF"v[8:11],a[248:251],v[196:199],v[8:11]\n"
BL"v236,s[12:15],s48 offen offset:2048 lds\n"
MF"v[12:15],a[252:255],v[196:199],v[12:15]\n"
MF"v[12:15],a[236:239],v[192:195],v[12:15]\n"
WT"lgkmcnt(1)\n"
RD"v[220:223],v243 offset:22528\n"
MF"v[16:19],a[224:227],v[200:203],v[16:19]\n"
BL"v237,s[12:15],s48 offen offset:3072 lds\n"
MF"v[16:19],a[240:243],v[204:207],v[16:19]\n"
MF"v[20:23],a[244:247],v[204:207],v[20:23]\n"
MF"v[20:23],a[228:231],v[200:203],v[20:23]\n"
RD"v[192:195],v242 offset:24576\n"
MF"v[24:27],a[232:235],v[200:203],v[24:27]\n"
MF"v[24:27],a[248:251],v[204:207],v[24:27]\n"
MF"v[28:31],a[252:255],v[204:207],v[28:31]\n"
MF"v[28:31],a[236:239],v[200:203],v[28:31]\n"
RD"v[196:199],v243 offset:24576\n"
MF"v[32:35],a[224:227],v[208:211],v[32:35]\n"
MF"v[32:35],a[240:243],v[212:215],v[32:35]\n"
MF"v[36:39],a[244:247],v[212:215],v[36:39]\n"
RD"v[200:203],v242 offset:26624\n"
MF"v[36:39],a[228:231],v[208:211],v[36:39]\n"
MF"v[40:43],a[232:235],v[208:211],v[40:43]\n"
MF"v[40:43],a[248:251],v[212:215],v[40:43]\n"
MF"v[44:47],a[252:255],v[212:215],v[44:47]\n"
RD"v[204:207],v243 offset:26624\n"
MF"v[44:47],a[236:239],v[208:211],v[44:47]\n"
WT"lgkmcnt(2)\n"
MF"v[48:51],a[224:227],v[216:219],v[48:51]\n"
MF"v[48:51],a[240:243],v[220:223],v[48:51]\n"
RD"v[208:211],v242 offset:28672\n"
MF"v[52:55],a[244:247],v[220:223],v[52:55]\n"
MF"v[52:55],a[228:231],v[216:219],v[52:55]\n"
MF"v[56:59],a[232:235],v[216:219],v[56:59]\n"
MF"v[56:59],a[248:251],v[220:223],v[56:59]\n"
RD"v[212:215],v243 offset:28672\n"
MF"v[60:63],a[252:255],v[220:223],v[60:63]\n"
MF"v[60:63],a[236:239],v[216:219],v[60:63]\n"
MF"v[64:67],a[224:227],v[192:195],v[64:67]\n"
RD"v[216:219],v242 offset:30720\n"
MF"v[64:67],a[240:243],v[196:199],v[64:67]\n"
MF"v[68:71],a[244:247],v[196:199],v[68:71]\n"
MF"v[68:71],a[228:231],v[192:195],v[68:71]\n"
MF"v[72:75],a[232:235],v[192:195],v[72:75]\n"
RD"v[220:223],v243 offset:30720\n"
MF"v[72:75],a[248:251],v[196:199],v[72:75]\n"
MF"v[76:79],a[252:255],v[196:199],v[76:79]\n"
MF"v[76:79],a[236:239],v[192:195],v[76:79]\n"
WT"lgkmcnt(1)\n"
MF"v[80:83],a[224:227],v[200:203],v[80:83]\n"
MF"v[80:83],a[240:243],v[204:207],v[80:83]\n"
MF"v[84:87],a[244:247],v[204:207],v[84:87]\n"
"v_add_u32 v244,114688,v242\n"
"v_add_u32 v245,114688,v243\n"
MF"v[84:87],a[228:231],v[200:203],v[84:87]\n"
"v_add_u32 v242,65536,v242\n"
"v_add_u32 v243,65536,v243\n"
MF"v[88:91],a[232:235],v[200:203],v[88:91]\n"
MF"v[88:91],a[248:251],v[204:207],v[88:91]\n"
WT"vmcnt(16) lgkmcnt(0)\n"
"s_barrier\n"
MF"v[92:95],a[252:255],v[204:207],v[92:95]\n"
RD"v[192:195],v242 offset:16384\n"
MF"v[92:95],a[236:239],v[200:203],v[92:95]\n"
MF"v[96:99],a[224:227],v[208:211],v[96:99]\n"
MF"v[96:99],a[240:243],v[212:215],v[96:99]\n"
MF"v[100:103],a[244:247],v[212:215],v[100:103]\n"
SA"m0,s36,49152\n"
RD"v[196:199],v243 offset:16384\n"
SA"s48,s60,s61\n"
MF"v[100:103],a[228:231],v[208:211],v[100:103]\n"
MF"v[104:107],a[232:235],v[208:211],v[104:107]\n"
BL"v238,s[20:23],s48 offen offset:0 lds\n"
MF"v[104:107],a[248:251],v[212:215],v[104:107]\n"
MF"v[108:111],a[252:255],v[212:215],v[108:111]\n"
RD"v[200:203],v242 offset:18432\n"
MF"v[108:111],a[236:239],v[208:211],v[108:111]\n"
MF"v[112:115],a[224:227],v[216:219],v[112:115]\n"
BL"v239,s[20:23],s48 offen offset:1024 lds\n"
MF"v[112:115],a[240:243],v[220:223],v[112:115]\n"
MF"v[116:119],a[244:247],v[220:223],v[116:119]\n"
RD"v[204:207],v243 offset:18432\n"
MF"v[116:119],a[228:231],v[216:219],v[116:119]\n"
MF"v[120:123],a[232:235],v[216:219],v[120:123]\n"
MF"v[120:123],a[248:251],v[220:223],v[120:123]\n"
BL"v240,s[20:23],s48 offen offset:2048 lds\n"
MF"v[124:127],a[252:255],v[220:223],v[124:127]\n"
SA"s61,s61,524288\n"
RD"v[208:211],v242 offset:20480\n"
MF"v[124:127],a[236:239],v[216:219],v[124:127]\n"
WT"lgkmcnt(2)\n"
MF"v[128:131],a[0:3],v[192:195],0\n"
MF"v[128:131],a[16:19],v[196:199],v[128:131]\n"
RD"v[212:215],v243 offset:20480\n"
MF"v[132:135],a[20:23],v[196:199],0\n"
BL"v241,s[20:23],s48 offen offset:3072 lds\n"
MF"v[132:135],a[4:7],v[192:195],v[132:135]\n"
MU"v0,s55,v0\n"
MU"v1,s55,v1\n"
MF"v[136:139],a[8:11],v[192:195],0\n"
MU"v2,s55,v2\n"
MU"v3,s55,v3\n"
MF"v[136:139],a[24:27],v[196:199],v[136:139]\n"
RD"v[216:219],v242 offset:22528\n"
MU"v4,s55,v4\n"
MF"v[140:143],a[28:31],v[196:199],0\n"
SA"m0,s36,32768\n"
MU"v5,s55,v5\n"
MU"v6,s55,v6\n"
MF"v[140:143],a[12:15],v[192:195],v[140:143]\n"
SA"s48,s60,s61\n"
MU"v7,s55,v7\n"
PK"v0,v0,v1\n"
MF"v[144:147],a[0:3],v[200:203],0\n"
WT"lgkmcnt(1)\n"
BL"v238,s[20:23],s48 offen offset:0 lds\n"
MF"v[144:147],a[16:19],v[204:207],v[144:147]\n"
RD"v[220:223],v243 offset:22528\n"
PK"v1,v2,v3\n"
MF"v[148:151],a[20:23],v[204:207],0\n"
PK"v2,v4,v5\n"
PK"v3,v6,v7\n"
MF"v[148:151],a[4:7],v[200:203],v[148:151]\n"
MF"v[152:155],a[8:11],v[200:203],0\n"
BL"v239,s[20:23],s48 offen offset:1024 lds\n"
MF"v[152:155],a[24:27],v[204:207],v[152:155]\n"
RD"v[192:195],v242 offset:40960\n"
MU"v8,s55,v8\n"
MF"v[156:159],a[28:31],v[204:207],0\n"
MU"v9,s55,v9\n"
MU"v10,s55,v10\n"
MF"v[156:159],a[12:15],v[200:203],v[156:159]\n"
MU"v11,s55,v11\n"
MU"v12,s55,v12\n"
MF"v[128:131],a[32:35],v[208:211],v[128:131]\n"
MU"v13,s55,v13\n"
MU"v14,s55,v14\n"
MF"v[128:131],a[48:51],v[212:215],v[128:131]\n"
RD"v[196:199],v243 offset:40960\n"
MU"v15,s55,v15\n"
MF"v[132:135],a[52:55],v[212:215],v[132:135]\n"
SA"s61,s61,524288\n"
BL"v240,s[20:23],s48 offen offset:2048 lds\n"
MF"v[132:135],a[36:39],v[208:211],v[132:135]\n"
"s_cmp_eq_u32 s61,5242880\n"
PK"v8,v8,v9\n"
PK"v9,v10,v11\n"
MF"v[136:139],a[40:43],v[208:211],v[136:139]\n"
"s_cselect_b32 s61,0,s61\n"
RD"v[200:203],v242 offset:43008\n"
PK"v10,v12,v13\n"
MF"v[136:139],a[56:59],v[212:215],v[136:139]\n"
BL"v241,s[20:23],s48 offen offset:3072 lds\n"
MF"v[140:143],a[60:63],v[212:215],v[140:143]\n"
PK"v11,v14,v15\n"
MU"v16,s55,v16\n"
MF"v[140:143],a[44:47],v[208:211],v[140:143]\n"
WT"lgkmcnt(2)\n"
MU"v17,s55,v17\n"
MU"v18,s55,v18\n"
MF"v[144:147],a[32:35],v[216:219],v[144:147]\n"
RD"v[204:207],v243 offset:43008\n"
MU"v19,s55,v19\n"
MF"v[144:147],a[48:51],v[220:223],v[144:147]\n"
MU"v20,s55,v20\n"
MU"v21,s55,v21\n"
MF"v[148:151],a[52:55],v[220:223],v[148:151]\n"
MU"v22,s55,v22\n"
MU"v23,s55,v23\n"
MF"v[148:151],a[36:39],v[216:219],v[148:151]\n"
PK"v16,v16,v17\n"
PK"v17,v18,v19\n"
MF"v[152:155],a[40:43],v[216:219],v[152:155]\n"
RD"v[208:211],v242 offset:45056\n"
PK"v18,v20,v21\n"
MF"v[152:155],a[56:59],v[220:223],v[152:155]\n"
PK"v19,v22,v23\n"
MF"v[156:159],a[60:63],v[220:223],v[156:159]\n"
MF"v[156:159],a[44:47],v[216:219],v[156:159]\n"
"s_mov_b32 s49,0\n"
MF"v[128:131],a[64:67],v[192:195],v[128:131]\n"
WT"lgkmcnt(1)\n"
RD"v[212:215],v243 offset:45056\n"
MU"v24,s55,v24\n"
MF"v[128:131],a[80:83],v[196:199],v[128:131]\n"
BS"v[0:3],v250,s[8:11],s49 offen offset:0 sc1\n"
MF"v[132:135],a[84:87],v[196:199],v[132:135]\n"
MU"v25,s55,v25\n"
MU"v26,s55,v26\n"
MF"v[132:135],a[68:71],v[192:195],v[132:135]\n"
RD"v[216:219],v242 offset:47104\n"
MU"v27,s55,v27\n"
MF"v[136:139],a[72:75],v[192:195],v[136:139]\n"
MU"v28,s55,v28\n"
MU"v29,s55,v29\n"
MF"v[136:139],a[88:91],v[196:199],v[136:139]\n"
MU"v30,s55,v30\n"
MU"v31,s55,v31\n"
MF"v[140:143],a[92:95],v[196:199],v[140:143]\n"
PK"v24,v24,v25\n"
PK"v25,v26,v27\n"
MF"v[140:143],a[76:79],v[192:195],v[140:143]\n"
RD"v[220:223],v243 offset:47104\n"
PK"v26,v28,v29\n"
MF"v[144:147],a[64:67],v[200:203],v[144:147]\n"
PK"v27,v30,v31\n"
MF"v[144:147],a[80:83],v[204:207],v[144:147]\n"
MF"v[148:151],a[84:87],v[204:207],v[148:151]\n"
MU"v32,s55,v32\n"
MU"v33,s55,v33\n"
MF"v[148:151],a[68:71],v[200:203],v[148:151]\n"
RD"v[192:195],v244 offset:16384\n"
MU"v34,s55,v34\n"
MF"v[152:155],a[72:75],v[200:203],v[152:155]\n"
MU"v35,s55,v35\n"
MU"v36,s55,v36\n"
MF"v[152:155],a[88:91],v[204:207],v[152:155]\n"
MU"v37,s55,v37\n"
MU"v38,s55,v38\n"
MF"v[156:159],a[92:95],v[204:207],v[156:159]\n"
MU"v39,s55,v39\n"
PK"v32,v32,v33\n"
MF"v[156:159],a[76:79],v[200:203],v[156:159]\n"
WT"lgkmcnt(1)\n"
RD"v[196:199],v245 offset:16384\n"
PK"v33,v34,v35\n"
MF"v[128:131],a[96:99],v[208:211],v[128:131]\n"
PK"v34,v36,v37\n"
PK"v35,v38,v39\n"
MF"v[128:131],a[112:115],v[212:215],v[128:131]\n"
MF"v[132:135],a[116:119],v[212:215],v[132:135]\n"
MF"v[132:135],a[100:103],v[208:211],v[132:135]\n"
RD"v[200:203],v244 offset:18432\n"
MF"v[136:139],a[104:107],v[208:211],v[136:139]\n"
MU"v40,s55,v40\n"
MU"v41,s55,v41\n"
MF"v[136:139],a[120:123],v[212:215],v[136:139]\n"
MU"v42,s55,v42\n"
MU"v43,s55,v43\n"
MF"v[140:143],a[124:127],v[212:215],v[140:143]\n"
RD"v[204:207],v245 offset:18432\n"
MU"v44,s55,v44\n"
MF"v[140:143],a[108:111],v[208:211],v[140:143]\n"
MU"v45,s55,v45\n"
MU"v46,s55,v46\n"
MF"v[144:147],a[96:99],v[216:219],v[144:147]\n"
MU"v47,s55,v47\n"
PK"v40,v40,v41\n"
MF"v[144:147],a[112:115],v[220:223],v[144:147]\n"
PK"v41,v42,v43\n"
PK"v42,v44,v45\n"
MF"v[148:151],a[116:119],v[220:223],v[148:151]\n"
RD"v[208:211],v244 offset:20480\n"
PK"v43,v46,v47\n"
MF"v[148:151],a[100:103],v[216:219],v[148:151]\n"
MF"v[152:155],a[104:107],v[216:219],v[152:155]\n"
MF"v[152:155],a[120:123],v[220:223],v[152:155]\n"
MF"v[156:159],a[124:127],v[220:223],v[156:159]\n"
RD"v[212:215],v245 offset:20480\n"
MU"v48,s55,v48\n"
MF"v[156:159],a[108:111],v[216:219],v[156:159]\n"
WT"lgkmcnt(2)\n"
MU"v49,s55,v49\n"
MU"v50,s55,v50\n"
MF"v[128:131],a[128:131],v[192:195],v[128:131]\n"
BS"v[8:11],v250,s[8:11],s49 offen offset:1024 sc1\n"
MF"v[128:131],a[144:147],v[196:199],v[128:131]\n"
MU"v51,s55,v51\n"
MU"v52,s55,v52\n"
MF"v[132:135],a[148:151],v[196:199],v[132:135]\n"
RD"v[216:219],v244 offset:22528\n"
MU"v53,s55,v53\n"
MF"v[132:135],a[132:135],v[192:195],v[132:135]\n"
MU"v54,s55,v54\n"
MU"v55,s55,v55\n"
MF"v[136:139],a[136:139],v[192:195],v[136:139]\n"
PK"v48,v48,v49\n"
PK"v49,v50,v51\n"
MF"v[136:139],a[152:155],v[196:199],v[136:139]\n"
RD"v[220:223],v245 offset:22528\n"
PK"v50,v52,v53\n"
MF"v[140:143],a[156:159],v[196:199],v[140:143]\n"
PK"v51,v54,v55\n"
MF"v[140:143],a[140:143],v[192:195],v[140:143]\n"
MF"v[144:147],a[128:131],v[200:203],v[144:147]\n"
MU"v56,s55,v56\n"
MU"v57,s55,v57\n"
MF"v[144:147],a[144:147],v[204:207],v[144:147]\n"
RD"v[192:195],v244 offset:40960\n"
MU"v58,s55,v58\n"
MF"v[148:151],a[148:151],v[204:207],v[148:151]\n"
MU"v59,s55,v59\n"
MU"v60,s55,v60\n"
MF"v[148:151],a[132:135],v[200:203],v[148:151]\n"
MU"v61,s55,v61\n"
MU"v62,s55,v62\n"
MF"v[152:155],a[136:139],v[200:203],v[152:155]\n"
MU"v63,s55,v63\n"
PK"v56,v56,v57\n"
MF"v[152:155],a[152:155],v[204:207],v[152:155]\n"
RD"v[196:199],v245 offset:40960\n"
PK"v57,v58,v59\n"
MF"v[156:159],a[156:159],v[204:207],v[156:159]\n"
PK"v58,v60,v61\n"
PK"v59,v62,v63\n"
MF"v[156:159],a[140:143],v[200:203],v[156:159]\n"
WT"lgkmcnt(2)\n"
MF"v[128:131],a[160:163],v[208:211],v[128:131]\n"
MF"v[128:131],a[176:179],v[212:215],v[128:131]\n"
RD"v[200:203],v244 offset:43008\n"
MF"v[132:135],a[180:183],v[212:215],v[132:135]\n"
MU"v64,s55,v64\n"
MU"v65,s55,v65\n"
MF"v[132:135],a[164:167],v[208:211],v[132:135]\n"
MU"v66,s55,v66\n"
MU"v67,s55,v67\n"
MF"v[136:139],a[168:171],v[208:211],v[136:139]\n"
MU"v68,s55,v68\n"
MU"v69,s55,v69\n"
MF"v[136:139],a[184:187],v[212:215],v[136:139]\n"
RD"v[204:207],v245 offset:43008\n"
MU"v70,s55,v70\n"
MF"v[140:143],a[188:191],v[212:215],v[140:143]\n"
MU"v71,s55,v71\n"
PK"v64,v64,v65\n"
MF"v[140:143],a[172:175],v[208:211],v[140:143]\n"
PK"v65,v66,v67\n"
PK"v66,v68,v69\n"
MF"v[144:147],a[160:163],v[216:219],v[144:147]\n"
RD"v[208:211],v244 offset:45056\n"
PK"v67,v70,v71\n"
MF"v[144:147],a[176:179],v[220:223],v[144:147]\n"
MF"v[148:151],a[180:183],v[220:223],v[148:151]\n"
MF"v[148:151],a[164:167],v[216:219],v[148:151]\n"
MF"v[152:155],a[168:171],v[216:219],v[152:155]\n"
RD"v[212:215],v245 offset:45056\n"
MU"v72,s55,v72\n"
MF"v[152:155],a[184:187],v[220:223],v[152:155]\n"
"s_mov_b32 s49,4096\n"
MU"v73,s55,v73\n"
MU"v74,s55,v74\n"
MF"v[156:159],a[188:191],v[220:223],v[156:159]\n"
MU"v75,s55,v75\n"
MU"v76,s55,v76\n"
MF"v[156:159],a[172:175],v[216:219],v[156:159]\n"
WT"lgkmcnt(1)\n"
BS"v[16:19],v250,s[8:11],s49 offen offset:0 sc1\n"
MF"v[128:131],a[192:195],v[192:195],v[128:131]\n"
RD"v[216:219],v244 offset:47104\n"
MU"v77,s55,v77\n"
MF"v[128:131],a[208:211],v[196:199],v[128:131]\n"
MU"v78,s55,v78\n"
MU"v79,s55,v79\n"
MF"v[132:135],a[212:215],v[196:199],v[132:135]\n"
PK"v72,v72,v73\n"
PK"v73,v74,v75\n"
MF"v[132:135],a[196:199],v[192:195],v[132:135]\n"
PK"v74,v76,v77\n"
PK"v75,v78,v79\n"
MF"v[136:139],a[200:203],v[192:195],v[136:139]\n"
RD"v[220:223],v245 offset:47104\n"
MF"v[136:139],a[216:219],v[196:199],v[136:139]\n"
MF"v[140:143],a[220:223],v[196:199],v[140:143]\n"
MU"v80,s55,v80\n"
MU"v81,s55,v81\n"
MF"v[140:143],a[204:207],v[192:195],v[140:143]\n"
MU"v82,s55,v82\n"
MU"v83,s55,v83\n"
MF"v[144:147],a[192:195],v[200:203],v[144:147]\n"
MU"v84,s55,v84\n"
MU"v85,s55,v85\n"
MF"v[144:147],a[208:211],v[204:207],v[144:147]\n"
MU"v86,s55,v86\n"
MU"v87,s55,v87\n"
MF"v[148:151],a[212:215],v[204:207],v[148:151]\n"
PK"v80,v80,v81\n"
PK"v81,v82,v83\n"
MF"v[148:151],a[196:199],v[200:203],v[148:151]\n"
PK"v82,v84,v85\n"
PK"v83,v86,v87\n"
MF"v[152:155],a[200:203],v[200:203],v[152:155]\n"
MF"v[152:155],a[216:219],v[204:207],v[152:155]\n"
WT"vmcnt(19) lgkmcnt(0)\n"
"s_barrier\n"
MF"v[156:159],a[220:223],v[204:207],v[156:159]\n"
RD"v[192:195],v242 offset:0\n"
MF"v[156:159],a[204:207],v[200:203],v[156:159]\n"
MF"v[128:131],a[224:227],v[208:211],v[128:131]\n"
MU"v88,s55,v88\n"
MU"v89,s55,v89\n"
MF"v[128:131],a[240:243],v[212:215],v[128:131]\n"
MU"v90,s55,v90\n"
MU"v91,s55,v91\n"
MF"v[132:135],a[244:247],v[212:215],v[132:135]\n"
RD"v[196:199],v243 offset:0\n"
MU"v92,s55,v92\n"
MF"v[132:135],a[228:231],v[208:211],v[132:135]\n"
MU"v93,s55,v93\n"
MU"v94,s55,v94\n"
MF"v[136:139],a[232:235],v[208:211],v[136:139]\n"
MU"v95,s55,v95\n"
PK"v88,v88,v89\n"
MF"v[136:139],a[248:251],v[212:215],v[136:139]\n"
PK"v89,v90,v91\n"
PK"v90,v92,v93\n"
MF"v[140:143],a[252:255],v[212:215],v[140:143]\n"
RD"v[200:203],v242 offset:2048\n"
PK"v91,v94,v95\n"
MF"v[140:143],a[236:239],v[208:211],v[140:143]\n"
MF"v[144:147],a[224:227],v[216:219],v[144:147]\n"
MF"v[144:147],a[240:243],v[220:223],v[144:147]\n"
MF"v[148:151],a[244:247],v[220:223],v[148:151]\n"
RD"v[204:207],v243 offset:2048\n"
MF"v[148:151],a[228:231],v[216:219],v[148:151]\n"
MF"v[152:155],a[232:235],v[216:219],v[152:155]\n"
MF"v[152:155],a[248:251],v[220:223],v[152:155]\n"
MF"v[156:159],a[252:255],v[220:223],v[156:159]\n"
RD"v[208:211],v242 offset:4096\n"
MF"v[156:159],a[236:239],v[216:219],v[156:159]\n"
WT"lgkmcnt(2)\n"
BS"v[24:27],v250,s[8:11],s49 offen offset:1024 sc1\n"
MF"v[36:39],a[0:3],v[192:195],0\n"
MF"v[36:39],a[16:19],v[196:199],v[36:39]\n"
RD"v[212:215],v243 offset:4096\n"
MF"v[68:71],a[20:23],v[196:199],0\n"
MF"v[68:71],a[4:7],v[192:195],v[68:71]\n"
MU"v128,s55,v128\n"
MU"v129,s55,v129\n"
MF"v[44:47],a[8:11],v[192:195],0\n"
MU"v130,s55,v130\n"
MU"v131,s55,v131\n"
MF"v[44:47],a[24:27],v[196:199],v[44:47]\n"
RD"v[216:219],v242 offset:6144\n"
MU"v132,s55,v132\n"
MF"v[76:79],a[28:31],v[196:199],0\n"
MU"v133,s55,v133\n"
MU"v134,s55,v134\n"
MF"v[76:79],a[12:15],v[192:195],v[76:79]\n"
MU"v135,s55,v135\n"
PK"v4,v128,v129\n"
MF"v[52:55],a[0:3],v[200:203],0\n"
WT"lgkmcnt(1)\n"
PK"v5,v130,v131\n"
PK"v6,v132,v133\n"
MF"v[52:55],a[16:19],v[204:207],v[52:55]\n"
RD"v[220:223],v243 offset:6144\n"
PK"v7,v134,v135\n"
MF"v[84:87],a[20:23],v[204:207],0\n"
MF"v[84:87],a[4:7],v[200:203],v[84:87]\n"
MF"v[60:63],a[8:11],v[200:203],0\n"
MF"v[60:63],a[24:27],v[204:207],v[60:63]\n"
RD"v[192:195],v242 offset:24576\n"
MU"v136,s55,v136\n"
MF"v[92:95],a[28:31],v[204:207],0\n"
MU"v137,s55,v137\n"
MU"v138,s55,v138\n"
MF"v[92:95],a[12:15],v[200:203],v[92:95]\n"
MU"v139,s55,v139\n"
MU"v140,s55,v140\n"
MF"v[36:39],a[32:35],v[208:211],v[36:39]\n"
MU"v141,s55,v141\n"
MU"v142,s55,v142\n"
MF"v[36:39],a[48:51],v[212:215],v[36:39]\n"
RD"v[196:199],v243 offset:24576\n"
MU"v143,s55,v143\n"
MF"v[68:71],a[52:55],v[212:215],v[68:71]\n"
PK"v12,v136,v137\n"
PK"v13,v138,v139\n"
MF"v[68:71],a[36:39],v[208:211],v[68:71]\n"
PK"v14,v140,v141\n"
PK"v15,v142,v143\n"
MF"v[44:47],a[40:43],v[208:211],v[44:47]\n"
RD"v[200:203],v242 offset:26624\n"
MF"v[44:47],a[56:59],v[212:215],v[44:47]\n"
MF"v[76:79],a[60:63],v[212:215],v[76:79]\n"
MF"v[76:79],a[44:47],v[208:211],v[76:79]\n"
WT"lgkmcnt(2)\n"
MU"v144,s55,v144\n"
MU"v145,s55,v145\n"
MF"v[52:55],a[32:35],v[216:219],v[52:55]\n"
RD"v[204:207],v243 offset:26624\n"
MU"v146,s55,v146\n"
MF"v[52:55],a[48:51],v[220:223],v[52:55]\n"
MU"v147,s55,v147\n"
MU"v148,s55,v148\n"
MF"v[84:87],a[52:55],v[220:223],v[84:87]\n"
MU"v149,s55,v149\n"
MU"v150,s55,v150\n"
MF"v[84:87],a[36:39],v[216:219],v[84:87]\n"
"s_mov_b32 s49,8192\n"
MU"v151,s55,v151\n"
PK"v20,v144,v145\n"
MF"v[60:63],a[40:43],v[216:219],v[60:63]\n"
RD"v[208:211],v242 offset:28672\n"
PK"v21,v146,v147\n"
MF"v[60:63],a[56:59],v[220:223],v[60:63]\n"
BS"v[32:35],v250,s[8:11],s49 offen offset:0 sc1\n"
MF"v[92:95],a[60:63],v[220:223],v[92:95]\n"
PK"v22,v148,v149\n"
PK"v23,v150,v151\n"
MF"v[92:95],a[44:47],v[216:219],v[92:95]\n"
MF"v[36:39],a[64:67],v[192:195],v[36:39]\n"
WT"lgkmcnt(1)\n"
RD"v[212:215],v243 offset:28672\n"
MF"v[36:39],a[80:83],v[196:199],v[36:39]\n"
MU"v152,s55,v152\n"
MU"v153,s55,v153\n"
MF"v[68:71],a[84:87],v[196:199],v[68:71]\n"
MU"v154,s55,v154\n"
MU"v155,s55,v155\n"
MF"v[68:71],a[68:71],v[192:195],v[68:71]\n"
RD"v[216:219],v242 offset:30720\n"
MU"v156,s55,v156\n"
MF"v[44:47],a[72:75],v[192:195],v[44:47]\n"
MU"v157,s55,v157\n"
MU"v158,s55,v158\n"
MF"v[44:47],a[88:91],v[196:199],v[44:47]\n"
MU"v159,s55,v159\n"
PK"v28,v152,v153\n"
MF"v[76:79],a[92:95],v[196:199],v[76:79]\n"
PK"v29,v154,v155\n"
PK"v30,v156,v157\n"
MF"v[76:79],a[76:79],v[192:195],v[76:79]\n"
RD"v[220:223],v243 offset:30720\n"
PK"v31,v158,v159\n"
MF"v[52:55],a[64:67],v[200:203],v[52:55]\n"
MF"v[52:55],a[80:83],v[204:207],v[52:55]\n"
MF"v[84:87],a[84:87],v[204:207],v[84:87]\n"
MF"v[84:87],a[68:71],v[200:203],v[84:87]\n"
RD"v[192:195],v244 offset:0\n"
MF"v[60:63],a[72:75],v[200:203],v[60:63]\n"
MF"v[60:63],a[88:91],v[204:207],v[60:63]\n"
MF"v[92:95],a[92:95],v[204:207],v[92:95]\n"
MF"v[92:95],a[76:79],v[200:203],v[92:95]\n"
WT"lgkmcnt(1)\n"
RD"v[196:199],v245 offset:0\n"
MF"v[36:39],a[96:99],v[208:211],v[36:39]\n"
MF"v[36:39],a[112:115],v[212:215],v[36:39]\n"
MF"v[68:71],a[116:119],v[212:215],v[68:71]\n"
MF"v[68:71],a[100:103],v[208:211],v[68:71]\n"
RD"v[200:203],v244 offset:2048\n"
MF"v[44:47],a[104:107],v[208:211],v[44:47]\n"
MF"v[44:47],a[120:123],v[212:215],v[44:47]\n"
MF"v[76:79],a[124:127],v[212:215],v[76:79]\n"
RD"v[204:207],v245 offset:2048\n"
MF"v[76:79],a[108:111],v[208:211],v[76:79]\n"
MF"v[52:55],a[96:99],v[216:219],v[52:55]\n"
MF"v[52:55],a[112:115],v[220:223],v[52:55]\n"
MF"v[84:87],a[116:119],v[220:223],v[84:87]\n"
SA"m0,s37,16384\n"
RD"v[208:211],v244 offset:4096\n"
SA"s48,s39,98304\n"
MF"v[84:87],a[100:103],v[216:219],v[84:87]\n"
MF"v[60:63],a[104:107],v[216:219],v[60:63]\n"
BS"v[40:43],v250,s[8:11],s49 offen offset:1024 sc1\n"
MF"v[60:63],a[120:123],v[220:223],v[60:63]\n"
BL"v234,s[12:15],s48 offen offset:0 lds\n"
MF"v[92:95],a[124:127],v[220:223],v[92:95]\n"
RD"v[212:215],v245 offset:4096\n"
MU"v96,s55,v96\n"
MF"v[92:95],a[108:111],v[216:219],v[92:95]\n"
WT"lgkmcnt(2)\n"
MU"v97,s55,v97\n"
MU"v98,s55,v98\n"
MF"v[36:39],a[128:131],v[192:195],v[36:39]\n"
MU"v99,s55,v99\n"
MU"v100,s55,v100\n"
MF"v[36:39],a[144:147],v[196:199],v[36:39]\n"
MU"v101,s55,v101\n"
MU"v102,s55,v102\n"
MF"v[68:71],a[148:151],v[196:199],v[68:71]\n"
RD"v[216:219],v244 offset:6144\n"
MU"v103,s55,v103\n"
MF"v[68:71],a[132:135],v[192:195],v[68:71]\n"
BL"v235,s[12:15],s48 offen offset:1024 lds\n"
MF"v[44:47],a[136:139],v[192:195],v[44:47]\n"
PK"v96,v96,v97\n"
PK"v97,v98,v99\n"
MF"v[44:47],a[152:155],v[196:199],v[44:47]\n"
RD"v[220:223],v245 offset:6144\n"
PK"v98,v100,v101\n"
MF"v[76:79],a[156:159],v[196:199],v[76:79]\n"
PK"v99,v102,v103\n"
MU"v104,s55,v104\n"
MF"v[76:79],a[140:143],v[192:195],v[76:79]\n"
MU"v105,s55,v105\n"
MU"v106,s55,v106\n"
MF"v[52:55],a[128:131],v[200:203],v[52:55]\n"
BL"v236,s[12:15],s48 offen offset:2048 lds\n"
MF"v[52:55],a[144:147],v[204:207],v[52:55]\n"
RD"v[192:195],v244 offset:24576\n"
MU"v107,s55,v107\n"
MF"v[84:87],a[148:151],v[204:207],v[84:87]\n"
MU"v108,s55,v108\n"
MU"v109,s55,v109\n"
MF"v[84:87],a[132:135],v[200:203],v[84:87]\n"
MU"v110,s55,v110\n"
MU"v111,s55,v111\n"
MF"v[60:63],a[136:139],v[200:203],v[60:63]\n"
PK"v104,v104,v105\n"
PK"v105,v106,v107\n"
MF"v[60:63],a[152:155],v[204:207],v[60:63]\n"
RD"v[196:199],v245 offset:24576\n"
PK"v106,v108,v109\n"
MF"v[92:95],a[156:159],v[204:207],v[92:95]\n"
BL"v237,s[12:15],s48 offen offset:3072 lds\n"
MF"v[92:95],a[140:143],v[200:203],v[92:95]\n"
WT"lgkmcnt(2)\n"
PK"v107,v110,v111\n"
MF"v[36:39],a[160:163],v[208:211],v[36:39]\n"
MU"v112,s55,v112\n"
MU"v113,s55,v113\n"
MF"v[36:39],a[176:179],v[212:215],v[36:39]\n"
RD"v[200:203],v244 offset:26624\n"
MU"v114,s55,v114\n"
MF"v[68:71],a[180:183],v[212:215],v[68:71]\n"
SA"m0,s37,20480\n"
MU"v115,s55,v115\n"
MU"v116,s55,v116\n"
MF"v[68:71],a[164:167],v[208:211],v[68:71]\n"
SA"s48,s98,98304\n"
MU"v117,s55,v117\n"
MU"v118,s55,v118\n"
MF"v[44:47],a[168:171],v[208:211],v[44:47]\n"
BL"v234,s[12:15],s48 offen offset:0 lds\n"
MF"v[44:47],a[184:187],v[212:215],v[44:47]\n"
RD"v[204:207],v245 offset:26624\n"
MU"v119,s55,v119\n"
MF"v[76:79],a[188:191],v[212:215],v[76:79]\n"
PK"v112,v112,v113\n"
PK"v113,v114,v115\n"
MF"v[76:79],a[172:175],v[208:211],v[76:79]\n"
PK"v114,v116,v117\n"
PK"v115,v118,v119\n"
MF"v[52:55],a[160:163],v[216:219],v[52:55]\n"
RD"v[208:211],v244 offset:28672\n"
MF"v[52:55],a[176:179],v[220:223],v[52:55]\n"
"s_mov_b32 s49,12288\n"
MF"v[84:87],a[180:183],v[220:223],v[84:87]\n"
BL"v235,s[12:15],s48 offen offset:1024 lds\n"
MF"v[84:87],a[164:167],v[216:219],v[84:87]\n"
BS"v[48:51],v250,s[8:11],s49 offen offset:0 sc1\n"
MF"v[60:63],a[168:171],v[216:219],v[60:63]\n"
RD"v[212:215],v245 offset:28672\n"
MU"v120,s55,v120\n"
MF"v[60:63],a[184:187],v[220:223],v[60:63]\n"
MU"v121,s55,v121\n"
MU"v122,s55,v122\n"
MF"v[92:95],a[188:191],v[220:223],v[92:95]\n"
MU"v123,s55,v123\n"
MU"v124,s55,v124\n"
MF"v[92:95],a[172:175],v[216:219],v[92:95]\n"
WT"lgkmcnt(1)\n"
MU"v125,s55,v125\n"
MU"v126,s55,v126\n"
MF"v[36:39],a[192:195],v[192:195],v[36:39]\n"
RD"v[216:219],v244 offset:30720\n"
MU"v127,s55,v127\n"
MF"v[36:39],a[208:211],v[196:199],v[36:39]\n"
BL"v236,s[12:15],s48 offen offset:2048 lds\n"
MF"v[68:71],a[212:215],v[196:199],v[68:71]\n"
PK"v120,v120,v121\n"
PK"v121,v122,v123\n"
MF"v[68:71],a[196:199],v[192:195],v[68:71]\n"
PK"v122,v124,v125\n"
PK"v123,v126,v127\n"
MF"v[44:47],a[200:203],v[192:195],v[44:47]\n"
RD"v[220:223],v245 offset:30720\n"
MF"v[44:47],a[216:219],v[196:199],v[44:47]\n"
MF"v[76:79],a[220:223],v[196:199],v[76:79]\n"
BL"v237,s[12:15],s48 offen offset:3072 lds\n"
MF"v[76:79],a[204:207],v[192:195],v[76:79]\n"
MF"v[52:55],a[192:195],v[200:203],v[52:55]\n"
MF"v[52:55],a[208:211],v[204:207],v[52:55]\n"
MF"v[84:87],a[212:215],v[204:207],v[84:87]\n"
MF"v[84:87],a[196:199],v[200:203],v[84:87]\n"
"v_lshrrev_b32 v251,2,v224\n"
MF"v[60:63],a[200:203],v[200:203],v[60:63]\n"
"v_and_b32 v251,28,v251\n"
MF"v[60:63],a[216:219],v[204:207],v[60:63]\n"
WT"vmcnt(23) lgkmcnt(0)\n"
"s_barrier\n"
MF"v[92:95],a[220:223],v[204:207],v[92:95]\n"
RD"v[192:195],v242 offset:8192\n"
"ds_read_b32 v234,v251 offset:16384\n"
MF"v[92:95],a[204:207],v[200:203],v[92:95]\n"
MF"v[36:39],a[224:227],v[208:211],v[36:39]\n"
MF"v[36:39],a[240:243],v[212:215],v[36:39]\n"
MF"v[68:71],a[244:247],v[212:215],v[68:71]\n"
RD"v[196:199],v243 offset:8192\n"
MF"v[68:71],a[228:231],v[208:211],v[68:71]\n"
MF"v[44:47],a[232:235],v[208:211],v[44:47]\n"
MF"v[44:47],a[248:251],v[212:215],v[44:47]\n"
MF"v[76:79],a[252:255],v[212:215],v[76:79]\n"
RD"v[200:203],v242 offset:10240\n"
MF"v[76:79],a[236:239],v[208:211],v[76:79]\n"
WT"lgkmcnt(2)\n"
"v_not_b32 v251,v234\n"
"v_bfe_u32 v252,v251,4,7\n"
MF"v[52:55],a[224:227],v[216:219],v[52:55]\n"
"v_mul_u32_u24 v252,20480,v252\n"
MF"v[52:55],a[240:243],v[220:223],v[52:55]\n"
"v_and_b32 v251,15,v251\n"
MF"v[84:87],a[244:247],v[220:223],v[84:87]\n"
RD"v[204:207],v243 offset:10240\n"
"v_lshl_add_u32 v251,v251,4,v252\n"
MF"v[84:87],a[228:231],v[216:219],v[84:87]\n"
BS"v[56:59],v250,s[8:11],s49 offen offset:1024 sc1\n"
MF"v[60:63],a[232:235],v[216:219],v[60:63]\n"
"v_and_b32 v252,0x380,v224\n"
"v_lshl_add_u32 v251,v252,1,v251\n"
MF"v[60:63],a[248:251],v[220:223],v[60:63]\n"
"v_add_u32 v252,v252,v224\n"
MF"v[92:95],a[252:255],v[220:223],v[92:95]\n"
"s_lshl_b32 s92,s34,2\n"
RD"v[208:211],v242 offset:12288\n"
MF"v[92:95],a[236:239],v[216:219],v[92:95]\n"
SA"s92,s92,s30\n"
MF"v[128:131],a[0:3],v[192:195],0\n"
WT"lgkmcnt(2)\n"
"s_lshl_b32 s93,s92,11\n"
MF"v[128:131],a[16:19],v[196:199],v[128:131]\n"
"s_cmp_lt_u32 s92,10\n"
RD"v[212:215],v243 offset:12288\n"
"s_cselect_b32 s93,s93,2130706432\n"
MF"v[132:135],a[20:23],v[196:199],0\n"
SA"s92,s93,65536\n"
MF"v[132:135],a[4:7],v[192:195],v[132:135]\n"
BL"v[100:103],v251,s[8:11],s92 offen sc1\n"
MF"v[136:139],a[8:11],v[192:195],0\n"
"s_mul_i32 s92,s35,20480\n"
MU"v36,s55,v36\n"
MU"v37,s55,v37\n"
MF"v[136:139],a[24:27],v[196:199],v[136:139]\n"
SA"s92,s92,s93\n"
RD"v[216:219],v242 offset:14336\n"
MU"v38,s55,v38\n"
MF"v[140:143],a[28:31],v[196:199],0\n"
SA"s92,s92,2770944\n"
MU"v39,s55,v39\n"
MU"v68,s55,v68\n"
MF"v[140:143],a[12:15],v[192:195],v[140:143]\n"
MU"v69,s55,v69\n"
MU"v70,s55,v70\n"
MF"v[144:147],a[0:3],v[200:203],0\n"
WT"lgkmcnt(1)\n"
MU"v71,s55,v71\n"
PK"v36,v36,v37\n"
MF"v[144:147],a[16:19],v[204:207],v[144:147]\n"
RD"v[220:223],v243 offset:14336\n"
PK"v37,v38,v39\n"
MF"v[148:151],a[20:23],v[204:207],0\n"
PK"v38,v68,v69\n"
PK"v39,v70,v71\n"
MF"v[148:151],a[4:7],v[200:203],v[148:151]\n"
MF"v[152:155],a[8:11],v[200:203],0\n"
MF"v[152:155],a[24:27],v[204:207],v[152:155]\n"
RD"v[192:195],v242 offset:32768\n"
MU"v44,s55,v44\n"
MF"v[156:159],a[28:31],v[204:207],0\n"
MU"v45,s55,v45\n"
MU"v46,s55,v46\n"
MF"v[156:159],a[12:15],v[200:203],v[156:159]\n"
MU"v47,s55,v47\n"
MU"v76,s55,v76\n"
MF"v[128:131],a[32:35],v[208:211],v[128:131]\n"
MU"v77,s55,v77\n"
MU"v78,s55,v78\n"
MF"v[128:131],a[48:51],v[212:215],v[128:131]\n"
RD"v[196:199],v243 offset:32768\n"
MU"v79,s55,v79\n"
MF"v[132:135],a[52:55],v[212:215],v[132:135]\n"
PK"v44,v44,v45\n"
PK"v45,v46,v47\n"
MF"v[132:135],a[36:39],v[208:211],v[132:135]\n"
PK"v46,v76,v77\n"
PK"v47,v78,v79\n"
MF"v[136:139],a[40:43],v[208:211],v[136:139]\n"
SA"m0,s36,0\n"
RD"v[200:203],v242 offset:34816\n"
SA"s48,s60,s61\n"
MF"v[136:139],a[56:59],v[212:215],v[136:139]\n"
MF"v[140:143],a[60:63],v[212:215],v[140:143]\n"
BL"v238,s[20:23],s48 offen offset:0 lds\n"
MF"v[140:143],a[44:47],v[208:211],v[140:143]\n"
WT"lgkmcnt(2)\n"
MU"v52,s55,v52\n"
MU"v53,s55,v53\n"
MF"v[144:147],a[32:35],v[216:219],v[144:147]\n"
"s_mov_b32 s49,16384\n"
RD"v[204:207],v243 offset:34816\n"
MU"v54,s55,v54\n"
MF"v[144:147],a[48:51],v[220:223],v[144:147]\n"
BS"v[64:67],v250,s[8:11],s49 offen offset:0 sc1\n"
MF"v[148:151],a[52:55],v[220:223],v[148:151]\n"
MU"v55,s55,v55\n"
MU"v84,s55,v84\n"
MF"v[148:151],a[36:39],v[216:219],v[148:151]\n"
MU"v85,s55,v85\n"
MU"v86,s55,v86\n"
MF"v[152:155],a[40:43],v[216:219],v[152:155]\n"
RD"v[208:211],v242 offset:36864\n"
MU"v87,s55,v87\n"
MF"v[152:155],a[56:59],v[220:223],v[152:155]\n"
PK"v52,v52,v53\n"
PK"v53,v54,v55\n"
MF"v[156:159],a[60:63],v[220:223],v[156:159]\n"
BL"v239,s[20:23],s48 offen offset:1024 lds\n"
MF"v[156:159],a[44:47],v[216:219],v[156:159]\n"
PK"v54,v84,v85\n"
PK"v55,v86,v87\n"
MF"v[128:131],a[64:67],v[192:195],v[128:131]\n"
WT"lgkmcnt(1)\n"
RD"v[212:215],v243 offset:36864\n"
MF"v[128:131],a[80:83],v[196:199],v[128:131]\n"
MU"v60,s55,v60\n"
MU"v61,s55,v61\n"
MF"v[132:135],a[84:87],v[196:199],v[132:135]\n"
MU"v62,s55,v62\n"
MU"v63,s55,v63\n"
MF"v[132:135],a[68:71],v[192:195],v[132:135]\n"
RD"v[216:219],v242 offset:38912\n"
MU"v92,s55,v92\n"
MF"v[136:139],a[72:75],v[192:195],v[136:139]\n"
MU"v93,s55,v93\n"
MU"v94,s55,v94\n"
MF"v[136:139],a[88:91],v[196:199],v[136:139]\n"
MU"v95,s55,v95\n"
PK"v60,v60,v61\n"
MF"v[140:143],a[92:95],v[196:199],v[140:143]\n"
BL"v240,s[20:23],s48 offen offset:2048 lds\n"
MF"v[140:143],a[76:79],v[192:195],v[140:143]\n"
RD"v[220:223],v243 offset:38912\n"
PK"v61,v62,v63\n"
MF"v[144:147],a[64:67],v[200:203],v[144:147]\n"
PK"v62,v92,v93\n"
PK"v63,v94,v95\n"
MF"v[144:147],a[80:83],v[204:207],v[144:147]\n"
MF"v[148:151],a[84:87],v[204:207],v[148:151]\n"
MF"v[148:151],a[68:71],v[200:203],v[148:151]\n"
RD"v[192:195],v244 offset:8192\n"
MF"v[152:155],a[72:75],v[200:203],v[152:155]\n"
MF"v[152:155],a[88:91],v[204:207],v[152:155]\n"
MF"v[156:159],a[92:95],v[204:207],v[156:159]\n"
SA"s61,s61,524288\n"
BL"v241,s[20:23],s48 offen offset:3072 lds\n"
MF"v[156:159],a[76:79],v[200:203],v[156:159]\n"
WT"lgkmcnt(1)\n"
RD"v[196:199],v245 offset:8192\n"
MF"v[128:131],a[96:99],v[208:211],v[128:131]\n"
MF"v[128:131],a[112:115],v[212:215],v[128:131]\n"
MF"v[132:135],a[116:119],v[212:215],v[132:135]\n"
MF"v[132:135],a[100:103],v[208:211],v[132:135]\n"
RD"v[200:203],v244 offset:10240\n"
MF"v[136:139],a[104:107],v[208:211],v[136:139]\n"
MF"v[136:139],a[120:123],v[212:215],v[136:139]\n"
MF"v[140:143],a[124:127],v[212:215],v[140:143]\n"
RD"v[204:207],v245 offset:10240\n"
MF"v[140:143],a[108:111],v[208:211],v[140:143]\n"
MF"v[144:147],a[96:99],v[216:219],v[144:147]\n"
BS"v[72:75],v250,s[8:11],s49 offen offset:1024 sc1\n"
MF"v[144:147],a[112:115],v[220:223],v[144:147]\n"
MF"v[148:151],a[116:119],v[220:223],v[148:151]\n"
RD"v[208:211],v244 offset:12288\n"
MF"v[148:151],a[100:103],v[216:219],v[148:151]\n"
MF"v[152:155],a[104:107],v[216:219],v[152:155]\n"
SA"m0,s38,65536\n"
SA"s48,s60,s61\n"
MF"v[152:155],a[120:123],v[220:223],v[152:155]\n"
MF"v[156:159],a[124:127],v[220:223],v[156:159]\n"
RD"v[212:215],v245 offset:12288\n"
MF"v[156:159],a[108:111],v[216:219],v[156:159]\n"
WT"lgkmcnt(2)\n"
BL"v238,s[20:23],s48 offen offset:0 lds\n"
MF"v[128:131],a[128:131],v[192:195],v[128:131]\n"
MF"v[128:131],a[144:147],v[196:199],v[128:131]\n"
MF"v[132:135],a[148:151],v[196:199],v[132:135]\n"
RD"v[216:219],v244 offset:14336\n"
MF"v[132:135],a[132:135],v[192:195],v[132:135]\n"
MF"v[136:139],a[136:139],v[192:195],v[136:139]\n"
MF"v[136:139],a[152:155],v[196:199],v[136:139]\n"
RD"v[220:223],v245 offset:14336\n"
MF"v[140:143],a[156:159],v[196:199],v[140:143]\n"
BL"v239,s[20:23],s48 offen offset:1024 lds\n"
MF"v[140:143],a[140:143],v[192:195],v[140:143]\n"
MF"v[144:147],a[128:131],v[200:203],v[144:147]\n"
MF"v[144:147],a[144:147],v[204:207],v[144:147]\n"
RD"v[192:195],v244 offset:32768\n"
MF"v[148:151],a[148:151],v[204:207],v[148:151]\n"
MF"v[148:151],a[132:135],v[200:203],v[148:151]\n"
MF"v[152:155],a[136:139],v[200:203],v[152:155]\n"
MF"v[152:155],a[152:155],v[204:207],v[152:155]\n"
RD"v[196:199],v245 offset:32768\n"
MF"v[156:159],a[156:159],v[204:207],v[156:159]\n"
BL"v240,s[20:23],s48 offen offset:2048 lds\n"
MF"v[156:159],a[140:143],v[200:203],v[156:159]\n"
WT"lgkmcnt(2)\n"
MF"v[128:131],a[160:163],v[208:211],v[128:131]\n"
MF"v[128:131],a[176:179],v[212:215],v[128:131]\n"
RD"v[200:203],v244 offset:34816\n"
MF"v[132:135],a[180:183],v[212:215],v[132:135]\n"
MF"v[132:135],a[164:167],v[208:211],v[132:135]\n"
MF"v[136:139],a[168:171],v[208:211],v[136:139]\n"
MF"v[136:139],a[184:187],v[212:215],v[136:139]\n"
"s_mov_b32 s49,20480\n"
RD"v[204:207],v245 offset:34816\n"
MF"v[140:143],a[188:191],v[212:215],v[140:143]\n"
SA"s61,s61,524288\n"
BL"v241,s[20:23],s48 offen offset:3072 lds\n"
MF"v[140:143],a[172:175],v[208:211],v[140:143]\n"
"s_cmp_eq_u32 s61,5242880\n"
BS"v[80:83],v250,s[8:11],s49 offen offset:0 sc1\n"
MF"v[144:147],a[160:163],v[216:219],v[144:147]\n"
"s_cselect_b32 s61,0,s61\n"
RD"v[208:211],v244 offset:36864\n"
MF"v[144:147],a[176:179],v[220:223],v[144:147]\n"
MF"v[148:151],a[180:183],v[220:223],v[148:151]\n"
MF"v[148:151],a[164:167],v[216:219],v[148:151]\n"
MF"v[152:155],a[168:171],v[216:219],v[152:155]\n"
SA"m0,s38,114688\n"
RD"v[212:215],v245 offset:36864\n"
SA"s48,s60,s61\n"
MF"v[152:155],a[184:187],v[220:223],v[152:155]\n"
MF"v[156:159],a[188:191],v[220:223],v[156:159]\n"
BL"v238,s[20:23],s48 offen offset:0 lds\n"
MF"v[156:159],a[172:175],v[216:219],v[156:159]\n"
WT"lgkmcnt(1)\n"
MF"v[128:131],a[192:195],v[192:195],v[128:131]\n"
RD"v[216:219],v244 offset:38912\n"
MF"v[128:131],a[208:211],v[196:199],v[128:131]\n"
MF"v[132:135],a[212:215],v[196:199],v[132:135]\n"
MF"v[132:135],a[196:199],v[192:195],v[132:135]\n"
MF"v[136:139],a[200:203],v[192:195],v[136:139]\n"
RD"v[220:223],v245 offset:38912\n"
MF"v[136:139],a[216:219],v[196:199],v[136:139]\n"
MF"v[140:143],a[220:223],v[196:199],v[140:143]\n"
BL"v239,s[20:23],s48 offen offset:1024 lds\n"
MF"v[140:143],a[204:207],v[192:195],v[140:143]\n"
WT"vmcnt(13)\n"
BS"v[100:103],v252,s[8:11],s92 offen sc1\n"
MF"v[144:147],a[192:195],v[200:203],v[144:147]\n"
MF"v[144:147],a[208:211],v[204:207],v[144:147]\n"
MF"v[148:151],a[212:215],v[204:207],v[148:151]\n"
MF"v[148:151],a[196:199],v[200:203],v[148:151]\n"
MF"v[152:155],a[200:203],v[200:203],v[152:155]\n"
MF"v[152:155],a[216:219],v[204:207],v[152:155]\n"
WT"lgkmcnt(0)\n"
"s_barrier\n"
MF"v[156:159],a[220:223],v[204:207],v[156:159]\n"
RD"v[192:195],v242 offset:16384\n"
MF"v[156:159],a[204:207],v[200:203],v[156:159]\n"
BL"v240,s[20:23],s48 offen offset:2048 lds\n"
MF"v[128:131],a[224:227],v[208:211],v[128:131]\n"
MF"v[128:131],a[240:243],v[212:215],v[128:131]\n"
MF"v[132:135],a[244:247],v[212:215],v[132:135]\n"
RD"v[196:199],v243 offset:16384\n"
MF"v[132:135],a[228:231],v[208:211],v[132:135]\n"
MF"v[136:139],a[232:235],v[208:211],v[136:139]\n"
MF"v[136:139],a[248:251],v[212:215],v[136:139]\n"
MF"v[140:143],a[252:255],v[212:215],v[140:143]\n"
SA"s61,s61,524288\n"
RD"v[200:203],v242 offset:18432\n"
MF"v[140:143],a[236:239],v[208:211],v[140:143]\n"
BS"v[88:91],v250,s[8:11],s49 offen offset:1024 sc1\n"
MF"v[144:147],a[224:227],v[216:219],v[144:147]\n"
"s_lshl_b32 s50,s34,3\n"
BL"v241,s[20:23],s48 offen offset:3072 lds\n"
MF"v[144:147],a[240:243],v[220:223],v[144:147]\n"
SA"s51,s50,s30\n"
MF"v[148:151],a[244:247],v[220:223],v[148:151]\n"
"s_mul_i32 s51,s51,20480\n"
RD"v[204:207],v243 offset:18432\n"
MF"v[148:151],a[228:231],v[216:219],v[148:151]\n"
SA"s56,s51,65536\n"
MF"v[152:155],a[232:235],v[216:219],v[152:155]\n"
SA"m0,s36,16384\n"
SA"s48,s60,s61\n"
SA"s57,s56,81920\n"
MF"v[152:155],a[248:251],v[220:223],v[152:155]\n"
"s_sub_u32 s51,s30,s50\n"
MF"v[156:159],a[252:255],v[220:223],v[156:159]\n"
SA"s51,s51,120\n"
RD"v[208:211],v242 offset:20480\n"
MF"v[156:159],a[236:239],v[216:219],v[156:159]\n"
WT"lgkmcnt(2)\n"
BL"v238,s[20:23],s48 offen offset:0 lds\n"
MF"v[0:3],a[0:3],v[192:195],0\n"
"s_mul_i32 s51,s51,20480\n"
SA"s58,s51,65536\n"
"s_lshl_b32 s99,s99,12\n"
MF"v[0:3],a[16:19],v[196:199],v[0:3]\n"
SA"s59,s58,81920\n"
RD"v[212:215],v243 offset:20480\n"
MF"v[8:11],a[20:23],v[196:199],0\n"
"v_add_u32 v235,s99,v224\n"
MF"v[8:11],a[4:7],v[192:195],v[8:11]\n"
MU"v128,s55,v128\n"
MU"v129,s55,v129\n"
MF"v[16:19],a[8:11],v[192:195],0\n"
MU"v130,s55,v130\n"
MU"v131,s55,v131\n"
MF"v[16:19],a[24:27],v[196:199],v[16:19]\n"
RD"v[216:219],v242 offset:22528\n"
MU"v132,s55,v132\n"
MF"v[24:27],a[28:31],v[196:199],0\n"
BL"v239,s[20:23],s48 offen offset:1024 lds\n"
MF"v[24:27],a[12:15],v[192:195],v[24:27]\n"
MU"v133,s55,v133\n"
MU"v134,s55,v134\n"
MF"v[32:35],a[0:3],v[200:203],0\n"
WT"lgkmcnt(1)\n"
MU"v135,s55,v135\n"
PK"v64,v128,v129\n"
MF"v[32:35],a[16:19],v[204:207],v[32:35]\n"
RD"v[220:223],v243 offset:22528\n"
PK"v65,v130,v131\n"
MF"v[40:43],a[20:23],v[204:207],0\n"
PK"v66,v132,v133\n"
PK"v67,v134,v135\n"
MF"v[40:43],a[4:7],v[200:203],v[40:43]\n"
MF"v[48:51],a[8:11],v[200:203],0\n"
MF"v[48:51],a[24:27],v[204:207],v[48:51]\n"
RD"v[192:195],v242 offset:40960\n"
MU"v136,s55,v136\n"
MF"v[56:59],a[28:31],v[204:207],0\n"
BL"v240,s[20:23],s48 offen offset:2048 lds\n"
MF"v[56:59],a[12:15],v[200:203],v[56:59]\n"
MU"v137,s55,v137\n"
MU"v138,s55,v138\n"
MF"v[0:3],a[32:35],v[208:211],v[0:3]\n"
BL"a[0:3],v235,s[8:11],s56 offen offset:0 sc1\n"
MF"v[0:3],a[48:51],v[212:215],v[0:3]\n"
RD"v[196:199],v243 offset:40960\n"
MU"v139,s55,v139\n"
MF"v[8:11],a[52:55],v[212:215],v[8:11]\n"
MU"v140,s55,v140\n"
MU"v141,s55,v141\n"
MF"v[8:11],a[36:39],v[208:211],v[8:11]\n"
"s_mov_b32 s49,24576\n"
MU"v142,s55,v142\n"
MU"v143,s55,v143\n"
MF"v[16:19],a[40:43],v[208:211],v[16:19]\n"
RD"v[200:203],v242 offset:43008\n"
PK"v68,v136,v137\n"
MF"v[16:19],a[56:59],v[212:215],v[16:19]\n"
BS"v[96:99],v250,s[8:11],s49 offen offset:0 sc1\n"
MF"v[24:27],a[60:63],v[212:215],v[24:27]\n"
SA"s61,s61,524288\n"
BL"v241,s[20:23],s48 offen offset:3072 lds\n"
MF"v[24:27],a[44:47],v[208:211],v[24:27]\n"
WT"lgkmcnt(2)\n"
BL"a[4:7],v235,s[8:11],s57 offen offset:0 sc1\n"
MF"v[32:35],a[32:35],v[216:219],v[32:35]\n"
"s_cmp_eq_u32 s61,5242880\n"
RD"v[204:207],v243 offset:43008\n"
PK"v69,v138,v139\n"
MF"v[32:35],a[48:51],v[220:223],v[32:35]\n"
"s_cselect_b32 s61,0,s61\n"
PK"v70,v140,v141\n"
PK"v71,v142,v143\n"
MF"v[40:43],a[52:55],v[220:223],v[40:43]\n"
MU"v144,s55,v144\n"
MU"v145,s55,v145\n"
MF"v[40:43],a[36:39],v[216:219],v[40:43]\n"
MU"v146,s55,v146\n"
MU"v147,s55,v147\n"
MF"v[48:51],a[40:43],v[216:219],v[48:51]\n"
RD"v[208:211],v242 offset:45056\n"
MU"v148,s55,v148\n"
MF"v[48:51],a[56:59],v[220:223],v[48:51]\n"
BL"a[8:11],v235,s[8:11],s58 offen offset:0 sc1\n"
MF"v[56:59],a[60:63],v[220:223],v[56:59]\n"
MU"v149,s55,v149\n"
MU"v150,s55,v150\n"
MF"v[56:59],a[44:47],v[216:219],v[56:59]\n"
MU"v151,s55,v151\n"
PK"v72,v144,v145\n"
MF"v[0:3],a[64:67],v[192:195],v[0:3]\n"
WT"lgkmcnt(1)\n"
RD"v[212:215],v243 offset:45056\n"
PK"v73,v146,v147\n"
MF"v[0:3],a[80:83],v[196:199],v[0:3]\n"
PK"v74,v148,v149\n"
PK"v75,v150,v151\n"
MF"v[8:11],a[84:87],v[196:199],v[8:11]\n"
BL"a[12:15],v235,s[8:11],s59 offen offset:0 sc1\n"
MF"v[8:11],a[68:71],v[192:195],v[8:11]\n"
RD"v[216:219],v242 offset:47104\n"
MU"v152,s55,v152\n"
MF"v[16:19],a[72:75],v[192:195],v[16:19]\n"
MU"v153,s55,v153\n"
MU"v154,s55,v154\n"
MF"v[16:19],a[88:91],v[196:199],v[16:19]\n"
MU"v155,s55,v155\n"
MU"v156,s55,v156\n"
MF"v[24:27],a[92:95],v[196:199],v[24:27]\n"
MU"v157,s55,v157\n"
MU"v158,s55,v158\n"
MF"v[24:27],a[76:79],v[192:195],v[24:27]\n"
RD"v[220:223],v243 offset:47104\n"
MU"v159,s55,v159\n"
MF"v[32:35],a[64:67],v[200:203],v[32:35]\n"
BL"a[16:19],v235,s[8:11],s56 offen offset:1024 sc1\n"
MF"v[32:35],a[80:83],v[204:207],v[32:35]\n"
PK"v76,v152,v153\n"
PK"v77,v154,v155\n"
MF"v[40:43],a[84:87],v[204:207],v[40:43]\n"
PK"v78,v156,v157\n"
PK"v79,v158,v159\n"
MF"v[40:43],a[68:71],v[200:203],v[40:43]\n"
RD"v[192:195],v244 offset:16384\n"
MF"v[48:51],a[72:75],v[200:203],v[48:51]\n"
MF"v[48:51],a[88:91],v[204:207],v[48:51]\n"
MF"v[56:59],a[92:95],v[204:207],v[56:59]\n"
BL"a[20:23],v235,s[8:11],s57 offen offset:1024 sc1\n"
MF"v[56:59],a[76:79],v[200:203],v[56:59]\n"
WT"lgkmcnt(1)\n"
RD"v[196:199],v245 offset:16384\n"
MF"v[0:3],a[96:99],v[208:211],v[0:3]\n"
MF"v[0:3],a[112:115],v[212:215],v[0:3]\n"
MF"v[8:11],a[116:119],v[212:215],v[8:11]\n"
MF"v[8:11],a[100:103],v[208:211],v[8:11]\n"
RD"v[200:203],v244 offset:18432\n"
MF"v[16:19],a[104:107],v[208:211],v[16:19]\n"
BS"v[104:107],v250,s[8:11],s49 offen offset:1024 sc1\n"
MF"v[16:19],a[120:123],v[212:215],v[16:19]\n"
BL"a[24:27],v235,s[8:11],s58 offen offset:1024 sc1\n"
MF"v[24:27],a[124:127],v[212:215],v[24:27]\n"
RD"v[204:207],v245 offset:18432\n"
MF"v[24:27],a[108:111],v[208:211],v[24:27]\n"
MF"v[32:35],a[96:99],v[216:219],v[32:35]\n"
MF"v[32:35],a[112:115],v[220:223],v[32:35]\n"
MF"v[40:43],a[116:119],v[220:223],v[40:43]\n"
RD"v[208:211],v244 offset:20480\n"
MF"v[40:43],a[100:103],v[216:219],v[40:43]\n"
BL"a[28:31],v235,s[8:11],s59 offen offset:1024 sc1\n"
MF"v[48:51],a[104:107],v[216:219],v[48:51]\n"
MF"v[48:51],a[120:123],v[220:223],v[48:51]\n"
MF"v[56:59],a[124:127],v[220:223],v[56:59]\n"
RD"v[212:215],v245 offset:20480\n"
MF"v[56:59],a[108:111],v[216:219],v[56:59]\n"
WT"lgkmcnt(2)\n"
MF"v[0:3],a[128:131],v[192:195],v[0:3]\n"
BL"a[32:35],v235,s[8:11],s56 offen offset:2048 sc1\n"
MF"v[0:3],a[144:147],v[196:199],v[0:3]\n"
MF"v[8:11],a[148:151],v[196:199],v[8:11]\n"
RD"v[216:219],v244 offset:22528\n"
MF"v[8:11],a[132:135],v[192:195],v[8:11]\n"
MF"v[16:19],a[136:139],v[192:195],v[16:19]\n"
MF"v[16:19],a[152:155],v[196:199],v[16:19]\n"
RD"v[220:223],v245 offset:22528\n"
MF"v[24:27],a[156:159],v[196:199],v[24:27]\n"
BL"a[36:39],v235,s[8:11],s57 offen offset:2048 sc1\n"
MF"v[24:27],a[140:143],v[192:195],v[24:27]\n"
MF"v[32:35],a[128:131],v[200:203],v[32:35]\n"
MF"v[32:35],a[144:147],v[204:207],v[32:35]\n"
RD"v[192:195],v244 offset:40960\n"
MF"v[40:43],a[148:151],v[204:207],v[40:43]\n"
MF"v[40:43],a[132:135],v[200:203],v[40:43]\n"
MF"v[48:51],a[136:139],v[200:203],v[48:51]\n"
BL"a[40:43],v235,s[8:11],s58 offen offset:2048 sc1\n"
MF"v[48:51],a[152:155],v[204:207],v[48:51]\n"
RD"v[196:199],v245 offset:40960\n"
MF"v[56:59],a[156:159],v[204:207],v[56:59]\n"
MF"v[56:59],a[140:143],v[200:203],v[56:59]\n"
WT"lgkmcnt(2)\n"
MF"v[0:3],a[160:163],v[208:211],v[0:3]\n"
"s_sub_u32 s53,s37,65536\n"
MF"v[0:3],a[176:179],v[212:215],v[0:3]\n"
"s_mov_b32 s49,28672\n"
RD"v[200:203],v244 offset:43008\n"
MF"v[8:11],a[180:183],v[212:215],v[8:11]\n"
BL"a[44:47],v235,s[8:11],s59 offen offset:2048 sc1\n"
MF"v[8:11],a[164:167],v[208:211],v[8:11]\n"
BS"v[112:115],v250,s[8:11],s49 offen offset:0 sc1\n"
MF"v[16:19],a[168:171],v[208:211],v[16:19]\n"
"v_subrev_u32 v246,s53,v246\n"
"v_subrev_u32 v247,s53,v247\n"
MF"v[16:19],a[184:187],v[212:215],v[16:19]\n"
RD"v[204:207],v245 offset:43008\n"
"v_subrev_u32 v248,s53,v248\n"
MF"v[24:27],a[188:191],v[212:215],v[24:27]\n"
"v_subrev_u32 v249,s53,v249\n"
"v_subrev_u32 v236,65536,v246\n"
MF"v[24:27],a[172:175],v[208:211],v[24:27]\n"
"v_subrev_u32 v237,65536,v247\n"
"v_subrev_u32 v228,65536,v248\n"
MF"v[32:35],a[160:163],v[216:219],v[32:35]\n"
RD"v[208:211],v244 offset:45056\n"
"v_subrev_u32 v229,65536,v249\n"
MF"v[32:35],a[176:179],v[220:223],v[32:35]\n"
BL"a[48:51],v235,s[8:11],s56 offen offset:3072 sc1\n"
MF"v[40:43],a[180:183],v[220:223],v[40:43]\n"
MF"v[40:43],a[164:167],v[216:219],v[40:43]\n"
MF"v[48:51],a[168:171],v[216:219],v[48:51]\n"
RD"v[212:215],v245 offset:45056\n"
MF"v[48:51],a[184:187],v[220:223],v[48:51]\n"
MF"v[56:59],a[188:191],v[220:223],v[56:59]\n"
BL"a[52:55],v235,s[8:11],s57 offen offset:3072 sc1\n"
MF"v[56:59],a[172:175],v[216:219],v[56:59]\n"
WT"lgkmcnt(1)\n"
MF"v[0:3],a[192:195],v[192:195],v[0:3]\n"
RD"v[216:219],v244 offset:47104\n"
MF"v[0:3],a[208:211],v[196:199],v[0:3]\n"
MF"v[8:11],a[212:215],v[196:199],v[8:11]\n"
MF"v[8:11],a[196:199],v[192:195],v[8:11]\n"
MF"v[16:19],a[200:203],v[192:195],v[16:19]\n"
RD"v[220:223],v245 offset:47104\n"
MF"v[16:19],a[216:219],v[196:199],v[16:19]\n"
BL"a[56:59],v235,s[8:11],s58 offen offset:3072 sc1\n"
MF"v[24:27],a[220:223],v[196:199],v[24:27]\n"
MF"v[24:27],a[204:207],v[192:195],v[24:27]\n"
MF"v[32:35],a[192:195],v[200:203],v[32:35]\n"
SA"s99,s99,4096\n"
MF"v[32:35],a[208:211],v[204:207],v[32:35]\n"
"s_cmp_eq_u32 s99,20480\n"
"s_cselect_b32 s99,0,s99\n"
MF"v[40:43],a[212:215],v[204:207],v[40:43]\n"
BL"a[60:63],v235,s[8:11],s59 offen offset:3072 sc1\n"
MF"v[40:43],a[196:199],v[200:203],v[40:43]\n"
MF"v[48:51],a[200:203],v[200:203],v[48:51]\n"
MF"v[48:51],a[216:219],v[204:207],v[48:51]\n"
WT"lgkmcnt(0)\n"
"s_barrier\n"
MF"v[56:59],a[220:223],v[204:207],v[56:59]\n"
RT"a[128:129],v236 offset:49152\n"
MF"v[56:59],a[204:207],v[200:203],v[56:59]\n"
"v_add_u32 v235,s99,v224\n"
MF"v[0:3],a[224:227],v[208:211],v[0:3]\n"
SA"m0,s38,73728\n"
RT"a[130:131],v236 offset:49664\n"
SA"s48,s60,s61\n"
MF"v[0:3],a[240:243],v[212:215],v[0:3]\n"
BL"a[64:67],v235,s[8:11],s56 offen offset:0 sc1\n"
MF"v[8:11],a[244:247],v[212:215],v[8:11]\n"
RT"a[132:133],v236 offset:57344\n"
"v_add_u32 v242,49152,v246\n"
MF"v[8:11],a[228:231],v[208:211],v[8:11]\n"
BS"v[120:123],v250,s[8:11],s49 offen offset:1024 sc1\n"
MF"v[16:19],a[232:235],v[208:211],v[16:19]\n"
BL"v238,s[20:23],s48 offen offset:0 lds\n"
MF"v[16:19],a[248:251],v[212:215],v[16:19]\n"
RT"a[134:135],v236 offset:57856\n"
"v_add_u32 v243,49152,v247\n"
MF"v[24:27],a[252:255],v[212:215],v[24:27]\n"
RT"a[136:137],v237 offset:49152\n"
"v_add_u32 v244,49152,v248\n"
MF"v[24:27],a[236:239],v[208:211],v[24:27]\n"
BL"a[68:71],v235,s[8:11],s57 offen offset:0 sc1\n"
MF"v[32:35],a[224:227],v[216:219],v[32:35]\n"
RT"a[138:139],v237 offset:49664\n"
"v_add_u32 v245,49152,v249\n"
MF"v[32:35],a[240:243],v[220:223],v[32:35]\n"
MF"v[40:43],a[244:247],v[220:223],v[40:43]\n"
RT"a[140:141],v237 offset:57344\n"
MF"v[40:43],a[228:231],v[216:219],v[40:43]\n"
BL"v239,s[20:23],s48 offen offset:1024 lds\n"
MF"v[48:51],a[232:235],v[216:219],v[48:51]\n"
RT"a[142:143],v237 offset:57856\n"
MF"v[48:51],a[248:251],v[220:223],v[48:51]\n"
BL"a[72:75],v235,s[8:11],s58 offen offset:0 sc1\n"
MF"v[56:59],a[252:255],v[220:223],v[56:59]\n"
RT"a[144:145],v228 offset:49152\n"
MF"v[56:59],a[236:239],v[216:219],v[56:59]\n"
WT"vmcnt(25) lgkmcnt(3)\n"
MF"v[80:83],a[128:131],a[0:3],0\n"
WT"vmcnt(19)\n"
RT"a[146:147],v228 offset:49664\n"
MF"v[80:83],a[132:135],a[16:19],v[80:83]\n"
WT"vmcnt(18)\n"
MF"v[88:91],a[132:135],a[20:23],0\n"
RT"a[148:149],v228 offset:57344\n"
MF"v[88:91],a[128:131],a[4:7],v[88:91]\n"
BL"v240,s[20:23],s48 offen offset:2048 lds\n"
MF"v[96:99],a[128:131],a[8:11],0\n"
WT"vmcnt(17)\n"
BL"a[76:79],v235,s[8:11],s59 offen offset:0 sc1\n"
MF"v[96:99],a[132:135],a[24:27],v[96:99]\n"
WT"vmcnt(17)\n"
RT"a[150:151],v228 offset:57856\n"
RT"a[152:153],v229 offset:49152\n"
MF"v[104:107],a[132:135],a[28:31],0\n"
MU"v0,s55,v0\n"
MU"v1,s55,v1\n"
MF"v[104:107],a[128:131],a[12:15],v[104:107]\n"
RT"a[154:155],v229 offset:49664\n"
MU"v2,s55,v2\n"
MF"v[84:87],a[136:139],a[0:3],0\n"
WT"lgkmcnt(4)\n"
MU"v3,s55,v3\n"
MU"v8,s55,v8\n"
MF"v[84:87],a[140:143],a[16:19],v[84:87]\n"
RT"a[156:157],v229 offset:57344\n"
MU"v9,s55,v9\n"
MF"v[92:95],a[140:143],a[20:23],0\n"
SA"s61,s61,524288\n"
BL"a[80:83],v235,s[8:11],s56 offen offset:1024 sc1\n"
MF"v[92:95],a[136:139],a[4:7],v[92:95]\n"
BL"v241,s[20:23],s48 offen offset:3072 lds\n"
MF"v[100:103],a[136:139],a[8:11],0\n"
RT"a[158:159],v229 offset:57856\n"
MU"v10,s55,v10\n"
MF"v[100:103],a[140:143],a[24:27],v[100:103]\n"
RT"a[160:161],v236 offset:53248\n"
MU"v11,s55,v11\n"
MF"v[108:111],a[140:143],a[28:31],0\n"
PK"v0,v0,v1\n"
PK"v1,v2,v3\n"
MF"v[108:111],a[136:139],a[12:15],v[108:111]\n"
RT"a[162:163],v236 offset:53760\n"
PK"v2,v8,v9\n"
MF"v[112:115],a[144:147],a[0:3],0\n"
WT"lgkmcnt(3)\n"
BL"a[84:87],v235,s[8:11],s57 offen offset:1024 sc1\n"
MF"v[112:115],a[148:151],a[16:19],v[112:115]\n"
RT"a[164:165],v236 offset:61440\n"
PK"v3,v10,v11\n"
MF"v[120:123],a[148:151],a[20:23],0\n"
MU"v16,s55,v16\n"
MU"v17,s55,v17\n"
MF"v[120:123],a[144:147],a[4:7],v[120:123]\n"
RT"a[166:167],v236 offset:61952\n"
MU"v18,s55,v18\n"
MF"v[128:131],a[144:147],a[8:11],0\n"
MU"v19,s55,v19\n"
MU"v24,s55,v24\n"
MF"v[128:131],a[148:151],a[24:27],v[128:131]\n"
RT"a[168:169],v237 offset:53248\n"
MU"v25,s55,v25\n"
MF"v[136:139],a[148:151],a[28:31],0\n"
MU"v26,s55,v26\n"
MU"v27,s55,v27\n"
MF"v[136:139],a[144:147],a[12:15],v[136:139]\n"
RT"a[170:171],v237 offset:53760\n"
PK"v16,v16,v17\n"
MF"v[116:119],a[152:155],a[0:3],0\n"
WT"lgkmcnt(3)\n"
BL"a[88:91],v235,s[8:11],s58 offen offset:1024 sc1\n"
MF"v[116:119],a[156:159],a[16:19],v[116:119]\n"
"s_mov_b32 s49,262144\n"
RT"a[172:173],v237 offset:61440\n"
PK"v17,v18,v19\n"
MF"v[124:127],a[156:159],a[20:23],0\n"
RT"a[174:175],v237 offset:61952\n"
PK"v18,v24,v25\n"
MF"v[124:127],a[152:155],a[4:7],v[124:127]\n"
BS"v[4:7],v250,s[8:11],s49 offen offset:0 sc1\n"
MF"v[132:135],a[152:155],a[8:11],0\n"
RT"a[176:177],v228 offset:53248\n"
PK"v19,v26,v27\n"
MF"v[132:135],a[156:159],a[24:27],v[132:135]\n"
MU"v32,s55,v32\n"
MU"v33,s55,v33\n"
MF"v[140:143],a[156:159],a[28:31],0\n"
RT"a[178:179],v228 offset:53760\n"
MU"v34,s55,v34\n"
MF"v[140:143],a[152:155],a[12:15],v[140:143]\n"
BL"a[92:95],v235,s[8:11],s59 offen offset:1024 sc1\n"
MF"v[144:147],a[160:163],a[0:3],0\n"
WT"lgkmcnt(3)\n"
RT"a[180:181],v228 offset:61440\n"
MU"v35,s55,v35\n"
MF"v[144:147],a[164:167],a[16:19],v[144:147]\n"
MU"v40,s55,v40\n"
MU"v41,s55,v41\n"
MF"v[152:155],a[164:167],a[20:23],0\n"
SA"m0,s38,122880\n"
RT"a[182:183],v228 offset:61952\n"
MU"v42,s55,v42\n"
MF"v[152:155],a[160:163],a[4:7],v[152:155]\n"
SA"s48,s60,s61\n"
MU"v43,s55,v43\n"
PK"v32,v32,v33\n"
MF"v[160:163],a[160:163],a[8:11],0\n"
"s_mov_b32 s88,4096\n"
RT"a[184:185],v229 offset:53248\n"
PK"v33,v34,v35\n"
MF"v[160:163],a[164:167],a[24:27],v[160:163]\n"
BL"v238,s[20:23],s48 offen offset:0 lds\n"
MF"v[168:171],a[164:167],a[28:31],0\n"
BL"a[96:99],v235,s[8:11],s56 offen offset:2048 sc1\n"
MF"v[168:171],a[160:163],a[12:15],v[168:171]\n"
RT"a[186:187],v229 offset:53760\n"
PK"v34,v40,v41\n"
MF"v[148:151],a[168:171],a[0:3],0\n"
WT"lgkmcnt(3)\n"
RT"a[188:189],v229 offset:61440\n"
PK"v35,v42,v43\n"
MF"v[148:151],a[172:175],a[16:19],v[148:151]\n"
MU"v48,s55,v48\n"
MU"v49,s55,v49\n"
MF"v[156:159],a[172:175],a[20:23],0\n"
RT"a[190:191],v229 offset:61952\n"
MU"v50,s55,v50\n"
MF"v[156:159],a[168:171],a[4:7],v[156:159]\n"
RT"a[128:129],v236 offset:32768\n"
MU"v51,s55,v51\n"
MF"v[164:167],a[168:171],a[8:11],0\n"
BL"v239,s[20:23],s48 offen offset:1024 lds\n"
MF"v[164:167],a[172:175],a[24:27],v[164:167]\n"
BL"a[100:103],v235,s[8:11],s57 offen offset:2048 sc1\n"
MF"v[172:175],a[172:175],a[28:31],0\n"
RT"a[130:131],v236 offset:33280\n"
MU"v56,s55,v56\n"
MF"v[172:175],a[168:171],a[12:15],v[172:175]\n"
RT"a[132:133],v236 offset:40960\n"
MU"v57,s55,v57\n"
MF"v[176:179],a[176:179],a[0:3],0\n"
WT"lgkmcnt(4)\n"
MU"v58,s55,v58\n"
MU"v59,s55,v59\n"
MF"v[176:179],a[180:183],a[16:19],v[176:179]\n"
RT"a[134:135],v236 offset:41472\n"
PK"v48,v48,v49\n"
MF"v[184:187],a[180:183],a[20:23],0\n"
PK"v49,v50,v51\n"
PK"v50,v56,v57\n"
MF"v[184:187],a[176:179],a[4:7],v[184:187]\n"
RT"a[136:137],v237 offset:32768\n"
PK"v51,v58,v59\n"
MF"v[192:195],a[176:179],a[8:11],0\n"
BL"a[104:107],v235,s[8:11],s58 offen offset:2048 sc1\n"
MF"v[192:195],a[180:183],a[24:27],v[192:195]\n"
BL"v240,s[20:23],s48 offen offset:2048 lds\n"
MF"v[200:203],a[180:183],a[28:31],0\n"
RT"a[138:139],v237 offset:33280\n"
MF"v[200:203],a[176:179],a[12:15],v[200:203]\n"
RT"a[140:141],v237 offset:40960\n"
MF"v[180:183],a[184:187],a[0:3],0\n"
WT"lgkmcnt(3)\n"
MF"v[180:183],a[188:191],a[16:19],v[180:183]\n"
RT"a[142:143],v237 offset:41472\n"
MF"v[188:191],a[188:191],a[20:23],0\n"
BL"a[108:111],v235,s[8:11],s59 offen offset:2048 sc1\n"
MF"v[188:191],a[184:187],a[4:7],v[188:191]\n"
RT"a[144:145],v228 offset:32768\n"
MF"v[196:199],a[184:187],a[8:11],0\n"
SA"s61,s61,524288\n"
BS"v[12:15],v250,s[8:11],s49 offen offset:1024 sc1\n"
MF"v[196:199],a[188:191],a[24:27],v[196:199]\n"
"s_cmp_eq_u32 s61,5242880\n"
BL"v241,s[20:23],s48 offen offset:3072 lds\n"
MF"v[204:207],a[188:191],a[28:31],0\n"
"s_cselect_b32 s61,0,s61\n"
RT"a[146:147],v228 offset:33280\n"
MF"v[204:207],a[184:187],a[12:15],v[204:207]\n"
WT"vmcnt(31)\n"
RT"a[148:149],v228 offset:40960\n"
MF"v[80:83],a[128:131],a[32:35],v[80:83]\n"
WT"vmcnt(26)\n"
MF"v[80:83],a[132:135],a[48:51],v[80:83]\n"
WT"vmcnt(25)\n"
RT"a[150:151],v228 offset:41472\n"
MF"v[88:91],a[132:135],a[52:55],v[88:91]\n"
BL"a[112:115],v235,s[8:11],s56 offen offset:3072 sc1\n"
MF"v[88:91],a[128:131],a[36:39],v[88:91]\n"
RT"a[152:153],v229 offset:32768\n"
MF"v[96:99],a[128:131],a[40:43],v[96:99]\n"
WT"vmcnt(25)\n"
RT"a[154:155],v229 offset:33280\n"
MF"v[96:99],a[132:135],a[56:59],v[96:99]\n"
WT"vmcnt(24)\n"
MF"v[104:107],a[132:135],a[60:63],v[104:107]\n"
RT"a[156:157],v229 offset:40960\n"
MF"v[104:107],a[128:131],a[44:47],v[104:107]\n"
WT"lgkmcnt(4)\n"
MF"v[84:87],a[136:139],a[32:35],v[84:87]\n"
RT"a[158:159],v229 offset:41472\n"
MF"v[84:87],a[140:143],a[48:51],v[84:87]\n"
BL"a[116:119],v235,s[8:11],s57 offen offset:3072 sc1\n"
MF"v[92:95],a[140:143],a[52:55],v[92:95]\n"
RT"a[160:161],v236 offset:36864\n"
MF"v[92:95],a[136:139],a[36:39],v[92:95]\n"
MF"v[100:103],a[136:139],a[40:43],v[100:103]\n"
RT"a[162:163],v236 offset:37376\n"
MF"v[100:103],a[140:143],a[56:59],v[100:103]\n"
MF"v[108:111],a[140:143],a[60:63],v[108:111]\n"
RT"a[164:165],v236 offset:45056\n"
MF"v[108:111],a[136:139],a[44:47],v[108:111]\n"
BL"a[120:123],v235,s[8:11],s58 offen offset:3072 sc1\n"
MF"v[112:115],a[144:147],a[32:35],v[112:115]\n"
WT"lgkmcnt(3)\n"
RT"a[166:167],v236 offset:45568\n"
MF"v[112:115],a[148:151],a[48:51],v[112:115]\n"
MF"v[120:123],a[148:151],a[52:55],v[120:123]\n"
RT"a[168:169],v237 offset:36864\n"
MF"v[120:123],a[144:147],a[36:39],v[120:123]\n"
MF"v[128:131],a[144:147],a[40:43],v[128:131]\n"
RT"a[170:171],v237 offset:37376\n"
MF"v[128:131],a[148:151],a[56:59],v[128:131]\n"
SA"s99,s99,4096\n"
RT"a[172:173],v237 offset:45056\n"
MF"v[136:139],a[148:151],a[60:63],v[136:139]\n"
"s_cmp_eq_u32 s99,20480\n"
BL"a[124:127],v235,s[8:11],s59 offen offset:3072 sc1\n"
MF"v[136:139],a[144:147],a[44:47],v[136:139]\n"
"s_cselect_b32 s99,0,s99\n"
RT"a[174:175],v237 offset:45568\n"
MF"v[116:119],a[152:155],a[32:35],v[116:119]\n"
MF"v[116:119],a[156:159],a[48:51],v[116:119]\n"
RT"a[176:177],v228 offset:36864\n"
MF"v[124:127],a[156:159],a[52:55],v[124:127]\n"
MF"v[124:127],a[152:155],a[36:39],v[124:127]\n"
RT"a[178:179],v228 offset:37376\n"
MF"v[132:135],a[152:155],a[40:43],v[132:135]\n"
"s_mov_b32 s49,266240\n"
"v_add_u32 v235,s99,v224\n"
MF"v[132:135],a[156:159],a[56:59],v[132:135]\n"
RT"a[180:181],v228 offset:45056\n"
MF"v[140:143],a[156:159],a[60:63],v[140:143]\n"
BL"a[0:3],v235,s[8:11],s56 offen offset:0 sc1\n"
MF"v[140:143],a[152:155],a[44:47],v[140:143]\n"
WT"lgkmcnt(3)\n"
BS"v[20:23],v250,s[8:11],s49 offen offset:0 sc1\n"
MF"v[144:147],a[160:163],a[32:35],v[144:147]\n"
RT"a[182:183],v228 offset:45568\n"
MF"v[144:147],a[164:167],a[48:51],v[144:147]\n"
RT"a[184:185],v229 offset:36864\n"
MF"v[152:155],a[164:167],a[52:55],v[152:155]\n"
MF"v[152:155],a[160:163],a[36:39],v[152:155]\n"
RT"a[186:187],v229 offset:37376\n"
MF"v[160:163],a[160:163],a[40:43],v[160:163]\n"
BL"a[4:7],v235,s[8:11],s57 offen offset:0 sc1\n"
MF"v[160:163],a[164:167],a[56:59],v[160:163]\n"
RT"a[188:189],v229 offset:45056\n"
MF"v[168:171],a[164:167],a[60:63],v[168:171]\n"
MF"v[168:171],a[160:163],a[44:47],v[168:171]\n"
RT"a[190:191],v229 offset:45568\n"
MF"v[148:151],a[168:171],a[32:35],v[148:151]\n"
"s_lshl_b32 s50,s35,3\n"
SA"s50,s50,s34\n"
"s_lshl_b32 s50,s50,3\n"
MF"v[148:151],a[172:175],a[48:51],v[148:151]\n"
SA"s50,s50,2304\n"
SA"s51,s50,s45\n"
MF"v[156:159],a[172:175],a[52:55],v[156:159]\n"
MF"v[156:159],a[168:171],a[36:39],v[156:159]\n"
BL"a[8:11],v235,s[8:11],s58 offen offset:0 sc1\n"
MF"v[164:167],a[168:171],a[40:43],v[164:167]\n"
MF"v[164:167],a[172:175],a[56:59],v[164:167]\n"
WT"vmcnt(29) lgkmcnt(0)\n"
"s_barrier\n"
MF"v[172:175],a[172:175],a[60:63],v[172:175]\n"
RT"a[128:129],v236 offset:0\n"
MF"v[172:175],a[168:171],a[44:47],v[172:175]\n"
"buffer_store_dwordx2 v[232:233],v230,s[8:11],s51 offen sc1\n"
MF"v[176:179],a[176:179],a[32:35],v[176:179]\n"
RT"a[130:131],v236 offset:512\n"
MF"v[176:179],a[180:183],a[48:51],v[176:179]\n"
MF"v[184:187],a[180:183],a[52:55],v[184:187]\n"
SA"m0,s38,81920\n"
RT"a[132:133],v236 offset:8192\n"
SA"s48,s60,s61\n"
MF"v[184:187],a[176:179],a[36:39],v[184:187]\n"
BL"a[12:15],v235,s[8:11],s59 offen offset:0 sc1\n"
MF"v[192:195],a[176:179],a[40:43],v[192:195]\n"
RT"a[134:135],v236 offset:8704\n"
MF"v[192:195],a[180:183],a[56:59],v[192:195]\n"
BL"v238,s[20:23],s48 offen offset:0 lds\n"
MF"v[200:203],a[180:183],a[60:63],v[200:203]\n"
RT"a[136:137],v237 offset:0\n"
MF"v[200:203],a[176:179],a[44:47],v[200:203]\n"
MF"v[180:183],a[184:187],a[32:35],v[180:183]\n"
RT"a[138:139],v237 offset:512\n"
MF"v[180:183],a[188:191],a[48:51],v[180:183]\n"
BL"a[16:19],v235,s[8:11],s56 offen offset:1024 sc1\n"
MF"v[188:191],a[188:191],a[52:55],v[188:191]\n"
RT"a[140:141],v237 offset:8192\n"
MF"v[188:191],a[184:187],a[36:39],v[188:191]\n"
MF"v[196:199],a[184:187],a[40:43],v[196:199]\n"
RT"a[142:143],v237 offset:8704\n"
MF"v[196:199],a[188:191],a[56:59],v[196:199]\n"
BL"v239,s[20:23],s48 offen offset:1024 lds\n"
MF"v[204:207],a[188:191],a[60:63],v[204:207]\n"
SA"s49,s88,262144\n"
RT"a[144:145],v228 offset:0\n"
MF"v[204:207],a[184:187],a[44:47],v[204:207]\n"
"s_branch LQ_%=\n"
"LV_%=:\n"
WT"vmcnt(17)\n"
"v_lshlrev_b32 v148,16,v128\n"
"v_and_b32 v149,0xffff0000,v128\n"
"v_lshlrev_b32 v150,16,v129\n"
"v_and_b32 v151,0xffff0000,v129\n"
WT"vmcnt(16)\n"
"v_lshlrev_b32 v152,16,v130\n"
"v_and_b32 v153,0xffff0000,v130\n"
"v_lshlrev_b32 v154,16,v131\n"
"v_and_b32 v155,0xffff0000,v131\n"
WT"vmcnt(15)\n"
"v_lshlrev_b32 v156,16,v132\n"
"v_and_b32 v157,0xffff0000,v132\n"
"v_lshlrev_b32 v158,16,v133\n"
"v_and_b32 v159,0xffff0000,v133\n"
WT"vmcnt(14)\n"
"v_lshlrev_b32 v160,16,v134\n"
"v_and_b32 v161,0xffff0000,v134\n"
"v_lshlrev_b32 v162,16,v135\n"
"v_and_b32 v163,0xffff0000,v135\n"
WT"vmcnt(13)\n"
"v_lshlrev_b32 v164,16,v136\n"
"v_and_b32 v165,0xffff0000,v136\n"
"v_lshlrev_b32 v166,16,v137\n"
"v_and_b32 v167,0xffff0000,v137\n"
"v_add_f32 v188,v148,v149\n"
"v_add_f32 v189,v150,v151\n"
"v_add_f32 v188,v188,v152\n"
"v_add_f32 v189,v189,v153\n"
"v_add_f32 v188,v188,v154\n"
"v_add_f32 v189,v189,v155\n"
"v_add_f32 v188,v188,v156\n"
"v_add_f32 v189,v189,v157\n"
"v_add_f32 v188,v188,v158\n"
"v_add_f32 v189,v189,v159\n"
"v_add_f32 v188,v188,v160\n"
"v_add_f32 v189,v189,v161\n"
"v_add_f32 v188,v188,v162\n"
"v_add_f32 v189,v189,v163\n"
"v_add_f32 v188,v188,v164\n"
"v_add_f32 v189,v189,v165\n"
"v_add_f32 v188,v188,v166\n"
"v_add_f32 v189,v189,v167\n"
"v_add_f32 v188,v188,v189\n"
"s_nop 1\n"
"v_add_f32_dpp v188,v188,v188 quad_perm:[1,0,3,2] row_mask:0xf bank_mask:0xf\n"
WT"vmcnt(12)\n"
"v_lshlrev_b32 v168,16,v138\n"
"v_and_b32 v169,0xffff0000,v138\n"
"v_add_f32_dpp v188,v188,v188 quad_perm:[2,3,0,1] row_mask:0xf bank_mask:0xf\n"
"v_lshlrev_b32 v170,16,v139\n"
"v_and_b32 v171,0xffff0000,v139\n"
"v_add_f32_dpp v188,v188,v188 row_half_mirror row_mask:0xf bank_mask:0xf\n"
WT"vmcnt(11)\n"
"v_lshlrev_b32 v172,16,v140\n"
SA"m0,s36,0\n"
"s_bfe_u32 s48,s46,0x30000\n"
"s_lshl_b32 s48,s48,7\n"
BL"v234,s[12:15],s48 offen offset:0 lds\n"
BL"v235,s[12:15],s48 offen offset:1024 lds\n"
BL"v236,s[12:15],s48 offen offset:2048 lds\n"
BL"v237,s[12:15],s48 offen offset:3072 lds\n"
"v_and_b32 v173,0xffff0000,v140\n"
"v_add_f32_dpp v188,v188,v188 row_mirror row_mask:0xf bank_mask:0xf\n"
"v_lshlrev_b32 v174,16,v141\n"
"v_and_b32 v175,0xffff0000,v141\n"
"v_mov_b32 v189,v188\n"
WT"vmcnt(14)\n"
"v_lshlrev_b32 v176,16,v142\n"
"v_and_b32 v177,0xffff0000,v142\n"
"v_permlane16_swap_b32 v189,v188\n"
"v_lshlrev_b32 v178,16,v143\n"
"v_and_b32 v179,0xffff0000,v143\n"
"v_add_f32 v188,v188,v189\n"
WT"vmcnt(13)\n"
"v_lshlrev_b32 v180,16,v144\n"
"v_and_b32 v181,0xffff0000,v144\n"
MU"v191,0x3acccccd,v188\n"
"v_lshlrev_b32 v182,16,v145\n"
"v_and_b32 v183,0xffff0000,v145\n"
PK"v128,v191,v191\n"
WT"vmcnt(12)\n"
"v_lshlrev_b32 v184,16,v146\n"
"v_and_b32 v185,0xffff0000,v146\n"
"v_lshlrev_b32 v128,16,v128\n"
"v_lshlrev_b32 v186,16,v147\n"
"v_and_b32 v187,0xffff0000,v147\n"
"v_sub_f32 v148,v148,v128\n"
"v_sub_f32 v149,v149,v128\n"
"v_sub_f32 v150,v150,v128\n"
"v_sub_f32 v151,v151,v128\n"
"v_sub_f32 v152,v152,v128\n"
"v_sub_f32 v153,v153,v128\n"
"v_sub_f32 v154,v154,v128\n"
"v_sub_f32 v155,v155,v128\n"
"v_sub_f32 v156,v156,v128\n"
"v_sub_f32 v157,v157,v128\n"
"v_sub_f32 v158,v158,v128\n"
"v_sub_f32 v159,v159,v128\n"
"v_sub_f32 v160,v160,v128\n"
"v_sub_f32 v161,v161,v128\n"
"v_sub_f32 v162,v162,v128\n"
"v_sub_f32 v163,v163,v128\n"
"v_sub_f32 v164,v164,v128\n"
"v_sub_f32 v165,v165,v128\n"
"v_sub_f32 v166,v166,v128\n"
"v_sub_f32 v167,v167,v128\n"
MU"v188,v148,v148\n"
MU"v189,v149,v149\n"
"v_fmac_f32 v188,v150,v150\n"
"v_fmac_f32 v189,v151,v151\n"
"v_fmac_f32 v188,v152,v152\n"
SA"m0,s37,8192\n"
"s_bfe_u32 s48,s46,0x30004\n"
"s_lshl_b32 s48,s48,19\n"
BL"v238,s[16:19],s48 offen offset:0 lds\n"
BL"v239,s[16:19],s48 offen offset:1024 lds\n"
BL"v240,s[16:19],s48 offen offset:2048 lds\n"
BL"v241,s[16:19],s48 offen offset:3072 lds\n"
"v_fmac_f32 v189,v153,v153\n"
"v_fmac_f32 v188,v154,v154\n"
"v_fmac_f32 v189,v155,v155\n"
"v_fmac_f32 v188,v156,v156\n"
"v_fmac_f32 v189,v157,v157\n"
"v_fmac_f32 v188,v158,v158\n"
"v_fmac_f32 v189,v159,v159\n"
"v_fmac_f32 v188,v160,v160\n"
"v_fmac_f32 v189,v161,v161\n"
"v_fmac_f32 v188,v162,v162\n"
"v_fmac_f32 v189,v163,v163\n"
"v_fmac_f32 v188,v164,v164\n"
"v_fmac_f32 v189,v165,v165\n"
"v_fmac_f32 v188,v166,v166\n"
"v_fmac_f32 v189,v167,v167\n"
"v_add_f32 v188,v188,v189\n"
PK"v149,v148,v149\n"
"v_lshlrev_b32 v148,16,v149\n"
"v_add_f32_dpp v188,v188,v188 quad_perm:[1,0,3,2] row_mask:0xf bank_mask:0xf\n"
"v_and_b32 v149,0xffff0000,v149\n"
PK"v151,v150,v151\n"
"v_add_f32_dpp v188,v188,v188 quad_perm:[2,3,0,1] row_mask:0xf bank_mask:0xf\n"
"v_lshlrev_b32 v150,16,v151\n"
"v_and_b32 v151,0xffff0000,v151\n"
"v_add_f32_dpp v188,v188,v188 row_half_mirror row_mask:0xf bank_mask:0xf\n"
PK"v153,v152,v153\n"
"v_lshlrev_b32 v152,16,v153\n"
"v_add_f32_dpp v188,v188,v188 row_mirror row_mask:0xf bank_mask:0xf\n"
"v_and_b32 v153,0xffff0000,v153\n"
PK"v155,v154,v155\n"
"v_mov_b32 v189,v188\n"
"v_lshlrev_b32 v154,16,v155\n"
"v_and_b32 v155,0xffff0000,v155\n"
"v_permlane16_swap_b32 v189,v188\n"
PK"v157,v156,v157\n"
"v_lshlrev_b32 v156,16,v157\n"
"v_add_f32 v188,v188,v189\n"
"v_and_b32 v157,0xffff0000,v157\n"
PK"v159,v158,v159\n"
"v_sub_f32 v191,v191,v128\n"
"v_lshlrev_b32 v158,16,v159\n"
"v_and_b32 v159,0xffff0000,v159\n"
MU"v188,0x3acccccd,v188\n"
PK"v161,v160,v161\n"
"v_lshlrev_b32 v160,16,v161\n"
"v_fma_f32 v188,-v191,v191,v188\n"
"v_and_b32 v161,0xffff0000,v161\n"
PK"v163,v162,v163\n"
SA"m0,s37,12288\n"
"s_bfe_u32 s48,s46,0x30004\n"
"s_lshl_b32 s48,s48,19\n"
"s_bitset1_b32 s48,18\n"
BL"v238,s[16:19],s48 offen offset:0 lds\n"
BL"v239,s[16:19],s48 offen offset:1024 lds\n"
BL"v240,s[16:19],s48 offen offset:2048 lds\n"
BL"v241,s[16:19],s48 offen offset:3072 lds\n"
"v_max_f32 v188,0,v188\n"
"v_lshlrev_b32 v162,16,v163\n"
"v_and_b32 v163,0xffff0000,v163\n"
PK"v188,v188,v188\n"
PK"v165,v164,v165\n"
"v_lshlrev_b32 v164,16,v165\n"
"v_lshlrev_b32 v188,16,v188\n"
"v_and_b32 v165,0xffff0000,v165\n"
PK"v167,v166,v167\n"
"v_add_f32 v188,0x3727c5ac,v188\n"
"v_lshlrev_b32 v166,16,v167\n"
"v_and_b32 v167,0xffff0000,v167\n"
PK"v188,v188,v188\n"
"v_bfe_u32 v130,v224,5,4\n"
"v_mul_u32_u24 v130,144,v130\n"
"v_lshlrev_b32 v188,16,v188\n"
"v_bfe_u32 v131,v224,4,1\n"
"v_lshl_add_u32 v130,v131,3,v130\n"
"v_sqrt_f32 v189,v188\n"
"v_lshrrev_b32 v131,9,v224\n"
"v_lshl_add_u32 v130,v131,4,v130\n"
"v_rcp_f32 v190,v189\n"
"s_lshl_b32 s52,s30,5\n"
"v_add_u32 v130,s52,v130\n"
"v_fma_f32 v188,-v189,v189,v188\n"
MU"v190,0.5,v190\n"
"v_fma_f32 v189,v188,v190,v189\n"
PK"v189,v189,v189\n"
"v_lshlrev_b32 v189,16,v189\n"
"v_rcp_f32 v129,v189\n"
"s_nop 0\n"
MU"v148,v148,v129\n"
MU"v149,v149,v129\n"
PK"v138,v148,v149\n"
MU"v150,v150,v129\n"
MU"v151,v151,v129\n"
PK"v139,v150,v151\n"
"ds_write_b64 v130,v[138:139] offset:49152\n"
MU"v152,v152,v129\n"
MU"v153,v153,v129\n"
PK"v140,v152,v153\n"
MU"v154,v154,v129\n"
MU"v155,v155,v129\n"
PK"v141,v154,v155\n"
"ds_write_b64 v130,v[140:141] offset:51456\n"
MU"v156,v156,v129\n"
MU"v157,v157,v129\n"
PK"v142,v156,v157\n"
SA"m0,s36,16384\n"
"s_bfe_u32 s48,s46,0x30004\n"
"s_lshl_b32 s48,s48,7\n"
BL"v234,s[12:15],s48 offen offset:0 lds\n"
BL"v235,s[12:15],s48 offen offset:1024 lds\n"
BL"v236,s[12:15],s48 offen offset:2048 lds\n"
BL"v237,s[12:15],s48 offen offset:3072 lds\n"
MU"v158,v158,v129\n"
MU"v159,v159,v129\n"
PK"v143,v158,v159\n"
"ds_write_b64 v130,v[142:143] offset:53760\n"
MU"v160,v160,v129\n"
MU"v161,v161,v129\n"
PK"v144,v160,v161\n"
MU"v162,v162,v129\n"
MU"v163,v163,v129\n"
PK"v145,v162,v163\n"
"ds_write_b64 v130,v[144:145] offset:56064\n"
MU"v164,v164,v129\n"
MU"v165,v165,v129\n"
PK"v146,v164,v165\n"
MU"v166,v166,v129\n"
MU"v167,v167,v129\n"
PK"v147,v166,v167\n"
"ds_write_b64 v130,v[146:147] offset:58368\n"
"v_lshlrev_b32 v148,16,v138\n"
"v_and_b32 v149,0xffff0000,v138\n"
MU"v188,v148,v168\n"
MU"v189,v149,v169\n"
"v_lshlrev_b32 v150,16,v139\n"
"v_and_b32 v151,0xffff0000,v139\n"
"v_fmac_f32 v188,v150,v170\n"
"v_fmac_f32 v189,v151,v171\n"
"v_lshlrev_b32 v152,16,v140\n"
"v_and_b32 v153,0xffff0000,v140\n"
"v_fmac_f32 v188,v152,v172\n"
"v_fmac_f32 v189,v153,v173\n"
"v_lshlrev_b32 v154,16,v141\n"
"v_and_b32 v155,0xffff0000,v141\n"
"v_fmac_f32 v188,v154,v174\n"
"v_fmac_f32 v189,v155,v175\n"
"v_lshlrev_b32 v156,16,v142\n"
"v_and_b32 v157,0xffff0000,v142\n"
"v_fmac_f32 v188,v156,v176\n"
"v_fmac_f32 v189,v157,v177\n"
"v_lshlrev_b32 v158,16,v143\n"
"v_and_b32 v159,0xffff0000,v143\n"
"v_fmac_f32 v188,v158,v178\n"
"v_fmac_f32 v189,v159,v179\n"
"v_lshlrev_b32 v160,16,v144\n"
"v_and_b32 v161,0xffff0000,v144\n"
"v_fmac_f32 v188,v160,v180\n"
"v_fmac_f32 v189,v161,v181\n"
"v_lshlrev_b32 v162,16,v145\n"
"v_and_b32 v163,0xffff0000,v145\n"
WT"lgkmcnt(0)\n"
SA"s28,s28,0x7f4a7c15\n"
"s_addc_u32 s29,s29,0x9e3779b9\n"
"v_mov_b32 v232,s28\n"
"v_mov_b32 v233,s29\n"
WT"vmcnt(20)\n"
RT"a[0:1],v246 offset:0\n"
RT"a[2:3],v246 offset:512\n"
RT"a[4:5],v247 offset:0\n"
RT"a[6:7],v247 offset:512\n"
RT"a[8:9],v248 offset:0\n"
RT"a[10:11],v248 offset:512\n"
RT"a[12:13],v249 offset:0\n"
RT"a[14:15],v249 offset:512\n"
WT"vmcnt(16)\n"
RT"a[16:17],v246 offset:4096\n"
RT"a[18:19],v246 offset:4608\n"
RT"a[20:21],v247 offset:4096\n"
RT"a[22:23],v247 offset:4608\n"
RT"a[24:25],v248 offset:4096\n"
RT"a[26:27],v248 offset:4608\n"
RT"a[28:29],v249 offset:4096\n"
RT"a[30:31],v249 offset:4608\n"
WT"vmcnt(12) lgkmcnt(15)\n"
"s_barrier\n"
RD"v[192:195],v242 offset:0\n"
RD"v[196:199],v243 offset:0\n"
RD"v[200:203],v242 offset:2048\n"
RD"v[204:207],v243 offset:2048\n"
RD"v[208:211],v242 offset:4096\n"
"v_lshrrev_b32 v131,7,v224\n"
WT"lgkmcnt(4)\n"
MF"v[0:3],v[192:195],a[0:3],0\n"
WT"lgkmcnt(3)\n"
RD"v[212:215],v243 offset:4096\n"
"v_lshl_add_u32 v131,v131,4,v224\n"
MF"v[0:3],v[196:199],a[16:19],v[0:3]\n"
SA"m0,s37,16384\n"
"s_bfe_u32 s48,s46,0x30008\n"
"s_lshl_b32 s48,s48,19\n"
MF"v[8:11],v[196:199],a[20:23],0\n"
"s_mul_i32 s52,s30,1152\n"
BL"v238,s[16:19],s48 offen offset:0 lds\n"
MF"v[8:11],v[192:195],a[4:7],v[8:11]\n"
"v_add_u32 v128,s52,v131\n"
"s_and_b32 s52,s30,1\n"
MF"v[16:19],v[192:195],a[8:11],0\n"
"s_mul_i32 s51,s52,1152\n"
RD"v[216:219],v242 offset:6144\n"
"v_add_u32 v131,s51,v131\n"
MF"v[16:19],v[196:199],a[24:27],v[16:19]\n"
"s_lshl_b32 s52,s52,10\n"
"v_fmac_f32 v188,v162,v182\n"
"v_fmac_f32 v189,v163,v183\n"
MF"v[24:27],v[196:199],a[28:31],0\n"
BL"v239,s[16:19],s48 offen offset:1024 lds\n"
MF"v[24:27],v[192:195],a[12:15],v[24:27]\n"
WT"lgkmcnt(1)\n"
RD"v[220:223],v243 offset:6144\n"
RD"v[132:135],v128 offset:49152\n"
MF"v[4:7],v[200:203],a[0:3],0\n"
WT"vmcnt(10)\n"
RT"a[32:33],v246 offset:8192\n"
RD"a[240:243],v128 offset:53760\n"
MF"v[4:7],v[204:207],a[16:19],v[4:7]\n"
"s_lshr_b32 s50,s31,1\n"
RD"a[244:247],v131 offset:58368\n"
"v_lshlrev_b32 v164,16,v146\n"
MF"v[12:15],v[204:207],a[20:23],0\n"
"s_mul_i32 s50,s50,20480\n"
"v_and_b32 v129,0x70,v224\n"
"v_and_b32 v165,0xffff0000,v146\n"
MF"v[12:15],v[200:203],a[4:7],v[12:15]\n"
"s_and_b32 s51,s31,1\n"
RD"v[192:195],v242 offset:8192\n"
RT"a[34:35],v246 offset:8704\n"
MF"v[20:23],v[200:203],a[8:11],0\n"
"s_lshl_b32 s51,s51,7\n"
BL"v240,s[16:19],s48 offen offset:2048 lds\n"
MF"v[20:23],v[204:207],a[24:27],v[20:23]\n"
SA"s50,s50,s51\n"
"v_and_b32 v130,0x380,v224\n"
"v_fmac_f32 v188,v164,v184\n"
MF"v[28:31],v[204:207],a[28:31],0\n"
SA"s50,s50,65536\n"
RT"a[36:37],v247 offset:8192\n"
"v_lshl_add_u32 v129,v130,1,v129\n"
MF"v[28:31],v[200:203],a[12:15],v[28:31]\n"
"s_lshl_b32 s52,s52,1\n"
RD"v[196:199],v243 offset:8192\n"
"v_fmac_f32 v189,v165,v185\n"
MF"v[32:35],v[208:211],a[0:3],0\n"
SA"s52,s52,s50\n"
BL"v241,s[16:19],s48 offen offset:3072 lds\n"
MF"v[32:35],v[212:215],a[16:19],v[32:35]\n"
SA"s52,s52,16384\n"
RT"a[38:39],v247 offset:8704\n"
"v_lshlrev_b32 v166,16,v147\n"
MF"v[40:43],v[212:215],a[20:23],0\n"
SA"m0,s37,20480\n"
RD"v[200:203],v242 offset:10240\n"
"v_and_b32 v167,0xffff0000,v147\n"
MF"v[40:43],v[208:211],a[4:7],v[40:43]\n"
"s_bfe_u32 s48,s46,0x30008\n"
"v_fmac_f32 v188,v166,v186\n"
"v_fmac_f32 v189,v167,v187\n"
MF"v[48:51],v[208:211],a[8:11],0\n"
"s_lshl_b32 s48,s48,19\n"
RT"a[40:41],v248 offset:8192\n"
"v_add_f32 v188,v188,v189\n"
MF"v[48:51],v[212:215],a[24:27],v[48:51]\n"
"s_bitset1_b32 s48,18\n"
"v_add_f32_dpp v188,v188,v188 quad_perm:[1,0,3,2] row_mask:0xf bank_mask:0xf\n"
"s_lshl_b32 s51,s30,11\n"
MF"v[56:59],v[212:215],a[28:31],0\n"
SA"s50,s50,s51\n"
BL"v238,s[16:19],s48 offen offset:0 lds\n"
MF"v[56:59],v[208:211],a[12:15],v[56:59]\n"
WT"lgkmcnt(3)\n"
RD"v[204:207],v243 offset:10240\n"
RT"a[42:43],v248 offset:8704\n"
MF"v[36:39],v[216:219],a[0:3],0\n"
SA"s51,s50,8192\n"
BS"v[132:135],v129,s[8:11],s50 offen sc1\n"
MF"v[36:39],v[220:223],a[16:19],v[36:39]\n"
"s_lshl_b32 s90,s31,2\n"
BL"v239,s[16:19],s48 offen offset:1024 lds\n"
MF"v[44:47],v[220:223],a[20:23],0\n"
SA"s90,s90,s30\n"
BS"a[240:243],v129,s[8:11],s51 offen sc1\n"
MF"v[44:47],v[216:219],a[4:7],v[44:47]\n"
"s_lshl_b32 s90,s90,1\n"
RD"v[208:211],v242 offset:12288\n"
RT"a[44:45],v249 offset:8192\n"
MF"v[52:55],v[216:219],a[8:11],0\n"
"s_sub_u32 s91,2047,s90\n"
BS"a[244:247],v129,s[8:11],s52 offen sc1\n"
MF"v[52:55],v[220:223],a[24:27],v[52:55]\n"
RD"v[212:215],v243 offset:12288\n"
RT"a[46:47],v249 offset:8704\n"
MF"v[60:63],v[220:223],a[28:31],0\n"
BL"v240,s[16:19],s48 offen offset:2048 lds\n"
MF"v[60:63],v[216:219],a[12:15],v[60:63]\n"
"v_add_f32_dpp v188,v188,v188 quad_perm:[2,3,0,1] row_mask:0xf bank_mask:0xf\n"
MF"v[64:67],v[192:195],a[0:3],0\n"
RD"v[216:219],v242 offset:14336\n"
"v_add_f32_dpp v188,v188,v188 row_half_mirror row_mask:0xf bank_mask:0xf\n"
MF"v[64:67],v[196:199],a[16:19],v[64:67]\n"
WT"vmcnt(14)\n"
RT"a[48:49],v246 offset:12288\n"
"v_add_f32_dpp v188,v188,v188 row_mirror row_mask:0xf bank_mask:0xf\n"
MF"v[72:75],v[196:199],a[20:23],0\n"
"s_lshl_b32 s90,s90,2\n"
BL"v241,s[16:19],s48 offen offset:3072 lds\n"
MF"v[72:75],v[192:195],a[4:7],v[72:75]\n"
SA"s90,s90,2686976\n"
RT"a[50:51],v246 offset:12800\n"
"v_mov_b32 v189,v188\n"
MF"v[80:83],v[192:195],a[8:11],0\n"
SA"m0,s36,32768\n"
RD"v[220:223],v243 offset:14336\n"
"v_permlane16_swap_b32 v189,v188\n"
MF"v[80:83],v[196:199],a[24:27],v[80:83]\n"
"s_bfe_u32 s48,s46,0x30008\n"
"v_add_f32 v188,v188,v189\n"
PK"v188,v188,v188\n"
MF"v[88:91],v[196:199],a[28:31],0\n"
"s_lshl_b32 s48,s48,7\n"
RT"a[52:53],v247 offset:12288\n"
"v_lshlrev_b32 v188,16,v188\n"
MF"v[88:91],v[192:195],a[12:15],v[88:91]\n"
WT"lgkmcnt(4)\n"
BL"v234,s[12:15],s48 offen offset:0 lds\n"
MF"v[68:71],v[200:203],a[0:3],0\n"
"v_ashrrev_i32 v189,31,v188\n"
"v_or_b32 v189,0x80000000,v189\n"
MF"v[68:71],v[204:207],a[16:19],v[68:71]\n"
RT"a[54:55],v247 offset:12800\n"
"v_xor_b32 v188,v188,v189\n"
MF"v[76:79],v[204:207],a[20:23],0\n"
BL"v235,s[12:15],s48 offen offset:1024 lds\n"
MF"v[76:79],v[200:203],a[4:7],v[76:79]\n"
"v_and_b32 v188,0xffff0000,v188\n"
"v_lshrrev_b32 v189,9,v224\n"
MF"v[84:87],v[200:203],a[8:11],0\n"
RT"a[56:57],v248 offset:12288\n"
"v_sub_u32 v190,s91,v189\n"
MF"v[84:87],v[204:207],a[24:27],v[84:87]\n"
WT"vmcnt(13) lgkmcnt(3)\n"
"s_barrier\n"
MF"v[92:95],v[204:207],a[28:31],0\n"
RD"v[192:195],v242 offset:16384\n"
"v_or_b32 v188,v188,v190\n"
MF"v[92:95],v[200:203],a[12:15],v[92:95]\n"
RT"a[58:59],v248 offset:12800\n"
"v_and_b32 v190,0x1f0,v224\n"
MF"v[96:99],v[208:211],a[0:3],0\n"
BL"v236,s[12:15],s48 offen offset:2048 lds\n"
MF"v[96:99],v[212:215],a[16:19],v[96:99]\n"
"v_lshlrev_b32 v190,23,v190\n"
"v_lshl_add_u32 v190,v189,2,v190\n"
MF"v[104:107],v[212:215],a[20:23],0\n"
"buffer_store_dword v188,v190,s[8:11],s90 offen sc1\n"
MF"v[104:107],v[208:211],a[4:7],v[104:107]\n"
RD"v[196:199],v243 offset:16384\n"
RT"a[60:61],v249 offset:12288\n"
MF"v[112:115],v[208:211],a[8:11],0\n"
BL"v237,s[12:15],s48 offen offset:3072 lds\n"
MF"v[112:115],v[212:215],a[24:27],v[112:115]\n"
RT"a[62:63],v249 offset:12800\n"
MF"v[120:123],v[212:215],a[28:31],0\n"
SA"m0,s37,0\n"
RD"v[200:203],v242 offset:18432\n"
"s_bfe_u32 s48,s46,0x3000c\n"
MF"v[120:123],v[208:211],a[12:15],v[120:123]\n"
"s_lshl_b32 s48,s48,19\n"
MF"v[100:103],v[216:219],a[0:3],0\n"
BL"v238,s[16:19],s48 offen offset:0 lds\n"
MF"v[100:103],v[220:223],a[16:19],v[100:103]\n"
RD"v[204:207],v243 offset:18432\n"
MF"v[108:111],v[220:223],a[20:23],0\n"
MF"v[108:111],v[216:219],a[4:7],v[108:111]\n"
MF"v[116:119],v[216:219],a[8:11],0\n"
BL"v239,s[16:19],s48 offen offset:1024 lds\n"
MF"v[116:119],v[220:223],a[24:27],v[116:119]\n"
RD"v[208:211],v242 offset:20480\n"
MF"v[124:127],v[220:223],a[28:31],0\n"
MF"v[124:127],v[216:219],a[12:15],v[124:127]\n"
WT"lgkmcnt(2)\n"
MF"v[0:3],v[192:195],a[32:35],v[0:3]\n"
RD"v[212:215],v243 offset:20480\n"
MF"v[0:3],v[196:199],a[48:51],v[0:3]\n"
BL"v240,s[16:19],s48 offen offset:2048 lds\n"
MF"v[8:11],v[196:199],a[52:55],v[8:11]\n"
MF"v[8:11],v[192:195],a[36:39],v[8:11]\n"
MF"v[16:19],v[192:195],a[40:43],v[16:19]\n"
RD"v[216:219],v242 offset:22528\n"
MF"v[16:19],v[196:199],a[56:59],v[16:19]\n"
BL"v241,s[16:19],s48 offen offset:3072 lds\n"
MF"v[24:27],v[196:199],a[60:63],v[24:27]\n"
MF"v[24:27],v[192:195],a[44:47],v[24:27]\n"
RD"v[220:223],v243 offset:22528\n"
MF"v[4:7],v[200:203],a[32:35],v[4:7]\n"
WT"lgkmcnt(2)\n"
WT"vmcnt(16)\n"
RT"a[64:65],v246 offset:16384\n"
MF"v[4:7],v[204:207],a[48:51],v[4:7]\n"
SA"m0,s37,4096\n"
"s_bfe_u32 s48,s46,0x3000c\n"
"s_lshl_b32 s48,s48,19\n"
MF"v[12:15],v[204:207],a[52:55],v[12:15]\n"
"s_bitset1_b32 s48,18\n"
BL"v238,s[16:19],s48 offen offset:0 lds\n"
MF"v[12:15],v[200:203],a[36:39],v[12:15]\n"
RD"v[192:195],v242 offset:24576\n"
RT"a[66:67],v246 offset:16896\n"
MF"v[20:23],v[200:203],a[40:43],v[20:23]\n"
MF"v[20:23],v[204:207],a[56:59],v[20:23]\n"
MF"v[28:31],v[204:207],a[60:63],v[28:31]\n"
RT"a[68:69],v247 offset:16384\n"
MF"v[28:31],v[200:203],a[44:47],v[28:31]\n"
BL"v239,s[16:19],s48 offen offset:1024 lds\n"
MF"v[32:35],v[208:211],a[32:35],v[32:35]\n"
RD"v[196:199],v243 offset:24576\n"
MF"v[32:35],v[212:215],a[48:51],v[32:35]\n"
RT"a[70:71],v247 offset:16896\n"
MF"v[40:43],v[212:215],a[52:55],v[40:43]\n"
RD"v[200:203],v242 offset:26624\n"
MF"v[40:43],v[208:211],a[36:39],v[40:43]\n"
BL"v240,s[16:19],s48 offen offset:2048 lds\n"
MF"v[48:51],v[208:211],a[40:43],v[48:51]\n"
RT"a[72:73],v248 offset:16384\n"
MF"v[48:51],v[212:215],a[56:59],v[48:51]\n"
MF"v[56:59],v[212:215],a[60:63],v[56:59]\n"
RD"v[204:207],v243 offset:26624\n"
MF"v[56:59],v[208:211],a[44:47],v[56:59]\n"
WT"lgkmcnt(4)\n"
RT"a[74:75],v248 offset:16896\n"
MF"v[36:39],v[216:219],a[32:35],v[36:39]\n"
BL"v241,s[16:19],s48 offen offset:3072 lds\n"
MF"v[36:39],v[220:223],a[48:51],v[36:39]\n"
RD"v[208:211],v242 offset:28672\n"
MF"v[44:47],v[220:223],a[52:55],v[44:47]\n"
SA"m0,s36,0\n"
RT"a[76:77],v249 offset:16384\n"
"s_bfe_u32 s48,s46,0x3000c\n"
MF"v[44:47],v[216:219],a[36:39],v[44:47]\n"
"s_lshl_b32 s48,s48,7\n"
MF"v[52:55],v[216:219],a[40:43],v[52:55]\n"
BL"v234,s[12:15],s48 offen offset:0 lds\n"
MF"v[52:55],v[220:223],a[56:59],v[52:55]\n"
RD"v[212:215],v243 offset:28672\n"
RT"a[78:79],v249 offset:16896\n"
MF"v[60:63],v[220:223],a[60:63],v[60:63]\n"
MF"v[60:63],v[216:219],a[44:47],v[60:63]\n"
MF"v[64:67],v[192:195],a[32:35],v[64:67]\n"
RD"v[216:219],v242 offset:30720\n"
MF"v[64:67],v[196:199],a[48:51],v[64:67]\n"
WT"vmcnt(14)\n"
RT"a[80:81],v246 offset:20480\n"
MF"v[72:75],v[196:199],a[52:55],v[72:75]\n"
BL"v235,s[12:15],s48 offen offset:1024 lds\n"
MF"v[72:75],v[192:195],a[36:39],v[72:75]\n"
RT"a[82:83],v246 offset:20992\n"
MF"v[80:83],v[192:195],a[40:43],v[80:83]\n"
RD"v[220:223],v243 offset:30720\n"
MF"v[80:83],v[196:199],a[56:59],v[80:83]\n"
BL"v236,s[12:15],s48 offen offset:2048 lds\n"
MF"v[88:91],v[196:199],a[60:63],v[88:91]\n"
RT"a[84:85],v247 offset:20480\n"
MF"v[88:91],v[192:195],a[44:47],v[88:91]\n"
WT"lgkmcnt(4)\n"
MF"v[68:71],v[200:203],a[32:35],v[68:71]\n"
"s_lshl_b32 s51,s32,8\n"
"s_andn2_b32 s50,s31,7\n"
SA"s50,s50,s51\n"
MF"v[68:71],v[204:207],a[48:51],v[68:71]\n"
SA"s50,s50,256\n"
RT"a[86:87],v247 offset:20992\n"
SA"s50,s50,s45\n"
MF"v[76:79],v[204:207],a[52:55],v[76:79]\n"
BL"v237,s[12:15],s48 offen offset:3072 lds\n"
MF"v[76:79],v[200:203],a[36:39],v[76:79]\n"
MF"v[84:87],v[200:203],a[40:43],v[84:87]\n"
SA"m0,s37,8192\n"
RT"a[88:89],v248 offset:20480\n"
"s_bfe_u32 s48,s46,0x30010\n"
MF"v[84:87],v[204:207],a[56:59],v[84:87]\n"
"s_lshl_b32 s48,s48,19\n"
WT"vmcnt(12) lgkmcnt(3)\n"
"s_barrier\n"
MF"v[92:95],v[204:207],a[60:63],v[92:95]\n"
RD"v[192:195],v242 offset:32768\n"
MF"v[92:95],v[200:203],a[44:47],v[92:95]\n"
BL"v238,s[16:19],s48 offen offset:0 lds\n"
MF"v[96:99],v[208:211],a[32:35],v[96:99]\n"
"buffer_store_dwordx2 v[232:233],v230,s[8:11],s50 offen sc1\n"
MF"v[96:99],v[212:215],a[48:51],v[96:99]\n"
RT"a[90:91],v248 offset:20992\n"
MF"v[104:107],v[212:215],a[52:55],v[104:107]\n"
RD"v[196:199],v243 offset:32768\n"
RT"a[92:93],v249 offset:20480\n"
MF"v[104:107],v[208:211],a[36:39],v[104:107]\n"
BL"v239,s[16:19],s48 offen offset:1024 lds\n"
MF"v[112:115],v[208:211],a[40:43],v[112:115]\n"
MF"v[112:115],v[212:215],a[56:59],v[112:115]\n"
RT"a[94:95],v249 offset:20992\n"
MF"v[120:123],v[212:215],a[60:63],v[120:123]\n"
RD"v[200:203],v242 offset:34816\n"
MF"v[120:123],v[208:211],a[44:47],v[120:123]\n"
BL"v240,s[16:19],s48 offen offset:2048 lds\n"
MF"v[100:103],v[216:219],a[32:35],v[100:103]\n"
MF"v[100:103],v[220:223],a[48:51],v[100:103]\n"
RD"v[204:207],v243 offset:34816\n"
MF"v[108:111],v[220:223],a[52:55],v[108:111]\n"
MF"v[108:111],v[216:219],a[36:39],v[108:111]\n"
BL"v241,s[16:19],s48 offen offset:3072 lds\n"
MF"v[116:119],v[216:219],a[40:43],v[116:119]\n"
MF"v[116:119],v[220:223],a[56:59],v[116:119]\n"
RD"v[208:211],v242 offset:36864\n"
MF"v[124:127],v[220:223],a[60:63],v[124:127]\n"
SA"m0,s37,12288\n"
"s_bfe_u32 s48,s46,0x30010\n"
"s_lshl_b32 s48,s48,19\n"
MF"v[124:127],v[216:219],a[44:47],v[124:127]\n"
WT"lgkmcnt(2)\n"
"s_bitset1_b32 s48,18\n"
MF"v[0:3],v[192:195],a[64:67],v[0:3]\n"
RD"v[212:215],v243 offset:36864\n"
MF"v[0:3],v[196:199],a[80:83],v[0:3]\n"
BL"v238,s[16:19],s48 offen offset:0 lds\n"
MF"v[8:11],v[196:199],a[84:87],v[8:11]\n"
MF"v[8:11],v[192:195],a[68:71],v[8:11]\n"
MF"v[16:19],v[192:195],a[72:75],v[16:19]\n"
RD"v[216:219],v242 offset:38912\n"
MF"v[16:19],v[196:199],a[88:91],v[16:19]\n"
BL"v239,s[16:19],s48 offen offset:1024 lds\n"
MF"v[24:27],v[196:199],a[92:95],v[24:27]\n"
MF"v[24:27],v[192:195],a[76:79],v[24:27]\n"
RD"v[220:223],v243 offset:38912\n"
MF"v[4:7],v[200:203],a[64:67],v[4:7]\n"
WT"lgkmcnt(2)\n"
WT"vmcnt(15)\n"
RT"a[96:97],v246 offset:0\n"
MF"v[4:7],v[204:207],a[80:83],v[4:7]\n"
BL"v240,s[16:19],s48 offen offset:2048 lds\n"
MF"v[12:15],v[204:207],a[84:87],v[12:15]\n"
MF"v[12:15],v[200:203],a[68:71],v[12:15]\n"
RD"v[192:195],v242 offset:40960\n"
MF"v[20:23],v[200:203],a[72:75],v[20:23]\n"
RT"a[98:99],v246 offset:512\n"
MF"v[20:23],v[204:207],a[88:91],v[20:23]\n"
BL"v241,s[16:19],s48 offen offset:3072 lds\n"
MF"v[28:31],v[204:207],a[92:95],v[28:31]\n"
RT"a[100:101],v247 offset:0\n"
MF"v[28:31],v[200:203],a[76:79],v[28:31]\n"
RD"v[196:199],v243 offset:40960\n"
MF"v[32:35],v[208:211],a[64:67],v[32:35]\n"
SA"m0,s36,16384\n"
"s_bfe_u32 s48,s46,0x30010\n"
"s_lshl_b32 s48,s48,7\n"
MF"v[32:35],v[212:215],a[80:83],v[32:35]\n"
RT"a[102:103],v247 offset:512\n"
MF"v[40:43],v[212:215],a[84:87],v[40:43]\n"
RD"v[200:203],v242 offset:43008\n"
MF"v[40:43],v[208:211],a[68:71],v[40:43]\n"
BL"v234,s[12:15],s48 offen offset:0 lds\n"
MF"v[48:51],v[208:211],a[72:75],v[48:51]\n"
RT"a[104:105],v248 offset:0\n"
MF"v[48:51],v[212:215],a[88:91],v[48:51]\n"
MF"v[56:59],v[212:215],a[92:95],v[56:59]\n"
RD"v[204:207],v243 offset:43008\n"
MF"v[56:59],v[208:211],a[76:79],v[56:59]\n"
WT"lgkmcnt(4)\n"
BL"v235,s[12:15],s48 offen offset:1024 lds\n"
MF"v[36:39],v[216:219],a[64:67],v[36:39]\n"
RT"a[106:107],v248 offset:512\n"
MF"v[36:39],v[220:223],a[80:83],v[36:39]\n"
RD"v[208:211],v242 offset:45056\n"
MF"v[44:47],v[220:223],a[84:87],v[44:47]\n"
RT"a[108:109],v249 offset:0\n"
MF"v[44:47],v[216:219],a[68:71],v[44:47]\n"
BL"v236,s[12:15],s48 offen offset:2048 lds\n"
MF"v[52:55],v[216:219],a[72:75],v[52:55]\n"
MF"v[52:55],v[220:223],a[88:91],v[52:55]\n"
RD"v[212:215],v243 offset:45056\n"
MF"v[60:63],v[220:223],a[92:95],v[60:63]\n"
RT"a[110:111],v249 offset:512\n"
MF"v[60:63],v[216:219],a[76:79],v[60:63]\n"
BL"v237,s[12:15],s48 offen offset:3072 lds\n"
MF"v[64:67],v[192:195],a[64:67],v[64:67]\n"
RD"v[216:219],v242 offset:47104\n"
MF"v[64:67],v[196:199],a[80:83],v[64:67]\n"
WT"vmcnt(17)\n"
RT"a[112:113],v246 offset:4096\n"
MF"v[72:75],v[196:199],a[84:87],v[72:75]\n"
SA"m0,s37,16384\n"
"s_bfe_u32 s48,s46,0x30014\n"
"s_lshl_b32 s48,s48,19\n"
MF"v[72:75],v[192:195],a[68:71],v[72:75]\n"
RT"a[114:115],v246 offset:4608\n"
MF"v[80:83],v[192:195],a[72:75],v[80:83]\n"
RD"v[220:223],v243 offset:47104\n"
MF"v[80:83],v[196:199],a[88:91],v[80:83]\n"
BL"v238,s[16:19],s48 offen offset:0 lds\n"
MF"v[88:91],v[196:199],a[92:95],v[88:91]\n"
RT"a[116:117],v247 offset:4096\n"
MF"v[88:91],v[192:195],a[76:79],v[88:91]\n"
WT"lgkmcnt(4)\n"
MF"v[68:71],v[200:203],a[64:67],v[68:71]\n"
BL"v239,s[16:19],s48 offen offset:1024 lds\n"
MF"v[68:71],v[204:207],a[80:83],v[68:71]\n"
RT"a[118:119],v247 offset:4608\n"
MF"v[76:79],v[204:207],a[84:87],v[76:79]\n"
MF"v[76:79],v[200:203],a[68:71],v[76:79]\n"
MF"v[84:87],v[200:203],a[72:75],v[84:87]\n"
RT"a[120:121],v248 offset:4096\n"
MF"v[84:87],v[204:207],a[88:91],v[84:87]\n"
WT"vmcnt(15) lgkmcnt(3)\n"
"s_barrier\n"
MF"v[92:95],v[204:207],a[92:95],v[92:95]\n"
BL"v240,s[16:19],s48 offen offset:2048 lds\n"
MF"v[92:95],v[200:203],a[76:79],v[92:95]\n"
RD"v[192:195],v242 offset:0\n"
RT"a[122:123],v248 offset:4608\n"
MF"v[96:99],v[208:211],a[64:67],v[96:99]\n"
MF"v[96:99],v[212:215],a[80:83],v[96:99]\n"
BL"v241,s[16:19],s48 offen offset:3072 lds\n"
MF"v[104:107],v[212:215],a[84:87],v[104:107]\n"
RD"v[196:199],v243 offset:0\n"
RT"a[124:125],v249 offset:4096\n"
MF"v[104:107],v[208:211],a[68:71],v[104:107]\n"
MF"v[112:115],v[208:211],a[72:75],v[112:115]\n"
SA"m0,s37,20480\n"
"s_bfe_u32 s48,s46,0x30014\n"
"s_lshl_b32 s48,s48,19\n"
MF"v[112:115],v[212:215],a[88:91],v[112:115]\n"
"s_bitset1_b32 s48,18\n"
RT"a[126:127],v249 offset:4608\n"
MF"v[120:123],v[212:215],a[92:95],v[120:123]\n"
RD"v[200:203],v242 offset:2048\n"
MF"v[120:123],v[208:211],a[76:79],v[120:123]\n"
BL"v238,s[16:19],s48 offen offset:0 lds\n"
MF"v[100:103],v[216:219],a[64:67],v[100:103]\n"
MF"v[100:103],v[220:223],a[80:83],v[100:103]\n"
RD"v[204:207],v243 offset:2048\n"
MF"v[108:111],v[220:223],a[84:87],v[108:111]\n"
BL"v239,s[16:19],s48 offen offset:1024 lds\n"
MF"v[108:111],v[216:219],a[68:71],v[108:111]\n"
MF"v[116:119],v[216:219],a[72:75],v[116:119]\n"
MF"v[116:119],v[220:223],a[88:91],v[116:119]\n"
RD"v[208:211],v242 offset:4096\n"
MF"v[124:127],v[220:223],a[92:95],v[124:127]\n"
MF"v[124:127],v[216:219],a[76:79],v[124:127]\n"
WT"lgkmcnt(2)\n"
BL"v240,s[16:19],s48 offen offset:2048 lds\n"
MF"v[0:3],v[192:195],a[96:99],v[0:3]\n"
RD"v[212:215],v243 offset:4096\n"
MF"v[0:3],v[196:199],a[112:115],v[0:3]\n"
MF"v[8:11],v[196:199],a[116:119],v[8:11]\n"
MF"v[8:11],v[192:195],a[100:103],v[8:11]\n"
BL"v241,s[16:19],s48 offen offset:3072 lds\n"
MF"v[16:19],v[192:195],a[104:107],v[16:19]\n"
RD"v[216:219],v242 offset:6144\n"
MF"v[16:19],v[196:199],a[120:123],v[16:19]\n"
MF"v[24:27],v[196:199],a[124:127],v[24:27]\n"
SA"m0,s36,32768\n"
"s_bfe_u32 s48,s46,0x30014\n"
"s_lshl_b32 s48,s48,7\n"
MF"v[24:27],v[192:195],a[108:111],v[24:27]\n"
RD"v[220:223],v243 offset:6144\n"
MF"v[4:7],v[200:203],a[96:99],v[4:7]\n"
WT"lgkmcnt(2)\n"
WT"vmcnt(16)\n"
RT"a[128:129],v246 offset:8192\n"
MF"v[4:7],v[204:207],a[112:115],v[4:7]\n"
BL"v234,s[12:15],s48 offen offset:0 lds\n"
MF"v[12:15],v[204:207],a[116:119],v[12:15]\n"
MF"v[12:15],v[200:203],a[100:103],v[12:15]\n"
RD"v[192:195],v242 offset:8192\n"
MF"v[20:23],v[200:203],a[104:107],v[20:23]\n"
RT"a[130:131],v246 offset:8704\n"
MF"v[20:23],v[204:207],a[120:123],v[20:23]\n"
BL"v235,s[12:15],s48 offen offset:1024 lds\n"
MF"v[28:31],v[204:207],a[124:127],v[28:31]\n"
RT"a[132:133],v247 offset:8192\n"
MF"v[28:31],v[200:203],a[108:111],v[28:31]\n"
RD"v[196:199],v243 offset:8192\n"
MF"v[32:35],v[208:211],a[96:99],v[32:35]\n"
MF"v[32:35],v[212:215],a[112:115],v[32:35]\n"
RT"a[134:135],v247 offset:8704\n"
MF"v[40:43],v[212:215],a[116:119],v[40:43]\n"
BL"v236,s[12:15],s48 offen offset:2048 lds\n"
MF"v[40:43],v[208:211],a[100:103],v[40:43]\n"
RD"v[200:203],v242 offset:10240\n"
MF"v[48:51],v[208:211],a[104:107],v[48:51]\n"
RT"a[136:137],v248 offset:8192\n"
MF"v[48:51],v[212:215],a[120:123],v[48:51]\n"
BL"v237,s[12:15],s48 offen offset:3072 lds\n"
MF"v[56:59],v[212:215],a[124:127],v[56:59]\n"
RD"v[204:207],v243 offset:10240\n"
MF"v[56:59],v[208:211],a[108:111],v[56:59]\n"
WT"lgkmcnt(4)\n"
RT"a[138:139],v248 offset:8704\n"
MF"v[36:39],v[216:219],a[96:99],v[36:39]\n"
SA"m0,s37,0\n"
"s_bfe_u32 s48,s46,0x30018\n"
"s_lshl_b32 s48,s48,19\n"
MF"v[36:39],v[220:223],a[112:115],v[36:39]\n"
RD"v[208:211],v242 offset:12288\n"
MF"v[44:47],v[220:223],a[116:119],v[44:47]\n"
RT"a[140:141],v249 offset:8192\n"
MF"v[44:47],v[216:219],a[100:103],v[44:47]\n"
BL"v238,s[16:19],s48 offen offset:0 lds\n"
MF"v[52:55],v[216:219],a[104:107],v[52:55]\n"
MF"v[52:55],v[220:223],a[120:123],v[52:55]\n"
RD"v[212:215],v243 offset:12288\n"
MF"v[60:63],v[220:223],a[124:127],v[60:63]\n"
RT"a[142:143],v249 offset:8704\n"
MF"v[60:63],v[216:219],a[108:111],v[60:63]\n"
BL"v239,s[16:19],s48 offen offset:1024 lds\n"
MF"v[64:67],v[192:195],a[96:99],v[64:67]\n"
RD"v[216:219],v242 offset:14336\n"
MF"v[64:67],v[196:199],a[112:115],v[64:67]\n"
WT"vmcnt(18)\n"
RT"a[144:145],v246 offset:12288\n"
MF"v[72:75],v[196:199],a[116:119],v[72:75]\n"
MF"v[72:75],v[192:195],a[100:103],v[72:75]\n"
RT"a[146:147],v246 offset:12800\n"
MF"v[80:83],v[192:195],a[104:107],v[80:83]\n"
BL"v240,s[16:19],s48 offen offset:2048 lds\n"
MF"v[80:83],v[196:199],a[120:123],v[80:83]\n"
RD"v[220:223],v243 offset:14336\n"
MF"v[88:91],v[196:199],a[124:127],v[88:91]\n"
RT"a[148:149],v247 offset:12288\n"
MF"v[88:91],v[192:195],a[108:111],v[88:91]\n"
WT"lgkmcnt(4)\n"
BL"v241,s[16:19],s48 offen offset:3072 lds\n"
MF"v[68:71],v[200:203],a[96:99],v[68:71]\n"
MF"v[68:71],v[204:207],a[112:115],v[68:71]\n"
RT"a[150:151],v247 offset:12800\n"
MF"v[76:79],v[204:207],a[116:119],v[76:79]\n"
SA"m0,s37,4096\n"
"s_bfe_u32 s48,s46,0x30018\n"
"s_lshl_b32 s48,s48,19\n"
MF"v[76:79],v[200:203],a[100:103],v[76:79]\n"
"s_bitset1_b32 s48,18\n"
MF"v[84:87],v[200:203],a[104:107],v[84:87]\n"
RT"a[152:153],v248 offset:12288\n"
MF"v[84:87],v[204:207],a[120:123],v[84:87]\n"
BL"v238,s[16:19],s48 offen offset:0 lds\n"
MF"v[92:95],v[204:207],a[124:127],v[92:95]\n"
WT"vmcnt(17) lgkmcnt(3)\n"
"s_barrier\n"
MF"v[92:95],v[200:203],a[108:111],v[92:95]\n"
RD"v[192:195],v242 offset:16384\n"
RT"a[154:155],v248 offset:12800\n"
MF"v[96:99],v[208:211],a[96:99],v[96:99]\n"
BL"v239,s[16:19],s48 offen offset:1024 lds\n"
MF"v[96:99],v[212:215],a[112:115],v[96:99]\n"
MF"v[104:107],v[212:215],a[116:119],v[104:107]\n"
RD"v[196:199],v243 offset:16384\n"
MF"v[104:107],v[208:211],a[100:103],v[104:107]\n"
RT"a[156:157],v249 offset:12288\n"
MF"v[112:115],v[208:211],a[104:107],v[112:115]\n"
MF"v[112:115],v[212:215],a[120:123],v[112:115]\n"
RT"a[158:159],v249 offset:12800\n"
MF"v[120:123],v[212:215],a[124:127],v[120:123]\n"
BL"v240,s[16:19],s48 offen offset:2048 lds\n"
MF"v[120:123],v[208:211],a[108:111],v[120:123]\n"
RD"v[200:203],v242 offset:18432\n"
MF"v[100:103],v[216:219],a[96:99],v[100:103]\n"
MF"v[100:103],v[220:223],a[112:115],v[100:103]\n"
RD"v[204:207],v243 offset:18432\n"
MF"v[108:111],v[220:223],a[116:119],v[108:111]\n"
BL"v241,s[16:19],s48 offen offset:3072 lds\n"
MF"v[108:111],v[216:219],a[100:103],v[108:111]\n"
MF"v[116:119],v[216:219],a[104:107],v[116:119]\n"
SA"m0,s36,0\n"
"s_bfe_u32 s48,s46,0x30018\n"
"s_lshl_b32 s48,s48,7\n"
MF"v[116:119],v[220:223],a[120:123],v[116:119]\n"
RD"v[208:211],v242 offset:20480\n"
MF"v[124:127],v[220:223],a[124:127],v[124:127]\n"
BL"v234,s[12:15],s48 offen offset:0 lds\n"
MF"v[124:127],v[216:219],a[108:111],v[124:127]\n"
WT"lgkmcnt(2)\n"
MF"v[0:3],v[192:195],a[128:131],v[0:3]\n"
RD"v[212:215],v243 offset:20480\n"
MF"v[0:3],v[196:199],a[144:147],v[0:3]\n"
MF"v[8:11],v[196:199],a[148:151],v[8:11]\n"
BL"v235,s[12:15],s48 offen offset:1024 lds\n"
MF"v[8:11],v[192:195],a[132:135],v[8:11]\n"
MF"v[16:19],v[192:195],a[136:139],v[16:19]\n"
RD"v[216:219],v242 offset:22528\n"
MF"v[16:19],v[196:199],a[152:155],v[16:19]\n"
MF"v[24:27],v[196:199],a[156:159],v[24:27]\n"
MF"v[24:27],v[192:195],a[140:143],v[24:27]\n"
RD"v[220:223],v243 offset:22528\n"
MF"v[4:7],v[200:203],a[128:131],v[4:7]\n"
WT"lgkmcnt(2)\n"
BL"v236,s[12:15],s48 offen offset:2048 lds\n"
MF"v[4:7],v[204:207],a[144:147],v[4:7]\n"
WT"vmcnt(19)\n"
RT"a[160:161],v246 offset:16384\n"
MF"v[12:15],v[204:207],a[148:151],v[12:15]\n"
MF"v[12:15],v[200:203],a[132:135],v[12:15]\n"
RD"v[192:195],v242 offset:24576\n"
MF"v[20:23],v[200:203],a[136:139],v[20:23]\n"
RT"a[162:163],v246 offset:16896\n"
MF"v[20:23],v[204:207],a[152:155],v[20:23]\n"
BL"v237,s[12:15],s48 offen offset:3072 lds\n"
MF"v[28:31],v[204:207],a[156:159],v[28:31]\n"
SA"m0,s37,8192\n"
RT"a[164:165],v247 offset:16384\n"
"s_bfe_u32 s48,s46,0x3001c\n"
MF"v[28:31],v[200:203],a[140:143],v[28:31]\n"
"s_lshl_b32 s48,s48,19\n"
RD"v[196:199],v243 offset:24576\n"
MF"v[32:35],v[208:211],a[128:131],v[32:35]\n"
BL"v238,s[16:19],s48 offen offset:0 lds\n"
MF"v[32:35],v[212:215],a[144:147],v[32:35]\n"
RT"a[166:167],v247 offset:16896\n"
MF"v[40:43],v[212:215],a[148:151],v[40:43]\n"
RD"v[200:203],v242 offset:26624\n"
MF"v[40:43],v[208:211],a[132:135],v[40:43]\n"
MF"v[48:51],v[208:211],a[136:139],v[48:51]\n"
RT"a[168:169],v248 offset:16384\n"
MF"v[48:51],v[212:215],a[152:155],v[48:51]\n"
BL"v239,s[16:19],s48 offen offset:1024 lds\n"
MF"v[56:59],v[212:215],a[156:159],v[56:59]\n"
RD"v[204:207],v243 offset:26624\n"
MF"v[56:59],v[208:211],a[140:143],v[56:59]\n"
WT"lgkmcnt(4)\n"
RT"a[170:171],v248 offset:16896\n"
MF"v[36:39],v[216:219],a[128:131],v[36:39]\n"
MF"v[36:39],v[220:223],a[144:147],v[36:39]\n"
RD"v[208:211],v242 offset:28672\n"
MF"v[44:47],v[220:223],a[148:151],v[44:47]\n"
BL"v240,s[16:19],s48 offen offset:2048 lds\n"
MF"v[44:47],v[216:219],a[132:135],v[44:47]\n"
RT"a[172:173],v249 offset:16384\n"
MF"v[52:55],v[216:219],a[136:139],v[52:55]\n"
MF"v[52:55],v[220:223],a[152:155],v[52:55]\n"
RD"v[212:215],v243 offset:28672\n"
MF"v[60:63],v[220:223],a[156:159],v[60:63]\n"
RT"a[174:175],v249 offset:16896\n"
MF"v[60:63],v[216:219],a[140:143],v[60:63]\n"
BL"v241,s[16:19],s48 offen offset:3072 lds\n"
MF"v[64:67],v[192:195],a[128:131],v[64:67]\n"
SA"m0,s37,12288\n"
RD"v[216:219],v242 offset:30720\n"
"s_bfe_u32 s48,s46,0x3001c\n"
MF"v[64:67],v[196:199],a[144:147],v[64:67]\n"
"s_lshl_b32 s48,s48,19\n"
WT"vmcnt(20)\n"
RT"a[176:177],v246 offset:20480\n"
MF"v[72:75],v[196:199],a[148:151],v[72:75]\n"
"s_bitset1_b32 s48,18\n"
BL"v238,s[16:19],s48 offen offset:0 lds\n"
MF"v[72:75],v[192:195],a[132:135],v[72:75]\n"
RT"a[178:179],v246 offset:20992\n"
MF"v[80:83],v[192:195],a[136:139],v[80:83]\n"
RD"v[220:223],v243 offset:30720\n"
MF"v[80:83],v[196:199],a[152:155],v[80:83]\n"
MF"v[88:91],v[196:199],a[156:159],v[88:91]\n"
RT"a[180:181],v247 offset:20480\n"
MF"v[88:91],v[192:195],a[140:143],v[88:91]\n"
WT"lgkmcnt(4)\n"
BL"v239,s[16:19],s48 offen offset:1024 lds\n"
MF"v[68:71],v[200:203],a[128:131],v[68:71]\n"
MF"v[68:71],v[204:207],a[144:147],v[68:71]\n"
RT"a[182:183],v247 offset:20992\n"
MF"v[76:79],v[204:207],a[148:151],v[76:79]\n"
MF"v[76:79],v[200:203],a[132:135],v[76:79]\n"
BL"v240,s[16:19],s48 offen offset:2048 lds\n"
MF"v[84:87],v[200:203],a[136:139],v[84:87]\n"
RT"a[184:185],v248 offset:20480\n"
MF"v[84:87],v[204:207],a[152:155],v[84:87]\n"
WT"vmcnt(19) lgkmcnt(3)\n"
"s_barrier\n"
MF"v[92:95],v[204:207],a[156:159],v[92:95]\n"
RD"v[192:195],v242 offset:32768\n"
MF"v[92:95],v[200:203],a[140:143],v[92:95]\n"
RT"a[186:187],v248 offset:20992\n"
MF"v[96:99],v[208:211],a[128:131],v[96:99]\n"
BL"v241,s[16:19],s48 offen offset:3072 lds\n"
MF"v[96:99],v[212:215],a[144:147],v[96:99]\n"
MF"v[104:107],v[212:215],a[148:151],v[104:107]\n"
SA"m0,s36,16384\n"
RD"v[196:199],v243 offset:32768\n"
"s_bfe_u32 s48,s46,0x3001c\n"
MF"v[104:107],v[208:211],a[132:135],v[104:107]\n"
"s_lshl_b32 s48,s48,7\n"
RT"a[188:189],v249 offset:20480\n"
MF"v[112:115],v[208:211],a[136:139],v[112:115]\n"
BL"v234,s[12:15],s48 offen offset:0 lds\n"
MF"v[112:115],v[212:215],a[152:155],v[112:115]\n"
RT"a[190:191],v249 offset:20992\n"
MF"v[120:123],v[212:215],a[156:159],v[120:123]\n"
RD"v[200:203],v242 offset:34816\n"
MF"v[120:123],v[208:211],a[140:143],v[120:123]\n"
MF"v[100:103],v[216:219],a[128:131],v[100:103]\n"
BL"v235,s[12:15],s48 offen offset:1024 lds\n"
MF"v[100:103],v[220:223],a[144:147],v[100:103]\n"
RD"v[204:207],v243 offset:34816\n"
MF"v[108:111],v[220:223],a[148:151],v[108:111]\n"
MF"v[108:111],v[216:219],a[132:135],v[108:111]\n"
MF"v[116:119],v[216:219],a[136:139],v[116:119]\n"
MF"v[116:119],v[220:223],a[152:155],v[116:119]\n"
RD"v[208:211],v242 offset:36864\n"
MF"v[124:127],v[220:223],a[156:159],v[124:127]\n"
BL"v236,s[12:15],s48 offen offset:2048 lds\n"
MF"v[124:127],v[216:219],a[140:143],v[124:127]\n"
WT"lgkmcnt(2)\n"
MF"v[0:3],v[192:195],a[160:163],v[0:3]\n"
RD"v[212:215],v243 offset:36864\n"
MF"v[0:3],v[196:199],a[176:179],v[0:3]\n"
BL"v237,s[12:15],s48 offen offset:3072 lds\n"
MF"v[8:11],v[196:199],a[180:183],v[8:11]\n"
MF"v[8:11],v[192:195],a[164:167],v[8:11]\n"
MF"v[16:19],v[192:195],a[168:171],v[16:19]\n"
SA"m0,s37,16384\n"
RD"v[216:219],v242 offset:38912\n"
SA"s48,s39,0\n"
MF"v[16:19],v[196:199],a[184:187],v[16:19]\n"
MF"v[24:27],v[196:199],a[188:191],v[24:27]\n"
BL"v234,s[12:15],s48 offen offset:0 lds\n"
MF"v[24:27],v[192:195],a[172:175],v[24:27]\n"
RD"v[220:223],v243 offset:38912\n"
MF"v[4:7],v[200:203],a[160:163],v[4:7]\n"
WT"lgkmcnt(2)\n"
WT"vmcnt(21)\n"
RT"a[192:193],v246 offset:0\n"
MF"v[4:7],v[204:207],a[176:179],v[4:7]\n"
MF"v[12:15],v[204:207],a[180:183],v[12:15]\n"
BL"v235,s[12:15],s48 offen offset:1024 lds\n"
MF"v[12:15],v[200:203],a[164:167],v[12:15]\n"
RD"v[192:195],v242 offset:40960\n"
RT"a[194:195],v246 offset:512\n"
MF"v[20:23],v[200:203],a[168:171],v[20:23]\n"
MF"v[20:23],v[204:207],a[184:187],v[20:23]\n"
MF"v[28:31],v[204:207],a[188:191],v[28:31]\n"
RT"a[196:197],v247 offset:0\n"
MF"v[28:31],v[200:203],a[172:175],v[28:31]\n"
RD"v[196:199],v243 offset:40960\n"
MF"v[32:35],v[208:211],a[160:163],v[32:35]\n"
BL"v236,s[12:15],s48 offen offset:2048 lds\n"
MF"v[32:35],v[212:215],a[176:179],v[32:35]\n"
RT"a[198:199],v247 offset:512\n"
MF"v[40:43],v[212:215],a[180:183],v[40:43]\n"
RD"v[200:203],v242 offset:43008\n"
MF"v[40:43],v[208:211],a[164:167],v[40:43]\n"
BL"v237,s[12:15],s48 offen offset:3072 lds\n"
MF"v[48:51],v[208:211],a[168:171],v[48:51]\n"
RT"a[200:201],v248 offset:0\n"
MF"v[48:51],v[212:215],a[184:187],v[48:51]\n"
MF"v[56:59],v[212:215],a[188:191],v[56:59]\n"
SA"m0,s37,20480\n"
RD"v[204:207],v243 offset:43008\n"
SA"s48,s98,0\n"
MF"v[56:59],v[208:211],a[172:175],v[56:59]\n"
WT"lgkmcnt(4)\n"
RT"a[202:203],v248 offset:512\n"
"s_lshr_b32 s50,s30,1\n"
MF"v[36:39],v[216:219],a[160:163],v[36:39]\n"
"s_and_b32 s51,s30,1\n"
BL"v234,s[12:15],s48 offen offset:0 lds\n"
MF"v[36:39],v[220:223],a[176:179],v[36:39]\n"
"s_mul_i32 s38,s50,24576\n"
RD"v[208:211],v242 offset:45056\n"
MF"v[44:47],v[220:223],a[180:183],v[44:47]\n"
"s_lshl_b32 s60,s50,18\n"
RT"a[204:205],v249 offset:0\n"
MF"v[44:47],v[216:219],a[164:167],v[44:47]\n"
"s_lshl_b32 s50,s51,12\n"
MF"v[52:55],v[216:219],a[168:171],v[52:55]\n"
SA"s38,s38,s50\n"
BL"v235,s[12:15],s48 offen offset:1024 lds\n"
MF"v[52:55],v[220:223],a[184:187],v[52:55]\n"
"s_lshl_b32 s50,s51,7\n"
RD"v[212:215],v243 offset:45056\n"
RT"a[206:207],v249 offset:512\n"
MF"v[60:63],v[220:223],a[188:191],v[60:63]\n"
SA"s60,s60,s50\n"
MF"v[60:63],v[216:219],a[172:175],v[60:63]\n"
MF"v[64:67],v[192:195],a[160:163],v[64:67]\n"
RD"v[216:219],v242 offset:47104\n"
MF"v[64:67],v[196:199],a[176:179],v[64:67]\n"
"s_sub_u32 s99,s34,5\n"
WT"vmcnt(22)\n"
RT"a[208:209],v246 offset:4096\n"
MF"v[72:75],v[196:199],a[180:183],v[72:75]\n"
"s_cmp_lt_u32 s34,5\n"
BL"v236,s[12:15],s48 offen offset:2048 lds\n"
MF"v[72:75],v[192:195],a[164:167],v[72:75]\n"
"s_cselect_b32 s99,s34,s99\n"
RT"a[210:211],v246 offset:4608\n"
"s_mul_i32 s99,s99,1\n"
MF"v[80:83],v[192:195],a[168:171],v[80:83]\n"
"s_lshl_b32 s61,s99,20\n"
RD"v[220:223],v243 offset:47104\n"
MF"v[80:83],v[196:199],a[184:187],v[80:83]\n"
BL"v237,s[12:15],s48 offen offset:3072 lds\n"
MF"v[88:91],v[196:199],a[188:191],v[88:91]\n"
RT"a[212:213],v247 offset:4096\n"
MF"v[88:91],v[192:195],a[172:175],v[88:91]\n"
WT"lgkmcnt(4)\n"
MF"v[68:71],v[200:203],a[160:163],v[68:71]\n"
SA"m0,s37,0\n"
SA"s48,s39,32768\n"
MF"v[68:71],v[204:207],a[176:179],v[68:71]\n"
RT"a[214:215],v247 offset:4608\n"
MF"v[76:79],v[204:207],a[180:183],v[76:79]\n"
BL"v234,s[12:15],s48 offen offset:0 lds\n"
MF"v[76:79],v[200:203],a[164:167],v[76:79]\n"
MF"v[84:87],v[200:203],a[168:171],v[84:87]\n"
RT"a[216:217],v248 offset:4096\n"
MF"v[84:87],v[204:207],a[184:187],v[84:87]\n"
WT"vmcnt(21) lgkmcnt(3)\n"
"s_barrier\n"
MF"v[92:95],v[204:207],a[188:191],v[92:95]\n"
RD"v[192:195],v242 offset:0\n"
MF"v[92:95],v[200:203],a[172:175],v[92:95]\n"
BL"v235,s[12:15],s48 offen offset:1024 lds\n"
MF"v[96:99],v[208:211],a[160:163],v[96:99]\n"
RT"a[218:219],v248 offset:4608\n"
MF"v[96:99],v[212:215],a[176:179],v[96:99]\n"
MF"v[104:107],v[212:215],a[180:183],v[104:107]\n"
RD"v[196:199],v243 offset:0\n"
MF"v[104:107],v[208:211],a[164:167],v[104:107]\n"
RT"a[220:221],v249 offset:4096\n"
MF"v[112:115],v[208:211],a[168:171],v[112:115]\n"
BL"v236,s[12:15],s48 offen offset:2048 lds\n"
MF"v[112:115],v[212:215],a[184:187],v[112:115]\n"
RT"a[222:223],v249 offset:4608\n"
MF"v[120:123],v[212:215],a[188:191],v[120:123]\n"
RD"v[200:203],v242 offset:2048\n"
MF"v[120:123],v[208:211],a[172:175],v[120:123]\n"
BL"v237,s[12:15],s48 offen offset:3072 lds\n"
MF"v[100:103],v[216:219],a[160:163],v[100:103]\n"
MF"v[100:103],v[220:223],a[176:179],v[100:103]\n"
RD"v[204:207],v243 offset:2048\n"
MF"v[108:111],v[220:223],a[180:183],v[108:111]\n"
SA"m0,s37,4096\n"
SA"s48,s98,32768\n"
MF"v[108:111],v[216:219],a[164:167],v[108:111]\n"
MF"v[116:119],v[216:219],a[168:171],v[116:119]\n"
WT"lgkmcnt(2)\n"
BL"v234,s[12:15],s48 offen offset:0 lds\n"
MF"v[116:119],v[220:223],a[184:187],v[116:119]\n"
RD"v[208:211],v242 offset:4096\n"
MF"v[124:127],v[220:223],a[188:191],v[124:127]\n"
MF"v[124:127],v[216:219],a[172:175],v[124:127]\n"
MF"v[0:3],v[192:195],a[192:195],v[0:3]\n"
RD"v[212:215],v243 offset:4096\n"
MF"v[0:3],v[196:199],a[208:211],v[0:3]\n"
BL"v235,s[12:15],s48 offen offset:1024 lds\n"
MF"v[8:11],v[196:199],a[212:215],v[8:11]\n"
MF"v[8:11],v[192:195],a[196:199],v[8:11]\n"
MF"v[16:19],v[192:195],a[200:203],v[16:19]\n"
RD"v[216:219],v242 offset:6144\n"
MF"v[16:19],v[196:199],a[216:219],v[16:19]\n"
BL"v236,s[12:15],s48 offen offset:2048 lds\n"
MF"v[24:27],v[196:199],a[220:223],v[24:27]\n"
BL"v[160:163],v224,s[8:11],0 offen offset:256 sc1\n"
MF"v[24:27],v[192:195],a[204:207],v[24:27]\n"
WT"lgkmcnt(1)\n"
RD"v[220:223],v243 offset:6144\n"
MF"v[4:7],v[200:203],a[192:195],v[4:7]\n"
WT"vmcnt(24)\n"
RT"a[224:225],v246 offset:8192\n"
MF"v[4:7],v[204:207],a[208:211],v[4:7]\n"
BL"v[164:167],v224,s[8:11],0 offen offset:1280 sc1\n"
MF"v[12:15],v[204:207],a[212:215],v[12:15]\n"
BL"v237,s[12:15],s48 offen offset:3072 lds\n"
MF"v[12:15],v[200:203],a[196:199],v[12:15]\n"
RD"v[192:195],v242 offset:8192\n"
RT"a[226:227],v246 offset:8704\n"
MF"v[20:23],v[200:203],a[200:203],v[20:23]\n"
MF"v[20:23],v[204:207],a[216:219],v[20:23]\n"
MF"v[28:31],v[204:207],a[220:223],v[28:31]\n"
RT"a[228:229],v247 offset:8192\n"
MF"v[28:31],v[200:203],a[204:207],v[28:31]\n"
RD"v[196:199],v243 offset:8192\n"
MF"v[32:35],v[208:211],a[192:195],v[32:35]\n"
MF"v[32:35],v[212:215],a[208:211],v[32:35]\n"
RT"a[230:231],v247 offset:8704\n"
MF"v[40:43],v[212:215],a[212:215],v[40:43]\n"
RD"v[200:203],v242 offset:10240\n"
MF"v[40:43],v[208:211],a[196:199],v[40:43]\n"
MF"v[48:51],v[208:211],a[200:203],v[48:51]\n"
RT"a[232:233],v248 offset:8192\n"
MF"v[48:51],v[212:215],a[216:219],v[48:51]\n"
MF"v[56:59],v[212:215],a[220:223],v[56:59]\n"
RD"v[204:207],v243 offset:10240\n"
MF"v[56:59],v[208:211],a[204:207],v[56:59]\n"
WT"lgkmcnt(4)\n"
RT"a[234:235],v248 offset:8704\n"
MF"v[36:39],v[216:219],a[192:195],v[36:39]\n"
MF"v[36:39],v[220:223],a[208:211],v[36:39]\n"
RD"v[208:211],v242 offset:12288\n"
MF"v[44:47],v[220:223],a[212:215],v[44:47]\n"
RT"a[236:237],v249 offset:8192\n"
MF"v[44:47],v[216:219],a[196:199],v[44:47]\n"
MF"v[52:55],v[216:219],a[200:203],v[52:55]\n"
MF"v[52:55],v[220:223],a[216:219],v[52:55]\n"
RD"v[212:215],v243 offset:12288\n"
MF"v[60:63],v[220:223],a[220:223],v[60:63]\n"
RT"a[238:239],v249 offset:8704\n"
MF"v[60:63],v[216:219],a[204:207],v[60:63]\n"
MF"v[64:67],v[192:195],a[192:195],v[64:67]\n"
RD"v[216:219],v242 offset:14336\n"
MF"v[64:67],v[196:199],a[208:211],v[64:67]\n"
SA"m0,s37,8192\n"
WT"vmcnt(22)\n"
RT"a[240:241],v246 offset:12288\n"
MF"v[72:75],v[196:199],a[212:215],v[72:75]\n"
SA"s48,s39,65536\n"
MF"v[72:75],v[192:195],a[196:199],v[72:75]\n"
RT"a[242:243],v246 offset:12800\n"
MF"v[80:83],v[192:195],a[200:203],v[80:83]\n"
WT"lgkmcnt(3)\n"
BL"v234,s[12:15],s48 offen offset:0 lds\n"
MF"v[80:83],v[196:199],a[216:219],v[80:83]\n"
RD"v[220:223],v243 offset:14336\n"
MF"v[88:91],v[196:199],a[220:223],v[88:91]\n"
RT"a[244:245],v247 offset:12288\n"
MF"v[88:91],v[192:195],a[204:207],v[88:91]\n"
BL"v235,s[12:15],s48 offen offset:1024 lds\n"
MF"v[68:71],v[200:203],a[192:195],v[68:71]\n"
MF"v[68:71],v[204:207],a[208:211],v[68:71]\n"
RT"a[246:247],v247 offset:12800\n"
MF"v[76:79],v[204:207],a[212:215],v[76:79]\n"
MF"v[76:79],v[200:203],a[196:199],v[76:79]\n"
MF"v[84:87],v[200:203],a[200:203],v[84:87]\n"
RT"a[248:249],v248 offset:12288\n"
MF"v[84:87],v[204:207],a[216:219],v[84:87]\n"
BL"v236,s[12:15],s48 offen offset:2048 lds\n"
MF"v[92:95],v[204:207],a[220:223],v[92:95]\n"
WT"vmcnt(21) lgkmcnt(3)\n"
"s_barrier\n"
MF"v[92:95],v[200:203],a[204:207],v[92:95]\n"
RD"v[192:195],v242 offset:16384\n"
RT"a[250:251],v248 offset:12800\n"
MF"v[96:99],v[208:211],a[192:195],v[96:99]\n"
BL"v237,s[12:15],s48 offen offset:3072 lds\n"
MF"v[96:99],v[212:215],a[208:211],v[96:99]\n"
MF"v[104:107],v[212:215],a[212:215],v[104:107]\n"
RD"v[196:199],v243 offset:16384\n"
MF"v[104:107],v[208:211],a[196:199],v[104:107]\n"
RT"a[252:253],v249 offset:12288\n"
MF"v[112:115],v[208:211],a[200:203],v[112:115]\n"
MF"v[112:115],v[212:215],a[216:219],v[112:115]\n"
RT"a[254:255],v249 offset:12800\n"
MF"v[120:123],v[212:215],a[220:223],v[120:123]\n"
RD"v[200:203],v242 offset:18432\n"
MF"v[120:123],v[208:211],a[204:207],v[120:123]\n"
MF"v[100:103],v[216:219],a[192:195],v[100:103]\n"
"s_lshl_b32 s50,s35,20\n"
MF"v[100:103],v[220:223],a[208:211],v[100:103]\n"
"s_lshl_b32 s51,s34,15\n"
RD"v[204:207],v243 offset:18432\n"
SA"m0,s37,12288\n"
MF"v[108:111],v[220:223],a[212:215],v[108:111]\n"
SA"s48,s98,65536\n"
SA"s50,s50,s51\n"
MF"v[108:111],v[216:219],a[196:199],v[108:111]\n"
WT"lgkmcnt(2)\n"
BL"v234,s[12:15],s48 offen offset:0 lds\n"
MF"v[116:119],v[216:219],a[200:203],v[116:119]\n"
"s_and_b32 s51,s30,1\n"
"s_lshl_b32 s51,s51,12\n"
MF"v[116:119],v[220:223],a[216:219],v[116:119]\n"
SA"s50,s50,s51\n"
RD"v[208:211],v242 offset:20480\n"
MF"v[124:127],v[220:223],a[220:223],v[124:127]\n"
SA"s50,s50,9699328\n"
WT"vmcnt(6)\n"
"v_cmp_eq_u64 vcc,s[28:29],v[160:161]\n"
"v_cmp_eq_u64_e64 s[62:63],s[28:29],v[162:163]\n"
"s_and_b64 vcc,vcc,s[62:63]\n"
"v_cmp_eq_u64_e64 s[62:63],s[28:29],v[164:165]\n"
"s_and_b64 vcc,vcc,s[62:63]\n"
"v_cmp_eq_u64_e64 s[62:63],s[28:29],v[166:167]\n"
"s_and_b64 vcc,vcc,s[62:63]\n"
"s_cmp_eq_u64 vcc,exec\n"
"s_cbranch_scc0 L1004_%=\n"
"L1005_%=:\n"
MF"v[124:127],v[216:219],a[204:207],v[124:127]\n"
"s_mov_b32 s94,2686976\n"
BL"v235,s[12:15],s48 offen offset:1024 lds\n"
MF"v[0:3],v[192:195],a[224:227],v[0:3]\n"
"s_mov_b32 s95,2691072\n"
RD"v[212:215],v243 offset:20480\n"
"v_add_u32 v250,s50,v224\n"
MF"v[0:3],v[196:199],a[240:243],v[0:3]\n"
BL"v[160:163],v224,s[8:11],s94 offen offset:0 sc1\n"
MF"v[8:11],v[196:199],a[244:247],v[8:11]\n"
BL"v[164:167],v224,s[8:11],s94 offen offset:1024 sc1\n"
MF"v[8:11],v[192:195],a[228:231],v[8:11]\n"
MF"v[16:19],v[192:195],a[232:235],v[16:19]\n"
RD"v[216:219],v242 offset:22528\n"
MF"v[16:19],v[196:199],a[248:251],v[16:19]\n"
BL"v236,s[12:15],s48 offen offset:2048 lds\n"
MF"v[24:27],v[196:199],a[252:255],v[24:27]\n"
BL"v[168:171],v224,s[8:11],s94 offen offset:2048 sc1\n"
MF"v[24:27],v[192:195],a[236:239],v[24:27]\n"
WT"lgkmcnt(1)\n"
BL"v[172:175],v224,s[8:11],s94 offen offset:3072 sc1\n"
MF"v[4:7],v[200:203],a[224:227],v[4:7]\n"
RD"v[220:223],v243 offset:22528\n"
MF"v[4:7],v[204:207],a[240:243],v[4:7]\n"
BL"v237,s[12:15],s48 offen offset:3072 lds\n"
MF"v[12:15],v[204:207],a[244:247],v[12:15]\n"
BL"v[176:179],v224,s[8:11],s95 offen offset:0 sc1\n"
MF"v[12:15],v[200:203],a[228:231],v[12:15]\n"
BL"v[180:183],v224,s[8:11],s95 offen offset:1024 sc1\n"
MF"v[20:23],v[200:203],a[232:235],v[20:23]\n"
RD"v[192:195],v242 offset:24576\n"
MF"v[20:23],v[204:207],a[248:251],v[20:23]\n"
BL"v[184:187],v224,s[8:11],s95 offen offset:2048 sc1\n"
MF"v[28:31],v[204:207],a[252:255],v[28:31]\n"
BL"v[188:191],v224,s[8:11],s95 offen offset:3072 sc1\n"
MF"v[28:31],v[200:203],a[236:239],v[28:31]\n"
RD"v[196:199],v243 offset:24576\n"
MF"v[32:35],v[208:211],a[224:227],v[32:35]\n"
MF"v[32:35],v[212:215],a[240:243],v[32:35]\n"
MF"v[40:43],v[212:215],a[244:247],v[40:43]\n"
RD"v[200:203],v242 offset:26624\n"
MF"v[40:43],v[208:211],a[228:231],v[40:43]\n"
MF"v[48:51],v[208:211],a[232:235],v[48:51]\n"
MF"v[48:51],v[212:215],a[248:251],v[48:51]\n"
MF"v[56:59],v[212:215],a[252:255],v[56:59]\n"
RD"v[204:207],v243 offset:26624\n"
MF"v[56:59],v[208:211],a[236:239],v[56:59]\n"
WT"lgkmcnt(2)\n"
MF"v[36:39],v[216:219],a[224:227],v[36:39]\n"
MF"v[36:39],v[220:223],a[240:243],v[36:39]\n"
RD"v[208:211],v242 offset:28672\n"
MF"v[44:47],v[220:223],a[244:247],v[44:47]\n"
MF"v[44:47],v[216:219],a[228:231],v[44:47]\n"
MF"v[52:55],v[216:219],a[232:235],v[52:55]\n"
MF"v[52:55],v[220:223],a[248:251],v[52:55]\n"
RD"v[212:215],v243 offset:28672\n"
MF"v[60:63],v[220:223],a[252:255],v[60:63]\n"
MF"v[60:63],v[216:219],a[236:239],v[60:63]\n"
MF"v[64:67],v[192:195],a[224:227],v[64:67]\n"
RD"v[216:219],v242 offset:30720\n"
MF"v[64:67],v[196:199],a[240:243],v[64:67]\n"
MF"v[72:75],v[196:199],a[244:247],v[72:75]\n"
MF"v[72:75],v[192:195],a[228:231],v[72:75]\n"
MF"v[80:83],v[192:195],a[232:235],v[80:83]\n"
RD"v[220:223],v243 offset:30720\n"
MF"v[80:83],v[196:199],a[248:251],v[80:83]\n"
MF"v[88:91],v[196:199],a[252:255],v[88:91]\n"
MF"v[88:91],v[192:195],a[236:239],v[88:91]\n"
WT"lgkmcnt(1)\n"
MF"v[68:71],v[200:203],a[224:227],v[68:71]\n"
MF"v[68:71],v[204:207],a[240:243],v[68:71]\n"
MF"v[76:79],v[204:207],a[244:247],v[76:79]\n"
"v_add_u32 v244,114688,v242\n"
"v_add_u32 v245,114688,v243\n"
MF"v[76:79],v[200:203],a[228:231],v[76:79]\n"
"v_add_u32 v242,65536,v242\n"
"v_add_u32 v243,65536,v243\n"
MF"v[84:87],v[200:203],a[232:235],v[84:87]\n"
MF"v[84:87],v[204:207],a[248:251],v[84:87]\n"
WT"lgkmcnt(0)\n"
"s_barrier\n"
MF"v[92:95],v[204:207],a[252:255],v[92:95]\n"
RD"v[192:195],v242 offset:16384\n"
MF"v[92:95],v[200:203],a[236:239],v[92:95]\n"
MF"v[96:99],v[208:211],a[224:227],v[96:99]\n"
MF"v[96:99],v[212:215],a[240:243],v[96:99]\n"
MF"v[104:107],v[212:215],a[244:247],v[104:107]\n"
SA"m0,s36,49152\n"
RD"v[196:199],v243 offset:16384\n"
SA"s48,s60,s61\n"
MF"v[104:107],v[208:211],a[228:231],v[104:107]\n"
MF"v[112:115],v[208:211],a[232:235],v[112:115]\n"
BL"v238,s[20:23],s48 offen offset:0 lds\n"
MF"v[112:115],v[212:215],a[248:251],v[112:115]\n"
MF"v[120:123],v[212:215],a[252:255],v[120:123]\n"
RD"v[200:203],v242 offset:18432\n"
MF"v[120:123],v[208:211],a[236:239],v[120:123]\n"
MF"v[100:103],v[216:219],a[224:227],v[100:103]\n"
BL"v239,s[20:23],s48 offen offset:1024 lds\n"
MF"v[100:103],v[220:223],a[240:243],v[100:103]\n"
"s_cmp_eq_u32 s31,1\n"
"s_cselect_b32 s94,0,2130706432\n"
MF"v[108:111],v[220:223],a[244:247],v[108:111]\n"
"buffer_store_dwordx2 v[232:233],v230,s[8:11],s94 offen sc1\n"
MF"v[108:111],v[216:219],a[228:231],v[108:111]\n"
RD"v[204:207],v243 offset:18432\n"
"v_mov_b32 v227,0\n"
MF"v[116:119],v[216:219],a[232:235],v[116:119]\n"
WT"vmcnt(12)\n"
"v_mov_b32 v226,v160\n"
"v_mov_b32 v228,0\n"
MF"v[116:119],v[220:223],a[248:251],v[116:119]\n"
BL"v240,s[20:23],s48 offen offset:2048 lds\n"
MF"v[124:127],v[220:223],a[252:255],v[124:127]\n"
SA"s61,s61,524288\n"
RD"v[208:211],v242 offset:20480\n"
"v_med3_u32 v228,v227,v228,v161\n"
MF"v[124:127],v[216:219],a[236:239],v[124:127]\n"
WT"lgkmcnt(2)\n"
"v_med3_u32 v227,v226,v227,v161\n"
"v_max_u32 v226,v226,v161\n"
MF"v[128:131],v[192:195],a[0:3],0\n"
"v_med3_u32 v228,v227,v228,v162\n"
"v_med3_u32 v227,v226,v227,v162\n"
MF"v[128:131],v[196:199],a[16:19],v[128:131]\n"
RD"v[212:215],v243 offset:20480\n"
"v_max_u32 v226,v226,v162\n"
MF"v[136:139],v[196:199],a[20:23],0\n"
BL"v241,s[20:23],s48 offen offset:3072 lds\n"
MF"v[136:139],v[192:195],a[4:7],v[136:139]\n"
"v_med3_u32 v228,v227,v228,v163\n"
"v_med3_u32 v227,v226,v227,v163\n"
MF"v[144:147],v[192:195],a[8:11],0\n"
"v_max_u32 v226,v226,v163\n"
PK"v0,v0,v1\n"
MF"v[144:147],v[196:199],a[24:27],v[144:147]\n"
RD"v[216:219],v242 offset:22528\n"
PK"v1,v2,v3\n"
MF"v[152:155],v[196:199],a[28:31],0\n"
SA"m0,s36,32768\n"
WT"vmcnt(13)\n"
"v_med3_u32 v228,v227,v228,v164\n"
MF"v[152:155],v[192:195],a[12:15],v[152:155]\n"
SA"s48,s60,s61\n"
PK"v2,v4,v5\n"
PK"v3,v6,v7\n"
MF"v[132:135],v[200:203],a[0:3],0\n"
WT"lgkmcnt(1)\n"
BL"v238,s[20:23],s48 offen offset:0 lds\n"
MF"v[132:135],v[204:207],a[16:19],v[132:135]\n"
RD"v[220:223],v243 offset:22528\n"
"v_med3_u32 v227,v226,v227,v164\n"
MF"v[140:143],v[204:207],a[20:23],0\n"
"v_max_u32 v226,v226,v164\n"
"v_med3_u32 v228,v227,v228,v165\n"
MF"v[140:143],v[200:203],a[4:7],v[140:143]\n"
"v_med3_u32 v227,v226,v227,v165\n"
"v_max_u32 v226,v226,v165\n"
MF"v[148:151],v[200:203],a[8:11],0\n"
BL"v239,s[20:23],s48 offen offset:1024 lds\n"
MF"v[148:151],v[204:207],a[24:27],v[148:151]\n"
RD"v[192:195],v242 offset:40960\n"
"v_med3_u32 v228,v227,v228,v166\n"
MF"v[156:159],v[204:207],a[28:31],0\n"
PK"v8,v8,v9\n"
PK"v9,v10,v11\n"
MF"v[156:159],v[200:203],a[12:15],v[156:159]\n"
"v_med3_u32 v227,v226,v227,v166\n"
PK"v10,v12,v13\n"
MF"v[128:131],v[208:211],a[32:35],v[128:131]\n"
"s_mov_b32 s49,0\n"
PK"v11,v14,v15\n"
"v_max_u32 v226,v226,v166\n"
MF"v[128:131],v[212:215],a[48:51],v[128:131]\n"
RD"v[196:199],v243 offset:40960\n"
"v_med3_u32 v228,v227,v228,v167\n"
MF"v[136:139],v[212:215],a[52:55],v[136:139]\n"
SA"s61,s61,524288\n"
BL"v240,s[20:23],s48 offen offset:2048 lds\n"
MF"v[136:139],v[208:211],a[36:39],v[136:139]\n"
"s_cmp_eq_u32 s61,5242880\n"
BS"v[0:3],v250,s[8:11],s49 offen offset:0 sc1\n"
MF"v[144:147],v[208:211],a[40:43],v[144:147]\n"
"s_cselect_b32 s61,0,s61\n"
RD"v[200:203],v242 offset:43008\n"
"v_med3_u32 v227,v226,v227,v167\n"
MF"v[144:147],v[212:215],a[56:59],v[144:147]\n"
BL"v241,s[20:23],s48 offen offset:3072 lds\n"
MF"v[152:155],v[212:215],a[60:63],v[152:155]\n"
"v_max_u32 v226,v226,v167\n"
PK"v16,v16,v17\n"
MF"v[152:155],v[208:211],a[44:47],v[152:155]\n"
WT"lgkmcnt(2)\n"
WT"vmcnt(16)\n"
"v_med3_u32 v228,v227,v228,v168\n"
MF"v[132:135],v[216:219],a[32:35],v[132:135]\n"
RD"v[204:207],v243 offset:43008\n"
"v_med3_u32 v227,v226,v227,v168\n"
MF"v[132:135],v[220:223],a[48:51],v[132:135]\n"
PK"v17,v18,v19\n"
"v_max_u32 v226,v226,v168\n"
MF"v[140:143],v[220:223],a[52:55],v[140:143]\n"
"v_med3_u32 v228,v227,v228,v169\n"
PK"v18,v20,v21\n"
MF"v[140:143],v[216:219],a[36:39],v[140:143]\n"
PK"v19,v22,v23\n"
"v_med3_u32 v227,v226,v227,v169\n"
MF"v[148:151],v[216:219],a[40:43],v[148:151]\n"
RD"v[208:211],v242 offset:45056\n"
"v_max_u32 v226,v226,v169\n"
MF"v[148:151],v[220:223],a[56:59],v[148:151]\n"
"v_med3_u32 v228,v227,v228,v170\n"
"v_med3_u32 v227,v226,v227,v170\n"
MF"v[156:159],v[220:223],a[60:63],v[156:159]\n"
"v_max_u32 v226,v226,v170\n"
PK"v24,v24,v25\n"
MF"v[156:159],v[216:219],a[44:47],v[156:159]\n"
PK"v25,v26,v27\n"
"v_med3_u32 v228,v227,v228,v171\n"
MF"v[128:131],v[192:195],a[64:67],v[128:131]\n"
WT"lgkmcnt(1)\n"
RD"v[212:215],v243 offset:45056\n"
"v_med3_u32 v227,v226,v227,v171\n"
MF"v[128:131],v[196:199],a[80:83],v[128:131]\n"
PK"v26,v28,v29\n"
PK"v27,v30,v31\n"
MF"v[136:139],v[196:199],a[84:87],v[136:139]\n"
"v_max_u32 v226,v226,v171\n"
PK"v32,v32,v33\n"
MF"v[136:139],v[192:195],a[68:71],v[136:139]\n"
RD"v[216:219],v242 offset:47104\n"
PK"v33,v34,v35\n"
MF"v[144:147],v[192:195],a[72:75],v[144:147]\n"
WT"vmcnt(15)\n"
"v_med3_u32 v228,v227,v228,v172\n"
"v_med3_u32 v227,v226,v227,v172\n"
MF"v[144:147],v[196:199],a[88:91],v[144:147]\n"
"v_max_u32 v226,v226,v172\n"
"v_med3_u32 v228,v227,v228,v173\n"
MF"v[152:155],v[196:199],a[92:95],v[152:155]\n"
"v_med3_u32 v227,v226,v227,v173\n"
"v_max_u32 v226,v226,v173\n"
MF"v[152:155],v[192:195],a[76:79],v[152:155]\n"
RD"v[220:223],v243 offset:47104\n"
PK"v34,v36,v37\n"
MF"v[132:135],v[200:203],a[64:67],v[132:135]\n"
PK"v35,v38,v39\n"
"v_med3_u32 v228,v227,v228,v174\n"
MF"v[132:135],v[204:207],a[80:83],v[132:135]\n"
"v_med3_u32 v227,v226,v227,v174\n"
"v_max_u32 v226,v226,v174\n"
MF"v[140:143],v[204:207],a[84:87],v[140:143]\n"
"v_med3_u32 v228,v227,v228,v175\n"
"v_med3_u32 v227,v226,v227,v175\n"
MF"v[140:143],v[200:203],a[68:71],v[140:143]\n"
RD"v[192:195],v244 offset:16384\n"
"v_max_u32 v226,v226,v175\n"
MF"v[148:151],v[200:203],a[72:75],v[148:151]\n"
PK"v40,v40,v41\n"
PK"v41,v42,v43\n"
MF"v[148:151],v[204:207],a[88:91],v[148:151]\n"
WT"vmcnt(13)\n"
"v_med3_u32 v228,v227,v228,v176\n"
PK"v42,v44,v45\n"
MF"v[156:159],v[204:207],a[92:95],v[156:159]\n"
PK"v43,v46,v47\n"
"v_med3_u32 v227,v226,v227,v176\n"
MF"v[156:159],v[200:203],a[76:79],v[156:159]\n"
WT"lgkmcnt(1)\n"
RD"v[196:199],v245 offset:16384\n"
"v_max_u32 v226,v226,v176\n"
MF"v[128:131],v[208:211],a[96:99],v[128:131]\n"
"v_med3_u32 v228,v227,v228,v177\n"
"v_med3_u32 v227,v226,v227,v177\n"
MF"v[128:131],v[212:215],a[112:115],v[128:131]\n"
BS"v[8:11],v250,s[8:11],s49 offen offset:1024 sc1\n"
MF"v[136:139],v[212:215],a[116:119],v[136:139]\n"
"v_max_u32 v226,v226,v177\n"
"v_med3_u32 v228,v227,v228,v178\n"
MF"v[136:139],v[208:211],a[100:103],v[136:139]\n"
RD"v[200:203],v244 offset:18432\n"
PK"v48,v48,v49\n"
MF"v[144:147],v[208:211],a[104:107],v[144:147]\n"
PK"v49,v50,v51\n"
"v_med3_u32 v227,v226,v227,v178\n"
MF"v[144:147],v[212:215],a[120:123],v[144:147]\n"
PK"v50,v52,v53\n"
PK"v51,v54,v55\n"
MF"v[152:155],v[212:215],a[124:127],v[152:155]\n"
RD"v[204:207],v245 offset:18432\n"
"v_max_u32 v226,v226,v178\n"
MF"v[152:155],v[208:211],a[108:111],v[152:155]\n"
"v_med3_u32 v228,v227,v228,v179\n"
"v_med3_u32 v227,v226,v227,v179\n"
MF"v[132:135],v[216:219],a[96:99],v[132:135]\n"
"v_max_u32 v226,v226,v179\n"
PK"v56,v56,v57\n"
MF"v[132:135],v[220:223],a[112:115],v[132:135]\n"
WT"vmcnt(13)\n"
"v_med3_u32 v228,v227,v228,v180\n"
"v_med3_u32 v227,v226,v227,v180\n"
MF"v[140:143],v[220:223],a[116:119],v[140:143]\n"
RD"v[208:211],v244 offset:20480\n"
PK"v57,v58,v59\n"
MF"v[140:143],v[216:219],a[100:103],v[140:143]\n"
"v_max_u32 v226,v226,v180\n"
PK"v58,v60,v61\n"
MF"v[148:151],v[216:219],a[104:107],v[148:151]\n"
PK"v59,v62,v63\n"
"v_med3_u32 v228,v227,v228,v181\n"
MF"v[148:151],v[220:223],a[120:123],v[148:151]\n"
"v_med3_u32 v227,v226,v227,v181\n"
"v_max_u32 v226,v226,v181\n"
MF"v[156:159],v[220:223],a[124:127],v[156:159]\n"
RD"v[212:215],v245 offset:20480\n"
"v_med3_u32 v228,v227,v228,v182\n"
MF"v[156:159],v[216:219],a[108:111],v[156:159]\n"
WT"lgkmcnt(2)\n"
"v_med3_u32 v227,v226,v227,v182\n"
"v_max_u32 v226,v226,v182\n"
MF"v[128:131],v[192:195],a[128:131],v[128:131]\n"
PK"v64,v64,v65\n"
PK"v65,v66,v67\n"
MF"v[128:131],v[196:199],a[144:147],v[128:131]\n"
"v_med3_u32 v228,v227,v228,v183\n"
PK"v66,v68,v69\n"
MF"v[136:139],v[196:199],a[148:151],v[136:139]\n"
RD"v[216:219],v244 offset:22528\n"
PK"v67,v70,v71\n"
MF"v[136:139],v[192:195],a[132:135],v[136:139]\n"
"v_med3_u32 v227,v226,v227,v183\n"
"v_max_u32 v226,v226,v183\n"
MF"v[144:147],v[192:195],a[136:139],v[144:147]\n"
WT"vmcnt(12)\n"
"v_med3_u32 v228,v227,v228,v184\n"
"v_med3_u32 v227,v226,v227,v184\n"
MF"v[144:147],v[196:199],a[152:155],v[144:147]\n"
RD"v[220:223],v245 offset:22528\n"
"v_max_u32 v226,v226,v184\n"
MF"v[152:155],v[196:199],a[156:159],v[152:155]\n"
"v_med3_u32 v228,v227,v228,v185\n"
PK"v72,v72,v73\n"
MF"v[152:155],v[192:195],a[140:143],v[152:155]\n"
PK"v73,v74,v75\n"
"v_med3_u32 v227,v226,v227,v185\n"
MF"v[132:135],v[200:203],a[128:131],v[132:135]\n"
"v_max_u32 v226,v226,v185\n"
PK"v74,v76,v77\n"
MF"v[132:135],v[204:207],a[144:147],v[132:135]\n"
RD"v[192:195],v244 offset:40960\n"
PK"v75,v78,v79\n"
MF"v[140:143],v[204:207],a[148:151],v[140:143]\n"
"v_med3_u32 v228,v227,v228,v186\n"
"v_med3_u32 v227,v226,v227,v186\n"
MF"v[140:143],v[200:203],a[132:135],v[140:143]\n"
"v_max_u32 v226,v226,v186\n"
"v_med3_u32 v228,v227,v228,v187\n"
MF"v[148:151],v[200:203],a[136:139],v[148:151]\n"
"v_med3_u32 v227,v226,v227,v187\n"
PK"v80,v80,v81\n"
MF"v[148:151],v[204:207],a[152:155],v[148:151]\n"
RD"v[196:199],v245 offset:40960\n"
PK"v81,v82,v83\n"
MF"v[156:159],v[204:207],a[156:159],v[156:159]\n"
"v_max_u32 v226,v226,v187\n"
PK"v82,v84,v85\n"
MF"v[156:159],v[200:203],a[140:143],v[156:159]\n"
WT"lgkmcnt(2)\n"
WT"vmcnt(11)\n"
"v_med3_u32 v228,v227,v228,v188\n"
MF"v[128:131],v[208:211],a[160:163],v[128:131]\n"
BS"v[16:19],v250,s[8:11],s49 offen offset:2048 sc1\n"
MF"v[128:131],v[212:215],a[176:179],v[128:131]\n"
RD"v[200:203],v244 offset:43008\n"
PK"v83,v86,v87\n"
MF"v[136:139],v[212:215],a[180:183],v[136:139]\n"
"v_med3_u32 v227,v226,v227,v188\n"
"v_max_u32 v226,v226,v188\n"
MF"v[136:139],v[208:211],a[164:167],v[136:139]\n"
"v_med3_u32 v228,v227,v228,v189\n"
"v_med3_u32 v227,v226,v227,v189\n"
MF"v[144:147],v[208:211],a[168:171],v[144:147]\n"
"v_max_u32 v226,v226,v189\n"
PK"v88,v88,v89\n"
MF"v[144:147],v[212:215],a[184:187],v[144:147]\n"
RD"v[204:207],v245 offset:43008\n"
PK"v89,v90,v91\n"
MF"v[152:155],v[212:215],a[188:191],v[152:155]\n"
"v_med3_u32 v228,v227,v228,v190\n"
"v_med3_u32 v227,v226,v227,v190\n"
MF"v[152:155],v[208:211],a[172:175],v[152:155]\n"
PK"v90,v92,v93\n"
PK"v91,v94,v95\n"
MF"v[132:135],v[216:219],a[160:163],v[132:135]\n"
RD"v[208:211],v244 offset:45056\n"
"v_max_u32 v226,v226,v190\n"
MF"v[132:135],v[220:223],a[176:179],v[132:135]\n"
"v_med3_u32 v228,v227,v228,v191\n"
"v_med3_u32 v227,v226,v227,v191\n"
MF"v[140:143],v[220:223],a[180:183],v[140:143]\n"
"v_max_u32 v226,v226,v191\n"
"v_mov_b32 v229,v226\n"
MF"v[140:143],v[216:219],a[164:167],v[140:143]\n"
MF"v[148:151],v[216:219],a[168:171],v[148:151]\n"
RD"v[212:215],v245 offset:45056\n"
"v_max_u32_dpp v229,v229,v229 row_shr:1 row_mask:0xf bank_mask:0xf\n"
MF"v[148:151],v[220:223],a[184:187],v[148:151]\n"
MF"v[156:159],v[220:223],a[188:191],v[156:159]\n"
"v_max_u32_dpp v229,v229,v229 row_shr:2 row_mask:0xf bank_mask:0xf\n"
MF"v[156:159],v[216:219],a[172:175],v[156:159]\n"
WT"lgkmcnt(1)\n"
"v_max_u32_dpp v229,v229,v229 row_shr:4 row_mask:0xf bank_mask:0xf\n"
MF"v[128:131],v[192:195],a[192:195],v[128:131]\n"
RD"v[216:219],v244 offset:47104\n"
"v_max_u32_dpp v229,v229,v229 row_shr:8 row_mask:0xf bank_mask:0xf\n"
MF"v[128:131],v[196:199],a[208:211],v[128:131]\n"
MF"v[136:139],v[196:199],a[212:215],v[136:139]\n"
"v_max_u32_dpp v229,v229,v229 row_bcast:15 row_mask:0xa bank_mask:0xf\n"
MF"v[136:139],v[192:195],a[196:199],v[136:139]\n"
MF"v[144:147],v[192:195],a[200:203],v[144:147]\n"
RD"v[220:223],v245 offset:47104\n"
"v_max_u32_dpp v229,v229,v229 row_bcast:31 row_mask:0xc bank_mask:0xf\n"
MF"v[144:147],v[196:199],a[216:219],v[144:147]\n"
"v_readlane_b32 s62,v229,63\n"
MF"v[152:155],v[196:199],a[220:223],v[152:155]\n"
MF"v[152:155],v[192:195],a[204:207],v[152:155]\n"
"v_cmp_eq_u32 vcc,s62,v226\n"
"v_writelane_b32 v225,s62,0\n"
MF"v[132:135],v[200:203],a[192:195],v[132:135]\n"
"v_cndmask_b32 v226,v226,v227,vcc\n"
"v_cndmask_b32_e64 v227,v227,0,vcc\n"
MF"v[132:135],v[204:207],a[208:211],v[132:135]\n"
"v_mov_b32 v229,v226\n"
MF"v[140:143],v[204:207],a[212:215],v[140:143]\n"
MF"v[140:143],v[200:203],a[196:199],v[140:143]\n"
"v_max_u32_dpp v229,v229,v229 row_shr:1 row_mask:0xf bank_mask:0xf\n"
MF"v[148:151],v[200:203],a[200:203],v[148:151]\n"
MF"v[148:151],v[204:207],a[216:219],v[148:151]\n"
WT"lgkmcnt(0)\n"
"s_barrier\n"
MF"v[156:159],v[204:207],a[220:223],v[156:159]\n"
RD"v[192:195],v242 offset:0\n"
"v_max_u32_dpp v229,v229,v229 row_shr:2 row_mask:0xf bank_mask:0xf\n"
MF"v[156:159],v[200:203],a[204:207],v[156:159]\n"
BS"v[24:27],v250,s[8:11],s49 offen offset:3072 sc1\n"
MF"v[128:131],v[208:211],a[224:227],v[128:131]\n"
"v_max_u32_dpp v229,v229,v229 row_shr:4 row_mask:0xf bank_mask:0xf\n"
MF"v[128:131],v[212:215],a[240:243],v[128:131]\n"
MF"v[136:139],v[212:215],a[244:247],v[136:139]\n"
RD"v[196:199],v243 offset:0\n"
"v_max_u32_dpp v229,v229,v229 row_shr:8 row_mask:0xf bank_mask:0xf\n"
MF"v[136:139],v[208:211],a[228:231],v[136:139]\n"
MF"v[144:147],v[208:211],a[232:235],v[144:147]\n"
"v_max_u32_dpp v229,v229,v229 row_bcast:15 row_mask:0xa bank_mask:0xf\n"
MF"v[144:147],v[212:215],a[248:251],v[144:147]\n"
MF"v[152:155],v[212:215],a[252:255],v[152:155]\n"
RD"v[200:203],v242 offset:2048\n"
"v_max_u32_dpp v229,v229,v229 row_bcast:31 row_mask:0xc bank_mask:0xf\n"
MF"v[152:155],v[208:211],a[236:239],v[152:155]\n"
"v_readlane_b32 s62,v229,63\n"
MF"v[132:135],v[216:219],a[224:227],v[132:135]\n"
MF"v[132:135],v[220:223],a[240:243],v[132:135]\n"
"v_cmp_eq_u32 vcc,s62,v226\n"
"v_writelane_b32 v225,s62,1\n"
MF"v[140:143],v[220:223],a[244:247],v[140:143]\n"
RD"v[204:207],v243 offset:2048\n"
"v_cndmask_b32 v226,v226,v227,vcc\n"
MF"v[140:143],v[216:219],a[228:231],v[140:143]\n"
"v_cndmask_b32_e64 v227,v227,0,vcc\n"
"v_mov_b32 v229,v226\n"
MF"v[148:151],v[216:219],a[232:235],v[148:151]\n"
MF"v[148:151],v[220:223],a[248:251],v[148:151]\n"
"v_max_u32_dpp v229,v229,v229 row_shr:1 row_mask:0xf bank_mask:0xf\n"
MF"v[156:159],v[220:223],a[252:255],v[156:159]\n"
RD"v[208:211],v242 offset:4096\n"
"v_max_u32_dpp v229,v229,v229 row_shr:2 row_mask:0xf bank_mask:0xf\n"
MF"v[156:159],v[216:219],a[236:239],v[156:159]\n"
WT"lgkmcnt(2)\n"
"v_max_u32_dpp v229,v229,v229 row_shr:4 row_mask:0xf bank_mask:0xf\n"
MF"v[36:39],v[192:195],a[0:3],0\n"
MF"v[36:39],v[196:199],a[16:19],v[36:39]\n"
RD"v[212:215],v243 offset:4096\n"
"v_max_u32_dpp v229,v229,v229 row_shr:8 row_mask:0xf bank_mask:0xf\n"
MF"v[44:47],v[196:199],a[20:23],0\n"
MF"v[44:47],v[192:195],a[4:7],v[44:47]\n"
"v_max_u32_dpp v229,v229,v229 row_bcast:15 row_mask:0xa bank_mask:0xf\n"
PK"v4,v128,v129\n"
MF"v[52:55],v[192:195],a[8:11],0\n"
"v_max_u32_dpp v229,v229,v229 row_bcast:31 row_mask:0xc bank_mask:0xf\n"
PK"v5,v130,v131\n"
MF"v[52:55],v[196:199],a[24:27],v[52:55]\n"
RD"v[216:219],v242 offset:6144\n"
"v_readlane_b32 s62,v229,63\n"
MF"v[60:63],v[196:199],a[28:31],0\n"
PK"v6,v132,v133\n"
"v_cmp_eq_u32 vcc,s62,v226\n"
MF"v[60:63],v[192:195],a[12:15],v[60:63]\n"
"v_writelane_b32 v225,s62,2\n"
"v_cndmask_b32 v226,v226,v227,vcc\n"
MF"v[68:71],v[200:203],a[0:3],0\n"
WT"lgkmcnt(1)\n"
"v_cndmask_b32_e64 v227,v227,0,vcc\n"
"v_mov_b32 v229,v226\n"
MF"v[68:71],v[204:207],a[16:19],v[68:71]\n"
RD"v[220:223],v243 offset:6144\n"
"v_max_u32_dpp v229,v229,v229 row_shr:1 row_mask:0xf bank_mask:0xf\n"
MF"v[76:79],v[204:207],a[20:23],0\n"
PK"v7,v134,v135\n"
"v_max_u32_dpp v229,v229,v229 row_shr:2 row_mask:0xf bank_mask:0xf\n"
MF"v[76:79],v[200:203],a[4:7],v[76:79]\n"
PK"v12,v136,v137\n"
"v_max_u32_dpp v229,v229,v229 row_shr:4 row_mask:0xf bank_mask:0xf\n"
MF"v[84:87],v[200:203],a[8:11],0\n"
"s_mov_b32 s49,8192\n"
"v_max_u32_dpp v229,v229,v229 row_shr:8 row_mask:0xf bank_mask:0xf\n"
PK"v13,v138,v139\n"
MF"v[84:87],v[204:207],a[24:27],v[84:87]\n"
RD"v[192:195],v242 offset:24576\n"
"v_max_u32_dpp v229,v229,v229 row_bcast:15 row_mask:0xa bank_mask:0xf\n"
MF"v[92:95],v[204:207],a[28:31],0\n"
BS"v[32:35],v250,s[8:11],s49 offen offset:0 sc1\n"
MF"v[92:95],v[200:203],a[12:15],v[92:95]\n"
"v_max_u32_dpp v229,v229,v229 row_bcast:31 row_mask:0xc bank_mask:0xf\n"
PK"v14,v140,v141\n"
MF"v[36:39],v[208:211],a[32:35],v[36:39]\n"
"v_readlane_b32 s62,v229,63\n"
PK"v15,v142,v143\n"
MF"v[36:39],v[212:215],a[48:51],v[36:39]\n"
RD"v[196:199],v243 offset:24576\n"
"v_cmp_eq_u32 vcc,s62,v226\n"
MF"v[44:47],v[212:215],a[52:55],v[44:47]\n"
"v_writelane_b32 v225,s62,3\n"
"v_cndmask_b32 v226,v226,v227,vcc\n"
MF"v[44:47],v[208:211],a[36:39],v[44:47]\n"
"v_cndmask_b32_e64 v227,v227,0,vcc\n"
"v_mov_b32 v229,v226\n"
MF"v[52:55],v[208:211],a[40:43],v[52:55]\n"
RD"v[200:203],v242 offset:26624\n"
"v_max_u32_dpp v229,v229,v229 row_shr:1 row_mask:0xf bank_mask:0xf\n"
MF"v[52:55],v[212:215],a[56:59],v[52:55]\n"
PK"v20,v144,v145\n"
"v_max_u32_dpp v229,v229,v229 row_shr:2 row_mask:0xf bank_mask:0xf\n"
MF"v[60:63],v[212:215],a[60:63],v[60:63]\n"
PK"v21,v146,v147\n"
"v_max_u32_dpp v229,v229,v229 row_shr:4 row_mask:0xf bank_mask:0xf\n"
MF"v[60:63],v[208:211],a[44:47],v[60:63]\n"
WT"lgkmcnt(2)\n"
"v_max_u32_dpp v229,v229,v229 row_shr:8 row_mask:0xf bank_mask:0xf\n"
PK"v22,v148,v149\n"
MF"v[68:71],v[216:219],a[32:35],v[68:71]\n"
RD"v[204:207],v243 offset:26624\n"
"v_max_u32_dpp v229,v229,v229 row_bcast:15 row_mask:0xa bank_mask:0xf\n"
MF"v[68:71],v[220:223],a[48:51],v[68:71]\n"
PK"v23,v150,v151\n"
"v_max_u32_dpp v229,v229,v229 row_bcast:31 row_mask:0xc bank_mask:0xf\n"
MF"v[76:79],v[220:223],a[52:55],v[76:79]\n"
"v_readlane_b32 s62,v229,63\n"
PK"v28,v152,v153\n"
MF"v[76:79],v[216:219],a[36:39],v[76:79]\n"
"v_cmp_eq_u32 vcc,s62,v226\n"
"v_writelane_b32 v225,s62,4\n"
MF"v[84:87],v[216:219],a[40:43],v[84:87]\n"
RD"v[208:211],v242 offset:28672\n"
"v_cndmask_b32 v226,v226,v227,vcc\n"
MF"v[84:87],v[220:223],a[56:59],v[84:87]\n"
"v_cndmask_b32_e64 v227,v227,0,vcc\n"
"v_mov_b32 v229,v226\n"
MF"v[92:95],v[220:223],a[60:63],v[92:95]\n"
PK"v29,v154,v155\n"
"v_max_u32_dpp v229,v229,v229 row_shr:1 row_mask:0xf bank_mask:0xf\n"
MF"v[92:95],v[216:219],a[44:47],v[92:95]\n"
PK"v30,v156,v157\n"
"v_max_u32_dpp v229,v229,v229 row_shr:2 row_mask:0xf bank_mask:0xf\n"
MF"v[36:39],v[192:195],a[64:67],v[36:39]\n"
WT"lgkmcnt(1)\n"
RD"v[212:215],v243 offset:28672\n"
"v_max_u32_dpp v229,v229,v229 row_shr:4 row_mask:0xf bank_mask:0xf\n"
MF"v[36:39],v[196:199],a[80:83],v[36:39]\n"
PK"v31,v158,v159\n"
"v_max_u32_dpp v229,v229,v229 row_shr:8 row_mask:0xf bank_mask:0xf\n"
MF"v[44:47],v[196:199],a[84:87],v[44:47]\n"
MF"v[44:47],v[192:195],a[68:71],v[44:47]\n"
RD"v[216:219],v242 offset:30720\n"
"v_max_u32_dpp v229,v229,v229 row_bcast:15 row_mask:0xa bank_mask:0xf\n"
MF"v[52:55],v[192:195],a[72:75],v[52:55]\n"
MF"v[52:55],v[196:199],a[88:91],v[52:55]\n"
"v_max_u32_dpp v229,v229,v229 row_bcast:31 row_mask:0xc bank_mask:0xf\n"
MF"v[60:63],v[196:199],a[92:95],v[60:63]\n"
"v_readlane_b32 s62,v229,63\n"
MF"v[60:63],v[192:195],a[76:79],v[60:63]\n"
RD"v[220:223],v243 offset:30720\n"
"v_cmp_eq_u32 vcc,s62,v226\n"
MF"v[68:71],v[200:203],a[64:67],v[68:71]\n"
"v_writelane_b32 v225,s62,5\n"
"v_cndmask_b32 v226,v226,v227,vcc\n"
MF"v[68:71],v[204:207],a[80:83],v[68:71]\n"
"v_cndmask_b32_e64 v227,v227,0,vcc\n"
"v_mov_b32 v229,v226\n"
MF"v[76:79],v[204:207],a[84:87],v[76:79]\n"
MF"v[76:79],v[200:203],a[68:71],v[76:79]\n"
RD"v[192:195],v244 offset:0\n"
"v_max_u32_dpp v229,v229,v229 row_shr:1 row_mask:0xf bank_mask:0xf\n"
MF"v[84:87],v[200:203],a[72:75],v[84:87]\n"
MF"v[84:87],v[204:207],a[88:91],v[84:87]\n"
BS"v[40:43],v250,s[8:11],s49 offen offset:1024 sc1\n"
MF"v[92:95],v[204:207],a[92:95],v[92:95]\n"
"v_max_u32_dpp v229,v229,v229 row_shr:2 row_mask:0xf bank_mask:0xf\n"
MF"v[92:95],v[200:203],a[76:79],v[92:95]\n"
WT"lgkmcnt(1)\n"
RD"v[196:199],v245 offset:0\n"
"v_max_u32_dpp v229,v229,v229 row_shr:4 row_mask:0xf bank_mask:0xf\n"
MF"v[36:39],v[208:211],a[96:99],v[36:39]\n"
MF"v[36:39],v[212:215],a[112:115],v[36:39]\n"
"v_max_u32_dpp v229,v229,v229 row_shr:8 row_mask:0xf bank_mask:0xf\n"
MF"v[44:47],v[212:215],a[116:119],v[44:47]\n"
MF"v[44:47],v[208:211],a[100:103],v[44:47]\n"
RD"v[200:203],v244 offset:2048\n"
"v_max_u32_dpp v229,v229,v229 row_bcast:15 row_mask:0xa bank_mask:0xf\n"
MF"v[52:55],v[208:211],a[104:107],v[52:55]\n"
MF"v[52:55],v[212:215],a[120:123],v[52:55]\n"
"v_max_u32_dpp v229,v229,v229 row_bcast:31 row_mask:0xc bank_mask:0xf\n"
MF"v[60:63],v[212:215],a[124:127],v[60:63]\n"
RD"v[204:207],v245 offset:2048\n"
"v_readlane_b32 s62,v229,63\n"
MF"v[60:63],v[208:211],a[108:111],v[60:63]\n"
MF"v[68:71],v[216:219],a[96:99],v[68:71]\n"
"v_cmp_eq_u32 vcc,s62,v226\n"
"v_writelane_b32 v225,s62,6\n"
MF"v[68:71],v[220:223],a[112:115],v[68:71]\n"
"v_cndmask_b32 v226,v226,v227,vcc\n"
"v_cndmask_b32_e64 v227,v227,0,vcc\n"
MF"v[76:79],v[220:223],a[116:119],v[76:79]\n"
SA"m0,s37,16384\n"
RD"v[208:211],v244 offset:4096\n"
"v_mov_b32 v229,v226\n"
MF"v[76:79],v[216:219],a[100:103],v[76:79]\n"
SA"s48,s39,98304\n"
"v_max_u32_dpp v229,v229,v229 row_shr:1 row_mask:0xf bank_mask:0xf\n"
MF"v[84:87],v[216:219],a[104:107],v[84:87]\n"
BL"v234,s[12:15],s48 offen offset:0 lds\n"
MF"v[84:87],v[220:223],a[120:123],v[84:87]\n"
"v_max_u32_dpp v229,v229,v229 row_shr:2 row_mask:0xf bank_mask:0xf\n"
PK"v96,v96,v97\n"
MF"v[92:95],v[220:223],a[124:127],v[92:95]\n"
RD"v[212:215],v245 offset:4096\n"
"v_max_u32_dpp v229,v229,v229 row_shr:4 row_mask:0xf bank_mask:0xf\n"
MF"v[92:95],v[216:219],a[108:111],v[92:95]\n"
WT"lgkmcnt(2)\n"
"v_max_u32_dpp v229,v229,v229 row_shr:8 row_mask:0xf bank_mask:0xf\n"
PK"v97,v98,v99\n"
MF"v[36:39],v[192:195],a[128:131],v[36:39]\n"
"v_max_u32_dpp v229,v229,v229 row_bcast:15 row_mask:0xa bank_mask:0xf\n"
PK"v98,v100,v101\n"
MF"v[36:39],v[196:199],a[144:147],v[36:39]\n"
"v_max_u32_dpp v229,v229,v229 row_bcast:31 row_mask:0xc bank_mask:0xf\n"
PK"v99,v102,v103\n"
MF"v[44:47],v[196:199],a[148:151],v[44:47]\n"
RD"v[216:219],v244 offset:6144\n"
"v_readlane_b32 s62,v229,63\n"
MF"v[44:47],v[192:195],a[132:135],v[44:47]\n"
BL"v235,s[12:15],s48 offen offset:1024 lds\n"
MF"v[52:55],v[192:195],a[136:139],v[52:55]\n"
"v_cmp_eq_u32 vcc,s62,v226\n"
"v_writelane_b32 v225,s62,7\n"
MF"v[52:55],v[196:199],a[152:155],v[52:55]\n"
RD"v[220:223],v245 offset:6144\n"
"v_cndmask_b32 v226,v226,v227,vcc\n"
MF"v[60:63],v[196:199],a[156:159],v[60:63]\n"
"v_cndmask_b32_e64 v227,v227,0,vcc\n"
"v_mov_b32 v229,v228\n"
MF"v[60:63],v[192:195],a[140:143],v[60:63]\n"
PK"v104,v104,v105\n"
"v_max_u32_dpp v229,v229,v229 row_shr:1 row_mask:0xf bank_mask:0xf\n"
MF"v[68:71],v[200:203],a[128:131],v[68:71]\n"
BL"v236,s[12:15],s48 offen offset:2048 lds\n"
MF"v[68:71],v[204:207],a[144:147],v[68:71]\n"
RD"v[192:195],v244 offset:24576\n"
"v_max_u32_dpp v229,v229,v229 row_shr:2 row_mask:0xf bank_mask:0xf\n"
MF"v[76:79],v[204:207],a[148:151],v[76:79]\n"
PK"v105,v106,v107\n"
"v_max_u32_dpp v229,v229,v229 row_shr:4 row_mask:0xf bank_mask:0xf\n"
MF"v[76:79],v[200:203],a[132:135],v[76:79]\n"
PK"v106,v108,v109\n"
"v_max_u32_dpp v229,v229,v229 row_shr:8 row_mask:0xf bank_mask:0xf\n"
MF"v[84:87],v[200:203],a[136:139],v[84:87]\n"
BS"v[48:51],v250,s[8:11],s49 offen offset:2048 sc1\n"
MF"v[84:87],v[204:207],a[152:155],v[84:87]\n"
RD"v[196:199],v245 offset:24576\n"
"v_max_u32_dpp v229,v229,v229 row_bcast:15 row_mask:0xa bank_mask:0xf\n"
MF"v[92:95],v[204:207],a[156:159],v[92:95]\n"
BL"v237,s[12:15],s48 offen offset:3072 lds\n"
MF"v[92:95],v[200:203],a[140:143],v[92:95]\n"
WT"lgkmcnt(2)\n"
"v_max_u32_dpp v229,v229,v229 row_bcast:31 row_mask:0xc bank_mask:0xf\n"
PK"v107,v110,v111\n"
MF"v[36:39],v[208:211],a[160:163],v[36:39]\n"
"v_readlane_b32 s63,v229,63\n"
PK"v112,v112,v113\n"
MF"v[36:39],v[212:215],a[176:179],v[36:39]\n"
"s_cmp_gt_u32 s63,s62\n"
"s_cbranch_scc1 L1007_%=\n"
"L1008_%=:\n"
MF"v[44:47],v[212:215],a[180:183],v[44:47]\n"
SA"m0,s37,20480\n"
RD"v[200:203],v244 offset:26624\n"
"v_lshrrev_b32 v229,2,v224\n"
MF"v[44:47],v[208:211],a[164:167],v[44:47]\n"
SA"s48,s98,98304\n"
"ds_write_b32 v229,v225 offset:16384\n"
MF"v[52:55],v[208:211],a[168:171],v[52:55]\n"
BL"v234,s[12:15],s48 offen offset:0 lds\n"
MF"v[52:55],v[212:215],a[184:187],v[52:55]\n"
RD"v[204:207],v245 offset:26624\n"
PK"v113,v114,v115\n"
MF"v[60:63],v[212:215],a[188:191],v[60:63]\n"
PK"v114,v116,v117\n"
PK"v115,v118,v119\n"
MF"v[60:63],v[208:211],a[172:175],v[60:63]\n"
PK"v120,v120,v121\n"
PK"v121,v122,v123\n"
MF"v[68:71],v[216:219],a[160:163],v[68:71]\n"
RD"v[208:211],v244 offset:28672\n"
PK"v122,v124,v125\n"
MF"v[68:71],v[220:223],a[176:179],v[68:71]\n"
PK"v123,v126,v127\n"
MF"v[76:79],v[220:223],a[180:183],v[76:79]\n"
BL"v235,s[12:15],s48 offen offset:1024 lds\n"
MF"v[76:79],v[216:219],a[164:167],v[76:79]\n"
MF"v[84:87],v[216:219],a[168:171],v[84:87]\n"
RD"v[212:215],v245 offset:28672\n"
MF"v[84:87],v[220:223],a[184:187],v[84:87]\n"
MF"v[92:95],v[220:223],a[188:191],v[92:95]\n"
MF"v[92:95],v[216:219],a[172:175],v[92:95]\n"
WT"lgkmcnt(1)\n"
MF"v[36:39],v[192:195],a[192:195],v[36:39]\n"
RD"v[216:219],v244 offset:30720\n"
MF"v[36:39],v[196:199],a[208:211],v[36:39]\n"
BL"v236,s[12:15],s48 offen offset:2048 lds\n"
MF"v[44:47],v[196:199],a[212:215],v[44:47]\n"
MF"v[44:47],v[192:195],a[196:199],v[44:47]\n"
MF"v[52:55],v[192:195],a[200:203],v[52:55]\n"
RD"v[220:223],v245 offset:30720\n"
MF"v[52:55],v[196:199],a[216:219],v[52:55]\n"
MF"v[60:63],v[196:199],a[220:223],v[60:63]\n"
BL"v237,s[12:15],s48 offen offset:3072 lds\n"
MF"v[60:63],v[192:195],a[204:207],v[60:63]\n"
MF"v[68:71],v[200:203],a[192:195],v[68:71]\n"
MF"v[68:71],v[204:207],a[208:211],v[68:71]\n"
MF"v[76:79],v[204:207],a[212:215],v[76:79]\n"
MF"v[76:79],v[200:203],a[196:199],v[76:79]\n"
BS"v[56:59],v250,s[8:11],s49 offen offset:3072 sc1\n"
MF"v[84:87],v[200:203],a[200:203],v[84:87]\n"
"v_lshrrev_b32 v251,2,v224\n"
"v_and_b32 v251,28,v251\n"
MF"v[84:87],v[204:207],a[216:219],v[84:87]\n"
WT"lgkmcnt(0)\n"
"s_barrier\n"
MF"v[92:95],v[204:207],a[220:223],v[92:95]\n"
RD"v[192:195],v242 offset:8192\n"
"ds_read_b32 v234,v251 offset:16384\n"
MF"v[92:95],v[200:203],a[204:207],v[92:95]\n"
MF"v[36:39],v[208:211],a[224:227],v[36:39]\n"
MF"v[36:39],v[212:215],a[240:243],v[36:39]\n"
MF"v[44:47],v[212:215],a[244:247],v[44:47]\n"
RD"v[196:199],v243 offset:8192\n"
MF"v[44:47],v[208:211],a[228:231],v[44:47]\n"
MF"v[52:55],v[208:211],a[232:235],v[52:55]\n"
MF"v[52:55],v[212:215],a[248:251],v[52:55]\n"
MF"v[60:63],v[212:215],a[252:255],v[60:63]\n"
RD"v[200:203],v242 offset:10240\n"
MF"v[60:63],v[208:211],a[236:239],v[60:63]\n"
WT"lgkmcnt(2)\n"
"v_not_b32 v251,v234\n"
"v_bfe_u32 v252,v251,4,7\n"
MF"v[68:71],v[216:219],a[224:227],v[68:71]\n"
"v_mul_u32_u24 v252,20480,v252\n"
MF"v[68:71],v[220:223],a[240:243],v[68:71]\n"
"v_and_b32 v251,15,v251\n"
MF"v[76:79],v[220:223],a[244:247],v[76:79]\n"
RD"v[204:207],v243 offset:10240\n"
"v_lshl_add_u32 v251,v251,4,v252\n"
MF"v[76:79],v[216:219],a[228:231],v[76:79]\n"
"v_and_b32 v252,0x380,v224\n"
MF"v[84:87],v[216:219],a[232:235],v[84:87]\n"
"v_lshl_add_u32 v251,v252,1,v251\n"
MF"v[84:87],v[220:223],a[248:251],v[84:87]\n"
"v_add_u32 v252,v252,v224\n"
MF"v[92:95],v[220:223],a[252:255],v[92:95]\n"
"s_lshl_b32 s92,s34,2\n"
RD"v[208:211],v242 offset:12288\n"
MF"v[92:95],v[216:219],a[236:239],v[92:95]\n"
SA"s92,s92,s30\n"
MF"v[128:131],v[192:195],a[0:3],0\n"
WT"lgkmcnt(2)\n"
"s_lshl_b32 s93,s92,11\n"
MF"v[128:131],v[196:199],a[16:19],v[128:131]\n"
"s_cmp_lt_u32 s92,10\n"
RD"v[212:215],v243 offset:12288\n"
"s_cselect_b32 s93,s93,2130706432\n"
MF"v[136:139],v[196:199],a[20:23],0\n"
SA"s92,s93,65536\n"
MF"v[136:139],v[192:195],a[4:7],v[136:139]\n"
BL"v[100:103],v251,s[8:11],s92 offen sc1\n"
MF"v[144:147],v[192:195],a[8:11],0\n"
"s_mul_i32 s92,s35,20480\n"
PK"v36,v36,v37\n"
PK"v37,v38,v39\n"
MF"v[144:147],v[196:199],a[24:27],v[144:147]\n"
SA"s92,s92,s93\n"
RD"v[216:219],v242 offset:14336\n"
PK"v38,v68,v69\n"
MF"v[152:155],v[196:199],a[28:31],0\n"
SA"s92,s92,2770944\n"
PK"v39,v70,v71\n"
MF"v[152:155],v[192:195],a[12:15],v[152:155]\n"
MF"v[132:135],v[200:203],a[0:3],0\n"
WT"lgkmcnt(1)\n"
PK"v44,v44,v45\n"
PK"v45,v46,v47\n"
MF"v[132:135],v[204:207],a[16:19],v[132:135]\n"
"s_mov_b32 s49,16384\n"
RD"v[220:223],v243 offset:14336\n"
PK"v46,v76,v77\n"
MF"v[140:143],v[204:207],a[20:23],0\n"
BS"v[64:67],v250,s[8:11],s49 offen offset:0 sc1\n"
MF"v[140:143],v[200:203],a[4:7],v[140:143]\n"
PK"v47,v78,v79\n"
MF"v[148:151],v[200:203],a[8:11],0\n"
MF"v[148:151],v[204:207],a[24:27],v[148:151]\n"
RD"v[192:195],v242 offset:32768\n"
PK"v52,v52,v53\n"
MF"v[156:159],v[204:207],a[28:31],0\n"
PK"v53,v54,v55\n"
PK"v54,v84,v85\n"
MF"v[156:159],v[200:203],a[12:15],v[156:159]\n"
PK"v55,v86,v87\n"
MF"v[128:131],v[208:211],a[32:35],v[128:131]\n"
MF"v[128:131],v[212:215],a[48:51],v[128:131]\n"
RD"v[196:199],v243 offset:32768\n"
MF"v[136:139],v[212:215],a[52:55],v[136:139]\n"
PK"v60,v60,v61\n"
PK"v61,v62,v63\n"
MF"v[136:139],v[208:211],a[36:39],v[136:139]\n"
PK"v62,v92,v93\n"
PK"v63,v94,v95\n"
MF"v[144:147],v[208:211],a[40:43],v[144:147]\n"
SA"m0,s36,0\n"
RD"v[200:203],v242 offset:34816\n"
SA"s48,s60,s61\n"
MF"v[144:147],v[212:215],a[56:59],v[144:147]\n"
MF"v[152:155],v[212:215],a[60:63],v[152:155]\n"
BL"v238,s[20:23],s48 offen offset:0 lds\n"
MF"v[152:155],v[208:211],a[44:47],v[152:155]\n"
WT"lgkmcnt(2)\n"
MF"v[132:135],v[216:219],a[32:35],v[132:135]\n"
RD"v[204:207],v243 offset:34816\n"
MF"v[132:135],v[220:223],a[48:51],v[132:135]\n"
MF"v[140:143],v[220:223],a[52:55],v[140:143]\n"
MF"v[140:143],v[216:219],a[36:39],v[140:143]\n"
MF"v[148:151],v[216:219],a[40:43],v[148:151]\n"
RD"v[208:211],v242 offset:36864\n"
MF"v[148:151],v[220:223],a[56:59],v[148:151]\n"
MF"v[156:159],v[220:223],a[60:63],v[156:159]\n"
BL"v239,s[20:23],s48 offen offset:1024 lds\n"
MF"v[156:159],v[216:219],a[44:47],v[156:159]\n"
MF"v[128:131],v[192:195],a[64:67],v[128:131]\n"
WT"lgkmcnt(1)\n"
RD"v[212:215],v243 offset:36864\n"
MF"v[128:131],v[196:199],a[80:83],v[128:131]\n"
MF"v[136:139],v[196:199],a[84:87],v[136:139]\n"
MF"v[136:139],v[192:195],a[68:71],v[136:139]\n"
RD"v[216:219],v242 offset:38912\n"
MF"v[144:147],v[192:195],a[72:75],v[144:147]\n"
MF"v[144:147],v[196:199],a[88:91],v[144:147]\n"
MF"v[152:155],v[196:199],a[92:95],v[152:155]\n"
BL"v240,s[20:23],s48 offen offset:2048 lds\n"
MF"v[152:155],v[192:195],a[76:79],v[152:155]\n"
RD"v[220:223],v243 offset:38912\n"
MF"v[132:135],v[200:203],a[64:67],v[132:135]\n"
MF"v[132:135],v[204:207],a[80:83],v[132:135]\n"
BS"v[72:75],v250,s[8:11],s49 offen offset:1024 sc1\n"
MF"v[140:143],v[204:207],a[84:87],v[140:143]\n"
MF"v[140:143],v[200:203],a[68:71],v[140:143]\n"
RD"v[192:195],v244 offset:8192\n"
MF"v[148:151],v[200:203],a[72:75],v[148:151]\n"
MF"v[148:151],v[204:207],a[88:91],v[148:151]\n"
MF"v[156:159],v[204:207],a[92:95],v[156:159]\n"
SA"s61,s61,524288\n"
BL"v241,s[20:23],s48 offen offset:3072 lds\n"
MF"v[156:159],v[200:203],a[76:79],v[156:159]\n"
WT"lgkmcnt(1)\n"
RD"v[196:199],v245 offset:8192\n"
MF"v[128:131],v[208:211],a[96:99],v[128:131]\n"
MF"v[128:131],v[212:215],a[112:115],v[128:131]\n"
MF"v[136:139],v[212:215],a[116:119],v[136:139]\n"
MF"v[136:139],v[208:211],a[100:103],v[136:139]\n"
RD"v[200:203],v244 offset:10240\n"
MF"v[144:147],v[208:211],a[104:107],v[144:147]\n"
MF"v[144:147],v[212:215],a[120:123],v[144:147]\n"
MF"v[152:155],v[212:215],a[124:127],v[152:155]\n"
RD"v[204:207],v245 offset:10240\n"
MF"v[152:155],v[208:211],a[108:111],v[152:155]\n"
MF"v[132:135],v[216:219],a[96:99],v[132:135]\n"
MF"v[132:135],v[220:223],a[112:115],v[132:135]\n"
MF"v[140:143],v[220:223],a[116:119],v[140:143]\n"
RD"v[208:211],v244 offset:12288\n"
MF"v[140:143],v[216:219],a[100:103],v[140:143]\n"
MF"v[148:151],v[216:219],a[104:107],v[148:151]\n"
SA"m0,s38,65536\n"
SA"s48,s60,s61\n"
MF"v[148:151],v[220:223],a[120:123],v[148:151]\n"
MF"v[156:159],v[220:223],a[124:127],v[156:159]\n"
RD"v[212:215],v245 offset:12288\n"
MF"v[156:159],v[216:219],a[108:111],v[156:159]\n"
WT"lgkmcnt(2)\n"
BL"v238,s[20:23],s48 offen offset:0 lds\n"
MF"v[128:131],v[192:195],a[128:131],v[128:131]\n"
MF"v[128:131],v[196:199],a[144:147],v[128:131]\n"
MF"v[136:139],v[196:199],a[148:151],v[136:139]\n"
RD"v[216:219],v244 offset:14336\n"
MF"v[136:139],v[192:195],a[132:135],v[136:139]\n"
MF"v[144:147],v[192:195],a[136:139],v[144:147]\n"
MF"v[144:147],v[196:199],a[152:155],v[144:147]\n"
RD"v[220:223],v245 offset:14336\n"
MF"v[152:155],v[196:199],a[156:159],v[152:155]\n"
BL"v239,s[20:23],s48 offen offset:1024 lds\n"
MF"v[152:155],v[192:195],a[140:143],v[152:155]\n"
MF"v[132:135],v[200:203],a[128:131],v[132:135]\n"
BS"v[80:83],v250,s[8:11],s49 offen offset:2048 sc1\n"
MF"v[132:135],v[204:207],a[144:147],v[132:135]\n"
RD"v[192:195],v244 offset:32768\n"
MF"v[140:143],v[204:207],a[148:151],v[140:143]\n"
MF"v[140:143],v[200:203],a[132:135],v[140:143]\n"
MF"v[148:151],v[200:203],a[136:139],v[148:151]\n"
MF"v[148:151],v[204:207],a[152:155],v[148:151]\n"
RD"v[196:199],v245 offset:32768\n"
MF"v[156:159],v[204:207],a[156:159],v[156:159]\n"
BL"v240,s[20:23],s48 offen offset:2048 lds\n"
MF"v[156:159],v[200:203],a[140:143],v[156:159]\n"
WT"lgkmcnt(2)\n"
MF"v[128:131],v[208:211],a[160:163],v[128:131]\n"
MF"v[128:131],v[212:215],a[176:179],v[128:131]\n"
RD"v[200:203],v244 offset:34816\n"
MF"v[136:139],v[212:215],a[180:183],v[136:139]\n"
MF"v[136:139],v[208:211],a[164:167],v[136:139]\n"
MF"v[144:147],v[208:211],a[168:171],v[144:147]\n"
MF"v[144:147],v[212:215],a[184:187],v[144:147]\n"
RD"v[204:207],v245 offset:34816\n"
MF"v[152:155],v[212:215],a[188:191],v[152:155]\n"
SA"s61,s61,524288\n"
BL"v241,s[20:23],s48 offen offset:3072 lds\n"
MF"v[152:155],v[208:211],a[172:175],v[152:155]\n"
"s_cmp_eq_u32 s61,5242880\n"
"s_cselect_b32 s61,0,s61\n"
MF"v[132:135],v[216:219],a[160:163],v[132:135]\n"
RD"v[208:211],v244 offset:36864\n"
MF"v[132:135],v[220:223],a[176:179],v[132:135]\n"
MF"v[140:143],v[220:223],a[180:183],v[140:143]\n"
MF"v[140:143],v[216:219],a[164:167],v[140:143]\n"
MF"v[148:151],v[216:219],a[168:171],v[148:151]\n"
SA"m0,s38,114688\n"
RD"v[212:215],v245 offset:36864\n"
SA"s48,s60,s61\n"
MF"v[148:151],v[220:223],a[184:187],v[148:151]\n"
MF"v[156:159],v[220:223],a[188:191],v[156:159]\n"
BL"v238,s[20:23],s48 offen offset:0 lds\n"
MF"v[156:159],v[216:219],a[172:175],v[156:159]\n"
WT"lgkmcnt(1)\n"
MF"v[128:131],v[192:195],a[192:195],v[128:131]\n"
RD"v[216:219],v244 offset:38912\n"
MF"v[128:131],v[196:199],a[208:211],v[128:131]\n"
MF"v[136:139],v[196:199],a[212:215],v[136:139]\n"
MF"v[136:139],v[192:195],a[196:199],v[136:139]\n"
MF"v[144:147],v[192:195],a[200:203],v[144:147]\n"
RD"v[220:223],v245 offset:38912\n"
MF"v[144:147],v[196:199],a[216:219],v[144:147]\n"
MF"v[152:155],v[196:199],a[220:223],v[152:155]\n"
BL"v239,s[20:23],s48 offen offset:1024 lds\n"
MF"v[152:155],v[192:195],a[204:207],v[152:155]\n"
BS"v[88:91],v250,s[8:11],s49 offen offset:3072 sc1\n"
MF"v[132:135],v[200:203],a[192:195],v[132:135]\n"
WT"vmcnt(14)\n"
BS"v[100:103],v252,s[8:11],s92 offen sc1\n"
MF"v[132:135],v[204:207],a[208:211],v[132:135]\n"
MF"v[140:143],v[204:207],a[212:215],v[140:143]\n"
MF"v[140:143],v[200:203],a[196:199],v[140:143]\n"
MF"v[148:151],v[200:203],a[200:203],v[148:151]\n"
MF"v[148:151],v[204:207],a[216:219],v[148:151]\n"
WT"lgkmcnt(0)\n"
"s_barrier\n"
MF"v[156:159],v[204:207],a[220:223],v[156:159]\n"
RD"v[192:195],v242 offset:16384\n"
MF"v[156:159],v[200:203],a[204:207],v[156:159]\n"
BL"v240,s[20:23],s48 offen offset:2048 lds\n"
MF"v[128:131],v[208:211],a[224:227],v[128:131]\n"
MF"v[128:131],v[212:215],a[240:243],v[128:131]\n"
MF"v[136:139],v[212:215],a[244:247],v[136:139]\n"
RD"v[196:199],v243 offset:16384\n"
MF"v[136:139],v[208:211],a[228:231],v[136:139]\n"
MF"v[144:147],v[208:211],a[232:235],v[144:147]\n"
MF"v[144:147],v[212:215],a[248:251],v[144:147]\n"
MF"v[152:155],v[212:215],a[252:255],v[152:155]\n"
SA"s61,s61,524288\n"
RD"v[200:203],v242 offset:18432\n"
MF"v[152:155],v[208:211],a[236:239],v[152:155]\n"
BL"v241,s[20:23],s48 offen offset:3072 lds\n"
MF"v[132:135],v[216:219],a[224:227],v[132:135]\n"
"s_lshl_b32 s50,s34,3\n"
MF"v[132:135],v[220:223],a[240:243],v[132:135]\n"
SA"s51,s50,s30\n"
MF"v[140:143],v[220:223],a[244:247],v[140:143]\n"
"s_mul_i32 s51,s51,20480\n"
RD"v[204:207],v243 offset:18432\n"
MF"v[140:143],v[216:219],a[228:231],v[140:143]\n"
SA"s56,s51,65536\n"
MF"v[148:151],v[216:219],a[232:235],v[148:151]\n"
SA"m0,s36,16384\n"
SA"s48,s60,s61\n"
SA"s57,s56,81920\n"
MF"v[148:151],v[220:223],a[248:251],v[148:151]\n"
"s_sub_u32 s51,s30,s50\n"
MF"v[156:159],v[220:223],a[252:255],v[156:159]\n"
SA"s51,s51,120\n"
RD"v[208:211],v242 offset:20480\n"
MF"v[156:159],v[216:219],a[236:239],v[156:159]\n"
WT"lgkmcnt(2)\n"
BL"v238,s[20:23],s48 offen offset:0 lds\n"
MF"v[0:3],v[192:195],a[0:3],0\n"
"s_mul_i32 s51,s51,20480\n"
SA"s58,s51,65536\n"
"s_lshl_b32 s99,s99,12\n"
MF"v[0:3],v[196:199],a[16:19],v[0:3]\n"
SA"s59,s58,81920\n"
RD"v[212:215],v243 offset:20480\n"
MF"v[16:19],v[196:199],a[20:23],0\n"
"v_add_u32 v235,s99,v224\n"
MF"v[16:19],v[192:195],a[4:7],v[16:19]\n"
PK"v64,v128,v129\n"
PK"v65,v130,v131\n"
MF"v[32:35],v[192:195],a[8:11],0\n"
"s_mov_b32 s49,24576\n"
PK"v66,v132,v133\n"
PK"v67,v134,v135\n"
MF"v[32:35],v[196:199],a[24:27],v[32:35]\n"
RD"v[216:219],v242 offset:22528\n"
MF"v[48:51],v[196:199],a[28:31],0\n"
BS"v[96:99],v250,s[8:11],s49 offen offset:0 sc1\n"
MF"v[48:51],v[192:195],a[12:15],v[48:51]\n"
BL"v239,s[20:23],s48 offen offset:1024 lds\n"
MF"v[8:11],v[200:203],a[0:3],0\n"
WT"lgkmcnt(1)\n"
PK"v68,v136,v137\n"
PK"v69,v138,v139\n"
MF"v[8:11],v[204:207],a[16:19],v[8:11]\n"
RD"v[220:223],v243 offset:22528\n"
PK"v70,v140,v141\n"
MF"v[24:27],v[204:207],a[20:23],0\n"
PK"v71,v142,v143\n"
MF"v[24:27],v[200:203],a[4:7],v[24:27]\n"
MF"v[40:43],v[200:203],a[8:11],0\n"
MF"v[40:43],v[204:207],a[24:27],v[40:43]\n"
RD"v[192:195],v242 offset:40960\n"
PK"v72,v144,v145\n"
MF"v[56:59],v[204:207],a[28:31],0\n"
BL"v240,s[20:23],s48 offen offset:2048 lds\n"
MF"v[56:59],v[200:203],a[12:15],v[56:59]\n"
PK"v73,v146,v147\n"
PK"v74,v148,v149\n"
MF"v[0:3],v[208:211],a[32:35],v[0:3]\n"
BL"a[0:3],v235,s[8:11],s56 offen offset:0 sc1\n"
MF"v[0:3],v[212:215],a[48:51],v[0:3]\n"
RD"v[196:199],v243 offset:40960\n"
PK"v75,v150,v151\n"
MF"v[16:19],v[212:215],a[52:55],v[16:19]\n"
PK"v76,v152,v153\n"
PK"v77,v154,v155\n"
MF"v[16:19],v[208:211],a[36:39],v[16:19]\n"
PK"v78,v156,v157\n"
PK"v79,v158,v159\n"
MF"v[32:35],v[208:211],a[40:43],v[32:35]\n"
RD"v[200:203],v242 offset:43008\n"
MF"v[32:35],v[212:215],a[56:59],v[32:35]\n"
MF"v[48:51],v[212:215],a[60:63],v[48:51]\n"
SA"s61,s61,524288\n"
BL"v241,s[20:23],s48 offen offset:3072 lds\n"
MF"v[48:51],v[208:211],a[44:47],v[48:51]\n"
WT"lgkmcnt(2)\n"
BL"a[4:7],v235,s[8:11],s57 offen offset:0 sc1\n"
MF"v[8:11],v[216:219],a[32:35],v[8:11]\n"
"s_cmp_eq_u32 s61,5242880\n"
RD"v[204:207],v243 offset:43008\n"
"s_cselect_b32 s61,0,s61\n"
MF"v[8:11],v[220:223],a[48:51],v[8:11]\n"
MF"v[24:27],v[220:223],a[52:55],v[24:27]\n"
MF"v[24:27],v[216:219],a[36:39],v[24:27]\n"
MF"v[40:43],v[216:219],a[40:43],v[40:43]\n"
RD"v[208:211],v242 offset:45056\n"
MF"v[40:43],v[220:223],a[56:59],v[40:43]\n"
BL"a[8:11],v235,s[8:11],s58 offen offset:0 sc1\n"
MF"v[56:59],v[220:223],a[60:63],v[56:59]\n"
MF"v[56:59],v[216:219],a[44:47],v[56:59]\n"
MF"v[0:3],v[192:195],a[64:67],v[0:3]\n"
WT"lgkmcnt(1)\n"
RD"v[212:215],v243 offset:45056\n"
MF"v[0:3],v[196:199],a[80:83],v[0:3]\n"
MF"v[16:19],v[196:199],a[84:87],v[16:19]\n"
BL"a[12:15],v235,s[8:11],s59 offen offset:0 sc1\n"
MF"v[16:19],v[192:195],a[68:71],v[16:19]\n"
RD"v[216:219],v242 offset:47104\n"
MF"v[32:35],v[192:195],a[72:75],v[32:35]\n"
MF"v[32:35],v[196:199],a[88:91],v[32:35]\n"
BS"v[104:107],v250,s[8:11],s49 offen offset:1024 sc1\n"
MF"v[48:51],v[196:199],a[92:95],v[48:51]\n"
MF"v[48:51],v[192:195],a[76:79],v[48:51]\n"
RD"v[220:223],v243 offset:47104\n"
MF"v[8:11],v[200:203],a[64:67],v[8:11]\n"
BL"a[16:19],v235,s[8:11],s56 offen offset:1024 sc1\n"
MF"v[8:11],v[204:207],a[80:83],v[8:11]\n"
MF"v[24:27],v[204:207],a[84:87],v[24:27]\n"
MF"v[24:27],v[200:203],a[68:71],v[24:27]\n"
RD"v[192:195],v244 offset:16384\n"
MF"v[40:43],v[200:203],a[72:75],v[40:43]\n"
MF"v[40:43],v[204:207],a[88:91],v[40:43]\n"
MF"v[56:59],v[204:207],a[92:95],v[56:59]\n"
BL"a[20:23],v235,s[8:11],s57 offen offset:1024 sc1\n"
MF"v[56:59],v[200:203],a[76:79],v[56:59]\n"
WT"lgkmcnt(1)\n"
RD"v[196:199],v245 offset:16384\n"
MF"v[0:3],v[208:211],a[96:99],v[0:3]\n"
MF"v[0:3],v[212:215],a[112:115],v[0:3]\n"
MF"v[16:19],v[212:215],a[116:119],v[16:19]\n"
MF"v[16:19],v[208:211],a[100:103],v[16:19]\n"
RD"v[200:203],v244 offset:18432\n"
MF"v[32:35],v[208:211],a[104:107],v[32:35]\n"
BL"a[24:27],v235,s[8:11],s58 offen offset:1024 sc1\n"
MF"v[32:35],v[212:215],a[120:123],v[32:35]\n"
MF"v[48:51],v[212:215],a[124:127],v[48:51]\n"
RD"v[204:207],v245 offset:18432\n"
MF"v[48:51],v[208:211],a[108:111],v[48:51]\n"
MF"v[8:11],v[216:219],a[96:99],v[8:11]\n"
MF"v[8:11],v[220:223],a[112:115],v[8:11]\n"
MF"v[24:27],v[220:223],a[116:119],v[24:27]\n"
RD"v[208:211],v244 offset:20480\n"
MF"v[24:27],v[216:219],a[100:103],v[24:27]\n"
BL"a[28:31],v235,s[8:11],s59 offen offset:1024 sc1\n"
MF"v[40:43],v[216:219],a[104:107],v[40:43]\n"
MF"v[40:43],v[220:223],a[120:123],v[40:43]\n"
MF"v[56:59],v[220:223],a[124:127],v[56:59]\n"
RD"v[212:215],v245 offset:20480\n"
MF"v[56:59],v[216:219],a[108:111],v[56:59]\n"
WT"lgkmcnt(2)\n"
MF"v[0:3],v[192:195],a[128:131],v[0:3]\n"
BL"a[32:35],v235,s[8:11],s56 offen offset:2048 sc1\n"
MF"v[0:3],v[196:199],a[144:147],v[0:3]\n"
MF"v[16:19],v[196:199],a[148:151],v[16:19]\n"
RD"v[216:219],v244 offset:22528\n"
MF"v[16:19],v[192:195],a[132:135],v[16:19]\n"
MF"v[32:35],v[192:195],a[136:139],v[32:35]\n"
BS"v[112:115],v250,s[8:11],s49 offen offset:2048 sc1\n"
MF"v[32:35],v[196:199],a[152:155],v[32:35]\n"
RD"v[220:223],v245 offset:22528\n"
MF"v[48:51],v[196:199],a[156:159],v[48:51]\n"
BL"a[36:39],v235,s[8:11],s57 offen offset:2048 sc1\n"
MF"v[48:51],v[192:195],a[140:143],v[48:51]\n"
MF"v[8:11],v[200:203],a[128:131],v[8:11]\n"
MF"v[8:11],v[204:207],a[144:147],v[8:11]\n"
RD"v[192:195],v244 offset:40960\n"
MF"v[24:27],v[204:207],a[148:151],v[24:27]\n"
MF"v[24:27],v[200:203],a[132:135],v[24:27]\n"
MF"v[40:43],v[200:203],a[136:139],v[40:43]\n"
BL"a[40:43],v235,s[8:11],s58 offen offset:2048 sc1\n"
MF"v[40:43],v[204:207],a[152:155],v[40:43]\n"
RD"v[196:199],v245 offset:40960\n"
MF"v[56:59],v[204:207],a[156:159],v[56:59]\n"
MF"v[56:59],v[200:203],a[140:143],v[56:59]\n"
WT"lgkmcnt(2)\n"
MF"v[0:3],v[208:211],a[160:163],v[0:3]\n"
"s_sub_u32 s53,s37,65536\n"
MF"v[0:3],v[212:215],a[176:179],v[0:3]\n"
RD"v[200:203],v244 offset:43008\n"
MF"v[16:19],v[212:215],a[180:183],v[16:19]\n"
BL"a[44:47],v235,s[8:11],s59 offen offset:2048 sc1\n"
MF"v[16:19],v[208:211],a[164:167],v[16:19]\n"
"v_subrev_u32 v246,s53,v246\n"
"v_subrev_u32 v247,s53,v247\n"
MF"v[32:35],v[208:211],a[168:171],v[32:35]\n"
"v_subrev_u32 v248,s53,v248\n"
"v_subrev_u32 v249,s53,v249\n"
MF"v[32:35],v[212:215],a[184:187],v[32:35]\n"
RD"v[204:207],v245 offset:43008\n"
"v_subrev_u32 v236,65536,v246\n"
MF"v[48:51],v[212:215],a[188:191],v[48:51]\n"
"v_subrev_u32 v237,65536,v247\n"
"v_subrev_u32 v228,65536,v248\n"
MF"v[48:51],v[208:211],a[172:175],v[48:51]\n"
"v_subrev_u32 v229,65536,v249\n"
MF"v[8:11],v[216:219],a[160:163],v[8:11]\n"
RD"v[208:211],v244 offset:45056\n"
MF"v[8:11],v[220:223],a[176:179],v[8:11]\n"
BL"a[48:51],v235,s[8:11],s56 offen offset:3072 sc1\n"
MF"v[24:27],v[220:223],a[180:183],v[24:27]\n"
MF"v[24:27],v[216:219],a[164:167],v[24:27]\n"
MF"v[40:43],v[216:219],a[168:171],v[40:43]\n"
RD"v[212:215],v245 offset:45056\n"
MF"v[40:43],v[220:223],a[184:187],v[40:43]\n"
MF"v[56:59],v[220:223],a[188:191],v[56:59]\n"
BL"a[52:55],v235,s[8:11],s57 offen offset:3072 sc1\n"
MF"v[56:59],v[216:219],a[172:175],v[56:59]\n"
WT"lgkmcnt(1)\n"
MF"v[0:3],v[192:195],a[192:195],v[0:3]\n"
RD"v[216:219],v244 offset:47104\n"
MF"v[0:3],v[196:199],a[208:211],v[0:3]\n"
MF"v[16:19],v[196:199],a[212:215],v[16:19]\n"
MF"v[16:19],v[192:195],a[196:199],v[16:19]\n"
BS"v[120:123],v250,s[8:11],s49 offen offset:3072 sc1\n"
MF"v[32:35],v[192:195],a[200:203],v[32:35]\n"
RD"v[220:223],v245 offset:47104\n"
MF"v[32:35],v[196:199],a[216:219],v[32:35]\n"
BL"a[56:59],v235,s[8:11],s58 offen offset:3072 sc1\n"
MF"v[48:51],v[196:199],a[220:223],v[48:51]\n"
MF"v[48:51],v[192:195],a[204:207],v[48:51]\n"
MF"v[8:11],v[200:203],a[192:195],v[8:11]\n"
SA"s99,s99,4096\n"
MF"v[8:11],v[204:207],a[208:211],v[8:11]\n"
"s_cmp_eq_u32 s99,20480\n"
"s_cselect_b32 s99,0,s99\n"
MF"v[24:27],v[204:207],a[212:215],v[24:27]\n"
BL"a[60:63],v235,s[8:11],s59 offen offset:3072 sc1\n"
MF"v[24:27],v[200:203],a[196:199],v[24:27]\n"
MF"v[40:43],v[200:203],a[200:203],v[40:43]\n"
MF"v[40:43],v[204:207],a[216:219],v[40:43]\n"
WT"lgkmcnt(0)\n"
"s_barrier\n"
MF"v[56:59],v[204:207],a[220:223],v[56:59]\n"
RT"a[128:129],v236 offset:49152\n"
MF"v[56:59],v[200:203],a[204:207],v[56:59]\n"
"v_add_u32 v235,s99,v224\n"
MF"v[0:3],v[208:211],a[224:227],v[0:3]\n"
SA"m0,s38,73728\n"
RT"a[130:131],v236 offset:49664\n"
SA"s48,s60,s61\n"
MF"v[0:3],v[212:215],a[240:243],v[0:3]\n"
BL"a[64:67],v235,s[8:11],s56 offen offset:0 sc1\n"
MF"v[16:19],v[212:215],a[244:247],v[16:19]\n"
RT"a[132:133],v236 offset:57344\n"
"v_add_u32 v242,49152,v246\n"
MF"v[16:19],v[208:211],a[228:231],v[16:19]\n"
BL"v238,s[20:23],s48 offen offset:0 lds\n"
MF"v[32:35],v[208:211],a[232:235],v[32:35]\n"
RT"a[134:135],v236 offset:57856\n"
"v_add_u32 v243,49152,v247\n"
MF"v[32:35],v[212:215],a[248:251],v[32:35]\n"
"v_add_u32 v244,49152,v248\n"
"v_add_u32 v245,49152,v249\n"
MF"v[48:51],v[212:215],a[252:255],v[48:51]\n"
RT"a[136:137],v237 offset:49152\n"
MF"v[48:51],v[208:211],a[236:239],v[48:51]\n"
BL"a[68:71],v235,s[8:11],s57 offen offset:0 sc1\n"
MF"v[8:11],v[216:219],a[224:227],v[8:11]\n"
RT"a[138:139],v237 offset:49664\n"
MF"v[8:11],v[220:223],a[240:243],v[8:11]\n"
MF"v[24:27],v[220:223],a[244:247],v[24:27]\n"
RT"a[140:141],v237 offset:57344\n"
MF"v[24:27],v[216:219],a[228:231],v[24:27]\n"
BL"v239,s[20:23],s48 offen offset:1024 lds\n"
MF"v[40:43],v[216:219],a[232:235],v[40:43]\n"
RT"a[142:143],v237 offset:57856\n"
MF"v[40:43],v[220:223],a[248:251],v[40:43]\n"
BL"a[72:75],v235,s[8:11],s58 offen offset:0 sc1\n"
MF"v[56:59],v[220:223],a[252:255],v[56:59]\n"
RT"a[144:145],v228 offset:49152\n"
MF"v[56:59],v[216:219],a[236:239],v[56:59]\n"
WT"vmcnt(24) lgkmcnt(3)\n"
MF"v[80:83],a[128:131],a[0:3],0\n"
WT"vmcnt(18)\n"
RT"a[146:147],v228 offset:49664\n"
MF"v[80:83],a[132:135],a[16:19],v[80:83]\n"
WT"vmcnt(17)\n"
MF"v[88:91],a[132:135],a[20:23],0\n"
RT"a[148:149],v228 offset:57344\n"
MF"v[88:91],a[128:131],a[4:7],v[88:91]\n"
BL"v240,s[20:23],s48 offen offset:2048 lds\n"
MF"v[96:99],a[128:131],a[8:11],0\n"
WT"vmcnt(17)\n"
BL"a[76:79],v235,s[8:11],s59 offen offset:0 sc1\n"
MF"v[96:99],a[132:135],a[24:27],v[96:99]\n"
WT"vmcnt(17)\n"
RT"a[150:151],v228 offset:57856\n"
RT"a[152:153],v229 offset:49152\n"
MF"v[104:107],a[132:135],a[28:31],0\n"
PK"v0,v0,v1\n"
PK"v1,v2,v3\n"
MF"v[104:107],a[128:131],a[12:15],v[104:107]\n"
RT"a[154:155],v229 offset:49664\n"
PK"v2,v8,v9\n"
MF"v[84:87],a[136:139],a[0:3],0\n"
WT"lgkmcnt(4)\n"
PK"v3,v10,v11\n"
PK"v16,v16,v17\n"
MF"v[84:87],a[140:143],a[16:19],v[84:87]\n"
RT"a[156:157],v229 offset:57344\n"
PK"v17,v18,v19\n"
MF"v[92:95],a[140:143],a[20:23],0\n"
SA"s61,s61,524288\n"
BL"a[80:83],v235,s[8:11],s56 offen offset:1024 sc1\n"
MF"v[92:95],a[136:139],a[4:7],v[92:95]\n"
BL"v241,s[20:23],s48 offen offset:3072 lds\n"
MF"v[100:103],a[136:139],a[8:11],0\n"
RT"a[158:159],v229 offset:57856\n"
PK"v18,v24,v25\n"
MF"v[100:103],a[140:143],a[24:27],v[100:103]\n"
RT"a[160:161],v236 offset:53248\n"
PK"v19,v26,v27\n"
MF"v[108:111],a[140:143],a[28:31],0\n"
PK"v32,v32,v33\n"
PK"v33,v34,v35\n"
MF"v[108:111],a[136:139],a[12:15],v[108:111]\n"
RT"a[162:163],v236 offset:53760\n"
PK"v34,v40,v41\n"
MF"v[112:115],a[144:147],a[0:3],0\n"
WT"lgkmcnt(3)\n"
BL"a[84:87],v235,s[8:11],s57 offen offset:1024 sc1\n"
MF"v[112:115],a[148:151],a[16:19],v[112:115]\n"
RT"a[164:165],v236 offset:61440\n"
PK"v35,v42,v43\n"
MF"v[120:123],a[148:151],a[20:23],0\n"
PK"v48,v48,v49\n"
PK"v49,v50,v51\n"
MF"v[120:123],a[144:147],a[4:7],v[120:123]\n"
RT"a[166:167],v236 offset:61952\n"
PK"v50,v56,v57\n"
MF"v[128:131],a[144:147],a[8:11],0\n"
PK"v51,v58,v59\n"
MF"v[128:131],a[148:151],a[24:27],v[128:131]\n"
RT"a[168:169],v237 offset:53248\n"
MF"v[136:139],a[148:151],a[28:31],0\n"
MF"v[136:139],a[144:147],a[12:15],v[136:139]\n"
RT"a[170:171],v237 offset:53760\n"
MF"v[116:119],a[152:155],a[0:3],0\n"
WT"lgkmcnt(3)\n"
BL"a[88:91],v235,s[8:11],s58 offen offset:1024 sc1\n"
MF"v[116:119],a[156:159],a[16:19],v[116:119]\n"
"s_mov_b32 s49,262144\n"
RT"a[172:173],v237 offset:61440\n"
MF"v[124:127],a[156:159],a[20:23],0\n"
RT"a[174:175],v237 offset:61952\n"
MF"v[124:127],a[152:155],a[4:7],v[124:127]\n"
BS"v[4:7],v250,s[8:11],s49 offen offset:0 sc1\n"
MF"v[132:135],a[152:155],a[8:11],0\n"
RT"a[176:177],v228 offset:53248\n"
MF"v[132:135],a[156:159],a[24:27],v[132:135]\n"
MF"v[140:143],a[156:159],a[28:31],0\n"
RT"a[178:179],v228 offset:53760\n"
MF"v[140:143],a[152:155],a[12:15],v[140:143]\n"
BL"a[92:95],v235,s[8:11],s59 offen offset:1024 sc1\n"
MF"v[144:147],a[160:163],a[0:3],0\n"
WT"lgkmcnt(3)\n"
RT"a[180:181],v228 offset:61440\n"
MF"v[144:147],a[164:167],a[16:19],v[144:147]\n"
MF"v[152:155],a[164:167],a[20:23],0\n"
SA"m0,s38,122880\n"
RT"a[182:183],v228 offset:61952\n"
SA"s48,s60,s61\n"
MF"v[152:155],a[160:163],a[4:7],v[152:155]\n"
MF"v[160:163],a[160:163],a[8:11],0\n"
"s_mov_b32 s88,2048\n"
RT"a[184:185],v229 offset:53248\n"
MF"v[160:163],a[164:167],a[24:27],v[160:163]\n"
BL"v238,s[20:23],s48 offen offset:0 lds\n"
MF"v[168:171],a[164:167],a[28:31],0\n"
BL"a[96:99],v235,s[8:11],s56 offen offset:2048 sc1\n"
MF"v[168:171],a[160:163],a[12:15],v[168:171]\n"
RT"a[186:187],v229 offset:53760\n"
MF"v[148:151],a[168:171],a[0:3],0\n"
WT"lgkmcnt(3)\n"
RT"a[188:189],v229 offset:61440\n"
MF"v[148:151],a[172:175],a[16:19],v[148:151]\n"
MF"v[156:159],a[172:175],a[20:23],0\n"
RT"a[190:191],v229 offset:61952\n"
MF"v[156:159],a[168:171],a[4:7],v[156:159]\n"
RT"a[128:129],v236 offset:32768\n"
MF"v[164:167],a[168:171],a[8:11],0\n"
BL"v239,s[20:23],s48 offen offset:1024 lds\n"
MF"v[164:167],a[172:175],a[24:27],v[164:167]\n"
BL"a[100:103],v235,s[8:11],s57 offen offset:2048 sc1\n"
MF"v[172:175],a[172:175],a[28:31],0\n"
RT"a[130:131],v236 offset:33280\n"
MF"v[172:175],a[168:171],a[12:15],v[172:175]\n"
RT"a[132:133],v236 offset:40960\n"
MF"v[176:179],a[176:179],a[0:3],0\n"
WT"lgkmcnt(4)\n"
MF"v[176:179],a[180:183],a[16:19],v[176:179]\n"
RT"a[134:135],v236 offset:41472\n"
MF"v[184:187],a[180:183],a[20:23],0\n"
MF"v[184:187],a[176:179],a[4:7],v[184:187]\n"
RT"a[136:137],v237 offset:32768\n"
MF"v[192:195],a[176:179],a[8:11],0\n"
BL"a[104:107],v235,s[8:11],s58 offen offset:2048 sc1\n"
MF"v[192:195],a[180:183],a[24:27],v[192:195]\n"
BL"v240,s[20:23],s48 offen offset:2048 lds\n"
MF"v[200:203],a[180:183],a[28:31],0\n"
RT"a[138:139],v237 offset:33280\n"
MF"v[200:203],a[176:179],a[12:15],v[200:203]\n"
RT"a[140:141],v237 offset:40960\n"
MF"v[180:183],a[184:187],a[0:3],0\n"
WT"lgkmcnt(3)\n"
MF"v[180:183],a[188:191],a[16:19],v[180:183]\n"
RT"a[142:143],v237 offset:41472\n"
MF"v[188:191],a[188:191],a[20:23],0\n"
BL"a[108:111],v235,s[8:11],s59 offen offset:2048 sc1\n"
MF"v[188:191],a[184:187],a[4:7],v[188:191]\n"
RT"a[144:145],v228 offset:32768\n"
MF"v[196:199],a[184:187],a[8:11],0\n"
SA"s61,s61,524288\n"
BS"v[12:15],v250,s[8:11],s49 offen offset:1024 sc1\n"
MF"v[196:199],a[188:191],a[24:27],v[196:199]\n"
"s_cmp_eq_u32 s61,5242880\n"
BL"v241,s[20:23],s48 offen offset:3072 lds\n"
MF"v[204:207],a[188:191],a[28:31],0\n"
"s_cselect_b32 s61,0,s61\n"
RT"a[146:147],v228 offset:33280\n"
MF"v[204:207],a[184:187],a[12:15],v[204:207]\n"
WT"vmcnt(31)\n"
RT"a[148:149],v228 offset:40960\n"
MF"v[80:83],a[128:131],a[32:35],v[80:83]\n"
WT"vmcnt(26)\n"
MF"v[80:83],a[132:135],a[48:51],v[80:83]\n"
WT"vmcnt(25)\n"
RT"a[150:151],v228 offset:41472\n"
MF"v[88:91],a[132:135],a[52:55],v[88:91]\n"
BL"a[112:115],v235,s[8:11],s56 offen offset:3072 sc1\n"
MF"v[88:91],a[128:131],a[36:39],v[88:91]\n"
RT"a[152:153],v229 offset:32768\n"
MF"v[96:99],a[128:131],a[40:43],v[96:99]\n"
WT"vmcnt(24)\n"
RT"a[154:155],v229 offset:33280\n"
MF"v[96:99],a[132:135],a[56:59],v[96:99]\n"
WT"vmcnt(23)\n"
MF"v[104:107],a[132:135],a[60:63],v[104:107]\n"
RT"a[156:157],v229 offset:40960\n"
MF"v[104:107],a[128:131],a[44:47],v[104:107]\n"
WT"lgkmcnt(4)\n"
MF"v[84:87],a[136:139],a[32:35],v[84:87]\n"
RT"a[158:159],v229 offset:41472\n"
MF"v[84:87],a[140:143],a[48:51],v[84:87]\n"
BL"a[116:119],v235,s[8:11],s57 offen offset:3072 sc1\n"
MF"v[92:95],a[140:143],a[52:55],v[92:95]\n"
RT"a[160:161],v236 offset:36864\n"
MF"v[92:95],a[136:139],a[36:39],v[92:95]\n"
MF"v[100:103],a[136:139],a[40:43],v[100:103]\n"
RT"a[162:163],v236 offset:37376\n"
MF"v[100:103],a[140:143],a[56:59],v[100:103]\n"
MF"v[108:111],a[140:143],a[60:63],v[108:111]\n"
RT"a[164:165],v236 offset:45056\n"
MF"v[108:111],a[136:139],a[44:47],v[108:111]\n"
BL"a[120:123],v235,s[8:11],s58 offen offset:3072 sc1\n"
MF"v[112:115],a[144:147],a[32:35],v[112:115]\n"
WT"lgkmcnt(3)\n"
RT"a[166:167],v236 offset:45568\n"
MF"v[112:115],a[148:151],a[48:51],v[112:115]\n"
MF"v[120:123],a[148:151],a[52:55],v[120:123]\n"
RT"a[168:169],v237 offset:36864\n"
MF"v[120:123],a[144:147],a[36:39],v[120:123]\n"
MF"v[128:131],a[144:147],a[40:43],v[128:131]\n"
RT"a[170:171],v237 offset:37376\n"
MF"v[128:131],a[148:151],a[56:59],v[128:131]\n"
SA"s99,s99,4096\n"
RT"a[172:173],v237 offset:45056\n"
MF"v[136:139],a[148:151],a[60:63],v[136:139]\n"
"s_cmp_eq_u32 s99,20480\n"
BL"a[124:127],v235,s[8:11],s59 offen offset:3072 sc1\n"
MF"v[136:139],a[144:147],a[44:47],v[136:139]\n"
"s_cselect_b32 s99,0,s99\n"
RT"a[174:175],v237 offset:45568\n"
MF"v[116:119],a[152:155],a[32:35],v[116:119]\n"
MF"v[116:119],a[156:159],a[48:51],v[116:119]\n"
RT"a[176:177],v228 offset:36864\n"
MF"v[124:127],a[156:159],a[52:55],v[124:127]\n"
MF"v[124:127],a[152:155],a[36:39],v[124:127]\n"
RT"a[178:179],v228 offset:37376\n"
MF"v[132:135],a[152:155],a[40:43],v[132:135]\n"
"v_add_u32 v235,s99,v224\n"
MF"v[132:135],a[156:159],a[56:59],v[132:135]\n"
RT"a[180:181],v228 offset:45056\n"
MF"v[140:143],a[156:159],a[60:63],v[140:143]\n"
BL"a[0:3],v235,s[8:11],s56 offen offset:0 sc1\n"
MF"v[140:143],a[152:155],a[44:47],v[140:143]\n"
WT"lgkmcnt(3)\n"
BS"v[20:23],v250,s[8:11],s49 offen offset:2048 sc1\n"
MF"v[144:147],a[160:163],a[32:35],v[144:147]\n"
RT"a[182:183],v228 offset:45568\n"
MF"v[144:147],a[164:167],a[48:51],v[144:147]\n"
RT"a[184:185],v229 offset:36864\n"
MF"v[152:155],a[164:167],a[52:55],v[152:155]\n"
MF"v[152:155],a[160:163],a[36:39],v[152:155]\n"
RT"a[186:187],v229 offset:37376\n"
MF"v[160:163],a[160:163],a[40:43],v[160:163]\n"
BL"a[4:7],v235,s[8:11],s57 offen offset:0 sc1\n"
MF"v[160:163],a[164:167],a[56:59],v[160:163]\n"
RT"a[188:189],v229 offset:45056\n"
MF"v[168:171],a[164:167],a[60:63],v[168:171]\n"
MF"v[168:171],a[160:163],a[44:47],v[168:171]\n"
RT"a[190:191],v229 offset:45568\n"
MF"v[148:151],a[168:171],a[32:35],v[148:151]\n"
"s_lshl_b32 s50,s35,3\n"
SA"s50,s50,s34\n"
"s_lshl_b32 s50,s50,3\n"
MF"v[148:151],a[172:175],a[48:51],v[148:151]\n"
SA"s50,s50,2304\n"
SA"s51,s50,s45\n"
MF"v[156:159],a[172:175],a[52:55],v[156:159]\n"
MF"v[156:159],a[168:171],a[36:39],v[156:159]\n"
BL"a[8:11],v235,s[8:11],s58 offen offset:0 sc1\n"
MF"v[164:167],a[168:171],a[40:43],v[164:167]\n"
MF"v[164:167],a[172:175],a[56:59],v[164:167]\n"
WT"lgkmcnt(0)\n"
"s_barrier\n"
MF"v[172:175],a[172:175],a[60:63],v[172:175]\n"
RT"a[128:129],v236 offset:0\n"
MF"v[172:175],a[168:171],a[44:47],v[172:175]\n"
"buffer_store_dwordx2 v[232:233],v230,s[8:11],s51 offen sc1\n"
MF"v[176:179],a[176:179],a[32:35],v[176:179]\n"
RT"a[130:131],v236 offset:512\n"
MF"v[176:179],a[180:183],a[48:51],v[176:179]\n"
MF"v[184:187],a[180:183],a[52:55],v[184:187]\n"
SA"m0,s38,81920\n"
RT"a[132:133],v236 offset:8192\n"
SA"s48,s60,s61\n"
MF"v[184:187],a[176:179],a[36:39],v[184:187]\n"
BL"a[12:15],v235,s[8:11],s59 offen offset:0 sc1\n"
MF"v[192:195],a[176:179],a[40:43],v[192:195]\n"
RT"a[134:135],v236 offset:8704\n"
MF"v[192:195],a[180:183],a[56:59],v[192:195]\n"
BL"v238,s[20:23],s48 offen offset:0 lds\n"
MF"v[200:203],a[180:183],a[60:63],v[200:203]\n"
RT"a[136:137],v237 offset:0\n"
MF"v[200:203],a[176:179],a[44:47],v[200:203]\n"
MF"v[180:183],a[184:187],a[32:35],v[180:183]\n"
RT"a[138:139],v237 offset:512\n"
MF"v[180:183],a[188:191],a[48:51],v[180:183]\n"
BL"a[16:19],v235,s[8:11],s56 offen offset:1024 sc1\n"
MF"v[188:191],a[188:191],a[52:55],v[188:191]\n"
RT"a[140:141],v237 offset:8192\n"
MF"v[188:191],a[184:187],a[36:39],v[188:191]\n"
MF"v[196:199],a[184:187],a[40:43],v[196:199]\n"
RT"a[142:143],v237 offset:8704\n"
MF"v[196:199],a[188:191],a[56:59],v[196:199]\n"
BL"v239,s[20:23],s48 offen offset:1024 lds\n"
MF"v[204:207],a[188:191],a[60:63],v[204:207]\n"
SA"s49,s88,262144\n"
RT"a[144:145],v228 offset:0\n"
MF"v[204:207],a[184:187],a[44:47],v[204:207]\n"
"LQ_%=:\n"
WT"vmcnt(34) lgkmcnt(7)\n"
MF"v[80:83],a[128:131],a[64:67],v[80:83]\n"
WT"vmcnt(27) lgkmcnt(3)\n"
RT"a[146:147],v228 offset:512\n"
MF"v[80:83],a[132:135],a[80:83],v[80:83]\n"
WT"vmcnt(25)\n"
BS"v[28:31],v250,s[8:11],s49 offen offset:1024 sc1\n"
MF"v[88:91],a[132:135],a[84:87],v[88:91]\n"
BL"a[20:23],v235,s[8:11],s57 offen offset:1024 sc1\n"
MF"v[88:91],a[128:131],a[68:71],v[88:91]\n"
RT"a[148:149],v228 offset:8192\n"
MF"v[96:99],a[128:131],a[72:75],v[96:99]\n"
WT"vmcnt(26)\n"
RT"a[150:151],v228 offset:8704\n"
MF"v[96:99],a[132:135],a[88:91],v[96:99]\n"
WT"vmcnt(24)\n"
BL"v240,s[20:23],s48 offen offset:2048 lds\n"
MF"v[104:107],a[132:135],a[92:95],v[104:107]\n"
RT"a[152:153],v229 offset:0\n"
MF"v[104:107],a[128:131],a[76:79],v[104:107]\n"
RT"a[154:155],v229 offset:512\n"
MF"v[84:87],a[136:139],a[64:67],v[84:87]\n"
WT"lgkmcnt(4)\n"
BL"a[24:27],v235,s[8:11],s58 offen offset:1024 sc1\n"
MF"v[84:87],a[140:143],a[80:83],v[84:87]\n"
RT"a[156:157],v229 offset:8192\n"
MF"v[92:95],a[140:143],a[84:87],v[92:95]\n"
MF"v[92:95],a[136:139],a[68:71],v[92:95]\n"
RT"a[158:159],v229 offset:8704\n"
MF"v[100:103],a[136:139],a[72:75],v[100:103]\n"
SA"s61,s61,524288\n"
BL"v241,s[20:23],s48 offen offset:3072 lds\n"
MF"v[100:103],a[140:143],a[88:91],v[100:103]\n"
RT"a[160:161],v236 offset:4096\n"
MF"v[108:111],a[140:143],a[92:95],v[108:111]\n"
BL"a[28:31],v235,s[8:11],s59 offen offset:1024 sc1\n"
MF"v[108:111],a[136:139],a[76:79],v[108:111]\n"
RT"a[162:163],v236 offset:4608\n"
MF"v[112:115],a[144:147],a[64:67],v[112:115]\n"
WT"lgkmcnt(3)\n"
MF"v[112:115],a[148:151],a[80:83],v[112:115]\n"
RT"a[164:165],v236 offset:12288\n"
MF"v[120:123],a[148:151],a[84:87],v[120:123]\n"
MF"v[120:123],a[144:147],a[68:71],v[120:123]\n"
RT"a[166:167],v236 offset:12800\n"
MF"v[128:131],a[144:147],a[72:75],v[128:131]\n"
MF"v[128:131],a[148:151],a[88:91],v[128:131]\n"
RT"a[168:169],v237 offset:4096\n"
MF"v[136:139],a[148:151],a[92:95],v[136:139]\n"
BL"a[32:35],v235,s[8:11],s56 offen offset:2048 sc1\n"
MF"v[136:139],a[144:147],a[76:79],v[136:139]\n"
RT"a[170:171],v237 offset:4608\n"
MF"v[116:119],a[152:155],a[64:67],v[116:119]\n"
WT"lgkmcnt(3)\n"
RT"a[172:173],v237 offset:12288\n"
MF"v[116:119],a[156:159],a[80:83],v[116:119]\n"
MF"v[124:127],a[156:159],a[84:87],v[124:127]\n"
RT"a[174:175],v237 offset:12800\n"
MF"v[124:127],a[152:155],a[68:71],v[124:127]\n"
MF"v[132:135],a[152:155],a[72:75],v[132:135]\n"
RT"a[176:177],v228 offset:4096\n"
MF"v[132:135],a[156:159],a[88:91],v[132:135]\n"
BL"a[36:39],v235,s[8:11],s57 offen offset:2048 sc1\n"
MF"v[140:143],a[156:159],a[92:95],v[140:143]\n"
RT"a[178:179],v228 offset:4608\n"
MF"v[140:143],a[152:155],a[76:79],v[140:143]\n"
MF"v[144:147],a[160:163],a[64:67],v[144:147]\n"
WT"lgkmcnt(3)\n"
RT"a[180:181],v228 offset:12288\n"
"s_mov_b32 s49,286720\n"
MF"v[144:147],a[164:167],a[80:83],v[144:147]\n"
MF"v[152:155],a[164:167],a[84:87],v[152:155]\n"
RT"a[182:183],v228 offset:12800\n"
MF"v[152:155],a[160:163],a[68:71],v[152:155]\n"
BS"v[0:3],v250,s[8:11],s49 offen offset:0 sc1\n"
MF"v[160:163],a[160:163],a[72:75],v[160:163]\n"
BL"a[40:43],v235,s[8:11],s58 offen offset:2048 sc1\n"
MF"v[160:163],a[164:167],a[88:91],v[160:163]\n"
RT"a[184:185],v229 offset:4096\n"
MF"v[168:171],a[164:167],a[92:95],v[168:171]\n"
RT"a[186:187],v229 offset:4608\n"
MF"v[168:171],a[160:163],a[76:79],v[168:171]\n"
MF"v[148:151],a[168:171],a[64:67],v[148:151]\n"
WT"lgkmcnt(3)\n"
RT"a[188:189],v229 offset:12288\n"
MF"v[148:151],a[172:175],a[80:83],v[148:151]\n"
MF"v[156:159],a[172:175],a[84:87],v[156:159]\n"
RT"a[190:191],v229 offset:12800\n"
MF"v[156:159],a[168:171],a[68:71],v[156:159]\n"
BL"a[44:47],v235,s[8:11],s59 offen offset:2048 sc1\n"
MF"v[164:167],a[168:171],a[72:75],v[164:167]\n"
RT"a[128:129],v246 offset:0\n"
MF"v[164:167],a[172:175],a[88:91],v[164:167]\n"
RT"a[130:131],v246 offset:512\n"
MF"v[172:175],a[172:175],a[92:95],v[172:175]\n"
SA"m0,s38,131072\n"
SA"s48,s60,s61\n"
MF"v[172:175],a[168:171],a[76:79],v[172:175]\n"
RT"a[132:133],v246 offset:24576\n"
MF"v[176:179],a[176:179],a[64:67],v[176:179]\n"
WT"lgkmcnt(4)\n"
BL"v238,s[20:23],s48 offen offset:0 lds\n"
MF"v[176:179],a[180:183],a[80:83],v[176:179]\n"
RT"a[134:135],v246 offset:25088\n"
MF"v[184:187],a[180:183],a[84:87],v[184:187]\n"
BL"a[48:51],v235,s[8:11],s56 offen offset:3072 sc1\n"
MF"v[184:187],a[176:179],a[68:71],v[184:187]\n"
RT"a[136:137],v247 offset:0\n"
MF"v[192:195],a[176:179],a[72:75],v[192:195]\n"
MF"v[192:195],a[180:183],a[88:91],v[192:195]\n"
RT"a[138:139],v247 offset:512\n"
MF"v[200:203],a[180:183],a[92:95],v[200:203]\n"
MF"v[200:203],a[176:179],a[76:79],v[200:203]\n"
RT"a[140:141],v247 offset:24576\n"
MF"v[180:183],a[184:187],a[64:67],v[180:183]\n"
WT"lgkmcnt(3)\n"
BL"v239,s[20:23],s48 offen offset:1024 lds\n"
MF"v[180:183],a[188:191],a[80:83],v[180:183]\n"
BL"a[52:55],v235,s[8:11],s57 offen offset:3072 sc1\n"
MF"v[188:191],a[188:191],a[84:87],v[188:191]\n"
RT"a[142:143],v247 offset:25088\n"
MF"v[188:191],a[184:187],a[68:71],v[188:191]\n"
RT"a[144:145],v248 offset:0\n"
MF"v[196:199],a[184:187],a[72:75],v[196:199]\n"
MF"v[196:199],a[188:191],a[88:91],v[196:199]\n"
RT"a[146:147],v248 offset:512\n"
MF"v[204:207],a[188:191],a[92:95],v[204:207]\n"
MF"v[204:207],a[184:187],a[76:79],v[204:207]\n"
WT"vmcnt(35)\n"
RT"a[148:149],v248 offset:24576\n"
MF"v[80:83],a[128:131],a[96:99],v[80:83]\n"
WT"vmcnt(27)\n"
BL"a[56:59],v235,s[8:11],s58 offen offset:3072 sc1\n"
MF"v[80:83],a[132:135],a[112:115],v[80:83]\n"
WT"vmcnt(27)\n"
BL"v240,s[20:23],s48 offen offset:2048 lds\n"
MF"v[88:91],a[132:135],a[116:119],v[88:91]\n"
RT"a[150:151],v248 offset:25088\n"
RT"a[152:153],v249 offset:0\n"
MF"v[88:91],a[128:131],a[100:103],v[88:91]\n"
MF"v[96:99],a[128:131],a[104:107],v[96:99]\n"
WT"vmcnt(27)\n"
RT"a[154:155],v249 offset:512\n"
MF"v[96:99],a[132:135],a[120:123],v[96:99]\n"
WT"vmcnt(26)\n"
BS"v[16:19],v250,s[8:11],s49 offen offset:1024 sc1\n"
MF"v[104:107],a[132:135],a[124:127],v[104:107]\n"
SA"s99,s99,4096\n"
RT"a[156:157],v249 offset:24576\n"
"s_cmp_eq_u32 s99,20480\n"
MF"v[104:107],a[128:131],a[108:111],v[104:107]\n"
WT"lgkmcnt(4)\n"
BL"a[60:63],v235,s[8:11],s59 offen offset:3072 sc1\n"
MF"v[84:87],a[136:139],a[96:99],v[84:87]\n"
"s_cselect_b32 s99,0,s99\n"
RT"a[158:159],v249 offset:25088\n"
SA"s61,s61,524288\n"
MF"v[84:87],a[140:143],a[112:115],v[84:87]\n"
"s_cmp_eq_u32 s61,5242880\n"
BL"v241,s[20:23],s48 offen offset:3072 lds\n"
MF"v[92:95],a[140:143],a[116:119],v[92:95]\n"
"s_cselect_b32 s61,0,s61\n"
RT"a[160:161],v246 offset:4096\n"
MF"v[92:95],a[136:139],a[100:103],v[92:95]\n"
MF"v[100:103],a[136:139],a[104:107],v[100:103]\n"
RT"a[162:163],v246 offset:4608\n"
"v_add_u32 v235,s99,v224\n"
MF"v[100:103],a[140:143],a[120:123],v[100:103]\n"
BL"a[64:67],v235,s[8:11],s56 offen offset:0 sc1\n"
MF"v[108:111],a[140:143],a[124:127],v[108:111]\n"
RT"a[164:165],v246 offset:28672\n"
MF"v[108:111],a[136:139],a[108:111],v[108:111]\n"
MF"v[112:115],a[144:147],a[96:99],v[112:115]\n"
WT"lgkmcnt(3)\n"
RT"a[166:167],v246 offset:29184\n"
"v_add_u32 v225,s36,v224\n"
MF"v[112:115],a[148:151],a[112:115],v[112:115]\n"
"s_lshl_b32 s39,s35,20\n"
SA"s39,s39,9175040\n"
MF"v[120:123],a[148:151],a[116:119],v[120:123]\n"
RT"a[168:169],v247 offset:4096\n"
MF"v[120:123],a[144:147],a[100:103],v[120:123]\n"
MF"v[128:131],a[144:147],a[104:107],v[128:131]\n"
RT"a[170:171],v247 offset:4608\n"
MF"v[128:131],a[148:151],a[120:123],v[128:131]\n"
BL"a[68:71],v235,s[8:11],s57 offen offset:0 sc1\n"
MF"v[136:139],a[148:151],a[124:127],v[136:139]\n"
RT"a[172:173],v247 offset:28672\n"
MF"v[136:139],a[144:147],a[108:111],v[136:139]\n"
RT"a[174:175],v247 offset:29184\n"
MF"v[116:119],a[152:155],a[96:99],v[116:119]\n"
MF"v[116:119],a[156:159],a[112:115],v[116:119]\n"
RT"a[176:177],v248 offset:4096\n"
MF"v[124:127],a[156:159],a[116:119],v[124:127]\n"
MF"v[124:127],a[152:155],a[100:103],v[124:127]\n"
RT"a[178:179],v248 offset:4608\n"
MF"v[132:135],a[152:155],a[104:107],v[132:135]\n"
BL"a[72:75],v235,s[8:11],s58 offen offset:0 sc1\n"
MF"v[132:135],a[156:159],a[120:123],v[132:135]\n"
RT"a[180:181],v248 offset:28672\n"
MF"v[140:143],a[156:159],a[124:127],v[140:143]\n"
MF"v[140:143],a[152:155],a[108:111],v[140:143]\n"
WT"lgkmcnt(3)\n"
RT"a[182:183],v248 offset:29184\n"
MF"v[144:147],a[160:163],a[96:99],v[144:147]\n"
MF"v[144:147],a[164:167],a[112:115],v[144:147]\n"
RT"a[184:185],v249 offset:4096\n"
MF"v[152:155],a[164:167],a[116:119],v[152:155]\n"
BL"a[76:79],v235,s[8:11],s59 offen offset:0 sc1\n"
MF"v[152:155],a[160:163],a[100:103],v[152:155]\n"
RT"a[186:187],v249 offset:4608\n"
MF"v[160:163],a[160:163],a[104:107],v[160:163]\n"
SA"s49,s88,286720\n"
MF"v[160:163],a[164:167],a[120:123],v[160:163]\n"
RT"a[188:189],v249 offset:28672\n"
MF"v[168:171],a[164:167],a[124:127],v[168:171]\n"
BS"v[32:35],v250,s[8:11],s49 offen offset:0 sc1\n"
MF"v[168:171],a[160:163],a[108:111],v[168:171]\n"
RT"a[190:191],v249 offset:29184\n"
MF"v[148:151],a[168:171],a[96:99],v[148:151]\n"
MF"v[148:151],a[172:175],a[112:115],v[148:151]\n"
BL"a[80:83],v235,s[8:11],s56 offen offset:1024 sc1\n"
MF"v[156:159],a[172:175],a[116:119],v[156:159]\n"
MF"v[156:159],a[168:171],a[100:103],v[156:159]\n"
MF"v[164:167],a[168:171],a[104:107],v[164:167]\n"
MF"v[164:167],a[172:175],a[120:123],v[164:167]\n"
"s_barrier\n"
MF"v[172:175],a[172:175],a[124:127],v[172:175]\n"
RT"a[128:129],v242 offset:0\n"
MF"v[172:175],a[168:171],a[108:111],v[172:175]\n"
WT"lgkmcnt(1)\n"
MF"v[176:179],a[176:179],a[96:99],v[176:179]\n"
RT"a[130:131],v242 offset:512\n"
MF"v[176:179],a[180:183],a[112:115],v[176:179]\n"
BL"a[84:87],v235,s[8:11],s57 offen offset:1024 sc1\n"
MF"v[184:187],a[180:183],a[116:119],v[184:187]\n"
RT"a[132:133],v242 offset:24576\n"
MF"v[184:187],a[176:179],a[100:103],v[184:187]\n"
MF"v[192:195],a[176:179],a[104:107],v[192:195]\n"
RT"a[134:135],v242 offset:25088\n"
MF"v[192:195],a[180:183],a[120:123],v[192:195]\n"
MF"v[200:203],a[180:183],a[124:127],v[200:203]\n"
RT"a[136:137],v243 offset:0\n"
MF"v[200:203],a[176:179],a[108:111],v[200:203]\n"
BL"a[88:91],v235,s[8:11],s58 offen offset:1024 sc1\n"
MF"v[180:183],a[184:187],a[96:99],v[180:183]\n"
RT"a[138:139],v243 offset:512\n"
MF"v[180:183],a[188:191],a[112:115],v[180:183]\n"
MF"v[188:191],a[188:191],a[116:119],v[188:191]\n"
RT"a[140:141],v243 offset:24576\n"
MF"v[188:191],a[184:187],a[100:103],v[188:191]\n"
MF"v[196:199],a[184:187],a[104:107],v[196:199]\n"
RT"a[142:143],v243 offset:25088\n"
MF"v[196:199],a[188:191],a[120:123],v[196:199]\n"
MF"v[204:207],a[188:191],a[124:127],v[204:207]\n"
RT"a[144:145],v244 offset:0\n"
MF"v[204:207],a[184:187],a[108:111],v[204:207]\n"
WT"vmcnt(36) lgkmcnt(3)\n"
BL"a[92:95],v235,s[8:11],s59 offen offset:1024 sc1\n"
MF"v[80:83],a[128:131],a[0:3],v[80:83]\n"
WT"vmcnt(30)\n"
RT"a[146:147],v244 offset:512\n"
MF"v[80:83],a[132:135],a[16:19],v[80:83]\n"
WT"vmcnt(27)\n"
MF"v[88:91],a[132:135],a[20:23],v[88:91]\n"
RT"a[148:149],v244 offset:24576\n"
MF"v[88:91],a[128:131],a[4:7],v[88:91]\n"
MF"v[96:99],a[128:131],a[8:11],v[96:99]\n"
WT"vmcnt(25)\n"
RT"a[150:151],v244 offset:25088\n"
MF"v[96:99],a[132:135],a[24:27],v[96:99]\n"
WT"vmcnt(23)\n"
RT"a[152:153],v245 offset:0\n"
MF"v[104:107],a[132:135],a[28:31],v[104:107]\n"
BL"a[96:99],v235,s[8:11],s56 offen offset:2048 sc1\n"
MF"v[104:107],a[128:131],a[12:15],v[104:107]\n"
RT"a[154:155],v245 offset:512\n"
MF"v[84:87],a[136:139],a[0:3],v[84:87]\n"
WT"lgkmcnt(4)\n"
BS"v[48:51],v250,s[8:11],s49 offen offset:1024 sc1\n"
MF"v[84:87],a[140:143],a[16:19],v[84:87]\n"
RT"a[156:157],v245 offset:24576\n"
MF"v[92:95],a[140:143],a[20:23],v[92:95]\n"
MF"v[92:95],a[136:139],a[4:7],v[92:95]\n"
RT"a[158:159],v245 offset:25088\n"
MF"v[100:103],a[136:139],a[8:11],v[100:103]\n"
BL"a[100:103],v235,s[8:11],s57 offen offset:2048 sc1\n"
MF"v[100:103],a[140:143],a[24:27],v[100:103]\n"
RT"a[160:161],v242 offset:4096\n"
MF"v[108:111],a[140:143],a[28:31],v[108:111]\n"
MF"v[108:111],a[136:139],a[12:15],v[108:111]\n"
RT"a[162:163],v242 offset:4608\n"
MF"v[112:115],a[144:147],a[0:3],v[112:115]\n"
WT"lgkmcnt(3)\n"
MF"v[112:115],a[148:151],a[16:19],v[112:115]\n"
RT"a[164:165],v242 offset:28672\n"
MF"v[120:123],a[148:151],a[20:23],v[120:123]\n"
MF"v[120:123],a[144:147],a[4:7],v[120:123]\n"
RT"a[166:167],v242 offset:29184\n"
MF"v[128:131],a[144:147],a[8:11],v[128:131]\n"
BL"a[104:107],v235,s[8:11],s58 offen offset:2048 sc1\n"
MF"v[128:131],a[148:151],a[24:27],v[128:131]\n"
RT"a[168:169],v243 offset:4096\n"
MF"v[136:139],a[148:151],a[28:31],v[136:139]\n"
MF"v[136:139],a[144:147],a[12:15],v[136:139]\n"
RT"a[170:171],v243 offset:4608\n"
MF"v[116:119],a[152:155],a[0:3],v[116:119]\n"
WT"lgkmcnt(3)\n"
RT"a[172:173],v243 offset:28672\n"
MF"v[116:119],a[156:159],a[16:19],v[116:119]\n"
MF"v[124:127],a[156:159],a[20:23],v[124:127]\n"
RT"a[174:175],v243 offset:29184\n"
MF"v[124:127],a[152:155],a[4:7],v[124:127]\n"
BL"a[108:111],v235,s[8:11],s59 offen offset:2048 sc1\n"
MF"v[132:135],a[152:155],a[8:11],v[132:135]\n"
RT"a[176:177],v244 offset:4096\n"
MF"v[132:135],a[156:159],a[24:27],v[132:135]\n"
MF"v[140:143],a[156:159],a[28:31],v[140:143]\n"
RT"a[178:179],v244 offset:4608\n"
MF"v[140:143],a[152:155],a[12:15],v[140:143]\n"
MF"v[144:147],a[160:163],a[0:3],v[144:147]\n"
WT"lgkmcnt(3)\n"
RT"a[180:181],v244 offset:28672\n"
MF"v[144:147],a[164:167],a[16:19],v[144:147]\n"
BL"a[112:115],v235,s[8:11],s56 offen offset:3072 sc1\n"
MF"v[152:155],a[164:167],a[20:23],v[152:155]\n"
RT"a[182:183],v244 offset:29184\n"
MF"v[152:155],a[160:163],a[4:7],v[152:155]\n"
MF"v[160:163],a[160:163],a[8:11],v[160:163]\n"
RT"a[184:185],v245 offset:4096\n"
MF"v[160:163],a[164:167],a[24:27],v[160:163]\n"
MF"v[168:171],a[164:167],a[28:31],v[168:171]\n"
RT"a[186:187],v245 offset:4608\n"
MF"v[168:171],a[160:163],a[12:15],v[168:171]\n"
MF"v[148:151],a[168:171],a[0:3],v[148:151]\n"
WT"lgkmcnt(3)\n"
RT"a[188:189],v245 offset:28672\n"
"s_mov_b32 s49,270336\n"
MF"v[148:151],a[172:175],a[16:19],v[148:151]\n"
BL"a[116:119],v235,s[8:11],s57 offen offset:3072 sc1\n"
MF"v[156:159],a[172:175],a[20:23],v[156:159]\n"
RT"a[190:191],v245 offset:29184\n"
MF"v[156:159],a[168:171],a[4:7],v[156:159]\n"
BS"v[36:39],v250,s[8:11],s49 offen offset:0 sc1\n"
MF"v[164:167],a[168:171],a[8:11],v[164:167]\n"
RT"a[128:129],v236 offset:16384\n"
MF"v[164:167],a[172:175],a[24:27],v[164:167]\n"
RT"a[130:131],v236 offset:16896\n"
MF"v[172:175],a[172:175],a[28:31],v[172:175]\n"
MF"v[172:175],a[168:171],a[12:15],v[172:175]\n"
RT"a[132:133],v236 offset:24576\n"
MF"v[176:179],a[176:179],a[0:3],v[176:179]\n"
WT"lgkmcnt(4)\n"
BL"a[120:123],v235,s[8:11],s58 offen offset:3072 sc1\n"
MF"v[176:179],a[180:183],a[16:19],v[176:179]\n"
RT"a[134:135],v236 offset:25088\n"
MF"v[184:187],a[180:183],a[20:23],v[184:187]\n"
MF"v[184:187],a[176:179],a[4:7],v[184:187]\n"
RT"a[136:137],v237 offset:16384\n"
MF"v[192:195],a[176:179],a[8:11],v[192:195]\n"
MF"v[192:195],a[180:183],a[24:27],v[192:195]\n"
SA"s99,s99,4096\n"
RT"a[138:139],v237 offset:16896\n"
MF"v[200:203],a[180:183],a[28:31],v[200:203]\n"
"s_cmp_eq_u32 s99,20480\n"
BL"a[124:127],v235,s[8:11],s59 offen offset:3072 sc1\n"
MF"v[200:203],a[176:179],a[12:15],v[200:203]\n"
"s_cselect_b32 s99,0,s99\n"
RT"a[140:141],v237 offset:24576\n"
MF"v[180:183],a[184:187],a[0:3],v[180:183]\n"
WT"lgkmcnt(3)\n"
"v_lshrrev_b32 v252,1,v224\n"
MF"v[180:183],a[188:191],a[16:19],v[180:183]\n"
RT"a[142:143],v237 offset:25088\n"
"v_and_b32 v252,56,v252\n"
MF"v[188:191],a[188:191],a[20:23],v[188:191]\n"
"s_lshl_b32 s50,s35,6\n"
MF"v[188:191],a[184:187],a[4:7],v[188:191]\n"
SA"s50,s50,2304\n"
RT"a[144:145],v228 offset:16384\n"
MF"v[196:199],a[184:187],a[8:11],v[196:199]\n"
"buffer_load_dwordx2 v[208:209],v252,s[8:11],s50 offen sc1\n"
MF"v[196:199],a[188:191],a[24:27],v[196:199]\n"
RT"a[146:147],v228 offset:16896\n"
"v_add_u32 v235,s99,v224\n"
MF"v[204:207],a[188:191],a[28:31],v[204:207]\n"
BL"a[192:195],v235,s[8:11],s56 offen offset:0 sc1\n"
MF"v[204:207],a[184:187],a[12:15],v[204:207]\n"
WT"vmcnt(34)\n"
RT"a[148:149],v228 offset:24576\n"
MF"v[80:83],a[128:131],a[32:35],v[80:83]\n"
WT"vmcnt(28)\n"
MF"v[80:83],a[132:135],a[48:51],v[80:83]\n"
WT"vmcnt(26)\n"
RT"a[150:151],v228 offset:25088\n"
MF"v[88:91],a[132:135],a[52:55],v[88:91]\n"
RT"a[152:153],v229 offset:16384\n"
MF"v[88:91],a[128:131],a[36:39],v[88:91]\n"
MF"v[96:99],a[128:131],a[40:43],v[96:99]\n"
WT"vmcnt(25)\n"
RT"a[154:155],v229 offset:16896\n"
MF"v[96:99],a[132:135],a[56:59],v[96:99]\n"
WT"vmcnt(22)\n"
BL"a[196:199],v235,s[8:11],s57 offen offset:0 sc1\n"
MF"v[104:107],a[132:135],a[60:63],v[104:107]\n"
RT"a[156:157],v229 offset:24576\n"
MF"v[104:107],a[128:131],a[44:47],v[104:107]\n"
WT"lgkmcnt(4)\n"
MF"v[84:87],a[136:139],a[32:35],v[84:87]\n"
RT"a[158:159],v229 offset:25088\n"
MF"v[84:87],a[140:143],a[48:51],v[84:87]\n"
MF"v[92:95],a[140:143],a[52:55],v[92:95]\n"
RT"a[160:161],v236 offset:20480\n"
MF"v[92:95],a[136:139],a[36:39],v[92:95]\n"
BL"a[200:203],v235,s[8:11],s58 offen offset:0 sc1\n"
MF"v[100:103],a[136:139],a[40:43],v[100:103]\n"
RT"a[162:163],v236 offset:20992\n"
MF"v[100:103],a[140:143],a[56:59],v[100:103]\n"
BS"v[44:47],v250,s[8:11],s49 offen offset:1024 sc1\n"
MF"v[108:111],a[140:143],a[60:63],v[108:111]\n"
RT"a[164:165],v236 offset:28672\n"
MF"v[108:111],a[136:139],a[44:47],v[108:111]\n"
MF"v[112:115],a[144:147],a[32:35],v[112:115]\n"
WT"lgkmcnt(3)\n"
RT"a[166:167],v236 offset:29184\n"
MF"v[112:115],a[148:151],a[48:51],v[112:115]\n"
MF"v[120:123],a[148:151],a[52:55],v[120:123]\n"
RT"a[168:169],v237 offset:20480\n"
MF"v[120:123],a[144:147],a[36:39],v[120:123]\n"
BL"a[204:207],v235,s[8:11],s59 offen offset:0 sc1\n"
MF"v[128:131],a[144:147],a[40:43],v[128:131]\n"
RT"a[170:171],v237 offset:20992\n"
MF"v[128:131],a[148:151],a[56:59],v[128:131]\n"
RT"a[172:173],v237 offset:28672\n"
MF"v[136:139],a[148:151],a[60:63],v[136:139]\n"
MF"v[136:139],a[144:147],a[44:47],v[136:139]\n"
RT"a[174:175],v237 offset:29184\n"
MF"v[116:119],a[152:155],a[32:35],v[116:119]\n"
MF"v[116:119],a[156:159],a[48:51],v[116:119]\n"
RT"a[176:177],v228 offset:20480\n"
MF"v[124:127],a[156:159],a[52:55],v[124:127]\n"
BL"a[208:211],v235,s[8:11],s56 offen offset:1024 sc1\n"
MF"v[124:127],a[152:155],a[36:39],v[124:127]\n"
RT"a[178:179],v228 offset:20992\n"
MF"v[132:135],a[152:155],a[40:43],v[132:135]\n"
MF"v[132:135],a[156:159],a[56:59],v[132:135]\n"
RT"a[180:181],v228 offset:28672\n"
MF"v[140:143],a[156:159],a[60:63],v[140:143]\n"
MF"v[140:143],a[152:155],a[44:47],v[140:143]\n"
WT"lgkmcnt(3)\n"
RT"a[182:183],v228 offset:29184\n"
MF"v[144:147],a[160:163],a[32:35],v[144:147]\n"
BL"a[212:215],v235,s[8:11],s57 offen offset:1024 sc1\n"
MF"v[144:147],a[164:167],a[48:51],v[144:147]\n"
RT"a[184:185],v229 offset:20480\n"
MF"v[152:155],a[164:167],a[52:55],v[152:155]\n"
MF"v[152:155],a[160:163],a[36:39],v[152:155]\n"
RT"a[186:187],v229 offset:20992\n"
MF"v[160:163],a[160:163],a[40:43],v[160:163]\n"
MF"v[160:163],a[164:167],a[56:59],v[160:163]\n"
RT"a[188:189],v229 offset:28672\n"
MF"v[168:171],a[164:167],a[60:63],v[168:171]\n"
MF"v[168:171],a[160:163],a[44:47],v[168:171]\n"
RT"a[190:191],v229 offset:29184\n"
MF"v[148:151],a[168:171],a[32:35],v[148:151]\n"
BL"a[216:219],v235,s[8:11],s58 offen offset:1024 sc1\n"
MF"v[148:151],a[172:175],a[48:51],v[148:151]\n"
WT"vmcnt(8)\n"
"v_cmp_eq_u64 vcc,s[28:29],v[208:209]\n"
"s_cmp_eq_u64 vcc,exec\n"
"s_cbranch_scc0 L1010_%=\n"
"L1011_%=:\n"
MF"v[156:159],a[172:175],a[52:55],v[156:159]\n"
MF"v[156:159],a[168:171],a[36:39],v[156:159]\n"
MF"v[164:167],a[168:171],a[40:43],v[164:167]\n"
SA"s49,s88,270336\n"
MF"v[164:167],a[172:175],a[56:59],v[164:167]\n"
WT"lgkmcnt(0)\n"
"s_barrier\n"
MF"v[172:175],a[172:175],a[60:63],v[172:175]\n"
SA"m0,s36,0\n"
RT"a[128:129],v246 offset:8192\n"
SA"s48,s39,0\n"
MF"v[172:175],a[168:171],a[44:47],v[172:175]\n"
BS"v[52:55],v250,s[8:11],s49 offen offset:0 sc1\n"
MF"v[176:179],a[176:179],a[32:35],v[176:179]\n"
BL"a[220:223],v235,s[8:11],s59 offen offset:1024 sc1\n"
MF"v[176:179],a[180:183],a[48:51],v[176:179]\n"
RT"a[130:131],v246 offset:8704\n"
MF"v[184:187],a[180:183],a[52:55],v[184:187]\n"
BL"v225,s[8:11],s48 offen offset:0 sc1 lds\n"
MF"v[184:187],a[176:179],a[36:39],v[184:187]\n"
RT"a[132:133],v246 offset:32768\n"
"v_lshlrev_b32 v254,10,v224\n"
MF"v[192:195],a[176:179],a[40:43],v[192:195]\n"
"s_lshl_b32 s94,s34,1\n"
RT"a[134:135],v246 offset:33280\n"
"v_lshrrev_b32 v255,4,v224\n"
MF"v[192:195],a[180:183],a[56:59],v[192:195]\n"
SA"s95,s94,2\n"
BL"a[224:227],v235,s[8:11],s56 offen offset:2048 sc1\n"
MF"v[200:203],a[180:183],a[60:63],v[200:203]\n"
"s_sub_u32 s94,32,s94\n"
RT"a[136:137],v247 offset:8192\n"
MF"v[200:203],a[176:179],a[44:47],v[200:203]\n"
"v_add_u32 v228,s39,v254\n"
MF"v[180:183],a[184:187],a[32:35],v[180:183]\n"
RT"a[138:139],v247 offset:8704\n"
"v_and_b32 v254,1,v255\n"
MF"v[180:183],a[188:191],a[48:51],v[180:183]\n"
BL"v225,s[8:11],s48 offen offset:1024 sc1 lds\n"
MF"v[188:191],a[188:191],a[52:55],v[188:191]\n"
RT"a[140:141],v247 offset:32768\n"
"v_mul_u32_u24 v254,491520,v254\n"
MF"v[188:191],a[184:187],a[36:39],v[188:191]\n"
"v_cmp_gt_u32 vcc,s95,v255\n"
"v_mov_b32 v229,16384\n"
MF"v[196:199],a[184:187],a[40:43],v[196:199]\n"
RT"a[142:143],v247 offset:33280\n"
"v_mov_b32 v253,516096\n"
MF"v[196:199],a[188:191],a[56:59],v[196:199]\n"
BL"a[228:231],v235,s[8:11],s57 offen offset:2048 sc1\n"
MF"v[204:207],a[188:191],a[60:63],v[204:207]\n"
RT"a[144:145],v248 offset:8192\n"
"v_cndmask_b32_e64 v254,v254,0,vcc\n"
MF"v[204:207],a[184:187],a[44:47],v[204:207]\n"
WT"lgkmcnt(3)\n"
"v_cndmask_b32 v229,v229,v253,vcc\n"
"v_cmp_eq_u32 vcc,s94,v255\n"
MF"v[80:83],a[128:131],a[64:67],v[80:83]\n"
RT"a[146:147],v248 offset:8704\n"
"v_mov_b32 v253,507904\n"
MF"v[80:83],a[132:135],a[80:83],v[80:83]\n"
BL"v225,s[8:11],s48 offen offset:2048 sc1 lds\n"
MF"v[88:91],a[132:135],a[84:87],v[88:91]\n"
RT"a[148:149],v248 offset:32768\n"
"v_cndmask_b32 v254,v254,v253,vcc\n"
MF"v[88:91],a[128:131],a[68:71],v[88:91]\n"
BL"a[232:235],v235,s[8:11],s58 offen offset:2048 sc1\n"
MF"v[96:99],a[128:131],a[72:75],v[96:99]\n"
RT"a[150:151],v248 offset:33280\n"
"v_mov_b32 v253,1862270976\n"
MF"v[96:99],a[132:135],a[88:91],v[96:99]\n"
"s_mul_i32 s95,s35,163840\n"
RT"a[152:153],v249 offset:8192\n"
"v_cndmask_b32 v229,v229,v253,vcc\n"
MF"v[104:107],a[132:135],a[92:95],v[104:107]\n"
SA"s95,s95,3395584\n"
"v_add_u32 v228,v228,v254\n"
MF"v[104:107],a[128:131],a[76:79],v[104:107]\n"
RT"a[154:155],v249 offset:8704\n"
"v_subrev_u32 v254,s94,v255\n"
MF"v[84:87],a[136:139],a[64:67],v[84:87]\n"
WT"lgkmcnt(4)\n"
BL"v225,s[8:11],s48 offen offset:3072 sc1 lds\n"
MF"v[84:87],a[140:143],a[80:83],v[84:87]\n"
RT"a[156:157],v249 offset:32768\n"
"v_lshlrev_b32 v254,15,v254\n"
MF"v[92:95],a[140:143],a[84:87],v[92:95]\n"
BL"a[236:239],v235,s[8:11],s59 offen offset:2048 sc1\n"
MF"v[92:95],a[136:139],a[68:71],v[92:95]\n"
RT"a[158:159],v249 offset:33280\n"
"v_add_u32 v254,s95,v254\n"
MF"v[100:103],a[136:139],a[72:75],v[100:103]\n"
"v_cmp_lt_u32 vcc,s94,v255\n"
MF"v[100:103],a[140:143],a[88:91],v[100:103]\n"
"s_mov_b32 s47,s36\n"
RT"a[160:161],v246 offset:12288\n"
"v_cndmask_b32 v228,v228,v254,vcc\n"
MF"v[108:111],a[140:143],a[92:95],v[108:111]\n"
SA"m0,s36,16384\n"
"v_add_u32 v229,v229,v228\n"
SA"s48,s39,516096\n"
MF"v[108:111],a[136:139],a[76:79],v[108:111]\n"
"s_cmp_lt_u32 s30,2\n"
RT"a[162:163],v246 offset:12800\n"
"s_cselect_b32 s48,2130706432,s48\n"
MF"v[112:115],a[144:147],a[64:67],v[112:115]\n"
WT"lgkmcnt(3)\n"
BS"v[60:63],v250,s[8:11],s49 offen offset:1024 sc1\n"
MF"v[112:115],a[148:151],a[80:83],v[112:115]\n"
SA"s46,s36,163840\n"
BL"v225,s[8:11],s48 offen offset:0 sc1 lds\n"
MF"v[120:123],a[148:151],a[84:87],v[120:123]\n"
SA"s40,s36,65536\n"
RT"a[164:165],v246 offset:36864\n"
"v_mov_b32 v226,v224\n"
MF"v[120:123],a[144:147],a[68:71],v[120:123]\n"
"s_mov_b32 s41,0\n"
BL"a[240:243],v235,s[8:11],s56 offen offset:3072 sc1\n"
MF"v[128:131],a[144:147],a[72:75],v[128:131]\n"
"s_mov_b32 s42,2\n"
RT"a[166:167],v246 offset:37376\n"
"v_bfe_u32 v254,v224,4,4\n"
MF"v[128:131],a[148:151],a[88:91],v[128:131]\n"
RT"a[168:169],v247 offset:12288\n"
"v_lshrrev_b32 v255,8,v224\n"
MF"v[136:139],a[148:151],a[92:95],v[136:139]\n"
"s_lshl_b32 s94,s30,4\n"
"v_lshlrev_b32 v255,2,v255\n"
"v_sub_u32 v254,v254,v255\n"
MF"v[136:139],a[144:147],a[76:79],v[136:139]\n"
RT"a[170:171],v247 offset:12800\n"
"v_add_u32 v254,s94,v254\n"
MF"v[116:119],a[152:155],a[64:67],v[116:119]\n"
WT"lgkmcnt(4)\n"
RT"a[172:173],v247 offset:36864\n"
"v_mov_b32 v255,0xff800000\n"
MF"v[116:119],a[156:159],a[80:83],v[116:119]\n"
BL"a[244:247],v235,s[8:11],s57 offen offset:3072 sc1\n"
MF"v[124:127],a[156:159],a[84:87],v[124:127]\n"
BL"v225,s[8:11],s48 offen offset:1024 sc1 lds\n"
MF"v[124:127],a[152:155],a[68:71],v[124:127]\n"
RT"a[174:175],v247 offset:37376\n"
"v_cmp_gt_i32_e64 vcc,0,v254\n"
MF"v[132:135],a[152:155],a[72:75],v[132:135]\n"
RT"a[176:177],v248 offset:12288\n"
"v_cndmask_b32_e64 v208,0,v255,vcc\n"
MF"v[132:135],a[156:159],a[88:91],v[132:135]\n"
"v_cmp_gt_i32_e64 s[52:53],1,v254\n"
"v_cmp_gt_i32_e64 s[62:63],2,v254\n"
MF"v[140:143],a[156:159],a[92:95],v[140:143]\n"
RT"a[178:179],v248 offset:12800\n"
"v_cndmask_b32_e64 v209,0,v255,s[52:53]\n"
MF"v[140:143],a[152:155],a[76:79],v[140:143]\n"
BL"a[248:251],v235,s[8:11],s58 offen offset:3072 sc1\n"
MF"v[144:147],a[160:163],a[64:67],v[144:147]\n"
WT"lgkmcnt(3)\n"
RT"a[180:181],v248 offset:36864\n"
"v_cndmask_b32_e64 v210,0,v255,s[62:63]\n"
MF"v[144:147],a[164:167],a[80:83],v[144:147]\n"
BL"v225,s[8:11],s48 offen offset:2048 sc1 lds\n"
MF"v[152:155],a[164:167],a[84:87],v[152:155]\n"
RT"a[182:183],v248 offset:37376\n"
"v_cmp_gt_i32_e64 vcc,3,v254\n"
MF"v[152:155],a[160:163],a[68:71],v[152:155]\n"
"v_cmp_gt_i32_e64 s[52:53],16,v254\n"
"v_cndmask_b32_e64 v211,0,v255,vcc\n"
MF"v[160:163],a[160:163],a[72:75],v[160:163]\n"
RT"a[184:185],v249 offset:12288\n"
"v_cndmask_b32_e64 v212,0,v255,s[52:53]\n"
MF"v[160:163],a[164:167],a[88:91],v[160:163]\n"
"v_cmp_gt_i32_e64 s[62:63],17,v254\n"
"v_cmp_gt_i32_e64 vcc,18,v254\n"
MF"v[168:171],a[164:167],a[92:95],v[168:171]\n"
RT"a[186:187],v249 offset:12800\n"
"v_cndmask_b32_e64 v213,0,v255,s[62:63]\n"
MF"v[168:171],a[160:163],a[76:79],v[168:171]\n"
BL"a[252:255],v235,s[8:11],s59 offen offset:3072 sc1\n"
MF"v[148:151],a[168:171],a[64:67],v[148:151]\n"
WT"lgkmcnt(3)\n"
RT"a[188:189],v249 offset:36864\n"
"v_cndmask_b32_e64 v214,0,v255,vcc\n"
MF"v[148:151],a[172:175],a[80:83],v[148:151]\n"
BL"v225,s[8:11],s48 offen offset:3072 sc1 lds\n"
MF"v[156:159],a[172:175],a[84:87],v[156:159]\n"
RT"a[190:191],v249 offset:37376\n"
"v_cmp_gt_i32_e64 s[52:53],19,v254\n"
MF"v[156:159],a[168:171],a[68:71],v[156:159]\n"
RT"a[128:129],v242 offset:8192\n"
"v_cndmask_b32_e64 v215,0,v255,s[52:53]\n"
MF"v[164:167],a[168:171],a[72:75],v[164:167]\n"
"v_cmp_gt_i32_e64 s[62:63],32,v254\n"
"v_cmp_gt_i32_e64 vcc,33,v254\n"
MF"v[164:167],a[172:175],a[88:91],v[164:167]\n"
RT"a[130:131],v242 offset:8704\n"
"v_cndmask_b32_e64 v216,0,v255,s[62:63]\n"
MF"v[172:175],a[172:175],a[92:95],v[172:175]\n"
SA"m0,s36,32768\n"
"v_cndmask_b32_e64 v217,0,v255,vcc\n"
"v_cmp_gt_i32_e64 s[52:53],34,v254\n"
MF"v[172:175],a[168:171],a[76:79],v[172:175]\n"
SA"s48,s39,16384\n"
RT"a[132:133],v242 offset:32768\n"
"v_cndmask_b32_e64 v218,0,v255,s[52:53]\n"
MF"v[176:179],a[176:179],a[64:67],v[176:179]\n"
WT"lgkmcnt(4)\n"
BL"v225,s[8:11],s48 offen offset:0 sc1 lds\n"
MF"v[176:179],a[180:183],a[80:83],v[176:179]\n"
"s_mov_b32 s49,278528\n"
RT"a[134:135],v242 offset:33280\n"
"v_cmp_gt_i32_e64 s[62:63],35,v254\n"
MF"v[184:187],a[180:183],a[84:87],v[184:187]\n"
BS"v[64:67],v250,s[8:11],s49 offen offset:0 sc1\n"
MF"v[184:187],a[176:179],a[68:71],v[184:187]\n"
RT"a[136:137],v243 offset:8192\n"
"v_cndmask_b32_e64 v219,0,v255,s[62:63]\n"
MF"v[192:195],a[176:179],a[72:75],v[192:195]\n"
"s_lshl_b32 s59,s34,5\n"
"v_cmp_gt_i32_e64 vcc,48,v254\n"
"v_cmp_gt_i32_e64 s[52:53],49,v254\n"
MF"v[192:195],a[180:183],a[88:91],v[192:195]\n"
"s_sub_u32 s60,480,s59\n"
RT"a[138:139],v243 offset:8704\n"
"v_cndmask_b32_e64 v220,0,v255,vcc\n"
MF"v[200:203],a[180:183],a[92:95],v[200:203]\n"
SA"s59,s59,s35\n"
"v_cndmask_b32_e64 v221,0,v255,s[52:53]\n"
"v_cmp_gt_i32_e64 s[62:63],50,v254\n"
MF"v[200:203],a[176:179],a[76:79],v[200:203]\n"
SA"s60,s60,s35\n"
RT"a[140:141],v243 offset:32768\n"
"v_cndmask_b32_e64 v222,0,v255,s[62:63]\n"
MF"v[180:183],a[184:187],a[64:67],v[180:183]\n"
WT"lgkmcnt(3)\n"
BL"v225,s[8:11],s48 offen offset:1024 sc1 lds\n"
MF"v[180:183],a[188:191],a[80:83],v[180:183]\n"
"s_lshl_b32 s59,s59,15\n"
RT"a[142:143],v243 offset:33280\n"
"v_cmp_gt_i32_e64 vcc,51,v254\n"
MF"v[188:191],a[188:191],a[84:87],v[188:191]\n"
"s_lshl_b32 s60,s60,15\n"
"v_cndmask_b32_e64 v223,0,v255,vcc\n"
SA"s50,s36,42729472\n"
MF"v[188:191],a[184:187],a[68:71],v[188:191]\n"
SA"s59,s59,s50\n"
RT"a[144:145],v244 offset:8192\n"
SA"s60,s60,s50\n"
MF"v[196:199],a[184:187],a[72:75],v[196:199]\n"
"s_lshl_b32 s61,s34,3\n"
SA"s61,s61,s32\n"
"s_lshl_b32 s61,s61,2\n"
MF"v[196:199],a[188:191],a[88:91],v[196:199]\n"
SA"s61,s61,s30\n"
RT"a[146:147],v244 offset:8704\n"
"s_lshl_b32 s61,s61,2\n"
MF"v[204:207],a[188:191],a[92:95],v[204:207]\n"
SA"s61,s61,s33\n"
"s_lshl_b32 s61,s61,6\n"
MF"v[204:207],a[184:187],a[76:79],v[204:207]\n"
SA"s61,s61,81002496\n"
RT"a[148:149],v244 offset:32768\n"
"s_lshl_b32 s98,s35,3\n"
MF"v[80:83],a[128:131],a[96:99],v[80:83]\n"
SA"s98,s98,s34\n"
BL"v225,s[8:11],s48 offen offset:2048 sc1 lds\n"
MF"v[80:83],a[132:135],a[112:115],v[80:83]\n"
"s_lshl_b32 s98,s98,3\n"
RT"a[150:151],v244 offset:33280\n"
MF"v[88:91],a[132:135],a[116:119],v[88:91]\n"
SA"s98,s98,4352\n"
RT"a[152:153],v245 offset:8192\n"
SA"s98,s98,s45\n"
MF"v[88:91],a[128:131],a[100:103],v[88:91]\n"
"s_lshl_b32 s50,s34,2\n"
MF"v[96:99],a[128:131],a[104:107],v[96:99]\n"
"s_mul_i32 s51,s33,0x1011100\n"
RT"a[154:155],v245 offset:8704\n"
SA"s51,s51,0xf48c00\n"
MF"v[96:99],a[132:135],a[120:123],v[96:99]\n"
"s_lshr_b32 s51,s51,s50\n"
MF"v[104:107],a[132:135],a[124:127],v[104:107]\n"
"s_and_b32 s51,s51,15\n"
RT"a[156:157],v245 offset:32768\n"
"s_lshr_b32 s50,s31,3\n"
MF"v[104:107],a[128:131],a[108:111],v[104:107]\n"
WT"lgkmcnt(4)\n"
"s_cmp_eq_u32 s50,6\n"
"s_cselect_b32 s51,15,s51\n"
MF"v[84:87],a[136:139],a[96:99],v[84:87]\n"
"s_cmp_eq_u32 s50,26\n"
RT"a[158:159],v245 offset:33280\n"
"s_cselect_b32 s51,0,s51\n"
MF"v[84:87],a[140:143],a[112:115],v[84:87]\n"
"s_mul_i32 s58,s32,14\n"
BL"v225,s[8:11],s48 offen offset:3072 sc1 lds\n"
MF"v[92:95],a[140:143],a[116:119],v[92:95]\n"
SA"s58,s58,s51\n"
RT"a[160:161],v242 offset:12288\n"
"s_sub_u32 s58,s58,1\n"
MF"v[92:95],a[136:139],a[100:103],v[92:95]\n"
"s_cmp_lt_u32 s58,108\n"
"s_cselect_b32 s90,s51,0\n"
"s_cmp_lg_u32 s90,0\n"
MF"v[100:103],a[136:139],a[104:107],v[100:103]\n"
"s_cselect_b32 s57,1,0\n"
RT"a[162:163],v242 offset:12800\n"
"s_cmp_eq_u32 s51,15\n"
MF"v[100:103],a[140:143],a[120:123],v[100:103]\n"
"s_cselect_b32 s57,2,s57\n"
"s_cmp_eq_u32 s50,6\n"
"s_cselect_b32 s90,4,s33\n"
MF"v[108:111],a[140:143],a[124:127],v[108:111]\n"
"s_mul_i32 s50,s32,5\n"
RT"a[164:165],v242 offset:36864\n"
SA"m0,s36,49152\n"
MF"v[108:111],a[136:139],a[108:111],v[108:111]\n"
SA"s48,s39,532480\n"
SA"s90,s90,s50\n"
"s_cmp_eq_u32 s57,2\n"
MF"v[112:115],a[144:147],a[96:99],v[112:115]\n"
WT"lgkmcnt(3)\n"
RT"a[166:167],v242 offset:37376\n"
"s_cselect_b32 s58,s90,s58\n"
MF"v[112:115],a[148:151],a[112:115],v[112:115]\n"
"s_lshl_b32 s56,s34,1\n"
BL"v225,s[8:11],s48 offen offset:0 sc1 lds\n"
MF"v[120:123],a[148:151],a[116:119],v[120:123]\n"
"s_sub_u32 s56,s56,2\n"
RT"a[168:169],v243 offset:12288\n"
"s_cmp_eq_u32 s34,7\n"
MF"v[120:123],a[144:147],a[100:103],v[120:123]\n"
"s_cselect_b32 s56,2,s56\n"
"s_cmp_eq_u32 s57,2\n"
"s_cselect_b32 s56,5,s56\n"
MF"v[128:131],a[144:147],a[104:107],v[128:131]\n"
"s_cmp_lt_u32 s30,2\n"
RT"a[170:171],v243 offset:12800\n"
"s_cselect_b64 s[24:25],s[78:79],s[80:81]\n"
MF"v[128:131],a[148:151],a[120:123],v[128:131]\n"
"s_cmp_eq_u32 s57,2\n"
BS"v[68:71],v250,s[8:11],s49 offen offset:1024 sc1\n"
MF"v[136:139],a[148:151],a[124:127],v[136:139]\n"
"s_cselect_b64 s[24:25],s[82:83],s[24:25]\n"
RT"a[172:173],v243 offset:36864\n"
"s_and_b32 s50,s30,1\n"
MF"v[136:139],a[144:147],a[108:111],v[136:139]\n"
"s_mul_i32 s50,s50,10\n"
RT"a[174:175],v243 offset:37376\n"
"s_mul_i32 s51,s30,14\n"
MF"v[116:119],a[152:155],a[96:99],v[116:119]\n"
"s_min_u32 s51,s51,41\n"
BL"v225,s[8:11],s48 offen offset:1024 sc1 lds\n"
MF"v[116:119],a[156:159],a[112:115],v[116:119]\n"
"s_cmp_eq_u32 s57,2\n"
RT"a[176:177],v244 offset:12288\n"
"s_cselect_b32 s50,s51,s50\n"
MF"v[124:127],a[156:159],a[116:119],v[124:127]\n"
"s_cmp_lt_u32 s30,2\n"
"s_cselect_b32 s51,14,13\n"
"s_cmp_lt_u32 s58,108\n"
MF"v[124:127],a[152:155],a[100:103],v[124:127]\n"
"s_cselect_b32 s92,10,0\n"
RT"a[178:179],v244 offset:12800\n"
"s_cmp_eq_u32 s57,2\n"
MF"v[132:135],a[152:155],a[104:107],v[132:135]\n"
"s_cselect_b32 s92,s51,s92\n"
"s_mov_b32 s51,3456\n"
"s_cmp_eq_u32 s57,2\n"
MF"v[132:135],a[156:159],a[120:123],v[132:135]\n"
"s_cselect_b32 s51,1280,s51\n"
RT"a[180:181],v244 offset:36864\n"
"s_lshl_b32 s62,s51,5\n"
MF"v[140:143],a[156:159],a[124:127],v[140:143]\n"
"s_mul_i32 s26,s62,s92\n"
"s_mul_i32 s90,s62,s50\n"
"s_lshl_b32 s91,s58,5\n"
MF"v[140:143],a[152:155],a[108:111],v[140:143]\n"
WT"lgkmcnt(3)\n"
RT"a[182:183],v244 offset:37376\n"
SA"s90,s90,s91\n"
MF"v[144:147],a[160:163],a[96:99],v[144:147]\n"
SA"s24,s24,s90\n"
BL"v225,s[8:11],s48 offen offset:2048 sc1 lds\n"
MF"v[144:147],a[164:167],a[112:115],v[144:147]\n"
"s_addc_u32 s25,s25,0\n"
RT"a[184:185],v245 offset:12288\n"
"s_and_b32 s25,s25,0xffff\n"
MF"v[152:155],a[164:167],a[116:119],v[152:155]\n"
"s_sub_u32 s90,s51,32\n"
"v_lshrrev_b32 v235,5,v224\n"
"v_mul_u32_u24 v235,s51,v235\n"
MF"v[152:155],a[160:163],a[100:103],v[152:155]\n"
"s_sub_u32 s26,s26,s90\n"
RT"a[186:187],v245 offset:12800\n"
"v_and_b32 v239,16,v224\n"
MF"v[160:163],a[160:163],a[104:107],v[160:163]\n"
"s_cselect_b32 s26,0,s26\n"
"v_add_u32 v235,v235,v239\n"
"s_mov_b32 s27,0x20000\n"
MF"v[160:163],a[164:167],a[120:123],v[160:163]\n"
"s_mul_i32 s63,s35,20480\n"
RT"a[188:189],v245 offset:36864\n"
SA"s63,s63,2770944\n"
MF"v[168:171],a[164:167],a[124:127],v[168:171]\n"
"s_cmp_eq_u32 s57,2\n"
"s_cselect_b32 s63,2695168,s63\n"
MF"v[168:171],a[160:163],a[108:111],v[168:171]\n"
"s_lshl_b32 s50,s50,10\n"
RT"a[190:191],v245 offset:37376\n"
SA"s63,s63,s50\n"
MF"v[148:151],a[168:171],a[96:99],v[148:151]\n"
"s_mov_b32 s50,1065353216\n"
BL"v225,s[8:11],s48 offen offset:3072 sc1 lds\n"
MF"v[148:151],a[172:175],a[112:115],v[148:151]\n"
"s_cmp_lg_u32 s92,0\n"
"s_cselect_b32 s50,s45,s50\n"
"s_mov_b32 s93,2695168\n"
MF"v[156:159],a[172:175],a[116:119],v[156:159]\n"
"s_cmp_eq_u32 s57,2\n"
"s_cselect_b32 s93,2750464,s93\n"
MF"v[156:159],a[168:171],a[100:103],v[156:159]\n"
"s_lshl_b32 s51,s58,9\n"
MF"v[164:167],a[168:171],a[104:107],v[164:167]\n"
SA"s93,s93,s51\n"
SA"s93,s93,s50\n"
MF"v[164:167],a[172:175],a[120:123],v[164:167]\n"
"s_mov_b32 s94,6912\n"
"s_barrier\n"
MF"v[172:175],a[172:175],a[124:127],v[172:175]\n"
"s_cmp_eq_u32 s57,2\n"
RT"a[128:129],v246 offset:16384\n"
"s_cselect_b32 s94,6400,s94\n"
MF"v[172:175],a[168:171],a[108:111],v[172:175]\n"
WT"lgkmcnt(1)\n"
"s_lshl_b32 s51,s58,3\n"
SA"s94,s94,s51\n"
MF"v[176:179],a[176:179],a[96:99],v[176:179]\n"
SA"s94,s94,s50\n"
RT"a[130:131],v246 offset:16896\n"
"s_mov_b32 s52,0\n"
MF"v[176:179],a[180:183],a[112:115],v[176:179]\n"
"s_lshl_b32 s50,s35,6\n"
RT"a[132:133],v246 offset:40960\n"
MF"v[184:187],a[180:183],a[116:119],v[184:187]\n"
SA"s50,s50,2304\n"
MF"v[184:187],a[176:179],a[100:103],v[184:187]\n"
"s_cmp_eq_u32 s57,2\n"
RT"a[134:135],v246 offset:41472\n"
"s_cselect_b32 s50,6912,s50\n"
MF"v[192:195],a[176:179],a[104:107],v[192:195]\n"
"s_mov_b32 s51,48\n"
RT"a[136:137],v242 offset:16384\n"
SA"s49,s88,278528\n"
MF"v[192:195],a[180:183],a[120:123],v[192:195]\n"
"s_cmp_eq_u32 s57,2\n"
"s_cselect_b32 s51,848,s51\n"
"v_min_u32 v238,s51,v224\n"
MF"v[200:203],a[180:183],a[124:127],v[200:203]\n"
RT"a[138:139],v242 offset:16896\n"
"v_add_u32 v238,s50,v238\n"
MF"v[200:203],a[176:179],a[108:111],v[200:203]\n"
"s_mov_b32 s16,0\n"
BS"v[72:75],v250,s[8:11],s49 offen offset:0 sc1\n"
MF"v[180:183],a[184:187],a[96:99],v[180:183]\n"
"s_mov_b32 s20,8\n"
RT"a[140:141],v242 offset:40960\n"
"v_and_b32 v239,0x80,v224\n"
MF"v[180:183],a[188:191],a[112:115],v[180:183]\n"
"s_mov_b32 s18,2130706432\n"
RT"a[142:143],v242 offset:41472\n"
"v_lshl_add_u32 v236,v239,23,v224\n"
MF"v[188:191],a[188:191],a[116:119],v[188:191]\n"
"s_lshl_b32 s21,s32,4\n"
"s_lshr_b32 s22,s30,1\n"
MF"v[188:191],a[184:187],a[100:103],v[188:191]\n"
SA"s21,s21,s22\n"
RT"a[144:145],v247 offset:16384\n"
MF"v[196:199],a[184:187],a[104:107],v[196:199]\n"
"s_mul_i32 s21,s21,40960\n"
"s_lshl_b32 s22,s33,1\n"
MF"v[196:199],a[188:191],a[120:123],v[196:199]\n"
"s_and_b32 s23,s30,1\n"
RT"a[146:147],v247 offset:16896\n"
MF"v[204:207],a[188:191],a[124:127],v[204:207]\n"
SA"s22,s22,s23\n"
RT"a[148:149],v247 offset:40960\n"
"s_mul_i32 s22,s22,160\n"
MF"v[204:207],a[184:187],a[108:111],v[204:207]\n"
WT"vmcnt(37) lgkmcnt(4)\n"
SA"s17,s21,s22\n"
MF"v[80:83],a[128:131],a[192:195],v[80:83]\n"
WT"vmcnt(32)\n"
RT"a[150:151],v247 offset:41472\n"
"v_lshrrev_b32 v240,4,v224\n"
MF"v[80:83],a[132:135],a[208:211],v[80:83]\n"
WT"vmcnt(31)\n"
RT"a[152:153],v243 offset:16384\n"
"v_add_u32 v240,0,v240\n"
MF"v[88:91],a[132:135],a[212:215],v[88:91]\n"
"v_mul_u32_u24 v240,205,v240\n"
"v_lshrrev_b32 v240,11,v240\n"
MF"v[88:91],a[128:131],a[196:199],v[88:91]\n"
RT"a[154:155],v243 offset:16896\n"
"v_mul_u32_u24 v240,1120,v240\n"
MF"v[96:99],a[128:131],a[200:203],v[96:99]\n"
WT"vmcnt(30)\n"
RT"a[156:157],v243 offset:40960\n"
"v_add_u32 v240,v240,v224\n"
MF"v[96:99],a[132:135],a[216:219],v[96:99]\n"
WT"vmcnt(28)\n"
"v_lshrrev_b32 v241,4,v224\n"
"v_add_u32 v241,64,v241\n"
MF"v[104:107],a[132:135],a[220:223],v[104:107]\n"
RT"a[158:159],v243 offset:41472\n"
"v_mul_u32_u24 v241,205,v241\n"
MF"v[104:107],a[128:131],a[204:207],v[104:107]\n"
WT"vmcnt(26)\n"
"v_lshrrev_b32 v241,11,v241\n"
"v_mul_u32_u24 v241,1120,v241\n"
MF"v[80:83],a[136:139],a[224:227],v[80:83]\n"
WT"vmcnt(17) lgkmcnt(3)\n"
RT"a[160:161],v248 offset:16384\n"
"v_add_u32 v241,v241,v224\n"
MF"v[80:83],a[140:143],a[240:243],v[80:83]\n"
WT"vmcnt(16)\n"
"v_lshrrev_b32 v251,4,v224\n"
"v_add_u32 v251,128,v251\n"
MF"v[88:91],a[140:143],a[244:247],v[88:91]\n"
RT"a[162:163],v248 offset:16896\n"
"v_mul_u32_u24 v251,205,v251\n"
MF"v[88:91],a[136:139],a[228:231],v[88:91]\n"
RT"a[164:165],v248 offset:40960\n"
"v_lshrrev_b32 v251,11,v251\n"
MF"v[96:99],a[136:139],a[232:235],v[96:99]\n"
WT"vmcnt(14)\n"
"v_mul_u32_u24 v251,1120,v251\n"
"v_add_u32 v251,v251,v224\n"
MF"v[96:99],a[140:143],a[248:251],v[96:99]\n"
WT"vmcnt(12)\n"
RT"a[166:167],v248 offset:41472\n"
"v_and_b32 v237,512,v224\n"
MF"v[104:107],a[140:143],a[252:255],v[104:107]\n"
RT"a[168:169],v244 offset:16384\n"
"v_lshl_add_u32 v251,v237,20,v251\n"
MF"v[104:107],a[136:139],a[236:239],v[104:107]\n"
"v_lshrrev_b32 v252,6,v224\n"
"v_mul_u32_u24 v252,160,v252\n"
MF"v[84:87],a[144:147],a[192:195],v[84:87]\n"
RT"a[170:171],v244 offset:16896\n"
"v_bfe_u32 v237,v224,4,2\n"
MF"v[84:87],a[148:151],a[208:211],v[84:87]\n"
"s_cmp_eq_u32 s34,7\n"
RT"a[172:173],v244 offset:40960\n"
"v_lshl_add_u32 v252,v237,3,v252\n"
MF"v[92:95],a[148:151],a[212:215],v[92:95]\n"
"s_cselect_b32 s57,3,s57\n"
"v_lshrrev_b32 v254,4,v224\n"
"v_min_u32 v254,49,v254\n"
MF"v[92:95],a[144:147],a[196:199],v[92:95]\n"
RT"a[174:175],v244 offset:41472\n"
"v_mul_u32_u24 v253,52,v254\n"
MF"v[100:103],a[144:147],a[200:203],v[100:103]\n"
"v_lshrrev_b32 v253,8,v253\n"
MF"v[100:103],a[148:151],a[216:219],v[100:103]\n"
RT"a[176:177],v249 offset:16384\n"
"v_mul_u32_u24 v253,2901,v253\n"
MF"v[108:111],a[148:151],a[220:223],v[108:111]\n"
"v_mul_u32_u24 v254,111,v254\n"
"v_add_u32 v254,v254,v253\n"
MF"v[108:111],a[144:147],a[204:207],v[108:111]\n"
WT"lgkmcnt(3)\n"
RT"a[178:179],v249 offset:16896\n"
"v_and_b32 v254,-4,v254\n"
MF"v[84:87],a[152:155],a[224:227],v[84:87]\n"
RT"a[180:181],v249 offset:40960\n"
"v_lshrrev_b32 v253,5,v224\n"
MF"v[84:87],a[156:159],a[240:243],v[84:87]\n"
BS"v[76:79],v250,s[8:11],s49 offen offset:1024 sc1\n"
MF"v[92:95],a[156:159],a[244:247],v[92:95]\n"
RT"a[182:183],v249 offset:41472\n"
"v_min_u32 v253,13,v253\n"
MF"v[92:95],a[152:155],a[228:231],v[92:95]\n"
RT"a[184:185],v245 offset:16384\n"
"v_mul_u32_u24 v253,1280,v253\n"
MF"v[100:103],a[152:155],a[232:235],v[100:103]\n"
"v_bfe_u32 v237,v224,4,1\n"
"v_mul_u32_u24 v237,156,v237\n"
MF"v[100:103],a[156:159],a[248:251],v[100:103]\n"
"s_cmp_lt_u32 s30,2\n"
RT"a[186:187],v245 offset:16896\n"
"v_add_u32 v253,v253,v237\n"
MF"v[108:111],a[156:159],a[252:255],v[108:111]\n"
"s_cselect_b64 s[12:13],s[78:79],s[80:81]\n"
RT"a[188:189],v245 offset:40960\n"
"s_and_b32 s13,s13,0xffff\n"
MF"v[108:111],a[152:155],a[236:239],v[108:111]\n"
"s_mov_b32 s14,2211840\n"
"s_mov_b32 s15,0x20000\n"
MF"v[112:115],a[160:163],a[192:195],v[112:115]\n"
"s_lshr_b32 s90,s31,3\n"
RT"a[190:191],v245 offset:41472\n"
MF"v[112:115],a[164:167],a[208:211],v[112:115]\n"
"s_lshl_b32 s91,s90,1\n"
MF"v[120:123],a[164:167],a[212:215],v[120:123]\n"
"s_and_b32 s50,s30,1\n"
RT"a[128:129],v246 offset:20480\n"
PK"v80,v80,v81\n"
MF"v[120:123],a[160:163],a[196:199],v[120:123]\n"
SA"s91,s91,s50\n"
PK"v81,v82,v83\n"
PK"v82,v84,v85\n"
MF"v[128:131],a[160:163],a[200:203],v[128:131]\n"
"s_mul_i32 s91,s91,34560\n"
RT"a[130:131],v246 offset:20992\n"
PK"v83,v86,v87\n"
MF"v[128:131],a[164:167],a[216:219],v[128:131]\n"
"s_mul_i32 s50,s32,448\n"
RT"a[132:133],v246 offset:45056\n"
"v_accvgpr_write_b32 a0,v80\n"
MF"v[136:139],a[164:167],a[220:223],v[136:139]\n"
SA"s91,s91,s50\n"
"v_accvgpr_write_b32 a1,v81\n"
"v_accvgpr_write_b32 a2,v82\n"
MF"v[136:139],a[160:163],a[204:207],v[136:139]\n"
"buffer_load_dword v255,v254,s[12:15],s91 offen\n"
MF"v[112:115],a[168:171],a[224:227],v[112:115]\n"
WT"lgkmcnt(3)\n"
RT"a[134:135],v246 offset:45568\n"
RT"a[136:137],v242 offset:20480\n"
MF"v[112:115],a[172:175],a[240:243],v[112:115]\n"
"s_mov_b32 s12,s82\n"
"v_accvgpr_write_b32 a3,v83\n"
PK"v88,v88,v89\n"
MF"v[120:123],a[172:175],a[244:247],v[120:123]\n"
"s_and_b32 s13,s83,0xffff\n"
RT"a[138:139],v242 offset:20992\n"
PK"v89,v90,v91\n"
MF"v[120:123],a[168:171],a[228:231],v[120:123]\n"
"s_lshl_b32 s90,s90,2\n"
RT"a[140:141],v242 offset:45056\n"
PK"v90,v92,v93\n"
MF"v[128:131],a[168:171],a[232:235],v[128:131]\n"
SA"s90,s90,s30\n"
PK"v91,v94,v95\n"
"v_accvgpr_write_b32 a16,v88\n"
MF"v[128:131],a[172:175],a[248:251],v[128:131]\n"
"s_mul_i32 s90,s90,17920\n"
RT"a[142:143],v242 offset:45568\n"
"v_accvgpr_write_b32 a17,v89\n"
MF"v[136:139],a[172:175],a[252:255],v[136:139]\n"
"s_mul_i32 s50,s32,160\n"
"v_accvgpr_write_b32 a18,v90\n"
"v_accvgpr_write_b32 a19,v91\n"
MF"v[136:139],a[168:171],a[236:239],v[136:139]\n"
SA"s90,s90,s50\n"
RT"a[144:145],v247 offset:20480\n"
PK"v96,v96,v97\n"
MF"v[116:119],a[176:179],a[192:195],v[116:119]\n"
"buffer_load_dword v255,v253,s[12:15],s90 offen\n"
MF"v[116:119],a[180:183],a[208:211],v[116:119]\n"
RT"a[146:147],v247 offset:20992\n"
"v_min_u32 v253,624,v224\n"
MF"v[124:127],a[180:183],a[212:215],v[124:127]\n"
RT"a[148:149],v247 offset:45056\n"
"v_lshlrev_b32 v253,3,v253\n"
MF"v[124:127],a[176:179],a[196:199],v[124:127]\n"
PK"v97,v98,v99\n"
PK"v98,v100,v101\n"
MF"v[132:135],a[176:179],a[200:203],v[132:135]\n"
RT"a[150:151],v247 offset:45568\n"
PK"v99,v102,v103\n"
MF"v[132:135],a[180:183],a[216:219],v[132:135]\n"
RT"a[152:153],v243 offset:20480\n"
"v_accvgpr_write_b32 a32,v96\n"
MF"v[140:143],a[180:183],a[220:223],v[140:143]\n"
"v_accvgpr_write_b32 a33,v97\n"
"v_accvgpr_write_b32 a34,v98\n"
MF"v[140:143],a[176:179],a[204:207],v[140:143]\n"
RT"a[154:155],v243 offset:20992\n"
"v_accvgpr_write_b32 a35,v99\n"
MF"v[116:119],a[184:187],a[224:227],v[116:119]\n"
RT"a[156:157],v243 offset:45056\n"
PK"v104,v104,v105\n"
MF"v[116:119],a[188:191],a[240:243],v[116:119]\n"
PK"v105,v106,v107\n"
PK"v106,v108,v109\n"
MF"v[124:127],a[188:191],a[244:247],v[124:127]\n"
RT"a[158:159],v243 offset:45568\n"
PK"v107,v110,v111\n"
MF"v[124:127],a[184:187],a[228:231],v[124:127]\n"
"v_accvgpr_write_b32 a48,v104\n"
"v_accvgpr_write_b32 a49,v105\n"
MF"v[132:135],a[184:187],a[232:235],v[132:135]\n"
RT"a[160:161],v248 offset:20480\n"
"v_accvgpr_write_b32 a50,v106\n"
MF"v[132:135],a[188:191],a[248:251],v[132:135]\n"
"v_accvgpr_write_b32 a51,v107\n"
MF"v[140:143],a[188:191],a[252:255],v[140:143]\n"
RT"a[162:163],v248 offset:20992\n"
MF"v[140:143],a[184:187],a[236:239],v[140:143]\n"
WT"lgkmcnt(3)\n"
RT"a[164:165],v248 offset:45056\n"
MF"v[144:147],a[128:131],a[192:195],v[144:147]\n"
MF"v[144:147],a[132:135],a[208:211],v[144:147]\n"
RT"a[166:167],v248 offset:45568\n"
MF"v[152:155],a[132:135],a[212:215],v[152:155]\n"
RT"a[168:169],v244 offset:20480\n"
PK"v112,v112,v113\n"
MF"v[152:155],a[128:131],a[196:199],v[152:155]\n"
PK"v113,v114,v115\n"
PK"v114,v116,v117\n"
MF"v[160:163],a[128:131],a[200:203],v[160:163]\n"
RT"a[170:171],v244 offset:20992\n"
PK"v115,v118,v119\n"
MF"v[160:163],a[132:135],a[216:219],v[160:163]\n"
RT"a[172:173],v244 offset:45056\n"
"v_accvgpr_write_b32 a4,v112\n"
MF"v[168:171],a[132:135],a[220:223],v[168:171]\n"
"v_accvgpr_write_b32 a5,v113\n"
"v_accvgpr_write_b32 a6,v114\n"
MF"v[168:171],a[128:131],a[204:207],v[168:171]\n"
RT"a[174:175],v244 offset:45568\n"
"v_accvgpr_write_b32 a7,v115\n"
MF"v[144:147],a[136:139],a[224:227],v[144:147]\n"
PK"v120,v120,v121\n"
PK"v121,v122,v123\n"
MF"v[144:147],a[140:143],a[240:243],v[144:147]\n"
RT"a[176:177],v249 offset:20480\n"
PK"v122,v124,v125\n"
MF"v[152:155],a[140:143],a[244:247],v[152:155]\n"
PK"v123,v126,v127\n"
"v_accvgpr_write_b32 a20,v120\n"
MF"v[152:155],a[136:139],a[228:231],v[152:155]\n"
RT"a[178:179],v249 offset:20992\n"
"v_accvgpr_write_b32 a21,v121\n"
MF"v[160:163],a[136:139],a[232:235],v[160:163]\n"
RT"a[180:181],v249 offset:45056\n"
"v_accvgpr_write_b32 a22,v122\n"
MF"v[160:163],a[140:143],a[248:251],v[160:163]\n"
"v_accvgpr_write_b32 a23,v123\n"
MF"v[168:171],a[140:143],a[252:255],v[168:171]\n"
RT"a[182:183],v249 offset:45568\n"
PK"v128,v128,v129\n"
MF"v[168:171],a[136:139],a[236:239],v[168:171]\n"
RT"a[184:185],v245 offset:20480\n"
PK"v129,v130,v131\n"
MF"v[148:151],a[144:147],a[192:195],v[148:151]\n"
PK"v130,v132,v133\n"
PK"v131,v134,v135\n"
MF"v[148:151],a[148:151],a[208:211],v[148:151]\n"
RT"a[186:187],v245 offset:20992\n"
"v_accvgpr_write_b32 a36,v128\n"
MF"v[156:159],a[148:151],a[212:215],v[156:159]\n"
RT"a[188:189],v245 offset:45056\n"
"v_accvgpr_write_b32 a37,v129\n"
MF"v[156:159],a[144:147],a[196:199],v[156:159]\n"
"v_accvgpr_write_b32 a38,v130\n"
"v_accvgpr_write_b32 a39,v131\n"
MF"v[164:167],a[144:147],a[200:203],v[164:167]\n"
RT"a[190:191],v245 offset:45568\n"
PK"v136,v136,v137\n"
MF"v[164:167],a[148:151],a[216:219],v[164:167]\n"
PK"v137,v138,v139\n"
PK"v138,v140,v141\n"
MF"v[172:175],a[148:151],a[220:223],v[172:175]\n"
PK"v139,v142,v143\n"
"v_accvgpr_write_b32 a52,v136\n"
MF"v[172:175],a[144:147],a[204:207],v[172:175]\n"
"v_accvgpr_write_b32 a53,v137\n"
"v_accvgpr_write_b32 a54,v138\n"
MF"v[148:151],a[152:155],a[224:227],v[148:151]\n"
WT"lgkmcnt(2)\n"
"v_accvgpr_write_b32 a55,v139\n"
MF"v[148:151],a[156:159],a[240:243],v[148:151]\n"
MF"v[156:159],a[156:159],a[244:247],v[156:159]\n"
MF"v[156:159],a[152:155],a[228:231],v[156:159]\n"
MF"v[164:167],a[152:155],a[232:235],v[164:167]\n"
MF"v[164:167],a[156:159],a[248:251],v[164:167]\n"
MF"v[172:175],a[156:159],a[252:255],v[172:175]\n"
MF"v[172:175],a[152:155],a[236:239],v[172:175]\n"
MF"v[176:179],a[160:163],a[192:195],v[176:179]\n"
MF"v[176:179],a[164:167],a[208:211],v[176:179]\n"
MF"v[176:179],a[168:171],a[224:227],v[176:179]\n"
PK"v144,v144,v145\n"
PK"v145,v146,v147\n"
MF"v[176:179],a[172:175],a[240:243],v[176:179]\n"
PK"v146,v148,v149\n"
PK"v147,v150,v151\n"
MF"v[180:183],a[176:179],a[192:195],v[180:183]\n"
"v_mov_b32 v254,0x3f803f80\n"
"v_accvgpr_write_b32 a8,v144\n"
MF"v[180:183],a[180:183],a[208:211],v[180:183]\n"
"v_accvgpr_write_b32 a9,v145\n"
"v_accvgpr_write_b32 a128,v254\n"
MF"v[180:183],a[184:187],a[224:227],v[180:183]\n"
WT"lgkmcnt(0)\n"
"v_accvgpr_write_b32 a129,v254\n"
"v_accvgpr_write_b32 a130,v254\n"
MF"v[180:183],a[188:191],a[240:243],v[180:183]\n"
"v_accvgpr_write_b32 a131,v254\n"
"v_accvgpr_write_b32 a10,v146\n"
MF"v[184:187],a[160:163],a[196:199],v[184:187]\n"
"v_accvgpr_write_b32 a11,v147\n"
PK"v152,v152,v153\n"
MF"v[184:187],a[164:167],a[212:215],v[184:187]\n"
PK"v153,v154,v155\n"
PK"v154,v156,v157\n"
MF"v[184:187],a[168:171],a[228:231],v[184:187]\n"
PK"v155,v158,v159\n"
"v_accvgpr_write_b32 a24,v152\n"
MF"v[184:187],a[172:175],a[244:247],v[184:187]\n"
"v_accvgpr_write_b32 a25,v153\n"
PK"v176,v176,v177\n"
MF"v[188:191],a[176:179],a[196:199],v[188:191]\n"
PK"v177,v178,v179\n"
"v_accvgpr_write_b32 a26,v154\n"
MF"v[188:191],a[180:183],a[212:215],v[188:191]\n"
"v_accvgpr_write_b32 a27,v155\n"
PK"v178,v180,v181\n"
MF"v[188:191],a[184:187],a[228:231],v[188:191]\n"
PK"v179,v182,v183\n"
"v_accvgpr_write_b32 a12,v176\n"
MF"v[188:191],a[188:191],a[244:247],v[188:191]\n"
"v_accvgpr_write_b32 a13,v177\n"
"v_accvgpr_write_b32 a14,v178\n"
MF"v[192:195],a[160:163],a[200:203],v[192:195]\n"
"v_accvgpr_write_b32 a15,v179\n"
PK"v160,v160,v161\n"
MF"v[192:195],a[164:167],a[216:219],v[192:195]\n"
WT"vmcnt(14)\n"
"s_barrier\n"
MF"v[192:195],a[168:171],a[232:235],v[192:195]\n"
RD"a[64:67],v226 offset:0\n"
PK"v161,v162,v163\n"
MF"v[192:195],a[172:175],a[248:251],v[192:195]\n"
PK"v162,v164,v165\n"
PK"v163,v166,v167\n"
MF"v[196:199],a[176:179],a[200:203],v[196:199]\n"
RD"a[68:71],v226 offset:1024\n"
"v_accvgpr_write_b32 a40,v160\n"
MF"v[196:199],a[180:183],a[216:219],v[196:199]\n"
"v_accvgpr_write_b32 a41,v161\n"
"v_accvgpr_write_b32 a42,v162\n"
MF"v[196:199],a[184:187],a[232:235],v[196:199]\n"
RD"a[72:75],v226 offset:2048\n"
"v_accvgpr_write_b32 a43,v163\n"
MF"v[196:199],a[188:191],a[248:251],v[196:199]\n"
PK"v184,v184,v185\n"
PK"v185,v186,v187\n"
MF"v[200:203],a[160:163],a[204:207],v[200:203]\n"
RD"a[76:79],v226 offset:3072\n"
PK"v186,v188,v189\n"
MF"v[200:203],a[164:167],a[220:223],v[200:203]\n"
PK"v187,v190,v191\n"
PK"v168,v168,v169\n"
MF"v[200:203],a[168:171],a[236:239],v[200:203]\n"
RD"a[80:83],v226 offset:4096\n"
PK"v169,v170,v171\n"
MF"v[200:203],a[172:175],a[252:255],v[200:203]\n"
"v_accvgpr_write_b32 a28,v184\n"
"v_accvgpr_write_b32 a29,v185\n"
MF"v[204:207],a[176:179],a[204:207],v[204:207]\n"
RD"a[84:87],v226 offset:5120\n"
PK"v170,v172,v173\n"
MF"v[204:207],a[180:183],a[220:223],v[204:207]\n"
PK"v171,v174,v175\n"
"v_accvgpr_write_b32 a30,v186\n"
MF"v[204:207],a[184:187],a[236:239],v[204:207]\n"
"v_accvgpr_write_b32 a31,v187\n"
"v_accvgpr_write_b32 a56,v168\n"
MF"v[204:207],a[188:191],a[252:255],v[204:207]\n"
"v_accvgpr_write_b32 a57,v169\n"
"v_accvgpr_write_b32 a58,v170\n"
"v_accvgpr_write_b32 a59,v171\n"
RD"a[88:91],v226 offset:6144\n"
"s_cmp_eq_u32 s34,0\n"
"s_cbranch_scc1 LFA_%=\n"
WT"lgkmcnt(6)\n"
MF"v[0:3],a[64:67],a[0:3],0\n"
WT"lgkmcnt(3)\n"
PK"v192,v192,v193\n"
PK"v193,v194,v195\n"
MF"v[0:3],a[68:71],a[4:7],v[0:3]\n"
"s_mov_b32 m0,s40\n"
"v_readlane_b32 s43,v228,s42\n"
PK"v194,v196,v197\n"
MF"v[8:11],a[68:71],a[20:23],0\n"
PK"v195,v198,v199\n"
"v_readlane_b32 s44,v229,s42\n"
MF"v[8:11],a[64:67],a[16:19],v[8:11]\n"
SA"s42,s42,1\n"
BL"v225,s[8:11],s43 offen offset:0 sc1 lds\n"
MF"v[16:19],a[64:67],a[32:35],0\n"
"v_accvgpr_write_b32 a44,v192\n"
"v_accvgpr_write_b32 a45,v193\n"
MF"v[16:19],a[68:71],a[36:39],v[16:19]\n"
RD"a[92:95],v226 offset:7168\n"
"v_accvgpr_write_b32 a46,v194\n"
MF"v[24:27],a[68:71],a[52:55],0\n"
"v_accvgpr_write_b32 a47,v195\n"
PK"v200,v200,v201\n"
MF"v[24:27],a[64:67],a[48:51],v[24:27]\n"
PK"v201,v202,v203\n"
PK"v202,v204,v205\n"
"v_mov_b32 v227,v226\n"
MF"v[0:3],a[72:75],a[8:11],v[0:3]\n"
BL"v225,s[8:11],s43 offen offset:1024 sc1 lds\n"
PK"v203,v206,v207\n"
MF"v[0:3],a[76:79],a[12:15],v[0:3]\n"
"v_accvgpr_write_b32 a60,v200\n"
"v_accvgpr_write_b32 a61,v201\n"
MF"v[8:11],a[76:79],a[28:31],v[8:11]\n"
"s_mul_i32 s48,s32,655360\n"
"v_accvgpr_write_b32 a62,v202\n"
"v_accvgpr_write_b32 a63,v203\n"
MF"v[8:11],a[72:75],a[24:27],v[8:11]\n"
SA"s12,s74,s48\n"
"s_addc_u32 s13,s75,0\n"
MF"v[16:19],a[72:75],a[40:43],v[16:19]\n"
"s_and_b32 s13,s13,0xffff\n"
MF"v[16:19],a[76:79],a[44:47],v[16:19]\n"
"s_mov_b32 s14,655360\n"
RD"a[96:99],v226 offset:8192\n"
MF"v[24:27],a[76:79],a[60:63],v[24:27]\n"
"s_lshl_b32 s48,s33,3\n"
BL"v225,s[8:11],s43 offen offset:2048 sc1 lds\n"
MF"v[24:27],a[72:75],a[56:59],v[24:27]\n"
WT"lgkmcnt(1)\n"
EX"v0,v0\n"
MF"v[4:7],a[80:83],a[0:3],0\n"
"s_and_b32 s49,s34,2\n"
EX"v1,v1\n"
MF"v[4:7],a[84:87],a[4:7],v[4:7]\n"
"s_lshl_b32 s49,s49,1\n"
EX"v2,v2\n"
MF"v[12:15],a[84:87],a[20:23],0\n"
SA"s48,s48,s49\n"
RD"a[100:103],v226 offset:9216\n"
PK"v0,v0,v1\n"
MF"v[12:15],a[80:83],a[16:19],v[12:15]\n"
SA"s48,s48,s30\n"
EX"v3,v3\n"
MF"v[20:23],a[80:83],a[32:35],0\n"
"s_mul_i32 s48,s48,5120\n"
EX"v8,v8\n"
MF"v[20:23],a[84:87],a[36:39],v[20:23]\n"
"s_or_b32 s49,s34,2\n"
BL"v225,s[8:11],s43 offen offset:3072 sc1 lds\n"
MF"v[28:31],a[84:87],a[52:55],0\n"
"s_cmp_eq_u32 s49,7\n"
EX"v9,v9\n"
PK"v1,v2,v3\n"
MF"v[28:31],a[80:83],a[48:51],v[28:31]\n"
"s_cselect_b32 s55,s48,2130706432\n"
RD"a[104:107],v226 offset:10240\n"
PK"v8,v8,v9\n"
MF"v[4:7],a[88:91],a[8:11],v[4:7]\n"
SA"m0,s40,16384\n"
EX"v10,v10\n"
MF"v[4:7],a[92:95],a[12:15],v[4:7]\n"
EX"v11,v11\n"
MF"v[12:15],a[92:95],a[28:31],v[12:15]\n"
EX"v16,v16\n"
MF"v[12:15],a[88:91],a[24:27],v[12:15]\n"
SA"s40,s40,32768\n"
RD"a[108:111],v226 offset:11264\n"
PK"v9,v10,v11\n"
MF"v[20:23],a[88:91],a[40:43],v[20:23]\n"
"s_cmp_eq_u32 s40,s46\n"
BL"v225,s[8:11],s44 offen offset:0 sc1 lds\n"
EX"v17,v17\n"
MF"v[20:23],a[92:95],a[44:47],v[20:23]\n"
"s_cselect_b32 s40,s47,s40\n"
EX"v18,v18\n"
MF"v[28:31],a[92:95],a[60:63],v[28:31]\n"
EX"v19,v19\n"
PK"v16,v16,v17\n"
MF"v[28:31],a[88:91],a[56:59],v[28:31]\n"
WT"lgkmcnt(1)\n"
RD"a[112:115],v226 offset:12288\n"
PK"v17,v18,v19\n"
MF"v[32:35],a[96:99],a[0:3],0\n"
EX"v24,v24\n"
MF"v[32:35],a[100:103],a[4:7],v[32:35]\n"
EX"v25,v25\n"
MF"v[40:43],a[100:103],a[20:23],0\n"
BL"v225,s[8:11],s44 offen offset:1024 sc1 lds\n"
EX"v26,v26\n"
MF"v[40:43],a[96:99],a[16:19],v[40:43]\n"
RD"a[116:119],v226 offset:13312\n"
PK"v24,v24,v25\n"
MF"v[48:51],a[96:99],a[32:35],0\n"
EX"v27,v27\n"
MF"v[48:51],a[100:103],a[36:39],v[48:51]\n"
EX"v4,v4\n"
MF"v[56:59],a[100:103],a[52:55],0\n"
EX"v5,v5\n"
PK"v25,v26,v27\n"
MF"v[56:59],a[96:99],a[48:51],v[56:59]\n"
RD"a[120:123],v226 offset:14336\n"
PK"v2,v4,v5\n"
MF"v[32:35],a[104:107],a[8:11],v[32:35]\n"
WT"lgkmcnt(2)\n"
BL"v225,s[8:11],s44 offen offset:2048 sc1 lds\n"
EX"v6,v6\n"
MF"v[32:35],a[108:111],a[12:15],v[32:35]\n"
EX"v7,v7\n"
MF"v[40:43],a[108:111],a[28:31],v[40:43]\n"
EX"v12,v12\n"
MF"v[40:43],a[104:107],a[24:27],v[40:43]\n"
RD"a[124:127],v226 offset:15360\n"
"v_readlane_b32 s43,v228,s42\n"
PK"v3,v6,v7\n"
MF"v[48:51],a[104:107],a[40:43],v[48:51]\n"
EX"v13,v13\n"
MF"v[48:51],a[108:111],a[44:47],v[48:51]\n"
EX"v14,v14\n"
MF"v[56:59],a[108:111],a[60:63],v[56:59]\n"
BL"v225,s[8:11],s44 offen offset:3072 sc1 lds\n"
EX"v15,v15\n"
MF"v[56:59],a[104:107],a[56:59],v[56:59]\n"
"s_mov_b32 m0,s40\n"
RD"a[96:99],v227 offset:24576\n"
PK"v10,v12,v13\n"
MF"v[36:39],a[112:115],a[0:3],0\n"
WT"lgkmcnt(2)\n"
EX"v20,v20\n"
PK"v11,v14,v15\n"
MF"v[36:39],a[116:119],a[4:7],v[36:39]\n"
EX"v21,v21\n"
MF"v[44:47],a[116:119],a[20:23],0\n"
EX"v22,v22\n"
MF"v[44:47],a[112:115],a[16:19],v[44:47]\n"
RD"a[100:103],v227 offset:25600\n"
"v_readlane_b32 s44,v229,s42\n"
PK"v18,v20,v21\n"
MF"v[52:55],a[112:115],a[32:35],0\n"
SA"s42,s42,1\n"
BL"v225,s[8:11],s43 offen offset:0 sc1 lds\n"
MF"v[52:55],a[116:119],a[36:39],v[52:55]\n"
EX"v23,v23\n"
MF"v[60:63],a[116:119],a[52:55],0\n"
EX"v28,v28\n"
MF"v[60:63],a[112:115],a[48:51],v[60:63]\n"
RD"a[104:107],v227 offset:26624\n"
PK"v19,v22,v23\n"
MF"v[36:39],a[120:123],a[8:11],v[36:39]\n"
WT"lgkmcnt(2)\n"
EX"v29,v29\n"
MF"v[36:39],a[124:127],a[12:15],v[36:39]\n"
BL"v225,s[8:11],s43 offen offset:1024 sc1 lds\n"
MF"v[44:47],a[124:127],a[28:31],v[44:47]\n"
EX"v30,v30\n"
PK"v26,v28,v29\n"
MF"v[44:47],a[120:123],a[24:27],v[44:47]\n"
RD"a[108:111],v227 offset:27648\n"
MF"v[52:55],a[120:123],a[40:43],v[52:55]\n"
EX"v31,v31\n"
MF"v[52:55],a[124:127],a[44:47],v[52:55]\n"
EX"v32,v32\n"
MF"v[60:63],a[124:127],a[60:63],v[60:63]\n"
EX"v33,v33\n"
PK"v27,v30,v31\n"
MF"v[60:63],a[120:123],a[56:59],v[60:63]\n"
SA"s41,s41,32768\n"
BL"v225,s[8:11],s43 offen offset:2048 sc1 lds\n"
RD"a[112:115],v227 offset:28672\n"
MF"v[80:83],a[96:99],v[0:3],0\n"
"s_cmp_eq_u32 s41,163840\n"
EX"v34,v34\n"
MF"v[112:115],a[96:99],v[8:11],0\n"
"s_cselect_b32 s41,0,s41\n"
EX"v35,v35\n"
MF"v[144:147],a[96:99],v[16:19],0\n"
EX"v40,v40\n"
"v_add_u32 v226,s41,v224\n"
MF"v[176:179],a[96:99],v[24:27],0\n"
WT"lgkmcnt(1)\n"
RD"a[116:119],v227 offset:29696\n"
MF"v[84:87],a[100:103],v[0:3],0\n"
EX"v41,v41\n"
MF"v[116:119],a[100:103],v[8:11],0\n"
BL"v225,s[8:11],s43 offen offset:3072 sc1 lds\n"
MF"v[148:151],a[100:103],v[16:19],0\n"
SA"m0,s40,16384\n"
EX"v42,v42\n"
MF"v[180:183],a[100:103],v[24:27],0\n"
RD"a[120:123],v227 offset:30720\n"
MF"v[88:91],a[104:107],v[0:3],0\n"
EX"v43,v43\n"
MF"v[120:123],a[104:107],v[8:11],0\n"
EX"v48,v48\n"
MF"v[152:155],a[104:107],v[16:19],0\n"
SA"s40,s40,32768\n"
EX"v49,v49\n"
MF"v[184:187],a[104:107],v[24:27],0\n"
"s_cmp_eq_u32 s40,s46\n"
BL"v225,s[8:11],s44 offen offset:0 sc1 lds\n"
RD"a[124:127],v227 offset:31744\n"
MF"v[92:95],a[108:111],v[0:3],0\n"
"s_cselect_b32 s40,s47,s40\n"
EX"v50,v50\n"
MF"v[124:127],a[108:111],v[8:11],0\n"
EX"v51,v51\n"
MF"v[156:159],a[108:111],v[16:19],0\n"
EX"v56,v56\n"
MF"v[188:191],a[108:111],v[24:27],0\n"
WT"lgkmcnt(1)\n"
EX"v57,v57\n"
MF"v[96:99],a[112:115],v[0:3],0\n"
EX"v58,v58\n"
MF"v[128:131],a[112:115],v[8:11],0\n"
BL"v225,s[8:11],s44 offen offset:1024 sc1 lds\n"
MF"v[160:163],a[112:115],v[16:19],0\n"
EX"v59,v59\n"
MF"v[192:195],a[112:115],v[24:27],0\n"
EX"v36,v36\n"
MF"v[100:103],a[116:119],v[0:3],0\n"
WT"vmcnt(16) lgkmcnt(0)\n"
"s_barrier\n"
MF"v[132:135],a[116:119],v[8:11],0\n"
RD"a[64:67],v226 offset:0\n"
RD"a[68:71],v226 offset:1024\n"
"buffer_store_dwordx2 v[232:233],v230,s[8:11],s98 offen sc1\n"
MF"v[164:167],a[116:119],v[16:19],0\n"
EX"v37,v37\n"
MF"v[196:199],a[116:119],v[24:27],0\n"
BL"v225,s[8:11],s44 offen offset:2048 sc1 lds\n"
MF"v[104:107],a[120:123],v[0:3],0\n"
EX"v38,v38\n"
MF"v[136:139],a[120:123],v[8:11],0\n"
EX"v39,v39\n"
MF"v[168:171],a[120:123],v[16:19],0\n"
EX"v44,v44\n"
MF"v[200:203],a[120:123],v[24:27],0\n"
RD"a[72:75],v226 offset:2048\n"
RD"a[76:79],v226 offset:3072\n"
MF"v[108:111],a[124:127],v[0:3],0\n"
EX"v45,v45\n"
MF"v[140:143],a[124:127],v[8:11],0\n"
BL"v225,s[8:11],s44 offen offset:3072 sc1 lds\n"
MF"v[172:175],a[124:127],v[16:19],0\n"
EX"v46,v46\n"
MF"v[204:207],a[124:127],v[24:27],0\n"
RD"a[80:83],v226 offset:4096\n"
MF"a[132:135],a[128:131],v[0:3],0\n"
EX"v47,v47\n"
MF"a[136:139],a[128:131],v[8:11],0\n"
"buffer_load_dword v255,v253,s[12:15],s55 offen\n"
EX"v52,v52\n"
MF"a[140:143],a[128:131],v[16:19],0\n"
SA"s55,s55,163840\n"
EX"v53,v53\n"
MF"a[144:147],a[128:131],v[24:27],0\n"
RD"a[84:87],v226 offset:5120\n"
RD"a[88:91],v226 offset:6144\n"
EX"v54,v54\n"
EX"v55,v55\n"
EX"v60,v60\n"
EX"v61,v61\n"
EX"v62,v62\n"
EX"v63,v63\n"
"s_cmp_eq_u32 s57,1\n"
"s_cbranch_scc1 LEV_%=\n"
".p2align 6\n"
"LP1_%=:\n"
WT"lgkmcnt(6)\n"
MF"v[0:3],a[64:67],a[0:3],0\n"
WT"lgkmcnt(5)\n"
"v_readlane_b32 s43,v228,s42\n"
PK"v64,v32,v33\n"
MF"v[0:3],a[68:71],a[4:7],v[0:3]\n"
"s_mov_b32 m0,s40\n"
"v_readlane_b32 s44,v229,s42\n"
PK"v65,v34,v35\n"
MF"v[8:11],a[68:71],a[20:23],0\n"
PK"v66,v36,v37\n"
PK"v67,v38,v39\n"
MF"v[8:11],a[64:67],a[16:19],v[8:11]\n"
SA"s42,s42,1\n"
BL"v225,s[8:11],s43 offen offset:0 sc1 lds\n"
MF"v[16:19],a[64:67],a[32:35],0\n"
PK"v68,v40,v41\n"
PK"v69,v42,v43\n"
MF"v[16:19],a[68:71],a[36:39],v[16:19]\n"
RD"a[92:95],v226 offset:7168\n"
PK"v70,v44,v45\n"
MF"v[24:27],a[68:71],a[52:55],0\n"
PK"v71,v46,v47\n"
PK"v72,v48,v49\n"
MF"v[24:27],a[64:67],a[48:51],v[24:27]\n"
WT"lgkmcnt(1)\n"
BL"v225,s[8:11],s43 offen offset:1024 sc1 lds\n"
MF"v[0:3],a[72:75],a[8:11],v[0:3]\n"
PK"v73,v50,v51\n"
"v_mov_b32 v227,v226\n"
MF"v[0:3],a[76:79],a[12:15],v[0:3]\n"
PK"v74,v52,v53\n"
PK"v75,v54,v55\n"
MF"v[8:11],a[76:79],a[28:31],v[8:11]\n"
PK"v76,v56,v57\n"
PK"v77,v58,v59\n"
MF"v[8:11],a[72:75],a[24:27],v[8:11]\n"
PK"v78,v60,v61\n"
PK"v79,v62,v63\n"
MF"v[16:19],a[72:75],a[40:43],v[16:19]\n"
MF"v[16:19],a[76:79],a[44:47],v[16:19]\n"
RD"a[96:99],v226 offset:8192\n"
MF"v[24:27],a[76:79],a[60:63],v[24:27]\n"
EX"v0,v0\n"
MF"v[24:27],a[72:75],a[56:59],v[24:27]\n"
EX"v1,v1\n"
MF"v[4:7],a[80:83],a[0:3],0\n"
EX"v2,v2\n"
MF"v[4:7],a[84:87],a[4:7],v[4:7]\n"
RD"a[100:103],v226 offset:9216\n"
PK"v0,v0,v1\n"
MF"v[12:15],a[84:87],a[20:23],0\n"
EX"v3,v3\n"
MF"v[12:15],a[80:83],a[16:19],v[12:15]\n"
EX"v8,v8\n"
MF"v[20:23],a[80:83],a[32:35],0\n"
EX"v9,v9\n"
MF"v[20:23],a[84:87],a[36:39],v[20:23]\n"
RD"a[104:107],v226 offset:10240\n"
PK"v1,v2,v3\n"
MF"v[28:31],a[84:87],a[52:55],0\n"
EX"v10,v10\n"
MF"v[28:31],a[80:83],a[48:51],v[28:31]\n"
EX"v11,v11\n"
MF"v[4:7],a[88:91],a[8:11],v[4:7]\n"
WT"lgkmcnt(1)\n"
EX"v16,v16\n"
MF"v[4:7],a[92:95],a[12:15],v[4:7]\n"
RD"a[108:111],v226 offset:11264\n"
PK"v8,v8,v9\n"
MF"v[12:15],a[92:95],a[28:31],v[12:15]\n"
EX"v17,v17\n"
MF"v[12:15],a[88:91],a[24:27],v[12:15]\n"
EX"v18,v18\n"
MF"v[20:23],a[88:91],a[40:43],v[20:23]\n"
EX"v19,v19\n"
MF"v[20:23],a[92:95],a[44:47],v[20:23]\n"
RD"a[112:115],v226 offset:12288\n"
PK"v9,v10,v11\n"
MF"v[28:31],a[92:95],a[60:63],v[28:31]\n"
EX"v24,v24\n"
MF"v[28:31],a[88:91],a[56:59],v[28:31]\n"
EX"v25,v25\n"
MF"v[32:35],a[96:99],a[0:3],0\n"
EX"v26,v26\n"
MF"v[32:35],a[100:103],a[4:7],v[32:35]\n"
RD"a[116:119],v226 offset:13312\n"
PK"v16,v16,v17\n"
MF"v[40:43],a[100:103],a[20:23],0\n"
EX"v27,v27\n"
MF"v[40:43],a[96:99],a[16:19],v[40:43]\n"
EX"v4,v4\n"
MF"v[48:51],a[96:99],a[32:35],0\n"
EX"v5,v5\n"
MF"v[48:51],a[100:103],a[36:39],v[48:51]\n"
RD"a[120:123],v226 offset:14336\n"
PK"v17,v18,v19\n"
MF"v[56:59],a[100:103],a[52:55],0\n"
EX"v6,v6\n"
MF"v[56:59],a[96:99],a[48:51],v[56:59]\n"
WT"lgkmcnt(2)\n"
EX"v7,v7\n"
MF"v[32:35],a[104:107],a[8:11],v[32:35]\n"
EX"v12,v12\n"
MF"v[32:35],a[108:111],a[12:15],v[32:35]\n"
RD"a[124:127],v226 offset:15360\n"
PK"v24,v24,v25\n"
MF"v[40:43],a[108:111],a[28:31],v[40:43]\n"
EX"v13,v13\n"
MF"v[40:43],a[104:107],a[24:27],v[40:43]\n"
EX"v14,v14\n"
MF"v[48:51],a[104:107],a[40:43],v[48:51]\n"
EX"v15,v15\n"
MF"v[48:51],a[108:111],a[44:47],v[48:51]\n"
RD"a[64:67],v227 offset:16384\n"
PK"v25,v26,v27\n"
MF"v[56:59],a[108:111],a[60:63],v[56:59]\n"
EX"v20,v20\n"
MF"v[56:59],a[104:107],a[56:59],v[56:59]\n"
EX"v21,v21\n"
MF"v[36:39],a[112:115],a[0:3],0\n"
WT"lgkmcnt(1)\n"
EX"v22,v22\n"
MF"v[36:39],a[116:119],a[4:7],v[36:39]\n"
RD"a[68:71],v227 offset:24576\n"
PK"v2,v4,v5\n"
MF"v[44:47],a[116:119],a[20:23],0\n"
EX"v23,v23\n"
MF"v[44:47],a[112:115],a[16:19],v[44:47]\n"
EX"v28,v28\n"
MF"v[52:55],a[112:115],a[32:35],0\n"
EX"v29,v29\n"
MF"v[52:55],a[116:119],a[36:39],v[52:55]\n"
RD"a[72:75],v227 offset:17408\n"
PK"v3,v6,v7\n"
MF"v[60:63],a[116:119],a[52:55],0\n"
EX"v30,v30\n"
MF"v[60:63],a[112:115],a[48:51],v[60:63]\n"
EX"v31,v31\n"
MF"v[36:39],a[120:123],a[8:11],v[36:39]\n"
PK"v10,v12,v13\n"
PK"v11,v14,v15\n"
MF"v[36:39],a[124:127],a[12:15],v[36:39]\n"
RD"a[76:79],v227 offset:25600\n"
PK"v18,v20,v21\n"
MF"v[44:47],a[124:127],a[28:31],v[44:47]\n"
PK"v19,v22,v23\n"
PK"v26,v28,v29\n"
MF"v[44:47],a[120:123],a[24:27],v[44:47]\n"
PK"v27,v30,v31\n"
MF"v[52:55],a[120:123],a[40:43],v[52:55]\n"
MF"v[52:55],a[124:127],a[44:47],v[52:55]\n"
RD"a[80:83],v227 offset:18432\n"
MF"v[60:63],a[124:127],a[60:63],v[60:63]\n"
EX"v32,v32\n"
MF"v[60:63],a[120:123],a[56:59],v[60:63]\n"
WT"lgkmcnt(2)\n"
SA"s41,s41,32768\n"
"s_cmp_eq_u32 s41,163840\n"
MF"v[80:83],a[64:67],v[64:67],v[80:83]\n"
"s_cselect_b32 s41,0,s41\n"
EX"v33,v33\n"
MF"v[80:83],a[68:71],v[0:3],v[80:83]\n"
RD"a[84:87],v227 offset:26624\n"
"v_add_u32 v226,s41,v224\n"
MF"v[112:115],a[68:71],v[8:11],v[112:115]\n"
EX"v34,v34\n"
MF"v[112:115],a[64:67],v[68:71],v[112:115]\n"
MF"v[144:147],a[64:67],v[72:75],v[144:147]\n"
EX"v35,v35\n"
MF"v[144:147],a[68:71],v[16:19],v[144:147]\n"
RD"a[88:91],v227 offset:19456\n"
MF"v[176:179],a[68:71],v[24:27],v[176:179]\n"
EX"v40,v40\n"
MF"v[176:179],a[64:67],v[76:79],v[176:179]\n"
BL"v225,s[8:11],s43 offen offset:2048 sc1 lds\n"
MF"v[84:87],a[72:75],v[64:67],v[84:87]\n"
WT"lgkmcnt(1)\n"
EX"v41,v41\n"
MF"v[84:87],a[76:79],v[0:3],v[84:87]\n"
RD"a[92:95],v227 offset:27648\n"
MF"v[116:119],a[76:79],v[8:11],v[116:119]\n"
EX"v42,v42\n"
MF"v[116:119],a[72:75],v[68:71],v[116:119]\n"
MF"v[148:151],a[72:75],v[72:75],v[148:151]\n"
EX"v43,v43\n"
MF"v[148:151],a[76:79],v[16:19],v[148:151]\n"
RD"a[96:99],v227 offset:20480\n"
MF"v[180:183],a[76:79],v[24:27],v[180:183]\n"
EX"v48,v48\n"
MF"v[180:183],a[72:75],v[76:79],v[180:183]\n"
BL"v225,s[8:11],s43 offen offset:3072 sc1 lds\n"
MF"v[88:91],a[80:83],v[64:67],v[88:91]\n"
EX"v49,v49\n"
MF"v[88:91],a[84:87],v[0:3],v[88:91]\n"
RD"a[100:103],v227 offset:28672\n"
MF"v[120:123],a[84:87],v[8:11],v[120:123]\n"
EX"v50,v50\n"
MF"v[120:123],a[80:83],v[68:71],v[120:123]\n"
SA"m0,s40,16384\n"
MF"v[152:155],a[80:83],v[72:75],v[152:155]\n"
EX"v51,v51\n"
MF"v[152:155],a[84:87],v[16:19],v[152:155]\n"
RD"a[104:107],v227 offset:21504\n"
MF"v[184:187],a[84:87],v[24:27],v[184:187]\n"
EX"v56,v56\n"
MF"v[184:187],a[80:83],v[76:79],v[184:187]\n"
WT"lgkmcnt(2)\n"
BL"v225,s[8:11],s44 offen offset:0 sc1 lds\n"
MF"v[92:95],a[88:91],v[64:67],v[92:95]\n"
SA"s40,s40,32768\n"
EX"v57,v57\n"
MF"v[92:95],a[92:95],v[0:3],v[92:95]\n"
"s_cmp_eq_u32 s40,s46\n"
RD"a[108:111],v227 offset:29696\n"
"s_cselect_b32 s40,s47,s40\n"
MF"v[124:127],a[92:95],v[8:11],v[124:127]\n"
EX"v58,v58\n"
MF"v[124:127],a[88:91],v[68:71],v[124:127]\n"
MF"v[156:159],a[88:91],v[72:75],v[156:159]\n"
EX"v59,v59\n"
MF"v[156:159],a[92:95],v[16:19],v[156:159]\n"
RD"a[112:115],v227 offset:22528\n"
MF"v[188:191],a[92:95],v[24:27],v[188:191]\n"
EX"v36,v36\n"
MF"v[188:191],a[88:91],v[76:79],v[188:191]\n"
BL"v225,s[8:11],s44 offen offset:1024 sc1 lds\n"
MF"v[96:99],a[96:99],v[64:67],v[96:99]\n"
WT"lgkmcnt(1)\n"
EX"v37,v37\n"
MF"v[96:99],a[100:103],v[0:3],v[96:99]\n"
RD"a[116:119],v227 offset:30720\n"
MF"v[128:131],a[100:103],v[8:11],v[128:131]\n"
EX"v38,v38\n"
MF"v[128:131],a[96:99],v[68:71],v[128:131]\n"
MF"v[160:163],a[96:99],v[72:75],v[160:163]\n"
EX"v39,v39\n"
MF"v[160:163],a[100:103],v[16:19],v[160:163]\n"
RD"a[120:123],v227 offset:23552\n"
MF"v[192:195],a[100:103],v[24:27],v[192:195]\n"
EX"v44,v44\n"
MF"v[192:195],a[96:99],v[76:79],v[192:195]\n"
BL"v225,s[8:11],s44 offen offset:2048 sc1 lds\n"
MF"v[100:103],a[104:107],v[64:67],v[100:103]\n"
EX"v45,v45\n"
MF"v[100:103],a[108:111],v[0:3],v[100:103]\n"
RD"a[124:127],v227 offset:31744\n"
MF"v[132:135],a[108:111],v[8:11],v[132:135]\n"
EX"v46,v46\n"
MF"v[132:135],a[104:107],v[68:71],v[132:135]\n"
MF"v[164:167],a[104:107],v[72:75],v[164:167]\n"
EX"v47,v47\n"
MF"v[164:167],a[108:111],v[16:19],v[164:167]\n"
MF"v[196:199],a[108:111],v[24:27],v[196:199]\n"
EX"v52,v52\n"
MF"v[196:199],a[104:107],v[76:79],v[196:199]\n"
WT"lgkmcnt(1)\n"
BL"v225,s[8:11],s44 offen offset:3072 sc1 lds\n"
MF"v[104:107],a[112:115],v[64:67],v[104:107]\n"
EX"v53,v53\n"
MF"v[104:107],a[116:119],v[0:3],v[104:107]\n"
"buffer_load_dword v255,v253,s[12:15],s55 offen\n"
MF"v[136:139],a[116:119],v[8:11],v[136:139]\n"
EX"v54,v54\n"
MF"v[136:139],a[112:115],v[68:71],v[136:139]\n"
SA"s55,s55,163840\n"
MF"v[168:171],a[112:115],v[72:75],v[168:171]\n"
WT"vmcnt(17) lgkmcnt(0)\n"
"s_barrier\n"
MF"v[168:171],a[116:119],v[16:19],v[168:171]\n"
RD"a[64:67],v226 offset:0\n"
MF"v[200:203],a[116:119],v[24:27],v[200:203]\n"
EX"v55,v55\n"
MF"v[200:203],a[112:115],v[76:79],v[200:203]\n"
MF"v[108:111],a[120:123],v[64:67],v[108:111]\n"
EX"v60,v60\n"
MF"v[108:111],a[124:127],v[0:3],v[108:111]\n"
RD"a[68:71],v226 offset:1024\n"
MF"v[140:143],a[124:127],v[8:11],v[140:143]\n"
EX"v61,v61\n"
MF"v[140:143],a[120:123],v[68:71],v[140:143]\n"
RD"a[72:75],v226 offset:2048\n"
MF"v[172:175],a[120:123],v[72:75],v[172:175]\n"
EX"v62,v62\n"
MF"v[172:175],a[124:127],v[16:19],v[172:175]\n"
RD"a[76:79],v226 offset:3072\n"
MF"v[204:207],a[124:127],v[24:27],v[204:207]\n"
EX"v63,v63\n"
MF"v[204:207],a[120:123],v[76:79],v[204:207]\n"
MF"a[132:135],a[128:131],v[64:67],a[132:135]\n"
MF"a[132:135],a[128:131],v[0:3],a[132:135]\n"
RD"a[80:83],v226 offset:4096\n"
MF"a[136:139],a[128:131],v[8:11],a[136:139]\n"
MF"a[136:139],a[128:131],v[68:71],a[136:139]\n"
RD"a[84:87],v226 offset:5120\n"
MF"a[140:143],a[128:131],v[72:75],a[140:143]\n"
MF"a[140:143],a[128:131],v[16:19],a[140:143]\n"
RD"a[88:91],v226 offset:6144\n"
MF"a[144:147],a[128:131],v[24:27],a[144:147]\n"
MF"a[144:147],a[128:131],v[76:79],a[144:147]\n"
"s_sub_u32 s56,s56,1\n"
"s_cbranch_scc0 LP1_%=\n"
"s_cmp_eq_u32 s57,2\n"
"s_cbranch_scc1 LEV_%=\n"
"s_cmp_eq_u32 s57,3\n"
"s_cbranch_scc1 LRT_%=\n"
"LDA_%=:\n"
WT"lgkmcnt(6)\n"
MF"v[0:3],a[64:67],a[0:3],v[208:211]\n"
WT"lgkmcnt(5)\n"
"v_readlane_b32 s43,v228,s42\n"
PK"v64,v32,v33\n"
MF"v[0:3],a[68:71],a[4:7],v[0:3]\n"
"s_mov_b32 m0,s40\n"
"v_readlane_b32 s44,v229,s42\n"
PK"v65,v34,v35\n"
MF"v[8:11],a[68:71],a[20:23],0\n"
PK"v66,v36,v37\n"
PK"v67,v38,v39\n"
MF"v[8:11],a[64:67],a[16:19],v[8:11]\n"
SA"s42,s42,1\n"
BL"v225,s[8:11],s43 offen offset:0 sc1 lds\n"
MF"v[16:19],a[64:67],a[32:35],0\n"
PK"v68,v40,v41\n"
PK"v69,v42,v43\n"
MF"v[16:19],a[68:71],a[36:39],v[16:19]\n"
RD"a[92:95],v226 offset:7168\n"
PK"v70,v44,v45\n"
MF"v[24:27],a[68:71],a[52:55],0\n"
PK"v71,v46,v47\n"
PK"v72,v48,v49\n"
MF"v[24:27],a[64:67],a[48:51],v[24:27]\n"
WT"lgkmcnt(1)\n"
BL"v225,s[8:11],s43 offen offset:1024 sc1 lds\n"
MF"v[0:3],a[72:75],a[8:11],v[0:3]\n"
PK"v73,v50,v51\n"
"v_mov_b32 v227,v226\n"
MF"v[0:3],a[76:79],a[12:15],v[0:3]\n"
PK"v74,v52,v53\n"
PK"v75,v54,v55\n"
MF"v[8:11],a[76:79],a[28:31],v[8:11]\n"
PK"v76,v56,v57\n"
PK"v77,v58,v59\n"
MF"v[8:11],a[72:75],a[24:27],v[8:11]\n"
PK"v78,v60,v61\n"
PK"v79,v62,v63\n"
MF"v[16:19],a[72:75],a[40:43],v[16:19]\n"
MF"v[16:19],a[76:79],a[44:47],v[16:19]\n"
RD"a[96:99],v226 offset:8192\n"
MF"v[24:27],a[76:79],a[60:63],v[24:27]\n"
EX"v0,v0\n"
MF"v[24:27],a[72:75],a[56:59],v[24:27]\n"
EX"v1,v1\n"
MF"v[4:7],a[80:83],a[0:3],v[212:215]\n"
EX"v2,v2\n"
MF"v[4:7],a[84:87],a[4:7],v[4:7]\n"
RD"a[100:103],v226 offset:9216\n"
PK"v0,v0,v1\n"
MF"v[12:15],a[84:87],a[20:23],0\n"
EX"v3,v3\n"
MF"v[12:15],a[80:83],a[16:19],v[12:15]\n"
EX"v8,v8\n"
MF"v[20:23],a[80:83],a[32:35],0\n"
EX"v9,v9\n"
MF"v[20:23],a[84:87],a[36:39],v[20:23]\n"
RD"a[104:107],v226 offset:10240\n"
PK"v1,v2,v3\n"
MF"v[28:31],a[84:87],a[52:55],0\n"
EX"v10,v10\n"
MF"v[28:31],a[80:83],a[48:51],v[28:31]\n"
EX"v11,v11\n"
MF"v[4:7],a[88:91],a[8:11],v[4:7]\n"
WT"lgkmcnt(1)\n"
EX"v16,v16\n"
MF"v[4:7],a[92:95],a[12:15],v[4:7]\n"
RD"a[108:111],v226 offset:11264\n"
PK"v8,v8,v9\n"
MF"v[12:15],a[92:95],a[28:31],v[12:15]\n"
EX"v17,v17\n"
MF"v[12:15],a[88:91],a[24:27],v[12:15]\n"
EX"v18,v18\n"
MF"v[20:23],a[88:91],a[40:43],v[20:23]\n"
EX"v19,v19\n"
MF"v[20:23],a[92:95],a[44:47],v[20:23]\n"
RD"a[112:115],v226 offset:12288\n"
PK"v9,v10,v11\n"
MF"v[28:31],a[92:95],a[60:63],v[28:31]\n"
EX"v24,v24\n"
MF"v[28:31],a[88:91],a[56:59],v[28:31]\n"
EX"v25,v25\n"
MF"v[32:35],a[96:99],a[0:3],v[216:219]\n"
EX"v26,v26\n"
MF"v[32:35],a[100:103],a[4:7],v[32:35]\n"
RD"a[116:119],v226 offset:13312\n"
PK"v16,v16,v17\n"
MF"v[40:43],a[100:103],a[20:23],0\n"
EX"v27,v27\n"
MF"v[40:43],a[96:99],a[16:19],v[40:43]\n"
EX"v4,v4\n"
MF"v[48:51],a[96:99],a[32:35],0\n"
EX"v5,v5\n"
MF"v[48:51],a[100:103],a[36:39],v[48:51]\n"
RD"a[120:123],v226 offset:14336\n"
PK"v17,v18,v19\n"
MF"v[56:59],a[100:103],a[52:55],0\n"
EX"v6,v6\n"
MF"v[56:59],a[96:99],a[48:51],v[56:59]\n"
WT"lgkmcnt(2)\n"
EX"v7,v7\n"
MF"v[32:35],a[104:107],a[8:11],v[32:35]\n"
EX"v12,v12\n"
MF"v[32:35],a[108:111],a[12:15],v[32:35]\n"
RD"a[124:127],v226 offset:15360\n"
PK"v24,v24,v25\n"
MF"v[40:43],a[108:111],a[28:31],v[40:43]\n"
EX"v13,v13\n"
MF"v[40:43],a[104:107],a[24:27],v[40:43]\n"
EX"v14,v14\n"
MF"v[48:51],a[104:107],a[40:43],v[48:51]\n"
EX"v15,v15\n"
MF"v[48:51],a[108:111],a[44:47],v[48:51]\n"
RD"a[64:67],v227 offset:16384\n"
PK"v25,v26,v27\n"
MF"v[56:59],a[108:111],a[60:63],v[56:59]\n"
EX"v20,v20\n"
MF"v[56:59],a[104:107],a[56:59],v[56:59]\n"
EX"v21,v21\n"
MF"v[36:39],a[112:115],a[0:3],v[220:223]\n"
WT"lgkmcnt(1)\n"
EX"v22,v22\n"
MF"v[36:39],a[116:119],a[4:7],v[36:39]\n"
RD"a[68:71],v227 offset:24576\n"
PK"v2,v4,v5\n"
MF"v[44:47],a[116:119],a[20:23],0\n"
EX"v23,v23\n"
MF"v[44:47],a[112:115],a[16:19],v[44:47]\n"
EX"v28,v28\n"
MF"v[52:55],a[112:115],a[32:35],0\n"
EX"v29,v29\n"
MF"v[52:55],a[116:119],a[36:39],v[52:55]\n"
RD"a[72:75],v227 offset:17408\n"
PK"v3,v6,v7\n"
MF"v[60:63],a[116:119],a[52:55],0\n"
EX"v30,v30\n"
MF"v[60:63],a[112:115],a[48:51],v[60:63]\n"
EX"v31,v31\n"
MF"v[36:39],a[120:123],a[8:11],v[36:39]\n"
PK"v10,v12,v13\n"
PK"v11,v14,v15\n"
MF"v[36:39],a[124:127],a[12:15],v[36:39]\n"
RD"a[76:79],v227 offset:25600\n"
PK"v18,v20,v21\n"
MF"v[44:47],a[124:127],a[28:31],v[44:47]\n"
PK"v19,v22,v23\n"
PK"v26,v28,v29\n"
MF"v[44:47],a[120:123],a[24:27],v[44:47]\n"
PK"v27,v30,v31\n"
MF"v[52:55],a[120:123],a[40:43],v[52:55]\n"
MF"v[52:55],a[124:127],a[44:47],v[52:55]\n"
RD"a[80:83],v227 offset:18432\n"
MF"v[60:63],a[124:127],a[60:63],v[60:63]\n"
EX"v32,v32\n"
MF"v[60:63],a[120:123],a[56:59],v[60:63]\n"
WT"lgkmcnt(2)\n"
SA"s41,s41,32768\n"
"s_cmp_eq_u32 s41,163840\n"
MF"v[80:83],a[64:67],v[64:67],v[80:83]\n"
"s_cselect_b32 s41,0,s41\n"
EX"v33,v33\n"
MF"v[80:83],a[68:71],v[0:3],v[80:83]\n"
RD"a[84:87],v227 offset:26624\n"
"v_add_u32 v226,s41,v224\n"
MF"v[112:115],a[68:71],v[8:11],v[112:115]\n"
EX"v34,v34\n"
MF"v[112:115],a[64:67],v[68:71],v[112:115]\n"
MF"v[144:147],a[64:67],v[72:75],v[144:147]\n"
EX"v35,v35\n"
MF"v[144:147],a[68:71],v[16:19],v[144:147]\n"
RD"a[88:91],v227 offset:19456\n"
MF"v[176:179],a[68:71],v[24:27],v[176:179]\n"
EX"v40,v40\n"
MF"v[176:179],a[64:67],v[76:79],v[176:179]\n"
BL"v225,s[8:11],s43 offen offset:2048 sc1 lds\n"
MF"v[84:87],a[72:75],v[64:67],v[84:87]\n"
WT"lgkmcnt(1)\n"
EX"v41,v41\n"
MF"v[84:87],a[76:79],v[0:3],v[84:87]\n"
RD"a[92:95],v227 offset:27648\n"
MF"v[116:119],a[76:79],v[8:11],v[116:119]\n"
EX"v42,v42\n"
MF"v[116:119],a[72:75],v[68:71],v[116:119]\n"
MF"v[148:151],a[72:75],v[72:75],v[148:151]\n"
EX"v43,v43\n"
MF"v[148:151],a[76:79],v[16:19],v[148:151]\n"
RD"a[96:99],v227 offset:20480\n"
MF"v[180:183],a[76:79],v[24:27],v[180:183]\n"
EX"v48,v48\n"
MF"v[180:183],a[72:75],v[76:79],v[180:183]\n"
BL"v225,s[8:11],s43 offen offset:3072 sc1 lds\n"
MF"v[88:91],a[80:83],v[64:67],v[88:91]\n"
EX"v49,v49\n"
MF"v[88:91],a[84:87],v[0:3],v[88:91]\n"
RD"a[100:103],v227 offset:28672\n"
MF"v[120:123],a[84:87],v[8:11],v[120:123]\n"
EX"v50,v50\n"
MF"v[120:123],a[80:83],v[68:71],v[120:123]\n"
SA"m0,s40,16384\n"
MF"v[152:155],a[80:83],v[72:75],v[152:155]\n"
EX"v51,v51\n"
MF"v[152:155],a[84:87],v[16:19],v[152:155]\n"
RD"a[104:107],v227 offset:21504\n"
MF"v[184:187],a[84:87],v[24:27],v[184:187]\n"
EX"v56,v56\n"
MF"v[184:187],a[80:83],v[76:79],v[184:187]\n"
WT"lgkmcnt(2)\n"
BL"v225,s[8:11],s44 offen offset:0 sc1 lds\n"
MF"v[92:95],a[88:91],v[64:67],v[92:95]\n"
SA"s40,s40,32768\n"
EX"v57,v57\n"
MF"v[92:95],a[92:95],v[0:3],v[92:95]\n"
"s_cmp_eq_u32 s40,s46\n"
RD"a[108:111],v227 offset:29696\n"
"s_cselect_b32 s40,s47,s40\n"
MF"v[124:127],a[92:95],v[8:11],v[124:127]\n"
EX"v58,v58\n"
MF"v[124:127],a[88:91],v[68:71],v[124:127]\n"
MF"v[156:159],a[88:91],v[72:75],v[156:159]\n"
EX"v59,v59\n"
MF"v[156:159],a[92:95],v[16:19],v[156:159]\n"
RD"a[112:115],v227 offset:22528\n"
MF"v[188:191],a[92:95],v[24:27],v[188:191]\n"
EX"v36,v36\n"
MF"v[188:191],a[88:91],v[76:79],v[188:191]\n"
BL"v225,s[8:11],s44 offen offset:1024 sc1 lds\n"
MF"v[96:99],a[96:99],v[64:67],v[96:99]\n"
WT"lgkmcnt(1)\n"
EX"v37,v37\n"
MF"v[96:99],a[100:103],v[0:3],v[96:99]\n"
RD"a[116:119],v227 offset:30720\n"
MF"v[128:131],a[100:103],v[8:11],v[128:131]\n"
EX"v38,v38\n"
MF"v[128:131],a[96:99],v[68:71],v[128:131]\n"
MF"v[160:163],a[96:99],v[72:75],v[160:163]\n"
EX"v39,v39\n"
MF"v[160:163],a[100:103],v[16:19],v[160:163]\n"
RD"a[120:123],v227 offset:23552\n"
MF"v[192:195],a[100:103],v[24:27],v[192:195]\n"
EX"v44,v44\n"
MF"v[192:195],a[96:99],v[76:79],v[192:195]\n"
BL"v225,s[8:11],s44 offen offset:2048 sc1 lds\n"
MF"v[100:103],a[104:107],v[64:67],v[100:103]\n"
EX"v45,v45\n"
MF"v[100:103],a[108:111],v[0:3],v[100:103]\n"
RD"a[124:127],v227 offset:31744\n"
MF"v[132:135],a[108:111],v[8:11],v[132:135]\n"
EX"v46,v46\n"
MF"v[132:135],a[104:107],v[68:71],v[132:135]\n"
MF"v[164:167],a[104:107],v[72:75],v[164:167]\n"
EX"v47,v47\n"
MF"v[164:167],a[108:111],v[16:19],v[164:167]\n"
MF"v[196:199],a[108:111],v[24:27],v[196:199]\n"
EX"v52,v52\n"
MF"v[196:199],a[104:107],v[76:79],v[196:199]\n"
WT"lgkmcnt(1)\n"
BL"v225,s[8:11],s44 offen offset:3072 sc1 lds\n"
MF"v[104:107],a[112:115],v[64:67],v[104:107]\n"
EX"v53,v53\n"
MF"v[104:107],a[116:119],v[0:3],v[104:107]\n"
MF"v[136:139],a[116:119],v[8:11],v[136:139]\n"
EX"v54,v54\n"
MF"v[136:139],a[112:115],v[68:71],v[136:139]\n"
MF"v[168:171],a[112:115],v[72:75],v[168:171]\n"
WT"vmcnt(16) lgkmcnt(0)\n"
"s_barrier\n"
MF"v[168:171],a[116:119],v[16:19],v[168:171]\n"
RD"a[64:67],v226 offset:0\n"
MF"v[200:203],a[116:119],v[24:27],v[200:203]\n"
EX"v55,v55\n"
MF"v[200:203],a[112:115],v[76:79],v[200:203]\n"
MF"v[108:111],a[120:123],v[64:67],v[108:111]\n"
EX"v60,v60\n"
MF"v[108:111],a[124:127],v[0:3],v[108:111]\n"
RD"a[68:71],v226 offset:1024\n"
MF"v[140:143],a[124:127],v[8:11],v[140:143]\n"
EX"v61,v61\n"
MF"v[140:143],a[120:123],v[68:71],v[140:143]\n"
RD"a[72:75],v226 offset:2048\n"
MF"v[172:175],a[120:123],v[72:75],v[172:175]\n"
EX"v62,v62\n"
MF"v[172:175],a[124:127],v[16:19],v[172:175]\n"
RD"a[76:79],v226 offset:3072\n"
MF"v[204:207],a[124:127],v[24:27],v[204:207]\n"
EX"v63,v63\n"
MF"v[204:207],a[120:123],v[76:79],v[204:207]\n"
MF"a[132:135],a[128:131],v[64:67],a[132:135]\n"
MF"a[132:135],a[128:131],v[0:3],a[132:135]\n"
RD"a[80:83],v226 offset:4096\n"
MF"a[136:139],a[128:131],v[8:11],a[136:139]\n"
MF"a[136:139],a[128:131],v[68:71],a[136:139]\n"
RD"a[84:87],v226 offset:5120\n"
MF"a[140:143],a[128:131],v[72:75],a[140:143]\n"
MF"a[140:143],a[128:131],v[16:19],a[140:143]\n"
RD"a[88:91],v226 offset:6144\n"
MF"a[144:147],a[128:131],v[24:27],a[144:147]\n"
MF"a[144:147],a[128:131],v[76:79],a[144:147]\n"
"LDB_%=:\n"
WT"lgkmcnt(6)\n"
MF"v[8:11],a[64:67],a[16:19],v[208:211]\n"
WT"lgkmcnt(5)\n"
"v_readlane_b32 s43,v228,s42\n"
PK"v64,v32,v33\n"
MF"v[8:11],a[68:71],a[20:23],v[8:11]\n"
RD"a[92:95],v226 offset:7168\n"
PK"v65,v34,v35\n"
MF"v[16:19],a[68:71],a[36:39],0\n"
PK"v66,v36,v37\n"
PK"v67,v38,v39\n"
MF"v[16:19],a[64:67],a[32:35],v[16:19]\n"
"s_mov_b32 m0,s40\n"
PK"v68,v40,v41\n"
PK"v69,v42,v43\n"
MF"v[24:27],a[64:67],a[48:51],0\n"
PK"v70,v44,v45\n"
PK"v71,v46,v47\n"
MF"v[24:27],a[68:71],a[52:55],v[24:27]\n"
WT"lgkmcnt(1)\n"
RD"a[96:99],v226 offset:8192\n"
"v_readlane_b32 s44,v229,s42\n"
MF"v[8:11],a[72:75],a[24:27],v[8:11]\n"
PK"v72,v48,v49\n"
PK"v73,v50,v51\n"
MF"v[8:11],a[76:79],a[28:31],v[8:11]\n"
SA"s42,s42,1\n"
BL"v225,s[8:11],s43 offen offset:0 sc1 lds\n"
"v_mov_b32 v227,v226\n"
MF"v[16:19],a[76:79],a[44:47],v[16:19]\n"
PK"v74,v52,v53\n"
PK"v75,v54,v55\n"
MF"v[16:19],a[72:75],a[40:43],v[16:19]\n"
RD"a[100:103],v226 offset:9216\n"
PK"v76,v56,v57\n"
PK"v77,v58,v59\n"
MF"v[24:27],a[72:75],a[56:59],v[24:27]\n"
PK"v78,v60,v61\n"
PK"v79,v62,v63\n"
MF"v[24:27],a[76:79],a[60:63],v[24:27]\n"
RD"a[104:107],v226 offset:10240\n"
MF"v[12:15],a[80:83],a[16:19],v[212:215]\n"
EX"v8,v8\n"
MF"v[12:15],a[84:87],a[20:23],v[12:15]\n"
RD"a[108:111],v226 offset:11264\n"
"v_lshrrev_b32 v235,7,v224\n"
MF"v[20:23],a[84:87],a[36:39],0\n"
EX"v9,v9\n"
MF"v[20:23],a[80:83],a[32:35],v[20:23]\n"
BL"v225,s[8:11],s43 offen offset:1024 sc1 lds\n"
MF"v[28:31],a[80:83],a[48:51],0\n"
EX"v10,v10\n"
"v_lshrrev_b32 v230,1,v224\n"
MF"v[28:31],a[84:87],a[52:55],v[28:31]\n"
RD"a[112:115],v226 offset:12288\n"
"v_lshl_add_u32 v230,v235,27,v230\n"
MF"v[12:15],a[88:91],a[24:27],v[12:15]\n"
WT"lgkmcnt(2)\n"
EX"v11,v11\n"
PK"v8,v8,v9\n"
MF"v[12:15],a[92:95],a[28:31],v[12:15]\n"
RD"a[116:119],v226 offset:13312\n"
PK"v9,v10,v11\n"
MF"v[20:23],a[92:95],a[44:47],v[20:23]\n"
"s_lshl_b32 s48,s35,8\n"
EX"v16,v16\n"
MF"v[20:23],a[88:91],a[40:43],v[20:23]\n"
MF"v[28:31],a[88:91],a[56:59],v[28:31]\n"
SA"s48,s48,81264640\n"
EX"v17,v17\n"
MF"v[28:31],a[92:95],a[60:63],v[28:31]\n"
BL"v225,s[8:11],s43 offen offset:2048 sc1 lds\n"
MF"v[40:43],a[96:99],a[16:19],v[216:219]\n"
"s_andn2_b32 s49,s61,255\n"
EX"v18,v18\n"
MF"v[40:43],a[100:103],a[20:23],v[40:43]\n"
RD"a[120:123],v226 offset:14336\n"
PK"v16,v16,v17\n"
MF"v[48:51],a[100:103],a[36:39],0\n"
EX"v19,v19\n"
MF"v[48:51],a[96:99],a[32:35],v[48:51]\n"
RD"a[124:127],v226 offset:15360\n"
"v_bfe_u32 v236,v224,4,2\n"
MF"v[56:59],a[96:99],a[48:51],0\n"
EX"v24,v24\n"
MF"v[56:59],a[100:103],a[52:55],v[56:59]\n"
PK"v17,v18,v19\n"
"v_lshl_add_u32 v236,v236,6,s49\n"
MF"v[40:43],a[104:107],a[24:27],v[40:43]\n"
WT"lgkmcnt(2)\n"
EX"v25,v25\n"
MF"v[40:43],a[108:111],a[28:31],v[40:43]\n"
BL"v225,s[8:11],s43 offen offset:3072 sc1 lds\n"
MF"v[48:51],a[108:111],a[44:47],v[48:51]\n"
EX"v26,v26\n"
MF"v[48:51],a[104:107],a[40:43],v[48:51]\n"
RD"a[64:67],v227 offset:16384\n"
"v_mov_b32 v237,s48\n"
MF"v[56:59],a[104:107],a[56:59],v[56:59]\n"
EX"v27,v27\n"
PK"v24,v24,v25\n"
MF"v[56:59],a[108:111],a[60:63],v[56:59]\n"
SA"m0,s40,16384\n"
"v_cmp_lt_u32 vcc,511,v224\n"
PK"v25,v26,v27\n"
MF"v[44:47],a[112:115],a[16:19],v[220:223]\n"
EX"v12,v12\n"
MF"v[44:47],a[116:119],a[20:23],v[44:47]\n"
RD"a[68:71],v227 offset:17408\n"
"v_cndmask_b32 v236,v236,v237,vcc\n"
MF"v[52:55],a[116:119],a[36:39],0\n"
EX"v13,v13\n"
MF"v[52:55],a[112:115],a[32:35],v[52:55]\n"
BL"v225,s[8:11],s44 offen offset:0 sc1 lds\n"
MF"v[60:63],a[112:115],a[48:51],0\n"
SA"s40,s40,32768\n"
EX"v14,v14\n"
MF"v[60:63],a[116:119],a[52:55],v[60:63]\n"
WT"lgkmcnt(1)\n"
RD"a[72:75],v227 offset:18432\n"
PK"v10,v12,v13\n"
MF"v[44:47],a[120:123],a[24:27],v[44:47]\n"
"s_cmp_eq_u32 s40,s46\n"
EX"v15,v15\n"
MF"v[44:47],a[124:127],a[28:31],v[44:47]\n"
"s_cselect_b32 s40,s47,s40\n"
PK"v11,v14,v15\n"
MF"v[52:55],a[124:127],a[44:47],v[52:55]\n"
EX"v20,v20\n"
MF"v[52:55],a[120:123],a[40:43],v[52:55]\n"
RD"a[76:79],v227 offset:19456\n"
MF"v[60:63],a[120:123],a[56:59],v[60:63]\n"
EX"v21,v21\n"
MF"v[60:63],a[124:127],a[60:63],v[60:63]\n"
SA"s41,s41,32768\n"
BL"v225,s[8:11],s44 offen offset:1024 sc1 lds\n"
MF"v[80:83],a[64:67],v[64:67],v[80:83]\n"
"s_cmp_eq_u32 s41,163840\n"
EX"v22,v22\n"
MF"v[112:115],a[64:67],v[68:71],v[112:115]\n"
"s_cselect_b32 s41,0,s41\n"
RD"a[80:83],v227 offset:20480\n"
"v_add_u32 v226,s41,v224\n"
MF"v[144:147],a[64:67],v[72:75],v[144:147]\n"
EX"v23,v23\n"
PK"v18,v20,v21\n"
MF"v[176:179],a[64:67],v[76:79],v[176:179]\n"
WT"lgkmcnt(2)\n"
PK"v19,v22,v23\n"
MF"v[84:87],a[68:71],v[64:67],v[84:87]\n"
EX"v28,v28\n"
MF"v[116:119],a[68:71],v[68:71],v[116:119]\n"
RD"a[84:87],v227 offset:21504\n"
MF"v[148:151],a[68:71],v[72:75],v[148:151]\n"
EX"v29,v29\n"
MF"v[180:183],a[68:71],v[76:79],v[180:183]\n"
BL"v225,s[8:11],s44 offen offset:2048 sc1 lds\n"
MF"v[88:91],a[72:75],v[64:67],v[88:91]\n"
EX"v30,v30\n"
MF"v[120:123],a[72:75],v[68:71],v[120:123]\n"
RD"a[88:91],v227 offset:22528\n"
PK"v26,v28,v29\n"
MF"v[152:155],a[72:75],v[72:75],v[152:155]\n"
EX"v31,v31\n"
MF"v[184:187],a[72:75],v[76:79],v[184:187]\n"
WT"lgkmcnt(2)\n"
PK"v27,v30,v31\n"
MF"v[92:95],a[76:79],v[64:67],v[92:95]\n"
EX"v40,v40\n"
MF"v[124:127],a[76:79],v[68:71],v[124:127]\n"
RD"a[92:95],v227 offset:23552\n"
MF"v[156:159],a[76:79],v[72:75],v[156:159]\n"
EX"v41,v41\n"
MF"v[188:191],a[76:79],v[76:79],v[188:191]\n"
BL"v225,s[8:11],s44 offen offset:3072 sc1 lds\n"
MF"v[96:99],a[80:83],v[64:67],v[96:99]\n"
EX"v42,v42\n"
MF"v[128:131],a[80:83],v[68:71],v[128:131]\n"
RD"a[96:99],v227 offset:24576\n"
MF"v[160:163],a[80:83],v[72:75],v[160:163]\n"
EX"v43,v43\n"
MF"v[192:195],a[80:83],v[76:79],v[192:195]\n"
WT"lgkmcnt(2)\n"
RD"a[100:103],v227 offset:25600\n"
MF"v[100:103],a[84:87],v[64:67],v[100:103]\n"
EX"v48,v48\n"
MF"v[132:135],a[84:87],v[68:71],v[132:135]\n"
MF"v[164:167],a[84:87],v[72:75],v[164:167]\n"
EX"v49,v49\n"
MF"v[196:199],a[84:87],v[76:79],v[196:199]\n"
MF"v[104:107],a[88:91],v[64:67],v[104:107]\n"
EX"v50,v50\n"
MF"v[136:139],a[88:91],v[68:71],v[136:139]\n"
RD"a[104:107],v227 offset:26624\n"
MF"v[168:171],a[88:91],v[72:75],v[168:171]\n"
EX"v51,v51\n"
MF"v[200:203],a[88:91],v[76:79],v[200:203]\n"
WT"lgkmcnt(1)\n"
RD"a[108:111],v227 offset:27648\n"
MF"v[108:111],a[92:95],v[64:67],v[108:111]\n"
EX"v44,v44\n"
MF"v[140:143],a[92:95],v[68:71],v[140:143]\n"
RD"a[112:115],v227 offset:28672\n"
MF"v[172:175],a[92:95],v[72:75],v[172:175]\n"
EX"v45,v45\n"
MF"v[204:207],a[92:95],v[76:79],v[204:207]\n"
MF"a[132:135],a[128:131],v[64:67],a[132:135]\n"
EX"v46,v46\n"
MF"a[136:139],a[128:131],v[68:71],a[136:139]\n"
RD"a[116:119],v227 offset:29696\n"
PK"v68,v40,v41\n"
PK"v69,v42,v43\n"
MF"a[140:143],a[128:131],v[72:75],a[140:143]\n"
EX"v47,v47\n"
PK"v72,v48,v49\n"
MF"a[144:147],a[128:131],v[76:79],a[144:147]\n"
RD"a[120:123],v227 offset:30720\n"
PK"v73,v50,v51\n"
PK"v70,v44,v45\n"
MF"v[112:115],a[96:99],v[8:11],v[112:115]\n"
EX"v52,v52\n"
MF"v[144:147],a[96:99],v[16:19],v[144:147]\n"
RD"a[124:127],v227 offset:31744\n"
PK"v71,v46,v47\n"
MF"v[176:179],a[96:99],v[24:27],v[176:179]\n"
EX"v53,v53\n"
MF"v[116:119],a[100:103],v[8:11],v[116:119]\n"
"v_accvgpr_read_b32 v251,a132\n"
"v_subrev_u32 v253,0x2b800000,v251\n"
MF"v[148:151],a[100:103],v[16:19],v[148:151]\n"
"v_cmp_le_u32 vcc,0x34000000,v253\n"
"s_cmp_lg_u64 vcc,0\n"
"s_cbranch_scc1 L1014_%=\n"
"L1013_%=:\n"
MF"v[180:183],a[100:103],v[24:27],v[180:183]\n"
WT"lgkmcnt(2)\n"
EX"v54,v54\n"
MF"v[120:123],a[104:107],v[8:11],v[120:123]\n"
"v_rcp_f32 v251,v251\n"
PK"v74,v52,v53\n"
MF"v[152:155],a[104:107],v[16:19],v[152:155]\n"
EX"v55,v55\n"
MF"v[184:187],a[104:107],v[24:27],v[184:187]\n"
WT"vmcnt(16) lgkmcnt(0)\n"
"s_barrier\n"
MF"v[124:127],a[108:111],v[8:11],v[124:127]\n"
RD"a[64:67],v226 offset:0\n"
MU"v80,v251,v80\n"
MU"v81,v251,v81\n"
MF"v[156:159],a[108:111],v[16:19],v[156:159]\n"
MU"v82,v251,v82\n"
MU"v83,v251,v83\n"
PK"v75,v54,v55\n"
MF"v[188:191],a[108:111],v[24:27],v[188:191]\n"
MU"v84,v251,v84\n"
MU"v85,v251,v85\n"
MU"v86,v251,v86\n"
MF"v[128:131],a[112:115],v[8:11],v[128:131]\n"
MU"v87,v251,v87\n"
PK"v80,v80,v81\n"
PK"v81,v82,v83\n"
MF"v[160:163],a[112:115],v[16:19],v[160:163]\n"
RD"a[68:71],v226 offset:1024\n"
PK"v82,v84,v85\n"
PK"v83,v86,v87\n"
MF"v[192:195],a[112:115],v[24:27],v[192:195]\n"
BS"v[80:83],v224,s[8:11],s59 offen offset:0 sc1\n"
MU"v88,v251,v88\n"
MF"v[132:135],a[116:119],v[8:11],v[132:135]\n"
RD"a[72:75],v226 offset:2048\n"
MU"v89,v251,v89\n"
MU"v90,v251,v90\n"
MF"v[164:167],a[116:119],v[16:19],v[164:167]\n"
MU"v91,v251,v91\n"
MU"v92,v251,v92\n"
MU"v93,v251,v93\n"
MF"v[196:199],a[116:119],v[24:27],v[196:199]\n"
RD"a[76:79],v226 offset:3072\n"
MU"v94,v251,v94\n"
MU"v95,v251,v95\n"
MF"v[136:139],a[120:123],v[8:11],v[136:139]\n"
PK"v88,v88,v89\n"
PK"v89,v90,v91\n"
MF"v[168:171],a[120:123],v[16:19],v[168:171]\n"
PK"v90,v92,v93\n"
PK"v91,v94,v95\n"
MF"v[200:203],a[120:123],v[24:27],v[200:203]\n"
BS"v[88:91],v224,s[8:11],s59 offen offset:1024 sc1\n"
MU"v96,v251,v96\n"
MF"v[140:143],a[124:127],v[8:11],v[140:143]\n"
RD"a[80:83],v226 offset:4096\n"
MU"v97,v251,v97\n"
MU"v98,v251,v98\n"
MF"v[172:175],a[124:127],v[16:19],v[172:175]\n"
MU"v99,v251,v99\n"
MU"v100,v251,v100\n"
MU"v101,v251,v101\n"
MF"v[204:207],a[124:127],v[24:27],v[204:207]\n"
MU"v104,v251,v104\n"
MU"v105,v251,v105\n"
MU"v102,v251,v102\n"
MF"a[136:139],a[128:131],v[8:11],a[136:139]\n"
PK"v96,v96,v97\n"
MU"v106,v251,v106\n"
MU"v103,v251,v103\n"
MF"a[140:143],a[128:131],v[16:19],a[140:143]\n"
MU"v107,v251,v107\n"
PK"v97,v98,v99\n"
PK"v98,v100,v101\n"
MF"a[144:147],a[128:131],v[24:27],a[144:147]\n"
MU"v108,v251,v108\n"
MU"v109,v251,v109\n"
PK"v99,v102,v103\n"
BS"v[96:99],v224,s[8:11],s59 offen offset:2048 sc1\n"
MU"v110,v251,v110\n"
MU"v111,v251,v111\n"
PK"v104,v104,v105\n"
PK"v105,v106,v107\n"
PK"v106,v108,v109\n"
PK"v107,v110,v111\n"
BS"v[104:107],v224,s[8:11],s59 offen offset:3072 sc1\n"
RD"a[84:87],v226 offset:5120\n"
RD"a[88:91],v226 offset:6144\n"
"s_cmp_eq_u32 s34,7\n"
"s_cbranch_scc1 LD2_%=\n"
WT"lgkmcnt(6)\n"
MF"v[0:3],a[64:67],a[32:35],0\n"
WT"lgkmcnt(5)\n"
EX"v56,v56\n"
MF"v[0:3],a[68:71],a[36:39],v[0:3]\n"
"s_mov_b32 m0,s40\n"
EX"v57,v57\n"
MF"v[8:11],a[68:71],a[52:55],0\n"
"s_lshl_b32 s50,s35,6\n"
RD"a[92:95],v226 offset:7168\n"
"v_readlane_b32 s43,v228,s42\n"
MF"v[8:11],a[64:67],a[48:51],v[8:11]\n"
WT"lgkmcnt(5)\n"
EX"v58,v58\n"
MF"v[0:3],a[72:75],a[40:43],v[0:3]\n"
WT"lgkmcnt(1)\n"
BL"v225,s[8:11],s43 offen offset:0 sc1 lds\n"
"v_readlane_b32 s44,v229,s42\n"
MF"v[0:3],a[76:79],a[44:47],v[0:3]\n"
SA"s50,s50,4352\n"
RD"a[96:99],v226 offset:8192\n"
RD"a[100:103],v226 offset:9216\n"
PK"v76,v56,v57\n"
MF"v[8:11],a[76:79],a[60:63],v[8:11]\n"
SA"s42,s42,1\n"
EX"v59,v59\n"
MF"v[8:11],a[72:75],a[56:59],v[8:11]\n"
SA"s41,s41,32768\n"
RD"a[104:107],v226 offset:10240\n"
"v_lshrrev_b32 v237,1,v224\n"
MF"v[4:7],a[80:83],a[32:35],0\n"
"s_cmp_eq_u32 s41,163840\n"
EX"v60,v60\n"
"v_and_b32 v237,56,v237\n"
MF"v[4:7],a[84:87],a[36:39],v[4:7]\n"
"s_cselect_b32 s41,0,s41\n"
EX"v61,v61\n"
"v_add_u32 v227,s41,v224\n"
MF"v[12:15],a[84:87],a[52:55],0\n"
RD"a[108:111],v226 offset:11264\n"
PK"v77,v58,v59\n"
MF"v[12:15],a[80:83],a[48:51],v[12:15]\n"
BL"v225,s[8:11],s43 offen offset:1024 sc1 lds\n"
MF"v[4:7],a[88:91],a[40:43],v[4:7]\n"
WT"lgkmcnt(2)\n"
EX"v62,v62\n"
MF"v[4:7],a[92:95],a[44:47],v[4:7]\n"
RD"a[112:115],v226 offset:12288\n"
RD"a[116:119],v226 offset:13312\n"
PK"v78,v60,v61\n"
MF"v[12:15],a[92:95],a[60:63],v[12:15]\n"
EX"v63,v63\n"
MF"v[12:15],a[88:91],a[56:59],v[12:15]\n"
RD"a[120:123],v226 offset:14336\n"
PK"v79,v62,v63\n"
MF"v[16:19],a[96:99],a[32:35],0\n"
EX"v0,v0\n"
MF"v[16:19],a[100:103],a[36:39],v[16:19]\n"
EX"v1,v1\n"
MF"v[24:27],a[100:103],a[52:55],0\n"
RD"a[124:127],v226 offset:15360\n"
PK"v0,v0,v1\n"
MF"v[24:27],a[96:99],a[48:51],v[24:27]\n"
WT"lgkmcnt(4)\n"
BL"v225,s[8:11],s43 offen offset:2048 sc1 lds\n"
MF"v[16:19],a[104:107],a[40:43],v[16:19]\n"
"s_lshl_b32 s56,s34,1\n"
EX"v2,v2\n"
MF"v[16:19],a[108:111],a[44:47],v[16:19]\n"
"s_sub_u32 s56,12,s56\n"
RD"a[64:67],v226 offset:16384\n"
RD"a[68:71],v226 offset:17408\n"
MF"v[24:27],a[108:111],a[60:63],v[24:27]\n"
"s_sub_u32 s22,s56,2\n"
EX"v3,v3\n"
MF"v[24:27],a[104:107],a[56:59],v[24:27]\n"
WT"lgkmcnt(3)\n"
RD"a[72:75],v226 offset:18432\n"
PK"v1,v2,v3\n"
MF"v[20:23],a[112:115],a[32:35],0\n"
"s_min_u32 s56,s56,1\n"
EX"v8,v8\n"
MF"v[20:23],a[116:119],a[36:39],v[20:23]\n"
"s_mov_b32 s57,s61\n"
EX"v9,v9\n"
MF"v[28:31],a[116:119],a[52:55],0\n"
BL"v225,s[8:11],s43 offen offset:3072 sc1 lds\n"
MF"v[28:31],a[112:115],a[48:51],v[28:31]\n"
RD"a[76:79],v226 offset:19456\n"
RD"a[80:83],v226 offset:20480\n"
MF"v[20:23],a[120:123],a[40:43],v[20:23]\n"
WT"lgkmcnt(3)\n"
EX"v10,v10\n"
PK"v8,v8,v9\n"
MF"v[20:23],a[124:127],a[44:47],v[20:23]\n"
SA"m0,s40,16384\n"
RD"a[84:87],v226 offset:21504\n"
RD"a[88:91],v226 offset:22528\n"
EX"v11,v11\n"
MF"v[28:31],a[124:127],a[60:63],v[28:31]\n"
EX"v4,v4\n"
MF"v[28:31],a[120:123],a[56:59],v[28:31]\n"
"buffer_load_dwordx2 v[238:239],v237,s[8:11],s50 offen sc1\n"
PK"v9,v10,v11\n"
MF"v[32:35],a[64:67],a[32:35],0\n"
EX"v5,v5\n"
MF"v[32:35],a[68:71],a[36:39],v[32:35]\n"
BL"v225,s[8:11],s44 offen offset:0 sc1 lds\n"
MF"v[40:43],a[68:71],a[52:55],0\n"
SA"s40,s40,32768\n"
RD"a[92:95],v226 offset:23552\n"
PK"v2,v4,v5\n"
MF"v[40:43],a[64:67],a[48:51],v[40:43]\n"
WT"lgkmcnt(3)\n"
EX"v6,v6\n"
MF"v[32:35],a[72:75],a[40:43],v[32:35]\n"
"s_cmp_eq_u32 s40,s46\n"
RD"a[96:99],v226 offset:24576\n"
RD"a[100:103],v226 offset:25600\n"
MF"v[32:35],a[76:79],a[44:47],v[32:35]\n"
"s_cselect_b32 s40,s47,s40\n"
EX"v7,v7\n"
MF"v[40:43],a[76:79],a[60:63],v[40:43]\n"
RD"a[104:107],v226 offset:26624\n"
PK"v3,v6,v7\n"
MF"v[40:43],a[72:75],a[56:59],v[40:43]\n"
EX"v12,v12\n"
MF"v[36:39],a[80:83],a[32:35],0\n"
WT"lgkmcnt(4)\n"
EX"v13,v13\n"
MF"v[36:39],a[84:87],a[36:39],v[36:39]\n"
BL"v225,s[8:11],s44 offen offset:1024 sc1 lds\n"
MF"v[44:47],a[84:87],a[52:55],0\n"
EX"v14,v14\n"
MF"v[44:47],a[80:83],a[48:51],v[44:47]\n"
RD"a[108:111],v226 offset:27648\n"
RD"a[112:115],v226 offset:28672\n"
PK"v10,v12,v13\n"
MF"v[36:39],a[88:91],a[40:43],v[36:39]\n"
WT"lgkmcnt(3)\n"
EX"v15,v15\n"
MF"v[36:39],a[92:95],a[44:47],v[36:39]\n"
RD"a[116:119],v226 offset:29696\n"
RD"a[120:123],v226 offset:30720\n"
MF"v[44:47],a[92:95],a[60:63],v[44:47]\n"
EX"v16,v16\n"
PK"v11,v14,v15\n"
MF"v[44:47],a[88:91],a[56:59],v[44:47]\n"
EX"v17,v17\n"
MF"v[48:51],a[96:99],a[32:35],0\n"
BL"v225,s[8:11],s44 offen offset:2048 sc1 lds\n"
MF"v[48:51],a[100:103],a[36:39],v[48:51]\n"
EX"v18,v18\n"
MF"v[56:59],a[100:103],a[52:55],0\n"
RD"a[124:127],v226 offset:31744\n"
PK"v16,v16,v17\n"
MF"v[56:59],a[96:99],a[48:51],v[56:59]\n"
WT"lgkmcnt(3)\n"
EX"v19,v19\n"
MF"v[48:51],a[104:107],a[40:43],v[48:51]\n"
WT"vmcnt(16)\n"
"s_barrier\n"
MF"v[48:51],a[108:111],a[44:47],v[48:51]\n"
RD"a[96:99],v227 offset:8192\n"
RD"a[100:103],v227 offset:9216\n"
MF"v[56:59],a[108:111],a[60:63],v[56:59]\n"
EX"v24,v24\n"
PK"v17,v18,v19\n"
MF"v[56:59],a[104:107],a[56:59],v[56:59]\n"
EX"v25,v25\n"
MF"v[52:55],a[112:115],a[32:35],0\n"
WT"lgkmcnt(3)\n"
BL"v225,s[8:11],s44 offen offset:3072 sc1 lds\n"
MF"v[52:55],a[116:119],a[36:39],v[52:55]\n"
EX"v26,v26\n"
PK"v24,v24,v25\n"
MF"v[60:63],a[116:119],a[52:55],0\n"
EX"v27,v27\n"
"v_readlane_b32 s43,v228,s42\n"
MF"v[60:63],a[112:115],a[48:51],v[60:63]\n"
"s_mov_b32 m0,s40\n"
RD"a[104:107],v227 offset:10240\n"
RD"a[108:111],v227 offset:11264\n"
MF"v[52:55],a[120:123],a[40:43],v[52:55]\n"
WT"lgkmcnt(2)\n"
EX"v20,v20\n"
MF"v[52:55],a[124:127],a[44:47],v[52:55]\n"
EX"v21,v21\n"
PK"v25,v26,v27\n"
MF"v[60:63],a[124:127],a[60:63],v[60:63]\n"
RD"a[112:115],v227 offset:12288\n"
"v_readlane_b32 s44,v229,s42\n"
MF"v[60:63],a[120:123],a[56:59],v[60:63]\n"
SA"s41,s41,32768\n"
EX"v22,v22\n"
MF"v[112:115],a[96:99],v[68:71],v[112:115]\n"
"s_cmp_eq_u32 s41,163840\n"
BL"v225,s[8:11],s43 offen offset:0 sc1 lds\n"
MF"v[144:147],a[96:99],v[72:75],v[144:147]\n"
"s_cselect_b32 s41,0,s41\n"
EX"v23,v23\n"
PK"v18,v20,v21\n"
MF"v[176:179],a[96:99],v[76:79],v[176:179]\n"
SA"s42,s42,1\n"
RD"a[116:119],v227 offset:13312\n"
"v_add_u32 v226,s41,v224\n"
MF"v[116:119],a[100:103],v[68:71],v[116:119]\n"
EX"v28,v28\n"
MF"v[148:151],a[100:103],v[72:75],v[148:151]\n"
RD"a[120:123],v227 offset:14336\n"
PK"v19,v22,v23\n"
MF"v[180:183],a[100:103],v[76:79],v[180:183]\n"
WT"lgkmcnt(2)\n"
EX"v29,v29\n"
MF"v[120:123],a[104:107],v[68:71],v[120:123]\n"
EX"v30,v30\n"
MF"v[152:155],a[104:107],v[72:75],v[152:155]\n"
BL"v225,s[8:11],s43 offen offset:1024 sc1 lds\n"
MF"v[184:187],a[104:107],v[76:79],v[184:187]\n"
EX"v31,v31\n"
PK"v26,v28,v29\n"
MF"v[124:127],a[108:111],v[68:71],v[124:127]\n"
RD"a[124:127],v227 offset:15360\n"
PK"v27,v30,v31\n"
MF"v[156:159],a[108:111],v[72:75],v[156:159]\n"
EX"v32,v32\n"
MF"v[188:191],a[108:111],v[76:79],v[188:191]\n"
MF"v[128:131],a[112:115],v[68:71],v[128:131]\n"
EX"v33,v33\n"
MF"v[160:163],a[112:115],v[72:75],v[160:163]\n"
RD"a[64:67],v227 offset:16384\n"
PK"v64,v32,v33\n"
MF"v[192:195],a[112:115],v[76:79],v[192:195]\n"
WT"lgkmcnt(2)\n"
BL"v225,s[8:11],s43 offen offset:2048 sc1 lds\n"
MF"v[132:135],a[116:119],v[68:71],v[132:135]\n"
EX"v34,v34\n"
MF"v[164:167],a[116:119],v[72:75],v[164:167]\n"
EX"v35,v35\n"
MF"v[196:199],a[116:119],v[76:79],v[196:199]\n"
EX"v40,v40\n"
MF"v[136:139],a[120:123],v[68:71],v[136:139]\n"
RD"a[68:71],v227 offset:24576\n"
PK"v65,v34,v35\n"
MF"v[168:171],a[120:123],v[72:75],v[168:171]\n"
EX"v41,v41\n"
MF"v[200:203],a[120:123],v[76:79],v[200:203]\n"
WT"lgkmcnt(1)\n"
RD"a[72:75],v227 offset:17408\n"
RD"a[76:79],v227 offset:25600\n"
MF"v[140:143],a[124:127],v[68:71],v[140:143]\n"
WT"vmcnt(7)\n"
"v_cmp_eq_u64 vcc,s[28:29],v[238:239]\n"
"s_cmp_eq_u64 vcc,exec\n"
"s_cbranch_scc0 L1019_%=\n"
"L1020_%=:\n"
MF"v[172:175],a[124:127],v[72:75],v[172:175]\n"
EX"v42,v42\n"
MF"v[204:207],a[124:127],v[76:79],v[204:207]\n"
RD"a[80:83],v227 offset:18432\n"
MF"a[136:139],a[128:131],v[68:71],a[136:139]\n"
BL"v225,s[8:11],s43 offen offset:3072 sc1 lds\n"
PK"v68,v40,v41\n"
MF"a[140:143],a[128:131],v[72:75],a[140:143]\n"
SA"m0,s40,16384\n"
EX"v43,v43\n"
MF"a[144:147],a[128:131],v[76:79],a[144:147]\n"
EX"v36,v36\n"
MF"v[144:147],a[64:67],v[0:3],v[144:147]\n"
WT"lgkmcnt(1)\n"
RD"a[84:87],v227 offset:26624\n"
RD"a[88:91],v227 offset:19456\n"
PK"v69,v42,v43\n"
MF"v[144:147],a[68:71],v[16:19],v[144:147]\n"
EX"v37,v37\n"
MF"v[176:179],a[68:71],v[24:27],v[176:179]\n"
SA"s51,s59,16384\n"
RD"a[92:95],v227 offset:27648\n"
RD"a[96:99],v227 offset:20480\n"
MF"v[176:179],a[64:67],v[8:11],v[176:179]\n"
SA"s40,s40,32768\n"
EX"v38,v38\n"
MF"v[148:151],a[72:75],v[0:3],v[148:151]\n"
"s_cmp_eq_u32 s40,s46\n"
BL"v225,s[8:11],s44 offen offset:0 sc1 lds\n"
PK"v66,v36,v37\n"
MF"v[148:151],a[76:79],v[16:19],v[148:151]\n"
"s_cselect_b32 s40,s47,s40\n"
EX"v39,v39\n"
"v_accvgpr_read_b32 v252,a136\n"
MF"v[180:183],a[76:79],v[24:27],v[180:183]\n"
EX"v44,v44\n"
"v_subrev_u32 v253,0x2b800000,v252\n"
MF"v[180:183],a[72:75],v[8:11],v[180:183]\n"
WT"lgkmcnt(2)\n"
"v_cmp_le_u32 vcc,0x34000000,v253\n"
"s_cmp_lg_u64 vcc,0\n"
"s_cbranch_scc1 L1023_%=\n"
"L1022_%=:\n"
MF"v[152:155],a[80:83],v[0:3],v[152:155]\n"
"v_rcp_f32 v252,v252\n"
PK"v67,v38,v39\n"
MF"v[152:155],a[84:87],v[16:19],v[152:155]\n"
RD"a[100:103],v227 offset:28672\n"
RD"a[104:107],v227 offset:21504\n"
EX"v45,v45\n"
MF"v[184:187],a[84:87],v[24:27],v[184:187]\n"
RD"a[108:111],v227 offset:29696\n"
RD"a[112:115],v227 offset:22528\n"
MU"v112,v252,v112\n"
MF"v[184:187],a[80:83],v[8:11],v[184:187]\n"
EX"v46,v46\n"
MU"v113,v252,v113\n"
MF"v[156:159],a[88:91],v[0:3],v[156:159]\n"
WT"lgkmcnt(4)\n"
BL"v225,s[8:11],s44 offen offset:1024 sc1 lds\n"
MU"v114,v252,v114\n"
MF"v[156:159],a[92:95],v[16:19],v[156:159]\n"
EX"v47,v47\n"
MU"v115,v252,v115\n"
MF"v[188:191],a[92:95],v[24:27],v[188:191]\n"
EX"v48,v48\n"
MU"v116,v252,v116\n"
MF"v[188:191],a[88:91],v[8:11],v[188:191]\n"
EX"v49,v49\n"
PK"v70,v44,v45\n"
MF"v[160:163],a[96:99],v[0:3],v[160:163]\n"
WT"lgkmcnt(2)\n"
RD"a[116:119],v227 offset:30720\n"
RD"a[120:123],v227 offset:23552\n"
MU"v117,v252,v117\n"
MF"v[160:163],a[100:103],v[16:19],v[160:163]\n"
EX"v50,v50\n"
MU"v118,v252,v118\n"
MF"v[192:195],a[100:103],v[24:27],v[192:195]\n"
RD"a[124:127],v227 offset:31744\n"
MU"v119,v252,v119\n"
PK"v112,v112,v113\n"
MF"v[192:195],a[96:99],v[8:11],v[192:195]\n"
BL"v225,s[8:11],s44 offen offset:2048 sc1 lds\n"
PK"v113,v114,v115\n"
MF"v[164:167],a[104:107],v[0:3],v[164:167]\n"
WT"lgkmcnt(3)\n"
EX"v51,v51\n"
PK"v114,v116,v117\n"
MF"v[164:167],a[108:111],v[16:19],v[164:167]\n"
EX"v52,v52\n"
PK"v115,v118,v119\n"
MF"v[196:199],a[108:111],v[24:27],v[196:199]\n"
BS"v[112:115],v224,s[8:11],s51 offen offset:0 sc1\n"
PK"v71,v46,v47\n"
MF"v[196:199],a[104:107],v[8:11],v[196:199]\n"
"s_barrier\n"
MF"v[168:171],a[112:115],v[0:3],v[168:171]\n"
WT"lgkmcnt(1)\n"
EX"v53,v53\n"
MU"v120,v252,v120\n"
MF"v[168:171],a[116:119],v[16:19],v[168:171]\n"
RD"a[64:67],v226 offset:0\n"
RD"a[68:71],v226 offset:1024\n"
MU"v121,v252,v121\n"
MF"v[200:203],a[116:119],v[24:27],v[200:203]\n"
EX"v54,v54\n"
MU"v122,v252,v122\n"
MF"v[200:203],a[112:115],v[8:11],v[200:203]\n"
BL"v225,s[8:11],s44 offen offset:3072 sc1 lds\n"
MU"v123,v252,v123\n"
MF"v[172:175],a[120:123],v[0:3],v[172:175]\n"
WT"lgkmcnt(2)\n"
EX"v55,v55\n"
MU"v124,v252,v124\n"
MF"v[172:175],a[124:127],v[16:19],v[172:175]\n"
RD"a[72:75],v226 offset:2048\n"
PK"v72,v48,v49\n"
MU"v125,v252,v125\n"
MF"v[204:207],a[124:127],v[24:27],v[204:207]\n"
RD"a[76:79],v226 offset:3072\n"
MU"v126,v252,v126\n"
MU"v127,v252,v127\n"
MF"v[204:207],a[120:123],v[8:11],v[204:207]\n"
PK"v120,v120,v121\n"
PK"v121,v122,v123\n"
PK"v122,v124,v125\n"
MF"a[140:143],a[128:131],v[0:3],a[140:143]\n"
MU"v128,v252,v128\n"
MU"v129,v252,v129\n"
MU"v136,v252,v136\n"
MF"a[140:143],a[128:131],v[16:19],a[140:143]\n"
MU"v137,v252,v137\n"
MU"v130,v252,v130\n"
MU"v131,v252,v131\n"
MF"a[144:147],a[128:131],v[24:27],a[144:147]\n"
PK"v128,v128,v129\n"
MU"v138,v252,v138\n"
MU"v139,v252,v139\n"
MF"a[144:147],a[128:131],v[8:11],a[144:147]\n"
PK"v136,v136,v137\n"
MU"v132,v252,v132\n"
MU"v133,v252,v133\n"
PK"v73,v50,v51\n"
PK"v74,v52,v53\n"
PK"v75,v54,v55\n"
PK"v123,v126,v127\n"
BS"v[120:123],v224,s[8:11],s51 offen offset:1024 sc1\n"
MU"v134,v252,v134\n"
MU"v135,v252,v135\n"
PK"v129,v130,v131\n"
PK"v130,v132,v133\n"
PK"v131,v134,v135\n"
BS"v[128:131],v224,s[8:11],s51 offen offset:2048 sc1\n"
MU"v140,v252,v140\n"
MU"v141,v252,v141\n"
MU"v142,v252,v142\n"
MU"v143,v252,v143\n"
PK"v137,v138,v139\n"
PK"v138,v140,v141\n"
PK"v139,v142,v143\n"
BS"v[136:139],v224,s[8:11],s51 offen offset:3072 sc1\n"
RD"a[80:83],v226 offset:4096\n"
RD"a[84:87],v226 offset:5120\n"
RD"a[88:91],v226 offset:6144\n"
".p2align 6\n"
"LP2_%=:\n"
WT"lgkmcnt(6)\n"
MF"v[0:3],a[64:67],a[32:35],0\n"
WT"lgkmcnt(5)\n"
EX"v56,v56\n"
MF"v[0:3],a[68:71],a[36:39],v[0:3]\n"
RD"a[92:95],v226 offset:7168\n"
"v_readlane_b32 s43,v228,s42\n"
MF"v[8:11],a[68:71],a[52:55],0\n"
EX"v57,v57\n"
MF"v[8:11],a[64:67],a[48:51],v[8:11]\n"
WT"lgkmcnt(5)\n"
RD"a[96:99],v226 offset:8192\n"
RD"a[100:103],v226 offset:9216\n"
MF"v[0:3],a[72:75],a[40:43],v[0:3]\n"
WT"lgkmcnt(3)\n"
EX"v58,v58\n"
MF"v[0:3],a[76:79],a[44:47],v[0:3]\n"
"s_mov_b32 m0,s40\n"
RD"a[104:107],v226 offset:10240\n"
"v_readlane_b32 s44,v229,s42\n"
MF"v[8:11],a[76:79],a[60:63],v[8:11]\n"
SA"s41,s41,32768\n"
EX"v59,v59\n"
MF"v[8:11],a[72:75],a[56:59],v[8:11]\n"
"s_cmp_eq_u32 s41,163840\n"
BL"v225,s[8:11],s43 offen offset:0 sc1 lds\n"
MF"v[4:7],a[80:83],a[32:35],0\n"
"s_cselect_b32 s41,0,s41\n"
EX"v60,v60\n"
MF"v[4:7],a[84:87],a[36:39],v[4:7]\n"
SA"s42,s42,1\n"
RD"a[108:111],v226 offset:11264\n"
"v_add_u32 v227,s41,v224\n"
MF"v[12:15],a[84:87],a[52:55],0\n"
EX"v61,v61\n"
MF"v[12:15],a[80:83],a[48:51],v[12:15]\n"
RD"a[112:115],v226 offset:12288\n"
RD"a[116:119],v226 offset:13312\n"
MF"v[4:7],a[88:91],a[40:43],v[4:7]\n"
WT"lgkmcnt(3)\n"
EX"v62,v62\n"
MF"v[4:7],a[92:95],a[44:47],v[4:7]\n"
RD"a[120:123],v226 offset:14336\n"
PK"v76,v56,v57\n"
MF"v[12:15],a[92:95],a[60:63],v[12:15]\n"
EX"v63,v63\n"
MF"v[12:15],a[88:91],a[56:59],v[12:15]\n"
BL"v225,s[8:11],s43 offen offset:1024 sc1 lds\n"
MF"v[16:19],a[96:99],a[32:35],0\n"
EX"v0,v0\n"
MF"v[16:19],a[100:103],a[36:39],v[16:19]\n"
RD"a[124:127],v226 offset:15360\n"
PK"v77,v58,v59\n"
MF"v[24:27],a[100:103],a[52:55],0\n"
EX"v1,v1\n"
MF"v[24:27],a[96:99],a[48:51],v[24:27]\n"
RD"a[64:67],v226 offset:16384\n"
RD"a[68:71],v226 offset:17408\n"
MF"v[16:19],a[104:107],a[40:43],v[16:19]\n"
WT"lgkmcnt(3)\n"
EX"v2,v2\n"
MF"v[16:19],a[108:111],a[44:47],v[16:19]\n"
RD"a[72:75],v226 offset:18432\n"
PK"v78,v60,v61\n"
MF"v[24:27],a[108:111],a[60:63],v[24:27]\n"
EX"v3,v3\n"
MF"v[24:27],a[104:107],a[56:59],v[24:27]\n"
BL"v225,s[8:11],s43 offen offset:2048 sc1 lds\n"
MF"v[20:23],a[112:115],a[32:35],0\n"
EX"v8,v8\n"
MF"v[20:23],a[116:119],a[36:39],v[20:23]\n"
RD"a[76:79],v226 offset:19456\n"
PK"v79,v62,v63\n"
MF"v[28:31],a[116:119],a[52:55],0\n"
EX"v9,v9\n"
MF"v[28:31],a[112:115],a[48:51],v[28:31]\n"
RD"a[80:83],v226 offset:20480\n"
RD"a[84:87],v226 offset:21504\n"
MF"v[20:23],a[120:123],a[40:43],v[20:23]\n"
WT"lgkmcnt(3)\n"
EX"v10,v10\n"
MF"v[20:23],a[124:127],a[44:47],v[20:23]\n"
RD"a[88:91],v226 offset:22528\n"
PK"v0,v0,v1\n"
MF"v[28:31],a[124:127],a[60:63],v[28:31]\n"
EX"v11,v11\n"
MF"v[28:31],a[120:123],a[56:59],v[28:31]\n"
BL"v225,s[8:11],s43 offen offset:3072 sc1 lds\n"
MF"v[32:35],a[64:67],a[32:35],0\n"
EX"v4,v4\n"
MF"v[32:35],a[68:71],a[36:39],v[32:35]\n"
RD"a[92:95],v226 offset:23552\n"
PK"v1,v2,v3\n"
MF"v[40:43],a[68:71],a[52:55],0\n"
EX"v5,v5\n"
MF"v[40:43],a[64:67],a[48:51],v[40:43]\n"
SA"m0,s40,16384\n"
RD"a[96:99],v226 offset:24576\n"
RD"a[100:103],v226 offset:25600\n"
MF"v[32:35],a[72:75],a[40:43],v[32:35]\n"
WT"lgkmcnt(3)\n"
EX"v6,v6\n"
MF"v[32:35],a[76:79],a[44:47],v[32:35]\n"
RD"a[104:107],v226 offset:26624\n"
PK"v8,v8,v9\n"
MF"v[40:43],a[76:79],a[60:63],v[40:43]\n"
EX"v7,v7\n"
MF"v[40:43],a[72:75],a[56:59],v[40:43]\n"
BL"v225,s[8:11],s44 offen offset:0 sc1 lds\n"
MF"v[36:39],a[80:83],a[32:35],0\n"
SA"s40,s40,32768\n"
EX"v12,v12\n"
MF"v[36:39],a[84:87],a[36:39],v[36:39]\n"
"s_cmp_eq_u32 s40,s46\n"
RD"a[108:111],v226 offset:27648\n"
PK"v9,v10,v11\n"
MF"v[44:47],a[84:87],a[52:55],0\n"
"s_cselect_b32 s40,s47,s40\n"
EX"v13,v13\n"
MF"v[44:47],a[80:83],a[48:51],v[44:47]\n"
RD"a[112:115],v226 offset:28672\n"
RD"a[116:119],v226 offset:29696\n"
MF"v[36:39],a[88:91],a[40:43],v[36:39]\n"
WT"lgkmcnt(3)\n"
EX"v14,v14\n"
MF"v[36:39],a[92:95],a[44:47],v[36:39]\n"
RD"a[120:123],v226 offset:30720\n"
PK"v2,v4,v5\n"
MF"v[44:47],a[92:95],a[60:63],v[44:47]\n"
EX"v15,v15\n"
MF"v[44:47],a[88:91],a[56:59],v[44:47]\n"
BL"v225,s[8:11],s44 offen offset:1024 sc1 lds\n"
MF"v[48:51],a[96:99],a[32:35],0\n"
EX"v16,v16\n"
MF"v[48:51],a[100:103],a[36:39],v[48:51]\n"
RD"a[124:127],v226 offset:31744\n"
PK"v3,v6,v7\n"
MF"v[56:59],a[100:103],a[52:55],0\n"
WT"vmcnt(14)\n"
"s_barrier\n"
MF"v[56:59],a[96:99],a[48:51],v[56:59]\n"
RD"a[64:67],v227 offset:0\n"
RD"a[68:71],v227 offset:8192\n"
MF"v[48:51],a[104:107],a[40:43],v[48:51]\n"
WT"lgkmcnt(3)\n"
EX"v17,v17\n"
MF"v[48:51],a[108:111],a[44:47],v[48:51]\n"
RD"a[72:75],v227 offset:1024\n"
PK"v10,v12,v13\n"
MF"v[56:59],a[108:111],a[60:63],v[56:59]\n"
EX"v18,v18\n"
MF"v[56:59],a[104:107],a[56:59],v[56:59]\n"
BL"v225,s[8:11],s44 offen offset:2048 sc1 lds\n"
MF"v[52:55],a[112:115],a[32:35],0\n"
EX"v19,v19\n"
MF"v[52:55],a[116:119],a[36:39],v[52:55]\n"
RD"a[76:79],v227 offset:9216\n"
PK"v11,v14,v15\n"
MF"v[60:63],a[116:119],a[52:55],0\n"
EX"v24,v24\n"
MF"v[60:63],a[112:115],a[48:51],v[60:63]\n"
RD"a[80:83],v227 offset:2048\n"
RD"a[84:87],v227 offset:10240\n"
MF"v[52:55],a[120:123],a[40:43],v[52:55]\n"
WT"lgkmcnt(3)\n"
EX"v25,v25\n"
MF"v[52:55],a[124:127],a[44:47],v[52:55]\n"
RD"a[88:91],v227 offset:3072\n"
PK"v16,v16,v17\n"
MF"v[60:63],a[124:127],a[60:63],v[60:63]\n"
EX"v26,v26\n"
MF"v[60:63],a[120:123],a[56:59],v[60:63]\n"
SA"s41,s41,32768\n"
BL"v225,s[8:11],s44 offen offset:3072 sc1 lds\n"
MF"v[144:147],a[64:67],v[64:67],v[144:147]\n"
"s_cmp_eq_u32 s41,163840\n"
EX"v27,v27\n"
MF"v[144:147],a[68:71],v[72:75],v[144:147]\n"
"s_cselect_b32 s41,0,s41\n"
RD"a[92:95],v227 offset:11264\n"
"v_add_u32 v226,s41,v224\n"
MF"v[176:179],a[68:71],v[76:79],v[176:179]\n"
EX"v20,v20\n"
MF"v[176:179],a[64:67],v[68:71],v[176:179]\n"
"s_mov_b32 m0,s40\n"
RD"a[96:99],v227 offset:4096\n"
RD"a[100:103],v227 offset:12288\n"
MF"v[148:151],a[72:75],v[64:67],v[148:151]\n"
WT"lgkmcnt(3)\n"
EX"v21,v21\n"
MF"v[148:151],a[76:79],v[72:75],v[148:151]\n"
RD"a[104:107],v227 offset:5120\n"
"v_readlane_b32 s43,v228,s42\n"
MF"v[180:183],a[76:79],v[76:79],v[180:183]\n"
EX"v22,v22\n"
MF"v[180:183],a[72:75],v[68:71],v[180:183]\n"
"v_readlane_b32 s44,v229,s42\n"
PK"v17,v18,v19\n"
MF"v[152:155],a[80:83],v[64:67],v[152:155]\n"
SA"s42,s42,1\n"
BL"v225,s[8:11],s43 offen offset:0 sc1 lds\n"
MF"v[152:155],a[84:87],v[72:75],v[152:155]\n"
EX"v23,v23\n"
MF"v[184:187],a[84:87],v[76:79],v[184:187]\n"
RD"a[108:111],v227 offset:13312\n"
PK"v24,v24,v25\n"
MF"v[184:187],a[80:83],v[68:71],v[184:187]\n"
EX"v28,v28\n"
MF"v[156:159],a[88:91],v[64:67],v[156:159]\n"
WT"lgkmcnt(1)\n"
RD"a[112:115],v227 offset:6144\n"
RD"a[116:119],v227 offset:14336\n"
MF"v[156:159],a[92:95],v[72:75],v[156:159]\n"
EX"v29,v29\n"
MF"v[188:191],a[92:95],v[76:79],v[188:191]\n"
RD"a[120:123],v227 offset:7168\n"
PK"v25,v26,v27\n"
MF"v[188:191],a[88:91],v[68:71],v[188:191]\n"
EX"v30,v30\n"
MF"v[160:163],a[96:99],v[64:67],v[160:163]\n"
BL"v225,s[8:11],s43 offen offset:1024 sc1 lds\n"
MF"v[160:163],a[100:103],v[72:75],v[160:163]\n"
EX"v31,v31\n"
MF"v[192:195],a[100:103],v[76:79],v[192:195]\n"
RD"a[124:127],v227 offset:15360\n"
PK"v18,v20,v21\n"
MF"v[192:195],a[96:99],v[68:71],v[192:195]\n"
EX"v32,v32\n"
MF"v[164:167],a[104:107],v[64:67],v[164:167]\n"
WT"lgkmcnt(2)\n"
EX"v33,v33\n"
MF"v[164:167],a[108:111],v[72:75],v[164:167]\n"
RD"a[64:67],v227 offset:16384\n"
PK"v19,v22,v23\n"
MF"v[196:199],a[108:111],v[76:79],v[196:199]\n"
EX"v34,v34\n"
MF"v[196:199],a[104:107],v[68:71],v[196:199]\n"
BL"v225,s[8:11],s43 offen offset:2048 sc1 lds\n"
MF"v[168:171],a[112:115],v[64:67],v[168:171]\n"
EX"v35,v35\n"
MF"v[168:171],a[116:119],v[72:75],v[168:171]\n"
RD"a[68:71],v227 offset:24576\n"
PK"v26,v28,v29\n"
MF"v[200:203],a[116:119],v[76:79],v[200:203]\n"
EX"v40,v40\n"
MF"v[200:203],a[112:115],v[68:71],v[200:203]\n"
WT"lgkmcnt(2)\n"
RD"a[72:75],v227 offset:17408\n"
RD"a[76:79],v227 offset:25600\n"
MF"v[172:175],a[120:123],v[64:67],v[172:175]\n"
EX"v41,v41\n"
MF"v[172:175],a[124:127],v[72:75],v[172:175]\n"
RD"a[80:83],v227 offset:18432\n"
PK"v27,v30,v31\n"
MF"v[204:207],a[124:127],v[76:79],v[204:207]\n"
EX"v42,v42\n"
MF"v[204:207],a[120:123],v[68:71],v[204:207]\n"
BL"v225,s[8:11],s43 offen offset:3072 sc1 lds\n"
MF"a[140:143],a[128:131],v[64:67],a[140:143]\n"
EX"v43,v43\n"
MF"a[140:143],a[128:131],v[72:75],a[140:143]\n"
RD"a[84:87],v227 offset:26624\n"
PK"v64,v32,v33\n"
MF"a[144:147],a[128:131],v[76:79],a[144:147]\n"
EX"v36,v36\n"
MF"a[144:147],a[128:131],v[68:71],a[144:147]\n"
WT"lgkmcnt(2)\n"
RD"a[88:91],v227 offset:19456\n"
RD"a[92:95],v227 offset:27648\n"
MF"v[144:147],a[64:67],v[0:3],v[144:147]\n"
SA"m0,s40,16384\n"
EX"v37,v37\n"
MF"v[144:147],a[68:71],v[16:19],v[144:147]\n"
"buffer_load_dwordx2 v[242:243],v236,s[8:11],0 offen sc1\n"
MF"v[176:179],a[68:71],v[24:27],v[176:179]\n"
RD"a[96:99],v227 offset:20480\n"
PK"v65,v34,v35\n"
MF"v[176:179],a[64:67],v[8:11],v[176:179]\n"
EX"v38,v38\n"
MF"v[148:151],a[72:75],v[0:3],v[148:151]\n"
SA"s40,s40,32768\n"
BL"v225,s[8:11],s44 offen offset:0 sc1 lds\n"
MF"v[148:151],a[76:79],v[16:19],v[148:151]\n"
"s_cmp_eq_u32 s40,s46\n"
EX"v39,v39\n"
MF"v[180:183],a[76:79],v[24:27],v[180:183]\n"
"s_cselect_b32 s40,s47,s40\n"
RD"a[100:103],v227 offset:28672\n"
PK"v68,v40,v41\n"
MF"v[180:183],a[72:75],v[8:11],v[180:183]\n"
WT"lgkmcnt(2)\n"
EX"v44,v44\n"
MF"v[152:155],a[80:83],v[0:3],v[152:155]\n"
RD"a[104:107],v227 offset:21504\n"
RD"a[108:111],v227 offset:29696\n"
MF"v[152:155],a[84:87],v[16:19],v[152:155]\n"
EX"v45,v45\n"
MF"v[184:187],a[84:87],v[24:27],v[184:187]\n"
RD"a[112:115],v227 offset:22528\n"
PK"v69,v42,v43\n"
MF"v[184:187],a[80:83],v[8:11],v[184:187]\n"
EX"v46,v46\n"
MF"v[156:159],a[88:91],v[0:3],v[156:159]\n"
BL"v225,s[8:11],s44 offen offset:1024 sc1 lds\n"
MF"v[156:159],a[92:95],v[16:19],v[156:159]\n"
EX"v47,v47\n"
MF"v[188:191],a[92:95],v[24:27],v[188:191]\n"
RD"a[116:119],v227 offset:30720\n"
PK"v66,v36,v37\n"
MF"v[188:191],a[88:91],v[8:11],v[188:191]\n"
WT"lgkmcnt(2)\n"
EX"v48,v48\n"
MF"v[160:163],a[96:99],v[0:3],v[160:163]\n"
RD"a[120:123],v227 offset:23552\n"
RD"a[124:127],v227 offset:31744\n"
MF"v[160:163],a[100:103],v[16:19],v[160:163]\n"
EX"v49,v49\n"
MF"v[192:195],a[100:103],v[24:27],v[192:195]\n"
EX"v50,v50\n"
MF"v[192:195],a[96:99],v[8:11],v[192:195]\n"
BL"v225,s[8:11],s44 offen offset:2048 sc1 lds\n"
MF"v[164:167],a[104:107],v[0:3],v[164:167]\n"
WT"vmcnt(16)\n"
"s_barrier\n"
MF"v[164:167],a[108:111],v[16:19],v[164:167]\n"
RD"a[64:67],v226 offset:0\n"
PK"v67,v38,v39\n"
MF"v[196:199],a[108:111],v[24:27],v[196:199]\n"
EX"v51,v51\n"
MF"v[196:199],a[104:107],v[8:11],v[196:199]\n"
WT"lgkmcnt(1)\n"
RD"a[68:71],v226 offset:1024\n"
PK"v70,v44,v45\n"
MF"v[168:171],a[112:115],v[0:3],v[168:171]\n"
EX"v52,v52\n"
MF"v[168:171],a[116:119],v[16:19],v[168:171]\n"
RD"a[72:75],v226 offset:2048\n"
PK"v71,v46,v47\n"
MF"v[200:203],a[116:119],v[24:27],v[200:203]\n"
EX"v53,v53\n"
MF"v[200:203],a[112:115],v[8:11],v[200:203]\n"
BL"v225,s[8:11],s44 offen offset:3072 sc1 lds\n"
MF"v[172:175],a[120:123],v[0:3],v[172:175]\n"
EX"v54,v54\n"
MF"v[172:175],a[124:127],v[16:19],v[172:175]\n"
"buffer_store_dwordx2 v[232:233],v230,s[8:11],s57 offen sc1\n"
MF"v[204:207],a[124:127],v[24:27],v[204:207]\n"
"s_mov_b32 s57,2130706432\n"
RD"a[76:79],v226 offset:3072\n"
PK"v72,v48,v49\n"
MF"v[204:207],a[120:123],v[8:11],v[204:207]\n"
EX"v55,v55\n"
MF"a[140:143],a[128:131],v[0:3],a[140:143]\n"
RD"a[80:83],v226 offset:4096\n"
RD"a[84:87],v226 offset:5120\n"
MF"a[140:143],a[128:131],v[16:19],a[140:143]\n"
RD"a[88:91],v226 offset:6144\n"
PK"v73,v50,v51\n"
MF"a[144:147],a[128:131],v[24:27],a[144:147]\n"
PK"v74,v52,v53\n"
PK"v75,v54,v55\n"
MF"a[144:147],a[128:131],v[8:11],a[144:147]\n"
"s_sub_u32 s56,s56,1\n"
"s_cbranch_scc0 LP2_%=\n"
"s_cmp_lt_i32 s22,0\n"
"s_cbranch_scc1 LE2_%=\n"
WT"lgkmcnt(6)\n"
MF"v[0:3],a[64:67],a[32:35],0\n"
WT"lgkmcnt(5)\n"
WT"vmcnt(5)\n"
"v_cmp_eq_u64 vcc,s[28:29],v[242:243]\n"
"s_or_b32 vcc_hi,vcc_hi,-1\n"
"s_cmp_eq_u64 vcc,exec\n"
"s_cbranch_scc0 L1028_%=\n"
"L1029_%=:\n"
MF"v[0:3],a[68:71],a[36:39],v[0:3]\n"
SA"s48,s59,32768\n"
EX"v56,v56\n"
MF"v[8:11],a[68:71],a[52:55],0\n"
"s_and_b32 s48,s48,0x18000\n"
RD"a[92:95],v226 offset:7168\n"
"v_readlane_b32 s43,v228,s42\n"
MF"v[8:11],a[64:67],a[48:51],v[8:11]\n"
WT"lgkmcnt(5)\n"
EX"v57,v57\n"
MF"v[0:3],a[72:75],a[40:43],v[0:3]\n"
WT"lgkmcnt(1)\n"
RD"a[96:99],v226 offset:8192\n"
RD"a[100:103],v226 offset:9216\n"
MF"v[0:3],a[76:79],a[44:47],v[0:3]\n"
"s_andn2_b32 s49,s59,0x18000\n"
EX"v58,v58\n"
MF"v[8:11],a[76:79],a[60:63],v[8:11]\n"
"s_or_b32 s52,s48,s49\n"
RD"a[104:107],v226 offset:10240\n"
"v_readlane_b32 s44,v229,s42\n"
MF"v[8:11],a[72:75],a[56:59],v[8:11]\n"
SA"s53,s52,16384\n"
BL"a[0:3],v224,s[8:11],s52 offen offset:0 sc1\n"
MF"v[4:7],a[80:83],a[32:35],0\n"
"s_mov_b32 m0,s40\n"
EX"v59,v59\n"
MF"v[4:7],a[84:87],a[36:39],v[4:7]\n"
SA"s41,s41,32768\n"
BL"v225,s[8:11],s43 offen offset:0 sc1 lds\n"
MF"v[12:15],a[84:87],a[52:55],0\n"
"s_cmp_eq_u32 s41,163840\n"
EX"v60,v60\n"
MF"v[12:15],a[80:83],a[48:51],v[12:15]\n"
"s_cselect_b32 s41,0,s41\n"
RD"a[108:111],v226 offset:11264\n"
RD"a[112:115],v226 offset:12288\n"
PK"v76,v56,v57\n"
MF"v[4:7],a[88:91],a[40:43],v[4:7]\n"
WT"lgkmcnt(3)\n"
BL"a[4:7],v224,s[8:11],s53 offen offset:0 sc1\n"
EX"v61,v61\n"
MF"v[4:7],a[92:95],a[44:47],v[4:7]\n"
SA"s42,s42,1\n"
RD"a[116:119],v226 offset:13312\n"
RD"a[120:123],v226 offset:14336\n"
"v_add_u32 v227,s41,v224\n"
MF"v[12:15],a[92:95],a[60:63],v[12:15]\n"
EX"v62,v62\n"
MF"v[12:15],a[88:91],a[56:59],v[12:15]\n"
EX"v63,v63\n"
PK"v77,v58,v59\n"
MF"v[16:19],a[96:99],a[32:35],0\n"
BL"a[8:11],v224,s[8:11],s52 offen offset:1024 sc1\n"
MF"v[16:19],a[100:103],a[36:39],v[16:19]\n"
BL"v225,s[8:11],s43 offen offset:1024 sc1 lds\n"
MF"v[24:27],a[100:103],a[52:55],0\n"
EX"v0,v0\n"
MF"v[24:27],a[96:99],a[48:51],v[24:27]\n"
WT"lgkmcnt(2)\n"
RD"a[124:127],v226 offset:15360\n"
RD"a[64:67],v226 offset:16384\n"
PK"v78,v60,v61\n"
MF"v[16:19],a[104:107],a[40:43],v[16:19]\n"
EX"v1,v1\n"
MF"v[16:19],a[108:111],a[44:47],v[16:19]\n"
RD"a[68:71],v226 offset:17408\n"
RD"a[72:75],v226 offset:18432\n"
MF"v[24:27],a[108:111],a[60:63],v[24:27]\n"
BL"a[12:15],v224,s[8:11],s53 offen offset:1024 sc1\n"
EX"v2,v2\n"
MF"v[24:27],a[104:107],a[56:59],v[24:27]\n"
EX"v3,v3\n"
PK"v79,v62,v63\n"
MF"v[20:23],a[112:115],a[32:35],0\n"
WT"lgkmcnt(4)\n"
BL"v225,s[8:11],s43 offen offset:2048 sc1 lds\n"
MF"v[20:23],a[116:119],a[36:39],v[20:23]\n"
EX"v8,v8\n"
MF"v[28:31],a[116:119],a[52:55],0\n"
RD"a[76:79],v226 offset:19456\n"
PK"v0,v0,v1\n"
MF"v[28:31],a[112:115],a[48:51],v[28:31]\n"
BL"a[16:19],v224,s[8:11],s52 offen offset:2048 sc1\n"
MF"v[20:23],a[120:123],a[40:43],v[20:23]\n"
WT"lgkmcnt(1)\n"
EX"v9,v9\n"
MF"v[20:23],a[124:127],a[44:47],v[20:23]\n"
RD"a[80:83],v226 offset:20480\n"
RD"a[84:87],v226 offset:21504\n"
MF"v[28:31],a[124:127],a[60:63],v[28:31]\n"
EX"v10,v10\n"
MF"v[28:31],a[120:123],a[56:59],v[28:31]\n"
RD"a[88:91],v226 offset:22528\n"
PK"v1,v2,v3\n"
MF"v[32:35],a[64:67],a[32:35],0\n"
EX"v11,v11\n"
MF"v[32:35],a[68:71],a[36:39],v[32:35]\n"
BL"a[20:23],v224,s[8:11],s53 offen offset:2048 sc1\n"
BL"v225,s[8:11],s43 offen offset:3072 sc1 lds\n"
MF"v[40:43],a[68:71],a[52:55],0\n"
EX"v4,v4\n"
MF"v[40:43],a[64:67],a[48:51],v[40:43]\n"
SA"m0,s40,16384\n"
RD"a[92:95],v226 offset:23552\n"
RD"a[96:99],v226 offset:24576\n"
PK"v8,v8,v9\n"
MF"v[32:35],a[72:75],a[40:43],v[32:35]\n"
WT"lgkmcnt(3)\n"
EX"v5,v5\n"
MF"v[32:35],a[76:79],a[44:47],v[32:35]\n"
RD"a[100:103],v226 offset:25600\n"
RD"a[104:107],v226 offset:26624\n"
MF"v[40:43],a[76:79],a[60:63],v[40:43]\n"
BL"a[24:27],v224,s[8:11],s52 offen offset:3072 sc1\n"
EX"v6,v6\n"
MF"v[40:43],a[72:75],a[56:59],v[40:43]\n"
EX"v7,v7\n"
PK"v9,v10,v11\n"
MF"v[36:39],a[80:83],a[32:35],0\n"
SA"s40,s40,32768\n"
BL"v225,s[8:11],s44 offen offset:0 sc1 lds\n"
MF"v[36:39],a[84:87],a[36:39],v[36:39]\n"
"s_cmp_eq_u32 s40,s46\n"
EX"v12,v12\n"
MF"v[44:47],a[84:87],a[52:55],0\n"
"s_cselect_b32 s40,s47,s40\n"
BL"a[28:31],v224,s[8:11],s53 offen offset:3072 sc1\n"
MF"v[44:47],a[80:83],a[48:51],v[44:47]\n"
WT"lgkmcnt(2)\n"
RD"a[108:111],v226 offset:27648\n"
RD"a[112:115],v226 offset:28672\n"
PK"v2,v4,v5\n"
MF"v[36:39],a[88:91],a[40:43],v[36:39]\n"
SA"s48,s59,65536\n"
EX"v13,v13\n"
MF"v[36:39],a[92:95],a[44:47],v[36:39]\n"
"s_and_b32 s48,s48,0x18000\n"
RD"a[116:119],v226 offset:29696\n"
RD"a[120:123],v226 offset:30720\n"
MF"v[44:47],a[92:95],a[60:63],v[44:47]\n"
"s_andn2_b32 s49,s59,0x18000\n"
EX"v14,v14\n"
MF"v[44:47],a[88:91],a[56:59],v[44:47]\n"
"s_or_b32 s52,s48,s49\n"
EX"v15,v15\n"
PK"v3,v6,v7\n"
MF"v[48:51],a[96:99],a[32:35],0\n"
WT"lgkmcnt(4)\n"
BL"a[180:183],v224,s[8:11],s52 offen offset:0 sc1\n"
MF"v[48:51],a[100:103],a[36:39],v[48:51]\n"
SA"s53,s52,16384\n"
BL"v225,s[8:11],s44 offen offset:1024 sc1 lds\n"
MF"v[56:59],a[100:103],a[52:55],0\n"
EX"v16,v16\n"
MF"v[56:59],a[96:99],a[48:51],v[56:59]\n"
RD"a[124:127],v226 offset:31744\n"
PK"v10,v12,v13\n"
MF"v[48:51],a[104:107],a[40:43],v[48:51]\n"
WT"lgkmcnt(1)\n"
"s_barrier\n"
MF"v[48:51],a[108:111],a[44:47],v[48:51]\n"
RD"a[64:67],v227 offset:0\n"
RD"a[68:71],v227 offset:8192\n"
MF"v[56:59],a[108:111],a[60:63],v[56:59]\n"
BL"a[184:187],v224,s[8:11],s53 offen offset:0 sc1\n"
EX"v17,v17\n"
MF"v[56:59],a[104:107],a[56:59],v[56:59]\n"
RD"a[72:75],v227 offset:1024\n"
PK"v11,v14,v15\n"
MF"v[52:55],a[112:115],a[32:35],0\n"
EX"v18,v18\n"
MF"v[52:55],a[116:119],a[36:39],v[52:55]\n"
BL"v225,s[8:11],s44 offen offset:2048 sc1 lds\n"
MF"v[60:63],a[116:119],a[52:55],0\n"
EX"v19,v19\n"
MF"v[60:63],a[112:115],a[48:51],v[60:63]\n"
BL"a[188:191],v224,s[8:11],s52 offen offset:1024 sc1\n"
RD"a[76:79],v227 offset:9216\n"
MF"v[52:55],a[120:123],a[40:43],v[52:55]\n"
WT"lgkmcnt(2)\n"
EX"v24,v24\n"
MF"v[52:55],a[124:127],a[44:47],v[52:55]\n"
RD"a[80:83],v227 offset:2048\n"
RD"a[84:87],v227 offset:10240\n"
PK"v16,v16,v17\n"
MF"v[60:63],a[124:127],a[60:63],v[60:63]\n"
EX"v25,v25\n"
MF"v[60:63],a[120:123],a[56:59],v[60:63]\n"
SA"s41,s41,32768\n"
RD"a[88:91],v227 offset:3072\n"
"v_readlane_b32 s43,v228,s42\n"
MF"v[144:147],a[64:67],v[64:67],v[144:147]\n"
"s_cmp_eq_u32 s41,163840\n"
BL"a[192:195],v224,s[8:11],s53 offen offset:1024 sc1\n"
EX"v26,v26\n"
MF"v[144:147],a[68:71],v[72:75],v[144:147]\n"
"s_cselect_b32 s41,0,s41\n"
BL"v225,s[8:11],s44 offen offset:3072 sc1 lds\n"
PK"v17,v18,v19\n"
MF"v[176:179],a[68:71],v[76:79],v[176:179]\n"
EX"v27,v27\n"
MF"v[176:179],a[64:67],v[68:71],v[176:179]\n"
WT"lgkmcnt(3)\n"
RD"a[92:95],v227 offset:11264\n"
RD"a[96:99],v227 offset:4096\n"
MF"v[148:151],a[72:75],v[64:67],v[148:151]\n"
"s_mov_b32 m0,s40\n"
EX"v20,v20\n"
MF"v[148:151],a[76:79],v[72:75],v[148:151]\n"
RD"a[100:103],v227 offset:12288\n"
RD"a[104:107],v227 offset:5120\n"
PK"v24,v24,v25\n"
MF"v[180:183],a[76:79],v[76:79],v[180:183]\n"
BL"a[196:199],v224,s[8:11],s52 offen offset:2048 sc1\n"
EX"v21,v21\n"
MF"v[180:183],a[72:75],v[68:71],v[180:183]\n"
WT"lgkmcnt(4)\n"
EX"v22,v22\n"
"v_add_u32 v226,s41,v224\n"
MF"v[152:155],a[80:83],v[64:67],v[152:155]\n"
BL"v225,s[8:11],s43 offen offset:0 sc1 lds\n"
MF"v[152:155],a[84:87],v[72:75],v[152:155]\n"
EX"v23,v23\n"
PK"v25,v26,v27\n"
MF"v[184:187],a[84:87],v[76:79],v[184:187]\n"
BL"a[200:203],v224,s[8:11],s53 offen offset:2048 sc1\n"
MF"v[184:187],a[80:83],v[68:71],v[184:187]\n"
RD"a[108:111],v227 offset:13312\n"
RD"a[112:115],v227 offset:6144\n"
MF"v[156:159],a[88:91],v[64:67],v[156:159]\n"
WT"lgkmcnt(2)\n"
EX"v28,v28\n"
MF"v[156:159],a[92:95],v[72:75],v[156:159]\n"
RD"a[116:119],v227 offset:14336\n"
RD"a[120:123],v227 offset:7168\n"
"v_readlane_b32 s44,v229,s42\n"
MF"v[188:191],a[92:95],v[76:79],v[188:191]\n"
SA"s42,s42,1\n"
EX"v29,v29\n"
PK"v18,v20,v21\n"
MF"v[188:191],a[88:91],v[68:71],v[188:191]\n"
BL"a[204:207],v224,s[8:11],s52 offen offset:3072 sc1\n"
MF"v[160:163],a[96:99],v[64:67],v[160:163]\n"
EX"v30,v30\n"
MF"v[160:163],a[100:103],v[72:75],v[160:163]\n"
BL"v225,s[8:11],s43 offen offset:1024 sc1 lds\n"
PK"v19,v22,v23\n"
MF"v[192:195],a[100:103],v[76:79],v[192:195]\n"
EX"v31,v31\n"
MF"v[192:195],a[96:99],v[68:71],v[192:195]\n"
RD"a[124:127],v227 offset:15360\n"
PK"v26,v28,v29\n"
MF"v[164:167],a[104:107],v[64:67],v[164:167]\n"
WT"lgkmcnt(1)\n"
EX"v32,v32\n"
MF"v[164:167],a[108:111],v[72:75],v[164:167]\n"
SA"s48,s59,98304\n"
BL"a[208:211],v224,s[8:11],s53 offen offset:3072 sc1\n"
MF"v[196:199],a[108:111],v[76:79],v[196:199]\n"
"s_and_b32 s48,s48,0x18000\n"
EX"v33,v33\n"
MF"v[196:199],a[104:107],v[68:71],v[196:199]\n"
"s_andn2_b32 s49,s59,0x18000\n"
RD"a[64:67],v227 offset:16384\n"
PK"v27,v30,v31\n"
MF"v[168:171],a[112:115],v[64:67],v[168:171]\n"
"s_or_b32 s52,s48,s49\n"
EX"v34,v34\n"
MF"v[168:171],a[116:119],v[72:75],v[168:171]\n"
SA"s53,s52,16384\n"
BL"v225,s[8:11],s43 offen offset:2048 sc1 lds\n"
MF"v[200:203],a[116:119],v[76:79],v[200:203]\n"
EX"v35,v35\n"
MF"v[200:203],a[112:115],v[68:71],v[200:203]\n"
BL"a[212:215],v224,s[8:11],s52 offen offset:0 sc1\n"
RD"a[68:71],v227 offset:24576\n"
MF"v[172:175],a[120:123],v[64:67],v[172:175]\n"
WT"lgkmcnt(2)\n"
EX"v40,v40\n"
MF"v[172:175],a[124:127],v[72:75],v[172:175]\n"
RD"a[72:75],v227 offset:17408\n"
RD"a[76:79],v227 offset:25600\n"
MF"v[204:207],a[124:127],v[76:79],v[204:207]\n"
EX"v41,v41\n"
MF"v[204:207],a[120:123],v[68:71],v[204:207]\n"
RD"a[80:83],v227 offset:18432\n"
MF"a[140:143],a[128:131],v[64:67],a[140:143]\n"
BL"a[216:219],v224,s[8:11],s53 offen offset:0 sc1\n"
EX"v42,v42\n"
MF"a[140:143],a[128:131],v[72:75],a[140:143]\n"
BL"v225,s[8:11],s43 offen offset:3072 sc1 lds\n"
PK"v64,v32,v33\n"
MF"a[144:147],a[128:131],v[76:79],a[144:147]\n"
EX"v43,v43\n"
PK"v65,v34,v35\n"
MF"a[144:147],a[128:131],v[68:71],a[144:147]\n"
WT"lgkmcnt(3)\n"
RD"a[84:87],v227 offset:26624\n"
RD"a[88:91],v227 offset:19456\n"
MF"v[144:147],a[64:67],v[0:3],v[144:147]\n"
SA"m0,s40,16384\n"
EX"v36,v36\n"
MF"v[144:147],a[68:71],v[16:19],v[144:147]\n"
BL"a[220:223],v224,s[8:11],s52 offen offset:1024 sc1\n"
RD"a[92:95],v227 offset:27648\n"
MF"v[176:179],a[68:71],v[24:27],v[176:179]\n"
"buffer_load_dwordx2 v[242:243],v236,s[8:11],0 offen sc1\n"
EX"v37,v37\n"
MF"v[176:179],a[64:67],v[8:11],v[176:179]\n"
WT"lgkmcnt(3)\n"
RD"a[96:99],v227 offset:20480\n"
PK"v68,v40,v41\n"
MF"v[148:151],a[72:75],v[0:3],v[148:151]\n"
SA"s40,s40,32768\n"
EX"v38,v38\n"
MF"v[148:151],a[76:79],v[16:19],v[148:151]\n"
"s_cmp_eq_u32 s40,s46\n"
BL"v225,s[8:11],s44 offen offset:0 sc1 lds\n"
PK"v69,v42,v43\n"
MF"v[180:183],a[76:79],v[24:27],v[180:183]\n"
"s_cselect_b32 s40,s47,s40\n"
BL"a[224:227],v224,s[8:11],s53 offen offset:1024 sc1\n"
EX"v39,v39\n"
MF"v[180:183],a[72:75],v[8:11],v[180:183]\n"
RD"a[100:103],v227 offset:28672\n"
RD"a[104:107],v227 offset:21504\n"
MF"v[152:155],a[80:83],v[0:3],v[152:155]\n"
WT"lgkmcnt(3)\n"
EX"v44,v44\n"
MF"v[152:155],a[84:87],v[16:19],v[152:155]\n"
RD"a[108:111],v227 offset:29696\n"
RD"a[112:115],v227 offset:22528\n"
PK"v66,v36,v37\n"
MF"v[184:187],a[84:87],v[24:27],v[184:187]\n"
EX"v45,v45\n"
MF"v[184:187],a[80:83],v[8:11],v[184:187]\n"
BL"a[228:231],v224,s[8:11],s52 offen offset:2048 sc1\n"
MF"v[156:159],a[88:91],v[0:3],v[156:159]\n"
EX"v46,v46\n"
MF"v[156:159],a[92:95],v[16:19],v[156:159]\n"
BL"v225,s[8:11],s44 offen offset:1024 sc1 lds\n"
PK"v67,v38,v39\n"
MF"v[188:191],a[92:95],v[24:27],v[188:191]\n"
EX"v47,v47\n"
MF"v[188:191],a[88:91],v[8:11],v[188:191]\n"
WT"lgkmcnt(2)\n"
RD"a[116:119],v227 offset:30720\n"
RD"a[120:123],v227 offset:23552\n"
MF"v[160:163],a[96:99],v[0:3],v[160:163]\n"
EX"v48,v48\n"
MF"v[160:163],a[100:103],v[16:19],v[160:163]\n"
BL"a[232:235],v224,s[8:11],s53 offen offset:2048 sc1\n"
RD"a[124:127],v227 offset:31744\n"
MF"v[192:195],a[100:103],v[24:27],v[192:195]\n"
EX"v49,v49\n"
PK"v70,v44,v45\n"
MF"v[192:195],a[96:99],v[8:11],v[192:195]\n"
EX"v50,v50\n"
MF"v[164:167],a[104:107],v[0:3],v[164:167]\n"
WT"lgkmcnt(3)\n"
BL"v225,s[8:11],s44 offen offset:2048 sc1 lds\n"
MF"v[164:167],a[108:111],v[16:19],v[164:167]\n"
BL"a[236:239],v224,s[8:11],s52 offen offset:3072 sc1\n"
PK"v71,v46,v47\n"
MF"v[196:199],a[108:111],v[24:27],v[196:199]\n"
WT"vmcnt(40)\n"
"s_barrier\n"
MF"v[196:199],a[104:107],v[8:11],v[196:199]\n"
RD"a[64:67],v226 offset:0\n"
RD"a[68:71],v226 offset:1024\n"
MF"v[168:171],a[112:115],v[0:3],v[168:171]\n"
WT"lgkmcnt(2)\n"
EX"v51,v51\n"
MF"v[168:171],a[116:119],v[16:19],v[168:171]\n"
EX"v52,v52\n"
PK"v72,v48,v49\n"
MF"v[200:203],a[116:119],v[24:27],v[200:203]\n"
BL"a[240:243],v224,s[8:11],s53 offen offset:3072 sc1\n"
MF"v[200:203],a[112:115],v[8:11],v[200:203]\n"
RD"a[72:75],v226 offset:2048\n"
PK"v73,v50,v51\n"
MF"v[172:175],a[120:123],v[0:3],v[172:175]\n"
EX"v53,v53\n"
MF"v[172:175],a[124:127],v[16:19],v[172:175]\n"
BL"v225,s[8:11],s44 offen offset:3072 sc1 lds\n"
MF"v[204:207],a[124:127],v[24:27],v[204:207]\n"
EX"v54,v54\n"
MF"v[204:207],a[120:123],v[8:11],v[204:207]\n"
RD"a[76:79],v226 offset:3072\n"
RD"a[80:83],v226 offset:4096\n"
MF"a[140:143],a[128:131],v[0:3],a[140:143]\n"
EX"v55,v55\n"
MF"a[140:143],a[128:131],v[16:19],a[140:143]\n"
RD"a[84:87],v226 offset:5120\n"
RD"a[88:91],v226 offset:6144\n"
MF"a[144:147],a[128:131],v[24:27],a[144:147]\n"
PK"v74,v52,v53\n"
PK"v75,v54,v55\n"
MF"a[144:147],a[128:131],v[8:11],a[144:147]\n"
"s_sub_u32 s56,s22,1\n"
"s_mov_b32 s22,-1\n"
"s_cbranch_scc0 LP2_%=\n"
"LE2_%=:\n"
WT"lgkmcnt(6)\n"
MF"v[0:3],a[64:67],a[32:35],v[208:211]\n"
WT"lgkmcnt(5)\n"
WT"vmcnt(5)\n"
"v_cmp_eq_u64 vcc,s[28:29],v[242:243]\n"
"s_or_b32 vcc_lo,vcc_lo,-1\n"
"s_cmp_eq_u64 vcc,exec\n"
"s_cbranch_scc0 L1031_%=\n"
"L1032_%=:\n"
MF"v[0:3],a[68:71],a[36:39],v[0:3]\n"
"s_mov_b32 m0,s40\n"
EX"v56,v56\n"
MF"v[8:11],a[68:71],a[52:55],0\n"
EX"v57,v57\n"
MF"v[8:11],a[64:67],a[48:51],v[8:11]\n"
WT"lgkmcnt(4)\n"
RD"a[92:95],v226 offset:7168\n"
RD"a[96:99],v226 offset:8192\n"
"v_readlane_b32 s43,v228,s42\n"
MF"v[0:3],a[72:75],a[40:43],v[0:3]\n"
WT"lgkmcnt(2)\n"
EX"v58,v58\n"
"v_readlane_b32 s44,v229,s42\n"
MF"v[0:3],a[76:79],a[44:47],v[0:3]\n"
SA"s42,s42,1\n"
BL"v225,s[8:11],s43 offen offset:0 sc1 lds\n"
RD"a[100:103],v226 offset:9216\n"
MF"v[8:11],a[76:79],a[60:63],v[8:11]\n"
SA"s41,s41,32768\n"
EX"v59,v59\n"
PK"v76,v56,v57\n"
MF"v[8:11],a[72:75],a[56:59],v[8:11]\n"
"s_cmp_eq_u32 s41,163840\n"
RD"a[104:107],v226 offset:10240\n"
PK"v77,v58,v59\n"
MF"v[4:7],a[80:83],a[32:35],v[212:215]\n"
"s_cselect_b32 s41,0,s41\n"
EX"v60,v60\n"
"v_add_u32 v227,s41,v224\n"
MF"v[4:7],a[84:87],a[36:39],v[4:7]\n"
BL"v225,s[8:11],s43 offen offset:1024 sc1 lds\n"
MF"v[12:15],a[84:87],a[52:55],0\n"
EX"v61,v61\n"
MF"v[12:15],a[80:83],a[48:51],v[12:15]\n"
RD"a[108:111],v226 offset:11264\n"
RD"a[112:115],v226 offset:12288\n"
MF"v[4:7],a[88:91],a[40:43],v[4:7]\n"
WT"lgkmcnt(3)\n"
EX"v62,v62\n"
MF"v[4:7],a[92:95],a[44:47],v[4:7]\n"
RD"a[116:119],v226 offset:13312\n"
RD"a[120:123],v226 offset:14336\n"
PK"v78,v60,v61\n"
MF"v[12:15],a[92:95],a[60:63],v[12:15]\n"
EX"v63,v63\n"
MF"v[12:15],a[88:91],a[56:59],v[12:15]\n"
BL"v225,s[8:11],s43 offen offset:2048 sc1 lds\n"
MF"v[16:19],a[96:99],a[32:35],v[216:219]\n"
EX"v0,v0\n"
MF"v[16:19],a[100:103],a[36:39],v[16:19]\n"
RD"a[124:127],v226 offset:15360\n"
RD"a[64:67],v226 offset:16384\n"
PK"v79,v62,v63\n"
MF"v[24:27],a[100:103],a[52:55],0\n"
EX"v1,v1\n"
MF"v[24:27],a[96:99],a[48:51],v[24:27]\n"
WT"lgkmcnt(4)\n"
RD"a[68:71],v226 offset:17408\n"
RD"a[72:75],v226 offset:18432\n"
MF"v[16:19],a[104:107],a[40:43],v[16:19]\n"
BL"v225,s[8:11],s43 offen offset:3072 sc1 lds\n"
MF"v[16:19],a[108:111],a[44:47],v[16:19]\n"
SA"m0,s40,16384\n"
EX"v2,v2\n"
PK"v0,v0,v1\n"
MF"v[24:27],a[108:111],a[60:63],v[24:27]\n"
RD"a[76:79],v226 offset:19456\n"
RD"a[80:83],v226 offset:20480\n"
MF"v[24:27],a[104:107],a[56:59],v[24:27]\n"
EX"v3,v3\n"
MF"v[20:23],a[112:115],a[32:35],v[220:223]\n"
WT"lgkmcnt(4)\n"
RD"a[84:87],v226 offset:21504\n"
RD"a[88:91],v226 offset:22528\n"
MF"v[20:23],a[116:119],a[36:39],v[20:23]\n"
EX"v8,v8\n"
PK"v1,v2,v3\n"
MF"v[28:31],a[116:119],a[52:55],0\n"
SA"s40,s40,32768\n"
BL"v225,s[8:11],s44 offen offset:0 sc1 lds\n"
MF"v[28:31],a[112:115],a[48:51],v[28:31]\n"
"s_cmp_eq_u32 s40,s46\n"
RD"a[92:95],v226 offset:23552\n"
RD"a[96:99],v226 offset:24576\n"
EX"v9,v9\n"
MF"v[20:23],a[120:123],a[40:43],v[20:23]\n"
"s_cselect_b32 s40,s47,s40\n"
EX"v10,v10\n"
MF"v[20:23],a[124:127],a[44:47],v[20:23]\n"
RD"a[100:103],v226 offset:25600\n"
RD"a[104:107],v226 offset:26624\n"
PK"v8,v8,v9\n"
MF"v[28:31],a[124:127],a[60:63],v[28:31]\n"
RD"a[108:111],v226 offset:27648\n"
RD"a[112:115],v226 offset:28672\n"
EX"v11,v11\n"
MF"v[28:31],a[120:123],a[56:59],v[28:31]\n"
BL"v225,s[8:11],s44 offen offset:1024 sc1 lds\n"
MF"v[40:43],a[64:67],a[48:51],v[208:211]\n"
WT"lgkmcnt(6)\n"
RD"a[116:119],v226 offset:29696\n"
RD"a[120:123],v226 offset:30720\n"
EX"v4,v4\n"
MF"v[40:43],a[68:71],a[52:55],v[40:43]\n"
EX"v5,v5\n"
PK"v9,v10,v11\n"
MF"v[40:43],a[72:75],a[56:59],v[40:43]\n"
RD"a[124:127],v226 offset:31744\n"
PK"v2,v4,v5\n"
MF"v[40:43],a[76:79],a[60:63],v[40:43]\n"
EX"v6,v6\n"
MF"v[44:47],a[80:83],a[48:51],v[212:215]\n"
"s_barrier\n"
MF"v[44:47],a[84:87],a[52:55],v[44:47]\n"
RD"a[64:67],v227 offset:0\n"
RD"a[68:71],v227 offset:8192\n"
MF"v[44:47],a[88:91],a[56:59],v[44:47]\n"
WT"lgkmcnt(5)\n"
BL"v225,s[8:11],s44 offen offset:2048 sc1 lds\n"
EX"v7,v7\n"
MF"v[44:47],a[92:95],a[60:63],v[44:47]\n"
RD"a[72:75],v227 offset:1024\n"
"v_readlane_b32 s43,v228,s42\n"
MF"v[56:59],a[96:99],a[48:51],v[216:219]\n"
EX"v12,v12\n"
PK"v3,v6,v7\n"
MF"v[56:59],a[100:103],a[52:55],v[56:59]\n"
EX"v13,v13\n"
MF"v[56:59],a[104:107],a[56:59],v[56:59]\n"
BL"v225,s[8:11],s44 offen offset:3072 sc1 lds\n"
MF"v[56:59],a[108:111],a[60:63],v[56:59]\n"
"s_mov_b32 m0,s40\n"
RD"a[76:79],v227 offset:9216\n"
RD"a[80:83],v227 offset:2048\n"
MF"v[60:63],a[112:115],a[48:51],v[220:223]\n"
WT"lgkmcnt(3)\n"
EX"v14,v14\n"
PK"v10,v12,v13\n"
MF"v[60:63],a[116:119],a[52:55],v[60:63]\n"
RD"a[84:87],v227 offset:10240\n"
RD"a[88:91],v227 offset:3072\n"
MF"v[60:63],a[120:123],a[56:59],v[60:63]\n"
EX"v15,v15\n"
MF"v[60:63],a[124:127],a[60:63],v[60:63]\n"
SA"s41,s41,32768\n"
EX"v16,v16\n"
MF"v[144:147],a[64:67],v[64:67],v[144:147]\n"
"s_cmp_eq_u32 s41,163840\n"
BL"v225,s[8:11],s43 offen offset:0 sc1 lds\n"
"v_readlane_b32 s44,v229,s42\n"
MF"v[144:147],a[68:71],v[72:75],v[144:147]\n"
"s_cselect_b32 s41,0,s41\n"
EX"v17,v17\n"
PK"v11,v14,v15\n"
MF"v[176:179],a[68:71],v[76:79],v[176:179]\n"
SA"s42,s42,1\n"
RD"a[92:95],v227 offset:11264\n"
"v_add_u32 v226,s41,v224\n"
MF"v[176:179],a[64:67],v[68:71],v[176:179]\n"
WT"lgkmcnt(3)\n"
EX"v18,v18\n"
MF"v[148:151],a[72:75],v[64:67],v[148:151]\n"
RD"a[96:99],v227 offset:4096\n"
RD"a[100:103],v227 offset:12288\n"
PK"v16,v16,v17\n"
MF"v[148:151],a[76:79],v[72:75],v[148:151]\n"
EX"v19,v19\n"
MF"v[180:183],a[76:79],v[76:79],v[180:183]\n"
BL"v225,s[8:11],s43 offen offset:1024 sc1 lds\n"
MF"v[180:183],a[72:75],v[68:71],v[180:183]\n"
RD"a[104:107],v227 offset:5120\n"
PK"v17,v18,v19\n"
MF"v[152:155],a[80:83],v[64:67],v[152:155]\n"
WT"lgkmcnt(4)\n"
EX"v24,v24\n"
MF"v[152:155],a[84:87],v[72:75],v[152:155]\n"
EX"v25,v25\n"
MF"v[184:187],a[84:87],v[76:79],v[184:187]\n"
EX"v26,v26\n"
MF"v[184:187],a[80:83],v[68:71],v[184:187]\n"
RD"a[108:111],v227 offset:13312\n"
RD"a[112:115],v227 offset:6144\n"
PK"v24,v24,v25\n"
MF"v[156:159],a[88:91],v[64:67],v[156:159]\n"
WT"lgkmcnt(3)\n"
BL"v225,s[8:11],s43 offen offset:2048 sc1 lds\n"
MF"v[156:159],a[92:95],v[72:75],v[156:159]\n"
EX"v27,v27\n"
MF"v[188:191],a[92:95],v[76:79],v[188:191]\n"
RD"a[116:119],v227 offset:14336\n"
RD"a[120:123],v227 offset:7168\n"
MF"v[188:191],a[88:91],v[68:71],v[188:191]\n"
EX"v20,v20\n"
PK"v25,v26,v27\n"
MF"v[160:163],a[96:99],v[64:67],v[160:163]\n"
EX"v21,v21\n"
MF"v[160:163],a[100:103],v[72:75],v[160:163]\n"
BL"v225,s[8:11],s43 offen offset:3072 sc1 lds\n"
MF"v[192:195],a[100:103],v[76:79],v[192:195]\n"
SA"m0,s40,16384\n"
EX"v22,v22\n"
MF"v[192:195],a[96:99],v[68:71],v[192:195]\n"
WT"lgkmcnt(2)\n"
RD"a[124:127],v227 offset:15360\n"
PK"v18,v20,v21\n"
MF"v[164:167],a[104:107],v[64:67],v[164:167]\n"
EX"v23,v23\n"
MF"v[164:167],a[108:111],v[72:75],v[164:167]\n"
EX"v28,v28\n"
MF"v[196:199],a[108:111],v[76:79],v[196:199]\n"
SA"s40,s40,32768\n"
BL"v225,s[8:11],s44 offen offset:0 sc1 lds\n"
MF"v[196:199],a[104:107],v[68:71],v[196:199]\n"
"s_cmp_eq_u32 s40,s46\n"
RD"a[64:67],v227 offset:16384\n"
PK"v19,v22,v23\n"
MF"v[168:171],a[112:115],v[64:67],v[168:171]\n"
WT"lgkmcnt(2)\n"
EX"v29,v29\n"
MF"v[168:171],a[116:119],v[72:75],v[168:171]\n"
"s_cselect_b32 s40,s47,s40\n"
EX"v30,v30\n"
MF"v[200:203],a[116:119],v[76:79],v[200:203]\n"
RD"a[68:71],v227 offset:24576\n"
PK"v26,v28,v29\n"
MF"v[200:203],a[112:115],v[68:71],v[200:203]\n"
BL"v225,s[8:11],s44 offen offset:1024 sc1 lds\n"
MF"v[172:175],a[120:123],v[64:67],v[172:175]\n"
WT"lgkmcnt(2)\n"
EX"v31,v31\n"
MF"v[172:175],a[124:127],v[72:75],v[172:175]\n"
RD"a[72:75],v227 offset:17408\n"
RD"a[76:79],v227 offset:25600\n"
MF"v[204:207],a[124:127],v[76:79],v[204:207]\n"
EX"v40,v40\n"
MF"v[204:207],a[120:123],v[68:71],v[204:207]\n"
RD"a[80:83],v227 offset:18432\n"
PK"v27,v30,v31\n"
MF"a[140:143],a[128:131],v[64:67],a[140:143]\n"
EX"v41,v41\n"
MF"a[140:143],a[128:131],v[72:75],a[140:143]\n"
BL"v225,s[8:11],s44 offen offset:2048 sc1 lds\n"
MF"a[144:147],a[128:131],v[76:79],a[144:147]\n"
EX"v42,v42\n"
MF"a[144:147],a[128:131],v[68:71],a[144:147]\n"
WT"lgkmcnt(3)\n"
RD"a[84:87],v227 offset:26624\n"
RD"a[88:91],v227 offset:19456\n"
PK"v68,v40,v41\n"
MF"v[144:147],a[64:67],v[0:3],v[144:147]\n"
EX"v43,v43\n"
MF"v[144:147],a[68:71],v[16:19],v[144:147]\n"
RD"a[92:95],v227 offset:27648\n"
RD"a[96:99],v227 offset:20480\n"
MF"v[176:179],a[68:71],v[24:27],v[176:179]\n"
EX"v44,v44\n"
"v_readlane_b32 s43,v228,s42\n"
MF"v[176:179],a[64:67],v[8:11],v[176:179]\n"
WT"lgkmcnt(4)\n"
BL"v225,s[8:11],s44 offen offset:3072 sc1 lds\n"
PK"v69,v42,v43\n"
MF"v[148:151],a[72:75],v[0:3],v[148:151]\n"
"s_mov_b32 m0,s40\n"
EX"v45,v45\n"
MF"v[148:151],a[76:79],v[16:19],v[148:151]\n"
EX"v46,v46\n"
MF"v[180:183],a[76:79],v[24:27],v[180:183]\n"
RD"a[100:103],v227 offset:28672\n"
"v_readlane_b32 s44,v229,s42\n"
PK"v70,v44,v45\n"
MF"v[180:183],a[72:75],v[8:11],v[180:183]\n"
SA"s42,s42,1\n"
EX"v47,v47\n"
MF"v[152:155],a[80:83],v[0:3],v[152:155]\n"
WT"lgkmcnt(1)\n"
RD"a[104:107],v227 offset:21504\n"
RD"a[108:111],v227 offset:29696\n"
MF"v[152:155],a[84:87],v[16:19],v[152:155]\n"
EX"v56,v56\n"
MF"v[184:187],a[84:87],v[24:27],v[184:187]\n"
RD"a[112:115],v227 offset:22528\n"
PK"v71,v46,v47\n"
MF"v[184:187],a[80:83],v[8:11],v[184:187]\n"
EX"v57,v57\n"
MF"v[156:159],a[88:91],v[0:3],v[156:159]\n"
EX"v58,v58\n"
MF"v[156:159],a[92:95],v[16:19],v[156:159]\n"
RD"a[116:119],v227 offset:30720\n"
PK"v76,v56,v57\n"
MF"v[188:191],a[92:95],v[24:27],v[188:191]\n"
EX"v59,v59\n"
MF"v[188:191],a[88:91],v[8:11],v[188:191]\n"
RD"a[120:123],v227 offset:23552\n"
RD"a[124:127],v227 offset:31744\n"
MF"v[160:163],a[96:99],v[0:3],v[160:163]\n"
WT"lgkmcnt(4)\n"
EX"v60,v60\n"
MF"v[160:163],a[100:103],v[16:19],v[160:163]\n"
PK"v77,v58,v59\n"
MF"v[192:195],a[100:103],v[24:27],v[192:195]\n"
EX"v61,v61\n"
MF"v[192:195],a[96:99],v[8:11],v[192:195]\n"
PK"v78,v60,v61\n"
MF"v[164:167],a[104:107],v[0:3],v[164:167]\n"
EX"v62,v62\n"
MF"v[164:167],a[108:111],v[16:19],v[164:167]\n"
SA"s41,s41,32768\n"
WT"vmcnt(16)\n"
"s_barrier\n"
MF"v[196:199],a[108:111],v[24:27],v[196:199]\n"
"s_cmp_eq_u32 s41,163840\n"
BL"v225,s[8:11],s43 offen offset:0 sc1 lds\n"
BL"v225,s[8:11],s43 offen offset:1024 sc1 lds\n"
MF"v[196:199],a[104:107],v[8:11],v[196:199]\n"
WT"lgkmcnt(0)\n"
BL"v225,s[8:11],s43 offen offset:2048 sc1 lds\n"
RD"a[64:67],v226 offset:0\n"
MF"v[168:171],a[112:115],v[0:3],v[168:171]\n"
"s_cselect_b32 s41,0,s41\n"
EX"v63,v63\n"
"v_add_u32 v227,s41,v224\n"
MF"v[168:171],a[116:119],v[16:19],v[168:171]\n"
RD"a[68:71],v226 offset:8192\n"
RD"a[72:75],v226 offset:1024\n"
BL"v225,s[8:11],s43 offen offset:3072 sc1 lds\n"
MF"v[200:203],a[116:119],v[24:27],v[200:203]\n"
SA"m0,s40,16384\n"
RD"a[76:79],v226 offset:9216\n"
RD"a[80:83],v226 offset:2048\n"
PK"v79,v62,v63\n"
MF"v[200:203],a[112:115],v[8:11],v[200:203]\n"
RD"a[84:87],v226 offset:10240\n"
MF"v[172:175],a[120:123],v[0:3],v[172:175]\n"
RD"a[88:91],v226 offset:3072\n"
MF"v[172:175],a[124:127],v[16:19],v[172:175]\n"
BL"v225,s[8:11],s44 offen offset:0 sc1 lds\n"
MF"v[204:207],a[124:127],v[24:27],v[204:207]\n"
SA"s40,s40,32768\n"
RD"a[92:95],v226 offset:11264\n"
RD"a[96:99],v226 offset:4096\n"
MF"v[204:207],a[120:123],v[8:11],v[204:207]\n"
"s_cmp_eq_u32 s40,s46\n"
RD"a[100:103],v226 offset:12288\n"
RD"a[104:107],v226 offset:5120\n"
MF"a[140:143],a[128:131],v[0:3],a[140:143]\n"
"s_cselect_b32 s40,s47,s40\n"
RD"a[108:111],v226 offset:13312\n"
MF"a[140:143],a[128:131],v[16:19],a[140:143]\n"
RD"a[112:115],v226 offset:6144\n"
MF"a[144:147],a[128:131],v[24:27],a[144:147]\n"
BL"v225,s[8:11],s44 offen offset:1024 sc1 lds\n"
MF"a[144:147],a[128:131],v[8:11],a[144:147]\n"
WT"lgkmcnt(6)\n"
RD"a[116:119],v226 offset:14336\n"
MF"v[176:179],a[64:67],v[68:71],v[176:179]\n"
RD"a[120:123],v226 offset:7168\n"
MF"v[176:179],a[68:71],v[76:79],v[176:179]\n"
RD"a[124:127],v226 offset:15360\n"
MF"v[180:183],a[72:75],v[68:71],v[180:183]\n"
WT"vmcnt(14)\n"
"s_barrier\n"
MF"v[180:183],a[76:79],v[76:79],v[180:183]\n"
RD"a[64:67],v227 offset:0\n"
MF"v[184:187],a[80:83],v[68:71],v[184:187]\n"
BL"v225,s[8:11],s44 offen offset:2048 sc1 lds\n"
MF"v[184:187],a[84:87],v[76:79],v[184:187]\n"
RD"a[68:71],v227 offset:1024\n"
MF"v[188:191],a[88:91],v[68:71],v[188:191]\n"
WT"lgkmcnt(4)\n"
MF"v[188:191],a[92:95],v[76:79],v[188:191]\n"
MF"v[192:195],a[96:99],v[68:71],v[192:195]\n"
MF"v[192:195],a[100:103],v[76:79],v[192:195]\n"
BL"v225,s[8:11],s44 offen offset:3072 sc1 lds\n"
MF"v[196:199],a[104:107],v[68:71],v[196:199]\n"
RD"a[72:75],v227 offset:2048\n"
MF"v[196:199],a[108:111],v[76:79],v[196:199]\n"
RD"a[76:79],v227 offset:3072\n"
RD"a[80:83],v227 offset:4096\n"
MF"v[200:203],a[112:115],v[68:71],v[200:203]\n"
MF"v[200:203],a[116:119],v[76:79],v[200:203]\n"
WT"lgkmcnt(3)\n"
RD"a[84:87],v227 offset:5120\n"
MF"v[204:207],a[120:123],v[68:71],v[204:207]\n"
MF"v[204:207],a[124:127],v[76:79],v[204:207]\n"
MF"a[144:147],a[128:131],v[68:71],a[144:147]\n"
MF"a[144:147],a[128:131],v[76:79],a[144:147]\n"
RD"a[88:91],v227 offset:6144\n"
"LTL_%=:\n"
"s_cmp_ge_u32 s34,6\n"
"s_cbranch_scc1 LTH_%=\n"
RD"a[92:95],v227 offset:7168\n"
MF"v[0:3],a[64:67],v[80:83],0\n"
"v_accvgpr_read_b32 v251,a140\n"
"v_subrev_u32 v253,0x2b800000,v251\n"
MF"v[0:3],a[68:71],v[88:91],v[0:3]\n"
RD"a[96:99],v227 offset:8192\n"
MF"v[40:43],a[68:71],v[120:123],0\n"
"v_cmp_le_u32 vcc,0x34000000,v253\n"
"s_cmp_lg_u64 vcc,0\n"
"s_cbranch_scc1 L1041_%=\n"
"L1040_%=:\n"
MF"v[40:43],a[64:67],v[112:115],v[40:43]\n"
WT"lgkmcnt(3)\n"
"v_rcp_f32 v251,v251\n"
MF"v[4:7],a[72:75],v[80:83],0\n"
SA"s41,s41,32768\n"
RD"a[100:103],v227 offset:9216\n"
MU"v144,v251,v144\n"
MF"v[4:7],a[76:79],v[88:91],v[4:7]\n"
"s_cmp_eq_u32 s41,163840\n"
RD"a[104:107],v227 offset:10240\n"
MU"v145,v251,v145\n"
MF"v[44:47],a[76:79],v[120:123],0\n"
"s_cselect_b32 s41,0,s41\n"
MU"v146,v251,v146\n"
"v_add_u32 v226,s41,v224\n"
MF"v[44:47],a[72:75],v[112:115],v[44:47]\n"
RD"a[108:111],v227 offset:11264\n"
MU"v147,v251,v147\n"
MF"v[8:11],a[80:83],v[80:83],0\n"
SA"s41,s41,32768\n"
MU"v148,v251,v148\n"
MU"v149,v251,v149\n"
MF"v[8:11],a[84:87],v[88:91],v[8:11]\n"
"s_cmp_eq_u32 s41,163840\n"
RD"a[112:115],v227 offset:12288\n"
MU"v150,v251,v150\n"
MU"v151,v251,v151\n"
MF"v[48:51],a[84:87],v[120:123],0\n"
"s_cselect_b32 s41,0,s41\n"
PK"v144,v144,v145\n"
"v_add_u32 v235,s41,v224\n"
PK"v145,v146,v147\n"
MF"v[48:51],a[80:83],v[112:115],v[48:51]\n"
WT"lgkmcnt(3)\n"
RD"a[116:119],v227 offset:13312\n"
PK"v146,v148,v149\n"
MF"v[12:15],a[88:91],v[80:83],0\n"
SA"s41,s41,32768\n"
PK"v147,v150,v151\n"
MU"v152,v251,v152\n"
MF"v[12:15],a[92:95],v[88:91],v[12:15]\n"
"s_cmp_eq_u32 s41,163840\n"
BS"v[144:147],v224,s[8:11],s60 offen offset:0 sc1\n"
MU"v153,v251,v153\n"
MF"v[52:55],a[92:95],v[120:123],0\n"
"s_cselect_b32 s41,0,s41\n"
RD"a[120:123],v227 offset:14336\n"
"v_add_u32 v236,s41,v224\n"
MU"v154,v251,v154\n"
MF"v[52:55],a[88:91],v[112:115],v[52:55]\n"
RD"a[124:127],v227 offset:15360\n"
MU"v155,v251,v155\n"
MU"v156,v251,v156\n"
MF"v[16:19],a[96:99],v[80:83],0\n"
SA"s41,s41,32768\n"
MU"v157,v251,v157\n"
MU"v158,v251,v158\n"
MU"v159,v251,v159\n"
MF"v[16:19],a[100:103],v[88:91],v[16:19]\n"
"s_cmp_eq_u32 s41,163840\n"
RD"a[64:67],v227 offset:16384\n"
PK"v152,v152,v153\n"
PK"v153,v154,v155\n"
MF"v[56:59],a[100:103],v[120:123],0\n"
"s_cselect_b32 s41,0,s41\n"
PK"v154,v156,v157\n"
"v_add_u32 v237,s41,v224\n"
PK"v155,v158,v159\n"
MF"v[56:59],a[96:99],v[112:115],v[56:59]\n"
WT"lgkmcnt(3)\n"
BS"v[152:155],v224,s[8:11],s60 offen offset:1024 sc1\n"
MU"v160,v251,v160\n"
MF"v[20:23],a[104:107],v[80:83],0\n"
RD"a[68:71],v227 offset:17408\n"
MU"v161,v251,v161\n"
MU"v162,v251,v162\n"
MF"v[20:23],a[108:111],v[88:91],v[20:23]\n"
RD"a[72:75],v227 offset:18432\n"
MU"v163,v251,v163\n"
MU"v164,v251,v164\n"
MF"v[60:63],a[108:111],v[120:123],0\n"
MU"v165,v251,v165\n"
MU"v166,v251,v166\n"
MF"v[60:63],a[104:107],v[112:115],v[60:63]\n"
RD"a[76:79],v227 offset:19456\n"
MU"v167,v251,v167\n"
PK"v160,v160,v161\n"
MF"v[24:27],a[112:115],v[80:83],0\n"
SA"s51,s60,16384\n"
"v_accvgpr_read_b32 v252,a144\n"
"v_subrev_u32 v253,0x2b800000,v252\n"
PK"v161,v162,v163\n"
MF"v[24:27],a[116:119],v[88:91],v[24:27]\n"
"s_lshl_b32 s62,s34,14\n"
RD"a[80:83],v227 offset:20480\n"
PK"v162,v164,v165\n"
PK"v163,v166,v167\n"
MF"v[64:67],a[116:119],v[120:123],0\n"
"s_sub_u32 s62,s61,s62\n"
BS"v[160:163],v224,s[8:11],s60 offen offset:2048 sc1\n"
MU"v168,v251,v168\n"
MF"v[64:67],a[112:115],v[112:115],v[64:67]\n"
WT"lgkmcnt(3)\n"
"v_cmp_le_u32 vcc,0x34000000,v253\n"
"s_cmp_lg_u64 vcc,0\n"
"s_cbranch_scc1 L1047_%=\n"
"L1046_%=:\n"
MF"v[28:31],a[120:123],v[80:83],0\n"
SA"s62,s62,122880\n"
"v_rcp_f32 v252,v252\n"
MU"v169,v251,v169\n"
MF"v[28:31],a[124:127],v[88:91],v[28:31]\n"
RD"a[84:87],v227 offset:21504\n"
RD"a[88:91],v227 offset:22528\n"
MU"v170,v251,v170\n"
MF"v[68:71],a[124:127],v[120:123],0\n"
MU"v171,v251,v171\n"
MU"v172,v251,v172\n"
MU"v173,v251,v173\n"
MF"v[68:71],a[120:123],v[112:115],v[68:71]\n"
RD"a[92:95],v227 offset:23552\n"
MU"v174,v251,v174\n"
MU"v175,v251,v175\n"
MF"v[32:35],a[64:67],v[80:83],0\n"
PK"v168,v168,v169\n"
PK"v169,v170,v171\n"
MU"v176,v252,v176\n"
MF"v[32:35],a[68:71],v[88:91],v[32:35]\n"
RD"a[96:99],v227 offset:24576\n"
MU"v177,v252,v177\n"
PK"v170,v172,v173\n"
MF"v[72:75],a[68:71],v[120:123],0\n"
PK"v171,v174,v175\n"
MU"v178,v252,v178\n"
MU"v179,v252,v179\n"
MF"v[72:75],a[64:67],v[112:115],v[72:75]\n"
WT"lgkmcnt(4)\n"
BS"v[168:171],v224,s[8:11],s60 offen offset:3072 sc1\n"
MU"v180,v252,v180\n"
MF"v[36:39],a[72:75],v[80:83],0\n"
RD"a[100:103],v227 offset:25600\n"
MU"v181,v252,v181\n"
MU"v182,v252,v182\n"
MF"v[36:39],a[76:79],v[88:91],v[36:39]\n"
RD"a[104:107],v227 offset:26624\n"
MU"v183,v252,v183\n"
PK"v176,v176,v177\n"
MF"v[76:79],a[76:79],v[120:123],0\n"
PK"v177,v178,v179\n"
PK"v178,v180,v181\n"
PK"v179,v182,v183\n"
MF"v[76:79],a[72:75],v[112:115],v[76:79]\n"
BS"v[176:179],v224,s[8:11],s51 offen offset:0 sc1\n"
MU"v184,v252,v184\n"
MF"v[0:3],a[80:83],v[96:99],v[0:3]\n"
WT"lgkmcnt(2)\n"
RD"a[108:111],v227 offset:27648\n"
MU"v185,v252,v185\n"
MU"v186,v252,v186\n"
MF"v[0:3],a[84:87],v[104:107],v[0:3]\n"
RD"a[112:115],v227 offset:28672\n"
MU"v187,v252,v187\n"
MU"v188,v252,v188\n"
MF"v[40:43],a[84:87],v[136:139],v[40:43]\n"
MU"v189,v252,v189\n"
MU"v190,v252,v190\n"
MU"v191,v252,v191\n"
MF"v[40:43],a[80:83],v[128:131],v[40:43]\n"
RD"a[116:119],v227 offset:29696\n"
PK"v184,v184,v185\n"
PK"v185,v186,v187\n"
MF"v[4:7],a[88:91],v[96:99],v[4:7]\n"
PK"v186,v188,v189\n"
PK"v187,v190,v191\n"
MU"v192,v252,v192\n"
MF"v[4:7],a[92:95],v[104:107],v[4:7]\n"
BS"v[184:187],v224,s[8:11],s51 offen offset:1024 sc1\n"
MU"v193,v252,v193\n"
MF"v[44:47],a[92:95],v[136:139],v[44:47]\n"
RD"a[120:123],v227 offset:30720\n"
MU"v194,v252,v194\n"
MU"v195,v252,v195\n"
MF"v[44:47],a[88:91],v[128:131],v[44:47]\n"
RD"a[124:127],v227 offset:31744\n"
MU"v196,v252,v196\n"
MU"v197,v252,v197\n"
MF"v[8:11],a[96:99],v[96:99],v[8:11]\n"
WT"lgkmcnt(3)\n"
WT"vmcnt(14)\n"
"s_barrier\n"
MF"v[8:11],a[100:103],v[104:107],v[8:11]\n"
RD"a[64:67],v226 offset:0\n"
MU"v198,v252,v198\n"
MU"v199,v252,v199\n"
MF"v[48:51],a[100:103],v[136:139],v[48:51]\n"
PK"v192,v192,v193\n"
PK"v193,v194,v195\n"
PK"v194,v196,v197\n"
MF"v[48:51],a[96:99],v[128:131],v[48:51]\n"
RD"a[68:71],v226 offset:1024\n"
PK"v195,v198,v199\n"
MU"v200,v252,v200\n"
MF"v[12:15],a[104:107],v[96:99],v[12:15]\n"
BS"v[192:195],v224,s[8:11],s51 offen offset:2048 sc1\n"
MU"v201,v252,v201\n"
MF"v[12:15],a[108:111],v[104:107],v[12:15]\n"
RD"a[72:75],v226 offset:2048\n"
MU"v202,v252,v202\n"
MU"v203,v252,v203\n"
MF"v[52:55],a[108:111],v[136:139],v[52:55]\n"
MU"v204,v252,v204\n"
MU"v205,v252,v205\n"
MU"v206,v252,v206\n"
MF"v[52:55],a[104:107],v[128:131],v[52:55]\n"
RD"a[76:79],v226 offset:3072\n"
MU"v207,v252,v207\n"
PK"v200,v200,v201\n"
MF"v[16:19],a[112:115],v[96:99],v[16:19]\n"
WT"lgkmcnt(3)\n"
PK"v201,v202,v203\n"
PK"v202,v204,v205\n"
PK"v203,v206,v207\n"
MF"v[16:19],a[116:119],v[104:107],v[16:19]\n"
BS"v[200:203],v224,s[8:11],s51 offen offset:3072 sc1\n"
MF"v[56:59],a[116:119],v[136:139],v[56:59]\n"
RD"a[80:83],v226 offset:4096\n"
"v_readlane_b32 s43,v228,s42\n"
MF"v[56:59],a[112:115],v[128:131],v[56:59]\n"
RD"a[84:87],v226 offset:5120\n"
MF"v[20:23],a[120:123],v[96:99],v[20:23]\n"
MF"v[20:23],a[124:127],v[104:107],v[20:23]\n"
RD"a[88:91],v226 offset:6144\n"
MF"v[60:63],a[124:127],v[136:139],v[60:63]\n"
"s_mov_b32 m0,s40\n"
MF"v[60:63],a[120:123],v[128:131],v[60:63]\n"
RD"a[92:95],v226 offset:7168\n"
MF"v[24:27],a[64:67],v[96:99],v[24:27]\n"
WT"lgkmcnt(4)\n"
"v_readlane_b32 s44,v229,s42\n"
MF"v[24:27],a[68:71],v[104:107],v[24:27]\n"
RD"a[96:99],v227 offset:0\n"
MF"v[64:67],a[68:71],v[136:139],v[64:67]\n"
SA"s42,s42,1\n"
BL"v225,s[8:11],s43 offen offset:0 sc1 lds\n"
MF"v[64:67],a[64:67],v[128:131],v[64:67]\n"
RD"a[100:103],v227 offset:1024\n"
MF"v[28:31],a[72:75],v[96:99],v[28:31]\n"
MF"v[28:31],a[76:79],v[104:107],v[28:31]\n"
RD"a[104:107],v227 offset:2048\n"
MF"v[68:71],a[76:79],v[136:139],v[68:71]\n"
MF"v[68:71],a[72:75],v[128:131],v[68:71]\n"
WT"lgkmcnt(3)\n"
RD"a[108:111],v227 offset:3072\n"
MF"v[32:35],a[80:83],v[96:99],v[32:35]\n"
MF"v[32:35],a[84:87],v[104:107],v[32:35]\n"
RD"a[112:115],v227 offset:4096\n"
MF"v[72:75],a[84:87],v[136:139],v[72:75]\n"
BL"v225,s[8:11],s43 offen offset:1024 sc1 lds\n"
MF"v[72:75],a[80:83],v[128:131],v[72:75]\n"
RD"a[116:119],v227 offset:5120\n"
MF"v[36:39],a[88:91],v[96:99],v[36:39]\n"
MF"v[36:39],a[92:95],v[104:107],v[36:39]\n"
RD"a[120:123],v227 offset:6144\n"
MF"v[76:79],a[92:95],v[136:139],v[76:79]\n"
MF"v[76:79],a[88:91],v[128:131],v[76:79]\n"
WT"lgkmcnt(3)\n"
RD"a[124:127],v227 offset:7168\n"
MF"v[84:87],a[96:99],v[144:147],0\n"
BL"v225,s[8:11],s43 offen offset:2048 sc1 lds\n"
MF"v[84:87],a[100:103],v[152:155],v[84:87]\n"
RD"a[64:67],v227 offset:8192\n"
MF"v[164:167],a[100:103],v[184:187],0\n"
MF"v[164:167],a[96:99],v[176:179],v[164:167]\n"
RD"a[68:71],v227 offset:9216\n"
MF"v[92:95],a[104:107],v[144:147],0\n"
MF"v[92:95],a[108:111],v[152:155],v[92:95]\n"
RD"a[72:75],v227 offset:10240\n"
MF"v[172:175],a[108:111],v[184:187],0\n"
MF"v[172:175],a[104:107],v[176:179],v[172:175]\n"
WT"lgkmcnt(3)\n"
RD"a[76:79],v227 offset:11264\n"
MF"v[100:103],a[112:115],v[144:147],0\n"
BL"v225,s[8:11],s43 offen offset:3072 sc1 lds\n"
MF"v[100:103],a[116:119],v[152:155],v[100:103]\n"
RD"a[80:83],v227 offset:12288\n"
MF"v[180:183],a[116:119],v[184:187],0\n"
SA"m0,s40,16384\n"
MF"v[180:183],a[112:115],v[176:179],v[180:183]\n"
RD"a[84:87],v227 offset:13312\n"
MF"v[108:111],a[120:123],v[144:147],0\n"
MF"v[108:111],a[124:127],v[152:155],v[108:111]\n"
RD"a[88:91],v227 offset:14336\n"
MF"v[188:191],a[124:127],v[184:187],0\n"
BL"v225,s[8:11],s44 offen offset:0 sc1 lds\n"
MF"v[188:191],a[120:123],v[176:179],v[188:191]\n"
WT"lgkmcnt(3)\n"
RD"a[92:95],v227 offset:15360\n"
SA"s40,s40,32768\n"
MF"v[116:119],a[64:67],v[144:147],0\n"
"s_cmp_eq_u32 s40,s46\n"
"s_cselect_b32 s40,s47,s40\n"
MF"v[116:119],a[68:71],v[152:155],v[116:119]\n"
RD"a[96:99],v227 offset:16384\n"
MF"v[196:199],a[68:71],v[184:187],0\n"
MF"v[196:199],a[64:67],v[176:179],v[196:199]\n"
RD"a[100:103],v227 offset:17408\n"
MF"v[124:127],a[72:75],v[144:147],0\n"
MF"v[124:127],a[76:79],v[152:155],v[124:127]\n"
RD"a[104:107],v227 offset:18432\n"
MF"v[204:207],a[76:79],v[184:187],0\n"
BL"v225,s[8:11],s44 offen offset:1024 sc1 lds\n"
MF"v[204:207],a[72:75],v[176:179],v[204:207]\n"
WT"lgkmcnt(3)\n"
RD"a[108:111],v227 offset:19456\n"
MF"v[132:135],a[80:83],v[144:147],0\n"
MF"v[132:135],a[84:87],v[152:155],v[132:135]\n"
RD"a[112:115],v227 offset:20480\n"
MF"v[208:211],a[84:87],v[184:187],0\n"
MF"v[208:211],a[80:83],v[176:179],v[208:211]\n"
RD"a[116:119],v227 offset:21504\n"
MF"v[140:143],a[88:91],v[144:147],0\n"
BL"v225,s[8:11],s44 offen offset:2048 sc1 lds\n"
MF"v[140:143],a[92:95],v[152:155],v[140:143]\n"
RD"a[120:123],v227 offset:22528\n"
MF"v[212:215],a[92:95],v[184:187],0\n"
WT"vmcnt(7)\n"
"buffer_store_dwordx2 v[232:233],v230,s[8:11],s62 offen sc1\n"
MF"v[212:215],a[88:91],v[176:179],v[212:215]\n"
WT"lgkmcnt(3)\n"
RD"a[124:127],v227 offset:23552\n"
MF"v[148:151],a[96:99],v[144:147],0\n"
MF"v[148:151],a[100:103],v[152:155],v[148:151]\n"
RD"a[64:67],v227 offset:24576\n"
MF"v[216:219],a[100:103],v[184:187],0\n"
"v_readlane_b32 s43,v228,s42\n"
MF"v[216:219],a[96:99],v[176:179],v[216:219]\n"
RD"a[68:71],v227 offset:25600\n"
MF"v[156:159],a[104:107],v[144:147],0\n"
BL"v225,s[8:11],s44 offen offset:3072 sc1 lds\n"
MF"v[156:159],a[108:111],v[152:155],v[156:159]\n"
RD"a[72:75],v227 offset:26624\n"
MF"v[220:223],a[108:111],v[184:187],0\n"
"s_mov_b32 m0,s40\n"
MF"v[220:223],a[104:107],v[176:179],v[220:223]\n"
WT"lgkmcnt(3)\n"
RD"a[76:79],v227 offset:27648\n"
MF"v[84:87],a[112:115],v[160:163],v[84:87]\n"
"s_lshr_b32 s89,s31,3\n"
"v_readlane_b32 s44,v229,s42\n"
MF"v[84:87],a[116:119],v[168:171],v[84:87]\n"
"s_lshl_b32 s89,s89,4\n"
RD"a[80:83],v227 offset:28672\n"
MF"v[164:167],a[116:119],v[200:203],v[164:167]\n"
SA"s42,s42,1\n"
BL"v225,s[8:11],s43 offen offset:0 sc1 lds\n"
MF"v[164:167],a[112:115],v[192:195],v[164:167]\n"
SA"s89,s89,s30\n"
RD"a[84:87],v227 offset:29696\n"
"s_lshl_b32 s89,s89,3\n"
MF"v[92:95],a[120:123],v[160:163],v[92:95]\n"
"s_xor_b32 s23,s32,s30\n"
MF"v[92:95],a[124:127],v[168:171],v[92:95]\n"
SA"s89,s89,s23\n"
RD"a[88:91],v227 offset:30720\n"
MF"v[172:175],a[124:127],v[200:203],v[172:175]\n"
"s_xor_b32 s90,s89,36\n"
MF"v[172:175],a[120:123],v[192:195],v[172:175]\n"
WT"lgkmcnt(3)\n"
RD"a[92:95],v227 offset:31744\n"
"s_mul_i32 s88,s89,5120\n"
MF"v[100:103],a[64:67],v[160:163],v[100:103]\n"
"s_mul_i32 s22,s90,5120\n"
MF"v[100:103],a[68:71],v[168:171],v[100:103]\n"
"s_sub_u32 s90,s90,s89\n"
RD"a[96:99],v226 offset:0\n"
MF"v[180:183],a[68:71],v[200:203],v[180:183]\n"
"s_lshl_b32 s90,s90,6\n"
BL"v225,s[8:11],s43 offen offset:1024 sc1 lds\n"
MF"v[180:183],a[64:67],v[192:195],v[180:183]\n"
SA"s88,s88,59506688\n"
RD"a[100:103],v226 offset:1024\n"
MF"v[108:111],a[72:75],v[160:163],v[108:111]\n"
SA"s22,s22,59506688\n"
MF"v[108:111],a[76:79],v[168:171],v[108:111]\n"
"s_cmp_eq_u32 s23,0\n"
RD"a[104:107],v226 offset:2048\n"
"s_cselect_b32 s88,2130706432,s88\n"
MF"v[188:191],a[76:79],v[200:203],v[188:191]\n"
"s_cmp_eq_u32 s23,4\n"
"s_cselect_b32 s22,2130706432,s22\n"
MF"v[188:191],a[72:75],v[192:195],v[188:191]\n"
WT"lgkmcnt(3)\n"
RD"a[108:111],v226 offset:3072\n"
"s_lshl_b32 s89,s89,6\n"
MF"v[116:119],a[80:83],v[160:163],v[116:119]\n"
SA"s89,s89,80478208\n"
BL"v225,s[8:11],s43 offen offset:2048 sc1 lds\n"
MF"v[116:119],a[84:87],v[168:171],v[116:119]\n"
RD"a[112:115],v226 offset:4096\n"
"v_bfe_u32 v239,v224,1,6\n"
MF"v[196:199],a[84:87],v[200:203],v[196:199]\n"
"v_bfe_u32 v242,v224,7,1\n"
MF"v[196:199],a[80:83],v[192:195],v[196:199]\n"
RD"a[116:119],v226 offset:5120\n"
"v_mad_u32_u24 v239,v242,s90,v239\n"
MF"v[124:127],a[88:91],v[160:163],v[124:127]\n"
"v_lshrrev_b32 v242,8,v224\n"
MF"v[124:127],a[92:95],v[168:171],v[124:127]\n"
RD"a[120:123],v226 offset:6144\n"
"v_lshl_add_u32 v239,v242,27,v239\n"
MF"v[204:207],a[92:95],v[200:203],v[204:207]\n"
MF"v[204:207],a[88:91],v[192:195],v[204:207]\n"
WT"lgkmcnt(3)\n"
RD"a[124:127],v226 offset:7168\n"
MF"v[132:135],a[96:99],v[160:163],v[132:135]\n"
BL"v225,s[8:11],s43 offen offset:3072 sc1 lds\n"
MF"v[132:135],a[100:103],v[168:171],v[132:135]\n"
RD"a[64:67],v226 offset:8192\n"
MF"v[208:211],a[100:103],v[200:203],v[208:211]\n"
SA"m0,s40,16384\n"
MF"v[208:211],a[96:99],v[192:195],v[208:211]\n"
RD"a[68:71],v226 offset:9216\n"
MF"v[140:143],a[104:107],v[160:163],v[140:143]\n"
MF"v[140:143],a[108:111],v[168:171],v[140:143]\n"
RD"a[72:75],v226 offset:10240\n"
MF"v[212:215],a[108:111],v[200:203],v[212:215]\n"
BL"v225,s[8:11],s44 offen offset:0 sc1 lds\n"
MF"v[212:215],a[104:107],v[192:195],v[212:215]\n"
WT"lgkmcnt(3)\n"
RD"a[76:79],v226 offset:11264\n"
SA"s40,s40,32768\n"
MF"v[148:151],a[112:115],v[160:163],v[148:151]\n"
"s_cmp_eq_u32 s40,s46\n"
"s_cselect_b32 s40,s47,s40\n"
MF"v[148:151],a[116:119],v[168:171],v[148:151]\n"
RD"a[80:83],v226 offset:12288\n"
MF"v[216:219],a[116:119],v[200:203],v[216:219]\n"
MF"v[216:219],a[112:115],v[192:195],v[216:219]\n"
RD"a[84:87],v226 offset:13312\n"
MF"v[156:159],a[120:123],v[160:163],v[156:159]\n"
MF"v[156:159],a[124:127],v[168:171],v[156:159]\n"
RD"a[88:91],v226 offset:14336\n"
MF"v[220:223],a[124:127],v[200:203],v[220:223]\n"
BL"v225,s[8:11],s44 offen offset:1024 sc1 lds\n"
MF"v[220:223],a[120:123],v[192:195],v[220:223]\n"
WT"lgkmcnt(3)\n"
RD"a[92:95],v226 offset:15360\n"
MF"v[0:3],a[64:67],a[0:3],v[0:3]\n"
MF"v[0:3],a[68:71],a[8:11],v[0:3]\n"
RD"a[96:99],v226 offset:16384\n"
MF"v[40:43],a[68:71],a[12:15],v[40:43]\n"
MF"v[40:43],a[64:67],a[4:7],v[40:43]\n"
RD"a[100:103],v226 offset:17408\n"
MF"v[4:7],a[72:75],a[0:3],v[4:7]\n"
BL"v225,s[8:11],s44 offen offset:2048 sc1 lds\n"
MF"v[4:7],a[76:79],a[8:11],v[4:7]\n"
RD"a[104:107],v226 offset:18432\n"
MF"v[44:47],a[76:79],a[12:15],v[44:47]\n"
MF"v[44:47],a[72:75],a[4:7],v[44:47]\n"
WT"lgkmcnt(3)\n"
RD"a[108:111],v226 offset:19456\n"
MF"v[8:11],a[80:83],a[0:3],v[8:11]\n"
MF"v[8:11],a[84:87],a[8:11],v[8:11]\n"
RD"a[112:115],v226 offset:20480\n"
MF"v[48:51],a[84:87],a[12:15],v[48:51]\n"
MF"v[48:51],a[80:83],a[4:7],v[48:51]\n"
RD"a[116:119],v226 offset:21504\n"
MF"v[12:15],a[88:91],a[0:3],v[12:15]\n"
BL"v225,s[8:11],s44 offen offset:3072 sc1 lds\n"
MF"v[12:15],a[92:95],a[8:11],v[12:15]\n"
RD"a[120:123],v226 offset:22528\n"
MF"v[52:55],a[92:95],a[12:15],v[52:55]\n"
MF"v[52:55],a[88:91],a[4:7],v[52:55]\n"
WT"lgkmcnt(3)\n"
RD"a[124:127],v226 offset:23552\n"
MF"v[16:19],a[96:99],a[0:3],v[16:19]\n"
MF"v[16:19],a[100:103],a[8:11],v[16:19]\n"
RD"a[64:67],v226 offset:24576\n"
MF"v[56:59],a[100:103],a[12:15],v[56:59]\n"
MF"v[56:59],a[96:99],a[4:7],v[56:59]\n"
RD"a[68:71],v226 offset:25600\n"
MF"v[20:23],a[104:107],a[0:3],v[20:23]\n"
MF"v[20:23],a[108:111],a[8:11],v[20:23]\n"
RD"a[72:75],v226 offset:26624\n"
MF"v[60:63],a[108:111],a[12:15],v[60:63]\n"
MF"v[60:63],a[104:107],a[4:7],v[60:63]\n"
WT"lgkmcnt(3)\n"
RD"a[76:79],v226 offset:27648\n"
MF"v[24:27],a[112:115],a[0:3],v[24:27]\n"
MF"v[24:27],a[116:119],a[8:11],v[24:27]\n"
RD"a[80:83],v226 offset:28672\n"
MF"v[64:67],a[116:119],a[12:15],v[64:67]\n"
MF"v[64:67],a[112:115],a[4:7],v[64:67]\n"
RD"a[84:87],v226 offset:29696\n"
MF"v[28:31],a[120:123],a[0:3],v[28:31]\n"
MF"v[28:31],a[124:127],a[8:11],v[28:31]\n"
RD"a[88:91],v226 offset:30720\n"
MF"v[68:71],a[124:127],a[12:15],v[68:71]\n"
MF"v[68:71],a[120:123],a[4:7],v[68:71]\n"
WT"lgkmcnt(3)\n"
RD"a[92:95],v226 offset:31744\n"
MF"v[32:35],a[64:67],a[0:3],v[32:35]\n"
"s_barrier\n"
MF"v[32:35],a[68:71],a[8:11],v[32:35]\n"
RD"a[96:99],v235 offset:0\n"
MF"v[72:75],a[68:71],a[12:15],v[72:75]\n"
MF"v[72:75],a[64:67],a[4:7],v[72:75]\n"
RD"a[100:103],v235 offset:1024\n"
MF"v[36:39],a[72:75],a[0:3],v[36:39]\n"
MF"v[36:39],a[76:79],a[8:11],v[36:39]\n"
RD"a[104:107],v235 offset:2048\n"
MF"v[76:79],a[76:79],a[12:15],v[76:79]\n"
MF"v[76:79],a[72:75],a[4:7],v[76:79]\n"
WT"lgkmcnt(3)\n"
RD"a[108:111],v235 offset:3072\n"
MF"v[0:3],a[80:83],a[16:19],v[0:3]\n"
MF"v[0:3],a[84:87],a[24:27],v[0:3]\n"
RD"a[112:115],v235 offset:4096\n"
MF"v[40:43],a[84:87],a[28:31],v[40:43]\n"
MF"v[40:43],a[80:83],a[20:23],v[40:43]\n"
RD"a[116:119],v235 offset:5120\n"
MF"v[4:7],a[88:91],a[16:19],v[4:7]\n"
MF"v[4:7],a[92:95],a[24:27],v[4:7]\n"
RD"a[120:123],v235 offset:6144\n"
MF"v[44:47],a[92:95],a[28:31],v[44:47]\n"
MF"v[44:47],a[88:91],a[20:23],v[44:47]\n"
WT"lgkmcnt(3)\n"
RD"a[124:127],v235 offset:7168\n"
MF"v[8:11],a[96:99],a[16:19],v[8:11]\n"
MF"v[8:11],a[100:103],a[24:27],v[8:11]\n"
RD"a[64:67],v235 offset:8192\n"
MF"v[48:51],a[100:103],a[28:31],v[48:51]\n"
MF"v[48:51],a[96:99],a[20:23],v[48:51]\n"
RD"a[68:71],v235 offset:9216\n"
MF"v[12:15],a[104:107],a[16:19],v[12:15]\n"
MF"v[12:15],a[108:111],a[24:27],v[12:15]\n"
RD"a[72:75],v235 offset:10240\n"
"v_bfe_u32 v238,v224,4,2\n"
MF"v[52:55],a[108:111],a[28:31],v[52:55]\n"
"s_andn2_b32 s63,s62,255\n"
"v_lshlrev_b32 v238,6,v238\n"
MF"v[52:55],a[104:107],a[20:23],v[52:55]\n"
WT"lgkmcnt(3)\n"
RD"a[76:79],v235 offset:11264\n"
MF"v[16:19],a[112:115],a[16:19],v[16:19]\n"
"buffer_load_dwordx2 v[240:241],v238,s[8:11],s63 offen sc1\n"
MF"v[16:19],a[116:119],a[24:27],v[16:19]\n"
RD"a[80:83],v235 offset:12288\n"
MF"v[56:59],a[116:119],a[28:31],v[56:59]\n"
MF"v[56:59],a[112:115],a[20:23],v[56:59]\n"
RD"a[84:87],v235 offset:13312\n"
MF"v[20:23],a[120:123],a[16:19],v[20:23]\n"
MF"v[20:23],a[124:127],a[24:27],v[20:23]\n"
RD"a[88:91],v235 offset:14336\n"
MF"v[60:63],a[124:127],a[28:31],v[60:63]\n"
MF"v[60:63],a[120:123],a[20:23],v[60:63]\n"
RD"a[92:95],v235 offset:15360\n"
"LTJ_%=:\n"
WT"lgkmcnt(7)\n"
MF"v[24:27],a[64:67],a[16:19],v[24:27]\n"
WT"lgkmcnt(3)\n"
MF"v[24:27],a[68:71],a[24:27],v[24:27]\n"
RD"a[96:99],v235 offset:16384\n"
MF"v[64:67],a[68:71],a[28:31],v[64:67]\n"
MF"v[64:67],a[64:67],a[20:23],v[64:67]\n"
RD"a[100:103],v235 offset:17408\n"
MF"v[28:31],a[72:75],a[16:19],v[28:31]\n"
MF"v[28:31],a[76:79],a[24:27],v[28:31]\n"
RD"a[104:107],v235 offset:18432\n"
MF"v[68:71],a[76:79],a[28:31],v[68:71]\n"
MF"v[68:71],a[72:75],a[20:23],v[68:71]\n"
RD"a[108:111],v235 offset:19456\n"
MF"v[32:35],a[80:83],a[16:19],v[32:35]\n"
WT"lgkmcnt(3)\n"
WT"vmcnt(1)\n"
"s_barrier\n"
MF"v[32:35],a[84:87],a[24:27],v[32:35]\n"
RD"a[112:115],v236 offset:4096\n"
MF"v[72:75],a[84:87],a[28:31],v[72:75]\n"
MF"v[72:75],a[80:83],a[20:23],v[72:75]\n"
RD"a[116:119],v236 offset:5120\n"
MF"v[36:39],a[88:91],a[16:19],v[36:39]\n"
MF"v[36:39],a[92:95],a[24:27],v[36:39]\n"
RD"a[120:123],v236 offset:6144\n"
MF"v[76:79],a[92:95],a[28:31],v[76:79]\n"
MF"v[76:79],a[88:91],a[20:23],v[76:79]\n"
RD"a[124:127],v236 offset:7168\n"
MF"v[0:3],a[96:99],a[180:183],v[0:3]\n"
WT"lgkmcnt(3)\n"
MF"v[0:3],a[100:103],a[188:191],v[0:3]\n"
RD"a[64:67],v236 offset:24576\n"
MF"v[40:43],a[100:103],a[192:195],v[40:43]\n"
MF"v[40:43],a[96:99],a[184:187],v[40:43]\n"
RD"a[68:71],v236 offset:25600\n"
MF"v[4:7],a[104:107],a[180:183],v[4:7]\n"
MF"v[4:7],a[108:111],a[188:191],v[4:7]\n"
RD"a[72:75],v236 offset:26624\n"
MF"v[44:47],a[108:111],a[192:195],v[44:47]\n"
MF"v[44:47],a[104:107],a[184:187],v[44:47]\n"
RD"a[76:79],v236 offset:27648\n"
MF"v[0:3],a[112:115],a[196:199],v[0:3]\n"
WT"lgkmcnt(3)\n"
MF"v[0:3],a[116:119],a[204:207],v[0:3]\n"
RD"a[80:83],v237 offset:12288\n"
MF"v[40:43],a[116:119],a[208:211],v[40:43]\n"
MF"v[40:43],a[112:115],a[200:203],v[40:43]\n"
RD"a[84:87],v237 offset:13312\n"
MF"v[4:7],a[120:123],a[196:199],v[4:7]\n"
MF"v[4:7],a[124:127],a[204:207],v[4:7]\n"
SA"s48,s60,32768\n"
RD"a[88:91],v237 offset:14336\n"
MF"v[44:47],a[124:127],a[208:211],v[44:47]\n"
"s_and_b32 s48,s48,0x18000\n"
MF"v[44:47],a[120:123],a[200:203],v[44:47]\n"
"s_andn2_b32 s49,s60,0x18000\n"
RD"a[92:95],v237 offset:15360\n"
MF"v[0:3],a[64:67],a[212:215],v[0:3]\n"
WT"lgkmcnt(3)\n"
WT"vmcnt(0)\n"
"v_cmp_eq_u64 vcc,s[28:29],v[240:241]\n"
"s_cmp_eq_u64 vcc,exec\n"
"s_cbranch_scc0 L1083_%=\n"
"L1084_%=:\n"
MF"v[0:3],a[68:71],a[220:223],v[0:3]\n"
"s_or_b32 s52,s48,s49\n"
RD"a[96:99],v235 offset:20480\n"
SA"s53,s52,16384\n"
MF"v[40:43],a[68:71],a[224:227],v[40:43]\n"
BL"a[244:247],v224,s[8:11],s52 offen offset:0 sc1\n"
MF"v[40:43],a[64:67],a[216:219],v[40:43]\n"
RD"a[100:103],v235 offset:21504\n"
MF"v[4:7],a[72:75],a[212:215],v[4:7]\n"
MF"v[4:7],a[76:79],a[220:223],v[4:7]\n"
RD"a[104:107],v235 offset:22528\n"
MF"v[44:47],a[76:79],a[224:227],v[44:47]\n"
"v_bfe_u32 v243,v224,6,2\n"
MF"v[44:47],a[72:75],a[216:219],v[44:47]\n"
RD"a[108:111],v235 offset:23552\n"
"v_bfe_u32 v244,v224,4,2\n"
MF"v[0:3],a[80:83],a[228:231],v[0:3]\n"
WT"lgkmcnt(3)\n"
BL"a[248:251],v224,s[8:11],s53 offen offset:0 sc1\n"
MF"v[0:3],a[84:87],a[236:239],v[0:3]\n"
RD"a[112:115],v236 offset:8192\n"
"v_lshrrev_b32 v245,8,v224\n"
MF"v[40:43],a[84:87],a[240:243],v[40:43]\n"
"v_lshlrev_b32 v245,3,v245\n"
"v_sub_u32 v244,v244,v245\n"
MF"v[40:43],a[80:83],a[232:235],v[40:43]\n"
RD"a[116:119],v236 offset:9216\n"
"v_and_b32 v245,16,v224\n"
MF"v[4:7],a[88:91],a[228:231],v[4:7]\n"
"v_mov_b32 v246,0x3f80\n"
MF"v[4:7],a[92:95],a[236:239],v[4:7]\n"
RD"a[120:123],v236 offset:10240\n"
"v_lshlrev_b32 v245,v245,v246\n"
MF"v[44:47],a[92:95],a[240:243],v[44:47]\n"
BL"a[252:255],v224,s[8:11],s52 offen offset:1024 sc1\n"
MF"v[44:47],a[88:91],a[232:235],v[44:47]\n"
RD"a[124:127],v236 offset:11264\n"
"v_lshlrev_b32 v246,4,v243\n"
MF"v[8:11],a[96:99],a[180:183],v[8:11]\n"
WT"lgkmcnt(3)\n"
"v_and_b32 v246,16,v246\n"
"v_and_b32 v247,2,v243\n"
MF"v[8:11],a[100:103],a[188:191],v[8:11]\n"
SA"s90,s88,0\n"
RD"a[64:67],v236 offset:28672\n"
PK"v0,v0,v1\n"
MF"v[48:51],a[100:103],a[192:195],v[48:51]\n"
BL"a[128:131],v224,s[8:11],s53 offen offset:1024 sc1\n"
MF"v[48:51],a[96:99],a[184:187],v[48:51]\n"
RD"a[68:71],v236 offset:29696\n"
PK"v1,v2,v3\n"
MF"v[12:15],a[104:107],a[180:183],v[12:15]\n"
"v_lshl_add_u32 v246,v247,1,v246\n"
PK"v2,v4,v5\n"
MF"v[12:15],a[108:111],a[188:191],v[12:15]\n"
RD"a[72:75],v236 offset:30720\n"
PK"v3,v6,v7\n"
"v_add3_u32 v246,v246,v244,0\n"
MF"v[52:55],a[108:111],a[192:195],v[52:55]\n"
BS"v[0:3],v224,s[8:11],s90 offen sc1\n"
"v_ashrrev_i32 v246,1,v246\n"
MF"v[52:55],a[104:107],a[184:187],v[52:55]\n"
RD"a[76:79],v236 offset:31744\n"
"v_cmp_eq_u32_e64 vcc,0,v246\n"
MF"v[8:11],a[112:115],a[196:199],v[8:11]\n"
WT"lgkmcnt(3)\n"
BL"a[132:135],v224,s[8:11],s52 offen offset:2048 sc1\n"
"v_cndmask_b32_e64 v80,0,v245,vcc\n"
MF"v[8:11],a[116:119],a[204:207],v[8:11]\n"
SA"s91,s22,0\n"
RD"a[80:83],v237 offset:16384\n"
"v_cmp_eq_u32_e64 s[92:93],1,v246\n"
MF"v[48:51],a[116:119],a[208:211],v[48:51]\n"
"v_cmp_eq_u32_e64 s[94:95],2,v246\n"
"v_cndmask_b32_e64 v81,0,v245,s[92:93]\n"
MF"v[48:51],a[112:115],a[200:203],v[48:51]\n"
RD"a[84:87],v237 offset:17408\n"
PK"v40,v40,v41\n"
MF"v[12:15],a[120:123],a[196:199],v[12:15]\n"
PK"v41,v42,v43\n"
"v_cndmask_b32_e64 v82,0,v245,s[94:95]\n"
MF"v[12:15],a[124:127],a[204:207],v[12:15]\n"
RD"a[88:91],v237 offset:18432\n"
PK"v42,v44,v45\n"
PK"v43,v46,v47\n"
MF"v[52:55],a[124:127],a[208:211],v[52:55]\n"
BS"v[40:43],v224,s[8:11],s91 offen sc1\n"
"v_cmp_eq_u32_e64 vcc,3,v246\n"
MF"v[52:55],a[120:123],a[200:203],v[52:55]\n"
BL"a[136:139],v224,s[8:11],s53 offen offset:2048 sc1\n"
"v_cndmask_b32_e64 v83,0,v245,vcc\n"
MF"v[8:11],a[64:67],a[212:215],v[8:11]\n"
WT"lgkmcnt(2)\n"
RD"a[92:95],v237 offset:19456\n"
"v_lshlrev_b32 v246,4,v243\n"
"v_and_b32 v246,16,v246\n"
MF"v[8:11],a[68:71],a[220:223],v[8:11]\n"
RD"a[96:99],v235 offset:24576\n"
"v_and_b32 v247,2,v243\n"
MF"v[48:51],a[68:71],a[224:227],v[48:51]\n"
"v_lshl_add_u32 v246,v247,1,v246\n"
"v_add3_u32 v246,v246,v244,8\n"
MF"v[48:51],a[64:67],a[216:219],v[48:51]\n"
RD"a[100:103],v235 offset:25600\n"
"v_ashrrev_i32 v246,1,v246\n"
MF"v[12:15],a[72:75],a[212:215],v[12:15]\n"
BL"a[140:143],v224,s[8:11],s52 offen offset:3072 sc1\n"
MF"v[12:15],a[76:79],a[220:223],v[12:15]\n"
RD"a[104:107],v235 offset:26624\n"
"v_cmp_eq_u32_e64 vcc,0,v246\n"
MF"v[52:55],a[76:79],a[224:227],v[52:55]\n"
"v_cmp_eq_u32_e64 s[92:93],1,v246\n"
"v_cndmask_b32_e64 v88,0,v245,vcc\n"
MF"v[52:55],a[72:75],a[216:219],v[52:55]\n"
RD"a[108:111],v235 offset:27648\n"
"v_cndmask_b32_e64 v89,0,v245,s[92:93]\n"
MF"v[8:11],a[80:83],a[228:231],v[8:11]\n"
WT"lgkmcnt(3)\n"
BL"a[144:147],v224,s[8:11],s53 offen offset:3072 sc1\n"
MF"v[8:11],a[84:87],a[236:239],v[8:11]\n"
SA"s48,s60,65536\n"
RD"a[112:115],v236 offset:12288\n"
"v_cmp_eq_u32_e64 s[94:95],2,v246\n"
MF"v[48:51],a[84:87],a[240:243],v[48:51]\n"
"s_and_b32 s48,s48,0x18000\n"
"v_cndmask_b32_e64 v90,0,v245,s[94:95]\n"
"v_cmp_eq_u32_e64 vcc,3,v246\n"
MF"v[48:51],a[80:83],a[232:235],v[48:51]\n"
"s_andn2_b32 s49,s60,0x18000\n"
RD"a[116:119],v236 offset:13312\n"
"v_cndmask_b32_e64 v91,0,v245,vcc\n"
MF"v[12:15],a[88:91],a[228:231],v[12:15]\n"
"s_or_b32 s52,s48,s49\n"
"v_lshl_add_u32 v246,v243,3,v244\n"
"v_add_u32 v246,0,v246\n"
MF"v[12:15],a[92:95],a[236:239],v[12:15]\n"
SA"s53,s52,16384\n"
RD"a[120:123],v236 offset:14336\n"
"v_ashrrev_i32 v246,1,v246\n"
MF"v[52:55],a[92:95],a[240:243],v[52:55]\n"
BL"a[148:151],v224,s[8:11],s52 offen offset:0 sc1\n"
MF"v[52:55],a[88:91],a[232:235],v[52:55]\n"
RD"a[124:127],v236 offset:15360\n"
"v_cmp_eq_u32_e64 vcc,0,v246\n"
MF"v[16:19],a[96:99],a[180:183],v[16:19]\n"
WT"lgkmcnt(3)\n"
"v_cndmask_b32_e64 v96,0,v245,vcc\n"
"v_cmp_eq_u32_e64 s[92:93],1,v246\n"
MF"v[16:19],a[100:103],a[188:191],v[16:19]\n"
SA"s90,s88,1024\n"
RD"a[64:67],v237 offset:0\n"
"v_cndmask_b32_e64 v97,0,v245,s[92:93]\n"
MF"v[56:59],a[100:103],a[192:195],v[56:59]\n"
PK"v8,v8,v9\n"
PK"v9,v10,v11\n"
MF"v[56:59],a[96:99],a[184:187],v[56:59]\n"
RD"a[68:71],v237 offset:1024\n"
"v_cmp_eq_u32_e64 s[94:95],2,v246\n"
MF"v[20:23],a[104:107],a[180:183],v[20:23]\n"
BL"a[152:155],v224,s[8:11],s53 offen offset:0 sc1\n"
MF"v[20:23],a[108:111],a[188:191],v[20:23]\n"
RD"a[72:75],v237 offset:2048\n"
PK"v10,v12,v13\n"
PK"v11,v14,v15\n"
MF"v[60:63],a[108:111],a[192:195],v[60:63]\n"
BS"v[8:11],v224,s[8:11],s90 offen sc1\n"
"v_cndmask_b32_e64 v98,0,v245,s[94:95]\n"
MF"v[60:63],a[104:107],a[184:187],v[60:63]\n"
RD"a[76:79],v237 offset:3072\n"
"v_cmp_eq_u32_e64 vcc,3,v246\n"
MF"v[16:19],a[112:115],a[196:199],v[16:19]\n"
WT"lgkmcnt(3)\n"
"v_cndmask_b32_e64 v99,0,v245,vcc\n"
"v_lshl_add_u32 v246,v243,3,v244\n"
MF"v[16:19],a[116:119],a[204:207],v[16:19]\n"
SA"s91,s22,1024\n"
RD"a[80:83],v237 offset:20480\n"
"v_add_u32 v246,4,v246\n"
MF"v[56:59],a[116:119],a[208:211],v[56:59]\n"
BL"a[156:159],v224,s[8:11],s52 offen offset:1024 sc1\n"
"v_ashrrev_i32 v246,1,v246\n"
MF"v[56:59],a[112:115],a[200:203],v[56:59]\n"
RD"a[84:87],v237 offset:21504\n"
"v_cmp_eq_u32_e64 vcc,0,v246\n"
MF"v[20:23],a[120:123],a[196:199],v[20:23]\n"
PK"v48,v48,v49\n"
PK"v49,v50,v51\n"
"v_cndmask_b32_e64 v104,0,v245,vcc\n"
MF"v[20:23],a[124:127],a[204:207],v[20:23]\n"
RD"a[88:91],v237 offset:22528\n"
PK"v50,v52,v53\n"
PK"v51,v54,v55\n"
MF"v[60:63],a[124:127],a[208:211],v[60:63]\n"
BS"v[48:51],v224,s[8:11],s91 offen sc1\n"
"v_cmp_eq_u32_e64 s[92:93],1,v246\n"
MF"v[60:63],a[120:123],a[200:203],v[60:63]\n"
BL"a[160:163],v224,s[8:11],s53 offen offset:1024 sc1\n"
"v_cndmask_b32_e64 v105,0,v245,s[92:93]\n"
MF"v[16:19],a[64:67],a[212:215],v[16:19]\n"
WT"lgkmcnt(2)\n"
RD"a[92:95],v237 offset:23552\n"
"v_cmp_eq_u32_e64 s[94:95],2,v246\n"
MF"v[16:19],a[68:71],a[220:223],v[16:19]\n"
"s_mov_b32 s12,s64\n"
RD"a[96:99],v235 offset:28672\n"
"v_cndmask_b32_e64 v106,0,v245,s[94:95]\n"
"v_cmp_eq_u32_e64 vcc,3,v246\n"
MF"v[56:59],a[68:71],a[224:227],v[56:59]\n"
"s_and_b32 s13,s65,0xffff\n"
"v_cndmask_b32_e64 v107,0,v245,vcc\n"
"s_mov_b32 s14,2621440\n"
MF"v[56:59],a[64:67],a[216:219],v[56:59]\n"
"s_mov_b32 s15,0x20000\n"
RD"a[100:103],v235 offset:29696\n"
MF"v[20:23],a[72:75],a[212:215],v[20:23]\n"
"s_mov_b32 s16,s86\n"
BL"a[164:167],v224,s[8:11],s52 offen offset:2048 sc1\n"
MF"v[20:23],a[76:79],a[220:223],v[20:23]\n"
"s_and_b32 s17,s87,0xffff\n"
RD"a[104:107],v235 offset:30720\n"
MF"v[60:63],a[76:79],a[224:227],v[60:63]\n"
"s_mov_b32 s18,2621440\n"
MF"v[60:63],a[72:75],a[216:219],v[60:63]\n"
"s_mov_b32 s19,0x20000\n"
RD"a[108:111],v235 offset:31744\n"
MF"v[16:19],a[80:83],a[228:231],v[16:19]\n"
WT"lgkmcnt(3)\n"
"v_bfe_u32 v243,v224,4,4\n"
MF"v[16:19],a[84:87],a[236:239],v[16:19]\n"
"s_lshl_b32 s92,s34,3\n"
RD"a[112:115],v236 offset:16384\n"
MF"v[56:59],a[84:87],a[240:243],v[56:59]\n"
SA"s93,s92,s32\n"
BL"a[168:171],v224,s[8:11],s53 offen offset:2048 sc1\n"
MF"v[56:59],a[80:83],a[232:235],v[56:59]\n"
"s_sub_u32 s92,s32,s92\n"
RD"a[116:119],v236 offset:17408\n"
MF"v[20:23],a[88:91],a[228:231],v[20:23]\n"
SA"s92,s92,120\n"
MF"v[20:23],a[92:95],a[236:239],v[20:23]\n"
RD"a[120:123],v236 offset:18432\n"
"v_lshl_add_u32 v244,s93,4,v243\n"
MF"v[60:63],a[92:95],a[240:243],v[60:63]\n"
"v_lshl_add_u32 v245,s92,4,v243\n"
MF"v[60:63],a[88:91],a[232:235],v[60:63]\n"
"s_mul_i32 s93,s93,20480\n"
RD"a[124:127],v236 offset:19456\n"
MF"v[24:27],a[96:99],a[180:183],v[24:27]\n"
WT"lgkmcnt(3)\n"
BL"a[172:175],v224,s[8:11],s52 offen offset:3072 sc1\n"
MF"v[24:27],a[100:103],a[188:191],v[24:27]\n"
"s_mul_i32 s92,s92,20480\n"
RD"a[64:67],v237 offset:4096\n"
PK"v16,v16,v17\n"
MF"v[64:67],a[100:103],a[192:195],v[64:67]\n"
SA"s90,s88,2048\n"
PK"v17,v18,v19\n"
"v_add_u32 v178,s93,v224\n"
MF"v[64:67],a[96:99],a[184:187],v[64:67]\n"
RD"a[68:71],v237 offset:5120\n"
PK"v18,v20,v21\n"
MF"v[28:31],a[104:107],a[180:183],v[28:31]\n"
BL"a[176:179],v224,s[8:11],s53 offen offset:3072 sc1\n"
MF"v[28:31],a[108:111],a[188:191],v[28:31]\n"
SA"s48,s60,98304\n"
RD"a[72:75],v237 offset:6144\n"
PK"v19,v22,v23\n"
"v_add_u32 v179,s92,v224\n"
MF"v[68:71],a[108:111],a[192:195],v[68:71]\n"
"s_and_b32 s48,s48,0x18000\n"
BS"v[16:19],v224,s[8:11],s90 offen sc1\n"
"v_lshrrev_b32 v243,4,v224\n"
MF"v[68:71],a[104:107],a[184:187],v[68:71]\n"
"s_andn2_b32 s49,s60,0x18000\n"
RD"a[76:79],v237 offset:7168\n"
"v_and_b32 v243,48,v243\n"
MF"v[24:27],a[112:115],a[196:199],v[24:27]\n"
WT"lgkmcnt(3)\n"
"v_mul_u32_u24 v176,1280,v244\n"
"v_mul_u32_u24 v177,1280,v245\n"
MF"v[24:27],a[116:119],a[204:207],v[24:27]\n"
"s_or_b32 s52,s48,s49\n"
RD"a[80:83],v237 offset:24576\n"
"v_add_u32 v176,v176,v243\n"
MF"v[64:67],a[116:119],a[208:211],v[64:67]\n"
SA"s53,s52,16384\n"
BL"a[32:35],v224,s[8:11],s52 offen offset:0 sc1\n"
MF"v[64:67],a[112:115],a[200:203],v[64:67]\n"
SA"s91,s22,2048\n"
RD"a[84:87],v237 offset:25600\n"
"v_add_u32 v177,v177,v243\n"
MF"v[28:31],a[120:123],a[196:199],v[28:31]\n"
PK"v56,v56,v57\n"
PK"v57,v58,v59\n"
"v_and_b32 v243,0x300,v224\n"
MF"v[28:31],a[124:127],a[204:207],v[28:31]\n"
RD"a[88:91],v237 offset:26624\n"
PK"v58,v60,v61\n"
PK"v59,v62,v63\n"
MF"v[68:71],a[124:127],a[208:211],v[68:71]\n"
BS"v[56:59],v224,s[8:11],s91 offen sc1\n"
"v_add_u32 v243,2684928,v243\n"
MF"v[68:71],a[120:123],a[200:203],v[68:71]\n"
RD"a[92:95],v237 offset:27648\n"
"v_sub_u32 v244,2047,v244\n"
"v_sub_u32 v245,2047,v245\n"
MF"v[24:27],a[64:67],a[212:215],v[24:27]\n"
WT"lgkmcnt(3)\n"
BL"a[36:39],v224,s[8:11],s53 offen offset:0 sc1\n"
"v_readlane_b32 s92,v234,0\n"
MF"v[24:27],a[68:71],a[220:223],v[24:27]\n"
"s_and_b32 s92,s92,0xffff\n"
RD"a[96:99],v236 offset:0\n"
"v_cmp_eq_u32 vcc,s92,v244\n"
MF"v[64:67],a[68:71],a[224:227],v[64:67]\n"
"v_cmp_eq_u32_e64 s[94:95],s92,v245\n"
"v_cndmask_b32 v178,v178,v243,vcc\n"
MF"v[64:67],a[64:67],a[216:219],v[64:67]\n"
RD"a[100:103],v236 offset:1024\n"
"v_cndmask_b32_e64 v179,v179,v243,s[94:95]\n"
MF"v[28:31],a[72:75],a[212:215],v[28:31]\n"
"v_add_u32 v243,16,v243\n"
"v_readlane_b32 s92,v234,1\n"
MF"v[28:31],a[76:79],a[220:223],v[28:31]\n"
"s_and_b32 s92,s92,0xffff\n"
RD"a[104:107],v236 offset:2048\n"
MF"v[68:71],a[76:79],a[224:227],v[68:71]\n"
BL"a[40:43],v224,s[8:11],s52 offen offset:1024 sc1\n"
MF"v[68:71],a[72:75],a[216:219],v[68:71]\n"
RD"a[108:111],v236 offset:3072\n"
"v_cmp_eq_u32 vcc,s92,v244\n"
MF"v[24:27],a[80:83],a[228:231],v[24:27]\n"
WT"lgkmcnt(3)\n"
"v_cmp_eq_u32_e64 s[94:95],s92,v245\n"
"v_cndmask_b32 v178,v178,v243,vcc\n"
MF"v[24:27],a[84:87],a[236:239],v[24:27]\n"
RD"a[112:115],v236 offset:20480\n"
"v_cndmask_b32_e64 v179,v179,v243,s[94:95]\n"
MF"v[64:67],a[84:87],a[240:243],v[64:67]\n"
BL"a[44:47],v224,s[8:11],s53 offen offset:1024 sc1\n"
MF"v[64:67],a[80:83],a[232:235],v[64:67]\n"
RD"a[116:119],v236 offset:21504\n"
"v_add_u32 v243,16,v243\n"
MF"v[28:31],a[88:91],a[228:231],v[28:31]\n"
"v_readlane_b32 s92,v234,2\n"
"s_and_b32 s92,s92,0xffff\n"
MF"v[28:31],a[92:95],a[236:239],v[28:31]\n"
RD"a[120:123],v236 offset:22528\n"
"v_cmp_eq_u32 vcc,s92,v244\n"
MF"v[68:71],a[92:95],a[240:243],v[68:71]\n"
"v_cmp_eq_u32_e64 s[94:95],s92,v245\n"
MF"v[68:71],a[88:91],a[232:235],v[68:71]\n"
RD"a[124:127],v236 offset:23552\n"
"v_cndmask_b32 v178,v178,v243,vcc\n"
MF"v[32:35],a[96:99],a[180:183],v[32:35]\n"
WT"lgkmcnt(3)\n"
BL"a[48:51],v224,s[8:11],s52 offen offset:2048 sc1\n"
MF"v[32:35],a[100:103],a[188:191],v[32:35]\n"
SA"s90,s88,3072\n"
RD"a[64:67],v237 offset:8192\n"
"v_cndmask_b32_e64 v179,v179,v243,s[94:95]\n"
MF"v[72:75],a[100:103],a[192:195],v[72:75]\n"
PK"v24,v24,v25\n"
PK"v25,v26,v27\n"
MF"v[72:75],a[96:99],a[184:187],v[72:75]\n"
RD"a[68:71],v237 offset:9216\n"
"v_add_u32 v243,16,v243\n"
MF"v[36:39],a[104:107],a[180:183],v[36:39]\n"
PK"v26,v28,v29\n"
PK"v27,v30,v31\n"
MF"v[36:39],a[108:111],a[188:191],v[36:39]\n"
BS"v[24:27],v224,s[8:11],s90 offen sc1\n"
"v_readlane_b32 s92,v234,3\n"
MF"v[76:79],a[108:111],a[192:195],v[76:79]\n"
"s_and_b32 s92,s92,0xffff\n"
RD"a[72:75],v237 offset:10240\n"
"v_cmp_eq_u32 vcc,s92,v244\n"
MF"v[76:79],a[104:107],a[184:187],v[76:79]\n"
BL"a[52:55],v224,s[8:11],s53 offen offset:2048 sc1\n"
MF"v[32:35],a[112:115],a[196:199],v[32:35]\n"
WT"lgkmcnt(2)\n"
RD"a[76:79],v237 offset:11264\n"
"v_cmp_eq_u32_e64 s[94:95],s92,v245\n"
MF"v[32:35],a[116:119],a[204:207],v[32:35]\n"
SA"s91,s22,3072\n"
RD"a[80:83],v237 offset:28672\n"
"v_cndmask_b32 v178,v178,v243,vcc\n"
MF"v[72:75],a[116:119],a[208:211],v[72:75]\n"
"v_cndmask_b32_e64 v179,v179,v243,s[94:95]\n"
"v_add_u32 v243,16,v243\n"
MF"v[72:75],a[112:115],a[200:203],v[72:75]\n"
RD"a[84:87],v237 offset:29696\n"
PK"v64,v64,v65\n"
MF"v[36:39],a[120:123],a[196:199],v[36:39]\n"
BL"a[56:59],v224,s[8:11],s52 offen offset:3072 sc1\n"
PK"v65,v66,v67\n"
MF"v[36:39],a[124:127],a[204:207],v[36:39]\n"
RD"a[88:91],v237 offset:30720\n"
"v_readlane_b32 s92,v234,4\n"
PK"v66,v68,v69\n"
MF"v[76:79],a[124:127],a[208:211],v[76:79]\n"
"s_and_b32 s92,s92,0xffff\n"
PK"v67,v70,v71\n"
"v_cmp_eq_u32 vcc,s92,v244\n"
MF"v[76:79],a[120:123],a[200:203],v[76:79]\n"
BS"v[64:67],v224,s[8:11],s91 offen sc1\n"
"v_cmp_eq_u32_e64 s[94:95],s92,v245\n"
MF"v[32:35],a[64:67],a[212:215],v[32:35]\n"
WT"lgkmcnt(2)\n"
RD"a[92:95],v237 offset:31744\n"
"v_cndmask_b32 v178,v178,v243,vcc\n"
MF"v[32:35],a[68:71],a[220:223],v[32:35]\n"
BL"a[60:63],v224,s[8:11],s53 offen offset:3072 sc1\n"
"v_cndmask_b32_e64 v179,v179,v243,s[94:95]\n"
MF"v[72:75],a[68:71],a[224:227],v[72:75]\n"
RD"a[96:99],v226 offset:8192\n"
"v_add_u32 v243,16,v243\n"
MF"v[72:75],a[64:67],a[216:219],v[72:75]\n"
RD"a[100:103],v226 offset:9216\n"
"v_readlane_b32 s92,v234,5\n"
MF"v[36:39],a[72:75],a[212:215],v[36:39]\n"
"s_and_b32 s92,s92,0xffff\n"
"v_cmp_eq_u32 vcc,s92,v244\n"
"v_cmp_eq_u32_e64 s[94:95],s92,v245\n"
MF"v[36:39],a[76:79],a[220:223],v[36:39]\n"
RD"a[104:107],v226 offset:10240\n"
"v_cndmask_b32 v178,v178,v243,vcc\n"
MF"v[76:79],a[76:79],a[224:227],v[76:79]\n"
"v_cndmask_b32_e64 v179,v179,v243,s[94:95]\n"
"v_add_u32 v243,16,v243\n"
MF"v[76:79],a[72:75],a[216:219],v[76:79]\n"
RD"a[108:111],v226 offset:11264\n"
"v_readlane_b32 s92,v234,6\n"
MF"v[32:35],a[80:83],a[228:231],v[32:35]\n"
WT"lgkmcnt(4)\n"
"s_and_b32 s92,s92,0xffff\n"
MF"v[32:35],a[84:87],a[236:239],v[32:35]\n"
RD"a[112:115],v226 offset:28672\n"
"v_cmp_eq_u32 vcc,s92,v244\n"
MF"v[72:75],a[84:87],a[240:243],v[72:75]\n"
"v_cmp_eq_u32_e64 s[94:95],s92,v245\n"
MF"v[72:75],a[80:83],a[232:235],v[72:75]\n"
RD"a[116:119],v226 offset:29696\n"
"v_cndmask_b32 v178,v178,v243,vcc\n"
MF"v[36:39],a[88:91],a[228:231],v[36:39]\n"
"v_cndmask_b32_e64 v179,v179,v243,s[94:95]\n"
MF"v[36:39],a[92:95],a[236:239],v[36:39]\n"
RD"a[120:123],v226 offset:30720\n"
"v_add_u32 v243,16,v243\n"
MF"v[76:79],a[92:95],a[240:243],v[76:79]\n"
"v_readlane_b32 s92,v234,7\n"
MF"v[76:79],a[88:91],a[232:235],v[76:79]\n"
WT"vmcnt(31) lgkmcnt(3)\n"
RD"a[124:127],v226 offset:31744\n"
"s_and_b32 s92,s92,0xffff\n"
MF"v[84:87],a[96:99],a[244:247],v[84:87]\n"
WT"vmcnt(29)\n"
"v_cmp_eq_u32 vcc,s92,v244\n"
MF"v[84:87],a[100:103],a[252:255],v[84:87]\n"
WT"vmcnt(28)\n"
RD"a[64:67],v235 offset:16384\n"
PK"v32,v32,v33\n"
MF"v[164:167],a[100:103],a[128:131],v[164:167]\n"
SA"s90,s88,4096\n"
PK"v33,v34,v35\n"
"v_cmp_eq_u32_e64 s[94:95],s92,v245\n"
MF"v[164:167],a[96:99],a[248:251],v[164:167]\n"
RD"a[68:71],v235 offset:17408\n"
PK"v34,v36,v37\n"
MF"v[92:95],a[104:107],a[244:247],v[92:95]\n"
"s_mul_i32 s92,s33,5\n"
PK"v35,v38,v39\n"
"v_cndmask_b32 v178,v178,v243,vcc\n"
MF"v[92:95],a[108:111],a[252:255],v[92:95]\n"
SA"s93,s92,s30\n"
BS"v[32:35],v224,s[8:11],s90 offen sc1\n"
MF"v[172:175],a[108:111],a[128:131],v[172:175]\n"
SA"s92,s92,4\n"
RD"a[72:75],v235 offset:18432\n"
"v_cndmask_b32_e64 v179,v179,v243,s[94:95]\n"
MF"v[172:175],a[104:107],a[248:251],v[172:175]\n"
WT"vmcnt(27) lgkmcnt(3)\n"
RD"a[76:79],v235 offset:19456\n"
"s_lshl_b32 s38,s93,6\n"
MF"v[84:87],a[112:115],a[132:135],v[84:87]\n"
WT"vmcnt(24)\n"
"s_lshl_b32 s55,s92,6\n"
MF"v[84:87],a[116:119],a[140:143],v[84:87]\n"
WT"vmcnt(23)\n"
RD"a[80:83],v236 offset:4096\n"
PK"v72,v72,v73\n"
MF"v[164:167],a[116:119],a[144:147],v[164:167]\n"
SA"s91,s22,4096\n"
PK"v73,v74,v75\n"
PK"v74,v76,v77\n"
MF"v[164:167],a[112:115],a[136:139],v[164:167]\n"
SA"s55,s55,s45\n"
RD"a[84:87],v236 offset:5120\n"
PK"v75,v78,v79\n"
MF"v[92:95],a[120:123],a[132:135],v[92:95]\n"
"s_lshl_b32 s36,s93,10\n"
BS"v[72:75],v224,s[8:11],s91 offen sc1\n"
MF"v[92:95],a[124:127],a[140:143],v[92:95]\n"
"s_lshl_b32 s37,s92,10\n"
RD"a[88:91],v236 offset:6144\n"
SA"s36,s36,65536\n"
MF"v[172:175],a[124:127],a[144:147],v[172:175]\n"
SA"s37,s37,65536\n"
SA"s37,s37,s45\n"
MF"v[172:175],a[120:123],a[136:139],v[172:175]\n"
WT"vmcnt(23) lgkmcnt(3)\n"
RD"a[92:95],v236 offset:7168\n"
"v_lshrrev_b32 v243,1,v224\n"
MF"v[84:87],a[64:67],a[148:151],v[84:87]\n"
WT"vmcnt(20)\n"
"v_min_u32 v244,312,v243\n"
MF"v[84:87],a[68:71],a[156:159],v[84:87]\n"
WT"vmcnt(18)\n"
RD"a[96:99],v236 offset:24576\n"
"v_and_b32 v243,56,v243\n"
MF"v[164:167],a[68:71],a[160:163],v[164:167]\n"
"v_lshlrev_b32 v243,3,v243\n"
MF"v[164:167],a[64:67],a[152:155],v[164:167]\n"
"s_mov_b32 s92,6400\n"
RD"a[100:103],v236 offset:25600\n"
MF"v[92:95],a[72:75],a[148:151],v[92:95]\n"
MF"v[92:95],a[76:79],a[156:159],v[92:95]\n"
RD"a[104:107],v236 offset:26624\n"
MF"v[172:175],a[76:79],a[160:163],v[172:175]\n"
MF"v[172:175],a[72:75],a[152:155],v[172:175]\n"
WT"vmcnt(17) lgkmcnt(3)\n"
RD"a[108:111],v236 offset:27648\n"
MF"v[84:87],a[80:83],a[164:167],v[84:87]\n"
WT"vmcnt(15)\n"
MF"v[84:87],a[84:87],a[172:175],v[84:87]\n"
WT"vmcnt(14)\n"
RD"a[112:115],v237 offset:12288\n"
MF"v[164:167],a[84:87],a[176:179],v[164:167]\n"
MF"v[164:167],a[80:83],a[168:171],v[164:167]\n"
RD"a[116:119],v237 offset:13312\n"
MF"v[92:95],a[88:91],a[164:167],v[92:95]\n"
MF"v[92:95],a[92:95],a[172:175],v[92:95]\n"
RD"a[120:123],v237 offset:14336\n"
MF"v[172:175],a[92:95],a[176:179],v[172:175]\n"
MF"v[172:175],a[88:91],a[168:171],v[172:175]\n"
WT"vmcnt(12) lgkmcnt(3)\n"
RD"a[124:127],v237 offset:15360\n"
MF"v[84:87],a[96:99],a[32:35],v[84:87]\n"
WT"vmcnt(9)\n"
MF"v[84:87],a[100:103],a[40:43],v[84:87]\n"
WT"vmcnt(8)\n"
RD"a[64:67],v226 offset:12288\n"
MF"v[164:167],a[100:103],a[44:47],v[164:167]\n"
MF"v[164:167],a[96:99],a[36:39],v[164:167]\n"
RD"a[68:71],v226 offset:13312\n"
MF"v[92:95],a[104:107],a[32:35],v[92:95]\n"
MF"v[92:95],a[108:111],a[40:43],v[92:95]\n"
RD"a[72:75],v226 offset:14336\n"
MF"v[172:175],a[108:111],a[44:47],v[172:175]\n"
MF"v[172:175],a[104:107],a[36:39],v[172:175]\n"
WT"vmcnt(7) lgkmcnt(3)\n"
RD"a[76:79],v226 offset:15360\n"
MF"v[84:87],a[112:115],a[48:51],v[84:87]\n"
WT"vmcnt(4)\n"
"buffer_load_dwordx2 v[240:241],v244,s[8:11],s92 offen sc1\n"
MF"v[84:87],a[116:119],a[56:59],v[84:87]\n"
WT"vmcnt(3)\n"
RD"a[80:83],v235 offset:0\n"
MF"v[164:167],a[116:119],a[60:63],v[164:167]\n"
MF"v[164:167],a[112:115],a[52:55],v[164:167]\n"
RD"a[84:87],v235 offset:1024\n"
MF"v[92:95],a[120:123],a[48:51],v[92:95]\n"
MF"v[92:95],a[124:127],a[56:59],v[92:95]\n"
RD"a[88:91],v235 offset:2048\n"
MF"v[172:175],a[124:127],a[60:63],v[172:175]\n"
MF"v[172:175],a[120:123],a[52:55],v[172:175]\n"
WT"lgkmcnt(3)\n"
RD"a[92:95],v235 offset:3072\n"
MF"v[100:103],a[64:67],a[244:247],v[100:103]\n"
MF"v[100:103],a[68:71],a[252:255],v[100:103]\n"
SA"s90,s88,327680\n"
RD"a[96:99],v235 offset:20480\n"
PK"v84,v84,v85\n"
MF"v[180:183],a[68:71],a[128:131],v[180:183]\n"
PK"v85,v86,v87\n"
PK"v86,v92,v93\n"
MF"v[180:183],a[64:67],a[248:251],v[180:183]\n"
RD"a[100:103],v235 offset:21504\n"
PK"v87,v94,v95\n"
MF"v[108:111],a[72:75],a[244:247],v[108:111]\n"
BS"v[84:87],v224,s[8:11],s90 offen sc1\n"
MF"v[108:111],a[76:79],a[252:255],v[108:111]\n"
RD"a[104:107],v235 offset:22528\n"
MF"v[188:191],a[76:79],a[128:131],v[188:191]\n"
MF"v[188:191],a[72:75],a[248:251],v[188:191]\n"
WT"lgkmcnt(3)\n"
RD"a[108:111],v235 offset:23552\n"
MF"v[100:103],a[80:83],a[132:135],v[100:103]\n"
MF"v[100:103],a[84:87],a[140:143],v[100:103]\n"
SA"s91,s22,327680\n"
RD"a[112:115],v236 offset:8192\n"
PK"v164,v164,v165\n"
MF"v[180:183],a[84:87],a[144:147],v[180:183]\n"
PK"v165,v166,v167\n"
PK"v166,v172,v173\n"
MF"v[180:183],a[80:83],a[136:139],v[180:183]\n"
RD"a[116:119],v236 offset:9216\n"
PK"v167,v174,v175\n"
MF"v[108:111],a[88:91],a[132:135],v[108:111]\n"
BS"v[164:167],v224,s[8:11],s91 offen sc1\n"
MF"v[108:111],a[92:95],a[140:143],v[108:111]\n"
RD"a[120:123],v236 offset:10240\n"
MF"v[188:191],a[92:95],a[144:147],v[188:191]\n"
MF"v[188:191],a[88:91],a[136:139],v[188:191]\n"
WT"lgkmcnt(3)\n"
RD"a[124:127],v236 offset:11264\n"
MF"v[100:103],a[96:99],a[148:151],v[100:103]\n"
MF"v[100:103],a[100:103],a[156:159],v[100:103]\n"
RD"a[64:67],v236 offset:28672\n"
MF"v[180:183],a[100:103],a[160:163],v[180:183]\n"
WT"vmcnt(3)\n"
"buffer_store_dwordx2 v[232:233],v239,s[8:11],s89 offen sc1\n"
MF"v[180:183],a[96:99],a[152:155],v[180:183]\n"
RD"a[68:71],v236 offset:29696\n"
MF"v[108:111],a[104:107],a[148:151],v[108:111]\n"
MF"v[108:111],a[108:111],a[156:159],v[108:111]\n"
RD"a[72:75],v236 offset:30720\n"
MF"v[188:191],a[108:111],a[160:163],v[188:191]\n"
MF"v[188:191],a[104:107],a[152:155],v[188:191]\n"
WT"lgkmcnt(3)\n"
RD"a[76:79],v236 offset:31744\n"
MF"v[100:103],a[112:115],a[164:167],v[100:103]\n"
MF"v[100:103],a[116:119],a[172:175],v[100:103]\n"
RD"a[80:83],v237 offset:16384\n"
MF"v[180:183],a[116:119],a[176:179],v[180:183]\n"
MF"v[180:183],a[112:115],a[168:171],v[180:183]\n"
RD"a[84:87],v237 offset:17408\n"
MF"v[108:111],a[120:123],a[164:167],v[108:111]\n"
MF"v[108:111],a[124:127],a[172:175],v[108:111]\n"
RD"a[88:91],v237 offset:18432\n"
MF"v[188:191],a[124:127],a[176:179],v[188:191]\n"
MF"v[188:191],a[120:123],a[168:171],v[188:191]\n"
WT"lgkmcnt(3)\n"
RD"a[92:95],v237 offset:19456\n"
MF"v[100:103],a[64:67],a[32:35],v[100:103]\n"
MF"v[100:103],a[68:71],a[40:43],v[100:103]\n"
RD"a[96:99],v226 offset:16384\n"
MF"v[180:183],a[68:71],a[44:47],v[180:183]\n"
MF"v[180:183],a[64:67],a[36:39],v[180:183]\n"
RD"a[100:103],v226 offset:17408\n"
MF"v[108:111],a[72:75],a[32:35],v[108:111]\n"
MF"v[108:111],a[76:79],a[40:43],v[108:111]\n"
RD"a[104:107],v226 offset:18432\n"
MF"v[188:191],a[76:79],a[44:47],v[188:191]\n"
MF"v[188:191],a[72:75],a[36:39],v[188:191]\n"
WT"lgkmcnt(3)\n"
RD"a[108:111],v226 offset:19456\n"
MF"v[100:103],a[80:83],a[48:51],v[100:103]\n"
MF"v[100:103],a[84:87],a[56:59],v[100:103]\n"
RD"a[112:115],v235 offset:4096\n"
MF"v[180:183],a[84:87],a[60:63],v[180:183]\n"
MF"v[180:183],a[80:83],a[52:55],v[180:183]\n"
RD"a[116:119],v235 offset:5120\n"
MF"v[108:111],a[88:91],a[48:51],v[108:111]\n"
MF"v[108:111],a[92:95],a[56:59],v[108:111]\n"
RD"a[120:123],v235 offset:6144\n"
MF"v[188:191],a[92:95],a[60:63],v[188:191]\n"
MF"v[188:191],a[88:91],a[52:55],v[188:191]\n"
WT"lgkmcnt(3)\n"
RD"a[124:127],v235 offset:7168\n"
MF"v[116:119],a[96:99],a[244:247],v[116:119]\n"
MF"v[116:119],a[100:103],a[252:255],v[116:119]\n"
SA"s90,s88,328704\n"
RD"a[64:67],v235 offset:24576\n"
PK"v100,v100,v101\n"
MF"v[196:199],a[100:103],a[128:131],v[196:199]\n"
PK"v101,v102,v103\n"
PK"v102,v108,v109\n"
MF"v[196:199],a[96:99],a[248:251],v[196:199]\n"
RD"a[68:71],v235 offset:25600\n"
PK"v103,v110,v111\n"
MF"v[124:127],a[104:107],a[244:247],v[124:127]\n"
BS"v[100:103],v224,s[8:11],s90 offen sc1\n"
MF"v[124:127],a[108:111],a[252:255],v[124:127]\n"
WT"vmcnt(4)\n"
"v_cmp_eq_u64 vcc,s[28:29],v[240:241]\n"
"s_cmp_eq_u64 vcc,exec\n"
"s_cbranch_scc0 L1086_%=\n"
"L1087_%=:\n"
MF"v[204:207],a[108:111],a[128:131],v[204:207]\n"
RD"a[72:75],v235 offset:26624\n"
MF"v[204:207],a[104:107],a[248:251],v[204:207]\n"
WT"lgkmcnt(3)\n"
RD"a[76:79],v235 offset:27648\n"
MF"v[116:119],a[112:115],a[132:135],v[116:119]\n"
BL"v[112:115],v176,s[12:15],s38 offen\n"
MF"v[116:119],a[116:119],a[140:143],v[116:119]\n"
SA"s91,s22,328704\n"
RD"a[80:83],v236 offset:12288\n"
PK"v180,v180,v181\n"
MF"v[196:199],a[116:119],a[144:147],v[196:199]\n"
BL"v[144:147],v178,s[8:11],s36 offen sc1\n"
MF"v[196:199],a[112:115],a[136:139],v[196:199]\n"
RD"a[84:87],v236 offset:13312\n"
PK"v181,v182,v183\n"
MF"v[124:127],a[120:123],a[132:135],v[124:127]\n"
PK"v182,v188,v189\n"
PK"v183,v190,v191\n"
MF"v[124:127],a[124:127],a[140:143],v[124:127]\n"
BS"v[180:183],v224,s[8:11],s91 offen sc1\n"
MF"v[204:207],a[124:127],a[144:147],v[204:207]\n"
RD"a[88:91],v236 offset:14336\n"
MF"v[204:207],a[120:123],a[136:139],v[204:207]\n"
WT"lgkmcnt(3)\n"
BL"v[120:123],v176,s[12:15],s55 offen\n"
MF"v[116:119],a[64:67],a[148:151],v[116:119]\n"
RD"a[92:95],v236 offset:15360\n"
MF"v[116:119],a[68:71],a[156:159],v[116:119]\n"
BL"v[152:155],v178,s[8:11],s37 offen sc1\n"
MF"v[196:199],a[68:71],a[160:163],v[196:199]\n"
RD"a[96:99],v237 offset:0\n"
MF"v[196:199],a[64:67],a[152:155],v[196:199]\n"
RD"a[100:103],v237 offset:1024\n"
MF"v[124:127],a[72:75],a[148:151],v[124:127]\n"
BL"v[128:131],v177,s[12:15],s38 offen\n"
MF"v[124:127],a[76:79],a[156:159],v[124:127]\n"
RD"a[104:107],v237 offset:2048\n"
MF"v[204:207],a[76:79],a[160:163],v[204:207]\n"
BL"v[160:163],v179,s[8:11],s36 offen sc1\n"
MF"v[204:207],a[72:75],a[152:155],v[204:207]\n"
WT"lgkmcnt(3)\n"
RD"a[108:111],v237 offset:3072\n"
MF"v[116:119],a[80:83],a[164:167],v[116:119]\n"
MF"v[116:119],a[84:87],a[172:175],v[116:119]\n"
RD"a[112:115],v237 offset:20480\n"
MF"v[196:199],a[84:87],a[176:179],v[196:199]\n"
BL"v[136:139],v177,s[12:15],s55 offen\n"
MF"v[196:199],a[80:83],a[168:171],v[196:199]\n"
RD"a[116:119],v237 offset:21504\n"
MF"v[124:127],a[88:91],a[164:167],v[124:127]\n"
BL"v[168:171],v179,s[8:11],s37 offen sc1\n"
MF"v[124:127],a[92:95],a[172:175],v[124:127]\n"
RD"a[120:123],v237 offset:22528\n"
MF"v[204:207],a[92:95],a[176:179],v[204:207]\n"
MF"v[204:207],a[88:91],a[168:171],v[204:207]\n"
WT"lgkmcnt(3)\n"
RD"a[124:127],v237 offset:23552\n"
MF"v[116:119],a[96:99],a[32:35],v[116:119]\n"
MF"v[116:119],a[100:103],a[40:43],v[116:119]\n"
RD"a[64:67],v226 offset:20480\n"
MF"v[196:199],a[100:103],a[44:47],v[196:199]\n"
"s_lshr_b32 s93,s31,3\n"
MF"v[196:199],a[96:99],a[36:39],v[196:199]\n"
"s_lshl_b32 s93,s93,4\n"
RD"a[68:71],v226 offset:21504\n"
MF"v[124:127],a[104:107],a[32:35],v[124:127]\n"
SA"s93,s93,s32\n"
MF"v[124:127],a[108:111],a[40:43],v[124:127]\n"
"s_mul_i32 s94,s93,40960\n"
RD"a[72:75],v226 offset:22528\n"
MF"v[204:207],a[108:111],a[44:47],v[204:207]\n"
"s_lshl_b32 s93,s93,9\n"
MF"v[204:207],a[104:107],a[36:39],v[204:207]\n"
WT"lgkmcnt(3)\n"
RD"a[76:79],v226 offset:23552\n"
SA"s93,s93,80478208\n"
MF"v[116:119],a[112:115],a[48:51],v[116:119]\n"
SA"s94,s94,59506688\n"
MF"v[116:119],a[116:119],a[56:59],v[116:119]\n"
"s_lshl_b32 s92,s30,10\n"
RD"a[80:83],v235 offset:8192\n"
MF"v[196:199],a[116:119],a[60:63],v[196:199]\n"
"v_add_u32 v244,s92,v224\n"
MF"v[196:199],a[112:115],a[52:55],v[196:199]\n"
SA"s92,s45,4096\n"
RD"a[84:87],v235 offset:9216\n"
MF"v[124:127],a[120:123],a[48:51],v[124:127]\n"
"v_add_u32 v245,s92,v224\n"
MF"v[124:127],a[124:127],a[56:59],v[124:127]\n"
RD"a[88:91],v235 offset:10240\n"
MF"v[204:207],a[124:127],a[60:63],v[204:207]\n"
"v_and_b32 v246,128,v224\n"
MF"v[204:207],a[120:123],a[52:55],v[204:207]\n"
WT"lgkmcnt(3)\n"
RD"a[92:95],v235 offset:11264\n"
"v_lshl_add_u32 v243,v246,5,v243\n"
MF"v[132:135],a[64:67],a[244:247],v[132:135]\n"
"v_lshrrev_b32 v246,8,v224\n"
MF"v[132:135],a[68:71],a[252:255],v[132:135]\n"
SA"s90,s88,329728\n"
RD"a[96:99],v235 offset:28672\n"
PK"v116,v116,v117\n"
MF"v[208:211],a[68:71],a[128:131],v[208:211]\n"
"s_mov_b32 s95,0xffff0101\n"
PK"v117,v118,v119\n"
"v_lshl_add_u32 v243,v246,27,v243\n"
MF"v[208:211],a[64:67],a[248:251],v[208:211]\n"
"s_lshl_b32 s92,s30,10\n"
RD"a[100:103],v235 offset:29696\n"
PK"v118,v124,v125\n"
MF"v[140:143],a[72:75],a[244:247],v[140:143]\n"
PK"v119,v126,v127\n"
"v_add_u32 v247,s92,v227\n"
MF"v[140:143],a[76:79],a[252:255],v[140:143]\n"
BS"v[116:119],v224,s[8:11],s90 offen sc1\n"
MF"v[212:215],a[76:79],a[128:131],v[212:215]\n"
RD"a[104:107],v235 offset:30720\n"
MF"v[212:215],a[72:75],a[248:251],v[212:215]\n"
WT"lgkmcnt(3)\n"
"buffer_load_dwordx2 v[240:241],v243,s[8:11],s93 offen sc1\n"
MF"v[132:135],a[80:83],a[132:135],v[132:135]\n"
RD"a[108:111],v235 offset:31744\n"
MF"v[132:135],a[84:87],a[140:143],v[132:135]\n"
SA"s91,s22,329728\n"
RD"a[112:115],v236 offset:16384\n"
PK"v196,v196,v197\n"
MF"v[208:211],a[84:87],a[144:147],v[208:211]\n"
PK"v197,v198,v199\n"
PK"v198,v204,v205\n"
MF"v[208:211],a[80:83],a[136:139],v[208:211]\n"
RD"a[116:119],v236 offset:17408\n"
PK"v199,v206,v207\n"
MF"v[140:143],a[88:91],a[132:135],v[140:143]\n"
BS"v[196:199],v224,s[8:11],s91 offen sc1\n"
MF"v[140:143],a[92:95],a[140:143],v[140:143]\n"
RD"a[120:123],v236 offset:18432\n"
MF"v[212:215],a[92:95],a[144:147],v[212:215]\n"
MF"v[212:215],a[88:91],a[136:139],v[212:215]\n"
WT"lgkmcnt(3)\n"
RD"a[124:127],v236 offset:19456\n"
MF"v[132:135],a[96:99],a[148:151],v[132:135]\n"
MF"v[132:135],a[100:103],a[156:159],v[132:135]\n"
RD"a[64:67],v237 offset:4096\n"
MF"v[208:211],a[100:103],a[160:163],v[208:211]\n"
MF"v[208:211],a[96:99],a[152:155],v[208:211]\n"
RD"a[68:71],v237 offset:5120\n"
MF"v[140:143],a[104:107],a[148:151],v[140:143]\n"
MF"v[140:143],a[108:111],a[156:159],v[140:143]\n"
RD"a[72:75],v237 offset:6144\n"
MF"v[212:215],a[108:111],a[160:163],v[212:215]\n"
MF"v[212:215],a[104:107],a[152:155],v[212:215]\n"
WT"lgkmcnt(3)\n"
RD"a[76:79],v237 offset:7168\n"
MF"v[132:135],a[112:115],a[164:167],v[132:135]\n"
MF"v[132:135],a[116:119],a[172:175],v[132:135]\n"
RD"a[80:83],v237 offset:24576\n"
MF"v[208:211],a[116:119],a[176:179],v[208:211]\n"
MF"v[208:211],a[112:115],a[168:171],v[208:211]\n"
RD"a[84:87],v237 offset:25600\n"
MF"v[140:143],a[120:123],a[164:167],v[140:143]\n"
MF"v[140:143],a[124:127],a[172:175],v[140:143]\n"
RD"a[88:91],v237 offset:26624\n"
MF"v[212:215],a[124:127],a[176:179],v[212:215]\n"
MF"v[212:215],a[120:123],a[168:171],v[212:215]\n"
WT"lgkmcnt(3)\n"
RD"a[92:95],v237 offset:27648\n"
MF"v[132:135],a[64:67],a[32:35],v[132:135]\n"
MF"v[132:135],a[68:71],a[40:43],v[132:135]\n"
RD"a[96:99],v226 offset:24576\n"
MF"v[208:211],a[68:71],a[44:47],v[208:211]\n"
MF"v[208:211],a[64:67],a[36:39],v[208:211]\n"
RD"a[100:103],v226 offset:25600\n"
MF"v[140:143],a[72:75],a[32:35],v[140:143]\n"
MF"v[140:143],a[76:79],a[40:43],v[140:143]\n"
RD"a[104:107],v226 offset:26624\n"
MF"v[212:215],a[76:79],a[44:47],v[212:215]\n"
MF"v[212:215],a[72:75],a[36:39],v[212:215]\n"
WT"lgkmcnt(3)\n"
RD"a[108:111],v226 offset:27648\n"
MF"v[132:135],a[80:83],a[48:51],v[132:135]\n"
MF"v[132:135],a[84:87],a[56:59],v[132:135]\n"
RD"a[112:115],v235 offset:12288\n"
MF"v[208:211],a[84:87],a[60:63],v[208:211]\n"
WT"vmcnt(1)\n"
"v_cmp_eq_u64 vcc,s[28:29],v[240:241]\n"
MF"v[208:211],a[80:83],a[52:55],v[208:211]\n"
"s_and_b32 s50,vcc_lo,0xff\n"
RD"a[116:119],v235 offset:13312\n"
"s_or_b32 s95,s95,s50\n"
MF"v[140:143],a[88:91],a[48:51],v[140:143]\n"
"s_bitcmp1_b32 s95,1\n"
"s_cselect_b32 s51,s94,1065353216\n"
MF"v[140:143],a[92:95],a[56:59],v[140:143]\n"
SA"s92,s51,5120\n"
RD"a[120:123],v235 offset:14336\n"
MF"v[212:215],a[92:95],a[60:63],v[212:215]\n"
BL"a[184:187],v244,s[8:11],s92 offen sc1\n"
MF"v[212:215],a[88:91],a[52:55],v[212:215]\n"
WT"lgkmcnt(3)\n"
RD"a[124:127],v235 offset:15360\n"
MF"v[148:151],a[96:99],a[244:247],v[148:151]\n"
BL"a[216:219],v245,s[8:11],s92 offen sc1\n"
MF"v[148:151],a[100:103],a[252:255],v[148:151]\n"
SA"s90,s88,330752\n"
RD"a[64:67],v236 offset:0\n"
PK"v132,v132,v133\n"
MF"v[216:219],a[100:103],a[128:131],v[216:219]\n"
"s_bitcmp1_b32 s95,2\n"
PK"v133,v134,v135\n"
PK"v134,v140,v141\n"
MF"v[216:219],a[96:99],a[248:251],v[216:219]\n"
"s_cselect_b32 s51,s94,1065353216\n"
RD"a[68:71],v236 offset:1024\n"
PK"v135,v142,v143\n"
MF"v[156:159],a[104:107],a[244:247],v[156:159]\n"
SA"s92,s51,10240\n"
BS"v[132:135],v224,s[8:11],s90 offen sc1\n"
MF"v[156:159],a[108:111],a[252:255],v[156:159]\n"
BL"a[188:191],v244,s[8:11],s92 offen sc1\n"
MF"v[220:223],a[108:111],a[128:131],v[220:223]\n"
RD"a[72:75],v236 offset:2048\n"
MF"v[220:223],a[104:107],a[248:251],v[220:223]\n"
WT"lgkmcnt(3)\n"
BL"a[220:223],v245,s[8:11],s92 offen sc1\n"
MF"v[148:151],a[112:115],a[132:135],v[148:151]\n"
"s_bitcmp1_b32 s95,3\n"
RD"a[76:79],v236 offset:3072\n"
"s_cselect_b32 s51,s94,1065353216\n"
MF"v[148:151],a[116:119],a[140:143],v[148:151]\n"
SA"s91,s22,330752\n"
RD"a[80:83],v236 offset:20480\n"
PK"v208,v208,v209\n"
MF"v[216:219],a[116:119],a[144:147],v[216:219]\n"
SA"s92,s51,15360\n"
PK"v209,v210,v211\n"
PK"v210,v212,v213\n"
MF"v[216:219],a[112:115],a[136:139],v[216:219]\n"
BL"a[192:195],v244,s[8:11],s92 offen sc1\n"
MF"v[156:159],a[120:123],a[132:135],v[156:159]\n"
RD"a[84:87],v236 offset:21504\n"
PK"v211,v214,v215\n"
MF"v[156:159],a[124:127],a[140:143],v[156:159]\n"
BS"v[208:211],v224,s[8:11],s91 offen sc1\n"
MF"v[220:223],a[124:127],a[144:147],v[220:223]\n"
"s_bitcmp1_b32 s95,4\n"
BL"a[224:227],v245,s[8:11],s92 offen sc1\n"
MF"v[220:223],a[120:123],a[136:139],v[220:223]\n"
WT"lgkmcnt(2)\n"
RD"a[88:91],v236 offset:22528\n"
RD"a[92:95],v236 offset:23552\n"
MF"v[148:151],a[64:67],a[148:151],v[148:151]\n"
"s_cselect_b32 s51,s94,1065353216\n"
SA"s92,s51,20480\n"
MF"v[148:151],a[68:71],a[156:159],v[148:151]\n"
BL"a[196:199],v244,s[8:11],s92 offen sc1\n"
MF"v[216:219],a[68:71],a[160:163],v[216:219]\n"
RD"a[96:99],v237 offset:8192\n"
MF"v[216:219],a[64:67],a[152:155],v[216:219]\n"
BL"a[228:231],v245,s[8:11],s92 offen sc1\n"
MF"v[156:159],a[72:75],a[148:151],v[156:159]\n"
"s_bitcmp1_b32 s95,5\n"
RD"a[100:103],v237 offset:9216\n"
"s_cselect_b32 s51,s94,1065353216\n"
MF"v[156:159],a[76:79],a[156:159],v[156:159]\n"
SA"s92,s51,25600\n"
RD"a[104:107],v237 offset:10240\n"
MF"v[220:223],a[76:79],a[160:163],v[220:223]\n"
BL"a[200:203],v244,s[8:11],s92 offen sc1\n"
MF"v[220:223],a[72:75],a[152:155],v[220:223]\n"
WT"lgkmcnt(3)\n"
RD"a[108:111],v237 offset:11264\n"
MF"v[148:151],a[80:83],a[164:167],v[148:151]\n"
BL"a[232:235],v245,s[8:11],s92 offen sc1\n"
MF"v[148:151],a[84:87],a[172:175],v[148:151]\n"
RD"a[112:115],v237 offset:28672\n"
MF"v[216:219],a[84:87],a[176:179],v[216:219]\n"
"s_bitcmp1_b32 s95,6\n"
"s_cselect_b32 s51,s94,1065353216\n"
MF"v[216:219],a[80:83],a[168:171],v[216:219]\n"
SA"s92,s51,30720\n"
RD"a[116:119],v237 offset:29696\n"
MF"v[156:159],a[88:91],a[164:167],v[156:159]\n"
BL"a[204:207],v244,s[8:11],s92 offen sc1\n"
MF"v[156:159],a[92:95],a[172:175],v[156:159]\n"
RD"a[120:123],v237 offset:30720\n"
MF"v[220:223],a[92:95],a[176:179],v[220:223]\n"
BL"a[236:239],v245,s[8:11],s92 offen sc1\n"
MF"v[220:223],a[88:91],a[168:171],v[220:223]\n"
WT"lgkmcnt(3)\n"
RD"a[124:127],v237 offset:31744\n"
MF"v[148:151],a[96:99],a[32:35],v[148:151]\n"
"s_bitcmp1_b32 s95,7\n"
"s_cselect_b32 s51,s94,1065353216\n"
MF"v[148:151],a[100:103],a[40:43],v[148:151]\n"
SA"s92,s51,35840\n"
MF"v[216:219],a[100:103],a[44:47],v[216:219]\n"
BL"a[208:211],v244,s[8:11],s92 offen sc1\n"
MF"v[216:219],a[96:99],a[36:39],v[216:219]\n"
MF"v[156:159],a[104:107],a[32:35],v[156:159]\n"
BL"a[240:243],v245,s[8:11],s92 offen sc1\n"
MF"v[156:159],a[108:111],a[40:43],v[156:159]\n"
MF"v[220:223],a[108:111],a[44:47],v[220:223]\n"
MF"v[220:223],a[104:107],a[36:39],v[220:223]\n"
WT"lgkmcnt(0)\n"
MF"v[148:151],a[112:115],a[48:51],v[148:151]\n"
MF"v[148:151],a[116:119],a[56:59],v[148:151]\n"
MF"v[216:219],a[116:119],a[60:63],v[216:219]\n"
MF"v[216:219],a[112:115],a[52:55],v[216:219]\n"
MF"v[156:159],a[120:123],a[48:51],v[156:159]\n"
MF"v[156:159],a[124:127],a[56:59],v[156:159]\n"
MF"v[220:223],a[124:127],a[60:63],v[220:223]\n"
MF"v[220:223],a[120:123],a[52:55],v[220:223]\n"
"s_nop 5\n"
PK"v148,v148,v149\n"
PK"v149,v150,v151\n"
PK"v150,v156,v157\n"
PK"v151,v158,v159\n"
SA"s90,s88,331776\n"
BS"v[148:151],v224,s[8:11],s90 offen sc1\n"
PK"v216,v216,v217\n"
PK"v217,v218,v219\n"
PK"v218,v220,v221\n"
PK"v219,v222,v223\n"
SA"s91,s22,331776\n"
BS"v[216:219],v224,s[8:11],s91 offen sc1\n"
"s_cmp_eq_u32 s23,0\n"
"s_cbranch_scc0 L1089_%=\n"
"ds_write_b128 v227,v[0:3] offset:0\n"
"ds_write_b128 v227,v[8:11] offset:1024\n"
"ds_write_b128 v227,v[16:19] offset:2048\n"
"ds_write_b128 v227,v[24:27] offset:3072\n"
"ds_write_b128 v227,v[32:35] offset:4096\n"
"ds_write_b128 v227,v[84:87] offset:5120\n"
"ds_write_b128 v227,v[100:103] offset:6144\n"
"ds_write_b128 v227,v[116:119] offset:7168\n"
"ds_write_b128 v227,v[132:135] offset:8192\n"
"ds_write_b128 v227,v[148:151] offset:9216\n"
"L1089_%=:\n"
"s_cmp_eq_u32 s23,4\n"
"s_cbranch_scc0 L1090_%=\n"
"ds_write_b128 v227,v[40:43] offset:0\n"
"ds_write_b128 v227,v[48:51] offset:1024\n"
"ds_write_b128 v227,v[56:59] offset:2048\n"
"ds_write_b128 v227,v[64:67] offset:3072\n"
"ds_write_b128 v227,v[72:75] offset:4096\n"
"ds_write_b128 v227,v[164:167] offset:5120\n"
"ds_write_b128 v227,v[180:183] offset:6144\n"
"ds_write_b128 v227,v[196:199] offset:7168\n"
"ds_write_b128 v227,v[208:211] offset:8192\n"
"ds_write_b128 v227,v[216:219] offset:9216\n"
"L1090_%=:\n"
MF"v[4:7],v[96:99],v[112:115],0\n"
MF"v[12:15],v[104:107],v[112:115],0\n"
MF"v[20:23],v[96:99],v[120:123],0\n"
MF"v[28:31],v[104:107],v[120:123],0\n"
MF"v[4:7],v[96:99],v[144:147],v[4:7]\n"
MF"v[12:15],v[104:107],v[144:147],v[12:15]\n"
MF"v[20:23],v[96:99],v[152:155],v[20:23]\n"
MF"v[28:31],v[104:107],v[152:155],v[28:31]\n"
MF"v[36:39],v[96:99],v[128:131],0\n"
MF"v[44:47],v[104:107],v[128:131],0\n"
MF"v[52:55],v[96:99],v[136:139],0\n"
MF"v[60:63],v[104:107],v[136:139],0\n"
MF"v[36:39],v[96:99],v[160:163],v[36:39]\n"
MF"v[44:47],v[104:107],v[160:163],v[44:47]\n"
MF"v[52:55],v[96:99],v[168:171],v[52:55]\n"
MF"v[60:63],v[104:107],v[168:171],v[60:63]\n"
SA"s89,s89,4096\n"
"s_mov_b32 s54,1048576\n"
WT"vmcnt(0)\n"
"buffer_store_dwordx2 v[232:233],v239,s[8:11],s89 offen sc1\n"
"buffer_load_dwordx2 v[240:241],v243,s[8:11],s93 offen sc1\n"
WT"lgkmcnt(0)\n"
"s_barrier\n"
RD"a[180:183],v247 offset:0\n"
RD"a[212:215],v227 offset:4096\n"
RD"a[64:67],v247 offset:5120\n"
RD"a[96:99],v227 offset:9216\n"
"s_getpc_b64 s[52:53]\n"
"L1097_%=:\n"
SA"s52,s52,L1096_%=-L1097_%=-32\n"
"s_addc_u32 s53,s53,0\n"
"s_mov_b32 s89,0\n"
WT"lgkmcnt(0)\n"
MF"v[4:7],v[80:83],a[180:183],v[4:7]\n"
MF"v[12:15],v[88:91],a[180:183],v[12:15]\n"
MF"v[20:23],v[80:83],a[212:215],v[20:23]\n"
MF"v[28:31],v[88:91],a[212:215],v[28:31]\n"
MF"v[36:39],v[80:83],a[64:67],v[36:39]\n"
MF"v[44:47],v[88:91],a[64:67],v[44:47]\n"
MF"v[52:55],v[80:83],a[96:99],v[52:55]\n"
MF"v[60:63],v[88:91],a[96:99],v[60:63]\n"
"s_branch L1092_%=\n"
"L1094_%=:\n"
"s_sleep 3\n"
"buffer_load_dwordx2 v[240:241],v243,s[8:11],s93 offen sc1\n"
"L1091_%=:\n"
WT"vmcnt(0)\n"
"v_cmp_eq_u64 vcc,s[28:29],v[240:241]\n"
"s_sub_u32 s54,s54,1\n"
"s_cselect_b32 s90,-1,vcc_lo\n"
"s_andn2_b32 s90,s90,s95\n"
"s_cbranch_scc0 L1094_%=\n"
"s_or_b32 s95,s95,s90\n"
"L1098_%=:\n"
"s_ff1_i32_b32 s50,s90\n"
"s_bitset0_b32 s90,s50\n"
"s_lshl_b32 s50,s50,5\n"
SA"s50,s50,s52\n"
"s_addc_u32 s51,s53,0\n"
"s_setpc_b64 s[50:51]\n"
"L1096_%=:\n"
SA"s92,s94,5120\n"
BL"a[184:187],v244,s[8:11],s92 offen sc1\n"
BL"a[216:219],v245,s[8:11],s92 offen sc1\n"
"s_branch L1093_%=\n"
"s_nop 0\n"
SA"s92,s94,10240\n"
BL"a[188:191],v244,s[8:11],s92 offen sc1\n"
BL"a[220:223],v245,s[8:11],s92 offen sc1\n"
"s_branch L1093_%=\n"
"s_nop 0\n"
SA"s92,s94,15360\n"
BL"a[192:195],v244,s[8:11],s92 offen sc1\n"
BL"a[224:227],v245,s[8:11],s92 offen sc1\n"
"s_branch L1093_%=\n"
"s_nop 0\n"
SA"s92,s94,20480\n"
BL"a[196:199],v244,s[8:11],s92 offen sc1\n"
BL"a[228:231],v245,s[8:11],s92 offen sc1\n"
"s_branch L1093_%=\n"
"s_nop 0\n"
SA"s92,s94,25600\n"
BL"a[200:203],v244,s[8:11],s92 offen sc1\n"
BL"a[232:235],v245,s[8:11],s92 offen sc1\n"
"s_branch L1093_%=\n"
"s_nop 0\n"
SA"s92,s94,30720\n"
BL"a[204:207],v244,s[8:11],s92 offen sc1\n"
BL"a[236:239],v245,s[8:11],s92 offen sc1\n"
"s_branch L1093_%=\n"
"s_nop 0\n"
SA"s92,s94,35840\n"
BL"a[208:211],v244,s[8:11],s92 offen sc1\n"
BL"a[240:243],v245,s[8:11],s92 offen sc1\n"
"s_branch L1093_%=\n"
"s_nop 0\n"
"s_nop 0\n"
"s_nop 0\n"
"s_nop 0\n"
"s_nop 0\n"
"s_nop 0\n"
"s_nop 0\n"
"s_nop 0\n"
"s_nop 0\n"
SA"s92,s94,332800\n"
BL"a[68:71],v244,s[8:11],s92 offen sc1\n"
BL"a[100:103],v245,s[8:11],s92 offen sc1\n"
"s_branch L1093_%=\n"
"s_nop 0\n"
SA"s92,s94,337920\n"
BL"a[72:75],v244,s[8:11],s92 offen sc1\n"
BL"a[104:107],v245,s[8:11],s92 offen sc1\n"
"s_branch L1093_%=\n"
"s_nop 0\n"
SA"s92,s94,343040\n"
BL"a[76:79],v244,s[8:11],s92 offen sc1\n"
BL"a[108:111],v245,s[8:11],s92 offen sc1\n"
"s_branch L1093_%=\n"
"s_nop 0\n"
SA"s92,s94,348160\n"
BL"a[80:83],v244,s[8:11],s92 offen sc1\n"
BL"a[112:115],v245,s[8:11],s92 offen sc1\n"
"s_branch L1093_%=\n"
"s_nop 0\n"
SA"s92,s94,353280\n"
BL"a[84:87],v244,s[8:11],s92 offen sc1\n"
BL"a[116:119],v245,s[8:11],s92 offen sc1\n"
"s_branch L1093_%=\n"
"s_nop 0\n"
SA"s92,s94,358400\n"
BL"a[88:91],v244,s[8:11],s92 offen sc1\n"
BL"a[120:123],v245,s[8:11],s92 offen sc1\n"
"s_branch L1093_%=\n"
"s_nop 0\n"
SA"s92,s94,363520\n"
BL"a[92:95],v244,s[8:11],s92 offen sc1\n"
BL"a[124:127],v245,s[8:11],s92 offen sc1\n"
"s_branch L1093_%=\n"
"s_nop 0\n"
"L1093_%=:\n"
"s_cmp_lg_u32 s90,0\n"
"s_cbranch_scc1 L1098_%=\n"
"s_and_b32 s92,s95,s89\n"
"s_cmp_eq_u32 s92,-1\n"
"s_cbranch_scc1 L1095_%=\n"
"buffer_load_dwordx2 v[240:241],v243,s[8:11],s93 offen sc1\n"
WT"vmcnt(1)\n"
"L1092_%=:\n"
"s_or_b32 s92,s95,0xff00\n"
"s_andn2_b32 s92,s92,s89\n"
"s_cmp_eq_u32 s92,-1\n"
"s_cbranch_scc0 L1091_%=\n"
MF"v[4:7],v[80:83],a[184:187],v[4:7]\n"
MF"v[12:15],v[88:91],a[184:187],v[12:15]\n"
MF"v[4:7],v[80:83],a[188:191],v[4:7]\n"
MF"v[12:15],v[88:91],a[188:191],v[12:15]\n"
MF"v[4:7],v[80:83],a[192:195],v[4:7]\n"
MF"v[12:15],v[88:91],a[192:195],v[12:15]\n"
MF"v[4:7],v[80:83],a[196:199],v[4:7]\n"
MF"v[12:15],v[88:91],a[196:199],v[12:15]\n"
MF"v[4:7],v[80:83],a[200:203],v[4:7]\n"
MF"v[12:15],v[88:91],a[200:203],v[12:15]\n"
MF"v[4:7],v[80:83],a[204:207],v[4:7]\n"
MF"v[12:15],v[88:91],a[204:207],v[12:15]\n"
MF"v[4:7],v[80:83],a[208:211],v[4:7]\n"
MF"v[12:15],v[88:91],a[208:211],v[12:15]\n"
MF"v[20:23],v[80:83],a[216:219],v[20:23]\n"
MF"v[28:31],v[88:91],a[216:219],v[28:31]\n"
MF"v[20:23],v[80:83],a[220:223],v[20:23]\n"
MF"v[28:31],v[88:91],a[220:223],v[28:31]\n"
MF"v[20:23],v[80:83],a[224:227],v[20:23]\n"
MF"v[28:31],v[88:91],a[224:227],v[28:31]\n"
MF"v[20:23],v[80:83],a[228:231],v[20:23]\n"
MF"v[28:31],v[88:91],a[228:231],v[28:31]\n"
PK"v4,v4,v5\n"
PK"v5,v6,v7\n"
PK"v6,v12,v13\n"
PK"v7,v14,v15\n"
BS"v[4:7],v176,s[16:19],s38 offen\n"
MF"v[20:23],v[80:83],a[232:235],v[20:23]\n"
MF"v[28:31],v[88:91],a[232:235],v[28:31]\n"
MF"v[20:23],v[80:83],a[236:239],v[20:23]\n"
MF"v[28:31],v[88:91],a[236:239],v[28:31]\n"
MF"v[20:23],v[80:83],a[240:243],v[20:23]\n"
MF"v[28:31],v[88:91],a[240:243],v[28:31]\n"
"s_nop 7\n"
PK"v20,v20,v21\n"
PK"v21,v22,v23\n"
PK"v22,v28,v29\n"
PK"v23,v30,v31\n"
BS"v[20:23],v176,s[16:19],s55 offen\n"
"s_mov_b32 s89,-1\n"
"s_cmp_eq_u32 s95,-1\n"
"s_cbranch_scc0 L1091_%=\n"
"L1095_%=:\n"
WT"vmcnt(0)\n"
MF"v[36:39],v[80:83],a[68:71],v[36:39]\n"
MF"v[44:47],v[88:91],a[68:71],v[44:47]\n"
MF"v[36:39],v[80:83],a[72:75],v[36:39]\n"
MF"v[44:47],v[88:91],a[72:75],v[44:47]\n"
MF"v[36:39],v[80:83],a[76:79],v[36:39]\n"
MF"v[44:47],v[88:91],a[76:79],v[44:47]\n"
MF"v[36:39],v[80:83],a[80:83],v[36:39]\n"
MF"v[44:47],v[88:91],a[80:83],v[44:47]\n"
MF"v[36:39],v[80:83],a[84:87],v[36:39]\n"
MF"v[44:47],v[88:91],a[84:87],v[44:47]\n"
MF"v[36:39],v[80:83],a[88:91],v[36:39]\n"
MF"v[44:47],v[88:91],a[88:91],v[44:47]\n"
MF"v[36:39],v[80:83],a[92:95],v[36:39]\n"
MF"v[44:47],v[88:91],a[92:95],v[44:47]\n"
MF"v[52:55],v[80:83],a[100:103],v[52:55]\n"
MF"v[60:63],v[88:91],a[100:103],v[60:63]\n"
MF"v[52:55],v[80:83],a[104:107],v[52:55]\n"
MF"v[60:63],v[88:91],a[104:107],v[60:63]\n"
MF"v[52:55],v[80:83],a[108:111],v[52:55]\n"
MF"v[60:63],v[88:91],a[108:111],v[60:63]\n"
MF"v[52:55],v[80:83],a[112:115],v[52:55]\n"
MF"v[60:63],v[88:91],a[112:115],v[60:63]\n"
PK"v36,v36,v37\n"
PK"v37,v38,v39\n"
PK"v38,v44,v45\n"
PK"v39,v46,v47\n"
BS"v[36:39],v177,s[16:19],s38 offen\n"
MF"v[52:55],v[80:83],a[116:119],v[52:55]\n"
MF"v[60:63],v[88:91],a[116:119],v[60:63]\n"
MF"v[52:55],v[80:83],a[120:123],v[52:55]\n"
MF"v[60:63],v[88:91],a[120:123],v[60:63]\n"
MF"v[52:55],v[80:83],a[124:127],v[52:55]\n"
MF"v[60:63],v[88:91],a[124:127],v[60:63]\n"
"s_nop 7\n"
PK"v52,v52,v53\n"
PK"v53,v54,v55\n"
PK"v54,v60,v61\n"
PK"v55,v62,v63\n"
BS"v[52:55],v177,s[16:19],s55 offen\n"
WT"vmcnt(0) lgkmcnt(0)\n"
"s_endpgm\n"
"L1004_%=:\n"
"s_mov_b32 s54,0\n"
"L1006_%=:\n"
"s_sleep 2\n"
BL"v[160:163],v224,s[8:11],0 offen offset:256 sc1\n"
BL"v[164:167],v224,s[8:11],0 offen offset:1280 sc1\n"
WT"vmcnt(0)\n"
SA"s54,s54,1\n"
"v_cmp_eq_u64 vcc,s[28:29],v[160:161]\n"
"v_cmp_eq_u64_e64 s[62:63],s[28:29],v[162:163]\n"
"s_and_b64 vcc,vcc,s[62:63]\n"
"v_cmp_eq_u64_e64 s[62:63],s[28:29],v[164:165]\n"
"s_and_b64 vcc,vcc,s[62:63]\n"
"v_cmp_eq_u64_e64 s[62:63],s[28:29],v[166:167]\n"
"s_and_b64 vcc,vcc,s[62:63]\n"
"s_cmp_eq_u64 vcc,exec\n"
"s_cbranch_scc1 L1005_%=\n"
"s_cmp_lt_u32 s54,1048576\n"
"s_cbranch_scc1 L1006_%=\n"
"s_branch L1005_%=\n"
"L1007_%=:\n"
"s_mov_b32 s54,0\n"
"L1009_%=:\n"
"v_max3_u32 v226,v160,v161,v162\n"
"v_max3_u32 v226,v226,v163,v164\n"
"v_max3_u32 v226,v226,v165,v166\n"
"v_max3_u32 v226,v226,v167,v168\n"
"v_max3_u32 v226,v226,v169,v170\n"
"v_max3_u32 v226,v226,v171,v172\n"
"v_max3_u32 v226,v226,v173,v174\n"
"v_max3_u32 v226,v226,v175,v176\n"
"v_max3_u32 v226,v226,v177,v178\n"
"v_max3_u32 v226,v226,v179,v180\n"
"v_max3_u32 v226,v226,v181,v182\n"
"v_max3_u32 v226,v226,v183,v184\n"
"v_max3_u32 v226,v226,v185,v186\n"
"v_max3_u32 v226,v226,v187,v188\n"
"v_max3_u32 v226,v226,v189,v190\n"
"v_max_u32 v226,v226,v191\n"
"v_mov_b32 v229,v226\n"
"s_nop 1\n"
"v_max_u32_dpp v229,v229,v229 row_shr:1 row_mask:0xf bank_mask:0xf\n"
"s_nop 1\n"
"v_max_u32_dpp v229,v229,v229 row_shr:2 row_mask:0xf bank_mask:0xf\n"
"s_nop 1\n"
"v_max_u32_dpp v229,v229,v229 row_shr:4 row_mask:0xf bank_mask:0xf\n"
"s_nop 1\n"
"v_max_u32_dpp v229,v229,v229 row_shr:8 row_mask:0xf bank_mask:0xf\n"
"s_nop 1\n"
"v_max_u32_dpp v229,v229,v229 row_bcast:15 row_mask:0xa bank_mask:0xf\n"
"s_nop 1\n"
"v_max_u32_dpp v229,v229,v229 row_bcast:31 row_mask:0xc bank_mask:0xf\n"
"s_nop 1\n"
"v_readlane_b32 s62,v229,63\n"
"s_nop 1\n"
"v_cmp_eq_u32 vcc,s62,v160\n"
"s_nop 1\n"
"v_cndmask_b32_e64 v160,v160,0,vcc\n"
"v_cmp_eq_u32 vcc,s62,v161\n"
"s_nop 1\n"
"v_cndmask_b32_e64 v161,v161,0,vcc\n"
"v_cmp_eq_u32 vcc,s62,v162\n"
"s_nop 1\n"
"v_cndmask_b32_e64 v162,v162,0,vcc\n"
"v_cmp_eq_u32 vcc,s62,v163\n"
"s_nop 1\n"
"v_cndmask_b32_e64 v163,v163,0,vcc\n"
"v_cmp_eq_u32 vcc,s62,v164\n"
"s_nop 1\n"
"v_cndmask_b32_e64 v164,v164,0,vcc\n"
"v_cmp_eq_u32 vcc,s62,v165\n"
"s_nop 1\n"
"v_cndmask_b32_e64 v165,v165,0,vcc\n"
"v_cmp_eq_u32 vcc,s62,v166\n"
"s_nop 1\n"
"v_cndmask_b32_e64 v166,v166,0,vcc\n"
"v_cmp_eq_u32 vcc,s62,v167\n"
"s_nop 1\n"
"v_cndmask_b32_e64 v167,v167,0,vcc\n"
"v_cmp_eq_u32 vcc,s62,v168\n"
"s_nop 1\n"
"v_cndmask_b32_e64 v168,v168,0,vcc\n"
"v_cmp_eq_u32 vcc,s62,v169\n"
"s_nop 1\n"
"v_cndmask_b32_e64 v169,v169,0,vcc\n"
"v_cmp_eq_u32 vcc,s62,v170\n"
"s_nop 1\n"
"v_cndmask_b32_e64 v170,v170,0,vcc\n"
"v_cmp_eq_u32 vcc,s62,v171\n"
"s_nop 1\n"
"v_cndmask_b32_e64 v171,v171,0,vcc\n"
"v_cmp_eq_u32 vcc,s62,v172\n"
"s_nop 1\n"
"v_cndmask_b32_e64 v172,v172,0,vcc\n"
"v_cmp_eq_u32 vcc,s62,v173\n"
"s_nop 1\n"
"v_cndmask_b32_e64 v173,v173,0,vcc\n"
"v_cmp_eq_u32 vcc,s62,v174\n"
"s_nop 1\n"
"v_cndmask_b32_e64 v174,v174,0,vcc\n"
"v_cmp_eq_u32 vcc,s62,v175\n"
"s_nop 1\n"
"v_cndmask_b32_e64 v175,v175,0,vcc\n"
"v_cmp_eq_u32 vcc,s62,v176\n"
"s_nop 1\n"
"v_cndmask_b32_e64 v176,v176,0,vcc\n"
"v_cmp_eq_u32 vcc,s62,v177\n"
"s_nop 1\n"
"v_cndmask_b32_e64 v177,v177,0,vcc\n"
"v_cmp_eq_u32 vcc,s62,v178\n"
"s_nop 1\n"
"v_cndmask_b32_e64 v178,v178,0,vcc\n"
"v_cmp_eq_u32 vcc,s62,v179\n"
"s_nop 1\n"
"v_cndmask_b32_e64 v179,v179,0,vcc\n"
"v_cmp_eq_u32 vcc,s62,v180\n"
"s_nop 1\n"
"v_cndmask_b32_e64 v180,v180,0,vcc\n"
"v_cmp_eq_u32 vcc,s62,v181\n"
"s_nop 1\n"
"v_cndmask_b32_e64 v181,v181,0,vcc\n"
"v_cmp_eq_u32 vcc,s62,v182\n"
"s_nop 1\n"
"v_cndmask_b32_e64 v182,v182,0,vcc\n"
"v_cmp_eq_u32 vcc,s62,v183\n"
"s_nop 1\n"
"v_cndmask_b32_e64 v183,v183,0,vcc\n"
"v_cmp_eq_u32 vcc,s62,v184\n"
"s_nop 1\n"
"v_cndmask_b32_e64 v184,v184,0,vcc\n"
"v_cmp_eq_u32 vcc,s62,v185\n"
"s_nop 1\n"
"v_cndmask_b32_e64 v185,v185,0,vcc\n"
"v_cmp_eq_u32 vcc,s62,v186\n"
"s_nop 1\n"
"v_cndmask_b32_e64 v186,v186,0,vcc\n"
"v_cmp_eq_u32 vcc,s62,v187\n"
"s_nop 1\n"
"v_cndmask_b32_e64 v187,v187,0,vcc\n"
"v_cmp_eq_u32 vcc,s62,v188\n"
"s_nop 1\n"
"v_cndmask_b32_e64 v188,v188,0,vcc\n"
"v_cmp_eq_u32 vcc,s62,v189\n"
"s_nop 1\n"
"v_cndmask_b32_e64 v189,v189,0,vcc\n"
"v_cmp_eq_u32 vcc,s62,v190\n"
"s_nop 1\n"
"v_cndmask_b32_e64 v190,v190,0,vcc\n"
"v_cmp_eq_u32 vcc,s62,v191\n"
"s_nop 1\n"
"v_cndmask_b32_e64 v191,v191,0,vcc\n"
"v_mov_b32 v229,s62\n"
"s_lshl_b64 exec,1,s54\n"
"v_mov_b32 v225,v229\n"
"s_mov_b64 exec,-1\n"
SA"s54,s54,1\n"
"s_cmp_lt_u32 s54,8\n"
"s_cbranch_scc1 L1009_%=\n"
"s_nop 4\n"
"s_branch L1008_%=\n"
"L1010_%=:\n"
"s_mov_b32 s54,0\n"
"L1012_%=:\n"
"s_sleep 2\n"
"buffer_load_dwordx2 v[208:209],v252,s[8:11],s50 offen sc1\n"
WT"vmcnt(0)\n"
SA"s54,s54,1\n"
"v_cmp_eq_u64 vcc,s[28:29],v[208:209]\n"
"s_cmp_eq_u64 vcc,exec\n"
"s_cbranch_scc1 L1011_%=\n"
"s_cmp_lt_u32 s54,1048576\n"
"s_cbranch_scc1 L1012_%=\n"
"s_branch L1011_%=\n"
"L1014_%=:\n"
"s_nop 15\n"
WT"vmcnt(0)\n"
"s_lshl_b32 s89,s34,3\n"
SA"s89,s89,s30\n"
SA"s89,s89,0\n"
"s_lshl_b32 s91,s89,4\n"
"s_lshr_b32 s89,s89,1\n"
"v_bfe_u32 v249,v224,4,4\n"
"v_lshrrev_b32 v254,8,v224\n"
"v_lshlrev_b32 v254,2,v254\n"
"v_sub_u32 v249,v249,v254\n"
"v_add_u32 v249,s91,v249\n"
"v_mov_b32 v255,0xff800000\n"
"v_mov_b32 v248,v255\n"
"s_mov_b32 s90,0\n"
"s_mov_b32 s88,0\n"
"v_mov_b32 v80,0\n"
"v_mov_b32 v81,0\n"
"v_mov_b32 v82,0\n"
"v_mov_b32 v83,0\n"
"v_mov_b32 v84,0\n"
"v_mov_b32 v85,0\n"
"v_mov_b32 v86,0\n"
"v_mov_b32 v87,0\n"
"v_mov_b32 v88,0\n"
"v_mov_b32 v89,0\n"
"v_mov_b32 v90,0\n"
"v_mov_b32 v91,0\n"
"v_mov_b32 v92,0\n"
"v_mov_b32 v93,0\n"
"v_mov_b32 v94,0\n"
"v_mov_b32 v95,0\n"
"v_mov_b32 v96,0\n"
"v_mov_b32 v97,0\n"
"v_mov_b32 v98,0\n"
"v_mov_b32 v99,0\n"
"v_mov_b32 v100,0\n"
"v_mov_b32 v101,0\n"
"v_mov_b32 v102,0\n"
"v_mov_b32 v103,0\n"
"v_mov_b32 v104,0\n"
"v_mov_b32 v105,0\n"
"v_mov_b32 v106,0\n"
"v_mov_b32 v107,0\n"
"v_mov_b32 v108,0\n"
"v_mov_b32 v109,0\n"
"v_mov_b32 v110,0\n"
"v_mov_b32 v111,0\n"
"v_accvgpr_write_b32 a132,0\n"
"v_accvgpr_write_b32 a133,0\n"
"v_accvgpr_write_b32 a134,0\n"
"v_accvgpr_write_b32 a135,0\n"
"L1015_%=:\n"
"s_lshl_b32 s91,s88,13\n"
SA"s91,s91,s39\n"
SA"s91,s91,0\n"
BL"a[148:151],v224,s[8:11],s91 offen offset:0 sc1\n"
BL"a[152:155],v224,s[8:11],s91 offen offset:1024 sc1\n"
BL"a[156:159],v224,s[8:11],s91 offen offset:2048 sc1\n"
BL"a[160:163],v224,s[8:11],s91 offen offset:3072 sc1\n"
SA"s91,s91,4096\n"
BL"a[164:167],v224,s[8:11],s91 offen offset:0 sc1\n"
BL"a[168:171],v224,s[8:11],s91 offen offset:1024 sc1\n"
BL"a[172:175],v224,s[8:11],s91 offen offset:2048 sc1\n"
BL"a[176:179],v224,s[8:11],s91 offen offset:3072 sc1\n"
WT"vmcnt(0)\n"
MF"v[240:243],a[148:151],a[0:3],0\n"
MF"v[240:243],a[152:155],a[4:7],v[240:243]\n"
MF"v[240:243],a[156:159],a[8:11],v[240:243]\n"
MF"v[240:243],a[160:163],a[12:15],v[240:243]\n"
MF"v[244:247],a[164:167],a[0:3],0\n"
MF"v[244:247],a[168:171],a[4:7],v[244:247]\n"
MF"v[244:247],a[172:175],a[8:11],v[244:247]\n"
MF"v[244:247],a[176:179],a[12:15],v[244:247]\n"
"s_lshl_b32 s91,s88,5\n"
"v_subrev_u32 v250,s91,v249\n"
"s_nop 7\n"
"v_cmp_gt_i32 vcc,0,v250\n"
"s_nop 1\n"
"v_cndmask_b32 v240,v240,v255,vcc\n"
"v_cmp_gt_i32 vcc,1,v250\n"
"s_nop 1\n"
"v_cndmask_b32 v241,v241,v255,vcc\n"
"v_cmp_gt_i32 vcc,2,v250\n"
"s_nop 1\n"
"v_cndmask_b32 v242,v242,v255,vcc\n"
"v_cmp_gt_i32 vcc,3,v250\n"
"s_nop 1\n"
"v_cndmask_b32 v243,v243,v255,vcc\n"
"v_cmp_gt_i32 vcc,16,v250\n"
"s_nop 1\n"
"v_cndmask_b32 v244,v244,v255,vcc\n"
"v_cmp_gt_i32 vcc,17,v250\n"
"s_nop 1\n"
"v_cndmask_b32 v245,v245,v255,vcc\n"
"v_cmp_gt_i32 vcc,18,v250\n"
"s_nop 1\n"
"v_cndmask_b32 v246,v246,v255,vcc\n"
"v_cmp_gt_i32 vcc,19,v250\n"
"s_nop 1\n"
"v_cndmask_b32 v247,v247,v255,vcc\n"
"s_cmp_lg_u32 s90,0\n"
"s_cbranch_scc1 L1016_%=\n"
"v_max3_f32 v248,v248,v240,v241\n"
"v_max3_f32 v248,v248,v242,v243\n"
"v_max3_f32 v248,v248,v244,v245\n"
"v_max3_f32 v248,v248,v246,v247\n"
"s_branch L1017_%=\n"
"L1016_%=:\n"
"s_lshl_b32 s91,s88,13\n"
SA"s91,s91,s39\n"
SA"s91,s91,524288\n"
BL"a[148:151],v224,s[8:11],s91 offen offset:0 sc1\n"
BL"a[152:155],v224,s[8:11],s91 offen offset:1024 sc1\n"
BL"a[156:159],v224,s[8:11],s91 offen offset:2048 sc1\n"
BL"a[160:163],v224,s[8:11],s91 offen offset:3072 sc1\n"
SA"s91,s91,4096\n"
BL"a[164:167],v224,s[8:11],s91 offen offset:0 sc1\n"
BL"a[168:171],v224,s[8:11],s91 offen offset:1024 sc1\n"
BL"a[172:175],v224,s[8:11],s91 offen offset:2048 sc1\n"
BL"a[176:179],v224,s[8:11],s91 offen offset:3072 sc1\n"
WT"vmcnt(0)\n"
"v_sub_f32 v240,v240,v248\n"
"v_sub_f32 v241,v241,v248\n"
"v_sub_f32 v242,v242,v248\n"
"v_sub_f32 v243,v243,v248\n"
"v_sub_f32 v244,v244,v248\n"
"v_sub_f32 v245,v245,v248\n"
"v_sub_f32 v246,v246,v248\n"
"v_sub_f32 v247,v247,v248\n"
EX"v240,v240\n"
EX"v241,v241\n"
EX"v242,v242\n"
EX"v243,v243\n"
EX"v244,v244\n"
EX"v245,v245\n"
EX"v246,v246\n"
EX"v247,v247\n"
"s_nop 0\n"
PK"v240,v240,v241\n"
PK"v241,v242,v243\n"
PK"v242,v244,v245\n"
PK"v243,v246,v247\n"
"s_nop 1\n"
MF"v[80:83],a[148:151],v[240:243],v[80:83]\n"
MF"v[84:87],a[152:155],v[240:243],v[84:87]\n"
MF"v[88:91],a[156:159],v[240:243],v[88:91]\n"
MF"v[92:95],a[160:163],v[240:243],v[92:95]\n"
MF"v[96:99],a[164:167],v[240:243],v[96:99]\n"
MF"v[100:103],a[168:171],v[240:243],v[100:103]\n"
MF"v[104:107],a[172:175],v[240:243],v[104:107]\n"
MF"v[108:111],a[176:179],v[240:243],v[108:111]\n"
MF"a[132:135],a[128:131],v[240:243],a[132:135]\n"
"s_nop 7\n"
"L1017_%=:\n"
SA"s88,s88,1\n"
"s_cmp_le_u32 s88,s89\n"
"s_cbranch_scc1 L1015_%=\n"
"s_cmp_lg_u32 s90,0\n"
"s_cbranch_scc1 L1018_%=\n"
"v_mov_b32 v254,v248\n"
"s_nop 1\n"
"v_permlane16_swap_b32 v254,v248\n"
"s_nop 1\n"
"v_max_f32 v248,v248,v254\n"
"v_mov_b32 v254,v248\n"
"s_nop 1\n"
"v_permlane32_swap_b32 v254,v248\n"
"s_nop 1\n"
"v_max_f32 v248,v248,v254\n"
"s_mov_b32 s90,1\n"
"s_mov_b32 s88,0\n"
"s_branch L1015_%=\n"
"L1018_%=:\n"
"s_nop 7\n"
"v_accvgpr_read_b32 v251,a132\n"
"s_nop 1\n"
"s_branch L1013_%=\n"
"L1019_%=:\n"
"s_mov_b32 s54,0\n"
"L1021_%=:\n"
"s_sleep 2\n"
"buffer_load_dwordx2 v[238:239],v237,s[8:11],s50 offen sc1\n"
WT"vmcnt(0)\n"
SA"s54,s54,1\n"
"v_cmp_eq_u64 vcc,s[28:29],v[238:239]\n"
"s_cmp_eq_u64 vcc,exec\n"
"s_cbranch_scc1 L1020_%=\n"
"s_cmp_lt_u32 s54,1048576\n"
"s_cbranch_scc1 L1021_%=\n"
"s_branch L1020_%=\n"
"L1023_%=:\n"
"s_nop 15\n"
WT"vmcnt(0)\n"
"s_lshl_b32 s89,s34,3\n"
SA"s89,s89,s30\n"
SA"s89,s89,4\n"
"s_lshl_b32 s91,s89,4\n"
"s_lshr_b32 s89,s89,1\n"
"v_bfe_u32 v249,v224,4,4\n"
"v_lshrrev_b32 v254,8,v224\n"
"v_lshlrev_b32 v254,2,v254\n"
"v_sub_u32 v249,v249,v254\n"
"v_add_u32 v249,s91,v249\n"
"v_mov_b32 v255,0xff800000\n"
"v_mov_b32 v248,v255\n"
"s_mov_b32 s90,0\n"
"s_mov_b32 s88,0\n"
"v_mov_b32 v112,0\n"
"v_mov_b32 v113,0\n"
"v_mov_b32 v114,0\n"
"v_mov_b32 v115,0\n"
"v_mov_b32 v116,0\n"
"v_mov_b32 v117,0\n"
"v_mov_b32 v118,0\n"
"v_mov_b32 v119,0\n"
"v_mov_b32 v120,0\n"
"v_mov_b32 v121,0\n"
"v_mov_b32 v122,0\n"
"v_mov_b32 v123,0\n"
"v_mov_b32 v124,0\n"
"v_mov_b32 v125,0\n"
"v_mov_b32 v126,0\n"
"v_mov_b32 v127,0\n"
"v_mov_b32 v128,0\n"
"v_mov_b32 v129,0\n"
"v_mov_b32 v130,0\n"
"v_mov_b32 v131,0\n"
"v_mov_b32 v132,0\n"
"v_mov_b32 v133,0\n"
"v_mov_b32 v134,0\n"
"v_mov_b32 v135,0\n"
"v_mov_b32 v136,0\n"
"v_mov_b32 v137,0\n"
"v_mov_b32 v138,0\n"
"v_mov_b32 v139,0\n"
"v_mov_b32 v140,0\n"
"v_mov_b32 v141,0\n"
"v_mov_b32 v142,0\n"
"v_mov_b32 v143,0\n"
"v_accvgpr_write_b32 a136,0\n"
"v_accvgpr_write_b32 a137,0\n"
"v_accvgpr_write_b32 a138,0\n"
"v_accvgpr_write_b32 a139,0\n"
"L1024_%=:\n"
"s_lshl_b32 s91,s88,13\n"
SA"s91,s91,s39\n"
SA"s91,s91,0\n"
BL"a[148:151],v224,s[8:11],s91 offen offset:0 sc1\n"
BL"a[152:155],v224,s[8:11],s91 offen offset:1024 sc1\n"
BL"a[156:159],v224,s[8:11],s91 offen offset:2048 sc1\n"
BL"a[160:163],v224,s[8:11],s91 offen offset:3072 sc1\n"
SA"s91,s91,4096\n"
BL"a[164:167],v224,s[8:11],s91 offen offset:0 sc1\n"
BL"a[168:171],v224,s[8:11],s91 offen offset:1024 sc1\n"
BL"a[172:175],v224,s[8:11],s91 offen offset:2048 sc1\n"
BL"a[176:179],v224,s[8:11],s91 offen offset:3072 sc1\n"
WT"vmcnt(0)\n"
MF"v[240:243],a[148:151],a[16:19],0\n"
MF"v[240:243],a[152:155],a[20:23],v[240:243]\n"
MF"v[240:243],a[156:159],a[24:27],v[240:243]\n"
MF"v[240:243],a[160:163],a[28:31],v[240:243]\n"
MF"v[244:247],a[164:167],a[16:19],0\n"
MF"v[244:247],a[168:171],a[20:23],v[244:247]\n"
MF"v[244:247],a[172:175],a[24:27],v[244:247]\n"
MF"v[244:247],a[176:179],a[28:31],v[244:247]\n"
"s_lshl_b32 s91,s88,5\n"
"v_subrev_u32 v250,s91,v249\n"
"s_nop 7\n"
"v_cmp_gt_i32 vcc,0,v250\n"
"s_nop 1\n"
"v_cndmask_b32 v240,v240,v255,vcc\n"
"v_cmp_gt_i32 vcc,1,v250\n"
"s_nop 1\n"
"v_cndmask_b32 v241,v241,v255,vcc\n"
"v_cmp_gt_i32 vcc,2,v250\n"
"s_nop 1\n"
"v_cndmask_b32 v242,v242,v255,vcc\n"
"v_cmp_gt_i32 vcc,3,v250\n"
"s_nop 1\n"
"v_cndmask_b32 v243,v243,v255,vcc\n"
"v_cmp_gt_i32 vcc,16,v250\n"
"s_nop 1\n"
"v_cndmask_b32 v244,v244,v255,vcc\n"
"v_cmp_gt_i32 vcc,17,v250\n"
"s_nop 1\n"
"v_cndmask_b32 v245,v245,v255,vcc\n"
"v_cmp_gt_i32 vcc,18,v250\n"
"s_nop 1\n"
"v_cndmask_b32 v246,v246,v255,vcc\n"
"v_cmp_gt_i32 vcc,19,v250\n"
"s_nop 1\n"
"v_cndmask_b32 v247,v247,v255,vcc\n"
"s_cmp_lg_u32 s90,0\n"
"s_cbranch_scc1 L1025_%=\n"
"v_max3_f32 v248,v248,v240,v241\n"
"v_max3_f32 v248,v248,v242,v243\n"
"v_max3_f32 v248,v248,v244,v245\n"
"v_max3_f32 v248,v248,v246,v247\n"
"s_branch L1026_%=\n"
"L1025_%=:\n"
"s_lshl_b32 s91,s88,13\n"
SA"s91,s91,s39\n"
SA"s91,s91,524288\n"
BL"a[148:151],v224,s[8:11],s91 offen offset:0 sc1\n"
BL"a[152:155],v224,s[8:11],s91 offen offset:1024 sc1\n"
BL"a[156:159],v224,s[8:11],s91 offen offset:2048 sc1\n"
BL"a[160:163],v224,s[8:11],s91 offen offset:3072 sc1\n"
SA"s91,s91,4096\n"
BL"a[164:167],v224,s[8:11],s91 offen offset:0 sc1\n"
BL"a[168:171],v224,s[8:11],s91 offen offset:1024 sc1\n"
BL"a[172:175],v224,s[8:11],s91 offen offset:2048 sc1\n"
BL"a[176:179],v224,s[8:11],s91 offen offset:3072 sc1\n"
WT"vmcnt(0)\n"
"v_sub_f32 v240,v240,v248\n"
"v_sub_f32 v241,v241,v248\n"
"v_sub_f32 v242,v242,v248\n"
"v_sub_f32 v243,v243,v248\n"
"v_sub_f32 v244,v244,v248\n"
"v_sub_f32 v245,v245,v248\n"
"v_sub_f32 v246,v246,v248\n"
"v_sub_f32 v247,v247,v248\n"
EX"v240,v240\n"
EX"v241,v241\n"
EX"v242,v242\n"
EX"v243,v243\n"
EX"v244,v244\n"
EX"v245,v245\n"
EX"v246,v246\n"
EX"v247,v247\n"
"s_nop 0\n"
PK"v240,v240,v241\n"
PK"v241,v242,v243\n"
PK"v242,v244,v245\n"
PK"v243,v246,v247\n"
"s_nop 1\n"
MF"v[112:115],a[148:151],v[240:243],v[112:115]\n"
MF"v[116:119],a[152:155],v[240:243],v[116:119]\n"
MF"v[120:123],a[156:159],v[240:243],v[120:123]\n"
MF"v[124:127],a[160:163],v[240:243],v[124:127]\n"
MF"v[128:131],a[164:167],v[240:243],v[128:131]\n"
MF"v[132:135],a[168:171],v[240:243],v[132:135]\n"
MF"v[136:139],a[172:175],v[240:243],v[136:139]\n"
MF"v[140:143],a[176:179],v[240:243],v[140:143]\n"
MF"a[136:139],a[128:131],v[240:243],a[136:139]\n"
"s_nop 7\n"
"L1026_%=:\n"
SA"s88,s88,1\n"
"s_cmp_le_u32 s88,s89\n"
"s_cbranch_scc1 L1024_%=\n"
"s_cmp_lg_u32 s90,0\n"
"s_cbranch_scc1 L1027_%=\n"
"v_mov_b32 v254,v248\n"
"s_nop 1\n"
"v_permlane16_swap_b32 v254,v248\n"
"s_nop 1\n"
"v_max_f32 v248,v248,v254\n"
"v_mov_b32 v254,v248\n"
"s_nop 1\n"
"v_permlane32_swap_b32 v254,v248\n"
"s_nop 1\n"
"v_max_f32 v248,v248,v254\n"
"s_mov_b32 s90,1\n"
"s_mov_b32 s88,0\n"
"s_branch L1024_%=\n"
"L1027_%=:\n"
"s_nop 7\n"
"v_accvgpr_read_b32 v252,a136\n"
"s_nop 1\n"
"s_branch L1022_%=\n"
"L1028_%=:\n"
"s_mov_b32 s54,0\n"
"L1030_%=:\n"
"s_sleep 2\n"
"buffer_load_dwordx2 v[242:243],v236,s[8:11],0 offen sc1\n"
WT"vmcnt(0)\n"
SA"s54,s54,1\n"
"v_cmp_eq_u64 vcc,s[28:29],v[242:243]\n"
"s_or_b32 vcc_hi,vcc_hi,-1\n"
"s_cmp_eq_u64 vcc,exec\n"
"s_cbranch_scc1 L1029_%=\n"
"s_cmp_lt_u32 s54,1048576\n"
"s_cbranch_scc1 L1030_%=\n"
"s_branch L1029_%=\n"
"L1031_%=:\n"
"s_mov_b32 s54,0\n"
"L1033_%=:\n"
"s_sleep 2\n"
"buffer_load_dwordx2 v[242:243],v236,s[8:11],0 offen sc1\n"
WT"vmcnt(0)\n"
SA"s54,s54,1\n"
"v_cmp_eq_u64 vcc,s[28:29],v[242:243]\n"
"s_or_b32 vcc_lo,vcc_lo,-1\n"
"s_cmp_eq_u64 vcc,exec\n"
"s_cbranch_scc1 L1032_%=\n"
"s_cmp_lt_u32 s54,1048576\n"
"s_cbranch_scc1 L1033_%=\n"
"s_branch L1032_%=\n"
"L1035_%=:\n"
"s_nop 15\n"
WT"vmcnt(0)\n"
"s_lshl_b32 s89,s34,3\n"
SA"s89,s89,s30\n"
SA"s89,s89,4\n"
"s_lshl_b32 s91,s89,4\n"
"s_lshr_b32 s89,s89,1\n"
"v_bfe_u32 v249,v224,4,4\n"
"v_lshrrev_b32 v254,8,v224\n"
"v_lshlrev_b32 v254,2,v254\n"
"v_sub_u32 v249,v249,v254\n"
"v_add_u32 v249,s91,v249\n"
"v_mov_b32 v255,0xff800000\n"
"v_mov_b32 v248,v255\n"
"s_mov_b32 s90,0\n"
"s_mov_b32 s88,0\n"
"v_mov_b32 v112,0\n"
"v_mov_b32 v113,0\n"
"v_mov_b32 v114,0\n"
"v_mov_b32 v115,0\n"
"v_mov_b32 v116,0\n"
"v_mov_b32 v117,0\n"
"v_mov_b32 v118,0\n"
"v_mov_b32 v119,0\n"
"v_mov_b32 v120,0\n"
"v_mov_b32 v121,0\n"
"v_mov_b32 v122,0\n"
"v_mov_b32 v123,0\n"
"v_mov_b32 v124,0\n"
"v_mov_b32 v125,0\n"
"v_mov_b32 v126,0\n"
"v_mov_b32 v127,0\n"
"v_mov_b32 v128,0\n"
"v_mov_b32 v129,0\n"
"v_mov_b32 v130,0\n"
"v_mov_b32 v131,0\n"
"v_mov_b32 v132,0\n"
"v_mov_b32 v133,0\n"
"v_mov_b32 v134,0\n"
"v_mov_b32 v135,0\n"
"v_mov_b32 v136,0\n"
"v_mov_b32 v137,0\n"
"v_mov_b32 v138,0\n"
"v_mov_b32 v139,0\n"
"v_mov_b32 v140,0\n"
"v_mov_b32 v141,0\n"
"v_mov_b32 v142,0\n"
"v_mov_b32 v143,0\n"
"v_accvgpr_write_b32 a136,0\n"
"v_accvgpr_write_b32 a137,0\n"
"v_accvgpr_write_b32 a138,0\n"
"v_accvgpr_write_b32 a139,0\n"
"L1036_%=:\n"
"s_lshl_b32 s91,s88,13\n"
SA"s91,s91,s39\n"
SA"s91,s91,0\n"
BL"a[148:151],v224,s[8:11],s91 offen offset:0 sc1\n"
BL"a[152:155],v224,s[8:11],s91 offen offset:1024 sc1\n"
BL"a[156:159],v224,s[8:11],s91 offen offset:2048 sc1\n"
BL"a[160:163],v224,s[8:11],s91 offen offset:3072 sc1\n"
SA"s91,s91,4096\n"
BL"a[164:167],v224,s[8:11],s91 offen offset:0 sc1\n"
BL"a[168:171],v224,s[8:11],s91 offen offset:1024 sc1\n"
BL"a[172:175],v224,s[8:11],s91 offen offset:2048 sc1\n"
BL"a[176:179],v224,s[8:11],s91 offen offset:3072 sc1\n"
WT"vmcnt(0)\n"
MF"v[240:243],a[148:151],a[16:19],0\n"
MF"v[240:243],a[152:155],a[20:23],v[240:243]\n"
MF"v[240:243],a[156:159],a[24:27],v[240:243]\n"
MF"v[240:243],a[160:163],a[28:31],v[240:243]\n"
MF"v[244:247],a[164:167],a[16:19],0\n"
MF"v[244:247],a[168:171],a[20:23],v[244:247]\n"
MF"v[244:247],a[172:175],a[24:27],v[244:247]\n"
MF"v[244:247],a[176:179],a[28:31],v[244:247]\n"
"s_lshl_b32 s91,s88,5\n"
"v_subrev_u32 v250,s91,v249\n"
"s_nop 7\n"
"v_cmp_gt_i32 vcc,0,v250\n"
"s_nop 1\n"
"v_cndmask_b32 v240,v240,v255,vcc\n"
"v_cmp_gt_i32 vcc,1,v250\n"
"s_nop 1\n"
"v_cndmask_b32 v241,v241,v255,vcc\n"
"v_cmp_gt_i32 vcc,2,v250\n"
"s_nop 1\n"
"v_cndmask_b32 v242,v242,v255,vcc\n"
"v_cmp_gt_i32 vcc,3,v250\n"
"s_nop 1\n"
"v_cndmask_b32 v243,v243,v255,vcc\n"
"v_cmp_gt_i32 vcc,16,v250\n"
"s_nop 1\n"
"v_cndmask_b32 v244,v244,v255,vcc\n"
"v_cmp_gt_i32 vcc,17,v250\n"
"s_nop 1\n"
"v_cndmask_b32 v245,v245,v255,vcc\n"
"v_cmp_gt_i32 vcc,18,v250\n"
"s_nop 1\n"
"v_cndmask_b32 v246,v246,v255,vcc\n"
"v_cmp_gt_i32 vcc,19,v250\n"
"s_nop 1\n"
"v_cndmask_b32 v247,v247,v255,vcc\n"
"s_cmp_lg_u32 s90,0\n"
"s_cbranch_scc1 L1037_%=\n"
"v_max3_f32 v248,v248,v240,v241\n"
"v_max3_f32 v248,v248,v242,v243\n"
"v_max3_f32 v248,v248,v244,v245\n"
"v_max3_f32 v248,v248,v246,v247\n"
"s_branch L1038_%=\n"
"L1037_%=:\n"
"s_lshl_b32 s91,s88,13\n"
SA"s91,s91,s39\n"
SA"s91,s91,524288\n"
BL"a[148:151],v224,s[8:11],s91 offen offset:0 sc1\n"
BL"a[152:155],v224,s[8:11],s91 offen offset:1024 sc1\n"
BL"a[156:159],v224,s[8:11],s91 offen offset:2048 sc1\n"
BL"a[160:163],v224,s[8:11],s91 offen offset:3072 sc1\n"
SA"s91,s91,4096\n"
BL"a[164:167],v224,s[8:11],s91 offen offset:0 sc1\n"
BL"a[168:171],v224,s[8:11],s91 offen offset:1024 sc1\n"
BL"a[172:175],v224,s[8:11],s91 offen offset:2048 sc1\n"
BL"a[176:179],v224,s[8:11],s91 offen offset:3072 sc1\n"
WT"vmcnt(0)\n"
"v_sub_f32 v240,v240,v248\n"
"v_sub_f32 v241,v241,v248\n"
"v_sub_f32 v242,v242,v248\n"
"v_sub_f32 v243,v243,v248\n"
"v_sub_f32 v244,v244,v248\n"
"v_sub_f32 v245,v245,v248\n"
"v_sub_f32 v246,v246,v248\n"
"v_sub_f32 v247,v247,v248\n"
EX"v240,v240\n"
EX"v241,v241\n"
EX"v242,v242\n"
EX"v243,v243\n"
EX"v244,v244\n"
EX"v245,v245\n"
EX"v246,v246\n"
EX"v247,v247\n"
"s_nop 0\n"
PK"v240,v240,v241\n"
PK"v241,v242,v243\n"
PK"v242,v244,v245\n"
PK"v243,v246,v247\n"
"s_nop 1\n"
MF"v[112:115],a[148:151],v[240:243],v[112:115]\n"
MF"v[116:119],a[152:155],v[240:243],v[116:119]\n"
MF"v[120:123],a[156:159],v[240:243],v[120:123]\n"
MF"v[124:127],a[160:163],v[240:243],v[124:127]\n"
MF"v[128:131],a[164:167],v[240:243],v[128:131]\n"
MF"v[132:135],a[168:171],v[240:243],v[132:135]\n"
MF"v[136:139],a[172:175],v[240:243],v[136:139]\n"
MF"v[140:143],a[176:179],v[240:243],v[140:143]\n"
MF"a[136:139],a[128:131],v[240:243],a[136:139]\n"
"s_nop 7\n"
"L1038_%=:\n"
SA"s88,s88,1\n"
"s_cmp_le_u32 s88,s89\n"
"s_cbranch_scc1 L1036_%=\n"
"s_cmp_lg_u32 s90,0\n"
"s_cbranch_scc1 L1039_%=\n"
"v_mov_b32 v254,v248\n"
"s_nop 1\n"
"v_permlane16_swap_b32 v254,v248\n"
"s_nop 1\n"
"v_max_f32 v248,v248,v254\n"
"v_mov_b32 v254,v248\n"
"s_nop 1\n"
"v_permlane32_swap_b32 v254,v248\n"
"s_nop 1\n"
"v_max_f32 v248,v248,v254\n"
"s_mov_b32 s90,1\n"
"s_mov_b32 s88,0\n"
"s_branch L1036_%=\n"
"L1039_%=:\n"
"s_nop 7\n"
"v_accvgpr_read_b32 v252,a136\n"
"s_nop 1\n"
"s_branch L1034_%=\n"
"L1041_%=:\n"
"s_nop 15\n"
WT"vmcnt(0)\n"
"s_lshl_b32 s89,s34,3\n"
"s_sub_u32 s89,120,s89\n"
SA"s89,s89,s30\n"
SA"s89,s89,0\n"
"s_lshl_b32 s91,s89,4\n"
"s_lshr_b32 s89,s89,1\n"
"v_bfe_u32 v249,v224,4,4\n"
"v_lshrrev_b32 v254,8,v224\n"
"v_lshlrev_b32 v254,2,v254\n"
"v_sub_u32 v249,v249,v254\n"
"v_add_u32 v249,s91,v249\n"
"v_mov_b32 v255,0xff800000\n"
"v_mov_b32 v248,v255\n"
"s_mov_b32 s90,0\n"
"s_mov_b32 s88,0\n"
"v_mov_b32 v144,0\n"
"v_mov_b32 v145,0\n"
"v_mov_b32 v146,0\n"
"v_mov_b32 v147,0\n"
"v_mov_b32 v148,0\n"
"v_mov_b32 v149,0\n"
"v_mov_b32 v150,0\n"
"v_mov_b32 v151,0\n"
"v_mov_b32 v152,0\n"
"v_mov_b32 v153,0\n"
"v_mov_b32 v154,0\n"
"v_mov_b32 v155,0\n"
"v_mov_b32 v156,0\n"
"v_mov_b32 v157,0\n"
"v_mov_b32 v158,0\n"
"v_mov_b32 v159,0\n"
"v_mov_b32 v160,0\n"
"v_mov_b32 v161,0\n"
"v_mov_b32 v162,0\n"
"v_mov_b32 v163,0\n"
"v_mov_b32 v164,0\n"
"v_mov_b32 v165,0\n"
"v_mov_b32 v166,0\n"
"v_mov_b32 v167,0\n"
"v_mov_b32 v168,0\n"
"v_mov_b32 v169,0\n"
"v_mov_b32 v170,0\n"
"v_mov_b32 v171,0\n"
"v_mov_b32 v172,0\n"
"v_mov_b32 v173,0\n"
"v_mov_b32 v174,0\n"
"v_mov_b32 v175,0\n"
"v_accvgpr_write_b32 a140,0\n"
"v_accvgpr_write_b32 a141,0\n"
"v_accvgpr_write_b32 a142,0\n"
"v_accvgpr_write_b32 a143,0\n"
"L1042_%=:\n"
"s_lshl_b32 s91,s88,13\n"
SA"s91,s91,s39\n"
SA"s91,s91,0\n"
BL"a[148:151],v224,s[8:11],s91 offen offset:0 sc1\n"
BL"a[152:155],v224,s[8:11],s91 offen offset:1024 sc1\n"
BL"a[156:159],v224,s[8:11],s91 offen offset:2048 sc1\n"
BL"a[160:163],v224,s[8:11],s91 offen offset:3072 sc1\n"
SA"s91,s91,4096\n"
BL"a[164:167],v224,s[8:11],s91 offen offset:0 sc1\n"
BL"a[168:171],v224,s[8:11],s91 offen offset:1024 sc1\n"
BL"a[172:175],v224,s[8:11],s91 offen offset:2048 sc1\n"
BL"a[176:179],v224,s[8:11],s91 offen offset:3072 sc1\n"
WT"vmcnt(0)\n"
MF"v[240:243],a[148:151],a[32:35],0\n"
MF"v[240:243],a[152:155],a[36:39],v[240:243]\n"
MF"v[240:243],a[156:159],a[40:43],v[240:243]\n"
MF"v[240:243],a[160:163],a[44:47],v[240:243]\n"
MF"v[244:247],a[164:167],a[32:35],0\n"
MF"v[244:247],a[168:171],a[36:39],v[244:247]\n"
MF"v[244:247],a[172:175],a[40:43],v[244:247]\n"
MF"v[244:247],a[176:179],a[44:47],v[244:247]\n"
"s_lshl_b32 s91,s88,5\n"
"v_subrev_u32 v250,s91,v249\n"
"s_nop 7\n"
"v_cmp_gt_i32 vcc,0,v250\n"
"s_nop 1\n"
"v_cndmask_b32 v240,v240,v255,vcc\n"
"v_cmp_gt_i32 vcc,1,v250\n"
"s_nop 1\n"
"v_cndmask_b32 v241,v241,v255,vcc\n"
"v_cmp_gt_i32 vcc,2,v250\n"
"s_nop 1\n"
"v_cndmask_b32 v242,v242,v255,vcc\n"
"v_cmp_gt_i32 vcc,3,v250\n"
"s_nop 1\n"
"v_cndmask_b32 v243,v243,v255,vcc\n"
"v_cmp_gt_i32 vcc,16,v250\n"
"s_nop 1\n"
"v_cndmask_b32 v244,v244,v255,vcc\n"
"v_cmp_gt_i32 vcc,17,v250\n"
"s_nop 1\n"
"v_cndmask_b32 v245,v245,v255,vcc\n"
"v_cmp_gt_i32 vcc,18,v250\n"
"s_nop 1\n"
"v_cndmask_b32 v246,v246,v255,vcc\n"
"v_cmp_gt_i32 vcc,19,v250\n"
"s_nop 1\n"
"v_cndmask_b32 v247,v247,v255,vcc\n"
"s_cmp_lg_u32 s90,0\n"
"s_cbranch_scc1 L1043_%=\n"
"v_max3_f32 v248,v248,v240,v241\n"
"v_max3_f32 v248,v248,v242,v243\n"
"v_max3_f32 v248,v248,v244,v245\n"
"v_max3_f32 v248,v248,v246,v247\n"
"s_branch L1044_%=\n"
"L1043_%=:\n"
"s_lshl_b32 s91,s88,13\n"
SA"s91,s91,s39\n"
SA"s91,s91,524288\n"
BL"a[148:151],v224,s[8:11],s91 offen offset:0 sc1\n"
BL"a[152:155],v224,s[8:11],s91 offen offset:1024 sc1\n"
BL"a[156:159],v224,s[8:11],s91 offen offset:2048 sc1\n"
BL"a[160:163],v224,s[8:11],s91 offen offset:3072 sc1\n"
SA"s91,s91,4096\n"
BL"a[164:167],v224,s[8:11],s91 offen offset:0 sc1\n"
BL"a[168:171],v224,s[8:11],s91 offen offset:1024 sc1\n"
BL"a[172:175],v224,s[8:11],s91 offen offset:2048 sc1\n"
BL"a[176:179],v224,s[8:11],s91 offen offset:3072 sc1\n"
WT"vmcnt(0)\n"
"v_sub_f32 v240,v240,v248\n"
"v_sub_f32 v241,v241,v248\n"
"v_sub_f32 v242,v242,v248\n"
"v_sub_f32 v243,v243,v248\n"
"v_sub_f32 v244,v244,v248\n"
"v_sub_f32 v245,v245,v248\n"
"v_sub_f32 v246,v246,v248\n"
"v_sub_f32 v247,v247,v248\n"
EX"v240,v240\n"
EX"v241,v241\n"
EX"v242,v242\n"
EX"v243,v243\n"
EX"v244,v244\n"
EX"v245,v245\n"
EX"v246,v246\n"
EX"v247,v247\n"
"s_nop 0\n"
PK"v240,v240,v241\n"
PK"v241,v242,v243\n"
PK"v242,v244,v245\n"
PK"v243,v246,v247\n"
"s_nop 1\n"
MF"v[144:147],a[148:151],v[240:243],v[144:147]\n"
MF"v[148:151],a[152:155],v[240:243],v[148:151]\n"
MF"v[152:155],a[156:159],v[240:243],v[152:155]\n"
MF"v[156:159],a[160:163],v[240:243],v[156:159]\n"
MF"v[160:163],a[164:167],v[240:243],v[160:163]\n"
MF"v[164:167],a[168:171],v[240:243],v[164:167]\n"
MF"v[168:171],a[172:175],v[240:243],v[168:171]\n"
MF"v[172:175],a[176:179],v[240:243],v[172:175]\n"
MF"a[140:143],a[128:131],v[240:243],a[140:143]\n"
"s_nop 7\n"
"L1044_%=:\n"
SA"s88,s88,1\n"
"s_cmp_le_u32 s88,s89\n"
"s_cbranch_scc1 L1042_%=\n"
"s_cmp_lg_u32 s90,0\n"
"s_cbranch_scc1 L1045_%=\n"
"v_mov_b32 v254,v248\n"
"s_nop 1\n"
"v_permlane16_swap_b32 v254,v248\n"
"s_nop 1\n"
"v_max_f32 v248,v248,v254\n"
"v_mov_b32 v254,v248\n"
"s_nop 1\n"
"v_permlane32_swap_b32 v254,v248\n"
"s_nop 1\n"
"v_max_f32 v248,v248,v254\n"
"s_mov_b32 s90,1\n"
"s_mov_b32 s88,0\n"
"s_branch L1042_%=\n"
"L1045_%=:\n"
"s_nop 7\n"
"v_accvgpr_read_b32 v251,a140\n"
"s_nop 1\n"
"s_branch L1040_%=\n"
"L1047_%=:\n"
"s_nop 15\n"
WT"vmcnt(0)\n"
"s_lshl_b32 s89,s34,3\n"
"s_sub_u32 s89,120,s89\n"
SA"s89,s89,s30\n"
SA"s89,s89,4\n"
"s_lshl_b32 s91,s89,4\n"
"s_lshr_b32 s89,s89,1\n"
"v_bfe_u32 v249,v224,4,4\n"
"v_lshrrev_b32 v254,8,v224\n"
"v_lshlrev_b32 v254,2,v254\n"
"v_sub_u32 v249,v249,v254\n"
"v_add_u32 v249,s91,v249\n"
"v_mov_b32 v255,0xff800000\n"
"v_mov_b32 v248,v255\n"
"s_mov_b32 s90,0\n"
"s_mov_b32 s88,0\n"
"v_mov_b32 v176,0\n"
"v_mov_b32 v177,0\n"
"v_mov_b32 v178,0\n"
"v_mov_b32 v179,0\n"
"v_mov_b32 v180,0\n"
"v_mov_b32 v181,0\n"
"v_mov_b32 v182,0\n"
"v_mov_b32 v183,0\n"
"v_mov_b32 v184,0\n"
"v_mov_b32 v185,0\n"
"v_mov_b32 v186,0\n"
"v_mov_b32 v187,0\n"
"v_mov_b32 v188,0\n"
"v_mov_b32 v189,0\n"
"v_mov_b32 v190,0\n"
"v_mov_b32 v191,0\n"
"v_mov_b32 v192,0\n"
"v_mov_b32 v193,0\n"
"v_mov_b32 v194,0\n"
"v_mov_b32 v195,0\n"
"v_mov_b32 v196,0\n"
"v_mov_b32 v197,0\n"
"v_mov_b32 v198,0\n"
"v_mov_b32 v199,0\n"
"v_mov_b32 v200,0\n"
"v_mov_b32 v201,0\n"
"v_mov_b32 v202,0\n"
"v_mov_b32 v203,0\n"
"v_mov_b32 v204,0\n"
"v_mov_b32 v205,0\n"
"v_mov_b32 v206,0\n"
"v_mov_b32 v207,0\n"
"v_accvgpr_write_b32 a144,0\n"
"v_accvgpr_write_b32 a145,0\n"
"v_accvgpr_write_b32 a146,0\n"
"v_accvgpr_write_b32 a147,0\n"
"L1048_%=:\n"
"s_lshl_b32 s91,s88,13\n"
SA"s91,s91,s39\n"
SA"s91,s91,0\n"
BL"a[148:151],v224,s[8:11],s91 offen offset:0 sc1\n"
BL"a[152:155],v224,s[8:11],s91 offen offset:1024 sc1\n"
BL"a[156:159],v224,s[8:11],s91 offen offset:2048 sc1\n"
BL"a[160:163],v224,s[8:11],s91 offen offset:3072 sc1\n"
SA"s91,s91,4096\n"
BL"a[164:167],v224,s[8:11],s91 offen offset:0 sc1\n"
BL"a[168:171],v224,s[8:11],s91 offen offset:1024 sc1\n"
BL"a[172:175],v224,s[8:11],s91 offen offset:2048 sc1\n"
BL"a[176:179],v224,s[8:11],s91 offen offset:3072 sc1\n"
WT"vmcnt(0)\n"
MF"v[240:243],a[148:151],a[48:51],0\n"
MF"v[240:243],a[152:155],a[52:55],v[240:243]\n"
MF"v[240:243],a[156:159],a[56:59],v[240:243]\n"
MF"v[240:243],a[160:163],a[60:63],v[240:243]\n"
MF"v[244:247],a[164:167],a[48:51],0\n"
MF"v[244:247],a[168:171],a[52:55],v[244:247]\n"
MF"v[244:247],a[172:175],a[56:59],v[244:247]\n"
MF"v[244:247],a[176:179],a[60:63],v[244:247]\n"
"s_lshl_b32 s91,s88,5\n"
"v_subrev_u32 v250,s91,v249\n"
"s_nop 7\n"
"v_cmp_gt_i32 vcc,0,v250\n"
"s_nop 1\n"
"v_cndmask_b32 v240,v240,v255,vcc\n"
"v_cmp_gt_i32 vcc,1,v250\n"
"s_nop 1\n"
"v_cndmask_b32 v241,v241,v255,vcc\n"
"v_cmp_gt_i32 vcc,2,v250\n"
"s_nop 1\n"
"v_cndmask_b32 v242,v242,v255,vcc\n"
"v_cmp_gt_i32 vcc,3,v250\n"
"s_nop 1\n"
"v_cndmask_b32 v243,v243,v255,vcc\n"
"v_cmp_gt_i32 vcc,16,v250\n"
"s_nop 1\n"
"v_cndmask_b32 v244,v244,v255,vcc\n"
"v_cmp_gt_i32 vcc,17,v250\n"
"s_nop 1\n"
"v_cndmask_b32 v245,v245,v255,vcc\n"
"v_cmp_gt_i32 vcc,18,v250\n"
"s_nop 1\n"
"v_cndmask_b32 v246,v246,v255,vcc\n"
"v_cmp_gt_i32 vcc,19,v250\n"
"s_nop 1\n"
"v_cndmask_b32 v247,v247,v255,vcc\n"
"s_cmp_lg_u32 s90,0\n"
"s_cbranch_scc1 L1049_%=\n"
"v_max3_f32 v248,v248,v240,v241\n"
"v_max3_f32 v248,v248,v242,v243\n"
"v_max3_f32 v248,v248,v244,v245\n"
"v_max3_f32 v248,v248,v246,v247\n"
"s_branch L1050_%=\n"
"L1049_%=:\n"
"s_lshl_b32 s91,s88,13\n"
SA"s91,s91,s39\n"
SA"s91,s91,524288\n"
BL"a[148:151],v224,s[8:11],s91 offen offset:0 sc1\n"
BL"a[152:155],v224,s[8:11],s91 offen offset:1024 sc1\n"
BL"a[156:159],v224,s[8:11],s91 offen offset:2048 sc1\n"
BL"a[160:163],v224,s[8:11],s91 offen offset:3072 sc1\n"
SA"s91,s91,4096\n"
BL"a[164:167],v224,s[8:11],s91 offen offset:0 sc1\n"
BL"a[168:171],v224,s[8:11],s91 offen offset:1024 sc1\n"
BL"a[172:175],v224,s[8:11],s91 offen offset:2048 sc1\n"
BL"a[176:179],v224,s[8:11],s91 offen offset:3072 sc1\n"
WT"vmcnt(0)\n"
"v_sub_f32 v240,v240,v248\n"
"v_sub_f32 v241,v241,v248\n"
"v_sub_f32 v242,v242,v248\n"
"v_sub_f32 v243,v243,v248\n"
"v_sub_f32 v244,v244,v248\n"
"v_sub_f32 v245,v245,v248\n"
"v_sub_f32 v246,v246,v248\n"
"v_sub_f32 v247,v247,v248\n"
EX"v240,v240\n"
EX"v241,v241\n"
EX"v242,v242\n"
EX"v243,v243\n"
EX"v244,v244\n"
EX"v245,v245\n"
EX"v246,v246\n"
EX"v247,v247\n"
"s_nop 0\n"
PK"v240,v240,v241\n"
PK"v241,v242,v243\n"
PK"v242,v244,v245\n"
PK"v243,v246,v247\n"
"s_nop 1\n"
MF"v[176:179],a[148:151],v[240:243],v[176:179]\n"
MF"v[180:183],a[152:155],v[240:243],v[180:183]\n"
MF"v[184:187],a[156:159],v[240:243],v[184:187]\n"
MF"v[188:191],a[160:163],v[240:243],v[188:191]\n"
MF"v[192:195],a[164:167],v[240:243],v[192:195]\n"
MF"v[196:199],a[168:171],v[240:243],v[196:199]\n"
MF"v[200:203],a[172:175],v[240:243],v[200:203]\n"
MF"v[204:207],a[176:179],v[240:243],v[204:207]\n"
MF"a[144:147],a[128:131],v[240:243],a[144:147]\n"
"s_nop 7\n"
"L1050_%=:\n"
SA"s88,s88,1\n"
"s_cmp_le_u32 s88,s89\n"
"s_cbranch_scc1 L1048_%=\n"
"s_cmp_lg_u32 s90,0\n"
"s_cbranch_scc1 L1051_%=\n"
"v_mov_b32 v254,v248\n"
"s_nop 1\n"
"v_permlane16_swap_b32 v254,v248\n"
"s_nop 1\n"
"v_max_f32 v248,v248,v254\n"
"v_mov_b32 v254,v248\n"
"s_nop 1\n"
"v_permlane32_swap_b32 v254,v248\n"
"s_nop 1\n"
"v_max_f32 v248,v248,v254\n"
"s_mov_b32 s90,1\n"
"s_mov_b32 s88,0\n"
"s_branch L1048_%=\n"
"L1051_%=:\n"
"s_nop 7\n"
"v_accvgpr_read_b32 v252,a144\n"
"s_nop 1\n"
"s_branch L1046_%=\n"
"L1069_%=:\n"
"s_nop 15\n"
WT"vmcnt(0)\n"
"s_lshl_b32 s89,s34,3\n"
"s_sub_u32 s89,120,s89\n"
SA"s89,s89,s30\n"
SA"s89,s89,0\n"
"s_lshl_b32 s91,s89,4\n"
"s_lshr_b32 s89,s89,1\n"
"v_bfe_u32 v249,v224,4,4\n"
"v_lshrrev_b32 v254,8,v224\n"
"v_lshlrev_b32 v254,2,v254\n"
"v_sub_u32 v249,v249,v254\n"
"v_add_u32 v249,s91,v249\n"
"v_mov_b32 v255,0xff800000\n"
"v_mov_b32 v248,v255\n"
"s_mov_b32 s90,0\n"
"s_mov_b32 s88,0\n"
"v_mov_b32 v144,0\n"
"v_mov_b32 v145,0\n"
"v_mov_b32 v146,0\n"
"v_mov_b32 v147,0\n"
"v_mov_b32 v148,0\n"
"v_mov_b32 v149,0\n"
"v_mov_b32 v150,0\n"
"v_mov_b32 v151,0\n"
"v_mov_b32 v152,0\n"
"v_mov_b32 v153,0\n"
"v_mov_b32 v154,0\n"
"v_mov_b32 v155,0\n"
"v_mov_b32 v156,0\n"
"v_mov_b32 v157,0\n"
"v_mov_b32 v158,0\n"
"v_mov_b32 v159,0\n"
"v_mov_b32 v160,0\n"
"v_mov_b32 v161,0\n"
"v_mov_b32 v162,0\n"
"v_mov_b32 v163,0\n"
"v_mov_b32 v164,0\n"
"v_mov_b32 v165,0\n"
"v_mov_b32 v166,0\n"
"v_mov_b32 v167,0\n"
"v_mov_b32 v168,0\n"
"v_mov_b32 v169,0\n"
"v_mov_b32 v170,0\n"
"v_mov_b32 v171,0\n"
"v_mov_b32 v172,0\n"
"v_mov_b32 v173,0\n"
"v_mov_b32 v174,0\n"
"v_mov_b32 v175,0\n"
"v_accvgpr_write_b32 a140,0\n"
"v_accvgpr_write_b32 a141,0\n"
"v_accvgpr_write_b32 a142,0\n"
"v_accvgpr_write_b32 a143,0\n"
"L1070_%=:\n"
"s_lshl_b32 s91,s88,13\n"
SA"s91,s91,s39\n"
SA"s91,s91,0\n"
BL"a[148:151],v224,s[8:11],s91 offen offset:0 sc1\n"
BL"a[152:155],v224,s[8:11],s91 offen offset:1024 sc1\n"
BL"a[156:159],v224,s[8:11],s91 offen offset:2048 sc1\n"
BL"a[160:163],v224,s[8:11],s91 offen offset:3072 sc1\n"
SA"s91,s91,4096\n"
BL"a[164:167],v224,s[8:11],s91 offen offset:0 sc1\n"
BL"a[168:171],v224,s[8:11],s91 offen offset:1024 sc1\n"
BL"a[172:175],v224,s[8:11],s91 offen offset:2048 sc1\n"
BL"a[176:179],v224,s[8:11],s91 offen offset:3072 sc1\n"
WT"vmcnt(0)\n"
MF"v[240:243],a[148:151],a[32:35],0\n"
MF"v[240:243],a[152:155],a[36:39],v[240:243]\n"
MF"v[240:243],a[156:159],a[40:43],v[240:243]\n"
MF"v[240:243],a[160:163],a[44:47],v[240:243]\n"
MF"v[244:247],a[164:167],a[32:35],0\n"
MF"v[244:247],a[168:171],a[36:39],v[244:247]\n"
MF"v[244:247],a[172:175],a[40:43],v[244:247]\n"
MF"v[244:247],a[176:179],a[44:47],v[244:247]\n"
"s_lshl_b32 s91,s88,5\n"
"v_subrev_u32 v250,s91,v249\n"
"s_nop 7\n"
"v_cmp_gt_i32 vcc,0,v250\n"
"s_nop 1\n"
"v_cndmask_b32 v240,v240,v255,vcc\n"
"v_cmp_gt_i32 vcc,1,v250\n"
"s_nop 1\n"
"v_cndmask_b32 v241,v241,v255,vcc\n"
"v_cmp_gt_i32 vcc,2,v250\n"
"s_nop 1\n"
"v_cndmask_b32 v242,v242,v255,vcc\n"
"v_cmp_gt_i32 vcc,3,v250\n"
"s_nop 1\n"
"v_cndmask_b32 v243,v243,v255,vcc\n"
"v_cmp_gt_i32 vcc,16,v250\n"
"s_nop 1\n"
"v_cndmask_b32 v244,v244,v255,vcc\n"
"v_cmp_gt_i32 vcc,17,v250\n"
"s_nop 1\n"
"v_cndmask_b32 v245,v245,v255,vcc\n"
"v_cmp_gt_i32 vcc,18,v250\n"
"s_nop 1\n"
"v_cndmask_b32 v246,v246,v255,vcc\n"
"v_cmp_gt_i32 vcc,19,v250\n"
"s_nop 1\n"
"v_cndmask_b32 v247,v247,v255,vcc\n"
"s_cmp_lg_u32 s90,0\n"
"s_cbranch_scc1 L1071_%=\n"
"v_max3_f32 v248,v248,v240,v241\n"
"v_max3_f32 v248,v248,v242,v243\n"
"v_max3_f32 v248,v248,v244,v245\n"
"v_max3_f32 v248,v248,v246,v247\n"
"s_branch L1072_%=\n"
"L1071_%=:\n"
"s_lshl_b32 s91,s88,13\n"
SA"s91,s91,s39\n"
SA"s91,s91,524288\n"
BL"a[148:151],v224,s[8:11],s91 offen offset:0 sc1\n"
BL"a[152:155],v224,s[8:11],s91 offen offset:1024 sc1\n"
BL"a[156:159],v224,s[8:11],s91 offen offset:2048 sc1\n"
BL"a[160:163],v224,s[8:11],s91 offen offset:3072 sc1\n"
SA"s91,s91,4096\n"
BL"a[164:167],v224,s[8:11],s91 offen offset:0 sc1\n"
BL"a[168:171],v224,s[8:11],s91 offen offset:1024 sc1\n"
BL"a[172:175],v224,s[8:11],s91 offen offset:2048 sc1\n"
BL"a[176:179],v224,s[8:11],s91 offen offset:3072 sc1\n"
WT"vmcnt(0)\n"
"v_sub_f32 v240,v240,v248\n"
"v_sub_f32 v241,v241,v248\n"
"v_sub_f32 v242,v242,v248\n"
"v_sub_f32 v243,v243,v248\n"
"v_sub_f32 v244,v244,v248\n"
"v_sub_f32 v245,v245,v248\n"
"v_sub_f32 v246,v246,v248\n"
"v_sub_f32 v247,v247,v248\n"
EX"v240,v240\n"
EX"v241,v241\n"
EX"v242,v242\n"
EX"v243,v243\n"
EX"v244,v244\n"
EX"v245,v245\n"
EX"v246,v246\n"
EX"v247,v247\n"
"s_nop 0\n"
PK"v240,v240,v241\n"
PK"v241,v242,v243\n"
PK"v242,v244,v245\n"
PK"v243,v246,v247\n"
"s_nop 1\n"
MF"v[144:147],a[148:151],v[240:243],v[144:147]\n"
MF"v[148:151],a[152:155],v[240:243],v[148:151]\n"
MF"v[152:155],a[156:159],v[240:243],v[152:155]\n"
MF"v[156:159],a[160:163],v[240:243],v[156:159]\n"
MF"v[160:163],a[164:167],v[240:243],v[160:163]\n"
MF"v[164:167],a[168:171],v[240:243],v[164:167]\n"
MF"v[168:171],a[172:175],v[240:243],v[168:171]\n"
MF"v[172:175],a[176:179],v[240:243],v[172:175]\n"
MF"a[140:143],a[128:131],v[240:243],a[140:143]\n"
"s_nop 7\n"
"L1072_%=:\n"
SA"s88,s88,1\n"
"s_cmp_le_u32 s88,s89\n"
"s_cbranch_scc1 L1070_%=\n"
"s_cmp_lg_u32 s90,0\n"
"s_cbranch_scc1 L1073_%=\n"
"v_mov_b32 v254,v248\n"
"s_nop 1\n"
"v_permlane16_swap_b32 v254,v248\n"
"s_nop 1\n"
"v_max_f32 v248,v248,v254\n"
"v_mov_b32 v254,v248\n"
"s_nop 1\n"
"v_permlane32_swap_b32 v254,v248\n"
"s_nop 1\n"
"v_max_f32 v248,v248,v254\n"
"s_mov_b32 s90,1\n"
"s_mov_b32 s88,0\n"
"s_branch L1070_%=\n"
"L1073_%=:\n"
"s_nop 7\n"
"v_accvgpr_read_b32 v251,a140\n"
"s_nop 1\n"
"s_branch L1068_%=\n"
"L1075_%=:\n"
"s_nop 15\n"
WT"vmcnt(0)\n"
"s_lshl_b32 s89,s34,3\n"
"s_sub_u32 s89,120,s89\n"
SA"s89,s89,s30\n"
SA"s89,s89,4\n"
"s_lshl_b32 s91,s89,4\n"
"s_lshr_b32 s89,s89,1\n"
"v_bfe_u32 v249,v224,4,4\n"
"v_lshrrev_b32 v254,8,v224\n"
"v_lshlrev_b32 v254,2,v254\n"
"v_sub_u32 v249,v249,v254\n"
"v_add_u32 v249,s91,v249\n"
"v_mov_b32 v255,0xff800000\n"
"v_mov_b32 v248,v255\n"
"s_mov_b32 s90,0\n"
"s_mov_b32 s88,0\n"
"v_mov_b32 v176,0\n"
"v_mov_b32 v177,0\n"
"v_mov_b32 v178,0\n"
"v_mov_b32 v179,0\n"
"v_mov_b32 v180,0\n"
"v_mov_b32 v181,0\n"
"v_mov_b32 v182,0\n"
"v_mov_b32 v183,0\n"
"v_mov_b32 v184,0\n"
"v_mov_b32 v185,0\n"
"v_mov_b32 v186,0\n"
"v_mov_b32 v187,0\n"
"v_mov_b32 v188,0\n"
"v_mov_b32 v189,0\n"
"v_mov_b32 v190,0\n"
"v_mov_b32 v191,0\n"
"v_mov_b32 v192,0\n"
"v_mov_b32 v193,0\n"
"v_mov_b32 v194,0\n"
"v_mov_b32 v195,0\n"
"v_mov_b32 v196,0\n"
"v_mov_b32 v197,0\n"
"v_mov_b32 v198,0\n"
"v_mov_b32 v199,0\n"
"v_mov_b32 v200,0\n"
"v_mov_b32 v201,0\n"
"v_mov_b32 v202,0\n"
"v_mov_b32 v203,0\n"
"v_mov_b32 v204,0\n"
"v_mov_b32 v205,0\n"
"v_mov_b32 v206,0\n"
"v_mov_b32 v207,0\n"
"v_accvgpr_write_b32 a144,0\n"
"v_accvgpr_write_b32 a145,0\n"
"v_accvgpr_write_b32 a146,0\n"
"v_accvgpr_write_b32 a147,0\n"
"L1076_%=:\n"
"s_lshl_b32 s91,s88,13\n"
SA"s91,s91,s39\n"
SA"s91,s91,0\n"
BL"a[148:151],v224,s[8:11],s91 offen offset:0 sc1\n"
BL"a[152:155],v224,s[8:11],s91 offen offset:1024 sc1\n"
BL"a[156:159],v224,s[8:11],s91 offen offset:2048 sc1\n"
BL"a[160:163],v224,s[8:11],s91 offen offset:3072 sc1\n"
SA"s91,s91,4096\n"
BL"a[164:167],v224,s[8:11],s91 offen offset:0 sc1\n"
BL"a[168:171],v224,s[8:11],s91 offen offset:1024 sc1\n"
BL"a[172:175],v224,s[8:11],s91 offen offset:2048 sc1\n"
BL"a[176:179],v224,s[8:11],s91 offen offset:3072 sc1\n"
WT"vmcnt(0)\n"
MF"v[240:243],a[148:151],a[48:51],0\n"
MF"v[240:243],a[152:155],a[52:55],v[240:243]\n"
MF"v[240:243],a[156:159],a[56:59],v[240:243]\n"
MF"v[240:243],a[160:163],a[60:63],v[240:243]\n"
MF"v[244:247],a[164:167],a[48:51],0\n"
MF"v[244:247],a[168:171],a[52:55],v[244:247]\n"
MF"v[244:247],a[172:175],a[56:59],v[244:247]\n"
MF"v[244:247],a[176:179],a[60:63],v[244:247]\n"
"s_lshl_b32 s91,s88,5\n"
"v_subrev_u32 v250,s91,v249\n"
"s_nop 7\n"
"v_cmp_gt_i32 vcc,0,v250\n"
"s_nop 1\n"
"v_cndmask_b32 v240,v240,v255,vcc\n"
"v_cmp_gt_i32 vcc,1,v250\n"
"s_nop 1\n"
"v_cndmask_b32 v241,v241,v255,vcc\n"
"v_cmp_gt_i32 vcc,2,v250\n"
"s_nop 1\n"
"v_cndmask_b32 v242,v242,v255,vcc\n"
"v_cmp_gt_i32 vcc,3,v250\n"
"s_nop 1\n"
"v_cndmask_b32 v243,v243,v255,vcc\n"
"v_cmp_gt_i32 vcc,16,v250\n"
"s_nop 1\n"
"v_cndmask_b32 v244,v244,v255,vcc\n"
"v_cmp_gt_i32 vcc,17,v250\n"
"s_nop 1\n"
"v_cndmask_b32 v245,v245,v255,vcc\n"
"v_cmp_gt_i32 vcc,18,v250\n"
"s_nop 1\n"
"v_cndmask_b32 v246,v246,v255,vcc\n"
"v_cmp_gt_i32 vcc,19,v250\n"
"s_nop 1\n"
"v_cndmask_b32 v247,v247,v255,vcc\n"
"s_cmp_lg_u32 s90,0\n"
"s_cbranch_scc1 L1077_%=\n"
"v_max3_f32 v248,v248,v240,v241\n"
"v_max3_f32 v248,v248,v242,v243\n"
"v_max3_f32 v248,v248,v244,v245\n"
"v_max3_f32 v248,v248,v246,v247\n"
"s_branch L1078_%=\n"
"L1077_%=:\n"
"s_lshl_b32 s91,s88,13\n"
SA"s91,s91,s39\n"
SA"s91,s91,524288\n"
BL"a[148:151],v224,s[8:11],s91 offen offset:0 sc1\n"
BL"a[152:155],v224,s[8:11],s91 offen offset:1024 sc1\n"
BL"a[156:159],v224,s[8:11],s91 offen offset:2048 sc1\n"
BL"a[160:163],v224,s[8:11],s91 offen offset:3072 sc1\n"
SA"s91,s91,4096\n"
BL"a[164:167],v224,s[8:11],s91 offen offset:0 sc1\n"
BL"a[168:171],v224,s[8:11],s91 offen offset:1024 sc1\n"
BL"a[172:175],v224,s[8:11],s91 offen offset:2048 sc1\n"
BL"a[176:179],v224,s[8:11],s91 offen offset:3072 sc1\n"
WT"vmcnt(0)\n"
"v_sub_f32 v240,v240,v248\n"
"v_sub_f32 v241,v241,v248\n"
"v_sub_f32 v242,v242,v248\n"
"v_sub_f32 v243,v243,v248\n"
"v_sub_f32 v244,v244,v248\n"
"v_sub_f32 v245,v245,v248\n"
"v_sub_f32 v246,v246,v248\n"
"v_sub_f32 v247,v247,v248\n"
EX"v240,v240\n"
EX"v241,v241\n"
EX"v242,v242\n"
EX"v243,v243\n"
EX"v244,v244\n"
EX"v245,v245\n"
EX"v246,v246\n"
EX"v247,v247\n"
"s_nop 0\n"
PK"v240,v240,v241\n"
PK"v241,v242,v243\n"
PK"v242,v244,v245\n"
PK"v243,v246,v247\n"
"s_nop 1\n"
MF"v[176:179],a[148:151],v[240:243],v[176:179]\n"
MF"v[180:183],a[152:155],v[240:243],v[180:183]\n"
MF"v[184:187],a[156:159],v[240:243],v[184:187]\n"
MF"v[188:191],a[160:163],v[240:243],v[188:191]\n"
MF"v[192:195],a[164:167],v[240:243],v[192:195]\n"
MF"v[196:199],a[168:171],v[240:243],v[196:199]\n"
MF"v[200:203],a[172:175],v[240:243],v[200:203]\n"
MF"v[204:207],a[176:179],v[240:243],v[204:207]\n"
MF"a[144:147],a[128:131],v[240:243],a[144:147]\n"
"s_nop 7\n"
"L1078_%=:\n"
SA"s88,s88,1\n"
"s_cmp_le_u32 s88,s89\n"
"s_cbranch_scc1 L1076_%=\n"
"s_cmp_lg_u32 s90,0\n"
"s_cbranch_scc1 L1079_%=\n"
"v_mov_b32 v254,v248\n"
"s_nop 1\n"
"v_permlane16_swap_b32 v254,v248\n"
"s_nop 1\n"
"v_max_f32 v248,v248,v254\n"
"v_mov_b32 v254,v248\n"
"s_nop 1\n"
"v_permlane32_swap_b32 v254,v248\n"
"s_nop 1\n"
"v_max_f32 v248,v248,v254\n"
"s_mov_b32 s90,1\n"
"s_mov_b32 s88,0\n"
"s_branch L1076_%=\n"
"L1079_%=:\n"
"s_nop 7\n"
"v_accvgpr_read_b32 v252,a144\n"
"s_nop 1\n"
"s_branch L1074_%=\n"
"L1080_%=:\n"
"s_mov_b32 s54,0\n"
"L1082_%=:\n"
"s_sleep 2\n"
"buffer_load_dwordx2 v[240:241],v238,s[8:11],s63 offen sc1\n"
WT"vmcnt(0)\n"
SA"s54,s54,1\n"
"v_cmp_eq_u64 vcc,s[28:29],v[240:241]\n"
"s_cmp_eq_u64 vcc,exec\n"
"s_cbranch_scc1 L1081_%=\n"
"s_cmp_lt_u32 s54,1048576\n"
"s_cbranch_scc1 L1082_%=\n"
"s_branch L1081_%=\n"
"L1083_%=:\n"
"s_mov_b32 s54,0\n"
"L1085_%=:\n"
"s_sleep 2\n"
"buffer_load_dwordx2 v[240:241],v238,s[8:11],s63 offen sc1\n"
WT"vmcnt(0)\n"
SA"s54,s54,1\n"
"v_cmp_eq_u64 vcc,s[28:29],v[240:241]\n"
"s_cmp_eq_u64 vcc,exec\n"
"s_cbranch_scc1 L1084_%=\n"
"s_cmp_lt_u32 s54,1048576\n"
"s_cbranch_scc1 L1085_%=\n"
"s_branch L1084_%=\n"
"L1086_%=:\n"
"s_mov_b32 s54,0\n"
"L1088_%=:\n"
"s_sleep 2\n"
"buffer_load_dwordx2 v[240:241],v244,s[8:11],s92 offen sc1\n"
WT"vmcnt(0)\n"
SA"s54,s54,1\n"
"v_cmp_eq_u64 vcc,s[28:29],v[240:241]\n"
"s_cmp_eq_u64 vcc,exec\n"
"s_cbranch_scc1 L1087_%=\n"
"s_cmp_lt_u32 s54,1048576\n"
"s_cbranch_scc1 L1088_%=\n"
"s_branch L1087_%=\n"
"LTH_%=:\n"
RD"a[92:95],v227 offset:7168\n"
MF"v[0:3],a[64:67],v[80:83],0\n"
WT"lgkmcnt(4)\n"
"v_accvgpr_read_b32 v251,a140\n"
"v_subrev_u32 v253,0x2b800000,v251\n"
MF"v[0:3],a[68:71],v[88:91],v[0:3]\n"
RD"a[96:99],v227 offset:8192\n"
MF"v[40:43],a[68:71],v[120:123],0\n"
"v_cmp_le_u32 vcc,0x34000000,v253\n"
"s_cmp_lg_u64 vcc,0\n"
"s_cbranch_scc1 L1069_%=\n"
"L1068_%=:\n"
MF"v[40:43],a[64:67],v[112:115],v[40:43]\n"
"v_rcp_f32 v251,v251\n"
MF"v[4:7],a[72:75],v[80:83],0\n"
SA"s41,s41,32768\n"
RD"a[100:103],v227 offset:9216\n"
MU"v144,v251,v144\n"
MF"v[4:7],a[76:79],v[88:91],v[4:7]\n"
"s_cmp_eq_u32 s41,163840\n"
RD"a[104:107],v227 offset:10240\n"
MU"v145,v251,v145\n"
MF"v[44:47],a[76:79],v[120:123],0\n"
"s_cselect_b32 s41,0,s41\n"
MU"v146,v251,v146\n"
"v_add_u32 v226,s41,v224\n"
MF"v[44:47],a[72:75],v[112:115],v[44:47]\n"
WT"lgkmcnt(3)\n"
RD"a[108:111],v227 offset:11264\n"
MU"v147,v251,v147\n"
MF"v[8:11],a[80:83],v[80:83],0\n"
SA"s41,s41,32768\n"
MU"v148,v251,v148\n"
MU"v149,v251,v149\n"
MF"v[8:11],a[84:87],v[88:91],v[8:11]\n"
"s_cmp_eq_u32 s41,163840\n"
RD"a[112:115],v227 offset:12288\n"
MU"v150,v251,v150\n"
MU"v151,v251,v151\n"
MF"v[48:51],a[84:87],v[120:123],0\n"
"s_cselect_b32 s41,0,s41\n"
PK"v144,v144,v145\n"
"v_add_u32 v235,s41,v224\n"
PK"v145,v146,v147\n"
MF"v[48:51],a[80:83],v[112:115],v[48:51]\n"
RD"a[116:119],v227 offset:13312\n"
PK"v146,v148,v149\n"
MF"v[12:15],a[88:91],v[80:83],0\n"
SA"s41,s41,32768\n"
PK"v147,v150,v151\n"
MU"v152,v251,v152\n"
MF"v[12:15],a[92:95],v[88:91],v[12:15]\n"
"s_cmp_eq_u32 s41,163840\n"
BS"v[144:147],v224,s[8:11],s60 offen offset:0 sc1\n"
MU"v153,v251,v153\n"
MF"v[52:55],a[92:95],v[120:123],0\n"
"s_cselect_b32 s41,0,s41\n"
RD"a[120:123],v227 offset:14336\n"
"v_add_u32 v236,s41,v224\n"
MU"v154,v251,v154\n"
MF"v[52:55],a[88:91],v[112:115],v[52:55]\n"
WT"lgkmcnt(3)\n"
RD"a[124:127],v227 offset:15360\n"
MU"v155,v251,v155\n"
MU"v156,v251,v156\n"
MF"v[16:19],a[96:99],v[80:83],0\n"
SA"s41,s41,32768\n"
MU"v157,v251,v157\n"
MU"v158,v251,v158\n"
MU"v159,v251,v159\n"
MF"v[16:19],a[100:103],v[88:91],v[16:19]\n"
"s_cmp_eq_u32 s41,163840\n"
RD"a[64:67],v227 offset:16384\n"
PK"v152,v152,v153\n"
PK"v153,v154,v155\n"
MF"v[56:59],a[100:103],v[120:123],0\n"
"s_cselect_b32 s41,0,s41\n"
PK"v154,v156,v157\n"
"v_add_u32 v237,s41,v224\n"
PK"v155,v158,v159\n"
MF"v[56:59],a[96:99],v[112:115],v[56:59]\n"
BS"v[152:155],v224,s[8:11],s60 offen offset:1024 sc1\n"
MU"v160,v251,v160\n"
MF"v[20:23],a[104:107],v[80:83],0\n"
RD"a[68:71],v227 offset:17408\n"
MU"v161,v251,v161\n"
MU"v162,v251,v162\n"
MF"v[20:23],a[108:111],v[88:91],v[20:23]\n"
WT"vmcnt(2)\n"
"buffer_store_dwordx2 v[232:233],v230,s[8:11],s61 offen sc1\n"
MU"v163,v251,v163\n"
MF"v[60:63],a[108:111],v[120:123],0\n"
RD"a[72:75],v227 offset:18432\n"
MU"v164,v251,v164\n"
MU"v165,v251,v165\n"
MF"v[60:63],a[104:107],v[112:115],v[60:63]\n"
WT"lgkmcnt(3)\n"
RD"a[76:79],v227 offset:19456\n"
MU"v166,v251,v166\n"
MU"v167,v251,v167\n"
MF"v[24:27],a[112:115],v[80:83],0\n"
SA"s51,s60,16384\n"
"v_accvgpr_read_b32 v252,a144\n"
"v_subrev_u32 v253,0x2b800000,v252\n"
PK"v160,v160,v161\n"
MF"v[24:27],a[116:119],v[88:91],v[24:27]\n"
"s_lshl_b32 s62,s34,14\n"
RD"a[80:83],v227 offset:20480\n"
PK"v161,v162,v163\n"
PK"v162,v164,v165\n"
MF"v[64:67],a[116:119],v[120:123],0\n"
"s_sub_u32 s62,s61,s62\n"
"v_cmp_le_u32 vcc,0x34000000,v253\n"
"s_cmp_lg_u64 vcc,0\n"
"s_cbranch_scc1 L1075_%=\n"
"L1074_%=:\n"
MF"v[64:67],a[112:115],v[112:115],v[64:67]\n"
SA"s62,s62,122880\n"
"v_rcp_f32 v252,v252\n"
PK"v163,v166,v167\n"
MF"v[28:31],a[120:123],v[80:83],0\n"
BS"v[160:163],v224,s[8:11],s60 offen offset:2048 sc1\n"
MU"v168,v251,v168\n"
MF"v[28:31],a[124:127],v[88:91],v[28:31]\n"
RD"a[84:87],v227 offset:21504\n"
RD"a[88:91],v227 offset:22528\n"
MU"v169,v251,v169\n"
MF"v[68:71],a[124:127],v[120:123],0\n"
MU"v170,v251,v170\n"
MU"v171,v251,v171\n"
MU"v172,v251,v172\n"
MF"v[68:71],a[120:123],v[112:115],v[68:71]\n"
WT"lgkmcnt(3)\n"
RD"a[92:95],v227 offset:23552\n"
MU"v173,v251,v173\n"
MU"v174,v251,v174\n"
MF"v[32:35],a[64:67],v[80:83],0\n"
MU"v175,v251,v175\n"
PK"v168,v168,v169\n"
PK"v169,v170,v171\n"
MF"v[32:35],a[68:71],v[88:91],v[32:35]\n"
RD"a[96:99],v227 offset:24576\n"
MU"v176,v252,v176\n"
MU"v177,v252,v177\n"
MF"v[72:75],a[68:71],v[120:123],0\n"
PK"v170,v172,v173\n"
PK"v171,v174,v175\n"
MU"v178,v252,v178\n"
MF"v[72:75],a[64:67],v[112:115],v[72:75]\n"
BS"v[168:171],v224,s[8:11],s60 offen offset:3072 sc1\n"
MU"v179,v252,v179\n"
MF"v[36:39],a[72:75],v[80:83],0\n"
RD"a[100:103],v227 offset:25600\n"
MU"v180,v252,v180\n"
MU"v181,v252,v181\n"
MF"v[36:39],a[76:79],v[88:91],v[36:39]\n"
RD"a[104:107],v227 offset:26624\n"
MU"v182,v252,v182\n"
MU"v183,v252,v183\n"
MF"v[76:79],a[76:79],v[120:123],0\n"
PK"v176,v176,v177\n"
PK"v177,v178,v179\n"
PK"v178,v180,v181\n"
MF"v[76:79],a[72:75],v[112:115],v[76:79]\n"
WT"lgkmcnt(3)\n"
RD"a[108:111],v227 offset:27648\n"
PK"v179,v182,v183\n"
MU"v184,v252,v184\n"
MF"v[0:3],a[80:83],v[96:99],v[0:3]\n"
BS"v[176:179],v224,s[8:11],s51 offen offset:0 sc1\n"
MU"v185,v252,v185\n"
MF"v[0:3],a[84:87],v[104:107],v[0:3]\n"
RD"a[112:115],v227 offset:28672\n"
MU"v186,v252,v186\n"
MU"v187,v252,v187\n"
MF"v[40:43],a[84:87],v[136:139],v[40:43]\n"
MU"v188,v252,v188\n"
MU"v189,v252,v189\n"
MU"v190,v252,v190\n"
MF"v[40:43],a[80:83],v[128:131],v[40:43]\n"
RD"a[116:119],v227 offset:29696\n"
MU"v191,v252,v191\n"
PK"v184,v184,v185\n"
MF"v[4:7],a[88:91],v[96:99],v[4:7]\n"
PK"v185,v186,v187\n"
PK"v186,v188,v189\n"
PK"v187,v190,v191\n"
MF"v[4:7],a[92:95],v[104:107],v[4:7]\n"
BS"v[184:187],v224,s[8:11],s51 offen offset:1024 sc1\n"
MU"v192,v252,v192\n"
MF"v[44:47],a[92:95],v[136:139],v[44:47]\n"
RD"a[120:123],v227 offset:30720\n"
MU"v193,v252,v193\n"
MU"v194,v252,v194\n"
MF"v[44:47],a[88:91],v[128:131],v[44:47]\n"
WT"lgkmcnt(3)\n"
RD"a[124:127],v227 offset:31744\n"
"v_bfe_u32 v238,v224,4,2\n"
MU"v195,v252,v195\n"
MF"v[8:11],a[96:99],v[96:99],v[8:11]\n"
"s_andn2_b32 s63,s61,255\n"
"s_barrier\n"
MF"v[8:11],a[100:103],v[104:107],v[8:11]\n"
RD"a[64:67],v226 offset:0\n"
"v_lshlrev_b32 v238,6,v238\n"
MU"v196,v252,v196\n"
MF"v[48:51],a[100:103],v[136:139],v[48:51]\n"
"buffer_load_dwordx2 v[240:241],v238,s[8:11],s63 offen sc1\n"
MU"v197,v252,v197\n"
MF"v[48:51],a[96:99],v[128:131],v[48:51]\n"
RD"a[68:71],v226 offset:1024\n"
MU"v198,v252,v198\n"
MU"v199,v252,v199\n"
MF"v[12:15],a[104:107],v[96:99],v[12:15]\n"
PK"v192,v192,v193\n"
PK"v193,v194,v195\n"
PK"v194,v196,v197\n"
MF"v[12:15],a[108:111],v[104:107],v[12:15]\n"
RD"a[72:75],v226 offset:2048\n"
PK"v195,v198,v199\n"
MU"v200,v252,v200\n"
MF"v[52:55],a[108:111],v[136:139],v[52:55]\n"
BS"v[192:195],v224,s[8:11],s51 offen offset:2048 sc1\n"
MU"v201,v252,v201\n"
MF"v[52:55],a[104:107],v[128:131],v[52:55]\n"
WT"lgkmcnt(3)\n"
RD"a[76:79],v226 offset:3072\n"
MU"v202,v252,v202\n"
MU"v203,v252,v203\n"
MF"v[16:19],a[112:115],v[96:99],v[16:19]\n"
MU"v204,v252,v204\n"
MU"v205,v252,v205\n"
MU"v206,v252,v206\n"
MF"v[16:19],a[116:119],v[104:107],v[16:19]\n"
RD"a[80:83],v226 offset:4096\n"
MU"v207,v252,v207\n"
PK"v200,v200,v201\n"
MF"v[56:59],a[116:119],v[136:139],v[56:59]\n"
"v_readlane_b32 s43,v228,s42\n"
PK"v201,v202,v203\n"
PK"v202,v204,v205\n"
MF"v[56:59],a[112:115],v[128:131],v[56:59]\n"
RD"a[84:87],v226 offset:5120\n"
PK"v203,v206,v207\n"
MF"v[20:23],a[120:123],v[96:99],v[20:23]\n"
BS"v[200:203],v224,s[8:11],s51 offen offset:3072 sc1\n"
MF"v[20:23],a[124:127],v[104:107],v[20:23]\n"
RD"a[88:91],v226 offset:6144\n"
MF"v[60:63],a[124:127],v[136:139],v[60:63]\n"
"s_mov_b32 m0,s40\n"
MF"v[60:63],a[120:123],v[128:131],v[60:63]\n"
WT"lgkmcnt(3)\n"
RD"a[92:95],v226 offset:7168\n"
MF"v[24:27],a[64:67],v[96:99],v[24:27]\n"
"v_readlane_b32 s44,v229,s42\n"
MF"v[24:27],a[68:71],v[104:107],v[24:27]\n"
RD"a[96:99],v227 offset:0\n"
MF"v[64:67],a[68:71],v[136:139],v[64:67]\n"
SA"s42,s42,1\n"
BL"v225,s[8:11],s43 offen offset:0 sc1 lds\n"
MF"v[64:67],a[64:67],v[128:131],v[64:67]\n"
RD"a[100:103],v227 offset:1024\n"
MF"v[28:31],a[72:75],v[96:99],v[28:31]\n"
MF"v[28:31],a[76:79],v[104:107],v[28:31]\n"
RD"a[104:107],v227 offset:2048\n"
MF"v[68:71],a[76:79],v[136:139],v[68:71]\n"
MF"v[68:71],a[72:75],v[128:131],v[68:71]\n"
WT"lgkmcnt(3)\n"
RD"a[108:111],v227 offset:3072\n"
MF"v[32:35],a[80:83],v[96:99],v[32:35]\n"
BL"v225,s[8:11],s43 offen offset:1024 sc1 lds\n"
MF"v[32:35],a[84:87],v[104:107],v[32:35]\n"
RD"a[112:115],v227 offset:4096\n"
MF"v[72:75],a[84:87],v[136:139],v[72:75]\n"
MF"v[72:75],a[80:83],v[128:131],v[72:75]\n"
RD"a[116:119],v227 offset:5120\n"
MF"v[36:39],a[88:91],v[96:99],v[36:39]\n"
MF"v[36:39],a[92:95],v[104:107],v[36:39]\n"
RD"a[120:123],v227 offset:6144\n"
MF"v[76:79],a[92:95],v[136:139],v[76:79]\n"
BL"v225,s[8:11],s43 offen offset:2048 sc1 lds\n"
MF"v[76:79],a[88:91],v[128:131],v[76:79]\n"
WT"lgkmcnt(3)\n"
RD"a[124:127],v227 offset:7168\n"
MF"v[84:87],a[96:99],v[144:147],0\n"
MF"v[84:87],a[100:103],v[152:155],v[84:87]\n"
RD"a[64:67],v227 offset:8192\n"
MF"v[164:167],a[100:103],v[184:187],0\n"
MF"v[164:167],a[96:99],v[176:179],v[164:167]\n"
RD"a[68:71],v227 offset:9216\n"
MF"v[92:95],a[104:107],v[144:147],0\n"
BL"v225,s[8:11],s43 offen offset:3072 sc1 lds\n"
MF"v[92:95],a[108:111],v[152:155],v[92:95]\n"
RD"a[72:75],v227 offset:10240\n"
MF"v[172:175],a[108:111],v[184:187],0\n"
SA"m0,s40,16384\n"
MF"v[172:175],a[104:107],v[176:179],v[172:175]\n"
WT"lgkmcnt(3)\n"
RD"a[76:79],v227 offset:11264\n"
MF"v[100:103],a[112:115],v[144:147],0\n"
MF"v[100:103],a[116:119],v[152:155],v[100:103]\n"
SA"s48,s59,32768\n"
RD"a[80:83],v227 offset:12288\n"
MF"v[180:183],a[116:119],v[184:187],0\n"
"s_and_b32 s48,s48,0x18000\n"
BL"v225,s[8:11],s44 offen offset:0 sc1 lds\n"
MF"v[180:183],a[112:115],v[176:179],v[180:183]\n"
SA"s40,s40,32768\n"
RD"a[84:87],v227 offset:13312\n"
"s_andn2_b32 s49,s59,0x18000\n"
MF"v[108:111],a[120:123],v[144:147],0\n"
"s_cmp_eq_u32 s40,s46\n"
"s_cselect_b32 s40,s47,s40\n"
WT"vmcnt(7)\n"
"v_cmp_eq_u64 vcc,s[28:29],v[240:241]\n"
"s_cmp_eq_u64 vcc,exec\n"
"s_cbranch_scc0 L1080_%=\n"
"L1081_%=:\n"
MF"v[108:111],a[124:127],v[152:155],v[108:111]\n"
"s_or_b32 s52,s48,s49\n"
RD"a[88:91],v227 offset:14336\n"
SA"s53,s52,16384\n"
MF"v[188:191],a[124:127],v[184:187],0\n"
BL"a[0:3],v224,s[8:11],s52 offen offset:0 sc1\n"
MF"v[188:191],a[120:123],v[176:179],v[188:191]\n"
WT"lgkmcnt(3)\n"
RD"a[92:95],v227 offset:15360\n"
MF"v[116:119],a[64:67],v[144:147],0\n"
BL"v225,s[8:11],s44 offen offset:1024 sc1 lds\n"
MF"v[116:119],a[68:71],v[152:155],v[116:119]\n"
RD"a[96:99],v227 offset:16384\n"
MF"v[196:199],a[68:71],v[184:187],0\n"
BL"a[4:7],v224,s[8:11],s53 offen offset:0 sc1\n"
MF"v[196:199],a[64:67],v[176:179],v[196:199]\n"
RD"a[100:103],v227 offset:17408\n"
MF"v[124:127],a[72:75],v[144:147],0\n"
MF"v[124:127],a[76:79],v[152:155],v[124:127]\n"
RD"a[104:107],v227 offset:18432\n"
MF"v[204:207],a[76:79],v[184:187],0\n"
BL"v225,s[8:11],s44 offen offset:2048 sc1 lds\n"
MF"v[204:207],a[72:75],v[176:179],v[204:207]\n"
WT"lgkmcnt(3)\n"
RD"a[108:111],v227 offset:19456\n"
MF"v[132:135],a[80:83],v[144:147],0\n"
BL"a[8:11],v224,s[8:11],s52 offen offset:1024 sc1\n"
MF"v[132:135],a[84:87],v[152:155],v[132:135]\n"
RD"a[112:115],v227 offset:20480\n"
MF"v[208:211],a[84:87],v[184:187],0\n"
"v_readlane_b32 s43,v228,s42\n"
MF"v[208:211],a[80:83],v[176:179],v[208:211]\n"
RD"a[116:119],v227 offset:21504\n"
MF"v[140:143],a[88:91],v[144:147],0\n"
BL"a[12:15],v224,s[8:11],s53 offen offset:1024 sc1\n"
MF"v[140:143],a[92:95],v[152:155],v[140:143]\n"
BL"v225,s[8:11],s44 offen offset:3072 sc1 lds\n"
MF"v[212:215],a[92:95],v[184:187],0\n"
"s_mov_b32 m0,s40\n"
RD"a[120:123],v227 offset:22528\n"
MF"v[212:215],a[88:91],v[176:179],v[212:215]\n"
WT"lgkmcnt(3)\n"
RD"a[124:127],v227 offset:23552\n"
MF"v[148:151],a[96:99],v[144:147],0\n"
WT"vmcnt(12)\n"
"buffer_store_dwordx2 v[232:233],v230,s[8:11],s62 offen sc1\n"
MF"v[148:151],a[100:103],v[152:155],v[148:151]\n"
BL"a[16:19],v224,s[8:11],s52 offen offset:2048 sc1\n"
MF"v[216:219],a[100:103],v[184:187],0\n"
RD"a[64:67],v227 offset:24576\n"
"v_readlane_b32 s44,v229,s42\n"
MF"v[216:219],a[96:99],v[176:179],v[216:219]\n"
SA"s42,s42,1\n"
BL"v225,s[8:11],s43 offen offset:0 sc1 lds\n"
MF"v[156:159],a[104:107],v[144:147],0\n"
RD"a[68:71],v227 offset:25600\n"
MF"v[156:159],a[108:111],v[152:155],v[156:159]\n"
BL"a[20:23],v224,s[8:11],s53 offen offset:2048 sc1\n"
MF"v[220:223],a[108:111],v[184:187],0\n"
RD"a[72:75],v227 offset:26624\n"
MF"v[220:223],a[104:107],v[176:179],v[220:223]\n"
WT"lgkmcnt(3)\n"
RD"a[76:79],v227 offset:27648\n"
MF"v[84:87],a[112:115],v[160:163],v[84:87]\n"
"s_lshr_b32 s89,s31,3\n"
BL"v225,s[8:11],s43 offen offset:1024 sc1 lds\n"
MF"v[84:87],a[116:119],v[168:171],v[84:87]\n"
"s_lshl_b32 s89,s89,4\n"
RD"a[80:83],v227 offset:28672\n"
MF"v[164:167],a[116:119],v[200:203],v[164:167]\n"
SA"s89,s89,s30\n"
BL"a[24:27],v224,s[8:11],s52 offen offset:3072 sc1\n"
MF"v[164:167],a[112:115],v[192:195],v[164:167]\n"
"s_lshl_b32 s89,s89,3\n"
RD"a[84:87],v227 offset:29696\n"
MF"v[92:95],a[120:123],v[160:163],v[92:95]\n"
"s_xor_b32 s23,s32,s30\n"
MF"v[92:95],a[124:127],v[168:171],v[92:95]\n"
SA"s48,s59,65536\n"
RD"a[88:91],v227 offset:30720\n"
SA"s89,s89,s23\n"
MF"v[172:175],a[124:127],v[200:203],v[172:175]\n"
"s_and_b32 s48,s48,0x18000\n"
BL"a[28:31],v224,s[8:11],s53 offen offset:3072 sc1\n"
MF"v[172:175],a[120:123],v[192:195],v[172:175]\n"
WT"lgkmcnt(3)\n"
BL"v225,s[8:11],s43 offen offset:2048 sc1 lds\n"
MF"v[100:103],a[64:67],v[160:163],v[100:103]\n"
"s_xor_b32 s90,s89,36\n"
RD"a[92:95],v227 offset:31744\n"
"s_andn2_b32 s49,s59,0x18000\n"
MF"v[100:103],a[68:71],v[168:171],v[100:103]\n"
"s_mul_i32 s88,s89,5120\n"
RD"a[96:99],v226 offset:0\n"
"s_or_b32 s52,s48,s49\n"
MF"v[180:183],a[68:71],v[200:203],v[180:183]\n"
SA"s53,s52,16384\n"
BL"a[180:183],v224,s[8:11],s52 offen offset:0 sc1\n"
MF"v[180:183],a[64:67],v[192:195],v[180:183]\n"
"s_mul_i32 s22,s90,5120\n"
RD"a[100:103],v226 offset:1024\n"
"s_sub_u32 s90,s90,s89\n"
MF"v[108:111],a[72:75],v[160:163],v[108:111]\n"
"s_lshl_b32 s90,s90,6\n"
BL"v225,s[8:11],s43 offen offset:3072 sc1 lds\n"
MF"v[108:111],a[76:79],v[168:171],v[108:111]\n"
SA"s88,s88,59506688\n"
RD"a[104:107],v226 offset:2048\n"
SA"s22,s22,59506688\n"
MF"v[188:191],a[76:79],v[200:203],v[188:191]\n"
"s_cmp_eq_u32 s23,0\n"
BL"a[184:187],v224,s[8:11],s53 offen offset:0 sc1\n"
MF"v[188:191],a[72:75],v[192:195],v[188:191]\n"
WT"lgkmcnt(3)\n"
RD"a[108:111],v226 offset:3072\n"
"s_cselect_b32 s88,2130706432,s88\n"
MF"v[116:119],a[80:83],v[160:163],v[116:119]\n"
SA"m0,s40,16384\n"
"s_cmp_eq_u32 s23,4\n"
"s_cselect_b32 s22,2130706432,s22\n"
MF"v[116:119],a[84:87],v[168:171],v[116:119]\n"
"s_lshl_b32 s89,s89,6\n"
RD"a[112:115],v226 offset:4096\n"
"v_bfe_u32 v239,v224,1,6\n"
MF"v[196:199],a[84:87],v[200:203],v[196:199]\n"
SA"s89,s89,80478208\n"
BL"v225,s[8:11],s44 offen offset:0 sc1 lds\n"
MF"v[196:199],a[80:83],v[192:195],v[196:199]\n"
SA"s40,s40,32768\n"
RD"a[116:119],v226 offset:5120\n"
"v_bfe_u32 v242,v224,7,1\n"
MF"v[124:127],a[88:91],v[160:163],v[124:127]\n"
"s_cmp_eq_u32 s40,s46\n"
BL"a[188:191],v224,s[8:11],s52 offen offset:1024 sc1\n"
MF"v[124:127],a[92:95],v[168:171],v[124:127]\n"
"s_cselect_b32 s40,s47,s40\n"
RD"a[120:123],v226 offset:6144\n"
"v_mad_u32_u24 v239,v242,s90,v239\n"
MF"v[204:207],a[92:95],v[200:203],v[204:207]\n"
"v_lshrrev_b32 v242,8,v224\n"
"v_lshl_add_u32 v239,v242,27,v239\n"
MF"v[204:207],a[88:91],v[192:195],v[204:207]\n"
WT"lgkmcnt(3)\n"
RD"a[124:127],v226 offset:7168\n"
MF"v[132:135],a[96:99],v[160:163],v[132:135]\n"
BL"a[192:195],v224,s[8:11],s53 offen offset:1024 sc1\n"
MF"v[132:135],a[100:103],v[168:171],v[132:135]\n"
BL"v225,s[8:11],s44 offen offset:1024 sc1 lds\n"
MF"v[208:211],a[100:103],v[200:203],v[208:211]\n"
RD"a[64:67],v226 offset:8192\n"
MF"v[208:211],a[96:99],v[192:195],v[208:211]\n"
RD"a[68:71],v226 offset:9216\n"
MF"v[140:143],a[104:107],v[160:163],v[140:143]\n"
BL"a[196:199],v224,s[8:11],s52 offen offset:2048 sc1\n"
MF"v[140:143],a[108:111],v[168:171],v[140:143]\n"
RD"a[72:75],v226 offset:10240\n"
MF"v[212:215],a[108:111],v[200:203],v[212:215]\n"
BL"v225,s[8:11],s44 offen offset:2048 sc1 lds\n"
MF"v[212:215],a[104:107],v[192:195],v[212:215]\n"
WT"lgkmcnt(3)\n"
RD"a[76:79],v226 offset:11264\n"
MF"v[148:151],a[112:115],v[160:163],v[148:151]\n"
BL"a[200:203],v224,s[8:11],s53 offen offset:2048 sc1\n"
MF"v[148:151],a[116:119],v[168:171],v[148:151]\n"
RD"a[80:83],v226 offset:12288\n"
MF"v[216:219],a[116:119],v[200:203],v[216:219]\n"
MF"v[216:219],a[112:115],v[192:195],v[216:219]\n"
RD"a[84:87],v226 offset:13312\n"
MF"v[156:159],a[120:123],v[160:163],v[156:159]\n"
BL"v225,s[8:11],s44 offen offset:3072 sc1 lds\n"
MF"v[156:159],a[124:127],v[168:171],v[156:159]\n"
RD"a[88:91],v226 offset:14336\n"
MF"v[220:223],a[124:127],v[200:203],v[220:223]\n"
BL"a[204:207],v224,s[8:11],s52 offen offset:3072 sc1\n"
MF"v[220:223],a[120:123],v[192:195],v[220:223]\n"
WT"vmcnt(26) lgkmcnt(3)\n"
RD"a[92:95],v226 offset:15360\n"
MF"v[0:3],a[64:67],a[0:3],v[0:3]\n"
WT"vmcnt(22)\n"
MF"v[0:3],a[68:71],a[8:11],v[0:3]\n"
WT"vmcnt(21)\n"
RD"a[96:99],v226 offset:16384\n"
SA"s48,s59,98304\n"
MF"v[40:43],a[68:71],a[12:15],v[40:43]\n"
"s_and_b32 s48,s48,0x18000\n"
BL"a[208:211],v224,s[8:11],s53 offen offset:3072 sc1\n"
MF"v[40:43],a[64:67],a[4:7],v[40:43]\n"
"s_andn2_b32 s49,s59,0x18000\n"
RD"a[100:103],v226 offset:17408\n"
MF"v[4:7],a[72:75],a[0:3],v[4:7]\n"
"s_or_b32 s52,s48,s49\n"
SA"s53,s52,16384\n"
MF"v[4:7],a[76:79],a[8:11],v[4:7]\n"
RD"a[104:107],v226 offset:18432\n"
MF"v[44:47],a[76:79],a[12:15],v[44:47]\n"
BL"a[212:215],v224,s[8:11],s52 offen offset:0 sc1\n"
MF"v[44:47],a[72:75],a[4:7],v[44:47]\n"
WT"lgkmcnt(3)\n"
RD"a[108:111],v226 offset:19456\n"
MF"v[8:11],a[80:83],a[0:3],v[8:11]\n"
MF"v[8:11],a[84:87],a[8:11],v[8:11]\n"
RD"a[112:115],v226 offset:20480\n"
MF"v[48:51],a[84:87],a[12:15],v[48:51]\n"
BL"a[216:219],v224,s[8:11],s53 offen offset:0 sc1\n"
MF"v[48:51],a[80:83],a[4:7],v[48:51]\n"
RD"a[116:119],v226 offset:21504\n"
MF"v[12:15],a[88:91],a[0:3],v[12:15]\n"
MF"v[12:15],a[92:95],a[8:11],v[12:15]\n"
RD"a[120:123],v226 offset:22528\n"
MF"v[52:55],a[92:95],a[12:15],v[52:55]\n"
MF"v[52:55],a[88:91],a[4:7],v[52:55]\n"
WT"lgkmcnt(3)\n"
RD"a[124:127],v226 offset:23552\n"
MF"v[16:19],a[96:99],a[0:3],v[16:19]\n"
BL"a[220:223],v224,s[8:11],s52 offen offset:1024 sc1\n"
MF"v[16:19],a[100:103],a[8:11],v[16:19]\n"
RD"a[64:67],v226 offset:24576\n"
MF"v[56:59],a[100:103],a[12:15],v[56:59]\n"
MF"v[56:59],a[96:99],a[4:7],v[56:59]\n"
RD"a[68:71],v226 offset:25600\n"
MF"v[20:23],a[104:107],a[0:3],v[20:23]\n"
BL"a[224:227],v224,s[8:11],s53 offen offset:1024 sc1\n"
MF"v[20:23],a[108:111],a[8:11],v[20:23]\n"
RD"a[72:75],v226 offset:26624\n"
MF"v[60:63],a[108:111],a[12:15],v[60:63]\n"
MF"v[60:63],a[104:107],a[4:7],v[60:63]\n"
WT"lgkmcnt(3)\n"
RD"a[76:79],v226 offset:27648\n"
MF"v[24:27],a[112:115],a[0:3],v[24:27]\n"
BL"a[228:231],v224,s[8:11],s52 offen offset:2048 sc1\n"
MF"v[24:27],a[116:119],a[8:11],v[24:27]\n"
RD"a[80:83],v226 offset:28672\n"
MF"v[64:67],a[116:119],a[12:15],v[64:67]\n"
MF"v[64:67],a[112:115],a[4:7],v[64:67]\n"
RD"a[84:87],v226 offset:29696\n"
MF"v[28:31],a[120:123],a[0:3],v[28:31]\n"
BL"a[232:235],v224,s[8:11],s53 offen offset:2048 sc1\n"
MF"v[28:31],a[124:127],a[8:11],v[28:31]\n"
RD"a[88:91],v226 offset:30720\n"
MF"v[68:71],a[124:127],a[12:15],v[68:71]\n"
MF"v[68:71],a[120:123],a[4:7],v[68:71]\n"
WT"lgkmcnt(3)\n"
RD"a[92:95],v226 offset:31744\n"
MF"v[32:35],a[64:67],a[0:3],v[32:35]\n"
"s_barrier\n"
MF"v[32:35],a[68:71],a[8:11],v[32:35]\n"
RD"a[96:99],v235 offset:0\n"
MF"v[72:75],a[68:71],a[12:15],v[72:75]\n"
BL"a[236:239],v224,s[8:11],s52 offen offset:3072 sc1\n"
MF"v[72:75],a[64:67],a[4:7],v[72:75]\n"
RD"a[100:103],v235 offset:1024\n"
MF"v[36:39],a[72:75],a[0:3],v[36:39]\n"
MF"v[36:39],a[76:79],a[8:11],v[36:39]\n"
RD"a[104:107],v235 offset:2048\n"
MF"v[76:79],a[76:79],a[12:15],v[76:79]\n"
BL"a[240:243],v224,s[8:11],s53 offen offset:3072 sc1\n"
MF"v[76:79],a[72:75],a[4:7],v[76:79]\n"
WT"vmcnt(27) lgkmcnt(3)\n"
RD"a[108:111],v235 offset:3072\n"
MF"v[0:3],a[80:83],a[16:19],v[0:3]\n"
WT"vmcnt(23)\n"
MF"v[0:3],a[84:87],a[24:27],v[0:3]\n"
WT"vmcnt(22)\n"
RD"a[112:115],v235 offset:4096\n"
MF"v[40:43],a[84:87],a[28:31],v[40:43]\n"
MF"v[40:43],a[80:83],a[20:23],v[40:43]\n"
RD"a[116:119],v235 offset:5120\n"
MF"v[4:7],a[88:91],a[16:19],v[4:7]\n"
MF"v[4:7],a[92:95],a[24:27],v[4:7]\n"
RD"a[120:123],v235 offset:6144\n"
MF"v[44:47],a[92:95],a[28:31],v[44:47]\n"
MF"v[44:47],a[88:91],a[20:23],v[44:47]\n"
WT"lgkmcnt(3)\n"
RD"a[124:127],v235 offset:7168\n"
MF"v[8:11],a[96:99],a[16:19],v[8:11]\n"
MF"v[8:11],a[100:103],a[24:27],v[8:11]\n"
RD"a[64:67],v235 offset:8192\n"
MF"v[48:51],a[100:103],a[28:31],v[48:51]\n"
MF"v[48:51],a[96:99],a[20:23],v[48:51]\n"
RD"a[68:71],v235 offset:9216\n"
MF"v[12:15],a[104:107],a[16:19],v[12:15]\n"
MF"v[12:15],a[108:111],a[24:27],v[12:15]\n"
RD"a[72:75],v235 offset:10240\n"
"v_bfe_u32 v238,v224,4,2\n"
MF"v[52:55],a[108:111],a[28:31],v[52:55]\n"
"s_andn2_b32 s63,s62,255\n"
"v_lshlrev_b32 v238,6,v238\n"
MF"v[52:55],a[104:107],a[20:23],v[52:55]\n"
WT"lgkmcnt(3)\n"
RD"a[76:79],v235 offset:11264\n"
MF"v[16:19],a[112:115],a[16:19],v[16:19]\n"
"buffer_load_dwordx2 v[240:241],v238,s[8:11],s63 offen sc1\n"
MF"v[16:19],a[116:119],a[24:27],v[16:19]\n"
RD"a[80:83],v235 offset:12288\n"
MF"v[56:59],a[116:119],a[28:31],v[56:59]\n"
MF"v[56:59],a[112:115],a[20:23],v[56:59]\n"
RD"a[84:87],v235 offset:13312\n"
MF"v[20:23],a[120:123],a[16:19],v[20:23]\n"
MF"v[20:23],a[124:127],a[24:27],v[20:23]\n"
RD"a[88:91],v235 offset:14336\n"
MF"v[60:63],a[124:127],a[28:31],v[60:63]\n"
MF"v[60:63],a[120:123],a[20:23],v[60:63]\n"
RD"a[92:95],v235 offset:15360\n"
"s_branch LTJ_%=\n"
"L1099_%=:\n"
"s_mov_b32 s54,0\n"
"L1101_%=:\n"
"s_sleep 2\n"
BL"v[240:243],v238,s[8:11],0 offen sc1\n"
WT"vmcnt(0)\n"
SA"s54,s54,1\n"
"v_cmp_eq_u64 vcc,s[28:29],v[240:241]\n"
"v_cmp_eq_u64_e64 s[50:51],s[28:29],v[242:243]\n"
"s_and_b64 vcc,vcc,s[50:51]\n"
"s_cmp_eq_u64 vcc,exec\n"
"s_cbranch_scc1 L1100_%=\n"
"s_cmp_lt_u32 s54,1048576\n"
"s_cbranch_scc1 L1101_%=\n"
"s_branch L1100_%=\n"
"L1102_%=:\n"
"s_mov_b32 s54,0\n"
"L1104_%=:\n"
"s_sleep 2\n"
"buffer_load_dwordx2 v[240:241],v238,s[8:11],s50 offen sc1\n"
WT"vmcnt(0)\n"
SA"s54,s54,1\n"
"v_cmp_eq_u64 vcc,s[28:29],v[240:241]\n"
"s_cmp_eq_u64 vcc,exec\n"
"s_cbranch_scc1 L1103_%=\n"
"s_cmp_lt_u32 s54,1048576\n"
"s_cbranch_scc1 L1104_%=\n"
"s_branch L1103_%=\n"
"L1105_%=:\n"
"s_mov_b32 s54,0\n"
"L1107_%=:\n"
"s_sleep 2\n"
"buffer_load_dwordx2 v[244:245],v242,s[8:11],s23 offen sc1\n"
WT"vmcnt(0)\n"
SA"s54,s54,1\n"
"v_cmp_eq_u64 vcc,s[28:29],v[244:245]\n"
"s_cmp_eq_u64 vcc,exec\n"
"s_cbranch_scc1 L1106_%=\n"
"s_cmp_lt_u32 s54,1048576\n"
"s_cbranch_scc1 L1107_%=\n"
"s_branch L1106_%=\n"
"LFA_%=:\n"
WT"lgkmcnt(6)\n"
MF"v[0:3],a[64:67],a[0:3],v[208:211]\n"
WT"lgkmcnt(3)\n"
PK"v192,v192,v193\n"
PK"v193,v194,v195\n"
MF"v[0:3],a[68:71],a[4:7],v[0:3]\n"
"s_mov_b32 m0,s40\n"
"v_readlane_b32 s43,v228,s42\n"
PK"v194,v196,v197\n"
MF"v[8:11],a[68:71],a[20:23],0\n"
PK"v195,v198,v199\n"
"v_readlane_b32 s44,v229,s42\n"
MF"v[8:11],a[64:67],a[16:19],v[8:11]\n"
SA"s42,s42,1\n"
BL"v225,s[8:11],s43 offen offset:0 sc1 lds\n"
MF"v[16:19],a[64:67],a[32:35],0\n"
"v_accvgpr_write_b32 a44,v192\n"
"v_accvgpr_write_b32 a45,v193\n"
MF"v[16:19],a[68:71],a[36:39],v[16:19]\n"
RD"a[92:95],v226 offset:7168\n"
"v_accvgpr_write_b32 a46,v194\n"
MF"v[24:27],a[68:71],a[52:55],0\n"
"v_accvgpr_write_b32 a47,v195\n"
PK"v200,v200,v201\n"
MF"v[24:27],a[64:67],a[48:51],v[24:27]\n"
PK"v201,v202,v203\n"
PK"v202,v204,v205\n"
"v_mov_b32 v227,v226\n"
MF"v[0:3],a[72:75],a[8:11],v[0:3]\n"
BL"v225,s[8:11],s43 offen offset:1024 sc1 lds\n"
PK"v203,v206,v207\n"
MF"v[0:3],a[76:79],a[12:15],v[0:3]\n"
"v_accvgpr_write_b32 a60,v200\n"
"v_accvgpr_write_b32 a61,v201\n"
MF"v[8:11],a[76:79],a[28:31],v[8:11]\n"
"s_mul_i32 s48,s32,655360\n"
"v_accvgpr_write_b32 a62,v202\n"
"v_accvgpr_write_b32 a63,v203\n"
MF"v[8:11],a[72:75],a[24:27],v[8:11]\n"
SA"s12,s74,s48\n"
"s_addc_u32 s13,s75,0\n"
MF"v[16:19],a[72:75],a[40:43],v[16:19]\n"
"s_and_b32 s13,s13,0xffff\n"
MF"v[16:19],a[76:79],a[44:47],v[16:19]\n"
"s_mov_b32 s14,655360\n"
RD"a[96:99],v226 offset:8192\n"
MF"v[24:27],a[76:79],a[60:63],v[24:27]\n"
"s_lshl_b32 s48,s33,3\n"
BL"v225,s[8:11],s43 offen offset:2048 sc1 lds\n"
MF"v[24:27],a[72:75],a[56:59],v[24:27]\n"
WT"lgkmcnt(1)\n"
EX"v0,v0\n"
MF"v[4:7],a[80:83],a[0:3],v[212:215]\n"
"s_and_b32 s49,s34,2\n"
EX"v1,v1\n"
MF"v[4:7],a[84:87],a[4:7],v[4:7]\n"
"s_lshl_b32 s49,s49,1\n"
EX"v2,v2\n"
MF"v[12:15],a[84:87],a[20:23],0\n"
SA"s48,s48,s49\n"
RD"a[100:103],v226 offset:9216\n"
PK"v0,v0,v1\n"
MF"v[12:15],a[80:83],a[16:19],v[12:15]\n"
SA"s48,s48,s30\n"
EX"v3,v3\n"
MF"v[20:23],a[80:83],a[32:35],0\n"
"s_mul_i32 s48,s48,5120\n"
EX"v8,v8\n"
MF"v[20:23],a[84:87],a[36:39],v[20:23]\n"
"s_or_b32 s49,s34,2\n"
BL"v225,s[8:11],s43 offen offset:3072 sc1 lds\n"
MF"v[28:31],a[84:87],a[52:55],0\n"
"s_cmp_eq_u32 s49,7\n"
EX"v9,v9\n"
PK"v1,v2,v3\n"
MF"v[28:31],a[80:83],a[48:51],v[28:31]\n"
"s_cselect_b32 s55,s48,2130706432\n"
RD"a[104:107],v226 offset:10240\n"
PK"v8,v8,v9\n"
MF"v[4:7],a[88:91],a[8:11],v[4:7]\n"
SA"m0,s40,16384\n"
EX"v10,v10\n"
MF"v[4:7],a[92:95],a[12:15],v[4:7]\n"
EX"v11,v11\n"
MF"v[12:15],a[92:95],a[28:31],v[12:15]\n"
EX"v16,v16\n"
MF"v[12:15],a[88:91],a[24:27],v[12:15]\n"
SA"s40,s40,32768\n"
RD"a[108:111],v226 offset:11264\n"
PK"v9,v10,v11\n"
MF"v[20:23],a[88:91],a[40:43],v[20:23]\n"
"s_cmp_eq_u32 s40,s46\n"
BL"v225,s[8:11],s44 offen offset:0 sc1 lds\n"
EX"v17,v17\n"
MF"v[20:23],a[92:95],a[44:47],v[20:23]\n"
"s_cselect_b32 s40,s47,s40\n"
EX"v18,v18\n"
MF"v[28:31],a[92:95],a[60:63],v[28:31]\n"
EX"v19,v19\n"
PK"v16,v16,v17\n"
MF"v[28:31],a[88:91],a[56:59],v[28:31]\n"
WT"lgkmcnt(1)\n"
RD"a[112:115],v226 offset:12288\n"
PK"v17,v18,v19\n"
MF"v[32:35],a[96:99],a[0:3],v[216:219]\n"
EX"v24,v24\n"
MF"v[32:35],a[100:103],a[4:7],v[32:35]\n"
EX"v25,v25\n"
MF"v[40:43],a[100:103],a[20:23],0\n"
BL"v225,s[8:11],s44 offen offset:1024 sc1 lds\n"
EX"v26,v26\n"
MF"v[40:43],a[96:99],a[16:19],v[40:43]\n"
RD"a[116:119],v226 offset:13312\n"
PK"v24,v24,v25\n"
MF"v[48:51],a[96:99],a[32:35],0\n"
EX"v27,v27\n"
MF"v[48:51],a[100:103],a[36:39],v[48:51]\n"
EX"v4,v4\n"
MF"v[56:59],a[100:103],a[52:55],0\n"
EX"v5,v5\n"
PK"v25,v26,v27\n"
MF"v[56:59],a[96:99],a[48:51],v[56:59]\n"
RD"a[120:123],v226 offset:14336\n"
PK"v2,v4,v5\n"
MF"v[32:35],a[104:107],a[8:11],v[32:35]\n"
WT"lgkmcnt(2)\n"
BL"v225,s[8:11],s44 offen offset:2048 sc1 lds\n"
EX"v6,v6\n"
MF"v[32:35],a[108:111],a[12:15],v[32:35]\n"
EX"v7,v7\n"
MF"v[40:43],a[108:111],a[28:31],v[40:43]\n"
EX"v12,v12\n"
MF"v[40:43],a[104:107],a[24:27],v[40:43]\n"
RD"a[124:127],v226 offset:15360\n"
"v_readlane_b32 s43,v228,s42\n"
PK"v3,v6,v7\n"
MF"v[48:51],a[104:107],a[40:43],v[48:51]\n"
EX"v13,v13\n"
MF"v[48:51],a[108:111],a[44:47],v[48:51]\n"
EX"v14,v14\n"
MF"v[56:59],a[108:111],a[60:63],v[56:59]\n"
BL"v225,s[8:11],s44 offen offset:3072 sc1 lds\n"
EX"v15,v15\n"
MF"v[56:59],a[104:107],a[56:59],v[56:59]\n"
"s_mov_b32 m0,s40\n"
RD"a[96:99],v227 offset:24576\n"
PK"v10,v12,v13\n"
MF"v[36:39],a[112:115],a[0:3],v[220:223]\n"
WT"lgkmcnt(2)\n"
EX"v20,v20\n"
PK"v11,v14,v15\n"
MF"v[36:39],a[116:119],a[4:7],v[36:39]\n"
EX"v21,v21\n"
MF"v[44:47],a[116:119],a[20:23],0\n"
EX"v22,v22\n"
MF"v[44:47],a[112:115],a[16:19],v[44:47]\n"
RD"a[100:103],v227 offset:25600\n"
"v_readlane_b32 s44,v229,s42\n"
PK"v18,v20,v21\n"
MF"v[52:55],a[112:115],a[32:35],0\n"
SA"s42,s42,1\n"
BL"v225,s[8:11],s43 offen offset:0 sc1 lds\n"
MF"v[52:55],a[116:119],a[36:39],v[52:55]\n"
EX"v23,v23\n"
MF"v[60:63],a[116:119],a[52:55],0\n"
EX"v28,v28\n"
MF"v[60:63],a[112:115],a[48:51],v[60:63]\n"
RD"a[104:107],v227 offset:26624\n"
PK"v19,v22,v23\n"
MF"v[36:39],a[120:123],a[8:11],v[36:39]\n"
WT"lgkmcnt(2)\n"
EX"v29,v29\n"
MF"v[36:39],a[124:127],a[12:15],v[36:39]\n"
BL"v225,s[8:11],s43 offen offset:1024 sc1 lds\n"
MF"v[44:47],a[124:127],a[28:31],v[44:47]\n"
EX"v30,v30\n"
PK"v26,v28,v29\n"
MF"v[44:47],a[120:123],a[24:27],v[44:47]\n"
RD"a[108:111],v227 offset:27648\n"
MF"v[52:55],a[120:123],a[40:43],v[52:55]\n"
EX"v31,v31\n"
MF"v[52:55],a[124:127],a[44:47],v[52:55]\n"
EX"v32,v32\n"
MF"v[60:63],a[124:127],a[60:63],v[60:63]\n"
EX"v33,v33\n"
PK"v27,v30,v31\n"
MF"v[60:63],a[120:123],a[56:59],v[60:63]\n"
SA"s41,s41,32768\n"
BL"v225,s[8:11],s43 offen offset:2048 sc1 lds\n"
RD"a[112:115],v227 offset:28672\n"
MF"v[80:83],a[96:99],v[0:3],0\n"
"s_cmp_eq_u32 s41,163840\n"
EX"v34,v34\n"
MF"v[112:115],a[96:99],v[8:11],0\n"
"s_cselect_b32 s41,0,s41\n"
EX"v35,v35\n"
MF"v[144:147],a[96:99],v[16:19],0\n"
EX"v40,v40\n"
"v_add_u32 v226,s41,v224\n"
MF"v[176:179],a[96:99],v[24:27],0\n"
WT"lgkmcnt(1)\n"
RD"a[116:119],v227 offset:29696\n"
MF"v[84:87],a[100:103],v[0:3],0\n"
EX"v41,v41\n"
MF"v[116:119],a[100:103],v[8:11],0\n"
BL"v225,s[8:11],s43 offen offset:3072 sc1 lds\n"
MF"v[148:151],a[100:103],v[16:19],0\n"
SA"m0,s40,16384\n"
EX"v42,v42\n"
MF"v[180:183],a[100:103],v[24:27],0\n"
RD"a[120:123],v227 offset:30720\n"
MF"v[88:91],a[104:107],v[0:3],0\n"
EX"v43,v43\n"
MF"v[120:123],a[104:107],v[8:11],0\n"
EX"v48,v48\n"
MF"v[152:155],a[104:107],v[16:19],0\n"
SA"s40,s40,32768\n"
EX"v49,v49\n"
MF"v[184:187],a[104:107],v[24:27],0\n"
"s_cmp_eq_u32 s40,s46\n"
BL"v225,s[8:11],s44 offen offset:0 sc1 lds\n"
RD"a[124:127],v227 offset:31744\n"
MF"v[92:95],a[108:111],v[0:3],0\n"
"s_cselect_b32 s40,s47,s40\n"
EX"v50,v50\n"
MF"v[124:127],a[108:111],v[8:11],0\n"
EX"v51,v51\n"
MF"v[156:159],a[108:111],v[16:19],0\n"
EX"v56,v56\n"
MF"v[188:191],a[108:111],v[24:27],0\n"
WT"lgkmcnt(1)\n"
EX"v57,v57\n"
MF"v[96:99],a[112:115],v[0:3],0\n"
EX"v58,v58\n"
MF"v[128:131],a[112:115],v[8:11],0\n"
BL"v225,s[8:11],s44 offen offset:1024 sc1 lds\n"
MF"v[160:163],a[112:115],v[16:19],0\n"
EX"v59,v59\n"
MF"v[192:195],a[112:115],v[24:27],0\n"
EX"v36,v36\n"
MF"v[100:103],a[116:119],v[0:3],0\n"
WT"vmcnt(16) lgkmcnt(0)\n"
"s_barrier\n"
MF"v[132:135],a[116:119],v[8:11],0\n"
RD"a[64:67],v226 offset:0\n"
RD"a[68:71],v226 offset:1024\n"
"buffer_store_dwordx2 v[232:233],v230,s[8:11],s98 offen sc1\n"
MF"v[164:167],a[116:119],v[16:19],0\n"
EX"v37,v37\n"
MF"v[196:199],a[116:119],v[24:27],0\n"
BL"v225,s[8:11],s44 offen offset:2048 sc1 lds\n"
MF"v[104:107],a[120:123],v[0:3],0\n"
EX"v38,v38\n"
MF"v[136:139],a[120:123],v[8:11],0\n"
EX"v39,v39\n"
MF"v[168:171],a[120:123],v[16:19],0\n"
EX"v44,v44\n"
MF"v[200:203],a[120:123],v[24:27],0\n"
RD"a[72:75],v226 offset:2048\n"
RD"a[76:79],v226 offset:3072\n"
MF"v[108:111],a[124:127],v[0:3],0\n"
EX"v45,v45\n"
MF"v[140:143],a[124:127],v[8:11],0\n"
BL"v225,s[8:11],s44 offen offset:3072 sc1 lds\n"
MF"v[172:175],a[124:127],v[16:19],0\n"
EX"v46,v46\n"
MF"v[204:207],a[124:127],v[24:27],0\n"
RD"a[80:83],v226 offset:4096\n"
MF"a[132:135],a[128:131],v[0:3],0\n"
EX"v47,v47\n"
MF"a[136:139],a[128:131],v[8:11],0\n"
"buffer_load_dword v255,v253,s[12:15],s55 offen\n"
EX"v52,v52\n"
MF"a[140:143],a[128:131],v[16:19],0\n"
SA"s55,s55,163840\n"
EX"v53,v53\n"
MF"a[144:147],a[128:131],v[24:27],0\n"
RD"a[84:87],v226 offset:5120\n"
RD"a[88:91],v226 offset:6144\n"
EX"v54,v54\n"
EX"v55,v55\n"
EX"v60,v60\n"
EX"v61,v61\n"
EX"v62,v62\n"
EX"v63,v63\n"
"s_branch LDB_%=\n"
"LD2_%=:\n"
WT"lgkmcnt(6)\n"
MF"v[0:3],a[64:67],a[32:35],v[208:211]\n"
WT"lgkmcnt(3)\n"
EX"v56,v56\n"
MF"v[0:3],a[68:71],a[36:39],v[0:3]\n"
"s_mov_b32 m0,s40\n"
EX"v57,v57\n"
MF"v[8:11],a[68:71],a[52:55],0\n"
RD"a[92:95],v226 offset:7168\n"
"v_readlane_b32 s43,v228,s42\n"
MF"v[8:11],a[64:67],a[48:51],v[8:11]\n"
EX"v58,v58\n"
MF"v[0:3],a[72:75],a[40:43],v[0:3]\n"
SA"s41,s41,32768\n"
RD"a[96:99],v226 offset:8192\n"
RD"a[100:103],v226 offset:9216\n"
"v_readlane_b32 s44,v229,s42\n"
MF"v[0:3],a[76:79],a[44:47],v[0:3]\n"
SA"s42,s42,1\n"
BL"v225,s[8:11],s43 offen offset:0 sc1 lds\n"
PK"v76,v56,v57\n"
MF"v[8:11],a[76:79],a[60:63],v[8:11]\n"
"s_cmp_eq_u32 s41,163840\n"
EX"v59,v59\n"
MF"v[8:11],a[72:75],a[56:59],v[8:11]\n"
WT"lgkmcnt(3)\n"
RD"a[104:107],v226 offset:10240\n"
PK"v77,v58,v59\n"
MF"v[4:7],a[80:83],a[32:35],v[212:215]\n"
"s_cselect_b32 s41,0,s41\n"
EX"v60,v60\n"
"v_add_u32 v227,s41,v224\n"
MF"v[4:7],a[84:87],a[36:39],v[4:7]\n"
EX"v61,v61\n"
MF"v[12:15],a[84:87],a[52:55],0\n"
BL"v225,s[8:11],s43 offen offset:1024 sc1 lds\n"
MF"v[12:15],a[80:83],a[48:51],v[12:15]\n"
RD"a[108:111],v226 offset:11264\n"
RD"a[112:115],v226 offset:12288\n"
MF"v[4:7],a[88:91],a[40:43],v[4:7]\n"
WT"lgkmcnt(3)\n"
EX"v62,v62\n"
PK"v78,v60,v61\n"
MF"v[4:7],a[92:95],a[44:47],v[4:7]\n"
RD"a[116:119],v226 offset:13312\n"
RD"a[120:123],v226 offset:14336\n"
MF"v[12:15],a[92:95],a[60:63],v[12:15]\n"
EX"v63,v63\n"
MF"v[12:15],a[88:91],a[56:59],v[12:15]\n"
BL"v225,s[8:11],s43 offen offset:2048 sc1 lds\n"
MF"v[16:19],a[96:99],a[32:35],v[216:219]\n"
EX"v0,v0\n"
PK"v79,v62,v63\n"
MF"v[16:19],a[100:103],a[36:39],v[16:19]\n"
EX"v1,v1\n"
MF"v[24:27],a[100:103],a[52:55],0\n"
RD"a[124:127],v226 offset:15360\n"
RD"a[64:67],v226 offset:16384\n"
MF"v[24:27],a[96:99],a[48:51],v[24:27]\n"
WT"lgkmcnt(4)\n"
EX"v2,v2\n"
PK"v0,v0,v1\n"
MF"v[16:19],a[104:107],a[40:43],v[16:19]\n"
BL"v225,s[8:11],s43 offen offset:3072 sc1 lds\n"
RD"a[68:71],v226 offset:17408\n"
MF"v[16:19],a[108:111],a[44:47],v[16:19]\n"
SA"m0,s40,16384\n"
RD"a[72:75],v226 offset:18432\n"
RD"a[76:79],v226 offset:19456\n"
EX"v3,v3\n"
MF"v[24:27],a[108:111],a[60:63],v[24:27]\n"
RD"a[80:83],v226 offset:20480\n"
RD"a[84:87],v226 offset:21504\n"
MF"v[24:27],a[104:107],a[56:59],v[24:27]\n"
EX"v8,v8\n"
PK"v1,v2,v3\n"
MF"v[20:23],a[112:115],a[32:35],v[220:223]\n"
WT"lgkmcnt(7)\n"
EX"v9,v9\n"
MF"v[20:23],a[116:119],a[36:39],v[20:23]\n"
SA"s40,s40,32768\n"
BL"v225,s[8:11],s44 offen offset:0 sc1 lds\n"
MF"v[28:31],a[116:119],a[52:55],0\n"
"s_cmp_eq_u32 s40,s46\n"
RD"a[88:91],v226 offset:22528\n"
RD"a[92:95],v226 offset:23552\n"
EX"v10,v10\n"
MF"v[28:31],a[112:115],a[48:51],v[28:31]\n"
"s_cselect_b32 s40,s47,s40\n"
RD"a[96:99],v226 offset:24576\n"
RD"a[100:103],v226 offset:25600\n"
PK"v8,v8,v9\n"
MF"v[20:23],a[120:123],a[40:43],v[20:23]\n"
WT"lgkmcnt(6)\n"
EX"v11,v11\n"
MF"v[20:23],a[124:127],a[44:47],v[20:23]\n"
RD"a[104:107],v226 offset:26624\n"
RD"a[108:111],v226 offset:27648\n"
MF"v[28:31],a[124:127],a[60:63],v[28:31]\n"
BL"v225,s[8:11],s44 offen offset:1024 sc1 lds\n"
EX"v4,v4\n"
MF"v[28:31],a[120:123],a[56:59],v[28:31]\n"
RD"a[112:115],v226 offset:28672\n"
RD"a[116:119],v226 offset:29696\n"
PK"v9,v10,v11\n"
MF"v[40:43],a[64:67],a[48:51],v[208:211]\n"
EX"v5,v5\n"
MF"v[40:43],a[68:71],a[52:55],v[40:43]\n"
EX"v6,v6\n"
MF"v[40:43],a[72:75],a[56:59],v[40:43]\n"
RD"a[120:123],v226 offset:30720\n"
RD"a[124:127],v226 offset:31744\n"
PK"v2,v4,v5\n"
MF"v[40:43],a[76:79],a[60:63],v[40:43]\n"
WT"lgkmcnt(6)\n"
BL"v225,s[8:11],s44 offen offset:2048 sc1 lds\n"
EX"v7,v7\n"
MF"v[44:47],a[80:83],a[48:51],v[212:215]\n"
WT"vmcnt(19)\n"
"s_barrier\n"
MF"v[44:47],a[84:87],a[52:55],v[44:47]\n"
EX"v12,v12\n"
PK"v3,v6,v7\n"
MF"v[44:47],a[88:91],a[56:59],v[44:47]\n"
EX"v13,v13\n"
MF"v[44:47],a[92:95],a[60:63],v[44:47]\n"
BL"v225,s[8:11],s44 offen offset:3072 sc1 lds\n"
"v_readlane_b32 s43,v228,s42\n"
MF"v[56:59],a[96:99],a[48:51],v[216:219]\n"
"s_mov_b32 m0,s40\n"
RD"a[96:99],v227 offset:8192\n"
PK"v10,v12,v13\n"
EX"v14,v14\n"
MF"v[56:59],a[100:103],a[52:55],v[56:59]\n"
WT"lgkmcnt(1)\n"
RD"a[100:103],v227 offset:9216\n"
MF"v[56:59],a[104:107],a[56:59],v[56:59]\n"
EX"v15,v15\n"
MF"v[56:59],a[108:111],a[60:63],v[56:59]\n"
RD"a[104:107],v227 offset:10240\n"
RD"a[108:111],v227 offset:11264\n"
MF"v[60:63],a[112:115],a[48:51],v[220:223]\n"
EX"v16,v16\n"
PK"v11,v14,v15\n"
MF"v[60:63],a[116:119],a[52:55],v[60:63]\n"
EX"v17,v17\n"
"v_readlane_b32 s44,v229,s42\n"
MF"v[60:63],a[120:123],a[56:59],v[60:63]\n"
SA"s42,s42,1\n"
BL"v225,s[8:11],s43 offen offset:0 sc1 lds\n"
MF"v[60:63],a[124:127],a[60:63],v[60:63]\n"
WT"lgkmcnt(3)\n"
EX"v18,v18\n"
MF"v[112:115],a[96:99],v[68:71],v[112:115]\n"
SA"s41,s41,32768\n"
RD"a[112:115],v227 offset:12288\n"
PK"v16,v16,v17\n"
MF"v[144:147],a[96:99],v[72:75],v[144:147]\n"
"s_cmp_eq_u32 s41,163840\n"
EX"v19,v19\n"
MF"v[176:179],a[96:99],v[76:79],v[176:179]\n"
WT"lgkmcnt(1)\n"
EX"v24,v24\n"
MF"v[116:119],a[100:103],v[68:71],v[116:119]\n"
"s_cselect_b32 s41,0,s41\n"
BL"v225,s[8:11],s43 offen offset:1024 sc1 lds\n"
RD"a[116:119],v227 offset:13312\n"
MF"v[148:151],a[100:103],v[72:75],v[148:151]\n"
EX"v25,v25\n"
"v_add_u32 v226,s41,v224\n"
MF"v[180:183],a[100:103],v[76:79],v[180:183]\n"
RD"a[120:123],v227 offset:14336\n"
PK"v17,v18,v19\n"
MF"v[120:123],a[104:107],v[68:71],v[120:123]\n"
EX"v26,v26\n"
MF"v[152:155],a[104:107],v[72:75],v[152:155]\n"
EX"v27,v27\n"
PK"v24,v24,v25\n"
MF"v[184:187],a[104:107],v[76:79],v[184:187]\n"
BL"v225,s[8:11],s43 offen offset:2048 sc1 lds\n"
MF"v[124:127],a[108:111],v[68:71],v[124:127]\n"
EX"v20,v20\n"
MF"v[156:159],a[108:111],v[72:75],v[156:159]\n"
RD"a[124:127],v227 offset:15360\n"
PK"v25,v26,v27\n"
MF"v[188:191],a[108:111],v[76:79],v[188:191]\n"
WT"lgkmcnt(2)\n"
EX"v21,v21\n"
MF"v[128:131],a[112:115],v[68:71],v[128:131]\n"
BL"v225,s[8:11],s43 offen offset:3072 sc1 lds\n"
MF"v[160:163],a[112:115],v[72:75],v[160:163]\n"
SA"m0,s40,16384\n"
EX"v22,v22\n"
MF"v[192:195],a[112:115],v[76:79],v[192:195]\n"
EX"v23,v23\n"
PK"v18,v20,v21\n"
MF"v[132:135],a[116:119],v[68:71],v[132:135]\n"
RD"a[64:67],v227 offset:16384\n"
PK"v19,v22,v23\n"
MF"v[164:167],a[116:119],v[72:75],v[164:167]\n"
EX"v28,v28\n"
MF"v[196:199],a[116:119],v[76:79],v[196:199]\n"
WT"lgkmcnt(1)\n"
BL"v225,s[8:11],s44 offen offset:0 sc1 lds\n"
MF"v[136:139],a[120:123],v[68:71],v[136:139]\n"
SA"s40,s40,32768\n"
EX"v29,v29\n"
MF"v[168:171],a[120:123],v[72:75],v[168:171]\n"
"s_cmp_eq_u32 s40,s46\n"
RD"a[68:71],v227 offset:24576\n"
RD"a[72:75],v227 offset:17408\n"
MF"v[200:203],a[120:123],v[76:79],v[200:203]\n"
"s_cselect_b32 s40,s47,s40\n"
EX"v30,v30\n"
MF"v[140:143],a[124:127],v[68:71],v[140:143]\n"
RD"a[76:79],v227 offset:25600\n"
RD"a[80:83],v227 offset:18432\n"
PK"v26,v28,v29\n"
MF"v[172:175],a[124:127],v[72:75],v[172:175]\n"
EX"v31,v31\n"
MF"v[204:207],a[124:127],v[76:79],v[204:207]\n"
BL"v225,s[8:11],s44 offen offset:1024 sc1 lds\n"
MF"a[136:139],a[128:131],v[68:71],a[136:139]\n"
EX"v40,v40\n"
PK"v27,v30,v31\n"
MF"a[140:143],a[128:131],v[72:75],a[140:143]\n"
EX"v41,v41\n"
MF"a[144:147],a[128:131],v[76:79],a[144:147]\n"
WT"lgkmcnt(2)\n"
RD"a[84:87],v227 offset:26624\n"
PK"v68,v40,v41\n"
MF"v[144:147],a[64:67],v[0:3],v[144:147]\n"
BL"v225,s[8:11],s44 offen offset:2048 sc1 lds\n"
MF"v[144:147],a[68:71],v[16:19],v[144:147]\n"
EX"v42,v42\n"
MF"v[176:179],a[68:71],v[24:27],v[176:179]\n"
SA"s51,s59,16384\n"
RD"a[88:91],v227 offset:19456\n"
RD"a[92:95],v227 offset:27648\n"
MF"v[176:179],a[64:67],v[8:11],v[176:179]\n"
EX"v43,v43\n"
MF"v[148:151],a[72:75],v[0:3],v[148:151]\n"
WT"lgkmcnt(3)\n"
EX"v44,v44\n"
MF"v[148:151],a[76:79],v[16:19],v[148:151]\n"
RD"a[96:99],v227 offset:20480\n"
"v_accvgpr_read_b32 v252,a136\n"
"v_subrev_u32 v253,0x2b800000,v252\n"
MF"v[180:183],a[76:79],v[24:27],v[180:183]\n"
"v_cmp_le_u32 vcc,0x34000000,v253\n"
"s_cmp_lg_u64 vcc,0\n"
"s_cbranch_scc1 L1035_%=\n"
"L1034_%=:\n"
MF"v[180:183],a[72:75],v[8:11],v[180:183]\n"
BL"v225,s[8:11],s44 offen offset:3072 sc1 lds\n"
EX"v45,v45\n"
MF"v[152:155],a[80:83],v[0:3],v[152:155]\n"
WT"lgkmcnt(3)\n"
"v_rcp_f32 v252,v252\n"
EX"v46,v46\n"
MF"v[152:155],a[84:87],v[16:19],v[152:155]\n"
"s_mov_b32 m0,s40\n"
RD"a[100:103],v227 offset:28672\n"
RD"a[104:107],v227 offset:21504\n"
"v_readlane_b32 s43,v228,s42\n"
MF"v[184:187],a[84:87],v[24:27],v[184:187]\n"
EX"v47,v47\n"
PK"v69,v42,v43\n"
MF"v[184:187],a[80:83],v[8:11],v[184:187]\n"
WT"lgkmcnt(3)\n"
RD"a[108:111],v227 offset:29696\n"
RD"a[112:115],v227 offset:22528\n"
EX"v56,v56\n"
MF"v[156:159],a[88:91],v[0:3],v[156:159]\n"
EX"v57,v57\n"
"v_readlane_b32 s44,v229,s42\n"
MF"v[156:159],a[92:95],v[16:19],v[156:159]\n"
SA"s42,s42,1\n"
EX"v58,v58\n"
PK"v70,v44,v45\n"
MF"v[188:191],a[92:95],v[24:27],v[188:191]\n"
RD"a[116:119],v227 offset:30720\n"
MU"v112,v252,v112\n"
MU"v113,v252,v113\n"
MF"v[188:191],a[88:91],v[8:11],v[188:191]\n"
WT"lgkmcnt(5)\n"
EX"v59,v59\n"
MU"v114,v252,v114\n"
MF"v[160:163],a[96:99],v[0:3],v[160:163]\n"
WT"lgkmcnt(3)\n"
EX"v60,v60\n"
MU"v115,v252,v115\n"
MF"v[160:163],a[100:103],v[16:19],v[160:163]\n"
RD"a[120:123],v227 offset:23552\n"
RD"a[124:127],v227 offset:31744\n"
MU"v116,v252,v116\n"
MF"v[192:195],a[100:103],v[24:27],v[192:195]\n"
EX"v61,v61\n"
MU"v117,v252,v117\n"
MF"v[192:195],a[96:99],v[8:11],v[192:195]\n"
MU"v118,v252,v118\n"
MU"v119,v252,v119\n"
PK"v71,v46,v47\n"
MF"v[164:167],a[104:107],v[0:3],v[164:167]\n"
WT"lgkmcnt(3)\n"
EX"v62,v62\n"
PK"v112,v112,v113\n"
MF"v[164:167],a[108:111],v[16:19],v[164:167]\n"
SA"s41,s41,32768\n"
WT"vmcnt(20)\n"
"s_barrier\n"
MF"v[196:199],a[108:111],v[24:27],v[196:199]\n"
"s_cmp_eq_u32 s41,163840\n"
BL"v225,s[8:11],s43 offen offset:0 sc1 lds\n"
BL"v225,s[8:11],s43 offen offset:1024 sc1 lds\n"
MF"v[196:199],a[104:107],v[8:11],v[196:199]\n"
"s_cselect_b32 s41,0,s41\n"
BL"v225,s[8:11],s43 offen offset:2048 sc1 lds\n"
RD"a[64:67],v226 offset:0\n"
MF"v[168:171],a[112:115],v[0:3],v[168:171]\n"
WT"lgkmcnt(1)\n"
EX"v63,v63\n"
"v_add_u32 v227,s41,v224\n"
MF"v[168:171],a[116:119],v[16:19],v[168:171]\n"
RD"a[68:71],v226 offset:8192\n"
RD"a[72:75],v226 offset:1024\n"
PK"v113,v114,v115\n"
MF"v[200:203],a[116:119],v[24:27],v[200:203]\n"
RD"a[76:79],v226 offset:9216\n"
RD"a[80:83],v226 offset:2048\n"
PK"v114,v116,v117\n"
MF"v[200:203],a[112:115],v[8:11],v[200:203]\n"
BL"v225,s[8:11],s43 offen offset:3072 sc1 lds\n"
PK"v115,v118,v119\n"
MF"v[172:175],a[120:123],v[0:3],v[172:175]\n"
SA"m0,s40,16384\n"
BS"v[112:115],v224,s[8:11],s51 offen offset:0 sc1\n"
RD"a[84:87],v226 offset:10240\n"
MF"v[172:175],a[124:127],v[16:19],v[172:175]\n"
RD"a[88:91],v226 offset:3072\n"
RD"a[92:95],v226 offset:11264\n"
MU"v120,v252,v120\n"
MF"v[204:207],a[124:127],v[24:27],v[204:207]\n"
BL"v225,s[8:11],s44 offen offset:0 sc1 lds\n"
MU"v121,v252,v121\n"
MF"v[204:207],a[120:123],v[8:11],v[204:207]\n"
SA"s40,s40,32768\n"
RD"a[96:99],v226 offset:4096\n"
RD"a[100:103],v226 offset:12288\n"
PK"v76,v56,v57\n"
MF"a[140:143],a[128:131],v[0:3],a[140:143]\n"
"s_cmp_eq_u32 s40,s46\n"
RD"a[104:107],v226 offset:5120\n"
RD"a[108:111],v226 offset:13312\n"
MU"v122,v252,v122\n"
MF"a[140:143],a[128:131],v[16:19],a[140:143]\n"
"s_cselect_b32 s40,s47,s40\n"
RD"a[112:115],v226 offset:6144\n"
MU"v123,v252,v123\n"
MU"v124,v252,v124\n"
MF"a[144:147],a[128:131],v[24:27],a[144:147]\n"
PK"v77,v58,v59\n"
PK"v78,v60,v61\n"
PK"v79,v62,v63\n"
MF"a[144:147],a[128:131],v[8:11],a[144:147]\n"
WT"lgkmcnt(7)\n"
RD"a[116:119],v226 offset:14336\n"
RD"a[120:123],v226 offset:7168\n"
MU"v125,v252,v125\n"
MF"v[176:179],a[64:67],v[68:71],v[176:179]\n"
BL"v225,s[8:11],s44 offen offset:1024 sc1 lds\n"
MU"v126,v252,v126\n"
MF"v[176:179],a[68:71],v[76:79],v[176:179]\n"
RD"a[124:127],v226 offset:15360\n"
MU"v127,v252,v127\n"
PK"v120,v120,v121\n"
MF"v[180:183],a[72:75],v[68:71],v[180:183]\n"
WT"vmcnt(15)\n"
"s_barrier\n"
MF"v[180:183],a[76:79],v[76:79],v[180:183]\n"
RD"a[64:67],v227 offset:0\n"
PK"v121,v122,v123\n"
PK"v122,v124,v125\n"
MF"v[184:187],a[80:83],v[68:71],v[184:187]\n"
BL"v225,s[8:11],s44 offen offset:2048 sc1 lds\n"
PK"v123,v126,v127\n"
MF"v[184:187],a[84:87],v[76:79],v[184:187]\n"
WT"lgkmcnt(4)\n"
BS"v[120:123],v224,s[8:11],s51 offen offset:1024 sc1\n"
MU"v128,v252,v128\n"
MF"v[188:191],a[88:91],v[68:71],v[188:191]\n"
RD"a[68:71],v227 offset:1024\n"
RD"a[72:75],v227 offset:2048\n"
MU"v129,v252,v129\n"
MF"v[188:191],a[92:95],v[76:79],v[188:191]\n"
MU"v130,v252,v130\n"
MU"v131,v252,v131\n"
MU"v132,v252,v132\n"
MF"v[192:195],a[96:99],v[68:71],v[192:195]\n"
MU"v133,v252,v133\n"
MU"v134,v252,v134\n"
MU"v135,v252,v135\n"
MF"v[192:195],a[100:103],v[76:79],v[192:195]\n"
BL"v225,s[8:11],s44 offen offset:3072 sc1 lds\n"
PK"v128,v128,v129\n"
MF"v[196:199],a[104:107],v[68:71],v[196:199]\n"
RD"a[76:79],v227 offset:3072\n"
PK"v129,v130,v131\n"
PK"v130,v132,v133\n"
MF"v[196:199],a[108:111],v[76:79],v[196:199]\n"
PK"v131,v134,v135\n"
MU"v136,v252,v136\n"
MU"v137,v252,v137\n"
MF"v[200:203],a[112:115],v[68:71],v[200:203]\n"
WT"lgkmcnt(3)\n"
BS"v[128:131],v224,s[8:11],s51 offen offset:2048 sc1\n"
MU"v138,v252,v138\n"
MF"v[200:203],a[116:119],v[76:79],v[200:203]\n"
RD"a[80:83],v227 offset:4096\n"
RD"a[84:87],v227 offset:5120\n"
MU"v139,v252,v139\n"
MF"v[204:207],a[120:123],v[68:71],v[204:207]\n"
RD"a[88:91],v227 offset:6144\n"
MU"v140,v252,v140\n"
MU"v141,v252,v141\n"
MF"v[204:207],a[124:127],v[76:79],v[204:207]\n"
PK"v136,v136,v137\n"
MU"v142,v252,v142\n"
MU"v143,v252,v143\n"
MF"a[144:147],a[128:131],v[68:71],a[144:147]\n"
PK"v137,v138,v139\n"
PK"v138,v140,v141\n"
PK"v139,v142,v143\n"
MF"a[144:147],a[128:131],v[76:79],a[144:147]\n"
BS"v[136:139],v224,s[8:11],s51 offen offset:3072 sc1\n"
"s_branch LTL_%=\n"
"LEV_%=:\n"
"s_mov_b32 s14,0\n"
WT"lgkmcnt(6)\n"
MF"v[0:3],a[64:67],a[0:3],0\n"
WT"lgkmcnt(5)\n"
PK"v64,v32,v33\n"
PK"v65,v34,v35\n"
MF"v[0:3],a[68:71],a[4:7],v[0:3]\n"
SA"s95,s40,32768\n"
PK"v66,v36,v37\n"
PK"v67,v38,v39\n"
MF"v[8:11],a[68:71],a[20:23],0\n"
"s_cmp_eq_u32 s95,s46\n"
PK"v68,v40,v41\n"
PK"v69,v42,v43\n"
MF"v[8:11],a[64:67],a[16:19],v[8:11]\n"
"s_cselect_b32 s95,s47,s95\n"
PK"v70,v44,v45\n"
PK"v71,v46,v47\n"
MF"v[16:19],a[64:67],a[32:35],0\n"
SA"m0,s95,0\n"
PK"v72,v48,v49\n"
PK"v73,v50,v51\n"
MF"v[16:19],a[68:71],a[36:39],v[16:19]\n"
RD"a[92:95],v226 offset:7168\n"
"v_mov_b32 v227,v226\n"
MF"v[24:27],a[68:71],a[52:55],0\n"
PK"v74,v52,v53\n"
PK"v75,v54,v55\n"
MF"v[24:27],a[64:67],a[48:51],v[24:27]\n"
WT"lgkmcnt(1)\n"
BL"v235,s[24:27],s52 offen lds\n"
MF"v[0:3],a[72:75],a[8:11],v[0:3]\n"
SA"s52,s52,s62\n"
PK"v76,v56,v57\n"
PK"v77,v58,v59\n"
MF"v[0:3],a[76:79],a[12:15],v[0:3]\n"
PK"v78,v60,v61\n"
PK"v79,v62,v63\n"
MF"v[8:11],a[76:79],a[28:31],v[8:11]\n"
MF"v[8:11],a[72:75],a[24:27],v[8:11]\n"
MF"v[16:19],a[72:75],a[40:43],v[16:19]\n"
SA"m0,s95,1024\n"
MF"v[16:19],a[76:79],a[44:47],v[16:19]\n"
RD"a[96:99],v226 offset:8192\n"
MF"v[24:27],a[76:79],a[60:63],v[24:27]\n"
EX"v0,v0\n"
MF"v[24:27],a[72:75],a[56:59],v[24:27]\n"
BL"v235,s[24:27],s52 offen lds\n"
MF"v[4:7],a[80:83],a[0:3],0\n"
SA"s52,s52,s62\n"
EX"v1,v1\n"
MF"v[4:7],a[84:87],a[4:7],v[4:7]\n"
RD"a[100:103],v226 offset:9216\n"
PK"v0,v0,v1\n"
MF"v[12:15],a[84:87],a[20:23],0\n"
EX"v2,v2\n"
MF"v[12:15],a[80:83],a[16:19],v[12:15]\n"
EX"v3,v3\n"
MF"v[20:23],a[80:83],a[32:35],0\n"
SA"m0,s95,2048\n"
EX"v8,v8\n"
MF"v[20:23],a[84:87],a[36:39],v[20:23]\n"
RD"a[104:107],v226 offset:10240\n"
PK"v1,v2,v3\n"
MF"v[28:31],a[84:87],a[52:55],0\n"
EX"v9,v9\n"
MF"v[28:31],a[80:83],a[48:51],v[28:31]\n"
BL"v235,s[24:27],s52 offen lds\n"
MF"v[4:7],a[88:91],a[8:11],v[4:7]\n"
WT"lgkmcnt(1)\n"
EX"v10,v10\n"
MF"v[4:7],a[92:95],a[12:15],v[4:7]\n"
SA"s52,s52,s62\n"
RD"a[108:111],v226 offset:11264\n"
PK"v8,v8,v9\n"
MF"v[12:15],a[92:95],a[28:31],v[12:15]\n"
EX"v11,v11\n"
MF"v[12:15],a[88:91],a[24:27],v[12:15]\n"
EX"v16,v16\n"
MF"v[20:23],a[88:91],a[40:43],v[20:23]\n"
SA"m0,s95,3072\n"
EX"v17,v17\n"
MF"v[20:23],a[92:95],a[44:47],v[20:23]\n"
RD"a[112:115],v226 offset:12288\n"
PK"v9,v10,v11\n"
MF"v[28:31],a[92:95],a[60:63],v[28:31]\n"
EX"v18,v18\n"
MF"v[28:31],a[88:91],a[56:59],v[28:31]\n"
BL"v235,s[24:27],s52 offen lds\n"
MF"v[32:35],a[96:99],a[0:3],0\n"
SA"s52,s52,s62\n"
EX"v19,v19\n"
MF"v[32:35],a[100:103],a[4:7],v[32:35]\n"
RD"a[116:119],v226 offset:13312\n"
PK"v16,v16,v17\n"
MF"v[40:43],a[100:103],a[20:23],0\n"
EX"v24,v24\n"
MF"v[40:43],a[96:99],a[16:19],v[40:43]\n"
EX"v25,v25\n"
MF"v[48:51],a[96:99],a[32:35],0\n"
SA"m0,s95,16384\n"
EX"v26,v26\n"
MF"v[48:51],a[100:103],a[36:39],v[48:51]\n"
RD"a[120:123],v226 offset:14336\n"
PK"v17,v18,v19\n"
MF"v[56:59],a[100:103],a[52:55],0\n"
EX"v27,v27\n"
MF"v[56:59],a[96:99],a[48:51],v[56:59]\n"
WT"lgkmcnt(2)\n"
BL"v235,s[24:27],s52 offen lds\n"
MF"v[32:35],a[104:107],a[8:11],v[32:35]\n"
SA"s52,s52,s62\n"
EX"v4,v4\n"
MF"v[32:35],a[108:111],a[12:15],v[32:35]\n"
RD"a[124:127],v226 offset:15360\n"
PK"v24,v24,v25\n"
MF"v[40:43],a[108:111],a[28:31],v[40:43]\n"
EX"v5,v5\n"
MF"v[40:43],a[104:107],a[24:27],v[40:43]\n"
EX"v6,v6\n"
MF"v[48:51],a[104:107],a[40:43],v[48:51]\n"
SA"m0,s95,17408\n"
EX"v7,v7\n"
MF"v[48:51],a[108:111],a[44:47],v[48:51]\n"
PK"v25,v26,v27\n"
PK"v2,v4,v5\n"
MF"v[56:59],a[108:111],a[60:63],v[56:59]\n"
EX"v12,v12\n"
MF"v[56:59],a[104:107],a[56:59],v[56:59]\n"
BL"v235,s[24:27],s52 offen lds\n"
MF"v[36:39],a[112:115],a[0:3],0\n"
WT"lgkmcnt(0)\n"
EX"v13,v13\n"
MF"v[36:39],a[116:119],a[4:7],v[36:39]\n"
SA"s52,s52,s62\n"
RD"a[64:67],v227 offset:16384\n"
PK"v3,v6,v7\n"
MF"v[44:47],a[116:119],a[20:23],0\n"
EX"v14,v14\n"
MF"v[44:47],a[112:115],a[16:19],v[44:47]\n"
PK"v10,v12,v13\n"
MF"v[52:55],a[112:115],a[32:35],0\n"
SA"m0,s95,18432\n"
EX"v15,v15\n"
MF"v[52:55],a[116:119],a[36:39],v[52:55]\n"
RD"a[68:71],v227 offset:17408\n"
PK"v11,v14,v15\n"
MF"v[60:63],a[116:119],a[52:55],0\n"
EX"v20,v20\n"
MF"v[60:63],a[112:115],a[48:51],v[60:63]\n"
BL"v235,s[24:27],s52 offen lds\n"
MF"v[36:39],a[120:123],a[8:11],v[36:39]\n"
SA"s52,s52,s62\n"
EX"v21,v21\n"
MF"v[36:39],a[124:127],a[12:15],v[36:39]\n"
RD"a[72:75],v227 offset:18432\n"
"v_readlane_b32 s43,v228,s42\n"
MF"v[44:47],a[124:127],a[28:31],v[44:47]\n"
EX"v22,v22\n"
MF"v[44:47],a[120:123],a[24:27],v[44:47]\n"
"s_mov_b32 m0,s40\n"
PK"v18,v20,v21\n"
MF"v[52:55],a[120:123],a[40:43],v[52:55]\n"
EX"v23,v23\n"
MF"v[52:55],a[124:127],a[44:47],v[52:55]\n"
RD"a[76:79],v227 offset:19456\n"
"v_readlane_b32 s44,v229,s42\n"
MF"v[60:63],a[124:127],a[60:63],v[60:63]\n"
EX"v28,v28\n"
MF"v[60:63],a[120:123],a[56:59],v[60:63]\n"
WT"lgkmcnt(2)\n"
BL"v225,s[8:11],s43 offen offset:0 sc1 lds\n"
MF"v[80:83],a[64:67],v[64:67],v[80:83]\n"
SA"s42,s42,1\n"
EX"v29,v29\n"
MF"v[112:115],a[64:67],v[68:71],v[112:115]\n"
SA"s41,s41,32768\n"
RD"a[80:83],v227 offset:20480\n"
PK"v19,v22,v23\n"
MF"v[144:147],a[64:67],v[72:75],v[144:147]\n"
"s_cmp_eq_u32 s41,163840\n"
EX"v30,v30\n"
MF"v[176:179],a[64:67],v[76:79],v[176:179]\n"
"s_cselect_b32 s41,0,s41\n"
PK"v26,v28,v29\n"
"v_add_u32 v226,s41,v224\n"
MF"v[84:87],a[68:71],v[64:67],v[84:87]\n"
EX"v31,v31\n"
MF"v[116:119],a[68:71],v[68:71],v[116:119]\n"
RD"a[84:87],v227 offset:21504\n"
PK"v27,v30,v31\n"
MF"v[148:151],a[68:71],v[72:75],v[148:151]\n"
EX"v32,v32\n"
MF"v[180:183],a[68:71],v[76:79],v[180:183]\n"
WT"lgkmcnt(2)\n"
BL"v225,s[8:11],s43 offen offset:1024 sc1 lds\n"
MF"v[88:91],a[72:75],v[64:67],v[88:91]\n"
EX"v33,v33\n"
MF"v[120:123],a[72:75],v[68:71],v[120:123]\n"
RD"a[88:91],v227 offset:22528\n"
MF"v[152:155],a[72:75],v[72:75],v[152:155]\n"
EX"v34,v34\n"
MF"v[184:187],a[72:75],v[76:79],v[184:187]\n"
MF"v[92:95],a[76:79],v[64:67],v[92:95]\n"
EX"v35,v35\n"
MF"v[124:127],a[76:79],v[68:71],v[124:127]\n"
RD"a[92:95],v227 offset:23552\n"
MF"v[156:159],a[76:79],v[72:75],v[156:159]\n"
EX"v40,v40\n"
MF"v[188:191],a[76:79],v[76:79],v[188:191]\n"
WT"lgkmcnt(2)\n"
BL"v225,s[8:11],s43 offen offset:2048 sc1 lds\n"
MF"v[96:99],a[80:83],v[64:67],v[96:99]\n"
EX"v41,v41\n"
MF"v[128:131],a[80:83],v[68:71],v[128:131]\n"
RD"a[96:99],v227 offset:24576\n"
MF"v[160:163],a[80:83],v[72:75],v[160:163]\n"
EX"v42,v42\n"
MF"v[192:195],a[80:83],v[76:79],v[192:195]\n"
MF"v[100:103],a[84:87],v[64:67],v[100:103]\n"
EX"v43,v43\n"
MF"v[132:135],a[84:87],v[68:71],v[132:135]\n"
RD"a[100:103],v227 offset:25600\n"
MF"v[164:167],a[84:87],v[72:75],v[164:167]\n"
EX"v48,v48\n"
MF"v[196:199],a[84:87],v[76:79],v[196:199]\n"
WT"lgkmcnt(2)\n"
BL"v225,s[8:11],s43 offen offset:3072 sc1 lds\n"
MF"v[104:107],a[88:91],v[64:67],v[104:107]\n"
EX"v49,v49\n"
MF"v[136:139],a[88:91],v[68:71],v[136:139]\n"
RD"a[104:107],v227 offset:26624\n"
MF"v[168:171],a[88:91],v[72:75],v[168:171]\n"
EX"v50,v50\n"
MF"v[200:203],a[88:91],v[76:79],v[200:203]\n"
SA"m0,s40,16384\n"
MF"v[108:111],a[92:95],v[64:67],v[108:111]\n"
EX"v51,v51\n"
MF"v[140:143],a[92:95],v[68:71],v[140:143]\n"
RD"a[108:111],v227 offset:27648\n"
MF"v[172:175],a[92:95],v[72:75],v[172:175]\n"
EX"v56,v56\n"
MF"v[204:207],a[92:95],v[76:79],v[204:207]\n"
BL"v225,s[8:11],s44 offen offset:0 sc1 lds\n"
MF"a[132:135],a[128:131],v[64:67],a[132:135]\n"
SA"s40,s40,32768\n"
EX"v57,v57\n"
MF"a[136:139],a[128:131],v[68:71],a[136:139]\n"
"s_cmp_eq_u32 s40,s46\n"
RD"a[112:115],v227 offset:28672\n"
"s_cselect_b32 s40,s47,s40\n"
MF"a[140:143],a[128:131],v[72:75],a[140:143]\n"
EX"v58,v58\n"
MF"a[144:147],a[128:131],v[76:79],a[144:147]\n"
WT"lgkmcnt(2)\n"
BL"v[240:243],v238,s[8:11],0 offen sc1\n"
MF"v[80:83],a[96:99],v[0:3],v[80:83]\n"
EX"v59,v59\n"
MF"v[112:115],a[96:99],v[8:11],v[112:115]\n"
RD"a[116:119],v227 offset:29696\n"
MF"v[144:147],a[96:99],v[16:19],v[144:147]\n"
EX"v36,v36\n"
MF"v[176:179],a[96:99],v[24:27],v[176:179]\n"
BL"v225,s[8:11],s44 offen offset:1024 sc1 lds\n"
MF"v[84:87],a[100:103],v[0:3],v[84:87]\n"
EX"v37,v37\n"
MF"v[116:119],a[100:103],v[8:11],v[116:119]\n"
RD"a[120:123],v227 offset:30720\n"
MF"v[148:151],a[100:103],v[16:19],v[148:151]\n"
EX"v38,v38\n"
MF"v[180:183],a[100:103],v[24:27],v[180:183]\n"
MF"v[88:91],a[104:107],v[0:3],v[88:91]\n"
EX"v39,v39\n"
MF"v[120:123],a[104:107],v[8:11],v[120:123]\n"
RD"a[124:127],v227 offset:31744\n"
MF"v[152:155],a[104:107],v[16:19],v[152:155]\n"
EX"v44,v44\n"
MF"v[184:187],a[104:107],v[24:27],v[184:187]\n"
WT"lgkmcnt(2)\n"
BL"v225,s[8:11],s44 offen offset:2048 sc1 lds\n"
MF"v[92:95],a[108:111],v[0:3],v[92:95]\n"
EX"v45,v45\n"
MF"v[124:127],a[108:111],v[8:11],v[124:127]\n"
MF"v[156:159],a[108:111],v[16:19],v[156:159]\n"
EX"v46,v46\n"
MF"v[188:191],a[108:111],v[24:27],v[188:191]\n"
MF"v[96:99],a[112:115],v[0:3],v[96:99]\n"
WT"vmcnt(23) lgkmcnt(0)\n"
"s_barrier\n"
MF"v[128:131],a[112:115],v[8:11],v[128:131]\n"
RD"a[64:67],v226 offset:0\n"
MF"v[160:163],a[112:115],v[16:19],v[160:163]\n"
EX"v47,v47\n"
MF"v[192:195],a[112:115],v[24:27],v[192:195]\n"
BL"v225,s[8:11],s44 offen offset:3072 sc1 lds\n"
MF"v[100:103],a[116:119],v[0:3],v[100:103]\n"
EX"v52,v52\n"
MF"v[132:135],a[116:119],v[8:11],v[132:135]\n"
RD"a[68:71],v226 offset:1024\n"
MF"v[164:167],a[116:119],v[16:19],v[164:167]\n"
EX"v53,v53\n"
MF"v[196:199],a[116:119],v[24:27],v[196:199]\n"
RD"a[72:75],v226 offset:2048\n"
MF"v[104:107],a[120:123],v[0:3],v[104:107]\n"
EX"v54,v54\n"
MF"v[136:139],a[120:123],v[8:11],v[136:139]\n"
RD"a[76:79],v226 offset:3072\n"
MF"v[168:171],a[120:123],v[16:19],v[168:171]\n"
EX"v55,v55\n"
MF"v[200:203],a[120:123],v[24:27],v[200:203]\n"
MF"v[108:111],a[124:127],v[0:3],v[108:111]\n"
EX"v60,v60\n"
MF"v[140:143],a[124:127],v[8:11],v[140:143]\n"
RD"a[80:83],v226 offset:4096\n"
MF"v[172:175],a[124:127],v[16:19],v[172:175]\n"
EX"v61,v61\n"
MF"v[204:207],a[124:127],v[24:27],v[204:207]\n"
RD"a[84:87],v226 offset:5120\n"
MF"a[132:135],a[128:131],v[0:3],a[132:135]\n"
EX"v62,v62\n"
MF"a[136:139],a[128:131],v[8:11],a[136:139]\n"
RD"a[88:91],v226 offset:6144\n"
MF"a[140:143],a[128:131],v[16:19],a[140:143]\n"
EX"v63,v63\n"
MF"a[144:147],a[128:131],v[24:27],a[144:147]\n"
WT"lgkmcnt(6)\n"
MF"v[0:3],a[64:67],a[0:3],0\n"
WT"lgkmcnt(2)\n"
"v_lshrrev_b32 v237,1,v224\n"
PK"v64,v32,v33\n"
MF"v[0:3],a[68:71],a[4:7],v[0:3]\n"
SA"s95,s40,32768\n"
"v_and_b32 v237,0x78,v237\n"
PK"v65,v34,v35\n"
MF"v[8:11],a[68:71],a[20:23],0\n"
"s_cmp_eq_u32 s95,s46\n"
"v_and_b32 v239,0x300,v224\n"
PK"v66,v36,v37\n"
MF"v[8:11],a[64:67],a[16:19],v[8:11]\n"
"s_cselect_b32 s95,s47,s95\n"
"v_add3_u32 v237,v237,v239,s40\n"
PK"v67,v38,v39\n"
MF"v[16:19],a[64:67],a[32:35],0\n"
SA"m0,s95,0\n"
PK"v68,v40,v41\n"
PK"v69,v42,v43\n"
MF"v[16:19],a[68:71],a[36:39],v[16:19]\n"
RD"a[92:95],v226 offset:7168\n"
PK"v70,v44,v45\n"
MF"v[24:27],a[68:71],a[52:55],0\n"
PK"v71,v46,v47\n"
PK"v72,v48,v49\n"
MF"v[24:27],a[64:67],a[48:51],v[24:27]\n"
BL"v235,s[24:27],s52 offen lds\n"
MF"v[0:3],a[72:75],a[8:11],v[0:3]\n"
SA"s52,s52,s62\n"
PK"v73,v50,v51\n"
"v_mov_b32 v227,v226\n"
MF"v[0:3],a[76:79],a[12:15],v[0:3]\n"
PK"v74,v52,v53\n"
PK"v75,v54,v55\n"
MF"v[8:11],a[76:79],a[28:31],v[8:11]\n"
PK"v76,v56,v57\n"
PK"v77,v58,v59\n"
MF"v[8:11],a[72:75],a[24:27],v[8:11]\n"
WT"vmcnt(4)\n"
"v_cmp_eq_u64 vcc,s[28:29],v[240:241]\n"
"v_cmp_eq_u64_e64 s[50:51],s[28:29],v[242:243]\n"
"s_and_b64 vcc,vcc,s[50:51]\n"
"s_cmp_eq_u64 vcc,exec\n"
"s_cbranch_scc0 L1099_%=\n"
"L1100_%=:\n"
MF"v[16:19],a[72:75],a[40:43],v[16:19]\n"
SA"m0,s95,1024\n"
PK"v78,v60,v61\n"
PK"v79,v62,v63\n"
MF"v[16:19],a[76:79],a[44:47],v[16:19]\n"
RD"a[96:99],v226 offset:8192\n"
MF"v[24:27],a[76:79],a[60:63],v[24:27]\n"
EX"v0,v0\n"
MF"v[24:27],a[72:75],a[56:59],v[24:27]\n"
BL"v235,s[24:27],s52 offen lds\n"
MF"v[4:7],a[80:83],a[0:3],0\n"
WT"lgkmcnt(1)\n"
EX"v1,v1\n"
MF"v[4:7],a[84:87],a[4:7],v[4:7]\n"
SA"s52,s52,s62\n"
RD"a[100:103],v226 offset:9216\n"
PK"v0,v0,v1\n"
MF"v[12:15],a[84:87],a[20:23],0\n"
EX"v2,v2\n"
MF"v[12:15],a[80:83],a[16:19],v[12:15]\n"
EX"v3,v3\n"
MF"v[20:23],a[80:83],a[32:35],0\n"
SA"m0,s95,2048\n"
EX"v8,v8\n"
MF"v[20:23],a[84:87],a[36:39],v[20:23]\n"
RD"a[104:107],v226 offset:10240\n"
PK"v1,v2,v3\n"
MF"v[28:31],a[84:87],a[52:55],0\n"
EX"v9,v9\n"
MF"v[28:31],a[80:83],a[48:51],v[28:31]\n"
BL"v235,s[24:27],s52 offen lds\n"
MF"v[4:7],a[88:91],a[8:11],v[4:7]\n"
SA"s52,s52,s62\n"
EX"v10,v10\n"
MF"v[4:7],a[92:95],a[12:15],v[4:7]\n"
RD"a[108:111],v226 offset:11264\n"
PK"v8,v8,v9\n"
MF"v[12:15],a[92:95],a[28:31],v[12:15]\n"
EX"v11,v11\n"
MF"v[12:15],a[88:91],a[24:27],v[12:15]\n"
RT"a[148:149],v237 offset:0\n"
RT"a[150:151],v237 offset:128\n"
MF"v[20:23],a[88:91],a[40:43],v[20:23]\n"
SA"m0,s95,3072\n"
EX"v16,v16\n"
MF"v[20:23],a[92:95],a[44:47],v[20:23]\n"
EX"v17,v17\n"
MF"v[28:31],a[92:95],a[60:63],v[28:31]\n"
RD"a[112:115],v226 offset:12288\n"
PK"v9,v10,v11\n"
MF"v[28:31],a[88:91],a[56:59],v[28:31]\n"
WT"lgkmcnt(4)\n"
EX"v18,v18\n"
MF"v[32:35],a[96:99],a[0:3],0\n"
RT"a[152:153],v237 offset:1024\n"
RT"a[154:155],v237 offset:1152\n"
MF"v[32:35],a[100:103],a[4:7],v[32:35]\n"
BL"v235,s[24:27],s52 offen lds\n"
MF"v[40:43],a[100:103],a[20:23],0\n"
SA"s52,s52,s62\n"
EX"v19,v19\n"
MF"v[40:43],a[96:99],a[16:19],v[40:43]\n"
RD"a[116:119],v226 offset:13312\n"
RT"a[156:157],v237 offset:2048\n"
MF"v[48:51],a[96:99],a[32:35],0\n"
SA"m0,s95,16384\n"
EX"v24,v24\n"
MF"v[48:51],a[100:103],a[36:39],v[48:51]\n"
RT"a[158:159],v237 offset:2176\n"
RD"a[120:123],v226 offset:14336\n"
EX"v25,v25\n"
MF"v[56:59],a[100:103],a[52:55],0\n"
EX"v26,v26\n"
PK"v16,v16,v17\n"
MF"v[56:59],a[96:99],a[48:51],v[56:59]\n"
EX"v27,v27\n"
MF"v[32:35],a[104:107],a[8:11],v[32:35]\n"
WT"lgkmcnt(4)\n"
RT"a[160:161],v237 offset:3072\n"
RT"a[162:163],v237 offset:3200\n"
MF"v[32:35],a[108:111],a[12:15],v[32:35]\n"
BL"v235,s[24:27],s52 offen lds\n"
PK"v17,v18,v19\n"
MF"v[40:43],a[108:111],a[28:31],v[40:43]\n"
SA"s52,s52,s62\n"
EX"v4,v4\n"
MF"v[40:43],a[104:107],a[24:27],v[40:43]\n"
RD"a[124:127],v226 offset:15360\n"
RT"a[164:165],v237 offset:16384\n"
MF"v[48:51],a[104:107],a[40:43],v[48:51]\n"
SA"m0,s95,17408\n"
EX"v5,v5\n"
PK"v24,v24,v25\n"
MF"v[48:51],a[108:111],a[44:47],v[48:51]\n"
RT"a[166:167],v237 offset:16512\n"
PK"v25,v26,v27\n"
EX"v6,v6\n"
MF"v[56:59],a[108:111],a[60:63],v[56:59]\n"
EX"v7,v7\n"
MF"v[56:59],a[104:107],a[56:59],v[56:59]\n"
EX"v12,v12\n"
MF"v[36:39],a[112:115],a[0:3],0\n"
WT"lgkmcnt(3)\n"
RT"a[168:169],v237 offset:17408\n"
RT"a[170:171],v237 offset:17536\n"
MF"v[36:39],a[116:119],a[4:7],v[36:39]\n"
BL"v235,s[24:27],s52 offen lds\n"
MF"v[44:47],a[116:119],a[20:23],0\n"
SA"s52,s52,s62\n"
EX"v13,v13\n"
MF"v[44:47],a[112:115],a[16:19],v[44:47]\n"
RD"a[64:67],v227 offset:16384\n"
RT"a[172:173],v237 offset:18432\n"
PK"v2,v4,v5\n"
MF"v[52:55],a[112:115],a[32:35],0\n"
SA"m0,s95,18432\n"
EX"v14,v14\n"
MF"v[52:55],a[116:119],a[36:39],v[52:55]\n"
RT"a[174:175],v237 offset:18560\n"
RD"a[68:71],v227 offset:17408\n"
PK"v3,v6,v7\n"
MF"v[60:63],a[116:119],a[52:55],0\n"
EX"v15,v15\n"
MF"v[60:63],a[112:115],a[48:51],v[60:63]\n"
EX"v20,v20\n"
MF"v[36:39],a[120:123],a[8:11],v[36:39]\n"
WT"lgkmcnt(4)\n"
BL"v235,s[24:27],s52 offen lds\n"
MF"v[36:39],a[124:127],a[12:15],v[36:39]\n"
SA"s52,s52,s62\n"
EX"v21,v21\n"
PK"v10,v12,v13\n"
MF"v[44:47],a[124:127],a[28:31],v[44:47]\n"
"s_cmp_gt_u32 s92,0\n"
RD"a[72:75],v227 offset:18432\n"
"v_readlane_b32 s43,v228,s42\n"
MF"v[44:47],a[120:123],a[24:27],v[44:47]\n"
"s_cselect_b32 s63,s63,1065353216\n"
EX"v22,v22\n"
MF"v[52:55],a[120:123],a[40:43],v[52:55]\n"
"s_mov_b32 m0,s40\n"
BL"a[204:207],v236,s[8:11],s63 offen offset:0 sc1\n"
MF"v[52:55],a[124:127],a[44:47],v[52:55]\n"
EX"v23,v23\n"
PK"v11,v14,v15\n"
MF"v[60:63],a[124:127],a[60:63],v[60:63]\n"
RD"a[76:79],v227 offset:19456\n"
"v_readlane_b32 s44,v229,s42\n"
MF"v[60:63],a[120:123],a[56:59],v[60:63]\n"
WT"lgkmcnt(2)\n"
EX"v28,v28\n"
MF"v[80:83],a[64:67],v[64:67],v[80:83]\n"
SA"s42,s42,1\n"
BL"v225,s[8:11],s43 offen offset:0 sc1 lds\n"
MF"v[112:115],a[64:67],v[68:71],v[112:115]\n"
SA"s41,s41,32768\n"
EX"v29,v29\n"
PK"v18,v20,v21\n"
MF"v[144:147],a[64:67],v[72:75],v[144:147]\n"
"s_cmp_eq_u32 s41,163840\n"
RD"a[80:83],v227 offset:20480\n"
PK"v19,v22,v23\n"
MF"v[176:179],a[64:67],v[76:79],v[176:179]\n"
"s_cselect_b32 s41,0,s41\n"
EX"v30,v30\n"
MF"v[84:87],a[68:71],v[64:67],v[84:87]\n"
BL"a[208:211],v236,s[8:11],s63 offen offset:1024 sc1\n"
MF"v[116:119],a[68:71],v[68:71],v[116:119]\n"
EX"v31,v31\n"
MF"v[148:151],a[68:71],v[72:75],v[148:151]\n"
RD"a[84:87],v227 offset:21504\n"
"v_add_u32 v226,s41,v224\n"
MF"v[180:183],a[68:71],v[76:79],v[180:183]\n"
WT"lgkmcnt(2)\n"
EX"v32,v32\n"
MF"v[88:91],a[72:75],v[64:67],v[88:91]\n"
BL"v225,s[8:11],s43 offen offset:1024 sc1 lds\n"
MF"v[120:123],a[72:75],v[68:71],v[120:123]\n"
EX"v33,v33\n"
PK"v26,v28,v29\n"
MF"v[152:155],a[72:75],v[72:75],v[152:155]\n"
RD"a[88:91],v227 offset:22528\n"
PK"v27,v30,v31\n"
MF"v[184:187],a[72:75],v[76:79],v[184:187]\n"
EX"v34,v34\n"
MF"v[92:95],a[76:79],v[64:67],v[92:95]\n"
BL"a[212:215],v236,s[8:11],s63 offen offset:2048 sc1\n"
MF"v[124:127],a[76:79],v[68:71],v[124:127]\n"
EX"v35,v35\n"
MF"v[156:159],a[76:79],v[72:75],v[156:159]\n"
RD"a[92:95],v227 offset:23552\n"
MF"v[188:191],a[76:79],v[76:79],v[188:191]\n"
WT"lgkmcnt(2)\n"
EX"v40,v40\n"
MF"v[96:99],a[80:83],v[64:67],v[96:99]\n"
BL"v225,s[8:11],s43 offen offset:2048 sc1 lds\n"
MF"v[128:131],a[80:83],v[68:71],v[128:131]\n"
EX"v41,v41\n"
MF"v[160:163],a[80:83],v[72:75],v[160:163]\n"
RD"a[96:99],v227 offset:24576\n"
MF"v[192:195],a[80:83],v[76:79],v[192:195]\n"
EX"v42,v42\n"
MF"v[100:103],a[84:87],v[64:67],v[100:103]\n"
BL"a[216:219],v236,s[8:11],s63 offen offset:3072 sc1\n"
MF"v[132:135],a[84:87],v[68:71],v[132:135]\n"
EX"v43,v43\n"
MF"v[164:167],a[84:87],v[72:75],v[164:167]\n"
RD"a[100:103],v227 offset:25600\n"
MF"v[196:199],a[84:87],v[76:79],v[196:199]\n"
WT"lgkmcnt(2)\n"
EX"v48,v48\n"
MF"v[104:107],a[88:91],v[64:67],v[104:107]\n"
SA"s63,s63,4096\n"
BL"v225,s[8:11],s43 offen offset:3072 sc1 lds\n"
MF"v[136:139],a[88:91],v[68:71],v[136:139]\n"
EX"v49,v49\n"
MF"v[168:171],a[88:91],v[72:75],v[168:171]\n"
RD"a[104:107],v227 offset:26624\n"
MF"v[200:203],a[88:91],v[76:79],v[200:203]\n"
SA"m0,s40,16384\n"
EX"v50,v50\n"
MF"v[108:111],a[92:95],v[64:67],v[108:111]\n"
BL"a[220:223],v236,s[8:11],s63 offen offset:0 sc1\n"
MF"v[140:143],a[92:95],v[68:71],v[140:143]\n"
EX"v51,v51\n"
MF"v[172:175],a[92:95],v[72:75],v[172:175]\n"
RD"a[108:111],v227 offset:27648\n"
MF"v[204:207],a[92:95],v[76:79],v[204:207]\n"
EX"v56,v56\n"
MF"a[132:135],a[128:131],v[64:67],a[132:135]\n"
SA"s40,s40,32768\n"
BL"v225,s[8:11],s44 offen offset:0 sc1 lds\n"
MF"a[136:139],a[128:131],v[68:71],a[136:139]\n"
"s_cmp_eq_u32 s40,s46\n"
EX"v57,v57\n"
MF"a[140:143],a[128:131],v[72:75],a[140:143]\n"
"s_cselect_b32 s40,s47,s40\n"
RD"a[112:115],v227 offset:28672\n"
MF"a[144:147],a[128:131],v[76:79],a[144:147]\n"
WT"lgkmcnt(2)\n"
EX"v58,v58\n"
MF"v[80:83],a[96:99],v[0:3],v[80:83]\n"
BL"a[224:227],v236,s[8:11],s63 offen offset:1024 sc1\n"
MF"v[112:115],a[96:99],v[8:11],v[112:115]\n"
EX"v59,v59\n"
MF"v[144:147],a[96:99],v[16:19],v[144:147]\n"
RD"a[116:119],v227 offset:29696\n"
MF"v[176:179],a[96:99],v[24:27],v[176:179]\n"
EX"v36,v36\n"
MF"v[84:87],a[100:103],v[0:3],v[84:87]\n"
BL"v225,s[8:11],s44 offen offset:1024 sc1 lds\n"
MF"v[116:119],a[100:103],v[8:11],v[116:119]\n"
EX"v37,v37\n"
MF"v[148:151],a[100:103],v[16:19],v[148:151]\n"
RD"a[120:123],v227 offset:30720\n"
MF"v[180:183],a[100:103],v[24:27],v[180:183]\n"
EX"v38,v38\n"
MF"v[88:91],a[104:107],v[0:3],v[88:91]\n"
BL"a[228:231],v236,s[8:11],s63 offen offset:2048 sc1\n"
MF"v[120:123],a[104:107],v[8:11],v[120:123]\n"
EX"v39,v39\n"
MF"v[152:155],a[104:107],v[16:19],v[152:155]\n"
RD"a[124:127],v227 offset:31744\n"
MF"v[184:187],a[104:107],v[24:27],v[184:187]\n"
WT"lgkmcnt(2)\n"
EX"v44,v44\n"
MF"v[92:95],a[108:111],v[0:3],v[92:95]\n"
BL"v225,s[8:11],s44 offen offset:2048 sc1 lds\n"
MF"v[124:127],a[108:111],v[8:11],v[124:127]\n"
EX"v45,v45\n"
MF"v[156:159],a[108:111],v[16:19],v[156:159]\n"
EX"v46,v46\n"
MF"v[188:191],a[108:111],v[24:27],v[188:191]\n"
BL"a[232:235],v236,s[8:11],s63 offen offset:3072 sc1\n"
MF"v[96:99],a[112:115],v[0:3],v[96:99]\n"
WT"lgkmcnt(0)\n"
"s_barrier\n"
MF"v[128:131],a[112:115],v[8:11],v[128:131]\n"
RD"a[64:67],v226 offset:0\n"
MF"v[160:163],a[112:115],v[16:19],v[160:163]\n"
EX"v47,v47\n"
MF"v[192:195],a[112:115],v[24:27],v[192:195]\n"
SA"s63,s63,4096\n"
BL"v225,s[8:11],s44 offen offset:3072 sc1 lds\n"
MF"v[100:103],a[116:119],v[0:3],v[100:103]\n"
EX"v52,v52\n"
MF"v[132:135],a[116:119],v[8:11],v[132:135]\n"
RD"a[68:71],v226 offset:1024\n"
MF"v[164:167],a[116:119],v[16:19],v[164:167]\n"
EX"v53,v53\n"
MF"v[196:199],a[116:119],v[24:27],v[196:199]\n"
BL"a[236:239],v236,s[8:11],s63 offen offset:0 sc1\n"
MF"v[104:107],a[120:123],v[0:3],v[104:107]\n"
RD"a[72:75],v226 offset:2048\n"
MF"v[136:139],a[120:123],v[8:11],v[136:139]\n"
EX"v54,v54\n"
MF"v[168:171],a[120:123],v[16:19],v[168:171]\n"
RD"a[76:79],v226 offset:3072\n"
MF"v[200:203],a[120:123],v[24:27],v[200:203]\n"
EX"v55,v55\n"
MF"v[108:111],a[124:127],v[0:3],v[108:111]\n"
EX"v60,v60\n"
MF"v[140:143],a[124:127],v[8:11],v[140:143]\n"
RD"a[80:83],v226 offset:4096\n"
MF"v[172:175],a[124:127],v[16:19],v[172:175]\n"
EX"v61,v61\n"
MF"v[204:207],a[124:127],v[24:27],v[204:207]\n"
RD"a[84:87],v226 offset:5120\n"
MF"a[132:135],a[128:131],v[0:3],a[132:135]\n"
EX"v62,v62\n"
MF"a[136:139],a[128:131],v[8:11],a[136:139]\n"
RD"a[88:91],v226 offset:6144\n"
MF"a[140:143],a[128:131],v[16:19],a[140:143]\n"
EX"v63,v63\n"
MF"a[144:147],a[128:131],v[24:27],a[144:147]\n"
WT"lgkmcnt(6)\n"
MF"v[0:3],a[64:67],a[0:3],0\n"
WT"lgkmcnt(2)\n"
"v_lshrrev_b32 v237,1,v224\n"
PK"v64,v32,v33\n"
MF"v[0:3],a[68:71],a[4:7],v[0:3]\n"
SA"s95,s40,32768\n"
"v_and_b32 v237,0x78,v237\n"
PK"v65,v34,v35\n"
MF"v[8:11],a[68:71],a[20:23],0\n"
"s_cmp_eq_u32 s95,s46\n"
"v_and_b32 v239,0x300,v224\n"
PK"v66,v36,v37\n"
MF"v[8:11],a[64:67],a[16:19],v[8:11]\n"
"s_cselect_b32 s95,s47,s95\n"
BL"a[240:243],v236,s[8:11],s63 offen offset:1024 sc1\n"
MF"v[16:19],a[64:67],a[32:35],0\n"
"s_sub_u32 s95,s95,s47\n"
"v_add3_u32 v237,v237,v239,s40\n"
PK"v67,v38,v39\n"
MF"v[16:19],a[68:71],a[36:39],v[16:19]\n"
"s_lshr_b32 s53,s47,2\n"
RD"a[92:95],v226 offset:7168\n"
PK"v68,v40,v41\n"
MF"v[24:27],a[68:71],a[52:55],0\n"
"s_cmp_gt_u32 s92,10\n"
PK"v69,v42,v43\n"
PK"v70,v44,v45\n"
MF"v[24:27],a[64:67],a[48:51],v[24:27]\n"
"s_cselect_b32 s63,s63,1065353216\n"
PK"v71,v46,v47\n"
"v_add_u32 v235,s95,v224\n"
MF"v[0:3],a[72:75],a[8:11],v[0:3]\n"
"s_lshl_b32 s50,s35,6\n"
BL"a[244:247],v236,s[8:11],s63 offen offset:2048 sc1\n"
MF"v[0:3],a[76:79],a[12:15],v[0:3]\n"
SA"s50,s50,4352\n"
PK"v72,v48,v49\n"
PK"v73,v50,v51\n"
MF"v[8:11],a[76:79],a[28:31],v[8:11]\n"
"v_mov_b32 v227,v226\n"
PK"v74,v52,v53\n"
MF"v[8:11],a[72:75],a[24:27],v[8:11]\n"
BL"a[248:251],v236,s[8:11],s63 offen offset:3072 sc1\n"
MF"v[16:19],a[72:75],a[40:43],v[16:19]\n"
SA"s63,s63,4096\n"
PK"v75,v54,v55\n"
PK"v76,v56,v57\n"
MF"v[16:19],a[76:79],a[44:47],v[16:19]\n"
RD"a[96:99],v226 offset:8192\n"
PK"v77,v58,v59\n"
MF"v[24:27],a[76:79],a[60:63],v[24:27]\n"
EX"v0,v0\n"
MF"v[24:27],a[72:75],a[56:59],v[24:27]\n"
BL"a[252:255],v236,s[8:11],s63 offen offset:0 sc1\n"
PK"v78,v60,v61\n"
MF"v[4:7],a[80:83],a[0:3],0\n"
WT"lgkmcnt(1)\n"
EX"v1,v1\n"
PK"v79,v62,v63\n"
MF"v[4:7],a[84:87],a[4:7],v[4:7]\n"
"s_cmp_gt_u32 s92,13\n"
RD"a[100:103],v226 offset:9216\n"
"v_lshrrev_b32 v238,1,v224\n"
"v_and_b32 v238,56,v238\n"
MF"v[12:15],a[84:87],a[20:23],0\n"
"s_cselect_b32 s63,s63,1065353216\n"
EX"v2,v2\n"
MF"v[12:15],a[80:83],a[16:19],v[12:15]\n"
BL"v[244:247],v236,s[8:11],s63 offen offset:1024 sc1\n"
"v_add_u32 v236,s53,v235\n"
MF"v[20:23],a[80:83],a[32:35],0\n"
EX"v3,v3\n"
MF"v[20:23],a[84:87],a[36:39],v[20:23]\n"
EX"v8,v8\n"
MF"v[28:31],a[84:87],a[52:55],0\n"
RD"a[104:107],v226 offset:10240\n"
PK"v0,v0,v1\n"
MF"v[28:31],a[80:83],a[48:51],v[28:31]\n"
EX"v9,v9\n"
MF"v[4:7],a[88:91],a[8:11],v[4:7]\n"
"buffer_load_dwordx2 v[240:241],v238,s[8:11],s50 offen sc1\n"
MF"v[4:7],a[92:95],a[12:15],v[4:7]\n"
EX"v10,v10\n"
MF"v[12:15],a[92:95],a[28:31],v[12:15]\n"
RD"a[108:111],v226 offset:11264\n"
PK"v1,v2,v3\n"
MF"v[12:15],a[88:91],a[24:27],v[12:15]\n"
EX"v11,v11\n"
MF"v[20:23],a[88:91],a[40:43],v[20:23]\n"
WT"vmcnt(23)\n"
RT"a[176:177],v237 offset:0\n"
RT"a[178:179],v237 offset:128\n"
MF"v[20:23],a[92:95],a[44:47],v[20:23]\n"
EX"v16,v16\n"
MF"v[28:31],a[92:95],a[60:63],v[28:31]\n"
EX"v17,v17\n"
MF"v[28:31],a[88:91],a[56:59],v[28:31]\n"
WT"lgkmcnt(3)\n"
RD"a[112:115],v226 offset:12288\n"
RT"a[180:181],v237 offset:1024\n"
PK"v8,v8,v9\n"
MF"v[32:35],a[96:99],a[0:3],0\n"
EX"v18,v18\n"
MF"v[32:35],a[100:103],a[4:7],v[32:35]\n"
RT"a[182:183],v237 offset:1152\n"
RD"a[116:119],v226 offset:13312\n"
MF"v[40:43],a[100:103],a[20:23],0\n"
EX"v19,v19\n"
MF"v[40:43],a[96:99],a[16:19],v[40:43]\n"
EX"v24,v24\n"
PK"v9,v10,v11\n"
MF"v[48:51],a[96:99],a[32:35],0\n"
RT"a[184:185],v237 offset:2048\n"
RT"a[186:187],v237 offset:2176\n"
MF"v[48:51],a[100:103],a[36:39],v[48:51]\n"
EX"v25,v25\n"
PK"v16,v16,v17\n"
MF"v[56:59],a[100:103],a[52:55],0\n"
EX"v26,v26\n"
MF"v[56:59],a[96:99],a[48:51],v[56:59]\n"
RD"a[120:123],v226 offset:14336\n"
RT"a[188:189],v237 offset:3072\n"
MF"v[32:35],a[104:107],a[8:11],v[32:35]\n"
WT"lgkmcnt(4)\n"
EX"v27,v27\n"
MF"v[32:35],a[108:111],a[12:15],v[32:35]\n"
RT"a[190:191],v237 offset:3200\n"
RD"a[124:127],v226 offset:15360\n"
PK"v17,v18,v19\n"
MF"v[40:43],a[108:111],a[28:31],v[40:43]\n"
EX"v4,v4\n"
MF"v[40:43],a[104:107],a[24:27],v[40:43]\n"
EX"v5,v5\n"
MF"v[48:51],a[104:107],a[40:43],v[48:51]\n"
RT"a[192:193],v237 offset:16384\n"
RT"a[194:195],v237 offset:16512\n"
PK"v24,v24,v25\n"
MF"v[48:51],a[108:111],a[44:47],v[48:51]\n"
EX"v6,v6\n"
MF"v[56:59],a[108:111],a[60:63],v[56:59]\n"
EX"v7,v7\n"
MF"v[56:59],a[104:107],a[56:59],v[56:59]\n"
EX"v12,v12\n"
PK"v25,v26,v27\n"
MF"v[36:39],a[112:115],a[0:3],0\n"
RT"a[196:197],v237 offset:17408\n"
RT"a[198:199],v237 offset:17536\n"
MF"v[36:39],a[116:119],a[4:7],v[36:39]\n"
EX"v13,v13\n"
MF"v[44:47],a[116:119],a[20:23],0\n"
RD"a[64:67],v227 offset:16384\n"
PK"v2,v4,v5\n"
MF"v[44:47],a[112:115],a[16:19],v[44:47]\n"
EX"v14,v14\n"
MF"v[52:55],a[112:115],a[32:35],0\n"
RT"a[200:201],v237 offset:18432\n"
RT"a[202:203],v237 offset:18560\n"
MF"v[52:55],a[116:119],a[36:39],v[52:55]\n"
EX"v15,v15\n"
PK"v3,v6,v7\n"
MF"v[60:63],a[116:119],a[52:55],0\n"
RD"a[68:71],v227 offset:17408\n"
PK"v10,v12,v13\n"
MF"v[60:63],a[112:115],a[48:51],v[60:63]\n"
WT"lgkmcnt(4)\n"
EX"v20,v20\n"
MF"v[36:39],a[120:123],a[8:11],v[36:39]\n"
EX"v21,v21\n"
MF"v[36:39],a[124:127],a[12:15],v[36:39]\n"
RD"a[72:75],v227 offset:18432\n"
"v_readlane_b32 s43,v228,s42\n"
MF"v[44:47],a[124:127],a[28:31],v[44:47]\n"
EX"v22,v22\n"
MF"v[44:47],a[120:123],a[24:27],v[44:47]\n"
"s_mov_b32 m0,s40\n"
PK"v11,v14,v15\n"
PK"v18,v20,v21\n"
MF"v[52:55],a[120:123],a[40:43],v[52:55]\n"
EX"v23,v23\n"
MF"v[52:55],a[124:127],a[44:47],v[52:55]\n"
RD"a[76:79],v227 offset:19456\n"
"v_readlane_b32 s44,v229,s42\n"
MF"v[60:63],a[124:127],a[60:63],v[60:63]\n"
EX"v28,v28\n"
MF"v[60:63],a[120:123],a[56:59],v[60:63]\n"
WT"lgkmcnt(2)\n"
BL"v225,s[8:11],s43 offen offset:0 sc1 lds\n"
MF"v[80:83],a[64:67],v[64:67],v[80:83]\n"
SA"s42,s42,1\n"
EX"v29,v29\n"
MF"v[112:115],a[64:67],v[68:71],v[112:115]\n"
SA"s41,s41,32768\n"
RD"a[80:83],v227 offset:20480\n"
PK"v19,v22,v23\n"
MF"v[144:147],a[64:67],v[72:75],v[144:147]\n"
"s_cmp_eq_u32 s41,163840\n"
EX"v30,v30\n"
MF"v[176:179],a[64:67],v[76:79],v[176:179]\n"
"s_cselect_b32 s41,0,s41\n"
PK"v26,v28,v29\n"
"v_add_u32 v226,s41,v224\n"
MF"v[84:87],a[68:71],v[64:67],v[84:87]\n"
EX"v31,v31\n"
MF"v[116:119],a[68:71],v[68:71],v[116:119]\n"
RD"a[84:87],v227 offset:21504\n"
PK"v27,v30,v31\n"
MF"v[148:151],a[68:71],v[72:75],v[148:151]\n"
WT"vmcnt(1)\n"
"v_cmp_eq_u64 vcc,s[28:29],v[240:241]\n"
"s_cmp_eq_u64 vcc,exec\n"
"s_cbranch_scc0 L1102_%=\n"
"L1103_%=:\n"
MF"v[180:183],a[68:71],v[76:79],v[180:183]\n"
WT"lgkmcnt(2)\n"
EX"v32,v32\n"
MF"v[88:91],a[72:75],v[64:67],v[88:91]\n"
BL"v225,s[8:11],s43 offen offset:1024 sc1 lds\n"
MF"v[120:123],a[72:75],v[68:71],v[120:123]\n"
EX"v33,v33\n"
MF"v[152:155],a[72:75],v[72:75],v[152:155]\n"
RD"a[88:91],v227 offset:22528\n"
MF"v[184:187],a[72:75],v[76:79],v[184:187]\n"
EX"v34,v34\n"
MF"v[92:95],a[76:79],v[64:67],v[92:95]\n"
EX"v35,v35\n"
MF"v[124:127],a[76:79],v[68:71],v[124:127]\n"
RD"a[92:95],v227 offset:23552\n"
MF"v[156:159],a[76:79],v[72:75],v[156:159]\n"
EX"v40,v40\n"
MF"v[188:191],a[76:79],v[76:79],v[188:191]\n"
WT"lgkmcnt(2)\n"
BL"v225,s[8:11],s43 offen offset:2048 sc1 lds\n"
MF"v[96:99],a[80:83],v[64:67],v[96:99]\n"
EX"v41,v41\n"
MF"v[128:131],a[80:83],v[68:71],v[128:131]\n"
RD"a[96:99],v227 offset:24576\n"
MF"v[160:163],a[80:83],v[72:75],v[160:163]\n"
EX"v42,v42\n"
MF"v[192:195],a[80:83],v[76:79],v[192:195]\n"
MF"v[100:103],a[84:87],v[64:67],v[100:103]\n"
EX"v43,v43\n"
MF"v[132:135],a[84:87],v[68:71],v[132:135]\n"
RD"a[100:103],v227 offset:25600\n"
MF"v[164:167],a[84:87],v[72:75],v[164:167]\n"
EX"v48,v48\n"
MF"v[196:199],a[84:87],v[76:79],v[196:199]\n"
WT"lgkmcnt(2)\n"
BL"v225,s[8:11],s43 offen offset:3072 sc1 lds\n"
MF"v[104:107],a[88:91],v[64:67],v[104:107]\n"
EX"v49,v49\n"
MF"v[136:139],a[88:91],v[68:71],v[136:139]\n"
RD"a[104:107],v227 offset:26624\n"
MF"v[168:171],a[88:91],v[72:75],v[168:171]\n"
EX"v50,v50\n"
MF"v[200:203],a[88:91],v[76:79],v[200:203]\n"
SA"m0,s40,16384\n"
MF"v[108:111],a[92:95],v[64:67],v[108:111]\n"
EX"v51,v51\n"
MF"v[140:143],a[92:95],v[68:71],v[140:143]\n"
RD"a[108:111],v227 offset:27648\n"
MF"v[172:175],a[92:95],v[72:75],v[172:175]\n"
EX"v56,v56\n"
MF"v[204:207],a[92:95],v[76:79],v[204:207]\n"
BL"v225,s[8:11],s44 offen offset:0 sc1 lds\n"
MF"a[132:135],a[128:131],v[64:67],a[132:135]\n"
SA"s40,s40,32768\n"
EX"v57,v57\n"
MF"a[136:139],a[128:131],v[68:71],a[136:139]\n"
"s_cmp_eq_u32 s40,s46\n"
RD"a[112:115],v227 offset:28672\n"
"s_cselect_b32 s40,s47,s40\n"
MF"a[140:143],a[128:131],v[72:75],a[140:143]\n"
EX"v58,v58\n"
MF"a[144:147],a[128:131],v[76:79],a[144:147]\n"
MF"v[240:243],a[148:151],a[204:207],0\n"
EX"v59,v59\n"
MF"v[240:243],a[152:155],a[208:211],v[240:243]\n"
RD"a[116:119],v227 offset:29696\n"
MF"v[240:243],a[156:159],a[212:215],v[240:243]\n"
EX"v36,v36\n"
MF"v[240:243],a[160:163],a[216:219],v[240:243]\n"
BL"v225,s[8:11],s44 offen offset:1024 sc1 lds\n"
MF"v[240:243],a[164:167],a[220:223],v[240:243]\n"
EX"v37,v37\n"
MF"v[240:243],a[168:171],a[224:227],v[240:243]\n"
RD"a[120:123],v227 offset:30720\n"
MF"v[240:243],a[172:175],a[228:231],v[240:243]\n"
EX"v38,v38\n"
MF"v[240:243],a[176:179],a[232:235],v[240:243]\n"
MF"v[240:243],a[180:183],a[236:239],v[240:243]\n"
EX"v39,v39\n"
MF"v[240:243],a[184:187],a[240:243],v[240:243]\n"
RD"a[124:127],v227 offset:31744\n"
MF"v[240:243],a[188:191],a[244:247],v[240:243]\n"
EX"v44,v44\n"
MF"v[240:243],a[192:195],a[248:251],v[240:243]\n"
BL"v225,s[8:11],s44 offen offset:2048 sc1 lds\n"
MF"v[240:243],a[196:199],a[252:255],v[240:243]\n"
EX"v45,v45\n"
MF"v[240:243],a[200:203],v[244:247],v[240:243]\n"
WT"lgkmcnt(1)\n"
MF"v[80:83],a[96:99],v[0:3],v[80:83]\n"
EX"v46,v46\n"
MF"v[112:115],a[96:99],v[8:11],v[112:115]\n"
MF"v[144:147],a[96:99],v[16:19],v[144:147]\n"
MF"v[176:179],a[96:99],v[24:27],v[176:179]\n"
MF"v[84:87],a[100:103],v[0:3],v[84:87]\n"
EX"v47,v47\n"
MF"v[116:119],a[100:103],v[8:11],v[116:119]\n"
"ds_write_b128 v236,v[240:243]\n"
MF"v[148:151],a[100:103],v[16:19],v[148:151]\n"
BL"v225,s[8:11],s44 offen offset:3072 sc1 lds\n"
MF"v[180:183],a[100:103],v[24:27],v[180:183]\n"
EX"v52,v52\n"
MF"v[88:91],a[104:107],v[0:3],v[88:91]\n"
EX"v53,v53\n"
MF"v[120:123],a[104:107],v[8:11],v[120:123]\n"
MF"v[152:155],a[104:107],v[16:19],v[152:155]\n"
EX"v54,v54\n"
MF"v[184:187],a[104:107],v[24:27],v[184:187]\n"
MF"v[92:95],a[108:111],v[0:3],v[92:95]\n"
EX"v55,v55\n"
MF"v[124:127],a[108:111],v[8:11],v[124:127]\n"
MF"v[156:159],a[108:111],v[16:19],v[156:159]\n"
EX"v60,v60\n"
MF"v[188:191],a[108:111],v[24:27],v[188:191]\n"
MF"v[96:99],a[112:115],v[0:3],v[96:99]\n"
EX"v61,v61\n"
MF"v[128:131],a[112:115],v[8:11],v[128:131]\n"
WT"lgkmcnt(0)\n"
"s_barrier\n"
MF"v[160:163],a[112:115],v[16:19],v[160:163]\n"
RD"a[64:67],v226 offset:0\n"
RD"v[240:243],v235 offset:0\n"
MF"v[192:195],a[112:115],v[24:27],v[192:195]\n"
EX"v62,v62\n"
MF"v[100:103],a[116:119],v[0:3],v[100:103]\n"
EX"v63,v63\n"
MF"v[132:135],a[116:119],v[8:11],v[132:135]\n"
RD"v[244:247],v235 offset:1024\n"
RD"a[68:71],v226 offset:1024\n"
MF"v[164:167],a[116:119],v[16:19],v[164:167]\n"
RD"v[248:251],v235 offset:2048\n"
MF"v[196:199],a[116:119],v[24:27],v[196:199]\n"
RD"a[72:75],v226 offset:2048\n"
MF"v[104:107],a[120:123],v[0:3],v[104:107]\n"
RD"v[252:255],v235 offset:3072\n"
MF"v[136:139],a[120:123],v[8:11],v[136:139]\n"
RD"a[76:79],v226 offset:3072\n"
MF"v[168:171],a[120:123],v[16:19],v[168:171]\n"
MF"v[200:203],a[120:123],v[24:27],v[200:203]\n"
MF"v[108:111],a[124:127],v[0:3],v[108:111]\n"
MF"v[140:143],a[124:127],v[8:11],v[140:143]\n"
RD"a[80:83],v226 offset:4096\n"
MF"v[172:175],a[124:127],v[16:19],v[172:175]\n"
MF"v[204:207],a[124:127],v[24:27],v[204:207]\n"
RD"a[84:87],v226 offset:5120\n"
MF"a[132:135],a[128:131],v[0:3],a[132:135]\n"
MF"a[136:139],a[128:131],v[8:11],a[136:139]\n"
RD"a[88:91],v226 offset:6144\n"
MF"a[140:143],a[128:131],v[16:19],a[140:143]\n"
MF"a[144:147],a[128:131],v[24:27],a[144:147]\n"
"s_cmp_eq_u32 s57,2\n"
"s_cbranch_scc1 LMD_%=\n"
"s_cmp_eq_u32 s34,2\n"
"s_cbranch_scc1 LMA_%=\n"
WT"lgkmcnt(10)\n"
MF"v[0:3],a[64:67],a[0:3],0\n"
WT"lgkmcnt(2)\n"
"v_add_f32 v240,v240,v244\n"
PK"v64,v32,v33\n"
MF"v[0:3],a[68:71],a[4:7],v[0:3]\n"
PK"v65,v34,v35\n"
"v_add_f32 v248,v248,v252\n"
MF"v[8:11],a[68:71],a[20:23],0\n"
PK"v66,v36,v37\n"
PK"v67,v38,v39\n"
MF"v[8:11],a[64:67],a[16:19],v[8:11]\n"
"v_add_f32 v241,v241,v245\n"
PK"v68,v40,v41\n"
MF"v[16:19],a[64:67],a[32:35],0\n"
PK"v69,v42,v43\n"
"v_add_f32 v249,v249,v253\n"
MF"v[16:19],a[68:71],a[36:39],v[16:19]\n"
RD"a[92:95],v226 offset:7168\n"
PK"v70,v44,v45\n"
MF"v[24:27],a[68:71],a[52:55],0\n"
PK"v71,v46,v47\n"
"v_add_f32 v242,v242,v246\n"
MF"v[24:27],a[64:67],a[48:51],v[24:27]\n"
PK"v72,v48,v49\n"
PK"v73,v50,v51\n"
MF"v[0:3],a[72:75],a[8:11],v[0:3]\n"
"v_mov_b32 v227,v226\n"
"v_add_f32 v250,v250,v254\n"
MF"v[0:3],a[76:79],a[12:15],v[0:3]\n"
PK"v74,v52,v53\n"
PK"v75,v54,v55\n"
MF"v[8:11],a[76:79],a[28:31],v[8:11]\n"
"v_add_f32 v243,v243,v247\n"
PK"v76,v56,v57\n"
MF"v[8:11],a[72:75],a[24:27],v[8:11]\n"
PK"v77,v58,v59\n"
"v_add_f32 v251,v251,v255\n"
MF"v[16:19],a[72:75],a[40:43],v[16:19]\n"
PK"v78,v60,v61\n"
PK"v79,v62,v63\n"
MF"v[16:19],a[76:79],a[44:47],v[16:19]\n"
RD"a[96:99],v226 offset:8192\n"
PK"v240,v240,v240\n"
MF"v[24:27],a[76:79],a[60:63],v[24:27]\n"
EX"v0,v0\n"
MF"v[24:27],a[72:75],a[56:59],v[24:27]\n"
"v_lshlrev_b32 v240,16,v240\n"
PK"v241,v241,v241\n"
MF"v[4:7],a[80:83],a[0:3],0\n"
WT"lgkmcnt(1)\n"
EX"v1,v1\n"
MF"v[4:7],a[84:87],a[4:7],v[4:7]\n"
RD"a[100:103],v226 offset:9216\n"
"v_lshlrev_b32 v241,16,v241\n"
MF"v[12:15],a[84:87],a[20:23],0\n"
EX"v2,v2\n"
MF"v[12:15],a[80:83],a[16:19],v[12:15]\n"
EX"v3,v3\n"
MF"v[20:23],a[80:83],a[32:35],0\n"
EX"v8,v8\n"
PK"v242,v242,v242\n"
MF"v[20:23],a[84:87],a[36:39],v[20:23]\n"
RD"a[104:107],v226 offset:10240\n"
"v_lshlrev_b32 v242,16,v242\n"
MF"v[28:31],a[84:87],a[52:55],0\n"
EX"v9,v9\n"
PK"v243,v243,v243\n"
MF"v[28:31],a[80:83],a[48:51],v[28:31]\n"
"v_lshlrev_b32 v243,16,v243\n"
MU"v244,0xbfb8aa3b,v240\n"
EX"v244,v244\n"
MF"v[4:7],a[88:91],a[8:11],v[4:7]\n"
EX"v10,v10\n"
MF"v[4:7],a[92:95],a[12:15],v[4:7]\n"
RD"a[108:111],v226 offset:11264\n"
MU"v245,0xbfb8aa3b,v241\n"
EX"v245,v245\n"
MF"v[12:15],a[92:95],a[28:31],v[12:15]\n"
EX"v11,v11\n"
PK"v0,v0,v1\n"
MF"v[12:15],a[88:91],a[24:27],v[12:15]\n"
EX"v16,v16\n"
MU"v246,0xbfb8aa3b,v242\n"
MF"v[20:23],a[88:91],a[40:43],v[20:23]\n"
EX"v246,v246\n"
MU"v247,0xbfb8aa3b,v243\n"
MF"v[20:23],a[92:95],a[44:47],v[20:23]\n"
EX"v17,v17\n"
PK"v1,v2,v3\n"
MF"v[28:31],a[92:95],a[60:63],v[28:31]\n"
EX"v247,v247\n"
PK"v248,v248,v248\n"
MF"v[28:31],a[88:91],a[56:59],v[28:31]\n"
WT"lgkmcnt(1)\n"
RD"a[112:115],v226 offset:12288\n"
"v_lshlrev_b32 v248,16,v248\n"
PK"v8,v8,v9\n"
MF"v[32:35],a[96:99],a[0:3],0\n"
EX"v18,v18\n"
PK"v249,v249,v249\n"
MF"v[32:35],a[100:103],a[4:7],v[32:35]\n"
EX"v19,v19\n"
"v_lshlrev_b32 v249,16,v249\n"
MF"v[40:43],a[100:103],a[20:23],0\n"
RD"a[116:119],v226 offset:13312\n"
PK"v250,v250,v250\n"
MF"v[40:43],a[96:99],a[16:19],v[40:43]\n"
EX"v24,v24\n"
PK"v9,v10,v11\n"
MF"v[48:51],a[96:99],a[32:35],0\n"
EX"v25,v25\n"
"v_lshlrev_b32 v250,16,v250\n"
MF"v[48:51],a[100:103],a[36:39],v[48:51]\n"
EX"v26,v26\n"
PK"v16,v16,v17\n"
MF"v[56:59],a[100:103],a[52:55],0\n"
RD"a[120:123],v226 offset:14336\n"
PK"v251,v251,v251\n"
MF"v[56:59],a[96:99],a[48:51],v[56:59]\n"
EX"v27,v27\n"
"v_lshlrev_b32 v251,16,v251\n"
MF"v[32:35],a[104:107],a[8:11],v[32:35]\n"
WT"lgkmcnt(2)\n"
EX"v4,v4\n"
"v_add_f32 v244,1.0,v244\n"
MF"v[32:35],a[108:111],a[12:15],v[32:35]\n"
"v_rcp_f32 v244,v244\n"
"v_add_f32 v245,1.0,v245\n"
MF"v[40:43],a[108:111],a[28:31],v[40:43]\n"
"v_rcp_f32 v245,v245\n"
PK"v17,v18,v19\n"
MF"v[40:43],a[104:107],a[24:27],v[40:43]\n"
RD"a[124:127],v226 offset:15360\n"
"v_add_f32 v246,1.0,v246\n"
"v_rcp_f32 v246,v246\n"
MF"v[48:51],a[104:107],a[40:43],v[48:51]\n"
EX"v5,v5\n"
"v_add_f32 v247,1.0,v247\n"
MF"v[48:51],a[108:111],a[44:47],v[48:51]\n"
"v_rcp_f32 v247,v247\n"
EX"v6,v6\n"
MF"v[56:59],a[108:111],a[60:63],v[56:59]\n"
EX"v7,v7\n"
PK"v24,v24,v25\n"
MF"v[56:59],a[104:107],a[56:59],v[56:59]\n"
EX"v12,v12\n"
PK"v25,v26,v27\n"
MF"v[36:39],a[112:115],a[0:3],0\n"
WT"lgkmcnt(1)\n"
EX"v13,v13\n"
MU"v240,v240,v244\n"
MF"v[36:39],a[116:119],a[4:7],v[36:39]\n"
RD"a[64:67],v227 offset:16384\n"
MU"v241,v241,v245\n"
MF"v[44:47],a[116:119],a[20:23],0\n"
EX"v14,v14\n"
MU"v242,v242,v246\n"
MF"v[44:47],a[112:115],a[16:19],v[44:47]\n"
MU"v243,v243,v247\n"
PK"v2,v4,v5\n"
MF"v[52:55],a[112:115],a[32:35],0\n"
EX"v15,v15\n"
PK"v240,v240,v240\n"
MF"v[52:55],a[116:119],a[36:39],v[52:55]\n"
RD"a[68:71],v227 offset:17408\n"
"v_lshlrev_b32 v240,16,v240\n"
PK"v3,v6,v7\n"
MF"v[60:63],a[116:119],a[52:55],0\n"
EX"v20,v20\n"
PK"v241,v241,v241\n"
MF"v[60:63],a[112:115],a[48:51],v[60:63]\n"
"v_readlane_b32 s43,v228,s42\n"
"v_lshlrev_b32 v241,16,v241\n"
MF"v[36:39],a[120:123],a[8:11],v[36:39]\n"
WT"lgkmcnt(1)\n"
EX"v21,v21\n"
PK"v242,v242,v242\n"
MF"v[36:39],a[124:127],a[12:15],v[36:39]\n"
RD"a[72:75],v227 offset:18432\n"
"v_lshlrev_b32 v242,16,v242\n"
PK"v10,v12,v13\n"
MF"v[44:47],a[124:127],a[28:31],v[44:47]\n"
EX"v22,v22\n"
PK"v243,v243,v243\n"
MF"v[44:47],a[120:123],a[24:27],v[44:47]\n"
"s_mov_b32 m0,s40\n"
"v_lshlrev_b32 v243,16,v243\n"
MU"v240,v240,v248\n"
MF"v[52:55],a[120:123],a[40:43],v[52:55]\n"
EX"v23,v23\n"
MF"v[52:55],a[124:127],a[44:47],v[52:55]\n"
RD"a[76:79],v227 offset:19456\n"
"v_readlane_b32 s44,v229,s42\n"
MU"v241,v241,v249\n"
MF"v[60:63],a[124:127],a[60:63],v[60:63]\n"
EX"v28,v28\n"
PK"v11,v14,v15\n"
MF"v[60:63],a[120:123],a[56:59],v[60:63]\n"
SA"s42,s42,1\n"
BL"v225,s[8:11],s43 offen offset:0 sc1 lds\n"
MU"v242,v242,v250\n"
MF"v[80:83],a[64:67],v[64:67],v[80:83]\n"
SA"s41,s41,32768\n"
EX"v29,v29\n"
MU"v243,v243,v251\n"
MF"v[112:115],a[64:67],v[68:71],v[112:115]\n"
"s_cmp_eq_u32 s41,163840\n"
RD"a[80:83],v227 offset:20480\n"
PK"v240,v240,v241\n"
PK"v241,v242,v243\n"
MF"v[144:147],a[64:67],v[72:75],v[144:147]\n"
"s_cselect_b32 s41,0,s41\n"
EX"v30,v30\n"
PK"v18,v20,v21\n"
MF"v[176:179],a[64:67],v[76:79],v[176:179]\n"
WT"lgkmcnt(2)\n"
"v_add_u32 v226,s41,v224\n"
"v_lshrrev_b32 v239,9,v224\n"
"v_lshlrev_b32 v239,8,v239\n"
MF"v[84:87],a[68:71],v[64:67],v[84:87]\n"
EX"v31,v31\n"
"v_bfe_u32 v237,v224,8,1\n"
MF"v[116:119],a[68:71],v[68:71],v[116:119]\n"
RD"a[84:87],v227 offset:21504\n"
"v_lshl_add_u32 v239,v237,3,v239\n"
PK"v19,v22,v23\n"
MF"v[148:151],a[68:71],v[72:75],v[148:151]\n"
EX"v32,v32\n"
"v_and_b32 v237,0x80,v224\n"
MF"v[180:183],a[68:71],v[76:79],v[180:183]\n"
BL"v225,s[8:11],s43 offen offset:1024 sc1 lds\n"
"v_lshl_add_u32 v239,v237,23,v239\n"
MF"v[88:91],a[72:75],v[64:67],v[88:91]\n"
EX"v33,v33\n"
"v_and_b32 v237,0x70,v224\n"
MF"v[120:123],a[72:75],v[68:71],v[120:123]\n"
RD"a[88:91],v227 offset:22528\n"
"v_add_u32 v239,v239,v237\n"
PK"v26,v28,v29\n"
MF"v[152:155],a[72:75],v[72:75],v[152:155]\n"
"buffer_store_dwordx2 v[240:241],v239,s[8:11],s93 offen sc1\n"
MF"v[184:187],a[72:75],v[76:79],v[184:187]\n"
WT"lgkmcnt(2)\n"
EX"v34,v34\n"
MF"v[92:95],a[76:79],v[64:67],v[92:95]\n"
EX"v35,v35\n"
MF"v[124:127],a[76:79],v[68:71],v[124:127]\n"
RD"a[92:95],v227 offset:23552\n"
PK"v27,v30,v31\n"
MF"v[156:159],a[76:79],v[72:75],v[156:159]\n"
EX"v40,v40\n"
MF"v[188:191],a[76:79],v[76:79],v[188:191]\n"
BL"v225,s[8:11],s43 offen offset:2048 sc1 lds\n"
MF"v[96:99],a[80:83],v[64:67],v[96:99]\n"
EX"v41,v41\n"
MF"v[128:131],a[80:83],v[68:71],v[128:131]\n"
RD"a[96:99],v227 offset:24576\n"
MF"v[160:163],a[80:83],v[72:75],v[160:163]\n"
EX"v42,v42\n"
MF"v[192:195],a[80:83],v[76:79],v[192:195]\n"
WT"lgkmcnt(2)\n"
MF"v[100:103],a[84:87],v[64:67],v[100:103]\n"
EX"v43,v43\n"
MF"v[132:135],a[84:87],v[68:71],v[132:135]\n"
RD"a[100:103],v227 offset:25600\n"
MF"v[164:167],a[84:87],v[72:75],v[164:167]\n"
EX"v48,v48\n"
MF"v[196:199],a[84:87],v[76:79],v[196:199]\n"
BL"v225,s[8:11],s43 offen offset:3072 sc1 lds\n"
MF"v[104:107],a[88:91],v[64:67],v[104:107]\n"
EX"v49,v49\n"
MF"v[136:139],a[88:91],v[68:71],v[136:139]\n"
RD"a[104:107],v227 offset:26624\n"
MF"v[168:171],a[88:91],v[72:75],v[168:171]\n"
EX"v50,v50\n"
MF"v[200:203],a[88:91],v[76:79],v[200:203]\n"
WT"lgkmcnt(2)\n"
SA"m0,s40,16384\n"
MF"v[108:111],a[92:95],v[64:67],v[108:111]\n"
EX"v51,v51\n"
MF"v[140:143],a[92:95],v[68:71],v[140:143]\n"
RD"a[108:111],v227 offset:27648\n"
MF"v[172:175],a[92:95],v[72:75],v[172:175]\n"
EX"v56,v56\n"
MF"v[204:207],a[92:95],v[76:79],v[204:207]\n"
BL"v225,s[8:11],s44 offen offset:0 sc1 lds\n"
MF"a[132:135],a[128:131],v[64:67],a[132:135]\n"
SA"s40,s40,32768\n"
EX"v57,v57\n"
MF"a[136:139],a[128:131],v[68:71],a[136:139]\n"
"s_cmp_eq_u32 s40,s46\n"
RD"a[112:115],v227 offset:28672\n"
"s_cselect_b32 s40,s47,s40\n"
MF"a[140:143],a[128:131],v[72:75],a[140:143]\n"
EX"v58,v58\n"
MF"a[144:147],a[128:131],v[76:79],a[144:147]\n"
MF"v[80:83],a[96:99],v[0:3],v[80:83]\n"
EX"v59,v59\n"
MF"v[112:115],a[96:99],v[8:11],v[112:115]\n"
RD"a[116:119],v227 offset:29696\n"
MF"v[144:147],a[96:99],v[16:19],v[144:147]\n"
EX"v36,v36\n"
MF"v[176:179],a[96:99],v[24:27],v[176:179]\n"
WT"lgkmcnt(2)\n"
BL"v225,s[8:11],s44 offen offset:1024 sc1 lds\n"
MF"v[84:87],a[100:103],v[0:3],v[84:87]\n"
EX"v37,v37\n"
MF"v[116:119],a[100:103],v[8:11],v[116:119]\n"
RD"a[120:123],v227 offset:30720\n"
MF"v[148:151],a[100:103],v[16:19],v[148:151]\n"
EX"v38,v38\n"
MF"v[180:183],a[100:103],v[24:27],v[180:183]\n"
MF"v[88:91],a[104:107],v[0:3],v[88:91]\n"
EX"v39,v39\n"
MF"v[120:123],a[104:107],v[8:11],v[120:123]\n"
RD"a[124:127],v227 offset:31744\n"
MF"v[152:155],a[104:107],v[16:19],v[152:155]\n"
EX"v44,v44\n"
MF"v[184:187],a[104:107],v[24:27],v[184:187]\n"
BL"v225,s[8:11],s44 offen offset:2048 sc1 lds\n"
MF"v[92:95],a[108:111],v[0:3],v[92:95]\n"
EX"v45,v45\n"
MF"v[124:127],a[108:111],v[8:11],v[124:127]\n"
MF"v[156:159],a[108:111],v[16:19],v[156:159]\n"
EX"v46,v46\n"
MF"v[188:191],a[108:111],v[24:27],v[188:191]\n"
WT"lgkmcnt(1)\n"
MF"v[96:99],a[112:115],v[0:3],v[96:99]\n"
WT"lgkmcnt(0)\n"
"s_barrier\n"
MF"v[128:131],a[112:115],v[8:11],v[128:131]\n"
RD"a[64:67],v226 offset:0\n"
MF"v[160:163],a[112:115],v[16:19],v[160:163]\n"
EX"v47,v47\n"
MF"v[192:195],a[112:115],v[24:27],v[192:195]\n"
BL"v225,s[8:11],s44 offen offset:3072 sc1 lds\n"
MF"v[100:103],a[116:119],v[0:3],v[100:103]\n"
EX"v52,v52\n"
MF"v[132:135],a[116:119],v[8:11],v[132:135]\n"
RD"a[68:71],v226 offset:1024\n"
MF"v[164:167],a[116:119],v[16:19],v[164:167]\n"
EX"v53,v53\n"
MF"v[196:199],a[116:119],v[24:27],v[196:199]\n"
RD"a[72:75],v226 offset:2048\n"
MF"v[104:107],a[120:123],v[0:3],v[104:107]\n"
EX"v54,v54\n"
MF"v[136:139],a[120:123],v[8:11],v[136:139]\n"
RD"a[76:79],v226 offset:3072\n"
MF"v[168:171],a[120:123],v[16:19],v[168:171]\n"
EX"v55,v55\n"
MF"v[200:203],a[120:123],v[24:27],v[200:203]\n"
MF"v[108:111],a[124:127],v[0:3],v[108:111]\n"
WT"vmcnt(6)\n"
"buffer_store_dwordx2 v[232:233],v230,s[8:11],s94 offen sc1\n"
MF"v[140:143],a[124:127],v[8:11],v[140:143]\n"
EX"v60,v60\n"
MF"v[172:175],a[124:127],v[16:19],v[172:175]\n"
RD"a[80:83],v226 offset:4096\n"
MF"v[204:207],a[124:127],v[24:27],v[204:207]\n"
EX"v61,v61\n"
MF"a[132:135],a[128:131],v[0:3],a[132:135]\n"
RD"a[84:87],v226 offset:5120\n"
MF"a[136:139],a[128:131],v[8:11],a[136:139]\n"
EX"v62,v62\n"
MF"a[140:143],a[128:131],v[16:19],a[140:143]\n"
RD"a[88:91],v226 offset:6144\n"
MF"a[144:147],a[128:131],v[24:27],a[144:147]\n"
EX"v63,v63\n"
"s_mov_b32 s57,0\n"
"s_lshl_b32 s56,s34,1\n"
"s_sub_u32 s56,s56,6\n"
"s_branch LP1_%=\n"
"LMA_%=:\n"
WT"lgkmcnt(10)\n"
MF"v[0:3],a[64:67],a[0:3],v[208:211]\n"
WT"lgkmcnt(2)\n"
"v_add_f32 v240,v240,v244\n"
PK"v64,v32,v33\n"
MF"v[0:3],a[68:71],a[4:7],v[0:3]\n"
PK"v65,v34,v35\n"
"v_add_f32 v248,v248,v252\n"
MF"v[8:11],a[68:71],a[20:23],0\n"
PK"v66,v36,v37\n"
PK"v67,v38,v39\n"
MF"v[8:11],a[64:67],a[16:19],v[8:11]\n"
"v_add_f32 v241,v241,v245\n"
PK"v68,v40,v41\n"
MF"v[16:19],a[64:67],a[32:35],0\n"
PK"v69,v42,v43\n"
"v_add_f32 v249,v249,v253\n"
MF"v[16:19],a[68:71],a[36:39],v[16:19]\n"
RD"a[92:95],v226 offset:7168\n"
PK"v70,v44,v45\n"
MF"v[24:27],a[68:71],a[52:55],0\n"
PK"v71,v46,v47\n"
"v_add_f32 v242,v242,v246\n"
MF"v[24:27],a[64:67],a[48:51],v[24:27]\n"
PK"v72,v48,v49\n"
PK"v73,v50,v51\n"
MF"v[0:3],a[72:75],a[8:11],v[0:3]\n"
"v_mov_b32 v227,v226\n"
"v_add_f32 v250,v250,v254\n"
MF"v[0:3],a[76:79],a[12:15],v[0:3]\n"
PK"v74,v52,v53\n"
PK"v75,v54,v55\n"
MF"v[8:11],a[76:79],a[28:31],v[8:11]\n"
"v_add_f32 v243,v243,v247\n"
PK"v76,v56,v57\n"
MF"v[8:11],a[72:75],a[24:27],v[8:11]\n"
PK"v77,v58,v59\n"
"v_add_f32 v251,v251,v255\n"
MF"v[16:19],a[72:75],a[40:43],v[16:19]\n"
PK"v78,v60,v61\n"
PK"v79,v62,v63\n"
MF"v[16:19],a[76:79],a[44:47],v[16:19]\n"
RD"a[96:99],v226 offset:8192\n"
PK"v240,v240,v240\n"
MF"v[24:27],a[76:79],a[60:63],v[24:27]\n"
EX"v0,v0\n"
MF"v[24:27],a[72:75],a[56:59],v[24:27]\n"
"v_lshlrev_b32 v240,16,v240\n"
PK"v241,v241,v241\n"
MF"v[4:7],a[80:83],a[0:3],v[212:215]\n"
WT"lgkmcnt(1)\n"
EX"v1,v1\n"
MF"v[4:7],a[84:87],a[4:7],v[4:7]\n"
RD"a[100:103],v226 offset:9216\n"
"v_lshlrev_b32 v241,16,v241\n"
MF"v[12:15],a[84:87],a[20:23],0\n"
EX"v2,v2\n"
MF"v[12:15],a[80:83],a[16:19],v[12:15]\n"
EX"v3,v3\n"
MF"v[20:23],a[80:83],a[32:35],0\n"
EX"v8,v8\n"
PK"v242,v242,v242\n"
MF"v[20:23],a[84:87],a[36:39],v[20:23]\n"
RD"a[104:107],v226 offset:10240\n"
"v_lshlrev_b32 v242,16,v242\n"
MF"v[28:31],a[84:87],a[52:55],0\n"
EX"v9,v9\n"
PK"v243,v243,v243\n"
MF"v[28:31],a[80:83],a[48:51],v[28:31]\n"
"v_lshlrev_b32 v243,16,v243\n"
MU"v244,0xbfb8aa3b,v240\n"
EX"v244,v244\n"
MF"v[4:7],a[88:91],a[8:11],v[4:7]\n"
EX"v10,v10\n"
MF"v[4:7],a[92:95],a[12:15],v[4:7]\n"
RD"a[108:111],v226 offset:11264\n"
MU"v245,0xbfb8aa3b,v241\n"
EX"v245,v245\n"
MF"v[12:15],a[92:95],a[28:31],v[12:15]\n"
EX"v11,v11\n"
PK"v0,v0,v1\n"
MF"v[12:15],a[88:91],a[24:27],v[12:15]\n"
EX"v16,v16\n"
MU"v246,0xbfb8aa3b,v242\n"
MF"v[20:23],a[88:91],a[40:43],v[20:23]\n"
EX"v246,v246\n"
MU"v247,0xbfb8aa3b,v243\n"
MF"v[20:23],a[92:95],a[44:47],v[20:23]\n"
EX"v17,v17\n"
PK"v1,v2,v3\n"
MF"v[28:31],a[92:95],a[60:63],v[28:31]\n"
EX"v247,v247\n"
PK"v248,v248,v248\n"
MF"v[28:31],a[88:91],a[56:59],v[28:31]\n"
WT"lgkmcnt(1)\n"
RD"a[112:115],v226 offset:12288\n"
"v_lshlrev_b32 v248,16,v248\n"
PK"v8,v8,v9\n"
MF"v[32:35],a[96:99],a[0:3],v[216:219]\n"
EX"v18,v18\n"
PK"v249,v249,v249\n"
MF"v[32:35],a[100:103],a[4:7],v[32:35]\n"
EX"v19,v19\n"
"v_lshlrev_b32 v249,16,v249\n"
MF"v[40:43],a[100:103],a[20:23],0\n"
RD"a[116:119],v226 offset:13312\n"
PK"v250,v250,v250\n"
MF"v[40:43],a[96:99],a[16:19],v[40:43]\n"
EX"v24,v24\n"
PK"v9,v10,v11\n"
MF"v[48:51],a[96:99],a[32:35],0\n"
EX"v25,v25\n"
"v_lshlrev_b32 v250,16,v250\n"
MF"v[48:51],a[100:103],a[36:39],v[48:51]\n"
EX"v26,v26\n"
PK"v16,v16,v17\n"
MF"v[56:59],a[100:103],a[52:55],0\n"
RD"a[120:123],v226 offset:14336\n"
PK"v251,v251,v251\n"
MF"v[56:59],a[96:99],a[48:51],v[56:59]\n"
EX"v27,v27\n"
"v_lshlrev_b32 v251,16,v251\n"
MF"v[32:35],a[104:107],a[8:11],v[32:35]\n"
WT"lgkmcnt(2)\n"
EX"v4,v4\n"
"v_add_f32 v244,1.0,v244\n"
MF"v[32:35],a[108:111],a[12:15],v[32:35]\n"
"v_rcp_f32 v244,v244\n"
"v_add_f32 v245,1.0,v245\n"
MF"v[40:43],a[108:111],a[28:31],v[40:43]\n"
"v_rcp_f32 v245,v245\n"
PK"v17,v18,v19\n"
MF"v[40:43],a[104:107],a[24:27],v[40:43]\n"
RD"a[124:127],v226 offset:15360\n"
"v_add_f32 v246,1.0,v246\n"
"v_rcp_f32 v246,v246\n"
MF"v[48:51],a[104:107],a[40:43],v[48:51]\n"
EX"v5,v5\n"
"v_add_f32 v247,1.0,v247\n"
MF"v[48:51],a[108:111],a[44:47],v[48:51]\n"
"v_rcp_f32 v247,v247\n"
EX"v6,v6\n"
MF"v[56:59],a[108:111],a[60:63],v[56:59]\n"
EX"v7,v7\n"
PK"v24,v24,v25\n"
MF"v[56:59],a[104:107],a[56:59],v[56:59]\n"
EX"v12,v12\n"
PK"v25,v26,v27\n"
MF"v[36:39],a[112:115],a[0:3],v[220:223]\n"
WT"lgkmcnt(1)\n"
EX"v13,v13\n"
MU"v240,v240,v244\n"
MF"v[36:39],a[116:119],a[4:7],v[36:39]\n"
RD"a[64:67],v227 offset:16384\n"
MU"v241,v241,v245\n"
MF"v[44:47],a[116:119],a[20:23],0\n"
EX"v14,v14\n"
MU"v242,v242,v246\n"
MF"v[44:47],a[112:115],a[16:19],v[44:47]\n"
MU"v243,v243,v247\n"
PK"v2,v4,v5\n"
MF"v[52:55],a[112:115],a[32:35],0\n"
EX"v15,v15\n"
PK"v240,v240,v240\n"
MF"v[52:55],a[116:119],a[36:39],v[52:55]\n"
RD"a[68:71],v227 offset:17408\n"
"v_lshlrev_b32 v240,16,v240\n"
PK"v3,v6,v7\n"
MF"v[60:63],a[116:119],a[52:55],0\n"
EX"v20,v20\n"
PK"v241,v241,v241\n"
MF"v[60:63],a[112:115],a[48:51],v[60:63]\n"
"v_readlane_b32 s43,v228,s42\n"
"v_lshlrev_b32 v241,16,v241\n"
MF"v[36:39],a[120:123],a[8:11],v[36:39]\n"
WT"lgkmcnt(1)\n"
EX"v21,v21\n"
PK"v242,v242,v242\n"
MF"v[36:39],a[124:127],a[12:15],v[36:39]\n"
RD"a[72:75],v227 offset:18432\n"
"v_lshlrev_b32 v242,16,v242\n"
PK"v10,v12,v13\n"
MF"v[44:47],a[124:127],a[28:31],v[44:47]\n"
EX"v22,v22\n"
PK"v243,v243,v243\n"
MF"v[44:47],a[120:123],a[24:27],v[44:47]\n"
"s_mov_b32 m0,s40\n"
"v_lshlrev_b32 v243,16,v243\n"
MU"v240,v240,v248\n"
MF"v[52:55],a[120:123],a[40:43],v[52:55]\n"
EX"v23,v23\n"
MF"v[52:55],a[124:127],a[44:47],v[52:55]\n"
RD"a[76:79],v227 offset:19456\n"
"v_readlane_b32 s44,v229,s42\n"
MU"v241,v241,v249\n"
MF"v[60:63],a[124:127],a[60:63],v[60:63]\n"
EX"v28,v28\n"
PK"v11,v14,v15\n"
MF"v[60:63],a[120:123],a[56:59],v[60:63]\n"
SA"s42,s42,1\n"
BL"v225,s[8:11],s43 offen offset:0 sc1 lds\n"
MU"v242,v242,v250\n"
MF"v[80:83],a[64:67],v[64:67],v[80:83]\n"
SA"s41,s41,32768\n"
EX"v29,v29\n"
MU"v243,v243,v251\n"
MF"v[112:115],a[64:67],v[68:71],v[112:115]\n"
"s_cmp_eq_u32 s41,163840\n"
RD"a[80:83],v227 offset:20480\n"
PK"v240,v240,v241\n"
PK"v241,v242,v243\n"
MF"v[144:147],a[64:67],v[72:75],v[144:147]\n"
"s_cselect_b32 s41,0,s41\n"
EX"v30,v30\n"
PK"v18,v20,v21\n"
MF"v[176:179],a[64:67],v[76:79],v[176:179]\n"
WT"lgkmcnt(2)\n"
"v_add_u32 v226,s41,v224\n"
"v_lshrrev_b32 v239,9,v224\n"
"v_lshlrev_b32 v239,8,v239\n"
MF"v[84:87],a[68:71],v[64:67],v[84:87]\n"
EX"v31,v31\n"
"v_bfe_u32 v237,v224,8,1\n"
MF"v[116:119],a[68:71],v[68:71],v[116:119]\n"
RD"a[84:87],v227 offset:21504\n"
"v_lshl_add_u32 v239,v237,3,v239\n"
PK"v19,v22,v23\n"
MF"v[148:151],a[68:71],v[72:75],v[148:151]\n"
EX"v32,v32\n"
"v_and_b32 v237,0x80,v224\n"
MF"v[180:183],a[68:71],v[76:79],v[180:183]\n"
BL"v225,s[8:11],s43 offen offset:1024 sc1 lds\n"
"v_lshl_add_u32 v239,v237,23,v239\n"
MF"v[88:91],a[72:75],v[64:67],v[88:91]\n"
EX"v33,v33\n"
"v_and_b32 v237,0x70,v224\n"
MF"v[120:123],a[72:75],v[68:71],v[120:123]\n"
RD"a[88:91],v227 offset:22528\n"
"v_add_u32 v239,v239,v237\n"
PK"v26,v28,v29\n"
MF"v[152:155],a[72:75],v[72:75],v[152:155]\n"
"buffer_store_dwordx2 v[240:241],v239,s[8:11],s93 offen sc1\n"
MF"v[184:187],a[72:75],v[76:79],v[184:187]\n"
WT"lgkmcnt(2)\n"
EX"v34,v34\n"
MF"v[92:95],a[76:79],v[64:67],v[92:95]\n"
EX"v35,v35\n"
MF"v[124:127],a[76:79],v[68:71],v[124:127]\n"
RD"a[92:95],v227 offset:23552\n"
PK"v27,v30,v31\n"
MF"v[156:159],a[76:79],v[72:75],v[156:159]\n"
EX"v40,v40\n"
MF"v[188:191],a[76:79],v[76:79],v[188:191]\n"
BL"v225,s[8:11],s43 offen offset:2048 sc1 lds\n"
MF"v[96:99],a[80:83],v[64:67],v[96:99]\n"
EX"v41,v41\n"
MF"v[128:131],a[80:83],v[68:71],v[128:131]\n"
RD"a[96:99],v227 offset:24576\n"
MF"v[160:163],a[80:83],v[72:75],v[160:163]\n"
EX"v42,v42\n"
MF"v[192:195],a[80:83],v[76:79],v[192:195]\n"
WT"lgkmcnt(2)\n"
MF"v[100:103],a[84:87],v[64:67],v[100:103]\n"
EX"v43,v43\n"
MF"v[132:135],a[84:87],v[68:71],v[132:135]\n"
RD"a[100:103],v227 offset:25600\n"
MF"v[164:167],a[84:87],v[72:75],v[164:167]\n"
EX"v48,v48\n"
MF"v[196:199],a[84:87],v[76:79],v[196:199]\n"
BL"v225,s[8:11],s43 offen offset:3072 sc1 lds\n"
MF"v[104:107],a[88:91],v[64:67],v[104:107]\n"
EX"v49,v49\n"
MF"v[136:139],a[88:91],v[68:71],v[136:139]\n"
RD"a[104:107],v227 offset:26624\n"
MF"v[168:171],a[88:91],v[72:75],v[168:171]\n"
EX"v50,v50\n"
MF"v[200:203],a[88:91],v[76:79],v[200:203]\n"
WT"lgkmcnt(2)\n"
SA"m0,s40,16384\n"
MF"v[108:111],a[92:95],v[64:67],v[108:111]\n"
EX"v51,v51\n"
MF"v[140:143],a[92:95],v[68:71],v[140:143]\n"
RD"a[108:111],v227 offset:27648\n"
MF"v[172:175],a[92:95],v[72:75],v[172:175]\n"
EX"v56,v56\n"
MF"v[204:207],a[92:95],v[76:79],v[204:207]\n"
BL"v225,s[8:11],s44 offen offset:0 sc1 lds\n"
MF"a[132:135],a[128:131],v[64:67],a[132:135]\n"
SA"s40,s40,32768\n"
EX"v57,v57\n"
MF"a[136:139],a[128:131],v[68:71],a[136:139]\n"
"s_cmp_eq_u32 s40,s46\n"
RD"a[112:115],v227 offset:28672\n"
"s_cselect_b32 s40,s47,s40\n"
MF"a[140:143],a[128:131],v[72:75],a[140:143]\n"
EX"v58,v58\n"
MF"a[144:147],a[128:131],v[76:79],a[144:147]\n"
MF"v[80:83],a[96:99],v[0:3],v[80:83]\n"
EX"v59,v59\n"
MF"v[112:115],a[96:99],v[8:11],v[112:115]\n"
RD"a[116:119],v227 offset:29696\n"
MF"v[144:147],a[96:99],v[16:19],v[144:147]\n"
EX"v36,v36\n"
MF"v[176:179],a[96:99],v[24:27],v[176:179]\n"
WT"lgkmcnt(2)\n"
BL"v225,s[8:11],s44 offen offset:1024 sc1 lds\n"
MF"v[84:87],a[100:103],v[0:3],v[84:87]\n"
EX"v37,v37\n"
MF"v[116:119],a[100:103],v[8:11],v[116:119]\n"
RD"a[120:123],v227 offset:30720\n"
MF"v[148:151],a[100:103],v[16:19],v[148:151]\n"
EX"v38,v38\n"
MF"v[180:183],a[100:103],v[24:27],v[180:183]\n"
MF"v[88:91],a[104:107],v[0:3],v[88:91]\n"
EX"v39,v39\n"
MF"v[120:123],a[104:107],v[8:11],v[120:123]\n"
RD"a[124:127],v227 offset:31744\n"
MF"v[152:155],a[104:107],v[16:19],v[152:155]\n"
EX"v44,v44\n"
MF"v[184:187],a[104:107],v[24:27],v[184:187]\n"
BL"v225,s[8:11],s44 offen offset:2048 sc1 lds\n"
MF"v[92:95],a[108:111],v[0:3],v[92:95]\n"
EX"v45,v45\n"
MF"v[124:127],a[108:111],v[8:11],v[124:127]\n"
MF"v[156:159],a[108:111],v[16:19],v[156:159]\n"
EX"v46,v46\n"
MF"v[188:191],a[108:111],v[24:27],v[188:191]\n"
WT"lgkmcnt(1)\n"
MF"v[96:99],a[112:115],v[0:3],v[96:99]\n"
WT"lgkmcnt(0)\n"
"s_barrier\n"
MF"v[128:131],a[112:115],v[8:11],v[128:131]\n"
RD"a[64:67],v226 offset:0\n"
MF"v[160:163],a[112:115],v[16:19],v[160:163]\n"
EX"v47,v47\n"
MF"v[192:195],a[112:115],v[24:27],v[192:195]\n"
BL"v225,s[8:11],s44 offen offset:3072 sc1 lds\n"
MF"v[100:103],a[116:119],v[0:3],v[100:103]\n"
EX"v52,v52\n"
MF"v[132:135],a[116:119],v[8:11],v[132:135]\n"
RD"a[68:71],v226 offset:1024\n"
MF"v[164:167],a[116:119],v[16:19],v[164:167]\n"
EX"v53,v53\n"
MF"v[196:199],a[116:119],v[24:27],v[196:199]\n"
RD"a[72:75],v226 offset:2048\n"
MF"v[104:107],a[120:123],v[0:3],v[104:107]\n"
EX"v54,v54\n"
MF"v[136:139],a[120:123],v[8:11],v[136:139]\n"
RD"a[76:79],v226 offset:3072\n"
MF"v[168:171],a[120:123],v[16:19],v[168:171]\n"
EX"v55,v55\n"
MF"v[200:203],a[120:123],v[24:27],v[200:203]\n"
MF"v[108:111],a[124:127],v[0:3],v[108:111]\n"
WT"vmcnt(6)\n"
"buffer_store_dwordx2 v[232:233],v230,s[8:11],s94 offen sc1\n"
MF"v[140:143],a[124:127],v[8:11],v[140:143]\n"
EX"v60,v60\n"
MF"v[172:175],a[124:127],v[16:19],v[172:175]\n"
RD"a[80:83],v226 offset:4096\n"
MF"v[204:207],a[124:127],v[24:27],v[204:207]\n"
EX"v61,v61\n"
MF"a[132:135],a[128:131],v[0:3],a[132:135]\n"
RD"a[84:87],v226 offset:5120\n"
MF"a[136:139],a[128:131],v[8:11],a[136:139]\n"
EX"v62,v62\n"
MF"a[140:143],a[128:131],v[16:19],a[140:143]\n"
RD"a[88:91],v226 offset:6144\n"
MF"a[144:147],a[128:131],v[24:27],a[144:147]\n"
EX"v63,v63\n"
"s_branch LDB_%=\n"
"LMD_%=:\n"
"s_cmp_eq_u32 s34,6\n"
"s_cbranch_scc1 LME_%=\n"
WT"lgkmcnt(10)\n"
MF"v[0:3],a[64:67],a[0:3],v[208:211]\n"
WT"lgkmcnt(2)\n"
"v_lshrrev_b32 v239,9,v224\n"
"v_lshlrev_b32 v239,8,v239\n"
MF"v[0:3],a[68:71],a[4:7],v[0:3]\n"
PK"v64,v32,v33\n"
PK"v65,v34,v35\n"
MF"v[8:11],a[68:71],a[20:23],0\n"
"v_bfe_u32 v237,v224,8,1\n"
PK"v66,v36,v37\n"
MF"v[8:11],a[64:67],a[16:19],v[8:11]\n"
PK"v67,v38,v39\n"
"v_lshl_add_u32 v239,v237,3,v239\n"
MF"v[16:19],a[64:67],a[32:35],0\n"
PK"v68,v40,v41\n"
PK"v69,v42,v43\n"
MF"v[16:19],a[68:71],a[36:39],v[16:19]\n"
RD"a[92:95],v226 offset:7168\n"
"v_and_b32 v237,0x80,v224\n"
MF"v[24:27],a[68:71],a[52:55],0\n"
PK"v70,v44,v45\n"
PK"v71,v46,v47\n"
MF"v[24:27],a[64:67],a[48:51],v[24:27]\n"
"v_lshl_add_u32 v239,v237,23,v239\n"
"v_not_b32 v236,v234\n"
MF"v[0:3],a[72:75],a[8:11],v[0:3]\n"
PK"v72,v48,v49\n"
PK"v73,v50,v51\n"
MF"v[0:3],a[76:79],a[12:15],v[0:3]\n"
SA"s53,s93,4292282368\n"
"v_mov_b32 v227,v226\n"
"v_and_b32 v237,15,v236\n"
MF"v[8:11],a[76:79],a[28:31],v[8:11]\n"
PK"v74,v52,v53\n"
PK"v75,v54,v55\n"
MF"v[8:11],a[72:75],a[24:27],v[8:11]\n"
"v_bfe_u32 v236,v236,4,7\n"
PK"v76,v56,v57\n"
MF"v[16:19],a[72:75],a[40:43],v[16:19]\n"
PK"v77,v58,v59\n"
"v_mul_u32_u24 v236,20480,v236\n"
MF"v[16:19],a[76:79],a[44:47],v[16:19]\n"
RD"a[96:99],v226 offset:8192\n"
PK"v78,v60,v61\n"
MF"v[24:27],a[76:79],a[60:63],v[24:27]\n"
EX"v0,v0\n"
MF"v[24:27],a[72:75],a[56:59],v[24:27]\n"
PK"v79,v62,v63\n"
"v_lshl_add_u32 v236,v237,4,v236\n"
MF"v[4:7],a[80:83],a[0:3],v[212:215]\n"
WT"lgkmcnt(1)\n"
EX"v1,v1\n"
"v_add_u32 v236,v236,v239\n"
MF"v[4:7],a[84:87],a[4:7],v[4:7]\n"
RD"a[100:103],v226 offset:9216\n"
"v_and_b32 v237,0x70,v224\n"
MF"v[12:15],a[84:87],a[20:23],0\n"
EX"v2,v2\n"
MF"v[12:15],a[80:83],a[16:19],v[12:15]\n"
EX"v3,v3\n"
"v_add_u32 v239,v239,v237\n"
MF"v[20:23],a[80:83],a[32:35],0\n"
EX"v8,v8\n"
"v_add_f32 v240,v240,v244\n"
MF"v[20:23],a[84:87],a[36:39],v[20:23]\n"
RD"a[104:107],v226 offset:10240\n"
"v_add_f32 v248,v248,v252\n"
"v_add_f32 v241,v241,v245\n"
MF"v[28:31],a[84:87],a[52:55],0\n"
EX"v9,v9\n"
"v_add_f32 v249,v249,v253\n"
MF"v[28:31],a[80:83],a[48:51],v[28:31]\n"
"v_add_f32 v242,v242,v246\n"
"v_add_f32 v250,v250,v254\n"
MF"v[4:7],a[88:91],a[8:11],v[4:7]\n"
EX"v10,v10\n"
"v_add_f32 v243,v243,v247\n"
MF"v[4:7],a[92:95],a[12:15],v[4:7]\n"
RD"a[108:111],v226 offset:11264\n"
"v_add_f32 v251,v251,v255\n"
PK"v0,v0,v1\n"
MF"v[12:15],a[92:95],a[28:31],v[12:15]\n"
"buffer_load_dwordx2 v[254:255],v236,s[8:11],s53 offen sc1\n"
"v_ashrrev_i32 v235,31,v234\n"
MF"v[12:15],a[88:91],a[24:27],v[12:15]\n"
EX"v11,v11\n"
"v_not_b32 v235,v235\n"
MF"v[20:23],a[88:91],a[40:43],v[20:23]\n"
EX"v16,v16\n"
"v_or_b32 v235,0x80000000,v235\n"
MF"v[20:23],a[92:95],a[44:47],v[20:23]\n"
EX"v17,v17\n"
PK"v1,v2,v3\n"
MF"v[28:31],a[92:95],a[60:63],v[28:31]\n"
RD"a[112:115],v226 offset:12288\n"
"v_xor_b32 v235,v235,v234\n"
"v_and_b32 v235,0xffff0000,v235\n"
MF"v[28:31],a[88:91],a[56:59],v[28:31]\n"
WT"lgkmcnt(2)\n"
EX"v18,v18\n"
MU"v235,0xbfb8aa3b,v235\n"
MF"v[32:35],a[96:99],a[0:3],v[216:219]\n"
EX"v235,v235\n"
PK"v8,v8,v9\n"
MF"v[32:35],a[100:103],a[4:7],v[32:35]\n"
EX"v19,v19\n"
"v_add_f32 v235,1.0,v235\n"
MF"v[40:43],a[100:103],a[20:23],0\n"
"v_rcp_f32 v235,v235\n"
MF"v[40:43],a[96:99],a[16:19],v[40:43]\n"
RD"a[116:119],v226 offset:13312\n"
PK"v235,v235,v235\n"
PK"v9,v10,v11\n"
MF"v[48:51],a[96:99],a[32:35],0\n"
EX"v24,v24\n"
"v_lshlrev_b32 v235,16,v235\n"
MF"v[48:51],a[100:103],a[36:39],v[48:51]\n"
EX"v25,v25\n"
"v_add_f32 v240,v240,v248\n"
MF"v[56:59],a[100:103],a[52:55],0\n"
EX"v26,v26\n"
PK"v16,v16,v17\n"
MF"v[56:59],a[96:99],a[48:51],v[56:59]\n"
RD"a[120:123],v226 offset:14336\n"
"v_add_f32 v241,v241,v249\n"
"v_add_f32 v242,v242,v250\n"
MF"v[32:35],a[104:107],a[8:11],v[32:35]\n"
WT"lgkmcnt(2)\n"
EX"v27,v27\n"
"v_add_f32 v243,v243,v251\n"
MF"v[32:35],a[108:111],a[12:15],v[32:35]\n"
EX"v4,v4\n"
PK"v240,v240,v240\n"
MF"v[40:43],a[108:111],a[28:31],v[40:43]\n"
RD"a[124:127],v226 offset:15360\n"
PK"v17,v18,v19\n"
"v_lshlrev_b32 v240,16,v240\n"
MF"v[40:43],a[104:107],a[24:27],v[40:43]\n"
EX"v5,v5\n"
MU"v240,v240,v235\n"
MF"v[48:51],a[104:107],a[40:43],v[48:51]\n"
EX"v6,v6\n"
PK"v240,v240,v240\n"
MF"v[48:51],a[108:111],a[44:47],v[48:51]\n"
EX"v7,v7\n"
PK"v24,v24,v25\n"
MF"v[56:59],a[108:111],a[60:63],v[56:59]\n"
EX"v12,v12\n"
"v_lshlrev_b32 v240,16,v240\n"
MF"v[56:59],a[104:107],a[56:59],v[56:59]\n"
PK"v241,v241,v241\n"
"v_lshlrev_b32 v241,16,v241\n"
PK"v25,v26,v27\n"
MF"v[36:39],a[112:115],a[0:3],v[220:223]\n"
WT"lgkmcnt(1)\n"
EX"v13,v13\n"
MU"v241,v241,v235\n"
MF"v[36:39],a[116:119],a[4:7],v[36:39]\n"
RD"a[64:67],v227 offset:16384\n"
PK"v241,v241,v241\n"
MF"v[44:47],a[116:119],a[20:23],0\n"
EX"v14,v14\n"
"v_lshlrev_b32 v241,16,v241\n"
MF"v[44:47],a[112:115],a[16:19],v[44:47]\n"
PK"v242,v242,v242\n"
PK"v2,v4,v5\n"
MF"v[52:55],a[112:115],a[32:35],0\n"
EX"v15,v15\n"
"v_lshlrev_b32 v242,16,v242\n"
MF"v[52:55],a[116:119],a[36:39],v[52:55]\n"
RD"a[68:71],v227 offset:17408\n"
MU"v242,v242,v235\n"
PK"v3,v6,v7\n"
MF"v[60:63],a[116:119],a[52:55],0\n"
EX"v20,v20\n"
PK"v242,v242,v242\n"
MF"v[60:63],a[112:115],a[48:51],v[60:63]\n"
"v_readlane_b32 s43,v228,s42\n"
"v_lshlrev_b32 v242,16,v242\n"
MF"v[36:39],a[120:123],a[8:11],v[36:39]\n"
WT"lgkmcnt(1)\n"
EX"v21,v21\n"
PK"v243,v243,v243\n"
MF"v[36:39],a[124:127],a[12:15],v[36:39]\n"
RD"a[72:75],v227 offset:18432\n"
"v_lshlrev_b32 v243,16,v243\n"
PK"v10,v12,v13\n"
MF"v[44:47],a[124:127],a[28:31],v[44:47]\n"
EX"v22,v22\n"
MU"v243,v243,v235\n"
MF"v[44:47],a[120:123],a[24:27],v[44:47]\n"
"s_mov_b32 m0,s40\n"
PK"v243,v243,v243\n"
"v_lshlrev_b32 v243,16,v243\n"
MF"v[52:55],a[120:123],a[40:43],v[52:55]\n"
EX"v23,v23\n"
MF"v[52:55],a[124:127],a[44:47],v[52:55]\n"
RD"a[76:79],v227 offset:19456\n"
"v_readlane_b32 s44,v229,s42\n"
PK"v11,v14,v15\n"
MF"v[60:63],a[124:127],a[60:63],v[60:63]\n"
EX"v28,v28\n"
MF"v[60:63],a[120:123],a[56:59],v[60:63]\n"
SA"s42,s42,1\n"
BL"v225,s[8:11],s43 offen offset:0 sc1 lds\n"
MF"v[80:83],a[64:67],v[64:67],v[80:83]\n"
SA"s41,s41,32768\n"
EX"v29,v29\n"
MF"v[112:115],a[64:67],v[68:71],v[112:115]\n"
"s_cmp_eq_u32 s41,163840\n"
RD"a[80:83],v227 offset:20480\n"
PK"v18,v20,v21\n"
MF"v[144:147],a[64:67],v[72:75],v[144:147]\n"
"s_cselect_b32 s41,0,s41\n"
EX"v30,v30\n"
MF"v[176:179],a[64:67],v[76:79],v[176:179]\n"
WT"lgkmcnt(2)\n"
"v_add_u32 v226,s41,v224\n"
PK"v19,v22,v23\n"
MF"v[84:87],a[68:71],v[64:67],v[84:87]\n"
EX"v31,v31\n"
MF"v[116:119],a[68:71],v[68:71],v[116:119]\n"
RD"a[84:87],v227 offset:21504\n"
PK"v26,v28,v29\n"
MF"v[148:151],a[68:71],v[72:75],v[148:151]\n"
EX"v32,v32\n"
MF"v[180:183],a[68:71],v[76:79],v[180:183]\n"
BL"v225,s[8:11],s43 offen offset:1024 sc1 lds\n"
MF"v[88:91],a[72:75],v[64:67],v[88:91]\n"
EX"v33,v33\n"
MF"v[120:123],a[72:75],v[68:71],v[120:123]\n"
RD"a[88:91],v227 offset:22528\n"
PK"v27,v30,v31\n"
MF"v[152:155],a[72:75],v[72:75],v[152:155]\n"
EX"v34,v34\n"
MF"v[184:187],a[72:75],v[76:79],v[184:187]\n"
WT"lgkmcnt(2)\n"
WT"vmcnt(2)\n"
"v_lshlrev_b32 v244,16,v254\n"
"v_and_b32 v245,0xffff0000,v254\n"
MF"v[92:95],a[76:79],v[64:67],v[92:95]\n"
EX"v35,v35\n"
"v_lshlrev_b32 v246,16,v255\n"
MF"v[124:127],a[76:79],v[68:71],v[124:127]\n"
RD"a[92:95],v227 offset:23552\n"
"v_and_b32 v247,0xffff0000,v255\n"
"v_add_f32 v240,v240,v244\n"
MF"v[156:159],a[76:79],v[72:75],v[156:159]\n"
EX"v40,v40\n"
"v_add_f32 v241,v241,v245\n"
MF"v[188:191],a[76:79],v[76:79],v[188:191]\n"
BL"v225,s[8:11],s43 offen offset:2048 sc1 lds\n"
"v_add_f32 v242,v242,v246\n"
MF"v[96:99],a[80:83],v[64:67],v[96:99]\n"
EX"v41,v41\n"
"v_add_f32 v243,v243,v247\n"
MF"v[128:131],a[80:83],v[68:71],v[128:131]\n"
RD"a[96:99],v227 offset:24576\n"
PK"v240,v240,v241\n"
PK"v241,v242,v243\n"
MF"v[160:163],a[80:83],v[72:75],v[160:163]\n"
"buffer_store_dwordx2 v[240:241],v239,s[8:11],s93 offen sc1\n"
MF"v[192:195],a[80:83],v[76:79],v[192:195]\n"
WT"lgkmcnt(2)\n"
EX"v42,v42\n"
MF"v[100:103],a[84:87],v[64:67],v[100:103]\n"
EX"v43,v43\n"
MF"v[132:135],a[84:87],v[68:71],v[132:135]\n"
RD"a[100:103],v227 offset:25600\n"
MF"v[164:167],a[84:87],v[72:75],v[164:167]\n"
EX"v48,v48\n"
MF"v[196:199],a[84:87],v[76:79],v[196:199]\n"
BL"v225,s[8:11],s43 offen offset:3072 sc1 lds\n"
MF"v[104:107],a[88:91],v[64:67],v[104:107]\n"
EX"v49,v49\n"
MF"v[136:139],a[88:91],v[68:71],v[136:139]\n"
RD"a[104:107],v227 offset:26624\n"
MF"v[168:171],a[88:91],v[72:75],v[168:171]\n"
EX"v50,v50\n"
MF"v[200:203],a[88:91],v[76:79],v[200:203]\n"
WT"lgkmcnt(2)\n"
SA"m0,s40,16384\n"
MF"v[108:111],a[92:95],v[64:67],v[108:111]\n"
EX"v51,v51\n"
MF"v[140:143],a[92:95],v[68:71],v[140:143]\n"
RD"a[108:111],v227 offset:27648\n"
MF"v[172:175],a[92:95],v[72:75],v[172:175]\n"
EX"v56,v56\n"
MF"v[204:207],a[92:95],v[76:79],v[204:207]\n"
BL"v225,s[8:11],s44 offen offset:0 sc1 lds\n"
MF"a[132:135],a[128:131],v[64:67],a[132:135]\n"
SA"s40,s40,32768\n"
EX"v57,v57\n"
MF"a[136:139],a[128:131],v[68:71],a[136:139]\n"
"s_cmp_eq_u32 s40,s46\n"
RD"a[112:115],v227 offset:28672\n"
"s_cselect_b32 s40,s47,s40\n"
MF"a[140:143],a[128:131],v[72:75],a[140:143]\n"
EX"v58,v58\n"
MF"a[144:147],a[128:131],v[76:79],a[144:147]\n"
MF"v[80:83],a[96:99],v[0:3],v[80:83]\n"
EX"v59,v59\n"
MF"v[112:115],a[96:99],v[8:11],v[112:115]\n"
RD"a[116:119],v227 offset:29696\n"
MF"v[144:147],a[96:99],v[16:19],v[144:147]\n"
EX"v36,v36\n"
MF"v[176:179],a[96:99],v[24:27],v[176:179]\n"
WT"lgkmcnt(2)\n"
BL"v225,s[8:11],s44 offen offset:1024 sc1 lds\n"
MF"v[84:87],a[100:103],v[0:3],v[84:87]\n"
EX"v37,v37\n"
MF"v[116:119],a[100:103],v[8:11],v[116:119]\n"
RD"a[120:123],v227 offset:30720\n"
MF"v[148:151],a[100:103],v[16:19],v[148:151]\n"
EX"v38,v38\n"
MF"v[180:183],a[100:103],v[24:27],v[180:183]\n"
MF"v[88:91],a[104:107],v[0:3],v[88:91]\n"
EX"v39,v39\n"
MF"v[120:123],a[104:107],v[8:11],v[120:123]\n"
RD"a[124:127],v227 offset:31744\n"
MF"v[152:155],a[104:107],v[16:19],v[152:155]\n"
EX"v44,v44\n"
MF"v[184:187],a[104:107],v[24:27],v[184:187]\n"
BL"v225,s[8:11],s44 offen offset:2048 sc1 lds\n"
MF"v[92:95],a[108:111],v[0:3],v[92:95]\n"
EX"v45,v45\n"
MF"v[124:127],a[108:111],v[8:11],v[124:127]\n"
MF"v[156:159],a[108:111],v[16:19],v[156:159]\n"
EX"v46,v46\n"
MF"v[188:191],a[108:111],v[24:27],v[188:191]\n"
WT"lgkmcnt(1)\n"
MF"v[96:99],a[112:115],v[0:3],v[96:99]\n"
WT"lgkmcnt(0)\n"
"s_barrier\n"
MF"v[128:131],a[112:115],v[8:11],v[128:131]\n"
RD"a[64:67],v226 offset:0\n"
MF"v[160:163],a[112:115],v[16:19],v[160:163]\n"
EX"v47,v47\n"
MF"v[192:195],a[112:115],v[24:27],v[192:195]\n"
BL"v225,s[8:11],s44 offen offset:3072 sc1 lds\n"
MF"v[100:103],a[116:119],v[0:3],v[100:103]\n"
EX"v52,v52\n"
MF"v[132:135],a[116:119],v[8:11],v[132:135]\n"
RD"a[68:71],v226 offset:1024\n"
MF"v[164:167],a[116:119],v[16:19],v[164:167]\n"
EX"v53,v53\n"
MF"v[196:199],a[116:119],v[24:27],v[196:199]\n"
RD"a[72:75],v226 offset:2048\n"
MF"v[104:107],a[120:123],v[0:3],v[104:107]\n"
EX"v54,v54\n"
MF"v[136:139],a[120:123],v[8:11],v[136:139]\n"
RD"a[76:79],v226 offset:3072\n"
MF"v[168:171],a[120:123],v[16:19],v[168:171]\n"
EX"v55,v55\n"
MF"v[200:203],a[120:123],v[24:27],v[200:203]\n"
MF"v[108:111],a[124:127],v[0:3],v[108:111]\n"
WT"vmcnt(5)\n"
"buffer_store_dwordx2 v[232:233],v230,s[8:11],s94 offen sc1\n"
MF"v[140:143],a[124:127],v[8:11],v[140:143]\n"
EX"v60,v60\n"
MF"v[172:175],a[124:127],v[16:19],v[172:175]\n"
RD"a[80:83],v226 offset:4096\n"
MF"v[204:207],a[124:127],v[24:27],v[204:207]\n"
EX"v61,v61\n"
MF"a[132:135],a[128:131],v[0:3],a[132:135]\n"
RD"a[84:87],v226 offset:5120\n"
MF"a[136:139],a[128:131],v[8:11],a[136:139]\n"
EX"v62,v62\n"
MF"a[140:143],a[128:131],v[16:19],a[140:143]\n"
RD"a[88:91],v226 offset:6144\n"
MF"a[144:147],a[128:131],v[24:27],a[144:147]\n"
EX"v63,v63\n"
"s_branch LDB_%=\n"
"LME_%=:\n"
WT"lgkmcnt(10)\n"
MF"v[0:3],a[64:67],a[0:3],0\n"
WT"lgkmcnt(2)\n"
"v_lshrrev_b32 v239,9,v224\n"
"v_lshlrev_b32 v239,8,v239\n"
MF"v[0:3],a[68:71],a[4:7],v[0:3]\n"
PK"v64,v32,v33\n"
PK"v65,v34,v35\n"
MF"v[8:11],a[68:71],a[20:23],0\n"
"v_bfe_u32 v237,v224,8,1\n"
PK"v66,v36,v37\n"
MF"v[8:11],a[64:67],a[16:19],v[8:11]\n"
PK"v67,v38,v39\n"
"v_lshl_add_u32 v239,v237,3,v239\n"
MF"v[16:19],a[64:67],a[32:35],0\n"
PK"v68,v40,v41\n"
PK"v69,v42,v43\n"
MF"v[16:19],a[68:71],a[36:39],v[16:19]\n"
RD"a[92:95],v226 offset:7168\n"
"v_and_b32 v237,0x80,v224\n"
MF"v[24:27],a[68:71],a[52:55],0\n"
PK"v70,v44,v45\n"
PK"v71,v46,v47\n"
MF"v[24:27],a[64:67],a[48:51],v[24:27]\n"
"v_lshl_add_u32 v239,v237,23,v239\n"
"v_not_b32 v236,v234\n"
MF"v[0:3],a[72:75],a[8:11],v[0:3]\n"
PK"v72,v48,v49\n"
PK"v73,v50,v51\n"
MF"v[0:3],a[76:79],a[12:15],v[0:3]\n"
SA"s53,s93,4292282368\n"
"v_mov_b32 v227,v226\n"
"v_and_b32 v237,15,v236\n"
MF"v[8:11],a[76:79],a[28:31],v[8:11]\n"
PK"v74,v52,v53\n"
PK"v75,v54,v55\n"
MF"v[8:11],a[72:75],a[24:27],v[8:11]\n"
"v_bfe_u32 v236,v236,4,7\n"
PK"v76,v56,v57\n"
MF"v[16:19],a[72:75],a[40:43],v[16:19]\n"
PK"v77,v58,v59\n"
"v_mul_u32_u24 v236,20480,v236\n"
MF"v[16:19],a[76:79],a[44:47],v[16:19]\n"
RD"a[96:99],v226 offset:8192\n"
PK"v78,v60,v61\n"
MF"v[24:27],a[76:79],a[60:63],v[24:27]\n"
EX"v0,v0\n"
MF"v[24:27],a[72:75],a[56:59],v[24:27]\n"
PK"v79,v62,v63\n"
"v_lshl_add_u32 v236,v237,4,v236\n"
MF"v[4:7],a[80:83],a[0:3],0\n"
WT"lgkmcnt(1)\n"
EX"v1,v1\n"
"v_add_u32 v236,v236,v239\n"
MF"v[4:7],a[84:87],a[4:7],v[4:7]\n"
RD"a[100:103],v226 offset:9216\n"
"v_and_b32 v237,0x70,v224\n"
MF"v[12:15],a[84:87],a[20:23],0\n"
EX"v2,v2\n"
MF"v[12:15],a[80:83],a[16:19],v[12:15]\n"
EX"v3,v3\n"
"v_add_u32 v239,v239,v237\n"
MF"v[20:23],a[80:83],a[32:35],0\n"
EX"v8,v8\n"
"v_add_f32 v240,v240,v244\n"
MF"v[20:23],a[84:87],a[36:39],v[20:23]\n"
RD"a[104:107],v226 offset:10240\n"
"v_add_f32 v248,v248,v252\n"
"v_add_f32 v241,v241,v245\n"
MF"v[28:31],a[84:87],a[52:55],0\n"
EX"v9,v9\n"
"v_add_f32 v249,v249,v253\n"
MF"v[28:31],a[80:83],a[48:51],v[28:31]\n"
"v_add_f32 v242,v242,v246\n"
"v_add_f32 v250,v250,v254\n"
MF"v[4:7],a[88:91],a[8:11],v[4:7]\n"
EX"v10,v10\n"
"v_add_f32 v243,v243,v247\n"
MF"v[4:7],a[92:95],a[12:15],v[4:7]\n"
RD"a[108:111],v226 offset:11264\n"
"v_add_f32 v251,v251,v255\n"
PK"v0,v0,v1\n"
MF"v[12:15],a[92:95],a[28:31],v[12:15]\n"
"buffer_load_dwordx2 v[254:255],v236,s[8:11],s53 offen sc1\n"
"v_ashrrev_i32 v235,31,v234\n"
MF"v[12:15],a[88:91],a[24:27],v[12:15]\n"
EX"v11,v11\n"
"v_not_b32 v235,v235\n"
MF"v[20:23],a[88:91],a[40:43],v[20:23]\n"
EX"v16,v16\n"
"v_or_b32 v235,0x80000000,v235\n"
MF"v[20:23],a[92:95],a[44:47],v[20:23]\n"
EX"v17,v17\n"
PK"v1,v2,v3\n"
MF"v[28:31],a[92:95],a[60:63],v[28:31]\n"
RD"a[112:115],v226 offset:12288\n"
"v_xor_b32 v235,v235,v234\n"
"v_and_b32 v235,0xffff0000,v235\n"
MF"v[28:31],a[88:91],a[56:59],v[28:31]\n"
WT"lgkmcnt(2)\n"
EX"v18,v18\n"
MU"v235,0xbfb8aa3b,v235\n"
MF"v[32:35],a[96:99],a[0:3],0\n"
EX"v235,v235\n"
PK"v8,v8,v9\n"
MF"v[32:35],a[100:103],a[4:7],v[32:35]\n"
EX"v19,v19\n"
"v_add_f32 v235,1.0,v235\n"
MF"v[40:43],a[100:103],a[20:23],0\n"
"v_rcp_f32 v235,v235\n"
MF"v[40:43],a[96:99],a[16:19],v[40:43]\n"
RD"a[116:119],v226 offset:13312\n"
PK"v235,v235,v235\n"
PK"v9,v10,v11\n"
MF"v[48:51],a[96:99],a[32:35],0\n"
EX"v24,v24\n"
"v_lshlrev_b32 v235,16,v235\n"
MF"v[48:51],a[100:103],a[36:39],v[48:51]\n"
EX"v25,v25\n"
"v_add_f32 v240,v240,v248\n"
MF"v[56:59],a[100:103],a[52:55],0\n"
EX"v26,v26\n"
PK"v16,v16,v17\n"
MF"v[56:59],a[96:99],a[48:51],v[56:59]\n"
RD"a[120:123],v226 offset:14336\n"
"v_add_f32 v241,v241,v249\n"
"v_add_f32 v242,v242,v250\n"
MF"v[32:35],a[104:107],a[8:11],v[32:35]\n"
WT"lgkmcnt(2)\n"
EX"v27,v27\n"
"v_add_f32 v243,v243,v251\n"
MF"v[32:35],a[108:111],a[12:15],v[32:35]\n"
EX"v4,v4\n"
PK"v240,v240,v240\n"
MF"v[40:43],a[108:111],a[28:31],v[40:43]\n"
RD"a[124:127],v226 offset:15360\n"
PK"v17,v18,v19\n"
"v_lshlrev_b32 v240,16,v240\n"
MF"v[40:43],a[104:107],a[24:27],v[40:43]\n"
EX"v5,v5\n"
MU"v240,v240,v235\n"
MF"v[48:51],a[104:107],a[40:43],v[48:51]\n"
EX"v6,v6\n"
PK"v240,v240,v240\n"
MF"v[48:51],a[108:111],a[44:47],v[48:51]\n"
EX"v7,v7\n"
PK"v24,v24,v25\n"
MF"v[56:59],a[108:111],a[60:63],v[56:59]\n"
EX"v12,v12\n"
"v_lshlrev_b32 v240,16,v240\n"
MF"v[56:59],a[104:107],a[56:59],v[56:59]\n"
PK"v241,v241,v241\n"
"v_lshlrev_b32 v241,16,v241\n"
PK"v25,v26,v27\n"
MF"v[36:39],a[112:115],a[0:3],0\n"
WT"lgkmcnt(1)\n"
EX"v13,v13\n"
MU"v241,v241,v235\n"
MF"v[36:39],a[116:119],a[4:7],v[36:39]\n"
RD"a[64:67],v227 offset:16384\n"
PK"v241,v241,v241\n"
MF"v[44:47],a[116:119],a[20:23],0\n"
EX"v14,v14\n"
"v_lshlrev_b32 v241,16,v241\n"
MF"v[44:47],a[112:115],a[16:19],v[44:47]\n"
PK"v242,v242,v242\n"
PK"v2,v4,v5\n"
MF"v[52:55],a[112:115],a[32:35],0\n"
EX"v15,v15\n"
"v_lshlrev_b32 v242,16,v242\n"
MF"v[52:55],a[116:119],a[36:39],v[52:55]\n"
RD"a[68:71],v227 offset:17408\n"
MU"v242,v242,v235\n"
PK"v3,v6,v7\n"
MF"v[60:63],a[116:119],a[52:55],0\n"
EX"v20,v20\n"
PK"v242,v242,v242\n"
MF"v[60:63],a[112:115],a[48:51],v[60:63]\n"
"v_readlane_b32 s43,v228,s42\n"
"v_lshlrev_b32 v242,16,v242\n"
MF"v[36:39],a[120:123],a[8:11],v[36:39]\n"
WT"lgkmcnt(1)\n"
EX"v21,v21\n"
PK"v243,v243,v243\n"
MF"v[36:39],a[124:127],a[12:15],v[36:39]\n"
RD"a[72:75],v227 offset:18432\n"
"v_lshlrev_b32 v243,16,v243\n"
PK"v10,v12,v13\n"
MF"v[44:47],a[124:127],a[28:31],v[44:47]\n"
EX"v22,v22\n"
MU"v243,v243,v235\n"
MF"v[44:47],a[120:123],a[24:27],v[44:47]\n"
"s_mov_b32 m0,s40\n"
PK"v243,v243,v243\n"
"v_lshlrev_b32 v243,16,v243\n"
MF"v[52:55],a[120:123],a[40:43],v[52:55]\n"
EX"v23,v23\n"
MF"v[52:55],a[124:127],a[44:47],v[52:55]\n"
RD"a[76:79],v227 offset:19456\n"
"v_readlane_b32 s44,v229,s42\n"
PK"v11,v14,v15\n"
MF"v[60:63],a[124:127],a[60:63],v[60:63]\n"
EX"v28,v28\n"
MF"v[60:63],a[120:123],a[56:59],v[60:63]\n"
SA"s42,s42,1\n"
BL"v225,s[8:11],s43 offen offset:0 sc1 lds\n"
MF"v[80:83],a[64:67],v[64:67],v[80:83]\n"
SA"s41,s41,32768\n"
EX"v29,v29\n"
MF"v[112:115],a[64:67],v[68:71],v[112:115]\n"
"s_cmp_eq_u32 s41,163840\n"
RD"a[80:83],v227 offset:20480\n"
PK"v18,v20,v21\n"
MF"v[144:147],a[64:67],v[72:75],v[144:147]\n"
"s_cselect_b32 s41,0,s41\n"
EX"v30,v30\n"
MF"v[176:179],a[64:67],v[76:79],v[176:179]\n"
WT"lgkmcnt(2)\n"
"v_add_u32 v226,s41,v224\n"
PK"v19,v22,v23\n"
MF"v[84:87],a[68:71],v[64:67],v[84:87]\n"
EX"v31,v31\n"
MF"v[116:119],a[68:71],v[68:71],v[116:119]\n"
RD"a[84:87],v227 offset:21504\n"
PK"v26,v28,v29\n"
MF"v[148:151],a[68:71],v[72:75],v[148:151]\n"
EX"v32,v32\n"
MF"v[180:183],a[68:71],v[76:79],v[180:183]\n"
BL"v225,s[8:11],s43 offen offset:1024 sc1 lds\n"
MF"v[88:91],a[72:75],v[64:67],v[88:91]\n"
EX"v33,v33\n"
MF"v[120:123],a[72:75],v[68:71],v[120:123]\n"
RD"a[88:91],v227 offset:22528\n"
PK"v27,v30,v31\n"
MF"v[152:155],a[72:75],v[72:75],v[152:155]\n"
EX"v34,v34\n"
MF"v[184:187],a[72:75],v[76:79],v[184:187]\n"
WT"lgkmcnt(2)\n"
WT"vmcnt(2)\n"
"v_lshlrev_b32 v244,16,v254\n"
"v_and_b32 v245,0xffff0000,v254\n"
MF"v[92:95],a[76:79],v[64:67],v[92:95]\n"
EX"v35,v35\n"
"v_lshlrev_b32 v246,16,v255\n"
MF"v[124:127],a[76:79],v[68:71],v[124:127]\n"
RD"a[92:95],v227 offset:23552\n"
"v_and_b32 v247,0xffff0000,v255\n"
"v_add_f32 v240,v240,v244\n"
MF"v[156:159],a[76:79],v[72:75],v[156:159]\n"
EX"v40,v40\n"
"v_add_f32 v241,v241,v245\n"
MF"v[188:191],a[76:79],v[76:79],v[188:191]\n"
BL"v225,s[8:11],s43 offen offset:2048 sc1 lds\n"
"v_add_f32 v242,v242,v246\n"
MF"v[96:99],a[80:83],v[64:67],v[96:99]\n"
EX"v41,v41\n"
"v_add_f32 v243,v243,v247\n"
MF"v[128:131],a[80:83],v[68:71],v[128:131]\n"
RD"a[96:99],v227 offset:24576\n"
PK"v240,v240,v241\n"
PK"v241,v242,v243\n"
MF"v[160:163],a[80:83],v[72:75],v[160:163]\n"
"buffer_store_dwordx2 v[240:241],v239,s[8:11],s93 offen sc1\n"
MF"v[192:195],a[80:83],v[76:79],v[192:195]\n"
WT"lgkmcnt(2)\n"
EX"v42,v42\n"
MF"v[100:103],a[84:87],v[64:67],v[100:103]\n"
EX"v43,v43\n"
MF"v[132:135],a[84:87],v[68:71],v[132:135]\n"
RD"a[100:103],v227 offset:25600\n"
MF"v[164:167],a[84:87],v[72:75],v[164:167]\n"
EX"v48,v48\n"
MF"v[196:199],a[84:87],v[76:79],v[196:199]\n"
BL"v225,s[8:11],s43 offen offset:3072 sc1 lds\n"
MF"v[104:107],a[88:91],v[64:67],v[104:107]\n"
EX"v49,v49\n"
MF"v[136:139],a[88:91],v[68:71],v[136:139]\n"
RD"a[104:107],v227 offset:26624\n"
MF"v[168:171],a[88:91],v[72:75],v[168:171]\n"
EX"v50,v50\n"
MF"v[200:203],a[88:91],v[76:79],v[200:203]\n"
WT"lgkmcnt(2)\n"
SA"m0,s40,16384\n"
MF"v[108:111],a[92:95],v[64:67],v[108:111]\n"
EX"v51,v51\n"
MF"v[140:143],a[92:95],v[68:71],v[140:143]\n"
RD"a[108:111],v227 offset:27648\n"
MF"v[172:175],a[92:95],v[72:75],v[172:175]\n"
EX"v56,v56\n"
MF"v[204:207],a[92:95],v[76:79],v[204:207]\n"
BL"v225,s[8:11],s44 offen offset:0 sc1 lds\n"
MF"a[132:135],a[128:131],v[64:67],a[132:135]\n"
SA"s40,s40,32768\n"
EX"v57,v57\n"
MF"a[136:139],a[128:131],v[68:71],a[136:139]\n"
"s_cmp_eq_u32 s40,s46\n"
RD"a[112:115],v227 offset:28672\n"
"s_cselect_b32 s40,s47,s40\n"
MF"a[140:143],a[128:131],v[72:75],a[140:143]\n"
EX"v58,v58\n"
MF"a[144:147],a[128:131],v[76:79],a[144:147]\n"
MF"v[80:83],a[96:99],v[0:3],v[80:83]\n"
EX"v59,v59\n"
MF"v[112:115],a[96:99],v[8:11],v[112:115]\n"
RD"a[116:119],v227 offset:29696\n"
MF"v[144:147],a[96:99],v[16:19],v[144:147]\n"
EX"v36,v36\n"
MF"v[176:179],a[96:99],v[24:27],v[176:179]\n"
WT"lgkmcnt(2)\n"
BL"v225,s[8:11],s44 offen offset:1024 sc1 lds\n"
MF"v[84:87],a[100:103],v[0:3],v[84:87]\n"
EX"v37,v37\n"
MF"v[116:119],a[100:103],v[8:11],v[116:119]\n"
RD"a[120:123],v227 offset:30720\n"
MF"v[148:151],a[100:103],v[16:19],v[148:151]\n"
EX"v38,v38\n"
MF"v[180:183],a[100:103],v[24:27],v[180:183]\n"
MF"v[88:91],a[104:107],v[0:3],v[88:91]\n"
EX"v39,v39\n"
MF"v[120:123],a[104:107],v[8:11],v[120:123]\n"
RD"a[124:127],v227 offset:31744\n"
MF"v[152:155],a[104:107],v[16:19],v[152:155]\n"
EX"v44,v44\n"
MF"v[184:187],a[104:107],v[24:27],v[184:187]\n"
BL"v225,s[8:11],s44 offen offset:2048 sc1 lds\n"
MF"v[92:95],a[108:111],v[0:3],v[92:95]\n"
EX"v45,v45\n"
MF"v[124:127],a[108:111],v[8:11],v[124:127]\n"
MF"v[156:159],a[108:111],v[16:19],v[156:159]\n"
EX"v46,v46\n"
MF"v[188:191],a[108:111],v[24:27],v[188:191]\n"
WT"lgkmcnt(1)\n"
MF"v[96:99],a[112:115],v[0:3],v[96:99]\n"
WT"lgkmcnt(0)\n"
"s_barrier\n"
MF"v[128:131],a[112:115],v[8:11],v[128:131]\n"
RD"a[64:67],v226 offset:0\n"
MF"v[160:163],a[112:115],v[16:19],v[160:163]\n"
EX"v47,v47\n"
MF"v[192:195],a[112:115],v[24:27],v[192:195]\n"
BL"v225,s[8:11],s44 offen offset:3072 sc1 lds\n"
MF"v[100:103],a[116:119],v[0:3],v[100:103]\n"
EX"v52,v52\n"
MF"v[132:135],a[116:119],v[8:11],v[132:135]\n"
RD"a[68:71],v226 offset:1024\n"
MF"v[164:167],a[116:119],v[16:19],v[164:167]\n"
EX"v53,v53\n"
MF"v[196:199],a[116:119],v[24:27],v[196:199]\n"
RD"a[72:75],v226 offset:2048\n"
MF"v[104:107],a[120:123],v[0:3],v[104:107]\n"
EX"v54,v54\n"
MF"v[136:139],a[120:123],v[8:11],v[136:139]\n"
RD"a[76:79],v226 offset:3072\n"
MF"v[168:171],a[120:123],v[16:19],v[168:171]\n"
EX"v55,v55\n"
MF"v[200:203],a[120:123],v[24:27],v[200:203]\n"
MF"v[108:111],a[124:127],v[0:3],v[108:111]\n"
WT"vmcnt(5)\n"
"buffer_store_dwordx2 v[232:233],v230,s[8:11],s94 offen sc1\n"
MF"v[140:143],a[124:127],v[8:11],v[140:143]\n"
EX"v60,v60\n"
MF"v[172:175],a[124:127],v[16:19],v[172:175]\n"
RD"a[80:83],v226 offset:4096\n"
MF"v[204:207],a[124:127],v[24:27],v[204:207]\n"
EX"v61,v61\n"
MF"a[132:135],a[128:131],v[0:3],a[132:135]\n"
RD"a[84:87],v226 offset:5120\n"
MF"a[136:139],a[128:131],v[8:11],a[136:139]\n"
EX"v62,v62\n"
MF"a[140:143],a[128:131],v[16:19],a[140:143]\n"
RD"a[88:91],v226 offset:6144\n"
MF"a[144:147],a[128:131],v[24:27],a[144:147]\n"
EX"v63,v63\n"
"s_mov_b32 s57,0\n"
"s_lshl_b32 s56,s34,1\n"
"s_sub_u32 s56,s56,12\n"
"s_branch LP1_%=\n"
"LRT_%=:\n"
WT"lgkmcnt(6)\n"
MF"v[0:3],a[64:67],a[0:3],0\n"
WT"lgkmcnt(5)\n"
"v_lshrrev_b32 v242,1,v224\n"
PK"v64,v32,v33\n"
MF"v[0:3],a[68:71],a[4:7],v[0:3]\n"
"s_mov_b32 s12,s74\n"
PK"v65,v34,v35\n"
"v_and_b32 v242,56,v242\n"
MF"v[8:11],a[68:71],a[20:23],0\n"
"s_and_b32 s13,s75,0xffff\n"
PK"v66,v36,v37\n"
PK"v67,v38,v39\n"
MF"v[8:11],a[64:67],a[16:19],v[8:11]\n"
"s_mov_b32 s14,5242880\n"
PK"v68,v40,v41\n"
PK"v69,v42,v43\n"
MF"v[16:19],a[64:67],a[32:35],0\n"
"s_lshl_b32 s23,s35,6\n"
PK"v70,v44,v45\n"
PK"v71,v46,v47\n"
MF"v[16:19],a[68:71],a[36:39],v[16:19]\n"
"s_mov_b32 s15,0x20000\n"
RD"a[92:95],v226 offset:7168\n"
"v_add_u32 v243,s40,v252\n"
MF"v[24:27],a[68:71],a[52:55],0\n"
SA"s23,s23,4352\n"
PK"v72,v48,v49\n"
PK"v73,v50,v51\n"
MF"v[24:27],a[64:67],a[48:51],v[24:27]\n"
WT"lgkmcnt(1)\n"
"buffer_load_dwordx2 v[244:245],v242,s[8:11],s23 offen sc1\n"
MF"v[0:3],a[72:75],a[8:11],v[0:3]\n"
SA"s19,s40,32768\n"
"v_mov_b32 v227,v226\n"
PK"v74,v52,v53\n"
MF"v[0:3],a[76:79],a[12:15],v[0:3]\n"
"s_cmp_eq_u32 s19,s46\n"
PK"v75,v54,v55\n"
PK"v76,v56,v57\n"
MF"v[8:11],a[76:79],a[28:31],v[8:11]\n"
"s_cselect_b32 s19,s47,s19\n"
PK"v77,v58,v59\n"
PK"v78,v60,v61\n"
MF"v[8:11],a[72:75],a[24:27],v[8:11]\n"
"s_cmp_eq_u32 s20,0\n"
PK"v79,v62,v63\n"
"s_cselect_b32 s17,2130706432,s17\n"
MF"v[16:19],a[72:75],a[40:43],v[16:19]\n"
SA"m0,s19,0\n"
MF"v[16:19],a[76:79],a[44:47],v[16:19]\n"
BL"v240,s[12:15],s17 offen offset:0 lds\n"
MF"v[24:27],a[76:79],a[60:63],v[24:27]\n"
RD"a[96:99],v226 offset:8192\n"
MF"v[24:27],a[72:75],a[56:59],v[24:27]\n"
EX"v0,v0\n"
MF"v[4:7],a[80:83],a[0:3],0\n"
BL"v241,s[12:15],s17 offen offset:1024 lds\n"
MF"v[4:7],a[84:87],a[4:7],v[4:7]\n"
EX"v1,v1\n"
MF"v[12:15],a[84:87],a[20:23],0\n"
RD"a[100:103],v226 offset:9216\n"
PK"v0,v0,v1\n"
MF"v[12:15],a[80:83],a[16:19],v[12:15]\n"
EX"v2,v2\n"
MF"v[20:23],a[80:83],a[32:35],0\n"
BL"v251,s[12:15],s17 offen offset:2048 lds\n"
MF"v[20:23],a[84:87],a[36:39],v[20:23]\n"
SA"m0,s19,16384\n"
EX"v3,v3\n"
MF"v[28:31],a[84:87],a[52:55],0\n"
SA"s17,s17,20480\n"
EX"v8,v8\n"
MF"v[28:31],a[80:83],a[48:51],v[28:31]\n"
RD"a[104:107],v226 offset:10240\n"
PK"v1,v2,v3\n"
MF"v[4:7],a[88:91],a[8:11],v[4:7]\n"
WT"lgkmcnt(2)\n"
EX"v9,v9\n"
MF"v[4:7],a[92:95],a[12:15],v[4:7]\n"
BL"v240,s[12:15],s17 offen offset:0 lds\n"
MF"v[12:15],a[92:95],a[28:31],v[12:15]\n"
EX"v10,v10\n"
MF"v[12:15],a[88:91],a[24:27],v[12:15]\n"
RD"a[108:111],v226 offset:11264\n"
PK"v8,v8,v9\n"
MF"v[20:23],a[88:91],a[40:43],v[20:23]\n"
EX"v11,v11\n"
MF"v[20:23],a[92:95],a[44:47],v[20:23]\n"
WT"vmcnt(13)\n"
RT"a[148:149],v243 offset:0\n"
RT"a[150:151],v243 offset:16384\n"
EX"v16,v16\n"
MF"v[28:31],a[92:95],a[60:63],v[28:31]\n"
EX"v17,v17\n"
MF"v[28:31],a[88:91],a[56:59],v[28:31]\n"
RD"a[112:115],v226 offset:12288\n"
PK"v9,v10,v11\n"
MF"v[32:35],a[96:99],a[0:3],0\n"
WT"lgkmcnt(4)\n"
EX"v18,v18\n"
MF"v[32:35],a[100:103],a[4:7],v[32:35]\n"
BL"v241,s[12:15],s17 offen offset:1024 lds\n"
MF"v[40:43],a[100:103],a[20:23],0\n"
EX"v19,v19\n"
MF"v[40:43],a[96:99],a[16:19],v[40:43]\n"
RD"a[116:119],v226 offset:13312\n"
RT"a[152:153],v243 offset:32\n"
MF"v[48:51],a[96:99],a[32:35],0\n"
EX"v24,v24\n"
MF"v[48:51],a[100:103],a[36:39],v[48:51]\n"
RT"a[154:155],v243 offset:16416\n"
RD"a[120:123],v226 offset:14336\n"
EX"v25,v25\n"
MF"v[56:59],a[100:103],a[52:55],0\n"
EX"v26,v26\n"
PK"v16,v16,v17\n"
MF"v[56:59],a[96:99],a[48:51],v[56:59]\n"
EX"v27,v27\n"
MF"v[32:35],a[104:107],a[8:11],v[32:35]\n"
WT"lgkmcnt(4)\n"
BL"v251,s[12:15],s17 offen offset:2048 lds\n"
MF"v[32:35],a[108:111],a[12:15],v[32:35]\n"
EX"v4,v4\n"
PK"v17,v18,v19\n"
MF"v[40:43],a[108:111],a[28:31],v[40:43]\n"
RD"a[124:127],v226 offset:15360\n"
PK"v24,v24,v25\n"
MF"v[40:43],a[104:107],a[24:27],v[40:43]\n"
EX"v5,v5\n"
MF"v[48:51],a[104:107],a[40:43],v[48:51]\n"
RT"a[156:157],v243 offset:64\n"
RT"a[158:159],v243 offset:16448\n"
MF"v[48:51],a[108:111],a[44:47],v[48:51]\n"
EX"v6,v6\n"
MF"v[56:59],a[108:111],a[60:63],v[56:59]\n"
EX"v7,v7\n"
MF"v[56:59],a[104:107],a[56:59],v[56:59]\n"
EX"v12,v12\n"
PK"v25,v26,v27\n"
MF"v[36:39],a[112:115],a[0:3],0\n"
WT"lgkmcnt(3)\n"
EX"v13,v13\n"
MF"v[36:39],a[116:119],a[4:7],v[36:39]\n"
RD"a[64:67],v227 offset:16384\n"
PK"v2,v4,v5\n"
MF"v[44:47],a[116:119],a[20:23],0\n"
EX"v14,v14\n"
MF"v[44:47],a[112:115],a[16:19],v[44:47]\n"
RT"a[160:161],v243 offset:96\n"
RT"a[162:163],v243 offset:16480\n"
MF"v[52:55],a[112:115],a[32:35],0\n"
EX"v15,v15\n"
MF"v[52:55],a[116:119],a[36:39],v[52:55]\n"
RD"a[68:71],v227 offset:17408\n"
PK"v3,v6,v7\n"
MF"v[60:63],a[116:119],a[52:55],0\n"
EX"v20,v20\n"
MF"v[60:63],a[112:115],a[48:51],v[60:63]\n"
"v_readlane_b32 s43,v228,s42\n"
PK"v10,v12,v13\n"
MF"v[36:39],a[120:123],a[8:11],v[36:39]\n"
WT"lgkmcnt(3)\n"
EX"v21,v21\n"
MF"v[36:39],a[124:127],a[12:15],v[36:39]\n"
RD"a[72:75],v227 offset:18432\n"
PK"v11,v14,v15\n"
MF"v[44:47],a[124:127],a[28:31],v[44:47]\n"
EX"v22,v22\n"
MF"v[44:47],a[120:123],a[24:27],v[44:47]\n"
"s_mov_b32 m0,s40\n"
RT"a[164:165],v243 offset:128\n"
RT"a[166:167],v243 offset:16512\n"
MF"v[52:55],a[120:123],a[40:43],v[52:55]\n"
SA"s17,s17,61440\n"
EX"v23,v23\n"
MF"v[52:55],a[124:127],a[44:47],v[52:55]\n"
RD"a[76:79],v227 offset:19456\n"
"v_readlane_b32 s44,v229,s42\n"
MF"v[60:63],a[124:127],a[60:63],v[60:63]\n"
EX"v28,v28\n"
MF"v[60:63],a[120:123],a[56:59],v[60:63]\n"
WT"lgkmcnt(1)\n"
BL"v225,s[8:11],s43 offen offset:0 sc1 lds\n"
MF"v[80:83],a[64:67],v[64:67],v[80:83]\n"
SA"s42,s42,1\n"
EX"v29,v29\n"
MF"v[112:115],a[64:67],v[68:71],v[112:115]\n"
SA"s41,s41,32768\n"
RD"a[80:83],v227 offset:20480\n"
PK"v18,v20,v21\n"
MF"v[144:147],a[64:67],v[72:75],v[144:147]\n"
"s_cmp_eq_u32 s41,163840\n"
EX"v30,v30\n"
MF"v[176:179],a[64:67],v[76:79],v[176:179]\n"
"s_cselect_b32 s41,0,s41\n"
BS"a[148:151],v224,s[8:11],s18 offen offset:0 sc1\n"
MF"v[84:87],a[68:71],v[64:67],v[84:87]\n"
EX"v31,v31\n"
MF"v[116:119],a[68:71],v[68:71],v[116:119]\n"
RD"a[84:87],v227 offset:21504\n"
"v_add_u32 v226,s41,v224\n"
PK"v19,v22,v23\n"
MF"v[148:151],a[68:71],v[72:75],v[148:151]\n"
EX"v32,v32\n"
MF"v[180:183],a[68:71],v[76:79],v[180:183]\n"
BL"v225,s[8:11],s43 offen offset:1024 sc1 lds\n"
MF"v[88:91],a[72:75],v[64:67],v[88:91]\n"
EX"v33,v33\n"
MF"v[120:123],a[72:75],v[68:71],v[120:123]\n"
RD"a[88:91],v227 offset:22528\n"
PK"v26,v28,v29\n"
MF"v[152:155],a[72:75],v[72:75],v[152:155]\n"
EX"v34,v34\n"
MF"v[184:187],a[72:75],v[76:79],v[184:187]\n"
WT"lgkmcnt(2)\n"
BS"a[152:155],v224,s[8:11],s18 offen offset:2048 sc1\n"
MF"v[92:95],a[76:79],v[64:67],v[92:95]\n"
EX"v35,v35\n"
MF"v[124:127],a[76:79],v[68:71],v[124:127]\n"
RD"a[92:95],v227 offset:23552\n"
PK"v27,v30,v31\n"
MF"v[156:159],a[76:79],v[72:75],v[156:159]\n"
SA"s18,s18,4096\n"
EX"v40,v40\n"
MF"v[188:191],a[76:79],v[76:79],v[188:191]\n"
BL"v225,s[8:11],s43 offen offset:2048 sc1 lds\n"
MF"v[96:99],a[80:83],v[64:67],v[96:99]\n"
EX"v41,v41\n"
MF"v[128:131],a[80:83],v[68:71],v[128:131]\n"
RD"a[96:99],v227 offset:24576\n"
MF"v[160:163],a[80:83],v[72:75],v[160:163]\n"
EX"v42,v42\n"
MF"v[192:195],a[80:83],v[76:79],v[192:195]\n"
WT"lgkmcnt(2)\n"
BS"a[156:159],v224,s[8:11],s18 offen offset:0 sc1\n"
MF"v[100:103],a[84:87],v[64:67],v[100:103]\n"
EX"v43,v43\n"
MF"v[132:135],a[84:87],v[68:71],v[132:135]\n"
RD"a[100:103],v227 offset:25600\n"
MF"v[164:167],a[84:87],v[72:75],v[164:167]\n"
EX"v48,v48\n"
MF"v[196:199],a[84:87],v[76:79],v[196:199]\n"
BL"v225,s[8:11],s43 offen offset:3072 sc1 lds\n"
MF"v[104:107],a[88:91],v[64:67],v[104:107]\n"
EX"v49,v49\n"
MF"v[136:139],a[88:91],v[68:71],v[136:139]\n"
RD"a[104:107],v227 offset:26624\n"
MF"v[168:171],a[88:91],v[72:75],v[168:171]\n"
EX"v50,v50\n"
MF"v[200:203],a[88:91],v[76:79],v[200:203]\n"
WT"lgkmcnt(2)\n"
BS"a[160:163],v224,s[8:11],s18 offen offset:2048 sc1\n"
MF"v[108:111],a[92:95],v[64:67],v[108:111]\n"
SA"m0,s40,16384\n"
EX"v51,v51\n"
MF"v[140:143],a[92:95],v[68:71],v[140:143]\n"
RD"a[108:111],v227 offset:27648\n"
MF"v[172:175],a[92:95],v[72:75],v[172:175]\n"
SA"s18,s18,4096\n"
EX"v56,v56\n"
MF"v[204:207],a[92:95],v[76:79],v[204:207]\n"
BL"v225,s[8:11],s44 offen offset:0 sc1 lds\n"
MF"a[132:135],a[128:131],v[64:67],a[132:135]\n"
SA"s40,s40,32768\n"
EX"v57,v57\n"
MF"a[136:139],a[128:131],v[68:71],a[136:139]\n"
"s_cmp_eq_u32 s40,s46\n"
RD"a[112:115],v227 offset:28672\n"
"s_cselect_b32 s40,s47,s40\n"
MF"a[140:143],a[128:131],v[72:75],a[140:143]\n"
EX"v58,v58\n"
MF"a[144:147],a[128:131],v[76:79],a[144:147]\n"
BS"a[164:167],v224,s[8:11],s18 offen offset:0 sc1\n"
MF"v[80:83],a[96:99],v[0:3],v[80:83]\n"
"s_lshr_b32 s21,s16,1\n"
EX"v59,v59\n"
MF"v[112:115],a[96:99],v[8:11],v[112:115]\n"
"s_sub_u32 s21,s21,s33\n"
RD"a[116:119],v227 offset:29696\n"
MF"v[144:147],a[96:99],v[16:19],v[144:147]\n"
"s_and_b32 s21,s21,3\n"
EX"v36,v36\n"
MF"v[176:179],a[96:99],v[24:27],v[176:179]\n"
WT"lgkmcnt(2)\n"
BL"v225,s[8:11],s44 offen offset:1024 sc1 lds\n"
MF"v[84:87],a[100:103],v[0:3],v[84:87]\n"
"s_lshl_b32 s21,s21,1\n"
EX"v37,v37\n"
MF"v[116:119],a[100:103],v[8:11],v[116:119]\n"
"s_and_b32 s22,s16,1\n"
RD"a[120:123],v227 offset:30720\n"
SA"s21,s21,s22\n"
MF"v[148:151],a[100:103],v[16:19],v[148:151]\n"
"s_lshl_b32 s21,s21,1\n"
EX"v38,v38\n"
MF"v[180:183],a[100:103],v[24:27],v[180:183]\n"
"s_and_b32 s22,s30,1\n"
MF"v[88:91],a[104:107],v[0:3],v[88:91]\n"
SA"s21,s21,s22\n"
EX"v39,v39\n"
MF"v[120:123],a[104:107],v[8:11],v[120:123]\n"
"s_mul_i32 s21,s21,10240\n"
RD"a[124:127],v227 offset:31744\n"
MF"v[152:155],a[104:107],v[16:19],v[152:155]\n"
"s_lshr_b32 s22,s30,1\n"
EX"v44,v44\n"
MF"v[184:187],a[104:107],v[24:27],v[184:187]\n"
"s_lshl_b32 s22,s22,10\n"
BL"v225,s[8:11],s44 offen offset:2048 sc1 lds\n"
MF"v[92:95],a[108:111],v[0:3],v[92:95]\n"
SA"s21,s21,s22\n"
EX"v45,v45\n"
MF"v[124:127],a[108:111],v[8:11],v[124:127]\n"
"s_mul_i32 s22,s35,163840\n"
MF"v[156:159],a[108:111],v[16:19],v[156:159]\n"
SA"s21,s21,s22\n"
EX"v46,v46\n"
MF"v[188:191],a[108:111],v[24:27],v[188:191]\n"
WT"lgkmcnt(1)\n"
SA"s18,s21,3428352\n"
MF"v[96:99],a[112:115],v[0:3],v[96:99]\n"
SA"s16,s16,1\n"
WT"lgkmcnt(0)\n"
"s_barrier\n"
MF"v[128:131],a[112:115],v[8:11],v[128:131]\n"
RD"a[64:67],v226 offset:0\n"
MF"v[160:163],a[112:115],v[16:19],v[160:163]\n"
EX"v47,v47\n"
MF"v[192:195],a[112:115],v[24:27],v[192:195]\n"
BL"v225,s[8:11],s44 offen offset:3072 sc1 lds\n"
MF"v[100:103],a[116:119],v[0:3],v[100:103]\n"
EX"v52,v52\n"
MF"v[132:135],a[116:119],v[8:11],v[132:135]\n"
RD"a[68:71],v226 offset:1024\n"
MF"v[164:167],a[116:119],v[16:19],v[164:167]\n"
EX"v53,v53\n"
MF"v[196:199],a[116:119],v[24:27],v[196:199]\n"
RD"a[72:75],v226 offset:2048\n"
MF"v[104:107],a[120:123],v[0:3],v[104:107]\n"
EX"v54,v54\n"
MF"v[136:139],a[120:123],v[8:11],v[136:139]\n"
RD"a[76:79],v226 offset:3072\n"
MF"v[168:171],a[120:123],v[16:19],v[168:171]\n"
EX"v55,v55\n"
MF"v[200:203],a[120:123],v[24:27],v[200:203]\n"
MF"v[108:111],a[124:127],v[0:3],v[108:111]\n"
EX"v60,v60\n"
MF"v[140:143],a[124:127],v[8:11],v[140:143]\n"
RD"a[80:83],v226 offset:4096\n"
MF"v[172:175],a[124:127],v[16:19],v[172:175]\n"
EX"v61,v61\n"
MF"v[204:207],a[124:127],v[24:27],v[204:207]\n"
RD"a[84:87],v226 offset:5120\n"
MF"a[132:135],a[128:131],v[0:3],a[132:135]\n"
EX"v62,v62\n"
MF"a[136:139],a[128:131],v[8:11],a[136:139]\n"
RD"a[88:91],v226 offset:6144\n"
MF"a[140:143],a[128:131],v[16:19],a[140:143]\n"
EX"v63,v63\n"
MF"a[144:147],a[128:131],v[24:27],a[144:147]\n"
"s_sub_u32 s20,s20,1\n"
"s_cbranch_scc0 LRT_%=\n"
"s_lshl_b32 s21,s35,8\n"
SA"s21,s21,81264640\n"
SA"s21,s21,s45\n"
WT"vmcnt(3)\n"
"s_barrier\n"
"buffer_store_dwordx2 v[232:233],v230,s[8:11],s21 offen sc1\n"
"v_cmp_eq_u64 vcc,s[28:29],v[244:245]\n"
"s_cmp_eq_u64 vcc,exec\n"
"s_cbranch_scc0 L1105_%=\n"
"L1106_%=:\n"
"s_mov_b32 s57,0\n"
"s_mov_b32 s56,0\n"
"s_mov_b32 s14,0\n"
"s_branch LP1_%=\n"
"LEND_%=:\n"
      : : [ka] "s"(__builtin_amdgcn_kernarg_segment_ptr()), [b] "s"(blockIdx.x), [t] "v"(threadIdx.x)
      : "v1", "v2", "v3", "v4", "v5", "v6", "v7", "v8", "v9", "v10", "v11", "v12", "v13", "v14", "v15", "v16", "v17", "v18", "v19", "v20", "v21", "v22", "v23", "v24", "v25", "v26", "v27", "v28", "v29", "v30", "v31", "v32", "v33", "v34", "v35", "v36", "v37", "v38", "v39", "v40", "v41", "v42", "v43", "v44", "v45", "v46", "v47", "v48", "v49", "v50", "v51", "v52", "v53", "v54", "v55", "v56", "v57", "v58", "v59", "v60", "v61", "v62", "v63", "v64", "v65", "v66", "v67", "v68", "v69", "v70", "v71", "v72", "v73", "v74", "v75", "v76", "v77", "v78", "v79", "v80", "v81", "v82", "v83", "v84", "v85", "v86", "v87", "v88", "v89", "v90", "v91", "v92", "v93", "v94", "v95", "v96", "v97", "v98", "v99", "v100", "v101", "v102", "v103", "v104", "v105", "v106", "v107", "v108", "v109", "v110", "v111", "v112", "v113", "v114", "v115", "v116", "v117", "v118", "v119", "v120", "v121", "v122", "v123", "v124", "v125", "v126", "v127", "v128", "v129", "v130", "v131", "v132", "v133", "v134", "v135", "v136", "v137", "v138", "v139", "v140", "v141", "v142", "v143", "v144", "v145", "v146", "v147", "v148", "v149", "v150", "v151", "v152", "v153", "v154", "v155", "v156", "v157", "v158", "v159", "v160", "v161", "v162", "v163", "v164", "v165", "v166", "v167", "v168", "v169", "v170", "v171", "v172", "v173", "v174", "v175", "v176", "v177", "v178", "v179", "v180", "v181", "v182", "v183", "v184", "v185", "v186", "v187", "v188", "v189", "v190", "v191", "v192", "v193", "v194", "v195", "v196", "v197", "v198", "v199", "v200", "v201", "v202", "v203", "v204", "v205", "v206", "v207", "v208", "v209", "v210", "v211", "v212", "v213", "v214", "v215", "v216", "v217", "v218", "v219", "v220", "v221", "v222", "v223", "v224", "v225", "v226", "v227", "v228", "v229", "v230", "v231", "v232", "v233", "v234", "v235", "v236", "v237", "v238", "v239", "v240", "v241", "v242", "v243", "v244", "v245", "v246", "v247", "v248", "v249", "v250", "v251", "v252", "v253", "v254", "v255", "a0", "a1", "a2", "a3", "a4", "a5", "a6", "a7", "a8", "a9", "a10", "a11", "a12", "a13", "a14", "a15", "a16", "a17", "a18", "a19", "a20", "a21", "a22", "a23", "a24", "a25", "a26", "a27", "a28", "a29", "a30", "a31", "a32", "a33", "a34", "a35", "a36", "a37", "a38", "a39", "a40", "a41", "a42", "a43", "a44", "a45", "a46", "a47", "a48", "a49", "a50", "a51", "a52", "a53", "a54", "a55", "a56", "a57", "a58", "a59", "a60", "a61", "a62", "a63", "a64", "a65", "a66", "a67", "a68", "a69", "a70", "a71", "a72", "a73", "a74", "a75", "a76", "a77", "a78", "a79", "a80", "a81", "a82", "a83", "a84", "a85", "a86", "a87", "a88", "a89", "a90", "a91", "a92", "a93", "a94", "a95", "a96", "a97", "a98", "a99", "a100", "a101", "a102", "a103", "a104", "a105", "a106", "a107", "a108", "a109", "a110", "a111", "a112", "a113", "a114", "a115", "a116", "a117", "a118", "a119", "a120", "a121", "a122", "a123", "a124", "a125", "a126", "a127", "a128", "a129", "a130", "a131", "a132", "a133", "a134", "a135", "a136", "a137", "a138", "a139", "a140", "a141", "a142", "a143", "a144", "a145", "a146", "a147", "a148", "a149", "a150", "a151", "a152", "a153", "a154", "a155", "a156", "a157", "a158", "a159", "a160", "a161", "a162", "a163", "a164", "a165", "a166", "a167", "a168", "a169", "a170", "a171", "a172", "a173", "a174", "a175", "a176", "a177", "a178", "a179", "a180", "a181", "a182", "a183", "a184", "a185", "a186", "a187", "a188", "a189", "a190", "a191", "a192", "a193", "a194", "a195", "a196", "a197", "a198", "a199", "a200", "a201", "a202", "a203", "a204", "a205", "a206", "a207", "a208", "a209", "a210", "a211", "a212", "a213", "a214", "a215", "a216", "a217", "a218", "a219", "a220", "a221", "a222", "a223", "a224", "a225", "a226", "a227", "a228", "a229", "a230", "a231", "a232", "a233", "a234", "a235", "a236", "a237", "a238", "a239", "a240", "a241", "a242", "a243", "a244", "a245", "a246", "a247", "a248", "a249", "a250", "a251", "a252", "a253", "a254", "a255", "s8", "s9", "s10", "s11", "s12", "s13", "s14", "s15", "s16", "s17", "s18", "s19", "s20", "s21", "s22", "s23", "s24", "s25", "s26", "s27", "s28", "s29", "s30", "s31", "s32", "s33", "s34", "s35", "s36", "s37", "s38", "s39", "s40", "s41", "s42", "s43", "s44", "s45", "s46", "s47", "s48", "s49", "s50", "s51", "s52", "s53", "s54", "s55", "s56", "s57", "s58", "s59", "s60", "s61", "s62", "s63", "s64", "s65", "s66", "s67", "s68", "s69", "s70", "s71", "s72", "s73", "s74", "s75", "s76", "s77", "s78", "s79", "s80", "s81", "s82", "s83", "s84", "s85", "s86", "s87", "s88", "s89", "s90", "s91", "s92", "s93", "s94", "s95", "s96", "s97", "s98", "s99", "scc", "vcc", "memory");
}

extern "C" void kernel_launch(void* const* d_in, const int* in_sizes, int n_in, void* d_out, int out_size, void* d_ws, size_t ws_size, hipStream_t stream) {
  (void)hipFuncSetAttribute((const void*)mega_kernel, hipFuncAttributeMaxDynamicSharedMemorySize, 163840);
  mega_kernel<<<256, 256, 163840, stream>>>((const bf16*)d_in[0], (const bf16*)d_in[1], (const bf16*)d_in[2], (const bf16*)d_in[3], (const bf16*)d_in[4], (const bf16*)d_in[5],
      (const bf16*)d_in[7], (const bf16*)d_in[8], (const bf16*)d_in[9], (const bf16*)d_in[10], (char*)d_ws + (size_t)0 * 1048576, (bf16*)d_out);
}
```

```cpp
#include <hip/hip_runtime.h>
#include <hip/hip_bf16.h>
typedef __hip_bfloat16 bf16;
#define MF "v_mfma_f32_16x16x32_bf16 "
#define RD "ds_read_b128 "
#define RT "ds_read_b64_tr_b16 "
#define BL "buffer_load_dwordx4 "
#define BS "buffer_store_dwordx4 "
#define PK "v_cvt_pk_bf16_f32 "
#define EX "v_exp_f32 "
#define WT "s_waitcnt "
#define MU "v_mul_f32 "
#define SA "s_add_u32 "

__global__ __launch_bounds__(256, 1) void mega_kernel(const bf16* x, const bf16* ckv, const bf16* wq, const bf16* wuk, const bf16* wuv, const bf16* wo, const bf16* w_r,
                                                      const bf16* wg, const bf16* wu, const bf16* wd, char* ws, bf16* out) {
  asm volatile(
"s_load_dwordx16 s[64:79],%[ka],0\n"
"s_load_dwordx8 s[80:87],%[ka],64\n"
"s_mov_b32 s31,%[b]\n"
"v_lshrrev_b32 v1,6,%[t]\n"
"v_and_b32 v2,63,%[t]\n"
"v_readfirstlane_b32 s30,v1\n"
"v_lshlrev_b32 v224,4,v2\n"
"s_and_b32 s32,s31,7\n"
"s_lshr_b32 s33,s31,6\n"
"s_bfe_u32 s34,s31,0x30003\n"
"s_lshl_b32 s35,s32,2\n"
SA"s35,s35,s33\n"
"s_mov_b32 s46,0x76543210\n"
"s_mov_b32 s47,0x43217650\n"
"s_bitcmp1_b32 s34,0\n"
"s_cselect_b32 s46,0x21765430,s46\n"
"s_cselect_b32 s47,0x65432170,s47\n"
"s_bitcmp1_b32 s34,1\n"
"s_cselect_b32 s46,s47,s46\n"
"s_mul_i32 s47,s32,0x11111111\n"
SA"s46,s46,s47\n"
"s_lshl_b32 s36,s30,12\n"
"s_mul_i32 s37,s30,24576\n"
SA"s37,s37,65536\n"
"v_lshrrev_b32 v3,3,v2\n"
"v_lshrrev_b32 v4,4,v2\n"
"v_and_b32 v5,7,v2\n"
"v_xor_b32 v6,v5,v4\n"
"v_lshlrev_b32 v6,4,v6\n"
"v_lshl_add_u32 v6,v3,10,v6\n"
"s_lshl_b32 s50,s30,15\n"
"v_add_u32 v234,s50,v6\n"
"v_xor_b32 v235,64,v234\n"
"v_add_u32 v235,7168,v235\n"
"v_add_u32 v236,14336,v234\n"
"v_add_u32 v237,14336,v235\n"
"v_and_b32 v7,1,v4\n"
"v_lshlrev_b32 v7,1,v7\n"
"v_xor_b32 v7,v7,v5\n"
"v_lshlrev_b32 v7,4,v7\n"
"v_lshl_add_u32 v238,v3,13,v7\n"
"v_xor_b32 v239,64,v238\n"
"v_add_u32 v239,64512,v239\n"
"v_add_u32 v240,129024,v238\n"
"v_add_u32 v241,129024,v239\n"
"v_and_b32 v8,31,v2\n"
"v_lshlrev_b32 v8,3,v8\n"
"v_lshrrev_b32 v9,5,v2\n"
"s_lshl_b32 s50,s31,2\n"
SA"s50,s50,s30\n"
"s_mul_i32 s50,s50,2560\n"
"v_mul_u32_u24 v9,1280,v9\n"
"v_add3_u32 v9,v9,v8,s50\n"
"s_lshl_b32 s50,s34,17\n"
"s_lshl_b32 s51,s35,8\n"
"s_and_b32 s52,s30,1\n"
"s_lshl_b32 s52,s52,7\n"
SA"s52,s52,s51\n"
"s_mov_b32 s14,0x120000\n"
"s_mov_b32 s15,0x20000\n"
"s_sub_u32 s18,4194304,s52\n"
"s_mov_b32 s19,0x20000\n"
"s_getpc_b64 s[24:25]\n"
"LPC_%=:\n"
"s_and_b32 s25,s25,0xffff\n"
"s_mov_b32 s26,LEND_%=-LPC_%=\n"
"s_mov_b32 s27,0x20000\n"
"s_lshr_b32 s49,s31,3\n"
"s_lshl_b32 s49,s49,2\n"
SA"s49,s49,s30\n"
"s_lshl_b32 s49,s49,7\n"
"v_lshlrev_b32 v10,14,v2\n"
"buffer_load_dword v255,v10,s[24:27],s49 offen\n"
WT"lgkmcnt(0)\n"
"s_load_dwordx2 s[28:29],s[84:85],0\n"
SA"s12,s66,s50\n"
"s_addc_u32 s13,s67,0\n"
"s_and_b32 s13,s13,0xffff\n"
"s_cmp_lt_u32 s30,2\n"
"s_cselect_b64 s[16:17],s[70:71],s[72:73]\n"
SA"s16,s16,s52\n"
"s_addc_u32 s17,s17,0\n"
"s_and_b32 s17,s17,0xffff\n"
"global_load_dwordx2 v[128:129],v9,s[64:65] offset:0\n"
"global_load_dwordx2 v[130:131],v9,s[64:65] offset:256\n"
"global_load_dwordx2 v[132:133],v9,s[64:65] offset:512\n"
"global_load_dwordx2 v[134:135],v9,s[64:65] offset:768\n"
"global_load_dwordx2 v[136:137],v9,s[64:65] offset:1024\n"
"global_load_dwordx2 v[138:139],v8,s[76:77] offset:0\n"
"global_load_dwordx2 v[140:141],v8,s[76:77] offset:256\n"
"global_load_dwordx2 v[142:143],v8,s[76:77] offset:512\n"
"global_load_dwordx2 v[144:145],v8,s[76:77] offset:768\n"
"global_load_dwordx2 v[146:147],v8,s[76:77] offset:1024\n"
SA"m0,s37,0\n"
"s_bfe_u32 s48,s46,0x30000\n"
"s_lshl_b32 s48,s48,19\n"
BL"v238,s[16:19],s48 offen offset:0 nt lds\n"
BL"v239,s[16:19],s48 offen offset:1024 nt lds\n"
BL"v240,s[16:19],s48 offen offset:2048 nt lds\n"
BL"v241,s[16:19],s48 offen offset:3072 nt lds\n"
SA"m0,s37,4096\n"
"s_bfe_u32 s48,s46,0x30000\n"
"s_lshl_b32 s48,s48,19\n"
"s_bitset1_b32 s48,18\n"
BL"v238,s[16:19],s48 offen offset:0 nt lds\n"
BL"v239,s[16:19],s48 offen offset:1024 nt lds\n"
BL"v240,s[16:19],s48 offen offset:2048 nt lds\n"
BL"v241,s[16:19],s48 offen offset:3072 nt lds\n"
"v_lshlrev_b32 v253,7,%[t]\n"
"s_mov_b32 s8,s84\n"
"s_and_b32 s9,s85,0xffff\n"
"s_mov_b32 s10,81272832\n"
"s_mov_b32 s11,0x20000\n"
SA"s20,s68,s51\n"
"s_addc_u32 s21,s69,0\n"
"s_and_b32 s21,s21,0xffff\n"
"s_sub_u32 s22,5242880,s51\n"
"s_mov_b32 s23,0x20000\n"
"v_and_b32 v11,15,v2\n"
"v_lshrrev_b32 v12,1,v11\n"
"v_xor_b32 v12,v12,v4\n"
"v_lshlrev_b32 v12,4,v12\n"
"v_lshl_add_u32 v242,v11,7,v12\n"
"v_xor_b32 v243,64,v242\n"
"v_lshrrev_b32 v12,2,v11\n"
"v_lshl_add_u32 v12,v4,3,v12\n"
"v_lshlrev_b32 v12,7,v12\n"
"v_and_b32 v13,3,v2\n"
"v_lshl_add_u32 v12,v13,3,v12\n"
"v_and_b32 v13,3,v3\n"
"v_lshl_add_u32 v12,v13,5,v12\n"
"v_add_u32 v246,s37,v12\n"
"v_xor_b32 v247,32,v246\n"
"v_xor_b32 v248,64,v246\n"
"v_xor_b32 v249,96,v246\n"
"v_mov_b32 v13,2130706432\n"
"v_cmp_eq_u32 vcc,0,v2\n"
"s_cmp_eq_u32 s30,0\n"
"s_cselect_b32 s45,0,2130706432\n"
"v_cndmask_b32_e64 v230,v13,0,vcc\n"
"s_lshl_b32 s39,s30,3\n"
"s_lshr_b32 s98,s46,s39\n"
"s_and_b32 s39,s98,7\n"
"s_lshl_b32 s39,s39,7\n"
"s_and_b32 s98,s98,0x70\n"
"s_lshl_b32 s98,s98,3\n"
"s_lshl_b32 s50,s30,15\n"
"s_sub_u32 s50,1048576,s50\n"
SA"s39,s39,s50\n"
SA"s98,s98,s50\n"
"s_mov_b32 s55,0x3e0293ee\n"
"s_cmp_lt_u32 s30,2\n"
"s_cbranch_scc0 LV_%=\n"
WT"vmcnt(17)\n"
"v_lshlrev_b32 v148,16,v128\n"
"v_and_b32 v149,0xffff0000,v128\n"
"v_lshlrev_b32 v150,16,v129\n"
"v_and_b32 v151,0xffff0000,v129\n"
WT"vmcnt(16)\n"
"v_lshlrev_b32 v152,16,v130\n"
"v_and_b32 v153,0xffff0000,v130\n"
"v_lshlrev_b32 v154,16,v131\n"
"v_and_b32 v155,0xffff0000,v131\n"
WT"vmcnt(15)\n"
"v_lshlrev_b32 v156,16,v132\n"
"v_and_b32 v157,0xffff0000,v132\n"
"v_lshlrev_b32 v158,16,v133\n"
"v_and_b32 v159,0xffff0000,v133\n"
WT"vmcnt(14)\n"
"v_lshlrev_b32 v160,16,v134\n"
"v_and_b32 v161,0xffff0000,v134\n"
"v_lshlrev_b32 v162,16,v135\n"
"v_and_b32 v163,0xffff0000,v135\n"
WT"vmcnt(13)\n"
"v_lshlrev_b32 v164,16,v136\n"
"v_and_b32 v165,0xffff0000,v136\n"
"v_lshlrev_b32 v166,16,v137\n"
"v_and_b32 v167,0xffff0000,v137\n"
"v_add_f32 v188,v148,v149\n"
"v_add_f32 v189,v150,v151\n"
"v_add_f32 v188,v188,v152\n"
"v_add_f32 v189,v189,v153\n"
"v_add_f32 v188,v188,v154\n"
"v_add_f32 v189,v189,v155\n"
"v_add_f32 v188,v188,v156\n"
"v_add_f32 v189,v189,v157\n"
"v_add_f32 v188,v188,v158\n"
"v_add_f32 v189,v189,v159\n"
"v_add_f32 v188,v188,v160\n"
"v_add_f32 v189,v189,v161\n"
"v_add_f32 v188,v188,v162\n"
"v_add_f32 v189,v189,v163\n"
"v_add_f32 v188,v188,v164\n"
"v_add_f32 v189,v189,v165\n"
"v_add_f32 v188,v188,v166\n"
"v_add_f32 v189,v189,v167\n"
"v_add_f32 v188,v188,v189\n"
"s_nop 1\n"
"v_add_f32_dpp v188,v188,v188 quad_perm:[1,0,3,2] row_mask:0xf bank_mask:0xf\n"
WT"vmcnt(12)\n"
"v_lshlrev_b32 v168,16,v138\n"
"v_and_b32 v169,0xffff0000,v138\n"
"v_add_f32_dpp v188,v188,v188 quad_perm:[2,3,0,1] row_mask:0xf bank_mask:0xf\n"
"v_lshlrev_b32 v170,16,v139\n"
"v_and_b32 v171,0xffff0000,v139\n"
"v_add_f32_dpp v188,v188,v188 row_half_mirror row_mask:0xf bank_mask:0xf\n"
WT"vmcnt(11)\n"
"v_lshlrev_b32 v172,16,v140\n"
SA"m0,s36,0\n"
"s_bfe_u32 s48,s46,0x30000\n"
"s_lshl_b32 s48,s48,7\n"
BL"v234,s[12:15],s48 offen offset:0 lds\n"
BL"v235,s[12:15],s48 offen offset:1024 lds\n"
BL"v236,s[12:15],s48 offen offset:2048 lds\n"
BL"v237,s[12:15],s48 offen offset:3072 lds\n"
"v_and_b32 v173,0xffff0000,v140\n"
"v_add_f32_dpp v188,v188,v188 row_mirror row_mask:0xf bank_mask:0xf\n"
"v_lshlrev_b32 v174,16,v141\n"
"v_and_b32 v175,0xffff0000,v141\n"
"v_mov_b32 v189,v188\n"
WT"vmcnt(14)\n"
"v_lshlrev_b32 v176,16,v142\n"
"v_and_b32 v177,0xffff0000,v142\n"
"v_permlane16_swap_b32 v189,v188\n"
"v_lshlrev_b32 v178,16,v143\n"
"v_and_b32 v179,0xffff0000,v143\n"
"v_add_f32 v188,v188,v189\n"
WT"vmcnt(13)\n"
"v_lshlrev_b32 v180,16,v144\n"
"v_and_b32 v181,0xffff0000,v144\n"
MU"v191,0x3acccccd,v188\n"
"v_lshlrev_b32 v182,16,v145\n"
"v_and_b32 v183,0xffff0000,v145\n"
PK"v128,v191,v191\n"
WT"vmcnt(12)\n"
"v_lshlrev_b32 v184,16,v146\n"
"v_and_b32 v185,0xffff0000,v146\n"
"v_lshlrev_b32 v128,16,v128\n"
"v_lshlrev_b32 v186,16,v147\n"
"v_and_b32 v187,0xffff0000,v147\n"
"v_sub_f32 v148,v148,v128\n"
"v_sub_f32 v149,v149,v128\n"
"v_sub_f32 v150,v150,v128\n"
"v_sub_f32 v151,v151,v128\n"
"v_sub_f32 v152,v152,v128\n"
"v_sub_f32 v153,v153,v128\n"
"v_sub_f32 v154,v154,v128\n"
"v_sub_f32 v155,v155,v128\n"
"v_sub_f32 v156,v156,v128\n"
"v_sub_f32 v157,v157,v128\n"
"v_sub_f32 v158,v158,v128\n"
"v_sub_f32 v159,v159,v128\n"
"v_sub_f32 v160,v160,v128\n"
"v_sub_f32 v161,v161,v128\n"
"v_sub_f32 v162,v162,v128\n"
"v_sub_f32 v163,v163,v128\n"
"v_sub_f32 v164,v164,v128\n"
"v_sub_f32 v165,v165,v128\n"
"v_sub_f32 v166,v166,v128\n"
"v_sub_f32 v167,v167,v128\n"
MU"v188,v148,v148\n"
MU"v189,v149,v149\n"
"v_fmac_f32 v188,v150,v150\n"
"v_fmac_f32 v189,v151,v151\n"
"v_fmac_f32 v188,v152,v152\n"
SA"m0,s37,8192\n"
"s_bfe_u32 s48,s46,0x30004\n"
"s_lshl_b32 s48,s48,19\n"
BL"v238,s[16:19],s48 offen offset:0 nt lds\n"
BL"v239,s[16:19],s48 offen offset:1024 nt lds\n"
BL"v240,s[16:19],s48 offen offset:2048 nt lds\n"
BL"v241,s[16:19],s48 offen offset:3072 nt lds\n"
"v_fmac_f32 v189,v153,v153\n"
"v_fmac_f32 v188,v154,v154\n"
"v_fmac_f32 v189,v155,v155\n"
"v_fmac_f32 v188,v156,v156\n"
"v_fmac_f32 v189,v157,v157\n"
"v_fmac_f32 v188,v158,v158\n"
"v_fmac_f32 v189,v159,v159\n"
"v_fmac_f32 v188,v160,v160\n"
"v_fmac_f32 v189,v161,v161\n"
"v_fmac_f32 v188,v162,v162\n"
"v_fmac_f32 v189,v163,v163\n"
"v_fmac_f32 v188,v164,v164\n"
"v_fmac_f32 v189,v165,v165\n"
"v_fmac_f32 v188,v166,v166\n"
"v_fmac_f32 v189,v167,v167\n"
"v_add_f32 v188,v188,v189\n"
PK"v149,v148,v149\n"
"v_lshlrev_b32 v148,16,v149\n"
"v_add_f32_dpp v188,v188,v188 quad_perm:[1,0,3,2] row_mask:0xf bank_mask:0xf\n"
"v_and_b32 v149,0xffff0000,v149\n"
PK"v151,v150,v151\n"
"v_add_f32_dpp v188,v188,v188 quad_perm:[2,3,0,1] row_mask:0xf bank_mask:0xf\n"
"v_lshlrev_b32 v150,16,v151\n"
"v_and_b32 v151,0xffff0000,v151\n"
"v_add_f32_dpp v188,v188,v188 row_half_mirror row_mask:0xf bank_mask:0xf\n"
PK"v153,v152,v153\n"
"v_lshlrev_b32 v152,16,v153\n"
"v_add_f32_dpp v188,v188,v188 row_mirror row_mask:0xf bank_mask:0xf\n"
"v_and_b32 v153,0xffff0000,v153\n"
PK"v155,v154,v155\n"
"v_mov_b32 v189,v188\n"
"v_lshlrev_b32 v154,16,v155\n"
"v_and_b32 v155,0xffff0000,v155\n"
"v_permlane16_swap_b32 v189,v188\n"
PK"v157,v156,v157\n"
"v_lshlrev_b32 v156,16,v157\n"
"v_add_f32 v188,v188,v189\n"
"v_and_b32 v157,0xffff0000,v157\n"
PK"v159,v158,v159\n"
"v_sub_f32 v191,v191,v128\n"
"v_lshlrev_b32 v158,16,v159\n"
"v_and_b32 v159,0xffff0000,v159\n"
MU"v188,0x3acccccd,v188\n"
PK"v161,v160,v161\n"
"v_lshlrev_b32 v160,16,v161\n"
"v_fma_f32 v188,-v191,v191,v188\n"
"v_and_b32 v161,0xffff0000,v161\n"
PK"v163,v162,v163\n"
SA"m0,s37,12288\n"
"s_bfe_u32 s48,s46,0x30004\n"
"s_lshl_b32 s48,s48,19\n"
"s_bitset1_b32 s48,18\n"
BL"v238,s[16:19],s48 offen offset:0 nt lds\n"
BL"v239,s[16:19],s48 offen offset:1024 nt lds\n"
BL"v240,s[16:19],s48 offen offset:2048 nt lds\n"
BL"v241,s[16:19],s48 offen offset:3072 nt lds\n"
"v_max_f32 v188,0,v188\n"
"v_lshlrev_b32 v162,16,v163\n"
"v_and_b32 v163,0xffff0000,v163\n"
PK"v188,v188,v188\n"
PK"v165,v164,v165\n"
"v_lshlrev_b32 v164,16,v165\n"
"v_lshlrev_b32 v188,16,v188\n"
"v_and_b32 v165,0xffff0000,v165\n"
PK"v167,v166,v167\n"
"v_add_f32 v188,0x3727c5ac,v188\n"
"v_lshlrev_b32 v166,16,v167\n"
"v_and_b32 v167,0xffff0000,v167\n"
PK"v188,v188,v188\n"
"v_bfe_u32 v130,v224,5,4\n"
"v_mul_u32_u24 v130,144,v130\n"
"v_lshlrev_b32 v188,16,v188\n"
"v_bfe_u32 v131,v224,4,1\n"
"v_lshl_add_u32 v130,v131,3,v130\n"
"v_sqrt_f32 v189,v188\n"
"v_lshrrev_b32 v131,9,v224\n"
"v_lshl_add_u32 v130,v131,4,v130\n"
"v_rcp_f32 v190,v189\n"
"s_lshl_b32 s52,s30,5\n"
"v_add_u32 v130,s52,v130\n"
"v_fma_f32 v188,-v189,v189,v188\n"
MU"v190,0.5,v190\n"
"v_fma_f32 v189,v188,v190,v189\n"
PK"v189,v189,v189\n"
"v_lshlrev_b32 v189,16,v189\n"
"v_rcp_f32 v129,v189\n"
"s_nop 0\n"
MU"v148,v148,v129\n"
MU"v149,v149,v129\n"
PK"v138,v148,v149\n"
MU"v150,v150,v129\n"
MU"v151,v151,v129\n"
PK"v139,v150,v151\n"
"ds_write_b64 v130,v[138:139] offset:49152\n"
MU"v152,v152,v129\n"
MU"v153,v153,v129\n"
PK"v140,v152,v153\n"
MU"v154,v154,v129\n"
MU"v155,v155,v129\n"
PK"v141,v154,v155\n"
"ds_write_b64 v130,v[140:141] offset:51456\n"
MU"v156,v156,v129\n"
MU"v157,v157,v129\n"
PK"v142,v156,v157\n"
SA"m0,s36,16384\n"
"s_bfe_u32 s48,s46,0x30004\n"
"s_lshl_b32 s48,s48,7\n"
BL"v234,s[12:15],s48 offen offset:0 lds\n"
BL"v235,s[12:15],s48 offen offset:1024 lds\n"
BL"v236,s[12:15],s48 offen offset:2048 lds\n"
BL"v237,s[12:15],s48 offen offset:3072 lds\n"
MU"v158,v158,v129\n"
MU"v159,v159,v129\n"
PK"v143,v158,v159\n"
"ds_write_b64 v130,v[142:143] offset:53760\n"
MU"v160,v160,v129\n"
MU"v161,v161,v129\n"
PK"v144,v160,v161\n"
MU"v162,v162,v129\n"
MU"v163,v163,v129\n"
PK"v145,v162,v163\n"
"ds_write_b64 v130,v[144:145] offset:56064\n"
MU"v164,v164,v129\n"
MU"v165,v165,v129\n"
PK"v146,v164,v165\n"
MU"v166,v166,v129\n"
MU"v167,v167,v129\n"
PK"v147,v166,v167\n"
"ds_write_b64 v130,v[146:147] offset:58368\n"
"v_lshlrev_b32 v148,16,v138\n"
"v_and_b32 v149,0xffff0000,v138\n"
MU"v188,v148,v168\n"
MU"v189,v149,v169\n"
"v_lshlrev_b32 v150,16,v139\n"
"v_and_b32 v151,0xffff0000,v139\n"
"v_fmac_f32 v188,v150,v170\n"
"v_fmac_f32 v189,v151,v171\n"
"v_lshlrev_b32 v152,16,v140\n"
"v_and_b32 v153,0xffff0000,v140\n"
"v_fmac_f32 v188,v152,v172\n"
"v_fmac_f32 v189,v153,v173\n"
"v_lshlrev_b32 v154,16,v141\n"
"v_and_b32 v155,0xffff0000,v141\n"
"v_fmac_f32 v188,v154,v174\n"
"v_fmac_f32 v189,v155,v175\n"
"v_lshlrev_b32 v156,16,v142\n"
"v_and_b32 v157,0xffff0000,v142\n"
"v_fmac_f32 v188,v156,v176\n"
"v_fmac_f32 v189,v157,v177\n"
"v_lshlrev_b32 v158,16,v143\n"
"v_and_b32 v159,0xffff0000,v143\n"
"v_fmac_f32 v188,v158,v178\n"
"v_fmac_f32 v189,v159,v179\n"
"v_lshlrev_b32 v160,16,v144\n"
"v_and_b32 v161,0xffff0000,v144\n"
"v_fmac_f32 v188,v160,v180\n"
"v_fmac_f32 v189,v161,v181\n"
"v_lshlrev_b32 v162,16,v145\n"
"v_and_b32 v163,0xffff0000,v145\n"
WT"lgkmcnt(0)\n"
SA"s28,s28,0x7f4a7c15\n"
"s_addc_u32 s29,s29,0x9e3779b9\n"
"v_mov_b32 v232,s28\n"
"v_mov_b32 v233,s29\n"
WT"vmcnt(20)\n"
RT"a[0:1],v246 offset:0\n"
RT"a[2:3],v246 offset:512\n"
RT"a[4:5],v247 offset:0\n"
RT"a[6:7],v247 offset:512\n"
RT"a[8:9],v248 offset:0\n"
RT"a[10:11],v248 offset:512\n"
RT"a[12:13],v249 offset:0\n"
RT"a[14:15],v249 offset:512\n"
WT"vmcnt(16)\n"
RT"a[16:17],v246 offset:4096\n"
RT"a[18:19],v246 offset:4608\n"
RT"a[20:21],v247 offset:4096\n"
RT"a[22:23],v247 offset:4608\n"
RT"a[24:25],v248 offset:4096\n"
RT"a[26:27],v248 offset:4608\n"
RT"a[28:29],v249 offset:4096\n"
RT"a[30:31],v249 offset:4608\n"
WT"vmcnt(12) lgkmcnt(15)\n"
"s_barrier\n"
RD"v[192:195],v242 offset:0\n"
RD"v[196:199],v243 offset:0\n"
RD"v[200:203],v242 offset:2048\n"
RD"v[204:207],v243 offset:2048\n"
RD"v[208:211],v242 offset:4096\n"
"v_lshrrev_b32 v131,7,v224\n"
WT"lgkmcnt(4)\n"
MF"v[0:3],a[0:3],v[192:195],0\n"
WT"lgkmcnt(3)\n"
RD"v[212:215],v243 offset:4096\n"
"v_lshl_add_u32 v131,v131,4,v224\n"
MF"v[0:3],a[16:19],v[196:199],v[0:3]\n"
SA"m0,s37,16384\n"
"s_bfe_u32 s48,s46,0x30008\n"
"s_lshl_b32 s48,s48,19\n"
MF"v[4:7],a[20:23],v[196:199],0\n"
"s_mul_i32 s52,s30,1152\n"
BL"v238,s[16:19],s48 offen offset:0 nt lds\n"
MF"v[4:7],a[4:7],v[192:195],v[4:7]\n"
"v_add_u32 v128,s52,v131\n"
"s_and_b32 s52,s30,1\n"
MF"v[8:11],a[8:11],v[192:195],0\n"
"s_mul_i32 s51,s52,1152\n"
RD"v[216:219],v242 offset:6144\n"
"v_add_u32 v131,s51,v131\n"
MF"v[8:11],a[24:27],v[196:199],v[8:11]\n"
"s_lshl_b32 s52,s52,10\n"
"v_fmac_f32 v188,v162,v182\n"
"v_fmac_f32 v189,v163,v183\n"
MF"v[12:15],a[28:31],v[196:199],0\n"
BL"v239,s[16:19],s48 offen offset:1024 nt lds\n"
MF"v[12:15],a[12:15],v[192:195],v[12:15]\n"
WT"lgkmcnt(1)\n"
RD"v[220:223],v243 offset:6144\n"
RD"v[132:135],v128 offset:49152\n"
MF"v[16:19],a[0:3],v[200:203],0\n"
WT"vmcnt(10)\n"
RT"a[32:33],v246 offset:8192\n"
RD"a[240:243],v128 offset:53760\n"
MF"v[16:19],a[16:19],v[204:207],v[16:19]\n"
"s_lshr_b32 s50,s31,1\n"
RD"a[244:247],v131 offset:58368\n"
"v_lshlrev_b32 v164,16,v146\n"
MF"v[20:23],a[20:23],v[204:207],0\n"
"s_mul_i32 s50,s50,20480\n"
"v_and_b32 v129,0x70,v224\n"
"v_and_b32 v165,0xffff0000,v146\n"
MF"v[20:23],a[4:7],v[200:203],v[20:23]\n"
"s_and_b32 s51,s31,1\n"
RD"v[192:195],v242 offset:8192\n"
RT"a[34:35],v246 offset:8704\n"
MF"v[24:27],a[8:11],v[200:203],0\n"
"s_lshl_b32 s51,s51,7\n"
BL"v240,s[16:19],s48 offen offset:2048 nt lds\n"
MF"v[24:27],a[24:27],v[204:207],v[24:27]\n"
SA"s50,s50,s51\n"
"v_and_b32 v130,0x380,v224\n"
"v_fmac_f32 v188,v164,v184\n"
MF"v[28:31],a[28:31],v[204:207],0\n"
SA"s50,s50,65536\n"
RT"a[36:37],v247 offset:8192\n"
"v_lshl_add_u32 v129,v130,1,v129\n"
MF"v[28:31],a[12:15],v[200:203],v[28:31]\n"
"s_lshl_b32 s52,s52,1\n"
RD"v[196:199],v243 offset:8192\n"
"v_fmac_f32 v189,v165,v185\n"
MF"v[32:35],a[0:3],v[208:211],0\n"
SA"s52,s52,s50\n"
BL"v241,s[16:19],s48 offen offset:3072 nt lds\n"
MF"v[32:35],a[16:19],v[212:215],v[32:35]\n"
SA"s52,s52,16384\n"
RT"a[38:39],v247 offset:8704\n"
"v_lshlrev_b32 v166,16,v147\n"
MF"v[36:39],a[20:23],v[212:215],0\n"
SA"m0,s37,20480\n"
RD"v[200:203],v242 offset:10240\n"
"v_and_b32 v167,0xffff0000,v147\n"
MF"v[36:39],a[4:7],v[208:211],v[36:39]\n"
"s_bfe_u32 s48,s46,0x30008\n"
"v_fmac_f32 v188,v166,v186\n"
"v_fmac_f32 v189,v167,v187\n"
MF"v[40:43],a[8:11],v[208:211],0\n"
"s_lshl_b32 s48,s48,19\n"
RT"a[40:41],v248 offset:8192\n"
"v_add_f32 v188,v188,v189\n"
MF"v[40:43],a[24:27],v[212:215],v[40:43]\n"
"s_bitset1_b32 s48,18\n"
"v_add_f32_dpp v188,v188,v188 quad_perm:[1,0,3,2] row_mask:0xf bank_mask:0xf\n"
"s_lshl_b32 s51,s30,11\n"
MF"v[44:47],a[28:31],v[212:215],0\n"
SA"s50,s50,s51\n"
BL"v238,s[16:19],s48 offen offset:0 nt lds\n"
MF"v[44:47],a[12:15],v[208:211],v[44:47]\n"
WT"lgkmcnt(3)\n"
RD"v[204:207],v243 offset:10240\n"
RT"a[42:43],v248 offset:8704\n"
MF"v[48:51],a[0:3],v[216:219],0\n"
SA"s51,s50,8192\n"
BS"v[132:135],v129,s[8:11],s50 offen sc1\n"
MF"v[48:51],a[16:19],v[220:223],v[48:51]\n"
"s_lshl_b32 s90,s31,2\n"
BL"v239,s[16:19],s48 offen offset:1024 nt lds\n"
MF"v[52:55],a[20:23],v[220:223],0\n"
SA"s90,s90,s30\n"
BS"a[240:243],v129,s[8:11],s51 offen sc1\n"
MF"v[52:55],a[4:7],v[216:219],v[52:55]\n"
"s_lshl_b32 s90,s90,1\n"
RD"v[208:211],v242 offset:12288\n"
RT"a[44:45],v249 offset:8192\n"
MF"v[56:59],a[8:11],v[216:219],0\n"
"s_sub_u32 s91,2047,s90\n"
BS"a[244:247],v129,s[8:11],s52 offen sc1\n"
MF"v[56:59],a[24:27],v[220:223],v[56:59]\n"
RD"v[212:215],v243 offset:12288\n"
RT"a[46:47],v249 offset:8704\n"
MF"v[60:63],a[28:31],v[220:223],0\n"
BL"v240,s[16:19],s48 offen offset:2048 nt lds\n"
MF"v[60:63],a[12:15],v[216:219],v[60:63]\n"
"v_add_f32_dpp v188,v188,v188 quad_perm:[2,3,0,1] row_mask:0xf bank_mask:0xf\n"
MF"v[64:67],a[0:3],v[192:195],0\n"
RD"v[216:219],v242 offset:14336\n"
"v_add_f32_dpp v188,v188,v188 row_half_mirror row_mask:0xf bank_mask:0xf\n"
MF"v[64:67],a[16:19],v[196:199],v[64:67]\n"
WT"vmcnt(14)\n"
RT"a[48:49],v246 offset:12288\n"
"v_add_f32_dpp v188,v188,v188 row_mirror row_mask:0xf bank_mask:0xf\n"
MF"v[68:71],a[20:23],v[196:199],0\n"
"s_lshl_b32 s90,s90,2\n"
BL"v241,s[16:19],s48 offen offset:3072 nt lds\n"
MF"v[68:71],a[4:7],v[192:195],v[68:71]\n"
SA"s90,s90,2686976\n"
RT"a[50:51],v246 offset:12800\n"
"v_mov_b32 v189,v188\n"
MF"v[72:75],a[8:11],v[192:195],0\n"
SA"m0,s36,32768\n"
RD"v[220:223],v243 offset:14336\n"
"v_permlane16_swap_b32 v189,v188\n"
MF"v[72:75],a[24:27],v[196:199],v[72:75]\n"
"s_bfe_u32 s48,s46,0x30008\n"
"v_add_f32 v188,v188,v189\n"
PK"v188,v188,v188\n"
MF"v[76:79],a[28:31],v[196:199],0\n"
"s_lshl_b32 s48,s48,7\n"
RT"a[52:53],v247 offset:12288\n"
"v_lshlrev_b32 v188,16,v188\n"
MF"v[76:79],a[12:15],v[192:195],v[76:79]\n"
WT"lgkmcnt(4)\n"
BL"v234,s[12:15],s48 offen offset:0 lds\n"
MF"v[80:83],a[0:3],v[200:203],0\n"
"v_ashrrev_i32 v189,31,v188\n"
"v_or_b32 v189,0x80000000,v189\n"
MF"v[80:83],a[16:19],v[204:207],v[80:83]\n"
RT"a[54:55],v247 offset:12800\n"
"v_xor_b32 v188,v188,v189\n"
MF"v[84:87],a[20:23],v[204:207],0\n"
BL"v235,s[12:15],s48 offen offset:1024 lds\n"
MF"v[84:87],a[4:7],v[200:203],v[84:87]\n"
"v_and_b32 v188,0xffff0000,v188\n"
"v_lshrrev_b32 v189,9,v224\n"
MF"v[88:91],a[8:11],v[200:203],0\n"
RT"a[56:57],v248 offset:12288\n"
"v_sub_u32 v190,s91,v189\n"
MF"v[88:91],a[24:27],v[204:207],v[88:91]\n"
WT"vmcnt(13) lgkmcnt(3)\n"
"s_barrier\n"
MF"v[92:95],a[28:31],v[204:207],0\n"
RD"v[192:195],v242 offset:16384\n"
"v_or_b32 v188,v188,v190\n"
MF"v[92:95],a[12:15],v[200:203],v[92:95]\n"
RT"a[58:59],v248 offset:12800\n"
"v_and_b32 v190,0x1f0,v224\n"
MF"v[96:99],a[0:3],v[208:211],0\n"
BL"v236,s[12:15],s48 offen offset:2048 lds\n"
MF"v[96:99],a[16:19],v[212:215],v[96:99]\n"
"v_lshlrev_b32 v190,23,v190\n"
"v_lshl_add_u32 v190,v189,2,v190\n"
MF"v[100:103],a[20:23],v[212:215],0\n"
"buffer_store_dword v188,v190,s[8:11],s90 offen sc1\n"
MF"v[100:103],a[4:7],v[208:211],v[100:103]\n"
RD"v[196:199],v243 offset:16384\n"
RT"a[60:61],v249 offset:12288\n"
MF"v[104:107],a[8:11],v[208:211],0\n"
BL"v237,s[12:15],s48 offen offset:3072 lds\n"
MF"v[104:107],a[24:27],v[212:215],v[104:107]\n"
RT"a[62:63],v249 offset:12800\n"
MF"v[108:111],a[28:31],v[212:215],0\n"
SA"m0,s37,0\n"
RD"v[200:203],v242 offset:18432\n"
"s_bfe_u32 s48,s46,0x3000c\n"
MF"v[108:111],a[12:15],v[208:211],v[108:111]\n"
"s_lshl_b32 s48,s48,19\n"
MF"v[112:115],a[0:3],v[216:219],0\n"
BL"v238,s[16:19],s48 offen offset:0 lds\n"
MF"v[112:115],a[16:19],v[220:223],v[112:115]\n"
RD"v[204:207],v243 offset:18432\n"
MF"v[116:119],a[20:23],v[220:223],0\n"
MF"v[116:119],a[4:7],v[216:219],v[116:119]\n"
MF"v[120:123],a[8:11],v[216:219],0\n"
BL"v239,s[16:19],s48 offen offset:1024 lds\n"
MF"v[120:123],a[24:27],v[220:223],v[120:123]\n"
RD"v[208:211],v242 offset:20480\n"
MF"v[124:127],a[28:31],v[220:223],0\n"
MF"v[124:127],a[12:15],v[216:219],v[124:127]\n"
WT"lgkmcnt(2)\n"
MF"v[0:3],a[32:35],v[192:195],v[0:3]\n"
RD"v[212:215],v243 offset:20480\n"
MF"v[0:3],a[48:51],v[196:199],v[0:3]\n"
BL"v240,s[16:19],s48 offen offset:2048 lds\n"
MF"v[4:7],a[52:55],v[196:199],v[4:7]\n"
MF"v[4:7],a[36:39],v[192:195],v[4:7]\n"
MF"v[8:11],a[40:43],v[192:195],v[8:11]\n"
RD"v[216:219],v242 offset:22528\n"
MF"v[8:11],a[56:59],v[196:199],v[8:11]\n"
BL"v241,s[16:19],s48 offen offset:3072 lds\n"
MF"v[12:15],a[60:63],v[196:199],v[12:15]\n"
MF"v[12:15],a[44:47],v[192:195],v[12:15]\n"
RD"v[220:223],v243 offset:22528\n"
MF"v[16:19],a[32:35],v[200:203],v[16:19]\n"
WT"lgkmcnt(2)\n"
WT"vmcnt(16)\n"
RT"a[64:65],v246 offset:16384\n"
MF"v[16:19],a[48:51],v[204:207],v[16:19]\n"
SA"m0,s37,4096\n"
"s_bfe_u32 s48,s46,0x3000c\n"
"s_lshl_b32 s48,s48,19\n"
MF"v[20:23],a[52:55],v[204:207],v[20:23]\n"
"s_bitset1_b32 s48,18\n"
BL"v238,s[16:19],s48 offen offset:0 lds\n"
MF"v[20:23],a[36:39],v[200:203],v[20:23]\n"
RD"v[192:195],v242 offset:24576\n"
RT"a[66:67],v246 offset:16896\n"
MF"v[24:27],a[40:43],v[200:203],v[24:27]\n"
MF"v[24:27],a[56:59],v[204:207],v[24:27]\n"
MF"v[28:31],a[60:63],v[204:207],v[28:31]\n"
RT"a[68:69],v247 offset:16384\n"
MF"v[28:31],a[44:47],v[200:203],v[28:31]\n"
BL"v239,s[16:19],s48 offen offset:1024 lds\n"
MF"v[32:35],a[32:35],v[208:211],v[32:35]\n"
RD"v[196:199],v243 offset:24576\n"
MF"v[32:35],a[48:51],v[212:215],v[32:35]\n"
RT"a[70:71],v247 offset:16896\n"
MF"v[36:39],a[52:55],v[212:215],v[36:39]\n"
RD"v[200:203],v242 offset:26624\n"
MF"v[36:39],a[36:39],v[208:211],v[36:39]\n"
BL"v240,s[16:19],s48 offen offset:2048 lds\n"
MF"v[40:43],a[40:43],v[208:211],v[40:43]\n"
RT"a[72:73],v248 offset:16384\n"
MF"v[40:43],a[56:59],v[212:215],v[40:43]\n"
MF"v[44:47],a[60:63],v[212:215],v[44:47]\n"
RD"v[204:207],v243 offset:26624\n"
MF"v[44:47],a[44:47],v[208:211],v[44:47]\n"
WT"lgkmcnt(4)\n"
RT"a[74:75],v248 offset:16896\n"
MF"v[48:51],a[32:35],v[216:219],v[48:51]\n"
BL"v241,s[16:19],s48 offen offset:3072 lds\n"
MF"v[48:51],a[48:51],v[220:223],v[48:51]\n"
RD"v[208:211],v242 offset:28672\n"
MF"v[52:55],a[52:55],v[220:223],v[52:55]\n"
SA"m0,s36,0\n"
RT"a[76:77],v249 offset:16384\n"
"s_bfe_u32 s48,s46,0x3000c\n"
MF"v[52:55],a[36:39],v[216:219],v[52:55]\n"
"s_lshl_b32 s48,s48,7\n"
MF"v[56:59],a[40:43],v[216:219],v[56:59]\n"
BL"v234,s[12:15],s48 offen offset:0 lds\n"
MF"v[56:59],a[56:59],v[220:223],v[56:59]\n"
RD"v[212:215],v243 offset:28672\n"
RT"a[78:79],v249 offset:16896\n"
MF"v[60:63],a[60:63],v[220:223],v[60:63]\n"
MF"v[60:63],a[44:47],v[216:219],v[60:63]\n"
MF"v[64:67],a[32:35],v[192:195],v[64:67]\n"
RD"v[216:219],v242 offset:30720\n"
MF"v[64:67],a[48:51],v[196:199],v[64:67]\n"
WT"vmcnt(14)\n"
RT"a[80:81],v246 offset:20480\n"
MF"v[68:71],a[52:55],v[196:199],v[68:71]\n"
BL"v235,s[12:15],s48 offen offset:1024 lds\n"
MF"v[68:71],a[36:39],v[192:195],v[68:71]\n"
RT"a[82:83],v246 offset:20992\n"
MF"v[72:75],a[40:43],v[192:195],v[72:75]\n"
RD"v[220:223],v243 offset:30720\n"
MF"v[72:75],a[56:59],v[196:199],v[72:75]\n"
BL"v236,s[12:15],s48 offen offset:2048 lds\n"
MF"v[76:79],a[60:63],v[196:199],v[76:79]\n"
RT"a[84:85],v247 offset:20480\n"
MF"v[76:79],a[44:47],v[192:195],v[76:79]\n"
WT"lgkmcnt(4)\n"
MF"v[80:83],a[32:35],v[200:203],v[80:83]\n"
"s_lshl_b32 s51,s32,8\n"
"s_andn2_b32 s50,s31,7\n"
SA"s50,s50,s51\n"
MF"v[80:83],a[48:51],v[204:207],v[80:83]\n"
SA"s50,s50,256\n"
RT"a[86:87],v247 offset:20992\n"
SA"s50,s50,s45\n"
MF"v[84:87],a[52:55],v[204:207],v[84:87]\n"
BL"v237,s[12:15],s48 offen offset:3072 lds\n"
MF"v[84:87],a[36:39],v[200:203],v[84:87]\n"
MF"v[88:91],a[40:43],v[200:203],v[88:91]\n"
SA"m0,s37,8192\n"
RT"a[88:89],v248 offset:20480\n"
"s_bfe_u32 s48,s46,0x30010\n"
MF"v[88:91],a[56:59],v[204:207],v[88:91]\n"
"s_lshl_b32 s48,s48,19\n"
WT"vmcnt(12) lgkmcnt(3)\n"
"s_barrier\n"
MF"v[92:95],a[60:63],v[204:207],v[92:95]\n"
RD"v[192:195],v242 offset:32768\n"
MF"v[92:95],a[44:47],v[200:203],v[92:95]\n"
BL"v238,s[16:19],s48 offen offset:0 lds\n"
MF"v[96:99],a[32:35],v[208:211],v[96:99]\n"
"buffer_store_dwordx2 v[232:233],v230,s[8:11],s50 offen sc1\n"
MF"v[96:99],a[48:51],v[212:215],v[96:99]\n"
RT"a[90:91],v248 offset:20992\n"
MF"v[100:103],a[52:55],v[212:215],v[100:103]\n"
RD"v[196:199],v243 offset:32768\n"
RT"a[92:93],v249 offset:20480\n"
MF"v[100:103],a[36:39],v[208:211],v[100:103]\n"
BL"v239,s[16:19],s48 offen offset:1024 lds\n"
MF"v[104:107],a[40:43],v[208:211],v[104:107]\n"
MF"v[104:107],a[56:59],v[212:215],v[104:107]\n"
RT"a[94:95],v249 offset:20992\n"
MF"v[108:111],a[60:63],v[212:215],v[108:111]\n"
RD"v[200:203],v242 offset:34816\n"
MF"v[108:111],a[44:47],v[208:211],v[108:111]\n"
BL"v240,s[16:19],s48 offen offset:2048 lds\n"
MF"v[112:115],a[32:35],v[216:219],v[112:115]\n"
MF"v[112:115],a[48:51],v[220:223],v[112:115]\n"
RD"v[204:207],v243 offset:34816\n"
MF"v[116:119],a[52:55],v[220:223],v[116:119]\n"
MF"v[116:119],a[36:39],v[216:219],v[116:119]\n"
BL"v241,s[16:19],s48 offen offset:3072 lds\n"
MF"v[120:123],a[40:43],v[216:219],v[120:123]\n"
MF"v[120:123],a[56:59],v[220:223],v[120:123]\n"
RD"v[208:211],v242 offset:36864\n"
MF"v[124:127],a[60:63],v[220:223],v[124:127]\n"
SA"m0,s37,12288\n"
"s_bfe_u32 s48,s46,0x30010\n"
"s_lshl_b32 s48,s48,19\n"
MF"v[124:127],a[44:47],v[216:219],v[124:127]\n"
WT"lgkmcnt(2)\n"
"s_bitset1_b32 s48,18\n"
MF"v[0:3],a[64:67],v[192:195],v[0:3]\n"
RD"v[212:215],v243 offset:36864\n"
MF"v[0:3],a[80:83],v[196:199],v[0:3]\n"
BL"v238,s[16:19],s48 offen offset:0 lds\n"
MF"v[4:7],a[84:87],v[196:199],v[4:7]\n"
MF"v[4:7],a[68:71],v[192:195],v[4:7]\n"
MF"v[8:11],a[72:75],v[192:195],v[8:11]\n"
RD"v[216:219],v242 offset:38912\n"
MF"v[8:11],a[88:91],v[196:199],v[8:11]\n"
BL"v239,s[16:19],s48 offen offset:1024 lds\n"
MF"v[12:15],a[92:95],v[196:199],v[12:15]\n"
MF"v[12:15],a[76:79],v[192:195],v[12:15]\n"
RD"v[220:223],v243 offset:38912\n"
MF"v[16:19],a[64:67],v[200:203],v[16:19]\n"
WT"lgkmcnt(2)\n"
WT"vmcnt(15)\n"
RT"a[96:97],v246 offset:0\n"
MF"v[16:19],a[80:83],v[204:207],v[16:19]\n"
BL"v240,s[16:19],s48 offen offset:2048 lds\n"
MF"v[20:23],a[84:87],v[204:207],v[20:23]\n"
MF"v[20:23],a[68:71],v[200:203],v[20:23]\n"
RD"v[192:195],v242 offset:40960\n"
MF"v[24:27],a[72:75],v[200:203],v[24:27]\n"
RT"a[98:99],v246 offset:512\n"
MF"v[24:27],a[88:91],v[204:207],v[24:27]\n"
BL"v241,s[16:19],s48 offen offset:3072 lds\n"
MF"v[28:31],a[92:95],v[204:207],v[28:31]\n"
RT"a[100:101],v247 offset:0\n"
MF"v[28:31],a[76:79],v[200:203],v[28:31]\n"
RD"v[196:199],v243 offset:40960\n"
MF"v[32:35],a[64:67],v[208:211],v[32:35]\n"
SA"m0,s36,16384\n"
"s_bfe_u32 s48,s46,0x30010\n"
"s_lshl_b32 s48,s48,7\n"
MF"v[32:35],a[80:83],v[212:215],v[32:35]\n"
RT"a[102:103],v247 offset:512\n"
MF"v[36:39],a[84:87],v[212:215],v[36:39]\n"
RD"v[200:203],v242 offset:43008\n"
MF"v[36:39],a[68:71],v[208:211],v[36:39]\n"
BL"v234,s[12:15],s48 offen offset:0 lds\n"
MF"v[40:43],a[72:75],v[208:211],v[40:43]\n"
RT"a[104:105],v248 offset:0\n"
MF"v[40:43],a[88:91],v[212:215],v[40:43]\n"
MF"v[44:47],a[92:95],v[212:215],v[44:47]\n"
RD"v[204:207],v243 offset:43008\n"
MF"v[44:47],a[76:79],v[208:211],v[44:47]\n"
WT"lgkmcnt(4)\n"
BL"v235,s[12:15],s48 offen offset:1024 lds\n"
MF"v[48:51],a[64:67],v[216:219],v[48:51]\n"
RT"a[106:107],v248 offset:512\n"
MF"v[48:51],a[80:83],v[220:223],v[48:51]\n"
RD"v[208:211],v242 offset:45056\n"
MF"v[52:55],a[84:87],v[220:223],v[52:55]\n"
RT"a[108:109],v249 offset:0\n"
MF"v[52:55],a[68:71],v[216:219],v[52:55]\n"
BL"v236,s[12:15],s48 offen offset:2048 lds\n"
MF"v[56:59],a[72:75],v[216:219],v[56:59]\n"
MF"v[56:59],a[88:91],v[220:223],v[56:59]\n"
RD"v[212:215],v243 offset:45056\n"
MF"v[60:63],a[92:95],v[220:223],v[60:63]\n"
RT"a[110:111],v249 offset:512\n"
MF"v[60:63],a[76:79],v[216:219],v[60:63]\n"
BL"v237,s[12:15],s48 offen offset:3072 lds\n"
MF"v[64:67],a[64:67],v[192:195],v[64:67]\n"
RD"v[216:219],v242 offset:47104\n"
MF"v[64:67],a[80:83],v[196:199],v[64:67]\n"
WT"vmcnt(17)\n"
RT"a[112:113],v246 offset:4096\n"
MF"v[68:71],a[84:87],v[196:199],v[68:71]\n"
SA"m0,s37,16384\n"
"s_bfe_u32 s48,s46,0x30014\n"
"s_lshl_b32 s48,s48,19\n"
MF"v[68:71],a[68:71],v[192:195],v[68:71]\n"
RT"a[114:115],v246 offset:4608\n"
MF"v[72:75],a[72:75],v[192:195],v[72:75]\n"
RD"v[220:223],v243 offset:47104\n"
MF"v[72:75],a[88:91],v[196:199],v[72:75]\n"
BL"v238,s[16:19],s48 offen offset:0 lds\n"
MF"v[76:79],a[92:95],v[196:199],v[76:79]\n"
RT"a[116:117],v247 offset:4096\n"
MF"v[76:79],a[76:79],v[192:195],v[76:79]\n"
WT"lgkmcnt(4)\n"
MF"v[80:83],a[64:67],v[200:203],v[80:83]\n"
BL"v239,s[16:19],s48 offen offset:1024 lds\n"
MF"v[80:83],a[80:83],v[204:207],v[80:83]\n"
RT"a[118:119],v247 offset:4608\n"
MF"v[84:87],a[84:87],v[204:207],v[84:87]\n"
MF"v[84:87],a[68:71],v[200:203],v[84:87]\n"
MF"v[88:91],a[72:75],v[200:203],v[88:91]\n"
RT"a[120:121],v248 offset:4096\n"
MF"v[88:91],a[88:91],v[204:207],v[88:91]\n"
WT"vmcnt(15) lgkmcnt(3)\n"
"s_barrier\n"
MF"v[92:95],a[92:95],v[204:207],v[92:95]\n"
BL"v240,s[16:19],s48 offen offset:2048 lds\n"
MF"v[92:95],a[76:79],v[200:203],v[92:95]\n"
RD"v[192:195],v242 offset:0\n"
RT"a[122:123],v248 offset:4608\n"
MF"v[96:99],a[64:67],v[208:211],v[96:99]\n"
MF"v[96:99],a[80:83],v[212:215],v[96:99]\n"
BL"v241,s[16:19],s48 offen offset:3072 lds\n"
MF"v[100:103],a[84:87],v[212:215],v[100:103]\n"
RD"v[196:199],v243 offset:0\n"
RT"a[124:125],v249 offset:4096\n"
MF"v[100:103],a[68:71],v[208:211],v[100:103]\n"
MF"v[104:107],a[72:75],v[208:211],v[104:107]\n"
SA"m0,s37,20480\n"
"s_bfe_u32 s48,s46,0x30014\n"
"s_lshl_b32 s48,s48,19\n"
MF"v[104:107],a[88:91],v[212:215],v[104:107]\n"
"s_bitset1_b32 s48,18\n"
RT"a[126:127],v249 offset:4608\n"
MF"v[108:111],a[92:95],v[212:215],v[108:111]\n"
RD"v[200:203],v242 offset:2048\n"
MF"v[108:111],a[76:79],v[208:211],v[108:111]\n"
BL"v238,s[16:19],s48 offen offset:0 lds\n"
MF"v[112:115],a[64:67],v[216:219],v[112:115]\n"
MF"v[112:115],a[80:83],v[220:223],v[112:115]\n"
RD"v[204:207],v243 offset:2048\n"
MF"v[116:119],a[84:87],v[220:223],v[116:119]\n"
BL"v239,s[16:19],s48 offen offset:1024 lds\n"
MF"v[116:119],a[68:71],v[216:219],v[116:119]\n"
MF"v[120:123],a[72:75],v[216:219],v[120:123]\n"
MF"v[120:123],a[88:91],v[220:223],v[120:123]\n"
RD"v[208:211],v242 offset:4096\n"
MF"v[124:127],a[92:95],v[220:223],v[124:127]\n"
MF"v[124:127],a[76:79],v[216:219],v[124:127]\n"
WT"lgkmcnt(2)\n"
BL"v240,s[16:19],s48 offen offset:2048 lds\n"
MF"v[0:3],a[96:99],v[192:195],v[0:3]\n"
RD"v[212:215],v243 offset:4096\n"
MF"v[0:3],a[112:115],v[196:199],v[0:3]\n"
MF"v[4:7],a[116:119],v[196:199],v[4:7]\n"
MF"v[4:7],a[100:103],v[192:195],v[4:7]\n"
BL"v241,s[16:19],s48 offen offset:3072 lds\n"
MF"v[8:11],a[104:107],v[192:195],v[8:11]\n"
RD"v[216:219],v242 offset:6144\n"
MF"v[8:11],a[120:123],v[196:199],v[8:11]\n"
MF"v[12:15],a[124:127],v[196:199],v[12:15]\n"
SA"m0,s36,32768\n"
"s_bfe_u32 s48,s46,0x30014\n"
"s_lshl_b32 s48,s48,7\n"
MF"v[12:15],a[108:111],v[192:195],v[12:15]\n"
RD"v[220:223],v243 offset:6144\n"
MF"v[16:19],a[96:99],v[200:203],v[16:19]\n"
WT"lgkmcnt(2)\n"
WT"vmcnt(16)\n"
RT"a[128:129],v246 offset:8192\n"
MF"v[16:19],a[112:115],v[204:207],v[16:19]\n"
BL"v234,s[12:15],s48 offen offset:0 lds\n"
MF"v[20:23],a[116:119],v[204:207],v[20:23]\n"
MF"v[20:23],a[100:103],v[200:203],v[20:23]\n"
RD"v[192:195],v242 offset:8192\n"
MF"v[24:27],a[104:107],v[200:203],v[24:27]\n"
RT"a[130:131],v246 offset:8704\n"
MF"v[24:27],a[120:123],v[204:207],v[24:27]\n"
BL"v235,s[12:15],s48 offen offset:1024 lds\n"
MF"v[28:31],a[124:127],v[204:207],v[28:31]\n"
RT"a[132:133],v247 offset:8192\n"
MF"v[28:31],a[108:111],v[200:203],v[28:31]\n"
RD"v[196:199],v243 offset:8192\n"
MF"v[32:35],a[96:99],v[208:211],v[32:35]\n"
MF"v[32:35],a[112:115],v[212:215],v[32:35]\n"
RT"a[134:135],v247 offset:8704\n"
MF"v[36:39],a[116:119],v[212:215],v[36:39]\n"
BL"v236,s[12:15],s48 offen offset:2048 lds\n"
MF"v[36:39],a[100:103],v[208:211],v[36:39]\n"
RD"v[200:203],v242 offset:10240\n"
MF"v[40:43],a[104:107],v[208:211],v[40:43]\n"
RT"a[136:137],v248 offset:8192\n"
MF"v[40:43],a[120:123],v[212:215],v[40:43]\n"
BL"v237,s[12:15],s48 offen offset:3072 lds\n"
MF"v[44:47],a[124:127],v[212:215],v[44:47]\n"
RD"v[204:207],v243 offset:10240\n"
MF"v[44:47],a[108:111],v[208:211],v[44:47]\n"
WT"lgkmcnt(4)\n"
RT"a[138:139],v248 offset:8704\n"
MF"v[48:51],a[96:99],v[216:219],v[48:51]\n"
SA"m0,s37,0\n"
"s_bfe_u32 s48,s46,0x30018\n"
"s_lshl_b32 s48,s48,19\n"
MF"v[48:51],a[112:115],v[220:223],v[48:51]\n"
RD"v[208:211],v242 offset:12288\n"
MF"v[52:55],a[116:119],v[220:223],v[52:55]\n"
RT"a[140:141],v249 offset:8192\n"
MF"v[52:55],a[100:103],v[216:219],v[52:55]\n"
BL"v238,s[16:19],s48 offen offset:0 lds\n"
MF"v[56:59],a[104:107],v[216:219],v[56:59]\n"
MF"v[56:59],a[120:123],v[220:223],v[56:59]\n"
RD"v[212:215],v243 offset:12288\n"
MF"v[60:63],a[124:127],v[220:223],v[60:63]\n"
RT"a[142:143],v249 offset:8704\n"
MF"v[60:63],a[108:111],v[216:219],v[60:63]\n"
BL"v239,s[16:19],s48 offen offset:1024 lds\n"
MF"v[64:67],a[96:99],v[192:195],v[64:67]\n"
RD"v[216:219],v242 offset:14336\n"
MF"v[64:67],a[112:115],v[196:199],v[64:67]\n"
WT"vmcnt(18)\n"
RT"a[144:145],v246 offset:12288\n"
MF"v[68:71],a[116:119],v[196:199],v[68:71]\n"
MF"v[68:71],a[100:103],v[192:195],v[68:71]\n"
RT"a[146:147],v246 offset:12800\n"
MF"v[72:75],a[104:107],v[192:195],v[72:75]\n"
BL"v240,s[16:19],s48 offen offset:2048 lds\n"
MF"v[72:75],a[120:123],v[196:199],v[72:75]\n"
RD"v[220:223],v243 offset:14336\n"
MF"v[76:79],a[124:127],v[196:199],v[76:79]\n"
RT"a[148:149],v247 offset:12288\n"
MF"v[76:79],a[108:111],v[192:195],v[76:79]\n"
WT"lgkmcnt(4)\n"
BL"v241,s[16:19],s48 offen offset:3072 lds\n"
MF"v[80:83],a[96:99],v[200:203],v[80:83]\n"
MF"v[80:83],a[112:115],v[204:207],v[80:83]\n"
RT"a[150:151],v247 offset:12800\n"
MF"v[84:87],a[116:119],v[204:207],v[84:87]\n"
SA"m0,s37,4096\n"
"s_bfe_u32 s48,s46,0x30018\n"
"s_lshl_b32 s48,s48,19\n"
MF"v[84:87],a[100:103],v[200:203],v[84:87]\n"
"s_bitset1_b32 s48,18\n"
MF"v[88:91],a[104:107],v[200:203],v[88:91]\n"
RT"a[152:153],v248 offset:12288\n"
MF"v[88:91],a[120:123],v[204:207],v[88:91]\n"
BL"v238,s[16:19],s48 offen offset:0 lds\n"
MF"v[92:95],a[124:127],v[204:207],v[92:95]\n"
WT"vmcnt(17) lgkmcnt(3)\n"
"s_barrier\n"
MF"v[92:95],a[108:111],v[200:203],v[92:95]\n"
RD"v[192:195],v242 offset:16384\n"
RT"a[154:155],v248 offset:12800\n"
MF"v[96:99],a[96:99],v[208:211],v[96:99]\n"
BL"v239,s[16:19],s48 offen offset:1024 lds\n"
MF"v[96:99],a[112:115],v[212:215],v[96:99]\n"
MF"v[100:103],a[116:119],v[212:215],v[100:103]\n"
RD"v[196:199],v243 offset:16384\n"
MF"v[100:103],a[100:103],v[208:211],v[100:103]\n"
RT"a[156:157],v249 offset:12288\n"
MF"v[104:107],a[104:107],v[208:211],v[104:107]\n"
MF"v[104:107],a[120:123],v[212:215],v[104:107]\n"
RT"a[158:159],v249 offset:12800\n"
MF"v[108:111],a[124:127],v[212:215],v[108:111]\n"
BL"v240,s[16:19],s48 offen offset:2048 lds\n"
MF"v[108:111],a[108:111],v[208:211],v[108:111]\n"
RD"v[200:203],v242 offset:18432\n"
MF"v[112:115],a[96:99],v[216:219],v[112:115]\n"
MF"v[112:115],a[112:115],v[220:223],v[112:115]\n"
RD"v[204:207],v243 offset:18432\n"
MF"v[116:119],a[116:119],v[220:223],v[116:119]\n"
BL"v241,s[16:19],s48 offen offset:3072 lds\n"
MF"v[116:119],a[100:103],v[216:219],v[116:119]\n"
MF"v[120:123],a[104:107],v[216:219],v[120:123]\n"
SA"m0,s36,0\n"
"s_bfe_u32 s48,s46,0x30018\n"
"s_lshl_b32 s48,s48,7\n"
MF"v[120:123],a[120:123],v[220:223],v[120:123]\n"
RD"v[208:211],v242 offset:20480\n"
MF"v[124:127],a[124:127],v[220:223],v[124:127]\n"
BL"v234,s[12:15],s48 offen offset:0 lds\n"
MF"v[124:127],a[108:111],v[216:219],v[124:127]\n"
WT"lgkmcnt(2)\n"
MF"v[0:3],a[128:131],v[192:195],v[0:3]\n"
RD"v[212:215],v243 offset:20480\n"
MF"v[0:3],a[144:147],v[196:199],v[0:3]\n"
MF"v[4:7],a[148:151],v[196:199],v[4:7]\n"
BL"v235,s[12:15],s48 offen offset:1024 lds\n"
MF"v[4:7],a[132:135],v[192:195],v[4:7]\n"
MF"v[8:11],a[136:139],v[192:195],v[8:11]\n"
RD"v[216:219],v242 offset:22528\n"
MF"v[8:11],a[152:155],v[196:199],v[8:11]\n"
MF"v[12:15],a[156:159],v[196:199],v[12:15]\n"
MF"v[12:15],a[140:143],v[192:195],v[12:15]\n"
RD"v[220:223],v243 offset:22528\n"
MF"v[16:19],a[128:131],v[200:203],v[16:19]\n"
WT"lgkmcnt(2)\n"
BL"v236,s[12:15],s48 offen offset:2048 lds\n"
MF"v[16:19],a[144:147],v[204:207],v[16:19]\n"
WT"vmcnt(19)\n"
RT"a[160:161],v246 offset:16384\n"
MF"v[20:23],a[148:151],v[204:207],v[20:23]\n"
MF"v[20:23],a[132:135],v[200:203],v[20:23]\n"
RD"v[192:195],v242 offset:24576\n"
MF"v[24:27],a[136:139],v[200:203],v[24:27]\n"
RT"a[162:163],v246 offset:16896\n"
MF"v[24:27],a[152:155],v[204:207],v[24:27]\n"
BL"v237,s[12:15],s48 offen offset:3072 lds\n"
MF"v[28:31],a[156:159],v[204:207],v[28:31]\n"
SA"m0,s37,8192\n"
RT"a[164:165],v247 offset:16384\n"
"s_bfe_u32 s48,s46,0x3001c\n"
MF"v[28:31],a[140:143],v[200:203],v[28:31]\n"
"s_lshl_b32 s48,s48,19\n"
RD"v[196:199],v243 offset:24576\n"
MF"v[32:35],a[128:131],v[208:211],v[32:35]\n"
BL"v238,s[16:19],s48 offen offset:0 lds\n"
MF"v[32:35],a[144:147],v[212:215],v[32:35]\n"
RT"a[166:167],v247 offset:16896\n"
MF"v[36:39],a[148:151],v[212:215],v[36:39]\n"
RD"v[200:203],v242 offset:26624\n"
MF"v[36:39],a[132:135],v[208:211],v[36:39]\n"
MF"v[40:43],a[136:139],v[208:211],v[40:43]\n"
RT"a[168:169],v248 offset:16384\n"
MF"v[40:43],a[152:155],v[212:215],v[40:43]\n"
BL"v239,s[16:19],s48 offen offset:1024 lds\n"
MF"v[44:47],a[156:159],v[212:215],v[44:47]\n"
RD"v[204:207],v243 offset:26624\n"
MF"v[44:47],a[140:143],v[208:211],v[44:47]\n"
WT"lgkmcnt(4)\n"
RT"a[170:171],v248 offset:16896\n"
MF"v[48:51],a[128:131],v[216:219],v[48:51]\n"
MF"v[48:51],a[144:147],v[220:223],v[48:51]\n"
RD"v[208:211],v242 offset:28672\n"
MF"v[52:55],a[148:151],v[220:223],v[52:55]\n"
BL"v240,s[16:19],s48 offen offset:2048 lds\n"
MF"v[52:55],a[132:135],v[216:219],v[52:55]\n"
RT"a[172:173],v249 offset:16384\n"
MF"v[56:59],a[136:139],v[216:219],v[56:59]\n"
MF"v[56:59],a[152:155],v[220:223],v[56:59]\n"
RD"v[212:215],v243 offset:28672\n"
MF"v[60:63],a[156:159],v[220:223],v[60:63]\n"
RT"a[174:175],v249 offset:16896\n"
MF"v[60:63],a[140:143],v[216:219],v[60:63]\n"
BL"v241,s[16:19],s48 offen offset:3072 lds\n"
MF"v[64:67],a[128:131],v[192:195],v[64:67]\n"
SA"m0,s37,12288\n"
RD"v[216:219],v242 offset:30720\n"
"s_bfe_u32 s48,s46,0x3001c\n"
MF"v[64:67],a[144:147],v[196:199],v[64:67]\n"
"s_lshl_b32 s48,s48,19\n"
WT"vmcnt(20)\n"
RT"a[176:177],v246 offset:20480\n"
MF"v[68:71],a[148:151],v[196:199],v[68:71]\n"
"s_bitset1_b32 s48,18\n"
BL"v238,s[16:19],s48 offen offset:0 lds\n"
MF"v[68:71],a[132:135],v[192:195],v[68:71]\n"
RT"a[178:179],v246 offset:20992\n"
MF"v[72:75],a[136:139],v[192:195],v[72:75]\n"
RD"v[220:223],v243 offset:30720\n"
MF"v[72:75],a[152:155],v[196:199],v[72:75]\n"
MF"v[76:79],a[156:159],v[196:199],v[76:79]\n"
RT"a[180:181],v247 offset:20480\n"
MF"v[76:79],a[140:143],v[192:195],v[76:79]\n"
WT"lgkmcnt(4)\n"
BL"v239,s[16:19],s48 offen offset:1024 lds\n"
MF"v[80:83],a[128:131],v[200:203],v[80:83]\n"
MF"v[80:83],a[144:147],v[204:207],v[80:83]\n"
RT"a[182:183],v247 offset:20992\n"
MF"v[84:87],a[148:151],v[204:207],v[84:87]\n"
MF"v[84:87],a[132:135],v[200:203],v[84:87]\n"
BL"v240,s[16:19],s48 offen offset:2048 lds\n"
MF"v[88:91],a[136:139],v[200:203],v[88:91]\n"
RT"a[184:185],v248 offset:20480\n"
MF"v[88:91],a[152:155],v[204:207],v[88:91]\n"
WT"vmcnt(19) lgkmcnt(3)\n"
"s_barrier\n"
MF"v[92:95],a[156:159],v[204:207],v[92:95]\n"
RD"v[192:195],v242 offset:32768\n"
MF"v[92:95],a[140:143],v[200:203],v[92:95]\n"
RT"a[186:187],v248 offset:20992\n"
MF"v[96:99],a[128:131],v[208:211],v[96:99]\n"
BL"v241,s[16:19],s48 offen offset:3072 lds\n"
MF"v[96:99],a[144:147],v[212:215],v[96:99]\n"
MF"v[100:103],a[148:151],v[212:215],v[100:103]\n"
SA"m0,s36,16384\n"
RD"v[196:199],v243 offset:32768\n"
"s_bfe_u32 s48,s46,0x3001c\n"
MF"v[100:103],a[132:135],v[208:211],v[100:103]\n"
"s_lshl_b32 s48,s48,7\n"
RT"a[188:189],v249 offset:20480\n"
MF"v[104:107],a[136:139],v[208:211],v[104:107]\n"
BL"v234,s[12:15],s48 offen offset:0 lds\n"
MF"v[104:107],a[152:155],v[212:215],v[104:107]\n"
RT"a[190:191],v249 offset:20992\n"
MF"v[108:111],a[156:159],v[212:215],v[108:111]\n"
RD"v[200:203],v242 offset:34816\n"
MF"v[108:111],a[140:143],v[208:211],v[108:111]\n"
MF"v[112:115],a[128:131],v[216:219],v[112:115]\n"
BL"v235,s[12:15],s48 offen offset:1024 lds\n"
MF"v[112:115],a[144:147],v[220:223],v[112:115]\n"
RD"v[204:207],v243 offset:34816\n"
MF"v[116:119],a[148:151],v[220:223],v[116:119]\n"
MF"v[116:119],a[132:135],v[216:219],v[116:119]\n"
MF"v[120:123],a[136:139],v[216:219],v[120:123]\n"
MF"v[120:123],a[152:155],v[220:223],v[120:123]\n"
RD"v[208:211],v242 offset:36864\n"
MF"v[124:127],a[156:159],v[220:223],v[124:127]\n"
BL"v236,s[12:15],s48 offen offset:2048 lds\n"
MF"v[124:127],a[140:143],v[216:219],v[124:127]\n"
WT"lgkmcnt(2)\n"
MF"v[0:3],a[160:163],v[192:195],v[0:3]\n"
RD"v[212:215],v243 offset:36864\n"
MF"v[0:3],a[176:179],v[196:199],v[0:3]\n"
BL"v237,s[12:15],s48 offen offset:3072 lds\n"
MF"v[4:7],a[180:183],v[196:199],v[4:7]\n"
MF"v[4:7],a[164:167],v[192:195],v[4:7]\n"
MF"v[8:11],a[168:171],v[192:195],v[8:11]\n"
SA"m0,s37,16384\n"
RD"v[216:219],v242 offset:38912\n"
SA"s48,s39,0\n"
MF"v[8:11],a[184:187],v[196:199],v[8:11]\n"
MF"v[12:15],a[188:191],v[196:199],v[12:15]\n"
BL"v234,s[12:15],s48 offen offset:0 lds\n"
MF"v[12:15],a[172:175],v[192:195],v[12:15]\n"
RD"v[220:223],v243 offset:38912\n"
MF"v[16:19],a[160:163],v[200:203],v[16:19]\n"
WT"lgkmcnt(2)\n"
WT"vmcnt(21)\n"
RT"a[192:193],v246 offset:0\n"
MF"v[16:19],a[176:179],v[204:207],v[16:19]\n"
MF"v[20:23],a[180:183],v[204:207],v[20:23]\n"
BL"v235,s[12:15],s48 offen offset:1024 lds\n"
MF"v[20:23],a[164:167],v[200:203],v[20:23]\n"
RD"v[192:195],v242 offset:40960\n"
RT"a[194:195],v246 offset:512\n"
MF"v[24:27],a[168:171],v[200:203],v[24:27]\n"
MF"v[24:27],a[184:187],v[204:207],v[24:27]\n"
MF"v[28:31],a[188:191],v[204:207],v[28:31]\n"
RT"a[196:197],v247 offset:0\n"
MF"v[28:31],a[172:175],v[200:203],v[28:31]\n"
RD"v[196:199],v243 offset:40960\n"
MF"v[32:35],a[160:163],v[208:211],v[32:35]\n"
BL"v236,s[12:15],s48 offen offset:2048 lds\n"
MF"v[32:35],a[176:179],v[212:215],v[32:35]\n"
RT"a[198:199],v247 offset:512\n"
MF"v[36:39],a[180:183],v[212:215],v[36:39]\n"
RD"v[200:203],v242 offset:43008\n"
MF"v[36:39],a[164:167],v[208:211],v[36:39]\n"
BL"v237,s[12:15],s48 offen offset:3072 lds\n"
MF"v[40:43],a[168:171],v[208:211],v[40:43]\n"
RT"a[200:201],v248 offset:0\n"
MF"v[40:43],a[184:187],v[212:215],v[40:43]\n"
MF"v[44:47],a[188:191],v[212:215],v[44:47]\n"
SA"m0,s37,20480\n"
RD"v[204:207],v243 offset:43008\n"
SA"s48,s98,0\n"
MF"v[44:47],a[172:175],v[208:211],v[44:47]\n"
WT"lgkmcnt(4)\n"
RT"a[202:203],v248 offset:512\n"
"s_lshr_b32 s50,s30,1\n"
MF"v[48:51],a[160:163],v[216:219],v[48:51]\n"
"s_and_b32 s51,s30,1\n"
BL"v234,s[12:15],s48 offen offset:0 lds\n"
MF"v[48:51],a[176:179],v[220:223],v[48:51]\n"
"s_mul_i32 s38,s50,24576\n"
RD"v[208:211],v242 offset:45056\n"
MF"v[52:55],a[180:183],v[220:223],v[52:55]\n"
"s_lshl_b32 s60,s50,18\n"
RT"a[204:205],v249 offset:0\n"
MF"v[52:55],a[164:167],v[216:219],v[52:55]\n"
"s_lshl_b32 s50,s51,12\n"
MF"v[56:59],a[168:171],v[216:219],v[56:59]\n"
SA"s38,s38,s50\n"
BL"v235,s[12:15],s48 offen offset:1024 lds\n"
MF"v[56:59],a[184:187],v[220:223],v[56:59]\n"
"s_lshl_b32 s50,s51,7\n"
RD"v[212:215],v243 offset:45056\n"
RT"a[206:207],v249 offset:512\n"
MF"v[60:63],a[188:191],v[220:223],v[60:63]\n"
SA"s60,s60,s50\n"
MF"v[60:63],a[172:175],v[216:219],v[60:63]\n"
MF"v[64:67],a[160:163],v[192:195],v[64:67]\n"
RD"v[216:219],v242 offset:47104\n"
MF"v[64:67],a[176:179],v[196:199],v[64:67]\n"
"s_sub_u32 s99,s34,5\n"
WT"vmcnt(22)\n"
RT"a[208:209],v246 offset:4096\n"
MF"v[68:71],a[180:183],v[196:199],v[68:71]\n"
"s_cmp_lt_u32 s34,5\n"
BL"v236,s[12:15],s48 offen offset:2048 lds\n"
MF"v[68:71],a[164:167],v[192:195],v[68:71]\n"
"s_cselect_b32 s99,s34,s99\n"
RT"a[210:211],v246 offset:4608\n"
"s_mul_i32 s99,s99,1\n"
MF"v[72:75],a[168:171],v[192:195],v[72:75]\n"
"s_lshl_b32 s61,s99,20\n"
RD"v[220:223],v243 offset:47104\n"
MF"v[72:75],a[184:187],v[196:199],v[72:75]\n"
BL"v237,s[12:15],s48 offen offset:3072 lds\n"
MF"v[76:79],a[188:191],v[196:199],v[76:79]\n"
RT"a[212:213],v247 offset:4096\n"
MF"v[76:79],a[172:175],v[192:195],v[76:79]\n"
WT"lgkmcnt(4)\n"
MF"v[80:83],a[160:163],v[200:203],v[80:83]\n"
SA"m0,s37,0\n"
SA"s48,s39,32768\n"
MF"v[80:83],a[176:179],v[204:207],v[80:83]\n"
RT"a[214:215],v247 offset:4608\n"
MF"v[84:87],a[180:183],v[204:207],v[84:87]\n"
BL"v234,s[12:15],s48 offen offset:0 lds\n"
MF"v[84:87],a[164:167],v[200:203],v[84:87]\n"
MF"v[88:91],a[168:171],v[200:203],v[88:91]\n"
RT"a[216:217],v248 offset:4096\n"
MF"v[88:91],a[184:187],v[204:207],v[88:91]\n"
WT"vmcnt(21) lgkmcnt(3)\n"
"s_barrier\n"
MF"v[92:95],a[188:191],v[204:207],v[92:95]\n"
RD"v[192:195],v242 offset:0\n"
MF"v[92:95],a[172:175],v[200:203],v[92:95]\n"
BL"v235,s[12:15],s48 offen offset:1024 lds\n"
MF"v[96:99],a[160:163],v[208:211],v[96:99]\n"
RT"a[218:219],v248 offset:4608\n"
MF"v[96:99],a[176:179],v[212:215],v[96:99]\n"
MF"v[100:103],a[180:183],v[212:215],v[100:103]\n"
RD"v[196:199],v243 offset:0\n"
MF"v[100:103],a[164:167],v[208:211],v[100:103]\n"
RT"a[220:221],v249 offset:4096\n"
MF"v[104:107],a[168:171],v[208:211],v[104:107]\n"
BL"v236,s[12:15],s48 offen offset:2048 lds\n"
MF"v[104:107],a[184:187],v[212:215],v[104:107]\n"
RT"a[222:223],v249 offset:4608\n"
MF"v[108:111],a[188:191],v[212:215],v[108:111]\n"
RD"v[200:203],v242 offset:2048\n"
MF"v[108:111],a[172:175],v[208:211],v[108:111]\n"
BL"v237,s[12:15],s48 offen offset:3072 lds\n"
MF"v[112:115],a[160:163],v[216:219],v[112:115]\n"
MF"v[112:115],a[176:179],v[220:223],v[112:115]\n"
RD"v[204:207],v243 offset:2048\n"
MF"v[116:119],a[180:183],v[220:223],v[116:119]\n"
SA"m0,s37,4096\n"
SA"s48,s98,32768\n"
MF"v[116:119],a[164:167],v[216:219],v[116:119]\n"
MF"v[120:123],a[168:171],v[216:219],v[120:123]\n"
WT"lgkmcnt(2)\n"
BL"v234,s[12:15],s48 offen offset:0 lds\n"
MF"v[120:123],a[184:187],v[220:223],v[120:123]\n"
RD"v[208:211],v242 offset:4096\n"
MF"v[124:127],a[188:191],v[220:223],v[124:127]\n"
MF"v[124:127],a[172:175],v[216:219],v[124:127]\n"
MF"v[0:3],a[192:195],v[192:195],v[0:3]\n"
RD"v[212:215],v243 offset:4096\n"
MF"v[0:3],a[208:211],v[196:199],v[0:3]\n"
BL"v235,s[12:15],s48 offen offset:1024 lds\n"
MF"v[4:7],a[212:215],v[196:199],v[4:7]\n"
MF"v[4:7],a[196:199],v[192:195],v[4:7]\n"
MF"v[8:11],a[200:203],v[192:195],v[8:11]\n"
RD"v[216:219],v242 offset:6144\n"
MF"v[8:11],a[216:219],v[196:199],v[8:11]\n"
BL"v236,s[12:15],s48 offen offset:2048 lds\n"
MF"v[12:15],a[220:223],v[196:199],v[12:15]\n"
MF"v[12:15],a[204:207],v[192:195],v[12:15]\n"
WT"lgkmcnt(1)\n"
RD"v[220:223],v243 offset:6144\n"
MF"v[16:19],a[192:195],v[200:203],v[16:19]\n"
WT"vmcnt(23)\n"
RT"a[224:225],v246 offset:8192\n"
MF"v[16:19],a[208:211],v[204:207],v[16:19]\n"
BL"v237,s[12:15],s48 offen offset:3072 lds\n"
MF"v[20:23],a[212:215],v[204:207],v[20:23]\n"
MF"v[20:23],a[196:199],v[200:203],v[20:23]\n"
RD"v[192:195],v242 offset:8192\n"
MF"v[24:27],a[200:203],v[200:203],v[24:27]\n"
RT"a[226:227],v246 offset:8704\n"
MF"v[24:27],a[216:219],v[204:207],v[24:27]\n"
MF"v[28:31],a[220:223],v[204:207],v[28:31]\n"
RT"a[228:229],v247 offset:8192\n"
MF"v[28:31],a[204:207],v[200:203],v[28:31]\n"
RD"v[196:199],v243 offset:8192\n"
MF"v[32:35],a[192:195],v[208:211],v[32:35]\n"
MF"v[32:35],a[208:211],v[212:215],v[32:35]\n"
RT"a[230:231],v247 offset:8704\n"
MF"v[36:39],a[212:215],v[212:215],v[36:39]\n"
RD"v[200:203],v242 offset:10240\n"
MF"v[36:39],a[196:199],v[208:211],v[36:39]\n"
MF"v[40:43],a[200:203],v[208:211],v[40:43]\n"
RT"a[232:233],v248 offset:8192\n"
MF"v[40:43],a[216:219],v[212:215],v[40:43]\n"
MF"v[44:47],a[220:223],v[212:215],v[44:47]\n"
RD"v[204:207],v243 offset:10240\n"
MF"v[44:47],a[204:207],v[208:211],v[44:47]\n"
WT"lgkmcnt(4)\n"
RT"a[234:235],v248 offset:8704\n"
MF"v[48:51],a[192:195],v[216:219],v[48:51]\n"
MF"v[48:51],a[208:211],v[220:223],v[48:51]\n"
RD"v[208:211],v242 offset:12288\n"
MF"v[52:55],a[212:215],v[220:223],v[52:55]\n"
RT"a[236:237],v249 offset:8192\n"
MF"v[52:55],a[196:199],v[216:219],v[52:55]\n"
MF"v[56:59],a[200:203],v[216:219],v[56:59]\n"
MF"v[56:59],a[216:219],v[220:223],v[56:59]\n"
RD"v[212:215],v243 offset:12288\n"
MF"v[60:63],a[220:223],v[220:223],v[60:63]\n"
RT"a[238:239],v249 offset:8704\n"
MF"v[60:63],a[204:207],v[216:219],v[60:63]\n"
MF"v[64:67],a[192:195],v[192:195],v[64:67]\n"
RD"v[216:219],v242 offset:14336\n"
MF"v[64:67],a[208:211],v[196:199],v[64:67]\n"
SA"m0,s37,8192\n"
WT"vmcnt(20)\n"
RT"a[240:241],v246 offset:12288\n"
MF"v[68:71],a[212:215],v[196:199],v[68:71]\n"
SA"s48,s39,65536\n"
MF"v[68:71],a[196:199],v[192:195],v[68:71]\n"
RT"a[242:243],v246 offset:12800\n"
MF"v[72:75],a[200:203],v[192:195],v[72:75]\n"
WT"lgkmcnt(3)\n"
BL"v234,s[12:15],s48 offen offset:0 lds\n"
MF"v[72:75],a[216:219],v[196:199],v[72:75]\n"
RD"v[220:223],v243 offset:14336\n"
MF"v[76:79],a[220:223],v[196:199],v[76:79]\n"
RT"a[244:245],v247 offset:12288\n"
MF"v[76:79],a[204:207],v[192:195],v[76:79]\n"
BL"v235,s[12:15],s48 offen offset:1024 lds\n"
MF"v[80:83],a[192:195],v[200:203],v[80:83]\n"
MF"v[80:83],a[208:211],v[204:207],v[80:83]\n"
RT"a[246:247],v247 offset:12800\n"
MF"v[84:87],a[212:215],v[204:207],v[84:87]\n"
MF"v[84:87],a[196:199],v[200:203],v[84:87]\n"
MF"v[88:91],a[200:203],v[200:203],v[88:91]\n"
RT"a[248:249],v248 offset:12288\n"
MF"v[88:91],a[216:219],v[204:207],v[88:91]\n"
BL"v236,s[12:15],s48 offen offset:2048 lds\n"
MF"v[92:95],a[220:223],v[204:207],v[92:95]\n"
WT"vmcnt(19) lgkmcnt(3)\n"
"s_barrier\n"
MF"v[92:95],a[204:207],v[200:203],v[92:95]\n"
RD"v[192:195],v242 offset:16384\n"
RT"a[250:251],v248 offset:12800\n"
MF"v[96:99],a[192:195],v[208:211],v[96:99]\n"
BL"v237,s[12:15],s48 offen offset:3072 lds\n"
MF"v[96:99],a[208:211],v[212:215],v[96:99]\n"
MF"v[100:103],a[212:215],v[212:215],v[100:103]\n"
RD"v[196:199],v243 offset:16384\n"
MF"v[100:103],a[196:199],v[208:211],v[100:103]\n"
RT"a[252:253],v249 offset:12288\n"
MF"v[104:107],a[200:203],v[208:211],v[104:107]\n"
MF"v[104:107],a[216:219],v[212:215],v[104:107]\n"
RT"a[254:255],v249 offset:12800\n"
MF"v[108:111],a[220:223],v[212:215],v[108:111]\n"
RD"v[200:203],v242 offset:18432\n"
MF"v[108:111],a[204:207],v[208:211],v[108:111]\n"
MF"v[112:115],a[192:195],v[216:219],v[112:115]\n"
"s_lshl_b32 s50,s35,20\n"
MF"v[112:115],a[208:211],v[220:223],v[112:115]\n"
"s_lshl_b32 s51,s34,15\n"
RD"v[204:207],v243 offset:18432\n"
SA"m0,s37,12288\n"
MF"v[116:119],a[212:215],v[220:223],v[116:119]\n"
SA"s48,s98,65536\n"
SA"s50,s50,s51\n"
MF"v[116:119],a[196:199],v[216:219],v[116:119]\n"
WT"lgkmcnt(2)\n"
BL"v234,s[12:15],s48 offen offset:0 lds\n"
MF"v[120:123],a[200:203],v[216:219],v[120:123]\n"
"s_and_b32 s51,s30,1\n"
"s_lshl_b32 s51,s51,11\n"
MF"v[120:123],a[216:219],v[220:223],v[120:123]\n"
SA"s50,s50,s51\n"
RD"v[208:211],v242 offset:20480\n"
MF"v[124:127],a[220:223],v[220:223],v[124:127]\n"
SA"s50,s50,9175040\n"
MF"v[124:127],a[204:207],v[216:219],v[124:127]\n"
BL"v235,s[12:15],s48 offen offset:1024 lds\n"
MF"v[0:3],a[224:227],v[192:195],v[0:3]\n"
RD"v[212:215],v243 offset:20480\n"
"v_add_u32 v250,s50,v224\n"
MF"v[0:3],a[240:243],v[196:199],v[0:3]\n"
MF"v[4:7],a[244:247],v[196:199],v[4:7]\n"
MF"v[4:7],a[228:231],v[192:195],v[4:7]\n"
MF"v[8:11],a[232:235],v[192:195],v[8:11]\n"
RD"v[216:219],v242 offset:22528\n"
MF"v[8:11],a[248:251],v[196:199],v[8:11]\n"
BL"v236,s[12:15],s48 offen offset:2048 lds\n"
MF"v[12:15],a[252:255],v[196:199],v[12:15]\n"
MF"v[12:15],a[236:239],v[192:195],v[12:15]\n"
WT"lgkmcnt(1)\n"
RD"v[220:223],v243 offset:22528\n"
MF"v[16:19],a[224:227],v[200:203],v[16:19]\n"
BL"v237,s[12:15],s48 offen offset:3072 lds\n"
MF"v[16:19],a[240:243],v[204:207],v[16:19]\n"
MF"v[20:23],a[244:247],v[204:207],v[20:23]\n"
MF"v[20:23],a[228:231],v[200:203],v[20:23]\n"
RD"v[192:195],v242 offset:24576\n"
MF"v[24:27],a[232:235],v[200:203],v[24:27]\n"
MF"v[24:27],a[248:251],v[204:207],v[24:27]\n"
MF"v[28:31],a[252:255],v[204:207],v[28:31]\n"
MF"v[28:31],a[236:239],v[200:203],v[28:31]\n"
RD"v[196:199],v243 offset:24576\n"
MF"v[32:35],a[224:227],v[208:211],v[32:35]\n"
MF"v[32:35],a[240:243],v[212:215],v[32:35]\n"
MF"v[36:39],a[244:247],v[212:215],v[36:39]\n"
RD"v[200:203],v242 offset:26624\n"
MF"v[36:39],a[228:231],v[208:211],v[36:39]\n"
MF"v[40:43],a[232:235],v[208:211],v[40:43]\n"
MF"v[40:43],a[248:251],v[212:215],v[40:43]\n"
MF"v[44:47],a[252:255],v[212:215],v[44:47]\n"
RD"v[204:207],v243 offset:26624\n"
MF"v[44:47],a[236:239],v[208:211],v[44:47]\n"
WT"lgkmcnt(2)\n"
MF"v[48:51],a[224:227],v[216:219],v[48:51]\n"
MF"v[48:51],a[240:243],v[220:223],v[48:51]\n"
RD"v[208:211],v242 offset:28672\n"
MF"v[52:55],a[244:247],v[220:223],v[52:55]\n"
MF"v[52:55],a[228:231],v[216:219],v[52:55]\n"
MF"v[56:59],a[232:235],v[216:219],v[56:59]\n"
MF"v[56:59],a[248:251],v[220:223],v[56:59]\n"
RD"v[212:215],v243 offset:28672\n"
MF"v[60:63],a[252:255],v[220:223],v[60:63]\n"
MF"v[60:63],a[236:239],v[216:219],v[60:63]\n"
MF"v[64:67],a[224:227],v[192:195],v[64:67]\n"
RD"v[216:219],v242 offset:30720\n"
MF"v[64:67],a[240:243],v[196:199],v[64:67]\n"
MF"v[68:71],a[244:247],v[196:199],v[68:71]\n"
MF"v[68:71],a[228:231],v[192:195],v[68:71]\n"
MF"v[72:75],a[232:235],v[192:195],v[72:75]\n"
RD"v[220:223],v243 offset:30720\n"
MF"v[72:75],a[248:251],v[196:199],v[72:75]\n"
MF"v[76:79],a[252:255],v[196:199],v[76:79]\n"
MF"v[76:79],a[236:239],v[192:195],v[76:79]\n"
WT"lgkmcnt(1)\n"
MF"v[80:83],a[224:227],v[200:203],v[80:83]\n"
MF"v[80:83],a[240:243],v[204:207],v[80:83]\n"
MF"v[84:87],a[244:247],v[204:207],v[84:87]\n"
"v_add_u32 v244,114688,v242\n"
"v_add_u32 v245,114688,v243\n"
MF"v[84:87],a[228:231],v[200:203],v[84:87]\n"
"v_add_u32 v242,65536,v242\n"
"v_add_u32 v243,65536,v243\n"
MF"v[88:91],a[232:235],v[200:203],v[88:91]\n"
MF"v[88:91],a[248:251],v[204:207],v[88:91]\n"
WT"vmcnt(16) lgkmcnt(0)\n"
"s_barrier\n"
MF"v[92:95],a[252:255],v[204:207],v[92:95]\n"
RD"v[192:195],v242 offset:16384\n"
MF"v[92:95],a[236:239],v[200:203],v[92:95]\n"
MF"v[96:99],a[224:227],v[208:211],v[96:99]\n"
MF"v[96:99],a[240:243],v[212:215],v[96:99]\n"
MF"v[100:103],a[244:247],v[212:215],v[100:103]\n"
SA"m0,s36,49152\n"
RD"v[196:199],v243 offset:16384\n"
SA"s48,s60,s61\n"
MF"v[100:103],a[228:231],v[208:211],v[100:103]\n"
MF"v[104:107],a[232:235],v[208:211],v[104:107]\n"
BL"v238,s[20:23],s48 offen offset:0 lds\n"
MF"v[104:107],a[248:251],v[212:215],v[104:107]\n"
MF"v[108:111],a[252:255],v[212:215],v[108:111]\n"
RD"v[200:203],v242 offset:18432\n"
MF"v[108:111],a[236:239],v[208:211],v[108:111]\n"
MF"v[112:115],a[224:227],v[216:219],v[112:115]\n"
BL"v239,s[20:23],s48 offen offset:1024 lds\n"
MF"v[112:115],a[240:243],v[220:223],v[112:115]\n"
MF"v[116:119],a[244:247],v[220:223],v[116:119]\n"
RD"v[204:207],v243 offset:18432\n"
MF"v[116:119],a[228:231],v[216:219],v[116:119]\n"
MF"v[120:123],a[232:235],v[216:219],v[120:123]\n"
MF"v[120:123],a[248:251],v[220:223],v[120:123]\n"
BL"v240,s[20:23],s48 offen offset:2048 lds\n"
MF"v[124:127],a[252:255],v[220:223],v[124:127]\n"
SA"s61,s61,524288\n"
RD"v[208:211],v242 offset:20480\n"
MF"v[124:127],a[236:239],v[216:219],v[124:127]\n"
WT"lgkmcnt(2)\n"
MF"v[128:131],a[0:3],v[192:195],0\n"
MF"v[128:131],a[16:19],v[196:199],v[128:131]\n"
RD"v[212:215],v243 offset:20480\n"
MF"v[132:135],a[20:23],v[196:199],0\n"
BL"v241,s[20:23],s48 offen offset:3072 lds\n"
MF"v[132:135],a[4:7],v[192:195],v[132:135]\n"
MU"v0,s55,v0\n"
MU"v1,s55,v1\n"
MF"v[136:139],a[8:11],v[192:195],0\n"
MU"v2,s55,v2\n"
MU"v3,s55,v3\n"
MF"v[136:139],a[24:27],v[196:199],v[136:139]\n"
RD"v[216:219],v242 offset:22528\n"
MU"v4,s55,v4\n"
MF"v[140:143],a[28:31],v[196:199],0\n"
SA"m0,s36,32768\n"
MU"v5,s55,v5\n"
MU"v6,s55,v6\n"
MF"v[140:143],a[12:15],v[192:195],v[140:143]\n"
SA"s48,s60,s61\n"
MU"v7,s55,v7\n"
PK"v0,v0,v1\n"
MF"v[144:147],a[0:3],v[200:203],0\n"
WT"lgkmcnt(1)\n"
BL"v238,s[20:23],s48 offen offset:0 lds\n"
MF"v[144:147],a[16:19],v[204:207],v[144:147]\n"
RD"v[220:223],v243 offset:22528\n"
PK"v1,v2,v3\n"
MF"v[148:151],a[20:23],v[204:207],0\n"
PK"v2,v4,v5\n"
PK"v3,v6,v7\n"
MF"v[148:151],a[4:7],v[200:203],v[148:151]\n"
MF"v[152:155],a[8:11],v[200:203],0\n"
BL"v239,s[20:23],s48 offen offset:1024 lds\n"
MF"v[152:155],a[24:27],v[204:207],v[152:155]\n"
RD"v[192:195],v242 offset:40960\n"
MU"v8,s55,v8\n"
MF"v[156:159],a[28:31],v[204:207],0\n"
MU"v9,s55,v9\n"
MU"v10,s55,v10\n"
MF"v[156:159],a[12:15],v[200:203],v[156:159]\n"
MU"v11,s55,v11\n"
MU"v12,s55,v12\n"
MF"v[128:131],a[32:35],v[208:211],v[128:131]\n"
MU"v13,s55,v13\n"
MU"v14,s55,v14\n"
MF"v[128:131],a[48:51],v[212:215],v[128:131]\n"
RD"v[196:199],v243 offset:40960\n"
MU"v15,s55,v15\n"
MF"v[132:135],a[52:55],v[212:215],v[132:135]\n"
SA"s61,s61,524288\n"
BL"v240,s[20:23],s48 offen offset:2048 lds\n"
MF"v[132:135],a[36:39],v[208:211],v[132:135]\n"
"s_cmp_eq_u32 s61,5242880\n"
PK"v8,v8,v9\n"
PK"v9,v10,v11\n"
MF"v[136:139],a[40:43],v[208:211],v[136:139]\n"
"s_cselect_b32 s61,0,s61\n"
RD"v[200:203],v242 offset:43008\n"
PK"v10,v12,v13\n"
MF"v[136:139],a[56:59],v[212:215],v[136:139]\n"
BL"v241,s[20:23],s48 offen offset:3072 lds\n"
MF"v[140:143],a[60:63],v[212:215],v[140:143]\n"
PK"v11,v14,v15\n"
MU"v16,s55,v16\n"
MF"v[140:143],a[44:47],v[208:211],v[140:143]\n"
WT"lgkmcnt(2)\n"
MU"v17,s55,v17\n"
MU"v18,s55,v18\n"
MF"v[144:147],a[32:35],v[216:219],v[144:147]\n"
RD"v[204:207],v243 offset:43008\n"
MU"v19,s55,v19\n"
MF"v[144:147],a[48:51],v[220:223],v[144:147]\n"
MU"v20,s55,v20\n"
MU"v21,s55,v21\n"
MF"v[148:151],a[52:55],v[220:223],v[148:151]\n"
MU"v22,s55,v22\n"
MU"v23,s55,v23\n"
MF"v[148:151],a[36:39],v[216:219],v[148:151]\n"
PK"v16,v16,v17\n"
PK"v17,v18,v19\n"
MF"v[152:155],a[40:43],v[216:219],v[152:155]\n"
RD"v[208:211],v242 offset:45056\n"
PK"v18,v20,v21\n"
MF"v[152:155],a[56:59],v[220:223],v[152:155]\n"
PK"v19,v22,v23\n"
MF"v[156:159],a[60:63],v[220:223],v[156:159]\n"
MF"v[156:159],a[44:47],v[216:219],v[156:159]\n"
"s_mov_b32 s49,0\n"
MF"v[128:131],a[64:67],v[192:195],v[128:131]\n"
WT"lgkmcnt(1)\n"
RD"v[212:215],v243 offset:45056\n"
MU"v24,s55,v24\n"
MF"v[128:131],a[80:83],v[196:199],v[128:131]\n"
BS"v[0:3],v250,s[8:11],s49 offen offset:0 sc1\n"
MF"v[132:135],a[84:87],v[196:199],v[132:135]\n"
MU"v25,s55,v25\n"
MU"v26,s55,v26\n"
MF"v[132:135],a[68:71],v[192:195],v[132:135]\n"
RD"v[216:219],v242 offset:47104\n"
MU"v27,s55,v27\n"
MF"v[136:139],a[72:75],v[192:195],v[136:139]\n"
MU"v28,s55,v28\n"
MU"v29,s55,v29\n"
MF"v[136:139],a[88:91],v[196:199],v[136:139]\n"
MU"v30,s55,v30\n"
MU"v31,s55,v31\n"
MF"v[140:143],a[92:95],v[196:199],v[140:143]\n"
PK"v24,v24,v25\n"
PK"v25,v26,v27\n"
MF"v[140:143],a[76:79],v[192:195],v[140:143]\n"
RD"v[220:223],v243 offset:47104\n"
PK"v26,v28,v29\n"
MF"v[144:147],a[64:67],v[200:203],v[144:147]\n"
PK"v27,v30,v31\n"
MF"v[144:147],a[80:83],v[204:207],v[144:147]\n"
MF"v[148:151],a[84:87],v[204:207],v[148:151]\n"
MU"v32,s55,v32\n"
MU"v33,s55,v33\n"
MF"v[148:151],a[68:71],v[200:203],v[148:151]\n"
RD"v[192:195],v244 offset:16384\n"
MU"v34,s55,v34\n"
MF"v[152:155],a[72:75],v[200:203],v[152:155]\n"
MU"v35,s55,v35\n"
MU"v36,s55,v36\n"
MF"v[152:155],a[88:91],v[204:207],v[152:155]\n"
MU"v37,s55,v37\n"
MU"v38,s55,v38\n"
MF"v[156:159],a[92:95],v[204:207],v[156:159]\n"
MU"v39,s55,v39\n"
PK"v32,v32,v33\n"
MF"v[156:159],a[76:79],v[200:203],v[156:159]\n"
WT"lgkmcnt(1)\n"
RD"v[196:199],v245 offset:16384\n"
PK"v33,v34,v35\n"
MF"v[128:131],a[96:99],v[208:211],v[128:131]\n"
PK"v34,v36,v37\n"
PK"v35,v38,v39\n"
MF"v[128:131],a[112:115],v[212:215],v[128:131]\n"
MF"v[132:135],a[116:119],v[212:215],v[132:135]\n"
MF"v[132:135],a[100:103],v[208:211],v[132:135]\n"
RD"v[200:203],v244 offset:18432\n"
MF"v[136:139],a[104:107],v[208:211],v[136:139]\n"
MU"v40,s55,v40\n"
MU"v41,s55,v41\n"
MF"v[136:139],a[120:123],v[212:215],v[136:139]\n"
MU"v42,s55,v42\n"
MU"v43,s55,v43\n"
MF"v[140:143],a[124:127],v[212:215],v[140:143]\n"
RD"v[204:207],v245 offset:18432\n"
MU"v44,s55,v44\n"
MF"v[140:143],a[108:111],v[208:211],v[140:143]\n"
MU"v45,s55,v45\n"
MU"v46,s55,v46\n"
MF"v[144:147],a[96:99],v[216:219],v[144:147]\n"
MU"v47,s55,v47\n"
PK"v40,v40,v41\n"
MF"v[144:147],a[112:115],v[220:223],v[144:147]\n"
PK"v41,v42,v43\n"
PK"v42,v44,v45\n"
MF"v[148:151],a[116:119],v[220:223],v[148:151]\n"
RD"v[208:211],v244 offset:20480\n"
PK"v43,v46,v47\n"
MF"v[148:151],a[100:103],v[216:219],v[148:151]\n"
MF"v[152:155],a[104:107],v[216:219],v[152:155]\n"
MF"v[152:155],a[120:123],v[220:223],v[152:155]\n"
MF"v[156:159],a[124:127],v[220:223],v[156:159]\n"
RD"v[212:215],v245 offset:20480\n"
MU"v48,s55,v48\n"
MF"v[156:159],a[108:111],v[216:219],v[156:159]\n"
WT"lgkmcnt(2)\n"
MU"v49,s55,v49\n"
MU"v50,s55,v50\n"
MF"v[128:131],a[128:131],v[192:195],v[128:131]\n"
BS"v[8:11],v250,s[8:11],s49 offen offset:1024 sc1\n"
MF"v[128:131],a[144:147],v[196:199],v[128:131]\n"
MU"v51,s55,v51\n"
MU"v52,s55,v52\n"
MF"v[132:135],a[148:151],v[196:199],v[132:135]\n"
RD"v[216:219],v244 offset:22528\n"
MU"v53,s55,v53\n"
MF"v[132:135],a[132:135],v[192:195],v[132:135]\n"
MU"v54,s55,v54\n"
MU"v55,s55,v55\n"
MF"v[136:139],a[136:139],v[192:195],v[136:139]\n"
PK"v48,v48,v49\n"
PK"v49,v50,v51\n"
MF"v[136:139],a[152:155],v[196:199],v[136:139]\n"
RD"v[220:223],v245 offset:22528\n"
PK"v50,v52,v53\n"
MF"v[140:143],a[156:159],v[196:199],v[140:143]\n"
PK"v51,v54,v55\n"
MF"v[140:143],a[140:143],v[192:195],v[140:143]\n"
MF"v[144:147],a[128:131],v[200:203],v[144:147]\n"
MU"v56,s55,v56\n"
MU"v57,s55,v57\n"
MF"v[144:147],a[144:147],v[204:207],v[144:147]\n"
RD"v[192:195],v244 offset:40960\n"
MU"v58,s55,v58\n"
MF"v[148:151],a[148:151],v[204:207],v[148:151]\n"
MU"v59,s55,v59\n"
MU"v60,s55,v60\n"
MF"v[148:151],a[132:135],v[200:203],v[148:151]\n"
MU"v61,s55,v61\n"
MU"v62,s55,v62\n"
MF"v[152:155],a[136:139],v[200:203],v[152:155]\n"
MU"v63,s55,v63\n"
PK"v56,v56,v57\n"
MF"v[152:155],a[152:155],v[204:207],v[152:155]\n"
RD"v[196:199],v245 offset:40960\n"
PK"v57,v58,v59\n"
MF"v[156:159],a[156:159],v[204:207],v[156:159]\n"
PK"v58,v60,v61\n"
PK"v59,v62,v63\n"
MF"v[156:159],a[140:143],v[200:203],v[156:159]\n"
WT"lgkmcnt(2)\n"
MF"v[128:131],a[160:163],v[208:211],v[128:131]\n"
MF"v[128:131],a[176:179],v[212:215],v[128:131]\n"
RD"v[200:203],v244 offset:43008\n"
MF"v[132:135],a[180:183],v[212:215],v[132:135]\n"
MU"v64,s55,v64\n"
MU"v65,s55,v65\n"
MF"v[132:135],a[164:167],v[208:211],v[132:135]\n"
MU"v66,s55,v66\n"
MU"v67,s55,v67\n"
MF"v[136:139],a[168:171],v[208:211],v[136:139]\n"
MU"v68,s55,v68\n"
MU"v69,s55,v69\n"
MF"v[136:139],a[184:187],v[212:215],v[136:139]\n"
RD"v[204:207],v245 offset:43008\n"
MU"v70,s55,v70\n"
MF"v[140:143],a[188:191],v[212:215],v[140:143]\n"
MU"v71,s55,v71\n"
PK"v64,v64,v65\n"
MF"v[140:143],a[172:175],v[208:211],v[140:143]\n"
PK"v65,v66,v67\n"
PK"v66,v68,v69\n"
MF"v[144:147],a[160:163],v[216:219],v[144:147]\n"
RD"v[208:211],v244 offset:45056\n"
PK"v67,v70,v71\n"
MF"v[144:147],a[176:179],v[220:223],v[144:147]\n"
MF"v[148:151],a[180:183],v[220:223],v[148:151]\n"
MF"v[148:151],a[164:167],v[216:219],v[148:151]\n"
MF"v[152:155],a[168:171],v[216:219],v[152:155]\n"
RD"v[212:215],v245 offset:45056\n"
MU"v72,s55,v72\n"
MF"v[152:155],a[184:187],v[220:223],v[152:155]\n"
"s_mov_b32 s49,4096\n"
MU"v73,s55,v73\n"
MU"v74,s55,v74\n"
MF"v[156:159],a[188:191],v[220:223],v[156:159]\n"
MU"v75,s55,v75\n"
MU"v76,s55,v76\n"
MF"v[156:159],a[172:175],v[216:219],v[156:159]\n"
WT"lgkmcnt(1)\n"
BS"v[16:19],v250,s[8:11],s49 offen offset:0 sc1\n"
MF"v[128:131],a[192:195],v[192:195],v[128:131]\n"
RD"v[216:219],v244 offset:47104\n"
MU"v77,s55,v77\n"
MF"v[128:131],a[208:211],v[196:199],v[128:131]\n"
MU"v78,s55,v78\n"
MU"v79,s55,v79\n"
MF"v[132:135],a[212:215],v[196:199],v[132:135]\n"
PK"v72,v72,v73\n"
PK"v73,v74,v75\n"
MF"v[132:135],a[196:199],v[192:195],v[132:135]\n"
PK"v74,v76,v77\n"
PK"v75,v78,v79\n"
MF"v[136:139],a[200:203],v[192:195],v[136:139]\n"
RD"v[220:223],v245 offset:47104\n"
MF"v[136:139],a[216:219],v[196:199],v[136:139]\n"
MF"v[140:143],a[220:223],v[196:199],v[140:143]\n"
MU"v80,s55,v80\n"
MU"v81,s55,v81\n"
MF"v[140:143],a[204:207],v[192:195],v[140:143]\n"
MU"v82,s55,v82\n"
MU"v83,s55,v83\n"
MF"v[144:147],a[192:195],v[200:203],v[144:147]\n"
MU"v84,s55,v84\n"
MU"v85,s55,v85\n"
MF"v[144:147],a[208:211],v[204:207],v[144:147]\n"
MU"v86,s55,v86\n"
MU"v87,s55,v87\n"
MF"v[148:151],a[212:215],v[204:207],v[148:151]\n"
PK"v80,v80,v81\n"
PK"v81,v82,v83\n"
MF"v[148:151],a[196:199],v[200:203],v[148:151]\n"
PK"v82,v84,v85\n"
PK"v83,v86,v87\n"
MF"v[152:155],a[200:203],v[200:203],v[152:155]\n"
MF"v[152:155],a[216:219],v[204:207],v[152:155]\n"
WT"vmcnt(19) lgkmcnt(0)\n"
"s_barrier\n"
MF"v[156:159],a[220:223],v[204:207],v[156:159]\n"
RD"v[192:195],v242 offset:0\n"
MF"v[156:159],a[204:207],v[200:203],v[156:159]\n"
MF"v[128:131],a[224:227],v[208:211],v[128:131]\n"
MU"v88,s55,v88\n"
MU"v89,s55,v89\n"
MF"v[128:131],a[240:243],v[212:215],v[128:131]\n"
MU"v90,s55,v90\n"
MU"v91,s55,v91\n"
MF"v[132:135],a[244:247],v[212:215],v[132:135]\n"
RD"v[196:199],v243 offset:0\n"
MU"v92,s55,v92\n"
MF"v[132:135],a[228:231],v[208:211],v[132:135]\n"
MU"v93,s55,v93\n"
MU"v94,s55,v94\n"
MF"v[136:139],a[232:235],v[208:211],v[136:139]\n"
MU"v95,s55,v95\n"
PK"v88,v88,v89\n"
MF"v[136:139],a[248:251],v[212:215],v[136:139]\n"
PK"v89,v90,v91\n"
PK"v90,v92,v93\n"
MF"v[140:143],a[252:255],v[212:215],v[140:143]\n"
RD"v[200:203],v242 offset:2048\n"
PK"v91,v94,v95\n"
MF"v[140:143],a[236:239],v[208:211],v[140:143]\n"
MF"v[144:147],a[224:227],v[216:219],v[144:147]\n"
MF"v[144:147],a[240:243],v[220:223],v[144:147]\n"
MF"v[148:151],a[244:247],v[220:223],v[148:151]\n"
RD"v[204:207],v243 offset:2048\n"
MF"v[148:151],a[228:231],v[216:219],v[148:151]\n"
MF"v[152:155],a[232:235],v[216:219],v[152:155]\n"
MF"v[152:155],a[248:251],v[220:223],v[152:155]\n"
MF"v[156:159],a[252:255],v[220:223],v[156:159]\n"
RD"v[208:211],v242 offset:4096\n"
MF"v[156:159],a[236:239],v[216:219],v[156:159]\n"
WT"lgkmcnt(2)\n"
BS"v[24:27],v250,s[8:11],s49 offen offset:1024 sc1\n"
MF"v[36:39],a[0:3],v[192:195],0\n"
MF"v[36:39],a[16:19],v[196:199],v[36:39]\n"
RD"v[212:215],v243 offset:4096\n"
MF"v[68:71],a[20:23],v[196:199],0\n"
MF"v[68:71],a[4:7],v[192:195],v[68:71]\n"
MU"v128,s55,v128\n"
MU"v129,s55,v129\n"
MF"v[44:47],a[8:11],v[192:195],0\n"
MU"v130,s55,v130\n"
MU"v131,s55,v131\n"
MF"v[44:47],a[24:27],v[196:199],v[44:47]\n"
RD"v[216:219],v242 offset:6144\n"
MU"v132,s55,v132\n"
MF"v[76:79],a[28:31],v[196:199],0\n"
MU"v133,s55,v133\n"
MU"v134,s55,v134\n"
MF"v[76:79],a[12:15],v[192:195],v[76:79]\n"
MU"v135,s55,v135\n"
PK"v4,v128,v129\n"
MF"v[52:55],a[0:3],v[200:203],0\n"
WT"lgkmcnt(1)\n"
PK"v5,v130,v131\n"
PK"v6,v132,v133\n"
MF"v[52:55],a[16:19],v[204:207],v[52:55]\n"
RD"v[220:223],v243 offset:6144\n"
PK"v7,v134,v135\n"
MF"v[84:87],a[20:23],v[204:207],0\n"
MF"v[84:87],a[4:7],v[200:203],v[84:87]\n"
MF"v[60:63],a[8:11],v[200:203],0\n"
MF"v[60:63],a[24:27],v[204:207],v[60:63]\n"
RD"v[192:195],v242 offset:24576\n"
MU"v136,s55,v136\n"
MF"v[92:95],a[28:31],v[204:207],0\n"
MU"v137,s55,v137\n"
MU"v138,s55,v138\n"
MF"v[92:95],a[12:15],v[200:203],v[92:95]\n"
MU"v139,s55,v139\n"
MU"v140,s55,v140\n"
MF"v[36:39],a[32:35],v[208:211],v[36:39]\n"
MU"v141,s55,v141\n"
MU"v142,s55,v142\n"
MF"v[36:39],a[48:51],v[212:215],v[36:39]\n"
RD"v[196:199],v243 offset:24576\n"
MU"v143,s55,v143\n"
MF"v[68:71],a[52:55],v[212:215],v[68:71]\n"
PK"v12,v136,v137\n"
PK"v13,v138,v139\n"
MF"v[68:71],a[36:39],v[208:211],v[68:71]\n"
PK"v14,v140,v141\n"
PK"v15,v142,v143\n"
MF"v[44:47],a[40:43],v[208:211],v[44:47]\n"
RD"v[200:203],v242 offset:26624\n"
MF"v[44:47],a[56:59],v[212:215],v[44:47]\n"
MF"v[76:79],a[60:63],v[212:215],v[76:79]\n"
MF"v[76:79],a[44:47],v[208:211],v[76:79]\n"
WT"lgkmcnt(2)\n"
MU"v144,s55,v144\n"
MU"v145,s55,v145\n"
MF"v[52:55],a[32:35],v[216:219],v[52:55]\n"
RD"v[204:207],v243 offset:26624\n"
MU"v146,s55,v146\n"
MF"v[52:55],a[48:51],v[220:223],v[52:55]\n"
MU"v147,s55,v147\n"
MU"v148,s55,v148\n"
MF"v[84:87],a[52:55],v[220:223],v[84:87]\n"
MU"v149,s55,v149\n"
MU"v150,s55,v150\n"
MF"v[84:87],a[36:39],v[216:219],v[84:87]\n"
"s_mov_b32 s49,8192\n"
MU"v151,s55,v151\n"
PK"v20,v144,v145\n"
MF"v[60:63],a[40:43],v[216:219],v[60:63]\n"
RD"v[208:211],v242 offset:28672\n"
PK"v21,v146,v147\n"
MF"v[60:63],a[56:59],v[220:223],v[60:63]\n"
BS"v[32:35],v250,s[8:11],s49 offen offset:0 sc1\n"
MF"v[92:95],a[60:63],v[220:223],v[92:95]\n"
PK"v22,v148,v149\n"
PK"v23,v150,v151\n"
MF"v[92:95],a[44:47],v[216:219],v[92:95]\n"
MF"v[36:39],a[64:67],v[192:195],v[36:39]\n"
WT"lgkmcnt(1)\n"
RD"v[212:215],v243 offset:28672\n"
MF"v[36:39],a[80:83],v[196:199],v[36:39]\n"
MU"v152,s55,v152\n"
MU"v153,s55,v153\n"
MF"v[68:71],a[84:87],v[196:199],v[68:71]\n"
MU"v154,s55,v154\n"
MU"v155,s55,v155\n"
MF"v[68:71],a[68:71],v[192:195],v[68:71]\n"
RD"v[216:219],v242 offset:30720\n"
MU"v156,s55,v156\n"
MF"v[44:47],a[72:75],v[192:195],v[44:47]\n"
MU"v157,s55,v157\n"
MU"v158,s55,v158\n"
MF"v[44:47],a[88:91],v[196:199],v[44:47]\n"
MU"v159,s55,v159\n"
PK"v28,v152,v153\n"
MF"v[76:79],a[92:95],v[196:199],v[76:79]\n"
PK"v29,v154,v155\n"
PK"v30,v156,v157\n"
MF"v[76:79],a[76:79],v[192:195],v[76:79]\n"
RD"v[220:223],v243 offset:30720\n"
PK"v31,v158,v159\n"
MF"v[52:55],a[64:67],v[200:203],v[52:55]\n"
MF"v[52:55],a[80:83],v[204:207],v[52:55]\n"
MF"v[84:87],a[84:87],v[204:207],v[84:87]\n"
MF"v[84:87],a[68:71],v[200:203],v[84:87]\n"
RD"v[192:195],v244 offset:0\n"
MF"v[60:63],a[72:75],v[200:203],v[60:63]\n"
MF"v[60:63],a[88:91],v[204:207],v[60:63]\n"
MF"v[92:95],a[92:95],v[204:207],v[92:95]\n"
MF"v[92:95],a[76:79],v[200:203],v[92:95]\n"
WT"lgkmcnt(1)\n"
RD"v[196:199],v245 offset:0\n"
MF"v[36:39],a[96:99],v[208:211],v[36:39]\n"
MF"v[36:39],a[112:115],v[212:215],v[36:39]\n"
MF"v[68:71],a[116:119],v[212:215],v[68:71]\n"
MF"v[68:71],a[100:103],v[208:211],v[68:71]\n"
RD"v[200:203],v244 offset:2048\n"
MF"v[44:47],a[104:107],v[208:211],v[44:47]\n"
MF"v[44:47],a[120:123],v[212:215],v[44:47]\n"
MF"v[76:79],a[124:127],v[212:215],v[76:79]\n"
RD"v[204:207],v245 offset:2048\n"
MF"v[76:79],a[108:111],v[208:211],v[76:79]\n"
MF"v[52:55],a[96:99],v[216:219],v[52:55]\n"
MF"v[52:55],a[112:115],v[220:223],v[52:55]\n"
MF"v[84:87],a[116:119],v[220:223],v[84:87]\n"
SA"m0,s37,16384\n"
RD"v[208:211],v244 offset:4096\n"
SA"s48,s39,98304\n"
MF"v[84:87],a[100:103],v[216:219],v[84:87]\n"
MF"v[60:63],a[104:107],v[216:219],v[60:63]\n"
BS"v[40:43],v250,s[8:11],s49 offen offset:1024 sc1\n"
MF"v[60:63],a[120:123],v[220:223],v[60:63]\n"
BL"v234,s[12:15],s48 offen offset:0 lds\n"
MF"v[92:95],a[124:127],v[220:223],v[92:95]\n"
RD"v[212:215],v245 offset:4096\n"
MU"v96,s55,v96\n"
MF"v[92:95],a[108:111],v[216:219],v[92:95]\n"
WT"lgkmcnt(2)\n"
MU"v97,s55,v97\n"
MU"v98,s55,v98\n"
MF"v[36:39],a[128:131],v[192:195],v[36:39]\n"
MU"v99,s55,v99\n"
MU"v100,s55,v100\n"
MF"v[36:39],a[144:147],v[196:199],v[36:39]\n"
MU"v101,s55,v101\n"
MU"v102,s55,v102\n"
MF"v[68:71],a[148:151],v[196:199],v[68:71]\n"
RD"v[216:219],v244 offset:6144\n"
MU"v103,s55,v103\n"
MF"v[68:71],a[132:135],v[192:195],v[68:71]\n"
BL"v235,s[12:15],s48 offen offset:1024 lds\n"
MF"v[44:47],a[136:139],v[192:195],v[44:47]\n"
PK"v96,v96,v97\n"
PK"v97,v98,v99\n"
MF"v[44:47],a[152:155],v[196:199],v[44:47]\n"
RD"v[220:223],v245 offset:6144\n"
PK"v98,v100,v101\n"
MF"v[76:79],a[156:159],v[196:199],v[76:79]\n"
PK"v99,v102,v103\n"
MU"v104,s55,v104\n"
MF"v[76:79],a[140:143],v[192:195],v[76:79]\n"
MU"v105,s55,v105\n"
MU"v106,s55,v106\n"
MF"v[52:55],a[128:131],v[200:203],v[52:55]\n"
BL"v236,s[12:15],s48 offen offset:2048 lds\n"
MF"v[52:55],a[144:147],v[204:207],v[52:55]\n"
RD"v[192:195],v244 offset:24576\n"
MU"v107,s55,v107\n"
MF"v[84:87],a[148:151],v[204:207],v[84:87]\n"
MU"v108,s55,v108\n"
MU"v109,s55,v109\n"
MF"v[84:87],a[132:135],v[200:203],v[84:87]\n"
MU"v110,s55,v110\n"
MU"v111,s55,v111\n"
MF"v[60:63],a[136:139],v[200:203],v[60:63]\n"
PK"v104,v104,v105\n"
PK"v105,v106,v107\n"
MF"v[60:63],a[152:155],v[204:207],v[60:63]\n"
RD"v[196:199],v245 offset:24576\n"
PK"v106,v108,v109\n"
MF"v[92:95],a[156:159],v[204:207],v[92:95]\n"
BL"v237,s[12:15],s48 offen offset:3072 lds\n"
MF"v[92:95],a[140:143],v[200:203],v[92:95]\n"
WT"lgkmcnt(2)\n"
PK"v107,v110,v111\n"
MF"v[36:39],a[160:163],v[208:211],v[36:39]\n"
MU"v112,s55,v112\n"
MU"v113,s55,v113\n"
MF"v[36:39],a[176:179],v[212:215],v[36:39]\n"
RD"v[200:203],v244 offset:26624\n"
MU"v114,s55,v114\n"
MF"v[68:71],a[180:183],v[212:215],v[68:71]\n"
SA"m0,s37,20480\n"
MU"v115,s55,v115\n"
MU"v116,s55,v116\n"
MF"v[68:71],a[164:167],v[208:211],v[68:71]\n"
SA"s48,s98,98304\n"
MU"v117,s55,v117\n"
MU"v118,s55,v118\n"
MF"v[44:47],a[168:171],v[208:211],v[44:47]\n"
BL"v234,s[12:15],s48 offen offset:0 lds\n"
MF"v[44:47],a[184:187],v[212:215],v[44:47]\n"
RD"v[204:207],v245 offset:26624\n"
MU"v119,s55,v119\n"
MF"v[76:79],a[188:191],v[212:215],v[76:79]\n"
PK"v112,v112,v113\n"
PK"v113,v114,v115\n"
MF"v[76:79],a[172:175],v[208:211],v[76:79]\n"
PK"v114,v116,v117\n"
PK"v115,v118,v119\n"
MF"v[52:55],a[160:163],v[216:219],v[52:55]\n"
RD"v[208:211],v244 offset:28672\n"
MF"v[52:55],a[176:179],v[220:223],v[52:55]\n"
"s_mov_b32 s49,12288\n"
MF"v[84:87],a[180:183],v[220:223],v[84:87]\n"
BL"v235,s[12:15],s48 offen offset:1024 lds\n"
MF"v[84:87],a[164:167],v[216:219],v[84:87]\n"
BS"v[48:51],v250,s[8:11],s49 offen offset:0 sc1\n"
MF"v[60:63],a[168:171],v[216:219],v[60:63]\n"
RD"v[212:215],v245 offset:28672\n"
MU"v120,s55,v120\n"
MF"v[60:63],a[184:187],v[220:223],v[60:63]\n"
MU"v121,s55,v121\n"
MU"v122,s55,v122\n"
MF"v[92:95],a[188:191],v[220:223],v[92:95]\n"
MU"v123,s55,v123\n"
MU"v124,s55,v124\n"
MF"v[92:95],a[172:175],v[216:219],v[92:95]\n"
WT"lgkmcnt(1)\n"
MU"v125,s55,v125\n"
MU"v126,s55,v126\n"
MF"v[36:39],a[192:195],v[192:195],v[36:39]\n"
RD"v[216:219],v244 offset:30720\n"
MU"v127,s55,v127\n"
MF"v[36:39],a[208:211],v[196:199],v[36:39]\n"
BL"v236,s[12:15],s48 offen offset:2048 lds\n"
MF"v[68:71],a[212:215],v[196:199],v[68:71]\n"
PK"v120,v120,v121\n"
PK"v121,v122,v123\n"
MF"v[68:71],a[196:199],v[192:195],v[68:71]\n"
PK"v122,v124,v125\n"
PK"v123,v126,v127\n"
MF"v[44:47],a[200:203],v[192:195],v[44:47]\n"
RD"v[220:223],v245 offset:30720\n"
MF"v[44:47],a[216:219],v[196:199],v[44:47]\n"
MF"v[76:79],a[220:223],v[196:199],v[76:79]\n"
BL"v237,s[12:15],s48 offen offset:3072 lds\n"
MF"v[76:79],a[204:207],v[192:195],v[76:79]\n"
MF"v[52:55],a[192:195],v[200:203],v[52:55]\n"
MF"v[52:55],a[208:211],v[204:207],v[52:55]\n"
MF"v[84:87],a[212:215],v[204:207],v[84:87]\n"
MF"v[84:87],a[196:199],v[200:203],v[84:87]\n"
"v_lshrrev_b32 v251,2,v224\n"
MF"v[60:63],a[200:203],v[200:203],v[60:63]\n"
"v_and_b32 v251,28,v251\n"
MF"v[60:63],a[216:219],v[204:207],v[60:63]\n"
WT"vmcnt(23) lgkmcnt(0)\n"
"s_barrier\n"
MF"v[92:95],a[220:223],v[204:207],v[92:95]\n"
RD"v[192:195],v242 offset:8192\n"
"ds_read_b32 v234,v251 offset:16384\n"
MF"v[92:95],a[204:207],v[200:203],v[92:95]\n"
MF"v[36:39],a[224:227],v[208:211],v[36:39]\n"
MF"v[36:39],a[240:243],v[212:215],v[36:39]\n"
MF"v[68:71],a[244:247],v[212:215],v[68:71]\n"
RD"v[196:199],v243 offset:8192\n"
MF"v[68:71],a[228:231],v[208:211],v[68:71]\n"
MF"v[44:47],a[232:235],v[208:211],v[44:47]\n"
MF"v[44:47],a[248:251],v[212:215],v[44:47]\n"
MF"v[76:79],a[252:255],v[212:215],v[76:79]\n"
RD"v[200:203],v242 offset:10240\n"
MF"v[76:79],a[236:239],v[208:211],v[76:79]\n"
WT"lgkmcnt(2)\n"
"v_not_b32 v251,v234\n"
"v_bfe_u32 v252,v251,4,7\n"
MF"v[52:55],a[224:227],v[216:219],v[52:55]\n"
"v_mul_u32_u24 v252,20480,v252\n"
MF"v[52:55],a[240:243],v[220:223],v[52:55]\n"
"v_and_b32 v251,15,v251\n"
MF"v[84:87],a[244:247],v[220:223],v[84:87]\n"
RD"v[204:207],v243 offset:10240\n"
"v_lshl_add_u32 v251,v251,4,v252\n"
MF"v[84:87],a[228:231],v[216:219],v[84:87]\n"
BS"v[56:59],v250,s[8:11],s49 offen offset:1024 sc1\n"
MF"v[60:63],a[232:235],v[216:219],v[60:63]\n"
"v_and_b32 v252,0x380,v224\n"
"v_lshl_add_u32 v251,v252,1,v251\n"
MF"v[60:63],a[248:251],v[220:223],v[60:63]\n"
"v_add_u32 v252,v252,v224\n"
MF"v[92:95],a[252:255],v[220:223],v[92:95]\n"
"s_lshl_b32 s92,s34,2\n"
RD"v[208:211],v242 offset:12288\n"
MF"v[92:95],a[236:239],v[216:219],v[92:95]\n"
SA"s92,s92,s30\n"
MF"v[128:131],a[0:3],v[192:195],0\n"
WT"lgkmcnt(2)\n"
"s_lshl_b32 s93,s92,11\n"
MF"v[128:131],a[16:19],v[196:199],v[128:131]\n"
"s_cmp_lt_u32 s92,10\n"
RD"v[212:215],v243 offset:12288\n"
"s_cselect_b32 s93,s93,2130706432\n"
MF"v[132:135],a[20:23],v[196:199],0\n"
SA"s92,s93,65536\n"
MF"v[132:135],a[4:7],v[192:195],v[132:135]\n"
BL"v[100:103],v251,s[8:11],s92 offen sc1\n"
MF"v[136:139],a[8:11],v[192:195],0\n"
"s_mul_i32 s92,s35,20480\n"
MU"v36,s55,v36\n"
MU"v37,s55,v37\n"
MF"v[136:139],a[24:27],v[196:199],v[136:139]\n"
SA"s92,s92,s93\n"
RD"v[216:219],v242 offset:14336\n"
MU"v38,s55,v38\n"
MF"v[140:143],a[28:31],v[196:199],0\n"
SA"s92,s92,2770944\n"
MU"v39,s55,v39\n"
MU"v68,s55,v68\n"
MF"v[140:143],a[12:15],v[192:195],v[140:143]\n"
MU"v69,s55,v69\n"
MU"v70,s55,v70\n"
MF"v[144:147],a[0:3],v[200:203],0\n"
WT"lgkmcnt(1)\n"
MU"v71,s55,v71\n"
PK"v36,v36,v37\n"
MF"v[144:147],a[16:19],v[204:207],v[144:147]\n"
RD"v[220:223],v243 offset:14336\n"
PK"v37,v38,v39\n"
MF"v[148:151],a[20:23],v[204:207],0\n"
PK"v38,v68,v69\n"
PK"v39,v70,v71\n"
MF"v[148:151],a[4:7],v[200:203],v[148:151]\n"
MF"v[152:155],a[8:11],v[200:203],0\n"
MF"v[152:155],a[24:27],v[204:207],v[152:155]\n"
RD"v[192:195],v242 offset:32768\n"
MU"v44,s55,v44\n"
MF"v[156:159],a[28:31],v[204:207],0\n"
MU"v45,s55,v45\n"
MU"v46,s55,v46\n"
MF"v[156:159],a[12:15],v[200:203],v[156:159]\n"
MU"v47,s55,v47\n"
MU"v76,s55,v76\n"
MF"v[128:131],a[32:35],v[208:211],v[128:131]\n"
MU"v77,s55,v77\n"
MU"v78,s55,v78\n"
MF"v[128:131],a[48:51],v[212:215],v[128:131]\n"
RD"v[196:199],v243 offset:32768\n"
MU"v79,s55,v79\n"
MF"v[132:135],a[52:55],v[212:215],v[132:135]\n"
PK"v44,v44,v45\n"
PK"v45,v46,v47\n"
MF"v[132:135],a[36:39],v[208:211],v[132:135]\n"
PK"v46,v76,v77\n"
PK"v47,v78,v79\n"
MF"v[136:139],a[40:43],v[208:211],v[136:139]\n"
SA"m0,s36,0\n"
RD"v[200:203],v242 offset:34816\n"
SA"s48,s60,s61\n"
MF"v[136:139],a[56:59],v[212:215],v[136:139]\n"
MF"v[140:143],a[60:63],v[212:215],v[140:143]\n"
BL"v238,s[20:23],s48 offen offset:0 lds\n"
MF"v[140:143],a[44:47],v[208:211],v[140:143]\n"
WT"lgkmcnt(2)\n"
MU"v52,s55,v52\n"
MU"v53,s55,v53\n"
MF"v[144:147],a[32:35],v[216:219],v[144:147]\n"
"s_mov_b32 s49,16384\n"
RD"v[204:207],v243 offset:34816\n"
MU"v54,s55,v54\n"
MF"v[144:147],a[48:51],v[220:223],v[144:147]\n"
BS"v[64:67],v250,s[8:11],s49 offen offset:0 sc1\n"
MF"v[148:151],a[52:55],v[220:223],v[148:151]\n"
MU"v55,s55,v55\n"
MU"v84,s55,v84\n"
MF"v[148:151],a[36:39],v[216:219],v[148:151]\n"
MU"v85,s55,v85\n"
MU"v86,s55,v86\n"
MF"v[152:155],a[40:43],v[216:219],v[152:155]\n"
RD"v[208:211],v242 offset:36864\n"
MU"v87,s55,v87\n"
MF"v[152:155],a[56:59],v[220:223],v[152:155]\n"
PK"v52,v52,v53\n"
PK"v53,v54,v55\n"
MF"v[156:159],a[60:63],v[220:223],v[156:159]\n"
BL"v239,s[20:23],s48 offen offset:1024 lds\n"
MF"v[156:159],a[44:47],v[216:219],v[156:159]\n"
PK"v54,v84,v85\n"
PK"v55,v86,v87\n"
MF"v[128:131],a[64:67],v[192:195],v[128:131]\n"
WT"lgkmcnt(1)\n"
RD"v[212:215],v243 offset:36864\n"
MF"v[128:131],a[80:83],v[196:199],v[128:131]\n"
MU"v60,s55,v60\n"
MU"v61,s55,v61\n"
MF"v[132:135],a[84:87],v[196:199],v[132:135]\n"
MU"v62,s55,v62\n"
MU"v63,s55,v63\n"
MF"v[132:135],a[68:71],v[192:195],v[132:135]\n"
RD"v[216:219],v242 offset:38912\n"
MU"v92,s55,v92\n"
MF"v[136:139],a[72:75],v[192:195],v[136:139]\n"
MU"v93,s55,v93\n"
MU"v94,s55,v94\n"
MF"v[136:139],a[88:91],v[196:199],v[136:139]\n"
MU"v95,s55,v95\n"
PK"v60,v60,v61\n"
MF"v[140:143],a[92:95],v[196:199],v[140:143]\n"
BL"v240,s[20:23],s48 offen offset:2048 lds\n"
MF"v[140:143],a[76:79],v[192:195],v[140:143]\n"
RD"v[220:223],v243 offset:38912\n"
PK"v61,v62,v63\n"
MF"v[144:147],a[64:67],v[200:203],v[144:147]\n"
PK"v62,v92,v93\n"
PK"v63,v94,v95\n"
MF"v[144:147],a[80:83],v[204:207],v[144:147]\n"
MF"v[148:151],a[84:87],v[204:207],v[148:151]\n"
MF"v[148:151],a[68:71],v[200:203],v[148:151]\n"
RD"v[192:195],v244 offset:8192\n"
MF"v[152:155],a[72:75],v[200:203],v[152:155]\n"
MF"v[152:155],a[88:91],v[204:207],v[152:155]\n"
MF"v[156:159],a[92:95],v[204:207],v[156:159]\n"
SA"s61,s61,524288\n"
BL"v241,s[20:23],s48 offen offset:3072 lds\n"
MF"v[156:159],a[76:79],v[200:203],v[156:159]\n"
WT"lgkmcnt(1)\n"
RD"v[196:199],v245 offset:8192\n"
MF"v[128:131],a[96:99],v[208:211],v[128:131]\n"
MF"v[128:131],a[112:115],v[212:215],v[128:131]\n"
MF"v[132:135],a[116:119],v[212:215],v[132:135]\n"
MF"v[132:135],a[100:103],v[208:211],v[132:135]\n"
RD"v[200:203],v244 offset:10240\n"
MF"v[136:139],a[104:107],v[208:211],v[136:139]\n"
MF"v[136:139],a[120:123],v[212:215],v[136:139]\n"
MF"v[140:143],a[124:127],v[212:215],v[140:143]\n"
RD"v[204:207],v245 offset:10240\n"
MF"v[140:143],a[108:111],v[208:211],v[140:143]\n"
MF"v[144:147],a[96:99],v[216:219],v[144:147]\n"
BS"v[72:75],v250,s[8:11],s49 offen offset:1024 sc1\n"
MF"v[144:147],a[112:115],v[220:223],v[144:147]\n"
MF"v[148:151],a[116:119],v[220:223],v[148:151]\n"
RD"v[208:211],v244 offset:12288\n"
MF"v[148:151],a[100:103],v[216:219],v[148:151]\n"
MF"v[152:155],a[104:107],v[216:219],v[152:155]\n"
SA"m0,s38,65536\n"
SA"s48,s60,s61\n"
MF"v[152:155],a[120:123],v[220:223],v[152:155]\n"
MF"v[156:159],a[124:127],v[220:223],v[156:159]\n"
RD"v[212:215],v245 offset:12288\n"
MF"v[156:159],a[108:111],v[216:219],v[156:159]\n"
WT"lgkmcnt(2)\n"
BL"v238,s[20:23],s48 offen offset:0 lds\n"
MF"v[128:131],a[128:131],v[192:195],v[128:131]\n"
MF"v[128:131],a[144:147],v[196:199],v[128:131]\n"
MF"v[132:135],a[148:151],v[196:199],v[132:135]\n"
RD"v[216:219],v244 offset:14336\n"
MF"v[132:135],a[132:135],v[192:195],v[132:135]\n"
MF"v[136:139],a[136:139],v[192:195],v[136:139]\n"
MF"v[136:139],a[152:155],v[196:199],v[136:139]\n"
RD"v[220:223],v245 offset:14336\n"
MF"v[140:143],a[156:159],v[196:199],v[140:143]\n"
BL"v239,s[20:23],s48 offen offset:1024 lds\n"
MF"v[140:143],a[140:143],v[192:195],v[140:143]\n"
MF"v[144:147],a[128:131],v[200:203],v[144:147]\n"
MF"v[144:147],a[144:147],v[204:207],v[144:147]\n"
RD"v[192:195],v244 offset:32768\n"
MF"v[148:151],a[148:151],v[204:207],v[148:151]\n"
MF"v[148:151],a[132:135],v[200:203],v[148:151]\n"
MF"v[152:155],a[136:139],v[200:203],v[152:155]\n"
MF"v[152:155],a[152:155],v[204:207],v[152:155]\n"
RD"v[196:199],v245 offset:32768\n"
MF"v[156:159],a[156:159],v[204:207],v[156:159]\n"
BL"v240,s[20:23],s48 offen offset:2048 lds\n"
MF"v[156:159],a[140:143],v[200:203],v[156:159]\n"
WT"lgkmcnt(2)\n"
MF"v[128:131],a[160:163],v[208:211],v[128:131]\n"
MF"v[128:131],a[176:179],v[212:215],v[128:131]\n"
RD"v[200:203],v244 offset:34816\n"
MF"v[132:135],a[180:183],v[212:215],v[132:135]\n"
MF"v[132:135],a[164:167],v[208:211],v[132:135]\n"
MF"v[136:139],a[168:171],v[208:211],v[136:139]\n"
MF"v[136:139],a[184:187],v[212:215],v[136:139]\n"
"s_mov_b32 s49,20480\n"
RD"v[204:207],v245 offset:34816\n"
MF"v[140:143],a[188:191],v[212:215],v[140:143]\n"
SA"s61,s61,524288\n"
BL"v241,s[20:23],s48 offen offset:3072 lds\n"
MF"v[140:143],a[172:175],v[208:211],v[140:143]\n"
"s_cmp_eq_u32 s61,5242880\n"
BS"v[80:83],v250,s[8:11],s49 offen offset:0 sc1\n"
MF"v[144:147],a[160:163],v[216:219],v[144:147]\n"
"s_cselect_b32 s61,0,s61\n"
RD"v[208:211],v244 offset:36864\n"
MF"v[144:147],a[176:179],v[220:223],v[144:147]\n"
MF"v[148:151],a[180:183],v[220:223],v[148:151]\n"
MF"v[148:151],a[164:167],v[216:219],v[148:151]\n"
MF"v[152:155],a[168:171],v[216:219],v[152:155]\n"
SA"m0,s38,114688\n"
RD"v[212:215],v245 offset:36864\n"
SA"s48,s60,s61\n"
MF"v[152:155],a[184:187],v[220:223],v[152:155]\n"
MF"v[156:159],a[188:191],v[220:223],v[156:159]\n"
BL"v238,s[20:23],s48 offen offset:0 lds\n"
MF"v[156:159],a[172:175],v[216:219],v[156:159]\n"
WT"lgkmcnt(1)\n"
MF"v[128:131],a[192:195],v[192:195],v[128:131]\n"
RD"v[216:219],v244 offset:38912\n"
MF"v[128:131],a[208:211],v[196:199],v[128:131]\n"
MF"v[132:135],a[212:215],v[196:199],v[132:135]\n"
MF"v[132:135],a[196:199],v[192:195],v[132:135]\n"
MF"v[136:139],a[200:203],v[192:195],v[136:139]\n"
RD"v[220:223],v245 offset:38912\n"
MF"v[136:139],a[216:219],v[196:199],v[136:139]\n"
MF"v[140:143],a[220:223],v[196:199],v[140:143]\n"
BL"v239,s[20:23],s48 offen offset:1024 lds\n"
MF"v[140:143],a[204:207],v[192:195],v[140:143]\n"
WT"vmcnt(13)\n"
BS"v[100:103],v252,s[8:11],s92 offen sc1\n"
MF"v[144:147],a[192:195],v[200:203],v[144:147]\n"
MF"v[144:147],a[208:211],v[204:207],v[144:147]\n"
MF"v[148:151],a[212:215],v[204:207],v[148:151]\n"
MF"v[148:151],a[196:199],v[200:203],v[148:151]\n"
MF"v[152:155],a[200:203],v[200:203],v[152:155]\n"
MF"v[152:155],a[216:219],v[204:207],v[152:155]\n"
WT"lgkmcnt(0)\n"
"s_barrier\n"
MF"v[156:159],a[220:223],v[204:207],v[156:159]\n"
RD"v[192:195],v242 offset:16384\n"
MF"v[156:159],a[204:207],v[200:203],v[156:159]\n"
BL"v240,s[20:23],s48 offen offset:2048 lds\n"
MF"v[128:131],a[224:227],v[208:211],v[128:131]\n"
MF"v[128:131],a[240:243],v[212:215],v[128:131]\n"
MF"v[132:135],a[244:247],v[212:215],v[132:135]\n"
RD"v[196:199],v243 offset:16384\n"
MF"v[132:135],a[228:231],v[208:211],v[132:135]\n"
MF"v[136:139],a[232:235],v[208:211],v[136:139]\n"
MF"v[136:139],a[248:251],v[212:215],v[136:139]\n"
MF"v[140:143],a[252:255],v[212:215],v[140:143]\n"
SA"s61,s61,524288\n"
RD"v[200:203],v242 offset:18432\n"
MF"v[140:143],a[236:239],v[208:211],v[140:143]\n"
BS"v[88:91],v250,s[8:11],s49 offen offset:1024 sc1\n"
MF"v[144:147],a[224:227],v[216:219],v[144:147]\n"
"s_lshl_b32 s50,s34,3\n"
BL"v241,s[20:23],s48 offen offset:3072 lds\n"
MF"v[144:147],a[240:243],v[220:223],v[144:147]\n"
SA"s51,s50,s30\n"
MF"v[148:151],a[244:247],v[220:223],v[148:151]\n"
"s_mul_i32 s51,s51,20480\n"
RD"v[204:207],v243 offset:18432\n"
MF"v[148:151],a[228:231],v[216:219],v[148:151]\n"
SA"s56,s51,65536\n"
MF"v[152:155],a[232:235],v[216:219],v[152:155]\n"
SA"m0,s36,16384\n"
SA"s48,s60,s61\n"
SA"s57,s56,81920\n"
MF"v[152:155],a[248:251],v[220:223],v[152:155]\n"
"s_sub_u32 s51,s30,s50\n"
MF"v[156:159],a[252:255],v[220:223],v[156:159]\n"
SA"s51,s51,120\n"
RD"v[208:211],v242 offset:20480\n"
MF"v[156:159],a[236:239],v[216:219],v[156:159]\n"
WT"lgkmcnt(2)\n"
BL"v238,s[20:23],s48 offen offset:0 lds\n"
MF"v[0:3],a[0:3],v[192:195],0\n"
"s_mul_i32 s51,s51,20480\n"
SA"s58,s51,65536\n"
"s_lshl_b32 s99,s99,12\n"
MF"v[0:3],a[16:19],v[196:199],v[0:3]\n"
SA"s59,s58,81920\n"
RD"v[212:215],v243 offset:20480\n"
MF"v[8:11],a[20:23],v[196:199],0\n"
"v_add_u32 v235,s99,v224\n"
MF"v[8:11],a[4:7],v[192:195],v[8:11]\n"
MU"v128,s55,v128\n"
MU"v129,s55,v129\n"
MF"v[16:19],a[8:11],v[192:195],0\n"
MU"v130,s55,v130\n"
MU"v131,s55,v131\n"
MF"v[16:19],a[24:27],v[196:199],v[16:19]\n"
RD"v[216:219],v242 offset:22528\n"
MU"v132,s55,v132\n"
MF"v[24:27],a[28:31],v[196:199],0\n"
BL"v239,s[20:23],s48 offen offset:1024 lds\n"
MF"v[24:27],a[12:15],v[192:195],v[24:27]\n"
MU"v133,s55,v133\n"
MU"v134,s55,v134\n"
MF"v[32:35],a[0:3],v[200:203],0\n"
WT"lgkmcnt(1)\n"
MU"v135,s55,v135\n"
PK"v64,v128,v129\n"
MF"v[32:35],a[16:19],v[204:207],v[32:35]\n"
RD"v[220:223],v243 offset:22528\n"
PK"v65,v130,v131\n"
MF"v[40:43],a[20:23],v[204:207],0\n"
PK"v66,v132,v133\n"
PK"v67,v134,v135\n"
MF"v[40:43],a[4:7],v[200:203],v[40:43]\n"
MF"v[48:51],a[8:11],v[200:203],0\n"
MF"v[48:51],a[24:27],v[204:207],v[48:51]\n"
RD"v[192:195],v242 offset:40960\n"
MU"v136,s55,v136\n"
MF"v[56:59],a[28:31],v[204:207],0\n"
BL"v240,s[20:23],s48 offen offset:2048 lds\n"
MF"v[56:59],a[12:15],v[200:203],v[56:59]\n"
MU"v137,s55,v137\n"
MU"v138,s55,v138\n"
MF"v[0:3],a[32:35],v[208:211],v[0:3]\n"
BL"a[0:3],v235,s[8:11],s56 offen offset:0 sc1\n"
MF"v[0:3],a[48:51],v[212:215],v[0:3]\n"
RD"v[196:199],v243 offset:40960\n"
MU"v139,s55,v139\n"
MF"v[8:11],a[52:55],v[212:215],v[8:11]\n"
MU"v140,s55,v140\n"
MU"v141,s55,v141\n"
MF"v[8:11],a[36:39],v[208:211],v[8:11]\n"
"s_mov_b32 s49,24576\n"
MU"v142,s55,v142\n"
MU"v143,s55,v143\n"
MF"v[16:19],a[40:43],v[208:211],v[16:19]\n"
RD"v[200:203],v242 offset:43008\n"
PK"v68,v136,v137\n"
MF"v[16:19],a[56:59],v[212:215],v[16:19]\n"
BS"v[96:99],v250,s[8:11],s49 offen offset:0 sc1\n"
MF"v[24:27],a[60:63],v[212:215],v[24:27]\n"
SA"s61,s61,524288\n"
BL"v241,s[20:23],s48 offen offset:3072 lds\n"
MF"v[24:27],a[44:47],v[208:211],v[24:27]\n"
WT"lgkmcnt(2)\n"
BL"a[4:7],v235,s[8:11],s57 offen offset:0 sc1\n"
MF"v[32:35],a[32:35],v[216:219],v[32:35]\n"
"s_cmp_eq_u32 s61,5242880\n"
RD"v[204:207],v243 offset:43008\n"
PK"v69,v138,v139\n"
MF"v[32:35],a[48:51],v[220:223],v[32:35]\n"
"s_cselect_b32 s61,0,s61\n"
PK"v70,v140,v141\n"
PK"v71,v142,v143\n"
MF"v[40:43],a[52:55],v[220:223],v[40:43]\n"
MU"v144,s55,v144\n"
MU"v145,s55,v145\n"
MF"v[40:43],a[36:39],v[216:219],v[40:43]\n"
MU"v146,s55,v146\n"
MU"v147,s55,v147\n"
MF"v[48:51],a[40:43],v[216:219],v[48:51]\n"
RD"v[208:211],v242 offset:45056\n"
MU"v148,s55,v148\n"
MF"v[48:51],a[56:59],v[220:223],v[48:51]\n"
BL"a[8:11],v235,s[8:11],s58 offen offset:0 sc1\n"
MF"v[56:59],a[60:63],v[220:223],v[56:59]\n"
MU"v149,s55,v149\n"
MU"v150,s55,v150\n"
MF"v[56:59],a[44:47],v[216:219],v[56:59]\n"
MU"v151,s55,v151\n"
PK"v72,v144,v145\n"
MF"v[0:3],a[64:67],v[192:195],v[0:3]\n"
WT"lgkmcnt(1)\n"
RD"v[212:215],v243 offset:45056\n"
PK"v73,v146,v147\n"
MF"v[0:3],a[80:83],v[196:199],v[0:3]\n"
PK"v74,v148,v149\n"
PK"v75,v150,v151\n"
MF"v[8:11],a[84:87],v[196:199],v[8:11]\n"
BL"a[12:15],v235,s[8:11],s59 offen offset:0 sc1\n"
MF"v[8:11],a[68:71],v[192:195],v[8:11]\n"
RD"v[216:219],v242 offset:47104\n"
MU"v152,s55,v152\n"
MF"v[16:19],a[72:75],v[192:195],v[16:19]\n"
MU"v153,s55,v153\n"
MU"v154,s55,v154\n"
MF"v[16:19],a[88:91],v[196:199],v[16:19]\n"
MU"v155,s55,v155\n"
MU"v156,s55,v156\n"
MF"v[24:27],a[92:95],v[196:199],v[24:27]\n"
MU"v157,s55,v157\n"
MU"v158,s55,v158\n"
MF"v[24:27],a[76:79],v[192:195],v[24:27]\n"
RD"v[220:223],v243 offset:47104\n"
MU"v159,s55,v159\n"
MF"v[32:35],a[64:67],v[200:203],v[32:35]\n"
BL"a[16:19],v235,s[8:11],s56 offen offset:1024 sc1\n"
MF"v[32:35],a[80:83],v[204:207],v[32:35]\n"
PK"v76,v152,v153\n"
PK"v77,v154,v155\n"
MF"v[40:43],a[84:87],v[204:207],v[40:43]\n"
PK"v78,v156,v157\n"
PK"v79,v158,v159\n"
MF"v[40:43],a[68:71],v[200:203],v[40:43]\n"
RD"v[192:195],v244 offset:16384\n"
MF"v[48:51],a[72:75],v[200:203],v[48:51]\n"
MF"v[48:51],a[88:91],v[204:207],v[48:51]\n"
MF"v[56:59],a[92:95],v[204:207],v[56:59]\n"
BL"a[20:23],v235,s[8:11],s57 offen offset:1024 sc1\n"
MF"v[56:59],a[76:79],v[200:203],v[56:59]\n"
WT"lgkmcnt(1)\n"
RD"v[196:199],v245 offset:16384\n"
MF"v[0:3],a[96:99],v[208:211],v[0:3]\n"
MF"v[0:3],a[112:115],v[212:215],v[0:3]\n"
MF"v[8:11],a[116:119],v[212:215],v[8:11]\n"
MF"v[8:11],a[100:103],v[208:211],v[8:11]\n"
RD"v[200:203],v244 offset:18432\n"
MF"v[16:19],a[104:107],v[208:211],v[16:19]\n"
BS"v[104:107],v250,s[8:11],s49 offen offset:1024 sc1\n"
MF"v[16:19],a[120:123],v[212:215],v[16:19]\n"
BL"a[24:27],v235,s[8:11],s58 offen offset:1024 sc1\n"
MF"v[24:27],a[124:127],v[212:215],v[24:27]\n"
RD"v[204:207],v245 offset:18432\n"
MF"v[24:27],a[108:111],v[208:211],v[24:27]\n"
MF"v[32:35],a[96:99],v[216:219],v[32:35]\n"
MF"v[32:35],a[112:115],v[220:223],v[32:35]\n"
MF"v[40:43],a[116:119],v[220:223],v[40:43]\n"
RD"v[208:211],v244 offset:20480\n"
MF"v[40:43],a[100:103],v[216:219],v[40:43]\n"
BL"a[28:31],v235,s[8:11],s59 offen offset:1024 sc1\n"
MF"v[48:51],a[104:107],v[216:219],v[48:51]\n"
MF"v[48:51],a[120:123],v[220:223],v[48:51]\n"
MF"v[56:59],a[124:127],v[220:223],v[56:59]\n"
RD"v[212:215],v245 offset:20480\n"
MF"v[56:59],a[108:111],v[216:219],v[56:59]\n"
WT"lgkmcnt(2)\n"
MF"v[0:3],a[128:131],v[192:195],v[0:3]\n"
BL"a[32:35],v235,s[8:11],s56 offen offset:2048 sc1\n"
MF"v[0:3],a[144:147],v[196:199],v[0:3]\n"
MF"v[8:11],a[148:151],v[196:199],v[8:11]\n"
RD"v[216:219],v244 offset:22528\n"
MF"v[8:11],a[132:135],v[192:195],v[8:11]\n"
MF"v[16:19],a[136:139],v[192:195],v[16:19]\n"
MF"v[16:19],a[152:155],v[196:199],v[16:19]\n"
RD"v[220:223],v245 offset:22528\n"
MF"v[24:27],a[156:159],v[196:199],v[24:27]\n"
BL"a[36:39],v235,s[8:11],s57 offen offset:2048 sc1\n"
MF"v[24:27],a[140:143],v[192:195],v[24:27]\n"
MF"v[32:35],a[128:131],v[200:203],v[32:35]\n"
MF"v[32:35],a[144:147],v[204:207],v[32:35]\n"
RD"v[192:195],v244 offset:40960\n"
MF"v[40:43],a[148:151],v[204:207],v[40:43]\n"
MF"v[40:43],a[132:135],v[200:203],v[40:43]\n"
MF"v[48:51],a[136:139],v[200:203],v[48:51]\n"
BL"a[40:43],v235,s[8:11],s58 offen offset:2048 sc1\n"
MF"v[48:51],a[152:155],v[204:207],v[48:51]\n"
RD"v[196:199],v245 offset:40960\n"
MF"v[56:59],a[156:159],v[204:207],v[56:59]\n"
MF"v[56:59],a[140:143],v[200:203],v[56:59]\n"
WT"lgkmcnt(2)\n"
MF"v[0:3],a[160:163],v[208:211],v[0:3]\n"
"s_sub_u32 s53,s37,65536\n"
MF"v[0:3],a[176:179],v[212:215],v[0:3]\n"
"s_mov_b32 s49,28672\n"
RD"v[200:203],v244 offset:43008\n"
MF"v[8:11],a[180:183],v[212:215],v[8:11]\n"
BL"a[44:47],v235,s[8:11],s59 offen offset:2048 sc1\n"
MF"v[8:11],a[164:167],v[208:211],v[8:11]\n"
BS"v[112:115],v250,s[8:11],s49 offen offset:0 sc1\n"
MF"v[16:19],a[168:171],v[208:211],v[16:19]\n"
"v_subrev_u32 v246,s53,v246\n"
"v_subrev_u32 v247,s53,v247\n"
MF"v[16:19],a[184:187],v[212:215],v[16:19]\n"
RD"v[204:207],v245 offset:43008\n"
"v_subrev_u32 v248,s53,v248\n"
MF"v[24:27],a[188:191],v[212:215],v[24:27]\n"
"v_subrev_u32 v249,s53,v249\n"
"v_subrev_u32 v236,65536,v246\n"
MF"v[24:27],a[172:175],v[208:211],v[24:27]\n"
"v_subrev_u32 v237,65536,v247\n"
"v_subrev_u32 v228,65536,v248\n"
MF"v[32:35],a[160:163],v[216:219],v[32:35]\n"
RD"v[208:211],v244 offset:45056\n"
"v_subrev_u32 v229,65536,v249\n"
MF"v[32:35],a[176:179],v[220:223],v[32:35]\n"
BL"a[48:51],v235,s[8:11],s56 offen offset:3072 sc1\n"
MF"v[40:43],a[180:183],v[220:223],v[40:43]\n"
MF"v[40:43],a[164:167],v[216:219],v[40:43]\n"
MF"v[48:51],a[168:171],v[216:219],v[48:51]\n"
RD"v[212:215],v245 offset:45056\n"
MF"v[48:51],a[184:187],v[220:223],v[48:51]\n"
MF"v[56:59],a[188:191],v[220:223],v[56:59]\n"
BL"a[52:55],v235,s[8:11],s57 offen offset:3072 sc1\n"
MF"v[56:59],a[172:175],v[216:219],v[56:59]\n"
WT"lgkmcnt(1)\n"
MF"v[0:3],a[192:195],v[192:195],v[0:3]\n"
RD"v[216:219],v244 offset:47104\n"
MF"v[0:3],a[208:211],v[196:199],v[0:3]\n"
MF"v[8:11],a[212:215],v[196:199],v[8:11]\n"
MF"v[8:11],a[196:199],v[192:195],v[8:11]\n"
MF"v[16:19],a[200:203],v[192:195],v[16:19]\n"
RD"v[220:223],v245 offset:47104\n"
MF"v[16:19],a[216:219],v[196:199],v[16:19]\n"
BL"a[56:59],v235,s[8:11],s58 offen offset:3072 sc1\n"
MF"v[24:27],a[220:223],v[196:199],v[24:27]\n"
MF"v[24:27],a[204:207],v[192:195],v[24:27]\n"
MF"v[32:35],a[192:195],v[200:203],v[32:35]\n"
SA"s99,s99,4096\n"
MF"v[32:35],a[208:211],v[204:207],v[32:35]\n"
"s_cmp_eq_u32 s99,20480\n"
"s_cselect_b32 s99,0,s99\n"
MF"v[40:43],a[212:215],v[204:207],v[40:43]\n"
BL"a[60:63],v235,s[8:11],s59 offen offset:3072 sc1\n"
MF"v[40:43],a[196:199],v[200:203],v[40:43]\n"
MF"v[48:51],a[200:203],v[200:203],v[48:51]\n"
MF"v[48:51],a[216:219],v[204:207],v[48:51]\n"
WT"lgkmcnt(0)\n"
"s_barrier\n"
MF"v[56:59],a[220:223],v[204:207],v[56:59]\n"
RT"a[128:129],v236 offset:49152\n"
MF"v[56:59],a[204:207],v[200:203],v[56:59]\n"
"v_add_u32 v235,s99,v224\n"
MF"v[0:3],a[224:227],v[208:211],v[0:3]\n"
SA"m0,s38,73728\n"
RT"a[130:131],v236 offset:49664\n"
SA"s48,s60,s61\n"
MF"v[0:3],a[240:243],v[212:215],v[0:3]\n"
BL"a[64:67],v235,s[8:11],s56 offen offset:0 sc1\n"
MF"v[8:11],a[244:247],v[212:215],v[8:11]\n"
RT"a[132:133],v236 offset:57344\n"
"v_add_u32 v242,49152,v246\n"
MF"v[8:11],a[228:231],v[208:211],v[8:11]\n"
BS"v[120:123],v250,s[8:11],s49 offen offset:1024 sc1\n"
MF"v[16:19],a[232:235],v[208:211],v[16:19]\n"
BL"v238,s[20:23],s48 offen offset:0 lds\n"
MF"v[16:19],a[248:251],v[212:215],v[16:19]\n"
RT"a[134:135],v236 offset:57856\n"
"v_add_u32 v243,49152,v247\n"
MF"v[24:27],a[252:255],v[212:215],v[24:27]\n"
RT"a[136:137],v237 offset:49152\n"
"v_add_u32 v244,49152,v248\n"
MF"v[24:27],a[236:239],v[208:211],v[24:27]\n"
BL"a[68:71],v235,s[8:11],s57 offen offset:0 sc1\n"
MF"v[32:35],a[224:227],v[216:219],v[32:35]\n"
RT"a[138:139],v237 offset:49664\n"
"v_add_u32 v245,49152,v249\n"
MF"v[32:35],a[240:243],v[220:223],v[32:35]\n"
MF"v[40:43],a[244:247],v[220:223],v[40:43]\n"
RT"a[140:141],v237 offset:57344\n"
MF"v[40:43],a[228:231],v[216:219],v[40:43]\n"
BL"v239,s[20:23],s48 offen offset:1024 lds\n"
MF"v[48:51],a[232:235],v[216:219],v[48:51]\n"
RT"a[142:143],v237 offset:57856\n"
MF"v[48:51],a[248:251],v[220:223],v[48:51]\n"
BL"a[72:75],v235,s[8:11],s58 offen offset:0 sc1\n"
MF"v[56:59],a[252:255],v[220:223],v[56:59]\n"
RT"a[144:145],v228 offset:49152\n"
MF"v[56:59],a[236:239],v[216:219],v[56:59]\n"
WT"vmcnt(25) lgkmcnt(3)\n"
MF"v[80:83],a[128:131],a[0:3],0\n"
WT"vmcnt(19)\n"
RT"a[146:147],v228 offset:49664\n"
MF"v[80:83],a[132:135],a[16:19],v[80:83]\n"
WT"vmcnt(18)\n"
MF"v[88:91],a[132:135],a[20:23],0\n"
RT"a[148:149],v228 offset:57344\n"
MF"v[88:91],a[128:131],a[4:7],v[88:91]\n"
BL"v240,s[20:23],s48 offen offset:2048 lds\n"
MF"v[96:99],a[128:131],a[8:11],0\n"
WT"vmcnt(17)\n"
BL"a[76:79],v235,s[8:11],s59 offen offset:0 sc1\n"
MF"v[96:99],a[132:135],a[24:27],v[96:99]\n"
WT"vmcnt(17)\n"
RT"a[150:151],v228 offset:57856\n"
RT"a[152:153],v229 offset:49152\n"
MF"v[104:107],a[132:135],a[28:31],0\n"
MU"v0,s55,v0\n"
MU"v1,s55,v1\n"
MF"v[104:107],a[128:131],a[12:15],v[104:107]\n"
RT"a[154:155],v229 offset:49664\n"
MU"v2,s55,v2\n"
MF"v[84:87],a[136:139],a[0:3],0\n"
WT"lgkmcnt(4)\n"
MU"v3,s55,v3\n"
MU"v8,s55,v8\n"
MF"v[84:87],a[140:143],a[16:19],v[84:87]\n"
RT"a[156:157],v229 offset:57344\n"
MU"v9,s55,v9\n"
MF"v[92:95],a[140:143],a[20:23],0\n"
SA"s61,s61,524288\n"
BL"a[80:83],v235,s[8:11],s56 offen offset:1024 sc1\n"
MF"v[92:95],a[136:139],a[4:7],v[92:95]\n"
BL"v241,s[20:23],s48 offen offset:3072 lds\n"
MF"v[100:103],a[136:139],a[8:11],0\n"
RT"a[158:159],v229 offset:57856\n"
MU"v10,s55,v10\n"
MF"v[100:103],a[140:143],a[24:27],v[100:103]\n"
RT"a[160:161],v236 offset:53248\n"
MU"v11,s55,v11\n"
MF"v[108:111],a[140:143],a[28:31],0\n"
PK"v0,v0,v1\n"
PK"v1,v2,v3\n"
MF"v[108:111],a[136:139],a[12:15],v[108:111]\n"
RT"a[162:163],v236 offset:53760\n"
PK"v2,v8,v9\n"
MF"v[112:115],a[144:147],a[0:3],0\n"
WT"lgkmcnt(3)\n"
BL"a[84:87],v235,s[8:11],s57 offen offset:1024 sc1\n"
MF"v[112:115],a[148:151],a[16:19],v[112:115]\n"
RT"a[164:165],v236 offset:61440\n"
PK"v3,v10,v11\n"
MF"v[120:123],a[148:151],a[20:23],0\n"
MU"v16,s55,v16\n"
MU"v17,s55,v17\n"
MF"v[120:123],a[144:147],a[4:7],v[120:123]\n"
RT"a[166:167],v236 offset:61952\n"
MU"v18,s55,v18\n"
MF"v[128:131],a[144:147],a[8:11],0\n"
MU"v19,s55,v19\n"
MU"v24,s55,v24\n"
MF"v[128:131],a[148:151],a[24:27],v[128:131]\n"
RT"a[168:169],v237 offset:53248\n"
MU"v25,s55,v25\n"
MF"v[136:139],a[148:151],a[28:31],0\n"
MU"v26,s55,v26\n"
MU"v27,s55,v27\n"
MF"v[136:139],a[144:147],a[12:15],v[136:139]\n"
RT"a[170:171],v237 offset:53760\n"
PK"v16,v16,v17\n"
MF"v[116:119],a[152:155],a[0:3],0\n"
WT"lgkmcnt(3)\n"
BL"a[88:91],v235,s[8:11],s58 offen offset:1024 sc1\n"
MF"v[116:119],a[156:159],a[16:19],v[116:119]\n"
"s_mov_b32 s49,262144\n"
RT"a[172:173],v237 offset:61440\n"
PK"v17,v18,v19\n"
MF"v[124:127],a[156:159],a[20:23],0\n"
RT"a[174:175],v237 offset:61952\n"
PK"v18,v24,v25\n"
MF"v[124:127],a[152:155],a[4:7],v[124:127]\n"
BS"v[4:7],v250,s[8:11],s49 offen offset:0 sc1\n"
MF"v[132:135],a[152:155],a[8:11],0\n"
RT"a[176:177],v228 offset:53248\n"
PK"v19,v26,v27\n"
MF"v[132:135],a[156:159],a[24:27],v[132:135]\n"
MU"v32,s55,v32\n"
MU"v33,s55,v33\n"
MF"v[140:143],a[156:159],a[28:31],0\n"
RT"a[178:179],v228 offset:53760\n"
MU"v34,s55,v34\n"
MF"v[140:143],a[152:155],a[12:15],v[140:143]\n"
BL"a[92:95],v235,s[8:11],s59 offen offset:1024 sc1\n"
MF"v[144:147],a[160:163],a[0:3],0\n"
WT"lgkmcnt(3)\n"
RT"a[180:181],v228 offset:61440\n"
MU"v35,s55,v35\n"
MF"v[144:147],a[164:167],a[16:19],v[144:147]\n"
MU"v40,s55,v40\n"
MU"v41,s55,v41\n"
MF"v[152:155],a[164:167],a[20:23],0\n"
SA"m0,s38,122880\n"
RT"a[182:183],v228 offset:61952\n"
MU"v42,s55,v42\n"
MF"v[152:155],a[160:163],a[4:7],v[152:155]\n"
SA"s48,s60,s61\n"
MU"v43,s55,v43\n"
PK"v32,v32,v33\n"
MF"v[160:163],a[160:163],a[8:11],0\n"
"s_mov_b32 s88,4096\n"
RT"a[184:185],v229 offset:53248\n"
PK"v33,v34,v35\n"
MF"v[160:163],a[164:167],a[24:27],v[160:163]\n"
BL"v238,s[20:23],s48 offen offset:0 lds\n"
MF"v[168:171],a[164:167],a[28:31],0\n"
BL"a[96:99],v235,s[8:11],s56 offen offset:2048 sc1\n"
MF"v[168:171],a[160:163],a[12:15],v[168:171]\n"
RT"a[186:187],v229 offset:53760\n"
PK"v34,v40,v41\n"
MF"v[148:151],a[168:171],a[0:3],0\n"
WT"lgkmcnt(3)\n"
RT"a[188:189],v229 offset:61440\n"
PK"v35,v42,v43\n"
MF"v[148:151],a[172:175],a[16:19],v[148:151]\n"
MU"v48,s55,v48\n"
MU"v49,s55,v49\n"
MF"v[156:159],a[172:175],a[20:23],0\n"
RT"a[190:191],v229 offset:61952\n"
MU"v50,s55,v50\n"
MF"v[156:159],a[168:171],a[4:7],v[156:159]\n"
RT"a[128:129],v236 offset:32768\n"
MU"v51,s55,v51\n"
MF"v[164:167],a[168:171],a[8:11],0\n"
BL"v239,s[20:23],s48 offen offset:1024 lds\n"
MF"v[164:167],a[172:175],a[24:27],v[164:167]\n"
BL"a[100:103],v235,s[8:11],s57 offen offset:2048 sc1\n"
MF"v[172:175],a[172:175],a[28:31],0\n"
RT"a[130:131],v236 offset:33280\n"
MU"v56,s55,v56\n"
MF"v[172:175],a[168:171],a[12:15],v[172:175]\n"
RT"a[132:133],v236 offset:40960\n"
MU"v57,s55,v57\n"
MF"v[176:179],a[176:179],a[0:3],0\n"
WT"lgkmcnt(4)\n"
MU"v58,s55,v58\n"
MU"v59,s55,v59\n"
MF"v[176:179],a[180:183],a[16:19],v[176:179]\n"
RT"a[134:135],v236 offset:41472\n"
PK"v48,v48,v49\n"
MF"v[184:187],a[180:183],a[20:23],0\n"
PK"v49,v50,v51\n"
PK"v50,v56,v57\n"
MF"v[184:187],a[176:179],a[4:7],v[184:187]\n"
RT"a[136:137],v237 offset:32768\n"
PK"v51,v58,v59\n"
MF"v[192:195],a[176:179],a[8:11],0\n"
BL"a[104:107],v235,s[8:11],s58 offen offset:2048 sc1\n"
MF"v[192:195],a[180:183],a[24:27],v[192:195]\n"
BL"v240,s[20:23],s48 offen offset:2048 lds\n"
MF"v[200:203],a[180:183],a[28:31],0\n"
RT"a[138:139],v237 offset:33280\n"
MF"v[200:203],a[176:179],a[12:15],v[200:203]\n"
RT"a[140:141],v237 offset:40960\n"
MF"v[180:183],a[184:187],a[0:3],0\n"
WT"lgkmcnt(3)\n"
MF"v[180:183],a[188:191],a[16:19],v[180:183]\n"
RT"a[142:143],v237 offset:41472\n"
MF"v[188:191],a[188:191],a[20:23],0\n"
BL"a[108:111],v235,s[8:11],s59 offen offset:2048 sc1\n"
MF"v[188:191],a[184:187],a[4:7],v[188:191]\n"
RT"a[144:145],v228 offset:32768\n"
MF"v[196:199],a[184:187],a[8:11],0\n"
SA"s61,s61,524288\n"
BS"v[12:15],v250,s[8:11],s49 offen offset:1024 sc1\n"
MF"v[196:199],a[188:191],a[24:27],v[196:199]\n"
"s_cmp_eq_u32 s61,5242880\n"
BL"v241,s[20:23],s48 offen offset:3072 lds\n"
MF"v[204:207],a[188:191],a[28:31],0\n"
"s_cselect_b32 s61,0,s61\n"
RT"a[146:147],v228 offset:33280\n"
MF"v[204:207],a[184:187],a[12:15],v[204:207]\n"
WT"vmcnt(31)\n"
RT"a[148:149],v228 offset:40960\n"
MF"v[80:83],a[128:131],a[32:35],v[80:83]\n"
WT"vmcnt(26)\n"
MF"v[80:83],a[132:135],a[48:51],v[80:83]\n"
WT"vmcnt(25)\n"
RT"a[150:151],v228 offset:41472\n"
MF"v[88:91],a[132:135],a[52:55],v[88:91]\n"
BL"a[112:115],v235,s[8:11],s56 offen offset:3072 sc1\n"
MF"v[88:91],a[128:131],a[36:39],v[88:91]\n"
RT"a[152:153],v229 offset:32768\n"
MF"v[96:99],a[128:131],a[40:43],v[96:99]\n"
WT"vmcnt(25)\n"
RT"a[154:155],v229 offset:33280\n"
MF"v[96:99],a[132:135],a[56:59],v[96:99]\n"
WT"vmcnt(24)\n"
MF"v[104:107],a[132:135],a[60:63],v[104:107]\n"
RT"a[156:157],v229 offset:40960\n"
MF"v[104:107],a[128:131],a[44:47],v[104:107]\n"
WT"lgkmcnt(4)\n"
MF"v[84:87],a[136:139],a[32:35],v[84:87]\n"
RT"a[158:159],v229 offset:41472\n"
MF"v[84:87],a[140:143],a[48:51],v[84:87]\n"
BL"a[116:119],v235,s[8:11],s57 offen offset:3072 sc1\n"
MF"v[92:95],a[140:143],a[52:55],v[92:95]\n"
RT"a[160:161],v236 offset:36864\n"
MF"v[92:95],a[136:139],a[36:39],v[92:95]\n"
MF"v[100:103],a[136:139],a[40:43],v[100:103]\n"
RT"a[162:163],v236 offset:37376\n"
MF"v[100:103],a[140:143],a[56:59],v[100:103]\n"
MF"v[108:111],a[140:143],a[60:63],v[108:111]\n"
RT"a[164:165],v236 offset:45056\n"
MF"v[108:111],a[136:139],a[44:47],v[108:111]\n"
BL"a[120:123],v235,s[8:11],s58 offen offset:3072 sc1\n"
MF"v[112:115],a[144:147],a[32:35],v[112:115]\n"
WT"lgkmcnt(3)\n"
RT"a[166:167],v236 offset:45568\n"
MF"v[112:115],a[148:151],a[48:51],v[112:115]\n"
MF"v[120:123],a[148:151],a[52:55],v[120:123]\n"
RT"a[168:169],v237 offset:36864\n"
MF"v[120:123],a[144:147],a[36:39],v[120:123]\n"
MF"v[128:131],a[144:147],a[40:43],v[128:131]\n"
RT"a[170:171],v237 offset:37376\n"
MF"v[128:131],a[148:151],a[56:59],v[128:131]\n"
SA"s99,s99,4096\n"
RT"a[172:173],v237 offset:45056\n"
MF"v[136:139],a[148:151],a[60:63],v[136:139]\n"
"s_cmp_eq_u32 s99,20480\n"
BL"a[124:127],v235,s[8:11],s59 offen offset:3072 sc1\n"
MF"v[136:139],a[144:147],a[44:47],v[136:139]\n"
"s_cselect_b32 s99,0,s99\n"
RT"a[174:175],v237 offset:45568\n"
MF"v[116:119],a[152:155],a[32:35],v[116:119]\n"
MF"v[116:119],a[156:159],a[48:51],v[116:119]\n"
RT"a[176:177],v228 offset:36864\n"
MF"v[124:127],a[156:159],a[52:55],v[124:127]\n"
MF"v[124:127],a[152:155],a[36:39],v[124:127]\n"
RT"a[178:179],v228 offset:37376\n"
MF"v[132:135],a[152:155],a[40:43],v[132:135]\n"
"s_mov_b32 s49,266240\n"
"v_add_u32 v235,s99,v224\n"
MF"v[132:135],a[156:159],a[56:59],v[132:135]\n"
RT"a[180:181],v228 offset:45056\n"
MF"v[140:143],a[156:159],a[60:63],v[140:143]\n"
BL"a[0:3],v235,s[8:11],s56 offen offset:0 sc1\n"
MF"v[140:143],a[152:155],a[44:47],v[140:143]\n"
WT"lgkmcnt(3)\n"
BS"v[20:23],v250,s[8:11],s49 offen offset:0 sc1\n"
MF"v[144:147],a[160:163],a[32:35],v[144:147]\n"
RT"a[182:183],v228 offset:45568\n"
MF"v[144:147],a[164:167],a[48:51],v[144:147]\n"
RT"a[184:185],v229 offset:36864\n"
MF"v[152:155],a[164:167],a[52:55],v[152:155]\n"
MF"v[152:155],a[160:163],a[36:39],v[152:155]\n"
RT"a[186:187],v229 offset:37376\n"
MF"v[160:163],a[160:163],a[40:43],v[160:163]\n"
BL"a[4:7],v235,s[8:11],s57 offen offset:0 sc1\n"
MF"v[160:163],a[164:167],a[56:59],v[160:163]\n"
RT"a[188:189],v229 offset:45056\n"
MF"v[168:171],a[164:167],a[60:63],v[168:171]\n"
MF"v[168:171],a[160:163],a[44:47],v[168:171]\n"
RT"a[190:191],v229 offset:45568\n"
MF"v[148:151],a[168:171],a[32:35],v[148:151]\n"
"s_lshl_b32 s50,s35,3\n"
SA"s50,s50,s34\n"
"s_lshl_b32 s50,s50,3\n"
MF"v[148:151],a[172:175],a[48:51],v[148:151]\n"
SA"s50,s50,2304\n"
SA"s51,s50,s45\n"
MF"v[156:159],a[172:175],a[52:55],v[156:159]\n"
MF"v[156:159],a[168:171],a[36:39],v[156:159]\n"
BL"a[8:11],v235,s[8:11],s58 offen offset:0 sc1\n"
MF"v[164:167],a[168:171],a[40:43],v[164:167]\n"
MF"v[164:167],a[172:175],a[56:59],v[164:167]\n"
WT"vmcnt(29) lgkmcnt(0)\n"
"s_barrier\n"
MF"v[172:175],a[172:175],a[60:63],v[172:175]\n"
RT"a[128:129],v236 offset:0\n"
MF"v[172:175],a[168:171],a[44:47],v[172:175]\n"
"buffer_store_dwordx2 v[232:233],v230,s[8:11],s51 offen sc1\n"
MF"v[176:179],a[176:179],a[32:35],v[176:179]\n"
RT"a[130:131],v236 offset:512\n"
MF"v[176:179],a[180:183],a[48:51],v[176:179]\n"
MF"v[184:187],a[180:183],a[52:55],v[184:187]\n"
SA"m0,s38,81920\n"
RT"a[132:133],v236 offset:8192\n"
SA"s48,s60,s61\n"
MF"v[184:187],a[176:179],a[36:39],v[184:187]\n"
BL"a[12:15],v235,s[8:11],s59 offen offset:0 sc1\n"
MF"v[192:195],a[176:179],a[40:43],v[192:195]\n"
RT"a[134:135],v236 offset:8704\n"
MF"v[192:195],a[180:183],a[56:59],v[192:195]\n"
BL"v238,s[20:23],s48 offen offset:0 lds\n"
MF"v[200:203],a[180:183],a[60:63],v[200:203]\n"
RT"a[136:137],v237 offset:0\n"
MF"v[200:203],a[176:179],a[44:47],v[200:203]\n"
MF"v[180:183],a[184:187],a[32:35],v[180:183]\n"
RT"a[138:139],v237 offset:512\n"
MF"v[180:183],a[188:191],a[48:51],v[180:183]\n"
BL"a[16:19],v235,s[8:11],s56 offen offset:1024 sc1\n"
MF"v[188:191],a[188:191],a[52:55],v[188:191]\n"
RT"a[140:141],v237 offset:8192\n"
MF"v[188:191],a[184:187],a[36:39],v[188:191]\n"
MF"v[196:199],a[184:187],a[40:43],v[196:199]\n"
RT"a[142:143],v237 offset:8704\n"
MF"v[196:199],a[188:191],a[56:59],v[196:199]\n"
BL"v239,s[20:23],s48 offen offset:1024 lds\n"
MF"v[204:207],a[188:191],a[60:63],v[204:207]\n"
SA"s49,s88,262144\n"
RT"a[144:145],v228 offset:0\n"
MF"v[204:207],a[184:187],a[44:47],v[204:207]\n"
"s_branch LQ_%=\n"
"LV_%=:\n"
WT"vmcnt(17)\n"
"v_lshlrev_b32 v148,16,v128\n"
"v_and_b32 v149,0xffff0000,v128\n"
"v_lshlrev_b32 v150,16,v129\n"
"v_and_b32 v151,0xffff0000,v129\n"
WT"vmcnt(16)\n"
"v_lshlrev_b32 v152,16,v130\n"
"v_and_b32 v153,0xffff0000,v130\n"
"v_lshlrev_b32 v154,16,v131\n"
"v_and_b32 v155,0xffff0000,v131\n"
WT"vmcnt(15)\n"
"v_lshlrev_b32 v156,16,v132\n"
"v_and_b32 v157,0xffff0000,v132\n"
"v_lshlrev_b32 v158,16,v133\n"
"v_and_b32 v159,0xffff0000,v133\n"
WT"vmcnt(14)\n"
"v_lshlrev_b32 v160,16,v134\n"
"v_and_b32 v161,0xffff0000,v134\n"
"v_lshlrev_b32 v162,16,v135\n"
"v_and_b32 v163,0xffff0000,v135\n"
WT"vmcnt(13)\n"
"v_lshlrev_b32 v164,16,v136\n"
"v_and_b32 v165,0xffff0000,v136\n"
"v_lshlrev_b32 v166,16,v137\n"
"v_and_b32 v167,0xffff0000,v137\n"
"v_add_f32 v188,v148,v149\n"
"v_add_f32 v189,v150,v151\n"
"v_add_f32 v188,v188,v152\n"
"v_add_f32 v189,v189,v153\n"
"v_add_f32 v188,v188,v154\n"
"v_add_f32 v189,v189,v155\n"
"v_add_f32 v188,v188,v156\n"
"v_add_f32 v189,v189,v157\n"
"v_add_f32 v188,v188,v158\n"
"v_add_f32 v189,v189,v159\n"
"v_add_f32 v188,v188,v160\n"
"v_add_f32 v189,v189,v161\n"
"v_add_f32 v188,v188,v162\n"
"v_add_f32 v189,v189,v163\n"
"v_add_f32 v188,v188,v164\n"
"v_add_f32 v189,v189,v165\n"
"v_add_f32 v188,v188,v166\n"
"v_add_f32 v189,v189,v167\n"
"v_add_f32 v188,v188,v189\n"
"s_nop 1\n"
"v_add_f32_dpp v188,v188,v188 quad_perm:[1,0,3,2] row_mask:0xf bank_mask:0xf\n"
WT"vmcnt(12)\n"
"v_lshlrev_b32 v168,16,v138\n"
"v_and_b32 v169,0xffff0000,v138\n"
"v_add_f32_dpp v188,v188,v188 quad_perm:[2,3,0,1] row_mask:0xf bank_mask:0xf\n"
"v_lshlrev_b32 v170,16,v139\n"
"v_and_b32 v171,0xffff0000,v139\n"
"v_add_f32_dpp v188,v188,v188 row_half_mirror row_mask:0xf bank_mask:0xf\n"
WT"vmcnt(11)\n"
"v_lshlrev_b32 v172,16,v140\n"
SA"m0,s36,0\n"
"s_bfe_u32 s48,s46,0x30000\n"
"s_lshl_b32 s48,s48,7\n"
BL"v234,s[12:15],s48 offen offset:0 lds\n"
BL"v235,s[12:15],s48 offen offset:1024 lds\n"
BL"v236,s[12:15],s48 offen offset:2048 lds\n"
BL"v237,s[12:15],s48 offen offset:3072 lds\n"
"v_and_b32 v173,0xffff0000,v140\n"
"v_add_f32_dpp v188,v188,v188 row_mirror row_mask:0xf bank_mask:0xf\n"
"v_lshlrev_b32 v174,16,v141\n"
"v_and_b32 v175,0xffff0000,v141\n"
"v_mov_b32 v189,v188\n"
WT"vmcnt(14)\n"
"v_lshlrev_b32 v176,16,v142\n"
"v_and_b32 v177,0xffff0000,v142\n"
"v_permlane16_swap_b32 v189,v188\n"
"v_lshlrev_b32 v178,16,v143\n"
"v_and_b32 v179,0xffff0000,v143\n"
"v_add_f32 v188,v188,v189\n"
WT"vmcnt(13)\n"
"v_lshlrev_b32 v180,16,v144\n"
"v_and_b32 v181,0xffff0000,v144\n"
MU"v191,0x3acccccd,v188\n"
"v_lshlrev_b32 v182,16,v145\n"
"v_and_b32 v183,0xffff0000,v145\n"
PK"v128,v191,v191\n"
WT"vmcnt(12)\n"
"v_lshlrev_b32 v184,16,v146\n"
"v_and_b32 v185,0xffff0000,v146\n"
"v_lshlrev_b32 v128,16,v128\n"
"v_lshlrev_b32 v186,16,v147\n"
"v_and_b32 v187,0xffff0000,v147\n"
"v_sub_f32 v148,v148,v128\n"
"v_sub_f32 v149,v149,v128\n"
"v_sub_f32 v150,v150,v128\n"
"v_sub_f32 v151,v151,v128\n"
"v_sub_f32 v152,v152,v128\n"
"v_sub_f32 v153,v153,v128\n"
"v_sub_f32 v154,v154,v128\n"
"v_sub_f32 v155,v155,v128\n"
"v_sub_f32 v156,v156,v128\n"
"v_sub_f32 v157,v157,v128\n"
"v_sub_f32 v158,v158,v128\n"
"v_sub_f32 v159,v159,v128\n"
"v_sub_f32 v160,v160,v128\n"
"v_sub_f32 v161,v161,v128\n"
"v_sub_f32 v162,v162,v128\n"
"v_sub_f32 v163,v163,v128\n"
"v_sub_f32 v164,v164,v128\n"
"v_sub_f32 v165,v165,v128\n"
"v_sub_f32 v166,v166,v128\n"
"v_sub_f32 v167,v167,v128\n"
MU"v188,v148,v148\n"
MU"v189,v149,v149\n"
"v_fmac_f32 v188,v150,v150\n"
"v_fmac_f32 v189,v151,v151\n"
"v_fmac_f32 v188,v152,v152\n"
SA"m0,s37,8192\n"
"s_bfe_u32 s48,s46,0x30004\n"
"s_lshl_b32 s48,s48,19\n"
BL"v238,s[16:19],s48 offen offset:0 nt lds\n"
BL"v239,s[16:19],s48 offen offset:1024 nt lds\n"
BL"v240,s[16:19],s48 offen offset:2048 nt lds\n"
BL"v241,s[16:19],s48 offen offset:3072 nt lds\n"
"v_fmac_f32 v189,v153,v153\n"
"v_fmac_f32 v188,v154,v154\n"
"v_fmac_f32 v189,v155,v155\n"
"v_fmac_f32 v188,v156,v156\n"
"v_fmac_f32 v189,v157,v157\n"
"v_fmac_f32 v188,v158,v158\n"
"v_fmac_f32 v189,v159,v159\n"
"v_fmac_f32 v188,v160,v160\n"
"v_fmac_f32 v189,v161,v161\n"
"v_fmac_f32 v188,v162,v162\n"
"v_fmac_f32 v189,v163,v163\n"
"v_fmac_f32 v188,v164,v164\n"
"v_fmac_f32 v189,v165,v165\n"
"v_fmac_f32 v188,v166,v166\n"
"v_fmac_f32 v189,v167,v167\n"
"v_add_f32 v188,v188,v189\n"
PK"v149,v148,v149\n"
"v_lshlrev_b32 v148,16,v149\n"
"v_add_f32_dpp v188,v188,v188 quad_perm:[1,0,3,2] row_mask:0xf bank_mask:0xf\n"
"v_and_b32 v149,0xffff0000,v149\n"
PK"v151,v150,v151\n"
"v_add_f32_dpp v188,v188,v188 quad_perm:[2,3,0,1] row_mask:0xf bank_mask:0xf\n"
"v_lshlrev_b32 v150,16,v151\n"
"v_and_b32 v151,0xffff0000,v151\n"
"v_add_f32_dpp v188,v188,v188 row_half_mirror row_mask:0xf bank_mask:0xf\n"
PK"v153,v152,v153\n"
"v_lshlrev_b32 v152,16,v153\n"
"v_add_f32_dpp v188,v188,v188 row_mirror row_mask:0xf bank_mask:0xf\n"
"v_and_b32 v153,0xffff0000,v153\n"
PK"v155,v154,v155\n"
"v_mov_b32 v189,v188\n"
"v_lshlrev_b32 v154,16,v155\n"
"v_and_b32 v155,0xffff0000,v155\n"
"v_permlane16_swap_b32 v189,v188\n"
PK"v157,v156,v157\n"
"v_lshlrev_b32 v156,16,v157\n"
"v_add_f32 v188,v188,v189\n"
"v_and_b32 v157,0xffff0000,v157\n"
PK"v159,v158,v159\n"
"v_sub_f32 v191,v191,v128\n"
"v_lshlrev_b32 v158,16,v159\n"
"v_and_b32 v159,0xffff0000,v159\n"
MU"v188,0x3acccccd,v188\n"
PK"v161,v160,v161\n"
"v_lshlrev_b32 v160,16,v161\n"
"v_fma_f32 v188,-v191,v191,v188\n"
"v_and_b32 v161,0xffff0000,v161\n"
PK"v163,v162,v163\n"
SA"m0,s37,12288\n"
"s_bfe_u32 s48,s46,0x30004\n"
"s_lshl_b32 s48,s48,19\n"
"s_bitset1_b32 s48,18\n"
BL"v238,s[16:19],s48 offen offset:0 nt lds\n"
BL"v239,s[16:19],s48 offen offset:1024 nt lds\n"
BL"v240,s[16:19],s48 offen offset:2048 nt lds\n"
BL"v241,s[16:19],s48 offen offset:3072 nt lds\n"
"v_max_f32 v188,0,v188\n"
"v_lshlrev_b32 v162,16,v163\n"
"v_and_b32 v163,0xffff0000,v163\n"
PK"v188,v188,v188\n"
PK"v165,v164,v165\n"
"v_lshlrev_b32 v164,16,v165\n"
"v_lshlrev_b32 v188,16,v188\n"
"v_and_b32 v165,0xffff0000,v165\n"
PK"v167,v166,v167\n"
"v_add_f32 v188,0x3727c5ac,v188\n"
"v_lshlrev_b32 v166,16,v167\n"
"v_and_b32 v167,0xffff0000,v167\n"
PK"v188,v188,v188\n"
"v_bfe_u32 v130,v224,5,4\n"
"v_mul_u32_u24 v130,144,v130\n"
"v_lshlrev_b32 v188,16,v188\n"
"v_bfe_u32 v131,v224,4,1\n"
"v_lshl_add_u32 v130,v131,3,v130\n"
"v_sqrt_f32 v189,v188\n"
"v_lshrrev_b32 v131,9,v224\n"
"v_lshl_add_u32 v130,v131,4,v130\n"
"v_rcp_f32 v190,v189\n"
"s_lshl_b32 s52,s30,5\n"
"v_add_u32 v130,s52,v130\n"
"v_fma_f32 v188,-v189,v189,v188\n"
MU"v190,0.5,v190\n"
"v_fma_f32 v189,v188,v190,v189\n"
PK"v189,v189,v189\n"
"v_lshlrev_b32 v189,16,v189\n"
"v_rcp_f32 v129,v189\n"
"s_nop 0\n"
MU"v148,v148,v129\n"
MU"v149,v149,v129\n"
PK"v138,v148,v149\n"
MU"v150,v150,v129\n"
MU"v151,v151,v129\n"
PK"v139,v150,v151\n"
"ds_write_b64 v130,v[138:139] offset:49152\n"
MU"v152,v152,v129\n"
MU"v153,v153,v129\n"
PK"v140,v152,v153\n"
MU"v154,v154,v129\n"
MU"v155,v155,v129\n"
PK"v141,v154,v155\n"
"ds_write_b64 v130,v[140:141] offset:51456\n"
MU"v156,v156,v129\n"
MU"v157,v157,v129\n"
PK"v142,v156,v157\n"
SA"m0,s36,16384\n"
"s_bfe_u32 s48,s46,0x30004\n"
"s_lshl_b32 s48,s48,7\n"
BL"v234,s[12:15],s48 offen offset:0 lds\n"
BL"v235,s[12:15],s48 offen offset:1024 lds\n"
BL"v236,s[12:15],s48 offen offset:2048 lds\n"
BL"v237,s[12:15],s48 offen offset:3072 lds\n"
MU"v158,v158,v129\n"
MU"v159,v159,v129\n"
PK"v143,v158,v159\n"
"ds_write_b64 v130,v[142:143] offset:53760\n"
MU"v160,v160,v129\n"
MU"v161,v161,v129\n"
PK"v144,v160,v161\n"
MU"v162,v162,v129\n"
MU"v163,v163,v129\n"
PK"v145,v162,v163\n"
"ds_write_b64 v130,v[144:145] offset:56064\n"
MU"v164,v164,v129\n"
MU"v165,v165,v129\n"
PK"v146,v164,v165\n"
MU"v166,v166,v129\n"
MU"v167,v167,v129\n"
PK"v147,v166,v167\n"
"ds_write_b64 v130,v[146:147] offset:58368\n"
"v_lshlrev_b32 v148,16,v138\n"
"v_and_b32 v149,0xffff0000,v138\n"
MU"v188,v148,v168\n"
MU"v189,v149,v169\n"
"v_lshlrev_b32 v150,16,v139\n"
"v_and_b32 v151,0xffff0000,v139\n"
"v_fmac_f32 v188,v150,v170\n"
"v_fmac_f32 v189,v151,v171\n"
"v_lshlrev_b32 v152,16,v140\n"
"v_and_b32 v153,0xffff0000,v140\n"
"v_fmac_f32 v188,v152,v172\n"
"v_fmac_f32 v189,v153,v173\n"
"v_lshlrev_b32 v154,16,v141\n"
"v_and_b32 v155,0xffff0000,v141\n"
"v_fmac_f32 v188,v154,v174\n"
"v_fmac_f32 v189,v155,v175\n"
"v_lshlrev_b32 v156,16,v142\n"
"v_and_b32 v157,0xffff0000,v142\n"
"v_fmac_f32 v188,v156,v176\n"
"v_fmac_f32 v189,v157,v177\n"
"v_lshlrev_b32 v158,16,v143\n"
"v_and_b32 v159,0xffff0000,v143\n"
"v_fmac_f32 v188,v158,v178\n"
"v_fmac_f32 v189,v159,v179\n"
"v_lshlrev_b32 v160,16,v144\n"
"v_and_b32 v161,0xffff0000,v144\n"
"v_fmac_f32 v188,v160,v180\n"
"v_fmac_f32 v189,v161,v181\n"
"v_lshlrev_b32 v162,16,v145\n"
"v_and_b32 v163,0xffff0000,v145\n"
WT"lgkmcnt(0)\n"
SA"s28,s28,0x7f4a7c15\n"
"s_addc_u32 s29,s29,0x9e3779b9\n"
"v_mov_b32 v232,s28\n"
"v_mov_b32 v233,s29\n"
WT"vmcnt(20)\n"
RT"a[0:1],v246 offset:0\n"
RT"a[2:3],v246 offset:512\n"
RT"a[4:5],v247 offset:0\n"
RT"a[6:7],v247 offset:512\n"
RT"a[8:9],v248 offset:0\n"
RT"a[10:11],v248 offset:512\n"
RT"a[12:13],v249 offset:0\n"
RT"a[14:15],v249 offset:512\n"
WT"vmcnt(16)\n"
RT"a[16:17],v246 offset:4096\n"
RT"a[18:19],v246 offset:4608\n"
RT"a[20:21],v247 offset:4096\n"
RT"a[22:23],v247 offset:4608\n"
RT"a[24:25],v248 offset:4096\n"
RT"a[26:27],v248 offset:4608\n"
RT"a[28:29],v249 offset:4096\n"
RT"a[30:31],v249 offset:4608\n"
WT"vmcnt(12) lgkmcnt(15)\n"
"s_barrier\n"
RD"v[192:195],v242 offset:0\n"
RD"v[196:199],v243 offset:0\n"
RD"v[200:203],v242 offset:2048\n"
RD"v[204:207],v243 offset:2048\n"
RD"v[208:211],v242 offset:4096\n"
"v_lshrrev_b32 v131,7,v224\n"
WT"lgkmcnt(4)\n"
MF"v[0:3],v[192:195],a[0:3],0\n"
WT"lgkmcnt(3)\n"
RD"v[212:215],v243 offset:4096\n"
"v_lshl_add_u32 v131,v131,4,v224\n"
MF"v[0:3],v[196:199],a[16:19],v[0:3]\n"
SA"m0,s37,16384\n"
"s_bfe_u32 s48,s46,0x30008\n"
"s_lshl_b32 s48,s48,19\n"
MF"v[8:11],v[196:199],a[20:23],0\n"
"s_mul_i32 s52,s30,1152\n"
BL"v238,s[16:19],s48 offen offset:0 nt lds\n"
MF"v[8:11],v[192:195],a[4:7],v[8:11]\n"
"v_add_u32 v128,s52,v131\n"
"s_and_b32 s52,s30,1\n"
MF"v[16:19],v[192:195],a[8:11],0\n"
"s_mul_i32 s51,s52,1152\n"
RD"v[216:219],v242 offset:6144\n"
"v_add_u32 v131,s51,v131\n"
MF"v[16:19],v[196:199],a[24:27],v[16:19]\n"
"s_lshl_b32 s52,s52,10\n"
"v_fmac_f32 v188,v162,v182\n"
"v_fmac_f32 v189,v163,v183\n"
MF"v[24:27],v[196:199],a[28:31],0\n"
BL"v239,s[16:19],s48 offen offset:1024 nt lds\n"
MF"v[24:27],v[192:195],a[12:15],v[24:27]\n"
WT"lgkmcnt(1)\n"
RD"v[220:223],v243 offset:6144\n"
RD"v[132:135],v128 offset:49152\n"
MF"v[4:7],v[200:203],a[0:3],0\n"
WT"vmcnt(10)\n"
RT"a[32:33],v246 offset:8192\n"
RD"a[240:243],v128 offset:53760\n"
MF"v[4:7],v[204:207],a[16:19],v[4:7]\n"
"s_lshr_b32 s50,s31,1\n"
RD"a[244:247],v131 offset:58368\n"
"v_lshlrev_b32 v164,16,v146\n"
MF"v[12:15],v[204:207],a[20:23],0\n"
"s_mul_i32 s50,s50,20480\n"
"v_and_b32 v129,0x70,v224\n"
"v_and_b32 v165,0xffff0000,v146\n"
MF"v[12:15],v[200:203],a[4:7],v[12:15]\n"
"s_and_b32 s51,s31,1\n"
RD"v[192:195],v242 offset:8192\n"
RT"a[34:35],v246 offset:8704\n"
MF"v[20:23],v[200:203],a[8:11],0\n"
"s_lshl_b32 s51,s51,7\n"
BL"v240,s[16:19],s48 offen offset:2048 nt lds\n"
MF"v[20:23],v[204:207],a[24:27],v[20:23]\n"
SA"s50,s50,s51\n"
"v_and_b32 v130,0x380,v224\n"
"v_fmac_f32 v188,v164,v184\n"
MF"v[28:31],v[204:207],a[28:31],0\n"
SA"s50,s50,65536\n"
RT"a[36:37],v247 offset:8192\n"
"v_lshl_add_u32 v129,v130,1,v129\n"
MF"v[28:31],v[200:203],a[12:15],v[28:31]\n"
"s_lshl_b32 s52,s52,1\n"
RD"v[196:199],v243 offset:8192\n"
"v_fmac_f32 v189,v165,v185\n"
MF"v[32:35],v[208:211],a[0:3],0\n"
SA"s52,s52,s50\n"
BL"v241,s[16:19],s48 offen offset:3072 nt lds\n"
MF"v[32:35],v[212:215],a[16:19],v[32:35]\n"
SA"s52,s52,16384\n"
RT"a[38:39],v247 offset:8704\n"
"v_lshlrev_b32 v166,16,v147\n"
MF"v[40:43],v[212:215],a[20:23],0\n"
SA"m0,s37,20480\n"
RD"v[200:203],v242 offset:10240\n"
"v_and_b32 v167,0xffff0000,v147\n"
MF"v[40:43],v[208:211],a[4:7],v[40:43]\n"
"s_bfe_u32 s48,s46,0x30008\n"
"v_fmac_f32 v188,v166,v186\n"
"v_fmac_f32 v189,v167,v187\n"
MF"v[48:51],v[208:211],a[8:11],0\n"
"s_lshl_b32 s48,s48,19\n"
RT"a[40:41],v248 offset:8192\n"
"v_add_f32 v188,v188,v189\n"
MF"v[48:51],v[212:215],a[24:27],v[48:51]\n"
"s_bitset1_b32 s48,18\n"
"v_add_f32_dpp v188,v188,v188 quad_perm:[1,0,3,2] row_mask:0xf bank_mask:0xf\n"
"s_lshl_b32 s51,s30,11\n"
MF"v[56:59],v[212:215],a[28:31],0\n"
SA"s50,s50,s51\n"
BL"v238,s[16:19],s48 offen offset:0 nt lds\n"
MF"v[56:59],v[208:211],a[12:15],v[56:59]\n"
WT"lgkmcnt(3)\n"
RD"v[204:207],v243 offset:10240\n"
RT"a[42:43],v248 offset:8704\n"
MF"v[36:39],v[216:219],a[0:3],0\n"
SA"s51,s50,8192\n"
BS"v[132:135],v129,s[8:11],s50 offen sc1\n"
MF"v[36:39],v[220:223],a[16:19],v[36:39]\n"
"s_lshl_b32 s90,s31,2\n"
BL"v239,s[16:19],s48 offen offset:1024 nt lds\n"
MF"v[44:47],v[220:223],a[20:23],0\n"
SA"s90,s90,s30\n"
BS"a[240:243],v129,s[8:11],s51 offen sc1\n"
MF"v[44:47],v[216:219],a[4:7],v[44:47]\n"
"s_lshl_b32 s90,s90,1\n"
RD"v[208:211],v242 offset:12288\n"
RT"a[44:45],v249 offset:8192\n"
MF"v[52:55],v[216:219],a[8:11],0\n"
"s_sub_u32 s91,2047,s90\n"
BS"a[244:247],v129,s[8:11],s52 offen sc1\n"
MF"v[52:55],v[220:223],a[24:27],v[52:55]\n"
RD"v[212:215],v243 offset:12288\n"
RT"a[46:47],v249 offset:8704\n"
MF"v[60:63],v[220:223],a[28:31],0\n"
BL"v240,s[16:19],s48 offen offset:2048 nt lds\n"
MF"v[60:63],v[216:219],a[12:15],v[60:63]\n"
"v_add_f32_dpp v188,v188,v188 quad_perm:[2,3,0,1] row_mask:0xf bank_mask:0xf\n"
MF"v[64:67],v[192:195],a[0:3],0\n"
RD"v[216:219],v242 offset:14336\n"
"v_add_f32_dpp v188,v188,v188 row_half_mirror row_mask:0xf bank_mask:0xf\n"
MF"v[64:67],v[196:199],a[16:19],v[64:67]\n"
WT"vmcnt(14)\n"
RT"a[48:49],v246 offset:12288\n"
"v_add_f32_dpp v188,v188,v188 row_mirror row_mask:0xf bank_mask:0xf\n"
MF"v[72:75],v[196:199],a[20:23],0\n"
"s_lshl_b32 s90,s90,2\n"
BL"v241,s[16:19],s48 offen offset:3072 nt lds\n"
MF"v[72:75],v[192:195],a[4:7],v[72:75]\n"
SA"s90,s90,2686976\n"
RT"a[50:51],v246 offset:12800\n"
"v_mov_b32 v189,v188\n"
MF"v[80:83],v[192:195],a[8:11],0\n"
SA"m0,s36,32768\n"
RD"v[220:223],v243 offset:14336\n"
"v_permlane16_swap_b32 v189,v188\n"
MF"v[80:83],v[196:199],a[24:27],v[80:83]\n"
"s_bfe_u32 s48,s46,0x30008\n"
"v_add_f32 v188,v188,v189\n"
PK"v188,v188,v188\n"
MF"v[88:91],v[196:199],a[28:31],0\n"
"s_lshl_b32 s48,s48,7\n"
RT"a[52:53],v247 offset:12288\n"
"v_lshlrev_b32 v188,16,v188\n"
MF"v[88:91],v[192:195],a[12:15],v[88:91]\n"
WT"lgkmcnt(4)\n"
BL"v234,s[12:15],s48 offen offset:0 lds\n"
MF"v[68:71],v[200:203],a[0:3],0\n"
"v_ashrrev_i32 v189,31,v188\n"
"v_or_b32 v189,0x80000000,v189\n"
MF"v[68:71],v[204:207],a[16:19],v[68:71]\n"
RT"a[54:55],v247 offset:12800\n"
"v_xor_b32 v188,v188,v189\n"
MF"v[76:79],v[204:207],a[20:23],0\n"
BL"v235,s[12:15],s48 offen offset:1024 lds\n"
MF"v[76:79],v[200:203],a[4:7],v[76:79]\n"
"v_and_b32 v188,0xffff0000,v188\n"
"v_lshrrev_b32 v189,9,v224\n"
MF"v[84:87],v[200:203],a[8:11],0\n"
RT"a[56:57],v248 offset:12288\n"
"v_sub_u32 v190,s91,v189\n"
MF"v[84:87],v[204:207],a[24:27],v[84:87]\n"
WT"vmcnt(13) lgkmcnt(3)\n"
"s_barrier\n"
MF"v[92:95],v[204:207],a[28:31],0\n"
RD"v[192:195],v242 offset:16384\n"
"v_or_b32 v188,v188,v190\n"
MF"v[92:95],v[200:203],a[12:15],v[92:95]\n"
RT"a[58:59],v248 offset:12800\n"
"v_and_b32 v190,0x1f0,v224\n"
MF"v[96:99],v[208:211],a[0:3],0\n"
BL"v236,s[12:15],s48 offen offset:2048 lds\n"
MF"v[96:99],v[212:215],a[16:19],v[96:99]\n"
"v_lshlrev_b32 v190,23,v190\n"
"v_lshl_add_u32 v190,v189,2,v190\n"
MF"v[104:107],v[212:215],a[20:23],0\n"
"buffer_store_dword v188,v190,s[8:11],s90 offen sc1\n"
MF"v[104:107],v[208:211],a[4:7],v[104:107]\n"
RD"v[196:199],v243 offset:16384\n"
RT"a[60:61],v249 offset:12288\n"
MF"v[112:115],v[208:211],a[8:11],0\n"
BL"v237,s[12:15],s48 offen offset:3072 lds\n"
MF"v[112:115],v[212:215],a[24:27],v[112:115]\n"
RT"a[62:63],v249 offset:12800\n"
MF"v[120:123],v[212:215],a[28:31],0\n"
SA"m0,s37,0\n"
RD"v[200:203],v242 offset:18432\n"
"s_bfe_u32 s48,s46,0x3000c\n"
MF"v[120:123],v[208:211],a[12:15],v[120:123]\n"
"s_lshl_b32 s48,s48,19\n"
MF"v[100:103],v[216:219],a[0:3],0\n"
BL"v238,s[16:19],s48 offen offset:0 lds\n"
MF"v[100:103],v[220:223],a[16:19],v[100:103]\n"
RD"v[204:207],v243 offset:18432\n"
MF"v[108:111],v[220:223],a[20:23],0\n"
MF"v[108:111],v[216:219],a[4:7],v[108:111]\n"
MF"v[116:119],v[216:219],a[8:11],0\n"
BL"v239,s[16:19],s48 offen offset:1024 lds\n"
MF"v[116:119],v[220:223],a[24:27],v[116:119]\n"
RD"v[208:211],v242 offset:20480\n"
MF"v[124:127],v[220:223],a[28:31],0\n"
MF"v[124:127],v[216:219],a[12:15],v[124:127]\n"
WT"lgkmcnt(2)\n"
MF"v[0:3],v[192:195],a[32:35],v[0:3]\n"
RD"v[212:215],v243 offset:20480\n"
MF"v[0:3],v[196:199],a[48:51],v[0:3]\n"
BL"v240,s[16:19],s48 offen offset:2048 lds\n"
MF"v[8:11],v[196:199],a[52:55],v[8:11]\n"
MF"v[8:11],v[192:195],a[36:39],v[8:11]\n"
MF"v[16:19],v[192:195],a[40:43],v[16:19]\n"
RD"v[216:219],v242 offset:22528\n"
MF"v[16:19],v[196:199],a[56:59],v[16:19]\n"
BL"v241,s[16:19],s48 offen offset:3072 lds\n"
MF"v[24:27],v[196:199],a[60:63],v[24:27]\n"
MF"v[24:27],v[192:195],a[44:47],v[24:27]\n"
RD"v[220:223],v243 offset:22528\n"
MF"v[4:7],v[200:203],a[32:35],v[4:7]\n"
WT"lgkmcnt(2)\n"
WT"vmcnt(16)\n"
RT"a[64:65],v246 offset:16384\n"
MF"v[4:7],v[204:207],a[48:51],v[4:7]\n"
SA"m0,s37,4096\n"
"s_bfe_u32 s48,s46,0x3000c\n"
"s_lshl_b32 s48,s48,19\n"
MF"v[12:15],v[204:207],a[52:55],v[12:15]\n"
"s_bitset1_b32 s48,18\n"
BL"v238,s[16:19],s48 offen offset:0 lds\n"
MF"v[12:15],v[200:203],a[36:39],v[12:15]\n"
RD"v[192:195],v242 offset:24576\n"
RT"a[66:67],v246 offset:16896\n"
MF"v[20:23],v[200:203],a[40:43],v[20:23]\n"
MF"v[20:23],v[204:207],a[56:59],v[20:23]\n"
MF"v[28:31],v[204:207],a[60:63],v[28:31]\n"
RT"a[68:69],v247 offset:16384\n"
MF"v[28:31],v[200:203],a[44:47],v[28:31]\n"
BL"v239,s[16:19],s48 offen offset:1024 lds\n"
MF"v[32:35],v[208:211],a[32:35],v[32:35]\n"
RD"v[196:199],v243 offset:24576\n"
MF"v[32:35],v[212:215],a[48:51],v[32:35]\n"
RT"a[70:71],v247 offset:16896\n"
MF"v[40:43],v[212:215],a[52:55],v[40:43]\n"
RD"v[200:203],v242 offset:26624\n"
MF"v[40:43],v[208:211],a[36:39],v[40:43]\n"
BL"v240,s[16:19],s48 offen offset:2048 lds\n"
MF"v[48:51],v[208:211],a[40:43],v[48:51]\n"
RT"a[72:73],v248 offset:16384\n"
MF"v[48:51],v[212:215],a[56:59],v[48:51]\n"
MF"v[56:59],v[212:215],a[60:63],v[56:59]\n"
RD"v[204:207],v243 offset:26624\n"
MF"v[56:59],v[208:211],a[44:47],v[56:59]\n"
WT"lgkmcnt(4)\n"
RT"a[74:75],v248 offset:16896\n"
MF"v[36:39],v[216:219],a[32:35],v[36:39]\n"
BL"v241,s[16:19],s48 offen offset:3072 lds\n"
MF"v[36:39],v[220:223],a[48:51],v[36:39]\n"
RD"v[208:211],v242 offset:28672\n"
MF"v[44:47],v[220:223],a[52:55],v[44:47]\n"
SA"m0,s36,0\n"
RT"a[76:77],v249 offset:16384\n"
"s_bfe_u32 s48,s46,0x3000c\n"
MF"v[44:47],v[216:219],a[36:39],v[44:47]\n"
"s_lshl_b32 s48,s48,7\n"
MF"v[52:55],v[216:219],a[40:43],v[52:55]\n"
BL"v234,s[12:15],s48 offen offset:0 lds\n"
MF"v[52:55],v[220:223],a[56:59],v[52:55]\n"
RD"v[212:215],v243 offset:28672\n"
RT"a[78:79],v249 offset:16896\n"
MF"v[60:63],v[220:223],a[60:63],v[60:63]\n"
MF"v[60:63],v[216:219],a[44:47],v[60:63]\n"
MF"v[64:67],v[192:195],a[32:35],v[64:67]\n"
RD"v[216:219],v242 offset:30720\n"
MF"v[64:67],v[196:199],a[48:51],v[64:67]\n"
WT"vmcnt(14)\n"
RT"a[80:81],v246 offset:20480\n"
MF"v[72:75],v[196:199],a[52:55],v[72:75]\n"
BL"v235,s[12:15],s48 offen offset:1024 lds\n"
MF"v[72:75],v[192:195],a[36:39],v[72:75]\n"
RT"a[82:83],v246 offset:20992\n"
MF"v[80:83],v[192:195],a[40:43],v[80:83]\n"
RD"v[220:223],v243 offset:30720\n"
MF"v[80:83],v[196:199],a[56:59],v[80:83]\n"
BL"v236,s[12:15],s48 offen offset:2048 lds\n"
MF"v[88:91],v[196:199],a[60:63],v[88:91]\n"
RT"a[84:85],v247 offset:20480\n"
MF"v[88:91],v[192:195],a[44:47],v[88:91]\n"
WT"lgkmcnt(4)\n"
MF"v[68:71],v[200:203],a[32:35],v[68:71]\n"
"s_lshl_b32 s51,s32,8\n"
"s_andn2_b32 s50,s31,7\n"
SA"s50,s50,s51\n"
MF"v[68:71],v[204:207],a[48:51],v[68:71]\n"
SA"s50,s50,256\n"
RT"a[86:87],v247 offset:20992\n"
SA"s50,s50,s45\n"
MF"v[76:79],v[204:207],a[52:55],v[76:79]\n"
BL"v237,s[12:15],s48 offen offset:3072 lds\n"
MF"v[76:79],v[200:203],a[36:39],v[76:79]\n"
MF"v[84:87],v[200:203],a[40:43],v[84:87]\n"
SA"m0,s37,8192\n"
RT"a[88:89],v248 offset:20480\n"
"s_bfe_u32 s48,s46,0x30010\n"
MF"v[84:87],v[204:207],a[56:59],v[84:87]\n"
"s_lshl_b32 s48,s48,19\n"
WT"vmcnt(12) lgkmcnt(3)\n"
"s_barrier\n"
MF"v[92:95],v[204:207],a[60:63],v[92:95]\n"
RD"v[192:195],v242 offset:32768\n"
MF"v[92:95],v[200:203],a[44:47],v[92:95]\n"
BL"v238,s[16:19],s48 offen offset:0 lds\n"
MF"v[96:99],v[208:211],a[32:35],v[96:99]\n"
"buffer_store_dwordx2 v[232:233],v230,s[8:11],s50 offen sc1\n"
MF"v[96:99],v[212:215],a[48:51],v[96:99]\n"
RT"a[90:91],v248 offset:20992\n"
MF"v[104:107],v[212:215],a[52:55],v[104:107]\n"
RD"v[196:199],v243 offset:32768\n"
RT"a[92:93],v249 offset:20480\n"
MF"v[104:107],v[208:211],a[36:39],v[104:107]\n"
BL"v239,s[16:19],s48 offen offset:1024 lds\n"
MF"v[112:115],v[208:211],a[40:43],v[112:115]\n"
MF"v[112:115],v[212:215],a[56:59],v[112:115]\n"
RT"a[94:95],v249 offset:20992\n"
MF"v[120:123],v[212:215],a[60:63],v[120:123]\n"
RD"v[200:203],v242 offset:34816\n"
MF"v[120:123],v[208:211],a[44:47],v[120:123]\n"
BL"v240,s[16:19],s48 offen offset:2048 lds\n"
MF"v[100:103],v[216:219],a[32:35],v[100:103]\n"
MF"v[100:103],v[220:223],a[48:51],v[100:103]\n"
RD"v[204:207],v243 offset:34816\n"
MF"v[108:111],v[220:223],a[52:55],v[108:111]\n"
MF"v[108:111],v[216:219],a[36:39],v[108:111]\n"
BL"v241,s[16:19],s48 offen offset:3072 lds\n"
MF"v[116:119],v[216:219],a[40:43],v[116:119]\n"
MF"v[116:119],v[220:223],a[56:59],v[116:119]\n"
RD"v[208:211],v242 offset:36864\n"
MF"v[124:127],v[220:223],a[60:63],v[124:127]\n"
SA"m0,s37,12288\n"
"s_bfe_u32 s48,s46,0x30010\n"
"s_lshl_b32 s48,s48,19\n"
MF"v[124:127],v[216:219],a[44:47],v[124:127]\n"
WT"lgkmcnt(2)\n"
"s_bitset1_b32 s48,18\n"
MF"v[0:3],v[192:195],a[64:67],v[0:3]\n"
RD"v[212:215],v243 offset:36864\n"
MF"v[0:3],v[196:199],a[80:83],v[0:3]\n"
BL"v238,s[16:19],s48 offen offset:0 lds\n"
MF"v[8:11],v[196:199],a[84:87],v[8:11]\n"
MF"v[8:11],v[192:195],a[68:71],v[8:11]\n"
MF"v[16:19],v[192:195],a[72:75],v[16:19]\n"
RD"v[216:219],v242 offset:38912\n"
MF"v[16:19],v[196:199],a[88:91],v[16:19]\n"
BL"v239,s[16:19],s48 offen offset:1024 lds\n"
MF"v[24:27],v[196:199],a[92:95],v[24:27]\n"
MF"v[24:27],v[192:195],a[76:79],v[24:27]\n"
RD"v[220:223],v243 offset:38912\n"
MF"v[4:7],v[200:203],a[64:67],v[4:7]\n"
WT"lgkmcnt(2)\n"
WT"vmcnt(15)\n"
RT"a[96:97],v246 offset:0\n"
MF"v[4:7],v[204:207],a[80:83],v[4:7]\n"
BL"v240,s[16:19],s48 offen offset:2048 lds\n"
MF"v[12:15],v[204:207],a[84:87],v[12:15]\n"
MF"v[12:15],v[200:203],a[68:71],v[12:15]\n"
RD"v[192:195],v242 offset:40960\n"
MF"v[20:23],v[200:203],a[72:75],v[20:23]\n"
RT"a[98:99],v246 offset:512\n"
MF"v[20:23],v[204:207],a[88:91],v[20:23]\n"
BL"v241,s[16:19],s48 offen offset:3072 lds\n"
MF"v[28:31],v[204:207],a[92:95],v[28:31]\n"
RT"a[100:101],v247 offset:0\n"
MF"v[28:31],v[200:203],a[76:79],v[28:31]\n"
RD"v[196:199],v243 offset:40960\n"
MF"v[32:35],v[208:211],a[64:67],v[32:35]\n"
SA"m0,s36,16384\n"
"s_bfe_u32 s48,s46,0x30010\n"
"s_lshl_b32 s48,s48,7\n"
MF"v[32:35],v[212:215],a[80:83],v[32:35]\n"
RT"a[102:103],v247 offset:512\n"
MF"v[40:43],v[212:215],a[84:87],v[40:43]\n"
RD"v[200:203],v242 offset:43008\n"
MF"v[40:43],v[208:211],a[68:71],v[40:43]\n"
BL"v234,s[12:15],s48 offen offset:0 lds\n"
MF"v[48:51],v[208:211],a[72:75],v[48:51]\n"
RT"a[104:105],v248 offset:0\n"
MF"v[48:51],v[212:215],a[88:91],v[48:51]\n"
MF"v[56:59],v[212:215],a[92:95],v[56:59]\n"
RD"v[204:207],v243 offset:43008\n"
MF"v[56:59],v[208:211],a[76:79],v[56:59]\n"
WT"lgkmcnt(4)\n"
BL"v235,s[12:15],s48 offen offset:1024 lds\n"
MF"v[36:39],v[216:219],a[64:67],v[36:39]\n"
RT"a[106:107],v248 offset:512\n"
MF"v[36:39],v[220:223],a[80:83],v[36:39]\n"
RD"v[208:211],v242 offset:45056\n"
MF"v[44:47],v[220:223],a[84:87],v[44:47]\n"
RT"a[108:109],v249 offset:0\n"
MF"v[44:47],v[216:219],a[68:71],v[44:47]\n"
BL"v236,s[12:15],s48 offen offset:2048 lds\n"
MF"v[52:55],v[216:219],a[72:75],v[52:55]\n"
MF"v[52:55],v[220:223],a[88:91],v[52:55]\n"
RD"v[212:215],v243 offset:45056\n"
MF"v[60:63],v[220:223],a[92:95],v[60:63]\n"
RT"a[110:111],v249 offset:512\n"
MF"v[60:63],v[216:219],a[76:79],v[60:63]\n"
BL"v237,s[12:15],s48 offen offset:3072 lds\n"
MF"v[64:67],v[192:195],a[64:67],v[64:67]\n"
RD"v[216:219],v242 offset:47104\n"
MF"v[64:67],v[196:199],a[80:83],v[64:67]\n"
WT"vmcnt(17)\n"
RT"a[112:113],v246 offset:4096\n"
MF"v[72:75],v[196:199],a[84:87],v[72:75]\n"
SA"m0,s37,16384\n"
"s_bfe_u32 s48,s46,0x30014\n"
"s_lshl_b32 s48,s48,19\n"
MF"v[72:75],v[192:195],a[68:71],v[72:75]\n"
RT"a[114:115],v246 offset:4608\n"
MF"v[80:83],v[192:195],a[72:75],v[80:83]\n"
RD"v[220:223],v243 offset:47104\n"
MF"v[80:83],v[196:199],a[88:91],v[80:83]\n"
BL"v238,s[16:19],s48 offen offset:0 lds\n"
MF"v[88:91],v[196:199],a[92:95],v[88:91]\n"
RT"a[116:117],v247 offset:4096\n"
MF"v[88:91],v[192:195],a[76:79],v[88:91]\n"
WT"lgkmcnt(4)\n"
MF"v[68:71],v[200:203],a[64:67],v[68:71]\n"
BL"v239,s[16:19],s48 offen offset:1024 lds\n"
MF"v[68:71],v[204:207],a[80:83],v[68:71]\n"
RT"a[118:119],v247 offset:4608\n"
MF"v[76:79],v[204:207],a[84:87],v[76:79]\n"
MF"v[76:79],v[200:203],a[68:71],v[76:79]\n"
MF"v[84:87],v[200:203],a[72:75],v[84:87]\n"
RT"a[120:121],v248 offset:4096\n"
MF"v[84:87],v[204:207],a[88:91],v[84:87]\n"
WT"vmcnt(15) lgkmcnt(3)\n"
"s_barrier\n"
MF"v[92:95],v[204:207],a[92:95],v[92:95]\n"
BL"v240,s[16:19],s48 offen offset:2048 lds\n"
MF"v[92:95],v[200:203],a[76:79],v[92:95]\n"
RD"v[192:195],v242 offset:0\n"
RT"a[122:123],v248 offset:4608\n"
MF"v[96:99],v[208:211],a[64:67],v[96:99]\n"
MF"v[96:99],v[212:215],a[80:83],v[96:99]\n"
BL"v241,s[16:19],s48 offen offset:3072 lds\n"
MF"v[104:107],v[212:215],a[84:87],v[104:107]\n"
RD"v[196:199],v243 offset:0\n"
RT"a[124:125],v249 offset:4096\n"
MF"v[104:107],v[208:211],a[68:71],v[104:107]\n"
MF"v[112:115],v[208:211],a[72:75],v[112:115]\n"
SA"m0,s37,20480\n"
"s_bfe_u32 s48,s46,0x30014\n"
"s_lshl_b32 s48,s48,19\n"
MF"v[112:115],v[212:215],a[88:91],v[112:115]\n"
"s_bitset1_b32 s48,18\n"
RT"a[126:127],v249 offset:4608\n"
MF"v[120:123],v[212:215],a[92:95],v[120:123]\n"
RD"v[200:203],v242 offset:2048\n"
MF"v[120:123],v[208:211],a[76:79],v[120:123]\n"
BL"v238,s[16:19],s48 offen offset:0 lds\n"
MF"v[100:103],v[216:219],a[64:67],v[100:103]\n"
MF"v[100:103],v[220:223],a[80:83],v[100:103]\n"
RD"v[204:207],v243 offset:2048\n"
MF"v[108:111],v[220:223],a[84:87],v[108:111]\n"
BL"v239,s[16:19],s48 offen offset:1024 lds\n"
MF"v[108:111],v[216:219],a[68:71],v[108:111]\n"
MF"v[116:119],v[216:219],a[72:75],v[116:119]\n"
MF"v[116:119],v[220:223],a[88:91],v[116:119]\n"
RD"v[208:211],v242 offset:4096\n"
MF"v[124:127],v[220:223],a[92:95],v[124:127]\n"
MF"v[124:127],v[216:219],a[76:79],v[124:127]\n"
WT"lgkmcnt(2)\n"
BL"v240,s[16:19],s48 offen offset:2048 lds\n"
MF"v[0:3],v[192:195],a[96:99],v[0:3]\n"
RD"v[212:215],v243 offset:4096\n"
MF"v[0:3],v[196:199],a[112:115],v[0:3]\n"
MF"v[8:11],v[196:199],a[116:119],v[8:11]\n"
MF"v[8:11],v[192:195],a[100:103],v[8:11]\n"
BL"v241,s[16:19],s48 offen offset:3072 lds\n"
MF"v[16:19],v[192:195],a[104:107],v[16:19]\n"
RD"v[216:219],v242 offset:6144\n"
MF"v[16:19],v[196:199],a[120:123],v[16:19]\n"
MF"v[24:27],v[196:199],a[124:127],v[24:27]\n"
SA"m0,s36,32768\n"
"s_bfe_u32 s48,s46,0x30014\n"
"s_lshl_b32 s48,s48,7\n"
MF"v[24:27],v[192:195],a[108:111],v[24:27]\n"
RD"v[220:223],v243 offset:6144\n"
MF"v[4:7],v[200:203],a[96:99],v[4:7]\n"
WT"lgkmcnt(2)\n"
WT"vmcnt(16)\n"
RT"a[128:129],v246 offset:8192\n"
MF"v[4:7],v[204:207],a[112:115],v[4:7]\n"
BL"v234,s[12:15],s48 offen offset:0 lds\n"
MF"v[12:15],v[204:207],a[116:119],v[12:15]\n"
MF"v[12:15],v[200:203],a[100:103],v[12:15]\n"
RD"v[192:195],v242 offset:8192\n"
MF"v[20:23],v[200:203],a[104:107],v[20:23]\n"
RT"a[130:131],v246 offset:8704\n"
MF"v[20:23],v[204:207],a[120:123],v[20:23]\n"
BL"v235,s[12:15],s48 offen offset:1024 lds\n"
MF"v[28:31],v[204:207],a[124:127],v[28:31]\n"
RT"a[132:133],v247 offset:8192\n"
MF"v[28:31],v[200:203],a[108:111],v[28:31]\n"
RD"v[196:199],v243 offset:8192\n"
MF"v[32:35],v[208:211],a[96:99],v[32:35]\n"
MF"v[32:35],v[212:215],a[112:115],v[32:35]\n"
RT"a[134:135],v247 offset:8704\n"
MF"v[40:43],v[212:215],a[116:119],v[40:43]\n"
BL"v236,s[12:15],s48 offen offset:2048 lds\n"
MF"v[40:43],v[208:211],a[100:103],v[40:43]\n"
RD"v[200:203],v242 offset:10240\n"
MF"v[48:51],v[208:211],a[104:107],v[48:51]\n"
RT"a[136:137],v248 offset:8192\n"
MF"v[48:51],v[212:215],a[120:123],v[48:51]\n"
BL"v237,s[12:15],s48 offen offset:3072 lds\n"
MF"v[56:59],v[212:215],a[124:127],v[56:59]\n"
RD"v[204:207],v243 offset:10240\n"
MF"v[56:59],v[208:211],a[108:111],v[56:59]\n"
WT"lgkmcnt(4)\n"
RT"a[138:139],v248 offset:8704\n"
MF"v[36:39],v[216:219],a[96:99],v[36:39]\n"
SA"m0,s37,0\n"
"s_bfe_u32 s48,s46,0x30018\n"
"s_lshl_b32 s48,s48,19\n"
MF"v[36:39],v[220:223],a[112:115],v[36:39]\n"
RD"v[208:211],v242 offset:12288\n"
MF"v[44:47],v[220:223],a[116:119],v[44:47]\n"
RT"a[140:141],v249 offset:8192\n"
MF"v[44:47],v[216:219],a[100:103],v[44:47]\n"
BL"v238,s[16:19],s48 offen offset:0 lds\n"
MF"v[52:55],v[216:219],a[104:107],v[52:55]\n"
MF"v[52:55],v[220:223],a[120:123],v[52:55]\n"
RD"v[212:215],v243 offset:12288\n"
MF"v[60:63],v[220:223],a[124:127],v[60:63]\n"
RT"a[142:143],v249 offset:8704\n"
MF"v[60:63],v[216:219],a[108:111],v[60:63]\n"
BL"v239,s[16:19],s48 offen offset:1024 lds\n"
MF"v[64:67],v[192:195],a[96:99],v[64:67]\n"
RD"v[216:219],v242 offset:14336\n"
MF"v[64:67],v[196:199],a[112:115],v[64:67]\n"
WT"vmcnt(18)\n"
RT"a[144:145],v246 offset:12288\n"
MF"v[72:75],v[196:199],a[116:119],v[72:75]\n"
MF"v[72:75],v[192:195],a[100:103],v[72:75]\n"
RT"a[146:147],v246 offset:12800\n"
MF"v[80:83],v[192:195],a[104:107],v[80:83]\n"
BL"v240,s[16:19],s48 offen offset:2048 lds\n"
MF"v[80:83],v[196:199],a[120:123],v[80:83]\n"
RD"v[220:223],v243 offset:14336\n"
MF"v[88:91],v[196:199],a[124:127],v[88:91]\n"
RT"a[148:149],v247 offset:12288\n"
MF"v[88:91],v[192:195],a[108:111],v[88:91]\n"
WT"lgkmcnt(4)\n"
BL"v241,s[16:19],s48 offen offset:3072 lds\n"
MF"v[68:71],v[200:203],a[96:99],v[68:71]\n"
MF"v[68:71],v[204:207],a[112:115],v[68:71]\n"
RT"a[150:151],v247 offset:12800\n"
MF"v[76:79],v[204:207],a[116:119],v[76:79]\n"
SA"m0,s37,4096\n"
"s_bfe_u32 s48,s46,0x30018\n"
"s_lshl_b32 s48,s48,19\n"
MF"v[76:79],v[200:203],a[100:103],v[76:79]\n"
"s_bitset1_b32 s48,18\n"
MF"v[84:87],v[200:203],a[104:107],v[84:87]\n"
RT"a[152:153],v248 offset:12288\n"
MF"v[84:87],v[204:207],a[120:123],v[84:87]\n"
BL"v238,s[16:19],s48 offen offset:0 lds\n"
MF"v[92:95],v[204:207],a[124:127],v[92:95]\n"
WT"vmcnt(17) lgkmcnt(3)\n"
"s_barrier\n"
MF"v[92:95],v[200:203],a[108:111],v[92:95]\n"
RD"v[192:195],v242 offset:16384\n"
RT"a[154:155],v248 offset:12800\n"
MF"v[96:99],v[208:211],a[96:99],v[96:99]\n"
BL"v239,s[16:19],s48 offen offset:1024 lds\n"
MF"v[96:99],v[212:215],a[112:115],v[96:99]\n"
MF"v[104:107],v[212:215],a[116:119],v[104:107]\n"
RD"v[196:199],v243 offset:16384\n"
MF"v[104:107],v[208:211],a[100:103],v[104:107]\n"
RT"a[156:157],v249 offset:12288\n"
MF"v[112:115],v[208:211],a[104:107],v[112:115]\n"
MF"v[112:115],v[212:215],a[120:123],v[112:115]\n"
RT"a[158:159],v249 offset:12800\n"
MF"v[120:123],v[212:215],a[124:127],v[120:123]\n"
BL"v240,s[16:19],s48 offen offset:2048 lds\n"
MF"v[120:123],v[208:211],a[108:111],v[120:123]\n"
RD"v[200:203],v242 offset:18432\n"
MF"v[100:103],v[216:219],a[96:99],v[100:103]\n"
MF"v[100:103],v[220:223],a[112:115],v[100:103]\n"
RD"v[204:207],v243 offset:18432\n"
MF"v[108:111],v[220:223],a[116:119],v[108:111]\n"
BL"v241,s[16:19],s48 offen offset:3072 lds\n"
MF"v[108:111],v[216:219],a[100:103],v[108:111]\n"
MF"v[116:119],v[216:219],a[104:107],v[116:119]\n"
SA"m0,s36,0\n"
"s_bfe_u32 s48,s46,0x30018\n"
"s_lshl_b32 s48,s48,7\n"
MF"v[116:119],v[220:223],a[120:123],v[116:119]\n"
RD"v[208:211],v242 offset:20480\n"
MF"v[124:127],v[220:223],a[124:127],v[124:127]\n"
BL"v234,s[12:15],s48 offen offset:0 lds\n"
MF"v[124:127],v[216:219],a[108:111],v[124:127]\n"
WT"lgkmcnt(2)\n"
MF"v[0:3],v[192:195],a[128:131],v[0:3]\n"
RD"v[212:215],v243 offset:20480\n"
MF"v[0:3],v[196:199],a[144:147],v[0:3]\n"
MF"v[8:11],v[196:199],a[148:151],v[8:11]\n"
BL"v235,s[12:15],s48 offen offset:1024 lds\n"
MF"v[8:11],v[192:195],a[132:135],v[8:11]\n"
MF"v[16:19],v[192:195],a[136:139],v[16:19]\n"
RD"v[216:219],v242 offset:22528\n"
MF"v[16:19],v[196:199],a[152:155],v[16:19]\n"
MF"v[24:27],v[196:199],a[156:159],v[24:27]\n"
MF"v[24:27],v[192:195],a[140:143],v[24:27]\n"
RD"v[220:223],v243 offset:22528\n"
MF"v[4:7],v[200:203],a[128:131],v[4:7]\n"
WT"lgkmcnt(2)\n"
BL"v236,s[12:15],s48 offen offset:2048 lds\n"
MF"v[4:7],v[204:207],a[144:147],v[4:7]\n"
WT"vmcnt(19)\n"
RT"a[160:161],v246 offset:16384\n"
MF"v[12:15],v[204:207],a[148:151],v[12:15]\n"
MF"v[12:15],v[200:203],a[132:135],v[12:15]\n"
RD"v[192:195],v242 offset:24576\n"
MF"v[20:23],v[200:203],a[136:139],v[20:23]\n"
RT"a[162:163],v246 offset:16896\n"
MF"v[20:23],v[204:207],a[152:155],v[20:23]\n"
BL"v237,s[12:15],s48 offen offset:3072 lds\n"
MF"v[28:31],v[204:207],a[156:159],v[28:31]\n"
SA"m0,s37,8192\n"
RT"a[164:165],v247 offset:16384\n"
"s_bfe_u32 s48,s46,0x3001c\n"
MF"v[28:31],v[200:203],a[140:143],v[28:31]\n"
"s_lshl_b32 s48,s48,19\n"
RD"v[196:199],v243 offset:24576\n"
MF"v[32:35],v[208:211],a[128:131],v[32:35]\n"
BL"v238,s[16:19],s48 offen offset:0 lds\n"
MF"v[32:35],v[212:215],a[144:147],v[32:35]\n"
RT"a[166:167],v247 offset:16896\n"
MF"v[40:43],v[212:215],a[148:151],v[40:43]\n"
RD"v[200:203],v242 offset:26624\n"
MF"v[40:43],v[208:211],a[132:135],v[40:43]\n"
MF"v[48:51],v[208:211],a[136:139],v[48:51]\n"
RT"a[168:169],v248 offset:16384\n"
MF"v[48:51],v[212:215],a[152:155],v[48:51]\n"
BL"v239,s[16:19],s48 offen offset:1024 lds\n"
MF"v[56:59],v[212:215],a[156:159],v[56:59]\n"
RD"v[204:207],v243 offset:26624\n"
MF"v[56:59],v[208:211],a[140:143],v[56:59]\n"
WT"lgkmcnt(4)\n"
RT"a[170:171],v248 offset:16896\n"
MF"v[36:39],v[216:219],a[128:131],v[36:39]\n"
MF"v[36:39],v[220:223],a[144:147],v[36:39]\n"
RD"v[208:211],v242 offset:28672\n"
MF"v[44:47],v[220:223],a[148:151],v[44:47]\n"
BL"v240,s[16:19],s48 offen offset:2048 lds\n"
MF"v[44:47],v[216:219],a[132:135],v[44:47]\n"
RT"a[172:173],v249 offset:16384\n"
MF"v[52:55],v[216:219],a[136:139],v[52:55]\n"
MF"v[52:55],v[220:223],a[152:155],v[52:55]\n"
RD"v[212:215],v243 offset:28672\n"
MF"v[60:63],v[220:223],a[156:159],v[60:63]\n"
RT"a[174:175],v249 offset:16896\n"
MF"v[60:63],v[216:219],a[140:143],v[60:63]\n"
BL"v241,s[16:19],s48 offen offset:3072 lds\n"
MF"v[64:67],v[192:195],a[128:131],v[64:67]\n"
SA"m0,s37,12288\n"
RD"v[216:219],v242 offset:30720\n"
"s_bfe_u32 s48,s46,0x3001c\n"
MF"v[64:67],v[196:199],a[144:147],v[64:67]\n"
"s_lshl_b32 s48,s48,19\n"
WT"vmcnt(20)\n"
RT"a[176:177],v246 offset:20480\n"
MF"v[72:75],v[196:199],a[148:151],v[72:75]\n"
"s_bitset1_b32 s48,18\n"
BL"v238,s[16:19],s48 offen offset:0 lds\n"
MF"v[72:75],v[192:195],a[132:135],v[72:75]\n"
RT"a[178:179],v246 offset:20992\n"
MF"v[80:83],v[192:195],a[136:139],v[80:83]\n"
RD"v[220:223],v243 offset:30720\n"
MF"v[80:83],v[196:199],a[152:155],v[80:83]\n"
MF"v[88:91],v[196:199],a[156:159],v[88:91]\n"
RT"a[180:181],v247 offset:20480\n"
MF"v[88:91],v[192:195],a[140:143],v[88:91]\n"
WT"lgkmcnt(4)\n"
BL"v239,s[16:19],s48 offen offset:1024 lds\n"
MF"v[68:71],v[200:203],a[128:131],v[68:71]\n"
MF"v[68:71],v[204:207],a[144:147],v[68:71]\n"
RT"a[182:183],v247 offset:20992\n"
MF"v[76:79],v[204:207],a[148:151],v[76:79]\n"
MF"v[76:79],v[200:203],a[132:135],v[76:79]\n"
BL"v240,s[16:19],s48 offen offset:2048 lds\n"
MF"v[84:87],v[200:203],a[136:139],v[84:87]\n"
RT"a[184:185],v248 offset:20480\n"
MF"v[84:87],v[204:207],a[152:155],v[84:87]\n"
WT"vmcnt(19) lgkmcnt(3)\n"
"s_barrier\n"
MF"v[92:95],v[204:207],a[156:159],v[92:95]\n"
RD"v[192:195],v242 offset:32768\n"
MF"v[92:95],v[200:203],a[140:143],v[92:95]\n"
RT"a[186:187],v248 offset:20992\n"
MF"v[96:99],v[208:211],a[128:131],v[96:99]\n"
BL"v241,s[16:19],s48 offen offset:3072 lds\n"
MF"v[96:99],v[212:215],a[144:147],v[96:99]\n"
MF"v[104:107],v[212:215],a[148:151],v[104:107]\n"
SA"m0,s36,16384\n"
RD"v[196:199],v243 offset:32768\n"
"s_bfe_u32 s48,s46,0x3001c\n"
MF"v[104:107],v[208:211],a[132:135],v[104:107]\n"
"s_lshl_b32 s48,s48,7\n"
RT"a[188:189],v249 offset:20480\n"
MF"v[112:115],v[208:211],a[136:139],v[112:115]\n"
BL"v234,s[12:15],s48 offen offset:0 lds\n"
MF"v[112:115],v[212:215],a[152:155],v[112:115]\n"
RT"a[190:191],v249 offset:20992\n"
MF"v[120:123],v[212:215],a[156:159],v[120:123]\n"
RD"v[200:203],v242 offset:34816\n"
MF"v[120:123],v[208:211],a[140:143],v[120:123]\n"
MF"v[100:103],v[216:219],a[128:131],v[100:103]\n"
BL"v235,s[12:15],s48 offen offset:1024 lds\n"
MF"v[100:103],v[220:223],a[144:147],v[100:103]\n"
RD"v[204:207],v243 offset:34816\n"
MF"v[108:111],v[220:223],a[148:151],v[108:111]\n"
MF"v[108:111],v[216:219],a[132:135],v[108:111]\n"
MF"v[116:119],v[216:219],a[136:139],v[116:119]\n"
MF"v[116:119],v[220:223],a[152:155],v[116:119]\n"
RD"v[208:211],v242 offset:36864\n"
MF"v[124:127],v[220:223],a[156:159],v[124:127]\n"
BL"v236,s[12:15],s48 offen offset:2048 lds\n"
MF"v[124:127],v[216:219],a[140:143],v[124:127]\n"
WT"lgkmcnt(2)\n"
MF"v[0:3],v[192:195],a[160:163],v[0:3]\n"
RD"v[212:215],v243 offset:36864\n"
MF"v[0:3],v[196:199],a[176:179],v[0:3]\n"
BL"v237,s[12:15],s48 offen offset:3072 lds\n"
MF"v[8:11],v[196:199],a[180:183],v[8:11]\n"
MF"v[8:11],v[192:195],a[164:167],v[8:11]\n"
MF"v[16:19],v[192:195],a[168:171],v[16:19]\n"
SA"m0,s37,16384\n"
RD"v[216:219],v242 offset:38912\n"
SA"s48,s39,0\n"
MF"v[16:19],v[196:199],a[184:187],v[16:19]\n"
MF"v[24:27],v[196:199],a[188:191],v[24:27]\n"
BL"v234,s[12:15],s48 offen offset:0 lds\n"
MF"v[24:27],v[192:195],a[172:175],v[24:27]\n"
RD"v[220:223],v243 offset:38912\n"
MF"v[4:7],v[200:203],a[160:163],v[4:7]\n"
WT"lgkmcnt(2)\n"
WT"vmcnt(21)\n"
RT"a[192:193],v246 offset:0\n"
MF"v[4:7],v[204:207],a[176:179],v[4:7]\n"
MF"v[12:15],v[204:207],a[180:183],v[12:15]\n"
BL"v235,s[12:15],s48 offen offset:1024 lds\n"
MF"v[12:15],v[200:203],a[164:167],v[12:15]\n"
RD"v[192:195],v242 offset:40960\n"
RT"a[194:195],v246 offset:512\n"
MF"v[20:23],v[200:203],a[168:171],v[20:23]\n"
MF"v[20:23],v[204:207],a[184:187],v[20:23]\n"
MF"v[28:31],v[204:207],a[188:191],v[28:31]\n"
RT"a[196:197],v247 offset:0\n"
MF"v[28:31],v[200:203],a[172:175],v[28:31]\n"
RD"v[196:199],v243 offset:40960\n"
MF"v[32:35],v[208:211],a[160:163],v[32:35]\n"
BL"v236,s[12:15],s48 offen offset:2048 lds\n"
MF"v[32:35],v[212:215],a[176:179],v[32:35]\n"
RT"a[198:199],v247 offset:512\n"
MF"v[40:43],v[212:215],a[180:183],v[40:43]\n"
RD"v[200:203],v242 offset:43008\n"
MF"v[40:43],v[208:211],a[164:167],v[40:43]\n"
BL"v237,s[12:15],s48 offen offset:3072 lds\n"
MF"v[48:51],v[208:211],a[168:171],v[48:51]\n"
RT"a[200:201],v248 offset:0\n"
MF"v[48:51],v[212:215],a[184:187],v[48:51]\n"
MF"v[56:59],v[212:215],a[188:191],v[56:59]\n"
SA"m0,s37,20480\n"
RD"v[204:207],v243 offset:43008\n"
SA"s48,s98,0\n"
MF"v[56:59],v[208:211],a[172:175],v[56:59]\n"
WT"lgkmcnt(4)\n"
RT"a[202:203],v248 offset:512\n"
"s_lshr_b32 s50,s30,1\n"
MF"v[36:39],v[216:219],a[160:163],v[36:39]\n"
"s_and_b32 s51,s30,1\n"
BL"v234,s[12:15],s48 offen offset:0 lds\n"
MF"v[36:39],v[220:223],a[176:179],v[36:39]\n"
"s_mul_i32 s38,s50,24576\n"
RD"v[208:211],v242 offset:45056\n"
MF"v[44:47],v[220:223],a[180:183],v[44:47]\n"
"s_lshl_b32 s60,s50,18\n"
RT"a[204:205],v249 offset:0\n"
MF"v[44:47],v[216:219],a[164:167],v[44:47]\n"
"s_lshl_b32 s50,s51,12\n"
MF"v[52:55],v[216:219],a[168:171],v[52:55]\n"
SA"s38,s38,s50\n"
BL"v235,s[12:15],s48 offen offset:1024 lds\n"
MF"v[52:55],v[220:223],a[184:187],v[52:55]\n"
"s_lshl_b32 s50,s51,7\n"
RD"v[212:215],v243 offset:45056\n"
RT"a[206:207],v249 offset:512\n"
MF"v[60:63],v[220:223],a[188:191],v[60:63]\n"
SA"s60,s60,s50\n"
MF"v[60:63],v[216:219],a[172:175],v[60:63]\n"
MF"v[64:67],v[192:195],a[160:163],v[64:67]\n"
RD"v[216:219],v242 offset:47104\n"
MF"v[64:67],v[196:199],a[176:179],v[64:67]\n"
"s_sub_u32 s99,s34,5\n"
WT"vmcnt(22)\n"
RT"a[208:209],v246 offset:4096\n"
MF"v[72:75],v[196:199],a[180:183],v[72:75]\n"
"s_cmp_lt_u32 s34,5\n"
BL"v236,s[12:15],s48 offen offset:2048 lds\n"
MF"v[72:75],v[192:195],a[164:167],v[72:75]\n"
"s_cselect_b32 s99,s34,s99\n"
RT"a[210:211],v246 offset:4608\n"
"s_mul_i32 s99,s99,1\n"
MF"v[80:83],v[192:195],a[168:171],v[80:83]\n"
"s_lshl_b32 s61,s99,20\n"
RD"v[220:223],v243 offset:47104\n"
MF"v[80:83],v[196:199],a[184:187],v[80:83]\n"
BL"v237,s[12:15],s48 offen offset:3072 lds\n"
MF"v[88:91],v[196:199],a[188:191],v[88:91]\n"
RT"a[212:213],v247 offset:4096\n"
MF"v[88:91],v[192:195],a[172:175],v[88:91]\n"
WT"lgkmcnt(4)\n"
MF"v[68:71],v[200:203],a[160:163],v[68:71]\n"
SA"m0,s37,0\n"
SA"s48,s39,32768\n"
MF"v[68:71],v[204:207],a[176:179],v[68:71]\n"
RT"a[214:215],v247 offset:4608\n"
MF"v[76:79],v[204:207],a[180:183],v[76:79]\n"
BL"v234,s[12:15],s48 offen offset:0 lds\n"
MF"v[76:79],v[200:203],a[164:167],v[76:79]\n"
MF"v[84:87],v[200:203],a[168:171],v[84:87]\n"
RT"a[216:217],v248 offset:4096\n"
MF"v[84:87],v[204:207],a[184:187],v[84:87]\n"
WT"vmcnt(21) lgkmcnt(3)\n"
"s_barrier\n"
MF"v[92:95],v[204:207],a[188:191],v[92:95]\n"
RD"v[192:195],v242 offset:0\n"
MF"v[92:95],v[200:203],a[172:175],v[92:95]\n"
BL"v235,s[12:15],s48 offen offset:1024 lds\n"
MF"v[96:99],v[208:211],a[160:163],v[96:99]\n"
RT"a[218:219],v248 offset:4608\n"
MF"v[96:99],v[212:215],a[176:179],v[96:99]\n"
MF"v[104:107],v[212:215],a[180:183],v[104:107]\n"
RD"v[196:199],v243 offset:0\n"
MF"v[104:107],v[208:211],a[164:167],v[104:107]\n"
RT"a[220:221],v249 offset:4096\n"
MF"v[112:115],v[208:211],a[168:171],v[112:115]\n"
BL"v236,s[12:15],s48 offen offset:2048 lds\n"
MF"v[112:115],v[212:215],a[184:187],v[112:115]\n"
RT"a[222:223],v249 offset:4608\n"
MF"v[120:123],v[212:215],a[188:191],v[120:123]\n"
RD"v[200:203],v242 offset:2048\n"
MF"v[120:123],v[208:211],a[172:175],v[120:123]\n"
BL"v237,s[12:15],s48 offen offset:3072 lds\n"
MF"v[100:103],v[216:219],a[160:163],v[100:103]\n"
MF"v[100:103],v[220:223],a[176:179],v[100:103]\n"
RD"v[204:207],v243 offset:2048\n"
MF"v[108:111],v[220:223],a[180:183],v[108:111]\n"
SA"m0,s37,4096\n"
SA"s48,s98,32768\n"
MF"v[108:111],v[216:219],a[164:167],v[108:111]\n"
MF"v[116:119],v[216:219],a[168:171],v[116:119]\n"
WT"lgkmcnt(2)\n"
BL"v234,s[12:15],s48 offen offset:0 lds\n"
MF"v[116:119],v[220:223],a[184:187],v[116:119]\n"
RD"v[208:211],v242 offset:4096\n"
MF"v[124:127],v[220:223],a[188:191],v[124:127]\n"
MF"v[124:127],v[216:219],a[172:175],v[124:127]\n"
MF"v[0:3],v[192:195],a[192:195],v[0:3]\n"
RD"v[212:215],v243 offset:4096\n"
MF"v[0:3],v[196:199],a[208:211],v[0:3]\n"
BL"v235,s[12:15],s48 offen offset:1024 lds\n"
MF"v[8:11],v[196:199],a[212:215],v[8:11]\n"
MF"v[8:11],v[192:195],a[196:199],v[8:11]\n"
MF"v[16:19],v[192:195],a[200:203],v[16:19]\n"
RD"v[216:219],v242 offset:6144\n"
MF"v[16:19],v[196:199],a[216:219],v[16:19]\n"
BL"v236,s[12:15],s48 offen offset:2048 lds\n"
MF"v[24:27],v[196:199],a[220:223],v[24:27]\n"
BL"v[160:163],v224,s[8:11],0 offen offset:256 sc1\n"
MF"v[24:27],v[192:195],a[204:207],v[24:27]\n"
WT"lgkmcnt(1)\n"
RD"v[220:223],v243 offset:6144\n"
MF"v[4:7],v[200:203],a[192:195],v[4:7]\n"
WT"vmcnt(24)\n"
RT"a[224:225],v246 offset:8192\n"
MF"v[4:7],v[204:207],a[208:211],v[4:7]\n"
BL"v[164:167],v224,s[8:11],0 offen offset:1280 sc1\n"
MF"v[12:15],v[204:207],a[212:215],v[12:15]\n"
BL"v237,s[12:15],s48 offen offset:3072 lds\n"
MF"v[12:15],v[200:203],a[196:199],v[12:15]\n"
RD"v[192:195],v242 offset:8192\n"
RT"a[226:227],v246 offset:8704\n"
MF"v[20:23],v[200:203],a[200:203],v[20:23]\n"
MF"v[20:23],v[204:207],a[216:219],v[20:23]\n"
MF"v[28:31],v[204:207],a[220:223],v[28:31]\n"
RT"a[228:229],v247 offset:8192\n"
MF"v[28:31],v[200:203],a[204:207],v[28:31]\n"
RD"v[196:199],v243 offset:8192\n"
MF"v[32:35],v[208:211],a[192:195],v[32:35]\n"
MF"v[32:35],v[212:215],a[208:211],v[32:35]\n"
RT"a[230:231],v247 offset:8704\n"
MF"v[40:43],v[212:215],a[212:215],v[40:43]\n"
RD"v[200:203],v242 offset:10240\n"
MF"v[40:43],v[208:211],a[196:199],v[40:43]\n"
MF"v[48:51],v[208:211],a[200:203],v[48:51]\n"
RT"a[232:233],v248 offset:8192\n"
MF"v[48:51],v[212:215],a[216:219],v[48:51]\n"
MF"v[56:59],v[212:215],a[220:223],v[56:59]\n"
RD"v[204:207],v243 offset:10240\n"
MF"v[56:59],v[208:211],a[204:207],v[56:59]\n"
WT"lgkmcnt(4)\n"
RT"a[234:235],v248 offset:8704\n"
MF"v[36:39],v[216:219],a[192:195],v[36:39]\n"
MF"v[36:39],v[220:223],a[208:211],v[36:39]\n"
RD"v[208:211],v242 offset:12288\n"
MF"v[44:47],v[220:223],a[212:215],v[44:47]\n"
RT"a[236:237],v249 offset:8192\n"
MF"v[44:47],v[216:219],a[196:199],v[44:47]\n"
MF"v[52:55],v[216:219],a[200:203],v[52:55]\n"
MF"v[52:55],v[220:223],a[216:219],v[52:55]\n"
RD"v[212:215],v243 offset:12288\n"
MF"v[60:63],v[220:223],a[220:223],v[60:63]\n"
RT"a[238:239],v249 offset:8704\n"
MF"v[60:63],v[216:219],a[204:207],v[60:63]\n"
MF"v[64:67],v[192:195],a[192:195],v[64:67]\n"
RD"v[216:219],v242 offset:14336\n"
MF"v[64:67],v[196:199],a[208:211],v[64:67]\n"
SA"m0,s37,8192\n"
WT"vmcnt(22)\n"
RT"a[240:241],v246 offset:12288\n"
MF"v[72:75],v[196:199],a[212:215],v[72:75]\n"
SA"s48,s39,65536\n"
MF"v[72:75],v[192:195],a[196:199],v[72:75]\n"
RT"a[242:243],v246 offset:12800\n"
MF"v[80:83],v[192:195],a[200:203],v[80:83]\n"
WT"lgkmcnt(3)\n"
BL"v234,s[12:15],s48 offen offset:0 lds\n"
MF"v[80:83],v[196:199],a[216:219],v[80:83]\n"
RD"v[220:223],v243 offset:14336\n"
MF"v[88:91],v[196:199],a[220:223],v[88:91]\n"
RT"a[244:245],v247 offset:12288\n"
MF"v[88:91],v[192:195],a[204:207],v[88:91]\n"
BL"v235,s[12:15],s48 offen offset:1024 lds\n"
MF"v[68:71],v[200:203],a[192:195],v[68:71]\n"
MF"v[68:71],v[204:207],a[208:211],v[68:71]\n"
RT"a[246:247],v247 offset:12800\n"
MF"v[76:79],v[204:207],a[212:215],v[76:79]\n"
MF"v[76:79],v[200:203],a[196:199],v[76:79]\n"
MF"v[84:87],v[200:203],a[200:203],v[84:87]\n"
RT"a[248:249],v248 offset:12288\n"
MF"v[84:87],v[204:207],a[216:219],v[84:87]\n"
BL"v236,s[12:15],s48 offen offset:2048 lds\n"
MF"v[92:95],v[204:207],a[220:223],v[92:95]\n"
WT"vmcnt(21) lgkmcnt(3)\n"
"s_barrier\n"
MF"v[92:95],v[200:203],a[204:207],v[92:95]\n"
RD"v[192:195],v242 offset:16384\n"
RT"a[250:251],v248 offset:12800\n"
MF"v[96:99],v[208:211],a[192:195],v[96:99]\n"
BL"v237,s[12:15],s48 offen offset:3072 lds\n"
MF"v[96:99],v[212:215],a[208:211],v[96:99]\n"
MF"v[104:107],v[212:215],a[212:215],v[104:107]\n"
RD"v[196:199],v243 offset:16384\n"
MF"v[104:107],v[208:211],a[196:199],v[104:107]\n"
RT"a[252:253],v249 offset:12288\n"
MF"v[112:115],v[208:211],a[200:203],v[112:115]\n"
MF"v[112:115],v[212:215],a[216:219],v[112:115]\n"
RT"a[254:255],v249 offset:12800\n"
MF"v[120:123],v[212:215],a[220:223],v[120:123]\n"
RD"v[200:203],v242 offset:18432\n"
MF"v[120:123],v[208:211],a[204:207],v[120:123]\n"
MF"v[100:103],v[216:219],a[192:195],v[100:103]\n"
"s_lshl_b32 s50,s35,20\n"
MF"v[100:103],v[220:223],a[208:211],v[100:103]\n"
"s_lshl_b32 s51,s34,15\n"
RD"v[204:207],v243 offset:18432\n"
SA"m0,s37,12288\n"
MF"v[108:111],v[220:223],a[212:215],v[108:111]\n"
SA"s48,s98,65536\n"
SA"s50,s50,s51\n"
MF"v[108:111],v[216:219],a[196:199],v[108:111]\n"
WT"lgkmcnt(2)\n"
BL"v234,s[12:15],s48 offen offset:0 lds\n"
MF"v[116:119],v[216:219],a[200:203],v[116:119]\n"
"s_and_b32 s51,s30,1\n"
"s_lshl_b32 s51,s51,12\n"
MF"v[116:119],v[220:223],a[216:219],v[116:119]\n"
SA"s50,s50,s51\n"
RD"v[208:211],v242 offset:20480\n"
MF"v[124:127],v[220:223],a[220:223],v[124:127]\n"
SA"s50,s50,9699328\n"
WT"vmcnt(6)\n"
"v_cmp_eq_u64 vcc,s[28:29],v[160:161]\n"
"v_cmp_eq_u64_e64 s[62:63],s[28:29],v[162:163]\n"
"s_and_b64 vcc,vcc,s[62:63]\n"
"v_cmp_eq_u64_e64 s[62:63],s[28:29],v[164:165]\n"
"s_and_b64 vcc,vcc,s[62:63]\n"
"v_cmp_eq_u64_e64 s[62:63],s[28:29],v[166:167]\n"
"s_and_b64 vcc,vcc,s[62:63]\n"
"s_cmp_eq_u64 vcc,exec\n"
"s_cbranch_scc0 L1004_%=\n"
"L1005_%=:\n"
MF"v[124:127],v[216:219],a[204:207],v[124:127]\n"
"s_mov_b32 s94,2686976\n"
BL"v235,s[12:15],s48 offen offset:1024 lds\n"
MF"v[0:3],v[192:195],a[224:227],v[0:3]\n"
"s_mov_b32 s95,2691072\n"
RD"v[212:215],v243 offset:20480\n"
"v_add_u32 v250,s50,v224\n"
MF"v[0:3],v[196:199],a[240:243],v[0:3]\n"
BL"v[160:163],v224,s[8:11],s94 offen offset:0 sc1\n"
MF"v[8:11],v[196:199],a[244:247],v[8:11]\n"
BL"v[164:167],v224,s[8:11],s94 offen offset:1024 sc1\n"
MF"v[8:11],v[192:195],a[228:231],v[8:11]\n"
MF"v[16:19],v[192:195],a[232:235],v[16:19]\n"
RD"v[216:219],v242 offset:22528\n"
MF"v[16:19],v[196:199],a[248:251],v[16:19]\n"
BL"v236,s[12:15],s48 offen offset:2048 lds\n"
MF"v[24:27],v[196:199],a[252:255],v[24:27]\n"
BL"v[168:171],v224,s[8:11],s94 offen offset:2048 sc1\n"
MF"v[24:27],v[192:195],a[236:239],v[24:27]\n"
WT"lgkmcnt(1)\n"
BL"v[172:175],v224,s[8:11],s94 offen offset:3072 sc1\n"
MF"v[4:7],v[200:203],a[224:227],v[4:7]\n"
RD"v[220:223],v243 offset:22528\n"
MF"v[4:7],v[204:207],a[240:243],v[4:7]\n"
BL"v237,s[12:15],s48 offen offset:3072 lds\n"
MF"v[12:15],v[204:207],a[244:247],v[12:15]\n"
BL"v[176:179],v224,s[8:11],s95 offen offset:0 sc1\n"
MF"v[12:15],v[200:203],a[228:231],v[12:15]\n"
BL"v[180:183],v224,s[8:11],s95 offen offset:1024 sc1\n"
MF"v[20:23],v[200:203],a[232:235],v[20:23]\n"
RD"v[192:195],v242 offset:24576\n"
MF"v[20:23],v[204:207],a[248:251],v[20:23]\n"
BL"v[184:187],v224,s[8:11],s95 offen offset:2048 sc1\n"
MF"v[28:31],v[204:207],a[252:255],v[28:31]\n"
BL"v[188:191],v224,s[8:11],s95 offen offset:3072 sc1\n"
MF"v[28:31],v[200:203],a[236:239],v[28:31]\n"
RD"v[196:199],v243 offset:24576\n"
MF"v[32:35],v[208:211],a[224:227],v[32:35]\n"
MF"v[32:35],v[212:215],a[240:243],v[32:35]\n"
MF"v[40:43],v[212:215],a[244:247],v[40:43]\n"
RD"v[200:203],v242 offset:26624\n"
MF"v[40:43],v[208:211],a[228:231],v[40:43]\n"
MF"v[48:51],v[208:211],a[232:235],v[48:51]\n"
MF"v[48:51],v[212:215],a[248:251],v[48:51]\n"
MF"v[56:59],v[212:215],a[252:255],v[56:59]\n"
RD"v[204:207],v243 offset:26624\n"
MF"v[56:59],v[208:211],a[236:239],v[56:59]\n"
WT"lgkmcnt(2)\n"
MF"v[36:39],v[216:219],a[224:227],v[36:39]\n"
MF"v[36:39],v[220:223],a[240:243],v[36:39]\n"
RD"v[208:211],v242 offset:28672\n"
MF"v[44:47],v[220:223],a[244:247],v[44:47]\n"
MF"v[44:47],v[216:219],a[228:231],v[44:47]\n"
MF"v[52:55],v[216:219],a[232:235],v[52:55]\n"
MF"v[52:55],v[220:223],a[248:251],v[52:55]\n"
RD"v[212:215],v243 offset:28672\n"
MF"v[60:63],v[220:223],a[252:255],v[60:63]\n"
MF"v[60:63],v[216:219],a[236:239],v[60:63]\n"
MF"v[64:67],v[192:195],a[224:227],v[64:67]\n"
RD"v[216:219],v242 offset:30720\n"
MF"v[64:67],v[196:199],a[240:243],v[64:67]\n"
MF"v[72:75],v[196:199],a[244:247],v[72:75]\n"
MF"v[72:75],v[192:195],a[228:231],v[72:75]\n"
MF"v[80:83],v[192:195],a[232:235],v[80:83]\n"
RD"v[220:223],v243 offset:30720\n"
MF"v[80:83],v[196:199],a[248:251],v[80:83]\n"
MF"v[88:91],v[196:199],a[252:255],v[88:91]\n"
MF"v[88:91],v[192:195],a[236:239],v[88:91]\n"
WT"lgkmcnt(1)\n"
MF"v[68:71],v[200:203],a[224:227],v[68:71]\n"
MF"v[68:71],v[204:207],a[240:243],v[68:71]\n"
MF"v[76:79],v[204:207],a[244:247],v[76:79]\n"
"v_add_u32 v244,114688,v242\n"
"v_add_u32 v245,114688,v243\n"
MF"v[76:79],v[200:203],a[228:231],v[76:79]\n"
"v_add_u32 v242,65536,v242\n"
"v_add_u32 v243,65536,v243\n"
MF"v[84:87],v[200:203],a[232:235],v[84:87]\n"
MF"v[84:87],v[204:207],a[248:251],v[84:87]\n"
WT"lgkmcnt(0)\n"
"s_barrier\n"
MF"v[92:95],v[204:207],a[252:255],v[92:95]\n"
RD"v[192:195],v242 offset:16384\n"
MF"v[92:95],v[200:203],a[236:239],v[92:95]\n"
MF"v[96:99],v[208:211],a[224:227],v[96:99]\n"
MF"v[96:99],v[212:215],a[240:243],v[96:99]\n"
MF"v[104:107],v[212:215],a[244:247],v[104:107]\n"
SA"m0,s36,49152\n"
RD"v[196:199],v243 offset:16384\n"
SA"s48,s60,s61\n"
MF"v[104:107],v[208:211],a[228:231],v[104:107]\n"
MF"v[112:115],v[208:211],a[232:235],v[112:115]\n"
BL"v238,s[20:23],s48 offen offset:0 lds\n"
MF"v[112:115],v[212:215],a[248:251],v[112:115]\n"
MF"v[120:123],v[212:215],a[252:255],v[120:123]\n"
RD"v[200:203],v242 offset:18432\n"
MF"v[120:123],v[208:211],a[236:239],v[120:123]\n"
MF"v[100:103],v[216:219],a[224:227],v[100:103]\n"
BL"v239,s[20:23],s48 offen offset:1024 lds\n"
MF"v[100:103],v[220:223],a[240:243],v[100:103]\n"
"s_cmp_eq_u32 s31,1\n"
"s_cselect_b32 s94,0,2130706432\n"
MF"v[108:111],v[220:223],a[244:247],v[108:111]\n"
"buffer_store_dwordx2 v[232:233],v230,s[8:11],s94 offen sc1\n"
MF"v[108:111],v[216:219],a[228:231],v[108:111]\n"
RD"v[204:207],v243 offset:18432\n"
"v_mov_b32 v227,0\n"
MF"v[116:119],v[216:219],a[232:235],v[116:119]\n"
WT"vmcnt(12)\n"
"v_mov_b32 v226,v160\n"
"v_mov_b32 v228,0\n"
MF"v[116:119],v[220:223],a[248:251],v[116:119]\n"
BL"v240,s[20:23],s48 offen offset:2048 lds\n"
MF"v[124:127],v[220:223],a[252:255],v[124:127]\n"
SA"s61,s61,524288\n"
RD"v[208:211],v242 offset:20480\n"
"v_med3_u32 v228,v227,v228,v161\n"
MF"v[124:127],v[216:219],a[236:239],v[124:127]\n"
WT"lgkmcnt(2)\n"
"v_med3_u32 v227,v226,v227,v161\n"
"v_max_u32 v226,v226,v161\n"
MF"v[128:131],v[192:195],a[0:3],0\n"
"v_med3_u32 v228,v227,v228,v162\n"
"v_med3_u32 v227,v226,v227,v162\n"
MF"v[128:131],v[196:199],a[16:19],v[128:131]\n"
RD"v[212:215],v243 offset:20480\n"
"v_max_u32 v226,v226,v162\n"
MF"v[136:139],v[196:199],a[20:23],0\n"
BL"v241,s[20:23],s48 offen offset:3072 lds\n"
MF"v[136:139],v[192:195],a[4:7],v[136:139]\n"
"v_med3_u32 v228,v227,v228,v163\n"
"v_med3_u32 v227,v226,v227,v163\n"
MF"v[144:147],v[192:195],a[8:11],0\n"
"v_max_u32 v226,v226,v163\n"
PK"v0,v0,v1\n"
MF"v[144:147],v[196:199],a[24:27],v[144:147]\n"
RD"v[216:219],v242 offset:22528\n"
PK"v1,v2,v3\n"
MF"v[152:155],v[196:199],a[28:31],0\n"
SA"m0,s36,32768\n"
WT"vmcnt(13)\n"
"v_med3_u32 v228,v227,v228,v164\n"
MF"v[152:155],v[192:195],a[12:15],v[152:155]\n"
SA"s48,s60,s61\n"
PK"v2,v4,v5\n"
PK"v3,v6,v7\n"
MF"v[132:135],v[200:203],a[0:3],0\n"
WT"lgkmcnt(1)\n"
BL"v238,s[20:23],s48 offen offset:0 lds\n"
MF"v[132:135],v[204:207],a[16:19],v[132:135]\n"
RD"v[220:223],v243 offset:22528\n"
"v_med3_u32 v227,v226,v227,v164\n"
MF"v[140:143],v[204:207],a[20:23],0\n"
"v_max_u32 v226,v226,v164\n"
"v_med3_u32 v228,v227,v228,v165\n"
MF"v[140:143],v[200:203],a[4:7],v[140:143]\n"
"v_med3_u32 v227,v226,v227,v165\n"
"v_max_u32 v226,v226,v165\n"
MF"v[148:151],v[200:203],a[8:11],0\n"
BL"v239,s[20:23],s48 offen offset:1024 lds\n"
MF"v[148:151],v[204:207],a[24:27],v[148:151]\n"
RD"v[192:195],v242 offset:40960\n"
"v_med3_u32 v228,v227,v228,v166\n"
MF"v[156:159],v[204:207],a[28:31],0\n"
PK"v8,v8,v9\n"
PK"v9,v10,v11\n"
MF"v[156:159],v[200:203],a[12:15],v[156:159]\n"
"v_med3_u32 v227,v226,v227,v166\n"
PK"v10,v12,v13\n"
MF"v[128:131],v[208:211],a[32:35],v[128:131]\n"
"s_mov_b32 s49,0\n"
PK"v11,v14,v15\n"
"v_max_u32 v226,v226,v166\n"
MF"v[128:131],v[212:215],a[48:51],v[128:131]\n"
RD"v[196:199],v243 offset:40960\n"
"v_med3_u32 v228,v227,v228,v167\n"
MF"v[136:139],v[212:215],a[52:55],v[136:139]\n"
SA"s61,s61,524288\n"
BL"v240,s[20:23],s48 offen offset:2048 lds\n"
MF"v[136:139],v[208:211],a[36:39],v[136:139]\n"
"s_cmp_eq_u32 s61,5242880\n"
BS"v[0:3],v250,s[8:11],s49 offen offset:0 sc1\n"
MF"v[144:147],v[208:211],a[40:43],v[144:147]\n"
"s_cselect_b32 s61,0,s61\n"
RD"v[200:203],v242 offset:43008\n"
"v_med3_u32 v227,v226,v227,v167\n"
MF"v[144:147],v[212:215],a[56:59],v[144:147]\n"
BL"v241,s[20:23],s48 offen offset:3072 lds\n"
MF"v[152:155],v[212:215],a[60:63],v[152:155]\n"
"v_max_u32 v226,v226,v167\n"
PK"v16,v16,v17\n"
MF"v[152:155],v[208:211],a[44:47],v[152:155]\n"
WT"lgkmcnt(2)\n"
WT"vmcnt(16)\n"
"v_med3_u32 v228,v227,v228,v168\n"
MF"v[132:135],v[216:219],a[32:35],v[132:135]\n"
RD"v[204:207],v243 offset:43008\n"
"v_med3_u32 v227,v226,v227,v168\n"
MF"v[132:135],v[220:223],a[48:51],v[132:135]\n"
PK"v17,v18,v19\n"
"v_max_u32 v226,v226,v168\n"
MF"v[140:143],v[220:223],a[52:55],v[140:143]\n"
"v_med3_u32 v228,v227,v228,v169\n"
PK"v18,v20,v21\n"
MF"v[140:143],v[216:219],a[36:39],v[140:143]\n"
PK"v19,v22,v23\n"
"v_med3_u32 v227,v226,v227,v169\n"
MF"v[148:151],v[216:219],a[40:43],v[148:151]\n"
RD"v[208:211],v242 offset:45056\n"
"v_max_u32 v226,v226,v169\n"
MF"v[148:151],v[220:223],a[56:59],v[148:151]\n"
"v_med3_u32 v228,v227,v228,v170\n"
"v_med3_u32 v227,v226,v227,v170\n"
MF"v[156:159],v[220:223],a[60:63],v[156:159]\n"
"v_max_u32 v226,v226,v170\n"
PK"v24,v24,v25\n"
MF"v[156:159],v[216:219],a[44:47],v[156:159]\n"
PK"v25,v26,v27\n"
"v_med3_u32 v228,v227,v228,v171\n"
MF"v[128:131],v[192:195],a[64:67],v[128:131]\n"
WT"lgkmcnt(1)\n"
RD"v[212:215],v243 offset:45056\n"
"v_med3_u32 v227,v226,v227,v171\n"
MF"v[128:131],v[196:199],a[80:83],v[128:131]\n"
PK"v26,v28,v29\n"
PK"v27,v30,v31\n"
MF"v[136:139],v[196:199],a[84:87],v[136:139]\n"
"v_max_u32 v226,v226,v171\n"
PK"v32,v32,v33\n"
MF"v[136:139],v[192:195],a[68:71],v[136:139]\n"
RD"v[216:219],v242 offset:47104\n"
PK"v33,v34,v35\n"
MF"v[144:147],v[192:195],a[72:75],v[144:147]\n"
WT"vmcnt(15)\n"
"v_med3_u32 v228,v227,v228,v172\n"
"v_med3_u32 v227,v226,v227,v172\n"
MF"v[144:147],v[196:199],a[88:91],v[144:147]\n"
"v_max_u32 v226,v226,v172\n"
"v_med3_u32 v228,v227,v228,v173\n"
MF"v[152:155],v[196:199],a[92:95],v[152:155]\n"
"v_med3_u32 v227,v226,v227,v173\n"
"v_max_u32 v226,v226,v173\n"
MF"v[152:155],v[192:195],a[76:79],v[152:155]\n"
RD"v[220:223],v243 offset:47104\n"
PK"v34,v36,v37\n"
MF"v[132:135],v[200:203],a[64:67],v[132:135]\n"
PK"v35,v38,v39\n"
"v_med3_u32 v228,v227,v228,v174\n"
MF"v[132:135],v[204:207],a[80:83],v[132:135]\n"
"v_med3_u32 v227,v226,v227,v174\n"
"v_max_u32 v226,v226,v174\n"
MF"v[140:143],v[204:207],a[84:87],v[140:143]\n"
"v_med3_u32 v228,v227,v228,v175\n"
"v_med3_u32 v227,v226,v227,v175\n"
MF"v[140:143],v[200:203],a[68:71],v[140:143]\n"
RD"v[192:195],v244 offset:16384\n"
"v_max_u32 v226,v226,v175\n"
MF"v[148:151],v[200:203],a[72:75],v[148:151]\n"
PK"v40,v40,v41\n"
PK"v41,v42,v43\n"
MF"v[148:151],v[204:207],a[88:91],v[148:151]\n"
WT"vmcnt(13)\n"
"v_med3_u32 v228,v227,v228,v176\n"
PK"v42,v44,v45\n"
MF"v[156:159],v[204:207],a[92:95],v[156:159]\n"
PK"v43,v46,v47\n"
"v_med3_u32 v227,v226,v227,v176\n"
MF"v[156:159],v[200:203],a[76:79],v[156:159]\n"
WT"lgkmcnt(1)\n"
RD"v[196:199],v245 offset:16384\n"
"v_max_u32 v226,v226,v176\n"
MF"v[128:131],v[208:211],a[96:99],v[128:131]\n"
"v_med3_u32 v228,v227,v228,v177\n"
"v_med3_u32 v227,v226,v227,v177\n"
MF"v[128:131],v[212:215],a[112:115],v[128:131]\n"
BS"v[8:11],v250,s[8:11],s49 offen offset:1024 sc1\n"
MF"v[136:139],v[212:215],a[116:119],v[136:139]\n"
"v_max_u32 v226,v226,v177\n"
"v_med3_u32 v228,v227,v228,v178\n"
MF"v[136:139],v[208:211],a[100:103],v[136:139]\n"
RD"v[200:203],v244 offset:18432\n"
PK"v48,v48,v49\n"
MF"v[144:147],v[208:211],a[104:107],v[144:147]\n"
PK"v49,v50,v51\n"
"v_med3_u32 v227,v226,v227,v178\n"
MF"v[144:147],v[212:215],a[120:123],v[144:147]\n"
PK"v50,v52,v53\n"
PK"v51,v54,v55\n"
MF"v[152:155],v[212:215],a[124:127],v[152:155]\n"
RD"v[204:207],v245 offset:18432\n"
"v_max_u32 v226,v226,v178\n"
MF"v[152:155],v[208:211],a[108:111],v[152:155]\n"
"v_med3_u32 v228,v227,v228,v179\n"
"v_med3_u32 v227,v226,v227,v179\n"
MF"v[132:135],v[216:219],a[96:99],v[132:135]\n"
"v_max_u32 v226,v226,v179\n"
PK"v56,v56,v57\n"
MF"v[132:135],v[220:223],a[112:115],v[132:135]\n"
WT"vmcnt(13)\n"
"v_med3_u32 v228,v227,v228,v180\n"
"v_med3_u32 v227,v226,v227,v180\n"
MF"v[140:143],v[220:223],a[116:119],v[140:143]\n"
RD"v[208:211],v244 offset:20480\n"
PK"v57,v58,v59\n"
MF"v[140:143],v[216:219],a[100:103],v[140:143]\n"
"v_max_u32 v226,v226,v180\n"
PK"v58,v60,v61\n"
MF"v[148:151],v[216:219],a[104:107],v[148:151]\n"
PK"v59,v62,v63\n"
"v_med3_u32 v228,v227,v228,v181\n"
MF"v[148:151],v[220:223],a[120:123],v[148:151]\n"
"v_med3_u32 v227,v226,v227,v181\n"
"v_max_u32 v226,v226,v181\n"
MF"v[156:159],v[220:223],a[124:127],v[156:159]\n"
RD"v[212:215],v245 offset:20480\n"
"v_med3_u32 v228,v227,v228,v182\n"
MF"v[156:159],v[216:219],a[108:111],v[156:159]\n"
WT"lgkmcnt(2)\n"
"v_med3_u32 v227,v226,v227,v182\n"
"v_max_u32 v226,v226,v182\n"
MF"v[128:131],v[192:195],a[128:131],v[128:131]\n"
PK"v64,v64,v65\n"
PK"v65,v66,v67\n"
MF"v[128:131],v[196:199],a[144:147],v[128:131]\n"
"v_med3_u32 v228,v227,v228,v183\n"
PK"v66,v68,v69\n"
MF"v[136:139],v[196:199],a[148:151],v[136:139]\n"
RD"v[216:219],v244 offset:22528\n"
PK"v67,v70,v71\n"
MF"v[136:139],v[192:195],a[132:135],v[136:139]\n"
"v_med3_u32 v227,v226,v227,v183\n"
"v_max_u32 v226,v226,v183\n"
MF"v[144:147],v[192:195],a[136:139],v[144:147]\n"
WT"vmcnt(12)\n"
"v_med3_u32 v228,v227,v228,v184\n"
"v_med3_u32 v227,v226,v227,v184\n"
MF"v[144:147],v[196:199],a[152:155],v[144:147]\n"
RD"v[220:223],v245 offset:22528\n"
"v_max_u32 v226,v226,v184\n"
MF"v[152:155],v[196:199],a[156:159],v[152:155]\n"
"v_med3_u32 v228,v227,v228,v185\n"
PK"v72,v72,v73\n"
MF"v[152:155],v[192:195],a[140:143],v[152:155]\n"
PK"v73,v74,v75\n"
"v_med3_u32 v227,v226,v227,v185\n"
MF"v[132:135],v[200:203],a[128:131],v[132:135]\n"
"v_max_u32 v226,v226,v185\n"
PK"v74,v76,v77\n"
MF"v[132:135],v[204:207],a[144:147],v[132:135]\n"
RD"v[192:195],v244 offset:40960\n"
PK"v75,v78,v79\n"
MF"v[140:143],v[204:207],a[148:151],v[140:143]\n"
"v_med3_u32 v228,v227,v228,v186\n"
"v_med3_u32 v227,v226,v227,v186\n"
MF"v[140:143],v[200:203],a[132:135],v[140:143]\n"
"v_max_u32 v226,v226,v186\n"
"v_med3_u32 v228,v227,v228,v187\n"
MF"v[148:151],v[200:203],a[136:139],v[148:151]\n"
"v_med3_u32 v227,v226,v227,v187\n"
PK"v80,v80,v81\n"
MF"v[148:151],v[204:207],a[152:155],v[148:151]\n"
RD"v[196:199],v245 offset:40960\n"
PK"v81,v82,v83\n"
MF"v[156:159],v[204:207],a[156:159],v[156:159]\n"
"v_max_u32 v226,v226,v187\n"
PK"v82,v84,v85\n"
MF"v[156:159],v[200:203],a[140:143],v[156:159]\n"
WT"lgkmcnt(2)\n"
WT"vmcnt(11)\n"
"v_med3_u32 v228,v227,v228,v188\n"
MF"v[128:131],v[208:211],a[160:163],v[128:131]\n"
BS"v[16:19],v250,s[8:11],s49 offen offset:2048 sc1\n"
MF"v[128:131],v[212:215],a[176:179],v[128:131]\n"
RD"v[200:203],v244 offset:43008\n"
PK"v83,v86,v87\n"
MF"v[136:139],v[212:215],a[180:183],v[136:139]\n"
"v_med3_u32 v227,v226,v227,v188\n"
"v_max_u32 v226,v226,v188\n"
MF"v[136:139],v[208:211],a[164:167],v[136:139]\n"
"v_med3_u32 v228,v227,v228,v189\n"
"v_med3_u32 v227,v226,v227,v189\n"
MF"v[144:147],v[208:211],a[168:171],v[144:147]\n"
"v_max_u32 v226,v226,v189\n"
PK"v88,v88,v89\n"
MF"v[144:147],v[212:215],a[184:187],v[144:147]\n"
RD"v[204:207],v245 offset:43008\n"
PK"v89,v90,v91\n"
MF"v[152:155],v[212:215],a[188:191],v[152:155]\n"
"v_med3_u32 v228,v227,v228,v190\n"
"v_med3_u32 v227,v226,v227,v190\n"
MF"v[152:155],v[208:211],a[172:175],v[152:155]\n"
PK"v90,v92,v93\n"
PK"v91,v94,v95\n"
MF"v[132:135],v[216:219],a[160:163],v[132:135]\n"
RD"v[208:211],v244 offset:45056\n"
"v_max_u32 v226,v226,v190\n"
MF"v[132:135],v[220:223],a[176:179],v[132:135]\n"
"v_med3_u32 v228,v227,v228,v191\n"
"v_med3_u32 v227,v226,v227,v191\n"
MF"v[140:143],v[220:223],a[180:183],v[140:143]\n"
"v_max_u32 v226,v226,v191\n"
"v_mov_b32 v229,v226\n"
MF"v[140:143],v[216:219],a[164:167],v[140:143]\n"
MF"v[148:151],v[216:219],a[168:171],v[148:151]\n"
RD"v[212:215],v245 offset:45056\n"
"v_max_u32_dpp v229,v229,v229 row_shr:1 row_mask:0xf bank_mask:0xf\n"
MF"v[148:151],v[220:223],a[184:187],v[148:151]\n"
MF"v[156:159],v[220:223],a[188:191],v[156:159]\n"
"v_max_u32_dpp v229,v229,v229 row_shr:2 row_mask:0xf bank_mask:0xf\n"
MF"v[156:159],v[216:219],a[172:175],v[156:159]\n"
WT"lgkmcnt(1)\n"
"v_max_u32_dpp v229,v229,v229 row_shr:4 row_mask:0xf bank_mask:0xf\n"
MF"v[128:131],v[192:195],a[192:195],v[128:131]\n"
RD"v[216:219],v244 offset:47104\n"
"v_max_u32_dpp v229,v229,v229 row_shr:8 row_mask:0xf bank_mask:0xf\n"
MF"v[128:131],v[196:199],a[208:211],v[128:131]\n"
MF"v[136:139],v[196:199],a[212:215],v[136:139]\n"
"v_max_u32_dpp v229,v229,v229 row_bcast:15 row_mask:0xa bank_mask:0xf\n"
MF"v[136:139],v[192:195],a[196:199],v[136:139]\n"
MF"v[144:147],v[192:195],a[200:203],v[144:147]\n"
RD"v[220:223],v245 offset:47104\n"
"v_max_u32_dpp v229,v229,v229 row_bcast:31 row_mask:0xc bank_mask:0xf\n"
MF"v[144:147],v[196:199],a[216:219],v[144:147]\n"
"v_readlane_b32 s62,v229,63\n"
MF"v[152:155],v[196:199],a[220:223],v[152:155]\n"
MF"v[152:155],v[192:195],a[204:207],v[152:155]\n"
"v_cmp_eq_u32 vcc,s62,v226\n"
"v_writelane_b32 v225,s62,0\n"
MF"v[132:135],v[200:203],a[192:195],v[132:135]\n"
"v_cndmask_b32 v226,v226,v227,vcc\n"
"v_cndmask_b32_e64 v227,v227,0,vcc\n"
MF"v[132:135],v[204:207],a[208:211],v[132:135]\n"
"v_mov_b32 v229,v226\n"
MF"v[140:143],v[204:207],a[212:215],v[140:143]\n"
MF"v[140:143],v[200:203],a[196:199],v[140:143]\n"
"v_max_u32_dpp v229,v229,v229 row_shr:1 row_mask:0xf bank_mask:0xf\n"
MF"v[148:151],v[200:203],a[200:203],v[148:151]\n"
MF"v[148:151],v[204:207],a[216:219],v[148:151]\n"
WT"lgkmcnt(0)\n"
"s_barrier\n"
MF"v[156:159],v[204:207],a[220:223],v[156:159]\n"
RD"v[192:195],v242 offset:0\n"
"v_max_u32_dpp v229,v229,v229 row_shr:2 row_mask:0xf bank_mask:0xf\n"
MF"v[156:159],v[200:203],a[204:207],v[156:159]\n"
BS"v[24:27],v250,s[8:11],s49 offen offset:3072 sc1\n"
MF"v[128:131],v[208:211],a[224:227],v[128:131]\n"
"v_max_u32_dpp v229,v229,v229 row_shr:4 row_mask:0xf bank_mask:0xf\n"
MF"v[128:131],v[212:215],a[240:243],v[128:131]\n"
MF"v[136:139],v[212:215],a[244:247],v[136:139]\n"
RD"v[196:199],v243 offset:0\n"
"v_max_u32_dpp v229,v229,v229 row_shr:8 row_mask:0xf bank_mask:0xf\n"
MF"v[136:139],v[208:211],a[228:231],v[136:139]\n"
MF"v[144:147],v[208:211],a[232:235],v[144:147]\n"
"v_max_u32_dpp v229,v229,v229 row_bcast:15 row_mask:0xa bank_mask:0xf\n"
MF"v[144:147],v[212:215],a[248:251],v[144:147]\n"
MF"v[152:155],v[212:215],a[252:255],v[152:155]\n"
RD"v[200:203],v242 offset:2048\n"
"v_max_u32_dpp v229,v229,v229 row_bcast:31 row_mask:0xc bank_mask:0xf\n"
MF"v[152:155],v[208:211],a[236:239],v[152:155]\n"
"v_readlane_b32 s62,v229,63\n"
MF"v[132:135],v[216:219],a[224:227],v[132:135]\n"
MF"v[132:135],v[220:223],a[240:243],v[132:135]\n"
"v_cmp_eq_u32 vcc,s62,v226\n"
"v_writelane_b32 v225,s62,1\n"
MF"v[140:143],v[220:223],a[244:247],v[140:143]\n"
RD"v[204:207],v243 offset:2048\n"
"v_cndmask_b32 v226,v226,v227,vcc\n"
MF"v[140:143],v[216:219],a[228:231],v[140:143]\n"
"v_cndmask_b32_e64 v227,v227,0,vcc\n"
"v_mov_b32 v229,v226\n"
MF"v[148:151],v[216:219],a[232:235],v[148:151]\n"
MF"v[148:151],v[220:223],a[248:251],v[148:151]\n"
"v_max_u32_dpp v229,v229,v229 row_shr:1 row_mask:0xf bank_mask:0xf\n"
MF"v[156:159],v[220:223],a[252:255],v[156:159]\n"
RD"v[208:211],v242 offset:4096\n"
"v_max_u32_dpp v229,v229,v229 row_shr:2 row_mask:0xf bank_mask:0xf\n"
MF"v[156:159],v[216:219],a[236:239],v[156:159]\n"
WT"lgkmcnt(2)\n"
"v_max_u32_dpp v229,v229,v229 row_shr:4 row_mask:0xf bank_mask:0xf\n"
MF"v[36:39],v[192:195],a[0:3],0\n"
MF"v[36:39],v[196:199],a[16:19],v[36:39]\n"
RD"v[212:215],v243 offset:4096\n"
"v_max_u32_dpp v229,v229,v229 row_shr:8 row_mask:0xf bank_mask:0xf\n"
MF"v[44:47],v[196:199],a[20:23],0\n"
MF"v[44:47],v[192:195],a[4:7],v[44:47]\n"
"v_max_u32_dpp v229,v229,v229 row_bcast:15 row_mask:0xa bank_mask:0xf\n"
PK"v4,v128,v129\n"
MF"v[52:55],v[192:195],a[8:11],0\n"
"v_max_u32_dpp v229,v229,v229 row_bcast:31 row_mask:0xc bank_mask:0xf\n"
PK"v5,v130,v131\n"
MF"v[52:55],v[196:199],a[24:27],v[52:55]\n"
RD"v[216:219],v242 offset:6144\n"
"v_readlane_b32 s62,v229,63\n"
MF"v[60:63],v[196:199],a[28:31],0\n"
PK"v6,v132,v133\n"
"v_cmp_eq_u32 vcc,s62,v226\n"
MF"v[60:63],v[192:195],a[12:15],v[60:63]\n"
"v_writelane_b32 v225,s62,2\n"
"v_cndmask_b32 v226,v226,v227,vcc\n"
MF"v[68:71],v[200:203],a[0:3],0\n"
WT"lgkmcnt(1)\n"
"v_cndmask_b32_e64 v227,v227,0,vcc\n"
"v_mov_b32 v229,v226\n"
MF"v[68:71],v[204:207],a[16:19],v[68:71]\n"
RD"v[220:223],v243 offset:6144\n"
"v_max_u32_dpp v229,v229,v229 row_shr:1 row_mask:0xf bank_mask:0xf\n"
MF"v[76:79],v[204:207],a[20:23],0\n"
PK"v7,v134,v135\n"
"v_max_u32_dpp v229,v229,v229 row_shr:2 row_mask:0xf bank_mask:0xf\n"
MF"v[76:79],v[200:203],a[4:7],v[76:79]\n"
PK"v12,v136,v137\n"
"v_max_u32_dpp v229,v229,v229 row_shr:4 row_mask:0xf bank_mask:0xf\n"
MF"v[84:87],v[200:203],a[8:11],0\n"
"s_mov_b32 s49,8192\n"
"v_max_u32_dpp v229,v229,v229 row_shr:8 row_mask:0xf bank_mask:0xf\n"
PK"v13,v138,v139\n"
MF"v[84:87],v[204:207],a[24:27],v[84:87]\n"
RD"v[192:195],v242 offset:24576\n"
"v_max_u32_dpp v229,v229,v229 row_bcast:15 row_mask:0xa bank_mask:0xf\n"
MF"v[92:95],v[204:207],a[28:31],0\n"
BS"v[32:35],v250,s[8:11],s49 offen offset:0 sc1\n"
MF"v[92:95],v[200:203],a[12:15],v[92:95]\n"
"v_max_u32_dpp v229,v229,v229 row_bcast:31 row_mask:0xc bank_mask:0xf\n"
PK"v14,v140,v141\n"
MF"v[36:39],v[208:211],a[32:35],v[36:39]\n"
"v_readlane_b32 s62,v229,63\n"
PK"v15,v142,v143\n"
MF"v[36:39],v[212:215],a[48:51],v[36:39]\n"
RD"v[196:199],v243 offset:24576\n"
"v_cmp_eq_u32 vcc,s62,v226\n"
MF"v[44:47],v[212:215],a[52:55],v[44:47]\n"
"v_writelane_b32 v225,s62,3\n"
"v_cndmask_b32 v226,v226,v227,vcc\n"
MF"v[44:47],v[208:211],a[36:39],v[44:47]\n"
"v_cndmask_b32_e64 v227,v227,0,vcc\n"
"v_mov_b32 v229,v226\n"
MF"v[52:55],v[208:211],a[40:43],v[52:55]\n"
RD"v[200:203],v242 offset:26624\n"
"v_max_u32_dpp v229,v229,v229 row_shr:1 row_mask:0xf bank_mask:0xf\n"
MF"v[52:55],v[212:215],a[56:59],v[52:55]\n"
PK"v20,v144,v145\n"
"v_max_u32_dpp v229,v229,v229 row_shr:2 row_mask:0xf bank_mask:0xf\n"
MF"v[60:63],v[212:215],a[60:63],v[60:63]\n"
PK"v21,v146,v147\n"
"v_max_u32_dpp v229,v229,v229 row_shr:4 row_mask:0xf bank_mask:0xf\n"
MF"v[60:63],v[208:211],a[44:47],v[60:63]\n"
WT"lgkmcnt(2)\n"
"v_max_u32_dpp v229,v229,v229 row_shr:8 row_mask:0xf bank_mask:0xf\n"
PK"v22,v148,v149\n"
MF"v[68:71],v[216:219],a[32:35],v[68:71]\n"
RD"v[204:207],v243 offset:26624\n"
"v_max_u32_dpp v229,v229,v229 row_bcast:15 row_mask:0xa bank_mask:0xf\n"
MF"v[68:71],v[220:223],a[48:51],v[68:71]\n"
PK"v23,v150,v151\n"
"v_max_u32_dpp v229,v229,v229 row_bcast:31 row_mask:0xc bank_mask:0xf\n"
MF"v[76:79],v[220:223],a[52:55],v[76:79]\n"
"v_readlane_b32 s62,v229,63\n"
PK"v28,v152,v153\n"
MF"v[76:79],v[216:219],a[36:39],v[76:79]\n"
"v_cmp_eq_u32 vcc,s62,v226\n"
"v_writelane_b32 v225,s62,4\n"
MF"v[84:87],v[216:219],a[40:43],v[84:87]\n"
RD"v[208:211],v242 offset:28672\n"
"v_cndmask_b32 v226,v226,v227,vcc\n"
MF"v[84:87],v[220:223],a[56:59],v[84:87]\n"
"v_cndmask_b32_e64 v227,v227,0,vcc\n"
"v_mov_b32 v229,v226\n"
MF"v[92:95],v[220:223],a[60:63],v[92:95]\n"
PK"v29,v154,v155\n"
"v_max_u32_dpp v229,v229,v229 row_shr:1 row_mask:0xf bank_mask:0xf\n"
MF"v[92:95],v[216:219],a[44:47],v[92:95]\n"
PK"v30,v156,v157\n"
"v_max_u32_dpp v229,v229,v229 row_shr:2 row_mask:0xf bank_mask:0xf\n"
MF"v[36:39],v[192:195],a[64:67],v[36:39]\n"
WT"lgkmcnt(1)\n"
RD"v[212:215],v243 offset:28672\n"
"v_max_u32_dpp v229,v229,v229 row_shr:4 row_mask:0xf bank_mask:0xf\n"
MF"v[36:39],v[196:199],a[80:83],v[36:39]\n"
PK"v31,v158,v159\n"
"v_max_u32_dpp v229,v229,v229 row_shr:8 row_mask:0xf bank_mask:0xf\n"
MF"v[44:47],v[196:199],a[84:87],v[44:47]\n"
MF"v[44:47],v[192:195],a[68:71],v[44:47]\n"
RD"v[216:219],v242 offset:30720\n"
"v_max_u32_dpp v229,v229,v229 row_bcast:15 row_mask:0xa bank_mask:0xf\n"
MF"v[52:55],v[192:195],a[72:75],v[52:55]\n"
MF"v[52:55],v[196:199],a[88:91],v[52:55]\n"
"v_max_u32_dpp v229,v229,v229 row_bcast:31 row_mask:0xc bank_mask:0xf\n"
MF"v[60:63],v[196:199],a[92:95],v[60:63]\n"
"v_readlane_b32 s62,v229,63\n"
MF"v[60:63],v[192:195],a[76:79],v[60:63]\n"
RD"v[220:223],v243 offset:30720\n"
"v_cmp_eq_u32 vcc,s62,v226\n"
MF"v[68:71],v[200:203],a[64:67],v[68:71]\n"
"v_writelane_b32 v225,s62,5\n"
"v_cndmask_b32 v226,v226,v227,vcc\n"
MF"v[68:71],v[204:207],a[80:83],v[68:71]\n"
"v_cndmask_b32_e64 v227,v227,0,vcc\n"
"v_mov_b32 v229,v226\n"
MF"v[76:79],v[204:207],a[84:87],v[76:79]\n"
MF"v[76:79],v[200:203],a[68:71],v[76:79]\n"
RD"v[192:195],v244 offset:0\n"
"v_max_u32_dpp v229,v229,v229 row_shr:1 row_mask:0xf bank_mask:0xf\n"
MF"v[84:87],v[200:203],a[72:75],v[84:87]\n"
MF"v[84:87],v[204:207],a[88:91],v[84:87]\n"
BS"v[40:43],v250,s[8:11],s49 offen offset:1024 sc1\n"
MF"v[92:95],v[204:207],a[92:95],v[92:95]\n"
"v_max_u32_dpp v229,v229,v229 row_shr:2 row_mask:0xf bank_mask:0xf\n"
MF"v[92:95],v[200:203],a[76:79],v[92:95]\n"
WT"lgkmcnt(1)\n"
RD"v[196:199],v245 offset:0\n"
"v_max_u32_dpp v229,v229,v229 row_shr:4 row_mask:0xf bank_mask:0xf\n"
MF"v[36:39],v[208:211],a[96:99],v[36:39]\n"
MF"v[36:39],v[212:215],a[112:115],v[36:39]\n"
"v_max_u32_dpp v229,v229,v229 row_shr:8 row_mask:0xf bank_mask:0xf\n"
MF"v[44:47],v[212:215],a[116:119],v[44:47]\n"
MF"v[44:47],v[208:211],a[100:103],v[44:47]\n"
RD"v[200:203],v244 offset:2048\n"
"v_max_u32_dpp v229,v229,v229 row_bcast:15 row_mask:0xa bank_mask:0xf\n"
MF"v[52:55],v[208:211],a[104:107],v[52:55]\n"
MF"v[52:55],v[212:215],a[120:123],v[52:55]\n"
"v_max_u32_dpp v229,v229,v229 row_bcast:31 row_mask:0xc bank_mask:0xf\n"
MF"v[60:63],v[212:215],a[124:127],v[60:63]\n"
RD"v[204:207],v245 offset:2048\n"
"v_readlane_b32 s62,v229,63\n"
MF"v[60:63],v[208:211],a[108:111],v[60:63]\n"
MF"v[68:71],v[216:219],a[96:99],v[68:71]\n"
"v_cmp_eq_u32 vcc,s62,v226\n"
"v_writelane_b32 v225,s62,6\n"
MF"v[68:71],v[220:223],a[112:115],v[68:71]\n"
"v_cndmask_b32 v226,v226,v227,vcc\n"
"v_cndmask_b32_e64 v227,v227,0,vcc\n"
MF"v[76:79],v[220:223],a[116:119],v[76:79]\n"
SA"m0,s37,16384\n"
RD"v[208:211],v244 offset:4096\n"
"v_mov_b32 v229,v226\n"
MF"v[76:79],v[216:219],a[100:103],v[76:79]\n"
SA"s48,s39,98304\n"
"v_max_u32_dpp v229,v229,v229 row_shr:1 row_mask:0xf bank_mask:0xf\n"
MF"v[84:87],v[216:219],a[104:107],v[84:87]\n"
BL"v234,s[12:15],s48 offen offset:0 lds\n"
MF"v[84:87],v[220:223],a[120:123],v[84:87]\n"
"v_max_u32_dpp v229,v229,v229 row_shr:2 row_mask:0xf bank_mask:0xf\n"
PK"v96,v96,v97\n"
MF"v[92:95],v[220:223],a[124:127],v[92:95]\n"
RD"v[212:215],v245 offset:4096\n"
"v_max_u32_dpp v229,v229,v229 row_shr:4 row_mask:0xf bank_mask:0xf\n"
MF"v[92:95],v[216:219],a[108:111],v[92:95]\n"
WT"lgkmcnt(2)\n"
"v_max_u32_dpp v229,v229,v229 row_shr:8 row_mask:0xf bank_mask:0xf\n"
PK"v97,v98,v99\n"
MF"v[36:39],v[192:195],a[128:131],v[36:39]\n"
"v_max_u32_dpp v229,v229,v229 row_bcast:15 row_mask:0xa bank_mask:0xf\n"
PK"v98,v100,v101\n"
MF"v[36:39],v[196:199],a[144:147],v[36:39]\n"
"v_max_u32_dpp v229,v229,v229 row_bcast:31 row_mask:0xc bank_mask:0xf\n"
PK"v99,v102,v103\n"
MF"v[44:47],v[196:199],a[148:151],v[44:47]\n"
RD"v[216:219],v244 offset:6144\n"
"v_readlane_b32 s62,v229,63\n"
MF"v[44:47],v[192:195],a[132:135],v[44:47]\n"
BL"v235,s[12:15],s48 offen offset:1024 lds\n"
MF"v[52:55],v[192:195],a[136:139],v[52:55]\n"
"v_cmp_eq_u32 vcc,s62,v226\n"
"v_writelane_b32 v225,s62,7\n"
MF"v[52:55],v[196:199],a[152:155],v[52:55]\n"
RD"v[220:223],v245 offset:6144\n"
"v_cndmask_b32 v226,v226,v227,vcc\n"
MF"v[60:63],v[196:199],a[156:159],v[60:63]\n"
"v_cndmask_b32_e64 v227,v227,0,vcc\n"
"v_mov_b32 v229,v228\n"
MF"v[60:63],v[192:195],a[140:143],v[60:63]\n"
PK"v104,v104,v105\n"
"v_max_u32_dpp v229,v229,v229 row_shr:1 row_mask:0xf bank_mask:0xf\n"
MF"v[68:71],v[200:203],a[128:131],v[68:71]\n"
BL"v236,s[12:15],s48 offen offset:2048 lds\n"
MF"v[68:71],v[204:207],a[144:147],v[68:71]\n"
RD"v[192:195],v244 offset:24576\n"
"v_max_u32_dpp v229,v229,v229 row_shr:2 row_mask:0xf bank_mask:0xf\n"
MF"v[76:79],v[204:207],a[148:151],v[76:79]\n"
PK"v105,v106,v107\n"
"v_max_u32_dpp v229,v229,v229 row_shr:4 row_mask:0xf bank_mask:0xf\n"
MF"v[76:79],v[200:203],a[132:135],v[76:79]\n"
PK"v106,v108,v109\n"
"v_max_u32_dpp v229,v229,v229 row_shr:8 row_mask:0xf bank_mask:0xf\n"
MF"v[84:87],v[200:203],a[136:139],v[84:87]\n"
BS"v[48:51],v250,s[8:11],s49 offen offset:2048 sc1\n"
MF"v[84:87],v[204:207],a[152:155],v[84:87]\n"
RD"v[196:199],v245 offset:24576\n"
"v_max_u32_dpp v229,v229,v229 row_bcast:15 row_mask:0xa bank_mask:0xf\n"
MF"v[92:95],v[204:207],a[156:159],v[92:95]\n"
BL"v237,s[12:15],s48 offen offset:3072 lds\n"
MF"v[92:95],v[200:203],a[140:143],v[92:95]\n"
WT"lgkmcnt(2)\n"
"v_max_u32_dpp v229,v229,v229 row_bcast:31 row_mask:0xc bank_mask:0xf\n"
PK"v107,v110,v111\n"
MF"v[36:39],v[208:211],a[160:163],v[36:39]\n"
"v_readlane_b32 s63,v229,63\n"
PK"v112,v112,v113\n"
MF"v[36:39],v[212:215],a[176:179],v[36:39]\n"
"s_cmp_gt_u32 s63,s62\n"
"s_cbranch_scc1 L1007_%=\n"
"L1008_%=:\n"
MF"v[44:47],v[212:215],a[180:183],v[44:47]\n"
SA"m0,s37,20480\n"
RD"v[200:203],v244 offset:26624\n"
"v_lshrrev_b32 v229,2,v224\n"
MF"v[44:47],v[208:211],a[164:167],v[44:47]\n"
SA"s48,s98,98304\n"
"ds_write_b32 v229,v225 offset:16384\n"
MF"v[52:55],v[208:211],a[168:171],v[52:55]\n"
BL"v234,s[12:15],s48 offen offset:0 lds\n"
MF"v[52:55],v[212:215],a[184:187],v[52:55]\n"
RD"v[204:207],v245 offset:26624\n"
PK"v113,v114,v115\n"
MF"v[60:63],v[212:215],a[188:191],v[60:63]\n"
PK"v114,v116,v117\n"
PK"v115,v118,v119\n"
MF"v[60:63],v[208:211],a[172:175],v[60:63]\n"
PK"v120,v120,v121\n"
PK"v121,v122,v123\n"
MF"v[68:71],v[216:219],a[160:163],v[68:71]\n"
RD"v[208:211],v244 offset:28672\n"
PK"v122,v124,v125\n"
MF"v[68:71],v[220:223],a[176:179],v[68:71]\n"
PK"v123,v126,v127\n"
MF"v[76:79],v[220:223],a[180:183],v[76:79]\n"
BL"v235,s[12:15],s48 offen offset:1024 lds\n"
MF"v[76:79],v[216:219],a[164:167],v[76:79]\n"
MF"v[84:87],v[216:219],a[168:171],v[84:87]\n"
RD"v[212:215],v245 offset:28672\n"
MF"v[84:87],v[220:223],a[184:187],v[84:87]\n"
MF"v[92:95],v[220:223],a[188:191],v[92:95]\n"
MF"v[92:95],v[216:219],a[172:175],v[92:95]\n"
WT"lgkmcnt(1)\n"
MF"v[36:39],v[192:195],a[192:195],v[36:39]\n"
RD"v[216:219],v244 offset:30720\n"
MF"v[36:39],v[196:199],a[208:211],v[36:39]\n"
BL"v236,s[12:15],s48 offen offset:2048 lds\n"
MF"v[44:47],v[196:199],a[212:215],v[44:47]\n"
MF"v[44:47],v[192:195],a[196:199],v[44:47]\n"
MF"v[52:55],v[192:195],a[200:203],v[52:55]\n"
RD"v[220:223],v245 offset:30720\n"
MF"v[52:55],v[196:199],a[216:219],v[52:55]\n"
MF"v[60:63],v[196:199],a[220:223],v[60:63]\n"
BL"v237,s[12:15],s48 offen offset:3072 lds\n"
MF"v[60:63],v[192:195],a[204:207],v[60:63]\n"
MF"v[68:71],v[200:203],a[192:195],v[68:71]\n"
MF"v[68:71],v[204:207],a[208:211],v[68:71]\n"
MF"v[76:79],v[204:207],a[212:215],v[76:79]\n"
MF"v[76:79],v[200:203],a[196:199],v[76:79]\n"
BS"v[56:59],v250,s[8:11],s49 offen offset:3072 sc1\n"
MF"v[84:87],v[200:203],a[200:203],v[84:87]\n"
"v_lshrrev_b32 v251,2,v224\n"
"v_and_b32 v251,28,v251\n"
MF"v[84:87],v[204:207],a[216:219],v[84:87]\n"
WT"lgkmcnt(0)\n"
"s_barrier\n"
MF"v[92:95],v[204:207],a[220:223],v[92:95]\n"
RD"v[192:195],v242 offset:8192\n"
"ds_read_b32 v234,v251 offset:16384\n"
MF"v[92:95],v[200:203],a[204:207],v[92:95]\n"
MF"v[36:39],v[208:211],a[224:227],v[36:39]\n"
MF"v[36:39],v[212:215],a[240:243],v[36:39]\n"
MF"v[44:47],v[212:215],a[244:247],v[44:47]\n"
RD"v[196:199],v243 offset:8192\n"
MF"v[44:47],v[208:211],a[228:231],v[44:47]\n"
MF"v[52:55],v[208:211],a[232:235],v[52:55]\n"
MF"v[52:55],v[212:215],a[248:251],v[52:55]\n"
MF"v[60:63],v[212:215],a[252:255],v[60:63]\n"
RD"v[200:203],v242 offset:10240\n"
MF"v[60:63],v[208:211],a[236:239],v[60:63]\n"
WT"lgkmcnt(2)\n"
"v_not_b32 v251,v234\n"
"v_bfe_u32 v252,v251,4,7\n"
MF"v[68:71],v[216:219],a[224:227],v[68:71]\n"
"v_mul_u32_u24 v252,20480,v252\n"
MF"v[68:71],v[220:223],a[240:243],v[68:71]\n"
"v_and_b32 v251,15,v251\n"
MF"v[76:79],v[220:223],a[244:247],v[76:79]\n"
RD"v[204:207],v243 offset:10240\n"
"v_lshl_add_u32 v251,v251,4,v252\n"
MF"v[76:79],v[216:219],a[228:231],v[76:79]\n"
"v_and_b32 v252,0x380,v224\n"
MF"v[84:87],v[216:219],a[232:235],v[84:87]\n"
"v_lshl_add_u32 v251,v252,1,v251\n"
MF"v[84:87],v[220:223],a[248:251],v[84:87]\n"
"v_add_u32 v252,v252,v224\n"
MF"v[92:95],v[220:223],a[252:255],v[92:95]\n"
"s_lshl_b32 s92,s34,2\n"
RD"v[208:211],v242 offset:12288\n"
MF"v[92:95],v[216:219],a[236:239],v[92:95]\n"
SA"s92,s92,s30\n"
MF"v[128:131],v[192:195],a[0:3],0\n"
WT"lgkmcnt(2)\n"
"s_lshl_b32 s93,s92,11\n"
MF"v[128:131],v[196:199],a[16:19],v[128:131]\n"
"s_cmp_lt_u32 s92,10\n"
RD"v[212:215],v243 offset:12288\n"
"s_cselect_b32 s93,s93,2130706432\n"
MF"v[136:139],v[196:199],a[20:23],0\n"
SA"s92,s93,65536\n"
MF"v[136:139],v[192:195],a[4:7],v[136:139]\n"
BL"v[100:103],v251,s[8:11],s92 offen sc1\n"
MF"v[144:147],v[192:195],a[8:11],0\n"
"s_mul_i32 s92,s35,20480\n"
PK"v36,v36,v37\n"
PK"v37,v38,v39\n"
MF"v[144:147],v[196:199],a[24:27],v[144:147]\n"
SA"s92,s92,s93\n"
RD"v[216:219],v242 offset:14336\n"
PK"v38,v68,v69\n"
MF"v[152:155],v[196:199],a[28:31],0\n"
SA"s92,s92,2770944\n"
PK"v39,v70,v71\n"
MF"v[152:155],v[192:195],a[12:15],v[152:155]\n"
MF"v[132:135],v[200:203],a[0:3],0\n"
WT"lgkmcnt(1)\n"
PK"v44,v44,v45\n"
PK"v45,v46,v47\n"
MF"v[132:135],v[204:207],a[16:19],v[132:135]\n"
"s_mov_b32 s49,16384\n"
RD"v[220:223],v243 offset:14336\n"
PK"v46,v76,v77\n"
MF"v[140:143],v[204:207],a[20:23],0\n"
BS"v[64:67],v250,s[8:11],s49 offen offset:0 sc1\n"
MF"v[140:143],v[200:203],a[4:7],v[140:143]\n"
PK"v47,v78,v79\n"
MF"v[148:151],v[200:203],a[8:11],0\n"
MF"v[148:151],v[204:207],a[24:27],v[148:151]\n"
RD"v[192:195],v242 offset:32768\n"
PK"v52,v52,v53\n"
MF"v[156:159],v[204:207],a[28:31],0\n"
PK"v53,v54,v55\n"
PK"v54,v84,v85\n"
MF"v[156:159],v[200:203],a[12:15],v[156:159]\n"
PK"v55,v86,v87\n"
MF"v[128:131],v[208:211],a[32:35],v[128:131]\n"
MF"v[128:131],v[212:215],a[48:51],v[128:131]\n"
RD"v[196:199],v243 offset:32768\n"
MF"v[136:139],v[212:215],a[52:55],v[136:139]\n"
PK"v60,v60,v61\n"
PK"v61,v62,v63\n"
MF"v[136:139],v[208:211],a[36:39],v[136:139]\n"
PK"v62,v92,v93\n"
PK"v63,v94,v95\n"
MF"v[144:147],v[208:211],a[40:43],v[144:147]\n"
SA"m0,s36,0\n"
RD"v[200:203],v242 offset:34816\n"
SA"s48,s60,s61\n"
MF"v[144:147],v[212:215],a[56:59],v[144:147]\n"
MF"v[152:155],v[212:215],a[60:63],v[152:155]\n"
BL"v238,s[20:23],s48 offen offset:0 lds\n"
MF"v[152:155],v[208:211],a[44:47],v[152:155]\n"
WT"lgkmcnt(2)\n"
MF"v[132:135],v[216:219],a[32:35],v[132:135]\n"
RD"v[204:207],v243 offset:34816\n"
MF"v[132:135],v[220:223],a[48:51],v[132:135]\n"
MF"v[140:143],v[220:223],a[52:55],v[140:143]\n"
MF"v[140:143],v[216:219],a[36:39],v[140:143]\n"
MF"v[148:151],v[216:219],a[40:43],v[148:151]\n"
RD"v[208:211],v242 offset:36864\n"
MF"v[148:151],v[220:223],a[56:59],v[148:151]\n"
MF"v[156:159],v[220:223],a[60:63],v[156:159]\n"
BL"v239,s[20:23],s48 offen offset:1024 lds\n"
MF"v[156:159],v[216:219],a[44:47],v[156:159]\n"
MF"v[128:131],v[192:195],a[64:67],v[128:131]\n"
WT"lgkmcnt(1)\n"
RD"v[212:215],v243 offset:36864\n"
MF"v[128:131],v[196:199],a[80:83],v[128:131]\n"
MF"v[136:139],v[196:199],a[84:87],v[136:139]\n"
MF"v[136:139],v[192:195],a[68:71],v[136:139]\n"
RD"v[216:219],v242 offset:38912\n"
MF"v[144:147],v[192:195],a[72:75],v[144:147]\n"
MF"v[144:147],v[196:199],a[88:91],v[144:147]\n"
MF"v[152:155],v[196:199],a[92:95],v[152:155]\n"
BL"v240,s[20:23],s48 offen offset:2048 lds\n"
MF"v[152:155],v[192:195],a[76:79],v[152:155]\n"
RD"v[220:223],v243 offset:38912\n"
MF"v[132:135],v[200:203],a[64:67],v[132:135]\n"
MF"v[132:135],v[204:207],a[80:83],v[132:135]\n"
BS"v[72:75],v250,s[8:11],s49 offen offset:1024 sc1\n"
MF"v[140:143],v[204:207],a[84:87],v[140:143]\n"
MF"v[140:143],v[200:203],a[68:71],v[140:143]\n"
RD"v[192:195],v244 offset:8192\n"
MF"v[148:151],v[200:203],a[72:75],v[148:151]\n"
MF"v[148:151],v[204:207],a[88:91],v[148:151]\n"
MF"v[156:159],v[204:207],a[92:95],v[156:159]\n"
SA"s61,s61,524288\n"
BL"v241,s[20:23],s48 offen offset:3072 lds\n"
MF"v[156:159],v[200:203],a[76:79],v[156:159]\n"
WT"lgkmcnt(1)\n"
RD"v[196:199],v245 offset:8192\n"
MF"v[128:131],v[208:211],a[96:99],v[128:131]\n"
MF"v[128:131],v[212:215],a[112:115],v[128:131]\n"
MF"v[136:139],v[212:215],a[116:119],v[136:139]\n"
MF"v[136:139],v[208:211],a[100:103],v[136:139]\n"
RD"v[200:203],v244 offset:10240\n"
MF"v[144:147],v[208:211],a[104:107],v[144:147]\n"
MF"v[144:147],v[212:215],a[120:123],v[144:147]\n"
MF"v[152:155],v[212:215],a[124:127],v[152:155]\n"
RD"v[204:207],v245 offset:10240\n"
MF"v[152:155],v[208:211],a[108:111],v[152:155]\n"
MF"v[132:135],v[216:219],a[96:99],v[132:135]\n"
MF"v[132:135],v[220:223],a[112:115],v[132:135]\n"
MF"v[140:143],v[220:223],a[116:119],v[140:143]\n"
RD"v[208:211],v244 offset:12288\n"
MF"v[140:143],v[216:219],a[100:103],v[140:143]\n"
MF"v[148:151],v[216:219],a[104:107],v[148:151]\n"
SA"m0,s38,65536\n"
SA"s48,s60,s61\n"
MF"v[148:151],v[220:223],a[120:123],v[148:151]\n"
MF"v[156:159],v[220:223],a[124:127],v[156:159]\n"
RD"v[212:215],v245 offset:12288\n"
MF"v[156:159],v[216:219],a[108:111],v[156:159]\n"
WT"lgkmcnt(2)\n"
BL"v238,s[20:23],s48 offen offset:0 lds\n"
MF"v[128:131],v[192:195],a[128:131],v[128:131]\n"
MF"v[128:131],v[196:199],a[144:147],v[128:131]\n"
MF"v[136:139],v[196:199],a[148:151],v[136:139]\n"
RD"v[216:219],v244 offset:14336\n"
MF"v[136:139],v[192:195],a[132:135],v[136:139]\n"
MF"v[144:147],v[192:195],a[136:139],v[144:147]\n"
MF"v[144:147],v[196:199],a[152:155],v[144:147]\n"
RD"v[220:223],v245 offset:14336\n"
MF"v[152:155],v[196:199],a[156:159],v[152:155]\n"
BL"v239,s[20:23],s48 offen offset:1024 lds\n"
MF"v[152:155],v[192:195],a[140:143],v[152:155]\n"
MF"v[132:135],v[200:203],a[128:131],v[132:135]\n"
BS"v[80:83],v250,s[8:11],s49 offen offset:2048 sc1\n"
MF"v[132:135],v[204:207],a[144:147],v[132:135]\n"
RD"v[192:195],v244 offset:32768\n"
MF"v[140:143],v[204:207],a[148:151],v[140:143]\n"
MF"v[140:143],v[200:203],a[132:135],v[140:143]\n"
MF"v[148:151],v[200:203],a[136:139],v[148:151]\n"
MF"v[148:151],v[204:207],a[152:155],v[148:151]\n"
RD"v[196:199],v245 offset:32768\n"
MF"v[156:159],v[204:207],a[156:159],v[156:159]\n"
BL"v240,s[20:23],s48 offen offset:2048 lds\n"
MF"v[156:159],v[200:203],a[140:143],v[156:159]\n"
WT"lgkmcnt(2)\n"
MF"v[128:131],v[208:211],a[160:163],v[128:131]\n"
MF"v[128:131],v[212:215],a[176:179],v[128:131]\n"
RD"v[200:203],v244 offset:34816\n"
MF"v[136:139],v[212:215],a[180:183],v[136:139]\n"
MF"v[136:139],v[208:211],a[164:167],v[136:139]\n"
MF"v[144:147],v[208:211],a[168:171],v[144:147]\n"
MF"v[144:147],v[212:215],a[184:187],v[144:147]\n"
RD"v[204:207],v245 offset:34816\n"
MF"v[152:155],v[212:215],a[188:191],v[152:155]\n"
SA"s61,s61,524288\n"
BL"v241,s[20:23],s48 offen offset:3072 lds\n"
MF"v[152:155],v[208:211],a[172:175],v[152:155]\n"
"s_cmp_eq_u32 s61,5242880\n"
"s_cselect_b32 s61,0,s61\n"
MF"v[132:135],v[216:219],a[160:163],v[132:135]\n"
RD"v[208:211],v244 offset:36864\n"
MF"v[132:135],v[220:223],a[176:179],v[132:135]\n"
MF"v[140:143],v[220:223],a[180:183],v[140:143]\n"
MF"v[140:143],v[216:219],a[164:167],v[140:143]\n"
MF"v[148:151],v[216:219],a[168:171],v[148:151]\n"
SA"m0,s38,114688\n"
RD"v[212:215],v245 offset:36864\n"
SA"s48,s60,s61\n"
MF"v[148:151],v[220:223],a[184:187],v[148:151]\n"
MF"v[156:159],v[220:223],a[188:191],v[156:159]\n"
BL"v238,s[20:23],s48 offen offset:0 lds\n"
MF"v[156:159],v[216:219],a[172:175],v[156:159]\n"
WT"lgkmcnt(1)\n"
MF"v[128:131],v[192:195],a[192:195],v[128:131]\n"
RD"v[216:219],v244 offset:38912\n"
MF"v[128:131],v[196:199],a[208:211],v[128:131]\n"
MF"v[136:139],v[196:199],a[212:215],v[136:139]\n"
MF"v[136:139],v[192:195],a[196:199],v[136:139]\n"
MF"v[144:147],v[192:195],a[200:203],v[144:147]\n"
RD"v[220:223],v245 offset:38912\n"
MF"v[144:147],v[196:199],a[216:219],v[144:147]\n"
MF"v[152:155],v[196:199],a[220:223],v[152:155]\n"
BL"v239,s[20:23],s48 offen offset:1024 lds\n"
MF"v[152:155],v[192:195],a[204:207],v[152:155]\n"
BS"v[88:91],v250,s[8:11],s49 offen offset:3072 sc1\n"
MF"v[132:135],v[200:203],a[192:195],v[132:135]\n"
WT"vmcnt(14)\n"
BS"v[100:103],v252,s[8:11],s92 offen sc1\n"
MF"v[132:135],v[204:207],a[208:211],v[132:135]\n"
MF"v[140:143],v[204:207],a[212:215],v[140:143]\n"
MF"v[140:143],v[200:203],a[196:199],v[140:143]\n"
MF"v[148:151],v[200:203],a[200:203],v[148:151]\n"
MF"v[148:151],v[204:207],a[216:219],v[148:151]\n"
WT"lgkmcnt(0)\n"
"s_barrier\n"
MF"v[156:159],v[204:207],a[220:223],v[156:159]\n"
RD"v[192:195],v242 offset:16384\n"
MF"v[156:159],v[200:203],a[204:207],v[156:159]\n"
BL"v240,s[20:23],s48 offen offset:2048 lds\n"
MF"v[128:131],v[208:211],a[224:227],v[128:131]\n"
MF"v[128:131],v[212:215],a[240:243],v[128:131]\n"
MF"v[136:139],v[212:215],a[244:247],v[136:139]\n"
RD"v[196:199],v243 offset:16384\n"
MF"v[136:139],v[208:211],a[228:231],v[136:139]\n"
MF"v[144:147],v[208:211],a[232:235],v[144:147]\n"
MF"v[144:147],v[212:215],a[248:251],v[144:147]\n"
MF"v[152:155],v[212:215],a[252:255],v[152:155]\n"
SA"s61,s61,524288\n"
RD"v[200:203],v242 offset:18432\n"
MF"v[152:155],v[208:211],a[236:239],v[152:155]\n"
BL"v241,s[20:23],s48 offen offset:3072 lds\n"
MF"v[132:135],v[216:219],a[224:227],v[132:135]\n"
"s_lshl_b32 s50,s34,3\n"
MF"v[132:135],v[220:223],a[240:243],v[132:135]\n"
SA"s51,s50,s30\n"
MF"v[140:143],v[220:223],a[244:247],v[140:143]\n"
"s_mul_i32 s51,s51,20480\n"
RD"v[204:207],v243 offset:18432\n"
MF"v[140:143],v[216:219],a[228:231],v[140:143]\n"
SA"s56,s51,65536\n"
MF"v[148:151],v[216:219],a[232:235],v[148:151]\n"
SA"m0,s36,16384\n"
SA"s48,s60,s61\n"
SA"s57,s56,81920\n"
MF"v[148:151],v[220:223],a[248:251],v[148:151]\n"
"s_sub_u32 s51,s30,s50\n"
MF"v[156:159],v[220:223],a[252:255],v[156:159]\n"
SA"s51,s51,120\n"
RD"v[208:211],v242 offset:20480\n"
MF"v[156:159],v[216:219],a[236:239],v[156:159]\n"
WT"lgkmcnt(2)\n"
BL"v238,s[20:23],s48 offen offset:0 lds\n"
MF"v[0:3],v[192:195],a[0:3],0\n"
"s_mul_i32 s51,s51,20480\n"
SA"s58,s51,65536\n"
"s_lshl_b32 s99,s99,12\n"
MF"v[0:3],v[196:199],a[16:19],v[0:3]\n"
SA"s59,s58,81920\n"
RD"v[212:215],v243 offset:20480\n"
MF"v[16:19],v[196:199],a[20:23],0\n"
"v_add_u32 v235,s99,v224\n"
MF"v[16:19],v[192:195],a[4:7],v[16:19]\n"
PK"v64,v128,v129\n"
PK"v65,v130,v131\n"
MF"v[32:35],v[192:195],a[8:11],0\n"
"s_mov_b32 s49,24576\n"
PK"v66,v132,v133\n"
PK"v67,v134,v135\n"
MF"v[32:35],v[196:199],a[24:27],v[32:35]\n"
RD"v[216:219],v242 offset:22528\n"
MF"v[48:51],v[196:199],a[28:31],0\n"
BS"v[96:99],v250,s[8:11],s49 offen offset:0 sc1\n"
MF"v[48:51],v[192:195],a[12:15],v[48:51]\n"
BL"v239,s[20:23],s48 offen offset:1024 lds\n"
MF"v[8:11],v[200:203],a[0:3],0\n"
WT"lgkmcnt(1)\n"
PK"v68,v136,v137\n"
PK"v69,v138,v139\n"
MF"v[8:11],v[204:207],a[16:19],v[8:11]\n"
RD"v[220:223],v243 offset:22528\n"
PK"v70,v140,v141\n"
MF"v[24:27],v[204:207],a[20:23],0\n"
PK"v71,v142,v143\n"
MF"v[24:27],v[200:203],a[4:7],v[24:27]\n"
MF"v[40:43],v[200:203],a[8:11],0\n"
MF"v[40:43],v[204:207],a[24:27],v[40:43]\n"
RD"v[192:195],v242 offset:40960\n"
PK"v72,v144,v145\n"
MF"v[56:59],v[204:207],a[28:31],0\n"
BL"v240,s[20:23],s48 offen offset:2048 lds\n"
MF"v[56:59],v[200:203],a[12:15],v[56:59]\n"
PK"v73,v146,v147\n"
PK"v74,v148,v149\n"
MF"v[0:3],v[208:211],a[32:35],v[0:3]\n"
BL"a[0:3],v235,s[8:11],s56 offen offset:0 sc1\n"
MF"v[0:3],v[212:215],a[48:51],v[0:3]\n"
RD"v[196:199],v243 offset:40960\n"
PK"v75,v150,v151\n"
MF"v[16:19],v[212:215],a[52:55],v[16:19]\n"
PK"v76,v152,v153\n"
PK"v77,v154,v155\n"
MF"v[16:19],v[208:211],a[36:39],v[16:19]\n"
PK"v78,v156,v157\n"
PK"v79,v158,v159\n"
MF"v[32:35],v[208:211],a[40:43],v[32:35]\n"
RD"v[200:203],v242 offset:43008\n"
MF"v[32:35],v[212:215],a[56:59],v[32:35]\n"
MF"v[48:51],v[212:215],a[60:63],v[48:51]\n"
SA"s61,s61,524288\n"
BL"v241,s[20:23],s48 offen offset:3072 lds\n"
MF"v[48:51],v[208:211],a[44:47],v[48:51]\n"
WT"lgkmcnt(2)\n"
BL"a[4:7],v235,s[8:11],s57 offen offset:0 sc1\n"
MF"v[8:11],v[216:219],a[32:35],v[8:11]\n"
"s_cmp_eq_u32 s61,5242880\n"
RD"v[204:207],v243 offset:43008\n"
"s_cselect_b32 s61,0,s61\n"
MF"v[8:11],v[220:223],a[48:51],v[8:11]\n"
MF"v[24:27],v[220:223],a[52:55],v[24:27]\n"
MF"v[24:27],v[216:219],a[36:39],v[24:27]\n"
MF"v[40:43],v[216:219],a[40:43],v[40:43]\n"
RD"v[208:211],v242 offset:45056\n"
MF"v[40:43],v[220:223],a[56:59],v[40:43]\n"
BL"a[8:11],v235,s[8:11],s58 offen offset:0 sc1\n"
MF"v[56:59],v[220:223],a[60:63],v[56:59]\n"
MF"v[56:59],v[216:219],a[44:47],v[56:59]\n"
MF"v[0:3],v[192:195],a[64:67],v[0:3]\n"
WT"lgkmcnt(1)\n"
RD"v[212:215],v243 offset:45056\n"
MF"v[0:3],v[196:199],a[80:83],v[0:3]\n"
MF"v[16:19],v[196:199],a[84:87],v[16:19]\n"
BL"a[12:15],v235,s[8:11],s59 offen offset:0 sc1\n"
MF"v[16:19],v[192:195],a[68:71],v[16:19]\n"
RD"v[216:219],v242 offset:47104\n"
MF"v[32:35],v[192:195],a[72:75],v[32:35]\n"
MF"v[32:35],v[196:199],a[88:91],v[32:35]\n"
BS"v[104:107],v250,s[8:11],s49 offen offset:1024 sc1\n"
MF"v[48:51],v[196:199],a[92:95],v[48:51]\n"
MF"v[48:51],v[192:195],a[76:79],v[48:51]\n"
RD"v[220:223],v243 offset:47104\n"
MF"v[8:11],v[200:203],a[64:67],v[8:11]\n"
BL"a[16:19],v235,s[8:11],s56 offen offset:1024 sc1\n"
MF"v[8:11],v[204:207],a[80:83],v[8:11]\n"
MF"v[24:27],v[204:207],a[84:87],v[24:27]\n"
MF"v[24:27],v[200:203],a[68:71],v[24:27]\n"
RD"v[192:195],v244 offset:16384\n"
MF"v[40:43],v[200:203],a[72:75],v[40:43]\n"
MF"v[40:43],v[204:207],a[88:91],v[40:43]\n"
MF"v[56:59],v[204:207],a[92:95],v[56:59]\n"
BL"a[20:23],v235,s[8:11],s57 offen offset:1024 sc1\n"
MF"v[56:59],v[200:203],a[76:79],v[56:59]\n"
WT"lgkmcnt(1)\n"
RD"v[196:199],v245 offset:16384\n"
MF"v[0:3],v[208:211],a[96:99],v[0:3]\n"
MF"v[0:3],v[212:215],a[112:115],v[0:3]\n"
MF"v[16:19],v[212:215],a[116:119],v[16:19]\n"
MF"v[16:19],v[208:211],a[100:103],v[16:19]\n"
RD"v[200:203],v244 offset:18432\n"
MF"v[32:35],v[208:211],a[104:107],v[32:35]\n"
BL"a[24:27],v235,s[8:11],s58 offen offset:1024 sc1\n"
MF"v[32:35],v[212:215],a[120:123],v[32:35]\n"
MF"v[48:51],v[212:215],a[124:127],v[48:51]\n"
RD"v[204:207],v245 offset:18432\n"
MF"v[48:51],v[208:211],a[108:111],v[48:51]\n"
MF"v[8:11],v[216:219],a[96:99],v[8:11]\n"
MF"v[8:11],v[220:223],a[112:115],v[8:11]\n"
MF"v[24:27],v[220:223],a[116:119],v[24:27]\n"
RD"v[208:211],v244 offset:20480\n"
MF"v[24:27],v[216:219],a[100:103],v[24:27]\n"
BL"a[28:31],v235,s[8:11],s59 offen offset:1024 sc1\n"
MF"v[40:43],v[216:219],a[104:107],v[40:43]\n"
MF"v[40:43],v[220:223],a[120:123],v[40:43]\n"
MF"v[56:59],v[220:223],a[124:127],v[56:59]\n"
RD"v[212:215],v245 offset:20480\n"
MF"v[56:59],v[216:219],a[108:111],v[56:59]\n"
WT"lgkmcnt(2)\n"
MF"v[0:3],v[192:195],a[128:131],v[0:3]\n"
BL"a[32:35],v235,s[8:11],s56 offen offset:2048 sc1\n"
MF"v[0:3],v[196:199],a[144:147],v[0:3]\n"
MF"v[16:19],v[196:199],a[148:151],v[16:19]\n"
RD"v[216:219],v244 offset:22528\n"
MF"v[16:19],v[192:195],a[132:135],v[16:19]\n"
MF"v[32:35],v[192:195],a[136:139],v[32:35]\n"
BS"v[112:115],v250,s[8:11],s49 offen offset:2048 sc1\n"
MF"v[32:35],v[196:199],a[152:155],v[32:35]\n"
RD"v[220:223],v245 offset:22528\n"
MF"v[48:51],v[196:199],a[156:159],v[48:51]\n"
BL"a[36:39],v235,s[8:11],s57 offen offset:2048 sc1\n"
MF"v[48:51],v[192:195],a[140:143],v[48:51]\n"
MF"v[8:11],v[200:203],a[128:131],v[8:11]\n"
MF"v[8:11],v[204:207],a[144:147],v[8:11]\n"
RD"v[192:195],v244 offset:40960\n"
MF"v[24:27],v[204:207],a[148:151],v[24:27]\n"
MF"v[24:27],v[200:203],a[132:135],v[24:27]\n"
MF"v[40:43],v[200:203],a[136:139],v[40:43]\n"
BL"a[40:43],v235,s[8:11],s58 offen offset:2048 sc1\n"
MF"v[40:43],v[204:207],a[152:155],v[40:43]\n"
RD"v[196:199],v245 offset:40960\n"
MF"v[56:59],v[204:207],a[156:159],v[56:59]\n"
MF"v[56:59],v[200:203],a[140:143],v[56:59]\n"
WT"lgkmcnt(2)\n"
MF"v[0:3],v[208:211],a[160:163],v[0:3]\n"
"s_sub_u32 s53,s37,65536\n"
MF"v[0:3],v[212:215],a[176:179],v[0:3]\n"
RD"v[200:203],v244 offset:43008\n"
MF"v[16:19],v[212:215],a[180:183],v[16:19]\n"
BL"a[44:47],v235,s[8:11],s59 offen offset:2048 sc1\n"
MF"v[16:19],v[208:211],a[164:167],v[16:19]\n"
"v_subrev_u32 v246,s53,v246\n"
"v_subrev_u32 v247,s53,v247\n"
MF"v[32:35],v[208:211],a[168:171],v[32:35]\n"
"v_subrev_u32 v248,s53,v248\n"
"v_subrev_u32 v249,s53,v249\n"
MF"v[32:35],v[212:215],a[184:187],v[32:35]\n"
RD"v[204:207],v245 offset:43008\n"
"v_subrev_u32 v236,65536,v246\n"
MF"v[48:51],v[212:215],a[188:191],v[48:51]\n"
"v_subrev_u32 v237,65536,v247\n"
"v_subrev_u32 v228,65536,v248\n"
MF"v[48:51],v[208:211],a[172:175],v[48:51]\n"
"v_subrev_u32 v229,65536,v249\n"
MF"v[8:11],v[216:219],a[160:163],v[8:11]\n"
RD"v[208:211],v244 offset:45056\n"
MF"v[8:11],v[220:223],a[176:179],v[8:11]\n"
BL"a[48:51],v235,s[8:11],s56 offen offset:3072 sc1\n"
MF"v[24:27],v[220:223],a[180:183],v[24:27]\n"
MF"v[24:27],v[216:219],a[164:167],v[24:27]\n"
MF"v[40:43],v[216:219],a[168:171],v[40:43]\n"
RD"v[212:215],v245 offset:45056\n"
MF"v[40:43],v[220:223],a[184:187],v[40:43]\n"
MF"v[56:59],v[220:223],a[188:191],v[56:59]\n"
BL"a[52:55],v235,s[8:11],s57 offen offset:3072 sc1\n"
MF"v[56:59],v[216:219],a[172:175],v[56:59]\n"
WT"lgkmcnt(1)\n"
MF"v[0:3],v[192:195],a[192:195],v[0:3]\n"
RD"v[216:219],v244 offset:47104\n"
MF"v[0:3],v[196:199],a[208:211],v[0:3]\n"
MF"v[16:19],v[196:199],a[212:215],v[16:19]\n"
MF"v[16:19],v[192:195],a[196:199],v[16:19]\n"
BS"v[120:123],v250,s[8:11],s49 offen offset:3072 sc1\n"
MF"v[32:35],v[192:195],a[200:203],v[32:35]\n"
RD"v[220:223],v245 offset:47104\n"
MF"v[32:35],v[196:199],a[216:219],v[32:35]\n"
BL"a[56:59],v235,s[8:11],s58 offen offset:3072 sc1\n"
MF"v[48:51],v[196:199],a[220:223],v[48:51]\n"
MF"v[48:51],v[192:195],a[204:207],v[48:51]\n"
MF"v[8:11],v[200:203],a[192:195],v[8:11]\n"
SA"s99,s99,4096\n"
MF"v[8:11],v[204:207],a[208:211],v[8:11]\n"
"s_cmp_eq_u32 s99,20480\n"
"s_cselect_b32 s99,0,s99\n"
MF"v[24:27],v[204:207],a[212:215],v[24:27]\n"
BL"a[60:63],v235,s[8:11],s59 offen offset:3072 sc1\n"
MF"v[24:27],v[200:203],a[196:199],v[24:27]\n"
MF"v[40:43],v[200:203],a[200:203],v[40:43]\n"
MF"v[40:43],v[204:207],a[216:219],v[40:43]\n"
WT"lgkmcnt(0)\n"
"s_barrier\n"
MF"v[56:59],v[204:207],a[220:223],v[56:59]\n"
RT"a[128:129],v236 offset:49152\n"
MF"v[56:59],v[200:203],a[204:207],v[56:59]\n"
"v_add_u32 v235,s99,v224\n"
MF"v[0:3],v[208:211],a[224:227],v[0:3]\n"
SA"m0,s38,73728\n"
RT"a[130:131],v236 offset:49664\n"
SA"s48,s60,s61\n"
MF"v[0:3],v[212:215],a[240:243],v[0:3]\n"
BL"a[64:67],v235,s[8:11],s56 offen offset:0 sc1\n"
MF"v[16:19],v[212:215],a[244:247],v[16:19]\n"
RT"a[132:133],v236 offset:57344\n"
"v_add_u32 v242,49152,v246\n"
MF"v[16:19],v[208:211],a[228:231],v[16:19]\n"
BL"v238,s[20:23],s48 offen offset:0 lds\n"
MF"v[32:35],v[208:211],a[232:235],v[32:35]\n"
RT"a[134:135],v236 offset:57856\n"
"v_add_u32 v243,49152,v247\n"
MF"v[32:35],v[212:215],a[248:251],v[32:35]\n"
"v_add_u32 v244,49152,v248\n"
"v_add_u32 v245,49152,v249\n"
MF"v[48:51],v[212:215],a[252:255],v[48:51]\n"
RT"a[136:137],v237 offset:49152\n"
MF"v[48:51],v[208:211],a[236:239],v[48:51]\n"
BL"a[68:71],v235,s[8:11],s57 offen offset:0 sc1\n"
MF"v[8:11],v[216:219],a[224:227],v[8:11]\n"
RT"a[138:139],v237 offset:49664\n"
MF"v[8:11],v[220:223],a[240:243],v[8:11]\n"
MF"v[24:27],v[220:223],a[244:247],v[24:27]\n"
RT"a[140:141],v237 offset:57344\n"
MF"v[24:27],v[216:219],a[228:231],v[24:27]\n"
BL"v239,s[20:23],s48 offen offset:1024 lds\n"
MF"v[40:43],v[216:219],a[232:235],v[40:43]\n"
RT"a[142:143],v237 offset:57856\n"
MF"v[40:43],v[220:223],a[248:251],v[40:43]\n"
BL"a[72:75],v235,s[8:11],s58 offen offset:0 sc1\n"
MF"v[56:59],v[220:223],a[252:255],v[56:59]\n"
RT"a[144:145],v228 offset:49152\n"
MF"v[56:59],v[216:219],a[236:239],v[56:59]\n"
WT"vmcnt(24) lgkmcnt(3)\n"
MF"v[80:83],a[128:131],a[0:3],0\n"
WT"vmcnt(18)\n"
RT"a[146:147],v228 offset:49664\n"
MF"v[80:83],a[132:135],a[16:19],v[80:83]\n"
WT"vmcnt(17)\n"
MF"v[88:91],a[132:135],a[20:23],0\n"
RT"a[148:149],v228 offset:57344\n"
MF"v[88:91],a[128:131],a[4:7],v[88:91]\n"
BL"v240,s[20:23],s48 offen offset:2048 lds\n"
MF"v[96:99],a[128:131],a[8:11],0\n"
WT"vmcnt(17)\n"
BL"a[76:79],v235,s[8:11],s59 offen offset:0 sc1\n"
MF"v[96:99],a[132:135],a[24:27],v[96:99]\n"
WT"vmcnt(17)\n"
RT"a[150:151],v228 offset:57856\n"
RT"a[152:153],v229 offset:49152\n"
MF"v[104:107],a[132:135],a[28:31],0\n"
PK"v0,v0,v1\n"
PK"v1,v2,v3\n"
MF"v[104:107],a[128:131],a[12:15],v[104:107]\n"
RT"a[154:155],v229 offset:49664\n"
PK"v2,v8,v9\n"
MF"v[84:87],a[136:139],a[0:3],0\n"
WT"lgkmcnt(4)\n"
PK"v3,v10,v11\n"
PK"v16,v16,v17\n"
MF"v[84:87],a[140:143],a[16:19],v[84:87]\n"
RT"a[156:157],v229 offset:57344\n"
PK"v17,v18,v19\n"
MF"v[92:95],a[140:143],a[20:23],0\n"
SA"s61,s61,524288\n"
BL"a[80:83],v235,s[8:11],s56 offen offset:1024 sc1\n"
MF"v[92:95],a[136:139],a[4:7],v[92:95]\n"
BL"v241,s[20:23],s48 offen offset:3072 lds\n"
MF"v[100:103],a[136:139],a[8:11],0\n"
RT"a[158:159],v229 offset:57856\n"
PK"v18,v24,v25\n"
MF"v[100:103],a[140:143],a[24:27],v[100:103]\n"
RT"a[160:161],v236 offset:53248\n"
PK"v19,v26,v27\n"
MF"v[108:111],a[140:143],a[28:31],0\n"
PK"v32,v32,v33\n"
PK"v33,v34,v35\n"
MF"v[108:111],a[136:139],a[12:15],v[108:111]\n"
RT"a[162:163],v236 offset:53760\n"
PK"v34,v40,v41\n"
MF"v[112:115],a[144:147],a[0:3],0\n"
WT"lgkmcnt(3)\n"
BL"a[84:87],v235,s[8:11],s57 offen offset:1024 sc1\n"
MF"v[112:115],a[148:151],a[16:19],v[112:115]\n"
RT"a[164:165],v236 offset:61440\n"
PK"v35,v42,v43\n"
MF"v[120:123],a[148:151],a[20:23],0\n"
PK"v48,v48,v49\n"
PK"v49,v50,v51\n"
MF"v[120:123],a[144:147],a[4:7],v[120:123]\n"
RT"a[166:167],v236 offset:61952\n"
PK"v50,v56,v57\n"
MF"v[128:131],a[144:147],a[8:11],0\n"
PK"v51,v58,v59\n"
MF"v[128:131],a[148:151],a[24:27],v[128:131]\n"
RT"a[168:169],v237 offset:53248\n"
MF"v[136:139],a[148:151],a[28:31],0\n"
MF"v[136:139],a[144:147],a[12:15],v[136:139]\n"
RT"a[170:171],v237 offset:53760\n"
MF"v[116:119],a[152:155],a[0:3],0\n"
WT"lgkmcnt(3)\n"
BL"a[88:91],v235,s[8:11],s58 offen offset:1024 sc1\n"
MF"v[116:119],a[156:159],a[16:19],v[116:119]\n"
"s_mov_b32 s49,262144\n"
RT"a[172:173],v237 offset:61440\n"
MF"v[124:127],a[156:159],a[20:23],0\n"
RT"a[174:175],v237 offset:61952\n"
MF"v[124:127],a[152:155],a[4:7],v[124:127]\n"
BS"v[4:7],v250,s[8:11],s49 offen offset:0 sc1\n"
MF"v[132:135],a[152:155],a[8:11],0\n"
RT"a[176:177],v228 offset:53248\n"
MF"v[132:135],a[156:159],a[24:27],v[132:135]\n"
MF"v[140:143],a[156:159],a[28:31],0\n"
RT"a[178:179],v228 offset:53760\n"
MF"v[140:143],a[152:155],a[12:15],v[140:143]\n"
BL"a[92:95],v235,s[8:11],s59 offen offset:1024 sc1\n"
MF"v[144:147],a[160:163],a[0:3],0\n"
WT"lgkmcnt(3)\n"
RT"a[180:181],v228 offset:61440\n"
MF"v[144:147],a[164:167],a[16:19],v[144:147]\n"
MF"v[152:155],a[164:167],a[20:23],0\n"
SA"m0,s38,122880\n"
RT"a[182:183],v228 offset:61952\n"
SA"s48,s60,s61\n"
MF"v[152:155],a[160:163],a[4:7],v[152:155]\n"
MF"v[160:163],a[160:163],a[8:11],0\n"
"s_mov_b32 s88,2048\n"
RT"a[184:185],v229 offset:53248\n"
MF"v[160:163],a[164:167],a[24:27],v[160:163]\n"
BL"v238,s[20:23],s48 offen offset:0 lds\n"
MF"v[168:171],a[164:167],a[28:31],0\n"
BL"a[96:99],v235,s[8:11],s56 offen offset:2048 sc1\n"
MF"v[168:171],a[160:163],a[12:15],v[168:171]\n"
RT"a[186:187],v229 offset:53760\n"
MF"v[148:151],a[168:171],a[0:3],0\n"
WT"lgkmcnt(3)\n"
RT"a[188:189],v229 offset:61440\n"
MF"v[148:151],a[172:175],a[16:19],v[148:151]\n"
MF"v[156:159],a[172:175],a[20:23],0\n"
RT"a[190:191],v229 offset:61952\n"
MF"v[156:159],a[168:171],a[4:7],v[156:159]\n"
RT"a[128:129],v236 offset:32768\n"
MF"v[164:167],a[168:171],a[8:11],0\n"
BL"v239,s[20:23],s48 offen offset:1024 lds\n"
MF"v[164:167],a[172:175],a[24:27],v[164:167]\n"
BL"a[100:103],v235,s[8:11],s57 offen offset:2048 sc1\n"
MF"v[172:175],a[172:175],a[28:31],0\n"
RT"a[130:131],v236 offset:33280\n"
MF"v[172:175],a[168:171],a[12:15],v[172:175]\n"
RT"a[132:133],v236 offset:40960\n"
MF"v[176:179],a[176:179],a[0:3],0\n"
WT"lgkmcnt(4)\n"
MF"v[176:179],a[180:183],a[16:19],v[176:179]\n"
RT"a[134:135],v236 offset:41472\n"
MF"v[184:187],a[180:183],a[20:23],0\n"
MF"v[184:187],a[176:179],a[4:7],v[184:187]\n"
RT"a[136:137],v237 offset:32768\n"
MF"v[192:195],a[176:179],a[8:11],0\n"
BL"a[104:107],v235,s[8:11],s58 offen offset:2048 sc1\n"
MF"v[192:195],a[180:183],a[24:27],v[192:195]\n"
BL"v240,s[20:23],s48 offen offset:2048 lds\n"
MF"v[200:203],a[180:183],a[28:31],0\n"
RT"a[138:139],v237 offset:33280\n"
MF"v[200:203],a[176:179],a[12:15],v[200:203]\n"
RT"a[140:141],v237 offset:40960\n"
MF"v[180:183],a[184:187],a[0:3],0\n"
WT"lgkmcnt(3)\n"
MF"v[180:183],a[188:191],a[16:19],v[180:183]\n"
RT"a[142:143],v237 offset:41472\n"
MF"v[188:191],a[188:191],a[20:23],0\n"
BL"a[108:111],v235,s[8:11],s59 offen offset:2048 sc1\n"
MF"v[188:191],a[184:187],a[4:7],v[188:191]\n"
RT"a[144:145],v228 offset:32768\n"
MF"v[196:199],a[184:187],a[8:11],0\n"
SA"s61,s61,524288\n"
BS"v[12:15],v250,s[8:11],s49 offen offset:1024 sc1\n"
MF"v[196:199],a[188:191],a[24:27],v[196:199]\n"
"s_cmp_eq_u32 s61,5242880\n"
BL"v241,s[20:23],s48 offen offset:3072 lds\n"
MF"v[204:207],a[188:191],a[28:31],0\n"
"s_cselect_b32 s61,0,s61\n"
RT"a[146:147],v228 offset:33280\n"
MF"v[204:207],a[184:187],a[12:15],v[204:207]\n"
WT"vmcnt(31)\n"
RT"a[148:149],v228 offset:40960\n"
MF"v[80:83],a[128:131],a[32:35],v[80:83]\n"
WT"vmcnt(26)\n"
MF"v[80:83],a[132:135],a[48:51],v[80:83]\n"
WT"vmcnt(25)\n"
RT"a[150:151],v228 offset:41472\n"
MF"v[88:91],a[132:135],a[52:55],v[88:91]\n"
BL"a[112:115],v235,s[8:11],s56 offen offset:3072 sc1\n"
MF"v[88:91],a[128:131],a[36:39],v[88:91]\n"
RT"a[152:153],v229 offset:32768\n"
MF"v[96:99],a[128:131],a[40:43],v[96:99]\n"
WT"vmcnt(24)\n"
RT"a[154:155],v229 offset:33280\n"
MF"v[96:99],a[132:135],a[56:59],v[96:99]\n"
WT"vmcnt(23)\n"
MF"v[104:107],a[132:135],a[60:63],v[104:107]\n"
RT"a[156:157],v229 offset:40960\n"
MF"v[104:107],a[128:131],a[44:47],v[104:107]\n"
WT"lgkmcnt(4)\n"
MF"v[84:87],a[136:139],a[32:35],v[84:87]\n"
RT"a[158:159],v229 offset:41472\n"
MF"v[84:87],a[140:143],a[48:51],v[84:87]\n"
BL"a[116:119],v235,s[8:11],s57 offen offset:3072 sc1\n"
MF"v[92:95],a[140:143],a[52:55],v[92:95]\n"
RT"a[160:161],v236 offset:36864\n"
MF"v[92:95],a[136:139],a[36:39],v[92:95]\n"
MF"v[100:103],a[136:139],a[40:43],v[100:103]\n"
RT"a[162:163],v236 offset:37376\n"
MF"v[100:103],a[140:143],a[56:59],v[100:103]\n"
MF"v[108:111],a[140:143],a[60:63],v[108:111]\n"
RT"a[164:165],v236 offset:45056\n"
MF"v[108:111],a[136:139],a[44:47],v[108:111]\n"
BL"a[120:123],v235,s[8:11],s58 offen offset:3072 sc1\n"
MF"v[112:115],a[144:147],a[32:35],v[112:115]\n"
WT"lgkmcnt(3)\n"
RT"a[166:167],v236 offset:45568\n"
MF"v[112:115],a[148:151],a[48:51],v[112:115]\n"
MF"v[120:123],a[148:151],a[52:55],v[120:123]\n"
RT"a[168:169],v237 offset:36864\n"
MF"v[120:123],a[144:147],a[36:39],v[120:123]\n"
MF"v[128:131],a[144:147],a[40:43],v[128:131]\n"
RT"a[170:171],v237 offset:37376\n"
MF"v[128:131],a[148:151],a[56:59],v[128:131]\n"
SA"s99,s99,4096\n"
RT"a[172:173],v237 offset:45056\n"
MF"v[136:139],a[148:151],a[60:63],v[136:139]\n"
"s_cmp_eq_u32 s99,20480\n"
BL"a[124:127],v235,s[8:11],s59 offen offset:3072 sc1\n"
MF"v[136:139],a[144:147],a[44:47],v[136:139]\n"
"s_cselect_b32 s99,0,s99\n"
RT"a[174:175],v237 offset:45568\n"
MF"v[116:119],a[152:155],a[32:35],v[116:119]\n"
MF"v[116:119],a[156:159],a[48:51],v[116:119]\n"
RT"a[176:177],v228 offset:36864\n"
MF"v[124:127],a[156:159],a[52:55],v[124:127]\n"
MF"v[124:127],a[152:155],a[36:39],v[124:127]\n"
RT"a[178:179],v228 offset:37376\n"
MF"v[132:135],a[152:155],a[40:43],v[132:135]\n"
"v_add_u32 v235,s99,v224\n"
MF"v[132:135],a[156:159],a[56:59],v[132:135]\n"
RT"a[180:181],v228 offset:45056\n"
MF"v[140:143],a[156:159],a[60:63],v[140:143]\n"
BL"a[0:3],v235,s[8:11],s56 offen offset:0 sc1\n"
MF"v[140:143],a[152:155],a[44:47],v[140:143]\n"
WT"lgkmcnt(3)\n"
BS"v[20:23],v250,s[8:11],s49 offen offset:2048 sc1\n"
MF"v[144:147],a[160:163],a[32:35],v[144:147]\n"
RT"a[182:183],v228 offset:45568\n"
MF"v[144:147],a[164:167],a[48:51],v[144:147]\n"
RT"a[184:185],v229 offset:36864\n"
MF"v[152:155],a[164:167],a[52:55],v[152:155]\n"
MF"v[152:155],a[160:163],a[36:39],v[152:155]\n"
RT"a[186:187],v229 offset:37376\n"
MF"v[160:163],a[160:163],a[40:43],v[160:163]\n"
BL"a[4:7],v235,s[8:11],s57 offen offset:0 sc1\n"
MF"v[160:163],a[164:167],a[56:59],v[160:163]\n"
RT"a[188:189],v229 offset:45056\n"
MF"v[168:171],a[164:167],a[60:63],v[168:171]\n"
MF"v[168:171],a[160:163],a[44:47],v[168:171]\n"
RT"a[190:191],v229 offset:45568\n"
MF"v[148:151],a[168:171],a[32:35],v[148:151]\n"
"s_lshl_b32 s50,s35,3\n"
SA"s50,s50,s34\n"
"s_lshl_b32 s50,s50,3\n"
MF"v[148:151],a[172:175],a[48:51],v[148:151]\n"
SA"s50,s50,2304\n"
SA"s51,s50,s45\n"
MF"v[156:159],a[172:175],a[52:55],v[156:159]\n"
MF"v[156:159],a[168:171],a[36:39],v[156:159]\n"
BL"a[8:11],v235,s[8:11],s58 offen offset:0 sc1\n"
MF"v[164:167],a[168:171],a[40:43],v[164:167]\n"
MF"v[164:167],a[172:175],a[56:59],v[164:167]\n"
WT"lgkmcnt(0)\n"
"s_barrier\n"
MF"v[172:175],a[172:175],a[60:63],v[172:175]\n"
RT"a[128:129],v236 offset:0\n"
MF"v[172:175],a[168:171],a[44:47],v[172:175]\n"
"buffer_store_dwordx2 v[232:233],v230,s[8:11],s51 offen sc1\n"
MF"v[176:179],a[176:179],a[32:35],v[176:179]\n"
RT"a[130:131],v236 offset:512\n"
MF"v[176:179],a[180:183],a[48:51],v[176:179]\n"
MF"v[184:187],a[180:183],a[52:55],v[184:187]\n"
SA"m0,s38,81920\n"
RT"a[132:133],v236 offset:8192\n"
SA"s48,s60,s61\n"
MF"v[184:187],a[176:179],a[36:39],v[184:187]\n"
BL"a[12:15],v235,s[8:11],s59 offen offset:0 sc1\n"
MF"v[192:195],a[176:179],a[40:43],v[192:195]\n"
RT"a[134:135],v236 offset:8704\n"
MF"v[192:195],a[180:183],a[56:59],v[192:195]\n"
BL"v238,s[20:23],s48 offen offset:0 lds\n"
MF"v[200:203],a[180:183],a[60:63],v[200:203]\n"
RT"a[136:137],v237 offset:0\n"
MF"v[200:203],a[176:179],a[44:47],v[200:203]\n"
MF"v[180:183],a[184:187],a[32:35],v[180:183]\n"
RT"a[138:139],v237 offset:512\n"
MF"v[180:183],a[188:191],a[48:51],v[180:183]\n"
BL"a[16:19],v235,s[8:11],s56 offen offset:1024 sc1\n"
MF"v[188:191],a[188:191],a[52:55],v[188:191]\n"
RT"a[140:141],v237 offset:8192\n"
MF"v[188:191],a[184:187],a[36:39],v[188:191]\n"
MF"v[196:199],a[184:187],a[40:43],v[196:199]\n"
RT"a[142:143],v237 offset:8704\n"
MF"v[196:199],a[188:191],a[56:59],v[196:199]\n"
BL"v239,s[20:23],s48 offen offset:1024 lds\n"
MF"v[204:207],a[188:191],a[60:63],v[204:207]\n"
SA"s49,s88,262144\n"
RT"a[144:145],v228 offset:0\n"
MF"v[204:207],a[184:187],a[44:47],v[204:207]\n"
"LQ_%=:\n"
WT"vmcnt(34) lgkmcnt(7)\n"
MF"v[80:83],a[128:131],a[64:67],v[80:83]\n"
WT"vmcnt(27) lgkmcnt(3)\n"
RT"a[146:147],v228 offset:512\n"
MF"v[80:83],a[132:135],a[80:83],v[80:83]\n"
WT"vmcnt(25)\n"
BS"v[28:31],v250,s[8:11],s49 offen offset:1024 sc1\n"
MF"v[88:91],a[132:135],a[84:87],v[88:91]\n"
BL"a[20:23],v235,s[8:11],s57 offen offset:1024 sc1\n"
MF"v[88:91],a[128:131],a[68:71],v[88:91]\n"
RT"a[148:149],v228 offset:8192\n"
MF"v[96:99],a[128:131],a[72:75],v[96:99]\n"
WT"vmcnt(26)\n"
RT"a[150:151],v228 offset:8704\n"
MF"v[96:99],a[132:135],a[88:91],v[96:99]\n"
WT"vmcnt(24)\n"
BL"v240,s[20:23],s48 offen offset:2048 lds\n"
MF"v[104:107],a[132:135],a[92:95],v[104:107]\n"
RT"a[152:153],v229 offset:0\n"
MF"v[104:107],a[128:131],a[76:79],v[104:107]\n"
RT"a[154:155],v229 offset:512\n"
MF"v[84:87],a[136:139],a[64:67],v[84:87]\n"
WT"lgkmcnt(4)\n"
BL"a[24:27],v235,s[8:11],s58 offen offset:1024 sc1\n"
MF"v[84:87],a[140:143],a[80:83],v[84:87]\n"
RT"a[156:157],v229 offset:8192\n"
MF"v[92:95],a[140:143],a[84:87],v[92:95]\n"
MF"v[92:95],a[136:139],a[68:71],v[92:95]\n"
RT"a[158:159],v229 offset:8704\n"
MF"v[100:103],a[136:139],a[72:75],v[100:103]\n"
SA"s61,s61,524288\n"
BL"v241,s[20:23],s48 offen offset:3072 lds\n"
MF"v[100:103],a[140:143],a[88:91],v[100:103]\n"
RT"a[160:161],v236 offset:4096\n"
MF"v[108:111],a[140:143],a[92:95],v[108:111]\n"
BL"a[28:31],v235,s[8:11],s59 offen offset:1024 sc1\n"
MF"v[108:111],a[136:139],a[76:79],v[108:111]\n"
RT"a[162:163],v236 offset:4608\n"
MF"v[112:115],a[144:147],a[64:67],v[112:115]\n"
WT"lgkmcnt(3)\n"
MF"v[112:115],a[148:151],a[80:83],v[112:115]\n"
RT"a[164:165],v236 offset:12288\n"
MF"v[120:123],a[148:151],a[84:87],v[120:123]\n"
MF"v[120:123],a[144:147],a[68:71],v[120:123]\n"
RT"a[166:167],v236 offset:12800\n"
MF"v[128:131],a[144:147],a[72:75],v[128:131]\n"
MF"v[128:131],a[148:151],a[88:91],v[128:131]\n"
RT"a[168:169],v237 offset:4096\n"
MF"v[136:139],a[148:151],a[92:95],v[136:139]\n"
BL"a[32:35],v235,s[8:11],s56 offen offset:2048 sc1\n"
MF"v[136:139],a[144:147],a[76:79],v[136:139]\n"
RT"a[170:171],v237 offset:4608\n"
MF"v[116:119],a[152:155],a[64:67],v[116:119]\n"
WT"lgkmcnt(3)\n"
RT"a[172:173],v237 offset:12288\n"
MF"v[116:119],a[156:159],a[80:83],v[116:119]\n"
MF"v[124:127],a[156:159],a[84:87],v[124:127]\n"
RT"a[174:175],v237 offset:12800\n"
MF"v[124:127],a[152:155],a[68:71],v[124:127]\n"
MF"v[132:135],a[152:155],a[72:75],v[132:135]\n"
RT"a[176:177],v228 offset:4096\n"
MF"v[132:135],a[156:159],a[88:91],v[132:135]\n"
BL"a[36:39],v235,s[8:11],s57 offen offset:2048 sc1\n"
MF"v[140:143],a[156:159],a[92:95],v[140:143]\n"
RT"a[178:179],v228 offset:4608\n"
MF"v[140:143],a[152:155],a[76:79],v[140:143]\n"
MF"v[144:147],a[160:163],a[64:67],v[144:147]\n"
WT"lgkmcnt(3)\n"
RT"a[180:181],v228 offset:12288\n"
"s_mov_b32 s49,286720\n"
MF"v[144:147],a[164:167],a[80:83],v[144:147]\n"
MF"v[152:155],a[164:167],a[84:87],v[152:155]\n"
RT"a[182:183],v228 offset:12800\n"
MF"v[152:155],a[160:163],a[68:71],v[152:155]\n"
BS"v[0:3],v250,s[8:11],s49 offen offset:0 sc1\n"
MF"v[160:163],a[160:163],a[72:75],v[160:163]\n"
BL"a[40:43],v235,s[8:11],s58 offen offset:2048 sc1\n"
MF"v[160:163],a[164:167],a[88:91],v[160:163]\n"
RT"a[184:185],v229 offset:4096\n"
MF"v[168:171],a[164:167],a[92:95],v[168:171]\n"
RT"a[186:187],v229 offset:4608\n"
MF"v[168:171],a[160:163],a[76:79],v[168:171]\n"
MF"v[148:151],a[168:171],a[64:67],v[148:151]\n"
WT"lgkmcnt(3)\n"
RT"a[188:189],v229 offset:12288\n"
MF"v[148:151],a[172:175],a[80:83],v[148:151]\n"
MF"v[156:159],a[172:175],a[84:87],v[156:159]\n"
RT"a[190:191],v229 offset:12800\n"
MF"v[156:159],a[168:171],a[68:71],v[156:159]\n"
BL"a[44:47],v235,s[8:11],s59 offen offset:2048 sc1\n"
MF"v[164:167],a[168:171],a[72:75],v[164:167]\n"
RT"a[128:129],v246 offset:0\n"
MF"v[164:167],a[172:175],a[88:91],v[164:167]\n"
RT"a[130:131],v246 offset:512\n"
MF"v[172:175],a[172:175],a[92:95],v[172:175]\n"
SA"m0,s38,131072\n"
SA"s48,s60,s61\n"
MF"v[172:175],a[168:171],a[76:79],v[172:175]\n"
RT"a[132:133],v246 offset:24576\n"
MF"v[176:179],a[176:179],a[64:67],v[176:179]\n"
WT"lgkmcnt(4)\n"
BL"v238,s[20:23],s48 offen offset:0 lds\n"
MF"v[176:179],a[180:183],a[80:83],v[176:179]\n"
RT"a[134:135],v246 offset:25088\n"
MF"v[184:187],a[180:183],a[84:87],v[184:187]\n"
BL"a[48:51],v235,s[8:11],s56 offen offset:3072 sc1\n"
MF"v[184:187],a[176:179],a[68:71],v[184:187]\n"
RT"a[136:137],v247 offset:0\n"
MF"v[192:195],a[176:179],a[72:75],v[192:195]\n"
MF"v[192:195],a[180:183],a[88:91],v[192:195]\n"
RT"a[138:139],v247 offset:512\n"
MF"v[200:203],a[180:183],a[92:95],v[200:203]\n"
MF"v[200:203],a[176:179],a[76:79],v[200:203]\n"
RT"a[140:141],v247 offset:24576\n"
MF"v[180:183],a[184:187],a[64:67],v[180:183]\n"
WT"lgkmcnt(3)\n"
BL"v239,s[20:23],s48 offen offset:1024 lds\n"
MF"v[180:183],a[188:191],a[80:83],v[180:183]\n"
BL"a[52:55],v235,s[8:11],s57 offen offset:3072 sc1\n"
MF"v[188:191],a[188:191],a[84:87],v[188:191]\n"
RT"a[142:143],v247 offset:25088\n"
MF"v[188:191],a[184:187],a[68:71],v[188:191]\n"
RT"a[144:145],v248 offset:0\n"
MF"v[196:199],a[184:187],a[72:75],v[196:199]\n"
MF"v[196:199],a[188:191],a[88:91],v[196:199]\n"
RT"a[146:147],v248 offset:512\n"
MF"v[204:207],a[188:191],a[92:95],v[204:207]\n"
MF"v[204:207],a[184:187],a[76:79],v[204:207]\n"
WT"vmcnt(35)\n"
RT"a[148:149],v248 offset:24576\n"
MF"v[80:83],a[128:131],a[96:99],v[80:83]\n"
WT"vmcnt(27)\n"
BL"a[56:59],v235,s[8:11],s58 offen offset:3072 sc1\n"
MF"v[80:83],a[132:135],a[112:115],v[80:83]\n"
WT"vmcnt(27)\n"
BL"v240,s[20:23],s48 offen offset:2048 lds\n"
MF"v[88:91],a[132:135],a[116:119],v[88:91]\n"
RT"a[150:151],v248 offset:25088\n"
RT"a[152:153],v249 offset:0\n"
MF"v[88:91],a[128:131],a[100:103],v[88:91]\n"
MF"v[96:99],a[128:131],a[104:107],v[96:99]\n"
WT"vmcnt(27)\n"
RT"a[154:155],v249 offset:512\n"
MF"v[96:99],a[132:135],a[120:123],v[96:99]\n"
WT"vmcnt(26)\n"
BS"v[16:19],v250,s[8:11],s49 offen offset:1024 sc1\n"
MF"v[104:107],a[132:135],a[124:127],v[104:107]\n"
SA"s99,s99,4096\n"
RT"a[156:157],v249 offset:24576\n"
"s_cmp_eq_u32 s99,20480\n"
MF"v[104:107],a[128:131],a[108:111],v[104:107]\n"
WT"lgkmcnt(4)\n"
BL"a[60:63],v235,s[8:11],s59 offen offset:3072 sc1\n"
MF"v[84:87],a[136:139],a[96:99],v[84:87]\n"
"s_cselect_b32 s99,0,s99\n"
RT"a[158:159],v249 offset:25088\n"
SA"s61,s61,524288\n"
MF"v[84:87],a[140:143],a[112:115],v[84:87]\n"
"s_cmp_eq_u32 s61,5242880\n"
BL"v241,s[20:23],s48 offen offset:3072 lds\n"
MF"v[92:95],a[140:143],a[116:119],v[92:95]\n"
"s_cselect_b32 s61,0,s61\n"
RT"a[160:161],v246 offset:4096\n"
MF"v[92:95],a[136:139],a[100:103],v[92:95]\n"
MF"v[100:103],a[136:139],a[104:107],v[100:103]\n"
RT"a[162:163],v246 offset:4608\n"
"v_add_u32 v235,s99,v224\n"
MF"v[100:103],a[140:143],a[120:123],v[100:103]\n"
BL"a[64:67],v235,s[8:11],s56 offen offset:0 sc1\n"
MF"v[108:111],a[140:143],a[124:127],v[108:111]\n"
RT"a[164:165],v246 offset:28672\n"
MF"v[108:111],a[136:139],a[108:111],v[108:111]\n"
MF"v[112:115],a[144:147],a[96:99],v[112:115]\n"
WT"lgkmcnt(3)\n"
RT"a[166:167],v246 offset:29184\n"
"v_add_u32 v225,s36,v224\n"
MF"v[112:115],a[148:151],a[112:115],v[112:115]\n"
"s_lshl_b32 s39,s35,20\n"
SA"s39,s39,9175040\n"
MF"v[120:123],a[148:151],a[116:119],v[120:123]\n"
RT"a[168:169],v247 offset:4096\n"
MF"v[120:123],a[144:147],a[100:103],v[120:123]\n"
MF"v[128:131],a[144:147],a[104:107],v[128:131]\n"
RT"a[170:171],v247 offset:4608\n"
MF"v[128:131],a[148:151],a[120:123],v[128:131]\n"
BL"a[68:71],v235,s[8:11],s57 offen offset:0 sc1\n"
MF"v[136:139],a[148:151],a[124:127],v[136:139]\n"
RT"a[172:173],v247 offset:28672\n"
MF"v[136:139],a[144:147],a[108:111],v[136:139]\n"
RT"a[174:175],v247 offset:29184\n"
MF"v[116:119],a[152:155],a[96:99],v[116:119]\n"
MF"v[116:119],a[156:159],a[112:115],v[116:119]\n"
RT"a[176:177],v248 offset:4096\n"
MF"v[124:127],a[156:159],a[116:119],v[124:127]\n"
MF"v[124:127],a[152:155],a[100:103],v[124:127]\n"
RT"a[178:179],v248 offset:4608\n"
MF"v[132:135],a[152:155],a[104:107],v[132:135]\n"
BL"a[72:75],v235,s[8:11],s58 offen offset:0 sc1\n"
MF"v[132:135],a[156:159],a[120:123],v[132:135]\n"
RT"a[180:181],v248 offset:28672\n"
MF"v[140:143],a[156:159],a[124:127],v[140:143]\n"
MF"v[140:143],a[152:155],a[108:111],v[140:143]\n"
WT"lgkmcnt(3)\n"
RT"a[182:183],v248 offset:29184\n"
MF"v[144:147],a[160:163],a[96:99],v[144:147]\n"
MF"v[144:147],a[164:167],a[112:115],v[144:147]\n"
RT"a[184:185],v249 offset:4096\n"
MF"v[152:155],a[164:167],a[116:119],v[152:155]\n"
BL"a[76:79],v235,s[8:11],s59 offen offset:0 sc1\n"
MF"v[152:155],a[160:163],a[100:103],v[152:155]\n"
RT"a[186:187],v249 offset:4608\n"
MF"v[160:163],a[160:163],a[104:107],v[160:163]\n"
SA"s49,s88,286720\n"
MF"v[160:163],a[164:167],a[120:123],v[160:163]\n"
RT"a[188:189],v249 offset:28672\n"
MF"v[168:171],a[164:167],a[124:127],v[168:171]\n"
BS"v[32:35],v250,s[8:11],s49 offen offset:0 sc1\n"
MF"v[168:171],a[160:163],a[108:111],v[168:171]\n"
RT"a[190:191],v249 offset:29184\n"
MF"v[148:151],a[168:171],a[96:99],v[148:151]\n"
MF"v[148:151],a[172:175],a[112:115],v[148:151]\n"
BL"a[80:83],v235,s[8:11],s56 offen offset:1024 sc1\n"
MF"v[156:159],a[172:175],a[116:119],v[156:159]\n"
MF"v[156:159],a[168:171],a[100:103],v[156:159]\n"
MF"v[164:167],a[168:171],a[104:107],v[164:167]\n"
MF"v[164:167],a[172:175],a[120:123],v[164:167]\n"
"s_barrier\n"
MF"v[172:175],a[172:175],a[124:127],v[172:175]\n"
RT"a[128:129],v242 offset:0\n"
MF"v[172:175],a[168:171],a[108:111],v[172:175]\n"
WT"lgkmcnt(1)\n"
MF"v[176:179],a[176:179],a[96:99],v[176:179]\n"
RT"a[130:131],v242 offset:512\n"
MF"v[176:179],a[180:183],a[112:115],v[176:179]\n"
BL"a[84:87],v235,s[8:11],s57 offen offset:1024 sc1\n"
MF"v[184:187],a[180:183],a[116:119],v[184:187]\n"
RT"a[132:133],v242 offset:24576\n"
MF"v[184:187],a[176:179],a[100:103],v[184:187]\n"
MF"v[192:195],a[176:179],a[104:107],v[192:195]\n"
RT"a[134:135],v242 offset:25088\n"
MF"v[192:195],a[180:183],a[120:123],v[192:195]\n"
MF"v[200:203],a[180:183],a[124:127],v[200:203]\n"
RT"a[136:137],v243 offset:0\n"
MF"v[200:203],a[176:179],a[108:111],v[200:203]\n"
BL"a[88:91],v235,s[8:11],s58 offen offset:1024 sc1\n"
MF"v[180:183],a[184:187],a[96:99],v[180:183]\n"
RT"a[138:139],v243 offset:512\n"
MF"v[180:183],a[188:191],a[112:115],v[180:183]\n"
MF"v[188:191],a[188:191],a[116:119],v[188:191]\n"
RT"a[140:141],v243 offset:24576\n"
MF"v[188:191],a[184:187],a[100:103],v[188:191]\n"
MF"v[196:199],a[184:187],a[104:107],v[196:199]\n"
RT"a[142:143],v243 offset:25088\n"
MF"v[196:199],a[188:191],a[120:123],v[196:199]\n"
MF"v[204:207],a[188:191],a[124:127],v[204:207]\n"
RT"a[144:145],v244 offset:0\n"
MF"v[204:207],a[184:187],a[108:111],v[204:207]\n"
WT"vmcnt(36) lgkmcnt(3)\n"
BL"a[92:95],v235,s[8:11],s59 offen offset:1024 sc1\n"
MF"v[80:83],a[128:131],a[0:3],v[80:83]\n"
WT"vmcnt(30)\n"
RT"a[146:147],v244 offset:512\n"
MF"v[80:83],a[132:135],a[16:19],v[80:83]\n"
WT"vmcnt(27)\n"
MF"v[88:91],a[132:135],a[20:23],v[88:91]\n"
RT"a[148:149],v244 offset:24576\n"
MF"v[88:91],a[128:131],a[4:7],v[88:91]\n"
MF"v[96:99],a[128:131],a[8:11],v[96:99]\n"
WT"vmcnt(25)\n"
RT"a[150:151],v244 offset:25088\n"
MF"v[96:99],a[132:135],a[24:27],v[96:99]\n"
WT"vmcnt(23)\n"
RT"a[152:153],v245 offset:0\n"
MF"v[104:107],a[132:135],a[28:31],v[104:107]\n"
BL"a[96:99],v235,s[8:11],s56 offen offset:2048 sc1\n"
MF"v[104:107],a[128:131],a[12:15],v[104:107]\n"
RT"a[154:155],v245 offset:512\n"
MF"v[84:87],a[136:139],a[0:3],v[84:87]\n"
WT"lgkmcnt(4)\n"
BS"v[48:51],v250,s[8:11],s49 offen offset:1024 sc1\n"
MF"v[84:87],a[140:143],a[16:19],v[84:87]\n"
RT"a[156:157],v245 offset:24576\n"
MF"v[92:95],a[140:143],a[20:23],v[92:95]\n"
MF"v[92:95],a[136:139],a[4:7],v[92:95]\n"
RT"a[158:159],v245 offset:25088\n"
MF"v[100:103],a[136:139],a[8:11],v[100:103]\n"
BL"a[100:103],v235,s[8:11],s57 offen offset:2048 sc1\n"
MF"v[100:103],a[140:143],a[24:27],v[100:103]\n"
RT"a[160:161],v242 offset:4096\n"
MF"v[108:111],a[140:143],a[28:31],v[108:111]\n"
MF"v[108:111],a[136:139],a[12:15],v[108:111]\n"
RT"a[162:163],v242 offset:4608\n"
MF"v[112:115],a[144:147],a[0:3],v[112:115]\n"
WT"lgkmcnt(3)\n"
MF"v[112:115],a[148:151],a[16:19],v[112:115]\n"
RT"a[164:165],v242 offset:28672\n"
MF"v[120:123],a[148:151],a[20:23],v[120:123]\n"
MF"v[120:123],a[144:147],a[4:7],v[120:123]\n"
RT"a[166:167],v242 offset:29184\n"
MF"v[128:131],a[144:147],a[8:11],v[128:131]\n"
BL"a[104:107],v235,s[8:11],s58 offen offset:2048 sc1\n"
MF"v[128:131],a[148:151],a[24:27],v[128:131]\n"
RT"a[168:169],v243 offset:4096\n"
MF"v[136:139],a[148:151],a[28:31],v[136:139]\n"
MF"v[136:139],a[144:147],a[12:15],v[136:139]\n"
RT"a[170:171],v243 offset:4608\n"
MF"v[116:119],a[152:155],a[0:3],v[116:119]\n"
WT"lgkmcnt(3)\n"
RT"a[172:173],v243 offset:28672\n"
MF"v[116:119],a[156:159],a[16:19],v[116:119]\n"
MF"v[124:127],a[156:159],a[20:23],v[124:127]\n"
RT"a[174:175],v243 offset:29184\n"
MF"v[124:127],a[152:155],a[4:7],v[124:127]\n"
BL"a[108:111],v235,s[8:11],s59 offen offset:2048 sc1\n"
MF"v[132:135],a[152:155],a[8:11],v[132:135]\n"
RT"a[176:177],v244 offset:4096\n"
MF"v[132:135],a[156:159],a[24:27],v[132:135]\n"
MF"v[140:143],a[156:159],a[28:31],v[140:143]\n"
RT"a[178:179],v244 offset:4608\n"
MF"v[140:143],a[152:155],a[12:15],v[140:143]\n"
MF"v[144:147],a[160:163],a[0:3],v[144:147]\n"
WT"lgkmcnt(3)\n"
RT"a[180:181],v244 offset:28672\n"
MF"v[144:147],a[164:167],a[16:19],v[144:147]\n"
BL"a[112:115],v235,s[8:11],s56 offen offset:3072 sc1\n"
MF"v[152:155],a[164:167],a[20:23],v[152:155]\n"
RT"a[182:183],v244 offset:29184\n"
MF"v[152:155],a[160:163],a[4:7],v[152:155]\n"
MF"v[160:163],a[160:163],a[8:11],v[160:163]\n"
RT"a[184:185],v245 offset:4096\n"
MF"v[160:163],a[164:167],a[24:27],v[160:163]\n"
MF"v[168:171],a[164:167],a[28:31],v[168:171]\n"
RT"a[186:187],v245 offset:4608\n"
MF"v[168:171],a[160:163],a[12:15],v[168:171]\n"
MF"v[148:151],a[168:171],a[0:3],v[148:151]\n"
WT"lgkmcnt(3)\n"
RT"a[188:189],v245 offset:28672\n"
"s_mov_b32 s49,270336\n"
MF"v[148:151],a[172:175],a[16:19],v[148:151]\n"
BL"a[116:119],v235,s[8:11],s57 offen offset:3072 sc1\n"
MF"v[156:159],a[172:175],a[20:23],v[156:159]\n"
RT"a[190:191],v245 offset:29184\n"
MF"v[156:159],a[168:171],a[4:7],v[156:159]\n"
BS"v[36:39],v250,s[8:11],s49 offen offset:0 sc1\n"
MF"v[164:167],a[168:171],a[8:11],v[164:167]\n"
RT"a[128:129],v236 offset:16384\n"
MF"v[164:167],a[172:175],a[24:27],v[164:167]\n"
RT"a[130:131],v236 offset:16896\n"
MF"v[172:175],a[172:175],a[28:31],v[172:175]\n"
MF"v[172:175],a[168:171],a[12:15],v[172:175]\n"
RT"a[132:133],v236 offset:24576\n"
MF"v[176:179],a[176:179],a[0:3],v[176:179]\n"
WT"lgkmcnt(4)\n"
BL"a[120:123],v235,s[8:11],s58 offen offset:3072 sc1\n"
MF"v[176:179],a[180:183],a[16:19],v[176:179]\n"
RT"a[134:135],v236 offset:25088\n"
MF"v[184:187],a[180:183],a[20:23],v[184:187]\n"
MF"v[184:187],a[176:179],a[4:7],v[184:187]\n"
RT"a[136:137],v237 offset:16384\n"
MF"v[192:195],a[176:179],a[8:11],v[192:195]\n"
MF"v[192:195],a[180:183],a[24:27],v[192:195]\n"
SA"s99,s99,4096\n"
RT"a[138:139],v237 offset:16896\n"
MF"v[200:203],a[180:183],a[28:31],v[200:203]\n"
"s_cmp_eq_u32 s99,20480\n"
BL"a[124:127],v235,s[8:11],s59 offen offset:3072 sc1\n"
MF"v[200:203],a[176:179],a[12:15],v[200:203]\n"
"s_cselect_b32 s99,0,s99\n"
RT"a[140:141],v237 offset:24576\n"
MF"v[180:183],a[184:187],a[0:3],v[180:183]\n"
WT"lgkmcnt(3)\n"
"v_lshrrev_b32 v252,1,v224\n"
MF"v[180:183],a[188:191],a[16:19],v[180:183]\n"
RT"a[142:143],v237 offset:25088\n"
"v_and_b32 v252,56,v252\n"
MF"v[188:191],a[188:191],a[20:23],v[188:191]\n"
"s_lshl_b32 s50,s35,6\n"
MF"v[188:191],a[184:187],a[4:7],v[188:191]\n"
SA"s50,s50,2304\n"
RT"a[144:145],v228 offset:16384\n"
MF"v[196:199],a[184:187],a[8:11],v[196:199]\n"
"buffer_load_dwordx2 v[208:209],v252,s[8:11],s50 offen sc1\n"
MF"v[196:199],a[188:191],a[24:27],v[196:199]\n"
RT"a[146:147],v228 offset:16896\n"
"v_add_u32 v235,s99,v224\n"
MF"v[204:207],a[188:191],a[28:31],v[204:207]\n"
BL"a[192:195],v235,s[8:11],s56 offen offset:0 sc1\n"
MF"v[204:207],a[184:187],a[12:15],v[204:207]\n"
WT"vmcnt(34)\n"
RT"a[148:149],v228 offset:24576\n"
MF"v[80:83],a[128:131],a[32:35],v[80:83]\n"
WT"vmcnt(28)\n"
MF"v[80:83],a[132:135],a[48:51],v[80:83]\n"
WT"vmcnt(26)\n"
RT"a[150:151],v228 offset:25088\n"
MF"v[88:91],a[132:135],a[52:55],v[88:91]\n"
RT"a[152:153],v229 offset:16384\n"
MF"v[88:91],a[128:131],a[36:39],v[88:91]\n"
MF"v[96:99],a[128:131],a[40:43],v[96:99]\n"
WT"vmcnt(25)\n"
RT"a[154:155],v229 offset:16896\n"
MF"v[96:99],a[132:135],a[56:59],v[96:99]\n"
WT"vmcnt(22)\n"
BL"a[196:199],v235,s[8:11],s57 offen offset:0 sc1\n"
MF"v[104:107],a[132:135],a[60:63],v[104:107]\n"
RT"a[156:157],v229 offset:24576\n"
MF"v[104:107],a[128:131],a[44:47],v[104:107]\n"
WT"lgkmcnt(4)\n"
MF"v[84:87],a[136:139],a[32:35],v[84:87]\n"
RT"a[158:159],v229 offset:25088\n"
MF"v[84:87],a[140:143],a[48:51],v[84:87]\n"
MF"v[92:95],a[140:143],a[52:55],v[92:95]\n"
RT"a[160:161],v236 offset:20480\n"
MF"v[92:95],a[136:139],a[36:39],v[92:95]\n"
BL"a[200:203],v235,s[8:11],s58 offen offset:0 sc1\n"
MF"v[100:103],a[136:139],a[40:43],v[100:103]\n"
RT"a[162:163],v236 offset:20992\n"
MF"v[100:103],a[140:143],a[56:59],v[100:103]\n"
BS"v[44:47],v250,s[8:11],s49 offen offset:1024 sc1\n"
MF"v[108:111],a[140:143],a[60:63],v[108:111]\n"
RT"a[164:165],v236 offset:28672\n"
MF"v[108:111],a[136:139],a[44:47],v[108:111]\n"
MF"v[112:115],a[144:147],a[32:35],v[112:115]\n"
WT"lgkmcnt(3)\n"
RT"a[166:167],v236 offset:29184\n"
MF"v[112:115],a[148:151],a[48:51],v[112:115]\n"
MF"v[120:123],a[148:151],a[52:55],v[120:123]\n"
RT"a[168:169],v237 offset:20480\n"
MF"v[120:123],a[144:147],a[36:39],v[120:123]\n"
BL"a[204:207],v235,s[8:11],s59 offen offset:0 sc1\n"
MF"v[128:131],a[144:147],a[40:43],v[128:131]\n"
RT"a[170:171],v237 offset:20992\n"
MF"v[128:131],a[148:151],a[56:59],v[128:131]\n"
RT"a[172:173],v237 offset:28672\n"
MF"v[136:139],a[148:151],a[60:63],v[136:139]\n"
MF"v[136:139],a[144:147],a[44:47],v[136:139]\n"
RT"a[174:175],v237 offset:29184\n"
MF"v[116:119],a[152:155],a[32:35],v[116:119]\n"
MF"v[116:119],a[156:159],a[48:51],v[116:119]\n"
RT"a[176:177],v228 offset:20480\n"
MF"v[124:127],a[156:159],a[52:55],v[124:127]\n"
BL"a[208:211],v235,s[8:11],s56 offen offset:1024 sc1\n"
MF"v[124:127],a[152:155],a[36:39],v[124:127]\n"
RT"a[178:179],v228 offset:20992\n"
MF"v[132:135],a[152:155],a[40:43],v[132:135]\n"
MF"v[132:135],a[156:159],a[56:59],v[132:135]\n"
RT"a[180:181],v228 offset:28672\n"
MF"v[140:143],a[156:159],a[60:63],v[140:143]\n"
MF"v[140:143],a[152:155],a[44:47],v[140:143]\n"
WT"lgkmcnt(3)\n"
RT"a[182:183],v228 offset:29184\n"
MF"v[144:147],a[160:163],a[32:35],v[144:147]\n"
BL"a[212:215],v235,s[8:11],s57 offen offset:1024 sc1\n"
MF"v[144:147],a[164:167],a[48:51],v[144:147]\n"
RT"a[184:185],v229 offset:20480\n"
MF"v[152:155],a[164:167],a[52:55],v[152:155]\n"
MF"v[152:155],a[160:163],a[36:39],v[152:155]\n"
RT"a[186:187],v229 offset:20992\n"
MF"v[160:163],a[160:163],a[40:43],v[160:163]\n"
MF"v[160:163],a[164:167],a[56:59],v[160:163]\n"
RT"a[188:189],v229 offset:28672\n"
MF"v[168:171],a[164:167],a[60:63],v[168:171]\n"
MF"v[168:171],a[160:163],a[44:47],v[168:171]\n"
RT"a[190:191],v229 offset:29184\n"
MF"v[148:151],a[168:171],a[32:35],v[148:151]\n"
BL"a[216:219],v235,s[8:11],s58 offen offset:1024 sc1\n"
MF"v[148:151],a[172:175],a[48:51],v[148:151]\n"
WT"vmcnt(8)\n"
"v_cmp_eq_u64 vcc,s[28:29],v[208:209]\n"
"s_cmp_eq_u64 vcc,exec\n"
"s_cbranch_scc0 L1010_%=\n"
"L1011_%=:\n"
MF"v[156:159],a[172:175],a[52:55],v[156:159]\n"
MF"v[156:159],a[168:171],a[36:39],v[156:159]\n"
MF"v[164:167],a[168:171],a[40:43],v[164:167]\n"
SA"s49,s88,270336\n"
MF"v[164:167],a[172:175],a[56:59],v[164:167]\n"
WT"lgkmcnt(0)\n"
"s_barrier\n"
MF"v[172:175],a[172:175],a[60:63],v[172:175]\n"
SA"m0,s36,0\n"
RT"a[128:129],v246 offset:8192\n"
SA"s48,s39,0\n"
MF"v[172:175],a[168:171],a[44:47],v[172:175]\n"
BS"v[52:55],v250,s[8:11],s49 offen offset:0 sc1\n"
MF"v[176:179],a[176:179],a[32:35],v[176:179]\n"
BL"a[220:223],v235,s[8:11],s59 offen offset:1024 sc1\n"
MF"v[176:179],a[180:183],a[48:51],v[176:179]\n"
RT"a[130:131],v246 offset:8704\n"
MF"v[184:187],a[180:183],a[52:55],v[184:187]\n"
BL"v225,s[8:11],s48 offen offset:0 sc1 lds\n"
MF"v[184:187],a[176:179],a[36:39],v[184:187]\n"
RT"a[132:133],v246 offset:32768\n"
"v_lshlrev_b32 v254,10,v224\n"
MF"v[192:195],a[176:179],a[40:43],v[192:195]\n"
"s_lshl_b32 s94,s34,1\n"
RT"a[134:135],v246 offset:33280\n"
"v_lshrrev_b32 v255,4,v224\n"
MF"v[192:195],a[180:183],a[56:59],v[192:195]\n"
SA"s95,s94,2\n"
BL"a[224:227],v235,s[8:11],s56 offen offset:2048 sc1\n"
MF"v[200:203],a[180:183],a[60:63],v[200:203]\n"
"s_sub_u32 s94,32,s94\n"
RT"a[136:137],v247 offset:8192\n"
MF"v[200:203],a[176:179],a[44:47],v[200:203]\n"
"v_add_u32 v228,s39,v254\n"
MF"v[180:183],a[184:187],a[32:35],v[180:183]\n"
RT"a[138:139],v247 offset:8704\n"
"v_and_b32 v254,1,v255\n"
MF"v[180:183],a[188:191],a[48:51],v[180:183]\n"
BL"v225,s[8:11],s48 offen offset:1024 sc1 lds\n"
MF"v[188:191],a[188:191],a[52:55],v[188:191]\n"
RT"a[140:141],v247 offset:32768\n"
"v_mul_u32_u24 v254,491520,v254\n"
MF"v[188:191],a[184:187],a[36:39],v[188:191]\n"
"v_cmp_gt_u32 vcc,s95,v255\n"
"v_mov_b32 v229,16384\n"
MF"v[196:199],a[184:187],a[40:43],v[196:199]\n"
RT"a[142:143],v247 offset:33280\n"
"v_mov_b32 v253,516096\n"
MF"v[196:199],a[188:191],a[56:59],v[196:199]\n"
BL"a[228:231],v235,s[8:11],s57 offen offset:2048 sc1\n"
MF"v[204:207],a[188:191],a[60:63],v[204:207]\n"
RT"a[144:145],v248 offset:8192\n"
"v_cndmask_b32_e64 v254,v254,0,vcc\n"
MF"v[204:207],a[184:187],a[44:47],v[204:207]\n"
WT"lgkmcnt(3)\n"
"v_cndmask_b32 v229,v229,v253,vcc\n"
"v_cmp_eq_u32 vcc,s94,v255\n"
MF"v[80:83],a[128:131],a[64:67],v[80:83]\n"
RT"a[146:147],v248 offset:8704\n"
"v_mov_b32 v253,507904\n"
MF"v[80:83],a[132:135],a[80:83],v[80:83]\n"
BL"v225,s[8:11],s48 offen offset:2048 sc1 lds\n"
MF"v[88:91],a[132:135],a[84:87],v[88:91]\n"
RT"a[148:149],v248 offset:32768\n"
"v_cndmask_b32 v254,v254,v253,vcc\n"
MF"v[88:91],a[128:131],a[68:71],v[88:91]\n"
BL"a[232:235],v235,s[8:11],s58 offen offset:2048 sc1\n"
MF"v[96:99],a[128:131],a[72:75],v[96:99]\n"
RT"a[150:151],v248 offset:33280\n"
"v_mov_b32 v253,1862270976\n"
MF"v[96:99],a[132:135],a[88:91],v[96:99]\n"
"s_mul_i32 s95,s35,163840\n"
RT"a[152:153],v249 offset:8192\n"
"v_cndmask_b32 v229,v229,v253,vcc\n"
MF"v[104:107],a[132:135],a[92:95],v[104:107]\n"
SA"s95,s95,3395584\n"
"v_add_u32 v228,v228,v254\n"
MF"v[104:107],a[128:131],a[76:79],v[104:107]\n"
RT"a[154:155],v249 offset:8704\n"
"v_subrev_u32 v254,s94,v255\n"
MF"v[84:87],a[136:139],a[64:67],v[84:87]\n"
WT"lgkmcnt(4)\n"
BL"v225,s[8:11],s48 offen offset:3072 sc1 lds\n"
MF"v[84:87],a[140:143],a[80:83],v[84:87]\n"
RT"a[156:157],v249 offset:32768\n"
"v_lshlrev_b32 v254,15,v254\n"
MF"v[92:95],a[140:143],a[84:87],v[92:95]\n"
BL"a[236:239],v235,s[8:11],s59 offen offset:2048 sc1\n"
MF"v[92:95],a[136:139],a[68:71],v[92:95]\n"
RT"a[158:159],v249 offset:33280\n"
"v_add_u32 v254,s95,v254\n"
MF"v[100:103],a[136:139],a[72:75],v[100:103]\n"
"v_cmp_lt_u32 vcc,s94,v255\n"
MF"v[100:103],a[140:143],a[88:91],v[100:103]\n"
"s_mov_b32 s47,s36\n"
RT"a[160:161],v246 offset:12288\n"
"v_cndmask_b32 v228,v228,v254,vcc\n"
MF"v[108:111],a[140:143],a[92:95],v[108:111]\n"
SA"m0,s36,16384\n"
"v_add_u32 v229,v229,v228\n"
SA"s48,s39,516096\n"
MF"v[108:111],a[136:139],a[76:79],v[108:111]\n"
"s_cmp_lt_u32 s30,2\n"
RT"a[162:163],v246 offset:12800\n"
"s_cselect_b32 s48,2130706432,s48\n"
MF"v[112:115],a[144:147],a[64:67],v[112:115]\n"
WT"lgkmcnt(3)\n"
BS"v[60:63],v250,s[8:11],s49 offen offset:1024 sc1\n"
MF"v[112:115],a[148:151],a[80:83],v[112:115]\n"
SA"s46,s36,163840\n"
BL"v225,s[8:11],s48 offen offset:0 sc1 lds\n"
MF"v[120:123],a[148:151],a[84:87],v[120:123]\n"
SA"s40,s36,65536\n"
RT"a[164:165],v246 offset:36864\n"
"v_mov_b32 v226,v224\n"
MF"v[120:123],a[144:147],a[68:71],v[120:123]\n"
"s_mov_b32 s41,0\n"
BL"a[240:243],v235,s[8:11],s56 offen offset:3072 sc1\n"
MF"v[128:131],a[144:147],a[72:75],v[128:131]\n"
"s_mov_b32 s42,2\n"
RT"a[166:167],v246 offset:37376\n"
"v_bfe_u32 v254,v224,4,4\n"
MF"v[128:131],a[148:151],a[88:91],v[128:131]\n"
RT"a[168:169],v247 offset:12288\n"
"v_lshrrev_b32 v255,8,v224\n"
MF"v[136:139],a[148:151],a[92:95],v[136:139]\n"
"s_lshl_b32 s94,s30,4\n"
"v_lshlrev_b32 v255,2,v255\n"
"v_sub_u32 v254,v254,v255\n"
MF"v[136:139],a[144:147],a[76:79],v[136:139]\n"
RT"a[170:171],v247 offset:12800\n"
"v_add_u32 v254,s94,v254\n"
MF"v[116:119],a[152:155],a[64:67],v[116:119]\n"
WT"lgkmcnt(4)\n"
RT"a[172:173],v247 offset:36864\n"
"v_mov_b32 v255,0xff800000\n"
MF"v[116:119],a[156:159],a[80:83],v[116:119]\n"
BL"a[244:247],v235,s[8:11],s57 offen offset:3072 sc1\n"
MF"v[124:127],a[156:159],a[84:87],v[124:127]\n"
BL"v225,s[8:11],s48 offen offset:1024 sc1 lds\n"
MF"v[124:127],a[152:155],a[68:71],v[124:127]\n"
RT"a[174:175],v247 offset:37376\n"
"v_cmp_gt_i32_e64 vcc,0,v254\n"
MF"v[132:135],a[152:155],a[72:75],v[132:135]\n"
RT"a[176:177],v248 offset:12288\n"
"v_cndmask_b32_e64 v208,0,v255,vcc\n"
MF"v[132:135],a[156:159],a[88:91],v[132:135]\n"
"v_cmp_gt_i32_e64 s[52:53],1,v254\n"
"v_cmp_gt_i32_e64 s[62:63],2,v254\n"
MF"v[140:143],a[156:159],a[92:95],v[140:143]\n"
RT"a[178:179],v248 offset:12800\n"
"v_cndmask_b32_e64 v209,0,v255,s[52:53]\n"
MF"v[140:143],a[152:155],a[76:79],v[140:143]\n"
BL"a[248:251],v235,s[8:11],s58 offen offset:3072 sc1\n"
MF"v[144:147],a[160:163],a[64:67],v[144:147]\n"
WT"lgkmcnt(3)\n"
RT"a[180:181],v248 offset:36864\n"
"v_cndmask_b32_e64 v210,0,v255,s[62:63]\n"
MF"v[144:147],a[164:167],a[80:83],v[144:147]\n"
BL"v225,s[8:11],s48 offen offset:2048 sc1 lds\n"
MF"v[152:155],a[164:167],a[84:87],v[152:155]\n"
RT"a[182:183],v248 offset:37376\n"
"v_cmp_gt_i32_e64 vcc,3,v254\n"
MF"v[152:155],a[160:163],a[68:71],v[152:155]\n"
"v_cmp_gt_i32_e64 s[52:53],16,v254\n"
"v_cndmask_b32_e64 v211,0,v255,vcc\n"
MF"v[160:163],a[160:163],a[72:75],v[160:163]\n"
RT"a[184:185],v249 offset:12288\n"
"v_cndmask_b32_e64 v212,0,v255,s[52:53]\n"
MF"v[160:163],a[164:167],a[88:91],v[160:163]\n"
"v_cmp_gt_i32_e64 s[62:63],17,v254\n"
"v_cmp_gt_i32_e64 vcc,18,v254\n"
MF"v[168:171],a[164:167],a[92:95],v[168:171]\n"
RT"a[186:187],v249 offset:12800\n"
"v_cndmask_b32_e64 v213,0,v255,s[62:63]\n"
MF"v[168:171],a[160:163],a[76:79],v[168:171]\n"
BL"a[252:255],v235,s[8:11],s59 offen offset:3072 sc1\n"
MF"v[148:151],a[168:171],a[64:67],v[148:151]\n"
WT"lgkmcnt(3)\n"
RT"a[188:189],v249 offset:36864\n"
"v_cndmask_b32_e64 v214,0,v255,vcc\n"
MF"v[148:151],a[172:175],a[80:83],v[148:151]\n"
BL"v225,s[8:11],s48 offen offset:3072 sc1 lds\n"
MF"v[156:159],a[172:175],a[84:87],v[156:159]\n"
RT"a[190:191],v249 offset:37376\n"
"v_cmp_gt_i32_e64 s[52:53],19,v254\n"
MF"v[156:159],a[168:171],a[68:71],v[156:159]\n"
RT"a[128:129],v242 offset:8192\n"
"v_cndmask_b32_e64 v215,0,v255,s[52:53]\n"
MF"v[164:167],a[168:171],a[72:75],v[164:167]\n"
"v_cmp_gt_i32_e64 s[62:63],32,v254\n"
"v_cmp_gt_i32_e64 vcc,33,v254\n"
MF"v[164:167],a[172:175],a[88:91],v[164:167]\n"
RT"a[130:131],v242 offset:8704\n"
"v_cndmask_b32_e64 v216,0,v255,s[62:63]\n"
MF"v[172:175],a[172:175],a[92:95],v[172:175]\n"
SA"m0,s36,32768\n"
"v_cndmask_b32_e64 v217,0,v255,vcc\n"
"v_cmp_gt_i32_e64 s[52:53],34,v254\n"
MF"v[172:175],a[168:171],a[76:79],v[172:175]\n"
SA"s48,s39,16384\n"
RT"a[132:133],v242 offset:32768\n"
"v_cndmask_b32_e64 v218,0,v255,s[52:53]\n"
MF"v[176:179],a[176:179],a[64:67],v[176:179]\n"
WT"lgkmcnt(4)\n"
BL"v225,s[8:11],s48 offen offset:0 sc1 lds\n"
MF"v[176:179],a[180:183],a[80:83],v[176:179]\n"
"s_mov_b32 s49,278528\n"
RT"a[134:135],v242 offset:33280\n"
"v_cmp_gt_i32_e64 s[62:63],35,v254\n"
MF"v[184:187],a[180:183],a[84:87],v[184:187]\n"
BS"v[64:67],v250,s[8:11],s49 offen offset:0 sc1\n"
MF"v[184:187],a[176:179],a[68:71],v[184:187]\n"
RT"a[136:137],v243 offset:8192\n"
"v_cndmask_b32_e64 v219,0,v255,s[62:63]\n"
MF"v[192:195],a[176:179],a[72:75],v[192:195]\n"
"s_lshl_b32 s59,s34,5\n"
"v_cmp_gt_i32_e64 vcc,48,v254\n"
"v_cmp_gt_i32_e64 s[52:53],49,v254\n"
MF"v[192:195],a[180:183],a[88:91],v[192:195]\n"
"s_sub_u32 s60,480,s59\n"
RT"a[138:139],v243 offset:8704\n"
"v_cndmask_b32_e64 v220,0,v255,vcc\n"
MF"v[200:203],a[180:183],a[92:95],v[200:203]\n"
SA"s59,s59,s35\n"
"v_cndmask_b32_e64 v221,0,v255,s[52:53]\n"
"v_cmp_gt_i32_e64 s[62:63],50,v254\n"
MF"v[200:203],a[176:179],a[76:79],v[200:203]\n"
SA"s60,s60,s35\n"
RT"a[140:141],v243 offset:32768\n"
"v_cndmask_b32_e64 v222,0,v255,s[62:63]\n"
MF"v[180:183],a[184:187],a[64:67],v[180:183]\n"
WT"lgkmcnt(3)\n"
BL"v225,s[8:11],s48 offen offset:1024 sc1 lds\n"
MF"v[180:183],a[188:191],a[80:83],v[180:183]\n"
"s_lshl_b32 s59,s59,15\n"
RT"a[142:143],v243 offset:33280\n"
"v_cmp_gt_i32_e64 vcc,51,v254\n"
MF"v[188:191],a[188:191],a[84:87],v[188:191]\n"
"s_lshl_b32 s60,s60,15\n"
"v_cndmask_b32_e64 v223,0,v255,vcc\n"
SA"s50,s36,42729472\n"
MF"v[188:191],a[184:187],a[68:71],v[188:191]\n"
SA"s59,s59,s50\n"
RT"a[144:145],v244 offset:8192\n"
SA"s60,s60,s50\n"
MF"v[196:199],a[184:187],a[72:75],v[196:199]\n"
"s_lshl_b32 s61,s34,3\n"
SA"s61,s61,s32\n"
"s_lshl_b32 s61,s61,2\n"
MF"v[196:199],a[188:191],a[88:91],v[196:199]\n"
SA"s61,s61,s30\n"
RT"a[146:147],v244 offset:8704\n"
"s_lshl_b32 s61,s61,2\n"
MF"v[204:207],a[188:191],a[92:95],v[204:207]\n"
SA"s61,s61,s33\n"
"s_lshl_b32 s61,s61,6\n"
MF"v[204:207],a[184:187],a[76:79],v[204:207]\n"
SA"s61,s61,81002496\n"
RT"a[148:149],v244 offset:32768\n"
"s_lshl_b32 s98,s35,3\n"
MF"v[80:83],a[128:131],a[96:99],v[80:83]\n"
SA"s98,s98,s34\n"
BL"v225,s[8:11],s48 offen offset:2048 sc1 lds\n"
MF"v[80:83],a[132:135],a[112:115],v[80:83]\n"
"s_lshl_b32 s98,s98,3\n"
RT"a[150:151],v244 offset:33280\n"
MF"v[88:91],a[132:135],a[116:119],v[88:91]\n"
SA"s98,s98,4352\n"
RT"a[152:153],v245 offset:8192\n"
SA"s98,s98,s45\n"
MF"v[88:91],a[128:131],a[100:103],v[88:91]\n"
"s_lshl_b32 s50,s34,2\n"
MF"v[96:99],a[128:131],a[104:107],v[96:99]\n"
"s_mul_i32 s51,s33,0x1011100\n"
RT"a[154:155],v245 offset:8704\n"
SA"s51,s51,0xf48c00\n"
MF"v[96:99],a[132:135],a[120:123],v[96:99]\n"
"s_lshr_b32 s51,s51,s50\n"
MF"v[104:107],a[132:135],a[124:127],v[104:107]\n"
"s_and_b32 s51,s51,15\n"
RT"a[156:157],v245 offset:32768\n"
"s_lshr_b32 s50,s31,3\n"
MF"v[104:107],a[128:131],a[108:111],v[104:107]\n"
WT"lgkmcnt(4)\n"
"s_cmp_eq_u32 s50,6\n"
"s_cselect_b32 s51,15,s51\n"
MF"v[84:87],a[136:139],a[96:99],v[84:87]\n"
"s_cmp_eq_u32 s50,26\n"
RT"a[158:159],v245 offset:33280\n"
"s_cselect_b32 s51,0,s51\n"
MF"v[84:87],a[140:143],a[112:115],v[84:87]\n"
"s_mul_i32 s58,s32,14\n"
BL"v225,s[8:11],s48 offen offset:3072 sc1 lds\n"
MF"v[92:95],a[140:143],a[116:119],v[92:95]\n"
SA"s58,s58,s51\n"
RT"a[160:161],v242 offset:12288\n"
"s_sub_u32 s58,s58,1\n"
MF"v[92:95],a[136:139],a[100:103],v[92:95]\n"
"s_cmp_lt_u32 s58,108\n"
"s_cselect_b32 s90,s51,0\n"
"s_cmp_lg_u32 s90,0\n"
MF"v[100:103],a[136:139],a[104:107],v[100:103]\n"
"s_cselect_b32 s57,1,0\n"
RT"a[162:163],v242 offset:12800\n"
"s_cmp_eq_u32 s51,15\n"
MF"v[100:103],a[140:143],a[120:123],v[100:103]\n"
"s_cselect_b32 s57,2,s57\n"
"s_cmp_eq_u32 s50,6\n"
"s_cselect_b32 s90,4,s33\n"
MF"v[108:111],a[140:143],a[124:127],v[108:111]\n"
"s_mul_i32 s50,s32,5\n"
RT"a[164:165],v242 offset:36864\n"
SA"m0,s36,49152\n"
MF"v[108:111],a[136:139],a[108:111],v[108:111]\n"
SA"s48,s39,532480\n"
SA"s90,s90,s50\n"
"s_cmp_eq_u32 s57,2\n"
MF"v[112:115],a[144:147],a[96:99],v[112:115]\n"
WT"lgkmcnt(3)\n"
RT"a[166:167],v242 offset:37376\n"
"s_cselect_b32 s58,s90,s58\n"
MF"v[112:115],a[148:151],a[112:115],v[112:115]\n"
"s_lshl_b32 s56,s34,1\n"
BL"v225,s[8:11],s48 offen offset:0 sc1 lds\n"
MF"v[120:123],a[148:151],a[116:119],v[120:123]\n"
"s_sub_u32 s56,s56,2\n"
RT"a[168:169],v243 offset:12288\n"
"s_cmp_eq_u32 s34,7\n"
MF"v[120:123],a[144:147],a[100:103],v[120:123]\n"
"s_cselect_b32 s56,2,s56\n"
"s_cmp_eq_u32 s57,2\n"
"s_cselect_b32 s56,5,s56\n"
MF"v[128:131],a[144:147],a[104:107],v[128:131]\n"
"s_cmp_lt_u32 s30,2\n"
RT"a[170:171],v243 offset:12800\n"
"s_cselect_b64 s[24:25],s[78:79],s[80:81]\n"
MF"v[128:131],a[148:151],a[120:123],v[128:131]\n"
"s_cmp_eq_u32 s57,2\n"
BS"v[68:71],v250,s[8:11],s49 offen offset:1024 sc1\n"
MF"v[136:139],a[148:151],a[124:127],v[136:139]\n"
"s_cselect_b64 s[24:25],s[82:83],s[24:25]\n"
RT"a[172:173],v243 offset:36864\n"
"s_and_b32 s50,s30,1\n"
MF"v[136:139],a[144:147],a[108:111],v[136:139]\n"
"s_mul_i32 s50,s50,10\n"
RT"a[174:175],v243 offset:37376\n"
"s_mul_i32 s51,s30,14\n"
MF"v[116:119],a[152:155],a[96:99],v[116:119]\n"
"s_min_u32 s51,s51,41\n"
BL"v225,s[8:11],s48 offen offset:1024 sc1 lds\n"
MF"v[116:119],a[156:159],a[112:115],v[116:119]\n"
"s_cmp_eq_u32 s57,2\n"
RT"a[176:177],v244 offset:12288\n"
"s_cselect_b32 s50,s51,s50\n"
MF"v[124:127],a[156:159],a[116:119],v[124:127]\n"
"s_cmp_lt_u32 s30,2\n"
"s_cselect_b32 s51,14,13\n"
"s_cmp_lt_u32 s58,108\n"
MF"v[124:127],a[152:155],a[100:103],v[124:127]\n"
"s_cselect_b32 s92,10,0\n"
RT"a[178:179],v244 offset:12800\n"
"s_cmp_eq_u32 s57,2\n"
MF"v[132:135],a[152:155],a[104:107],v[132:135]\n"
"s_cselect_b32 s92,s51,s92\n"
"s_mov_b32 s51,3456\n"
"s_cmp_eq_u32 s57,2\n"
MF"v[132:135],a[156:159],a[120:123],v[132:135]\n"
"s_cselect_b32 s51,1280,s51\n"
RT"a[180:181],v244 offset:36864\n"
"s_lshl_b32 s62,s51,5\n"
MF"v[140:143],a[156:159],a[124:127],v[140:143]\n"
"s_mul_i32 s26,s62,s92\n"
"s_mul_i32 s90,s62,s50\n"
"s_lshl_b32 s91,s58,5\n"
MF"v[140:143],a[152:155],a[108:111],v[140:143]\n"
WT"lgkmcnt(3)\n"
RT"a[182:183],v244 offset:37376\n"
SA"s90,s90,s91\n"
MF"v[144:147],a[160:163],a[96:99],v[144:147]\n"
SA"s24,s24,s90\n"
BL"v225,s[8:11],s48 offen offset:2048 sc1 lds\n"
MF"v[144:147],a[164:167],a[112:115],v[144:147]\n"
"s_addc_u32 s25,s25,0\n"
RT"a[184:185],v245 offset:12288\n"
"s_and_b32 s25,s25,0xffff\n"
MF"v[152:155],a[164:167],a[116:119],v[152:155]\n"
"s_sub_u32 s90,s51,32\n"
"v_lshrrev_b32 v235,5,v224\n"
"v_mul_u32_u24 v235,s51,v235\n"
MF"v[152:155],a[160:163],a[100:103],v[152:155]\n"
"s_sub_u32 s26,s26,s90\n"
RT"a[186:187],v245 offset:12800\n"
"v_and_b32 v239,16,v224\n"
MF"v[160:163],a[160:163],a[104:107],v[160:163]\n"
"s_cselect_b32 s26,0,s26\n"
"v_add_u32 v235,v235,v239\n"
"s_mov_b32 s27,0x20000\n"
MF"v[160:163],a[164:167],a[120:123],v[160:163]\n"
"s_mul_i32 s63,s35,20480\n"
RT"a[188:189],v245 offset:36864\n"
SA"s63,s63,2770944\n"
MF"v[168:171],a[164:167],a[124:127],v[168:171]\n"
"s_cmp_eq_u32 s57,2\n"
"s_cselect_b32 s63,2695168,s63\n"
MF"v[168:171],a[160:163],a[108:111],v[168:171]\n"
"s_lshl_b32 s50,s50,10\n"
RT"a[190:191],v245 offset:37376\n"
SA"s63,s63,s50\n"
MF"v[148:151],a[168:171],a[96:99],v[148:151]\n"
"s_mov_b32 s50,1065353216\n"
BL"v225,s[8:11],s48 offen offset:3072 sc1 lds\n"
MF"v[148:151],a[172:175],a[112:115],v[148:151]\n"
"s_cmp_lg_u32 s92,0\n"
"s_cselect_b32 s50,s45,s50\n"
"s_mov_b32 s93,2695168\n"
MF"v[156:159],a[172:175],a[116:119],v[156:159]\n"
"s_cmp_eq_u32 s57,2\n"
"s_cselect_b32 s93,2750464,s93\n"
MF"v[156:159],a[168:171],a[100:103],v[156:159]\n"
"s_lshl_b32 s51,s58,9\n"
MF"v[164:167],a[168:171],a[104:107],v[164:167]\n"
SA"s93,s93,s51\n"
SA"s93,s93,s50\n"
MF"v[164:167],a[172:175],a[120:123],v[164:167]\n"
"s_mov_b32 s94,6912\n"
"s_barrier\n"
MF"v[172:175],a[172:175],a[124:127],v[172:175]\n"
"s_cmp_eq_u32 s57,2\n"
RT"a[128:129],v246 offset:16384\n"
"s_cselect_b32 s94,6400,s94\n"
MF"v[172:175],a[168:171],a[108:111],v[172:175]\n"
WT"lgkmcnt(1)\n"
"s_lshl_b32 s51,s58,3\n"
SA"s94,s94,s51\n"
MF"v[176:179],a[176:179],a[96:99],v[176:179]\n"
SA"s94,s94,s50\n"
RT"a[130:131],v246 offset:16896\n"
"s_mov_b32 s52,0\n"
MF"v[176:179],a[180:183],a[112:115],v[176:179]\n"
"s_lshl_b32 s50,s35,6\n"
RT"a[132:133],v246 offset:40960\n"
MF"v[184:187],a[180:183],a[116:119],v[184:187]\n"
SA"s50,s50,2304\n"
MF"v[184:187],a[176:179],a[100:103],v[184:187]\n"
"s_cmp_eq_u32 s57,2\n"
RT"a[134:135],v246 offset:41472\n"
"s_cselect_b32 s50,6912,s50\n"
MF"v[192:195],a[176:179],a[104:107],v[192:195]\n"
"s_mov_b32 s51,48\n"
RT"a[136:137],v242 offset:16384\n"
SA"s49,s88,278528\n"
MF"v[192:195],a[180:183],a[120:123],v[192:195]\n"
"s_cmp_eq_u32 s57,2\n"
"s_cselect_b32 s51,848,s51\n"
"v_min_u32 v238,s51,v224\n"
MF"v[200:203],a[180:183],a[124:127],v[200:203]\n"
RT"a[138:139],v242 offset:16896\n"
"v_add_u32 v238,s50,v238\n"
MF"v[200:203],a[176:179],a[108:111],v[200:203]\n"
"s_mov_b32 s16,0\n"
BS"v[72:75],v250,s[8:11],s49 offen offset:0 sc1\n"
MF"v[180:183],a[184:187],a[96:99],v[180:183]\n"
"s_mov_b32 s20,8\n"
RT"a[140:141],v242 offset:40960\n"
"v_and_b32 v239,0x80,v224\n"
MF"v[180:183],a[188:191],a[112:115],v[180:183]\n"
"s_mov_b32 s18,2130706432\n"
RT"a[142:143],v242 offset:41472\n"
"v_lshl_add_u32 v236,v239,23,v224\n"
MF"v[188:191],a[188:191],a[116:119],v[188:191]\n"
"s_lshl_b32 s21,s32,4\n"
"s_lshr_b32 s22,s30,1\n"
MF"v[188:191],a[184:187],a[100:103],v[188:191]\n"
SA"s21,s21,s22\n"
RT"a[144:145],v247 offset:16384\n"
MF"v[196:199],a[184:187],a[104:107],v[196:199]\n"
"s_mul_i32 s21,s21,40960\n"
"s_lshl_b32 s22,s33,1\n"
MF"v[196:199],a[188:191],a[120:123],v[196:199]\n"
"s_and_b32 s23,s30,1\n"
RT"a[146:147],v247 offset:16896\n"
MF"v[204:207],a[188:191],a[124:127],v[204:207]\n"
SA"s22,s22,s23\n"
RT"a[148:149],v247 offset:40960\n"
"s_mul_i32 s22,s22,160\n"
MF"v[204:207],a[184:187],a[108:111],v[204:207]\n"
WT"vmcnt(37) lgkmcnt(4)\n"
SA"s17,s21,s22\n"
MF"v[80:83],a[128:131],a[192:195],v[80:83]\n"
WT"vmcnt(32)\n"
RT"a[150:151],v247 offset:41472\n"
"v_lshrrev_b32 v240,4,v224\n"
MF"v[80:83],a[132:135],a[208:211],v[80:83]\n"
WT"vmcnt(31)\n"
RT"a[152:153],v243 offset:16384\n"
"v_add_u32 v240,0,v240\n"
MF"v[88:91],a[132:135],a[212:215],v[88:91]\n"
"v_mul_u32_u24 v240,205,v240\n"
"v_lshrrev_b32 v240,11,v240\n"
MF"v[88:91],a[128:131],a[196:199],v[88:91]\n"
RT"a[154:155],v243 offset:16896\n"
"v_mul_u32_u24 v240,1120,v240\n"
MF"v[96:99],a[128:131],a[200:203],v[96:99]\n"
WT"vmcnt(30)\n"
RT"a[156:157],v243 offset:40960\n"
"v_add_u32 v240,v240,v224\n"
MF"v[96:99],a[132:135],a[216:219],v[96:99]\n"
WT"vmcnt(28)\n"
"v_lshrrev_b32 v241,4,v224\n"
"v_add_u32 v241,64,v241\n"
MF"v[104:107],a[132:135],a[220:223],v[104:107]\n"
RT"a[158:159],v243 offset:41472\n"
"v_mul_u32_u24 v241,205,v241\n"
MF"v[104:107],a[128:131],a[204:207],v[104:107]\n"
WT"vmcnt(26)\n"
"v_lshrrev_b32 v241,11,v241\n"
"v_mul_u32_u24 v241,1120,v241\n"
MF"v[80:83],a[136:139],a[224:227],v[80:83]\n"
WT"vmcnt(17) lgkmcnt(3)\n"
RT"a[160:161],v248 offset:16384\n"
"v_add_u32 v241,v241,v224\n"
MF"v[80:83],a[140:143],a[240:243],v[80:83]\n"
WT"vmcnt(16)\n"
"v_lshrrev_b32 v251,4,v224\n"
"v_add_u32 v251,128,v251\n"
MF"v[88:91],a[140:143],a[244:247],v[88:91]\n"
RT"a[162:163],v248 offset:16896\n"
"v_mul_u32_u24 v251,205,v251\n"
MF"v[88:91],a[136:139],a[228:231],v[88:91]\n"
RT"a[164:165],v248 offset:40960\n"
"v_lshrrev_b32 v251,11,v251\n"
MF"v[96:99],a[136:139],a[232:235],v[96:99]\n"
WT"vmcnt(14)\n"
"v_mul_u32_u24 v251,1120,v251\n"
"v_add_u32 v251,v251,v224\n"
MF"v[96:99],a[140:143],a[248:251],v[96:99]\n"
WT"vmcnt(12)\n"
RT"a[166:167],v248 offset:41472\n"
"v_and_b32 v237,512,v224\n"
MF"v[104:107],a[140:143],a[252:255],v[104:107]\n"
RT"a[168:169],v244 offset:16384\n"
"v_lshl_add_u32 v251,v237,20,v251\n"
MF"v[104:107],a[136:139],a[236:239],v[104:107]\n"
"v_lshrrev_b32 v252,6,v224\n"
"v_mul_u32_u24 v252,160,v252\n"
MF"v[84:87],a[144:147],a[192:195],v[84:87]\n"
RT"a[170:171],v244 offset:16896\n"
"v_bfe_u32 v237,v224,4,2\n"
MF"v[84:87],a[148:151],a[208:211],v[84:87]\n"
"s_cmp_eq_u32 s34,7\n"
RT"a[172:173],v244 offset:40960\n"
"v_lshl_add_u32 v252,v237,3,v252\n"
MF"v[92:95],a[148:151],a[212:215],v[92:95]\n"
"s_cselect_b32 s57,3,s57\n"
"v_lshrrev_b32 v254,4,v224\n"
"v_min_u32 v254,49,v254\n"
MF"v[92:95],a[144:147],a[196:199],v[92:95]\n"
RT"a[174:175],v244 offset:41472\n"
"v_mul_u32_u24 v253,52,v254\n"
MF"v[100:103],a[144:147],a[200:203],v[100:103]\n"
"v_lshrrev_b32 v253,8,v253\n"
MF"v[100:103],a[148:151],a[216:219],v[100:103]\n"
RT"a[176:177],v249 offset:16384\n"
"v_mul_u32_u24 v253,2901,v253\n"
MF"v[108:111],a[148:151],a[220:223],v[108:111]\n"
"v_mul_u32_u24 v254,111,v254\n"
"v_add_u32 v254,v254,v253\n"
MF"v[108:111],a[144:147],a[204:207],v[108:111]\n"
WT"lgkmcnt(3)\n"
RT"a[178:179],v249 offset:16896\n"
"v_and_b32 v254,-4,v254\n"
MF"v[84:87],a[152:155],a[224:227],v[84:87]\n"
RT"a[180:181],v249 offset:40960\n"
"v_lshrrev_b32 v253,5,v224\n"
MF"v[84:87],a[156:159],a[240:243],v[84:87]\n"
BS"v[76:79],v250,s[8:11],s49 offen offset:1024 sc1\n"
MF"v[92:95],a[156:159],a[244:247],v[92:95]\n"
RT"a[182:183],v249 offset:41472\n"
"v_min_u32 v253,13,v253\n"
MF"v[92:95],a[152:155],a[228:231],v[92:95]\n"
RT"a[184:185],v245 offset:16384\n"
"v_mul_u32_u24 v253,1280,v253\n"
MF"v[100:103],a[152:155],a[232:235],v[100:103]\n"
"v_bfe_u32 v237,v224,4,1\n"
"v_mul_u32_u24 v237,156,v237\n"
MF"v[100:103],a[156:159],a[248:251],v[100:103]\n"
"s_cmp_lt_u32 s30,2\n"
RT"a[186:187],v245 offset:16896\n"
"v_add_u32 v253,v253,v237\n"
MF"v[108:111],a[156:159],a[252:255],v[108:111]\n"
"s_cselect_b64 s[12:13],s[78:79],s[80:81]\n"
RT"a[188:189],v245 offset:40960\n"
"s_and_b32 s13,s13,0xffff\n"
MF"v[108:111],a[152:155],a[236:239],v[108:111]\n"
"s_mov_b32 s14,2211840\n"
"s_mov_b32 s15,0x20000\n"
MF"v[112:115],a[160:163],a[192:195],v[112:115]\n"
"s_lshr_b32 s90,s31,3\n"
RT"a[190:191],v245 offset:41472\n"
MF"v[112:115],a[164:167],a[208:211],v[112:115]\n"
"s_lshl_b32 s91,s90,1\n"
MF"v[120:123],a[164:167],a[212:215],v[120:123]\n"
"s_and_b32 s50,s30,1\n"
RT"a[128:129],v246 offset:20480\n"
PK"v80,v80,v81\n"
MF"v[120:123],a[160:163],a[196:199],v[120:123]\n"
SA"s91,s91,s50\n"
PK"v81,v82,v83\n"
PK"v82,v84,v85\n"
MF"v[128:131],a[160:163],a[200:203],v[128:131]\n"
"s_mul_i32 s91,s91,34560\n"
RT"a[130:131],v246 offset:20992\n"
PK"v83,v86,v87\n"
MF"v[128:131],a[164:167],a[216:219],v[128:131]\n"
"s_mul_i32 s50,s32,448\n"
RT"a[132:133],v246 offset:45056\n"
"v_accvgpr_write_b32 a0,v80\n"
MF"v[136:139],a[164:167],a[220:223],v[136:139]\n"
SA"s91,s91,s50\n"
"v_accvgpr_write_b32 a1,v81\n"
"v_accvgpr_write_b32 a2,v82\n"
MF"v[136:139],a[160:163],a[204:207],v[136:139]\n"
"buffer_load_dword v255,v254,s[12:15],s91 offen\n"
MF"v[112:115],a[168:171],a[224:227],v[112:115]\n"
WT"lgkmcnt(3)\n"
RT"a[134:135],v246 offset:45568\n"
RT"a[136:137],v242 offset:20480\n"
MF"v[112:115],a[172:175],a[240:243],v[112:115]\n"
"s_mov_b32 s12,s82\n"
"v_accvgpr_write_b32 a3,v83\n"
PK"v88,v88,v89\n"
MF"v[120:123],a[172:175],a[244:247],v[120:123]\n"
"s_and_b32 s13,s83,0xffff\n"
RT"a[138:139],v242 offset:20992\n"
PK"v89,v90,v91\n"
MF"v[120:123],a[168:171],a[228:231],v[120:123]\n"
"s_lshl_b32 s90,s90,2\n"
RT"a[140:141],v242 offset:45056\n"
PK"v90,v92,v93\n"
MF"v[128:131],a[168:171],a[232:235],v[128:131]\n"
SA"s90,s90,s30\n"
PK"v91,v94,v95\n"
"v_accvgpr_write_b32 a16,v88\n"
MF"v[128:131],a[172:175],a[248:251],v[128:131]\n"
"s_mul_i32 s90,s90,17920\n"
RT"a[142:143],v242 offset:45568\n"
"v_accvgpr_write_b32 a17,v89\n"
MF"v[136:139],a[172:175],a[252:255],v[136:139]\n"
"s_mul_i32 s50,s32,160\n"
"v_accvgpr_write_b32 a18,v90\n"
"v_accvgpr_write_b32 a19,v91\n"
MF"v[136:139],a[168:171],a[236:239],v[136:139]\n"
SA"s90,s90,s50\n"
RT"a[144:145],v247 offset:20480\n"
PK"v96,v96,v97\n"
MF"v[116:119],a[176:179],a[192:195],v[116:119]\n"
"buffer_load_dword v255,v253,s[12:15],s90 offen\n"
MF"v[116:119],a[180:183],a[208:211],v[116:119]\n"
RT"a[146:147],v247 offset:20992\n"
"v_min_u32 v253,624,v224\n"
MF"v[124:127],a[180:183],a[212:215],v[124:127]\n"
RT"a[148:149],v247 offset:45056\n"
"v_lshlrev_b32 v253,3,v253\n"
MF"v[124:127],a[176:179],a[196:199],v[124:127]\n"
PK"v97,v98,v99\n"
PK"v98,v100,v101\n"
MF"v[132:135],a[176:179],a[200:203],v[132:135]\n"
RT"a[150:151],v247 offset:45568\n"
PK"v99,v102,v103\n"
MF"v[132:135],a[180:183],a[216:219],v[132:135]\n"
RT"a[152:153],v243 offset:20480\n"
"v_accvgpr_write_b32 a32,v96\n"
MF"v[140:143],a[180:183],a[220:223],v[140:143]\n"
"v_accvgpr_write_b32 a33,v97\n"
"v_accvgpr_write_b32 a34,v98\n"
MF"v[140:143],a[176:179],a[204:207],v[140:143]\n"
RT"a[154:155],v243 offset:20992\n"
"v_accvgpr_write_b32 a35,v99\n"
MF"v[116:119],a[184:187],a[224:227],v[116:119]\n"
RT"a[156:157],v243 offset:45056\n"
PK"v104,v104,v105\n"
MF"v[116:119],a[188:191],a[240:243],v[116:119]\n"
PK"v105,v106,v107\n"
PK"v106,v108,v109\n"
MF"v[124:127],a[188:191],a[244:247],v[124:127]\n"
RT"a[158:159],v243 offset:45568\n"
PK"v107,v110,v111\n"
MF"v[124:127],a[184:187],a[228:231],v[124:127]\n"
"v_accvgpr_write_b32 a48,v104\n"
"v_accvgpr_write_b32 a49,v105\n"
MF"v[132:135],a[184:187],a[232:235],v[132:135]\n"
RT"a[160:161],v248 offset:20480\n"
"v_accvgpr_write_b32 a50,v106\n"
MF"v[132:135],a[188:191],a[248:251],v[132:135]\n"
"v_accvgpr_write_b32 a51,v107\n"
MF"v[140:143],a[188:191],a[252:255],v[140:143]\n"
RT"a[162:163],v248 offset:20992\n"
MF"v[140:143],a[184:187],a[236:239],v[140:143]\n"
WT"lgkmcnt(3)\n"
RT"a[164:165],v248 offset:45056\n"
MF"v[144:147],a[128:131],a[192:195],v[144:147]\n"
MF"v[144:147],a[132:135],a[208:211],v[144:147]\n"
RT"a[166:167],v248 offset:45568\n"
MF"v[152:155],a[132:135],a[212:215],v[152:155]\n"
RT"a[168:169],v244 offset:20480\n"
PK"v112,v112,v113\n"
MF"v[152:155],a[128:131],a[196:199],v[152:155]\n"
PK"v113,v114,v115\n"
PK"v114,v116,v117\n"
MF"v[160:163],a[128:131],a[200:203],v[160:163]\n"
RT"a[170:171],v244 offset:20992\n"
PK"v115,v118,v119\n"
MF"v[160:163],a[132:135],a[216:219],v[160:163]\n"
RT"a[172:173],v244 offset:45056\n"
"v_accvgpr_write_b32 a4,v112\n"
MF"v[168:171],a[132:135],a[220:223],v[168:171]\n"
"v_accvgpr_write_b32 a5,v113\n"
"v_accvgpr_write_b32 a6,v114\n"
MF"v[168:171],a[128:131],a[204:207],v[168:171]\n"
RT"a[174:175],v244 offset:45568\n"
"v_accvgpr_write_b32 a7,v115\n"
MF"v[144:147],a[136:139],a[224:227],v[144:147]\n"
PK"v120,v120,v121\n"
PK"v121,v122,v123\n"
MF"v[144:147],a[140:143],a[240:243],v[144:147]\n"
RT"a[176:177],v249 offset:20480\n"
PK"v122,v124,v125\n"
MF"v[152:155],a[140:143],a[244:247],v[152:155]\n"
PK"v123,v126,v127\n"
"v_accvgpr_write_b32 a20,v120\n"
MF"v[152:155],a[136:139],a[228:231],v[152:155]\n"
RT"a[178:179],v249 offset:20992\n"
"v_accvgpr_write_b32 a21,v121\n"
MF"v[160:163],a[136:139],a[232:235],v[160:163]\n"
RT"a[180:181],v249 offset:45056\n"
"v_accvgpr_write_b32 a22,v122\n"
MF"v[160:163],a[140:143],a[248:251],v[160:163]\n"
"v_accvgpr_write_b32 a23,v123\n"
MF"v[168:171],a[140:143],a[252:255],v[168:171]\n"
RT"a[182:183],v249 offset:45568\n"
PK"v128,v128,v129\n"
MF"v[168:171],a[136:139],a[236:239],v[168:171]\n"
RT"a[184:185],v245 offset:20480\n"
PK"v129,v130,v131\n"
MF"v[148:151],a[144:147],a[192:195],v[148:151]\n"
PK"v130,v132,v133\n"
PK"v131,v134,v135\n"
MF"v[148:151],a[148:151],a[208:211],v[148:151]\n"
RT"a[186:187],v245 offset:20992\n"
"v_accvgpr_write_b32 a36,v128\n"
MF"v[156:159],a[148:151],a[212:215],v[156:159]\n"
RT"a[188:189],v245 offset:45056\n"
"v_accvgpr_write_b32 a37,v129\n"
MF"v[156:159],a[144:147],a[196:199],v[156:159]\n"
"v_accvgpr_write_b32 a38,v130\n"
"v_accvgpr_write_b32 a39,v131\n"
MF"v[164:167],a[144:147],a[200:203],v[164:167]\n"
RT"a[190:191],v245 offset:45568\n"
PK"v136,v136,v137\n"
MF"v[164:167],a[148:151],a[216:219],v[164:167]\n"
PK"v137,v138,v139\n"
PK"v138,v140,v141\n"
MF"v[172:175],a[148:151],a[220:223],v[172:175]\n"
PK"v139,v142,v143\n"
"v_accvgpr_write_b32 a52,v136\n"
MF"v[172:175],a[144:147],a[204:207],v[172:175]\n"
"v_accvgpr_write_b32 a53,v137\n"
"v_accvgpr_write_b32 a54,v138\n"
MF"v[148:151],a[152:155],a[224:227],v[148:151]\n"
WT"lgkmcnt(2)\n"
"v_accvgpr_write_b32 a55,v139\n"
MF"v[148:151],a[156:159],a[240:243],v[148:151]\n"
MF"v[156:159],a[156:159],a[244:247],v[156:159]\n"
MF"v[156:159],a[152:155],a[228:231],v[156:159]\n"
MF"v[164:167],a[152:155],a[232:235],v[164:167]\n"
MF"v[164:167],a[156:159],a[248:251],v[164:167]\n"
MF"v[172:175],a[156:159],a[252:255],v[172:175]\n"
MF"v[172:175],a[152:155],a[236:239],v[172:175]\n"
MF"v[176:179],a[160:163],a[192:195],v[176:179]\n"
MF"v[176:179],a[164:167],a[208:211],v[176:179]\n"
MF"v[176:179],a[168:171],a[224:227],v[176:179]\n"
PK"v144,v144,v145\n"
PK"v145,v146,v147\n"
MF"v[176:179],a[172:175],a[240:243],v[176:179]\n"
PK"v146,v148,v149\n"
PK"v147,v150,v151\n"
MF"v[180:183],a[176:179],a[192:195],v[180:183]\n"
"v_mov_b32 v254,0x3f803f80\n"
"v_accvgpr_write_b32 a8,v144\n"
MF"v[180:183],a[180:183],a[208:211],v[180:183]\n"
"v_accvgpr_write_b32 a9,v145\n"
"v_accvgpr_write_b32 a128,v254\n"
MF"v[180:183],a[184:187],a[224:227],v[180:183]\n"
WT"lgkmcnt(0)\n"
"v_accvgpr_write_b32 a129,v254\n"
"v_accvgpr_write_b32 a130,v254\n"
MF"v[180:183],a[188:191],a[240:243],v[180:183]\n"
"v_accvgpr_write_b32 a131,v254\n"
"v_accvgpr_write_b32 a10,v146\n"
MF"v[184:187],a[160:163],a[196:199],v[184:187]\n"
"v_accvgpr_write_b32 a11,v147\n"
PK"v152,v152,v153\n"
MF"v[184:187],a[164:167],a[212:215],v[184:187]\n"
PK"v153,v154,v155\n"
PK"v154,v156,v157\n"
MF"v[184:187],a[168:171],a[228:231],v[184:187]\n"
PK"v155,v158,v159\n"
"v_accvgpr_write_b32 a24,v152\n"
MF"v[184:187],a[172:175],a[244:247],v[184:187]\n"
"v_accvgpr_write_b32 a25,v153\n"
PK"v176,v176,v177\n"
MF"v[188:191],a[176:179],a[196:199],v[188:191]\n"
PK"v177,v178,v179\n"
"v_accvgpr_write_b32 a26,v154\n"
MF"v[188:191],a[180:183],a[212:215],v[188:191]\n"
"v_accvgpr_write_b32 a27,v155\n"
PK"v178,v180,v181\n"
MF"v[188:191],a[184:187],a[228:231],v[188:191]\n"
PK"v179,v182,v183\n"
"v_accvgpr_write_b32 a12,v176\n"
MF"v[188:191],a[188:191],a[244:247],v[188:191]\n"
"v_accvgpr_write_b32 a13,v177\n"
"v_accvgpr_write_b32 a14,v178\n"
MF"v[192:195],a[160:163],a[200:203],v[192:195]\n"
"v_accvgpr_write_b32 a15,v179\n"
PK"v160,v160,v161\n"
MF"v[192:195],a[164:167],a[216:219],v[192:195]\n"
WT"vmcnt(14)\n"
"s_barrier\n"
MF"v[192:195],a[168:171],a[232:235],v[192:195]\n"
RD"a[64:67],v226 offset:0\n"
PK"v161,v162,v163\n"
MF"v[192:195],a[172:175],a[248:251],v[192:195]\n"
PK"v162,v164,v165\n"
PK"v163,v166,v167\n"
MF"v[196:199],a[176:179],a[200:203],v[196:199]\n"
RD"a[68:71],v226 offset:1024\n"
"v_accvgpr_write_b32 a40,v160\n"
MF"v[196:199],a[180:183],a[216:219],v[196:199]\n"
"v_accvgpr_write_b32 a41,v161\n"
"v_accvgpr_write_b32 a42,v162\n"
MF"v[196:199],a[184:187],a[232:235],v[196:199]\n"
RD"a[72:75],v226 offset:2048\n"
"v_accvgpr_write_b32 a43,v163\n"
MF"v[196:199],a[188:191],a[248:251],v[196:199]\n"
PK"v184,v184,v185\n"
PK"v185,v186,v187\n"
MF"v[200:203],a[160:163],a[204:207],v[200:203]\n"
RD"a[76:79],v226 offset:3072\n"
PK"v186,v188,v189\n"
MF"v[200:203],a[164:167],a[220:223],v[200:203]\n"
PK"v187,v190,v191\n"
PK"v168,v168,v169\n"
MF"v[200:203],a[168:171],a[236:239],v[200:203]\n"
RD"a[80:83],v226 offset:4096\n"
PK"v169,v170,v171\n"
MF"v[200:203],a[172:175],a[252:255],v[200:203]\n"
"v_accvgpr_write_b32 a28,v184\n"
"v_accvgpr_write_b32 a29,v185\n"
MF"v[204:207],a[176:179],a[204:207],v[204:207]\n"
RD"a[84:87],v226 offset:5120\n"
PK"v170,v172,v173\n"
MF"v[204:207],a[180:183],a[220:223],v[204:207]\n"
PK"v171,v174,v175\n"
"v_accvgpr_write_b32 a30,v186\n"
MF"v[204:207],a[184:187],a[236:239],v[204:207]\n"
"v_accvgpr_write_b32 a31,v187\n"
"v_accvgpr_write_b32 a56,v168\n"
MF"v[204:207],a[188:191],a[252:255],v[204:207]\n"
"v_accvgpr_write_b32 a57,v169\n"
"v_accvgpr_write_b32 a58,v170\n"
"v_accvgpr_write_b32 a59,v171\n"
RD"a[88:91],v226 offset:6144\n"
"s_cmp_eq_u32 s34,0\n"
"s_cbranch_scc1 LFA_%=\n"
WT"lgkmcnt(6)\n"
MF"v[0:3],a[64:67],a[0:3],0\n"
WT"lgkmcnt(3)\n"
PK"v192,v192,v193\n"
PK"v193,v194,v195\n"
MF"v[0:3],a[68:71],a[4:7],v[0:3]\n"
"s_mov_b32 m0,s40\n"
"v_readlane_b32 s43,v228,s42\n"
PK"v194,v196,v197\n"
MF"v[8:11],a[68:71],a[20:23],0\n"
PK"v195,v198,v199\n"
"v_readlane_b32 s44,v229,s42\n"
MF"v[8:11],a[64:67],a[16:19],v[8:11]\n"
SA"s42,s42,1\n"
BL"v225,s[8:11],s43 offen offset:0 sc1 lds\n"
MF"v[16:19],a[64:67],a[32:35],0\n"
"v_accvgpr_write_b32 a44,v192\n"
"v_accvgpr_write_b32 a45,v193\n"
MF"v[16:19],a[68:71],a[36:39],v[16:19]\n"
RD"a[92:95],v226 offset:7168\n"
"v_accvgpr_write_b32 a46,v194\n"
MF"v[24:27],a[68:71],a[52:55],0\n"
"v_accvgpr_write_b32 a47,v195\n"
PK"v200,v200,v201\n"
MF"v[24:27],a[64:67],a[48:51],v[24:27]\n"
PK"v201,v202,v203\n"
PK"v202,v204,v205\n"
"v_mov_b32 v227,v226\n"
MF"v[0:3],a[72:75],a[8:11],v[0:3]\n"
BL"v225,s[8:11],s43 offen offset:1024 sc1 lds\n"
PK"v203,v206,v207\n"
MF"v[0:3],a[76:79],a[12:15],v[0:3]\n"
"v_accvgpr_write_b32 a60,v200\n"
"v_accvgpr_write_b32 a61,v201\n"
MF"v[8:11],a[76:79],a[28:31],v[8:11]\n"
"s_mul_i32 s48,s32,655360\n"
"v_accvgpr_write_b32 a62,v202\n"
"v_accvgpr_write_b32 a63,v203\n"
MF"v[8:11],a[72:75],a[24:27],v[8:11]\n"
SA"s12,s74,s48\n"
"s_addc_u32 s13,s75,0\n"
MF"v[16:19],a[72:75],a[40:43],v[16:19]\n"
"s_and_b32 s13,s13,0xffff\n"
MF"v[16:19],a[76:79],a[44:47],v[16:19]\n"
"s_mov_b32 s14,655360\n"
RD"a[96:99],v226 offset:8192\n"
MF"v[24:27],a[76:79],a[60:63],v[24:27]\n"
"s_lshl_b32 s48,s33,3\n"
BL"v225,s[8:11],s43 offen offset:2048 sc1 lds\n"
MF"v[24:27],a[72:75],a[56:59],v[24:27]\n"
WT"lgkmcnt(1)\n"
EX"v0,v0\n"
MF"v[4:7],a[80:83],a[0:3],0\n"
"s_and_b32 s49,s34,2\n"
EX"v1,v1\n"
MF"v[4:7],a[84:87],a[4:7],v[4:7]\n"
"s_lshl_b32 s49,s49,1\n"
EX"v2,v2\n"
MF"v[12:15],a[84:87],a[20:23],0\n"
SA"s48,s48,s49\n"
RD"a[100:103],v226 offset:9216\n"
PK"v0,v0,v1\n"
MF"v[12:15],a[80:83],a[16:19],v[12:15]\n"
SA"s48,s48,s30\n"
EX"v3,v3\n"
MF"v[20:23],a[80:83],a[32:35],0\n"
"s_mul_i32 s48,s48,5120\n"
EX"v8,v8\n"
MF"v[20:23],a[84:87],a[36:39],v[20:23]\n"
"s_or_b32 s49,s34,2\n"
BL"v225,s[8:11],s43 offen offset:3072 sc1 lds\n"
MF"v[28:31],a[84:87],a[52:55],0\n"
"s_cmp_eq_u32 s49,7\n"
EX"v9,v9\n"
PK"v1,v2,v3\n"
MF"v[28:31],a[80:83],a[48:51],v[28:31]\n"
"s_cselect_b32 s55,s48,2130706432\n"
RD"a[104:107],v226 offset:10240\n"
PK"v8,v8,v9\n"
MF"v[4:7],a[88:91],a[8:11],v[4:7]\n"
SA"m0,s40,16384\n"
EX"v10,v10\n"
MF"v[4:7],a[92:95],a[12:15],v[4:7]\n"
EX"v11,v11\n"
MF"v[12:15],a[92:95],a[28:31],v[12:15]\n"
EX"v16,v16\n"
MF"v[12:15],a[88:91],a[24:27],v[12:15]\n"
SA"s40,s40,32768\n"
RD"a[108:111],v226 offset:11264\n"
PK"v9,v10,v11\n"
MF"v[20:23],a[88:91],a[40:43],v[20:23]\n"
"s_cmp_eq_u32 s40,s46\n"
BL"v225,s[8:11],s44 offen offset:0 sc1 lds\n"
EX"v17,v17\n"
MF"v[20:23],a[92:95],a[44:47],v[20:23]\n"
"s_cselect_b32 s40,s47,s40\n"
EX"v18,v18\n"
MF"v[28:31],a[92:95],a[60:63],v[28:31]\n"
EX"v19,v19\n"
PK"v16,v16,v17\n"
MF"v[28:31],a[88:91],a[56:59],v[28:31]\n"
WT"lgkmcnt(1)\n"
RD"a[112:115],v226 offset:12288\n"
PK"v17,v18,v19\n"
MF"v[32:35],a[96:99],a[0:3],0\n"
EX"v24,v24\n"
MF"v[32:35],a[100:103],a[4:7],v[32:35]\n"
EX"v25,v25\n"
MF"v[40:43],a[100:103],a[20:23],0\n"
BL"v225,s[8:11],s44 offen offset:1024 sc1 lds\n"
EX"v26,v26\n"
MF"v[40:43],a[96:99],a[16:19],v[40:43]\n"
RD"a[116:119],v226 offset:13312\n"
PK"v24,v24,v25\n"
MF"v[48:51],a[96:99],a[32:35],0\n"
EX"v27,v27\n"
MF"v[48:51],a[100:103],a[36:39],v[48:51]\n"
EX"v4,v4\n"
MF"v[56:59],a[100:103],a[52:55],0\n"
EX"v5,v5\n"
PK"v25,v26,v27\n"
MF"v[56:59],a[96:99],a[48:51],v[56:59]\n"
RD"a[120:123],v226 offset:14336\n"
PK"v2,v4,v5\n"
MF"v[32:35],a[104:107],a[8:11],v[32:35]\n"
WT"lgkmcnt(2)\n"
BL"v225,s[8:11],s44 offen offset:2048 sc1 lds\n"
EX"v6,v6\n"
MF"v[32:35],a[108:111],a[12:15],v[32:35]\n"
EX"v7,v7\n"
MF"v[40:43],a[108:111],a[28:31],v[40:43]\n"
EX"v12,v12\n"
MF"v[40:43],a[104:107],a[24:27],v[40:43]\n"
RD"a[124:127],v226 offset:15360\n"
"v_readlane_b32 s43,v228,s42\n"
PK"v3,v6,v7\n"
MF"v[48:51],a[104:107],a[40:43],v[48:51]\n"
EX"v13,v13\n"
MF"v[48:51],a[108:111],a[44:47],v[48:51]\n"
EX"v14,v14\n"
MF"v[56:59],a[108:111],a[60:63],v[56:59]\n"
BL"v225,s[8:11],s44 offen offset:3072 sc1 lds\n"
EX"v15,v15\n"
MF"v[56:59],a[104:107],a[56:59],v[56:59]\n"
"s_mov_b32 m0,s40\n"
RD"a[96:99],v227 offset:24576\n"
PK"v10,v12,v13\n"
MF"v[36:39],a[112:115],a[0:3],0\n"
WT"lgkmcnt(2)\n"
EX"v20,v20\n"
PK"v11,v14,v15\n"
MF"v[36:39],a[116:119],a[4:7],v[36:39]\n"
EX"v21,v21\n"
MF"v[44:47],a[116:119],a[20:23],0\n"
EX"v22,v22\n"
MF"v[44:47],a[112:115],a[16:19],v[44:47]\n"
RD"a[100:103],v227 offset:25600\n"
"v_readlane_b32 s44,v229,s42\n"
PK"v18,v20,v21\n"
MF"v[52:55],a[112:115],a[32:35],0\n"
SA"s42,s42,1\n"
BL"v225,s[8:11],s43 offen offset:0 sc1 lds\n"
MF"v[52:55],a[116:119],a[36:39],v[52:55]\n"
EX"v23,v23\n"
MF"v[60:63],a[116:119],a[52:55],0\n"
EX"v28,v28\n"
MF"v[60:63],a[112:115],a[48:51],v[60:63]\n"
RD"a[104:107],v227 offset:26624\n"
PK"v19,v22,v23\n"
MF"v[36:39],a[120:123],a[8:11],v[36:39]\n"
WT"lgkmcnt(2)\n"
EX"v29,v29\n"
MF"v[36:39],a[124:127],a[12:15],v[36:39]\n"
BL"v225,s[8:11],s43 offen offset:1024 sc1 lds\n"
MF"v[44:47],a[124:127],a[28:31],v[44:47]\n"
EX"v30,v30\n"
PK"v26,v28,v29\n"
MF"v[44:47],a[120:123],a[24:27],v[44:47]\n"
RD"a[108:111],v227 offset:27648\n"
MF"v[52:55],a[120:123],a[40:43],v[52:55]\n"
EX"v31,v31\n"
MF"v[52:55],a[124:127],a[44:47],v[52:55]\n"
EX"v32,v32\n"
MF"v[60:63],a[124:127],a[60:63],v[60:63]\n"
EX"v33,v33\n"
PK"v27,v30,v31\n"
MF"v[60:63],a[120:123],a[56:59],v[60:63]\n"
SA"s41,s41,32768\n"
BL"v225,s[8:11],s43 offen offset:2048 sc1 lds\n"
RD"a[112:115],v227 offset:28672\n"
MF"v[80:83],a[96:99],v[0:3],0\n"
"s_cmp_eq_u32 s41,163840\n"
EX"v34,v34\n"
MF"v[112:115],a[96:99],v[8:11],0\n"
"s_cselect_b32 s41,0,s41\n"
EX"v35,v35\n"
MF"v[144:147],a[96:99],v[16:19],0\n"
EX"v40,v40\n"
"v_add_u32 v226,s41,v224\n"
MF"v[176:179],a[96:99],v[24:27],0\n"
WT"lgkmcnt(1)\n"
RD"a[116:119],v227 offset:29696\n"
MF"v[84:87],a[100:103],v[0:3],0\n"
EX"v41,v41\n"
MF"v[116:119],a[100:103],v[8:11],0\n"
BL"v225,s[8:11],s43 offen offset:3072 sc1 lds\n"
MF"v[148:151],a[100:103],v[16:19],0\n"
SA"m0,s40,16384\n"
EX"v42,v42\n"
MF"v[180:183],a[100:103],v[24:27],0\n"
RD"a[120:123],v227 offset:30720\n"
MF"v[88:91],a[104:107],v[0:3],0\n"
EX"v43,v43\n"
MF"v[120:123],a[104:107],v[8:11],0\n"
EX"v48,v48\n"
MF"v[152:155],a[104:107],v[16:19],0\n"
SA"s40,s40,32768\n"
EX"v49,v49\n"
MF"v[184:187],a[104:107],v[24:27],0\n"
"s_cmp_eq_u32 s40,s46\n"
BL"v225,s[8:11],s44 offen offset:0 sc1 lds\n"
RD"a[124:127],v227 offset:31744\n"
MF"v[92:95],a[108:111],v[0:3],0\n"
"s_cselect_b32 s40,s47,s40\n"
EX"v50,v50\n"
MF"v[124:127],a[108:111],v[8:11],0\n"
EX"v51,v51\n"
MF"v[156:159],a[108:111],v[16:19],0\n"
EX"v56,v56\n"
MF"v[188:191],a[108:111],v[24:27],0\n"
WT"lgkmcnt(1)\n"
EX"v57,v57\n"
MF"v[96:99],a[112:115],v[0:3],0\n"
EX"v58,v58\n"
MF"v[128:131],a[112:115],v[8:11],0\n"
BL"v225,s[8:11],s44 offen offset:1024 sc1 lds\n"
MF"v[160:163],a[112:115],v[16:19],0\n"
EX"v59,v59\n"
MF"v[192:195],a[112:115],v[24:27],0\n"
EX"v36,v36\n"
MF"v[100:103],a[116:119],v[0:3],0\n"
WT"vmcnt(16) lgkmcnt(0)\n"
"s_barrier\n"
MF"v[132:135],a[116:119],v[8:11],0\n"
RD"a[64:67],v226 offset:0\n"
RD"a[68:71],v226 offset:1024\n"
"buffer_store_dwordx2 v[232:233],v230,s[8:11],s98 offen sc1\n"
MF"v[164:167],a[116:119],v[16:19],0\n"
EX"v37,v37\n"
MF"v[196:199],a[116:119],v[24:27],0\n"
BL"v225,s[8:11],s44 offen offset:2048 sc1 lds\n"
MF"v[104:107],a[120:123],v[0:3],0\n"
EX"v38,v38\n"
MF"v[136:139],a[120:123],v[8:11],0\n"
EX"v39,v39\n"
MF"v[168:171],a[120:123],v[16:19],0\n"
EX"v44,v44\n"
MF"v[200:203],a[120:123],v[24:27],0\n"
RD"a[72:75],v226 offset:2048\n"
RD"a[76:79],v226 offset:3072\n"
MF"v[108:111],a[124:127],v[0:3],0\n"
EX"v45,v45\n"
MF"v[140:143],a[124:127],v[8:11],0\n"
BL"v225,s[8:11],s44 offen offset:3072 sc1 lds\n"
MF"v[172:175],a[124:127],v[16:19],0\n"
EX"v46,v46\n"
MF"v[204:207],a[124:127],v[24:27],0\n"
RD"a[80:83],v226 offset:4096\n"
MF"a[132:135],a[128:131],v[0:3],0\n"
EX"v47,v47\n"
MF"a[136:139],a[128:131],v[8:11],0\n"
"buffer_load_dword v255,v253,s[12:15],s55 offen\n"
EX"v52,v52\n"
MF"a[140:143],a[128:131],v[16:19],0\n"
SA"s55,s55,163840\n"
EX"v53,v53\n"
MF"a[144:147],a[128:131],v[24:27],0\n"
RD"a[84:87],v226 offset:5120\n"
RD"a[88:91],v226 offset:6144\n"
EX"v54,v54\n"
EX"v55,v55\n"
EX"v60,v60\n"
EX"v61,v61\n"
EX"v62,v62\n"
EX"v63,v63\n"
"s_cmp_eq_u32 s57,1\n"
"s_cbranch_scc1 LEV_%=\n"
".p2align 6\n"
"LP1_%=:\n"
WT"lgkmcnt(6)\n"
MF"v[0:3],a[64:67],a[0:3],0\n"
WT"lgkmcnt(5)\n"
"v_readlane_b32 s43,v228,s42\n"
PK"v64,v32,v33\n"
MF"v[0:3],a[68:71],a[4:7],v[0:3]\n"
"s_mov_b32 m0,s40\n"
"v_readlane_b32 s44,v229,s42\n"
PK"v65,v34,v35\n"
MF"v[8:11],a[68:71],a[20:23],0\n"
PK"v66,v36,v37\n"
PK"v67,v38,v39\n"
MF"v[8:11],a[64:67],a[16:19],v[8:11]\n"
SA"s42,s42,1\n"
BL"v225,s[8:11],s43 offen offset:0 sc1 lds\n"
MF"v[16:19],a[64:67],a[32:35],0\n"
PK"v68,v40,v41\n"
PK"v69,v42,v43\n"
MF"v[16:19],a[68:71],a[36:39],v[16:19]\n"
RD"a[92:95],v226 offset:7168\n"
PK"v70,v44,v45\n"
MF"v[24:27],a[68:71],a[52:55],0\n"
PK"v71,v46,v47\n"
PK"v72,v48,v49\n"
MF"v[24:27],a[64:67],a[48:51],v[24:27]\n"
WT"lgkmcnt(1)\n"
BL"v225,s[8:11],s43 offen offset:1024 sc1 lds\n"
MF"v[0:3],a[72:75],a[8:11],v[0:3]\n"
PK"v73,v50,v51\n"
"v_mov_b32 v227,v226\n"
MF"v[0:3],a[76:79],a[12:15],v[0:3]\n"
PK"v74,v52,v53\n"
PK"v75,v54,v55\n"
MF"v[8:11],a[76:79],a[28:31],v[8:11]\n"
PK"v76,v56,v57\n"
PK"v77,v58,v59\n"
MF"v[8:11],a[72:75],a[24:27],v[8:11]\n"
PK"v78,v60,v61\n"
PK"v79,v62,v63\n"
MF"v[16:19],a[72:75],a[40:43],v[16:19]\n"
MF"v[16:19],a[76:79],a[44:47],v[16:19]\n"
RD"a[96:99],v226 offset:8192\n"
MF"v[24:27],a[76:79],a[60:63],v[24:27]\n"
EX"v0,v0\n"
MF"v[24:27],a[72:75],a[56:59],v[24:27]\n"
EX"v1,v1\n"
MF"v[4:7],a[80:83],a[0:3],0\n"
EX"v2,v2\n"
MF"v[4:7],a[84:87],a[4:7],v[4:7]\n"
RD"a[100:103],v226 offset:9216\n"
PK"v0,v0,v1\n"
MF"v[12:15],a[84:87],a[20:23],0\n"
EX"v3,v3\n"
MF"v[12:15],a[80:83],a[16:19],v[12:15]\n"
EX"v8,v8\n"
MF"v[20:23],a[80:83],a[32:35],0\n"
EX"v9,v9\n"
MF"v[20:23],a[84:87],a[36:39],v[20:23]\n"
RD"a[104:107],v226 offset:10240\n"
PK"v1,v2,v3\n"
MF"v[28:31],a[84:87],a[52:55],0\n"
EX"v10,v10\n"
MF"v[28:31],a[80:83],a[48:51],v[28:31]\n"
EX"v11,v11\n"
MF"v[4:7],a[88:91],a[8:11],v[4:7]\n"
WT"lgkmcnt(1)\n"
EX"v16,v16\n"
MF"v[4:7],a[92:95],a[12:15],v[4:7]\n"
RD"a[108:111],v226 offset:11264\n"
PK"v8,v8,v9\n"
MF"v[12:15],a[92:95],a[28:31],v[12:15]\n"
EX"v17,v17\n"
MF"v[12:15],a[88:91],a[24:27],v[12:15]\n"
EX"v18,v18\n"
MF"v[20:23],a[88:91],a[40:43],v[20:23]\n"
EX"v19,v19\n"
MF"v[20:23],a[92:95],a[44:47],v[20:23]\n"
RD"a[112:115],v226 offset:12288\n"
PK"v9,v10,v11\n"
MF"v[28:31],a[92:95],a[60:63],v[28:31]\n"
EX"v24,v24\n"
MF"v[28:31],a[88:91],a[56:59],v[28:31]\n"
EX"v25,v25\n"
MF"v[32:35],a[96:99],a[0:3],0\n"
EX"v26,v26\n"
MF"v[32:35],a[100:103],a[4:7],v[32:35]\n"
RD"a[116:119],v226 offset:13312\n"
PK"v16,v16,v17\n"
MF"v[40:43],a[100:103],a[20:23],0\n"
EX"v27,v27\n"
MF"v[40:43],a[96:99],a[16:19],v[40:43]\n"
EX"v4,v4\n"
MF"v[48:51],a[96:99],a[32:35],0\n"
EX"v5,v5\n"
MF"v[48:51],a[100:103],a[36:39],v[48:51]\n"
RD"a[120:123],v226 offset:14336\n"
PK"v17,v18,v19\n"
MF"v[56:59],a[100:103],a[52:55],0\n"
EX"v6,v6\n"
MF"v[56:59],a[96:99],a[48:51],v[56:59]\n"
WT"lgkmcnt(2)\n"
EX"v7,v7\n"
MF"v[32:35],a[104:107],a[8:11],v[32:35]\n"
EX"v12,v12\n"
MF"v[32:35],a[108:111],a[12:15],v[32:35]\n"
RD"a[124:127],v226 offset:15360\n"
PK"v24,v24,v25\n"
MF"v[40:43],a[108:111],a[28:31],v[40:43]\n"
EX"v13,v13\n"
MF"v[40:43],a[104:107],a[24:27],v[40:43]\n"
EX"v14,v14\n"
MF"v[48:51],a[104:107],a[40:43],v[48:51]\n"
EX"v15,v15\n"
MF"v[48:51],a[108:111],a[44:47],v[48:51]\n"
RD"a[64:67],v227 offset:16384\n"
PK"v25,v26,v27\n"
MF"v[56:59],a[108:111],a[60:63],v[56:59]\n"
EX"v20,v20\n"
MF"v[56:59],a[104:107],a[56:59],v[56:59]\n"
EX"v21,v21\n"
MF"v[36:39],a[112:115],a[0:3],0\n"
WT"lgkmcnt(1)\n"
EX"v22,v22\n"
MF"v[36:39],a[116:119],a[4:7],v[36:39]\n"
RD"a[68:71],v227 offset:24576\n"
PK"v2,v4,v5\n"
MF"v[44:47],a[116:119],a[20:23],0\n"
EX"v23,v23\n"
MF"v[44:47],a[112:115],a[16:19],v[44:47]\n"
EX"v28,v28\n"
MF"v[52:55],a[112:115],a[32:35],0\n"
EX"v29,v29\n"
MF"v[52:55],a[116:119],a[36:39],v[52:55]\n"
RD"a[72:75],v227 offset:17408\n"
PK"v3,v6,v7\n"
MF"v[60:63],a[116:119],a[52:55],0\n"
EX"v30,v30\n"
MF"v[60:63],a[112:115],a[48:51],v[60:63]\n"
EX"v31,v31\n"
MF"v[36:39],a[120:123],a[8:11],v[36:39]\n"
PK"v10,v12,v13\n"
PK"v11,v14,v15\n"
MF"v[36:39],a[124:127],a[12:15],v[36:39]\n"
RD"a[76:79],v227 offset:25600\n"
PK"v18,v20,v21\n"
MF"v[44:47],a[124:127],a[28:31],v[44:47]\n"
PK"v19,v22,v23\n"
PK"v26,v28,v29\n"
MF"v[44:47],a[120:123],a[24:27],v[44:47]\n"
PK"v27,v30,v31\n"
MF"v[52:55],a[120:123],a[40:43],v[52:55]\n"
MF"v[52:55],a[124:127],a[44:47],v[52:55]\n"
RD"a[80:83],v227 offset:18432\n"
MF"v[60:63],a[124:127],a[60:63],v[60:63]\n"
EX"v32,v32\n"
MF"v[60:63],a[120:123],a[56:59],v[60:63]\n"
WT"lgkmcnt(2)\n"
SA"s41,s41,32768\n"
"s_cmp_eq_u32 s41,163840\n"
MF"v[80:83],a[64:67],v[64:67],v[80:83]\n"
"s_cselect_b32 s41,0,s41\n"
EX"v33,v33\n"
MF"v[80:83],a[68:71],v[0:3],v[80:83]\n"
RD"a[84:87],v227 offset:26624\n"
"v_add_u32 v226,s41,v224\n"
MF"v[112:115],a[68:71],v[8:11],v[112:115]\n"
EX"v34,v34\n"
MF"v[112:115],a[64:67],v[68:71],v[112:115]\n"
MF"v[144:147],a[64:67],v[72:75],v[144:147]\n"
EX"v35,v35\n"
MF"v[144:147],a[68:71],v[16:19],v[144:147]\n"
RD"a[88:91],v227 offset:19456\n"
MF"v[176:179],a[68:71],v[24:27],v[176:179]\n"
EX"v40,v40\n"
MF"v[176:179],a[64:67],v[76:79],v[176:179]\n"
BL"v225,s[8:11],s43 offen offset:2048 sc1 lds\n"
MF"v[84:87],a[72:75],v[64:67],v[84:87]\n"
WT"lgkmcnt(1)\n"
EX"v41,v41\n"
MF"v[84:87],a[76:79],v[0:3],v[84:87]\n"
RD"a[92:95],v227 offset:27648\n"
MF"v[116:119],a[76:79],v[8:11],v[116:119]\n"
EX"v42,v42\n"
MF"v[116:119],a[72:75],v[68:71],v[116:119]\n"
MF"v[148:151],a[72:75],v[72:75],v[148:151]\n"
EX"v43,v43\n"
MF"v[148:151],a[76:79],v[16:19],v[148:151]\n"
RD"a[96:99],v227 offset:20480\n"
MF"v[180:183],a[76:79],v[24:27],v[180:183]\n"
EX"v48,v48\n"
MF"v[180:183],a[72:75],v[76:79],v[180:183]\n"
BL"v225,s[8:11],s43 offen offset:3072 sc1 lds\n"
MF"v[88:91],a[80:83],v[64:67],v[88:91]\n"
EX"v49,v49\n"
MF"v[88:91],a[84:87],v[0:3],v[88:91]\n"
RD"a[100:103],v227 offset:28672\n"
MF"v[120:123],a[84:87],v[8:11],v[120:123]\n"
EX"v50,v50\n"
MF"v[120:123],a[80:83],v[68:71],v[120:123]\n"
SA"m0,s40,16384\n"
MF"v[152:155],a[80:83],v[72:75],v[152:155]\n"
EX"v51,v51\n"
MF"v[152:155],a[84:87],v[16:19],v[152:155]\n"
RD"a[104:107],v227 offset:21504\n"
MF"v[184:187],a[84:87],v[24:27],v[184:187]\n"
EX"v56,v56\n"
MF"v[184:187],a[80:83],v[76:79],v[184:187]\n"
WT"lgkmcnt(2)\n"
BL"v225,s[8:11],s44 offen offset:0 sc1 lds\n"
MF"v[92:95],a[88:91],v[64:67],v[92:95]\n"
SA"s40,s40,32768\n"
EX"v57,v57\n"
MF"v[92:95],a[92:95],v[0:3],v[92:95]\n"
"s_cmp_eq_u32 s40,s46\n"
RD"a[108:111],v227 offset:29696\n"
"s_cselect_b32 s40,s47,s40\n"
MF"v[124:127],a[92:95],v[8:11],v[124:127]\n"
EX"v58,v58\n"
MF"v[124:127],a[88:91],v[68:71],v[124:127]\n"
MF"v[156:159],a[88:91],v[72:75],v[156:159]\n"
EX"v59,v59\n"
MF"v[156:159],a[92:95],v[16:19],v[156:159]\n"
RD"a[112:115],v227 offset:22528\n"
MF"v[188:191],a[92:95],v[24:27],v[188:191]\n"
EX"v36,v36\n"
MF"v[188:191],a[88:91],v[76:79],v[188:191]\n"
BL"v225,s[8:11],s44 offen offset:1024 sc1 lds\n"
MF"v[96:99],a[96:99],v[64:67],v[96:99]\n"
WT"lgkmcnt(1)\n"
EX"v37,v37\n"
MF"v[96:99],a[100:103],v[0:3],v[96:99]\n"
RD"a[116:119],v227 offset:30720\n"
MF"v[128:131],a[100:103],v[8:11],v[128:131]\n"
EX"v38,v38\n"
MF"v[128:131],a[96:99],v[68:71],v[128:131]\n"
MF"v[160:163],a[96:99],v[72:75],v[160:163]\n"
EX"v39,v39\n"
MF"v[160:163],a[100:103],v[16:19],v[160:163]\n"
RD"a[120:123],v227 offset:23552\n"
MF"v[192:195],a[100:103],v[24:27],v[192:195]\n"
EX"v44,v44\n"
MF"v[192:195],a[96:99],v[76:79],v[192:195]\n"
BL"v225,s[8:11],s44 offen offset:2048 sc1 lds\n"
MF"v[100:103],a[104:107],v[64:67],v[100:103]\n"
EX"v45,v45\n"
MF"v[100:103],a[108:111],v[0:3],v[100:103]\n"
RD"a[124:127],v227 offset:31744\n"
MF"v[132:135],a[108:111],v[8:11],v[132:135]\n"
EX"v46,v46\n"
MF"v[132:135],a[104:107],v[68:71],v[132:135]\n"
MF"v[164:167],a[104:107],v[72:75],v[164:167]\n"
EX"v47,v47\n"
MF"v[164:167],a[108:111],v[16:19],v[164:167]\n"
MF"v[196:199],a[108:111],v[24:27],v[196:199]\n"
EX"v52,v52\n"
MF"v[196:199],a[104:107],v[76:79],v[196:199]\n"
WT"lgkmcnt(1)\n"
BL"v225,s[8:11],s44 offen offset:3072 sc1 lds\n"
MF"v[104:107],a[112:115],v[64:67],v[104:107]\n"
EX"v53,v53\n"
MF"v[104:107],a[116:119],v[0:3],v[104:107]\n"
"buffer_load_dword v255,v253,s[12:15],s55 offen\n"
MF"v[136:139],a[116:119],v[8:11],v[136:139]\n"
EX"v54,v54\n"
MF"v[136:139],a[112:115],v[68:71],v[136:139]\n"
SA"s55,s55,163840\n"
MF"v[168:171],a[112:115],v[72:75],v[168:171]\n"
WT"vmcnt(17) lgkmcnt(0)\n"
"s_barrier\n"
MF"v[168:171],a[116:119],v[16:19],v[168:171]\n"
RD"a[64:67],v226 offset:0\n"
MF"v[200:203],a[116:119],v[24:27],v[200:203]\n"
EX"v55,v55\n"
MF"v[200:203],a[112:115],v[76:79],v[200:203]\n"
MF"v[108:111],a[120:123],v[64:67],v[108:111]\n"
EX"v60,v60\n"
MF"v[108:111],a[124:127],v[0:3],v[108:111]\n"
RD"a[68:71],v226 offset:1024\n"
MF"v[140:143],a[124:127],v[8:11],v[140:143]\n"
EX"v61,v61\n"
MF"v[140:143],a[120:123],v[68:71],v[140:143]\n"
RD"a[72:75],v226 offset:2048\n"
MF"v[172:175],a[120:123],v[72:75],v[172:175]\n"
EX"v62,v62\n"
MF"v[172:175],a[124:127],v[16:19],v[172:175]\n"
RD"a[76:79],v226 offset:3072\n"
MF"v[204:207],a[124:127],v[24:27],v[204:207]\n"
EX"v63,v63\n"
MF"v[204:207],a[120:123],v[76:79],v[204:207]\n"
MF"a[132:135],a[128:131],v[64:67],a[132:135]\n"
MF"a[132:135],a[128:131],v[0:3],a[132:135]\n"
RD"a[80:83],v226 offset:4096\n"
MF"a[136:139],a[128:131],v[8:11],a[136:139]\n"
MF"a[136:139],a[128:131],v[68:71],a[136:139]\n"
RD"a[84:87],v226 offset:5120\n"
MF"a[140:143],a[128:131],v[72:75],a[140:143]\n"
MF"a[140:143],a[128:131],v[16:19],a[140:143]\n"
RD"a[88:91],v226 offset:6144\n"
MF"a[144:147],a[128:131],v[24:27],a[144:147]\n"
MF"a[144:147],a[128:131],v[76:79],a[144:147]\n"
"s_sub_u32 s56,s56,1\n"
"s_cbranch_scc0 LP1_%=\n"
"s_cmp_eq_u32 s57,2\n"
"s_cbranch_scc1 LEV_%=\n"
"s_cmp_eq_u32 s57,3\n"
"s_cbranch_scc1 LRT_%=\n"
"LDA_%=:\n"
WT"lgkmcnt(6)\n"
MF"v[0:3],a[64:67],a[0:3],v[208:211]\n"
WT"lgkmcnt(5)\n"
"v_readlane_b32 s43,v228,s42\n"
PK"v64,v32,v33\n"
MF"v[0:3],a[68:71],a[4:7],v[0:3]\n"
"s_mov_b32 m0,s40\n"
"v_readlane_b32 s44,v229,s42\n"
PK"v65,v34,v35\n"
MF"v[8:11],a[68:71],a[20:23],0\n"
PK"v66,v36,v37\n"
PK"v67,v38,v39\n"
MF"v[8:11],a[64:67],a[16:19],v[8:11]\n"
SA"s42,s42,1\n"
BL"v225,s[8:11],s43 offen offset:0 sc1 lds\n"
MF"v[16:19],a[64:67],a[32:35],0\n"
PK"v68,v40,v41\n"
PK"v69,v42,v43\n"
MF"v[16:19],a[68:71],a[36:39],v[16:19]\n"
RD"a[92:95],v226 offset:7168\n"
PK"v70,v44,v45\n"
MF"v[24:27],a[68:71],a[52:55],0\n"
PK"v71,v46,v47\n"
PK"v72,v48,v49\n"
MF"v[24:27],a[64:67],a[48:51],v[24:27]\n"
WT"lgkmcnt(1)\n"
BL"v225,s[8:11],s43 offen offset:1024 sc1 lds\n"
MF"v[0:3],a[72:75],a[8:11],v[0:3]\n"
PK"v73,v50,v51\n"
"v_mov_b32 v227,v226\n"
MF"v[0:3],a[76:79],a[12:15],v[0:3]\n"
PK"v74,v52,v53\n"
PK"v75,v54,v55\n"
MF"v[8:11],a[76:79],a[28:31],v[8:11]\n"
PK"v76,v56,v57\n"
PK"v77,v58,v59\n"
MF"v[8:11],a[72:75],a[24:27],v[8:11]\n"
PK"v78,v60,v61\n"
PK"v79,v62,v63\n"
MF"v[16:19],a[72:75],a[40:43],v[16:19]\n"
MF"v[16:19],a[76:79],a[44:47],v[16:19]\n"
RD"a[96:99],v226 offset:8192\n"
MF"v[24:27],a[76:79],a[60:63],v[24:27]\n"
EX"v0,v0\n"
MF"v[24:27],a[72:75],a[56:59],v[24:27]\n"
EX"v1,v1\n"
MF"v[4:7],a[80:83],a[0:3],v[212:215]\n"
EX"v2,v2\n"
MF"v[4:7],a[84:87],a[4:7],v[4:7]\n"
RD"a[100:103],v226 offset:9216\n"
PK"v0,v0,v1\n"
MF"v[12:15],a[84:87],a[20:23],0\n"
EX"v3,v3\n"
MF"v[12:15],a[80:83],a[16:19],v[12:15]\n"
EX"v8,v8\n"
MF"v[20:23],a[80:83],a[32:35],0\n"
EX"v9,v9\n"
MF"v[20:23],a[84:87],a[36:39],v[20:23]\n"
RD"a[104:107],v226 offset:10240\n"
PK"v1,v2,v3\n"
MF"v[28:31],a[84:87],a[52:55],0\n"
EX"v10,v10\n"
MF"v[28:31],a[80:83],a[48:51],v[28:31]\n"
EX"v11,v11\n"
MF"v[4:7],a[88:91],a[8:11],v[4:7]\n"
WT"lgkmcnt(1)\n"
EX"v16,v16\n"
MF"v[4:7],a[92:95],a[12:15],v[4:7]\n"
RD"a[108:111],v226 offset:11264\n"
PK"v8,v8,v9\n"
MF"v[12:15],a[92:95],a[28:31],v[12:15]\n"
EX"v17,v17\n"
MF"v[12:15],a[88:91],a[24:27],v[12:15]\n"
EX"v18,v18\n"
MF"v[20:23],a[88:91],a[40:43],v[20:23]\n"
EX"v19,v19\n"
MF"v[20:23],a[92:95],a[44:47],v[20:23]\n"
RD"a[112:115],v226 offset:12288\n"
PK"v9,v10,v11\n"
MF"v[28:31],a[92:95],a[60:63],v[28:31]\n"
EX"v24,v24\n"
MF"v[28:31],a[88:91],a[56:59],v[28:31]\n"
EX"v25,v25\n"
MF"v[32:35],a[96:99],a[0:3],v[216:219]\n"
EX"v26,v26\n"
MF"v[32:35],a[100:103],a[4:7],v[32:35]\n"
RD"a[116:119],v226 offset:13312\n"
PK"v16,v16,v17\n"
MF"v[40:43],a[100:103],a[20:23],0\n"
EX"v27,v27\n"
MF"v[40:43],a[96:99],a[16:19],v[40:43]\n"
EX"v4,v4\n"
MF"v[48:51],a[96:99],a[32:35],0\n"
EX"v5,v5\n"
MF"v[48:51],a[100:103],a[36:39],v[48:51]\n"
RD"a[120:123],v226 offset:14336\n"
PK"v17,v18,v19\n"
MF"v[56:59],a[100:103],a[52:55],0\n"
EX"v6,v6\n"
MF"v[56:59],a[96:99],a[48:51],v[56:59]\n"
WT"lgkmcnt(2)\n"
EX"v7,v7\n"
MF"v[32:35],a[104:107],a[8:11],v[32:35]\n"
EX"v12,v12\n"
MF"v[32:35],a[108:111],a[12:15],v[32:35]\n"
RD"a[124:127],v226 offset:15360\n"
PK"v24,v24,v25\n"
MF"v[40:43],a[108:111],a[28:31],v[40:43]\n"
EX"v13,v13\n"
MF"v[40:43],a[104:107],a[24:27],v[40:43]\n"
EX"v14,v14\n"
MF"v[48:51],a[104:107],a[40:43],v[48:51]\n"
EX"v15,v15\n"
MF"v[48:51],a[108:111],a[44:47],v[48:51]\n"
RD"a[64:67],v227 offset:16384\n"
PK"v25,v26,v27\n"
MF"v[56:59],a[108:111],a[60:63],v[56:59]\n"
EX"v20,v20\n"
MF"v[56:59],a[104:107],a[56:59],v[56:59]\n"
EX"v21,v21\n"
MF"v[36:39],a[112:115],a[0:3],v[220:223]\n"
WT"lgkmcnt(1)\n"
EX"v22,v22\n"
MF"v[36:39],a[116:119],a[4:7],v[36:39]\n"
RD"a[68:71],v227 offset:24576\n"
PK"v2,v4,v5\n"
MF"v[44:47],a[116:119],a[20:23],0\n"
EX"v23,v23\n"
MF"v[44:47],a[112:115],a[16:19],v[44:47]\n"
EX"v28,v28\n"
MF"v[52:55],a[112:115],a[32:35],0\n"
EX"v29,v29\n"
MF"v[52:55],a[116:119],a[36:39],v[52:55]\n"
RD"a[72:75],v227 offset:17408\n"
PK"v3,v6,v7\n"
MF"v[60:63],a[116:119],a[52:55],0\n"
EX"v30,v30\n"
MF"v[60:63],a[112:115],a[48:51],v[60:63]\n"
EX"v31,v31\n"
MF"v[36:39],a[120:123],a[8:11],v[36:39]\n"
PK"v10,v12,v13\n"
PK"v11,v14,v15\n"
MF"v[36:39],a[124:127],a[12:15],v[36:39]\n"
RD"a[76:79],v227 offset:25600\n"
PK"v18,v20,v21\n"
MF"v[44:47],a[124:127],a[28:31],v[44:47]\n"
PK"v19,v22,v23\n"
PK"v26,v28,v29\n"
MF"v[44:47],a[120:123],a[24:27],v[44:47]\n"
PK"v27,v30,v31\n"
MF"v[52:55],a[120:123],a[40:43],v[52:55]\n"
MF"v[52:55],a[124:127],a[44:47],v[52:55]\n"
RD"a[80:83],v227 offset:18432\n"
MF"v[60:63],a[124:127],a[60:63],v[60:63]\n"
EX"v32,v32\n"
MF"v[60:63],a[120:123],a[56:59],v[60:63]\n"
WT"lgkmcnt(2)\n"
SA"s41,s41,32768\n"
"s_cmp_eq_u32 s41,163840\n"
MF"v[80:83],a[64:67],v[64:67],v[80:83]\n"
"s_cselect_b32 s41,0,s41\n"
EX"v33,v33\n"
MF"v[80:83],a[68:71],v[0:3],v[80:83]\n"
RD"a[84:87],v227 offset:26624\n"
"v_add_u32 v226,s41,v224\n"
MF"v[112:115],a[68:71],v[8:11],v[112:115]\n"
EX"v34,v34\n"
MF"v[112:115],a[64:67],v[68:71],v[112:115]\n"
MF"v[144:147],a[64:67],v[72:75],v[144:147]\n"
EX"v35,v35\n"
MF"v[144:147],a[68:71],v[16:19],v[144:147]\n"
RD"a[88:91],v227 offset:19456\n"
MF"v[176:179],a[68:71],v[24:27],v[176:179]\n"
EX"v40,v40\n"
MF"v[176:179],a[64:67],v[76:79],v[176:179]\n"
BL"v225,s[8:11],s43 offen offset:2048 sc1 lds\n"
MF"v[84:87],a[72:75],v[64:67],v[84:87]\n"
WT"lgkmcnt(1)\n"
EX"v41,v41\n"
MF"v[84:87],a[76:79],v[0:3],v[84:87]\n"
RD"a[92:95],v227 offset:27648\n"
MF"v[116:119],a[76:79],v[8:11],v[116:119]\n"
EX"v42,v42\n"
MF"v[116:119],a[72:75],v[68:71],v[116:119]\n"
MF"v[148:151],a[72:75],v[72:75],v[148:151]\n"
EX"v43,v43\n"
MF"v[148:151],a[76:79],v[16:19],v[148:151]\n"
RD"a[96:99],v227 offset:20480\n"
MF"v[180:183],a[76:79],v[24:27],v[180:183]\n"
EX"v48,v48\n"
MF"v[180:183],a[72:75],v[76:79],v[180:183]\n"
BL"v225,s[8:11],s43 offen offset:3072 sc1 lds\n"
MF"v[88:91],a[80:83],v[64:67],v[88:91]\n"
EX"v49,v49\n"
MF"v[88:91],a[84:87],v[0:3],v[88:91]\n"
RD"a[100:103],v227 offset:28672\n"
MF"v[120:123],a[84:87],v[8:11],v[120:123]\n"
EX"v50,v50\n"
MF"v[120:123],a[80:83],v[68:71],v[120:123]\n"
SA"m0,s40,16384\n"
MF"v[152:155],a[80:83],v[72:75],v[152:155]\n"
EX"v51,v51\n"
MF"v[152:155],a[84:87],v[16:19],v[152:155]\n"
RD"a[104:107],v227 offset:21504\n"
MF"v[184:187],a[84:87],v[24:27],v[184:187]\n"
EX"v56,v56\n"
MF"v[184:187],a[80:83],v[76:79],v[184:187]\n"
WT"lgkmcnt(2)\n"
BL"v225,s[8:11],s44 offen offset:0 sc1 lds\n"
MF"v[92:95],a[88:91],v[64:67],v[92:95]\n"
SA"s40,s40,32768\n"
EX"v57,v57\n"
MF"v[92:95],a[92:95],v[0:3],v[92:95]\n"
"s_cmp_eq_u32 s40,s46\n"
RD"a[108:111],v227 offset:29696\n"
"s_cselect_b32 s40,s47,s40\n"
MF"v[124:127],a[92:95],v[8:11],v[124:127]\n"
EX"v58,v58\n"
MF"v[124:127],a[88:91],v[68:71],v[124:127]\n"
MF"v[156:159],a[88:91],v[72:75],v[156:159]\n"
EX"v59,v59\n"
MF"v[156:159],a[92:95],v[16:19],v[156:159]\n"
RD"a[112:115],v227 offset:22528\n"
MF"v[188:191],a[92:95],v[24:27],v[188:191]\n"
EX"v36,v36\n"
MF"v[188:191],a[88:91],v[76:79],v[188:191]\n"
BL"v225,s[8:11],s44 offen offset:1024 sc1 lds\n"
MF"v[96:99],a[96:99],v[64:67],v[96:99]\n"
WT"lgkmcnt(1)\n"
EX"v37,v37\n"
MF"v[96:99],a[100:103],v[0:3],v[96:99]\n"
RD"a[116:119],v227 offset:30720\n"
MF"v[128:131],a[100:103],v[8:11],v[128:131]\n"
EX"v38,v38\n"
MF"v[128:131],a[96:99],v[68:71],v[128:131]\n"
MF"v[160:163],a[96:99],v[72:75],v[160:163]\n"
EX"v39,v39\n"
MF"v[160:163],a[100:103],v[16:19],v[160:163]\n"
RD"a[120:123],v227 offset:23552\n"
MF"v[192:195],a[100:103],v[24:27],v[192:195]\n"
EX"v44,v44\n"
MF"v[192:195],a[96:99],v[76:79],v[192:195]\n"
BL"v225,s[8:11],s44 offen offset:2048 sc1 lds\n"
MF"v[100:103],a[104:107],v[64:67],v[100:103]\n"
EX"v45,v45\n"
MF"v[100:103],a[108:111],v[0:3],v[100:103]\n"
RD"a[124:127],v227 offset:31744\n"
MF"v[132:135],a[108:111],v[8:11],v[132:135]\n"
EX"v46,v46\n"
MF"v[132:135],a[104:107],v[68:71],v[132:135]\n"
MF"v[164:167],a[104:107],v[72:75],v[164:167]\n"
EX"v47,v47\n"
MF"v[164:167],a[108:111],v[16:19],v[164:167]\n"
MF"v[196:199],a[108:111],v[24:27],v[196:199]\n"
EX"v52,v52\n"
MF"v[196:199],a[104:107],v[76:79],v[196:199]\n"
WT"lgkmcnt(1)\n"
BL"v225,s[8:11],s44 offen offset:3072 sc1 lds\n"
MF"v[104:107],a[112:115],v[64:67],v[104:107]\n"
EX"v53,v53\n"
MF"v[104:107],a[116:119],v[0:3],v[104:107]\n"
MF"v[136:139],a[116:119],v[8:11],v[136:139]\n"
EX"v54,v54\n"
MF"v[136:139],a[112:115],v[68:71],v[136:139]\n"
MF"v[168:171],a[112:115],v[72:75],v[168:171]\n"
WT"vmcnt(16) lgkmcnt(0)\n"
"s_barrier\n"
MF"v[168:171],a[116:119],v[16:19],v[168:171]\n"
RD"a[64:67],v226 offset:0\n"
MF"v[200:203],a[116:119],v[24:27],v[200:203]\n"
EX"v55,v55\n"
MF"v[200:203],a[112:115],v[76:79],v[200:203]\n"
MF"v[108:111],a[120:123],v[64:67],v[108:111]\n"
EX"v60,v60\n"
MF"v[108:111],a[124:127],v[0:3],v[108:111]\n"
RD"a[68:71],v226 offset:1024\n"
MF"v[140:143],a[124:127],v[8:11],v[140:143]\n"
EX"v61,v61\n"
MF"v[140:143],a[120:123],v[68:71],v[140:143]\n"
RD"a[72:75],v226 offset:2048\n"
MF"v[172:175],a[120:123],v[72:75],v[172:175]\n"
EX"v62,v62\n"
MF"v[172:175],a[124:127],v[16:19],v[172:175]\n"
RD"a[76:79],v226 offset:3072\n"
MF"v[204:207],a[124:127],v[24:27],v[204:207]\n"
EX"v63,v63\n"
MF"v[204:207],a[120:123],v[76:79],v[204:207]\n"
MF"a[132:135],a[128:131],v[64:67],a[132:135]\n"
MF"a[132:135],a[128:131],v[0:3],a[132:135]\n"
RD"a[80:83],v226 offset:4096\n"
MF"a[136:139],a[128:131],v[8:11],a[136:139]\n"
MF"a[136:139],a[128:131],v[68:71],a[136:139]\n"
RD"a[84:87],v226 offset:5120\n"
MF"a[140:143],a[128:131],v[72:75],a[140:143]\n"
MF"a[140:143],a[128:131],v[16:19],a[140:143]\n"
RD"a[88:91],v226 offset:6144\n"
MF"a[144:147],a[128:131],v[24:27],a[144:147]\n"
MF"a[144:147],a[128:131],v[76:79],a[144:147]\n"
"LDB_%=:\n"
WT"lgkmcnt(6)\n"
MF"v[8:11],a[64:67],a[16:19],v[208:211]\n"
WT"lgkmcnt(5)\n"
"v_readlane_b32 s43,v228,s42\n"
PK"v64,v32,v33\n"
MF"v[8:11],a[68:71],a[20:23],v[8:11]\n"
RD"a[92:95],v226 offset:7168\n"
PK"v65,v34,v35\n"
MF"v[16:19],a[68:71],a[36:39],0\n"
PK"v66,v36,v37\n"
PK"v67,v38,v39\n"
MF"v[16:19],a[64:67],a[32:35],v[16:19]\n"
"s_mov_b32 m0,s40\n"
PK"v68,v40,v41\n"
PK"v69,v42,v43\n"
MF"v[24:27],a[64:67],a[48:51],0\n"
PK"v70,v44,v45\n"
PK"v71,v46,v47\n"
MF"v[24:27],a[68:71],a[52:55],v[24:27]\n"
WT"lgkmcnt(1)\n"
RD"a[96:99],v226 offset:8192\n"
"v_readlane_b32 s44,v229,s42\n"
MF"v[8:11],a[72:75],a[24:27],v[8:11]\n"
PK"v72,v48,v49\n"
PK"v73,v50,v51\n"
MF"v[8:11],a[76:79],a[28:31],v[8:11]\n"
SA"s42,s42,1\n"
BL"v225,s[8:11],s43 offen offset:0 sc1 lds\n"
"v_mov_b32 v227,v226\n"
MF"v[16:19],a[76:79],a[44:47],v[16:19]\n"
PK"v74,v52,v53\n"
PK"v75,v54,v55\n"
MF"v[16:19],a[72:75],a[40:43],v[16:19]\n"
RD"a[100:103],v226 offset:9216\n"
PK"v76,v56,v57\n"
PK"v77,v58,v59\n"
MF"v[24:27],a[72:75],a[56:59],v[24:27]\n"
PK"v78,v60,v61\n"
PK"v79,v62,v63\n"
MF"v[24:27],a[76:79],a[60:63],v[24:27]\n"
RD"a[104:107],v226 offset:10240\n"
MF"v[12:15],a[80:83],a[16:19],v[212:215]\n"
EX"v8,v8\n"
MF"v[12:15],a[84:87],a[20:23],v[12:15]\n"
RD"a[108:111],v226 offset:11264\n"
"v_lshrrev_b32 v235,7,v224\n"
MF"v[20:23],a[84:87],a[36:39],0\n"
EX"v9,v9\n"
MF"v[20:23],a[80:83],a[32:35],v[20:23]\n"
BL"v225,s[8:11],s43 offen offset:1024 sc1 lds\n"
MF"v[28:31],a[80:83],a[48:51],0\n"
EX"v10,v10\n"
"v_lshrrev_b32 v230,1,v224\n"
MF"v[28:31],a[84:87],a[52:55],v[28:31]\n"
RD"a[112:115],v226 offset:12288\n"
"v_lshl_add_u32 v230,v235,27,v230\n"
MF"v[12:15],a[88:91],a[24:27],v[12:15]\n"
WT"lgkmcnt(2)\n"
EX"v11,v11\n"
PK"v8,v8,v9\n"
MF"v[12:15],a[92:95],a[28:31],v[12:15]\n"
RD"a[116:119],v226 offset:13312\n"
PK"v9,v10,v11\n"
MF"v[20:23],a[92:95],a[44:47],v[20:23]\n"
"s_lshl_b32 s48,s35,8\n"
EX"v16,v16\n"
MF"v[20:23],a[88:91],a[40:43],v[20:23]\n"
MF"v[28:31],a[88:91],a[56:59],v[28:31]\n"
SA"s48,s48,81264640\n"
EX"v17,v17\n"
MF"v[28:31],a[92:95],a[60:63],v[28:31]\n"
BL"v225,s[8:11],s43 offen offset:2048 sc1 lds\n"
MF"v[40:43],a[96:99],a[16:19],v[216:219]\n"
"s_andn2_b32 s49,s61,255\n"
EX"v18,v18\n"
MF"v[40:43],a[100:103],a[20:23],v[40:43]\n"
RD"a[120:123],v226 offset:14336\n"
PK"v16,v16,v17\n"
MF"v[48:51],a[100:103],a[36:39],0\n"
EX"v19,v19\n"
MF"v[48:51],a[96:99],a[32:35],v[48:51]\n"
RD"a[124:127],v226 offset:15360\n"
"v_bfe_u32 v236,v224,4,2\n"
MF"v[56:59],a[96:99],a[48:51],0\n"
EX"v24,v24\n"
MF"v[56:59],a[100:103],a[52:55],v[56:59]\n"
PK"v17,v18,v19\n"
"v_lshl_add_u32 v236,v236,6,s49\n"
MF"v[40:43],a[104:107],a[24:27],v[40:43]\n"
WT"lgkmcnt(2)\n"
EX"v25,v25\n"
MF"v[40:43],a[108:111],a[28:31],v[40:43]\n"
BL"v225,s[8:11],s43 offen offset:3072 sc1 lds\n"
MF"v[48:51],a[108:111],a[44:47],v[48:51]\n"
EX"v26,v26\n"
MF"v[48:51],a[104:107],a[40:43],v[48:51]\n"
RD"a[64:67],v227 offset:16384\n"
"v_mov_b32 v237,s48\n"
MF"v[56:59],a[104:107],a[56:59],v[56:59]\n"
EX"v27,v27\n"
PK"v24,v24,v25\n"
MF"v[56:59],a[108:111],a[60:63],v[56:59]\n"
SA"m0,s40,16384\n"
"v_cmp_lt_u32 vcc,511,v224\n"
PK"v25,v26,v27\n"
MF"v[44:47],a[112:115],a[16:19],v[220:223]\n"
EX"v12,v12\n"
MF"v[44:47],a[116:119],a[20:23],v[44:47]\n"
RD"a[68:71],v227 offset:17408\n"
"v_cndmask_b32 v236,v236,v237,vcc\n"
MF"v[52:55],a[116:119],a[36:39],0\n"
EX"v13,v13\n"
MF"v[52:55],a[112:115],a[32:35],v[52:55]\n"
BL"v225,s[8:11],s44 offen offset:0 sc1 lds\n"
MF"v[60:63],a[112:115],a[48:51],0\n"
SA"s40,s40,32768\n"
EX"v14,v14\n"
MF"v[60:63],a[116:119],a[52:55],v[60:63]\n"
WT"lgkmcnt(1)\n"
RD"a[72:75],v227 offset:18432\n"
PK"v10,v12,v13\n"
MF"v[44:47],a[120:123],a[24:27],v[44:47]\n"
"s_cmp_eq_u32 s40,s46\n"
EX"v15,v15\n"
MF"v[44:47],a[124:127],a[28:31],v[44:47]\n"
"s_cselect_b32 s40,s47,s40\n"
PK"v11,v14,v15\n"
MF"v[52:55],a[124:127],a[44:47],v[52:55]\n"
EX"v20,v20\n"
MF"v[52:55],a[120:123],a[40:43],v[52:55]\n"
RD"a[76:79],v227 offset:19456\n"
MF"v[60:63],a[120:123],a[56:59],v[60:63]\n"
EX"v21,v21\n"
MF"v[60:63],a[124:127],a[60:63],v[60:63]\n"
SA"s41,s41,32768\n"
BL"v225,s[8:11],s44 offen offset:1024 sc1 lds\n"
MF"v[80:83],a[64:67],v[64:67],v[80:83]\n"
"s_cmp_eq_u32 s41,163840\n"
EX"v22,v22\n"
MF"v[112:115],a[64:67],v[68:71],v[112:115]\n"
"s_cselect_b32 s41,0,s41\n"
RD"a[80:83],v227 offset:20480\n"
"v_add_u32 v226,s41,v224\n"
MF"v[144:147],a[64:67],v[72:75],v[144:147]\n"
EX"v23,v23\n"
PK"v18,v20,v21\n"
MF"v[176:179],a[64:67],v[76:79],v[176:179]\n"
WT"lgkmcnt(2)\n"
PK"v19,v22,v23\n"
MF"v[84:87],a[68:71],v[64:67],v[84:87]\n"
EX"v28,v28\n"
MF"v[116:119],a[68:71],v[68:71],v[116:119]\n"
RD"a[84:87],v227 offset:21504\n"
MF"v[148:151],a[68:71],v[72:75],v[148:151]\n"
EX"v29,v29\n"
MF"v[180:183],a[68:71],v[76:79],v[180:183]\n"
BL"v225,s[8:11],s44 offen offset:2048 sc1 lds\n"
MF"v[88:91],a[72:75],v[64:67],v[88:91]\n"
EX"v30,v30\n"
MF"v[120:123],a[72:75],v[68:71],v[120:123]\n"
RD"a[88:91],v227 offset:22528\n"
PK"v26,v28,v29\n"
MF"v[152:155],a[72:75],v[72:75],v[152:155]\n"
EX"v31,v31\n"
MF"v[184:187],a[72:75],v[76:79],v[184:187]\n"
WT"lgkmcnt(2)\n"
PK"v27,v30,v31\n"
MF"v[92:95],a[76:79],v[64:67],v[92:95]\n"
EX"v40,v40\n"
MF"v[124:127],a[76:79],v[68:71],v[124:127]\n"
RD"a[92:95],v227 offset:23552\n"
MF"v[156:159],a[76:79],v[72:75],v[156:159]\n"
EX"v41,v41\n"
MF"v[188:191],a[76:79],v[76:79],v[188:191]\n"
BL"v225,s[8:11],s44 offen offset:3072 sc1 lds\n"
MF"v[96:99],a[80:83],v[64:67],v[96:99]\n"
EX"v42,v42\n"
MF"v[128:131],a[80:83],v[68:71],v[128:131]\n"
RD"a[96:99],v227 offset:24576\n"
MF"v[160:163],a[80:83],v[72:75],v[160:163]\n"
EX"v43,v43\n"
MF"v[192:195],a[80:83],v[76:79],v[192:195]\n"
WT"lgkmcnt(2)\n"
RD"a[100:103],v227 offset:25600\n"
MF"v[100:103],a[84:87],v[64:67],v[100:103]\n"
EX"v48,v48\n"
MF"v[132:135],a[84:87],v[68:71],v[132:135]\n"
MF"v[164:167],a[84:87],v[72:75],v[164:167]\n"
EX"v49,v49\n"
MF"v[196:199],a[84:87],v[76:79],v[196:199]\n"
MF"v[104:107],a[88:91],v[64:67],v[104:107]\n"
EX"v50,v50\n"
MF"v[136:139],a[88:91],v[68:71],v[136:139]\n"
RD"a[104:107],v227 offset:26624\n"
MF"v[168:171],a[88:91],v[72:75],v[168:171]\n"
EX"v51,v51\n"
MF"v[200:203],a[88:91],v[76:79],v[200:203]\n"
WT"lgkmcnt(1)\n"
RD"a[108:111],v227 offset:27648\n"
MF"v[108:111],a[92:95],v[64:67],v[108:111]\n"
EX"v44,v44\n"
MF"v[140:143],a[92:95],v[68:71],v[140:143]\n"
RD"a[112:115],v227 offset:28672\n"
MF"v[172:175],a[92:95],v[72:75],v[172:175]\n"
EX"v45,v45\n"
MF"v[204:207],a[92:95],v[76:79],v[204:207]\n"
MF"a[132:135],a[128:131],v[64:67],a[132:135]\n"
EX"v46,v46\n"
MF"a[136:139],a[128:131],v[68:71],a[136:139]\n"
RD"a[116:119],v227 offset:29696\n"
PK"v68,v40,v41\n"
PK"v69,v42,v43\n"
MF"a[140:143],a[128:131],v[72:75],a[140:143]\n"
EX"v47,v47\n"
PK"v72,v48,v49\n"
MF"a[144:147],a[128:131],v[76:79],a[144:147]\n"
RD"a[120:123],v227 offset:30720\n"
PK"v73,v50,v51\n"
PK"v70,v44,v45\n"
MF"v[112:115],a[96:99],v[8:11],v[112:115]\n"
EX"v52,v52\n"
MF"v[144:147],a[96:99],v[16:19],v[144:147]\n"
RD"a[124:127],v227 offset:31744\n"
PK"v71,v46,v47\n"
MF"v[176:179],a[96:99],v[24:27],v[176:179]\n"
EX"v53,v53\n"
MF"v[116:119],a[100:103],v[8:11],v[116:119]\n"
"v_accvgpr_read_b32 v251,a132\n"
"v_subrev_u32 v253,0x2b800000,v251\n"
MF"v[148:151],a[100:103],v[16:19],v[148:151]\n"
"v_cmp_le_u32 vcc,0x34000000,v253\n"
"s_cmp_lg_u64 vcc,0\n"
"s_cbranch_scc1 L1014_%=\n"
"L1013_%=:\n"
MF"v[180:183],a[100:103],v[24:27],v[180:183]\n"
WT"lgkmcnt(2)\n"
EX"v54,v54\n"
MF"v[120:123],a[104:107],v[8:11],v[120:123]\n"
"v_rcp_f32 v251,v251\n"
PK"v74,v52,v53\n"
MF"v[152:155],a[104:107],v[16:19],v[152:155]\n"
EX"v55,v55\n"
MF"v[184:187],a[104:107],v[24:27],v[184:187]\n"
WT"vmcnt(16) lgkmcnt(0)\n"
"s_barrier\n"
MF"v[124:127],a[108:111],v[8:11],v[124:127]\n"
RD"a[64:67],v226 offset:0\n"
MU"v80,v251,v80\n"
MU"v81,v251,v81\n"
MF"v[156:159],a[108:111],v[16:19],v[156:159]\n"
MU"v82,v251,v82\n"
MU"v83,v251,v83\n"
PK"v75,v54,v55\n"
MF"v[188:191],a[108:111],v[24:27],v[188:191]\n"
MU"v84,v251,v84\n"
MU"v85,v251,v85\n"
MU"v86,v251,v86\n"
MF"v[128:131],a[112:115],v[8:11],v[128:131]\n"
MU"v87,v251,v87\n"
PK"v80,v80,v81\n"
PK"v81,v82,v83\n"
MF"v[160:163],a[112:115],v[16:19],v[160:163]\n"
RD"a[68:71],v226 offset:1024\n"
PK"v82,v84,v85\n"
PK"v83,v86,v87\n"
MF"v[192:195],a[112:115],v[24:27],v[192:195]\n"
BS"v[80:83],v224,s[8:11],s59 offen offset:0 sc1\n"
MU"v88,v251,v88\n"
MF"v[132:135],a[116:119],v[8:11],v[132:135]\n"
RD"a[72:75],v226 offset:2048\n"
MU"v89,v251,v89\n"
MU"v90,v251,v90\n"
MF"v[164:167],a[116:119],v[16:19],v[164:167]\n"
MU"v91,v251,v91\n"
MU"v92,v251,v92\n"
MU"v93,v251,v93\n"
MF"v[196:199],a[116:119],v[24:27],v[196:199]\n"
RD"a[76:79],v226 offset:3072\n"
MU"v94,v251,v94\n"
MU"v95,v251,v95\n"
MF"v[136:139],a[120:123],v[8:11],v[136:139]\n"
PK"v88,v88,v89\n"
PK"v89,v90,v91\n"
MF"v[168:171],a[120:123],v[16:19],v[168:171]\n"
PK"v90,v92,v93\n"
PK"v91,v94,v95\n"
MF"v[200:203],a[120:123],v[24:27],v[200:203]\n"
BS"v[88:91],v224,s[8:11],s59 offen offset:1024 sc1\n"
MU"v96,v251,v96\n"
MF"v[140:143],a[124:127],v[8:11],v[140:143]\n"
RD"a[80:83],v226 offset:4096\n"
MU"v97,v251,v97\n"
MU"v98,v251,v98\n"
MF"v[172:175],a[124:127],v[16:19],v[172:175]\n"
MU"v99,v251,v99\n"
MU"v100,v251,v100\n"
MU"v101,v251,v101\n"
MF"v[204:207],a[124:127],v[24:27],v[204:207]\n"
MU"v104,v251,v104\n"
MU"v105,v251,v105\n"
MU"v102,v251,v102\n"
MF"a[136:139],a[128:131],v[8:11],a[136:139]\n"
PK"v96,v96,v97\n"
MU"v106,v251,v106\n"
MU"v103,v251,v103\n"
MF"a[140:143],a[128:131],v[16:19],a[140:143]\n"
MU"v107,v251,v107\n"
PK"v97,v98,v99\n"
PK"v98,v100,v101\n"
MF"a[144:147],a[128:131],v[24:27],a[144:147]\n"
MU"v108,v251,v108\n"
MU"v109,v251,v109\n"
PK"v99,v102,v103\n"
BS"v[96:99],v224,s[8:11],s59 offen offset:2048 sc1\n"
MU"v110,v251,v110\n"
MU"v111,v251,v111\n"
PK"v104,v104,v105\n"
PK"v105,v106,v107\n"
PK"v106,v108,v109\n"
PK"v107,v110,v111\n"
BS"v[104:107],v224,s[8:11],s59 offen offset:3072 sc1\n"
RD"a[84:87],v226 offset:5120\n"
RD"a[88:91],v226 offset:6144\n"
"s_cmp_eq_u32 s34,7\n"
"s_cbranch_scc1 LD2_%=\n"
WT"lgkmcnt(6)\n"
MF"v[0:3],a[64:67],a[32:35],0\n"
WT"lgkmcnt(5)\n"
EX"v56,v56\n"
MF"v[0:3],a[68:71],a[36:39],v[0:3]\n"
"s_mov_b32 m0,s40\n"
EX"v57,v57\n"
MF"v[8:11],a[68:71],a[52:55],0\n"
"s_lshl_b32 s50,s35,6\n"
RD"a[92:95],v226 offset:7168\n"
"v_readlane_b32 s43,v228,s42\n"
MF"v[8:11],a[64:67],a[48:51],v[8:11]\n"
WT"lgkmcnt(5)\n"
EX"v58,v58\n"
MF"v[0:3],a[72:75],a[40:43],v[0:3]\n"
WT"lgkmcnt(1)\n"
BL"v225,s[8:11],s43 offen offset:0 sc1 lds\n"
"v_readlane_b32 s44,v229,s42\n"
MF"v[0:3],a[76:79],a[44:47],v[0:3]\n"
SA"s50,s50,4352\n"
RD"a[96:99],v226 offset:8192\n"
RD"a[100:103],v226 offset:9216\n"
PK"v76,v56,v57\n"
MF"v[8:11],a[76:79],a[60:63],v[8:11]\n"
SA"s42,s42,1\n"
EX"v59,v59\n"
MF"v[8:11],a[72:75],a[56:59],v[8:11]\n"
SA"s41,s41,32768\n"
RD"a[104:107],v226 offset:10240\n"
"v_lshrrev_b32 v237,1,v224\n"
MF"v[4:7],a[80:83],a[32:35],0\n"
"s_cmp_eq_u32 s41,163840\n"
EX"v60,v60\n"
"v_and_b32 v237,56,v237\n"
MF"v[4:7],a[84:87],a[36:39],v[4:7]\n"
"s_cselect_b32 s41,0,s41\n"
EX"v61,v61\n"
"v_add_u32 v227,s41,v224\n"
MF"v[12:15],a[84:87],a[52:55],0\n"
RD"a[108:111],v226 offset:11264\n"
PK"v77,v58,v59\n"
MF"v[12:15],a[80:83],a[48:51],v[12:15]\n"
BL"v225,s[8:11],s43 offen offset:1024 sc1 lds\n"
MF"v[4:7],a[88:91],a[40:43],v[4:7]\n"
WT"lgkmcnt(2)\n"
EX"v62,v62\n"
MF"v[4:7],a[92:95],a[44:47],v[4:7]\n"
RD"a[112:115],v226 offset:12288\n"
RD"a[116:119],v226 offset:13312\n"
PK"v78,v60,v61\n"
MF"v[12:15],a[92:95],a[60:63],v[12:15]\n"
EX"v63,v63\n"
MF"v[12:15],a[88:91],a[56:59],v[12:15]\n"
RD"a[120:123],v226 offset:14336\n"
PK"v79,v62,v63\n"
MF"v[16:19],a[96:99],a[32:35],0\n"
EX"v0,v0\n"
MF"v[16:19],a[100:103],a[36:39],v[16:19]\n"
EX"v1,v1\n"
MF"v[24:27],a[100:103],a[52:55],0\n"
RD"a[124:127],v226 offset:15360\n"
PK"v0,v0,v1\n"
MF"v[24:27],a[96:99],a[48:51],v[24:27]\n"
WT"lgkmcnt(4)\n"
BL"v225,s[8:11],s43 offen offset:2048 sc1 lds\n"
MF"v[16:19],a[104:107],a[40:43],v[16:19]\n"
"s_lshl_b32 s56,s34,1\n"
EX"v2,v2\n"
MF"v[16:19],a[108:111],a[44:47],v[16:19]\n"
"s_sub_u32 s56,12,s56\n"
RD"a[64:67],v226 offset:16384\n"
RD"a[68:71],v226 offset:17408\n"
MF"v[24:27],a[108:111],a[60:63],v[24:27]\n"
"s_sub_u32 s22,s56,2\n"
EX"v3,v3\n"
MF"v[24:27],a[104:107],a[56:59],v[24:27]\n"
WT"lgkmcnt(3)\n"
RD"a[72:75],v226 offset:18432\n"
PK"v1,v2,v3\n"
MF"v[20:23],a[112:115],a[32:35],0\n"
"s_min_u32 s56,s56,1\n"
EX"v8,v8\n"
MF"v[20:23],a[116:119],a[36:39],v[20:23]\n"
"s_mov_b32 s57,s61\n"
EX"v9,v9\n"
MF"v[28:31],a[116:119],a[52:55],0\n"
BL"v225,s[8:11],s43 offen offset:3072 sc1 lds\n"
MF"v[28:31],a[112:115],a[48:51],v[28:31]\n"
RD"a[76:79],v226 offset:19456\n"
RD"a[80:83],v226 offset:20480\n"
MF"v[20:23],a[120:123],a[40:43],v[20:23]\n"
WT"lgkmcnt(3)\n"
EX"v10,v10\n"
PK"v8,v8,v9\n"
MF"v[20:23],a[124:127],a[44:47],v[20:23]\n"
SA"m0,s40,16384\n"
RD"a[84:87],v226 offset:21504\n"
RD"a[88:91],v226 offset:22528\n"
EX"v11,v11\n"
MF"v[28:31],a[124:127],a[60:63],v[28:31]\n"
EX"v4,v4\n"
MF"v[28:31],a[120:123],a[56:59],v[28:31]\n"
"buffer_load_dwordx2 v[238:239],v237,s[8:11],s50 offen sc1\n"
PK"v9,v10,v11\n"
MF"v[32:35],a[64:67],a[32:35],0\n"
EX"v5,v5\n"
MF"v[32:35],a[68:71],a[36:39],v[32:35]\n"
BL"v225,s[8:11],s44 offen offset:0 sc1 lds\n"
MF"v[40:43],a[68:71],a[52:55],0\n"
SA"s40,s40,32768\n"
RD"a[92:95],v226 offset:23552\n"
PK"v2,v4,v5\n"
MF"v[40:43],a[64:67],a[48:51],v[40:43]\n"
WT"lgkmcnt(3)\n"
EX"v6,v6\n"
MF"v[32:35],a[72:75],a[40:43],v[32:35]\n"
"s_cmp_eq_u32 s40,s46\n"
RD"a[96:99],v226 offset:24576\n"
RD"a[100:103],v226 offset:25600\n"
MF"v[32:35],a[76:79],a[44:47],v[32:35]\n"
"s_cselect_b32 s40,s47,s40\n"
EX"v7,v7\n"
MF"v[40:43],a[76:79],a[60:63],v[40:43]\n"
RD"a[104:107],v226 offset:26624\n"
PK"v3,v6,v7\n"
MF"v[40:43],a[72:75],a[56:59],v[40:43]\n"
EX"v12,v12\n"
MF"v[36:39],a[80:83],a[32:35],0\n"
WT"lgkmcnt(4)\n"
EX"v13,v13\n"
MF"v[36:39],a[84:87],a[36:39],v[36:39]\n"
BL"v225,s[8:11],s44 offen offset:1024 sc1 lds\n"
MF"v[44:47],a[84:87],a[52:55],0\n"
EX"v14,v14\n"
MF"v[44:47],a[80:83],a[48:51],v[44:47]\n"
RD"a[108:111],v226 offset:27648\n"
RD"a[112:115],v226 offset:28672\n"
PK"v10,v12,v13\n"
MF"v[36:39],a[88:91],a[40:43],v[36:39]\n"
WT"lgkmcnt(3)\n"
EX"v15,v15\n"
MF"v[36:39],a[92:95],a[44:47],v[36:39]\n"
RD"a[116:119],v226 offset:29696\n"
RD"a[120:123],v226 offset:30720\n"
MF"v[44:47],a[92:95],a[60:63],v[44:47]\n"
EX"v16,v16\n"
PK"v11,v14,v15\n"
MF"v[44:47],a[88:91],a[56:59],v[44:47]\n"
EX"v17,v17\n"
MF"v[48:51],a[96:99],a[32:35],0\n"
BL"v225,s[8:11],s44 offen offset:2048 sc1 lds\n"
MF"v[48:51],a[100:103],a[36:39],v[48:51]\n"
EX"v18,v18\n"
MF"v[56:59],a[100:103],a[52:55],0\n"
RD"a[124:127],v226 offset:31744\n"
PK"v16,v16,v17\n"
MF"v[56:59],a[96:99],a[48:51],v[56:59]\n"
WT"lgkmcnt(3)\n"
EX"v19,v19\n"
MF"v[48:51],a[104:107],a[40:43],v[48:51]\n"
WT"vmcnt(16)\n"
"s_barrier\n"
MF"v[48:51],a[108:111],a[44:47],v[48:51]\n"
RD"a[96:99],v227 offset:8192\n"
RD"a[100:103],v227 offset:9216\n"
MF"v[56:59],a[108:111],a[60:63],v[56:59]\n"
EX"v24,v24\n"
PK"v17,v18,v19\n"
MF"v[56:59],a[104:107],a[56:59],v[56:59]\n"
EX"v25,v25\n"
MF"v[52:55],a[112:115],a[32:35],0\n"
WT"lgkmcnt(3)\n"
BL"v225,s[8:11],s44 offen offset:3072 sc1 lds\n"
MF"v[52:55],a[116:119],a[36:39],v[52:55]\n"
EX"v26,v26\n"
PK"v24,v24,v25\n"
MF"v[60:63],a[116:119],a[52:55],0\n"
EX"v27,v27\n"
"v_readlane_b32 s43,v228,s42\n"
MF"v[60:63],a[112:115],a[48:51],v[60:63]\n"
"s_mov_b32 m0,s40\n"
RD"a[104:107],v227 offset:10240\n"
RD"a[108:111],v227 offset:11264\n"
MF"v[52:55],a[120:123],a[40:43],v[52:55]\n"
WT"lgkmcnt(2)\n"
EX"v20,v20\n"
MF"v[52:55],a[124:127],a[44:47],v[52:55]\n"
EX"v21,v21\n"
PK"v25,v26,v27\n"
MF"v[60:63],a[124:127],a[60:63],v[60:63]\n"
RD"a[112:115],v227 offset:12288\n"
"v_readlane_b32 s44,v229,s42\n"
MF"v[60:63],a[120:123],a[56:59],v[60:63]\n"
SA"s41,s41,32768\n"
EX"v22,v22\n"
MF"v[112:115],a[96:99],v[68:71],v[112:115]\n"
"s_cmp_eq_u32 s41,163840\n"
BL"v225,s[8:11],s43 offen offset:0 sc1 lds\n"
MF"v[144:147],a[96:99],v[72:75],v[144:147]\n"
"s_cselect_b32 s41,0,s41\n"
EX"v23,v23\n"
PK"v18,v20,v21\n"
MF"v[176:179],a[96:99],v[76:79],v[176:179]\n"
SA"s42,s42,1\n"
RD"a[116:119],v227 offset:13312\n"
"v_add_u32 v226,s41,v224\n"
MF"v[116:119],a[100:103],v[68:71],v[116:119]\n"
EX"v28,v28\n"
MF"v[148:151],a[100:103],v[72:75],v[148:151]\n"
RD"a[120:123],v227 offset:14336\n"
PK"v19,v22,v23\n"
MF"v[180:183],a[100:103],v[76:79],v[180:183]\n"
WT"lgkmcnt(2)\n"
EX"v29,v29\n"
MF"v[120:123],a[104:107],v[68:71],v[120:123]\n"
EX"v30,v30\n"
MF"v[152:155],a[104:107],v[72:75],v[152:155]\n"
BL"v225,s[8:11],s43 offen offset:1024 sc1 lds\n"
MF"v[184:187],a[104:107],v[76:79],v[184:187]\n"
EX"v31,v31\n"
PK"v26,v28,v29\n"
MF"v[124:127],a[108:111],v[68:71],v[124:127]\n"
RD"a[124:127],v227 offset:15360\n"
PK"v27,v30,v31\n"
MF"v[156:159],a[108:111],v[72:75],v[156:159]\n"
EX"v32,v32\n"
MF"v[188:191],a[108:111],v[76:79],v[188:191]\n"
MF"v[128:131],a[112:115],v[68:71],v[128:131]\n"
EX"v33,v33\n"
MF"v[160:163],a[112:115],v[72:75],v[160:163]\n"
RD"a[64:67],v227 offset:16384\n"
PK"v64,v32,v33\n"
MF"v[192:195],a[112:115],v[76:79],v[192:195]\n"
WT"lgkmcnt(2)\n"
BL"v225,s[8:11],s43 offen offset:2048 sc1 lds\n"
MF"v[132:135],a[116:119],v[68:71],v[132:135]\n"
EX"v34,v34\n"
MF"v[164:167],a[116:119],v[72:75],v[164:167]\n"
EX"v35,v35\n"
MF"v[196:199],a[116:119],v[76:79],v[196:199]\n"
EX"v40,v40\n"
MF"v[136:139],a[120:123],v[68:71],v[136:139]\n"
RD"a[68:71],v227 offset:24576\n"
PK"v65,v34,v35\n"
MF"v[168:171],a[120:123],v[72:75],v[168:171]\n"
EX"v41,v41\n"
MF"v[200:203],a[120:123],v[76:79],v[200:203]\n"
WT"lgkmcnt(1)\n"
RD"a[72:75],v227 offset:17408\n"
RD"a[76:79],v227 offset:25600\n"
MF"v[140:143],a[124:127],v[68:71],v[140:143]\n"
WT"vmcnt(7)\n"
"v_cmp_eq_u64 vcc,s[28:29],v[238:239]\n"
"s_cmp_eq_u64 vcc,exec\n"
"s_cbranch_scc0 L1019_%=\n"
"L1020_%=:\n"
MF"v[172:175],a[124:127],v[72:75],v[172:175]\n"
EX"v42,v42\n"
MF"v[204:207],a[124:127],v[76:79],v[204:207]\n"
RD"a[80:83],v227 offset:18432\n"
MF"a[136:139],a[128:131],v[68:71],a[136:139]\n"
BL"v225,s[8:11],s43 offen offset:3072 sc1 lds\n"
PK"v68,v40,v41\n"
MF"a[140:143],a[128:131],v[72:75],a[140:143]\n"
SA"m0,s40,16384\n"
EX"v43,v43\n"
MF"a[144:147],a[128:131],v[76:79],a[144:147]\n"
EX"v36,v36\n"
MF"v[144:147],a[64:67],v[0:3],v[144:147]\n"
WT"lgkmcnt(1)\n"
RD"a[84:87],v227 offset:26624\n"
RD"a[88:91],v227 offset:19456\n"
PK"v69,v42,v43\n"
MF"v[144:147],a[68:71],v[16:19],v[144:147]\n"
EX"v37,v37\n"
MF"v[176:179],a[68:71],v[24:27],v[176:179]\n"
SA"s51,s59,16384\n"
RD"a[92:95],v227 offset:27648\n"
RD"a[96:99],v227 offset:20480\n"
MF"v[176:179],a[64:67],v[8:11],v[176:179]\n"
SA"s40,s40,32768\n"
EX"v38,v38\n"
MF"v[148:151],a[72:75],v[0:3],v[148:151]\n"
"s_cmp_eq_u32 s40,s46\n"
BL"v225,s[8:11],s44 offen offset:0 sc1 lds\n"
PK"v66,v36,v37\n"
MF"v[148:151],a[76:79],v[16:19],v[148:151]\n"
"s_cselect_b32 s40,s47,s40\n"
EX"v39,v39\n"
"v_accvgpr_read_b32 v252,a136\n"
MF"v[180:183],a[76:79],v[24:27],v[180:183]\n"
EX"v44,v44\n"
"v_subrev_u32 v253,0x2b800000,v252\n"
MF"v[180:183],a[72:75],v[8:11],v[180:183]\n"
WT"lgkmcnt(2)\n"
"v_cmp_le_u32 vcc,0x34000000,v253\n"
"s_cmp_lg_u64 vcc,0\n"
"s_cbranch_scc1 L1023_%=\n"
"L1022_%=:\n"
MF"v[152:155],a[80:83],v[0:3],v[152:155]\n"
"v_rcp_f32 v252,v252\n"
PK"v67,v38,v39\n"
MF"v[152:155],a[84:87],v[16:19],v[152:155]\n"
RD"a[100:103],v227 offset:28672\n"
RD"a[104:107],v227 offset:21504\n"
EX"v45,v45\n"
MF"v[184:187],a[84:87],v[24:27],v[184:187]\n"
RD"a[108:111],v227 offset:29696\n"
RD"a[112:115],v227 offset:22528\n"
MU"v112,v252,v112\n"
MF"v[184:187],a[80:83],v[8:11],v[184:187]\n"
EX"v46,v46\n"
MU"v113,v252,v113\n"
MF"v[156:159],a[88:91],v[0:3],v[156:159]\n"
WT"lgkmcnt(4)\n"
BL"v225,s[8:11],s44 offen offset:1024 sc1 lds\n"
MU"v114,v252,v114\n"
MF"v[156:159],a[92:95],v[16:19],v[156:159]\n"
EX"v47,v47\n"
MU"v115,v252,v115\n"
MF"v[188:191],a[92:95],v[24:27],v[188:191]\n"
EX"v48,v48\n"
MU"v116,v252,v116\n"
MF"v[188:191],a[88:91],v[8:11],v[188:191]\n"
EX"v49,v49\n"
PK"v70,v44,v45\n"
MF"v[160:163],a[96:99],v[0:3],v[160:163]\n"
WT"lgkmcnt(2)\n"
RD"a[116:119],v227 offset:30720\n"
RD"a[120:123],v227 offset:23552\n"
MU"v117,v252,v117\n"
MF"v[160:163],a[100:103],v[16:19],v[160:163]\n"
EX"v50,v50\n"
MU"v118,v252,v118\n"
MF"v[192:195],a[100:103],v[24:27],v[192:195]\n"
RD"a[124:127],v227 offset:31744\n"
MU"v119,v252,v119\n"
PK"v112,v112,v113\n"
MF"v[192:195],a[96:99],v[8:11],v[192:195]\n"
BL"v225,s[8:11],s44 offen offset:2048 sc1 lds\n"
PK"v113,v114,v115\n"
MF"v[164:167],a[104:107],v[0:3],v[164:167]\n"
WT"lgkmcnt(3)\n"
EX"v51,v51\n"
PK"v114,v116,v117\n"
MF"v[164:167],a[108:111],v[16:19],v[164:167]\n"
EX"v52,v52\n"
PK"v115,v118,v119\n"
MF"v[196:199],a[108:111],v[24:27],v[196:199]\n"
BS"v[112:115],v224,s[8:11],s51 offen offset:0 sc1\n"
PK"v71,v46,v47\n"
MF"v[196:199],a[104:107],v[8:11],v[196:199]\n"
"s_barrier\n"
MF"v[168:171],a[112:115],v[0:3],v[168:171]\n"
WT"lgkmcnt(1)\n"
EX"v53,v53\n"
MU"v120,v252,v120\n"
MF"v[168:171],a[116:119],v[16:19],v[168:171]\n"
RD"a[64:67],v226 offset:0\n"
RD"a[68:71],v226 offset:1024\n"
MU"v121,v252,v121\n"
MF"v[200:203],a[116:119],v[24:27],v[200:203]\n"
EX"v54,v54\n"
MU"v122,v252,v122\n"
MF"v[200:203],a[112:115],v[8:11],v[200:203]\n"
BL"v225,s[8:11],s44 offen offset:3072 sc1 lds\n"
MU"v123,v252,v123\n"
MF"v[172:175],a[120:123],v[0:3],v[172:175]\n"
WT"lgkmcnt(2)\n"
EX"v55,v55\n"
MU"v124,v252,v124\n"
MF"v[172:175],a[124:127],v[16:19],v[172:175]\n"
RD"a[72:75],v226 offset:2048\n"
PK"v72,v48,v49\n"
MU"v125,v252,v125\n"
MF"v[204:207],a[124:127],v[24:27],v[204:207]\n"
RD"a[76:79],v226 offset:3072\n"
MU"v126,v252,v126\n"
MU"v127,v252,v127\n"
MF"v[204:207],a[120:123],v[8:11],v[204:207]\n"
PK"v120,v120,v121\n"
PK"v121,v122,v123\n"
PK"v122,v124,v125\n"
MF"a[140:143],a[128:131],v[0:3],a[140:143]\n"
MU"v128,v252,v128\n"
MU"v129,v252,v129\n"
MU"v136,v252,v136\n"
MF"a[140:143],a[128:131],v[16:19],a[140:143]\n"
MU"v137,v252,v137\n"
MU"v130,v252,v130\n"
MU"v131,v252,v131\n"
MF"a[144:147],a[128:131],v[24:27],a[144:147]\n"
PK"v128,v128,v129\n"
MU"v138,v252,v138\n"
MU"v139,v252,v139\n"
MF"a[144:147],a[128:131],v[8:11],a[144:147]\n"
PK"v136,v136,v137\n"
MU"v132,v252,v132\n"
MU"v133,v252,v133\n"
PK"v73,v50,v51\n"
PK"v74,v52,v53\n"
PK"v75,v54,v55\n"
PK"v123,v126,v127\n"
BS"v[120:123],v224,s[8:11],s51 offen offset:1024 sc1\n"
MU"v134,v252,v134\n"
MU"v135,v252,v135\n"
PK"v129,v130,v131\n"
PK"v130,v132,v133\n"
PK"v131,v134,v135\n"
BS"v[128:131],v224,s[8:11],s51 offen offset:2048 sc1\n"
MU"v140,v252,v140\n"
MU"v141,v252,v141\n"
MU"v142,v252,v142\n"
MU"v143,v252,v143\n"
PK"v137,v138,v139\n"
PK"v138,v140,v141\n"
PK"v139,v142,v143\n"
BS"v[136:139],v224,s[8:11],s51 offen offset:3072 sc1\n"
RD"a[80:83],v226 offset:4096\n"
RD"a[84:87],v226 offset:5120\n"
RD"a[88:91],v226 offset:6144\n"
".p2align 6\n"
"LP2_%=:\n"
WT"lgkmcnt(6)\n"
MF"v[0:3],a[64:67],a[32:35],0\n"
WT"lgkmcnt(5)\n"
EX"v56,v56\n"
MF"v[0:3],a[68:71],a[36:39],v[0:3]\n"
RD"a[92:95],v226 offset:7168\n"
"v_readlane_b32 s43,v228,s42\n"
MF"v[8:11],a[68:71],a[52:55],0\n"
EX"v57,v57\n"
MF"v[8:11],a[64:67],a[48:51],v[8:11]\n"
WT"lgkmcnt(5)\n"
RD"a[96:99],v226 offset:8192\n"
RD"a[100:103],v226 offset:9216\n"
MF"v[0:3],a[72:75],a[40:43],v[0:3]\n"
WT"lgkmcnt(3)\n"
EX"v58,v58\n"
MF"v[0:3],a[76:79],a[44:47],v[0:3]\n"
"s_mov_b32 m0,s40\n"
RD"a[104:107],v226 offset:10240\n"
"v_readlane_b32 s44,v229,s42\n"
MF"v[8:11],a[76:79],a[60:63],v[8:11]\n"
SA"s41,s41,32768\n"
EX"v59,v59\n"
MF"v[8:11],a[72:75],a[56:59],v[8:11]\n"
"s_cmp_eq_u32 s41,163840\n"
BL"v225,s[8:11],s43 offen offset:0 sc1 lds\n"
MF"v[4:7],a[80:83],a[32:35],0\n"
"s_cselect_b32 s41,0,s41\n"
EX"v60,v60\n"
MF"v[4:7],a[84:87],a[36:39],v[4:7]\n"
SA"s42,s42,1\n"
RD"a[108:111],v226 offset:11264\n"
"v_add_u32 v227,s41,v224\n"
MF"v[12:15],a[84:87],a[52:55],0\n"
EX"v61,v61\n"
MF"v[12:15],a[80:83],a[48:51],v[12:15]\n"
RD"a[112:115],v226 offset:12288\n"
RD"a[116:119],v226 offset:13312\n"
MF"v[4:7],a[88:91],a[40:43],v[4:7]\n"
WT"lgkmcnt(3)\n"
EX"v62,v62\n"
MF"v[4:7],a[92:95],a[44:47],v[4:7]\n"
RD"a[120:123],v226 offset:14336\n"
PK"v76,v56,v57\n"
MF"v[12:15],a[92:95],a[60:63],v[12:15]\n"
EX"v63,v63\n"
MF"v[12:15],a[88:91],a[56:59],v[12:15]\n"
BL"v225,s[8:11],s43 offen offset:1024 sc1 lds\n"
MF"v[16:19],a[96:99],a[32:35],0\n"
EX"v0,v0\n"
MF"v[16:19],a[100:103],a[36:39],v[16:19]\n"
RD"a[124:127],v226 offset:15360\n"
PK"v77,v58,v59\n"
MF"v[24:27],a[100:103],a[52:55],0\n"
EX"v1,v1\n"
MF"v[24:27],a[96:99],a[48:51],v[24:27]\n"
RD"a[64:67],v226 offset:16384\n"
RD"a[68:71],v226 offset:17408\n"
MF"v[16:19],a[104:107],a[40:43],v[16:19]\n"
WT"lgkmcnt(3)\n"
EX"v2,v2\n"
MF"v[16:19],a[108:111],a[44:47],v[16:19]\n"
RD"a[72:75],v226 offset:18432\n"
PK"v78,v60,v61\n"
MF"v[24:27],a[108:111],a[60:63],v[24:27]\n"
EX"v3,v3\n"
MF"v[24:27],a[104:107],a[56:59],v[24:27]\n"
BL"v225,s[8:11],s43 offen offset:2048 sc1 lds\n"
MF"v[20:23],a[112:115],a[32:35],0\n"
EX"v8,v8\n"
MF"v[20:23],a[116:119],a[36:39],v[20:23]\n"
RD"a[76:79],v226 offset:19456\n"
PK"v79,v62,v63\n"
MF"v[28:31],a[116:119],a[52:55],0\n"
EX"v9,v9\n"
MF"v[28:31],a[112:115],a[48:51],v[28:31]\n"
RD"a[80:83],v226 offset:20480\n"
RD"a[84:87],v226 offset:21504\n"
MF"v[20:23],a[120:123],a[40:43],v[20:23]\n"
WT"lgkmcnt(3)\n"
EX"v10,v10\n"
MF"v[20:23],a[124:127],a[44:47],v[20:23]\n"
RD"a[88:91],v226 offset:22528\n"
PK"v0,v0,v1\n"
MF"v[28:31],a[124:127],a[60:63],v[28:31]\n"
EX"v11,v11\n"
MF"v[28:31],a[120:123],a[56:59],v[28:31]\n"
BL"v225,s[8:11],s43 offen offset:3072 sc1 lds\n"
MF"v[32:35],a[64:67],a[32:35],0\n"
EX"v4,v4\n"
MF"v[32:35],a[68:71],a[36:39],v[32:35]\n"
RD"a[92:95],v226 offset:23552\n"
PK"v1,v2,v3\n"
MF"v[40:43],a[68:71],a[52:55],0\n"
EX"v5,v5\n"
MF"v[40:43],a[64:67],a[48:51],v[40:43]\n"
SA"m0,s40,16384\n"
RD"a[96:99],v226 offset:24576\n"
RD"a[100:103],v226 offset:25600\n"
MF"v[32:35],a[72:75],a[40:43],v[32:35]\n"
WT"lgkmcnt(3)\n"
EX"v6,v6\n"
MF"v[32:35],a[76:79],a[44:47],v[32:35]\n"
RD"a[104:107],v226 offset:26624\n"
PK"v8,v8,v9\n"
MF"v[40:43],a[76:79],a[60:63],v[40:43]\n"
EX"v7,v7\n"
MF"v[40:43],a[72:75],a[56:59],v[40:43]\n"
BL"v225,s[8:11],s44 offen offset:0 sc1 lds\n"
MF"v[36:39],a[80:83],a[32:35],0\n"
SA"s40,s40,32768\n"
EX"v12,v12\n"
MF"v[36:39],a[84:87],a[36:39],v[36:39]\n"
"s_cmp_eq_u32 s40,s46\n"
RD"a[108:111],v226 offset:27648\n"
PK"v9,v10,v11\n"
MF"v[44:47],a[84:87],a[52:55],0\n"
"s_cselect_b32 s40,s47,s40\n"
EX"v13,v13\n"
MF"v[44:47],a[80:83],a[48:51],v[44:47]\n"
RD"a[112:115],v226 offset:28672\n"
RD"a[116:119],v226 offset:29696\n"
MF"v[36:39],a[88:91],a[40:43],v[36:39]\n"
WT"lgkmcnt(3)\n"
EX"v14,v14\n"
MF"v[36:39],a[92:95],a[44:47],v[36:39]\n"
RD"a[120:123],v226 offset:30720\n"
PK"v2,v4,v5\n"
MF"v[44:47],a[92:95],a[60:63],v[44:47]\n"
EX"v15,v15\n"
MF"v[44:47],a[88:91],a[56:59],v[44:47]\n"
BL"v225,s[8:11],s44 offen offset:1024 sc1 lds\n"
MF"v[48:51],a[96:99],a[32:35],0\n"
EX"v16,v16\n"
MF"v[48:51],a[100:103],a[36:39],v[48:51]\n"
RD"a[124:127],v226 offset:31744\n"
PK"v3,v6,v7\n"
MF"v[56:59],a[100:103],a[52:55],0\n"
WT"vmcnt(14)\n"
"s_barrier\n"
MF"v[56:59],a[96:99],a[48:51],v[56:59]\n"
RD"a[64:67],v227 offset:0\n"
RD"a[68:71],v227 offset:8192\n"
MF"v[48:51],a[104:107],a[40:43],v[48:51]\n"
WT"lgkmcnt(3)\n"
EX"v17,v17\n"
MF"v[48:51],a[108:111],a[44:47],v[48:51]\n"
RD"a[72:75],v227 offset:1024\n"
PK"v10,v12,v13\n"
MF"v[56:59],a[108:111],a[60:63],v[56:59]\n"
EX"v18,v18\n"
MF"v[56:59],a[104:107],a[56:59],v[56:59]\n"
BL"v225,s[8:11],s44 offen offset:2048 sc1 lds\n"
MF"v[52:55],a[112:115],a[32:35],0\n"
EX"v19,v19\n"
MF"v[52:55],a[116:119],a[36:39],v[52:55]\n"
RD"a[76:79],v227 offset:9216\n"
PK"v11,v14,v15\n"
MF"v[60:63],a[116:119],a[52:55],0\n"
EX"v24,v24\n"
MF"v[60:63],a[112:115],a[48:51],v[60:63]\n"
RD"a[80:83],v227 offset:2048\n"
RD"a[84:87],v227 offset:10240\n"
MF"v[52:55],a[120:123],a[40:43],v[52:55]\n"
WT"lgkmcnt(3)\n"
EX"v25,v25\n"
MF"v[52:55],a[124:127],a[44:47],v[52:55]\n"
RD"a[88:91],v227 offset:3072\n"
PK"v16,v16,v17\n"
MF"v[60:63],a[124:127],a[60:63],v[60:63]\n"
EX"v26,v26\n"
MF"v[60:63],a[120:123],a[56:59],v[60:63]\n"
SA"s41,s41,32768\n"
BL"v225,s[8:11],s44 offen offset:3072 sc1 lds\n"
MF"v[144:147],a[64:67],v[64:67],v[144:147]\n"
"s_cmp_eq_u32 s41,163840\n"
EX"v27,v27\n"
MF"v[144:147],a[68:71],v[72:75],v[144:147]\n"
"s_cselect_b32 s41,0,s41\n"
RD"a[92:95],v227 offset:11264\n"
"v_add_u32 v226,s41,v224\n"
MF"v[176:179],a[68:71],v[76:79],v[176:179]\n"
EX"v20,v20\n"
MF"v[176:179],a[64:67],v[68:71],v[176:179]\n"
"s_mov_b32 m0,s40\n"
RD"a[96:99],v227 offset:4096\n"
RD"a[100:103],v227 offset:12288\n"
MF"v[148:151],a[72:75],v[64:67],v[148:151]\n"
WT"lgkmcnt(3)\n"
EX"v21,v21\n"
MF"v[148:151],a[76:79],v[72:75],v[148:151]\n"
RD"a[104:107],v227 offset:5120\n"
"v_readlane_b32 s43,v228,s42\n"
MF"v[180:183],a[76:79],v[76:79],v[180:183]\n"
EX"v22,v22\n"
MF"v[180:183],a[72:75],v[68:71],v[180:183]\n"
"v_readlane_b32 s44,v229,s42\n"
PK"v17,v18,v19\n"
MF"v[152:155],a[80:83],v[64:67],v[152:155]\n"
SA"s42,s42,1\n"
BL"v225,s[8:11],s43 offen offset:0 sc1 lds\n"
MF"v[152:155],a[84:87],v[72:75],v[152:155]\n"
EX"v23,v23\n"
MF"v[184:187],a[84:87],v[76:79],v[184:187]\n"
RD"a[108:111],v227 offset:13312\n"
PK"v24,v24,v25\n"
MF"v[184:187],a[80:83],v[68:71],v[184:187]\n"
EX"v28,v28\n"
MF"v[156:159],a[88:91],v[64:67],v[156:159]\n"
WT"lgkmcnt(1)\n"
RD"a[112:115],v227 offset:6144\n"
RD"a[116:119],v227 offset:14336\n"
MF"v[156:159],a[92:95],v[72:75],v[156:159]\n"
EX"v29,v29\n"
MF"v[188:191],a[92:95],v[76:79],v[188:191]\n"
RD"a[120:123],v227 offset:7168\n"
PK"v25,v26,v27\n"
MF"v[188:191],a[88:91],v[68:71],v[188:191]\n"
EX"v30,v30\n"
MF"v[160:163],a[96:99],v[64:67],v[160:163]\n"
BL"v225,s[8:11],s43 offen offset:1024 sc1 lds\n"
MF"v[160:163],a[100:103],v[72:75],v[160:163]\n"
EX"v31,v31\n"
MF"v[192:195],a[100:103],v[76:79],v[192:195]\n"
RD"a[124:127],v227 offset:15360\n"
PK"v18,v20,v21\n"
MF"v[192:195],a[96:99],v[68:71],v[192:195]\n"
EX"v32,v32\n"
MF"v[164:167],a[104:107],v[64:67],v[164:167]\n"
WT"lgkmcnt(2)\n"
EX"v33,v33\n"
MF"v[164:167],a[108:111],v[72:75],v[164:167]\n"
RD"a[64:67],v227 offset:16384\n"
PK"v19,v22,v23\n"
MF"v[196:199],a[108:111],v[76:79],v[196:199]\n"
EX"v34,v34\n"
MF"v[196:199],a[104:107],v[68:71],v[196:199]\n"
BL"v225,s[8:11],s43 offen offset:2048 sc1 lds\n"
MF"v[168:171],a[112:115],v[64:67],v[168:171]\n"
EX"v35,v35\n"
MF"v[168:171],a[116:119],v[72:75],v[168:171]\n"
RD"a[68:71],v227 offset:24576\n"
PK"v26,v28,v29\n"
MF"v[200:203],a[116:119],v[76:79],v[200:203]\n"
EX"v40,v40\n"
MF"v[200:203],a[112:115],v[68:71],v[200:203]\n"
WT"lgkmcnt(2)\n"
RD"a[72:75],v227 offset:17408\n"
RD"a[76:79],v227 offset:25600\n"
MF"v[172:175],a[120:123],v[64:67],v[172:175]\n"
EX"v41,v41\n"
MF"v[172:175],a[124:127],v[72:75],v[172:175]\n"
RD"a[80:83],v227 offset:18432\n"
PK"v27,v30,v31\n"
MF"v[204:207],a[124:127],v[76:79],v[204:207]\n"
EX"v42,v42\n"
MF"v[204:207],a[120:123],v[68:71],v[204:207]\n"
BL"v225,s[8:11],s43 offen offset:3072 sc1 lds\n"
MF"a[140:143],a[128:131],v[64:67],a[140:143]\n"
EX"v43,v43\n"
MF"a[140:143],a[128:131],v[72:75],a[140:143]\n"
RD"a[84:87],v227 offset:26624\n"
PK"v64,v32,v33\n"
MF"a[144:147],a[128:131],v[76:79],a[144:147]\n"
EX"v36,v36\n"
MF"a[144:147],a[128:131],v[68:71],a[144:147]\n"
WT"lgkmcnt(2)\n"
RD"a[88:91],v227 offset:19456\n"
RD"a[92:95],v227 offset:27648\n"
MF"v[144:147],a[64:67],v[0:3],v[144:147]\n"
SA"m0,s40,16384\n"
EX"v37,v37\n"
MF"v[144:147],a[68:71],v[16:19],v[144:147]\n"
"buffer_load_dwordx2 v[242:243],v236,s[8:11],0 offen sc1\n"
MF"v[176:179],a[68:71],v[24:27],v[176:179]\n"
RD"a[96:99],v227 offset:20480\n"
PK"v65,v34,v35\n"
MF"v[176:179],a[64:67],v[8:11],v[176:179]\n"
EX"v38,v38\n"
MF"v[148:151],a[72:75],v[0:3],v[148:151]\n"
SA"s40,s40,32768\n"
BL"v225,s[8:11],s44 offen offset:0 sc1 lds\n"
MF"v[148:151],a[76:79],v[16:19],v[148:151]\n"
"s_cmp_eq_u32 s40,s46\n"
EX"v39,v39\n"
MF"v[180:183],a[76:79],v[24:27],v[180:183]\n"
"s_cselect_b32 s40,s47,s40\n"
RD"a[100:103],v227 offset:28672\n"
PK"v68,v40,v41\n"
MF"v[180:183],a[72:75],v[8:11],v[180:183]\n"
WT"lgkmcnt(2)\n"
EX"v44,v44\n"
MF"v[152:155],a[80:83],v[0:3],v[152:155]\n"
RD"a[104:107],v227 offset:21504\n"
RD"a[108:111],v227 offset:29696\n"
MF"v[152:155],a[84:87],v[16:19],v[152:155]\n"
EX"v45,v45\n"
MF"v[184:187],a[84:87],v[24:27],v[184:187]\n"
RD"a[112:115],v227 offset:22528\n"
PK"v69,v42,v43\n"
MF"v[184:187],a[80:83],v[8:11],v[184:187]\n"
EX"v46,v46\n"
MF"v[156:159],a[88:91],v[0:3],v[156:159]\n"
BL"v225,s[8:11],s44 offen offset:1024 sc1 lds\n"
MF"v[156:159],a[92:95],v[16:19],v[156:159]\n"
EX"v47,v47\n"
MF"v[188:191],a[92:95],v[24:27],v[188:191]\n"
RD"a[116:119],v227 offset:30720\n"
PK"v66,v36,v37\n"
MF"v[188:191],a[88:91],v[8:11],v[188:191]\n"
WT"lgkmcnt(2)\n"
EX"v48,v48\n"
MF"v[160:163],a[96:99],v[0:3],v[160:163]\n"
RD"a[120:123],v227 offset:23552\n"
RD"a[124:127],v227 offset:31744\n"
MF"v[160:163],a[100:103],v[16:19],v[160:163]\n"
EX"v49,v49\n"
MF"v[192:195],a[100:103],v[24:27],v[192:195]\n"
EX"v50,v50\n"
MF"v[192:195],a[96:99],v[8:11],v[192:195]\n"
BL"v225,s[8:11],s44 offen offset:2048 sc1 lds\n"
MF"v[164:167],a[104:107],v[0:3],v[164:167]\n"
WT"vmcnt(16)\n"
"s_barrier\n"
MF"v[164:167],a[108:111],v[16:19],v[164:167]\n"
RD"a[64:67],v226 offset:0\n"
PK"v67,v38,v39\n"
MF"v[196:199],a[108:111],v[24:27],v[196:199]\n"
EX"v51,v51\n"
MF"v[196:199],a[104:107],v[8:11],v[196:199]\n"
WT"lgkmcnt(1)\n"
RD"a[68:71],v226 offset:1024\n"
PK"v70,v44,v45\n"
MF"v[168:171],a[112:115],v[0:3],v[168:171]\n"
EX"v52,v52\n"
MF"v[168:171],a[116:119],v[16:19],v[168:171]\n"
RD"a[72:75],v226 offset:2048\n"
PK"v71,v46,v47\n"
MF"v[200:203],a[116:119],v[24:27],v[200:203]\n"
EX"v53,v53\n"
MF"v[200:203],a[112:115],v[8:11],v[200:203]\n"
BL"v225,s[8:11],s44 offen offset:3072 sc1 lds\n"
MF"v[172:175],a[120:123],v[0:3],v[172:175]\n"
EX"v54,v54\n"
MF"v[172:175],a[124:127],v[16:19],v[172:175]\n"
"buffer_store_dwordx2 v[232:233],v230,s[8:11],s57 offen sc1\n"
MF"v[204:207],a[124:127],v[24:27],v[204:207]\n"
"s_mov_b32 s57,2130706432\n"
RD"a[76:79],v226 offset:3072\n"
PK"v72,v48,v49\n"
MF"v[204:207],a[120:123],v[8:11],v[204:207]\n"
EX"v55,v55\n"
MF"a[140:143],a[128:131],v[0:3],a[140:143]\n"
RD"a[80:83],v226 offset:4096\n"
RD"a[84:87],v226 offset:5120\n"
MF"a[140:143],a[128:131],v[16:19],a[140:143]\n"
RD"a[88:91],v226 offset:6144\n"
PK"v73,v50,v51\n"
MF"a[144:147],a[128:131],v[24:27],a[144:147]\n"
PK"v74,v52,v53\n"
PK"v75,v54,v55\n"
MF"a[144:147],a[128:131],v[8:11],a[144:147]\n"
"s_sub_u32 s56,s56,1\n"
"s_cbranch_scc0 LP2_%=\n"
"s_cmp_lt_i32 s22,0\n"
"s_cbranch_scc1 LE2_%=\n"
WT"lgkmcnt(6)\n"
MF"v[0:3],a[64:67],a[32:35],0\n"
WT"lgkmcnt(5)\n"
WT"vmcnt(5)\n"
"v_cmp_eq_u64 vcc,s[28:29],v[242:243]\n"
"s_or_b32 vcc_hi,vcc_hi,-1\n"
"s_cmp_eq_u64 vcc,exec\n"
"s_cbranch_scc0 L1028_%=\n"
"L1029_%=:\n"
MF"v[0:3],a[68:71],a[36:39],v[0:3]\n"
SA"s48,s59,32768\n"
EX"v56,v56\n"
MF"v[8:11],a[68:71],a[52:55],0\n"
"s_and_b32 s48,s48,0x18000\n"
RD"a[92:95],v226 offset:7168\n"
"v_readlane_b32 s43,v228,s42\n"
MF"v[8:11],a[64:67],a[48:51],v[8:11]\n"
WT"lgkmcnt(5)\n"
EX"v57,v57\n"
MF"v[0:3],a[72:75],a[40:43],v[0:3]\n"
WT"lgkmcnt(1)\n"
RD"a[96:99],v226 offset:8192\n"
RD"a[100:103],v226 offset:9216\n"
MF"v[0:3],a[76:79],a[44:47],v[0:3]\n"
"s_andn2_b32 s49,s59,0x18000\n"
EX"v58,v58\n"
MF"v[8:11],a[76:79],a[60:63],v[8:11]\n"
"s_or_b32 s52,s48,s49\n"
RD"a[104:107],v226 offset:10240\n"
"v_readlane_b32 s44,v229,s42\n"
MF"v[8:11],a[72:75],a[56:59],v[8:11]\n"
SA"s53,s52,16384\n"
BL"a[0:3],v224,s[8:11],s52 offen offset:0 sc1\n"
MF"v[4:7],a[80:83],a[32:35],0\n"
"s_mov_b32 m0,s40\n"
EX"v59,v59\n"
MF"v[4:7],a[84:87],a[36:39],v[4:7]\n"
SA"s41,s41,32768\n"
BL"v225,s[8:11],s43 offen offset:0 sc1 lds\n"
MF"v[12:15],a[84:87],a[52:55],0\n"
"s_cmp_eq_u32 s41,163840\n"
EX"v60,v60\n"
MF"v[12:15],a[80:83],a[48:51],v[12:15]\n"
"s_cselect_b32 s41,0,s41\n"
RD"a[108:111],v226 offset:11264\n"
RD"a[112:115],v226 offset:12288\n"
PK"v76,v56,v57\n"
MF"v[4:7],a[88:91],a[40:43],v[4:7]\n"
WT"lgkmcnt(3)\n"
BL"a[4:7],v224,s[8:11],s53 offen offset:0 sc1\n"
EX"v61,v61\n"
MF"v[4:7],a[92:95],a[44:47],v[4:7]\n"
SA"s42,s42,1\n"
RD"a[116:119],v226 offset:13312\n"
RD"a[120:123],v226 offset:14336\n"
"v_add_u32 v227,s41,v224\n"
MF"v[12:15],a[92:95],a[60:63],v[12:15]\n"
EX"v62,v62\n"
MF"v[12:15],a[88:91],a[56:59],v[12:15]\n"
EX"v63,v63\n"
PK"v77,v58,v59\n"
MF"v[16:19],a[96:99],a[32:35],0\n"
BL"a[8:11],v224,s[8:11],s52 offen offset:1024 sc1\n"
MF"v[16:19],a[100:103],a[36:39],v[16:19]\n"
BL"v225,s[8:11],s43 offen offset:1024 sc1 lds\n"
MF"v[24:27],a[100:103],a[52:55],0\n"
EX"v0,v0\n"
MF"v[24:27],a[96:99],a[48:51],v[24:27]\n"
WT"lgkmcnt(2)\n"
RD"a[124:127],v226 offset:15360\n"
RD"a[64:67],v226 offset:16384\n"
PK"v78,v60,v61\n"
MF"v[16:19],a[104:107],a[40:43],v[16:19]\n"
EX"v1,v1\n"
MF"v[16:19],a[108:111],a[44:47],v[16:19]\n"
RD"a[68:71],v226 offset:17408\n"
RD"a[72:75],v226 offset:18432\n"
MF"v[24:27],a[108:111],a[60:63],v[24:27]\n"
BL"a[12:15],v224,s[8:11],s53 offen offset:1024 sc1\n"
EX"v2,v2\n"
MF"v[24:27],a[104:107],a[56:59],v[24:27]\n"
EX"v3,v3\n"
PK"v79,v62,v63\n"
MF"v[20:23],a[112:115],a[32:35],0\n"
WT"lgkmcnt(4)\n"
BL"v225,s[8:11],s43 offen offset:2048 sc1 lds\n"
MF"v[20:23],a[116:119],a[36:39],v[20:23]\n"
EX"v8,v8\n"
MF"v[28:31],a[116:119],a[52:55],0\n"
RD"a[76:79],v226 offset:19456\n"
PK"v0,v0,v1\n"
MF"v[28:31],a[112:115],a[48:51],v[28:31]\n"
BL"a[16:19],v224,s[8:11],s52 offen offset:2048 sc1\n"
MF"v[20:23],a[120:123],a[40:43],v[20:23]\n"
WT"lgkmcnt(1)\n"
EX"v9,v9\n"
MF"v[20:23],a[124:127],a[44:47],v[20:23]\n"
RD"a[80:83],v226 offset:20480\n"
RD"a[84:87],v226 offset:21504\n"
MF"v[28:31],a[124:127],a[60:63],v[28:31]\n"
EX"v10,v10\n"
MF"v[28:31],a[120:123],a[56:59],v[28:31]\n"
RD"a[88:91],v226 offset:22528\n"
PK"v1,v2,v3\n"
MF"v[32:35],a[64:67],a[32:35],0\n"
EX"v11,v11\n"
MF"v[32:35],a[68:71],a[36:39],v[32:35]\n"
BL"a[20:23],v224,s[8:11],s53 offen offset:2048 sc1\n"
BL"v225,s[8:11],s43 offen offset:3072 sc1 lds\n"
MF"v[40:43],a[68:71],a[52:55],0\n"
EX"v4,v4\n"
MF"v[40:43],a[64:67],a[48:51],v[40:43]\n"
SA"m0,s40,16384\n"
RD"a[92:95],v226 offset:23552\n"
RD"a[96:99],v226 offset:24576\n"
PK"v8,v8,v9\n"
MF"v[32:35],a[72:75],a[40:43],v[32:35]\n"
WT"lgkmcnt(3)\n"
EX"v5,v5\n"
MF"v[32:35],a[76:79],a[44:47],v[32:35]\n"
RD"a[100:103],v226 offset:25600\n"
RD"a[104:107],v226 offset:26624\n"
MF"v[40:43],a[76:79],a[60:63],v[40:43]\n"
BL"a[24:27],v224,s[8:11],s52 offen offset:3072 sc1\n"
EX"v6,v6\n"
MF"v[40:43],a[72:75],a[56:59],v[40:43]\n"
EX"v7,v7\n"
PK"v9,v10,v11\n"
MF"v[36:39],a[80:83],a[32:35],0\n"
SA"s40,s40,32768\n"
BL"v225,s[8:11],s44 offen offset:0 sc1 lds\n"
MF"v[36:39],a[84:87],a[36:39],v[36:39]\n"
"s_cmp_eq_u32 s40,s46\n"
EX"v12,v12\n"
MF"v[44:47],a[84:87],a[52:55],0\n"
"s_cselect_b32 s40,s47,s40\n"
BL"a[28:31],v224,s[8:11],s53 offen offset:3072 sc1\n"
MF"v[44:47],a[80:83],a[48:51],v[44:47]\n"
WT"lgkmcnt(2)\n"
RD"a[108:111],v226 offset:27648\n"
RD"a[112:115],v226 offset:28672\n"
PK"v2,v4,v5\n"
MF"v[36:39],a[88:91],a[40:43],v[36:39]\n"
SA"s48,s59,65536\n"
EX"v13,v13\n"
MF"v[36:39],a[92:95],a[44:47],v[36:39]\n"
"s_and_b32 s48,s48,0x18000\n"
RD"a[116:119],v226 offset:29696\n"
RD"a[120:123],v226 offset:30720\n"
MF"v[44:47],a[92:95],a[60:63],v[44:47]\n"
"s_andn2_b32 s49,s59,0x18000\n"
EX"v14,v14\n"
MF"v[44:47],a[88:91],a[56:59],v[44:47]\n"
"s_or_b32 s52,s48,s49\n"
EX"v15,v15\n"
PK"v3,v6,v7\n"
MF"v[48:51],a[96:99],a[32:35],0\n"
WT"lgkmcnt(4)\n"
BL"a[180:183],v224,s[8:11],s52 offen offset:0 sc1\n"
MF"v[48:51],a[100:103],a[36:39],v[48:51]\n"
SA"s53,s52,16384\n"
BL"v225,s[8:11],s44 offen offset:1024 sc1 lds\n"
MF"v[56:59],a[100:103],a[52:55],0\n"
EX"v16,v16\n"
MF"v[56:59],a[96:99],a[48:51],v[56:59]\n"
RD"a[124:127],v226 offset:31744\n"
PK"v10,v12,v13\n"
MF"v[48:51],a[104:107],a[40:43],v[48:51]\n"
WT"lgkmcnt(1)\n"
"s_barrier\n"
MF"v[48:51],a[108:111],a[44:47],v[48:51]\n"
RD"a[64:67],v227 offset:0\n"
RD"a[68:71],v227 offset:8192\n"
MF"v[56:59],a[108:111],a[60:63],v[56:59]\n"
BL"a[184:187],v224,s[8:11],s53 offen offset:0 sc1\n"
EX"v17,v17\n"
MF"v[56:59],a[104:107],a[56:59],v[56:59]\n"
RD"a[72:75],v227 offset:1024\n"
PK"v11,v14,v15\n"
MF"v[52:55],a[112:115],a[32:35],0\n"
EX"v18,v18\n"
MF"v[52:55],a[116:119],a[36:39],v[52:55]\n"
BL"v225,s[8:11],s44 offen offset:2048 sc1 lds\n"
MF"v[60:63],a[116:119],a[52:55],0\n"
EX"v19,v19\n"
MF"v[60:63],a[112:115],a[48:51],v[60:63]\n"
BL"a[188:191],v224,s[8:11],s52 offen offset:1024 sc1\n"
RD"a[76:79],v227 offset:9216\n"
MF"v[52:55],a[120:123],a[40:43],v[52:55]\n"
WT"lgkmcnt(2)\n"
EX"v24,v24\n"
MF"v[52:55],a[124:127],a[44:47],v[52:55]\n"
RD"a[80:83],v227 offset:2048\n"
RD"a[84:87],v227 offset:10240\n"
PK"v16,v16,v17\n"
MF"v[60:63],a[124:127],a[60:63],v[60:63]\n"
EX"v25,v25\n"
MF"v[60:63],a[120:123],a[56:59],v[60:63]\n"
SA"s41,s41,32768\n"
RD"a[88:91],v227 offset:3072\n"
"v_readlane_b32 s43,v228,s42\n"
MF"v[144:147],a[64:67],v[64:67],v[144:147]\n"
"s_cmp_eq_u32 s41,163840\n"
BL"a[192:195],v224,s[8:11],s53 offen offset:1024 sc1\n"
EX"v26,v26\n"
MF"v[144:147],a[68:71],v[72:75],v[144:147]\n"
"s_cselect_b32 s41,0,s41\n"
BL"v225,s[8:11],s44 offen offset:3072 sc1 lds\n"
PK"v17,v18,v19\n"
MF"v[176:179],a[68:71],v[76:79],v[176:179]\n"
EX"v27,v27\n"
MF"v[176:179],a[64:67],v[68:71],v[176:179]\n"
WT"lgkmcnt(3)\n"
RD"a[92:95],v227 offset:11264\n"
RD"a[96:99],v227 offset:4096\n"
MF"v[148:151],a[72:75],v[64:67],v[148:151]\n"
"s_mov_b32 m0,s40\n"
EX"v20,v20\n"
MF"v[148:151],a[76:79],v[72:75],v[148:151]\n"
RD"a[100:103],v227 offset:12288\n"
RD"a[104:107],v227 offset:5120\n"
PK"v24,v24,v25\n"
MF"v[180:183],a[76:79],v[76:79],v[180:183]\n"
BL"a[196:199],v224,s[8:11],s52 offen offset:2048 sc1\n"
EX"v21,v21\n"
MF"v[180:183],a[72:75],v[68:71],v[180:183]\n"
WT"lgkmcnt(4)\n"
EX"v22,v22\n"
"v_add_u32 v226,s41,v224\n"
MF"v[152:155],a[80:83],v[64:67],v[152:155]\n"
BL"v225,s[8:11],s43 offen offset:0 sc1 lds\n"
MF"v[152:155],a[84:87],v[72:75],v[152:155]\n"
EX"v23,v23\n"
PK"v25,v26,v27\n"
MF"v[184:187],a[84:87],v[76:79],v[184:187]\n"
BL"a[200:203],v224,s[8:11],s53 offen offset:2048 sc1\n"
MF"v[184:187],a[80:83],v[68:71],v[184:187]\n"
RD"a[108:111],v227 offset:13312\n"
RD"a[112:115],v227 offset:6144\n"
MF"v[156:159],a[88:91],v[64:67],v[156:159]\n"
WT"lgkmcnt(2)\n"
EX"v28,v28\n"
MF"v[156:159],a[92:95],v[72:75],v[156:159]\n"
RD"a[116:119],v227 offset:14336\n"
RD"a[120:123],v227 offset:7168\n"
"v_readlane_b32 s44,v229,s42\n"
MF"v[188:191],a[92:95],v[76:79],v[188:191]\n"
SA"s42,s42,1\n"
EX"v29,v29\n"
PK"v18,v20,v21\n"
MF"v[188:191],a[88:91],v[68:71],v[188:191]\n"
BL"a[204:207],v224,s[8:11],s52 offen offset:3072 sc1\n"
MF"v[160:163],a[96:99],v[64:67],v[160:163]\n"
EX"v30,v30\n"
MF"v[160:163],a[100:103],v[72:75],v[160:163]\n"
BL"v225,s[8:11],s43 offen offset:1024 sc1 lds\n"
PK"v19,v22,v23\n"
MF"v[192:195],a[100:103],v[76:79],v[192:195]\n"
EX"v31,v31\n"
MF"v[192:195],a[96:99],v[68:71],v[192:195]\n"
RD"a[124:127],v227 offset:15360\n"
PK"v26,v28,v29\n"
MF"v[164:167],a[104:107],v[64:67],v[164:167]\n"
WT"lgkmcnt(1)\n"
EX"v32,v32\n"
MF"v[164:167],a[108:111],v[72:75],v[164:167]\n"
SA"s48,s59,98304\n"
BL"a[208:211],v224,s[8:11],s53 offen offset:3072 sc1\n"
MF"v[196:199],a[108:111],v[76:79],v[196:199]\n"
"s_and_b32 s48,s48,0x18000\n"
EX"v33,v33\n"
MF"v[196:199],a[104:107],v[68:71],v[196:199]\n"
"s_andn2_b32 s49,s59,0x18000\n"
RD"a[64:67],v227 offset:16384\n"
PK"v27,v30,v31\n"
MF"v[168:171],a[112:115],v[64:67],v[168:171]\n"
"s_or_b32 s52,s48,s49\n"
EX"v34,v34\n"
MF"v[168:171],a[116:119],v[72:75],v[168:171]\n"
SA"s53,s52,16384\n"
BL"v225,s[8:11],s43 offen offset:2048 sc1 lds\n"
MF"v[200:203],a[116:119],v[76:79],v[200:203]\n"
EX"v35,v35\n"
MF"v[200:203],a[112:115],v[68:71],v[200:203]\n"
BL"a[212:215],v224,s[8:11],s52 offen offset:0 sc1\n"
RD"a[68:71],v227 offset:24576\n"
MF"v[172:175],a[120:123],v[64:67],v[172:175]\n"
WT"lgkmcnt(2)\n"
EX"v40,v40\n"
MF"v[172:175],a[124:127],v[72:75],v[172:175]\n"
RD"a[72:75],v227 offset:17408\n"
RD"a[76:79],v227 offset:25600\n"
MF"v[204:207],a[124:127],v[76:79],v[204:207]\n"
EX"v41,v41\n"
MF"v[204:207],a[120:123],v[68:71],v[204:207]\n"
RD"a[80:83],v227 offset:18432\n"
MF"a[140:143],a[128:131],v[64:67],a[140:143]\n"
BL"a[216:219],v224,s[8:11],s53 offen offset:0 sc1\n"
EX"v42,v42\n"
MF"a[140:143],a[128:131],v[72:75],a[140:143]\n"
BL"v225,s[8:11],s43 offen offset:3072 sc1 lds\n"
PK"v64,v32,v33\n"
MF"a[144:147],a[128:131],v[76:79],a[144:147]\n"
EX"v43,v43\n"
PK"v65,v34,v35\n"
MF"a[144:147],a[128:131],v[68:71],a[144:147]\n"
WT"lgkmcnt(3)\n"
RD"a[84:87],v227 offset:26624\n"
RD"a[88:91],v227 offset:19456\n"
MF"v[144:147],a[64:67],v[0:3],v[144:147]\n"
SA"m0,s40,16384\n"
EX"v36,v36\n"
MF"v[144:147],a[68:71],v[16:19],v[144:147]\n"
BL"a[220:223],v224,s[8:11],s52 offen offset:1024 sc1\n"
RD"a[92:95],v227 offset:27648\n"
MF"v[176:179],a[68:71],v[24:27],v[176:179]\n"
"buffer_load_dwordx2 v[242:243],v236,s[8:11],0 offen sc1\n"
EX"v37,v37\n"
MF"v[176:179],a[64:67],v[8:11],v[176:179]\n"
WT"lgkmcnt(3)\n"
RD"a[96:99],v227 offset:20480\n"
PK"v68,v40,v41\n"
MF"v[148:151],a[72:75],v[0:3],v[148:151]\n"
SA"s40,s40,32768\n"
EX"v38,v38\n"
MF"v[148:151],a[76:79],v[16:19],v[148:151]\n"
"s_cmp_eq_u32 s40,s46\n"
BL"v225,s[8:11],s44 offen offset:0 sc1 lds\n"
PK"v69,v42,v43\n"
MF"v[180:183],a[76:79],v[24:27],v[180:183]\n"
"s_cselect_b32 s40,s47,s40\n"
BL"a[224:227],v224,s[8:11],s53 offen offset:1024 sc1\n"
EX"v39,v39\n"
MF"v[180:183],a[72:75],v[8:11],v[180:183]\n"
RD"a[100:103],v227 offset:28672\n"
RD"a[104:107],v227 offset:21504\n"
MF"v[152:155],a[80:83],v[0:3],v[152:155]\n"
WT"lgkmcnt(3)\n"
EX"v44,v44\n"
MF"v[152:155],a[84:87],v[16:19],v[152:155]\n"
RD"a[108:111],v227 offset:29696\n"
RD"a[112:115],v227 offset:22528\n"
PK"v66,v36,v37\n"
MF"v[184:187],a[84:87],v[24:27],v[184:187]\n"
EX"v45,v45\n"
MF"v[184:187],a[80:83],v[8:11],v[184:187]\n"
BL"a[228:231],v224,s[8:11],s52 offen offset:2048 sc1\n"
MF"v[156:159],a[88:91],v[0:3],v[156:159]\n"
EX"v46,v46\n"
MF"v[156:159],a[92:95],v[16:19],v[156:159]\n"
BL"v225,s[8:11],s44 offen offset:1024 sc1 lds\n"
PK"v67,v38,v39\n"
MF"v[188:191],a[92:95],v[24:27],v[188:191]\n"
EX"v47,v47\n"
MF"v[188:191],a[88:91],v[8:11],v[188:191]\n"
WT"lgkmcnt(2)\n"
RD"a[116:119],v227 offset:30720\n"
RD"a[120:123],v227 offset:23552\n"
MF"v[160:163],a[96:99],v[0:3],v[160:163]\n"
EX"v48,v48\n"
MF"v[160:163],a[100:103],v[16:19],v[160:163]\n"
BL"a[232:235],v224,s[8:11],s53 offen offset:2048 sc1\n"
RD"a[124:127],v227 offset:31744\n"
MF"v[192:195],a[100:103],v[24:27],v[192:195]\n"
EX"v49,v49\n"
PK"v70,v44,v45\n"
MF"v[192:195],a[96:99],v[8:11],v[192:195]\n"
EX"v50,v50\n"
MF"v[164:167],a[104:107],v[0:3],v[164:167]\n"
WT"lgkmcnt(3)\n"
BL"v225,s[8:11],s44 offen offset:2048 sc1 lds\n"
MF"v[164:167],a[108:111],v[16:19],v[164:167]\n"
BL"a[236:239],v224,s[8:11],s52 offen offset:3072 sc1\n"
PK"v71,v46,v47\n"
MF"v[196:199],a[108:111],v[24:27],v[196:199]\n"
WT"vmcnt(40)\n"
"s_barrier\n"
MF"v[196:199],a[104:107],v[8:11],v[196:199]\n"
RD"a[64:67],v226 offset:0\n"
RD"a[68:71],v226 offset:1024\n"
MF"v[168:171],a[112:115],v[0:3],v[168:171]\n"
WT"lgkmcnt(2)\n"
EX"v51,v51\n"
MF"v[168:171],a[116:119],v[16:19],v[168:171]\n"
EX"v52,v52\n"
PK"v72,v48,v49\n"
MF"v[200:203],a[116:119],v[24:27],v[200:203]\n"
BL"a[240:243],v224,s[8:11],s53 offen offset:3072 sc1\n"
MF"v[200:203],a[112:115],v[8:11],v[200:203]\n"
RD"a[72:75],v226 offset:2048\n"
PK"v73,v50,v51\n"
MF"v[172:175],a[120:123],v[0:3],v[172:175]\n"
EX"v53,v53\n"
MF"v[172:175],a[124:127],v[16:19],v[172:175]\n"
BL"v225,s[8:11],s44 offen offset:3072 sc1 lds\n"
MF"v[204:207],a[124:127],v[24:27],v[204:207]\n"
EX"v54,v54\n"
MF"v[204:207],a[120:123],v[8:11],v[204:207]\n"
RD"a[76:79],v226 offset:3072\n"
RD"a[80:83],v226 offset:4096\n"
MF"a[140:143],a[128:131],v[0:3],a[140:143]\n"
EX"v55,v55\n"
MF"a[140:143],a[128:131],v[16:19],a[140:143]\n"
RD"a[84:87],v226 offset:5120\n"
RD"a[88:91],v226 offset:6144\n"
MF"a[144:147],a[128:131],v[24:27],a[144:147]\n"
PK"v74,v52,v53\n"
PK"v75,v54,v55\n"
MF"a[144:147],a[128:131],v[8:11],a[144:147]\n"
"s_sub_u32 s56,s22,1\n"
"s_mov_b32 s22,-1\n"
"s_cbranch_scc0 LP2_%=\n"
"LE2_%=:\n"
WT"lgkmcnt(6)\n"
MF"v[0:3],a[64:67],a[32:35],v[208:211]\n"
WT"lgkmcnt(5)\n"
WT"vmcnt(5)\n"
"v_cmp_eq_u64 vcc,s[28:29],v[242:243]\n"
"s_or_b32 vcc_lo,vcc_lo,-1\n"
"s_cmp_eq_u64 vcc,exec\n"
"s_cbranch_scc0 L1031_%=\n"
"L1032_%=:\n"
MF"v[0:3],a[68:71],a[36:39],v[0:3]\n"
"s_mov_b32 m0,s40\n"
EX"v56,v56\n"
MF"v[8:11],a[68:71],a[52:55],0\n"
EX"v57,v57\n"
MF"v[8:11],a[64:67],a[48:51],v[8:11]\n"
WT"lgkmcnt(4)\n"
RD"a[92:95],v226 offset:7168\n"
RD"a[96:99],v226 offset:8192\n"
"v_readlane_b32 s43,v228,s42\n"
MF"v[0:3],a[72:75],a[40:43],v[0:3]\n"
WT"lgkmcnt(2)\n"
EX"v58,v58\n"
"v_readlane_b32 s44,v229,s42\n"
MF"v[0:3],a[76:79],a[44:47],v[0:3]\n"
SA"s42,s42,1\n"
BL"v225,s[8:11],s43 offen offset:0 sc1 lds\n"
RD"a[100:103],v226 offset:9216\n"
MF"v[8:11],a[76:79],a[60:63],v[8:11]\n"
SA"s41,s41,32768\n"
EX"v59,v59\n"
PK"v76,v56,v57\n"
MF"v[8:11],a[72:75],a[56:59],v[8:11]\n"
"s_cmp_eq_u32 s41,163840\n"
RD"a[104:107],v226 offset:10240\n"
PK"v77,v58,v59\n"
MF"v[4:7],a[80:83],a[32:35],v[212:215]\n"
"s_cselect_b32 s41,0,s41\n"
EX"v60,v60\n"
"v_add_u32 v227,s41,v224\n"
MF"v[4:7],a[84:87],a[36:39],v[4:7]\n"
BL"v225,s[8:11],s43 offen offset:1024 sc1 lds\n"
MF"v[12:15],a[84:87],a[52:55],0\n"
EX"v61,v61\n"
MF"v[12:15],a[80:83],a[48:51],v[12:15]\n"
RD"a[108:111],v226 offset:11264\n"
RD"a[112:115],v226 offset:12288\n"
MF"v[4:7],a[88:91],a[40:43],v[4:7]\n"
WT"lgkmcnt(3)\n"
EX"v62,v62\n"
MF"v[4:7],a[92:95],a[44:47],v[4:7]\n"
RD"a[116:119],v226 offset:13312\n"
RD"a[120:123],v226 offset:14336\n"
PK"v78,v60,v61\n"
MF"v[12:15],a[92:95],a[60:63],v[12:15]\n"
EX"v63,v63\n"
MF"v[12:15],a[88:91],a[56:59],v[12:15]\n"
BL"v225,s[8:11],s43 offen offset:2048 sc1 lds\n"
MF"v[16:19],a[96:99],a[32:35],v[216:219]\n"
EX"v0,v0\n"
MF"v[16:19],a[100:103],a[36:39],v[16:19]\n"
RD"a[124:127],v226 offset:15360\n"
RD"a[64:67],v226 offset:16384\n"
PK"v79,v62,v63\n"
MF"v[24:27],a[100:103],a[52:55],0\n"
EX"v1,v1\n"
MF"v[24:27],a[96:99],a[48:51],v[24:27]\n"
WT"lgkmcnt(4)\n"
RD"a[68:71],v226 offset:17408\n"
RD"a[72:75],v226 offset:18432\n"
MF"v[16:19],a[104:107],a[40:43],v[16:19]\n"
BL"v225,s[8:11],s43 offen offset:3072 sc1 lds\n"
MF"v[16:19],a[108:111],a[44:47],v[16:19]\n"
SA"m0,s40,16384\n"
EX"v2,v2\n"
PK"v0,v0,v1\n"
MF"v[24:27],a[108:111],a[60:63],v[24:27]\n"
RD"a[76:79],v226 offset:19456\n"
RD"a[80:83],v226 offset:20480\n"
MF"v[24:27],a[104:107],a[56:59],v[24:27]\n"
EX"v3,v3\n"
MF"v[20:23],a[112:115],a[32:35],v[220:223]\n"
WT"lgkmcnt(4)\n"
RD"a[84:87],v226 offset:21504\n"
RD"a[88:91],v226 offset:22528\n"
MF"v[20:23],a[116:119],a[36:39],v[20:23]\n"
EX"v8,v8\n"
PK"v1,v2,v3\n"
MF"v[28:31],a[116:119],a[52:55],0\n"
SA"s40,s40,32768\n"
BL"v225,s[8:11],s44 offen offset:0 sc1 lds\n"
MF"v[28:31],a[112:115],a[48:51],v[28:31]\n"
"s_cmp_eq_u32 s40,s46\n"
RD"a[92:95],v226 offset:23552\n"
RD"a[96:99],v226 offset:24576\n"
EX"v9,v9\n"
MF"v[20:23],a[120:123],a[40:43],v[20:23]\n"
"s_cselect_b32 s40,s47,s40\n"
EX"v10,v10\n"
MF"v[20:23],a[124:127],a[44:47],v[20:23]\n"
RD"a[100:103],v226 offset:25600\n"
RD"a[104:107],v226 offset:26624\n"
PK"v8,v8,v9\n"
MF"v[28:31],a[124:127],a[60:63],v[28:31]\n"
RD"a[108:111],v226 offset:27648\n"
RD"a[112:115],v226 offset:28672\n"
EX"v11,v11\n"
MF"v[28:31],a[120:123],a[56:59],v[28:31]\n"
BL"v225,s[8:11],s44 offen offset:1024 sc1 lds\n"
MF"v[40:43],a[64:67],a[48:51],v[208:211]\n"
WT"lgkmcnt(6)\n"
RD"a[116:119],v226 offset:29696\n"
RD"a[120:123],v226 offset:30720\n"
EX"v4,v4\n"
MF"v[40:43],a[68:71],a[52:55],v[40:43]\n"
EX"v5,v5\n"
PK"v9,v10,v11\n"
MF"v[40:43],a[72:75],a[56:59],v[40:43]\n"
RD"a[124:127],v226 offset:31744\n"
PK"v2,v4,v5\n"
MF"v[40:43],a[76:79],a[60:63],v[40:43]\n"
EX"v6,v6\n"
MF"v[44:47],a[80:83],a[48:51],v[212:215]\n"
"s_barrier\n"
MF"v[44:47],a[84:87],a[52:55],v[44:47]\n"
RD"a[64:67],v227 offset:0\n"
RD"a[68:71],v227 offset:8192\n"
MF"v[44:47],a[88:91],a[56:59],v[44:47]\n"
WT"lgkmcnt(5)\n"
BL"v225,s[8:11],s44 offen offset:2048 sc1 lds\n"
EX"v7,v7\n"
MF"v[44:47],a[92:95],a[60:63],v[44:47]\n"
RD"a[72:75],v227 offset:1024\n"
"v_readlane_b32 s43,v228,s42\n"
MF"v[56:59],a[96:99],a[48:51],v[216:219]\n"
EX"v12,v12\n"
PK"v3,v6,v7\n"
MF"v[56:59],a[100:103],a[52:55],v[56:59]\n"
EX"v13,v13\n"
MF"v[56:59],a[104:107],a[56:59],v[56:59]\n"
BL"v225,s[8:11],s44 offen offset:3072 sc1 lds\n"
MF"v[56:59],a[108:111],a[60:63],v[56:59]\n"
"s_mov_b32 m0,s40\n"
RD"a[76:79],v227 offset:9216\n"
RD"a[80:83],v227 offset:2048\n"
MF"v[60:63],a[112:115],a[48:51],v[220:223]\n"
WT"lgkmcnt(3)\n"
EX"v14,v14\n"
PK"v10,v12,v13\n"
MF"v[60:63],a[116:119],a[52:55],v[60:63]\n"
RD"a[84:87],v227 offset:10240\n"
RD"a[88:91],v227 offset:3072\n"
MF"v[60:63],a[120:123],a[56:59],v[60:63]\n"
EX"v15,v15\n"
MF"v[60:63],a[124:127],a[60:63],v[60:63]\n"
SA"s41,s41,32768\n"
EX"v16,v16\n"
MF"v[144:147],a[64:67],v[64:67],v[144:147]\n"
"s_cmp_eq_u32 s41,163840\n"
BL"v225,s[8:11],s43 offen offset:0 sc1 lds\n"
"v_readlane_b32 s44,v229,s42\n"
MF"v[144:147],a[68:71],v[72:75],v[144:147]\n"
"s_cselect_b32 s41,0,s41\n"
EX"v17,v17\n"
PK"v11,v14,v15\n"
MF"v[176:179],a[68:71],v[76:79],v[176:179]\n"
SA"s42,s42,1\n"
RD"a[92:95],v227 offset:11264\n"
"v_add_u32 v226,s41,v224\n"
MF"v[176:179],a[64:67],v[68:71],v[176:179]\n"
WT"lgkmcnt(3)\n"
EX"v18,v18\n"
MF"v[148:151],a[72:75],v[64:67],v[148:151]\n"
RD"a[96:99],v227 offset:4096\n"
RD"a[100:103],v227 offset:12288\n"
PK"v16,v16,v17\n"
MF"v[148:151],a[76:79],v[72:75],v[148:151]\n"
EX"v19,v19\n"
MF"v[180:183],a[76:79],v[76:79],v[180:183]\n"
BL"v225,s[8:11],s43 offen offset:1024 sc1 lds\n"
MF"v[180:183],a[72:75],v[68:71],v[180:183]\n"
RD"a[104:107],v227 offset:5120\n"
PK"v17,v18,v19\n"
MF"v[152:155],a[80:83],v[64:67],v[152:155]\n"
WT"lgkmcnt(4)\n"
EX"v24,v24\n"
MF"v[152:155],a[84:87],v[72:75],v[152:155]\n"
EX"v25,v25\n"
MF"v[184:187],a[84:87],v[76:79],v[184:187]\n"
EX"v26,v26\n"
MF"v[184:187],a[80:83],v[68:71],v[184:187]\n"
RD"a[108:111],v227 offset:13312\n"
RD"a[112:115],v227 offset:6144\n"
PK"v24,v24,v25\n"
MF"v[156:159],a[88:91],v[64:67],v[156:159]\n"
WT"lgkmcnt(3)\n"
BL"v225,s[8:11],s43 offen offset:2048 sc1 lds\n"
MF"v[156:159],a[92:95],v[72:75],v[156:159]\n"
EX"v27,v27\n"
MF"v[188:191],a[92:95],v[76:79],v[188:191]\n"
RD"a[116:119],v227 offset:14336\n"
RD"a[120:123],v227 offset:7168\n"
MF"v[188:191],a[88:91],v[68:71],v[188:191]\n"
EX"v20,v20\n"
PK"v25,v26,v27\n"
MF"v[160:163],a[96:99],v[64:67],v[160:163]\n"
EX"v21,v21\n"
MF"v[160:163],a[100:103],v[72:75],v[160:163]\n"
BL"v225,s[8:11],s43 offen offset:3072 sc1 lds\n"
MF"v[192:195],a[100:103],v[76:79],v[192:195]\n"
SA"m0,s40,16384\n"
EX"v22,v22\n"
MF"v[192:195],a[96:99],v[68:71],v[192:195]\n"
WT"lgkmcnt(2)\n"
RD"a[124:127],v227 offset:15360\n"
PK"v18,v20,v21\n"
MF"v[164:167],a[104:107],v[64:67],v[164:167]\n"
EX"v23,v23\n"
MF"v[164:167],a[108:111],v[72:75],v[164:167]\n"
EX"v28,v28\n"
MF"v[196:199],a[108:111],v[76:79],v[196:199]\n"
SA"s40,s40,32768\n"
BL"v225,s[8:11],s44 offen offset:0 sc1 lds\n"
MF"v[196:199],a[104:107],v[68:71],v[196:199]\n"
"s_cmp_eq_u32 s40,s46\n"
RD"a[64:67],v227 offset:16384\n"
PK"v19,v22,v23\n"
MF"v[168:171],a[112:115],v[64:67],v[168:171]\n"
WT"lgkmcnt(2)\n"
EX"v29,v29\n"
MF"v[168:171],a[116:119],v[72:75],v[168:171]\n"
"s_cselect_b32 s40,s47,s40\n"
EX"v30,v30\n"
MF"v[200:203],a[116:119],v[76:79],v[200:203]\n"
RD"a[68:71],v227 offset:24576\n"
PK"v26,v28,v29\n"
MF"v[200:203],a[112:115],v[68:71],v[200:203]\n"
BL"v225,s[8:11],s44 offen offset:1024 sc1 lds\n"
MF"v[172:175],a[120:123],v[64:67],v[172:175]\n"
WT"lgkmcnt(2)\n"
EX"v31,v31\n"
MF"v[172:175],a[124:127],v[72:75],v[172:175]\n"
RD"a[72:75],v227 offset:17408\n"
RD"a[76:79],v227 offset:25600\n"
MF"v[204:207],a[124:127],v[76:79],v[204:207]\n"
EX"v40,v40\n"
MF"v[204:207],a[120:123],v[68:71],v[204:207]\n"
RD"a[80:83],v227 offset:18432\n"
PK"v27,v30,v31\n"
MF"a[140:143],a[128:131],v[64:67],a[140:143]\n"
EX"v41,v41\n"
MF"a[140:143],a[128:131],v[72:75],a[140:143]\n"
BL"v225,s[8:11],s44 offen offset:2048 sc1 lds\n"
MF"a[144:147],a[128:131],v[76:79],a[144:147]\n"
EX"v42,v42\n"
MF"a[144:147],a[128:131],v[68:71],a[144:147]\n"
WT"lgkmcnt(3)\n"
RD"a[84:87],v227 offset:26624\n"
RD"a[88:91],v227 offset:19456\n"
PK"v68,v40,v41\n"
MF"v[144:147],a[64:67],v[0:3],v[144:147]\n"
EX"v43,v43\n"
MF"v[144:147],a[68:71],v[16:19],v[144:147]\n"
RD"a[92:95],v227 offset:27648\n"
RD"a[96:99],v227 offset:20480\n"
MF"v[176:179],a[68:71],v[24:27],v[176:179]\n"
EX"v44,v44\n"
"v_readlane_b32 s43,v228,s42\n"
MF"v[176:179],a[64:67],v[8:11],v[176:179]\n"
WT"lgkmcnt(4)\n"
BL"v225,s[8:11],s44 offen offset:3072 sc1 lds\n"
PK"v69,v42,v43\n"
MF"v[148:151],a[72:75],v[0:3],v[148:151]\n"
"s_mov_b32 m0,s40\n"
EX"v45,v45\n"
MF"v[148:151],a[76:79],v[16:19],v[148:151]\n"
EX"v46,v46\n"
MF"v[180:183],a[76:79],v[24:27],v[180:183]\n"
RD"a[100:103],v227 offset:28672\n"
"v_readlane_b32 s44,v229,s42\n"
PK"v70,v44,v45\n"
MF"v[180:183],a[72:75],v[8:11],v[180:183]\n"
SA"s42,s42,1\n"
EX"v47,v47\n"
MF"v[152:155],a[80:83],v[0:3],v[152:155]\n"
WT"lgkmcnt(1)\n"
RD"a[104:107],v227 offset:21504\n"
RD"a[108:111],v227 offset:29696\n"
MF"v[152:155],a[84:87],v[16:19],v[152:155]\n"
EX"v56,v56\n"
MF"v[184:187],a[84:87],v[24:27],v[184:187]\n"
RD"a[112:115],v227 offset:22528\n"
PK"v71,v46,v47\n"
MF"v[184:187],a[80:83],v[8:11],v[184:187]\n"
EX"v57,v57\n"
MF"v[156:159],a[88:91],v[0:3],v[156:159]\n"
EX"v58,v58\n"
MF"v[156:159],a[92:95],v[16:19],v[156:159]\n"
RD"a[116:119],v227 offset:30720\n"
PK"v76,v56,v57\n"
MF"v[188:191],a[92:95],v[24:27],v[188:191]\n"
EX"v59,v59\n"
MF"v[188:191],a[88:91],v[8:11],v[188:191]\n"
RD"a[120:123],v227 offset:23552\n"
RD"a[124:127],v227 offset:31744\n"
MF"v[160:163],a[96:99],v[0:3],v[160:163]\n"
WT"lgkmcnt(4)\n"
EX"v60,v60\n"
MF"v[160:163],a[100:103],v[16:19],v[160:163]\n"
PK"v77,v58,v59\n"
MF"v[192:195],a[100:103],v[24:27],v[192:195]\n"
EX"v61,v61\n"
MF"v[192:195],a[96:99],v[8:11],v[192:195]\n"
PK"v78,v60,v61\n"
MF"v[164:167],a[104:107],v[0:3],v[164:167]\n"
EX"v62,v62\n"
MF"v[164:167],a[108:111],v[16:19],v[164:167]\n"
SA"s41,s41,32768\n"
WT"vmcnt(16)\n"
"s_barrier\n"
MF"v[196:199],a[108:111],v[24:27],v[196:199]\n"
"s_cmp_eq_u32 s41,163840\n"
BL"v225,s[8:11],s43 offen offset:0 sc1 lds\n"
BL"v225,s[8:11],s43 offen offset:1024 sc1 lds\n"
MF"v[196:199],a[104:107],v[8:11],v[196:199]\n"
WT"lgkmcnt(0)\n"
BL"v225,s[8:11],s43 offen offset:2048 sc1 lds\n"
RD"a[64:67],v226 offset:0\n"
MF"v[168:171],a[112:115],v[0:3],v[168:171]\n"
"s_cselect_b32 s41,0,s41\n"
EX"v63,v63\n"
"v_add_u32 v227,s41,v224\n"
MF"v[168:171],a[116:119],v[16:19],v[168:171]\n"
RD"a[68:71],v226 offset:8192\n"
RD"a[72:75],v226 offset:1024\n"
BL"v225,s[8:11],s43 offen offset:3072 sc1 lds\n"
MF"v[200:203],a[116:119],v[24:27],v[200:203]\n"
SA"m0,s40,16384\n"
RD"a[76:79],v226 offset:9216\n"
RD"a[80:83],v226 offset:2048\n"
PK"v79,v62,v63\n"
MF"v[200:203],a[112:115],v[8:11],v[200:203]\n"
RD"a[84:87],v226 offset:10240\n"
MF"v[172:175],a[120:123],v[0:3],v[172:175]\n"
RD"a[88:91],v226 offset:3072\n"
MF"v[172:175],a[124:127],v[16:19],v[172:175]\n"
BL"v225,s[8:11],s44 offen offset:0 sc1 lds\n"
MF"v[204:207],a[124:127],v[24:27],v[204:207]\n"
SA"s40,s40,32768\n"
RD"a[92:95],v226 offset:11264\n"
RD"a[96:99],v226 offset:4096\n"
MF"v[204:207],a[120:123],v[8:11],v[204:207]\n"
"s_cmp_eq_u32 s40,s46\n"
RD"a[100:103],v226 offset:12288\n"
RD"a[104:107],v226 offset:5120\n"
MF"a[140:143],a[128:131],v[0:3],a[140:143]\n"
"s_cselect_b32 s40,s47,s40\n"
RD"a[108:111],v226 offset:13312\n"
MF"a[140:143],a[128:131],v[16:19],a[140:143]\n"
RD"a[112:115],v226 offset:6144\n"
MF"a[144:147],a[128:131],v[24:27],a[144:147]\n"
BL"v225,s[8:11],s44 offen offset:1024 sc1 lds\n"
MF"a[144:147],a[128:131],v[8:11],a[144:147]\n"
WT"lgkmcnt(6)\n"
RD"a[116:119],v226 offset:14336\n"
MF"v[176:179],a[64:67],v[68:71],v[176:179]\n"
RD"a[120:123],v226 offset:7168\n"
MF"v[176:179],a[68:71],v[76:79],v[176:179]\n"
RD"a[124:127],v226 offset:15360\n"
MF"v[180:183],a[72:75],v[68:71],v[180:183]\n"
WT"vmcnt(14)\n"
"s_barrier\n"
MF"v[180:183],a[76:79],v[76:79],v[180:183]\n"
RD"a[64:67],v227 offset:0\n"
MF"v[184:187],a[80:83],v[68:71],v[184:187]\n"
BL"v225,s[8:11],s44 offen offset:2048 sc1 lds\n"
MF"v[184:187],a[84:87],v[76:79],v[184:187]\n"
RD"a[68:71],v227 offset:1024\n"
MF"v[188:191],a[88:91],v[68:71],v[188:191]\n"
WT"lgkmcnt(4)\n"
MF"v[188:191],a[92:95],v[76:79],v[188:191]\n"
MF"v[192:195],a[96:99],v[68:71],v[192:195]\n"
MF"v[192:195],a[100:103],v[76:79],v[192:195]\n"
BL"v225,s[8:11],s44 offen offset:3072 sc1 lds\n"
MF"v[196:199],a[104:107],v[68:71],v[196:199]\n"
RD"a[72:75],v227 offset:2048\n"
MF"v[196:199],a[108:111],v[76:79],v[196:199]\n"
RD"a[76:79],v227 offset:3072\n"
RD"a[80:83],v227 offset:4096\n"
MF"v[200:203],a[112:115],v[68:71],v[200:203]\n"
MF"v[200:203],a[116:119],v[76:79],v[200:203]\n"
WT"lgkmcnt(3)\n"
RD"a[84:87],v227 offset:5120\n"
MF"v[204:207],a[120:123],v[68:71],v[204:207]\n"
MF"v[204:207],a[124:127],v[76:79],v[204:207]\n"
MF"a[144:147],a[128:131],v[68:71],a[144:147]\n"
MF"a[144:147],a[128:131],v[76:79],a[144:147]\n"
RD"a[88:91],v227 offset:6144\n"
"LTL_%=:\n"
"s_cmp_ge_u32 s34,6\n"
"s_cbranch_scc1 LTH_%=\n"
RD"a[92:95],v227 offset:7168\n"
MF"v[0:3],a[64:67],v[80:83],0\n"
"v_accvgpr_read_b32 v251,a140\n"
"v_subrev_u32 v253,0x2b800000,v251\n"
MF"v[0:3],a[68:71],v[88:91],v[0:3]\n"
RD"a[96:99],v227 offset:8192\n"
MF"v[40:43],a[68:71],v[120:123],0\n"
"v_cmp_le_u32 vcc,0x34000000,v253\n"
"s_cmp_lg_u64 vcc,0\n"
"s_cbranch_scc1 L1041_%=\n"
"L1040_%=:\n"
MF"v[40:43],a[64:67],v[112:115],v[40:43]\n"
WT"lgkmcnt(3)\n"
"v_rcp_f32 v251,v251\n"
MF"v[4:7],a[72:75],v[80:83],0\n"
SA"s41,s41,32768\n"
RD"a[100:103],v227 offset:9216\n"
MU"v144,v251,v144\n"
MF"v[4:7],a[76:79],v[88:91],v[4:7]\n"
"s_cmp_eq_u32 s41,163840\n"
RD"a[104:107],v227 offset:10240\n"
MU"v145,v251,v145\n"
MF"v[44:47],a[76:79],v[120:123],0\n"
"s_cselect_b32 s41,0,s41\n"
MU"v146,v251,v146\n"
"v_add_u32 v226,s41,v224\n"
MF"v[44:47],a[72:75],v[112:115],v[44:47]\n"
RD"a[108:111],v227 offset:11264\n"
MU"v147,v251,v147\n"
MF"v[8:11],a[80:83],v[80:83],0\n"
SA"s41,s41,32768\n"
MU"v148,v251,v148\n"
MU"v149,v251,v149\n"
MF"v[8:11],a[84:87],v[88:91],v[8:11]\n"
"s_cmp_eq_u32 s41,163840\n"
RD"a[112:115],v227 offset:12288\n"
MU"v150,v251,v150\n"
MU"v151,v251,v151\n"
MF"v[48:51],a[84:87],v[120:123],0\n"
"s_cselect_b32 s41,0,s41\n"
PK"v144,v144,v145\n"
"v_add_u32 v235,s41,v224\n"
PK"v145,v146,v147\n"
MF"v[48:51],a[80:83],v[112:115],v[48:51]\n"
WT"lgkmcnt(3)\n"
RD"a[116:119],v227 offset:13312\n"
PK"v146,v148,v149\n"
MF"v[12:15],a[88:91],v[80:83],0\n"
SA"s41,s41,32768\n"
PK"v147,v150,v151\n"
MU"v152,v251,v152\n"
MF"v[12:15],a[92:95],v[88:91],v[12:15]\n"
"s_cmp_eq_u32 s41,163840\n"
BS"v[144:147],v224,s[8:11],s60 offen offset:0 sc1\n"
MU"v153,v251,v153\n"
MF"v[52:55],a[92:95],v[120:123],0\n"
"s_cselect_b32 s41,0,s41\n"
RD"a[120:123],v227 offset:14336\n"
"v_add_u32 v236,s41,v224\n"
MU"v154,v251,v154\n"
MF"v[52:55],a[88:91],v[112:115],v[52:55]\n"
RD"a[124:127],v227 offset:15360\n"
MU"v155,v251,v155\n"
MU"v156,v251,v156\n"
MF"v[16:19],a[96:99],v[80:83],0\n"
SA"s41,s41,32768\n"
MU"v157,v251,v157\n"
MU"v158,v251,v158\n"
MU"v159,v251,v159\n"
MF"v[16:19],a[100:103],v[88:91],v[16:19]\n"
"s_cmp_eq_u32 s41,163840\n"
RD"a[64:67],v227 offset:16384\n"
PK"v152,v152,v153\n"
PK"v153,v154,v155\n"
MF"v[56:59],a[100:103],v[120:123],0\n"
"s_cselect_b32 s41,0,s41\n"
PK"v154,v156,v157\n"
"v_add_u32 v237,s41,v224\n"
PK"v155,v158,v159\n"
MF"v[56:59],a[96:99],v[112:115],v[56:59]\n"
WT"lgkmcnt(3)\n"
BS"v[152:155],v224,s[8:11],s60 offen offset:1024 sc1\n"
MU"v160,v251,v160\n"
MF"v[20:23],a[104:107],v[80:83],0\n"
RD"a[68:71],v227 offset:17408\n"
MU"v161,v251,v161\n"
MU"v162,v251,v162\n"
MF"v[20:23],a[108:111],v[88:91],v[20:23]\n"
RD"a[72:75],v227 offset:18432\n"
MU"v163,v251,v163\n"
MU"v164,v251,v164\n"
MF"v[60:63],a[108:111],v[120:123],0\n"
MU"v165,v251,v165\n"
MU"v166,v251,v166\n"
MF"v[60:63],a[104:107],v[112:115],v[60:63]\n"
RD"a[76:79],v227 offset:19456\n"
MU"v167,v251,v167\n"
PK"v160,v160,v161\n"
MF"v[24:27],a[112:115],v[80:83],0\n"
SA"s51,s60,16384\n"
"v_accvgpr_read_b32 v252,a144\n"
"v_subrev_u32 v253,0x2b800000,v252\n"
PK"v161,v162,v163\n"
MF"v[24:27],a[116:119],v[88:91],v[24:27]\n"
"s_lshl_b32 s62,s34,14\n"
RD"a[80:83],v227 offset:20480\n"
PK"v162,v164,v165\n"
PK"v163,v166,v167\n"
MF"v[64:67],a[116:119],v[120:123],0\n"
"s_sub_u32 s62,s61,s62\n"
BS"v[160:163],v224,s[8:11],s60 offen offset:2048 sc1\n"
MU"v168,v251,v168\n"
MF"v[64:67],a[112:115],v[112:115],v[64:67]\n"
WT"lgkmcnt(3)\n"
"v_cmp_le_u32 vcc,0x34000000,v253\n"
"s_cmp_lg_u64 vcc,0\n"
"s_cbranch_scc1 L1047_%=\n"
"L1046_%=:\n"
MF"v[28:31],a[120:123],v[80:83],0\n"
SA"s62,s62,122880\n"
"v_rcp_f32 v252,v252\n"
MU"v169,v251,v169\n"
MF"v[28:31],a[124:127],v[88:91],v[28:31]\n"
RD"a[84:87],v227 offset:21504\n"
RD"a[88:91],v227 offset:22528\n"
MU"v170,v251,v170\n"
MF"v[68:71],a[124:127],v[120:123],0\n"
MU"v171,v251,v171\n"
MU"v172,v251,v172\n"
MU"v173,v251,v173\n"
MF"v[68:71],a[120:123],v[112:115],v[68:71]\n"
RD"a[92:95],v227 offset:23552\n"
MU"v174,v251,v174\n"
MU"v175,v251,v175\n"
MF"v[32:35],a[64:67],v[80:83],0\n"
PK"v168,v168,v169\n"
PK"v169,v170,v171\n"
MU"v176,v252,v176\n"
MF"v[32:35],a[68:71],v[88:91],v[32:35]\n"
RD"a[96:99],v227 offset:24576\n"
MU"v177,v252,v177\n"
PK"v170,v172,v173\n"
MF"v[72:75],a[68:71],v[120:123],0\n"
PK"v171,v174,v175\n"
MU"v178,v252,v178\n"
MU"v179,v252,v179\n"
MF"v[72:75],a[64:67],v[112:115],v[72:75]\n"
WT"lgkmcnt(4)\n"
BS"v[168:171],v224,s[8:11],s60 offen offset:3072 sc1\n"
MU"v180,v252,v180\n"
MF"v[36:39],a[72:75],v[80:83],0\n"
RD"a[100:103],v227 offset:25600\n"
MU"v181,v252,v181\n"
MU"v182,v252,v182\n"
MF"v[36:39],a[76:79],v[88:91],v[36:39]\n"
RD"a[104:107],v227 offset:26624\n"
MU"v183,v252,v183\n"
PK"v176,v176,v177\n"
MF"v[76:79],a[76:79],v[120:123],0\n"
PK"v177,v178,v179\n"
PK"v178,v180,v181\n"
PK"v179,v182,v183\n"
MF"v[76:79],a[72:75],v[112:115],v[76:79]\n"
BS"v[176:179],v224,s[8:11],s51 offen offset:0 sc1\n"
MU"v184,v252,v184\n"
MF"v[0:3],a[80:83],v[96:99],v[0:3]\n"
WT"lgkmcnt(2)\n"
RD"a[108:111],v227 offset:27648\n"
MU"v185,v252,v185\n"
MU"v186,v252,v186\n"
MF"v[0:3],a[84:87],v[104:107],v[0:3]\n"
RD"a[112:115],v227 offset:28672\n"
MU"v187,v252,v187\n"
MU"v188,v252,v188\n"
MF"v[40:43],a[84:87],v[136:139],v[40:43]\n"
MU"v189,v252,v189\n"
MU"v190,v252,v190\n"
MU"v191,v252,v191\n"
MF"v[40:43],a[80:83],v[128:131],v[40:43]\n"
RD"a[116:119],v227 offset:29696\n"
PK"v184,v184,v185\n"
PK"v185,v186,v187\n"
MF"v[4:7],a[88:91],v[96:99],v[4:7]\n"
PK"v186,v188,v189\n"
PK"v187,v190,v191\n"
MU"v192,v252,v192\n"
MF"v[4:7],a[92:95],v[104:107],v[4:7]\n"
BS"v[184:187],v224,s[8:11],s51 offen offset:1024 sc1\n"
MU"v193,v252,v193\n"
MF"v[44:47],a[92:95],v[136:139],v[44:47]\n"
RD"a[120:123],v227 offset:30720\n"
MU"v194,v252,v194\n"
MU"v195,v252,v195\n"
MF"v[44:47],a[88:91],v[128:131],v[44:47]\n"
RD"a[124:127],v227 offset:31744\n"
MU"v196,v252,v196\n"
MU"v197,v252,v197\n"
MF"v[8:11],a[96:99],v[96:99],v[8:11]\n"
WT"lgkmcnt(3)\n"
WT"vmcnt(14)\n"
"s_barrier\n"
MF"v[8:11],a[100:103],v[104:107],v[8:11]\n"
RD"a[64:67],v226 offset:0\n"
MU"v198,v252,v198\n"
MU"v199,v252,v199\n"
MF"v[48:51],a[100:103],v[136:139],v[48:51]\n"
PK"v192,v192,v193\n"
PK"v193,v194,v195\n"
PK"v194,v196,v197\n"
MF"v[48:51],a[96:99],v[128:131],v[48:51]\n"
RD"a[68:71],v226 offset:1024\n"
PK"v195,v198,v199\n"
MU"v200,v252,v200\n"
MF"v[12:15],a[104:107],v[96:99],v[12:15]\n"
BS"v[192:195],v224,s[8:11],s51 offen offset:2048 sc1\n"
MU"v201,v252,v201\n"
MF"v[12:15],a[108:111],v[104:107],v[12:15]\n"
RD"a[72:75],v226 offset:2048\n"
MU"v202,v252,v202\n"
MU"v203,v252,v203\n"
MF"v[52:55],a[108:111],v[136:139],v[52:55]\n"
MU"v204,v252,v204\n"
MU"v205,v252,v205\n"
MU"v206,v252,v206\n"
MF"v[52:55],a[104:107],v[128:131],v[52:55]\n"
RD"a[76:79],v226 offset:3072\n"
MU"v207,v252,v207\n"
PK"v200,v200,v201\n"
MF"v[16:19],a[112:115],v[96:99],v[16:19]\n"
WT"lgkmcnt(3)\n"
PK"v201,v202,v203\n"
PK"v202,v204,v205\n"
PK"v203,v206,v207\n"
MF"v[16:19],a[116:119],v[104:107],v[16:19]\n"
BS"v[200:203],v224,s[8:11],s51 offen offset:3072 sc1\n"
MF"v[56:59],a[116:119],v[136:139],v[56:59]\n"
RD"a[80:83],v226 offset:4096\n"
"v_readlane_b32 s43,v228,s42\n"
MF"v[56:59],a[112:115],v[128:131],v[56:59]\n"
RD"a[84:87],v226 offset:5120\n"
MF"v[20:23],a[120:123],v[96:99],v[20:23]\n"
MF"v[20:23],a[124:127],v[104:107],v[20:23]\n"
RD"a[88:91],v226 offset:6144\n"
MF"v[60:63],a[124:127],v[136:139],v[60:63]\n"
"s_mov_b32 m0,s40\n"
MF"v[60:63],a[120:123],v[128:131],v[60:63]\n"
RD"a[92:95],v226 offset:7168\n"
MF"v[24:27],a[64:67],v[96:99],v[24:27]\n"
WT"lgkmcnt(4)\n"
"v_readlane_b32 s44,v229,s42\n"
MF"v[24:27],a[68:71],v[104:107],v[24:27]\n"
RD"a[96:99],v227 offset:0\n"
MF"v[64:67],a[68:71],v[136:139],v[64:67]\n"
SA"s42,s42,1\n"
BL"v225,s[8:11],s43 offen offset:0 sc1 lds\n"
MF"v[64:67],a[64:67],v[128:131],v[64:67]\n"
RD"a[100:103],v227 offset:1024\n"
MF"v[28:31],a[72:75],v[96:99],v[28:31]\n"
MF"v[28:31],a[76:79],v[104:107],v[28:31]\n"
RD"a[104:107],v227 offset:2048\n"
MF"v[68:71],a[76:79],v[136:139],v[68:71]\n"
MF"v[68:71],a[72:75],v[128:131],v[68:71]\n"
WT"lgkmcnt(3)\n"
RD"a[108:111],v227 offset:3072\n"
MF"v[32:35],a[80:83],v[96:99],v[32:35]\n"
MF"v[32:35],a[84:87],v[104:107],v[32:35]\n"
RD"a[112:115],v227 offset:4096\n"
MF"v[72:75],a[84:87],v[136:139],v[72:75]\n"
BL"v225,s[8:11],s43 offen offset:1024 sc1 lds\n"
MF"v[72:75],a[80:83],v[128:131],v[72:75]\n"
RD"a[116:119],v227 offset:5120\n"
MF"v[36:39],a[88:91],v[96:99],v[36:39]\n"
MF"v[36:39],a[92:95],v[104:107],v[36:39]\n"
RD"a[120:123],v227 offset:6144\n"
MF"v[76:79],a[92:95],v[136:139],v[76:79]\n"
MF"v[76:79],a[88:91],v[128:131],v[76:79]\n"
WT"lgkmcnt(3)\n"
RD"a[124:127],v227 offset:7168\n"
MF"v[84:87],a[96:99],v[144:147],0\n"
BL"v225,s[8:11],s43 offen offset:2048 sc1 lds\n"
MF"v[84:87],a[100:103],v[152:155],v[84:87]\n"
RD"a[64:67],v227 offset:8192\n"
MF"v[164:167],a[100:103],v[184:187],0\n"
MF"v[164:167],a[96:99],v[176:179],v[164:167]\n"
RD"a[68:71],v227 offset:9216\n"
MF"v[92:95],a[104:107],v[144:147],0\n"
MF"v[92:95],a[108:111],v[152:155],v[92:95]\n"
RD"a[72:75],v227 offset:10240\n"
MF"v[172:175],a[108:111],v[184:187],0\n"
MF"v[172:175],a[104:107],v[176:179],v[172:175]\n"
WT"lgkmcnt(3)\n"
RD"a[76:79],v227 offset:11264\n"
MF"v[100:103],a[112:115],v[144:147],0\n"
BL"v225,s[8:11],s43 offen offset:3072 sc1 lds\n"
MF"v[100:103],a[116:119],v[152:155],v[100:103]\n"
RD"a[80:83],v227 offset:12288\n"
MF"v[180:183],a[116:119],v[184:187],0\n"
SA"m0,s40,16384\n"
MF"v[180:183],a[112:115],v[176:179],v[180:183]\n"
RD"a[84:87],v227 offset:13312\n"
MF"v[108:111],a[120:123],v[144:147],0\n"
MF"v[108:111],a[124:127],v[152:155],v[108:111]\n"
RD"a[88:91],v227 offset:14336\n"
MF"v[188:191],a[124:127],v[184:187],0\n"
BL"v225,s[8:11],s44 offen offset:0 sc1 lds\n"
MF"v[188:191],a[120:123],v[176:179],v[188:191]\n"
WT"lgkmcnt(3)\n"
RD"a[92:95],v227 offset:15360\n"
SA"s40,s40,32768\n"
MF"v[116:119],a[64:67],v[144:147],0\n"
"s_cmp_eq_u32 s40,s46\n"
"s_cselect_b32 s40,s47,s40\n"
MF"v[116:119],a[68:71],v[152:155],v[116:119]\n"
RD"a[96:99],v227 offset:16384\n"
MF"v[196:199],a[68:71],v[184:187],0\n"
MF"v[196:199],a[64:67],v[176:179],v[196:199]\n"
RD"a[100:103],v227 offset:17408\n"
MF"v[124:127],a[72:75],v[144:147],0\n"
MF"v[124:127],a[76:79],v[152:155],v[124:127]\n"
RD"a[104:107],v227 offset:18432\n"
MF"v[204:207],a[76:79],v[184:187],0\n"
BL"v225,s[8:11],s44 offen offset:1024 sc1 lds\n"
MF"v[204:207],a[72:75],v[176:179],v[204:207]\n"
WT"lgkmcnt(3)\n"
RD"a[108:111],v227 offset:19456\n"
MF"v[132:135],a[80:83],v[144:147],0\n"
MF"v[132:135],a[84:87],v[152:155],v[132:135]\n"
RD"a[112:115],v227 offset:20480\n"
MF"v[208:211],a[84:87],v[184:187],0\n"
MF"v[208:211],a[80:83],v[176:179],v[208:211]\n"
RD"a[116:119],v227 offset:21504\n"
MF"v[140:143],a[88:91],v[144:147],0\n"
BL"v225,s[8:11],s44 offen offset:2048 sc1 lds\n"
MF"v[140:143],a[92:95],v[152:155],v[140:143]\n"
RD"a[120:123],v227 offset:22528\n"
MF"v[212:215],a[92:95],v[184:187],0\n"
WT"vmcnt(7)\n"
"buffer_store_dwordx2 v[232:233],v230,s[8:11],s62 offen sc1\n"
MF"v[212:215],a[88:91],v[176:179],v[212:215]\n"
WT"lgkmcnt(3)\n"
RD"a[124:127],v227 offset:23552\n"
MF"v[148:151],a[96:99],v[144:147],0\n"
MF"v[148:151],a[100:103],v[152:155],v[148:151]\n"
RD"a[64:67],v227 offset:24576\n"
MF"v[216:219],a[100:103],v[184:187],0\n"
"v_readlane_b32 s43,v228,s42\n"
MF"v[216:219],a[96:99],v[176:179],v[216:219]\n"
RD"a[68:71],v227 offset:25600\n"
MF"v[156:159],a[104:107],v[144:147],0\n"
BL"v225,s[8:11],s44 offen offset:3072 sc1 lds\n"
MF"v[156:159],a[108:111],v[152:155],v[156:159]\n"
RD"a[72:75],v227 offset:26624\n"
MF"v[220:223],a[108:111],v[184:187],0\n"
"s_mov_b32 m0,s40\n"
MF"v[220:223],a[104:107],v[176:179],v[220:223]\n"
WT"lgkmcnt(3)\n"
RD"a[76:79],v227 offset:27648\n"
MF"v[84:87],a[112:115],v[160:163],v[84:87]\n"
"s_lshr_b32 s89,s31,3\n"
"v_readlane_b32 s44,v229,s42\n"
MF"v[84:87],a[116:119],v[168:171],v[84:87]\n"
"s_lshl_b32 s89,s89,4\n"
RD"a[80:83],v227 offset:28672\n"
MF"v[164:167],a[116:119],v[200:203],v[164:167]\n"
SA"s42,s42,1\n"
BL"v225,s[8:11],s43 offen offset:0 sc1 lds\n"
MF"v[164:167],a[112:115],v[192:195],v[164:167]\n"
SA"s89,s89,s30\n"
RD"a[84:87],v227 offset:29696\n"
"s_lshl_b32 s89,s89,3\n"
MF"v[92:95],a[120:123],v[160:163],v[92:95]\n"
"s_xor_b32 s23,s32,s30\n"
MF"v[92:95],a[124:127],v[168:171],v[92:95]\n"
SA"s89,s89,s23\n"
RD"a[88:91],v227 offset:30720\n"
MF"v[172:175],a[124:127],v[200:203],v[172:175]\n"
"s_xor_b32 s90,s89,36\n"
MF"v[172:175],a[120:123],v[192:195],v[172:175]\n"
WT"lgkmcnt(3)\n"
RD"a[92:95],v227 offset:31744\n"
"s_mul_i32 s88,s89,5120\n"
MF"v[100:103],a[64:67],v[160:163],v[100:103]\n"
"s_mul_i32 s22,s90,5120\n"
MF"v[100:103],a[68:71],v[168:171],v[100:103]\n"
"s_sub_u32 s90,s90,s89\n"
RD"a[96:99],v226 offset:0\n"
MF"v[180:183],a[68:71],v[200:203],v[180:183]\n"
"s_lshl_b32 s90,s90,6\n"
BL"v225,s[8:11],s43 offen offset:1024 sc1 lds\n"
MF"v[180:183],a[64:67],v[192:195],v[180:183]\n"
SA"s88,s88,59506688\n"
RD"a[100:103],v226 offset:1024\n"
MF"v[108:111],a[72:75],v[160:163],v[108:111]\n"
SA"s22,s22,59506688\n"
MF"v[108:111],a[76:79],v[168:171],v[108:111]\n"
"s_cmp_eq_u32 s23,0\n"
RD"a[104:107],v226 offset:2048\n"
"s_cselect_b32 s88,2130706432,s88\n"
MF"v[188:191],a[76:79],v[200:203],v[188:191]\n"
"s_cmp_eq_u32 s23,4\n"
"s_cselect_b32 s22,2130706432,s22\n"
MF"v[188:191],a[72:75],v[192:195],v[188:191]\n"
WT"lgkmcnt(3)\n"
RD"a[108:111],v226 offset:3072\n"
"s_lshl_b32 s89,s89,6\n"
MF"v[116:119],a[80:83],v[160:163],v[116:119]\n"
SA"s89,s89,80478208\n"
BL"v225,s[8:11],s43 offen offset:2048 sc1 lds\n"
MF"v[116:119],a[84:87],v[168:171],v[116:119]\n"
RD"a[112:115],v226 offset:4096\n"
"v_bfe_u32 v239,v224,1,6\n"
MF"v[196:199],a[84:87],v[200:203],v[196:199]\n"
"v_bfe_u32 v242,v224,7,1\n"
MF"v[196:199],a[80:83],v[192:195],v[196:199]\n"
RD"a[116:119],v226 offset:5120\n"
"v_mad_u32_u24 v239,v242,s90,v239\n"
MF"v[124:127],a[88:91],v[160:163],v[124:127]\n"
"v_lshrrev_b32 v242,8,v224\n"
MF"v[124:127],a[92:95],v[168:171],v[124:127]\n"
RD"a[120:123],v226 offset:6144\n"
"v_lshl_add_u32 v239,v242,27,v239\n"
MF"v[204:207],a[92:95],v[200:203],v[204:207]\n"
MF"v[204:207],a[88:91],v[192:195],v[204:207]\n"
WT"lgkmcnt(3)\n"
RD"a[124:127],v226 offset:7168\n"
MF"v[132:135],a[96:99],v[160:163],v[132:135]\n"
BL"v225,s[8:11],s43 offen offset:3072 sc1 lds\n"
MF"v[132:135],a[100:103],v[168:171],v[132:135]\n"
RD"a[64:67],v226 offset:8192\n"
MF"v[208:211],a[100:103],v[200:203],v[208:211]\n"
SA"m0,s40,16384\n"
MF"v[208:211],a[96:99],v[192:195],v[208:211]\n"
RD"a[68:71],v226 offset:9216\n"
MF"v[140:143],a[104:107],v[160:163],v[140:143]\n"
MF"v[140:143],a[108:111],v[168:171],v[140:143]\n"
RD"a[72:75],v226 offset:10240\n"
MF"v[212:215],a[108:111],v[200:203],v[212:215]\n"
BL"v225,s[8:11],s44 offen offset:0 sc1 lds\n"
MF"v[212:215],a[104:107],v[192:195],v[212:215]\n"
WT"lgkmcnt(3)\n"
RD"a[76:79],v226 offset:11264\n"
SA"s40,s40,32768\n"
MF"v[148:151],a[112:115],v[160:163],v[148:151]\n"
"s_cmp_eq_u32 s40,s46\n"
"s_cselect_b32 s40,s47,s40\n"
MF"v[148:151],a[116:119],v[168:171],v[148:151]\n"
RD"a[80:83],v226 offset:12288\n"
MF"v[216:219],a[116:119],v[200:203],v[216:219]\n"
MF"v[216:219],a[112:115],v[192:195],v[216:219]\n"
RD"a[84:87],v226 offset:13312\n"
MF"v[156:159],a[120:123],v[160:163],v[156:159]\n"
MF"v[156:159],a[124:127],v[168:171],v[156:159]\n"
RD"a[88:91],v226 offset:14336\n"
MF"v[220:223],a[124:127],v[200:203],v[220:223]\n"
BL"v225,s[8:11],s44 offen offset:1024 sc1 lds\n"
MF"v[220:223],a[120:123],v[192:195],v[220:223]\n"
WT"lgkmcnt(3)\n"
RD"a[92:95],v226 offset:15360\n"
MF"v[0:3],a[64:67],a[0:3],v[0:3]\n"
MF"v[0:3],a[68:71],a[8:11],v[0:3]\n"
RD"a[96:99],v226 offset:16384\n"
MF"v[40:43],a[68:71],a[12:15],v[40:43]\n"
MF"v[40:43],a[64:67],a[4:7],v[40:43]\n"
RD"a[100:103],v226 offset:17408\n"
MF"v[4:7],a[72:75],a[0:3],v[4:7]\n"
BL"v225,s[8:11],s44 offen offset:2048 sc1 lds\n"
MF"v[4:7],a[76:79],a[8:11],v[4:7]\n"
RD"a[104:107],v226 offset:18432\n"
MF"v[44:47],a[76:79],a[12:15],v[44:47]\n"
MF"v[44:47],a[72:75],a[4:7],v[44:47]\n"
WT"lgkmcnt(3)\n"
RD"a[108:111],v226 offset:19456\n"
MF"v[8:11],a[80:83],a[0:3],v[8:11]\n"
MF"v[8:11],a[84:87],a[8:11],v[8:11]\n"
RD"a[112:115],v226 offset:20480\n"
MF"v[48:51],a[84:87],a[12:15],v[48:51]\n"
MF"v[48:51],a[80:83],a[4:7],v[48:51]\n"
RD"a[116:119],v226 offset:21504\n"
MF"v[12:15],a[88:91],a[0:3],v[12:15]\n"
BL"v225,s[8:11],s44 offen offset:3072 sc1 lds\n"
MF"v[12:15],a[92:95],a[8:11],v[12:15]\n"
RD"a[120:123],v226 offset:22528\n"
MF"v[52:55],a[92:95],a[12:15],v[52:55]\n"
MF"v[52:55],a[88:91],a[4:7],v[52:55]\n"
WT"lgkmcnt(3)\n"
RD"a[124:127],v226 offset:23552\n"
MF"v[16:19],a[96:99],a[0:3],v[16:19]\n"
MF"v[16:19],a[100:103],a[8:11],v[16:19]\n"
RD"a[64:67],v226 offset:24576\n"
MF"v[56:59],a[100:103],a[12:15],v[56:59]\n"
MF"v[56:59],a[96:99],a[4:7],v[56:59]\n"
RD"a[68:71],v226 offset:25600\n"
MF"v[20:23],a[104:107],a[0:3],v[20:23]\n"
MF"v[20:23],a[108:111],a[8:11],v[20:23]\n"
RD"a[72:75],v226 offset:26624\n"
MF"v[60:63],a[108:111],a[12:15],v[60:63]\n"
MF"v[60:63],a[104:107],a[4:7],v[60:63]\n"
WT"lgkmcnt(3)\n"
RD"a[76:79],v226 offset:27648\n"
MF"v[24:27],a[112:115],a[0:3],v[24:27]\n"
MF"v[24:27],a[116:119],a[8:11],v[24:27]\n"
RD"a[80:83],v226 offset:28672\n"
MF"v[64:67],a[116:119],a[12:15],v[64:67]\n"
MF"v[64:67],a[112:115],a[4:7],v[64:67]\n"
RD"a[84:87],v226 offset:29696\n"
MF"v[28:31],a[120:123],a[0:3],v[28:31]\n"
MF"v[28:31],a[124:127],a[8:11],v[28:31]\n"
RD"a[88:91],v226 offset:30720\n"
MF"v[68:71],a[124:127],a[12:15],v[68:71]\n"
MF"v[68:71],a[120:123],a[4:7],v[68:71]\n"
WT"lgkmcnt(3)\n"
RD"a[92:95],v226 offset:31744\n"
MF"v[32:35],a[64:67],a[0:3],v[32:35]\n"
"s_barrier\n"
MF"v[32:35],a[68:71],a[8:11],v[32:35]\n"
RD"a[96:99],v235 offset:0\n"
MF"v[72:75],a[68:71],a[12:15],v[72:75]\n"
MF"v[72:75],a[64:67],a[4:7],v[72:75]\n"
RD"a[100:103],v235 offset:1024\n"
MF"v[36:39],a[72:75],a[0:3],v[36:39]\n"
MF"v[36:39],a[76:79],a[8:11],v[36:39]\n"
RD"a[104:107],v235 offset:2048\n"
MF"v[76:79],a[76:79],a[12:15],v[76:79]\n"
MF"v[76:79],a[72:75],a[4:7],v[76:79]\n"
WT"lgkmcnt(3)\n"
RD"a[108:111],v235 offset:3072\n"
MF"v[0:3],a[80:83],a[16:19],v[0:3]\n"
MF"v[0:3],a[84:87],a[24:27],v[0:3]\n"
RD"a[112:115],v235 offset:4096\n"
MF"v[40:43],a[84:87],a[28:31],v[40:43]\n"
MF"v[40:43],a[80:83],a[20:23],v[40:43]\n"
RD"a[116:119],v235 offset:5120\n"
MF"v[4:7],a[88:91],a[16:19],v[4:7]\n"
MF"v[4:7],a[92:95],a[24:27],v[4:7]\n"
RD"a[120:123],v235 offset:6144\n"
MF"v[44:47],a[92:95],a[28:31],v[44:47]\n"
MF"v[44:47],a[88:91],a[20:23],v[44:47]\n"
WT"lgkmcnt(3)\n"
RD"a[124:127],v235 offset:7168\n"
MF"v[8:11],a[96:99],a[16:19],v[8:11]\n"
MF"v[8:11],a[100:103],a[24:27],v[8:11]\n"
RD"a[64:67],v235 offset:8192\n"
MF"v[48:51],a[100:103],a[28:31],v[48:51]\n"
MF"v[48:51],a[96:99],a[20:23],v[48:51]\n"
RD"a[68:71],v235 offset:9216\n"
MF"v[12:15],a[104:107],a[16:19],v[12:15]\n"
MF"v[12:15],a[108:111],a[24:27],v[12:15]\n"
RD"a[72:75],v235 offset:10240\n"
"v_bfe_u32 v238,v224,4,2\n"
MF"v[52:55],a[108:111],a[28:31],v[52:55]\n"
"s_andn2_b32 s63,s62,255\n"
"v_lshlrev_b32 v238,6,v238\n"
MF"v[52:55],a[104:107],a[20:23],v[52:55]\n"
WT"lgkmcnt(3)\n"
RD"a[76:79],v235 offset:11264\n"
MF"v[16:19],a[112:115],a[16:19],v[16:19]\n"
"buffer_load_dwordx2 v[240:241],v238,s[8:11],s63 offen sc1\n"
MF"v[16:19],a[116:119],a[24:27],v[16:19]\n"
RD"a[80:83],v235 offset:12288\n"
MF"v[56:59],a[116:119],a[28:31],v[56:59]\n"
MF"v[56:59],a[112:115],a[20:23],v[56:59]\n"
RD"a[84:87],v235 offset:13312\n"
MF"v[20:23],a[120:123],a[16:19],v[20:23]\n"
MF"v[20:23],a[124:127],a[24:27],v[20:23]\n"
RD"a[88:91],v235 offset:14336\n"
MF"v[60:63],a[124:127],a[28:31],v[60:63]\n"
MF"v[60:63],a[120:123],a[20:23],v[60:63]\n"
RD"a[92:95],v235 offset:15360\n"
"LTJ_%=:\n"
WT"lgkmcnt(7)\n"
MF"v[24:27],a[64:67],a[16:19],v[24:27]\n"
WT"lgkmcnt(3)\n"
MF"v[24:27],a[68:71],a[24:27],v[24:27]\n"
RD"a[96:99],v235 offset:16384\n"
MF"v[64:67],a[68:71],a[28:31],v[64:67]\n"
MF"v[64:67],a[64:67],a[20:23],v[64:67]\n"
RD"a[100:103],v235 offset:17408\n"
MF"v[28:31],a[72:75],a[16:19],v[28:31]\n"
MF"v[28:31],a[76:79],a[24:27],v[28:31]\n"
RD"a[104:107],v235 offset:18432\n"
MF"v[68:71],a[76:79],a[28:31],v[68:71]\n"
MF"v[68:71],a[72:75],a[20:23],v[68:71]\n"
RD"a[108:111],v235 offset:19456\n"
MF"v[32:35],a[80:83],a[16:19],v[32:35]\n"
WT"lgkmcnt(3)\n"
WT"vmcnt(1)\n"
"s_barrier\n"
MF"v[32:35],a[84:87],a[24:27],v[32:35]\n"
RD"a[112:115],v236 offset:4096\n"
MF"v[72:75],a[84:87],a[28:31],v[72:75]\n"
MF"v[72:75],a[80:83],a[20:23],v[72:75]\n"
RD"a[116:119],v236 offset:5120\n"
MF"v[36:39],a[88:91],a[16:19],v[36:39]\n"
MF"v[36:39],a[92:95],a[24:27],v[36:39]\n"
RD"a[120:123],v236 offset:6144\n"
MF"v[76:79],a[92:95],a[28:31],v[76:79]\n"
MF"v[76:79],a[88:91],a[20:23],v[76:79]\n"
RD"a[124:127],v236 offset:7168\n"
MF"v[0:3],a[96:99],a[180:183],v[0:3]\n"
WT"lgkmcnt(3)\n"
MF"v[0:3],a[100:103],a[188:191],v[0:3]\n"
RD"a[64:67],v236 offset:24576\n"
MF"v[40:43],a[100:103],a[192:195],v[40:43]\n"
MF"v[40:43],a[96:99],a[184:187],v[40:43]\n"
RD"a[68:71],v236 offset:25600\n"
MF"v[4:7],a[104:107],a[180:183],v[4:7]\n"
MF"v[4:7],a[108:111],a[188:191],v[4:7]\n"
RD"a[72:75],v236 offset:26624\n"
MF"v[44:47],a[108:111],a[192:195],v[44:47]\n"
MF"v[44:47],a[104:107],a[184:187],v[44:47]\n"
RD"a[76:79],v236 offset:27648\n"
MF"v[0:3],a[112:115],a[196:199],v[0:3]\n"
WT"lgkmcnt(3)\n"
MF"v[0:3],a[116:119],a[204:207],v[0:3]\n"
RD"a[80:83],v237 offset:12288\n"
MF"v[40:43],a[116:119],a[208:211],v[40:43]\n"
MF"v[40:43],a[112:115],a[200:203],v[40:43]\n"
RD"a[84:87],v237 offset:13312\n"
MF"v[4:7],a[120:123],a[196:199],v[4:7]\n"
MF"v[4:7],a[124:127],a[204:207],v[4:7]\n"
SA"s48,s60,32768\n"
RD"a[88:91],v237 offset:14336\n"
MF"v[44:47],a[124:127],a[208:211],v[44:47]\n"
"s_and_b32 s48,s48,0x18000\n"
MF"v[44:47],a[120:123],a[200:203],v[44:47]\n"
"s_andn2_b32 s49,s60,0x18000\n"
RD"a[92:95],v237 offset:15360\n"
MF"v[0:3],a[64:67],a[212:215],v[0:3]\n"
WT"lgkmcnt(3)\n"
WT"vmcnt(0)\n"
"v_cmp_eq_u64 vcc,s[28:29],v[240:241]\n"
"s_cmp_eq_u64 vcc,exec\n"
"s_cbranch_scc0 L1083_%=\n"
"L1084_%=:\n"
MF"v[0:3],a[68:71],a[220:223],v[0:3]\n"
"s_or_b32 s52,s48,s49\n"
RD"a[96:99],v235 offset:20480\n"
SA"s53,s52,16384\n"
MF"v[40:43],a[68:71],a[224:227],v[40:43]\n"
BL"a[244:247],v224,s[8:11],s52 offen offset:0 sc1\n"
MF"v[40:43],a[64:67],a[216:219],v[40:43]\n"
RD"a[100:103],v235 offset:21504\n"
MF"v[4:7],a[72:75],a[212:215],v[4:7]\n"
MF"v[4:7],a[76:79],a[220:223],v[4:7]\n"
RD"a[104:107],v235 offset:22528\n"
MF"v[44:47],a[76:79],a[224:227],v[44:47]\n"
"v_bfe_u32 v243,v224,6,2\n"
MF"v[44:47],a[72:75],a[216:219],v[44:47]\n"
RD"a[108:111],v235 offset:23552\n"
"v_bfe_u32 v244,v224,4,2\n"
MF"v[0:3],a[80:83],a[228:231],v[0:3]\n"
WT"lgkmcnt(3)\n"
BL"a[248:251],v224,s[8:11],s53 offen offset:0 sc1\n"
MF"v[0:3],a[84:87],a[236:239],v[0:3]\n"
RD"a[112:115],v236 offset:8192\n"
"v_lshrrev_b32 v245,8,v224\n"
MF"v[40:43],a[84:87],a[240:243],v[40:43]\n"
"v_lshlrev_b32 v245,3,v245\n"
"v_sub_u32 v244,v244,v245\n"
MF"v[40:43],a[80:83],a[232:235],v[40:43]\n"
RD"a[116:119],v236 offset:9216\n"
"v_and_b32 v245,16,v224\n"
MF"v[4:7],a[88:91],a[228:231],v[4:7]\n"
"v_mov_b32 v246,0x3f80\n"
MF"v[4:7],a[92:95],a[236:239],v[4:7]\n"
RD"a[120:123],v236 offset:10240\n"
"v_lshlrev_b32 v245,v245,v246\n"
MF"v[44:47],a[92:95],a[240:243],v[44:47]\n"
BL"a[252:255],v224,s[8:11],s52 offen offset:1024 sc1\n"
MF"v[44:47],a[88:91],a[232:235],v[44:47]\n"
RD"a[124:127],v236 offset:11264\n"
"v_lshlrev_b32 v246,4,v243\n"
MF"v[8:11],a[96:99],a[180:183],v[8:11]\n"
WT"lgkmcnt(3)\n"
"v_and_b32 v246,16,v246\n"
"v_and_b32 v247,2,v243\n"
MF"v[8:11],a[100:103],a[188:191],v[8:11]\n"
SA"s90,s88,0\n"
RD"a[64:67],v236 offset:28672\n"
PK"v0,v0,v1\n"
MF"v[48:51],a[100:103],a[192:195],v[48:51]\n"
BL"a[128:131],v224,s[8:11],s53 offen offset:1024 sc1\n"
MF"v[48:51],a[96:99],a[184:187],v[48:51]\n"
RD"a[68:71],v236 offset:29696\n"
PK"v1,v2,v3\n"
MF"v[12:15],a[104:107],a[180:183],v[12:15]\n"
"v_lshl_add_u32 v246,v247,1,v246\n"
PK"v2,v4,v5\n"
MF"v[12:15],a[108:111],a[188:191],v[12:15]\n"
RD"a[72:75],v236 offset:30720\n"
PK"v3,v6,v7\n"
"v_add3_u32 v246,v246,v244,0\n"
MF"v[52:55],a[108:111],a[192:195],v[52:55]\n"
BS"v[0:3],v224,s[8:11],s90 offen sc1\n"
"v_ashrrev_i32 v246,1,v246\n"
MF"v[52:55],a[104:107],a[184:187],v[52:55]\n"
RD"a[76:79],v236 offset:31744\n"
"v_cmp_eq_u32_e64 vcc,0,v246\n"
MF"v[8:11],a[112:115],a[196:199],v[8:11]\n"
WT"lgkmcnt(3)\n"
BL"a[132:135],v224,s[8:11],s52 offen offset:2048 sc1\n"
"v_cndmask_b32_e64 v80,0,v245,vcc\n"
MF"v[8:11],a[116:119],a[204:207],v[8:11]\n"
SA"s91,s22,0\n"
RD"a[80:83],v237 offset:16384\n"
"v_cmp_eq_u32_e64 s[92:93],1,v246\n"
MF"v[48:51],a[116:119],a[208:211],v[48:51]\n"
"v_cmp_eq_u32_e64 s[94:95],2,v246\n"
"v_cndmask_b32_e64 v81,0,v245,s[92:93]\n"
MF"v[48:51],a[112:115],a[200:203],v[48:51]\n"
RD"a[84:87],v237 offset:17408\n"
PK"v40,v40,v41\n"
MF"v[12:15],a[120:123],a[196:199],v[12:15]\n"
PK"v41,v42,v43\n"
"v_cndmask_b32_e64 v82,0,v245,s[94:95]\n"
MF"v[12:15],a[124:127],a[204:207],v[12:15]\n"
RD"a[88:91],v237 offset:18432\n"
PK"v42,v44,v45\n"
PK"v43,v46,v47\n"
MF"v[52:55],a[124:127],a[208:211],v[52:55]\n"
BS"v[40:43],v224,s[8:11],s91 offen sc1\n"
"v_cmp_eq_u32_e64 vcc,3,v246\n"
MF"v[52:55],a[120:123],a[200:203],v[52:55]\n"
BL"a[136:139],v224,s[8:11],s53 offen offset:2048 sc1\n"
"v_cndmask_b32_e64 v83,0,v245,vcc\n"
MF"v[8:11],a[64:67],a[212:215],v[8:11]\n"
WT"lgkmcnt(2)\n"
RD"a[92:95],v237 offset:19456\n"
"v_lshlrev_b32 v246,4,v243\n"
"v_and_b32 v246,16,v246\n"
MF"v[8:11],a[68:71],a[220:223],v[8:11]\n"
RD"a[96:99],v235 offset:24576\n"
"v_and_b32 v247,2,v243\n"
MF"v[48:51],a[68:71],a[224:227],v[48:51]\n"
"v_lshl_add_u32 v246,v247,1,v246\n"
"v_add3_u32 v246,v246,v244,8\n"
MF"v[48:51],a[64:67],a[216:219],v[48:51]\n"
RD"a[100:103],v235 offset:25600\n"
"v_ashrrev_i32 v246,1,v246\n"
MF"v[12:15],a[72:75],a[212:215],v[12:15]\n"
BL"a[140:143],v224,s[8:11],s52 offen offset:3072 sc1\n"
MF"v[12:15],a[76:79],a[220:223],v[12:15]\n"
RD"a[104:107],v235 offset:26624\n"
"v_cmp_eq_u32_e64 vcc,0,v246\n"
MF"v[52:55],a[76:79],a[224:227],v[52:55]\n"
"v_cmp_eq_u32_e64 s[92:93],1,v246\n"
"v_cndmask_b32_e64 v88,0,v245,vcc\n"
MF"v[52:55],a[72:75],a[216:219],v[52:55]\n"
RD"a[108:111],v235 offset:27648\n"
"v_cndmask_b32_e64 v89,0,v245,s[92:93]\n"
MF"v[8:11],a[80:83],a[228:231],v[8:11]\n"
WT"lgkmcnt(3)\n"
BL"a[144:147],v224,s[8:11],s53 offen offset:3072 sc1\n"
MF"v[8:11],a[84:87],a[236:239],v[8:11]\n"
SA"s48,s60,65536\n"
RD"a[112:115],v236 offset:12288\n"
"v_cmp_eq_u32_e64 s[94:95],2,v246\n"
MF"v[48:51],a[84:87],a[240:243],v[48:51]\n"
"s_and_b32 s48,s48,0x18000\n"
"v_cndmask_b32_e64 v90,0,v245,s[94:95]\n"
"v_cmp_eq_u32_e64 vcc,3,v246\n"
MF"v[48:51],a[80:83],a[232:235],v[48:51]\n"
"s_andn2_b32 s49,s60,0x18000\n"
RD"a[116:119],v236 offset:13312\n"
"v_cndmask_b32_e64 v91,0,v245,vcc\n"
MF"v[12:15],a[88:91],a[228:231],v[12:15]\n"
"s_or_b32 s52,s48,s49\n"
"v_lshl_add_u32 v246,v243,3,v244\n"
"v_add_u32 v246,0,v246\n"
MF"v[12:15],a[92:95],a[236:239],v[12:15]\n"
SA"s53,s52,16384\n"
RD"a[120:123],v236 offset:14336\n"
"v_ashrrev_i32 v246,1,v246\n"
MF"v[52:55],a[92:95],a[240:243],v[52:55]\n"
BL"a[148:151],v224,s[8:11],s52 offen offset:0 sc1\n"
MF"v[52:55],a[88:91],a[232:235],v[52:55]\n"
RD"a[124:127],v236 offset:15360\n"
"v_cmp_eq_u32_e64 vcc,0,v246\n"
MF"v[16:19],a[96:99],a[180:183],v[16:19]\n"
WT"lgkmcnt(3)\n"
"v_cndmask_b32_e64 v96,0,v245,vcc\n"
"v_cmp_eq_u32_e64 s[92:93],1,v246\n"
MF"v[16:19],a[100:103],a[188:191],v[16:19]\n"
SA"s90,s88,1024\n"
RD"a[64:67],v237 offset:0\n"
"v_cndmask_b32_e64 v97,0,v245,s[92:93]\n"
MF"v[56:59],a[100:103],a[192:195],v[56:59]\n"
PK"v8,v8,v9\n"
PK"v9,v10,v11\n"
MF"v[56:59],a[96:99],a[184:187],v[56:59]\n"
RD"a[68:71],v237 offset:1024\n"
"v_cmp_eq_u32_e64 s[94:95],2,v246\n"
MF"v[20:23],a[104:107],a[180:183],v[20:23]\n"
BL"a[152:155],v224,s[8:11],s53 offen offset:0 sc1\n"
MF"v[20:23],a[108:111],a[188:191],v[20:23]\n"
RD"a[72:75],v237 offset:2048\n"
PK"v10,v12,v13\n"
PK"v11,v14,v15\n"
MF"v[60:63],a[108:111],a[192:195],v[60:63]\n"
BS"v[8:11],v224,s[8:11],s90 offen sc1\n"
"v_cndmask_b32_e64 v98,0,v245,s[94:95]\n"
MF"v[60:63],a[104:107],a[184:187],v[60:63]\n"
RD"a[76:79],v237 offset:3072\n"
"v_cmp_eq_u32_e64 vcc,3,v246\n"
MF"v[16:19],a[112:115],a[196:199],v[16:19]\n"
WT"lgkmcnt(3)\n"
"v_cndmask_b32_e64 v99,0,v245,vcc\n"
"v_lshl_add_u32 v246,v243,3,v244\n"
MF"v[16:19],a[116:119],a[204:207],v[16:19]\n"
SA"s91,s22,1024\n"
RD"a[80:83],v237 offset:20480\n"
"v_add_u32 v246,4,v246\n"
MF"v[56:59],a[116:119],a[208:211],v[56:59]\n"
BL"a[156:159],v224,s[8:11],s52 offen offset:1024 sc1\n"
"v_ashrrev_i32 v246,1,v246\n"
MF"v[56:59],a[112:115],a[200:203],v[56:59]\n"
RD"a[84:87],v237 offset:21504\n"
"v_cmp_eq_u32_e64 vcc,0,v246\n"
MF"v[20:23],a[120:123],a[196:199],v[20:23]\n"
PK"v48,v48,v49\n"
PK"v49,v50,v51\n"
"v_cndmask_b32_e64 v104,0,v245,vcc\n"
MF"v[20:23],a[124:127],a[204:207],v[20:23]\n"
RD"a[88:91],v237 offset:22528\n"
PK"v50,v52,v53\n"
PK"v51,v54,v55\n"
MF"v[60:63],a[124:127],a[208:211],v[60:63]\n"
BS"v[48:51],v224,s[8:11],s91 offen sc1\n"
"v_cmp_eq_u32_e64 s[92:93],1,v246\n"
MF"v[60:63],a[120:123],a[200:203],v[60:63]\n"
BL"a[160:163],v224,s[8:11],s53 offen offset:1024 sc1\n"
"v_cndmask_b32_e64 v105,0,v245,s[92:93]\n"
MF"v[16:19],a[64:67],a[212:215],v[16:19]\n"
WT"lgkmcnt(2)\n"
RD"a[92:95],v237 offset:23552\n"
"v_cmp_eq_u32_e64 s[94:95],2,v246\n"
MF"v[16:19],a[68:71],a[220:223],v[16:19]\n"
"s_mov_b32 s12,s64\n"
RD"a[96:99],v235 offset:28672\n"
"v_cndmask_b32_e64 v106,0,v245,s[94:95]\n"
"v_cmp_eq_u32_e64 vcc,3,v246\n"
MF"v[56:59],a[68:71],a[224:227],v[56:59]\n"
"s_and_b32 s13,s65,0xffff\n"
"v_cndmask_b32_e64 v107,0,v245,vcc\n"
"s_mov_b32 s14,2621440\n"
MF"v[56:59],a[64:67],a[216:219],v[56:59]\n"
"s_mov_b32 s15,0x20000\n"
RD"a[100:103],v235 offset:29696\n"
MF"v[20:23],a[72:75],a[212:215],v[20:23]\n"
"s_mov_b32 s16,s86\n"
BL"a[164:167],v224,s[8:11],s52 offen offset:2048 sc1\n"
MF"v[20:23],a[76:79],a[220:223],v[20:23]\n"
"s_and_b32 s17,s87,0xffff\n"
RD"a[104:107],v235 offset:30720\n"
MF"v[60:63],a[76:79],a[224:227],v[60:63]\n"
"s_mov_b32 s18,2621440\n"
MF"v[60:63],a[72:75],a[216:219],v[60:63]\n"
"s_mov_b32 s19,0x20000\n"
RD"a[108:111],v235 offset:31744\n"
MF"v[16:19],a[80:83],a[228:231],v[16:19]\n"
WT"lgkmcnt(3)\n"
"v_bfe_u32 v243,v224,4,4\n"
MF"v[16:19],a[84:87],a[236:239],v[16:19]\n"
"s_lshl_b32 s92,s34,3\n"
RD"a[112:115],v236 offset:16384\n"
MF"v[56:59],a[84:87],a[240:243],v[56:59]\n"
SA"s93,s92,s32\n"
BL"a[168:171],v224,s[8:11],s53 offen offset:2048 sc1\n"
MF"v[56:59],a[80:83],a[232:235],v[56:59]\n"
"s_sub_u32 s92,s32,s92\n"
RD"a[116:119],v236 offset:17408\n"
MF"v[20:23],a[88:91],a[228:231],v[20:23]\n"
SA"s92,s92,120\n"
MF"v[20:23],a[92:95],a[236:239],v[20:23]\n"
RD"a[120:123],v236 offset:18432\n"
"v_lshl_add_u32 v244,s93,4,v243\n"
MF"v[60:63],a[92:95],a[240:243],v[60:63]\n"
"v_lshl_add_u32 v245,s92,4,v243\n"
MF"v[60:63],a[88:91],a[232:235],v[60:63]\n"
"s_mul_i32 s93,s93,20480\n"
RD"a[124:127],v236 offset:19456\n"
MF"v[24:27],a[96:99],a[180:183],v[24:27]\n"
WT"lgkmcnt(3)\n"
BL"a[172:175],v224,s[8:11],s52 offen offset:3072 sc1\n"
MF"v[24:27],a[100:103],a[188:191],v[24:27]\n"
"s_mul_i32 s92,s92,20480\n"
RD"a[64:67],v237 offset:4096\n"
PK"v16,v16,v17\n"
MF"v[64:67],a[100:103],a[192:195],v[64:67]\n"
SA"s90,s88,2048\n"
PK"v17,v18,v19\n"
"v_add_u32 v178,s93,v224\n"
MF"v[64:67],a[96:99],a[184:187],v[64:67]\n"
RD"a[68:71],v237 offset:5120\n"
PK"v18,v20,v21\n"
MF"v[28:31],a[104:107],a[180:183],v[28:31]\n"
BL"a[176:179],v224,s[8:11],s53 offen offset:3072 sc1\n"
MF"v[28:31],a[108:111],a[188:191],v[28:31]\n"
SA"s48,s60,98304\n"
RD"a[72:75],v237 offset:6144\n"
PK"v19,v22,v23\n"
"v_add_u32 v179,s92,v224\n"
MF"v[68:71],a[108:111],a[192:195],v[68:71]\n"
"s_and_b32 s48,s48,0x18000\n"
BS"v[16:19],v224,s[8:11],s90 offen sc1\n"
"v_lshrrev_b32 v243,4,v224\n"
MF"v[68:71],a[104:107],a[184:187],v[68:71]\n"
"s_andn2_b32 s49,s60,0x18000\n"
RD"a[76:79],v237 offset:7168\n"
"v_and_b32 v243,48,v243\n"
MF"v[24:27],a[112:115],a[196:199],v[24:27]\n"
WT"lgkmcnt(3)\n"
"v_mul_u32_u24 v176,1280,v244\n"
"v_mul_u32_u24 v177,1280,v245\n"
MF"v[24:27],a[116:119],a[204:207],v[24:27]\n"
"s_or_b32 s52,s48,s49\n"
RD"a[80:83],v237 offset:24576\n"
"v_add_u32 v176,v176,v243\n"
MF"v[64:67],a[116:119],a[208:211],v[64:67]\n"
SA"s53,s52,16384\n"
BL"a[32:35],v224,s[8:11],s52 offen offset:0 sc1\n"
MF"v[64:67],a[112:115],a[200:203],v[64:67]\n"
SA"s91,s22,2048\n"
RD"a[84:87],v237 offset:25600\n"
"v_add_u32 v177,v177,v243\n"
MF"v[28:31],a[120:123],a[196:199],v[28:31]\n"
PK"v56,v56,v57\n"
PK"v57,v58,v59\n"
"v_and_b32 v243,0x300,v224\n"
MF"v[28:31],a[124:127],a[204:207],v[28:31]\n"
RD"a[88:91],v237 offset:26624\n"
PK"v58,v60,v61\n"
PK"v59,v62,v63\n"
MF"v[68:71],a[124:127],a[208:211],v[68:71]\n"
BS"v[56:59],v224,s[8:11],s91 offen sc1\n"
"v_add_u32 v243,2684928,v243\n"
MF"v[68:71],a[120:123],a[200:203],v[68:71]\n"
RD"a[92:95],v237 offset:27648\n"
"v_sub_u32 v244,2047,v244\n"
"v_sub_u32 v245,2047,v245\n"
MF"v[24:27],a[64:67],a[212:215],v[24:27]\n"
WT"lgkmcnt(3)\n"
BL"a[36:39],v224,s[8:11],s53 offen offset:0 sc1\n"
"v_readlane_b32 s92,v234,0\n"
MF"v[24:27],a[68:71],a[220:223],v[24:27]\n"
"s_and_b32 s92,s92,0xffff\n"
RD"a[96:99],v236 offset:0\n"
"v_cmp_eq_u32 vcc,s92,v244\n"
MF"v[64:67],a[68:71],a[224:227],v[64:67]\n"
"v_cmp_eq_u32_e64 s[94:95],s92,v245\n"
"v_cndmask_b32 v178,v178,v243,vcc\n"
MF"v[64:67],a[64:67],a[216:219],v[64:67]\n"
RD"a[100:103],v236 offset:1024\n"
"v_cndmask_b32_e64 v179,v179,v243,s[94:95]\n"
MF"v[28:31],a[72:75],a[212:215],v[28:31]\n"
"v_add_u32 v243,16,v243\n"
"v_readlane_b32 s92,v234,1\n"
MF"v[28:31],a[76:79],a[220:223],v[28:31]\n"
"s_and_b32 s92,s92,0xffff\n"
RD"a[104:107],v236 offset:2048\n"
MF"v[68:71],a[76:79],a[224:227],v[68:71]\n"
BL"a[40:43],v224,s[8:11],s52 offen offset:1024 sc1\n"
MF"v[68:71],a[72:75],a[216:219],v[68:71]\n"
RD"a[108:111],v236 offset:3072\n"
"v_cmp_eq_u32 vcc,s92,v244\n"
MF"v[24:27],a[80:83],a[228:231],v[24:27]\n"
WT"lgkmcnt(3)\n"
"v_cmp_eq_u32_e64 s[94:95],s92,v245\n"
"v_cndmask_b32 v178,v178,v243,vcc\n"
MF"v[24:27],a[84:87],a[236:239],v[24:27]\n"
RD"a[112:115],v236 offset:20480\n"
"v_cndmask_b32_e64 v179,v179,v243,s[94:95]\n"
MF"v[64:67],a[84:87],a[240:243],v[64:67]\n"
BL"a[44:47],v224,s[8:11],s53 offen offset:1024 sc1\n"
MF"v[64:67],a[80:83],a[232:235],v[64:67]\n"
RD"a[116:119],v236 offset:21504\n"
"v_add_u32 v243,16,v243\n"
MF"v[28:31],a[88:91],a[228:231],v[28:31]\n"
"v_readlane_b32 s92,v234,2\n"
"s_and_b32 s92,s92,0xffff\n"
MF"v[28:31],a[92:95],a[236:239],v[28:31]\n"
RD"a[120:123],v236 offset:22528\n"
"v_cmp_eq_u32 vcc,s92,v244\n"
MF"v[68:71],a[92:95],a[240:243],v[68:71]\n"
"v_cmp_eq_u32_e64 s[94:95],s92,v245\n"
MF"v[68:71],a[88:91],a[232:235],v[68:71]\n"
RD"a[124:127],v236 offset:23552\n"
"v_cndmask_b32 v178,v178,v243,vcc\n"
MF"v[32:35],a[96:99],a[180:183],v[32:35]\n"
WT"lgkmcnt(3)\n"
BL"a[48:51],v224,s[8:11],s52 offen offset:2048 sc1\n"
MF"v[32:35],a[100:103],a[188:191],v[32:35]\n"
SA"s90,s88,3072\n"
RD"a[64:67],v237 offset:8192\n"
"v_cndmask_b32_e64 v179,v179,v243,s[94:95]\n"
MF"v[72:75],a[100:103],a[192:195],v[72:75]\n"
PK"v24,v24,v25\n"
PK"v25,v26,v27\n"
MF"v[72:75],a[96:99],a[184:187],v[72:75]\n"
RD"a[68:71],v237 offset:9216\n"
"v_add_u32 v243,16,v243\n"
MF"v[36:39],a[104:107],a[180:183],v[36:39]\n"
PK"v26,v28,v29\n"
PK"v27,v30,v31\n"
MF"v[36:39],a[108:111],a[188:191],v[36:39]\n"
BS"v[24:27],v224,s[8:11],s90 offen sc1\n"
"v_readlane_b32 s92,v234,3\n"
MF"v[76:79],a[108:111],a[192:195],v[76:79]\n"
"s_and_b32 s92,s92,0xffff\n"
RD"a[72:75],v237 offset:10240\n"
"v_cmp_eq_u32 vcc,s92,v244\n"
MF"v[76:79],a[104:107],a[184:187],v[76:79]\n"
BL"a[52:55],v224,s[8:11],s53 offen offset:2048 sc1\n"
MF"v[32:35],a[112:115],a[196:199],v[32:35]\n"
WT"lgkmcnt(2)\n"
RD"a[76:79],v237 offset:11264\n"
"v_cmp_eq_u32_e64 s[94:95],s92,v245\n"
MF"v[32:35],a[116:119],a[204:207],v[32:35]\n"
SA"s91,s22,3072\n"
RD"a[80:83],v237 offset:28672\n"
"v_cndmask_b32 v178,v178,v243,vcc\n"
MF"v[72:75],a[116:119],a[208:211],v[72:75]\n"
"v_cndmask_b32_e64 v179,v179,v243,s[94:95]\n"
"v_add_u32 v243,16,v243\n"
MF"v[72:75],a[112:115],a[200:203],v[72:75]\n"
RD"a[84:87],v237 offset:29696\n"
PK"v64,v64,v65\n"
MF"v[36:39],a[120:123],a[196:199],v[36:39]\n"
BL"a[56:59],v224,s[8:11],s52 offen offset:3072 sc1\n"
PK"v65,v66,v67\n"
MF"v[36:39],a[124:127],a[204:207],v[36:39]\n"
RD"a[88:91],v237 offset:30720\n"
"v_readlane_b32 s92,v234,4\n"
PK"v66,v68,v69\n"
MF"v[76:79],a[124:127],a[208:211],v[76:79]\n"
"s_and_b32 s92,s92,0xffff\n"
PK"v67,v70,v71\n"
"v_cmp_eq_u32 vcc,s92,v244\n"
MF"v[76:79],a[120:123],a[200:203],v[76:79]\n"
BS"v[64:67],v224,s[8:11],s91 offen sc1\n"
"v_cmp_eq_u32_e64 s[94:95],s92,v245\n"
MF"v[32:35],a[64:67],a[212:215],v[32:35]\n"
WT"lgkmcnt(2)\n"
RD"a[92:95],v237 offset:31744\n"
"v_cndmask_b32 v178,v178,v243,vcc\n"
MF"v[32:35],a[68:71],a[220:223],v[32:35]\n"
BL"a[60:63],v224,s[8:11],s53 offen offset:3072 sc1\n"
"v_cndmask_b32_e64 v179,v179,v243,s[94:95]\n"
MF"v[72:75],a[68:71],a[224:227],v[72:75]\n"
RD"a[96:99],v226 offset:8192\n"
"v_add_u32 v243,16,v243\n"
MF"v[72:75],a[64:67],a[216:219],v[72:75]\n"
RD"a[100:103],v226 offset:9216\n"
"v_readlane_b32 s92,v234,5\n"
MF"v[36:39],a[72:75],a[212:215],v[36:39]\n"
"s_and_b32 s92,s92,0xffff\n"
"v_cmp_eq_u32 vcc,s92,v244\n"
"v_cmp_eq_u32_e64 s[94:95],s92,v245\n"
MF"v[36:39],a[76:79],a[220:223],v[36:39]\n"
RD"a[104:107],v226 offset:10240\n"
"v_cndmask_b32 v178,v178,v243,vcc\n"
MF"v[76:79],a[76:79],a[224:227],v[76:79]\n"
"v_cndmask_b32_e64 v179,v179,v243,s[94:95]\n"
"v_add_u32 v243,16,v243\n"
MF"v[76:79],a[72:75],a[216:219],v[76:79]\n"
RD"a[108:111],v226 offset:11264\n"
"v_readlane_b32 s92,v234,6\n"
MF"v[32:35],a[80:83],a[228:231],v[32:35]\n"
WT"lgkmcnt(4)\n"
"s_and_b32 s92,s92,0xffff\n"
MF"v[32:35],a[84:87],a[236:239],v[32:35]\n"
RD"a[112:115],v226 offset:28672\n"
"v_cmp_eq_u32 vcc,s92,v244\n"
MF"v[72:75],a[84:87],a[240:243],v[72:75]\n"
"v_cmp_eq_u32_e64 s[94:95],s92,v245\n"
MF"v[72:75],a[80:83],a[232:235],v[72:75]\n"
RD"a[116:119],v226 offset:29696\n"
"v_cndmask_b32 v178,v178,v243,vcc\n"
MF"v[36:39],a[88:91],a[228:231],v[36:39]\n"
"v_cndmask_b32_e64 v179,v179,v243,s[94:95]\n"
MF"v[36:39],a[92:95],a[236:239],v[36:39]\n"
RD"a[120:123],v226 offset:30720\n"
"v_add_u32 v243,16,v243\n"
MF"v[76:79],a[92:95],a[240:243],v[76:79]\n"
"v_readlane_b32 s92,v234,7\n"
MF"v[76:79],a[88:91],a[232:235],v[76:79]\n"
WT"vmcnt(31) lgkmcnt(3)\n"
RD"a[124:127],v226 offset:31744\n"
"s_and_b32 s92,s92,0xffff\n"
MF"v[84:87],a[96:99],a[244:247],v[84:87]\n"
WT"vmcnt(29)\n"
"v_cmp_eq_u32 vcc,s92,v244\n"
MF"v[84:87],a[100:103],a[252:255],v[84:87]\n"
WT"vmcnt(28)\n"
RD"a[64:67],v235 offset:16384\n"
PK"v32,v32,v33\n"
MF"v[164:167],a[100:103],a[128:131],v[164:167]\n"
SA"s90,s88,4096\n"
PK"v33,v34,v35\n"
"v_cmp_eq_u32_e64 s[94:95],s92,v245\n"
MF"v[164:167],a[96:99],a[248:251],v[164:167]\n"
RD"a[68:71],v235 offset:17408\n"
PK"v34,v36,v37\n"
MF"v[92:95],a[104:107],a[244:247],v[92:95]\n"
"s_mul_i32 s92,s33,5\n"
PK"v35,v38,v39\n"
"v_cndmask_b32 v178,v178,v243,vcc\n"
MF"v[92:95],a[108:111],a[252:255],v[92:95]\n"
SA"s93,s92,s30\n"
BS"v[32:35],v224,s[8:11],s90 offen sc1\n"
MF"v[172:175],a[108:111],a[128:131],v[172:175]\n"
SA"s92,s92,4\n"
RD"a[72:75],v235 offset:18432\n"
"v_cndmask_b32_e64 v179,v179,v243,s[94:95]\n"
MF"v[172:175],a[104:107],a[248:251],v[172:175]\n"
WT"vmcnt(27) lgkmcnt(3)\n"
RD"a[76:79],v235 offset:19456\n"
"s_lshl_b32 s38,s93,6\n"
MF"v[84:87],a[112:115],a[132:135],v[84:87]\n"
WT"vmcnt(24)\n"
"s_lshl_b32 s55,s92,6\n"
MF"v[84:87],a[116:119],a[140:143],v[84:87]\n"
WT"vmcnt(23)\n"
RD"a[80:83],v236 offset:4096\n"
PK"v72,v72,v73\n"
MF"v[164:167],a[116:119],a[144:147],v[164:167]\n"
SA"s91,s22,4096\n"
PK"v73,v74,v75\n"
PK"v74,v76,v77\n"
MF"v[164:167],a[112:115],a[136:139],v[164:167]\n"
SA"s55,s55,s45\n"
RD"a[84:87],v236 offset:5120\n"
PK"v75,v78,v79\n"
MF"v[92:95],a[120:123],a[132:135],v[92:95]\n"
"s_lshl_b32 s36,s93,10\n"
BS"v[72:75],v224,s[8:11],s91 offen sc1\n"
MF"v[92:95],a[124:127],a[140:143],v[92:95]\n"
"s_lshl_b32 s37,s92,10\n"
RD"a[88:91],v236 offset:6144\n"
SA"s36,s36,65536\n"
MF"v[172:175],a[124:127],a[144:147],v[172:175]\n"
SA"s37,s37,65536\n"
SA"s37,s37,s45\n"
MF"v[172:175],a[120:123],a[136:139],v[172:175]\n"
WT"vmcnt(23) lgkmcnt(3)\n"
RD"a[92:95],v236 offset:7168\n"
"v_lshrrev_b32 v243,1,v224\n"
MF"v[84:87],a[64:67],a[148:151],v[84:87]\n"
WT"vmcnt(20)\n"
"v_min_u32 v244,312,v243\n"
MF"v[84:87],a[68:71],a[156:159],v[84:87]\n"
WT"vmcnt(18)\n"
RD"a[96:99],v236 offset:24576\n"
"v_and_b32 v243,56,v243\n"
MF"v[164:167],a[68:71],a[160:163],v[164:167]\n"
"v_lshlrev_b32 v243,3,v243\n"
MF"v[164:167],a[64:67],a[152:155],v[164:167]\n"
"s_mov_b32 s92,6400\n"
RD"a[100:103],v236 offset:25600\n"
MF"v[92:95],a[72:75],a[148:151],v[92:95]\n"
MF"v[92:95],a[76:79],a[156:159],v[92:95]\n"
RD"a[104:107],v236 offset:26624\n"
MF"v[172:175],a[76:79],a[160:163],v[172:175]\n"
MF"v[172:175],a[72:75],a[152:155],v[172:175]\n"
WT"vmcnt(17) lgkmcnt(3)\n"
RD"a[108:111],v236 offset:27648\n"
MF"v[84:87],a[80:83],a[164:167],v[84:87]\n"
WT"vmcnt(15)\n"
MF"v[84:87],a[84:87],a[172:175],v[84:87]\n"
WT"vmcnt(14)\n"
RD"a[112:115],v237 offset:12288\n"
MF"v[164:167],a[84:87],a[176:179],v[164:167]\n"
MF"v[164:167],a[80:83],a[168:171],v[164:167]\n"
RD"a[116:119],v237 offset:13312\n"
MF"v[92:95],a[88:91],a[164:167],v[92:95]\n"
MF"v[92:95],a[92:95],a[172:175],v[92:95]\n"
RD"a[120:123],v237 offset:14336\n"
MF"v[172:175],a[92:95],a[176:179],v[172:175]\n"
MF"v[172:175],a[88:91],a[168:171],v[172:175]\n"
WT"vmcnt(12) lgkmcnt(3)\n"
RD"a[124:127],v237 offset:15360\n"
MF"v[84:87],a[96:99],a[32:35],v[84:87]\n"
WT"vmcnt(9)\n"
MF"v[84:87],a[100:103],a[40:43],v[84:87]\n"
WT"vmcnt(8)\n"
RD"a[64:67],v226 offset:12288\n"
MF"v[164:167],a[100:103],a[44:47],v[164:167]\n"
MF"v[164:167],a[96:99],a[36:39],v[164:167]\n"
RD"a[68:71],v226 offset:13312\n"
MF"v[92:95],a[104:107],a[32:35],v[92:95]\n"
MF"v[92:95],a[108:111],a[40:43],v[92:95]\n"
RD"a[72:75],v226 offset:14336\n"
MF"v[172:175],a[108:111],a[44:47],v[172:175]\n"
MF"v[172:175],a[104:107],a[36:39],v[172:175]\n"
WT"vmcnt(7) lgkmcnt(3)\n"
RD"a[76:79],v226 offset:15360\n"
MF"v[84:87],a[112:115],a[48:51],v[84:87]\n"
WT"vmcnt(4)\n"
"buffer_load_dwordx2 v[240:241],v244,s[8:11],s92 offen sc1\n"
MF"v[84:87],a[116:119],a[56:59],v[84:87]\n"
WT"vmcnt(3)\n"
RD"a[80:83],v235 offset:0\n"
MF"v[164:167],a[116:119],a[60:63],v[164:167]\n"
MF"v[164:167],a[112:115],a[52:55],v[164:167]\n"
RD"a[84:87],v235 offset:1024\n"
MF"v[92:95],a[120:123],a[48:51],v[92:95]\n"
MF"v[92:95],a[124:127],a[56:59],v[92:95]\n"
RD"a[88:91],v235 offset:2048\n"
MF"v[172:175],a[124:127],a[60:63],v[172:175]\n"
MF"v[172:175],a[120:123],a[52:55],v[172:175]\n"
WT"lgkmcnt(3)\n"
RD"a[92:95],v235 offset:3072\n"
MF"v[100:103],a[64:67],a[244:247],v[100:103]\n"
MF"v[100:103],a[68:71],a[252:255],v[100:103]\n"
SA"s90,s88,327680\n"
RD"a[96:99],v235 offset:20480\n"
PK"v84,v84,v85\n"
MF"v[180:183],a[68:71],a[128:131],v[180:183]\n"
PK"v85,v86,v87\n"
PK"v86,v92,v93\n"
MF"v[180:183],a[64:67],a[248:251],v[180:183]\n"
RD"a[100:103],v235 offset:21504\n"
PK"v87,v94,v95\n"
MF"v[108:111],a[72:75],a[244:247],v[108:111]\n"
BS"v[84:87],v224,s[8:11],s90 offen sc1\n"
MF"v[108:111],a[76:79],a[252:255],v[108:111]\n"
RD"a[104:107],v235 offset:22528\n"
MF"v[188:191],a[76:79],a[128:131],v[188:191]\n"
MF"v[188:191],a[72:75],a[248:251],v[188:191]\n"
WT"lgkmcnt(3)\n"
RD"a[108:111],v235 offset:23552\n"
MF"v[100:103],a[80:83],a[132:135],v[100:103]\n"
MF"v[100:103],a[84:87],a[140:143],v[100:103]\n"
SA"s91,s22,327680\n"
RD"a[112:115],v236 offset:8192\n"
PK"v164,v164,v165\n"
MF"v[180:183],a[84:87],a[144:147],v[180:183]\n"
PK"v165,v166,v167\n"
PK"v166,v172,v173\n"
MF"v[180:183],a[80:83],a[136:139],v[180:183]\n"
RD"a[116:119],v236 offset:9216\n"
PK"v167,v174,v175\n"
MF"v[108:111],a[88:91],a[132:135],v[108:111]\n"
BS"v[164:167],v224,s[8:11],s91 offen sc1\n"
MF"v[108:111],a[92:95],a[140:143],v[108:111]\n"
RD"a[120:123],v236 offset:10240\n"
MF"v[188:191],a[92:95],a[144:147],v[188:191]\n"
MF"v[188:191],a[88:91],a[136:139],v[188:191]\n"
WT"lgkmcnt(3)\n"
RD"a[124:127],v236 offset:11264\n"
MF"v[100:103],a[96:99],a[148:151],v[100:103]\n"
MF"v[100:103],a[100:103],a[156:159],v[100:103]\n"
RD"a[64:67],v236 offset:28672\n"
MF"v[180:183],a[100:103],a[160:163],v[180:183]\n"
WT"vmcnt(3)\n"
"buffer_store_dwordx2 v[232:233],v239,s[8:11],s89 offen sc1\n"
MF"v[180:183],a[96:99],a[152:155],v[180:183]\n"
RD"a[68:71],v236 offset:29696\n"
MF"v[108:111],a[104:107],a[148:151],v[108:111]\n"
MF"v[108:111],a[108:111],a[156:159],v[108:111]\n"
RD"a[72:75],v236 offset:30720\n"
MF"v[188:191],a[108:111],a[160:163],v[188:191]\n"
MF"v[188:191],a[104:107],a[152:155],v[188:191]\n"
WT"lgkmcnt(3)\n"
RD"a[76:79],v236 offset:31744\n"
MF"v[100:103],a[112:115],a[164:167],v[100:103]\n"
MF"v[100:103],a[116:119],a[172:175],v[100:103]\n"
RD"a[80:83],v237 offset:16384\n"
MF"v[180:183],a[116:119],a[176:179],v[180:183]\n"
MF"v[180:183],a[112:115],a[168:171],v[180:183]\n"
RD"a[84:87],v237 offset:17408\n"
MF"v[108:111],a[120:123],a[164:167],v[108:111]\n"
MF"v[108:111],a[124:127],a[172:175],v[108:111]\n"
RD"a[88:91],v237 offset:18432\n"
MF"v[188:191],a[124:127],a[176:179],v[188:191]\n"
MF"v[188:191],a[120:123],a[168:171],v[188:191]\n"
WT"lgkmcnt(3)\n"
RD"a[92:95],v237 offset:19456\n"
MF"v[100:103],a[64:67],a[32:35],v[100:103]\n"
MF"v[100:103],a[68:71],a[40:43],v[100:103]\n"
RD"a[96:99],v226 offset:16384\n"
MF"v[180:183],a[68:71],a[44:47],v[180:183]\n"
MF"v[180:183],a[64:67],a[36:39],v[180:183]\n"
RD"a[100:103],v226 offset:17408\n"
MF"v[108:111],a[72:75],a[32:35],v[108:111]\n"
MF"v[108:111],a[76:79],a[40:43],v[108:111]\n"
RD"a[104:107],v226 offset:18432\n"
MF"v[188:191],a[76:79],a[44:47],v[188:191]\n"
MF"v[188:191],a[72:75],a[36:39],v[188:191]\n"
WT"lgkmcnt(3)\n"
RD"a[108:111],v226 offset:19456\n"
MF"v[100:103],a[80:83],a[48:51],v[100:103]\n"
MF"v[100:103],a[84:87],a[56:59],v[100:103]\n"
RD"a[112:115],v235 offset:4096\n"
MF"v[180:183],a[84:87],a[60:63],v[180:183]\n"
MF"v[180:183],a[80:83],a[52:55],v[180:183]\n"
RD"a[116:119],v235 offset:5120\n"
MF"v[108:111],a[88:91],a[48:51],v[108:111]\n"
MF"v[108:111],a[92:95],a[56:59],v[108:111]\n"
RD"a[120:123],v235 offset:6144\n"
MF"v[188:191],a[92:95],a[60:63],v[188:191]\n"
MF"v[188:191],a[88:91],a[52:55],v[188:191]\n"
WT"lgkmcnt(3)\n"
RD"a[124:127],v235 offset:7168\n"
MF"v[116:119],a[96:99],a[244:247],v[116:119]\n"
MF"v[116:119],a[100:103],a[252:255],v[116:119]\n"
SA"s90,s88,328704\n"
RD"a[64:67],v235 offset:24576\n"
PK"v100,v100,v101\n"
MF"v[196:199],a[100:103],a[128:131],v[196:199]\n"
PK"v101,v102,v103\n"
PK"v102,v108,v109\n"
MF"v[196:199],a[96:99],a[248:251],v[196:199]\n"
RD"a[68:71],v235 offset:25600\n"
PK"v103,v110,v111\n"
MF"v[124:127],a[104:107],a[244:247],v[124:127]\n"
BS"v[100:103],v224,s[8:11],s90 offen sc1\n"
MF"v[124:127],a[108:111],a[252:255],v[124:127]\n"
WT"vmcnt(4)\n"
"v_cmp_eq_u64 vcc,s[28:29],v[240:241]\n"
"s_cmp_eq_u64 vcc,exec\n"
"s_cbranch_scc0 L1086_%=\n"
"L1087_%=:\n"
MF"v[204:207],a[108:111],a[128:131],v[204:207]\n"
RD"a[72:75],v235 offset:26624\n"
MF"v[204:207],a[104:107],a[248:251],v[204:207]\n"
WT"lgkmcnt(3)\n"
RD"a[76:79],v235 offset:27648\n"
MF"v[116:119],a[112:115],a[132:135],v[116:119]\n"
BL"v[112:115],v176,s[12:15],s38 offen\n"
MF"v[116:119],a[116:119],a[140:143],v[116:119]\n"
SA"s91,s22,328704\n"
RD"a[80:83],v236 offset:12288\n"
PK"v180,v180,v181\n"
MF"v[196:199],a[116:119],a[144:147],v[196:199]\n"
BL"v[144:147],v178,s[8:11],s36 offen sc1\n"
MF"v[196:199],a[112:115],a[136:139],v[196:199]\n"
RD"a[84:87],v236 offset:13312\n"
PK"v181,v182,v183\n"
MF"v[124:127],a[120:123],a[132:135],v[124:127]\n"
PK"v182,v188,v189\n"
PK"v183,v190,v191\n"
MF"v[124:127],a[124:127],a[140:143],v[124:127]\n"
BS"v[180:183],v224,s[8:11],s91 offen sc1\n"
MF"v[204:207],a[124:127],a[144:147],v[204:207]\n"
RD"a[88:91],v236 offset:14336\n"
MF"v[204:207],a[120:123],a[136:139],v[204:207]\n"
WT"lgkmcnt(3)\n"
BL"v[120:123],v176,s[12:15],s55 offen\n"
MF"v[116:119],a[64:67],a[148:151],v[116:119]\n"
RD"a[92:95],v236 offset:15360\n"
MF"v[116:119],a[68:71],a[156:159],v[116:119]\n"
BL"v[152:155],v178,s[8:11],s37 offen sc1\n"
MF"v[196:199],a[68:71],a[160:163],v[196:199]\n"
RD"a[96:99],v237 offset:0\n"
MF"v[196:199],a[64:67],a[152:155],v[196:199]\n"
RD"a[100:103],v237 offset:1024\n"
MF"v[124:127],a[72:75],a[148:151],v[124:127]\n"
BL"v[128:131],v177,s[12:15],s38 offen\n"
MF"v[124:127],a[76:79],a[156:159],v[124:127]\n"
RD"a[104:107],v237 offset:2048\n"
MF"v[204:207],a[76:79],a[160:163],v[204:207]\n"
BL"v[160:163],v179,s[8:11],s36 offen sc1\n"
MF"v[204:207],a[72:75],a[152:155],v[204:207]\n"
WT"lgkmcnt(3)\n"
RD"a[108:111],v237 offset:3072\n"
MF"v[116:119],a[80:83],a[164:167],v[116:119]\n"
MF"v[116:119],a[84:87],a[172:175],v[116:119]\n"
RD"a[112:115],v237 offset:20480\n"
MF"v[196:199],a[84:87],a[176:179],v[196:199]\n"
BL"v[136:139],v177,s[12:15],s55 offen\n"
MF"v[196:199],a[80:83],a[168:171],v[196:199]\n"
RD"a[116:119],v237 offset:21504\n"
MF"v[124:127],a[88:91],a[164:167],v[124:127]\n"
BL"v[168:171],v179,s[8:11],s37 offen sc1\n"
MF"v[124:127],a[92:95],a[172:175],v[124:127]\n"
RD"a[120:123],v237 offset:22528\n"
MF"v[204:207],a[92:95],a[176:179],v[204:207]\n"
MF"v[204:207],a[88:91],a[168:171],v[204:207]\n"
WT"lgkmcnt(3)\n"
RD"a[124:127],v237 offset:23552\n"
MF"v[116:119],a[96:99],a[32:35],v[116:119]\n"
MF"v[116:119],a[100:103],a[40:43],v[116:119]\n"
RD"a[64:67],v226 offset:20480\n"
MF"v[196:199],a[100:103],a[44:47],v[196:199]\n"
"s_lshr_b32 s93,s31,3\n"
MF"v[196:199],a[96:99],a[36:39],v[196:199]\n"
"s_lshl_b32 s93,s93,4\n"
RD"a[68:71],v226 offset:21504\n"
MF"v[124:127],a[104:107],a[32:35],v[124:127]\n"
SA"s93,s93,s32\n"
MF"v[124:127],a[108:111],a[40:43],v[124:127]\n"
"s_mul_i32 s94,s93,40960\n"
RD"a[72:75],v226 offset:22528\n"
MF"v[204:207],a[108:111],a[44:47],v[204:207]\n"
"s_lshl_b32 s93,s93,9\n"
MF"v[204:207],a[104:107],a[36:39],v[204:207]\n"
WT"lgkmcnt(3)\n"
RD"a[76:79],v226 offset:23552\n"
SA"s93,s93,80478208\n"
MF"v[116:119],a[112:115],a[48:51],v[116:119]\n"
SA"s94,s94,59506688\n"
MF"v[116:119],a[116:119],a[56:59],v[116:119]\n"
"s_lshl_b32 s92,s30,10\n"
RD"a[80:83],v235 offset:8192\n"
MF"v[196:199],a[116:119],a[60:63],v[196:199]\n"
"v_add_u32 v244,s92,v224\n"
MF"v[196:199],a[112:115],a[52:55],v[196:199]\n"
SA"s92,s45,4096\n"
RD"a[84:87],v235 offset:9216\n"
MF"v[124:127],a[120:123],a[48:51],v[124:127]\n"
"v_add_u32 v245,s92,v224\n"
MF"v[124:127],a[124:127],a[56:59],v[124:127]\n"
RD"a[88:91],v235 offset:10240\n"
MF"v[204:207],a[124:127],a[60:63],v[204:207]\n"
"v_and_b32 v246,128,v224\n"
MF"v[204:207],a[120:123],a[52:55],v[204:207]\n"
WT"lgkmcnt(3)\n"
RD"a[92:95],v235 offset:11264\n"
"v_lshl_add_u32 v243,v246,5,v243\n"
MF"v[132:135],a[64:67],a[244:247],v[132:135]\n"
"v_lshrrev_b32 v246,8,v224\n"
MF"v[132:135],a[68:71],a[252:255],v[132:135]\n"
SA"s90,s88,329728\n"
RD"a[96:99],v235 offset:28672\n"
PK"v116,v116,v117\n"
MF"v[208:211],a[68:71],a[128:131],v[208:211]\n"
"s_mov_b32 s95,0xffff0101\n"
PK"v117,v118,v119\n"
"v_lshl_add_u32 v243,v246,27,v243\n"
MF"v[208:211],a[64:67],a[248:251],v[208:211]\n"
"s_lshl_b32 s92,s30,10\n"
RD"a[100:103],v235 offset:29696\n"
PK"v118,v124,v125\n"
MF"v[140:143],a[72:75],a[244:247],v[140:143]\n"
PK"v119,v126,v127\n"
"v_add_u32 v247,s92,v227\n"
MF"v[140:143],a[76:79],a[252:255],v[140:143]\n"
BS"v[116:119],v224,s[8:11],s90 offen sc1\n"
MF"v[212:215],a[76:79],a[128:131],v[212:215]\n"
RD"a[104:107],v235 offset:30720\n"
MF"v[212:215],a[72:75],a[248:251],v[212:215]\n"
WT"lgkmcnt(3)\n"
"buffer_load_dwordx2 v[240:241],v243,s[8:11],s93 offen sc1\n"
MF"v[132:135],a[80:83],a[132:135],v[132:135]\n"
RD"a[108:111],v235 offset:31744\n"
MF"v[132:135],a[84:87],a[140:143],v[132:135]\n"
SA"s91,s22,329728\n"
RD"a[112:115],v236 offset:16384\n"
PK"v196,v196,v197\n"
MF"v[208:211],a[84:87],a[144:147],v[208:211]\n"
PK"v197,v198,v199\n"
PK"v198,v204,v205\n"
MF"v[208:211],a[80:83],a[136:139],v[208:211]\n"
RD"a[116:119],v236 offset:17408\n"
PK"v199,v206,v207\n"
MF"v[140:143],a[88:91],a[132:135],v[140:143]\n"
BS"v[196:199],v224,s[8:11],s91 offen sc1\n"
MF"v[140:143],a[92:95],a[140:143],v[140:143]\n"
RD"a[120:123],v236 offset:18432\n"
MF"v[212:215],a[92:95],a[144:147],v[212:215]\n"
MF"v[212:215],a[88:91],a[136:139],v[212:215]\n"
WT"lgkmcnt(3)\n"
RD"a[124:127],v236 offset:19456\n"
MF"v[132:135],a[96:99],a[148:151],v[132:135]\n"
MF"v[132:135],a[100:103],a[156:159],v[132:135]\n"
RD"a[64:67],v237 offset:4096\n"
MF"v[208:211],a[100:103],a[160:163],v[208:211]\n"
MF"v[208:211],a[96:99],a[152:155],v[208:211]\n"
RD"a[68:71],v237 offset:5120\n"
MF"v[140:143],a[104:107],a[148:151],v[140:143]\n"
MF"v[140:143],a[108:111],a[156:159],v[140:143]\n"
RD"a[72:75],v237 offset:6144\n"
MF"v[212:215],a[108:111],a[160:163],v[212:215]\n"
MF"v[212:215],a[104:107],a[152:155],v[212:215]\n"
WT"lgkmcnt(3)\n"
RD"a[76:79],v237 offset:7168\n"
MF"v[132:135],a[112:115],a[164:167],v[132:135]\n"
MF"v[132:135],a[116:119],a[172:175],v[132:135]\n"
RD"a[80:83],v237 offset:24576\n"
MF"v[208:211],a[116:119],a[176:179],v[208:211]\n"
MF"v[208:211],a[112:115],a[168:171],v[208:211]\n"
RD"a[84:87],v237 offset:25600\n"
MF"v[140:143],a[120:123],a[164:167],v[140:143]\n"
MF"v[140:143],a[124:127],a[172:175],v[140:143]\n"
RD"a[88:91],v237 offset:26624\n"
MF"v[212:215],a[124:127],a[176:179],v[212:215]\n"
MF"v[212:215],a[120:123],a[168:171],v[212:215]\n"
WT"lgkmcnt(3)\n"
RD"a[92:95],v237 offset:27648\n"
MF"v[132:135],a[64:67],a[32:35],v[132:135]\n"
MF"v[132:135],a[68:71],a[40:43],v[132:135]\n"
RD"a[96:99],v226 offset:24576\n"
MF"v[208:211],a[68:71],a[44:47],v[208:211]\n"
MF"v[208:211],a[64:67],a[36:39],v[208:211]\n"
RD"a[100:103],v226 offset:25600\n"
MF"v[140:143],a[72:75],a[32:35],v[140:143]\n"
MF"v[140:143],a[76:79],a[40:43],v[140:143]\n"
RD"a[104:107],v226 offset:26624\n"
MF"v[212:215],a[76:79],a[44:47],v[212:215]\n"
MF"v[212:215],a[72:75],a[36:39],v[212:215]\n"
WT"lgkmcnt(3)\n"
RD"a[108:111],v226 offset:27648\n"
MF"v[132:135],a[80:83],a[48:51],v[132:135]\n"
MF"v[132:135],a[84:87],a[56:59],v[132:135]\n"
RD"a[112:115],v235 offset:12288\n"
MF"v[208:211],a[84:87],a[60:63],v[208:211]\n"
WT"vmcnt(1)\n"
"v_cmp_eq_u64 vcc,s[28:29],v[240:241]\n"
MF"v[208:211],a[80:83],a[52:55],v[208:211]\n"
"s_and_b32 s50,vcc_lo,0xff\n"
RD"a[116:119],v235 offset:13312\n"
"s_or_b32 s95,s95,s50\n"
MF"v[140:143],a[88:91],a[48:51],v[140:143]\n"
"s_bitcmp1_b32 s95,1\n"
"s_cselect_b32 s51,s94,1065353216\n"
MF"v[140:143],a[92:95],a[56:59],v[140:143]\n"
SA"s92,s51,5120\n"
RD"a[120:123],v235 offset:14336\n"
MF"v[212:215],a[92:95],a[60:63],v[212:215]\n"
BL"a[184:187],v244,s[8:11],s92 offen sc1\n"
MF"v[212:215],a[88:91],a[52:55],v[212:215]\n"
WT"lgkmcnt(3)\n"
RD"a[124:127],v235 offset:15360\n"
MF"v[148:151],a[96:99],a[244:247],v[148:151]\n"
BL"a[216:219],v245,s[8:11],s92 offen sc1\n"
MF"v[148:151],a[100:103],a[252:255],v[148:151]\n"
SA"s90,s88,330752\n"
RD"a[64:67],v236 offset:0\n"
PK"v132,v132,v133\n"
MF"v[216:219],a[100:103],a[128:131],v[216:219]\n"
"s_bitcmp1_b32 s95,2\n"
PK"v133,v134,v135\n"
PK"v134,v140,v141\n"
MF"v[216:219],a[96:99],a[248:251],v[216:219]\n"
"s_cselect_b32 s51,s94,1065353216\n"
RD"a[68:71],v236 offset:1024\n"
PK"v135,v142,v143\n"
MF"v[156:159],a[104:107],a[244:247],v[156:159]\n"
SA"s92,s51,10240\n"
BS"v[132:135],v224,s[8:11],s90 offen sc1\n"
MF"v[156:159],a[108:111],a[252:255],v[156:159]\n"
BL"a[188:191],v244,s[8:11],s92 offen sc1\n"
MF"v[220:223],a[108:111],a[128:131],v[220:223]\n"
RD"a[72:75],v236 offset:2048\n"
MF"v[220:223],a[104:107],a[248:251],v[220:223]\n"
WT"lgkmcnt(3)\n"
BL"a[220:223],v245,s[8:11],s92 offen sc1\n"
MF"v[148:151],a[112:115],a[132:135],v[148:151]\n"
"s_bitcmp1_b32 s95,3\n"
RD"a[76:79],v236 offset:3072\n"
"s_cselect_b32 s51,s94,1065353216\n"
MF"v[148:151],a[116:119],a[140:143],v[148:151]\n"
SA"s91,s22,330752\n"
RD"a[80:83],v236 offset:20480\n"
PK"v208,v208,v209\n"
MF"v[216:219],a[116:119],a[144:147],v[216:219]\n"
SA"s92,s51,15360\n"
PK"v209,v210,v211\n"
PK"v210,v212,v213\n"
MF"v[216:219],a[112:115],a[136:139],v[216:219]\n"
BL"a[192:195],v244,s[8:11],s92 offen sc1\n"
MF"v[156:159],a[120:123],a[132:135],v[156:159]\n"
RD"a[84:87],v236 offset:21504\n"
PK"v211,v214,v215\n"
MF"v[156:159],a[124:127],a[140:143],v[156:159]\n"
BS"v[208:211],v224,s[8:11],s91 offen sc1\n"
MF"v[220:223],a[124:127],a[144:147],v[220:223]\n"
"s_bitcmp1_b32 s95,4\n"
BL"a[224:227],v245,s[8:11],s92 offen sc1\n"
MF"v[220:223],a[120:123],a[136:139],v[220:223]\n"
WT"lgkmcnt(2)\n"
RD"a[88:91],v236 offset:22528\n"
RD"a[92:95],v236 offset:23552\n"
MF"v[148:151],a[64:67],a[148:151],v[148:151]\n"
"s_cselect_b32 s51,s94,1065353216\n"
SA"s92,s51,20480\n"
MF"v[148:151],a[68:71],a[156:159],v[148:151]\n"
BL"a[196:199],v244,s[8:11],s92 offen sc1\n"
MF"v[216:219],a[68:71],a[160:163],v[216:219]\n"
RD"a[96:99],v237 offset:8192\n"
MF"v[216:219],a[64:67],a[152:155],v[216:219]\n"
BL"a[228:231],v245,s[8:11],s92 offen sc1\n"
MF"v[156:159],a[72:75],a[148:151],v[156:159]\n"
"s_bitcmp1_b32 s95,5\n"
RD"a[100:103],v237 offset:9216\n"
"s_cselect_b32 s51,s94,1065353216\n"
MF"v[156:159],a[76:79],a[156:159],v[156:159]\n"
SA"s92,s51,25600\n"
RD"a[104:107],v237 offset:10240\n"
MF"v[220:223],a[76:79],a[160:163],v[220:223]\n"
BL"a[200:203],v244,s[8:11],s92 offen sc1\n"
MF"v[220:223],a[72:75],a[152:155],v[220:223]\n"
WT"lgkmcnt(3)\n"
RD"a[108:111],v237 offset:11264\n"
MF"v[148:151],a[80:83],a[164:167],v[148:151]\n"
BL"a[232:235],v245,s[8:11],s92 offen sc1\n"
MF"v[148:151],a[84:87],a[172:175],v[148:151]\n"
RD"a[112:115],v237 offset:28672\n"
MF"v[216:219],a[84:87],a[176:179],v[216:219]\n"
"s_bitcmp1_b32 s95,6\n"
"s_cselect_b32 s51,s94,1065353216\n"
MF"v[216:219],a[80:83],a[168:171],v[216:219]\n"
SA"s92,s51,30720\n"
RD"a[116:119],v237 offset:29696\n"
MF"v[156:159],a[88:91],a[164:167],v[156:159]\n"
BL"a[204:207],v244,s[8:11],s92 offen sc1\n"
MF"v[156:159],a[92:95],a[172:175],v[156:159]\n"
RD"a[120:123],v237 offset:30720\n"
MF"v[220:223],a[92:95],a[176:179],v[220:223]\n"
BL"a[236:239],v245,s[8:11],s92 offen sc1\n"
MF"v[220:223],a[88:91],a[168:171],v[220:223]\n"
WT"lgkmcnt(3)\n"
RD"a[124:127],v237 offset:31744\n"
MF"v[148:151],a[96:99],a[32:35],v[148:151]\n"
"s_bitcmp1_b32 s95,7\n"
"s_cselect_b32 s51,s94,1065353216\n"
MF"v[148:151],a[100:103],a[40:43],v[148:151]\n"
SA"s92,s51,35840\n"
MF"v[216:219],a[100:103],a[44:47],v[216:219]\n"
BL"a[208:211],v244,s[8:11],s92 offen sc1\n"
MF"v[216:219],a[96:99],a[36:39],v[216:219]\n"
MF"v[156:159],a[104:107],a[32:35],v[156:159]\n"
BL"a[240:243],v245,s[8:11],s92 offen sc1\n"
MF"v[156:159],a[108:111],a[40:43],v[156:159]\n"
MF"v[220:223],a[108:111],a[44:47],v[220:223]\n"
MF"v[220:223],a[104:107],a[36:39],v[220:223]\n"
WT"lgkmcnt(0)\n"
MF"v[148:151],a[112:115],a[48:51],v[148:151]\n"
MF"v[148:151],a[116:119],a[56:59],v[148:151]\n"
MF"v[216:219],a[116:119],a[60:63],v[216:219]\n"
MF"v[216:219],a[112:115],a[52:55],v[216:219]\n"
MF"v[156:159],a[120:123],a[48:51],v[156:159]\n"
MF"v[156:159],a[124:127],a[56:59],v[156:159]\n"
MF"v[220:223],a[124:127],a[60:63],v[220:223]\n"
MF"v[220:223],a[120:123],a[52:55],v[220:223]\n"
"s_nop 5\n"
PK"v148,v148,v149\n"
PK"v149,v150,v151\n"
PK"v150,v156,v157\n"
PK"v151,v158,v159\n"
SA"s90,s88,331776\n"
BS"v[148:151],v224,s[8:11],s90 offen sc1\n"
PK"v216,v216,v217\n"
PK"v217,v218,v219\n"
PK"v218,v220,v221\n"
PK"v219,v222,v223\n"
SA"s91,s22,331776\n"
BS"v[216:219],v224,s[8:11],s91 offen sc1\n"
"s_cmp_eq_u32 s23,0\n"
"s_cbranch_scc0 L1089_%=\n"
"ds_write_b128 v227,v[0:3] offset:0\n"
"ds_write_b128 v227,v[8:11] offset:1024\n"
"ds_write_b128 v227,v[16:19] offset:2048\n"
"ds_write_b128 v227,v[24:27] offset:3072\n"
"ds_write_b128 v227,v[32:35] offset:4096\n"
"ds_write_b128 v227,v[84:87] offset:5120\n"
"ds_write_b128 v227,v[100:103] offset:6144\n"
"ds_write_b128 v227,v[116:119] offset:7168\n"
"ds_write_b128 v227,v[132:135] offset:8192\n"
"ds_write_b128 v227,v[148:151] offset:9216\n"
"L1089_%=:\n"
"s_cmp_eq_u32 s23,4\n"
"s_cbranch_scc0 L1090_%=\n"
"ds_write_b128 v227,v[40:43] offset:0\n"
"ds_write_b128 v227,v[48:51] offset:1024\n"
"ds_write_b128 v227,v[56:59] offset:2048\n"
"ds_write_b128 v227,v[64:67] offset:3072\n"
"ds_write_b128 v227,v[72:75] offset:4096\n"
"ds_write_b128 v227,v[164:167] offset:5120\n"
"ds_write_b128 v227,v[180:183] offset:6144\n"
"ds_write_b128 v227,v[196:199] offset:7168\n"
"ds_write_b128 v227,v[208:211] offset:8192\n"
"ds_write_b128 v227,v[216:219] offset:9216\n"
"L1090_%=:\n"
MF"v[4:7],v[96:99],v[112:115],0\n"
MF"v[12:15],v[104:107],v[112:115],0\n"
MF"v[20:23],v[96:99],v[120:123],0\n"
MF"v[28:31],v[104:107],v[120:123],0\n"
MF"v[4:7],v[96:99],v[144:147],v[4:7]\n"
MF"v[12:15],v[104:107],v[144:147],v[12:15]\n"
MF"v[20:23],v[96:99],v[152:155],v[20:23]\n"
MF"v[28:31],v[104:107],v[152:155],v[28:31]\n"
MF"v[36:39],v[96:99],v[128:131],0\n"
MF"v[44:47],v[104:107],v[128:131],0\n"
MF"v[52:55],v[96:99],v[136:139],0\n"
MF"v[60:63],v[104:107],v[136:139],0\n"
MF"v[36:39],v[96:99],v[160:163],v[36:39]\n"
MF"v[44:47],v[104:107],v[160:163],v[44:47]\n"
MF"v[52:55],v[96:99],v[168:171],v[52:55]\n"
MF"v[60:63],v[104:107],v[168:171],v[60:63]\n"
SA"s89,s89,4096\n"
"s_mov_b32 s54,1048576\n"
WT"vmcnt(0)\n"
"buffer_store_dwordx2 v[232:233],v239,s[8:11],s89 offen sc1\n"
"buffer_load_dwordx2 v[240:241],v243,s[8:11],s93 offen sc1\n"
WT"lgkmcnt(0)\n"
"s_barrier\n"
RD"a[180:183],v247 offset:0\n"
RD"a[212:215],v227 offset:4096\n"
RD"a[64:67],v247 offset:5120\n"
RD"a[96:99],v227 offset:9216\n"
"s_getpc_b64 s[52:53]\n"
"L1097_%=:\n"
SA"s52,s52,L1096_%=-L1097_%=-32\n"
"s_addc_u32 s53,s53,0\n"
"s_mov_b32 s89,0\n"
WT"lgkmcnt(0)\n"
MF"v[4:7],v[80:83],a[180:183],v[4:7]\n"
MF"v[12:15],v[88:91],a[180:183],v[12:15]\n"
MF"v[20:23],v[80:83],a[212:215],v[20:23]\n"
MF"v[28:31],v[88:91],a[212:215],v[28:31]\n"
MF"v[36:39],v[80:83],a[64:67],v[36:39]\n"
MF"v[44:47],v[88:91],a[64:67],v[44:47]\n"
MF"v[52:55],v[80:83],a[96:99],v[52:55]\n"
MF"v[60:63],v[88:91],a[96:99],v[60:63]\n"
"s_branch L1092_%=\n"
"L1094_%=:\n"
"s_sleep 3\n"
"buffer_load_dwordx2 v[240:241],v243,s[8:11],s93 offen sc1\n"
"L1091_%=:\n"
WT"vmcnt(0)\n"
"v_cmp_eq_u64 vcc,s[28:29],v[240:241]\n"
"s_sub_u32 s54,s54,1\n"
"s_cselect_b32 s90,-1,vcc_lo\n"
"s_andn2_b32 s90,s90,s95\n"
"s_cbranch_scc0 L1094_%=\n"
"s_or_b32 s95,s95,s90\n"
"L1098_%=:\n"
"s_ff1_i32_b32 s50,s90\n"
"s_bitset0_b32 s90,s50\n"
"s_lshl_b32 s50,s50,5\n"
SA"s50,s50,s52\n"
"s_addc_u32 s51,s53,0\n"
"s_setpc_b64 s[50:51]\n"
"L1096_%=:\n"
SA"s92,s94,5120\n"
BL"a[184:187],v244,s[8:11],s92 offen sc1\n"
BL"a[216:219],v245,s[8:11],s92 offen sc1\n"
"s_branch L1093_%=\n"
"s_nop 0\n"
SA"s92,s94,10240\n"
BL"a[188:191],v244,s[8:11],s92 offen sc1\n"
BL"a[220:223],v245,s[8:11],s92 offen sc1\n"
"s_branch L1093_%=\n"
"s_nop 0\n"
SA"s92,s94,15360\n"
BL"a[192:195],v244,s[8:11],s92 offen sc1\n"
BL"a[224:227],v245,s[8:11],s92 offen sc1\n"
"s_branch L1093_%=\n"
"s_nop 0\n"
SA"s92,s94,20480\n"
BL"a[196:199],v244,s[8:11],s92 offen sc1\n"
BL"a[228:231],v245,s[8:11],s92 offen sc1\n"
"s_branch L1093_%=\n"
"s_nop 0\n"
SA"s92,s94,25600\n"
BL"a[200:203],v244,s[8:11],s92 offen sc1\n"
BL"a[232:235],v245,s[8:11],s92 offen sc1\n"
"s_branch L1093_%=\n"
"s_nop 0\n"
SA"s92,s94,30720\n"
BL"a[204:207],v244,s[8:11],s92 offen sc1\n"
BL"a[236:239],v245,s[8:11],s92 offen sc1\n"
"s_branch L1093_%=\n"
"s_nop 0\n"
SA"s92,s94,35840\n"
BL"a[208:211],v244,s[8:11],s92 offen sc1\n"
BL"a[240:243],v245,s[8:11],s92 offen sc1\n"
"s_branch L1093_%=\n"
"s_nop 0\n"
"s_nop 0\n"
"s_nop 0\n"
"s_nop 0\n"
"s_nop 0\n"
"s_nop 0\n"
"s_nop 0\n"
"s_nop 0\n"
"s_nop 0\n"
SA"s92,s94,332800\n"
BL"a[68:71],v244,s[8:11],s92 offen sc1\n"
BL"a[100:103],v245,s[8:11],s92 offen sc1\n"
"s_branch L1093_%=\n"
"s_nop 0\n"
SA"s92,s94,337920\n"
BL"a[72:75],v244,s[8:11],s92 offen sc1\n"
BL"a[104:107],v245,s[8:11],s92 offen sc1\n"
"s_branch L1093_%=\n"
"s_nop 0\n"
SA"s92,s94,343040\n"
BL"a[76:79],v244,s[8:11],s92 offen sc1\n"
BL"a[108:111],v245,s[8:11],s92 offen sc1\n"
"s_branch L1093_%=\n"
"s_nop 0\n"
SA"s92,s94,348160\n"
BL"a[80:83],v244,s[8:11],s92 offen sc1\n"
BL"a[112:115],v245,s[8:11],s92 offen sc1\n"
"s_branch L1093_%=\n"
"s_nop 0\n"
SA"s92,s94,353280\n"
BL"a[84:87],v244,s[8:11],s92 offen sc1\n"
BL"a[116:119],v245,s[8:11],s92 offen sc1\n"
"s_branch L1093_%=\n"
"s_nop 0\n"
SA"s92,s94,358400\n"
BL"a[88:91],v244,s[8:11],s92 offen sc1\n"
BL"a[120:123],v245,s[8:11],s92 offen sc1\n"
"s_branch L1093_%=\n"
"s_nop 0\n"
SA"s92,s94,363520\n"
BL"a[92:95],v244,s[8:11],s92 offen sc1\n"
BL"a[124:127],v245,s[8:11],s92 offen sc1\n"
"s_branch L1093_%=\n"
"s_nop 0\n"
"L1093_%=:\n"
"s_cmp_lg_u32 s90,0\n"
"s_cbranch_scc1 L1098_%=\n"
"s_and_b32 s92,s95,s89\n"
"s_cmp_eq_u32 s92,-1\n"
"s_cbranch_scc1 L1095_%=\n"
"buffer_load_dwordx2 v[240:241],v243,s[8:11],s93 offen sc1\n"
WT"vmcnt(1)\n"
"L1092_%=:\n"
"s_or_b32 s92,s95,0xff00\n"
"s_andn2_b32 s92,s92,s89\n"
"s_cmp_eq_u32 s92,-1\n"
"s_cbranch_scc0 L1091_%=\n"
MF"v[4:7],v[80:83],a[184:187],v[4:7]\n"
MF"v[12:15],v[88:91],a[184:187],v[12:15]\n"
MF"v[4:7],v[80:83],a[188:191],v[4:7]\n"
MF"v[12:15],v[88:91],a[188:191],v[12:15]\n"
MF"v[4:7],v[80:83],a[192:195],v[4:7]\n"
MF"v[12:15],v[88:91],a[192:195],v[12:15]\n"
MF"v[4:7],v[80:83],a[196:199],v[4:7]\n"
MF"v[12:15],v[88:91],a[196:199],v[12:15]\n"
MF"v[4:7],v[80:83],a[200:203],v[4:7]\n"
MF"v[12:15],v[88:91],a[200:203],v[12:15]\n"
MF"v[4:7],v[80:83],a[204:207],v[4:7]\n"
MF"v[12:15],v[88:91],a[204:207],v[12:15]\n"
MF"v[4:7],v[80:83],a[208:211],v[4:7]\n"
MF"v[12:15],v[88:91],a[208:211],v[12:15]\n"
MF"v[20:23],v[80:83],a[216:219],v[20:23]\n"
MF"v[28:31],v[88:91],a[216:219],v[28:31]\n"
MF"v[20:23],v[80:83],a[220:223],v[20:23]\n"
MF"v[28:31],v[88:91],a[220:223],v[28:31]\n"
MF"v[20:23],v[80:83],a[224:227],v[20:23]\n"
MF"v[28:31],v[88:91],a[224:227],v[28:31]\n"
MF"v[20:23],v[80:83],a[228:231],v[20:23]\n"
MF"v[28:31],v[88:91],a[228:231],v[28:31]\n"
PK"v4,v4,v5\n"
PK"v5,v6,v7\n"
PK"v6,v12,v13\n"
PK"v7,v14,v15\n"
BS"v[4:7],v176,s[16:19],s38 offen\n"
MF"v[20:23],v[80:83],a[232:235],v[20:23]\n"
MF"v[28:31],v[88:91],a[232:235],v[28:31]\n"
MF"v[20:23],v[80:83],a[236:239],v[20:23]\n"
MF"v[28:31],v[88:91],a[236:239],v[28:31]\n"
MF"v[20:23],v[80:83],a[240:243],v[20:23]\n"
MF"v[28:31],v[88:91],a[240:243],v[28:31]\n"
"s_nop 7\n"
PK"v20,v20,v21\n"
PK"v21,v22,v23\n"
PK"v22,v28,v29\n"
PK"v23,v30,v31\n"
BS"v[20:23],v176,s[16:19],s55 offen\n"
"s_mov_b32 s89,-1\n"
"s_cmp_eq_u32 s95,-1\n"
"s_cbranch_scc0 L1091_%=\n"
"L1095_%=:\n"
WT"vmcnt(0)\n"
MF"v[36:39],v[80:83],a[68:71],v[36:39]\n"
MF"v[44:47],v[88:91],a[68:71],v[44:47]\n"
MF"v[36:39],v[80:83],a[72:75],v[36:39]\n"
MF"v[44:47],v[88:91],a[72:75],v[44:47]\n"
MF"v[36:39],v[80:83],a[76:79],v[36:39]\n"
MF"v[44:47],v[88:91],a[76:79],v[44:47]\n"
MF"v[36:39],v[80:83],a[80:83],v[36:39]\n"
MF"v[44:47],v[88:91],a[80:83],v[44:47]\n"
MF"v[36:39],v[80:83],a[84:87],v[36:39]\n"
MF"v[44:47],v[88:91],a[84:87],v[44:47]\n"
MF"v[36:39],v[80:83],a[88:91],v[36:39]\n"
MF"v[44:47],v[88:91],a[88:91],v[44:47]\n"
MF"v[36:39],v[80:83],a[92:95],v[36:39]\n"
MF"v[44:47],v[88:91],a[92:95],v[44:47]\n"
MF"v[52:55],v[80:83],a[100:103],v[52:55]\n"
MF"v[60:63],v[88:91],a[100:103],v[60:63]\n"
MF"v[52:55],v[80:83],a[104:107],v[52:55]\n"
MF"v[60:63],v[88:91],a[104:107],v[60:63]\n"
MF"v[52:55],v[80:83],a[108:111],v[52:55]\n"
MF"v[60:63],v[88:91],a[108:111],v[60:63]\n"
MF"v[52:55],v[80:83],a[112:115],v[52:55]\n"
MF"v[60:63],v[88:91],a[112:115],v[60:63]\n"
PK"v36,v36,v37\n"
PK"v37,v38,v39\n"
PK"v38,v44,v45\n"
PK"v39,v46,v47\n"
BS"v[36:39],v177,s[16:19],s38 offen\n"
MF"v[52:55],v[80:83],a[116:119],v[52:55]\n"
MF"v[60:63],v[88:91],a[116:119],v[60:63]\n"
MF"v[52:55],v[80:83],a[120:123],v[52:55]\n"
MF"v[60:63],v[88:91],a[120:123],v[60:63]\n"
MF"v[52:55],v[80:83],a[124:127],v[52:55]\n"
MF"v[60:63],v[88:91],a[124:127],v[60:63]\n"
"s_nop 7\n"
PK"v52,v52,v53\n"
PK"v53,v54,v55\n"
PK"v54,v60,v61\n"
PK"v55,v62,v63\n"
BS"v[52:55],v177,s[16:19],s55 offen\n"
WT"vmcnt(0) lgkmcnt(0)\n"
"s_endpgm\n"
"L1004_%=:\n"
"s_mov_b32 s54,0\n"
"L1006_%=:\n"
"s_sleep 2\n"
BL"v[160:163],v224,s[8:11],0 offen offset:256 sc1\n"
BL"v[164:167],v224,s[8:11],0 offen offset:1280 sc1\n"
WT"vmcnt(0)\n"
SA"s54,s54,1\n"
"v_cmp_eq_u64 vcc,s[28:29],v[160:161]\n"
"v_cmp_eq_u64_e64 s[62:63],s[28:29],v[162:163]\n"
"s_and_b64 vcc,vcc,s[62:63]\n"
"v_cmp_eq_u64_e64 s[62:63],s[28:29],v[164:165]\n"
"s_and_b64 vcc,vcc,s[62:63]\n"
"v_cmp_eq_u64_e64 s[62:63],s[28:29],v[166:167]\n"
"s_and_b64 vcc,vcc,s[62:63]\n"
"s_cmp_eq_u64 vcc,exec\n"
"s_cbranch_scc1 L1005_%=\n"
"s_cmp_lt_u32 s54,1048576\n"
"s_cbranch_scc1 L1006_%=\n"
"s_branch L1005_%=\n"
"L1007_%=:\n"
"s_mov_b32 s54,0\n"
"L1009_%=:\n"
"v_max3_u32 v226,v160,v161,v162\n"
"v_max3_u32 v226,v226,v163,v164\n"
"v_max3_u32 v226,v226,v165,v166\n"
"v_max3_u32 v226,v226,v167,v168\n"
"v_max3_u32 v226,v226,v169,v170\n"
"v_max3_u32 v226,v226,v171,v172\n"
"v_max3_u32 v226,v226,v173,v174\n"
"v_max3_u32 v226,v226,v175,v176\n"
"v_max3_u32 v226,v226,v177,v178\n"
"v_max3_u32 v226,v226,v179,v180\n"
"v_max3_u32 v226,v226,v181,v182\n"
"v_max3_u32 v226,v226,v183,v184\n"
"v_max3_u32 v226,v226,v185,v186\n"
"v_max3_u32 v226,v226,v187,v188\n"
"v_max3_u32 v226,v226,v189,v190\n"
"v_max_u32 v226,v226,v191\n"
"v_mov_b32 v229,v226\n"
"s_nop 1\n"
"v_max_u32_dpp v229,v229,v229 row_shr:1 row_mask:0xf bank_mask:0xf\n"
"s_nop 1\n"
"v_max_u32_dpp v229,v229,v229 row_shr:2 row_mask:0xf bank_mask:0xf\n"
"s_nop 1\n"
"v_max_u32_dpp v229,v229,v229 row_shr:4 row_mask:0xf bank_mask:0xf\n"
"s_nop 1\n"
"v_max_u32_dpp v229,v229,v229 row_shr:8 row_mask:0xf bank_mask:0xf\n"
"s_nop 1\n"
"v_max_u32_dpp v229,v229,v229 row_bcast:15 row_mask:0xa bank_mask:0xf\n"
"s_nop 1\n"
"v_max_u32_dpp v229,v229,v229 row_bcast:31 row_mask:0xc bank_mask:0xf\n"
"s_nop 1\n"
"v_readlane_b32 s62,v229,63\n"
"s_nop 1\n"
"v_cmp_eq_u32 vcc,s62,v160\n"
"s_nop 1\n"
"v_cndmask_b32_e64 v160,v160,0,vcc\n"
"v_cmp_eq_u32 vcc,s62,v161\n"
"s_nop 1\n"
"v_cndmask_b32_e64 v161,v161,0,vcc\n"
"v_cmp_eq_u32 vcc,s62,v162\n"
"s_nop 1\n"
"v_cndmask_b32_e64 v162,v162,0,vcc\n"
"v_cmp_eq_u32 vcc,s62,v163\n"
"s_nop 1\n"
"v_cndmask_b32_e64 v163,v163,0,vcc\n"
"v_cmp_eq_u32 vcc,s62,v164\n"
"s_nop 1\n"
"v_cndmask_b32_e64 v164,v164,0,vcc\n"
"v_cmp_eq_u32 vcc,s62,v165\n"
"s_nop 1\n"
"v_cndmask_b32_e64 v165,v165,0,vcc\n"
"v_cmp_eq_u32 vcc,s62,v166\n"
"s_nop 1\n"
"v_cndmask_b32_e64 v166,v166,0,vcc\n"
"v_cmp_eq_u32 vcc,s62,v167\n"
"s_nop 1\n"
"v_cndmask_b32_e64 v167,v167,0,vcc\n"
"v_cmp_eq_u32 vcc,s62,v168\n"
"s_nop 1\n"
"v_cndmask_b32_e64 v168,v168,0,vcc\n"
"v_cmp_eq_u32 vcc,s62,v169\n"
"s_nop 1\n"
"v_cndmask_b32_e64 v169,v169,0,vcc\n"
"v_cmp_eq_u32 vcc,s62,v170\n"
"s_nop 1\n"
"v_cndmask_b32_e64 v170,v170,0,vcc\n"
"v_cmp_eq_u32 vcc,s62,v171\n"
"s_nop 1\n"
"v_cndmask_b32_e64 v171,v171,0,vcc\n"
"v_cmp_eq_u32 vcc,s62,v172\n"
"s_nop 1\n"
"v_cndmask_b32_e64 v172,v172,0,vcc\n"
"v_cmp_eq_u32 vcc,s62,v173\n"
"s_nop 1\n"
"v_cndmask_b32_e64 v173,v173,0,vcc\n"
"v_cmp_eq_u32 vcc,s62,v174\n"
"s_nop 1\n"
"v_cndmask_b32_e64 v174,v174,0,vcc\n"
"v_cmp_eq_u32 vcc,s62,v175\n"
"s_nop 1\n"
"v_cndmask_b32_e64 v175,v175,0,vcc\n"
"v_cmp_eq_u32 vcc,s62,v176\n"
"s_nop 1\n"
"v_cndmask_b32_e64 v176,v176,0,vcc\n"
"v_cmp_eq_u32 vcc,s62,v177\n"
"s_nop 1\n"
"v_cndmask_b32_e64 v177,v177,0,vcc\n"
"v_cmp_eq_u32 vcc,s62,v178\n"
"s_nop 1\n"
"v_cndmask_b32_e64 v178,v178,0,vcc\n"
"v_cmp_eq_u32 vcc,s62,v179\n"
"s_nop 1\n"
"v_cndmask_b32_e64 v179,v179,0,vcc\n"
"v_cmp_eq_u32 vcc,s62,v180\n"
"s_nop 1\n"
"v_cndmask_b32_e64 v180,v180,0,vcc\n"
"v_cmp_eq_u32 vcc,s62,v181\n"
"s_nop 1\n"
"v_cndmask_b32_e64 v181,v181,0,vcc\n"
"v_cmp_eq_u32 vcc,s62,v182\n"
"s_nop 1\n"
"v_cndmask_b32_e64 v182,v182,0,vcc\n"
"v_cmp_eq_u32 vcc,s62,v183\n"
"s_nop 1\n"
"v_cndmask_b32_e64 v183,v183,0,vcc\n"
"v_cmp_eq_u32 vcc,s62,v184\n"
"s_nop 1\n"
"v_cndmask_b32_e64 v184,v184,0,vcc\n"
"v_cmp_eq_u32 vcc,s62,v185\n"
"s_nop 1\n"
"v_cndmask_b32_e64 v185,v185,0,vcc\n"
"v_cmp_eq_u32 vcc,s62,v186\n"
"s_nop 1\n"
"v_cndmask_b32_e64 v186,v186,0,vcc\n"
"v_cmp_eq_u32 vcc,s62,v187\n"
"s_nop 1\n"
"v_cndmask_b32_e64 v187,v187,0,vcc\n"
"v_cmp_eq_u32 vcc,s62,v188\n"
"s_nop 1\n"
"v_cndmask_b32_e64 v188,v188,0,vcc\n"
"v_cmp_eq_u32 vcc,s62,v189\n"
"s_nop 1\n"
"v_cndmask_b32_e64 v189,v189,0,vcc\n"
"v_cmp_eq_u32 vcc,s62,v190\n"
"s_nop 1\n"
"v_cndmask_b32_e64 v190,v190,0,vcc\n"
"v_cmp_eq_u32 vcc,s62,v191\n"
"s_nop 1\n"
"v_cndmask_b32_e64 v191,v191,0,vcc\n"
"v_mov_b32 v229,s62\n"
"s_lshl_b64 exec,1,s54\n"
"v_mov_b32 v225,v229\n"
"s_mov_b64 exec,-1\n"
SA"s54,s54,1\n"
"s_cmp_lt_u32 s54,8\n"
"s_cbranch_scc1 L1009_%=\n"
"s_nop 4\n"
"s_branch L1008_%=\n"
"L1010_%=:\n"
"s_mov_b32 s54,0\n"
"L1012_%=:\n"
"s_sleep 2\n"
"buffer_load_dwordx2 v[208:209],v252,s[8:11],s50 offen sc1\n"
WT"vmcnt(0)\n"
SA"s54,s54,1\n"
"v_cmp_eq_u64 vcc,s[28:29],v[208:209]\n"
"s_cmp_eq_u64 vcc,exec\n"
"s_cbranch_scc1 L1011_%=\n"
"s_cmp_lt_u32 s54,1048576\n"
"s_cbranch_scc1 L1012_%=\n"
"s_branch L1011_%=\n"
"L1014_%=:\n"
"s_nop 15\n"
WT"vmcnt(0)\n"
"s_lshl_b32 s89,s34,3\n"
SA"s89,s89,s30\n"
SA"s89,s89,0\n"
"s_lshl_b32 s91,s89,4\n"
"s_lshr_b32 s89,s89,1\n"
"v_bfe_u32 v249,v224,4,4\n"
"v_lshrrev_b32 v254,8,v224\n"
"v_lshlrev_b32 v254,2,v254\n"
"v_sub_u32 v249,v249,v254\n"
"v_add_u32 v249,s91,v249\n"
"v_mov_b32 v255,0xff800000\n"
"v_mov_b32 v248,v255\n"
"s_mov_b32 s90,0\n"
"s_mov_b32 s88,0\n"
"v_mov_b32 v80,0\n"
"v_mov_b32 v81,0\n"
"v_mov_b32 v82,0\n"
"v_mov_b32 v83,0\n"
"v_mov_b32 v84,0\n"
"v_mov_b32 v85,0\n"
"v_mov_b32 v86,0\n"
"v_mov_b32 v87,0\n"
"v_mov_b32 v88,0\n"
"v_mov_b32 v89,0\n"
"v_mov_b32 v90,0\n"
"v_mov_b32 v91,0\n"
"v_mov_b32 v92,0\n"
"v_mov_b32 v93,0\n"
"v_mov_b32 v94,0\n"
"v_mov_b32 v95,0\n"
"v_mov_b32 v96,0\n"
"v_mov_b32 v97,0\n"
"v_mov_b32 v98,0\n"
"v_mov_b32 v99,0\n"
"v_mov_b32 v100,0\n"
"v_mov_b32 v101,0\n"
"v_mov_b32 v102,0\n"
"v_mov_b32 v103,0\n"
"v_mov_b32 v104,0\n"
"v_mov_b32 v105,0\n"
"v_mov_b32 v106,0\n"
"v_mov_b32 v107,0\n"
"v_mov_b32 v108,0\n"
"v_mov_b32 v109,0\n"
"v_mov_b32 v110,0\n"
"v_mov_b32 v111,0\n"
"v_accvgpr_write_b32 a132,0\n"
"v_accvgpr_write_b32 a133,0\n"
"v_accvgpr_write_b32 a134,0\n"
"v_accvgpr_write_b32 a135,0\n"
"L1015_%=:\n"
"s_lshl_b32 s91,s88,13\n"
SA"s91,s91,s39\n"
SA"s91,s91,0\n"
BL"a[148:151],v224,s[8:11],s91 offen offset:0 sc1\n"
BL"a[152:155],v224,s[8:11],s91 offen offset:1024 sc1\n"
BL"a[156:159],v224,s[8:11],s91 offen offset:2048 sc1\n"
BL"a[160:163],v224,s[8:11],s91 offen offset:3072 sc1\n"
SA"s91,s91,4096\n"
BL"a[164:167],v224,s[8:11],s91 offen offset:0 sc1\n"
BL"a[168:171],v224,s[8:11],s91 offen offset:1024 sc1\n"
BL"a[172:175],v224,s[8:11],s91 offen offset:2048 sc1\n"
BL"a[176:179],v224,s[8:11],s91 offen offset:3072 sc1\n"
WT"vmcnt(0)\n"
MF"v[240:243],a[148:151],a[0:3],0\n"
MF"v[240:243],a[152:155],a[4:7],v[240:243]\n"
MF"v[240:243],a[156:159],a[8:11],v[240:243]\n"
MF"v[240:243],a[160:163],a[12:15],v[240:243]\n"
MF"v[244:247],a[164:167],a[0:3],0\n"
MF"v[244:247],a[168:171],a[4:7],v[244:247]\n"
MF"v[244:247],a[172:175],a[8:11],v[244:247]\n"
MF"v[244:247],a[176:179],a[12:15],v[244:247]\n"
"s_lshl_b32 s91,s88,5\n"
"v_subrev_u32 v250,s91,v249\n"
"s_nop 7\n"
"v_cmp_gt_i32 vcc,0,v250\n"
"s_nop 1\n"
"v_cndmask_b32 v240,v240,v255,vcc\n"
"v_cmp_gt_i32 vcc,1,v250\n"
"s_nop 1\n"
"v_cndmask_b32 v241,v241,v255,vcc\n"
"v_cmp_gt_i32 vcc,2,v250\n"
"s_nop 1\n"
"v_cndmask_b32 v242,v242,v255,vcc\n"
"v_cmp_gt_i32 vcc,3,v250\n"
"s_nop 1\n"
"v_cndmask_b32 v243,v243,v255,vcc\n"
"v_cmp_gt_i32 vcc,16,v250\n"
"s_nop 1\n"
"v_cndmask_b32 v244,v244,v255,vcc\n"
"v_cmp_gt_i32 vcc,17,v250\n"
"s_nop 1\n"
"v_cndmask_b32 v245,v245,v255,vcc\n"
"v_cmp_gt_i32 vcc,18,v250\n"
"s_nop 1\n"
"v_cndmask_b32 v246,v246,v255,vcc\n"
"v_cmp_gt_i32 vcc,19,v250\n"
"s_nop 1\n"
"v_cndmask_b32 v247,v247,v255,vcc\n"
"s_cmp_lg_u32 s90,0\n"
"s_cbranch_scc1 L1016_%=\n"
"v_max3_f32 v248,v248,v240,v241\n"
"v_max3_f32 v248,v248,v242,v243\n"
"v_max3_f32 v248,v248,v244,v245\n"
"v_max3_f32 v248,v248,v246,v247\n"
"s_branch L1017_%=\n"
"L1016_%=:\n"
"s_lshl_b32 s91,s88,13\n"
SA"s91,s91,s39\n"
SA"s91,s91,524288\n"
BL"a[148:151],v224,s[8:11],s91 offen offset:0 sc1\n"
BL"a[152:155],v224,s[8:11],s91 offen offset:1024 sc1\n"
BL"a[156:159],v224,s[8:11],s91 offen offset:2048 sc1\n"
BL"a[160:163],v224,s[8:11],s91 offen offset:3072 sc1\n"
SA"s91,s91,4096\n"
BL"a[164:167],v224,s[8:11],s91 offen offset:0 sc1\n"
BL"a[168:171],v224,s[8:11],s91 offen offset:1024 sc1\n"
BL"a[172:175],v224,s[8:11],s91 offen offset:2048 sc1\n"
BL"a[176:179],v224,s[8:11],s91 offen offset:3072 sc1\n"
WT"vmcnt(0)\n"
"v_sub_f32 v240,v240,v248\n"
"v_sub_f32 v241,v241,v248\n"
"v_sub_f32 v242,v242,v248\n"
"v_sub_f32 v243,v243,v248\n"
"v_sub_f32 v244,v244,v248\n"
"v_sub_f32 v245,v245,v248\n"
"v_sub_f32 v246,v246,v248\n"
"v_sub_f32 v247,v247,v248\n"
EX"v240,v240\n"
EX"v241,v241\n"
EX"v242,v242\n"
EX"v243,v243\n"
EX"v244,v244\n"
EX"v245,v245\n"
EX"v246,v246\n"
EX"v247,v247\n"
"s_nop 0\n"
PK"v240,v240,v241\n"
PK"v241,v242,v243\n"
PK"v242,v244,v245\n"
PK"v243,v246,v247\n"
"s_nop 1\n"
MF"v[80:83],a[148:151],v[240:243],v[80:83]\n"
MF"v[84:87],a[152:155],v[240:243],v[84:87]\n"
MF"v[88:91],a[156:159],v[240:243],v[88:91]\n"
MF"v[92:95],a[160:163],v[240:243],v[92:95]\n"
MF"v[96:99],a[164:167],v[240:243],v[96:99]\n"
MF"v[100:103],a[168:171],v[240:243],v[100:103]\n"
MF"v[104:107],a[172:175],v[240:243],v[104:107]\n"
MF"v[108:111],a[176:179],v[240:243],v[108:111]\n"
MF"a[132:135],a[128:131],v[240:243],a[132:135]\n"
"s_nop 7\n"
"L1017_%=:\n"
SA"s88,s88,1\n"
"s_cmp_le_u32 s88,s89\n"
"s_cbranch_scc1 L1015_%=\n"
"s_cmp_lg_u32 s90,0\n"
"s_cbranch_scc1 L1018_%=\n"
"v_mov_b32 v254,v248\n"
"s_nop 1\n"
"v_permlane16_swap_b32 v254,v248\n"
"s_nop 1\n"
"v_max_f32 v248,v248,v254\n"
"v_mov_b32 v254,v248\n"
"s_nop 1\n"
"v_permlane32_swap_b32 v254,v248\n"
"s_nop 1\n"
"v_max_f32 v248,v248,v254\n"
"s_mov_b32 s90,1\n"
"s_mov_b32 s88,0\n"
"s_branch L1015_%=\n"
"L1018_%=:\n"
"s_nop 7\n"
"v_accvgpr_read_b32 v251,a132\n"
"s_nop 1\n"
"s_branch L1013_%=\n"
"L1019_%=:\n"
"s_mov_b32 s54,0\n"
"L1021_%=:\n"
"s_sleep 2\n"
"buffer_load_dwordx2 v[238:239],v237,s[8:11],s50 offen sc1\n"
WT"vmcnt(0)\n"
SA"s54,s54,1\n"
"v_cmp_eq_u64 vcc,s[28:29],v[238:239]\n"
"s_cmp_eq_u64 vcc,exec\n"
"s_cbranch_scc1 L1020_%=\n"
"s_cmp_lt_u32 s54,1048576\n"
"s_cbranch_scc1 L1021_%=\n"
"s_branch L1020_%=\n"
"L1023_%=:\n"
"s_nop 15\n"
WT"vmcnt(0)\n"
"s_lshl_b32 s89,s34,3\n"
SA"s89,s89,s30\n"
SA"s89,s89,4\n"
"s_lshl_b32 s91,s89,4\n"
"s_lshr_b32 s89,s89,1\n"
"v_bfe_u32 v249,v224,4,4\n"
"v_lshrrev_b32 v254,8,v224\n"
"v_lshlrev_b32 v254,2,v254\n"
"v_sub_u32 v249,v249,v254\n"
"v_add_u32 v249,s91,v249\n"
"v_mov_b32 v255,0xff800000\n"
"v_mov_b32 v248,v255\n"
"s_mov_b32 s90,0\n"
"s_mov_b32 s88,0\n"
"v_mov_b32 v112,0\n"
"v_mov_b32 v113,0\n"
"v_mov_b32 v114,0\n"
"v_mov_b32 v115,0\n"
"v_mov_b32 v116,0\n"
"v_mov_b32 v117,0\n"
"v_mov_b32 v118,0\n"
"v_mov_b32 v119,0\n"
"v_mov_b32 v120,0\n"
"v_mov_b32 v121,0\n"
"v_mov_b32 v122,0\n"
"v_mov_b32 v123,0\n"
"v_mov_b32 v124,0\n"
"v_mov_b32 v125,0\n"
"v_mov_b32 v126,0\n"
"v_mov_b32 v127,0\n"
"v_mov_b32 v128,0\n"
"v_mov_b32 v129,0\n"
"v_mov_b32 v130,0\n"
"v_mov_b32 v131,0\n"
"v_mov_b32 v132,0\n"
"v_mov_b32 v133,0\n"
"v_mov_b32 v134,0\n"
"v_mov_b32 v135,0\n"
"v_mov_b32 v136,0\n"
"v_mov_b32 v137,0\n"
"v_mov_b32 v138,0\n"
"v_mov_b32 v139,0\n"
"v_mov_b32 v140,0\n"
"v_mov_b32 v141,0\n"
"v_mov_b32 v142,0\n"
"v_mov_b32 v143,0\n"
"v_accvgpr_write_b32 a136,0\n"
"v_accvgpr_write_b32 a137,0\n"
"v_accvgpr_write_b32 a138,0\n"
"v_accvgpr_write_b32 a139,0\n"
"L1024_%=:\n"
"s_lshl_b32 s91,s88,13\n"
SA"s91,s91,s39\n"
SA"s91,s91,0\n"
BL"a[148:151],v224,s[8:11],s91 offen offset:0 sc1\n"
BL"a[152:155],v224,s[8:11],s91 offen offset:1024 sc1\n"
BL"a[156:159],v224,s[8:11],s91 offen offset:2048 sc1\n"
BL"a[160:163],v224,s[8:11],s91 offen offset:3072 sc1\n"
SA"s91,s91,4096\n"
BL"a[164:167],v224,s[8:11],s91 offen offset:0 sc1\n"
BL"a[168:171],v224,s[8:11],s91 offen offset:1024 sc1\n"
BL"a[172:175],v224,s[8:11],s91 offen offset:2048 sc1\n"
BL"a[176:179],v224,s[8:11],s91 offen offset:3072 sc1\n"
WT"vmcnt(0)\n"
MF"v[240:243],a[148:151],a[16:19],0\n"
MF"v[240:243],a[152:155],a[20:23],v[240:243]\n"
MF"v[240:243],a[156:159],a[24:27],v[240:243]\n"
MF"v[240:243],a[160:163],a[28:31],v[240:243]\n"
MF"v[244:247],a[164:167],a[16:19],0\n"
MF"v[244:247],a[168:171],a[20:23],v[244:247]\n"
MF"v[244:247],a[172:175],a[24:27],v[244:247]\n"
MF"v[244:247],a[176:179],a[28:31],v[244:247]\n"
"s_lshl_b32 s91,s88,5\n"
"v_subrev_u32 v250,s91,v249\n"
"s_nop 7\n"
"v_cmp_gt_i32 vcc,0,v250\n"
"s_nop 1\n"
"v_cndmask_b32 v240,v240,v255,vcc\n"
"v_cmp_gt_i32 vcc,1,v250\n"
"s_nop 1\n"
"v_cndmask_b32 v241,v241,v255,vcc\n"
"v_cmp_gt_i32 vcc,2,v250\n"
"s_nop 1\n"
"v_cndmask_b32 v242,v242,v255,vcc\n"
"v_cmp_gt_i32 vcc,3,v250\n"
"s_nop 1\n"
"v_cndmask_b32 v243,v243,v255,vcc\n"
"v_cmp_gt_i32 vcc,16,v250\n"
"s_nop 1\n"
"v_cndmask_b32 v244,v244,v255,vcc\n"
"v_cmp_gt_i32 vcc,17,v250\n"
"s_nop 1\n"
"v_cndmask_b32 v245,v245,v255,vcc\n"
"v_cmp_gt_i32 vcc,18,v250\n"
"s_nop 1\n"
"v_cndmask_b32 v246,v246,v255,vcc\n"
"v_cmp_gt_i32 vcc,19,v250\n"
"s_nop 1\n"
"v_cndmask_b32 v247,v247,v255,vcc\n"
"s_cmp_lg_u32 s90,0\n"
"s_cbranch_scc1 L1025_%=\n"
"v_max3_f32 v248,v248,v240,v241\n"
"v_max3_f32 v248,v248,v242,v243\n"
"v_max3_f32 v248,v248,v244,v245\n"
"v_max3_f32 v248,v248,v246,v247\n"
"s_branch L1026_%=\n"
"L1025_%=:\n"
"s_lshl_b32 s91,s88,13\n"
SA"s91,s91,s39\n"
SA"s91,s91,524288\n"
BL"a[148:151],v224,s[8:11],s91 offen offset:0 sc1\n"
BL"a[152:155],v224,s[8:11],s91 offen offset:1024 sc1\n"
BL"a[156:159],v224,s[8:11],s91 offen offset:2048 sc1\n"
BL"a[160:163],v224,s[8:11],s91 offen offset:3072 sc1\n"
SA"s91,s91,4096\n"
BL"a[164:167],v224,s[8:11],s91 offen offset:0 sc1\n"
BL"a[168:171],v224,s[8:11],s91 offen offset:1024 sc1\n"
BL"a[172:175],v224,s[8:11],s91 offen offset:2048 sc1\n"
BL"a[176:179],v224,s[8:11],s91 offen offset:3072 sc1\n"
WT"vmcnt(0)\n"
"v_sub_f32 v240,v240,v248\n"
"v_sub_f32 v241,v241,v248\n"
"v_sub_f32 v242,v242,v248\n"
"v_sub_f32 v243,v243,v248\n"
"v_sub_f32 v244,v244,v248\n"
"v_sub_f32 v245,v245,v248\n"
"v_sub_f32 v246,v246,v248\n"
"v_sub_f32 v247,v247,v248\n"
EX"v240,v240\n"
EX"v241,v241\n"
EX"v242,v242\n"
EX"v243,v243\n"
EX"v244,v244\n"
EX"v245,v245\n"
EX"v246,v246\n"
EX"v247,v247\n"
"s_nop 0\n"
PK"v240,v240,v241\n"
PK"v241,v242,v243\n"
PK"v242,v244,v245\n"
PK"v243,v246,v247\n"
"s_nop 1\n"
MF"v[112:115],a[148:151],v[240:243],v[112:115]\n"
MF"v[116:119],a[152:155],v[240:243],v[116:119]\n"
MF"v[120:123],a[156:159],v[240:243],v[120:123]\n"
MF"v[124:127],a[160:163],v[240:243],v[124:127]\n"
MF"v[128:131],a[164:167],v[240:243],v[128:131]\n"
MF"v[132:135],a[168:171],v[240:243],v[132:135]\n"
MF"v[136:139],a[172:175],v[240:243],v[136:139]\n"
MF"v[140:143],a[176:179],v[240:243],v[140:143]\n"
MF"a[136:139],a[128:131],v[240:243],a[136:139]\n"
"s_nop 7\n"
"L1026_%=:\n"
SA"s88,s88,1\n"
"s_cmp_le_u32 s88,s89\n"
"s_cbranch_scc1 L1024_%=\n"
"s_cmp_lg_u32 s90,0\n"
"s_cbranch_scc1 L1027_%=\n"
"v_mov_b32 v254,v248\n"
"s_nop 1\n"
"v_permlane16_swap_b32 v254,v248\n"
"s_nop 1\n"
"v_max_f32 v248,v248,v254\n"
"v_mov_b32 v254,v248\n"
"s_nop 1\n"
"v_permlane32_swap_b32 v254,v248\n"
"s_nop 1\n"
"v_max_f32 v248,v248,v254\n"
"s_mov_b32 s90,1\n"
"s_mov_b32 s88,0\n"
"s_branch L1024_%=\n"
"L1027_%=:\n"
"s_nop 7\n"
"v_accvgpr_read_b32 v252,a136\n"
"s_nop 1\n"
"s_branch L1022_%=\n"
"L1028_%=:\n"
"s_mov_b32 s54,0\n"
"L1030_%=:\n"
"s_sleep 2\n"
"buffer_load_dwordx2 v[242:243],v236,s[8:11],0 offen sc1\n"
WT"vmcnt(0)\n"
SA"s54,s54,1\n"
"v_cmp_eq_u64 vcc,s[28:29],v[242:243]\n"
"s_or_b32 vcc_hi,vcc_hi,-1\n"
"s_cmp_eq_u64 vcc,exec\n"
"s_cbranch_scc1 L1029_%=\n"
"s_cmp_lt_u32 s54,1048576\n"
"s_cbranch_scc1 L1030_%=\n"
"s_branch L1029_%=\n"
"L1031_%=:\n"
"s_mov_b32 s54,0\n"
"L1033_%=:\n"
"s_sleep 2\n"
"buffer_load_dwordx2 v[242:243],v236,s[8:11],0 offen sc1\n"
WT"vmcnt(0)\n"
SA"s54,s54,1\n"
"v_cmp_eq_u64 vcc,s[28:29],v[242:243]\n"
"s_or_b32 vcc_lo,vcc_lo,-1\n"
"s_cmp_eq_u64 vcc,exec\n"
"s_cbranch_scc1 L1032_%=\n"
"s_cmp_lt_u32 s54,1048576\n"
"s_cbranch_scc1 L1033_%=\n"
"s_branch L1032_%=\n"
"L1035_%=:\n"
"s_nop 15\n"
WT"vmcnt(0)\n"
"s_lshl_b32 s89,s34,3\n"
SA"s89,s89,s30\n"
SA"s89,s89,4\n"
"s_lshl_b32 s91,s89,4\n"
"s_lshr_b32 s89,s89,1\n"
"v_bfe_u32 v249,v224,4,4\n"
"v_lshrrev_b32 v254,8,v224\n"
"v_lshlrev_b32 v254,2,v254\n"
"v_sub_u32 v249,v249,v254\n"
"v_add_u32 v249,s91,v249\n"
"v_mov_b32 v255,0xff800000\n"
"v_mov_b32 v248,v255\n"
"s_mov_b32 s90,0\n"
"s_mov_b32 s88,0\n"
"v_mov_b32 v112,0\n"
"v_mov_b32 v113,0\n"
"v_mov_b32 v114,0\n"
"v_mov_b32 v115,0\n"
"v_mov_b32 v116,0\n"
"v_mov_b32 v117,0\n"
"v_mov_b32 v118,0\n"
"v_mov_b32 v119,0\n"
"v_mov_b32 v120,0\n"
"v_mov_b32 v121,0\n"
"v_mov_b32 v122,0\n"
"v_mov_b32 v123,0\n"
"v_mov_b32 v124,0\n"
"v_mov_b32 v125,0\n"
"v_mov_b32 v126,0\n"
"v_mov_b32 v127,0\n"
"v_mov_b32 v128,0\n"
"v_mov_b32 v129,0\n"
"v_mov_b32 v130,0\n"
"v_mov_b32 v131,0\n"
"v_mov_b32 v132,0\n"
"v_mov_b32 v133,0\n"
"v_mov_b32 v134,0\n"
"v_mov_b32 v135,0\n"
"v_mov_b32 v136,0\n"
"v_mov_b32 v137,0\n"
"v_mov_b32 v138,0\n"
"v_mov_b32 v139,0\n"
"v_mov_b32 v140,0\n"
"v_mov_b32 v141,0\n"
"v_mov_b32 v142,0\n"
"v_mov_b32 v143,0\n"
"v_accvgpr_write_b32 a136,0\n"
"v_accvgpr_write_b32 a137,0\n"
"v_accvgpr_write_b32 a138,0\n"
"v_accvgpr_write_b32 a139,0\n"
"L1036_%=:\n"
"s_lshl_b32 s91,s88,13\n"
SA"s91,s91,s39\n"
SA"s91,s91,0\n"
BL"a[148:151],v224,s[8:11],s91 offen offset:0 sc1\n"
BL"a[152:155],v224,s[8:11],s91 offen offset:1024 sc1\n"
BL"a[156:159],v224,s[8:11],s91 offen offset:2048 sc1\n"
BL"a[160:163],v224,s[8:11],s91 offen offset:3072 sc1\n"
SA"s91,s91,4096\n"
BL"a[164:167],v224,s[8:11],s91 offen offset:0 sc1\n"
BL"a[168:171],v224,s[8:11],s91 offen offset:1024 sc1\n"
BL"a[172:175],v224,s[8:11],s91 offen offset:2048 sc1\n"
BL"a[176:179],v224,s[8:11],s91 offen offset:3072 sc1\n"
WT"vmcnt(0)\n"
MF"v[240:243],a[148:151],a[16:19],0\n"
MF"v[240:243],a[152:155],a[20:23],v[240:243]\n"
MF"v[240:243],a[156:159],a[24:27],v[240:243]\n"
MF"v[240:243],a[160:163],a[28:31],v[240:243]\n"
MF"v[244:247],a[164:167],a[16:19],0\n"
MF"v[244:247],a[168:171],a[20:23],v[244:247]\n"
MF"v[244:247],a[172:175],a[24:27],v[244:247]\n"
MF"v[244:247],a[176:179],a[28:31],v[244:247]\n"
"s_lshl_b32 s91,s88,5\n"
"v_subrev_u32 v250,s91,v249\n"
"s_nop 7\n"
"v_cmp_gt_i32 vcc,0,v250\n"
"s_nop 1\n"
"v_cndmask_b32 v240,v240,v255,vcc\n"
"v_cmp_gt_i32 vcc,1,v250\n"
"s_nop 1\n"
"v_cndmask_b32 v241,v241,v255,vcc\n"
"v_cmp_gt_i32 vcc,2,v250\n"
"s_nop 1\n"
"v_cndmask_b32 v242,v242,v255,vcc\n"
"v_cmp_gt_i32 vcc,3,v250\n"
"s_nop 1\n"
"v_cndmask_b32 v243,v243,v255,vcc\n"
"v_cmp_gt_i32 vcc,16,v250\n"
"s_nop 1\n"
"v_cndmask_b32 v244,v244,v255,vcc\n"
"v_cmp_gt_i32 vcc,17,v250\n"
"s_nop 1\n"
"v_cndmask_b32 v245,v245,v255,vcc\n"
"v_cmp_gt_i32 vcc,18,v250\n"
"s_nop 1\n"
"v_cndmask_b32 v246,v246,v255,vcc\n"
"v_cmp_gt_i32 vcc,19,v250\n"
"s_nop 1\n"
"v_cndmask_b32 v247,v247,v255,vcc\n"
"s_cmp_lg_u32 s90,0\n"
"s_cbranch_scc1 L1037_%=\n"
"v_max3_f32 v248,v248,v240,v241\n"
"v_max3_f32 v248,v248,v242,v243\n"
"v_max3_f32 v248,v248,v244,v245\n"
"v_max3_f32 v248,v248,v246,v247\n"
"s_branch L1038_%=\n"
"L1037_%=:\n"
"s_lshl_b32 s91,s88,13\n"
SA"s91,s91,s39\n"
SA"s91,s91,524288\n"
BL"a[148:151],v224,s[8:11],s91 offen offset:0 sc1\n"
BL"a[152:155],v224,s[8:11],s91 offen offset:1024 sc1\n"
BL"a[156:159],v224,s[8:11],s91 offen offset:2048 sc1\n"
BL"a[160:163],v224,s[8:11],s91 offen offset:3072 sc1\n"
SA"s91,s91,4096\n"
BL"a[164:167],v224,s[8:11],s91 offen offset:0 sc1\n"
BL"a[168:171],v224,s[8:11],s91 offen offset:1024 sc1\n"
BL"a[172:175],v224,s[8:11],s91 offen offset:2048 sc1\n"
BL"a[176:179],v224,s[8:11],s91 offen offset:3072 sc1\n"
WT"vmcnt(0)\n"
"v_sub_f32 v240,v240,v248\n"
"v_sub_f32 v241,v241,v248\n"
"v_sub_f32 v242,v242,v248\n"
"v_sub_f32 v243,v243,v248\n"
"v_sub_f32 v244,v244,v248\n"
"v_sub_f32 v245,v245,v248\n"
"v_sub_f32 v246,v246,v248\n"
"v_sub_f32 v247,v247,v248\n"
EX"v240,v240\n"
EX"v241,v241\n"
EX"v242,v242\n"
EX"v243,v243\n"
EX"v244,v244\n"
EX"v245,v245\n"
EX"v246,v246\n"
EX"v247,v247\n"
"s_nop 0\n"
PK"v240,v240,v241\n"
PK"v241,v242,v243\n"
PK"v242,v244,v245\n"
PK"v243,v246,v247\n"
"s_nop 1\n"
MF"v[112:115],a[148:151],v[240:243],v[112:115]\n"
MF"v[116:119],a[152:155],v[240:243],v[116:119]\n"
MF"v[120:123],a[156:159],v[240:243],v[120:123]\n"
MF"v[124:127],a[160:163],v[240:243],v[124:127]\n"
MF"v[128:131],a[164:167],v[240:243],v[128:131]\n"
MF"v[132:135],a[168:171],v[240:243],v[132:135]\n"
MF"v[136:139],a[172:175],v[240:243],v[136:139]\n"
MF"v[140:143],a[176:179],v[240:243],v[140:143]\n"
MF"a[136:139],a[128:131],v[240:243],a[136:139]\n"
"s_nop 7\n"
"L1038_%=:\n"
SA"s88,s88,1\n"
"s_cmp_le_u32 s88,s89\n"
"s_cbranch_scc1 L1036_%=\n"
"s_cmp_lg_u32 s90,0\n"
"s_cbranch_scc1 L1039_%=\n"
"v_mov_b32 v254,v248\n"
"s_nop 1\n"
"v_permlane16_swap_b32 v254,v248\n"
"s_nop 1\n"
"v_max_f32 v248,v248,v254\n"
"v_mov_b32 v254,v248\n"
"s_nop 1\n"
"v_permlane32_swap_b32 v254,v248\n"
"s_nop 1\n"
"v_max_f32 v248,v248,v254\n"
"s_mov_b32 s90,1\n"
"s_mov_b32 s88,0\n"
"s_branch L1036_%=\n"
"L1039_%=:\n"
"s_nop 7\n"
"v_accvgpr_read_b32 v252,a136\n"
"s_nop 1\n"
"s_branch L1034_%=\n"
"L1041_%=:\n"
"s_nop 15\n"
WT"vmcnt(0)\n"
"s_lshl_b32 s89,s34,3\n"
"s_sub_u32 s89,120,s89\n"
SA"s89,s89,s30\n"
SA"s89,s89,0\n"
"s_lshl_b32 s91,s89,4\n"
"s_lshr_b32 s89,s89,1\n"
"v_bfe_u32 v249,v224,4,4\n"
"v_lshrrev_b32 v254,8,v224\n"
"v_lshlrev_b32 v254,2,v254\n"
"v_sub_u32 v249,v249,v254\n"
"v_add_u32 v249,s91,v249\n"
"v_mov_b32 v255,0xff800000\n"
"v_mov_b32 v248,v255\n"
"s_mov_b32 s90,0\n"
"s_mov_b32 s88,0\n"
"v_mov_b32 v144,0\n"
"v_mov_b32 v145,0\n"
"v_mov_b32 v146,0\n"
"v_mov_b32 v147,0\n"
"v_mov_b32 v148,0\n"
"v_mov_b32 v149,0\n"
"v_mov_b32 v150,0\n"
"v_mov_b32 v151,0\n"
"v_mov_b32 v152,0\n"
"v_mov_b32 v153,0\n"
"v_mov_b32 v154,0\n"
"v_mov_b32 v155,0\n"
"v_mov_b32 v156,0\n"
"v_mov_b32 v157,0\n"
"v_mov_b32 v158,0\n"
"v_mov_b32 v159,0\n"
"v_mov_b32 v160,0\n"
"v_mov_b32 v161,0\n"
"v_mov_b32 v162,0\n"
"v_mov_b32 v163,0\n"
"v_mov_b32 v164,0\n"
"v_mov_b32 v165,0\n"
"v_mov_b32 v166,0\n"
"v_mov_b32 v167,0\n"
"v_mov_b32 v168,0\n"
"v_mov_b32 v169,0\n"
"v_mov_b32 v170,0\n"
"v_mov_b32 v171,0\n"
"v_mov_b32 v172,0\n"
"v_mov_b32 v173,0\n"
"v_mov_b32 v174,0\n"
"v_mov_b32 v175,0\n"
"v_accvgpr_write_b32 a140,0\n"
"v_accvgpr_write_b32 a141,0\n"
"v_accvgpr_write_b32 a142,0\n"
"v_accvgpr_write_b32 a143,0\n"
"L1042_%=:\n"
"s_lshl_b32 s91,s88,13\n"
SA"s91,s91,s39\n"
SA"s91,s91,0\n"
BL"a[148:151],v224,s[8:11],s91 offen offset:0 sc1\n"
BL"a[152:155],v224,s[8:11],s91 offen offset:1024 sc1\n"
BL"a[156:159],v224,s[8:11],s91 offen offset:2048 sc1\n"
BL"a[160:163],v224,s[8:11],s91 offen offset:3072 sc1\n"
SA"s91,s91,4096\n"
BL"a[164:167],v224,s[8:11],s91 offen offset:0 sc1\n"
BL"a[168:171],v224,s[8:11],s91 offen offset:1024 sc1\n"
BL"a[172:175],v224,s[8:11],s91 offen offset:2048 sc1\n"
BL"a[176:179],v224,s[8:11],s91 offen offset:3072 sc1\n"
WT"vmcnt(0)\n"
MF"v[240:243],a[148:151],a[32:35],0\n"
MF"v[240:243],a[152:155],a[36:39],v[240:243]\n"
MF"v[240:243],a[156:159],a[40:43],v[240:243]\n"
MF"v[240:243],a[160:163],a[44:47],v[240:243]\n"
MF"v[244:247],a[164:167],a[32:35],0\n"
MF"v[244:247],a[168:171],a[36:39],v[244:247]\n"
MF"v[244:247],a[172:175],a[40:43],v[244:247]\n"
MF"v[244:247],a[176:179],a[44:47],v[244:247]\n"
"s_lshl_b32 s91,s88,5\n"
"v_subrev_u32 v250,s91,v249\n"
"s_nop 7\n"
"v_cmp_gt_i32 vcc,0,v250\n"
"s_nop 1\n"
"v_cndmask_b32 v240,v240,v255,vcc\n"
"v_cmp_gt_i32 vcc,1,v250\n"
"s_nop 1\n"
"v_cndmask_b32 v241,v241,v255,vcc\n"
"v_cmp_gt_i32 vcc,2,v250\n"
"s_nop 1\n"
"v_cndmask_b32 v242,v242,v255,vcc\n"
"v_cmp_gt_i32 vcc,3,v250\n"
"s_nop 1\n"
"v_cndmask_b32 v243,v243,v255,vcc\n"
"v_cmp_gt_i32 vcc,16,v250\n"
"s_nop 1\n"
"v_cndmask_b32 v244,v244,v255,vcc\n"
"v_cmp_gt_i32 vcc,17,v250\n"
"s_nop 1\n"
"v_cndmask_b32 v245,v245,v255,vcc\n"
"v_cmp_gt_i32 vcc,18,v250\n"
"s_nop 1\n"
"v_cndmask_b32 v246,v246,v255,vcc\n"
"v_cmp_gt_i32 vcc,19,v250\n"
"s_nop 1\n"
"v_cndmask_b32 v247,v247,v255,vcc\n"
"s_cmp_lg_u32 s90,0\n"
"s_cbranch_scc1 L1043_%=\n"
"v_max3_f32 v248,v248,v240,v241\n"
"v_max3_f32 v248,v248,v242,v243\n"
"v_max3_f32 v248,v248,v244,v245\n"
"v_max3_f32 v248,v248,v246,v247\n"
"s_branch L1044_%=\n"
"L1043_%=:\n"
"s_lshl_b32 s91,s88,13\n"
SA"s91,s91,s39\n"
SA"s91,s91,524288\n"
BL"a[148:151],v224,s[8:11],s91 offen offset:0 sc1\n"
BL"a[152:155],v224,s[8:11],s91 offen offset:1024 sc1\n"
BL"a[156:159],v224,s[8:11],s91 offen offset:2048 sc1\n"
BL"a[160:163],v224,s[8:11],s91 offen offset:3072 sc1\n"
SA"s91,s91,4096\n"
BL"a[164:167],v224,s[8:11],s91 offen offset:0 sc1\n"
BL"a[168:171],v224,s[8:11],s91 offen offset:1024 sc1\n"
BL"a[172:175],v224,s[8:11],s91 offen offset:2048 sc1\n"
BL"a[176:179],v224,s[8:11],s91 offen offset:3072 sc1\n"
WT"vmcnt(0)\n"
"v_sub_f32 v240,v240,v248\n"
"v_sub_f32 v241,v241,v248\n"
"v_sub_f32 v242,v242,v248\n"
"v_sub_f32 v243,v243,v248\n"
"v_sub_f32 v244,v244,v248\n"
"v_sub_f32 v245,v245,v248\n"
"v_sub_f32 v246,v246,v248\n"
"v_sub_f32 v247,v247,v248\n"
EX"v240,v240\n"
EX"v241,v241\n"
EX"v242,v242\n"
EX"v243,v243\n"
EX"v244,v244\n"
EX"v245,v245\n"
EX"v246,v246\n"
EX"v247,v247\n"
"s_nop 0\n"
PK"v240,v240,v241\n"
PK"v241,v242,v243\n"
PK"v242,v244,v245\n"
PK"v243,v246,v247\n"
"s_nop 1\n"
MF"v[144:147],a[148:151],v[240:243],v[144:147]\n"
MF"v[148:151],a[152:155],v[240:243],v[148:151]\n"
MF"v[152:155],a[156:159],v[240:243],v[152:155]\n"
MF"v[156:159],a[160:163],v[240:243],v[156:159]\n"
MF"v[160:163],a[164:167],v[240:243],v[160:163]\n"
MF"v[164:167],a[168:171],v[240:243],v[164:167]\n"
MF"v[168:171],a[172:175],v[240:243],v[168:171]\n"
MF"v[172:175],a[176:179],v[240:243],v[172:175]\n"
MF"a[140:143],a[128:131],v[240:243],a[140:143]\n"
"s_nop 7\n"
"L1044_%=:\n"
SA"s88,s88,1\n"
"s_cmp_le_u32 s88,s89\n"
"s_cbranch_scc1 L1042_%=\n"
"s_cmp_lg_u32 s90,0\n"
"s_cbranch_scc1 L1045_%=\n"
"v_mov_b32 v254,v248\n"
"s_nop 1\n"
"v_permlane16_swap_b32 v254,v248\n"
"s_nop 1\n"
"v_max_f32 v248,v248,v254\n"
"v_mov_b32 v254,v248\n"
"s_nop 1\n"
"v_permlane32_swap_b32 v254,v248\n"
"s_nop 1\n"
"v_max_f32 v248,v248,v254\n"
"s_mov_b32 s90,1\n"
"s_mov_b32 s88,0\n"
"s_branch L1042_%=\n"
"L1045_%=:\n"
"s_nop 7\n"
"v_accvgpr_read_b32 v251,a140\n"
"s_nop 1\n"
"s_branch L1040_%=\n"
"L1047_%=:\n"
"s_nop 15\n"
WT"vmcnt(0)\n"
"s_lshl_b32 s89,s34,3\n"
"s_sub_u32 s89,120,s89\n"
SA"s89,s89,s30\n"
SA"s89,s89,4\n"
"s_lshl_b32 s91,s89,4\n"
"s_lshr_b32 s89,s89,1\n"
"v_bfe_u32 v249,v224,4,4\n"
"v_lshrrev_b32 v254,8,v224\n"
"v_lshlrev_b32 v254,2,v254\n"
"v_sub_u32 v249,v249,v254\n"
"v_add_u32 v249,s91,v249\n"
"v_mov_b32 v255,0xff800000\n"
"v_mov_b32 v248,v255\n"
"s_mov_b32 s90,0\n"
"s_mov_b32 s88,0\n"
"v_mov_b32 v176,0\n"
"v_mov_b32 v177,0\n"
"v_mov_b32 v178,0\n"
"v_mov_b32 v179,0\n"
"v_mov_b32 v180,0\n"
"v_mov_b32 v181,0\n"
"v_mov_b32 v182,0\n"
"v_mov_b32 v183,0\n"
"v_mov_b32 v184,0\n"
"v_mov_b32 v185,0\n"
"v_mov_b32 v186,0\n"
"v_mov_b32 v187,0\n"
"v_mov_b32 v188,0\n"
"v_mov_b32 v189,0\n"
"v_mov_b32 v190,0\n"
"v_mov_b32 v191,0\n"
"v_mov_b32 v192,0\n"
"v_mov_b32 v193,0\n"
"v_mov_b32 v194,0\n"
"v_mov_b32 v195,0\n"
"v_mov_b32 v196,0\n"
"v_mov_b32 v197,0\n"
"v_mov_b32 v198,0\n"
"v_mov_b32 v199,0\n"
"v_mov_b32 v200,0\n"
"v_mov_b32 v201,0\n"
"v_mov_b32 v202,0\n"
"v_mov_b32 v203,0\n"
"v_mov_b32 v204,0\n"
"v_mov_b32 v205,0\n"
"v_mov_b32 v206,0\n"
"v_mov_b32 v207,0\n"
"v_accvgpr_write_b32 a144,0\n"
"v_accvgpr_write_b32 a145,0\n"
"v_accvgpr_write_b32 a146,0\n"
"v_accvgpr_write_b32 a147,0\n"
"L1048_%=:\n"
"s_lshl_b32 s91,s88,13\n"
SA"s91,s91,s39\n"
SA"s91,s91,0\n"
BL"a[148:151],v224,s[8:11],s91 offen offset:0 sc1\n"
BL"a[152:155],v224,s[8:11],s91 offen offset:1024 sc1\n"
BL"a[156:159],v224,s[8:11],s91 offen offset:2048 sc1\n"
BL"a[160:163],v224,s[8:11],s91 offen offset:3072 sc1\n"
SA"s91,s91,4096\n"
BL"a[164:167],v224,s[8:11],s91 offen offset:0 sc1\n"
BL"a[168:171],v224,s[8:11],s91 offen offset:1024 sc1\n"
BL"a[172:175],v224,s[8:11],s91 offen offset:2048 sc1\n"
BL"a[176:179],v224,s[8:11],s91 offen offset:3072 sc1\n"
WT"vmcnt(0)\n"
MF"v[240:243],a[148:151],a[48:51],0\n"
MF"v[240:243],a[152:155],a[52:55],v[240:243]\n"
MF"v[240:243],a[156:159],a[56:59],v[240:243]\n"
MF"v[240:243],a[160:163],a[60:63],v[240:243]\n"
MF"v[244:247],a[164:167],a[48:51],0\n"
MF"v[244:247],a[168:171],a[52:55],v[244:247]\n"
MF"v[244:247],a[172:175],a[56:59],v[244:247]\n"
MF"v[244:247],a[176:179],a[60:63],v[244:247]\n"
"s_lshl_b32 s91,s88,5\n"
"v_subrev_u32 v250,s91,v249\n"
"s_nop 7\n"
"v_cmp_gt_i32 vcc,0,v250\n"
"s_nop 1\n"
"v_cndmask_b32 v240,v240,v255,vcc\n"
"v_cmp_gt_i32 vcc,1,v250\n"
"s_nop 1\n"
"v_cndmask_b32 v241,v241,v255,vcc\n"
"v_cmp_gt_i32 vcc,2,v250\n"
"s_nop 1\n"
"v_cndmask_b32 v242,v242,v255,vcc\n"
"v_cmp_gt_i32 vcc,3,v250\n"
"s_nop 1\n"
"v_cndmask_b32 v243,v243,v255,vcc\n"
"v_cmp_gt_i32 vcc,16,v250\n"
"s_nop 1\n"
"v_cndmask_b32 v244,v244,v255,vcc\n"
"v_cmp_gt_i32 vcc,17,v250\n"
"s_nop 1\n"
"v_cndmask_b32 v245,v245,v255,vcc\n"
"v_cmp_gt_i32 vcc,18,v250\n"
"s_nop 1\n"
"v_cndmask_b32 v246,v246,v255,vcc\n"
"v_cmp_gt_i32 vcc,19,v250\n"
"s_nop 1\n"
"v_cndmask_b32 v247,v247,v255,vcc\n"
"s_cmp_lg_u32 s90,0\n"
"s_cbranch_scc1 L1049_%=\n"
"v_max3_f32 v248,v248,v240,v241\n"
"v_max3_f32 v248,v248,v242,v243\n"
"v_max3_f32 v248,v248,v244,v245\n"
"v_max3_f32 v248,v248,v246,v247\n"
"s_branch L1050_%=\n"
"L1049_%=:\n"
"s_lshl_b32 s91,s88,13\n"
SA"s91,s91,s39\n"
SA"s91,s91,524288\n"
BL"a[148:151],v224,s[8:11],s91 offen offset:0 sc1\n"
BL"a[152:155],v224,s[8:11],s91 offen offset:1024 sc1\n"
BL"a[156:159],v224,s[8:11],s91 offen offset:2048 sc1\n"
BL"a[160:163],v224,s[8:11],s91 offen offset:3072 sc1\n"
SA"s91,s91,4096\n"
BL"a[164:167],v224,s[8:11],s91 offen offset:0 sc1\n"
BL"a[168:171],v224,s[8:11],s91 offen offset:1024 sc1\n"
BL"a[172:175],v224,s[8:11],s91 offen offset:2048 sc1\n"
BL"a[176:179],v224,s[8:11],s91 offen offset:3072 sc1\n"
WT"vmcnt(0)\n"
"v_sub_f32 v240,v240,v248\n"
"v_sub_f32 v241,v241,v248\n"
"v_sub_f32 v242,v242,v248\n"
"v_sub_f32 v243,v243,v248\n"
"v_sub_f32 v244,v244,v248\n"
"v_sub_f32 v245,v245,v248\n"
"v_sub_f32 v246,v246,v248\n"
"v_sub_f32 v247,v247,v248\n"
EX"v240,v240\n"
EX"v241,v241\n"
EX"v242,v242\n"
EX"v243,v243\n"
EX"v244,v244\n"
EX"v245,v245\n"
EX"v246,v246\n"
EX"v247,v247\n"
"s_nop 0\n"
PK"v240,v240,v241\n"
PK"v241,v242,v243\n"
PK"v242,v244,v245\n"
PK"v243,v246,v247\n"
"s_nop 1\n"
MF"v[176:179],a[148:151],v[240:243],v[176:179]\n"
MF"v[180:183],a[152:155],v[240:243],v[180:183]\n"
MF"v[184:187],a[156:159],v[240:243],v[184:187]\n"
MF"v[188:191],a[160:163],v[240:243],v[188:191]\n"
MF"v[192:195],a[164:167],v[240:243],v[192:195]\n"
MF"v[196:199],a[168:171],v[240:243],v[196:199]\n"
MF"v[200:203],a[172:175],v[240:243],v[200:203]\n"
MF"v[204:207],a[176:179],v[240:243],v[204:207]\n"
MF"a[144:147],a[128:131],v[240:243],a[144:147]\n"
"s_nop 7\n"
"L1050_%=:\n"
SA"s88,s88,1\n"
"s_cmp_le_u32 s88,s89\n"
"s_cbranch_scc1 L1048_%=\n"
"s_cmp_lg_u32 s90,0\n"
"s_cbranch_scc1 L1051_%=\n"
"v_mov_b32 v254,v248\n"
"s_nop 1\n"
"v_permlane16_swap_b32 v254,v248\n"
"s_nop 1\n"
"v_max_f32 v248,v248,v254\n"
"v_mov_b32 v254,v248\n"
"s_nop 1\n"
"v_permlane32_swap_b32 v254,v248\n"
"s_nop 1\n"
"v_max_f32 v248,v248,v254\n"
"s_mov_b32 s90,1\n"
"s_mov_b32 s88,0\n"
"s_branch L1048_%=\n"
"L1051_%=:\n"
"s_nop 7\n"
"v_accvgpr_read_b32 v252,a144\n"
"s_nop 1\n"
"s_branch L1046_%=\n"
"L1069_%=:\n"
"s_nop 15\n"
WT"vmcnt(0)\n"
"s_lshl_b32 s89,s34,3\n"
"s_sub_u32 s89,120,s89\n"
SA"s89,s89,s30\n"
SA"s89,s89,0\n"
"s_lshl_b32 s91,s89,4\n"
"s_lshr_b32 s89,s89,1\n"
"v_bfe_u32 v249,v224,4,4\n"
"v_lshrrev_b32 v254,8,v224\n"
"v_lshlrev_b32 v254,2,v254\n"
"v_sub_u32 v249,v249,v254\n"
"v_add_u32 v249,s91,v249\n"
"v_mov_b32 v255,0xff800000\n"
"v_mov_b32 v248,v255\n"
"s_mov_b32 s90,0\n"
"s_mov_b32 s88,0\n"
"v_mov_b32 v144,0\n"
"v_mov_b32 v145,0\n"
"v_mov_b32 v146,0\n"
"v_mov_b32 v147,0\n"
"v_mov_b32 v148,0\n"
"v_mov_b32 v149,0\n"
"v_mov_b32 v150,0\n"
"v_mov_b32 v151,0\n"
"v_mov_b32 v152,0\n"
"v_mov_b32 v153,0\n"
"v_mov_b32 v154,0\n"
"v_mov_b32 v155,0\n"
"v_mov_b32 v156,0\n"
"v_mov_b32 v157,0\n"
"v_mov_b32 v158,0\n"
"v_mov_b32 v159,0\n"
"v_mov_b32 v160,0\n"
"v_mov_b32 v161,0\n"
"v_mov_b32 v162,0\n"
"v_mov_b32 v163,0\n"
"v_mov_b32 v164,0\n"
"v_mov_b32 v165,0\n"
"v_mov_b32 v166,0\n"
"v_mov_b32 v167,0\n"
"v_mov_b32 v168,0\n"
"v_mov_b32 v169,0\n"
"v_mov_b32 v170,0\n"
"v_mov_b32 v171,0\n"
"v_mov_b32 v172,0\n"
"v_mov_b32 v173,0\n"
"v_mov_b32 v174,0\n"
"v_mov_b32 v175,0\n"
"v_accvgpr_write_b32 a140,0\n"
"v_accvgpr_write_b32 a141,0\n"
"v_accvgpr_write_b32 a142,0\n"
"v_accvgpr_write_b32 a143,0\n"
"L1070_%=:\n"
"s_lshl_b32 s91,s88,13\n"
SA"s91,s91,s39\n"
SA"s91,s91,0\n"
BL"a[148:151],v224,s[8:11],s91 offen offset:0 sc1\n"
BL"a[152:155],v224,s[8:11],s91 offen offset:1024 sc1\n"
BL"a[156:159],v224,s[8:11],s91 offen offset:2048 sc1\n"
BL"a[160:163],v224,s[8:11],s91 offen offset:3072 sc1\n"
SA"s91,s91,4096\n"
BL"a[164:167],v224,s[8:11],s91 offen offset:0 sc1\n"
BL"a[168:171],v224,s[8:11],s91 offen offset:1024 sc1\n"
BL"a[172:175],v224,s[8:11],s91 offen offset:2048 sc1\n"
BL"a[176:179],v224,s[8:11],s91 offen offset:3072 sc1\n"
WT"vmcnt(0)\n"
MF"v[240:243],a[148:151],a[32:35],0\n"
MF"v[240:243],a[152:155],a[36:39],v[240:243]\n"
MF"v[240:243],a[156:159],a[40:43],v[240:243]\n"
MF"v[240:243],a[160:163],a[44:47],v[240:243]\n"
MF"v[244:247],a[164:167],a[32:35],0\n"
MF"v[244:247],a[168:171],a[36:39],v[244:247]\n"
MF"v[244:247],a[172:175],a[40:43],v[244:247]\n"
MF"v[244:247],a[176:179],a[44:47],v[244:247]\n"
"s_lshl_b32 s91,s88,5\n"
"v_subrev_u32 v250,s91,v249\n"
"s_nop 7\n"
"v_cmp_gt_i32 vcc,0,v250\n"
"s_nop 1\n"
"v_cndmask_b32 v240,v240,v255,vcc\n"
"v_cmp_gt_i32 vcc,1,v250\n"
"s_nop 1\n"
"v_cndmask_b32 v241,v241,v255,vcc\n"
"v_cmp_gt_i32 vcc,2,v250\n"
"s_nop 1\n"
"v_cndmask_b32 v242,v242,v255,vcc\n"
"v_cmp_gt_i32 vcc,3,v250\n"
"s_nop 1\n"
"v_cndmask_b32 v243,v243,v255,vcc\n"
"v_cmp_gt_i32 vcc,16,v250\n"
"s_nop 1\n"
"v_cndmask_b32 v244,v244,v255,vcc\n"
"v_cmp_gt_i32 vcc,17,v250\n"
"s_nop 1\n"
"v_cndmask_b32 v245,v245,v255,vcc\n"
"v_cmp_gt_i32 vcc,18,v250\n"
"s_nop 1\n"
"v_cndmask_b32 v246,v246,v255,vcc\n"
"v_cmp_gt_i32 vcc,19,v250\n"
"s_nop 1\n"
"v_cndmask_b32 v247,v247,v255,vcc\n"
"s_cmp_lg_u32 s90,0\n"
"s_cbranch_scc1 L1071_%=\n"
"v_max3_f32 v248,v248,v240,v241\n"
"v_max3_f32 v248,v248,v242,v243\n"
"v_max3_f32 v248,v248,v244,v245\n"
"v_max3_f32 v248,v248,v246,v247\n"
"s_branch L1072_%=\n"
"L1071_%=:\n"
"s_lshl_b32 s91,s88,13\n"
SA"s91,s91,s39\n"
SA"s91,s91,524288\n"
BL"a[148:151],v224,s[8:11],s91 offen offset:0 sc1\n"
BL"a[152:155],v224,s[8:11],s91 offen offset:1024 sc1\n"
BL"a[156:159],v224,s[8:11],s91 offen offset:2048 sc1\n"
BL"a[160:163],v224,s[8:11],s91 offen offset:3072 sc1\n"
SA"s91,s91,4096\n"
BL"a[164:167],v224,s[8:11],s91 offen offset:0 sc1\n"
BL"a[168:171],v224,s[8:11],s91 offen offset:1024 sc1\n"
BL"a[172:175],v224,s[8:11],s91 offen offset:2048 sc1\n"
BL"a[176:179],v224,s[8:11],s91 offen offset:3072 sc1\n"
WT"vmcnt(0)\n"
"v_sub_f32 v240,v240,v248\n"
"v_sub_f32 v241,v241,v248\n"
"v_sub_f32 v242,v242,v248\n"
"v_sub_f32 v243,v243,v248\n"
"v_sub_f32 v244,v244,v248\n"
"v_sub_f32 v245,v245,v248\n"
"v_sub_f32 v246,v246,v248\n"
"v_sub_f32 v247,v247,v248\n"
EX"v240,v240\n"
EX"v241,v241\n"
EX"v242,v242\n"
EX"v243,v243\n"
EX"v244,v244\n"
EX"v245,v245\n"
EX"v246,v246\n"
EX"v247,v247\n"
"s_nop 0\n"
PK"v240,v240,v241\n"
PK"v241,v242,v243\n"
PK"v242,v244,v245\n"
PK"v243,v246,v247\n"
"s_nop 1\n"
MF"v[144:147],a[148:151],v[240:243],v[144:147]\n"
MF"v[148:151],a[152:155],v[240:243],v[148:151]\n"
MF"v[152:155],a[156:159],v[240:243],v[152:155]\n"
MF"v[156:159],a[160:163],v[240:243],v[156:159]\n"
MF"v[160:163],a[164:167],v[240:243],v[160:163]\n"
MF"v[164:167],a[168:171],v[240:243],v[164:167]\n"
MF"v[168:171],a[172:175],v[240:243],v[168:171]\n"
MF"v[172:175],a[176:179],v[240:243],v[172:175]\n"
MF"a[140:143],a[128:131],v[240:243],a[140:143]\n"
"s_nop 7\n"
"L1072_%=:\n"
SA"s88,s88,1\n"
"s_cmp_le_u32 s88,s89\n"
"s_cbranch_scc1 L1070_%=\n"
"s_cmp_lg_u32 s90,0\n"
"s_cbranch_scc1 L1073_%=\n"
"v_mov_b32 v254,v248\n"
"s_nop 1\n"
"v_permlane16_swap_b32 v254,v248\n"
"s_nop 1\n"
"v_max_f32 v248,v248,v254\n"
"v_mov_b32 v254,v248\n"
"s_nop 1\n"
"v_permlane32_swap_b32 v254,v248\n"
"s_nop 1\n"
"v_max_f32 v248,v248,v254\n"
"s_mov_b32 s90,1\n"
"s_mov_b32 s88,0\n"
"s_branch L1070_%=\n"
"L1073_%=:\n"
"s_nop 7\n"
"v_accvgpr_read_b32 v251,a140\n"
"s_nop 1\n"
"s_branch L1068_%=\n"
"L1075_%=:\n"
"s_nop 15\n"
WT"vmcnt(0)\n"
"s_lshl_b32 s89,s34,3\n"
"s_sub_u32 s89,120,s89\n"
SA"s89,s89,s30\n"
SA"s89,s89,4\n"
"s_lshl_b32 s91,s89,4\n"
"s_lshr_b32 s89,s89,1\n"
"v_bfe_u32 v249,v224,4,4\n"
"v_lshrrev_b32 v254,8,v224\n"
"v_lshlrev_b32 v254,2,v254\n"
"v_sub_u32 v249,v249,v254\n"
"v_add_u32 v249,s91,v249\n"
"v_mov_b32 v255,0xff800000\n"
"v_mov_b32 v248,v255\n"
"s_mov_b32 s90,0\n"
"s_mov_b32 s88,0\n"
"v_mov_b32 v176,0\n"
"v_mov_b32 v177,0\n"
"v_mov_b32 v178,0\n"
"v_mov_b32 v179,0\n"
"v_mov_b32 v180,0\n"
"v_mov_b32 v181,0\n"
"v_mov_b32 v182,0\n"
"v_mov_b32 v183,0\n"
"v_mov_b32 v184,0\n"
"v_mov_b32 v185,0\n"
"v_mov_b32 v186,0\n"
"v_mov_b32 v187,0\n"
"v_mov_b32 v188,0\n"
"v_mov_b32 v189,0\n"
"v_mov_b32 v190,0\n"
"v_mov_b32 v191,0\n"
"v_mov_b32 v192,0\n"
"v_mov_b32 v193,0\n"
"v_mov_b32 v194,0\n"
"v_mov_b32 v195,0\n"
"v_mov_b32 v196,0\n"
"v_mov_b32 v197,0\n"
"v_mov_b32 v198,0\n"
"v_mov_b32 v199,0\n"
"v_mov_b32 v200,0\n"
"v_mov_b32 v201,0\n"
"v_mov_b32 v202,0\n"
"v_mov_b32 v203,0\n"
"v_mov_b32 v204,0\n"
"v_mov_b32 v205,0\n"
"v_mov_b32 v206,0\n"
"v_mov_b32 v207,0\n"
"v_accvgpr_write_b32 a144,0\n"
"v_accvgpr_write_b32 a145,0\n"
"v_accvgpr_write_b32 a146,0\n"
"v_accvgpr_write_b32 a147,0\n"
"L1076_%=:\n"
"s_lshl_b32 s91,s88,13\n"
SA"s91,s91,s39\n"
SA"s91,s91,0\n"
BL"a[148:151],v224,s[8:11],s91 offen offset:0 sc1\n"
BL"a[152:155],v224,s[8:11],s91 offen offset:1024 sc1\n"
BL"a[156:159],v224,s[8:11],s91 offen offset:2048 sc1\n"
BL"a[160:163],v224,s[8:11],s91 offen offset:3072 sc1\n"
SA"s91,s91,4096\n"
BL"a[164:167],v224,s[8:11],s91 offen offset:0 sc1\n"
BL"a[168:171],v224,s[8:11],s91 offen offset:1024 sc1\n"
BL"a[172:175],v224,s[8:11],s91 offen offset:2048 sc1\n"
BL"a[176:179],v224,s[8:11],s91 offen offset:3072 sc1\n"
WT"vmcnt(0)\n"
MF"v[240:243],a[148:151],a[48:51],0\n"
MF"v[240:243],a[152:155],a[52:55],v[240:243]\n"
MF"v[240:243],a[156:159],a[56:59],v[240:243]\n"
MF"v[240:243],a[160:163],a[60:63],v[240:243]\n"
MF"v[244:247],a[164:167],a[48:51],0\n"
MF"v[244:247],a[168:171],a[52:55],v[244:247]\n"
MF"v[244:247],a[172:175],a[56:59],v[244:247]\n"
MF"v[244:247],a[176:179],a[60:63],v[244:247]\n"
"s_lshl_b32 s91,s88,5\n"
"v_subrev_u32 v250,s91,v249\n"
"s_nop 7\n"
"v_cmp_gt_i32 vcc,0,v250\n"
"s_nop 1\n"
"v_cndmask_b32 v240,v240,v255,vcc\n"
"v_cmp_gt_i32 vcc,1,v250\n"
"s_nop 1\n"
"v_cndmask_b32 v241,v241,v255,vcc\n"
"v_cmp_gt_i32 vcc,2,v250\n"
"s_nop 1\n"
"v_cndmask_b32 v242,v242,v255,vcc\n"
"v_cmp_gt_i32 vcc,3,v250\n"
"s_nop 1\n"
"v_cndmask_b32 v243,v243,v255,vcc\n"
"v_cmp_gt_i32 vcc,16,v250\n"
"s_nop 1\n"
"v_cndmask_b32 v244,v244,v255,vcc\n"
"v_cmp_gt_i32 vcc,17,v250\n"
"s_nop 1\n"
"v_cndmask_b32 v245,v245,v255,vcc\n"
"v_cmp_gt_i32 vcc,18,v250\n"
"s_nop 1\n"
"v_cndmask_b32 v246,v246,v255,vcc\n"
"v_cmp_gt_i32 vcc,19,v250\n"
"s_nop 1\n"
"v_cndmask_b32 v247,v247,v255,vcc\n"
"s_cmp_lg_u32 s90,0\n"
"s_cbranch_scc1 L1077_%=\n"
"v_max3_f32 v248,v248,v240,v241\n"
"v_max3_f32 v248,v248,v242,v243\n"
"v_max3_f32 v248,v248,v244,v245\n"
"v_max3_f32 v248,v248,v246,v247\n"
"s_branch L1078_%=\n"
"L1077_%=:\n"
"s_lshl_b32 s91,s88,13\n"
SA"s91,s91,s39\n"
SA"s91,s91,524288\n"
BL"a[148:151],v224,s[8:11],s91 offen offset:0 sc1\n"
BL"a[152:155],v224,s[8:11],s91 offen offset:1024 sc1\n"
BL"a[156:159],v224,s[8:11],s91 offen offset:2048 sc1\n"
BL"a[160:163],v224,s[8:11],s91 offen offset:3072 sc1\n"
SA"s91,s91,4096\n"
BL"a[164:167],v224,s[8:11],s91 offen offset:0 sc1\n"
BL"a[168:171],v224,s[8:11],s91 offen offset:1024 sc1\n"
BL"a[172:175],v224,s[8:11],s91 offen offset:2048 sc1\n"
BL"a[176:179],v224,s[8:11],s91 offen offset:3072 sc1\n"
WT"vmcnt(0)\n"
"v_sub_f32 v240,v240,v248\n"
"v_sub_f32 v241,v241,v248\n"
"v_sub_f32 v242,v242,v248\n"
"v_sub_f32 v243,v243,v248\n"
"v_sub_f32 v244,v244,v248\n"
"v_sub_f32 v245,v245,v248\n"
"v_sub_f32 v246,v246,v248\n"
"v_sub_f32 v247,v247,v248\n"
EX"v240,v240\n"
EX"v241,v241\n"
EX"v242,v242\n"
EX"v243,v243\n"
EX"v244,v244\n"
EX"v245,v245\n"
EX"v246,v246\n"
EX"v247,v247\n"
"s_nop 0\n"
PK"v240,v240,v241\n"
PK"v241,v242,v243\n"
PK"v242,v244,v245\n"
PK"v243,v246,v247\n"
"s_nop 1\n"
MF"v[176:179],a[148:151],v[240:243],v[176:179]\n"
MF"v[180:183],a[152:155],v[240:243],v[180:183]\n"
MF"v[184:187],a[156:159],v[240:243],v[184:187]\n"
MF"v[188:191],a[160:163],v[240:243],v[188:191]\n"
MF"v[192:195],a[164:167],v[240:243],v[192:195]\n"
MF"v[196:199],a[168:171],v[240:243],v[196:199]\n"
MF"v[200:203],a[172:175],v[240:243],v[200:203]\n"
MF"v[204:207],a[176:179],v[240:243],v[204:207]\n"
MF"a[144:147],a[128:131],v[240:243],a[144:147]\n"
"s_nop 7\n"
"L1078_%=:\n"
SA"s88,s88,1\n"
"s_cmp_le_u32 s88,s89\n"
"s_cbranch_scc1 L1076_%=\n"
"s_cmp_lg_u32 s90,0\n"
"s_cbranch_scc1 L1079_%=\n"
"v_mov_b32 v254,v248\n"
"s_nop 1\n"
"v_permlane16_swap_b32 v254,v248\n"
"s_nop 1\n"
"v_max_f32 v248,v248,v254\n"
"v_mov_b32 v254,v248\n"
"s_nop 1\n"
"v_permlane32_swap_b32 v254,v248\n"
"s_nop 1\n"
"v_max_f32 v248,v248,v254\n"
"s_mov_b32 s90,1\n"
"s_mov_b32 s88,0\n"
"s_branch L1076_%=\n"
"L1079_%=:\n"
"s_nop 7\n"
"v_accvgpr_read_b32 v252,a144\n"
"s_nop 1\n"
"s_branch L1074_%=\n"
"L1080_%=:\n"
"s_mov_b32 s54,0\n"
"L1082_%=:\n"
"s_sleep 2\n"
"buffer_load_dwordx2 v[240:241],v238,s[8:11],s63 offen sc1\n"
WT"vmcnt(0)\n"
SA"s54,s54,1\n"
"v_cmp_eq_u64 vcc,s[28:29],v[240:241]\n"
"s_cmp_eq_u64 vcc,exec\n"
"s_cbranch_scc1 L1081_%=\n"
"s_cmp_lt_u32 s54,1048576\n"
"s_cbranch_scc1 L1082_%=\n"
"s_branch L1081_%=\n"
"L1083_%=:\n"
"s_mov_b32 s54,0\n"
"L1085_%=:\n"
"s_sleep 2\n"
"buffer_load_dwordx2 v[240:241],v238,s[8:11],s63 offen sc1\n"
WT"vmcnt(0)\n"
SA"s54,s54,1\n"
"v_cmp_eq_u64 vcc,s[28:29],v[240:241]\n"
"s_cmp_eq_u64 vcc,exec\n"
"s_cbranch_scc1 L1084_%=\n"
"s_cmp_lt_u32 s54,1048576\n"
"s_cbranch_scc1 L1085_%=\n"
"s_branch L1084_%=\n"
"L1086_%=:\n"
"s_mov_b32 s54,0\n"
"L1088_%=:\n"
"s_sleep 2\n"
"buffer_load_dwordx2 v[240:241],v244,s[8:11],s92 offen sc1\n"
WT"vmcnt(0)\n"
SA"s54,s54,1\n"
"v_cmp_eq_u64 vcc,s[28:29],v[240:241]\n"
"s_cmp_eq_u64 vcc,exec\n"
"s_cbranch_scc1 L1087_%=\n"
"s_cmp_lt_u32 s54,1048576\n"
"s_cbranch_scc1 L1088_%=\n"
"s_branch L1087_%=\n"
"LTH_%=:\n"
RD"a[92:95],v227 offset:7168\n"
MF"v[0:3],a[64:67],v[80:83],0\n"
WT"lgkmcnt(4)\n"
"v_accvgpr_read_b32 v251,a140\n"
"v_subrev_u32 v253,0x2b800000,v251\n"
MF"v[0:3],a[68:71],v[88:91],v[0:3]\n"
RD"a[96:99],v227 offset:8192\n"
MF"v[40:43],a[68:71],v[120:123],0\n"
"v_cmp_le_u32 vcc,0x34000000,v253\n"
"s_cmp_lg_u64 vcc,0\n"
"s_cbranch_scc1 L1069_%=\n"
"L1068_%=:\n"
MF"v[40:43],a[64:67],v[112:115],v[40:43]\n"
"v_rcp_f32 v251,v251\n"
MF"v[4:7],a[72:75],v[80:83],0\n"
SA"s41,s41,32768\n"
RD"a[100:103],v227 offset:9216\n"
MU"v144,v251,v144\n"
MF"v[4:7],a[76:79],v[88:91],v[4:7]\n"
"s_cmp_eq_u32 s41,163840\n"
RD"a[104:107],v227 offset:10240\n"
MU"v145,v251,v145\n"
MF"v[44:47],a[76:79],v[120:123],0\n"
"s_cselect_b32 s41,0,s41\n"
MU"v146,v251,v146\n"
"v_add_u32 v226,s41,v224\n"
MF"v[44:47],a[72:75],v[112:115],v[44:47]\n"
WT"lgkmcnt(3)\n"
RD"a[108:111],v227 offset:11264\n"
MU"v147,v251,v147\n"
MF"v[8:11],a[80:83],v[80:83],0\n"
SA"s41,s41,32768\n"
MU"v148,v251,v148\n"
MU"v149,v251,v149\n"
MF"v[8:11],a[84:87],v[88:91],v[8:11]\n"
"s_cmp_eq_u32 s41,163840\n"
RD"a[112:115],v227 offset:12288\n"
MU"v150,v251,v150\n"
MU"v151,v251,v151\n"
MF"v[48:51],a[84:87],v[120:123],0\n"
"s_cselect_b32 s41,0,s41\n"
PK"v144,v144,v145\n"
"v_add_u32 v235,s41,v224\n"
PK"v145,v146,v147\n"
MF"v[48:51],a[80:83],v[112:115],v[48:51]\n"
RD"a[116:119],v227 offset:13312\n"
PK"v146,v148,v149\n"
MF"v[12:15],a[88:91],v[80:83],0\n"
SA"s41,s41,32768\n"
PK"v147,v150,v151\n"
MU"v152,v251,v152\n"
MF"v[12:15],a[92:95],v[88:91],v[12:15]\n"
"s_cmp_eq_u32 s41,163840\n"
BS"v[144:147],v224,s[8:11],s60 offen offset:0 sc1\n"
MU"v153,v251,v153\n"
MF"v[52:55],a[92:95],v[120:123],0\n"
"s_cselect_b32 s41,0,s41\n"
RD"a[120:123],v227 offset:14336\n"
"v_add_u32 v236,s41,v224\n"
MU"v154,v251,v154\n"
MF"v[52:55],a[88:91],v[112:115],v[52:55]\n"
WT"lgkmcnt(3)\n"
RD"a[124:127],v227 offset:15360\n"
MU"v155,v251,v155\n"
MU"v156,v251,v156\n"
MF"v[16:19],a[96:99],v[80:83],0\n"
SA"s41,s41,32768\n"
MU"v157,v251,v157\n"
MU"v158,v251,v158\n"
MU"v159,v251,v159\n"
MF"v[16:19],a[100:103],v[88:91],v[16:19]\n"
"s_cmp_eq_u32 s41,163840\n"
RD"a[64:67],v227 offset:16384\n"
PK"v152,v152,v153\n"
PK"v153,v154,v155\n"
MF"v[56:59],a[100:103],v[120:123],0\n"
"s_cselect_b32 s41,0,s41\n"
PK"v154,v156,v157\n"
"v_add_u32 v237,s41,v224\n"
PK"v155,v158,v159\n"
MF"v[56:59],a[96:99],v[112:115],v[56:59]\n"
BS"v[152:155],v224,s[8:11],s60 offen offset:1024 sc1\n"
MU"v160,v251,v160\n"
MF"v[20:23],a[104:107],v[80:83],0\n"
RD"a[68:71],v227 offset:17408\n"
MU"v161,v251,v161\n"
MU"v162,v251,v162\n"
MF"v[20:23],a[108:111],v[88:91],v[20:23]\n"
WT"vmcnt(2)\n"
"buffer_store_dwordx2 v[232:233],v230,s[8:11],s61 offen sc1\n"
MU"v163,v251,v163\n"
MF"v[60:63],a[108:111],v[120:123],0\n"
RD"a[72:75],v227 offset:18432\n"
MU"v164,v251,v164\n"
MU"v165,v251,v165\n"
MF"v[60:63],a[104:107],v[112:115],v[60:63]\n"
WT"lgkmcnt(3)\n"
RD"a[76:79],v227 offset:19456\n"
MU"v166,v251,v166\n"
MU"v167,v251,v167\n"
MF"v[24:27],a[112:115],v[80:83],0\n"
SA"s51,s60,16384\n"
"v_accvgpr_read_b32 v252,a144\n"
"v_subrev_u32 v253,0x2b800000,v252\n"
PK"v160,v160,v161\n"
MF"v[24:27],a[116:119],v[88:91],v[24:27]\n"
"s_lshl_b32 s62,s34,14\n"
RD"a[80:83],v227 offset:20480\n"
PK"v161,v162,v163\n"
PK"v162,v164,v165\n"
MF"v[64:67],a[116:119],v[120:123],0\n"
"s_sub_u32 s62,s61,s62\n"
"v_cmp_le_u32 vcc,0x34000000,v253\n"
"s_cmp_lg_u64 vcc,0\n"
"s_cbranch_scc1 L1075_%=\n"
"L1074_%=:\n"
MF"v[64:67],a[112:115],v[112:115],v[64:67]\n"
SA"s62,s62,122880\n"
"v_rcp_f32 v252,v252\n"
PK"v163,v166,v167\n"
MF"v[28:31],a[120:123],v[80:83],0\n"
BS"v[160:163],v224,s[8:11],s60 offen offset:2048 sc1\n"
MU"v168,v251,v168\n"
MF"v[28:31],a[124:127],v[88:91],v[28:31]\n"
RD"a[84:87],v227 offset:21504\n"
RD"a[88:91],v227 offset:22528\n"
MU"v169,v251,v169\n"
MF"v[68:71],a[124:127],v[120:123],0\n"
MU"v170,v251,v170\n"
MU"v171,v251,v171\n"
MU"v172,v251,v172\n"
MF"v[68:71],a[120:123],v[112:115],v[68:71]\n"
WT"lgkmcnt(3)\n"
RD"a[92:95],v227 offset:23552\n"
MU"v173,v251,v173\n"
MU"v174,v251,v174\n"
MF"v[32:35],a[64:67],v[80:83],0\n"
MU"v175,v251,v175\n"
PK"v168,v168,v169\n"
PK"v169,v170,v171\n"
MF"v[32:35],a[68:71],v[88:91],v[32:35]\n"
RD"a[96:99],v227 offset:24576\n"
MU"v176,v252,v176\n"
MU"v177,v252,v177\n"
MF"v[72:75],a[68:71],v[120:123],0\n"
PK"v170,v172,v173\n"
PK"v171,v174,v175\n"
MU"v178,v252,v178\n"
MF"v[72:75],a[64:67],v[112:115],v[72:75]\n"
BS"v[168:171],v224,s[8:11],s60 offen offset:3072 sc1\n"
MU"v179,v252,v179\n"
MF"v[36:39],a[72:75],v[80:83],0\n"
RD"a[100:103],v227 offset:25600\n"
MU"v180,v252,v180\n"
MU"v181,v252,v181\n"
MF"v[36:39],a[76:79],v[88:91],v[36:39]\n"
RD"a[104:107],v227 offset:26624\n"
MU"v182,v252,v182\n"
MU"v183,v252,v183\n"
MF"v[76:79],a[76:79],v[120:123],0\n"
PK"v176,v176,v177\n"
PK"v177,v178,v179\n"
PK"v178,v180,v181\n"
MF"v[76:79],a[72:75],v[112:115],v[76:79]\n"
WT"lgkmcnt(3)\n"
RD"a[108:111],v227 offset:27648\n"
PK"v179,v182,v183\n"
MU"v184,v252,v184\n"
MF"v[0:3],a[80:83],v[96:99],v[0:3]\n"
BS"v[176:179],v224,s[8:11],s51 offen offset:0 sc1\n"
MU"v185,v252,v185\n"
MF"v[0:3],a[84:87],v[104:107],v[0:3]\n"
RD"a[112:115],v227 offset:28672\n"
MU"v186,v252,v186\n"
MU"v187,v252,v187\n"
MF"v[40:43],a[84:87],v[136:139],v[40:43]\n"
MU"v188,v252,v188\n"
MU"v189,v252,v189\n"
MU"v190,v252,v190\n"
MF"v[40:43],a[80:83],v[128:131],v[40:43]\n"
RD"a[116:119],v227 offset:29696\n"
MU"v191,v252,v191\n"
PK"v184,v184,v185\n"
MF"v[4:7],a[88:91],v[96:99],v[4:7]\n"
PK"v185,v186,v187\n"
PK"v186,v188,v189\n"
PK"v187,v190,v191\n"
MF"v[4:7],a[92:95],v[104:107],v[4:7]\n"
BS"v[184:187],v224,s[8:11],s51 offen offset:1024 sc1\n"
MU"v192,v252,v192\n"
MF"v[44:47],a[92:95],v[136:139],v[44:47]\n"
RD"a[120:123],v227 offset:30720\n"
MU"v193,v252,v193\n"
MU"v194,v252,v194\n"
MF"v[44:47],a[88:91],v[128:131],v[44:47]\n"
WT"lgkmcnt(3)\n"
RD"a[124:127],v227 offset:31744\n"
"v_bfe_u32 v238,v224,4,2\n"
MU"v195,v252,v195\n"
MF"v[8:11],a[96:99],v[96:99],v[8:11]\n"
"s_andn2_b32 s63,s61,255\n"
"s_barrier\n"
MF"v[8:11],a[100:103],v[104:107],v[8:11]\n"
RD"a[64:67],v226 offset:0\n"
"v_lshlrev_b32 v238,6,v238\n"
MU"v196,v252,v196\n"
MF"v[48:51],a[100:103],v[136:139],v[48:51]\n"
"buffer_load_dwordx2 v[240:241],v238,s[8:11],s63 offen sc1\n"
MU"v197,v252,v197\n"
MF"v[48:51],a[96:99],v[128:131],v[48:51]\n"
RD"a[68:71],v226 offset:1024\n"
MU"v198,v252,v198\n"
MU"v199,v252,v199\n"
MF"v[12:15],a[104:107],v[96:99],v[12:15]\n"
PK"v192,v192,v193\n"
PK"v193,v194,v195\n"
PK"v194,v196,v197\n"
MF"v[12:15],a[108:111],v[104:107],v[12:15]\n"
RD"a[72:75],v226 offset:2048\n"
PK"v195,v198,v199\n"
MU"v200,v252,v200\n"
MF"v[52:55],a[108:111],v[136:139],v[52:55]\n"
BS"v[192:195],v224,s[8:11],s51 offen offset:2048 sc1\n"
MU"v201,v252,v201\n"
MF"v[52:55],a[104:107],v[128:131],v[52:55]\n"
WT"lgkmcnt(3)\n"
RD"a[76:79],v226 offset:3072\n"
MU"v202,v252,v202\n"
MU"v203,v252,v203\n"
MF"v[16:19],a[112:115],v[96:99],v[16:19]\n"
MU"v204,v252,v204\n"
MU"v205,v252,v205\n"
MU"v206,v252,v206\n"
MF"v[16:19],a[116:119],v[104:107],v[16:19]\n"
RD"a[80:83],v226 offset:4096\n"
MU"v207,v252,v207\n"
PK"v200,v200,v201\n"
MF"v[56:59],a[116:119],v[136:139],v[56:59]\n"
"v_readlane_b32 s43,v228,s42\n"
PK"v201,v202,v203\n"
PK"v202,v204,v205\n"
MF"v[56:59],a[112:115],v[128:131],v[56:59]\n"
RD"a[84:87],v226 offset:5120\n"
PK"v203,v206,v207\n"
MF"v[20:23],a[120:123],v[96:99],v[20:23]\n"
BS"v[200:203],v224,s[8:11],s51 offen offset:3072 sc1\n"
MF"v[20:23],a[124:127],v[104:107],v[20:23]\n"
RD"a[88:91],v226 offset:6144\n"
MF"v[60:63],a[124:127],v[136:139],v[60:63]\n"
"s_mov_b32 m0,s40\n"
MF"v[60:63],a[120:123],v[128:131],v[60:63]\n"
WT"lgkmcnt(3)\n"
RD"a[92:95],v226 offset:7168\n"
MF"v[24:27],a[64:67],v[96:99],v[24:27]\n"
"v_readlane_b32 s44,v229,s42\n"
MF"v[24:27],a[68:71],v[104:107],v[24:27]\n"
RD"a[96:99],v227 offset:0\n"
MF"v[64:67],a[68:71],v[136:139],v[64:67]\n"
SA"s42,s42,1\n"
BL"v225,s[8:11],s43 offen offset:0 sc1 lds\n"
MF"v[64:67],a[64:67],v[128:131],v[64:67]\n"
RD"a[100:103],v227 offset:1024\n"
MF"v[28:31],a[72:75],v[96:99],v[28:31]\n"
MF"v[28:31],a[76:79],v[104:107],v[28:31]\n"
RD"a[104:107],v227 offset:2048\n"
MF"v[68:71],a[76:79],v[136:139],v[68:71]\n"
MF"v[68:71],a[72:75],v[128:131],v[68:71]\n"
WT"lgkmcnt(3)\n"
RD"a[108:111],v227 offset:3072\n"
MF"v[32:35],a[80:83],v[96:99],v[32:35]\n"
BL"v225,s[8:11],s43 offen offset:1024 sc1 lds\n"
MF"v[32:35],a[84:87],v[104:107],v[32:35]\n"
RD"a[112:115],v227 offset:4096\n"
MF"v[72:75],a[84:87],v[136:139],v[72:75]\n"
MF"v[72:75],a[80:83],v[128:131],v[72:75]\n"
RD"a[116:119],v227 offset:5120\n"
MF"v[36:39],a[88:91],v[96:99],v[36:39]\n"
MF"v[36:39],a[92:95],v[104:107],v[36:39]\n"
RD"a[120:123],v227 offset:6144\n"
MF"v[76:79],a[92:95],v[136:139],v[76:79]\n"
BL"v225,s[8:11],s43 offen offset:2048 sc1 lds\n"
MF"v[76:79],a[88:91],v[128:131],v[76:79]\n"
WT"lgkmcnt(3)\n"
RD"a[124:127],v227 offset:7168\n"
MF"v[84:87],a[96:99],v[144:147],0\n"
MF"v[84:87],a[100:103],v[152:155],v[84:87]\n"
RD"a[64:67],v227 offset:8192\n"
MF"v[164:167],a[100:103],v[184:187],0\n"
MF"v[164:167],a[96:99],v[176:179],v[164:167]\n"
RD"a[68:71],v227 offset:9216\n"
MF"v[92:95],a[104:107],v[144:147],0\n"
BL"v225,s[8:11],s43 offen offset:3072 sc1 lds\n"
MF"v[92:95],a[108:111],v[152:155],v[92:95]\n"
RD"a[72:75],v227 offset:10240\n"
MF"v[172:175],a[108:111],v[184:187],0\n"
SA"m0,s40,16384\n"
MF"v[172:175],a[104:107],v[176:179],v[172:175]\n"
WT"lgkmcnt(3)\n"
RD"a[76:79],v227 offset:11264\n"
MF"v[100:103],a[112:115],v[144:147],0\n"
MF"v[100:103],a[116:119],v[152:155],v[100:103]\n"
SA"s48,s59,32768\n"
RD"a[80:83],v227 offset:12288\n"
MF"v[180:183],a[116:119],v[184:187],0\n"
"s_and_b32 s48,s48,0x18000\n"
BL"v225,s[8:11],s44 offen offset:0 sc1 lds\n"
MF"v[180:183],a[112:115],v[176:179],v[180:183]\n"
SA"s40,s40,32768\n"
RD"a[84:87],v227 offset:13312\n"
"s_andn2_b32 s49,s59,0x18000\n"
MF"v[108:111],a[120:123],v[144:147],0\n"
"s_cmp_eq_u32 s40,s46\n"
"s_cselect_b32 s40,s47,s40\n"
WT"vmcnt(7)\n"
"v_cmp_eq_u64 vcc,s[28:29],v[240:241]\n"
"s_cmp_eq_u64 vcc,exec\n"
"s_cbranch_scc0 L1080_%=\n"
"L1081_%=:\n"
MF"v[108:111],a[124:127],v[152:155],v[108:111]\n"
"s_or_b32 s52,s48,s49\n"
RD"a[88:91],v227 offset:14336\n"
SA"s53,s52,16384\n"
MF"v[188:191],a[124:127],v[184:187],0\n"
BL"a[0:3],v224,s[8:11],s52 offen offset:0 sc1\n"
MF"v[188:191],a[120:123],v[176:179],v[188:191]\n"
WT"lgkmcnt(3)\n"
RD"a[92:95],v227 offset:15360\n"
MF"v[116:119],a[64:67],v[144:147],0\n"
BL"v225,s[8:11],s44 offen offset:1024 sc1 lds\n"
MF"v[116:119],a[68:71],v[152:155],v[116:119]\n"
RD"a[96:99],v227 offset:16384\n"
MF"v[196:199],a[68:71],v[184:187],0\n"
BL"a[4:7],v224,s[8:11],s53 offen offset:0 sc1\n"
MF"v[196:199],a[64:67],v[176:179],v[196:199]\n"
RD"a[100:103],v227 offset:17408\n"
MF"v[124:127],a[72:75],v[144:147],0\n"
MF"v[124:127],a[76:79],v[152:155],v[124:127]\n"
RD"a[104:107],v227 offset:18432\n"
MF"v[204:207],a[76:79],v[184:187],0\n"
BL"v225,s[8:11],s44 offen offset:2048 sc1 lds\n"
MF"v[204:207],a[72:75],v[176:179],v[204:207]\n"
WT"lgkmcnt(3)\n"
RD"a[108:111],v227 offset:19456\n"
MF"v[132:135],a[80:83],v[144:147],0\n"
BL"a[8:11],v224,s[8:11],s52 offen offset:1024 sc1\n"
MF"v[132:135],a[84:87],v[152:155],v[132:135]\n"
RD"a[112:115],v227 offset:20480\n"
MF"v[208:211],a[84:87],v[184:187],0\n"
"v_readlane_b32 s43,v228,s42\n"
MF"v[208:211],a[80:83],v[176:179],v[208:211]\n"
RD"a[116:119],v227 offset:21504\n"
MF"v[140:143],a[88:91],v[144:147],0\n"
BL"a[12:15],v224,s[8:11],s53 offen offset:1024 sc1\n"
MF"v[140:143],a[92:95],v[152:155],v[140:143]\n"
BL"v225,s[8:11],s44 offen offset:3072 sc1 lds\n"
MF"v[212:215],a[92:95],v[184:187],0\n"
"s_mov_b32 m0,s40\n"
RD"a[120:123],v227 offset:22528\n"
MF"v[212:215],a[88:91],v[176:179],v[212:215]\n"
WT"lgkmcnt(3)\n"
RD"a[124:127],v227 offset:23552\n"
MF"v[148:151],a[96:99],v[144:147],0\n"
WT"vmcnt(12)\n"
"buffer_store_dwordx2 v[232:233],v230,s[8:11],s62 offen sc1\n"
MF"v[148:151],a[100:103],v[152:155],v[148:151]\n"
BL"a[16:19],v224,s[8:11],s52 offen offset:2048 sc1\n"
MF"v[216:219],a[100:103],v[184:187],0\n"
RD"a[64:67],v227 offset:24576\n"
"v_readlane_b32 s44,v229,s42\n"
MF"v[216:219],a[96:99],v[176:179],v[216:219]\n"
SA"s42,s42,1\n"
BL"v225,s[8:11],s43 offen offset:0 sc1 lds\n"
MF"v[156:159],a[104:107],v[144:147],0\n"
RD"a[68:71],v227 offset:25600\n"
MF"v[156:159],a[108:111],v[152:155],v[156:159]\n"
BL"a[20:23],v224,s[8:11],s53 offen offset:2048 sc1\n"
MF"v[220:223],a[108:111],v[184:187],0\n"
RD"a[72:75],v227 offset:26624\n"
MF"v[220:223],a[104:107],v[176:179],v[220:223]\n"
WT"lgkmcnt(3)\n"
RD"a[76:79],v227 offset:27648\n"
MF"v[84:87],a[112:115],v[160:163],v[84:87]\n"
"s_lshr_b32 s89,s31,3\n"
BL"v225,s[8:11],s43 offen offset:1024 sc1 lds\n"
MF"v[84:87],a[116:119],v[168:171],v[84:87]\n"
"s_lshl_b32 s89,s89,4\n"
RD"a[80:83],v227 offset:28672\n"
MF"v[164:167],a[116:119],v[200:203],v[164:167]\n"
SA"s89,s89,s30\n"
BL"a[24:27],v224,s[8:11],s52 offen offset:3072 sc1\n"
MF"v[164:167],a[112:115],v[192:195],v[164:167]\n"
"s_lshl_b32 s89,s89,3\n"
RD"a[84:87],v227 offset:29696\n"
MF"v[92:95],a[120:123],v[160:163],v[92:95]\n"
"s_xor_b32 s23,s32,s30\n"
MF"v[92:95],a[124:127],v[168:171],v[92:95]\n"
SA"s48,s59,65536\n"
RD"a[88:91],v227 offset:30720\n"
SA"s89,s89,s23\n"
MF"v[172:175],a[124:127],v[200:203],v[172:175]\n"
"s_and_b32 s48,s48,0x18000\n"
BL"a[28:31],v224,s[8:11],s53 offen offset:3072 sc1\n"
MF"v[172:175],a[120:123],v[192:195],v[172:175]\n"
WT"lgkmcnt(3)\n"
BL"v225,s[8:11],s43 offen offset:2048 sc1 lds\n"
MF"v[100:103],a[64:67],v[160:163],v[100:103]\n"
"s_xor_b32 s90,s89,36\n"
RD"a[92:95],v227 offset:31744\n"
"s_andn2_b32 s49,s59,0x18000\n"
MF"v[100:103],a[68:71],v[168:171],v[100:103]\n"
"s_mul_i32 s88,s89,5120\n"
RD"a[96:99],v226 offset:0\n"
"s_or_b32 s52,s48,s49\n"
MF"v[180:183],a[68:71],v[200:203],v[180:183]\n"
SA"s53,s52,16384\n"
BL"a[180:183],v224,s[8:11],s52 offen offset:0 sc1\n"
MF"v[180:183],a[64:67],v[192:195],v[180:183]\n"
"s_mul_i32 s22,s90,5120\n"
RD"a[100:103],v226 offset:1024\n"
"s_sub_u32 s90,s90,s89\n"
MF"v[108:111],a[72:75],v[160:163],v[108:111]\n"
"s_lshl_b32 s90,s90,6\n"
BL"v225,s[8:11],s43 offen offset:3072 sc1 lds\n"
MF"v[108:111],a[76:79],v[168:171],v[108:111]\n"
SA"s88,s88,59506688\n"
RD"a[104:107],v226 offset:2048\n"
SA"s22,s22,59506688\n"
MF"v[188:191],a[76:79],v[200:203],v[188:191]\n"
"s_cmp_eq_u32 s23,0\n"
BL"a[184:187],v224,s[8:11],s53 offen offset:0 sc1\n"
MF"v[188:191],a[72:75],v[192:195],v[188:191]\n"
WT"lgkmcnt(3)\n"
RD"a[108:111],v226 offset:3072\n"
"s_cselect_b32 s88,2130706432,s88\n"
MF"v[116:119],a[80:83],v[160:163],v[116:119]\n"
SA"m0,s40,16384\n"
"s_cmp_eq_u32 s23,4\n"
"s_cselect_b32 s22,2130706432,s22\n"
MF"v[116:119],a[84:87],v[168:171],v[116:119]\n"
"s_lshl_b32 s89,s89,6\n"
RD"a[112:115],v226 offset:4096\n"
"v_bfe_u32 v239,v224,1,6\n"
MF"v[196:199],a[84:87],v[200:203],v[196:199]\n"
SA"s89,s89,80478208\n"
BL"v225,s[8:11],s44 offen offset:0 sc1 lds\n"
MF"v[196:199],a[80:83],v[192:195],v[196:199]\n"
SA"s40,s40,32768\n"
RD"a[116:119],v226 offset:5120\n"
"v_bfe_u32 v242,v224,7,1\n"
MF"v[124:127],a[88:91],v[160:163],v[124:127]\n"
"s_cmp_eq_u32 s40,s46\n"
BL"a[188:191],v224,s[8:11],s52 offen offset:1024 sc1\n"
MF"v[124:127],a[92:95],v[168:171],v[124:127]\n"
"s_cselect_b32 s40,s47,s40\n"
RD"a[120:123],v226 offset:6144\n"
"v_mad_u32_u24 v239,v242,s90,v239\n"
MF"v[204:207],a[92:95],v[200:203],v[204:207]\n"
"v_lshrrev_b32 v242,8,v224\n"
"v_lshl_add_u32 v239,v242,27,v239\n"
MF"v[204:207],a[88:91],v[192:195],v[204:207]\n"
WT"lgkmcnt(3)\n"
RD"a[124:127],v226 offset:7168\n"
MF"v[132:135],a[96:99],v[160:163],v[132:135]\n"
BL"a[192:195],v224,s[8:11],s53 offen offset:1024 sc1\n"
MF"v[132:135],a[100:103],v[168:171],v[132:135]\n"
BL"v225,s[8:11],s44 offen offset:1024 sc1 lds\n"
MF"v[208:211],a[100:103],v[200:203],v[208:211]\n"
RD"a[64:67],v226 offset:8192\n"
MF"v[208:211],a[96:99],v[192:195],v[208:211]\n"
RD"a[68:71],v226 offset:9216\n"
MF"v[140:143],a[104:107],v[160:163],v[140:143]\n"
BL"a[196:199],v224,s[8:11],s52 offen offset:2048 sc1\n"
MF"v[140:143],a[108:111],v[168:171],v[140:143]\n"
RD"a[72:75],v226 offset:10240\n"
MF"v[212:215],a[108:111],v[200:203],v[212:215]\n"
BL"v225,s[8:11],s44 offen offset:2048 sc1 lds\n"
MF"v[212:215],a[104:107],v[192:195],v[212:215]\n"
WT"lgkmcnt(3)\n"
RD"a[76:79],v226 offset:11264\n"
MF"v[148:151],a[112:115],v[160:163],v[148:151]\n"
BL"a[200:203],v224,s[8:11],s53 offen offset:2048 sc1\n"
MF"v[148:151],a[116:119],v[168:171],v[148:151]\n"
RD"a[80:83],v226 offset:12288\n"
MF"v[216:219],a[116:119],v[200:203],v[216:219]\n"
MF"v[216:219],a[112:115],v[192:195],v[216:219]\n"
RD"a[84:87],v226 offset:13312\n"
MF"v[156:159],a[120:123],v[160:163],v[156:159]\n"
BL"v225,s[8:11],s44 offen offset:3072 sc1 lds\n"
MF"v[156:159],a[124:127],v[168:171],v[156:159]\n"
RD"a[88:91],v226 offset:14336\n"
MF"v[220:223],a[124:127],v[200:203],v[220:223]\n"
BL"a[204:207],v224,s[8:11],s52 offen offset:3072 sc1\n"
MF"v[220:223],a[120:123],v[192:195],v[220:223]\n"
WT"vmcnt(26) lgkmcnt(3)\n"
RD"a[92:95],v226 offset:15360\n"
MF"v[0:3],a[64:67],a[0:3],v[0:3]\n"
WT"vmcnt(22)\n"
MF"v[0:3],a[68:71],a[8:11],v[0:3]\n"
WT"vmcnt(21)\n"
RD"a[96:99],v226 offset:16384\n"
SA"s48,s59,98304\n"
MF"v[40:43],a[68:71],a[12:15],v[40:43]\n"
"s_and_b32 s48,s48,0x18000\n"
BL"a[208:211],v224,s[8:11],s53 offen offset:3072 sc1\n"
MF"v[40:43],a[64:67],a[4:7],v[40:43]\n"
"s_andn2_b32 s49,s59,0x18000\n"
RD"a[100:103],v226 offset:17408\n"
MF"v[4:7],a[72:75],a[0:3],v[4:7]\n"
"s_or_b32 s52,s48,s49\n"
SA"s53,s52,16384\n"
MF"v[4:7],a[76:79],a[8:11],v[4:7]\n"
RD"a[104:107],v226 offset:18432\n"
MF"v[44:47],a[76:79],a[12:15],v[44:47]\n"
BL"a[212:215],v224,s[8:11],s52 offen offset:0 sc1\n"
MF"v[44:47],a[72:75],a[4:7],v[44:47]\n"
WT"lgkmcnt(3)\n"
RD"a[108:111],v226 offset:19456\n"
MF"v[8:11],a[80:83],a[0:3],v[8:11]\n"
MF"v[8:11],a[84:87],a[8:11],v[8:11]\n"
RD"a[112:115],v226 offset:20480\n"
MF"v[48:51],a[84:87],a[12:15],v[48:51]\n"
BL"a[216:219],v224,s[8:11],s53 offen offset:0 sc1\n"
MF"v[48:51],a[80:83],a[4:7],v[48:51]\n"
RD"a[116:119],v226 offset:21504\n"
MF"v[12:15],a[88:91],a[0:3],v[12:15]\n"
MF"v[12:15],a[92:95],a[8:11],v[12:15]\n"
RD"a[120:123],v226 offset:22528\n"
MF"v[52:55],a[92:95],a[12:15],v[52:55]\n"
MF"v[52:55],a[88:91],a[4:7],v[52:55]\n"
WT"lgkmcnt(3)\n"
RD"a[124:127],v226 offset:23552\n"
MF"v[16:19],a[96:99],a[0:3],v[16:19]\n"
BL"a[220:223],v224,s[8:11],s52 offen offset:1024 sc1\n"
MF"v[16:19],a[100:103],a[8:11],v[16:19]\n"
RD"a[64:67],v226 offset:24576\n"
MF"v[56:59],a[100:103],a[12:15],v[56:59]\n"
MF"v[56:59],a[96:99],a[4:7],v[56:59]\n"
RD"a[68:71],v226 offset:25600\n"
MF"v[20:23],a[104:107],a[0:3],v[20:23]\n"
BL"a[224:227],v224,s[8:11],s53 offen offset:1024 sc1\n"
MF"v[20:23],a[108:111],a[8:11],v[20:23]\n"
RD"a[72:75],v226 offset:26624\n"
MF"v[60:63],a[108:111],a[12:15],v[60:63]\n"
MF"v[60:63],a[104:107],a[4:7],v[60:63]\n"
WT"lgkmcnt(3)\n"
RD"a[76:79],v226 offset:27648\n"
MF"v[24:27],a[112:115],a[0:3],v[24:27]\n"
BL"a[228:231],v224,s[8:11],s52 offen offset:2048 sc1\n"
MF"v[24:27],a[116:119],a[8:11],v[24:27]\n"
RD"a[80:83],v226 offset:28672\n"
MF"v[64:67],a[116:119],a[12:15],v[64:67]\n"
MF"v[64:67],a[112:115],a[4:7],v[64:67]\n"
RD"a[84:87],v226 offset:29696\n"
MF"v[28:31],a[120:123],a[0:3],v[28:31]\n"
BL"a[232:235],v224,s[8:11],s53 offen offset:2048 sc1\n"
MF"v[28:31],a[124:127],a[8:11],v[28:31]\n"
RD"a[88:91],v226 offset:30720\n"
MF"v[68:71],a[124:127],a[12:15],v[68:71]\n"
MF"v[68:71],a[120:123],a[4:7],v[68:71]\n"
WT"lgkmcnt(3)\n"
RD"a[92:95],v226 offset:31744\n"
MF"v[32:35],a[64:67],a[0:3],v[32:35]\n"
"s_barrier\n"
MF"v[32:35],a[68:71],a[8:11],v[32:35]\n"
RD"a[96:99],v235 offset:0\n"
MF"v[72:75],a[68:71],a[12:15],v[72:75]\n"
BL"a[236:239],v224,s[8:11],s52 offen offset:3072 sc1\n"
MF"v[72:75],a[64:67],a[4:7],v[72:75]\n"
RD"a[100:103],v235 offset:1024\n"
MF"v[36:39],a[72:75],a[0:3],v[36:39]\n"
MF"v[36:39],a[76:79],a[8:11],v[36:39]\n"
RD"a[104:107],v235 offset:2048\n"
MF"v[76:79],a[76:79],a[12:15],v[76:79]\n"
BL"a[240:243],v224,s[8:11],s53 offen offset:3072 sc1\n"
MF"v[76:79],a[72:75],a[4:7],v[76:79]\n"
WT"vmcnt(27) lgkmcnt(3)\n"
RD"a[108:111],v235 offset:3072\n"
MF"v[0:3],a[80:83],a[16:19],v[0:3]\n"
WT"vmcnt(23)\n"
MF"v[0:3],a[84:87],a[24:27],v[0:3]\n"
WT"vmcnt(22)\n"
RD"a[112:115],v235 offset:4096\n"
MF"v[40:43],a[84:87],a[28:31],v[40:43]\n"
MF"v[40:43],a[80:83],a[20:23],v[40:43]\n"
RD"a[116:119],v235 offset:5120\n"
MF"v[4:7],a[88:91],a[16:19],v[4:7]\n"
MF"v[4:7],a[92:95],a[24:27],v[4:7]\n"
RD"a[120:123],v235 offset:6144\n"
MF"v[44:47],a[92:95],a[28:31],v[44:47]\n"
MF"v[44:47],a[88:91],a[20:23],v[44:47]\n"
WT"lgkmcnt(3)\n"
RD"a[124:127],v235 offset:7168\n"
MF"v[8:11],a[96:99],a[16:19],v[8:11]\n"
MF"v[8:11],a[100:103],a[24:27],v[8:11]\n"
RD"a[64:67],v235 offset:8192\n"
MF"v[48:51],a[100:103],a[28:31],v[48:51]\n"
MF"v[48:51],a[96:99],a[20:23],v[48:51]\n"
RD"a[68:71],v235 offset:9216\n"
MF"v[12:15],a[104:107],a[16:19],v[12:15]\n"
MF"v[12:15],a[108:111],a[24:27],v[12:15]\n"
RD"a[72:75],v235 offset:10240\n"
"v_bfe_u32 v238,v224,4,2\n"
MF"v[52:55],a[108:111],a[28:31],v[52:55]\n"
"s_andn2_b32 s63,s62,255\n"
"v_lshlrev_b32 v238,6,v238\n"
MF"v[52:55],a[104:107],a[20:23],v[52:55]\n"
WT"lgkmcnt(3)\n"
RD"a[76:79],v235 offset:11264\n"
MF"v[16:19],a[112:115],a[16:19],v[16:19]\n"
"buffer_load_dwordx2 v[240:241],v238,s[8:11],s63 offen sc1\n"
MF"v[16:19],a[116:119],a[24:27],v[16:19]\n"
RD"a[80:83],v235 offset:12288\n"
MF"v[56:59],a[116:119],a[28:31],v[56:59]\n"
MF"v[56:59],a[112:115],a[20:23],v[56:59]\n"
RD"a[84:87],v235 offset:13312\n"
MF"v[20:23],a[120:123],a[16:19],v[20:23]\n"
MF"v[20:23],a[124:127],a[24:27],v[20:23]\n"
RD"a[88:91],v235 offset:14336\n"
MF"v[60:63],a[124:127],a[28:31],v[60:63]\n"
MF"v[60:63],a[120:123],a[20:23],v[60:63]\n"
RD"a[92:95],v235 offset:15360\n"
"s_branch LTJ_%=\n"
"L1099_%=:\n"
"s_mov_b32 s54,0\n"
"L1101_%=:\n"
"s_sleep 2\n"
BL"v[240:243],v238,s[8:11],0 offen sc1\n"
WT"vmcnt(0)\n"
SA"s54,s54,1\n"
"v_cmp_eq_u64 vcc,s[28:29],v[240:241]\n"
"v_cmp_eq_u64_e64 s[50:51],s[28:29],v[242:243]\n"
"s_and_b64 vcc,vcc,s[50:51]\n"
"s_cmp_eq_u64 vcc,exec\n"
"s_cbranch_scc1 L1100_%=\n"
"s_cmp_lt_u32 s54,1048576\n"
"s_cbranch_scc1 L1101_%=\n"
"s_branch L1100_%=\n"
"L1102_%=:\n"
"s_mov_b32 s54,0\n"
"L1104_%=:\n"
"s_sleep 2\n"
"buffer_load_dwordx2 v[240:241],v238,s[8:11],s50 offen sc1\n"
WT"vmcnt(0)\n"
SA"s54,s54,1\n"
"v_cmp_eq_u64 vcc,s[28:29],v[240:241]\n"
"s_cmp_eq_u64 vcc,exec\n"
"s_cbranch_scc1 L1103_%=\n"
"s_cmp_lt_u32 s54,1048576\n"
"s_cbranch_scc1 L1104_%=\n"
"s_branch L1103_%=\n"
"L1105_%=:\n"
"s_mov_b32 s54,0\n"
"L1107_%=:\n"
"s_sleep 2\n"
"buffer_load_dwordx2 v[244:245],v242,s[8:11],s23 offen sc1\n"
WT"vmcnt(0)\n"
SA"s54,s54,1\n"
"v_cmp_eq_u64 vcc,s[28:29],v[244:245]\n"
"s_cmp_eq_u64 vcc,exec\n"
"s_cbranch_scc1 L1106_%=\n"
"s_cmp_lt_u32 s54,1048576\n"
"s_cbranch_scc1 L1107_%=\n"
"s_branch L1106_%=\n"
"LFA_%=:\n"
WT"lgkmcnt(6)\n"
MF"v[0:3],a[64:67],a[0:3],v[208:211]\n"
WT"lgkmcnt(3)\n"
PK"v192,v192,v193\n"
PK"v193,v194,v195\n"
MF"v[0:3],a[68:71],a[4:7],v[0:3]\n"
"s_mov_b32 m0,s40\n"
"v_readlane_b32 s43,v228,s42\n"
PK"v194,v196,v197\n"
MF"v[8:11],a[68:71],a[20:23],0\n"
PK"v195,v198,v199\n"
"v_readlane_b32 s44,v229,s42\n"
MF"v[8:11],a[64:67],a[16:19],v[8:11]\n"
SA"s42,s42,1\n"
BL"v225,s[8:11],s43 offen offset:0 sc1 lds\n"
MF"v[16:19],a[64:67],a[32:35],0\n"
"v_accvgpr_write_b32 a44,v192\n"
"v_accvgpr_write_b32 a45,v193\n"
MF"v[16:19],a[68:71],a[36:39],v[16:19]\n"
RD"a[92:95],v226 offset:7168\n"
"v_accvgpr_write_b32 a46,v194\n"
MF"v[24:27],a[68:71],a[52:55],0\n"
"v_accvgpr_write_b32 a47,v195\n"
PK"v200,v200,v201\n"
MF"v[24:27],a[64:67],a[48:51],v[24:27]\n"
PK"v201,v202,v203\n"
PK"v202,v204,v205\n"
"v_mov_b32 v227,v226\n"
MF"v[0:3],a[72:75],a[8:11],v[0:3]\n"
BL"v225,s[8:11],s43 offen offset:1024 sc1 lds\n"
PK"v203,v206,v207\n"
MF"v[0:3],a[76:79],a[12:15],v[0:3]\n"
"v_accvgpr_write_b32 a60,v200\n"
"v_accvgpr_write_b32 a61,v201\n"
MF"v[8:11],a[76:79],a[28:31],v[8:11]\n"
"s_mul_i32 s48,s32,655360\n"
"v_accvgpr_write_b32 a62,v202\n"
"v_accvgpr_write_b32 a63,v203\n"
MF"v[8:11],a[72:75],a[24:27],v[8:11]\n"
SA"s12,s74,s48\n"
"s_addc_u32 s13,s75,0\n"
MF"v[16:19],a[72:75],a[40:43],v[16:19]\n"
"s_and_b32 s13,s13,0xffff\n"
MF"v[16:19],a[76:79],a[44:47],v[16:19]\n"
"s_mov_b32 s14,655360\n"
RD"a[96:99],v226 offset:8192\n"
MF"v[24:27],a[76:79],a[60:63],v[24:27]\n"
"s_lshl_b32 s48,s33,3\n"
BL"v225,s[8:11],s43 offen offset:2048 sc1 lds\n"
MF"v[24:27],a[72:75],a[56:59],v[24:27]\n"
WT"lgkmcnt(1)\n"
EX"v0,v0\n"
MF"v[4:7],a[80:83],a[0:3],v[212:215]\n"
"s_and_b32 s49,s34,2\n"
EX"v1,v1\n"
MF"v[4:7],a[84:87],a[4:7],v[4:7]\n"
"s_lshl_b32 s49,s49,1\n"
EX"v2,v2\n"
MF"v[12:15],a[84:87],a[20:23],0\n"
SA"s48,s48,s49\n"
RD"a[100:103],v226 offset:9216\n"
PK"v0,v0,v1\n"
MF"v[12:15],a[80:83],a[16:19],v[12:15]\n"
SA"s48,s48,s30\n"
EX"v3,v3\n"
MF"v[20:23],a[80:83],a[32:35],0\n"
"s_mul_i32 s48,s48,5120\n"
EX"v8,v8\n"
MF"v[20:23],a[84:87],a[36:39],v[20:23]\n"
"s_or_b32 s49,s34,2\n"
BL"v225,s[8:11],s43 offen offset:3072 sc1 lds\n"
MF"v[28:31],a[84:87],a[52:55],0\n"
"s_cmp_eq_u32 s49,7\n"
EX"v9,v9\n"
PK"v1,v2,v3\n"
MF"v[28:31],a[80:83],a[48:51],v[28:31]\n"
"s_cselect_b32 s55,s48,2130706432\n"
RD"a[104:107],v226 offset:10240\n"
PK"v8,v8,v9\n"
MF"v[4:7],a[88:91],a[8:11],v[4:7]\n"
SA"m0,s40,16384\n"
EX"v10,v10\n"
MF"v[4:7],a[92:95],a[12:15],v[4:7]\n"
EX"v11,v11\n"
MF"v[12:15],a[92:95],a[28:31],v[12:15]\n"
EX"v16,v16\n"
MF"v[12:15],a[88:91],a[24:27],v[12:15]\n"
SA"s40,s40,32768\n"
RD"a[108:111],v226 offset:11264\n"
PK"v9,v10,v11\n"
MF"v[20:23],a[88:91],a[40:43],v[20:23]\n"
"s_cmp_eq_u32 s40,s46\n"
BL"v225,s[8:11],s44 offen offset:0 sc1 lds\n"
EX"v17,v17\n"
MF"v[20:23],a[92:95],a[44:47],v[20:23]\n"
"s_cselect_b32 s40,s47,s40\n"
EX"v18,v18\n"
MF"v[28:31],a[92:95],a[60:63],v[28:31]\n"
EX"v19,v19\n"
PK"v16,v16,v17\n"
MF"v[28:31],a[88:91],a[56:59],v[28:31]\n"
WT"lgkmcnt(1)\n"
RD"a[112:115],v226 offset:12288\n"
PK"v17,v18,v19\n"
MF"v[32:35],a[96:99],a[0:3],v[216:219]\n"
EX"v24,v24\n"
MF"v[32:35],a[100:103],a[4:7],v[32:35]\n"
EX"v25,v25\n"
MF"v[40:43],a[100:103],a[20:23],0\n"
BL"v225,s[8:11],s44 offen offset:1024 sc1 lds\n"
EX"v26,v26\n"
MF"v[40:43],a[96:99],a[16:19],v[40:43]\n"
RD"a[116:119],v226 offset:13312\n"
PK"v24,v24,v25\n"
MF"v[48:51],a[96:99],a[32:35],0\n"
EX"v27,v27\n"
MF"v[48:51],a[100:103],a[36:39],v[48:51]\n"
EX"v4,v4\n"
MF"v[56:59],a[100:103],a[52:55],0\n"
EX"v5,v5\n"
PK"v25,v26,v27\n"
MF"v[56:59],a[96:99],a[48:51],v[56:59]\n"
RD"a[120:123],v226 offset:14336\n"
PK"v2,v4,v5\n"
MF"v[32:35],a[104:107],a[8:11],v[32:35]\n"
WT"lgkmcnt(2)\n"
BL"v225,s[8:11],s44 offen offset:2048 sc1 lds\n"
EX"v6,v6\n"
MF"v[32:35],a[108:111],a[12:15],v[32:35]\n"
EX"v7,v7\n"
MF"v[40:43],a[108:111],a[28:31],v[40:43]\n"
EX"v12,v12\n"
MF"v[40:43],a[104:107],a[24:27],v[40:43]\n"
RD"a[124:127],v226 offset:15360\n"
"v_readlane_b32 s43,v228,s42\n"
PK"v3,v6,v7\n"
MF"v[48:51],a[104:107],a[40:43],v[48:51]\n"
EX"v13,v13\n"
MF"v[48:51],a[108:111],a[44:47],v[48:51]\n"
EX"v14,v14\n"
MF"v[56:59],a[108:111],a[60:63],v[56:59]\n"
BL"v225,s[8:11],s44 offen offset:3072 sc1 lds\n"
EX"v15,v15\n"
MF"v[56:59],a[104:107],a[56:59],v[56:59]\n"
"s_mov_b32 m0,s40\n"
RD"a[96:99],v227 offset:24576\n"
PK"v10,v12,v13\n"
MF"v[36:39],a[112:115],a[0:3],v[220:223]\n"
WT"lgkmcnt(2)\n"
EX"v20,v20\n"
PK"v11,v14,v15\n"
MF"v[36:39],a[116:119],a[4:7],v[36:39]\n"
EX"v21,v21\n"
MF"v[44:47],a[116:119],a[20:23],0\n"
EX"v22,v22\n"
MF"v[44:47],a[112:115],a[16:19],v[44:47]\n"
RD"a[100:103],v227 offset:25600\n"
"v_readlane_b32 s44,v229,s42\n"
PK"v18,v20,v21\n"
MF"v[52:55],a[112:115],a[32:35],0\n"
SA"s42,s42,1\n"
BL"v225,s[8:11],s43 offen offset:0 sc1 lds\n"
MF"v[52:55],a[116:119],a[36:39],v[52:55]\n"
EX"v23,v23\n"
MF"v[60:63],a[116:119],a[52:55],0\n"
EX"v28,v28\n"
MF"v[60:63],a[112:115],a[48:51],v[60:63]\n"
RD"a[104:107],v227 offset:26624\n"
PK"v19,v22,v23\n"
MF"v[36:39],a[120:123],a[8:11],v[36:39]\n"
WT"lgkmcnt(2)\n"
EX"v29,v29\n"
MF"v[36:39],a[124:127],a[12:15],v[36:39]\n"
BL"v225,s[8:11],s43 offen offset:1024 sc1 lds\n"
MF"v[44:47],a[124:127],a[28:31],v[44:47]\n"
EX"v30,v30\n"
PK"v26,v28,v29\n"
MF"v[44:47],a[120:123],a[24:27],v[44:47]\n"
RD"a[108:111],v227 offset:27648\n"
MF"v[52:55],a[120:123],a[40:43],v[52:55]\n"
EX"v31,v31\n"
MF"v[52:55],a[124:127],a[44:47],v[52:55]\n"
EX"v32,v32\n"
MF"v[60:63],a[124:127],a[60:63],v[60:63]\n"
EX"v33,v33\n"
PK"v27,v30,v31\n"
MF"v[60:63],a[120:123],a[56:59],v[60:63]\n"
SA"s41,s41,32768\n"
BL"v225,s[8:11],s43 offen offset:2048 sc1 lds\n"
RD"a[112:115],v227 offset:28672\n"
MF"v[80:83],a[96:99],v[0:3],0\n"
"s_cmp_eq_u32 s41,163840\n"
EX"v34,v34\n"
MF"v[112:115],a[96:99],v[8:11],0\n"
"s_cselect_b32 s41,0,s41\n"
EX"v35,v35\n"
MF"v[144:147],a[96:99],v[16:19],0\n"
EX"v40,v40\n"
"v_add_u32 v226,s41,v224\n"
MF"v[176:179],a[96:99],v[24:27],0\n"
WT"lgkmcnt(1)\n"
RD"a[116:119],v227 offset:29696\n"
MF"v[84:87],a[100:103],v[0:3],0\n"
EX"v41,v41\n"
MF"v[116:119],a[100:103],v[8:11],0\n"
BL"v225,s[8:11],s43 offen offset:3072 sc1 lds\n"
MF"v[148:151],a[100:103],v[16:19],0\n"
SA"m0,s40,16384\n"
EX"v42,v42\n"
MF"v[180:183],a[100:103],v[24:27],0\n"
RD"a[120:123],v227 offset:30720\n"
MF"v[88:91],a[104:107],v[0:3],0\n"
EX"v43,v43\n"
MF"v[120:123],a[104:107],v[8:11],0\n"
EX"v48,v48\n"
MF"v[152:155],a[104:107],v[16:19],0\n"
SA"s40,s40,32768\n"
EX"v49,v49\n"
MF"v[184:187],a[104:107],v[24:27],0\n"
"s_cmp_eq_u32 s40,s46\n"
BL"v225,s[8:11],s44 offen offset:0 sc1 lds\n"
RD"a[124:127],v227 offset:31744\n"
MF"v[92:95],a[108:111],v[0:3],0\n"
"s_cselect_b32 s40,s47,s40\n"
EX"v50,v50\n"
MF"v[124:127],a[108:111],v[8:11],0\n"
EX"v51,v51\n"
MF"v[156:159],a[108:111],v[16:19],0\n"
EX"v56,v56\n"
MF"v[188:191],a[108:111],v[24:27],0\n"
WT"lgkmcnt(1)\n"
EX"v57,v57\n"
MF"v[96:99],a[112:115],v[0:3],0\n"
EX"v58,v58\n"
MF"v[128:131],a[112:115],v[8:11],0\n"
BL"v225,s[8:11],s44 offen offset:1024 sc1 lds\n"
MF"v[160:163],a[112:115],v[16:19],0\n"
EX"v59,v59\n"
MF"v[192:195],a[112:115],v[24:27],0\n"
EX"v36,v36\n"
MF"v[100:103],a[116:119],v[0:3],0\n"
WT"vmcnt(16) lgkmcnt(0)\n"
"s_barrier\n"
MF"v[132:135],a[116:119],v[8:11],0\n"
RD"a[64:67],v226 offset:0\n"
RD"a[68:71],v226 offset:1024\n"
"buffer_store_dwordx2 v[232:233],v230,s[8:11],s98 offen sc1\n"
MF"v[164:167],a[116:119],v[16:19],0\n"
EX"v37,v37\n"
MF"v[196:199],a[116:119],v[24:27],0\n"
BL"v225,s[8:11],s44 offen offset:2048 sc1 lds\n"
MF"v[104:107],a[120:123],v[0:3],0\n"
EX"v38,v38\n"
MF"v[136:139],a[120:123],v[8:11],0\n"
EX"v39,v39\n"
MF"v[168:171],a[120:123],v[16:19],0\n"
EX"v44,v44\n"
MF"v[200:203],a[120:123],v[24:27],0\n"
RD"a[72:75],v226 offset:2048\n"
RD"a[76:79],v226 offset:3072\n"
MF"v[108:111],a[124:127],v[0:3],0\n"
EX"v45,v45\n"
MF"v[140:143],a[124:127],v[8:11],0\n"
BL"v225,s[8:11],s44 offen offset:3072 sc1 lds\n"
MF"v[172:175],a[124:127],v[16:19],0\n"
EX"v46,v46\n"
MF"v[204:207],a[124:127],v[24:27],0\n"
RD"a[80:83],v226 offset:4096\n"
MF"a[132:135],a[128:131],v[0:3],0\n"
EX"v47,v47\n"
MF"a[136:139],a[128:131],v[8:11],0\n"
"buffer_load_dword v255,v253,s[12:15],s55 offen\n"
EX"v52,v52\n"
MF"a[140:143],a[128:131],v[16:19],0\n"
SA"s55,s55,163840\n"
EX"v53,v53\n"
MF"a[144:147],a[128:131],v[24:27],0\n"
RD"a[84:87],v226 offset:5120\n"
RD"a[88:91],v226 offset:6144\n"
EX"v54,v54\n"
EX"v55,v55\n"
EX"v60,v60\n"
EX"v61,v61\n"
EX"v62,v62\n"
EX"v63,v63\n"
"s_branch LDB_%=\n"
"LD2_%=:\n"
WT"lgkmcnt(6)\n"
MF"v[0:3],a[64:67],a[32:35],v[208:211]\n"
WT"lgkmcnt(3)\n"
EX"v56,v56\n"
MF"v[0:3],a[68:71],a[36:39],v[0:3]\n"
"s_mov_b32 m0,s40\n"
EX"v57,v57\n"
MF"v[8:11],a[68:71],a[52:55],0\n"
RD"a[92:95],v226 offset:7168\n"
"v_readlane_b32 s43,v228,s42\n"
MF"v[8:11],a[64:67],a[48:51],v[8:11]\n"
EX"v58,v58\n"
MF"v[0:3],a[72:75],a[40:43],v[0:3]\n"
SA"s41,s41,32768\n"
RD"a[96:99],v226 offset:8192\n"
RD"a[100:103],v226 offset:9216\n"
"v_readlane_b32 s44,v229,s42\n"
MF"v[0:3],a[76:79],a[44:47],v[0:3]\n"
SA"s42,s42,1\n"
BL"v225,s[8:11],s43 offen offset:0 sc1 lds\n"
PK"v76,v56,v57\n"
MF"v[8:11],a[76:79],a[60:63],v[8:11]\n"
"s_cmp_eq_u32 s41,163840\n"
EX"v59,v59\n"
MF"v[8:11],a[72:75],a[56:59],v[8:11]\n"
WT"lgkmcnt(3)\n"
RD"a[104:107],v226 offset:10240\n"
PK"v77,v58,v59\n"
MF"v[4:7],a[80:83],a[32:35],v[212:215]\n"
"s_cselect_b32 s41,0,s41\n"
EX"v60,v60\n"
"v_add_u32 v227,s41,v224\n"
MF"v[4:7],a[84:87],a[36:39],v[4:7]\n"
EX"v61,v61\n"
MF"v[12:15],a[84:87],a[52:55],0\n"
BL"v225,s[8:11],s43 offen offset:1024 sc1 lds\n"
MF"v[12:15],a[80:83],a[48:51],v[12:15]\n"
RD"a[108:111],v226 offset:11264\n"
RD"a[112:115],v226 offset:12288\n"
MF"v[4:7],a[88:91],a[40:43],v[4:7]\n"
WT"lgkmcnt(3)\n"
EX"v62,v62\n"
PK"v78,v60,v61\n"
MF"v[4:7],a[92:95],a[44:47],v[4:7]\n"
RD"a[116:119],v226 offset:13312\n"
RD"a[120:123],v226 offset:14336\n"
MF"v[12:15],a[92:95],a[60:63],v[12:15]\n"
EX"v63,v63\n"
MF"v[12:15],a[88:91],a[56:59],v[12:15]\n"
BL"v225,s[8:11],s43 offen offset:2048 sc1 lds\n"
MF"v[16:19],a[96:99],a[32:35],v[216:219]\n"
EX"v0,v0\n"
PK"v79,v62,v63\n"
MF"v[16:19],a[100:103],a[36:39],v[16:19]\n"
EX"v1,v1\n"
MF"v[24:27],a[100:103],a[52:55],0\n"
RD"a[124:127],v226 offset:15360\n"
RD"a[64:67],v226 offset:16384\n"
MF"v[24:27],a[96:99],a[48:51],v[24:27]\n"
WT"lgkmcnt(4)\n"
EX"v2,v2\n"
PK"v0,v0,v1\n"
MF"v[16:19],a[104:107],a[40:43],v[16:19]\n"
BL"v225,s[8:11],s43 offen offset:3072 sc1 lds\n"
RD"a[68:71],v226 offset:17408\n"
MF"v[16:19],a[108:111],a[44:47],v[16:19]\n"
SA"m0,s40,16384\n"
RD"a[72:75],v226 offset:18432\n"
RD"a[76:79],v226 offset:19456\n"
EX"v3,v3\n"
MF"v[24:27],a[108:111],a[60:63],v[24:27]\n"
RD"a[80:83],v226 offset:20480\n"
RD"a[84:87],v226 offset:21504\n"
MF"v[24:27],a[104:107],a[56:59],v[24:27]\n"
EX"v8,v8\n"
PK"v1,v2,v3\n"
MF"v[20:23],a[112:115],a[32:35],v[220:223]\n"
WT"lgkmcnt(7)\n"
EX"v9,v9\n"
MF"v[20:23],a[116:119],a[36:39],v[20:23]\n"
SA"s40,s40,32768\n"
BL"v225,s[8:11],s44 offen offset:0 sc1 lds\n"
MF"v[28:31],a[116:119],a[52:55],0\n"
"s_cmp_eq_u32 s40,s46\n"
RD"a[88:91],v226 offset:22528\n"
RD"a[92:95],v226 offset:23552\n"
EX"v10,v10\n"
MF"v[28:31],a[112:115],a[48:51],v[28:31]\n"
"s_cselect_b32 s40,s47,s40\n"
RD"a[96:99],v226 offset:24576\n"
RD"a[100:103],v226 offset:25600\n"
PK"v8,v8,v9\n"
MF"v[20:23],a[120:123],a[40:43],v[20:23]\n"
WT"lgkmcnt(6)\n"
EX"v11,v11\n"
MF"v[20:23],a[124:127],a[44:47],v[20:23]\n"
RD"a[104:107],v226 offset:26624\n"
RD"a[108:111],v226 offset:27648\n"
MF"v[28:31],a[124:127],a[60:63],v[28:31]\n"
BL"v225,s[8:11],s44 offen offset:1024 sc1 lds\n"
EX"v4,v4\n"
MF"v[28:31],a[120:123],a[56:59],v[28:31]\n"
RD"a[112:115],v226 offset:28672\n"
RD"a[116:119],v226 offset:29696\n"
PK"v9,v10,v11\n"
MF"v[40:43],a[64:67],a[48:51],v[208:211]\n"
EX"v5,v5\n"
MF"v[40:43],a[68:71],a[52:55],v[40:43]\n"
EX"v6,v6\n"
MF"v[40:43],a[72:75],a[56:59],v[40:43]\n"
RD"a[120:123],v226 offset:30720\n"
RD"a[124:127],v226 offset:31744\n"
PK"v2,v4,v5\n"
MF"v[40:43],a[76:79],a[60:63],v[40:43]\n"
WT"lgkmcnt(6)\n"
BL"v225,s[8:11],s44 offen offset:2048 sc1 lds\n"
EX"v7,v7\n"
MF"v[44:47],a[80:83],a[48:51],v[212:215]\n"
WT"vmcnt(19)\n"
"s_barrier\n"
MF"v[44:47],a[84:87],a[52:55],v[44:47]\n"
EX"v12,v12\n"
PK"v3,v6,v7\n"
MF"v[44:47],a[88:91],a[56:59],v[44:47]\n"
EX"v13,v13\n"
MF"v[44:47],a[92:95],a[60:63],v[44:47]\n"
BL"v225,s[8:11],s44 offen offset:3072 sc1 lds\n"
"v_readlane_b32 s43,v228,s42\n"
MF"v[56:59],a[96:99],a[48:51],v[216:219]\n"
"s_mov_b32 m0,s40\n"
RD"a[96:99],v227 offset:8192\n"
PK"v10,v12,v13\n"
EX"v14,v14\n"
MF"v[56:59],a[100:103],a[52:55],v[56:59]\n"
WT"lgkmcnt(1)\n"
RD"a[100:103],v227 offset:9216\n"
MF"v[56:59],a[104:107],a[56:59],v[56:59]\n"
EX"v15,v15\n"
MF"v[56:59],a[108:111],a[60:63],v[56:59]\n"
RD"a[104:107],v227 offset:10240\n"
RD"a[108:111],v227 offset:11264\n"
MF"v[60:63],a[112:115],a[48:51],v[220:223]\n"
EX"v16,v16\n"
PK"v11,v14,v15\n"
MF"v[60:63],a[116:119],a[52:55],v[60:63]\n"
EX"v17,v17\n"
"v_readlane_b32 s44,v229,s42\n"
MF"v[60:63],a[120:123],a[56:59],v[60:63]\n"
SA"s42,s42,1\n"
BL"v225,s[8:11],s43 offen offset:0 sc1 lds\n"
MF"v[60:63],a[124:127],a[60:63],v[60:63]\n"
WT"lgkmcnt(3)\n"
EX"v18,v18\n"
MF"v[112:115],a[96:99],v[68:71],v[112:115]\n"
SA"s41,s41,32768\n"
RD"a[112:115],v227 offset:12288\n"
PK"v16,v16,v17\n"
MF"v[144:147],a[96:99],v[72:75],v[144:147]\n"
"s_cmp_eq_u32 s41,163840\n"
EX"v19,v19\n"
MF"v[176:179],a[96:99],v[76:79],v[176:179]\n"
WT"lgkmcnt(1)\n"
EX"v24,v24\n"
MF"v[116:119],a[100:103],v[68:71],v[116:119]\n"
"s_cselect_b32 s41,0,s41\n"
BL"v225,s[8:11],s43 offen offset:1024 sc1 lds\n"
RD"a[116:119],v227 offset:13312\n"
MF"v[148:151],a[100:103],v[72:75],v[148:151]\n"
EX"v25,v25\n"
"v_add_u32 v226,s41,v224\n"
MF"v[180:183],a[100:103],v[76:79],v[180:183]\n"
RD"a[120:123],v227 offset:14336\n"
PK"v17,v18,v19\n"
MF"v[120:123],a[104:107],v[68:71],v[120:123]\n"
EX"v26,v26\n"
MF"v[152:155],a[104:107],v[72:75],v[152:155]\n"
EX"v27,v27\n"
PK"v24,v24,v25\n"
MF"v[184:187],a[104:107],v[76:79],v[184:187]\n"
BL"v225,s[8:11],s43 offen offset:2048 sc1 lds\n"
MF"v[124:127],a[108:111],v[68:71],v[124:127]\n"
EX"v20,v20\n"
MF"v[156:159],a[108:111],v[72:75],v[156:159]\n"
RD"a[124:127],v227 offset:15360\n"
PK"v25,v26,v27\n"
MF"v[188:191],a[108:111],v[76:79],v[188:191]\n"
WT"lgkmcnt(2)\n"
EX"v21,v21\n"
MF"v[128:131],a[112:115],v[68:71],v[128:131]\n"
BL"v225,s[8:11],s43 offen offset:3072 sc1 lds\n"
MF"v[160:163],a[112:115],v[72:75],v[160:163]\n"
SA"m0,s40,16384\n"
EX"v22,v22\n"
MF"v[192:195],a[112:115],v[76:79],v[192:195]\n"
EX"v23,v23\n"
PK"v18,v20,v21\n"
MF"v[132:135],a[116:119],v[68:71],v[132:135]\n"
RD"a[64:67],v227 offset:16384\n"
PK"v19,v22,v23\n"
MF"v[164:167],a[116:119],v[72:75],v[164:167]\n"
EX"v28,v28\n"
MF"v[196:199],a[116:119],v[76:79],v[196:199]\n"
WT"lgkmcnt(1)\n"
BL"v225,s[8:11],s44 offen offset:0 sc1 lds\n"
MF"v[136:139],a[120:123],v[68:71],v[136:139]\n"
SA"s40,s40,32768\n"
EX"v29,v29\n"
MF"v[168:171],a[120:123],v[72:75],v[168:171]\n"
"s_cmp_eq_u32 s40,s46\n"
RD"a[68:71],v227 offset:24576\n"
RD"a[72:75],v227 offset:17408\n"
MF"v[200:203],a[120:123],v[76:79],v[200:203]\n"
"s_cselect_b32 s40,s47,s40\n"
EX"v30,v30\n"
MF"v[140:143],a[124:127],v[68:71],v[140:143]\n"
RD"a[76:79],v227 offset:25600\n"
RD"a[80:83],v227 offset:18432\n"
PK"v26,v28,v29\n"
MF"v[172:175],a[124:127],v[72:75],v[172:175]\n"
EX"v31,v31\n"
MF"v[204:207],a[124:127],v[76:79],v[204:207]\n"
BL"v225,s[8:11],s44 offen offset:1024 sc1 lds\n"
MF"a[136:139],a[128:131],v[68:71],a[136:139]\n"
EX"v40,v40\n"
PK"v27,v30,v31\n"
MF"a[140:143],a[128:131],v[72:75],a[140:143]\n"
EX"v41,v41\n"
MF"a[144:147],a[128:131],v[76:79],a[144:147]\n"
WT"lgkmcnt(2)\n"
RD"a[84:87],v227 offset:26624\n"
PK"v68,v40,v41\n"
MF"v[144:147],a[64:67],v[0:3],v[144:147]\n"
BL"v225,s[8:11],s44 offen offset:2048 sc1 lds\n"
MF"v[144:147],a[68:71],v[16:19],v[144:147]\n"
EX"v42,v42\n"
MF"v[176:179],a[68:71],v[24:27],v[176:179]\n"
SA"s51,s59,16384\n"
RD"a[88:91],v227 offset:19456\n"
RD"a[92:95],v227 offset:27648\n"
MF"v[176:179],a[64:67],v[8:11],v[176:179]\n"
EX"v43,v43\n"
MF"v[148:151],a[72:75],v[0:3],v[148:151]\n"
WT"lgkmcnt(3)\n"
EX"v44,v44\n"
MF"v[148:151],a[76:79],v[16:19],v[148:151]\n"
RD"a[96:99],v227 offset:20480\n"
"v_accvgpr_read_b32 v252,a136\n"
"v_subrev_u32 v253,0x2b800000,v252\n"
MF"v[180:183],a[76:79],v[24:27],v[180:183]\n"
"v_cmp_le_u32 vcc,0x34000000,v253\n"
"s_cmp_lg_u64 vcc,0\n"
"s_cbranch_scc1 L1035_%=\n"
"L1034_%=:\n"
MF"v[180:183],a[72:75],v[8:11],v[180:183]\n"
BL"v225,s[8:11],s44 offen offset:3072 sc1 lds\n"
EX"v45,v45\n"
MF"v[152:155],a[80:83],v[0:3],v[152:155]\n"
WT"lgkmcnt(3)\n"
"v_rcp_f32 v252,v252\n"
EX"v46,v46\n"
MF"v[152:155],a[84:87],v[16:19],v[152:155]\n"
"s_mov_b32 m0,s40\n"
RD"a[100:103],v227 offset:28672\n"
RD"a[104:107],v227 offset:21504\n"
"v_readlane_b32 s43,v228,s42\n"
MF"v[184:187],a[84:87],v[24:27],v[184:187]\n"
EX"v47,v47\n"
PK"v69,v42,v43\n"
MF"v[184:187],a[80:83],v[8:11],v[184:187]\n"
WT"lgkmcnt(3)\n"
RD"a[108:111],v227 offset:29696\n"
RD"a[112:115],v227 offset:22528\n"
EX"v56,v56\n"
MF"v[156:159],a[88:91],v[0:3],v[156:159]\n"
EX"v57,v57\n"
"v_readlane_b32 s44,v229,s42\n"
MF"v[156:159],a[92:95],v[16:19],v[156:159]\n"
SA"s42,s42,1\n"
EX"v58,v58\n"
PK"v70,v44,v45\n"
MF"v[188:191],a[92:95],v[24:27],v[188:191]\n"
RD"a[116:119],v227 offset:30720\n"
MU"v112,v252,v112\n"
MU"v113,v252,v113\n"
MF"v[188:191],a[88:91],v[8:11],v[188:191]\n"
WT"lgkmcnt(5)\n"
EX"v59,v59\n"
MU"v114,v252,v114\n"
MF"v[160:163],a[96:99],v[0:3],v[160:163]\n"
WT"lgkmcnt(3)\n"
EX"v60,v60\n"
MU"v115,v252,v115\n"
MF"v[160:163],a[100:103],v[16:19],v[160:163]\n"
RD"a[120:123],v227 offset:23552\n"
RD"a[124:127],v227 offset:31744\n"
MU"v116,v252,v116\n"
MF"v[192:195],a[100:103],v[24:27],v[192:195]\n"
EX"v61,v61\n"
MU"v117,v252,v117\n"
MF"v[192:195],a[96:99],v[8:11],v[192:195]\n"
MU"v118,v252,v118\n"
MU"v119,v252,v119\n"
PK"v71,v46,v47\n"
MF"v[164:167],a[104:107],v[0:3],v[164:167]\n"
WT"lgkmcnt(3)\n"
EX"v62,v62\n"
PK"v112,v112,v113\n"
MF"v[164:167],a[108:111],v[16:19],v[164:167]\n"
SA"s41,s41,32768\n"
WT"vmcnt(20)\n"
"s_barrier\n"
MF"v[196:199],a[108:111],v[24:27],v[196:199]\n"
"s_cmp_eq_u32 s41,163840\n"
BL"v225,s[8:11],s43 offen offset:0 sc1 lds\n"
BL"v225,s[8:11],s43 offen offset:1024 sc1 lds\n"
MF"v[196:199],a[104:107],v[8:11],v[196:199]\n"
"s_cselect_b32 s41,0,s41\n"
BL"v225,s[8:11],s43 offen offset:2048 sc1 lds\n"
RD"a[64:67],v226 offset:0\n"
MF"v[168:171],a[112:115],v[0:3],v[168:171]\n"
WT"lgkmcnt(1)\n"
EX"v63,v63\n"
"v_add_u32 v227,s41,v224\n"
MF"v[168:171],a[116:119],v[16:19],v[168:171]\n"
RD"a[68:71],v226 offset:8192\n"
RD"a[72:75],v226 offset:1024\n"
PK"v113,v114,v115\n"
MF"v[200:203],a[116:119],v[24:27],v[200:203]\n"
RD"a[76:79],v226 offset:9216\n"
RD"a[80:83],v226 offset:2048\n"
PK"v114,v116,v117\n"
MF"v[200:203],a[112:115],v[8:11],v[200:203]\n"
BL"v225,s[8:11],s43 offen offset:3072 sc1 lds\n"
PK"v115,v118,v119\n"
MF"v[172:175],a[120:123],v[0:3],v[172:175]\n"
SA"m0,s40,16384\n"
BS"v[112:115],v224,s[8:11],s51 offen offset:0 sc1\n"
RD"a[84:87],v226 offset:10240\n"
MF"v[172:175],a[124:127],v[16:19],v[172:175]\n"
RD"a[88:91],v226 offset:3072\n"
RD"a[92:95],v226 offset:11264\n"
MU"v120,v252,v120\n"
MF"v[204:207],a[124:127],v[24:27],v[204:207]\n"
BL"v225,s[8:11],s44 offen offset:0 sc1 lds\n"
MU"v121,v252,v121\n"
MF"v[204:207],a[120:123],v[8:11],v[204:207]\n"
SA"s40,s40,32768\n"
RD"a[96:99],v226 offset:4096\n"
RD"a[100:103],v226 offset:12288\n"
PK"v76,v56,v57\n"
MF"a[140:143],a[128:131],v[0:3],a[140:143]\n"
"s_cmp_eq_u32 s40,s46\n"
RD"a[104:107],v226 offset:5120\n"
RD"a[108:111],v226 offset:13312\n"
MU"v122,v252,v122\n"
MF"a[140:143],a[128:131],v[16:19],a[140:143]\n"
"s_cselect_b32 s40,s47,s40\n"
RD"a[112:115],v226 offset:6144\n"
MU"v123,v252,v123\n"
MU"v124,v252,v124\n"
MF"a[144:147],a[128:131],v[24:27],a[144:147]\n"
PK"v77,v58,v59\n"
PK"v78,v60,v61\n"
PK"v79,v62,v63\n"
MF"a[144:147],a[128:131],v[8:11],a[144:147]\n"
WT"lgkmcnt(7)\n"
RD"a[116:119],v226 offset:14336\n"
RD"a[120:123],v226 offset:7168\n"
MU"v125,v252,v125\n"
MF"v[176:179],a[64:67],v[68:71],v[176:179]\n"
BL"v225,s[8:11],s44 offen offset:1024 sc1 lds\n"
MU"v126,v252,v126\n"
MF"v[176:179],a[68:71],v[76:79],v[176:179]\n"
RD"a[124:127],v226 offset:15360\n"
MU"v127,v252,v127\n"
PK"v120,v120,v121\n"
MF"v[180:183],a[72:75],v[68:71],v[180:183]\n"
WT"vmcnt(15)\n"
"s_barrier\n"
MF"v[180:183],a[76:79],v[76:79],v[180:183]\n"
RD"a[64:67],v227 offset:0\n"
PK"v121,v122,v123\n"
PK"v122,v124,v125\n"
MF"v[184:187],a[80:83],v[68:71],v[184:187]\n"
BL"v225,s[8:11],s44 offen offset:2048 sc1 lds\n"
PK"v123,v126,v127\n"
MF"v[184:187],a[84:87],v[76:79],v[184:187]\n"
WT"lgkmcnt(4)\n"
BS"v[120:123],v224,s[8:11],s51 offen offset:1024 sc1\n"
MU"v128,v252,v128\n"
MF"v[188:191],a[88:91],v[68:71],v[188:191]\n"
RD"a[68:71],v227 offset:1024\n"
RD"a[72:75],v227 offset:2048\n"
MU"v129,v252,v129\n"
MF"v[188:191],a[92:95],v[76:79],v[188:191]\n"
MU"v130,v252,v130\n"
MU"v131,v252,v131\n"
MU"v132,v252,v132\n"
MF"v[192:195],a[96:99],v[68:71],v[192:195]\n"
MU"v133,v252,v133\n"
MU"v134,v252,v134\n"
MU"v135,v252,v135\n"
MF"v[192:195],a[100:103],v[76:79],v[192:195]\n"
BL"v225,s[8:11],s44 offen offset:3072 sc1 lds\n"
PK"v128,v128,v129\n"
MF"v[196:199],a[104:107],v[68:71],v[196:199]\n"
RD"a[76:79],v227 offset:3072\n"
PK"v129,v130,v131\n"
PK"v130,v132,v133\n"
MF"v[196:199],a[108:111],v[76:79],v[196:199]\n"
PK"v131,v134,v135\n"
MU"v136,v252,v136\n"
MU"v137,v252,v137\n"
MF"v[200:203],a[112:115],v[68:71],v[200:203]\n"
WT"lgkmcnt(3)\n"
BS"v[128:131],v224,s[8:11],s51 offen offset:2048 sc1\n"
MU"v138,v252,v138\n"
MF"v[200:203],a[116:119],v[76:79],v[200:203]\n"
RD"a[80:83],v227 offset:4096\n"
RD"a[84:87],v227 offset:5120\n"
MU"v139,v252,v139\n"
MF"v[204:207],a[120:123],v[68:71],v[204:207]\n"
RD"a[88:91],v227 offset:6144\n"
MU"v140,v252,v140\n"
MU"v141,v252,v141\n"
MF"v[204:207],a[124:127],v[76:79],v[204:207]\n"
PK"v136,v136,v137\n"
MU"v142,v252,v142\n"
MU"v143,v252,v143\n"
MF"a[144:147],a[128:131],v[68:71],a[144:147]\n"
PK"v137,v138,v139\n"
PK"v138,v140,v141\n"
PK"v139,v142,v143\n"
MF"a[144:147],a[128:131],v[76:79],a[144:147]\n"
BS"v[136:139],v224,s[8:11],s51 offen offset:3072 sc1\n"
"s_branch LTL_%=\n"
"LEV_%=:\n"
"s_mov_b32 s14,0\n"
WT"lgkmcnt(6)\n"
MF"v[0:3],a[64:67],a[0:3],0\n"
WT"lgkmcnt(5)\n"
PK"v64,v32,v33\n"
PK"v65,v34,v35\n"
MF"v[0:3],a[68:71],a[4:7],v[0:3]\n"
SA"s95,s40,32768\n"
PK"v66,v36,v37\n"
PK"v67,v38,v39\n"
MF"v[8:11],a[68:71],a[20:23],0\n"
"s_cmp_eq_u32 s95,s46\n"
PK"v68,v40,v41\n"
PK"v69,v42,v43\n"
MF"v[8:11],a[64:67],a[16:19],v[8:11]\n"
"s_cselect_b32 s95,s47,s95\n"
PK"v70,v44,v45\n"
PK"v71,v46,v47\n"
MF"v[16:19],a[64:67],a[32:35],0\n"
SA"m0,s95,0\n"
PK"v72,v48,v49\n"
PK"v73,v50,v51\n"
MF"v[16:19],a[68:71],a[36:39],v[16:19]\n"
RD"a[92:95],v226 offset:7168\n"
"v_mov_b32 v227,v226\n"
MF"v[24:27],a[68:71],a[52:55],0\n"
PK"v74,v52,v53\n"
PK"v75,v54,v55\n"
MF"v[24:27],a[64:67],a[48:51],v[24:27]\n"
WT"lgkmcnt(1)\n"
BL"v235,s[24:27],s52 offen lds\n"
MF"v[0:3],a[72:75],a[8:11],v[0:3]\n"
SA"s52,s52,s62\n"
PK"v76,v56,v57\n"
PK"v77,v58,v59\n"
MF"v[0:3],a[76:79],a[12:15],v[0:3]\n"
PK"v78,v60,v61\n"
PK"v79,v62,v63\n"
MF"v[8:11],a[76:79],a[28:31],v[8:11]\n"
MF"v[8:11],a[72:75],a[24:27],v[8:11]\n"
MF"v[16:19],a[72:75],a[40:43],v[16:19]\n"
SA"m0,s95,1024\n"
MF"v[16:19],a[76:79],a[44:47],v[16:19]\n"
RD"a[96:99],v226 offset:8192\n"
MF"v[24:27],a[76:79],a[60:63],v[24:27]\n"
EX"v0,v0\n"
MF"v[24:27],a[72:75],a[56:59],v[24:27]\n"
BL"v235,s[24:27],s52 offen lds\n"
MF"v[4:7],a[80:83],a[0:3],0\n"
SA"s52,s52,s62\n"
EX"v1,v1\n"
MF"v[4:7],a[84:87],a[4:7],v[4:7]\n"
RD"a[100:103],v226 offset:9216\n"
PK"v0,v0,v1\n"
MF"v[12:15],a[84:87],a[20:23],0\n"
EX"v2,v2\n"
MF"v[12:15],a[80:83],a[16:19],v[12:15]\n"
EX"v3,v3\n"
MF"v[20:23],a[80:83],a[32:35],0\n"
SA"m0,s95,2048\n"
EX"v8,v8\n"
MF"v[20:23],a[84:87],a[36:39],v[20:23]\n"
RD"a[104:107],v226 offset:10240\n"
PK"v1,v2,v3\n"
MF"v[28:31],a[84:87],a[52:55],0\n"
EX"v9,v9\n"
MF"v[28:31],a[80:83],a[48:51],v[28:31]\n"
BL"v235,s[24:27],s52 offen lds\n"
MF"v[4:7],a[88:91],a[8:11],v[4:7]\n"
WT"lgkmcnt(1)\n"
EX"v10,v10\n"
MF"v[4:7],a[92:95],a[12:15],v[4:7]\n"
SA"s52,s52,s62\n"
RD"a[108:111],v226 offset:11264\n"
PK"v8,v8,v9\n"
MF"v[12:15],a[92:95],a[28:31],v[12:15]\n"
EX"v11,v11\n"
MF"v[12:15],a[88:91],a[24:27],v[12:15]\n"
EX"v16,v16\n"
MF"v[20:23],a[88:91],a[40:43],v[20:23]\n"
SA"m0,s95,3072\n"
EX"v17,v17\n"
MF"v[20:23],a[92:95],a[44:47],v[20:23]\n"
RD"a[112:115],v226 offset:12288\n"
PK"v9,v10,v11\n"
MF"v[28:31],a[92:95],a[60:63],v[28:31]\n"
EX"v18,v18\n"
MF"v[28:31],a[88:91],a[56:59],v[28:31]\n"
BL"v235,s[24:27],s52 offen lds\n"
MF"v[32:35],a[96:99],a[0:3],0\n"
SA"s52,s52,s62\n"
EX"v19,v19\n"
MF"v[32:35],a[100:103],a[4:7],v[32:35]\n"
RD"a[116:119],v226 offset:13312\n"
PK"v16,v16,v17\n"
MF"v[40:43],a[100:103],a[20:23],0\n"
EX"v24,v24\n"
MF"v[40:43],a[96:99],a[16:19],v[40:43]\n"
EX"v25,v25\n"
MF"v[48:51],a[96:99],a[32:35],0\n"
SA"m0,s95,16384\n"
EX"v26,v26\n"
MF"v[48:51],a[100:103],a[36:39],v[48:51]\n"
RD"a[120:123],v226 offset:14336\n"
PK"v17,v18,v19\n"
MF"v[56:59],a[100:103],a[52:55],0\n"
EX"v27,v27\n"
MF"v[56:59],a[96:99],a[48:51],v[56:59]\n"
WT"lgkmcnt(2)\n"
BL"v235,s[24:27],s52 offen lds\n"
MF"v[32:35],a[104:107],a[8:11],v[32:35]\n"
SA"s52,s52,s62\n"
EX"v4,v4\n"
MF"v[32:35],a[108:111],a[12:15],v[32:35]\n"
RD"a[124:127],v226 offset:15360\n"
PK"v24,v24,v25\n"
MF"v[40:43],a[108:111],a[28:31],v[40:43]\n"
EX"v5,v5\n"
MF"v[40:43],a[104:107],a[24:27],v[40:43]\n"
EX"v6,v6\n"
MF"v[48:51],a[104:107],a[40:43],v[48:51]\n"
SA"m0,s95,17408\n"
EX"v7,v7\n"
MF"v[48:51],a[108:111],a[44:47],v[48:51]\n"
PK"v25,v26,v27\n"
PK"v2,v4,v5\n"
MF"v[56:59],a[108:111],a[60:63],v[56:59]\n"
EX"v12,v12\n"
MF"v[56:59],a[104:107],a[56:59],v[56:59]\n"
BL"v235,s[24:27],s52 offen lds\n"
MF"v[36:39],a[112:115],a[0:3],0\n"
WT"lgkmcnt(0)\n"
EX"v13,v13\n"
MF"v[36:39],a[116:119],a[4:7],v[36:39]\n"
SA"s52,s52,s62\n"
RD"a[64:67],v227 offset:16384\n"
PK"v3,v6,v7\n"
MF"v[44:47],a[116:119],a[20:23],0\n"
EX"v14,v14\n"
MF"v[44:47],a[112:115],a[16:19],v[44:47]\n"
PK"v10,v12,v13\n"
MF"v[52:55],a[112:115],a[32:35],0\n"
SA"m0,s95,18432\n"
EX"v15,v15\n"
MF"v[52:55],a[116:119],a[36:39],v[52:55]\n"
RD"a[68:71],v227 offset:17408\n"
PK"v11,v14,v15\n"
MF"v[60:63],a[116:119],a[52:55],0\n"
EX"v20,v20\n"
MF"v[60:63],a[112:115],a[48:51],v[60:63]\n"
BL"v235,s[24:27],s52 offen lds\n"
MF"v[36:39],a[120:123],a[8:11],v[36:39]\n"
SA"s52,s52,s62\n"
EX"v21,v21\n"
MF"v[36:39],a[124:127],a[12:15],v[36:39]\n"
RD"a[72:75],v227 offset:18432\n"
"v_readlane_b32 s43,v228,s42\n"
MF"v[44:47],a[124:127],a[28:31],v[44:47]\n"
EX"v22,v22\n"
MF"v[44:47],a[120:123],a[24:27],v[44:47]\n"
"s_mov_b32 m0,s40\n"
PK"v18,v20,v21\n"
MF"v[52:55],a[120:123],a[40:43],v[52:55]\n"
EX"v23,v23\n"
MF"v[52:55],a[124:127],a[44:47],v[52:55]\n"
RD"a[76:79],v227 offset:19456\n"
"v_readlane_b32 s44,v229,s42\n"
MF"v[60:63],a[124:127],a[60:63],v[60:63]\n"
EX"v28,v28\n"
MF"v[60:63],a[120:123],a[56:59],v[60:63]\n"
WT"lgkmcnt(2)\n"
BL"v225,s[8:11],s43 offen offset:0 sc1 lds\n"
MF"v[80:83],a[64:67],v[64:67],v[80:83]\n"
SA"s42,s42,1\n"
EX"v29,v29\n"
MF"v[112:115],a[64:67],v[68:71],v[112:115]\n"
SA"s41,s41,32768\n"
RD"a[80:83],v227 offset:20480\n"
PK"v19,v22,v23\n"
MF"v[144:147],a[64:67],v[72:75],v[144:147]\n"
"s_cmp_eq_u32 s41,163840\n"
EX"v30,v30\n"
MF"v[176:179],a[64:67],v[76:79],v[176:179]\n"
"s_cselect_b32 s41,0,s41\n"
PK"v26,v28,v29\n"
"v_add_u32 v226,s41,v224\n"
MF"v[84:87],a[68:71],v[64:67],v[84:87]\n"
EX"v31,v31\n"
MF"v[116:119],a[68:71],v[68:71],v[116:119]\n"
RD"a[84:87],v227 offset:21504\n"
PK"v27,v30,v31\n"
MF"v[148:151],a[68:71],v[72:75],v[148:151]\n"
EX"v32,v32\n"
MF"v[180:183],a[68:71],v[76:79],v[180:183]\n"
WT"lgkmcnt(2)\n"
BL"v225,s[8:11],s43 offen offset:1024 sc1 lds\n"
MF"v[88:91],a[72:75],v[64:67],v[88:91]\n"
EX"v33,v33\n"
MF"v[120:123],a[72:75],v[68:71],v[120:123]\n"
RD"a[88:91],v227 offset:22528\n"
MF"v[152:155],a[72:75],v[72:75],v[152:155]\n"
EX"v34,v34\n"
MF"v[184:187],a[72:75],v[76:79],v[184:187]\n"
MF"v[92:95],a[76:79],v[64:67],v[92:95]\n"
EX"v35,v35\n"
MF"v[124:127],a[76:79],v[68:71],v[124:127]\n"
RD"a[92:95],v227 offset:23552\n"
MF"v[156:159],a[76:79],v[72:75],v[156:159]\n"
EX"v40,v40\n"
MF"v[188:191],a[76:79],v[76:79],v[188:191]\n"
WT"lgkmcnt(2)\n"
BL"v225,s[8:11],s43 offen offset:2048 sc1 lds\n"
MF"v[96:99],a[80:83],v[64:67],v[96:99]\n"
EX"v41,v41\n"
MF"v[128:131],a[80:83],v[68:71],v[128:131]\n"
RD"a[96:99],v227 offset:24576\n"
MF"v[160:163],a[80:83],v[72:75],v[160:163]\n"
EX"v42,v42\n"
MF"v[192:195],a[80:83],v[76:79],v[192:195]\n"
MF"v[100:103],a[84:87],v[64:67],v[100:103]\n"
EX"v43,v43\n"
MF"v[132:135],a[84:87],v[68:71],v[132:135]\n"
RD"a[100:103],v227 offset:25600\n"
MF"v[164:167],a[84:87],v[72:75],v[164:167]\n"
EX"v48,v48\n"
MF"v[196:199],a[84:87],v[76:79],v[196:199]\n"
WT"lgkmcnt(2)\n"
BL"v225,s[8:11],s43 offen offset:3072 sc1 lds\n"
MF"v[104:107],a[88:91],v[64:67],v[104:107]\n"
EX"v49,v49\n"
MF"v[136:139],a[88:91],v[68:71],v[136:139]\n"
RD"a[104:107],v227 offset:26624\n"
MF"v[168:171],a[88:91],v[72:75],v[168:171]\n"
EX"v50,v50\n"
MF"v[200:203],a[88:91],v[76:79],v[200:203]\n"
SA"m0,s40,16384\n"
MF"v[108:111],a[92:95],v[64:67],v[108:111]\n"
EX"v51,v51\n"
MF"v[140:143],a[92:95],v[68:71],v[140:143]\n"
RD"a[108:111],v227 offset:27648\n"
MF"v[172:175],a[92:95],v[72:75],v[172:175]\n"
EX"v56,v56\n"
MF"v[204:207],a[92:95],v[76:79],v[204:207]\n"
BL"v225,s[8:11],s44 offen offset:0 sc1 lds\n"
MF"a[132:135],a[128:131],v[64:67],a[132:135]\n"
SA"s40,s40,32768\n"
EX"v57,v57\n"
MF"a[136:139],a[128:131],v[68:71],a[136:139]\n"
"s_cmp_eq_u32 s40,s46\n"
RD"a[112:115],v227 offset:28672\n"
"s_cselect_b32 s40,s47,s40\n"
MF"a[140:143],a[128:131],v[72:75],a[140:143]\n"
EX"v58,v58\n"
MF"a[144:147],a[128:131],v[76:79],a[144:147]\n"
WT"lgkmcnt(2)\n"
BL"v[240:243],v238,s[8:11],0 offen sc1\n"
MF"v[80:83],a[96:99],v[0:3],v[80:83]\n"
EX"v59,v59\n"
MF"v[112:115],a[96:99],v[8:11],v[112:115]\n"
RD"a[116:119],v227 offset:29696\n"
MF"v[144:147],a[96:99],v[16:19],v[144:147]\n"
EX"v36,v36\n"
MF"v[176:179],a[96:99],v[24:27],v[176:179]\n"
BL"v225,s[8:11],s44 offen offset:1024 sc1 lds\n"
MF"v[84:87],a[100:103],v[0:3],v[84:87]\n"
EX"v37,v37\n"
MF"v[116:119],a[100:103],v[8:11],v[116:119]\n"
RD"a[120:123],v227 offset:30720\n"
MF"v[148:151],a[100:103],v[16:19],v[148:151]\n"
EX"v38,v38\n"
MF"v[180:183],a[100:103],v[24:27],v[180:183]\n"
MF"v[88:91],a[104:107],v[0:3],v[88:91]\n"
EX"v39,v39\n"
MF"v[120:123],a[104:107],v[8:11],v[120:123]\n"
RD"a[124:127],v227 offset:31744\n"
MF"v[152:155],a[104:107],v[16:19],v[152:155]\n"
EX"v44,v44\n"
MF"v[184:187],a[104:107],v[24:27],v[184:187]\n"
WT"lgkmcnt(2)\n"
BL"v225,s[8:11],s44 offen offset:2048 sc1 lds\n"
MF"v[92:95],a[108:111],v[0:3],v[92:95]\n"
EX"v45,v45\n"
MF"v[124:127],a[108:111],v[8:11],v[124:127]\n"
MF"v[156:159],a[108:111],v[16:19],v[156:159]\n"
EX"v46,v46\n"
MF"v[188:191],a[108:111],v[24:27],v[188:191]\n"
MF"v[96:99],a[112:115],v[0:3],v[96:99]\n"
WT"vmcnt(23) lgkmcnt(0)\n"
"s_barrier\n"
MF"v[128:131],a[112:115],v[8:11],v[128:131]\n"
RD"a[64:67],v226 offset:0\n"
MF"v[160:163],a[112:115],v[16:19],v[160:163]\n"
EX"v47,v47\n"
MF"v[192:195],a[112:115],v[24:27],v[192:195]\n"
BL"v225,s[8:11],s44 offen offset:3072 sc1 lds\n"
MF"v[100:103],a[116:119],v[0:3],v[100:103]\n"
EX"v52,v52\n"
MF"v[132:135],a[116:119],v[8:11],v[132:135]\n"
RD"a[68:71],v226 offset:1024\n"
MF"v[164:167],a[116:119],v[16:19],v[164:167]\n"
EX"v53,v53\n"
MF"v[196:199],a[116:119],v[24:27],v[196:199]\n"
RD"a[72:75],v226 offset:2048\n"
MF"v[104:107],a[120:123],v[0:3],v[104:107]\n"
EX"v54,v54\n"
MF"v[136:139],a[120:123],v[8:11],v[136:139]\n"
RD"a[76:79],v226 offset:3072\n"
MF"v[168:171],a[120:123],v[16:19],v[168:171]\n"
EX"v55,v55\n"
MF"v[200:203],a[120:123],v[24:27],v[200:203]\n"
MF"v[108:111],a[124:127],v[0:3],v[108:111]\n"
EX"v60,v60\n"
MF"v[140:143],a[124:127],v[8:11],v[140:143]\n"
RD"a[80:83],v226 offset:4096\n"
MF"v[172:175],a[124:127],v[16:19],v[172:175]\n"
EX"v61,v61\n"
MF"v[204:207],a[124:127],v[24:27],v[204:207]\n"
RD"a[84:87],v226 offset:5120\n"
MF"a[132:135],a[128:131],v[0:3],a[132:135]\n"
EX"v62,v62\n"
MF"a[136:139],a[128:131],v[8:11],a[136:139]\n"
RD"a[88:91],v226 offset:6144\n"
MF"a[140:143],a[128:131],v[16:19],a[140:143]\n"
EX"v63,v63\n"
MF"a[144:147],a[128:131],v[24:27],a[144:147]\n"
WT"lgkmcnt(6)\n"
MF"v[0:3],a[64:67],a[0:3],0\n"
WT"lgkmcnt(2)\n"
"v_lshrrev_b32 v237,1,v224\n"
PK"v64,v32,v33\n"
MF"v[0:3],a[68:71],a[4:7],v[0:3]\n"
SA"s95,s40,32768\n"
"v_and_b32 v237,0x78,v237\n"
PK"v65,v34,v35\n"
MF"v[8:11],a[68:71],a[20:23],0\n"
"s_cmp_eq_u32 s95,s46\n"
"v_and_b32 v239,0x300,v224\n"
PK"v66,v36,v37\n"
MF"v[8:11],a[64:67],a[16:19],v[8:11]\n"
"s_cselect_b32 s95,s47,s95\n"
"v_add3_u32 v237,v237,v239,s40\n"
PK"v67,v38,v39\n"
MF"v[16:19],a[64:67],a[32:35],0\n"
SA"m0,s95,0\n"
PK"v68,v40,v41\n"
PK"v69,v42,v43\n"
MF"v[16:19],a[68:71],a[36:39],v[16:19]\n"
RD"a[92:95],v226 offset:7168\n"
PK"v70,v44,v45\n"
MF"v[24:27],a[68:71],a[52:55],0\n"
PK"v71,v46,v47\n"
PK"v72,v48,v49\n"
MF"v[24:27],a[64:67],a[48:51],v[24:27]\n"
BL"v235,s[24:27],s52 offen lds\n"
MF"v[0:3],a[72:75],a[8:11],v[0:3]\n"
SA"s52,s52,s62\n"
PK"v73,v50,v51\n"
"v_mov_b32 v227,v226\n"
MF"v[0:3],a[76:79],a[12:15],v[0:3]\n"
PK"v74,v52,v53\n"
PK"v75,v54,v55\n"
MF"v[8:11],a[76:79],a[28:31],v[8:11]\n"
PK"v76,v56,v57\n"
PK"v77,v58,v59\n"
MF"v[8:11],a[72:75],a[24:27],v[8:11]\n"
WT"vmcnt(4)\n"
"v_cmp_eq_u64 vcc,s[28:29],v[240:241]\n"
"v_cmp_eq_u64_e64 s[50:51],s[28:29],v[242:243]\n"
"s_and_b64 vcc,vcc,s[50:51]\n"
"s_cmp_eq_u64 vcc,exec\n"
"s_cbranch_scc0 L1099_%=\n"
"L1100_%=:\n"
MF"v[16:19],a[72:75],a[40:43],v[16:19]\n"
SA"m0,s95,1024\n"
PK"v78,v60,v61\n"
PK"v79,v62,v63\n"
MF"v[16:19],a[76:79],a[44:47],v[16:19]\n"
RD"a[96:99],v226 offset:8192\n"
MF"v[24:27],a[76:79],a[60:63],v[24:27]\n"
EX"v0,v0\n"
MF"v[24:27],a[72:75],a[56:59],v[24:27]\n"
BL"v235,s[24:27],s52 offen lds\n"
MF"v[4:7],a[80:83],a[0:3],0\n"
WT"lgkmcnt(1)\n"
EX"v1,v1\n"
MF"v[4:7],a[84:87],a[4:7],v[4:7]\n"
SA"s52,s52,s62\n"
RD"a[100:103],v226 offset:9216\n"
PK"v0,v0,v1\n"
MF"v[12:15],a[84:87],a[20:23],0\n"
EX"v2,v2\n"
MF"v[12:15],a[80:83],a[16:19],v[12:15]\n"
EX"v3,v3\n"
MF"v[20:23],a[80:83],a[32:35],0\n"
SA"m0,s95,2048\n"
EX"v8,v8\n"
MF"v[20:23],a[84:87],a[36:39],v[20:23]\n"
RD"a[104:107],v226 offset:10240\n"
PK"v1,v2,v3\n"
MF"v[28:31],a[84:87],a[52:55],0\n"
EX"v9,v9\n"
MF"v[28:31],a[80:83],a[48:51],v[28:31]\n"
BL"v235,s[24:27],s52 offen lds\n"
MF"v[4:7],a[88:91],a[8:11],v[4:7]\n"
SA"s52,s52,s62\n"
EX"v10,v10\n"
MF"v[4:7],a[92:95],a[12:15],v[4:7]\n"
RD"a[108:111],v226 offset:11264\n"
PK"v8,v8,v9\n"
MF"v[12:15],a[92:95],a[28:31],v[12:15]\n"
EX"v11,v11\n"
MF"v[12:15],a[88:91],a[24:27],v[12:15]\n"
RT"a[148:149],v237 offset:0\n"
RT"a[150:151],v237 offset:128\n"
MF"v[20:23],a[88:91],a[40:43],v[20:23]\n"
SA"m0,s95,3072\n"
EX"v16,v16\n"
MF"v[20:23],a[92:95],a[44:47],v[20:23]\n"
EX"v17,v17\n"
MF"v[28:31],a[92:95],a[60:63],v[28:31]\n"
RD"a[112:115],v226 offset:12288\n"
PK"v9,v10,v11\n"
MF"v[28:31],a[88:91],a[56:59],v[28:31]\n"
WT"lgkmcnt(4)\n"
EX"v18,v18\n"
MF"v[32:35],a[96:99],a[0:3],0\n"
RT"a[152:153],v237 offset:1024\n"
RT"a[154:155],v237 offset:1152\n"
MF"v[32:35],a[100:103],a[4:7],v[32:35]\n"
BL"v235,s[24:27],s52 offen lds\n"
MF"v[40:43],a[100:103],a[20:23],0\n"
SA"s52,s52,s62\n"
EX"v19,v19\n"
MF"v[40:43],a[96:99],a[16:19],v[40:43]\n"
RD"a[116:119],v226 offset:13312\n"
RT"a[156:157],v237 offset:2048\n"
MF"v[48:51],a[96:99],a[32:35],0\n"
SA"m0,s95,16384\n"
EX"v24,v24\n"
MF"v[48:51],a[100:103],a[36:39],v[48:51]\n"
RT"a[158:159],v237 offset:2176\n"
RD"a[120:123],v226 offset:14336\n"
EX"v25,v25\n"
MF"v[56:59],a[100:103],a[52:55],0\n"
EX"v26,v26\n"
PK"v16,v16,v17\n"
MF"v[56:59],a[96:99],a[48:51],v[56:59]\n"
EX"v27,v27\n"
MF"v[32:35],a[104:107],a[8:11],v[32:35]\n"
WT"lgkmcnt(4)\n"
RT"a[160:161],v237 offset:3072\n"
RT"a[162:163],v237 offset:3200\n"
MF"v[32:35],a[108:111],a[12:15],v[32:35]\n"
BL"v235,s[24:27],s52 offen lds\n"
PK"v17,v18,v19\n"
MF"v[40:43],a[108:111],a[28:31],v[40:43]\n"
SA"s52,s52,s62\n"
EX"v4,v4\n"
MF"v[40:43],a[104:107],a[24:27],v[40:43]\n"
RD"a[124:127],v226 offset:15360\n"
RT"a[164:165],v237 offset:16384\n"
MF"v[48:51],a[104:107],a[40:43],v[48:51]\n"
SA"m0,s95,17408\n"
EX"v5,v5\n"
PK"v24,v24,v25\n"
MF"v[48:51],a[108:111],a[44:47],v[48:51]\n"
RT"a[166:167],v237 offset:16512\n"
PK"v25,v26,v27\n"
EX"v6,v6\n"
MF"v[56:59],a[108:111],a[60:63],v[56:59]\n"
EX"v7,v7\n"
MF"v[56:59],a[104:107],a[56:59],v[56:59]\n"
EX"v12,v12\n"
MF"v[36:39],a[112:115],a[0:3],0\n"
WT"lgkmcnt(3)\n"
RT"a[168:169],v237 offset:17408\n"
RT"a[170:171],v237 offset:17536\n"
MF"v[36:39],a[116:119],a[4:7],v[36:39]\n"
BL"v235,s[24:27],s52 offen lds\n"
MF"v[44:47],a[116:119],a[20:23],0\n"
SA"s52,s52,s62\n"
EX"v13,v13\n"
MF"v[44:47],a[112:115],a[16:19],v[44:47]\n"
RD"a[64:67],v227 offset:16384\n"
RT"a[172:173],v237 offset:18432\n"
PK"v2,v4,v5\n"
MF"v[52:55],a[112:115],a[32:35],0\n"
SA"m0,s95,18432\n"
EX"v14,v14\n"
MF"v[52:55],a[116:119],a[36:39],v[52:55]\n"
RT"a[174:175],v237 offset:18560\n"
RD"a[68:71],v227 offset:17408\n"
PK"v3,v6,v7\n"
MF"v[60:63],a[116:119],a[52:55],0\n"
EX"v15,v15\n"
MF"v[60:63],a[112:115],a[48:51],v[60:63]\n"
EX"v20,v20\n"
MF"v[36:39],a[120:123],a[8:11],v[36:39]\n"
WT"lgkmcnt(4)\n"
BL"v235,s[24:27],s52 offen lds\n"
MF"v[36:39],a[124:127],a[12:15],v[36:39]\n"
SA"s52,s52,s62\n"
EX"v21,v21\n"
PK"v10,v12,v13\n"
MF"v[44:47],a[124:127],a[28:31],v[44:47]\n"
"s_cmp_gt_u32 s92,0\n"
RD"a[72:75],v227 offset:18432\n"
"v_readlane_b32 s43,v228,s42\n"
MF"v[44:47],a[120:123],a[24:27],v[44:47]\n"
"s_cselect_b32 s63,s63,1065353216\n"
EX"v22,v22\n"
MF"v[52:55],a[120:123],a[40:43],v[52:55]\n"
"s_mov_b32 m0,s40\n"
BL"a[204:207],v236,s[8:11],s63 offen offset:0 sc1\n"
MF"v[52:55],a[124:127],a[44:47],v[52:55]\n"
EX"v23,v23\n"
PK"v11,v14,v15\n"
MF"v[60:63],a[124:127],a[60:63],v[60:63]\n"
RD"a[76:79],v227 offset:19456\n"
"v_readlane_b32 s44,v229,s42\n"
MF"v[60:63],a[120:123],a[56:59],v[60:63]\n"
WT"lgkmcnt(2)\n"
EX"v28,v28\n"
MF"v[80:83],a[64:67],v[64:67],v[80:83]\n"
SA"s42,s42,1\n"
BL"v225,s[8:11],s43 offen offset:0 sc1 lds\n"
MF"v[112:115],a[64:67],v[68:71],v[112:115]\n"
SA"s41,s41,32768\n"
EX"v29,v29\n"
PK"v18,v20,v21\n"
MF"v[144:147],a[64:67],v[72:75],v[144:147]\n"
"s_cmp_eq_u32 s41,163840\n"
RD"a[80:83],v227 offset:20480\n"
PK"v19,v22,v23\n"
MF"v[176:179],a[64:67],v[76:79],v[176:179]\n"
"s_cselect_b32 s41,0,s41\n"
EX"v30,v30\n"
MF"v[84:87],a[68:71],v[64:67],v[84:87]\n"
BL"a[208:211],v236,s[8:11],s63 offen offset:1024 sc1\n"
MF"v[116:119],a[68:71],v[68:71],v[116:119]\n"
EX"v31,v31\n"
MF"v[148:151],a[68:71],v[72:75],v[148:151]\n"
RD"a[84:87],v227 offset:21504\n"
"v_add_u32 v226,s41,v224\n"
MF"v[180:183],a[68:71],v[76:79],v[180:183]\n"
WT"lgkmcnt(2)\n"
EX"v32,v32\n"
MF"v[88:91],a[72:75],v[64:67],v[88:91]\n"
BL"v225,s[8:11],s43 offen offset:1024 sc1 lds\n"
MF"v[120:123],a[72:75],v[68:71],v[120:123]\n"
EX"v33,v33\n"
PK"v26,v28,v29\n"
MF"v[152:155],a[72:75],v[72:75],v[152:155]\n"
RD"a[88:91],v227 offset:22528\n"
PK"v27,v30,v31\n"
MF"v[184:187],a[72:75],v[76:79],v[184:187]\n"
EX"v34,v34\n"
MF"v[92:95],a[76:79],v[64:67],v[92:95]\n"
BL"a[212:215],v236,s[8:11],s63 offen offset:2048 sc1\n"
MF"v[124:127],a[76:79],v[68:71],v[124:127]\n"
EX"v35,v35\n"
MF"v[156:159],a[76:79],v[72:75],v[156:159]\n"
RD"a[92:95],v227 offset:23552\n"
MF"v[188:191],a[76:79],v[76:79],v[188:191]\n"
WT"lgkmcnt(2)\n"
EX"v40,v40\n"
MF"v[96:99],a[80:83],v[64:67],v[96:99]\n"
BL"v225,s[8:11],s43 offen offset:2048 sc1 lds\n"
MF"v[128:131],a[80:83],v[68:71],v[128:131]\n"
EX"v41,v41\n"
MF"v[160:163],a[80:83],v[72:75],v[160:163]\n"
RD"a[96:99],v227 offset:24576\n"
MF"v[192:195],a[80:83],v[76:79],v[192:195]\n"
EX"v42,v42\n"
MF"v[100:103],a[84:87],v[64:67],v[100:103]\n"
BL"a[216:219],v236,s[8:11],s63 offen offset:3072 sc1\n"
MF"v[132:135],a[84:87],v[68:71],v[132:135]\n"
EX"v43,v43\n"
MF"v[164:167],a[84:87],v[72:75],v[164:167]\n"
RD"a[100:103],v227 offset:25600\n"
MF"v[196:199],a[84:87],v[76:79],v[196:199]\n"
WT"lgkmcnt(2)\n"
EX"v48,v48\n"
MF"v[104:107],a[88:91],v[64:67],v[104:107]\n"
SA"s63,s63,4096\n"
BL"v225,s[8:11],s43 offen offset:3072 sc1 lds\n"
MF"v[136:139],a[88:91],v[68:71],v[136:139]\n"
EX"v49,v49\n"
MF"v[168:171],a[88:91],v[72:75],v[168:171]\n"
RD"a[104:107],v227 offset:26624\n"
MF"v[200:203],a[88:91],v[76:79],v[200:203]\n"
SA"m0,s40,16384\n"
EX"v50,v50\n"
MF"v[108:111],a[92:95],v[64:67],v[108:111]\n"
BL"a[220:223],v236,s[8:11],s63 offen offset:0 sc1\n"
MF"v[140:143],a[92:95],v[68:71],v[140:143]\n"
EX"v51,v51\n"
MF"v[172:175],a[92:95],v[72:75],v[172:175]\n"
RD"a[108:111],v227 offset:27648\n"
MF"v[204:207],a[92:95],v[76:79],v[204:207]\n"
EX"v56,v56\n"
MF"a[132:135],a[128:131],v[64:67],a[132:135]\n"
SA"s40,s40,32768\n"
BL"v225,s[8:11],s44 offen offset:0 sc1 lds\n"
MF"a[136:139],a[128:131],v[68:71],a[136:139]\n"
"s_cmp_eq_u32 s40,s46\n"
EX"v57,v57\n"
MF"a[140:143],a[128:131],v[72:75],a[140:143]\n"
"s_cselect_b32 s40,s47,s40\n"
RD"a[112:115],v227 offset:28672\n"
MF"a[144:147],a[128:131],v[76:79],a[144:147]\n"
WT"lgkmcnt(2)\n"
EX"v58,v58\n"
MF"v[80:83],a[96:99],v[0:3],v[80:83]\n"
BL"a[224:227],v236,s[8:11],s63 offen offset:1024 sc1\n"
MF"v[112:115],a[96:99],v[8:11],v[112:115]\n"
EX"v59,v59\n"
MF"v[144:147],a[96:99],v[16:19],v[144:147]\n"
RD"a[116:119],v227 offset:29696\n"
MF"v[176:179],a[96:99],v[24:27],v[176:179]\n"
EX"v36,v36\n"
MF"v[84:87],a[100:103],v[0:3],v[84:87]\n"
BL"v225,s[8:11],s44 offen offset:1024 sc1 lds\n"
MF"v[116:119],a[100:103],v[8:11],v[116:119]\n"
EX"v37,v37\n"
MF"v[148:151],a[100:103],v[16:19],v[148:151]\n"
RD"a[120:123],v227 offset:30720\n"
MF"v[180:183],a[100:103],v[24:27],v[180:183]\n"
EX"v38,v38\n"
MF"v[88:91],a[104:107],v[0:3],v[88:91]\n"
BL"a[228:231],v236,s[8:11],s63 offen offset:2048 sc1\n"
MF"v[120:123],a[104:107],v[8:11],v[120:123]\n"
EX"v39,v39\n"
MF"v[152:155],a[104:107],v[16:19],v[152:155]\n"
RD"a[124:127],v227 offset:31744\n"
MF"v[184:187],a[104:107],v[24:27],v[184:187]\n"
WT"lgkmcnt(2)\n"
EX"v44,v44\n"
MF"v[92:95],a[108:111],v[0:3],v[92:95]\n"
BL"v225,s[8:11],s44 offen offset:2048 sc1 lds\n"
MF"v[124:127],a[108:111],v[8:11],v[124:127]\n"
EX"v45,v45\n"
MF"v[156:159],a[108:111],v[16:19],v[156:159]\n"
EX"v46,v46\n"
MF"v[188:191],a[108:111],v[24:27],v[188:191]\n"
BL"a[232:235],v236,s[8:11],s63 offen offset:3072 sc1\n"
MF"v[96:99],a[112:115],v[0:3],v[96:99]\n"
WT"lgkmcnt(0)\n"
"s_barrier\n"
MF"v[128:131],a[112:115],v[8:11],v[128:131]\n"
RD"a[64:67],v226 offset:0\n"
MF"v[160:163],a[112:115],v[16:19],v[160:163]\n"
EX"v47,v47\n"
MF"v[192:195],a[112:115],v[24:27],v[192:195]\n"
SA"s63,s63,4096\n"
BL"v225,s[8:11],s44 offen offset:3072 sc1 lds\n"
MF"v[100:103],a[116:119],v[0:3],v[100:103]\n"
EX"v52,v52\n"
MF"v[132:135],a[116:119],v[8:11],v[132:135]\n"
RD"a[68:71],v226 offset:1024\n"
MF"v[164:167],a[116:119],v[16:19],v[164:167]\n"
EX"v53,v53\n"
MF"v[196:199],a[116:119],v[24:27],v[196:199]\n"
BL"a[236:239],v236,s[8:11],s63 offen offset:0 sc1\n"
MF"v[104:107],a[120:123],v[0:3],v[104:107]\n"
RD"a[72:75],v226 offset:2048\n"
MF"v[136:139],a[120:123],v[8:11],v[136:139]\n"
EX"v54,v54\n"
MF"v[168:171],a[120:123],v[16:19],v[168:171]\n"
RD"a[76:79],v226 offset:3072\n"
MF"v[200:203],a[120:123],v[24:27],v[200:203]\n"
EX"v55,v55\n"
MF"v[108:111],a[124:127],v[0:3],v[108:111]\n"
EX"v60,v60\n"
MF"v[140:143],a[124:127],v[8:11],v[140:143]\n"
RD"a[80:83],v226 offset:4096\n"
MF"v[172:175],a[124:127],v[16:19],v[172:175]\n"
EX"v61,v61\n"
MF"v[204:207],a[124:127],v[24:27],v[204:207]\n"
RD"a[84:87],v226 offset:5120\n"
MF"a[132:135],a[128:131],v[0:3],a[132:135]\n"
EX"v62,v62\n"
MF"a[136:139],a[128:131],v[8:11],a[136:139]\n"
RD"a[88:91],v226 offset:6144\n"
MF"a[140:143],a[128:131],v[16:19],a[140:143]\n"
EX"v63,v63\n"
MF"a[144:147],a[128:131],v[24:27],a[144:147]\n"
WT"lgkmcnt(6)\n"
MF"v[0:3],a[64:67],a[0:3],0\n"
WT"lgkmcnt(2)\n"
"v_lshrrev_b32 v237,1,v224\n"
PK"v64,v32,v33\n"
MF"v[0:3],a[68:71],a[4:7],v[0:3]\n"
SA"s95,s40,32768\n"
"v_and_b32 v237,0x78,v237\n"
PK"v65,v34,v35\n"
MF"v[8:11],a[68:71],a[20:23],0\n"
"s_cmp_eq_u32 s95,s46\n"
"v_and_b32 v239,0x300,v224\n"
PK"v66,v36,v37\n"
MF"v[8:11],a[64:67],a[16:19],v[8:11]\n"
"s_cselect_b32 s95,s47,s95\n"
BL"a[240:243],v236,s[8:11],s63 offen offset:1024 sc1\n"
MF"v[16:19],a[64:67],a[32:35],0\n"
"s_sub_u32 s95,s95,s47\n"
"v_add3_u32 v237,v237,v239,s40\n"
PK"v67,v38,v39\n"
MF"v[16:19],a[68:71],a[36:39],v[16:19]\n"
"s_lshr_b32 s53,s47,2\n"
RD"a[92:95],v226 offset:7168\n"
PK"v68,v40,v41\n"
MF"v[24:27],a[68:71],a[52:55],0\n"
"s_cmp_gt_u32 s92,10\n"
PK"v69,v42,v43\n"
PK"v70,v44,v45\n"
MF"v[24:27],a[64:67],a[48:51],v[24:27]\n"
"s_cselect_b32 s63,s63,1065353216\n"
PK"v71,v46,v47\n"
"v_add_u32 v235,s95,v224\n"
MF"v[0:3],a[72:75],a[8:11],v[0:3]\n"
"s_lshl_b32 s50,s35,6\n"
BL"a[244:247],v236,s[8:11],s63 offen offset:2048 sc1\n"
MF"v[0:3],a[76:79],a[12:15],v[0:3]\n"
SA"s50,s50,4352\n"
PK"v72,v48,v49\n"
PK"v73,v50,v51\n"
MF"v[8:11],a[76:79],a[28:31],v[8:11]\n"
"v_mov_b32 v227,v226\n"
PK"v74,v52,v53\n"
MF"v[8:11],a[72:75],a[24:27],v[8:11]\n"
BL"a[248:251],v236,s[8:11],s63 offen offset:3072 sc1\n"
MF"v[16:19],a[72:75],a[40:43],v[16:19]\n"
SA"s63,s63,4096\n"
PK"v75,v54,v55\n"
PK"v76,v56,v57\n"
MF"v[16:19],a[76:79],a[44:47],v[16:19]\n"
RD"a[96:99],v226 offset:8192\n"
PK"v77,v58,v59\n"
MF"v[24:27],a[76:79],a[60:63],v[24:27]\n"
EX"v0,v0\n"
MF"v[24:27],a[72:75],a[56:59],v[24:27]\n"
BL"a[252:255],v236,s[8:11],s63 offen offset:0 sc1\n"
PK"v78,v60,v61\n"
MF"v[4:7],a[80:83],a[0:3],0\n"
WT"lgkmcnt(1)\n"
EX"v1,v1\n"
PK"v79,v62,v63\n"
MF"v[4:7],a[84:87],a[4:7],v[4:7]\n"
"s_cmp_gt_u32 s92,13\n"
RD"a[100:103],v226 offset:9216\n"
"v_lshrrev_b32 v238,1,v224\n"
"v_and_b32 v238,56,v238\n"
MF"v[12:15],a[84:87],a[20:23],0\n"
"s_cselect_b32 s63,s63,1065353216\n"
EX"v2,v2\n"
MF"v[12:15],a[80:83],a[16:19],v[12:15]\n"
BL"v[244:247],v236,s[8:11],s63 offen offset:1024 sc1\n"
"v_add_u32 v236,s53,v235\n"
MF"v[20:23],a[80:83],a[32:35],0\n"
EX"v3,v3\n"
MF"v[20:23],a[84:87],a[36:39],v[20:23]\n"
EX"v8,v8\n"
MF"v[28:31],a[84:87],a[52:55],0\n"
RD"a[104:107],v226 offset:10240\n"
PK"v0,v0,v1\n"
MF"v[28:31],a[80:83],a[48:51],v[28:31]\n"
EX"v9,v9\n"
MF"v[4:7],a[88:91],a[8:11],v[4:7]\n"
"buffer_load_dwordx2 v[240:241],v238,s[8:11],s50 offen sc1\n"
MF"v[4:7],a[92:95],a[12:15],v[4:7]\n"
EX"v10,v10\n"
MF"v[12:15],a[92:95],a[28:31],v[12:15]\n"
RD"a[108:111],v226 offset:11264\n"
PK"v1,v2,v3\n"
MF"v[12:15],a[88:91],a[24:27],v[12:15]\n"
EX"v11,v11\n"
MF"v[20:23],a[88:91],a[40:43],v[20:23]\n"
WT"vmcnt(23)\n"
RT"a[176:177],v237 offset:0\n"
RT"a[178:179],v237 offset:128\n"
MF"v[20:23],a[92:95],a[44:47],v[20:23]\n"
EX"v16,v16\n"
MF"v[28:31],a[92:95],a[60:63],v[28:31]\n"
EX"v17,v17\n"
MF"v[28:31],a[88:91],a[56:59],v[28:31]\n"
WT"lgkmcnt(3)\n"
RD"a[112:115],v226 offset:12288\n"
RT"a[180:181],v237 offset:1024\n"
PK"v8,v8,v9\n"
MF"v[32:35],a[96:99],a[0:3],0\n"
EX"v18,v18\n"
MF"v[32:35],a[100:103],a[4:7],v[32:35]\n"
RT"a[182:183],v237 offset:1152\n"
RD"a[116:119],v226 offset:13312\n"
MF"v[40:43],a[100:103],a[20:23],0\n"
EX"v19,v19\n"
MF"v[40:43],a[96:99],a[16:19],v[40:43]\n"
EX"v24,v24\n"
PK"v9,v10,v11\n"
MF"v[48:51],a[96:99],a[32:35],0\n"
RT"a[184:185],v237 offset:2048\n"
RT"a[186:187],v237 offset:2176\n"
MF"v[48:51],a[100:103],a[36:39],v[48:51]\n"
EX"v25,v25\n"
PK"v16,v16,v17\n"
MF"v[56:59],a[100:103],a[52:55],0\n"
EX"v26,v26\n"
MF"v[56:59],a[96:99],a[48:51],v[56:59]\n"
RD"a[120:123],v226 offset:14336\n"
RT"a[188:189],v237 offset:3072\n"
MF"v[32:35],a[104:107],a[8:11],v[32:35]\n"
WT"lgkmcnt(4)\n"
EX"v27,v27\n"
MF"v[32:35],a[108:111],a[12:15],v[32:35]\n"
RT"a[190:191],v237 offset:3200\n"
RD"a[124:127],v226 offset:15360\n"
PK"v17,v18,v19\n"
MF"v[40:43],a[108:111],a[28:31],v[40:43]\n"
EX"v4,v4\n"
MF"v[40:43],a[104:107],a[24:27],v[40:43]\n"
EX"v5,v5\n"
MF"v[48:51],a[104:107],a[40:43],v[48:51]\n"
RT"a[192:193],v237 offset:16384\n"
RT"a[194:195],v237 offset:16512\n"
PK"v24,v24,v25\n"
MF"v[48:51],a[108:111],a[44:47],v[48:51]\n"
EX"v6,v6\n"
MF"v[56:59],a[108:111],a[60:63],v[56:59]\n"
EX"v7,v7\n"
MF"v[56:59],a[104:107],a[56:59],v[56:59]\n"
EX"v12,v12\n"
PK"v25,v26,v27\n"
MF"v[36:39],a[112:115],a[0:3],0\n"
RT"a[196:197],v237 offset:17408\n"
RT"a[198:199],v237 offset:17536\n"
MF"v[36:39],a[116:119],a[4:7],v[36:39]\n"
EX"v13,v13\n"
MF"v[44:47],a[116:119],a[20:23],0\n"
RD"a[64:67],v227 offset:16384\n"
PK"v2,v4,v5\n"
MF"v[44:47],a[112:115],a[16:19],v[44:47]\n"
EX"v14,v14\n"
MF"v[52:55],a[112:115],a[32:35],0\n"
RT"a[200:201],v237 offset:18432\n"
RT"a[202:203],v237 offset:18560\n"
MF"v[52:55],a[116:119],a[36:39],v[52:55]\n"
EX"v15,v15\n"
PK"v3,v6,v7\n"
MF"v[60:63],a[116:119],a[52:55],0\n"
RD"a[68:71],v227 offset:17408\n"
PK"v10,v12,v13\n"
MF"v[60:63],a[112:115],a[48:51],v[60:63]\n"
WT"lgkmcnt(4)\n"
EX"v20,v20\n"
MF"v[36:39],a[120:123],a[8:11],v[36:39]\n"
EX"v21,v21\n"
MF"v[36:39],a[124:127],a[12:15],v[36:39]\n"
RD"a[72:75],v227 offset:18432\n"
"v_readlane_b32 s43,v228,s42\n"
MF"v[44:47],a[124:127],a[28:31],v[44:47]\n"
EX"v22,v22\n"
MF"v[44:47],a[120:123],a[24:27],v[44:47]\n"
"s_mov_b32 m0,s40\n"
PK"v11,v14,v15\n"
PK"v18,v20,v21\n"
MF"v[52:55],a[120:123],a[40:43],v[52:55]\n"
EX"v23,v23\n"
MF"v[52:55],a[124:127],a[44:47],v[52:55]\n"
RD"a[76:79],v227 offset:19456\n"
"v_readlane_b32 s44,v229,s42\n"
MF"v[60:63],a[124:127],a[60:63],v[60:63]\n"
EX"v28,v28\n"
MF"v[60:63],a[120:123],a[56:59],v[60:63]\n"
WT"lgkmcnt(2)\n"
BL"v225,s[8:11],s43 offen offset:0 sc1 lds\n"
MF"v[80:83],a[64:67],v[64:67],v[80:83]\n"
SA"s42,s42,1\n"
EX"v29,v29\n"
MF"v[112:115],a[64:67],v[68:71],v[112:115]\n"
SA"s41,s41,32768\n"
RD"a[80:83],v227 offset:20480\n"
PK"v19,v22,v23\n"
MF"v[144:147],a[64:67],v[72:75],v[144:147]\n"
"s_cmp_eq_u32 s41,163840\n"
EX"v30,v30\n"
MF"v[176:179],a[64:67],v[76:79],v[176:179]\n"
"s_cselect_b32 s41,0,s41\n"
PK"v26,v28,v29\n"
"v_add_u32 v226,s41,v224\n"
MF"v[84:87],a[68:71],v[64:67],v[84:87]\n"
EX"v31,v31\n"
MF"v[116:119],a[68:71],v[68:71],v[116:119]\n"
RD"a[84:87],v227 offset:21504\n"
PK"v27,v30,v31\n"
MF"v[148:151],a[68:71],v[72:75],v[148:151]\n"
WT"vmcnt(1)\n"
"v_cmp_eq_u64 vcc,s[28:29],v[240:241]\n"
"s_cmp_eq_u64 vcc,exec\n"
"s_cbranch_scc0 L1102_%=\n"
"L1103_%=:\n"
MF"v[180:183],a[68:71],v[76:79],v[180:183]\n"
WT"lgkmcnt(2)\n"
EX"v32,v32\n"
MF"v[88:91],a[72:75],v[64:67],v[88:91]\n"
BL"v225,s[8:11],s43 offen offset:1024 sc1 lds\n"
MF"v[120:123],a[72:75],v[68:71],v[120:123]\n"
EX"v33,v33\n"
MF"v[152:155],a[72:75],v[72:75],v[152:155]\n"
RD"a[88:91],v227 offset:22528\n"
MF"v[184:187],a[72:75],v[76:79],v[184:187]\n"
EX"v34,v34\n"
MF"v[92:95],a[76:79],v[64:67],v[92:95]\n"
EX"v35,v35\n"
MF"v[124:127],a[76:79],v[68:71],v[124:127]\n"
RD"a[92:95],v227 offset:23552\n"
MF"v[156:159],a[76:79],v[72:75],v[156:159]\n"
EX"v40,v40\n"
MF"v[188:191],a[76:79],v[76:79],v[188:191]\n"
WT"lgkmcnt(2)\n"
BL"v225,s[8:11],s43 offen offset:2048 sc1 lds\n"
MF"v[96:99],a[80:83],v[64:67],v[96:99]\n"
EX"v41,v41\n"
MF"v[128:131],a[80:83],v[68:71],v[128:131]\n"
RD"a[96:99],v227 offset:24576\n"
MF"v[160:163],a[80:83],v[72:75],v[160:163]\n"
EX"v42,v42\n"
MF"v[192:195],a[80:83],v[76:79],v[192:195]\n"
MF"v[100:103],a[84:87],v[64:67],v[100:103]\n"
EX"v43,v43\n"
MF"v[132:135],a[84:87],v[68:71],v[132:135]\n"
RD"a[100:103],v227 offset:25600\n"
MF"v[164:167],a[84:87],v[72:75],v[164:167]\n"
EX"v48,v48\n"
MF"v[196:199],a[84:87],v[76:79],v[196:199]\n"
WT"lgkmcnt(2)\n"
BL"v225,s[8:11],s43 offen offset:3072 sc1 lds\n"
MF"v[104:107],a[88:91],v[64:67],v[104:107]\n"
EX"v49,v49\n"
MF"v[136:139],a[88:91],v[68:71],v[136:139]\n"
RD"a[104:107],v227 offset:26624\n"
MF"v[168:171],a[88:91],v[72:75],v[168:171]\n"
EX"v50,v50\n"
MF"v[200:203],a[88:91],v[76:79],v[200:203]\n"
SA"m0,s40,16384\n"
MF"v[108:111],a[92:95],v[64:67],v[108:111]\n"
EX"v51,v51\n"
MF"v[140:143],a[92:95],v[68:71],v[140:143]\n"
RD"a[108:111],v227 offset:27648\n"
MF"v[172:175],a[92:95],v[72:75],v[172:175]\n"
EX"v56,v56\n"
MF"v[204:207],a[92:95],v[76:79],v[204:207]\n"
BL"v225,s[8:11],s44 offen offset:0 sc1 lds\n"
MF"a[132:135],a[128:131],v[64:67],a[132:135]\n"
SA"s40,s40,32768\n"
EX"v57,v57\n"
MF"a[136:139],a[128:131],v[68:71],a[136:139]\n"
"s_cmp_eq_u32 s40,s46\n"
RD"a[112:115],v227 offset:28672\n"
"s_cselect_b32 s40,s47,s40\n"
MF"a[140:143],a[128:131],v[72:75],a[140:143]\n"
EX"v58,v58\n"
MF"a[144:147],a[128:131],v[76:79],a[144:147]\n"
MF"v[240:243],a[148:151],a[204:207],0\n"
EX"v59,v59\n"
MF"v[240:243],a[152:155],a[208:211],v[240:243]\n"
RD"a[116:119],v227 offset:29696\n"
MF"v[240:243],a[156:159],a[212:215],v[240:243]\n"
EX"v36,v36\n"
MF"v[240:243],a[160:163],a[216:219],v[240:243]\n"
BL"v225,s[8:11],s44 offen offset:1024 sc1 lds\n"
MF"v[240:243],a[164:167],a[220:223],v[240:243]\n"
EX"v37,v37\n"
MF"v[240:243],a[168:171],a[224:227],v[240:243]\n"
RD"a[120:123],v227 offset:30720\n"
MF"v[240:243],a[172:175],a[228:231],v[240:243]\n"
EX"v38,v38\n"
MF"v[240:243],a[176:179],a[232:235],v[240:243]\n"
MF"v[240:243],a[180:183],a[236:239],v[240:243]\n"
EX"v39,v39\n"
MF"v[240:243],a[184:187],a[240:243],v[240:243]\n"
RD"a[124:127],v227 offset:31744\n"
MF"v[240:243],a[188:191],a[244:247],v[240:243]\n"
EX"v44,v44\n"
MF"v[240:243],a[192:195],a[248:251],v[240:243]\n"
BL"v225,s[8:11],s44 offen offset:2048 sc1 lds\n"
MF"v[240:243],a[196:199],a[252:255],v[240:243]\n"
EX"v45,v45\n"
MF"v[240:243],a[200:203],v[244:247],v[240:243]\n"
WT"lgkmcnt(1)\n"
MF"v[80:83],a[96:99],v[0:3],v[80:83]\n"
EX"v46,v46\n"
MF"v[112:115],a[96:99],v[8:11],v[112:115]\n"
MF"v[144:147],a[96:99],v[16:19],v[144:147]\n"
MF"v[176:179],a[96:99],v[24:27],v[176:179]\n"
MF"v[84:87],a[100:103],v[0:3],v[84:87]\n"
EX"v47,v47\n"
MF"v[116:119],a[100:103],v[8:11],v[116:119]\n"
"ds_write_b128 v236,v[240:243]\n"
MF"v[148:151],a[100:103],v[16:19],v[148:151]\n"
BL"v225,s[8:11],s44 offen offset:3072 sc1 lds\n"
MF"v[180:183],a[100:103],v[24:27],v[180:183]\n"
EX"v52,v52\n"
MF"v[88:91],a[104:107],v[0:3],v[88:91]\n"
EX"v53,v53\n"
MF"v[120:123],a[104:107],v[8:11],v[120:123]\n"
MF"v[152:155],a[104:107],v[16:19],v[152:155]\n"
EX"v54,v54\n"
MF"v[184:187],a[104:107],v[24:27],v[184:187]\n"
MF"v[92:95],a[108:111],v[0:3],v[92:95]\n"
EX"v55,v55\n"
MF"v[124:127],a[108:111],v[8:11],v[124:127]\n"
MF"v[156:159],a[108:111],v[16:19],v[156:159]\n"
EX"v60,v60\n"
MF"v[188:191],a[108:111],v[24:27],v[188:191]\n"
MF"v[96:99],a[112:115],v[0:3],v[96:99]\n"
EX"v61,v61\n"
MF"v[128:131],a[112:115],v[8:11],v[128:131]\n"
WT"lgkmcnt(0)\n"
"s_barrier\n"
MF"v[160:163],a[112:115],v[16:19],v[160:163]\n"
RD"a[64:67],v226 offset:0\n"
RD"v[240:243],v235 offset:0\n"
MF"v[192:195],a[112:115],v[24:27],v[192:195]\n"
EX"v62,v62\n"
MF"v[100:103],a[116:119],v[0:3],v[100:103]\n"
EX"v63,v63\n"
MF"v[132:135],a[116:119],v[8:11],v[132:135]\n"
RD"v[244:247],v235 offset:1024\n"
RD"a[68:71],v226 offset:1024\n"
MF"v[164:167],a[116:119],v[16:19],v[164:167]\n"
RD"v[248:251],v235 offset:2048\n"
MF"v[196:199],a[116:119],v[24:27],v[196:199]\n"
RD"a[72:75],v226 offset:2048\n"
MF"v[104:107],a[120:123],v[0:3],v[104:107]\n"
RD"v[252:255],v235 offset:3072\n"
MF"v[136:139],a[120:123],v[8:11],v[136:139]\n"
RD"a[76:79],v226 offset:3072\n"
MF"v[168:171],a[120:123],v[16:19],v[168:171]\n"
MF"v[200:203],a[120:123],v[24:27],v[200:203]\n"
MF"v[108:111],a[124:127],v[0:3],v[108:111]\n"
MF"v[140:143],a[124:127],v[8:11],v[140:143]\n"
RD"a[80:83],v226 offset:4096\n"
MF"v[172:175],a[124:127],v[16:19],v[172:175]\n"
MF"v[204:207],a[124:127],v[24:27],v[204:207]\n"
RD"a[84:87],v226 offset:5120\n"
MF"a[132:135],a[128:131],v[0:3],a[132:135]\n"
MF"a[136:139],a[128:131],v[8:11],a[136:139]\n"
RD"a[88:91],v226 offset:6144\n"
MF"a[140:143],a[128:131],v[16:19],a[140:143]\n"
MF"a[144:147],a[128:131],v[24:27],a[144:147]\n"
"s_cmp_eq_u32 s57,2\n"
"s_cbranch_scc1 LMD_%=\n"
"s_cmp_eq_u32 s34,2\n"
"s_cbranch_scc1 LMA_%=\n"
WT"lgkmcnt(10)\n"
MF"v[0:3],a[64:67],a[0:3],0\n"
WT"lgkmcnt(2)\n"
"v_add_f32 v240,v240,v244\n"
PK"v64,v32,v33\n"
MF"v[0:3],a[68:71],a[4:7],v[0:3]\n"
PK"v65,v34,v35\n"
"v_add_f32 v248,v248,v252\n"
MF"v[8:11],a[68:71],a[20:23],0\n"
PK"v66,v36,v37\n"
PK"v67,v38,v39\n"
MF"v[8:11],a[64:67],a[16:19],v[8:11]\n"
"v_add_f32 v241,v241,v245\n"
PK"v68,v40,v41\n"
MF"v[16:19],a[64:67],a[32:35],0\n"
PK"v69,v42,v43\n"
"v_add_f32 v249,v249,v253\n"
MF"v[16:19],a[68:71],a[36:39],v[16:19]\n"
RD"a[92:95],v226 offset:7168\n"
PK"v70,v44,v45\n"
MF"v[24:27],a[68:71],a[52:55],0\n"
PK"v71,v46,v47\n"
"v_add_f32 v242,v242,v246\n"
MF"v[24:27],a[64:67],a[48:51],v[24:27]\n"
PK"v72,v48,v49\n"
PK"v73,v50,v51\n"
MF"v[0:3],a[72:75],a[8:11],v[0:3]\n"
"v_mov_b32 v227,v226\n"
"v_add_f32 v250,v250,v254\n"
MF"v[0:3],a[76:79],a[12:15],v[0:3]\n"
PK"v74,v52,v53\n"
PK"v75,v54,v55\n"
MF"v[8:11],a[76:79],a[28:31],v[8:11]\n"
"v_add_f32 v243,v243,v247\n"
PK"v76,v56,v57\n"
MF"v[8:11],a[72:75],a[24:27],v[8:11]\n"
PK"v77,v58,v59\n"
"v_add_f32 v251,v251,v255\n"
MF"v[16:19],a[72:75],a[40:43],v[16:19]\n"
PK"v78,v60,v61\n"
PK"v79,v62,v63\n"
MF"v[16:19],a[76:79],a[44:47],v[16:19]\n"
RD"a[96:99],v226 offset:8192\n"
PK"v240,v240,v240\n"
MF"v[24:27],a[76:79],a[60:63],v[24:27]\n"
EX"v0,v0\n"
MF"v[24:27],a[72:75],a[56:59],v[24:27]\n"
"v_lshlrev_b32 v240,16,v240\n"
PK"v241,v241,v241\n"
MF"v[4:7],a[80:83],a[0:3],0\n"
WT"lgkmcnt(1)\n"
EX"v1,v1\n"
MF"v[4:7],a[84:87],a[4:7],v[4:7]\n"
RD"a[100:103],v226 offset:9216\n"
"v_lshlrev_b32 v241,16,v241\n"
MF"v[12:15],a[84:87],a[20:23],0\n"
EX"v2,v2\n"
MF"v[12:15],a[80:83],a[16:19],v[12:15]\n"
EX"v3,v3\n"
MF"v[20:23],a[80:83],a[32:35],0\n"
EX"v8,v8\n"
PK"v242,v242,v242\n"
MF"v[20:23],a[84:87],a[36:39],v[20:23]\n"
RD"a[104:107],v226 offset:10240\n"
"v_lshlrev_b32 v242,16,v242\n"
MF"v[28:31],a[84:87],a[52:55],0\n"
EX"v9,v9\n"
PK"v243,v243,v243\n"
MF"v[28:31],a[80:83],a[48:51],v[28:31]\n"
"v_lshlrev_b32 v243,16,v243\n"
MU"v244,0xbfb8aa3b,v240\n"
EX"v244,v244\n"
MF"v[4:7],a[88:91],a[8:11],v[4:7]\n"
EX"v10,v10\n"
MF"v[4:7],a[92:95],a[12:15],v[4:7]\n"
RD"a[108:111],v226 offset:11264\n"
MU"v245,0xbfb8aa3b,v241\n"
EX"v245,v245\n"
MF"v[12:15],a[92:95],a[28:31],v[12:15]\n"
EX"v11,v11\n"
PK"v0,v0,v1\n"
MF"v[12:15],a[88:91],a[24:27],v[12:15]\n"
EX"v16,v16\n"
MU"v246,0xbfb8aa3b,v242\n"
MF"v[20:23],a[88:91],a[40:43],v[20:23]\n"
EX"v246,v246\n"
MU"v247,0xbfb8aa3b,v243\n"
MF"v[20:23],a[92:95],a[44:47],v[20:23]\n"
EX"v17,v17\n"
PK"v1,v2,v3\n"
MF"v[28:31],a[92:95],a[60:63],v[28:31]\n"
EX"v247,v247\n"
PK"v248,v248,v248\n"
MF"v[28:31],a[88:91],a[56:59],v[28:31]\n"
WT"lgkmcnt(1)\n"
RD"a[112:115],v226 offset:12288\n"
"v_lshlrev_b32 v248,16,v248\n"
PK"v8,v8,v9\n"
MF"v[32:35],a[96:99],a[0:3],0\n"
EX"v18,v18\n"
PK"v249,v249,v249\n"
MF"v[32:35],a[100:103],a[4:7],v[32:35]\n"
EX"v19,v19\n"
"v_lshlrev_b32 v249,16,v249\n"
MF"v[40:43],a[100:103],a[20:23],0\n"
RD"a[116:119],v226 offset:13312\n"
PK"v250,v250,v250\n"
MF"v[40:43],a[96:99],a[16:19],v[40:43]\n"
EX"v24,v24\n"
PK"v9,v10,v11\n"
MF"v[48:51],a[96:99],a[32:35],0\n"
EX"v25,v25\n"
"v_lshlrev_b32 v250,16,v250\n"
MF"v[48:51],a[100:103],a[36:39],v[48:51]\n"
EX"v26,v26\n"
PK"v16,v16,v17\n"
MF"v[56:59],a[100:103],a[52:55],0\n"
RD"a[120:123],v226 offset:14336\n"
PK"v251,v251,v251\n"
MF"v[56:59],a[96:99],a[48:51],v[56:59]\n"
EX"v27,v27\n"
"v_lshlrev_b32 v251,16,v251\n"
MF"v[32:35],a[104:107],a[8:11],v[32:35]\n"
WT"lgkmcnt(2)\n"
EX"v4,v4\n"
"v_add_f32 v244,1.0,v244\n"
MF"v[32:35],a[108:111],a[12:15],v[32:35]\n"
"v_rcp_f32 v244,v244\n"
"v_add_f32 v245,1.0,v245\n"
MF"v[40:43],a[108:111],a[28:31],v[40:43]\n"
"v_rcp_f32 v245,v245\n"
PK"v17,v18,v19\n"
MF"v[40:43],a[104:107],a[24:27],v[40:43]\n"
RD"a[124:127],v226 offset:15360\n"
"v_add_f32 v246,1.0,v246\n"
"v_rcp_f32 v246,v246\n"
MF"v[48:51],a[104:107],a[40:43],v[48:51]\n"
EX"v5,v5\n"
"v_add_f32 v247,1.0,v247\n"
MF"v[48:51],a[108:111],a[44:47],v[48:51]\n"
"v_rcp_f32 v247,v247\n"
EX"v6,v6\n"
MF"v[56:59],a[108:111],a[60:63],v[56:59]\n"
EX"v7,v7\n"
PK"v24,v24,v25\n"
MF"v[56:59],a[104:107],a[56:59],v[56:59]\n"
EX"v12,v12\n"
PK"v25,v26,v27\n"
MF"v[36:39],a[112:115],a[0:3],0\n"
WT"lgkmcnt(1)\n"
EX"v13,v13\n"
MU"v240,v240,v244\n"
MF"v[36:39],a[116:119],a[4:7],v[36:39]\n"
RD"a[64:67],v227 offset:16384\n"
MU"v241,v241,v245\n"
MF"v[44:47],a[116:119],a[20:23],0\n"
EX"v14,v14\n"
MU"v242,v242,v246\n"
MF"v[44:47],a[112:115],a[16:19],v[44:47]\n"
MU"v243,v243,v247\n"
PK"v2,v4,v5\n"
MF"v[52:55],a[112:115],a[32:35],0\n"
EX"v15,v15\n"
PK"v240,v240,v240\n"
MF"v[52:55],a[116:119],a[36:39],v[52:55]\n"
RD"a[68:71],v227 offset:17408\n"
"v_lshlrev_b32 v240,16,v240\n"
PK"v3,v6,v7\n"
MF"v[60:63],a[116:119],a[52:55],0\n"
EX"v20,v20\n"
PK"v241,v241,v241\n"
MF"v[60:63],a[112:115],a[48:51],v[60:63]\n"
"v_readlane_b32 s43,v228,s42\n"
"v_lshlrev_b32 v241,16,v241\n"
MF"v[36:39],a[120:123],a[8:11],v[36:39]\n"
WT"lgkmcnt(1)\n"
EX"v21,v21\n"
PK"v242,v242,v242\n"
MF"v[36:39],a[124:127],a[12:15],v[36:39]\n"
RD"a[72:75],v227 offset:18432\n"
"v_lshlrev_b32 v242,16,v242\n"
PK"v10,v12,v13\n"
MF"v[44:47],a[124:127],a[28:31],v[44:47]\n"
EX"v22,v22\n"
PK"v243,v243,v243\n"
MF"v[44:47],a[120:123],a[24:27],v[44:47]\n"
"s_mov_b32 m0,s40\n"
"v_lshlrev_b32 v243,16,v243\n"
MU"v240,v240,v248\n"
MF"v[52:55],a[120:123],a[40:43],v[52:55]\n"
EX"v23,v23\n"
MF"v[52:55],a[124:127],a[44:47],v[52:55]\n"
RD"a[76:79],v227 offset:19456\n"
"v_readlane_b32 s44,v229,s42\n"
MU"v241,v241,v249\n"
MF"v[60:63],a[124:127],a[60:63],v[60:63]\n"
EX"v28,v28\n"
PK"v11,v14,v15\n"
MF"v[60:63],a[120:123],a[56:59],v[60:63]\n"
SA"s42,s42,1\n"
BL"v225,s[8:11],s43 offen offset:0 sc1 lds\n"
MU"v242,v242,v250\n"
MF"v[80:83],a[64:67],v[64:67],v[80:83]\n"
SA"s41,s41,32768\n"
EX"v29,v29\n"
MU"v243,v243,v251\n"
MF"v[112:115],a[64:67],v[68:71],v[112:115]\n"
"s_cmp_eq_u32 s41,163840\n"
RD"a[80:83],v227 offset:20480\n"
PK"v240,v240,v241\n"
PK"v241,v242,v243\n"
MF"v[144:147],a[64:67],v[72:75],v[144:147]\n"
"s_cselect_b32 s41,0,s41\n"
EX"v30,v30\n"
PK"v18,v20,v21\n"
MF"v[176:179],a[64:67],v[76:79],v[176:179]\n"
WT"lgkmcnt(2)\n"
"v_add_u32 v226,s41,v224\n"
"v_lshrrev_b32 v239,9,v224\n"
"v_lshlrev_b32 v239,8,v239\n"
MF"v[84:87],a[68:71],v[64:67],v[84:87]\n"
EX"v31,v31\n"
"v_bfe_u32 v237,v224,8,1\n"
MF"v[116:119],a[68:71],v[68:71],v[116:119]\n"
RD"a[84:87],v227 offset:21504\n"
"v_lshl_add_u32 v239,v237,3,v239\n"
PK"v19,v22,v23\n"
MF"v[148:151],a[68:71],v[72:75],v[148:151]\n"
EX"v32,v32\n"
"v_and_b32 v237,0x80,v224\n"
MF"v[180:183],a[68:71],v[76:79],v[180:183]\n"
BL"v225,s[8:11],s43 offen offset:1024 sc1 lds\n"
"v_lshl_add_u32 v239,v237,23,v239\n"
MF"v[88:91],a[72:75],v[64:67],v[88:91]\n"
EX"v33,v33\n"
"v_and_b32 v237,0x70,v224\n"
MF"v[120:123],a[72:75],v[68:71],v[120:123]\n"
RD"a[88:91],v227 offset:22528\n"
"v_add_u32 v239,v239,v237\n"
PK"v26,v28,v29\n"
MF"v[152:155],a[72:75],v[72:75],v[152:155]\n"
"buffer_store_dwordx2 v[240:241],v239,s[8:11],s93 offen sc1\n"
MF"v[184:187],a[72:75],v[76:79],v[184:187]\n"
WT"lgkmcnt(2)\n"
EX"v34,v34\n"
MF"v[92:95],a[76:79],v[64:67],v[92:95]\n"
EX"v35,v35\n"
MF"v[124:127],a[76:79],v[68:71],v[124:127]\n"
RD"a[92:95],v227 offset:23552\n"
PK"v27,v30,v31\n"
MF"v[156:159],a[76:79],v[72:75],v[156:159]\n"
EX"v40,v40\n"
MF"v[188:191],a[76:79],v[76:79],v[188:191]\n"
BL"v225,s[8:11],s43 offen offset:2048 sc1 lds\n"
MF"v[96:99],a[80:83],v[64:67],v[96:99]\n"
EX"v41,v41\n"
MF"v[128:131],a[80:83],v[68:71],v[128:131]\n"
RD"a[96:99],v227 offset:24576\n"
MF"v[160:163],a[80:83],v[72:75],v[160:163]\n"
EX"v42,v42\n"
MF"v[192:195],a[80:83],v[76:79],v[192:195]\n"
WT"lgkmcnt(2)\n"
MF"v[100:103],a[84:87],v[64:67],v[100:103]\n"
EX"v43,v43\n"
MF"v[132:135],a[84:87],v[68:71],v[132:135]\n"
RD"a[100:103],v227 offset:25600\n"
MF"v[164:167],a[84:87],v[72:75],v[164:167]\n"
EX"v48,v48\n"
MF"v[196:199],a[84:87],v[76:79],v[196:199]\n"
BL"v225,s[8:11],s43 offen offset:3072 sc1 lds\n"
MF"v[104:107],a[88:91],v[64:67],v[104:107]\n"
EX"v49,v49\n"
MF"v[136:139],a[88:91],v[68:71],v[136:139]\n"
RD"a[104:107],v227 offset:26624\n"
MF"v[168:171],a[88:91],v[72:75],v[168:171]\n"
EX"v50,v50\n"
MF"v[200:203],a[88:91],v[76:79],v[200:203]\n"
WT"lgkmcnt(2)\n"
SA"m0,s40,16384\n"
MF"v[108:111],a[92:95],v[64:67],v[108:111]\n"
EX"v51,v51\n"
MF"v[140:143],a[92:95],v[68:71],v[140:143]\n"
RD"a[108:111],v227 offset:27648\n"
MF"v[172:175],a[92:95],v[72:75],v[172:175]\n"
EX"v56,v56\n"
MF"v[204:207],a[92:95],v[76:79],v[204:207]\n"
BL"v225,s[8:11],s44 offen offset:0 sc1 lds\n"
MF"a[132:135],a[128:131],v[64:67],a[132:135]\n"
SA"s40,s40,32768\n"
EX"v57,v57\n"
MF"a[136:139],a[128:131],v[68:71],a[136:139]\n"
"s_cmp_eq_u32 s40,s46\n"
RD"a[112:115],v227 offset:28672\n"
"s_cselect_b32 s40,s47,s40\n"
MF"a[140:143],a[128:131],v[72:75],a[140:143]\n"
EX"v58,v58\n"
MF"a[144:147],a[128:131],v[76:79],a[144:147]\n"
MF"v[80:83],a[96:99],v[0:3],v[80:83]\n"
EX"v59,v59\n"
MF"v[112:115],a[96:99],v[8:11],v[112:115]\n"
RD"a[116:119],v227 offset:29696\n"
MF"v[144:147],a[96:99],v[16:19],v[144:147]\n"
EX"v36,v36\n"
MF"v[176:179],a[96:99],v[24:27],v[176:179]\n"
WT"lgkmcnt(2)\n"
BL"v225,s[8:11],s44 offen offset:1024 sc1 lds\n"
MF"v[84:87],a[100:103],v[0:3],v[84:87]\n"
EX"v37,v37\n"
MF"v[116:119],a[100:103],v[8:11],v[116:119]\n"
RD"a[120:123],v227 offset:30720\n"
MF"v[148:151],a[100:103],v[16:19],v[148:151]\n"
EX"v38,v38\n"
MF"v[180:183],a[100:103],v[24:27],v[180:183]\n"
MF"v[88:91],a[104:107],v[0:3],v[88:91]\n"
EX"v39,v39\n"
MF"v[120:123],a[104:107],v[8:11],v[120:123]\n"
RD"a[124:127],v227 offset:31744\n"
MF"v[152:155],a[104:107],v[16:19],v[152:155]\n"
EX"v44,v44\n"
MF"v[184:187],a[104:107],v[24:27],v[184:187]\n"
BL"v225,s[8:11],s44 offen offset:2048 sc1 lds\n"
MF"v[92:95],a[108:111],v[0:3],v[92:95]\n"
EX"v45,v45\n"
MF"v[124:127],a[108:111],v[8:11],v[124:127]\n"
MF"v[156:159],a[108:111],v[16:19],v[156:159]\n"
EX"v46,v46\n"
MF"v[188:191],a[108:111],v[24:27],v[188:191]\n"
WT"lgkmcnt(1)\n"
MF"v[96:99],a[112:115],v[0:3],v[96:99]\n"
WT"lgkmcnt(0)\n"
"s_barrier\n"
MF"v[128:131],a[112:115],v[8:11],v[128:131]\n"
RD"a[64:67],v226 offset:0\n"
MF"v[160:163],a[112:115],v[16:19],v[160:163]\n"
EX"v47,v47\n"
MF"v[192:195],a[112:115],v[24:27],v[192:195]\n"
BL"v225,s[8:11],s44 offen offset:3072 sc1 lds\n"
MF"v[100:103],a[116:119],v[0:3],v[100:103]\n"
EX"v52,v52\n"
MF"v[132:135],a[116:119],v[8:11],v[132:135]\n"
RD"a[68:71],v226 offset:1024\n"
MF"v[164:167],a[116:119],v[16:19],v[164:167]\n"
EX"v53,v53\n"
MF"v[196:199],a[116:119],v[24:27],v[196:199]\n"
RD"a[72:75],v226 offset:2048\n"
MF"v[104:107],a[120:123],v[0:3],v[104:107]\n"
EX"v54,v54\n"
MF"v[136:139],a[120:123],v[8:11],v[136:139]\n"
RD"a[76:79],v226 offset:3072\n"
MF"v[168:171],a[120:123],v[16:19],v[168:171]\n"
EX"v55,v55\n"
MF"v[200:203],a[120:123],v[24:27],v[200:203]\n"
MF"v[108:111],a[124:127],v[0:3],v[108:111]\n"
WT"vmcnt(6)\n"
"buffer_store_dwordx2 v[232:233],v230,s[8:11],s94 offen sc1\n"
MF"v[140:143],a[124:127],v[8:11],v[140:143]\n"
EX"v60,v60\n"
MF"v[172:175],a[124:127],v[16:19],v[172:175]\n"
RD"a[80:83],v226 offset:4096\n"
MF"v[204:207],a[124:127],v[24:27],v[204:207]\n"
EX"v61,v61\n"
MF"a[132:135],a[128:131],v[0:3],a[132:135]\n"
RD"a[84:87],v226 offset:5120\n"
MF"a[136:139],a[128:131],v[8:11],a[136:139]\n"
EX"v62,v62\n"
MF"a[140:143],a[128:131],v[16:19],a[140:143]\n"
RD"a[88:91],v226 offset:6144\n"
MF"a[144:147],a[128:131],v[24:27],a[144:147]\n"
EX"v63,v63\n"
"s_mov_b32 s57,0\n"
"s_lshl_b32 s56,s34,1\n"
"s_sub_u32 s56,s56,6\n"
"s_branch LP1_%=\n"
"LMA_%=:\n"
WT"lgkmcnt(10)\n"
MF"v[0:3],a[64:67],a[0:3],v[208:211]\n"
WT"lgkmcnt(2)\n"
"v_add_f32 v240,v240,v244\n"
PK"v64,v32,v33\n"
MF"v[0:3],a[68:71],a[4:7],v[0:3]\n"
PK"v65,v34,v35\n"
"v_add_f32 v248,v248,v252\n"
MF"v[8:11],a[68:71],a[20:23],0\n"
PK"v66,v36,v37\n"
PK"v67,v38,v39\n"
MF"v[8:11],a[64:67],a[16:19],v[8:11]\n"
"v_add_f32 v241,v241,v245\n"
PK"v68,v40,v41\n"
MF"v[16:19],a[64:67],a[32:35],0\n"
PK"v69,v42,v43\n"
"v_add_f32 v249,v249,v253\n"
MF"v[16:19],a[68:71],a[36:39],v[16:19]\n"
RD"a[92:95],v226 offset:7168\n"
PK"v70,v44,v45\n"
MF"v[24:27],a[68:71],a[52:55],0\n"
PK"v71,v46,v47\n"
"v_add_f32 v242,v242,v246\n"
MF"v[24:27],a[64:67],a[48:51],v[24:27]\n"
PK"v72,v48,v49\n"
PK"v73,v50,v51\n"
MF"v[0:3],a[72:75],a[8:11],v[0:3]\n"
"v_mov_b32 v227,v226\n"
"v_add_f32 v250,v250,v254\n"
MF"v[0:3],a[76:79],a[12:15],v[0:3]\n"
PK"v74,v52,v53\n"
PK"v75,v54,v55\n"
MF"v[8:11],a[76:79],a[28:31],v[8:11]\n"
"v_add_f32 v243,v243,v247\n"
PK"v76,v56,v57\n"
MF"v[8:11],a[72:75],a[24:27],v[8:11]\n"
PK"v77,v58,v59\n"
"v_add_f32 v251,v251,v255\n"
MF"v[16:19],a[72:75],a[40:43],v[16:19]\n"
PK"v78,v60,v61\n"
PK"v79,v62,v63\n"
MF"v[16:19],a[76:79],a[44:47],v[16:19]\n"
RD"a[96:99],v226 offset:8192\n"
PK"v240,v240,v240\n"
MF"v[24:27],a[76:79],a[60:63],v[24:27]\n"
EX"v0,v0\n"
MF"v[24:27],a[72:75],a[56:59],v[24:27]\n"
"v_lshlrev_b32 v240,16,v240\n"
PK"v241,v241,v241\n"
MF"v[4:7],a[80:83],a[0:3],v[212:215]\n"
WT"lgkmcnt(1)\n"
EX"v1,v1\n"
MF"v[4:7],a[84:87],a[4:7],v[4:7]\n"
RD"a[100:103],v226 offset:9216\n"
"v_lshlrev_b32 v241,16,v241\n"
MF"v[12:15],a[84:87],a[20:23],0\n"
EX"v2,v2\n"
MF"v[12:15],a[80:83],a[16:19],v[12:15]\n"
EX"v3,v3\n"
MF"v[20:23],a[80:83],a[32:35],0\n"
EX"v8,v8\n"
PK"v242,v242,v242\n"
MF"v[20:23],a[84:87],a[36:39],v[20:23]\n"
RD"a[104:107],v226 offset:10240\n"
"v_lshlrev_b32 v242,16,v242\n"
MF"v[28:31],a[84:87],a[52:55],0\n"
EX"v9,v9\n"
PK"v243,v243,v243\n"
MF"v[28:31],a[80:83],a[48:51],v[28:31]\n"
"v_lshlrev_b32 v243,16,v243\n"
MU"v244,0xbfb8aa3b,v240\n"
EX"v244,v244\n"
MF"v[4:7],a[88:91],a[8:11],v[4:7]\n"
EX"v10,v10\n"
MF"v[4:7],a[92:95],a[12:15],v[4:7]\n"
RD"a[108:111],v226 offset:11264\n"
MU"v245,0xbfb8aa3b,v241\n"
EX"v245,v245\n"
MF"v[12:15],a[92:95],a[28:31],v[12:15]\n"
EX"v11,v11\n"
PK"v0,v0,v1\n"
MF"v[12:15],a[88:91],a[24:27],v[12:15]\n"
EX"v16,v16\n"
MU"v246,0xbfb8aa3b,v242\n"
MF"v[20:23],a[88:91],a[40:43],v[20:23]\n"
EX"v246,v246\n"
MU"v247,0xbfb8aa3b,v243\n"
MF"v[20:23],a[92:95],a[44:47],v[20:23]\n"
EX"v17,v17\n"
PK"v1,v2,v3\n"
MF"v[28:31],a[92:95],a[60:63],v[28:31]\n"
EX"v247,v247\n"
PK"v248,v248,v248\n"
MF"v[28:31],a[88:91],a[56:59],v[28:31]\n"
WT"lgkmcnt(1)\n"
RD"a[112:115],v226 offset:12288\n"
"v_lshlrev_b32 v248,16,v248\n"
PK"v8,v8,v9\n"
MF"v[32:35],a[96:99],a[0:3],v[216:219]\n"
EX"v18,v18\n"
PK"v249,v249,v249\n"
MF"v[32:35],a[100:103],a[4:7],v[32:35]\n"
EX"v19,v19\n"
"v_lshlrev_b32 v249,16,v249\n"
MF"v[40:43],a[100:103],a[20:23],0\n"
RD"a[116:119],v226 offset:13312\n"
PK"v250,v250,v250\n"
MF"v[40:43],a[96:99],a[16:19],v[40:43]\n"
EX"v24,v24\n"
PK"v9,v10,v11\n"
MF"v[48:51],a[96:99],a[32:35],0\n"
EX"v25,v25\n"
"v_lshlrev_b32 v250,16,v250\n"
MF"v[48:51],a[100:103],a[36:39],v[48:51]\n"
EX"v26,v26\n"
PK"v16,v16,v17\n"
MF"v[56:59],a[100:103],a[52:55],0\n"
RD"a[120:123],v226 offset:14336\n"
PK"v251,v251,v251\n"
MF"v[56:59],a[96:99],a[48:51],v[56:59]\n"
EX"v27,v27\n"
"v_lshlrev_b32 v251,16,v251\n"
MF"v[32:35],a[104:107],a[8:11],v[32:35]\n"
WT"lgkmcnt(2)\n"
EX"v4,v4\n"
"v_add_f32 v244,1.0,v244\n"
MF"v[32:35],a[108:111],a[12:15],v[32:35]\n"
"v_rcp_f32 v244,v244\n"
"v_add_f32 v245,1.0,v245\n"
MF"v[40:43],a[108:111],a[28:31],v[40:43]\n"
"v_rcp_f32 v245,v245\n"
PK"v17,v18,v19\n"
MF"v[40:43],a[104:107],a[24:27],v[40:43]\n"
RD"a[124:127],v226 offset:15360\n"
"v_add_f32 v246,1.0,v246\n"
"v_rcp_f32 v246,v246\n"
MF"v[48:51],a[104:107],a[40:43],v[48:51]\n"
EX"v5,v5\n"
"v_add_f32 v247,1.0,v247\n"
MF"v[48:51],a[108:111],a[44:47],v[48:51]\n"
"v_rcp_f32 v247,v247\n"
EX"v6,v6\n"
MF"v[56:59],a[108:111],a[60:63],v[56:59]\n"
EX"v7,v7\n"
PK"v24,v24,v25\n"
MF"v[56:59],a[104:107],a[56:59],v[56:59]\n"
EX"v12,v12\n"
PK"v25,v26,v27\n"
MF"v[36:39],a[112:115],a[0:3],v[220:223]\n"
WT"lgkmcnt(1)\n"
EX"v13,v13\n"
MU"v240,v240,v244\n"
MF"v[36:39],a[116:119],a[4:7],v[36:39]\n"
RD"a[64:67],v227 offset:16384\n"
MU"v241,v241,v245\n"
MF"v[44:47],a[116:119],a[20:23],0\n"
EX"v14,v14\n"
MU"v242,v242,v246\n"
MF"v[44:47],a[112:115],a[16:19],v[44:47]\n"
MU"v243,v243,v247\n"
PK"v2,v4,v5\n"
MF"v[52:55],a[112:115],a[32:35],0\n"
EX"v15,v15\n"
PK"v240,v240,v240\n"
MF"v[52:55],a[116:119],a[36:39],v[52:55]\n"
RD"a[68:71],v227 offset:17408\n"
"v_lshlrev_b32 v240,16,v240\n"
PK"v3,v6,v7\n"
MF"v[60:63],a[116:119],a[52:55],0\n"
EX"v20,v20\n"
PK"v241,v241,v241\n"
MF"v[60:63],a[112:115],a[48:51],v[60:63]\n"
"v_readlane_b32 s43,v228,s42\n"
"v_lshlrev_b32 v241,16,v241\n"
MF"v[36:39],a[120:123],a[8:11],v[36:39]\n"
WT"lgkmcnt(1)\n"
EX"v21,v21\n"
PK"v242,v242,v242\n"
MF"v[36:39],a[124:127],a[12:15],v[36:39]\n"
RD"a[72:75],v227 offset:18432\n"
"v_lshlrev_b32 v242,16,v242\n"
PK"v10,v12,v13\n"
MF"v[44:47],a[124:127],a[28:31],v[44:47]\n"
EX"v22,v22\n"
PK"v243,v243,v243\n"
MF"v[44:47],a[120:123],a[24:27],v[44:47]\n"
"s_mov_b32 m0,s40\n"
"v_lshlrev_b32 v243,16,v243\n"
MU"v240,v240,v248\n"
MF"v[52:55],a[120:123],a[40:43],v[52:55]\n"
EX"v23,v23\n"
MF"v[52:55],a[124:127],a[44:47],v[52:55]\n"
RD"a[76:79],v227 offset:19456\n"
"v_readlane_b32 s44,v229,s42\n"
MU"v241,v241,v249\n"
MF"v[60:63],a[124:127],a[60:63],v[60:63]\n"
EX"v28,v28\n"
PK"v11,v14,v15\n"
MF"v[60:63],a[120:123],a[56:59],v[60:63]\n"
SA"s42,s42,1\n"
BL"v225,s[8:11],s43 offen offset:0 sc1 lds\n"
MU"v242,v242,v250\n"
MF"v[80:83],a[64:67],v[64:67],v[80:83]\n"
SA"s41,s41,32768\n"
EX"v29,v29\n"
MU"v243,v243,v251\n"
MF"v[112:115],a[64:67],v[68:71],v[112:115]\n"
"s_cmp_eq_u32 s41,163840\n"
RD"a[80:83],v227 offset:20480\n"
PK"v240,v240,v241\n"
PK"v241,v242,v243\n"
MF"v[144:147],a[64:67],v[72:75],v[144:147]\n"
"s_cselect_b32 s41,0,s41\n"
EX"v30,v30\n"
PK"v18,v20,v21\n"
MF"v[176:179],a[64:67],v[76:79],v[176:179]\n"
WT"lgkmcnt(2)\n"
"v_add_u32 v226,s41,v224\n"
"v_lshrrev_b32 v239,9,v224\n"
"v_lshlrev_b32 v239,8,v239\n"
MF"v[84:87],a[68:71],v[64:67],v[84:87]\n"
EX"v31,v31\n"
"v_bfe_u32 v237,v224,8,1\n"
MF"v[116:119],a[68:71],v[68:71],v[116:119]\n"
RD"a[84:87],v227 offset:21504\n"
"v_lshl_add_u32 v239,v237,3,v239\n"
PK"v19,v22,v23\n"
MF"v[148:151],a[68:71],v[72:75],v[148:151]\n"
EX"v32,v32\n"
"v_and_b32 v237,0x80,v224\n"
MF"v[180:183],a[68:71],v[76:79],v[180:183]\n"
BL"v225,s[8:11],s43 offen offset:1024 sc1 lds\n"
"v_lshl_add_u32 v239,v237,23,v239\n"
MF"v[88:91],a[72:75],v[64:67],v[88:91]\n"
EX"v33,v33\n"
"v_and_b32 v237,0x70,v224\n"
MF"v[120:123],a[72:75],v[68:71],v[120:123]\n"
RD"a[88:91],v227 offset:22528\n"
"v_add_u32 v239,v239,v237\n"
PK"v26,v28,v29\n"
MF"v[152:155],a[72:75],v[72:75],v[152:155]\n"
"buffer_store_dwordx2 v[240:241],v239,s[8:11],s93 offen sc1\n"
MF"v[184:187],a[72:75],v[76:79],v[184:187]\n"
WT"lgkmcnt(2)\n"
EX"v34,v34\n"
MF"v[92:95],a[76:79],v[64:67],v[92:95]\n"
EX"v35,v35\n"
MF"v[124:127],a[76:79],v[68:71],v[124:127]\n"
RD"a[92:95],v227 offset:23552\n"
PK"v27,v30,v31\n"
MF"v[156:159],a[76:79],v[72:75],v[156:159]\n"
EX"v40,v40\n"
MF"v[188:191],a[76:79],v[76:79],v[188:191]\n"
BL"v225,s[8:11],s43 offen offset:2048 sc1 lds\n"
MF"v[96:99],a[80:83],v[64:67],v[96:99]\n"
EX"v41,v41\n"
MF"v[128:131],a[80:83],v[68:71],v[128:131]\n"
RD"a[96:99],v227 offset:24576\n"
MF"v[160:163],a[80:83],v[72:75],v[160:163]\n"
EX"v42,v42\n"
MF"v[192:195],a[80:83],v[76:79],v[192:195]\n"
WT"lgkmcnt(2)\n"
MF"v[100:103],a[84:87],v[64:67],v[100:103]\n"
EX"v43,v43\n"
MF"v[132:135],a[84:87],v[68:71],v[132:135]\n"
RD"a[100:103],v227 offset:25600\n"
MF"v[164:167],a[84:87],v[72:75],v[164:167]\n"
EX"v48,v48\n"
MF"v[196:199],a[84:87],v[76:79],v[196:199]\n"
BL"v225,s[8:11],s43 offen offset:3072 sc1 lds\n"
MF"v[104:107],a[88:91],v[64:67],v[104:107]\n"
EX"v49,v49\n"
MF"v[136:139],a[88:91],v[68:71],v[136:139]\n"
RD"a[104:107],v227 offset:26624\n"
MF"v[168:171],a[88:91],v[72:75],v[168:171]\n"
EX"v50,v50\n"
MF"v[200:203],a[88:91],v[76:79],v[200:203]\n"
WT"lgkmcnt(2)\n"
SA"m0,s40,16384\n"
MF"v[108:111],a[92:95],v[64:67],v[108:111]\n"
EX"v51,v51\n"
MF"v[140:143],a[92:95],v[68:71],v[140:143]\n"
RD"a[108:111],v227 offset:27648\n"
MF"v[172:175],a[92:95],v[72:75],v[172:175]\n"
EX"v56,v56\n"
MF"v[204:207],a[92:95],v[76:79],v[204:207]\n"
BL"v225,s[8:11],s44 offen offset:0 sc1 lds\n"
MF"a[132:135],a[128:131],v[64:67],a[132:135]\n"
SA"s40,s40,32768\n"
EX"v57,v57\n"
MF"a[136:139],a[128:131],v[68:71],a[136:139]\n"
"s_cmp_eq_u32 s40,s46\n"
RD"a[112:115],v227 offset:28672\n"
"s_cselect_b32 s40,s47,s40\n"
MF"a[140:143],a[128:131],v[72:75],a[140:143]\n"
EX"v58,v58\n"
MF"a[144:147],a[128:131],v[76:79],a[144:147]\n"
MF"v[80:83],a[96:99],v[0:3],v[80:83]\n"
EX"v59,v59\n"
MF"v[112:115],a[96:99],v[8:11],v[112:115]\n"
RD"a[116:119],v227 offset:29696\n"
MF"v[144:147],a[96:99],v[16:19],v[144:147]\n"
EX"v36,v36\n"
MF"v[176:179],a[96:99],v[24:27],v[176:179]\n"
WT"lgkmcnt(2)\n"
BL"v225,s[8:11],s44 offen offset:1024 sc1 lds\n"
MF"v[84:87],a[100:103],v[0:3],v[84:87]\n"
EX"v37,v37\n"
MF"v[116:119],a[100:103],v[8:11],v[116:119]\n"
RD"a[120:123],v227 offset:30720\n"
MF"v[148:151],a[100:103],v[16:19],v[148:151]\n"
EX"v38,v38\n"
MF"v[180:183],a[100:103],v[24:27],v[180:183]\n"
MF"v[88:91],a[104:107],v[0:3],v[88:91]\n"
EX"v39,v39\n"
MF"v[120:123],a[104:107],v[8:11],v[120:123]\n"
RD"a[124:127],v227 offset:31744\n"
MF"v[152:155],a[104:107],v[16:19],v[152:155]\n"
EX"v44,v44\n"
MF"v[184:187],a[104:107],v[24:27],v[184:187]\n"
BL"v225,s[8:11],s44 offen offset:2048 sc1 lds\n"
MF"v[92:95],a[108:111],v[0:3],v[92:95]\n"
EX"v45,v45\n"
MF"v[124:127],a[108:111],v[8:11],v[124:127]\n"
MF"v[156:159],a[108:111],v[16:19],v[156:159]\n"
EX"v46,v46\n"
MF"v[188:191],a[108:111],v[24:27],v[188:191]\n"
WT"lgkmcnt(1)\n"
MF"v[96:99],a[112:115],v[0:3],v[96:99]\n"
WT"lgkmcnt(0)\n"
"s_barrier\n"
MF"v[128:131],a[112:115],v[8:11],v[128:131]\n"
RD"a[64:67],v226 offset:0\n"
MF"v[160:163],a[112:115],v[16:19],v[160:163]\n"
EX"v47,v47\n"
MF"v[192:195],a[112:115],v[24:27],v[192:195]\n"
BL"v225,s[8:11],s44 offen offset:3072 sc1 lds\n"
MF"v[100:103],a[116:119],v[0:3],v[100:103]\n"
EX"v52,v52\n"
MF"v[132:135],a[116:119],v[8:11],v[132:135]\n"
RD"a[68:71],v226 offset:1024\n"
MF"v[164:167],a[116:119],v[16:19],v[164:167]\n"
EX"v53,v53\n"
MF"v[196:199],a[116:119],v[24:27],v[196:199]\n"
RD"a[72:75],v226 offset:2048\n"
MF"v[104:107],a[120:123],v[0:3],v[104:107]\n"
EX"v54,v54\n"
MF"v[136:139],a[120:123],v[8:11],v[136:139]\n"
RD"a[76:79],v226 offset:3072\n"
MF"v[168:171],a[120:123],v[16:19],v[168:171]\n"
EX"v55,v55\n"
MF"v[200:203],a[120:123],v[24:27],v[200:203]\n"
MF"v[108:111],a[124:127],v[0:3],v[108:111]\n"
WT"vmcnt(6)\n"
"buffer_store_dwordx2 v[232:233],v230,s[8:11],s94 offen sc1\n"
MF"v[140:143],a[124:127],v[8:11],v[140:143]\n"
EX"v60,v60\n"
MF"v[172:175],a[124:127],v[16:19],v[172:175]\n"
RD"a[80:83],v226 offset:4096\n"
MF"v[204:207],a[124:127],v[24:27],v[204:207]\n"
EX"v61,v61\n"
MF"a[132:135],a[128:131],v[0:3],a[132:135]\n"
RD"a[84:87],v226 offset:5120\n"
MF"a[136:139],a[128:131],v[8:11],a[136:139]\n"
EX"v62,v62\n"
MF"a[140:143],a[128:131],v[16:19],a[140:143]\n"
RD"a[88:91],v226 offset:6144\n"
MF"a[144:147],a[128:131],v[24:27],a[144:147]\n"
EX"v63,v63\n"
"s_branch LDB_%=\n"
"LMD_%=:\n"
"s_cmp_eq_u32 s34,6\n"
"s_cbranch_scc1 LME_%=\n"
WT"lgkmcnt(10)\n"
MF"v[0:3],a[64:67],a[0:3],v[208:211]\n"
WT"lgkmcnt(2)\n"
"v_lshrrev_b32 v239,9,v224\n"
"v_lshlrev_b32 v239,8,v239\n"
MF"v[0:3],a[68:71],a[4:7],v[0:3]\n"
PK"v64,v32,v33\n"
PK"v65,v34,v35\n"
MF"v[8:11],a[68:71],a[20:23],0\n"
"v_bfe_u32 v237,v224,8,1\n"
PK"v66,v36,v37\n"
MF"v[8:11],a[64:67],a[16:19],v[8:11]\n"
PK"v67,v38,v39\n"
"v_lshl_add_u32 v239,v237,3,v239\n"
MF"v[16:19],a[64:67],a[32:35],0\n"
PK"v68,v40,v41\n"
PK"v69,v42,v43\n"
MF"v[16:19],a[68:71],a[36:39],v[16:19]\n"
RD"a[92:95],v226 offset:7168\n"
"v_and_b32 v237,0x80,v224\n"
MF"v[24:27],a[68:71],a[52:55],0\n"
PK"v70,v44,v45\n"
PK"v71,v46,v47\n"
MF"v[24:27],a[64:67],a[48:51],v[24:27]\n"
"v_lshl_add_u32 v239,v237,23,v239\n"
"v_not_b32 v236,v234\n"
MF"v[0:3],a[72:75],a[8:11],v[0:3]\n"
PK"v72,v48,v49\n"
PK"v73,v50,v51\n"
MF"v[0:3],a[76:79],a[12:15],v[0:3]\n"
SA"s53,s93,4292282368\n"
"v_mov_b32 v227,v226\n"
"v_and_b32 v237,15,v236\n"
MF"v[8:11],a[76:79],a[28:31],v[8:11]\n"
PK"v74,v52,v53\n"
PK"v75,v54,v55\n"
MF"v[8:11],a[72:75],a[24:27],v[8:11]\n"
"v_bfe_u32 v236,v236,4,7\n"
PK"v76,v56,v57\n"
MF"v[16:19],a[72:75],a[40:43],v[16:19]\n"
PK"v77,v58,v59\n"
"v_mul_u32_u24 v236,20480,v236\n"
MF"v[16:19],a[76:79],a[44:47],v[16:19]\n"
RD"a[96:99],v226 offset:8192\n"
PK"v78,v60,v61\n"
MF"v[24:27],a[76:79],a[60:63],v[24:27]\n"
EX"v0,v0\n"
MF"v[24:27],a[72:75],a[56:59],v[24:27]\n"
PK"v79,v62,v63\n"
"v_lshl_add_u32 v236,v237,4,v236\n"
MF"v[4:7],a[80:83],a[0:3],v[212:215]\n"
WT"lgkmcnt(1)\n"
EX"v1,v1\n"
"v_add_u32 v236,v236,v239\n"
MF"v[4:7],a[84:87],a[4:7],v[4:7]\n"
RD"a[100:103],v226 offset:9216\n"
"v_and_b32 v237,0x70,v224\n"
MF"v[12:15],a[84:87],a[20:23],0\n"
EX"v2,v2\n"
MF"v[12:15],a[80:83],a[16:19],v[12:15]\n"
EX"v3,v3\n"
"v_add_u32 v239,v239,v237\n"
MF"v[20:23],a[80:83],a[32:35],0\n"
EX"v8,v8\n"
"v_add_f32 v240,v240,v244\n"
MF"v[20:23],a[84:87],a[36:39],v[20:23]\n"
RD"a[104:107],v226 offset:10240\n"
"v_add_f32 v248,v248,v252\n"
"v_add_f32 v241,v241,v245\n"
MF"v[28:31],a[84:87],a[52:55],0\n"
EX"v9,v9\n"
"v_add_f32 v249,v249,v253\n"
MF"v[28:31],a[80:83],a[48:51],v[28:31]\n"
"v_add_f32 v242,v242,v246\n"
"v_add_f32 v250,v250,v254\n"
MF"v[4:7],a[88:91],a[8:11],v[4:7]\n"
EX"v10,v10\n"
"v_add_f32 v243,v243,v247\n"
MF"v[4:7],a[92:95],a[12:15],v[4:7]\n"
RD"a[108:111],v226 offset:11264\n"
"v_add_f32 v251,v251,v255\n"
PK"v0,v0,v1\n"
MF"v[12:15],a[92:95],a[28:31],v[12:15]\n"
"buffer_load_dwordx2 v[254:255],v236,s[8:11],s53 offen sc1\n"
"v_ashrrev_i32 v235,31,v234\n"
MF"v[12:15],a[88:91],a[24:27],v[12:15]\n"
EX"v11,v11\n"
"v_not_b32 v235,v235\n"
MF"v[20:23],a[88:91],a[40:43],v[20:23]\n"
EX"v16,v16\n"
"v_or_b32 v235,0x80000000,v235\n"
MF"v[20:23],a[92:95],a[44:47],v[20:23]\n"
EX"v17,v17\n"
PK"v1,v2,v3\n"
MF"v[28:31],a[92:95],a[60:63],v[28:31]\n"
RD"a[112:115],v226 offset:12288\n"
"v_xor_b32 v235,v235,v234\n"
"v_and_b32 v235,0xffff0000,v235\n"
MF"v[28:31],a[88:91],a[56:59],v[28:31]\n"
WT"lgkmcnt(2)\n"
EX"v18,v18\n"
MU"v235,0xbfb8aa3b,v235\n"
MF"v[32:35],a[96:99],a[0:3],v[216:219]\n"
EX"v235,v235\n"
PK"v8,v8,v9\n"
MF"v[32:35],a[100:103],a[4:7],v[32:35]\n"
EX"v19,v19\n"
"v_add_f32 v235,1.0,v235\n"
MF"v[40:43],a[100:103],a[20:23],0\n"
"v_rcp_f32 v235,v235\n"
MF"v[40:43],a[96:99],a[16:19],v[40:43]\n"
RD"a[116:119],v226 offset:13312\n"
PK"v235,v235,v235\n"
PK"v9,v10,v11\n"
MF"v[48:51],a[96:99],a[32:35],0\n"
EX"v24,v24\n"
"v_lshlrev_b32 v235,16,v235\n"
MF"v[48:51],a[100:103],a[36:39],v[48:51]\n"
EX"v25,v25\n"
"v_add_f32 v240,v240,v248\n"
MF"v[56:59],a[100:103],a[52:55],0\n"
EX"v26,v26\n"
PK"v16,v16,v17\n"
MF"v[56:59],a[96:99],a[48:51],v[56:59]\n"
RD"a[120:123],v226 offset:14336\n"
"v_add_f32 v241,v241,v249\n"
"v_add_f32 v242,v242,v250\n"
MF"v[32:35],a[104:107],a[8:11],v[32:35]\n"
WT"lgkmcnt(2)\n"
EX"v27,v27\n"
"v_add_f32 v243,v243,v251\n"
MF"v[32:35],a[108:111],a[12:15],v[32:35]\n"
EX"v4,v4\n"
PK"v240,v240,v240\n"
MF"v[40:43],a[108:111],a[28:31],v[40:43]\n"
RD"a[124:127],v226 offset:15360\n"
PK"v17,v18,v19\n"
"v_lshlrev_b32 v240,16,v240\n"
MF"v[40:43],a[104:107],a[24:27],v[40:43]\n"
EX"v5,v5\n"
MU"v240,v240,v235\n"
MF"v[48:51],a[104:107],a[40:43],v[48:51]\n"
EX"v6,v6\n"
PK"v240,v240,v240\n"
MF"v[48:51],a[108:111],a[44:47],v[48:51]\n"
EX"v7,v7\n"
PK"v24,v24,v25\n"
MF"v[56:59],a[108:111],a[60:63],v[56:59]\n"
EX"v12,v12\n"
"v_lshlrev_b32 v240,16,v240\n"
MF"v[56:59],a[104:107],a[56:59],v[56:59]\n"
PK"v241,v241,v241\n"
"v_lshlrev_b32 v241,16,v241\n"
PK"v25,v26,v27\n"
MF"v[36:39],a[112:115],a[0:3],v[220:223]\n"
WT"lgkmcnt(1)\n"
EX"v13,v13\n"
MU"v241,v241,v235\n"
MF"v[36:39],a[116:119],a[4:7],v[36:39]\n"
RD"a[64:67],v227 offset:16384\n"
PK"v241,v241,v241\n"
MF"v[44:47],a[116:119],a[20:23],0\n"
EX"v14,v14\n"
"v_lshlrev_b32 v241,16,v241\n"
MF"v[44:47],a[112:115],a[16:19],v[44:47]\n"
PK"v242,v242,v242\n"
PK"v2,v4,v5\n"
MF"v[52:55],a[112:115],a[32:35],0\n"
EX"v15,v15\n"
"v_lshlrev_b32 v242,16,v242\n"
MF"v[52:55],a[116:119],a[36:39],v[52:55]\n"
RD"a[68:71],v227 offset:17408\n"
MU"v242,v242,v235\n"
PK"v3,v6,v7\n"
MF"v[60:63],a[116:119],a[52:55],0\n"
EX"v20,v20\n"
PK"v242,v242,v242\n"
MF"v[60:63],a[112:115],a[48:51],v[60:63]\n"
"v_readlane_b32 s43,v228,s42\n"
"v_lshlrev_b32 v242,16,v242\n"
MF"v[36:39],a[120:123],a[8:11],v[36:39]\n"
WT"lgkmcnt(1)\n"
EX"v21,v21\n"
PK"v243,v243,v243\n"
MF"v[36:39],a[124:127],a[12:15],v[36:39]\n"
RD"a[72:75],v227 offset:18432\n"
"v_lshlrev_b32 v243,16,v243\n"
PK"v10,v12,v13\n"
MF"v[44:47],a[124:127],a[28:31],v[44:47]\n"
EX"v22,v22\n"
MU"v243,v243,v235\n"
MF"v[44:47],a[120:123],a[24:27],v[44:47]\n"
"s_mov_b32 m0,s40\n"
PK"v243,v243,v243\n"
"v_lshlrev_b32 v243,16,v243\n"
MF"v[52:55],a[120:123],a[40:43],v[52:55]\n"
EX"v23,v23\n"
MF"v[52:55],a[124:127],a[44:47],v[52:55]\n"
RD"a[76:79],v227 offset:19456\n"
"v_readlane_b32 s44,v229,s42\n"
PK"v11,v14,v15\n"
MF"v[60:63],a[124:127],a[60:63],v[60:63]\n"
EX"v28,v28\n"
MF"v[60:63],a[120:123],a[56:59],v[60:63]\n"
SA"s42,s42,1\n"
BL"v225,s[8:11],s43 offen offset:0 sc1 lds\n"
MF"v[80:83],a[64:67],v[64:67],v[80:83]\n"
SA"s41,s41,32768\n"
EX"v29,v29\n"
MF"v[112:115],a[64:67],v[68:71],v[112:115]\n"
"s_cmp_eq_u32 s41,163840\n"
RD"a[80:83],v227 offset:20480\n"
PK"v18,v20,v21\n"
MF"v[144:147],a[64:67],v[72:75],v[144:147]\n"
"s_cselect_b32 s41,0,s41\n"
EX"v30,v30\n"
MF"v[176:179],a[64:67],v[76:79],v[176:179]\n"
WT"lgkmcnt(2)\n"
"v_add_u32 v226,s41,v224\n"
PK"v19,v22,v23\n"
MF"v[84:87],a[68:71],v[64:67],v[84:87]\n"
EX"v31,v31\n"
MF"v[116:119],a[68:71],v[68:71],v[116:119]\n"
RD"a[84:87],v227 offset:21504\n"
PK"v26,v28,v29\n"
MF"v[148:151],a[68:71],v[72:75],v[148:151]\n"
EX"v32,v32\n"
MF"v[180:183],a[68:71],v[76:79],v[180:183]\n"
BL"v225,s[8:11],s43 offen offset:1024 sc1 lds\n"
MF"v[88:91],a[72:75],v[64:67],v[88:91]\n"
EX"v33,v33\n"
MF"v[120:123],a[72:75],v[68:71],v[120:123]\n"
RD"a[88:91],v227 offset:22528\n"
PK"v27,v30,v31\n"
MF"v[152:155],a[72:75],v[72:75],v[152:155]\n"
EX"v34,v34\n"
MF"v[184:187],a[72:75],v[76:79],v[184:187]\n"
WT"lgkmcnt(2)\n"
WT"vmcnt(2)\n"
"v_lshlrev_b32 v244,16,v254\n"
"v_and_b32 v245,0xffff0000,v254\n"
MF"v[92:95],a[76:79],v[64:67],v[92:95]\n"
EX"v35,v35\n"
"v_lshlrev_b32 v246,16,v255\n"
MF"v[124:127],a[76:79],v[68:71],v[124:127]\n"
RD"a[92:95],v227 offset:23552\n"
"v_and_b32 v247,0xffff0000,v255\n"
"v_add_f32 v240,v240,v244\n"
MF"v[156:159],a[76:79],v[72:75],v[156:159]\n"
EX"v40,v40\n"
"v_add_f32 v241,v241,v245\n"
MF"v[188:191],a[76:79],v[76:79],v[188:191]\n"
BL"v225,s[8:11],s43 offen offset:2048 sc1 lds\n"
"v_add_f32 v242,v242,v246\n"
MF"v[96:99],a[80:83],v[64:67],v[96:99]\n"
EX"v41,v41\n"
"v_add_f32 v243,v243,v247\n"
MF"v[128:131],a[80:83],v[68:71],v[128:131]\n"
RD"a[96:99],v227 offset:24576\n"
PK"v240,v240,v241\n"
PK"v241,v242,v243\n"
MF"v[160:163],a[80:83],v[72:75],v[160:163]\n"
"buffer_store_dwordx2 v[240:241],v239,s[8:11],s93 offen sc1\n"
MF"v[192:195],a[80:83],v[76:79],v[192:195]\n"
WT"lgkmcnt(2)\n"
EX"v42,v42\n"
MF"v[100:103],a[84:87],v[64:67],v[100:103]\n"
EX"v43,v43\n"
MF"v[132:135],a[84:87],v[68:71],v[132:135]\n"
RD"a[100:103],v227 offset:25600\n"
MF"v[164:167],a[84:87],v[72:75],v[164:167]\n"
EX"v48,v48\n"
MF"v[196:199],a[84:87],v[76:79],v[196:199]\n"
BL"v225,s[8:11],s43 offen offset:3072 sc1 lds\n"
MF"v[104:107],a[88:91],v[64:67],v[104:107]\n"
EX"v49,v49\n"
MF"v[136:139],a[88:91],v[68:71],v[136:139]\n"
RD"a[104:107],v227 offset:26624\n"
MF"v[168:171],a[88:91],v[72:75],v[168:171]\n"
EX"v50,v50\n"
MF"v[200:203],a[88:91],v[76:79],v[200:203]\n"
WT"lgkmcnt(2)\n"
SA"m0,s40,16384\n"
MF"v[108:111],a[92:95],v[64:67],v[108:111]\n"
EX"v51,v51\n"
MF"v[140:143],a[92:95],v[68:71],v[140:143]\n"
RD"a[108:111],v227 offset:27648\n"
MF"v[172:175],a[92:95],v[72:75],v[172:175]\n"
EX"v56,v56\n"
MF"v[204:207],a[92:95],v[76:79],v[204:207]\n"
BL"v225,s[8:11],s44 offen offset:0 sc1 lds\n"
MF"a[132:135],a[128:131],v[64:67],a[132:135]\n"
SA"s40,s40,32768\n"
EX"v57,v57\n"
MF"a[136:139],a[128:131],v[68:71],a[136:139]\n"
"s_cmp_eq_u32 s40,s46\n"
RD"a[112:115],v227 offset:28672\n"
"s_cselect_b32 s40,s47,s40\n"
MF"a[140:143],a[128:131],v[72:75],a[140:143]\n"
EX"v58,v58\n"
MF"a[144:147],a[128:131],v[76:79],a[144:147]\n"
MF"v[80:83],a[96:99],v[0:3],v[80:83]\n"
EX"v59,v59\n"
MF"v[112:115],a[96:99],v[8:11],v[112:115]\n"
RD"a[116:119],v227 offset:29696\n"
MF"v[144:147],a[96:99],v[16:19],v[144:147]\n"
EX"v36,v36\n"
MF"v[176:179],a[96:99],v[24:27],v[176:179]\n"
WT"lgkmcnt(2)\n"
BL"v225,s[8:11],s44 offen offset:1024 sc1 lds\n"
MF"v[84:87],a[100:103],v[0:3],v[84:87]\n"
EX"v37,v37\n"
MF"v[116:119],a[100:103],v[8:11],v[116:119]\n"
RD"a[120:123],v227 offset:30720\n"
MF"v[148:151],a[100:103],v[16:19],v[148:151]\n"
EX"v38,v38\n"
MF"v[180:183],a[100:103],v[24:27],v[180:183]\n"
MF"v[88:91],a[104:107],v[0:3],v[88:91]\n"
EX"v39,v39\n"
MF"v[120:123],a[104:107],v[8:11],v[120:123]\n"
RD"a[124:127],v227 offset:31744\n"
MF"v[152:155],a[104:107],v[16:19],v[152:155]\n"
EX"v44,v44\n"
MF"v[184:187],a[104:107],v[24:27],v[184:187]\n"
BL"v225,s[8:11],s44 offen offset:2048 sc1 lds\n"
MF"v[92:95],a[108:111],v[0:3],v[92:95]\n"
EX"v45,v45\n"
MF"v[124:127],a[108:111],v[8:11],v[124:127]\n"
MF"v[156:159],a[108:111],v[16:19],v[156:159]\n"
EX"v46,v46\n"
MF"v[188:191],a[108:111],v[24:27],v[188:191]\n"
WT"lgkmcnt(1)\n"
MF"v[96:99],a[112:115],v[0:3],v[96:99]\n"
WT"lgkmcnt(0)\n"
"s_barrier\n"
MF"v[128:131],a[112:115],v[8:11],v[128:131]\n"
RD"a[64:67],v226 offset:0\n"
MF"v[160:163],a[112:115],v[16:19],v[160:163]\n"
EX"v47,v47\n"
MF"v[192:195],a[112:115],v[24:27],v[192:195]\n"
BL"v225,s[8:11],s44 offen offset:3072 sc1 lds\n"
MF"v[100:103],a[116:119],v[0:3],v[100:103]\n"
EX"v52,v52\n"
MF"v[132:135],a[116:119],v[8:11],v[132:135]\n"
RD"a[68:71],v226 offset:1024\n"
MF"v[164:167],a[116:119],v[16:19],v[164:167]\n"
EX"v53,v53\n"
MF"v[196:199],a[116:119],v[24:27],v[196:199]\n"
RD"a[72:75],v226 offset:2048\n"
MF"v[104:107],a[120:123],v[0:3],v[104:107]\n"
EX"v54,v54\n"
MF"v[136:139],a[120:123],v[8:11],v[136:139]\n"
RD"a[76:79],v226 offset:3072\n"
MF"v[168:171],a[120:123],v[16:19],v[168:171]\n"
EX"v55,v55\n"
MF"v[200:203],a[120:123],v[24:27],v[200:203]\n"
MF"v[108:111],a[124:127],v[0:3],v[108:111]\n"
WT"vmcnt(5)\n"
"buffer_store_dwordx2 v[232:233],v230,s[8:11],s94 offen sc1\n"
MF"v[140:143],a[124:127],v[8:11],v[140:143]\n"
EX"v60,v60\n"
MF"v[172:175],a[124:127],v[16:19],v[172:175]\n"
RD"a[80:83],v226 offset:4096\n"
MF"v[204:207],a[124:127],v[24:27],v[204:207]\n"
EX"v61,v61\n"
MF"a[132:135],a[128:131],v[0:3],a[132:135]\n"
RD"a[84:87],v226 offset:5120\n"
MF"a[136:139],a[128:131],v[8:11],a[136:139]\n"
EX"v62,v62\n"
MF"a[140:143],a[128:131],v[16:19],a[140:143]\n"
RD"a[88:91],v226 offset:6144\n"
MF"a[144:147],a[128:131],v[24:27],a[144:147]\n"
EX"v63,v63\n"
"s_branch LDB_%=\n"
"LME_%=:\n"
WT"lgkmcnt(10)\n"
MF"v[0:3],a[64:67],a[0:3],0\n"
WT"lgkmcnt(2)\n"
"v_lshrrev_b32 v239,9,v224\n"
"v_lshlrev_b32 v239,8,v239\n"
MF"v[0:3],a[68:71],a[4:7],v[0:3]\n"
PK"v64,v32,v33\n"
PK"v65,v34,v35\n"
MF"v[8:11],a[68:71],a[20:23],0\n"
"v_bfe_u32 v237,v224,8,1\n"
PK"v66,v36,v37\n"
MF"v[8:11],a[64:67],a[16:19],v[8:11]\n"
PK"v67,v38,v39\n"
"v_lshl_add_u32 v239,v237,3,v239\n"
MF"v[16:19],a[64:67],a[32:35],0\n"
PK"v68,v40,v41\n"
PK"v69,v42,v43\n"
MF"v[16:19],a[68:71],a[36:39],v[16:19]\n"
RD"a[92:95],v226 offset:7168\n"
"v_and_b32 v237,0x80,v224\n"
MF"v[24:27],a[68:71],a[52:55],0\n"
PK"v70,v44,v45\n"
PK"v71,v46,v47\n"
MF"v[24:27],a[64:67],a[48:51],v[24:27]\n"
"v_lshl_add_u32 v239,v237,23,v239\n"
"v_not_b32 v236,v234\n"
MF"v[0:3],a[72:75],a[8:11],v[0:3]\n"
PK"v72,v48,v49\n"
PK"v73,v50,v51\n"
MF"v[0:3],a[76:79],a[12:15],v[0:3]\n"
SA"s53,s93,4292282368\n"
"v_mov_b32 v227,v226\n"
"v_and_b32 v237,15,v236\n"
MF"v[8:11],a[76:79],a[28:31],v[8:11]\n"
PK"v74,v52,v53\n"
PK"v75,v54,v55\n"
MF"v[8:11],a[72:75],a[24:27],v[8:11]\n"
"v_bfe_u32 v236,v236,4,7\n"
PK"v76,v56,v57\n"
MF"v[16:19],a[72:75],a[40:43],v[16:19]\n"
PK"v77,v58,v59\n"
"v_mul_u32_u24 v236,20480,v236\n"
MF"v[16:19],a[76:79],a[44:47],v[16:19]\n"
RD"a[96:99],v226 offset:8192\n"
PK"v78,v60,v61\n"
MF"v[24:27],a[76:79],a[60:63],v[24:27]\n"
EX"v0,v0\n"
MF"v[24:27],a[72:75],a[56:59],v[24:27]\n"
PK"v79,v62,v63\n"
"v_lshl_add_u32 v236,v237,4,v236\n"
MF"v[4:7],a[80:83],a[0:3],0\n"
WT"lgkmcnt(1)\n"
EX"v1,v1\n"
"v_add_u32 v236,v236,v239\n"
MF"v[4:7],a[84:87],a[4:7],v[4:7]\n"
RD"a[100:103],v226 offset:9216\n"
"v_and_b32 v237,0x70,v224\n"
MF"v[12:15],a[84:87],a[20:23],0\n"
EX"v2,v2\n"
MF"v[12:15],a[80:83],a[16:19],v[12:15]\n"
EX"v3,v3\n"
"v_add_u32 v239,v239,v237\n"
MF"v[20:23],a[80:83],a[32:35],0\n"
EX"v8,v8\n"
"v_add_f32 v240,v240,v244\n"
MF"v[20:23],a[84:87],a[36:39],v[20:23]\n"
RD"a[104:107],v226 offset:10240\n"
"v_add_f32 v248,v248,v252\n"
"v_add_f32 v241,v241,v245\n"
MF"v[28:31],a[84:87],a[52:55],0\n"
EX"v9,v9\n"
"v_add_f32 v249,v249,v253\n"
MF"v[28:31],a[80:83],a[48:51],v[28:31]\n"
"v_add_f32 v242,v242,v246\n"
"v_add_f32 v250,v250,v254\n"
MF"v[4:7],a[88:91],a[8:11],v[4:7]\n"
EX"v10,v10\n"
"v_add_f32 v243,v243,v247\n"
MF"v[4:7],a[92:95],a[12:15],v[4:7]\n"
RD"a[108:111],v226 offset:11264\n"
"v_add_f32 v251,v251,v255\n"
PK"v0,v0,v1\n"
MF"v[12:15],a[92:95],a[28:31],v[12:15]\n"
"buffer_load_dwordx2 v[254:255],v236,s[8:11],s53 offen sc1\n"
"v_ashrrev_i32 v235,31,v234\n"
MF"v[12:15],a[88:91],a[24:27],v[12:15]\n"
EX"v11,v11\n"
"v_not_b32 v235,v235\n"
MF"v[20:23],a[88:91],a[40:43],v[20:23]\n"
EX"v16,v16\n"
"v_or_b32 v235,0x80000000,v235\n"
MF"v[20:23],a[92:95],a[44:47],v[20:23]\n"
EX"v17,v17\n"
PK"v1,v2,v3\n"
MF"v[28:31],a[92:95],a[60:63],v[28:31]\n"
RD"a[112:115],v226 offset:12288\n"
"v_xor_b32 v235,v235,v234\n"
"v_and_b32 v235,0xffff0000,v235\n"
MF"v[28:31],a[88:91],a[56:59],v[28:31]\n"
WT"lgkmcnt(2)\n"
EX"v18,v18\n"
MU"v235,0xbfb8aa3b,v235\n"
MF"v[32:35],a[96:99],a[0:3],0\n"
EX"v235,v235\n"
PK"v8,v8,v9\n"
MF"v[32:35],a[100:103],a[4:7],v[32:35]\n"
EX"v19,v19\n"
"v_add_f32 v235,1.0,v235\n"
MF"v[40:43],a[100:103],a[20:23],0\n"
"v_rcp_f32 v235,v235\n"
MF"v[40:43],a[96:99],a[16:19],v[40:43]\n"
RD"a[116:119],v226 offset:13312\n"
PK"v235,v235,v235\n"
PK"v9,v10,v11\n"
MF"v[48:51],a[96:99],a[32:35],0\n"
EX"v24,v24\n"
"v_lshlrev_b32 v235,16,v235\n"
MF"v[48:51],a[100:103],a[36:39],v[48:51]\n"
EX"v25,v25\n"
"v_add_f32 v240,v240,v248\n"
MF"v[56:59],a[100:103],a[52:55],0\n"
EX"v26,v26\n"
PK"v16,v16,v17\n"
MF"v[56:59],a[96:99],a[48:51],v[56:59]\n"
RD"a[120:123],v226 offset:14336\n"
"v_add_f32 v241,v241,v249\n"
"v_add_f32 v242,v242,v250\n"
MF"v[32:35],a[104:107],a[8:11],v[32:35]\n"
WT"lgkmcnt(2)\n"
EX"v27,v27\n"
"v_add_f32 v243,v243,v251\n"
MF"v[32:35],a[108:111],a[12:15],v[32:35]\n"
EX"v4,v4\n"
PK"v240,v240,v240\n"
MF"v[40:43],a[108:111],a[28:31],v[40:43]\n"
RD"a[124:127],v226 offset:15360\n"
PK"v17,v18,v19\n"
"v_lshlrev_b32 v240,16,v240\n"
MF"v[40:43],a[104:107],a[24:27],v[40:43]\n"
EX"v5,v5\n"
MU"v240,v240,v235\n"
MF"v[48:51],a[104:107],a[40:43],v[48:51]\n"
EX"v6,v6\n"
PK"v240,v240,v240\n"
MF"v[48:51],a[108:111],a[44:47],v[48:51]\n"
EX"v7,v7\n"
PK"v24,v24,v25\n"
MF"v[56:59],a[108:111],a[60:63],v[56:59]\n"
EX"v12,v12\n"
"v_lshlrev_b32 v240,16,v240\n"
MF"v[56:59],a[104:107],a[56:59],v[56:59]\n"
PK"v241,v241,v241\n"
"v_lshlrev_b32 v241,16,v241\n"
PK"v25,v26,v27\n"
MF"v[36:39],a[112:115],a[0:3],0\n"
WT"lgkmcnt(1)\n"
EX"v13,v13\n"
MU"v241,v241,v235\n"
MF"v[36:39],a[116:119],a[4:7],v[36:39]\n"
RD"a[64:67],v227 offset:16384\n"
PK"v241,v241,v241\n"
MF"v[44:47],a[116:119],a[20:23],0\n"
EX"v14,v14\n"
"v_lshlrev_b32 v241,16,v241\n"
MF"v[44:47],a[112:115],a[16:19],v[44:47]\n"
PK"v242,v242,v242\n"
PK"v2,v4,v5\n"
MF"v[52:55],a[112:115],a[32:35],0\n"
EX"v15,v15\n"
"v_lshlrev_b32 v242,16,v242\n"
MF"v[52:55],a[116:119],a[36:39],v[52:55]\n"
RD"a[68:71],v227 offset:17408\n"
MU"v242,v242,v235\n"
PK"v3,v6,v7\n"
MF"v[60:63],a[116:119],a[52:55],0\n"
EX"v20,v20\n"
PK"v242,v242,v242\n"
MF"v[60:63],a[112:115],a[48:51],v[60:63]\n"
"v_readlane_b32 s43,v228,s42\n"
"v_lshlrev_b32 v242,16,v242\n"
MF"v[36:39],a[120:123],a[8:11],v[36:39]\n"
WT"lgkmcnt(1)\n"
EX"v21,v21\n"
PK"v243,v243,v243\n"
MF"v[36:39],a[124:127],a[12:15],v[36:39]\n"
RD"a[72:75],v227 offset:18432\n"
"v_lshlrev_b32 v243,16,v243\n"
PK"v10,v12,v13\n"
MF"v[44:47],a[124:127],a[28:31],v[44:47]\n"
EX"v22,v22\n"
MU"v243,v243,v235\n"
MF"v[44:47],a[120:123],a[24:27],v[44:47]\n"
"s_mov_b32 m0,s40\n"
PK"v243,v243,v243\n"
"v_lshlrev_b32 v243,16,v243\n"
MF"v[52:55],a[120:123],a[40:43],v[52:55]\n"
EX"v23,v23\n"
MF"v[52:55],a[124:127],a[44:47],v[52:55]\n"
RD"a[76:79],v227 offset:19456\n"
"v_readlane_b32 s44,v229,s42\n"
PK"v11,v14,v15\n"
MF"v[60:63],a[124:127],a[60:63],v[60:63]\n"
EX"v28,v28\n"
MF"v[60:63],a[120:123],a[56:59],v[60:63]\n"
SA"s42,s42,1\n"
BL"v225,s[8:11],s43 offen offset:0 sc1 lds\n"
MF"v[80:83],a[64:67],v[64:67],v[80:83]\n"
SA"s41,s41,32768\n"
EX"v29,v29\n"
MF"v[112:115],a[64:67],v[68:71],v[112:115]\n"
"s_cmp_eq_u32 s41,163840\n"
RD"a[80:83],v227 offset:20480\n"
PK"v18,v20,v21\n"
MF"v[144:147],a[64:67],v[72:75],v[144:147]\n"
"s_cselect_b32 s41,0,s41\n"
EX"v30,v30\n"
MF"v[176:179],a[64:67],v[76:79],v[176:179]\n"
WT"lgkmcnt(2)\n"
"v_add_u32 v226,s41,v224\n"
PK"v19,v22,v23\n"
MF"v[84:87],a[68:71],v[64:67],v[84:87]\n"
EX"v31,v31\n"
MF"v[116:119],a[68:71],v[68:71],v[116:119]\n"
RD"a[84:87],v227 offset:21504\n"
PK"v26,v28,v29\n"
MF"v[148:151],a[68:71],v[72:75],v[148:151]\n"
EX"v32,v32\n"
MF"v[180:183],a[68:71],v[76:79],v[180:183]\n"
BL"v225,s[8:11],s43 offen offset:1024 sc1 lds\n"
MF"v[88:91],a[72:75],v[64:67],v[88:91]\n"
EX"v33,v33\n"
MF"v[120:123],a[72:75],v[68:71],v[120:123]\n"
RD"a[88:91],v227 offset:22528\n"
PK"v27,v30,v31\n"
MF"v[152:155],a[72:75],v[72:75],v[152:155]\n"
EX"v34,v34\n"
MF"v[184:187],a[72:75],v[76:79],v[184:187]\n"
WT"lgkmcnt(2)\n"
WT"vmcnt(2)\n"
"v_lshlrev_b32 v244,16,v254\n"
"v_and_b32 v245,0xffff0000,v254\n"
MF"v[92:95],a[76:79],v[64:67],v[92:95]\n"
EX"v35,v35\n"
"v_lshlrev_b32 v246,16,v255\n"
MF"v[124:127],a[76:79],v[68:71],v[124:127]\n"
RD"a[92:95],v227 offset:23552\n"
"v_and_b32 v247,0xffff0000,v255\n"
"v_add_f32 v240,v240,v244\n"
MF"v[156:159],a[76:79],v[72:75],v[156:159]\n"
EX"v40,v40\n"
"v_add_f32 v241,v241,v245\n"
MF"v[188:191],a[76:79],v[76:79],v[188:191]\n"
BL"v225,s[8:11],s43 offen offset:2048 sc1 lds\n"
"v_add_f32 v242,v242,v246\n"
MF"v[96:99],a[80:83],v[64:67],v[96:99]\n"
EX"v41,v41\n"
"v_add_f32 v243,v243,v247\n"
MF"v[128:131],a[80:83],v[68:71],v[128:131]\n"
RD"a[96:99],v227 offset:24576\n"
PK"v240,v240,v241\n"
PK"v241,v242,v243\n"
MF"v[160:163],a[80:83],v[72:75],v[160:163]\n"
"buffer_store_dwordx2 v[240:241],v239,s[8:11],s93 offen sc1\n"
MF"v[192:195],a[80:83],v[76:79],v[192:195]\n"
WT"lgkmcnt(2)\n"
EX"v42,v42\n"
MF"v[100:103],a[84:87],v[64:67],v[100:103]\n"
EX"v43,v43\n"
MF"v[132:135],a[84:87],v[68:71],v[132:135]\n"
RD"a[100:103],v227 offset:25600\n"
MF"v[164:167],a[84:87],v[72:75],v[164:167]\n"
EX"v48,v48\n"
MF"v[196:199],a[84:87],v[76:79],v[196:199]\n"
BL"v225,s[8:11],s43 offen offset:3072 sc1 lds\n"
MF"v[104:107],a[88:91],v[64:67],v[104:107]\n"
EX"v49,v49\n"
MF"v[136:139],a[88:91],v[68:71],v[136:139]\n"
RD"a[104:107],v227 offset:26624\n"
MF"v[168:171],a[88:91],v[72:75],v[168:171]\n"
EX"v50,v50\n"
MF"v[200:203],a[88:91],v[76:79],v[200:203]\n"
WT"lgkmcnt(2)\n"
SA"m0,s40,16384\n"
MF"v[108:111],a[92:95],v[64:67],v[108:111]\n"
EX"v51,v51\n"
MF"v[140:143],a[92:95],v[68:71],v[140:143]\n"
RD"a[108:111],v227 offset:27648\n"
MF"v[172:175],a[92:95],v[72:75],v[172:175]\n"
EX"v56,v56\n"
MF"v[204:207],a[92:95],v[76:79],v[204:207]\n"
BL"v225,s[8:11],s44 offen offset:0 sc1 lds\n"
MF"a[132:135],a[128:131],v[64:67],a[132:135]\n"
SA"s40,s40,32768\n"
EX"v57,v57\n"
MF"a[136:139],a[128:131],v[68:71],a[136:139]\n"
"s_cmp_eq_u32 s40,s46\n"
RD"a[112:115],v227 offset:28672\n"
"s_cselect_b32 s40,s47,s40\n"
MF"a[140:143],a[128:131],v[72:75],a[140:143]\n"
EX"v58,v58\n"
MF"a[144:147],a[128:131],v[76:79],a[144:147]\n"
MF"v[80:83],a[96:99],v[0:3],v[80:83]\n"
EX"v59,v59\n"
MF"v[112:115],a[96:99],v[8:11],v[112:115]\n"
RD"a[116:119],v227 offset:29696\n"
MF"v[144:147],a[96:99],v[16:19],v[144:147]\n"
EX"v36,v36\n"
MF"v[176:179],a[96:99],v[24:27],v[176:179]\n"
WT"lgkmcnt(2)\n"
BL"v225,s[8:11],s44 offen offset:1024 sc1 lds\n"
MF"v[84:87],a[100:103],v[0:3],v[84:87]\n"
EX"v37,v37\n"
MF"v[116:119],a[100:103],v[8:11],v[116:119]\n"
RD"a[120:123],v227 offset:30720\n"
MF"v[148:151],a[100:103],v[16:19],v[148:151]\n"
EX"v38,v38\n"
MF"v[180:183],a[100:103],v[24:27],v[180:183]\n"
MF"v[88:91],a[104:107],v[0:3],v[88:91]\n"
EX"v39,v39\n"
MF"v[120:123],a[104:107],v[8:11],v[120:123]\n"
RD"a[124:127],v227 offset:31744\n"
MF"v[152:155],a[104:107],v[16:19],v[152:155]\n"
EX"v44,v44\n"
MF"v[184:187],a[104:107],v[24:27],v[184:187]\n"
BL"v225,s[8:11],s44 offen offset:2048 sc1 lds\n"
MF"v[92:95],a[108:111],v[0:3],v[92:95]\n"
EX"v45,v45\n"
MF"v[124:127],a[108:111],v[8:11],v[124:127]\n"
MF"v[156:159],a[108:111],v[16:19],v[156:159]\n"
EX"v46,v46\n"
MF"v[188:191],a[108:111],v[24:27],v[188:191]\n"
WT"lgkmcnt(1)\n"
MF"v[96:99],a[112:115],v[0:3],v[96:99]\n"
WT"lgkmcnt(0)\n"
"s_barrier\n"
MF"v[128:131],a[112:115],v[8:11],v[128:131]\n"
RD"a[64:67],v226 offset:0\n"
MF"v[160:163],a[112:115],v[16:19],v[160:163]\n"
EX"v47,v47\n"
MF"v[192:195],a[112:115],v[24:27],v[192:195]\n"
BL"v225,s[8:11],s44 offen offset:3072 sc1 lds\n"
MF"v[100:103],a[116:119],v[0:3],v[100:103]\n"
EX"v52,v52\n"
MF"v[132:135],a[116:119],v[8:11],v[132:135]\n"
RD"a[68:71],v226 offset:1024\n"
MF"v[164:167],a[116:119],v[16:19],v[164:167]\n"
EX"v53,v53\n"
MF"v[196:199],a[116:119],v[24:27],v[196:199]\n"
RD"a[72:75],v226 offset:2048\n"
MF"v[104:107],a[120:123],v[0:3],v[104:107]\n"
EX"v54,v54\n"
MF"v[136:139],a[120:123],v[8:11],v[136:139]\n"
RD"a[76:79],v226 offset:3072\n"
MF"v[168:171],a[120:123],v[16:19],v[168:171]\n"
EX"v55,v55\n"
MF"v[200:203],a[120:123],v[24:27],v[200:203]\n"
MF"v[108:111],a[124:127],v[0:3],v[108:111]\n"
WT"vmcnt(5)\n"
"buffer_store_dwordx2 v[232:233],v230,s[8:11],s94 offen sc1\n"
MF"v[140:143],a[124:127],v[8:11],v[140:143]\n"
EX"v60,v60\n"
MF"v[172:175],a[124:127],v[16:19],v[172:175]\n"
RD"a[80:83],v226 offset:4096\n"
MF"v[204:207],a[124:127],v[24:27],v[204:207]\n"
EX"v61,v61\n"
MF"a[132:135],a[128:131],v[0:3],a[132:135]\n"
RD"a[84:87],v226 offset:5120\n"
MF"a[136:139],a[128:131],v[8:11],a[136:139]\n"
EX"v62,v62\n"
MF"a[140:143],a[128:131],v[16:19],a[140:143]\n"
RD"a[88:91],v226 offset:6144\n"
MF"a[144:147],a[128:131],v[24:27],a[144:147]\n"
EX"v63,v63\n"
"s_mov_b32 s57,0\n"
"s_lshl_b32 s56,s34,1\n"
"s_sub_u32 s56,s56,12\n"
"s_branch LP1_%=\n"
"LRT_%=:\n"
WT"lgkmcnt(6)\n"
MF"v[0:3],a[64:67],a[0:3],0\n"
WT"lgkmcnt(5)\n"
"v_lshrrev_b32 v242,1,v224\n"
PK"v64,v32,v33\n"
MF"v[0:3],a[68:71],a[4:7],v[0:3]\n"
"s_mov_b32 s12,s74\n"
PK"v65,v34,v35\n"
"v_and_b32 v242,56,v242\n"
MF"v[8:11],a[68:71],a[20:23],0\n"
"s_and_b32 s13,s75,0xffff\n"
PK"v66,v36,v37\n"
PK"v67,v38,v39\n"
MF"v[8:11],a[64:67],a[16:19],v[8:11]\n"
"s_mov_b32 s14,5242880\n"
PK"v68,v40,v41\n"
PK"v69,v42,v43\n"
MF"v[16:19],a[64:67],a[32:35],0\n"
"s_lshl_b32 s23,s35,6\n"
PK"v70,v44,v45\n"
PK"v71,v46,v47\n"
MF"v[16:19],a[68:71],a[36:39],v[16:19]\n"
"s_mov_b32 s15,0x20000\n"
RD"a[92:95],v226 offset:7168\n"
"v_add_u32 v243,s40,v252\n"
MF"v[24:27],a[68:71],a[52:55],0\n"
SA"s23,s23,4352\n"
PK"v72,v48,v49\n"
PK"v73,v50,v51\n"
MF"v[24:27],a[64:67],a[48:51],v[24:27]\n"
WT"lgkmcnt(1)\n"
"buffer_load_dwordx2 v[244:245],v242,s[8:11],s23 offen sc1\n"
MF"v[0:3],a[72:75],a[8:11],v[0:3]\n"
SA"s19,s40,32768\n"
"v_mov_b32 v227,v226\n"
PK"v74,v52,v53\n"
MF"v[0:3],a[76:79],a[12:15],v[0:3]\n"
"s_cmp_eq_u32 s19,s46\n"
PK"v75,v54,v55\n"
PK"v76,v56,v57\n"
MF"v[8:11],a[76:79],a[28:31],v[8:11]\n"
"s_cselect_b32 s19,s47,s19\n"
PK"v77,v58,v59\n"
PK"v78,v60,v61\n"
MF"v[8:11],a[72:75],a[24:27],v[8:11]\n"
"s_cmp_eq_u32 s20,0\n"
PK"v79,v62,v63\n"
"s_cselect_b32 s17,2130706432,s17\n"
MF"v[16:19],a[72:75],a[40:43],v[16:19]\n"
SA"m0,s19,0\n"
MF"v[16:19],a[76:79],a[44:47],v[16:19]\n"
BL"v240,s[12:15],s17 offen offset:0 lds\n"
MF"v[24:27],a[76:79],a[60:63],v[24:27]\n"
RD"a[96:99],v226 offset:8192\n"
MF"v[24:27],a[72:75],a[56:59],v[24:27]\n"
EX"v0,v0\n"
MF"v[4:7],a[80:83],a[0:3],0\n"
BL"v241,s[12:15],s17 offen offset:1024 lds\n"
MF"v[4:7],a[84:87],a[4:7],v[4:7]\n"
EX"v1,v1\n"
MF"v[12:15],a[84:87],a[20:23],0\n"
RD"a[100:103],v226 offset:9216\n"
PK"v0,v0,v1\n"
MF"v[12:15],a[80:83],a[16:19],v[12:15]\n"
EX"v2,v2\n"
MF"v[20:23],a[80:83],a[32:35],0\n"
BL"v251,s[12:15],s17 offen offset:2048 lds\n"
MF"v[20:23],a[84:87],a[36:39],v[20:23]\n"
SA"m0,s19,16384\n"
EX"v3,v3\n"
MF"v[28:31],a[84:87],a[52:55],0\n"
SA"s17,s17,20480\n"
EX"v8,v8\n"
MF"v[28:31],a[80:83],a[48:51],v[28:31]\n"
RD"a[104:107],v226 offset:10240\n"
PK"v1,v2,v3\n"
MF"v[4:7],a[88:91],a[8:11],v[4:7]\n"
WT"lgkmcnt(2)\n"
EX"v9,v9\n"
MF"v[4:7],a[92:95],a[12:15],v[4:7]\n"
BL"v240,s[12:15],s17 offen offset:0 lds\n"
MF"v[12:15],a[92:95],a[28:31],v[12:15]\n"
EX"v10,v10\n"
MF"v[12:15],a[88:91],a[24:27],v[12:15]\n"
RD"a[108:111],v226 offset:11264\n"
PK"v8,v8,v9\n"
MF"v[20:23],a[88:91],a[40:43],v[20:23]\n"
EX"v11,v11\n"
MF"v[20:23],a[92:95],a[44:47],v[20:23]\n"
WT"vmcnt(13)\n"
RT"a[148:149],v243 offset:0\n"
RT"a[150:151],v243 offset:16384\n"
EX"v16,v16\n"
MF"v[28:31],a[92:95],a[60:63],v[28:31]\n"
EX"v17,v17\n"
MF"v[28:31],a[88:91],a[56:59],v[28:31]\n"
RD"a[112:115],v226 offset:12288\n"
PK"v9,v10,v11\n"
MF"v[32:35],a[96:99],a[0:3],0\n"
WT"lgkmcnt(4)\n"
EX"v18,v18\n"
MF"v[32:35],a[100:103],a[4:7],v[32:35]\n"
BL"v241,s[12:15],s17 offen offset:1024 lds\n"
MF"v[40:43],a[100:103],a[20:23],0\n"
EX"v19,v19\n"
MF"v[40:43],a[96:99],a[16:19],v[40:43]\n"
RD"a[116:119],v226 offset:13312\n"
RT"a[152:153],v243 offset:32\n"
MF"v[48:51],a[96:99],a[32:35],0\n"
EX"v24,v24\n"
MF"v[48:51],a[100:103],a[36:39],v[48:51]\n"
RT"a[154:155],v243 offset:16416\n"
RD"a[120:123],v226 offset:14336\n"
EX"v25,v25\n"
MF"v[56:59],a[100:103],a[52:55],0\n"
EX"v26,v26\n"
PK"v16,v16,v17\n"
MF"v[56:59],a[96:99],a[48:51],v[56:59]\n"
EX"v27,v27\n"
MF"v[32:35],a[104:107],a[8:11],v[32:35]\n"
WT"lgkmcnt(4)\n"
BL"v251,s[12:15],s17 offen offset:2048 lds\n"
MF"v[32:35],a[108:111],a[12:15],v[32:35]\n"
EX"v4,v4\n"
PK"v17,v18,v19\n"
MF"v[40:43],a[108:111],a[28:31],v[40:43]\n"
RD"a[124:127],v226 offset:15360\n"
PK"v24,v24,v25\n"
MF"v[40:43],a[104:107],a[24:27],v[40:43]\n"
EX"v5,v5\n"
MF"v[48:51],a[104:107],a[40:43],v[48:51]\n"
RT"a[156:157],v243 offset:64\n"
RT"a[158:159],v243 offset:16448\n"
MF"v[48:51],a[108:111],a[44:47],v[48:51]\n"
EX"v6,v6\n"
MF"v[56:59],a[108:111],a[60:63],v[56:59]\n"
EX"v7,v7\n"
MF"v[56:59],a[104:107],a[56:59],v[56:59]\n"
EX"v12,v12\n"
PK"v25,v26,v27\n"
MF"v[36:39],a[112:115],a[0:3],0\n"
WT"lgkmcnt(3)\n"
EX"v13,v13\n"
MF"v[36:39],a[116:119],a[4:7],v[36:39]\n"
RD"a[64:67],v227 offset:16384\n"
PK"v2,v4,v5\n"
MF"v[44:47],a[116:119],a[20:23],0\n"
EX"v14,v14\n"
MF"v[44:47],a[112:115],a[16:19],v[44:47]\n"
RT"a[160:161],v243 offset:96\n"
RT"a[162:163],v243 offset:16480\n"
MF"v[52:55],a[112:115],a[32:35],0\n"
EX"v15,v15\n"
MF"v[52:55],a[116:119],a[36:39],v[52:55]\n"
RD"a[68:71],v227 offset:17408\n"
PK"v3,v6,v7\n"
MF"v[60:63],a[116:119],a[52:55],0\n"
EX"v20,v20\n"
MF"v[60:63],a[112:115],a[48:51],v[60:63]\n"
"v_readlane_b32 s43,v228,s42\n"
PK"v10,v12,v13\n"
MF"v[36:39],a[120:123],a[8:11],v[36:39]\n"
WT"lgkmcnt(3)\n"
EX"v21,v21\n"
MF"v[36:39],a[124:127],a[12:15],v[36:39]\n"
RD"a[72:75],v227 offset:18432\n"
PK"v11,v14,v15\n"
MF"v[44:47],a[124:127],a[28:31],v[44:47]\n"
EX"v22,v22\n"
MF"v[44:47],a[120:123],a[24:27],v[44:47]\n"
"s_mov_b32 m0,s40\n"
RT"a[164:165],v243 offset:128\n"
RT"a[166:167],v243 offset:16512\n"
MF"v[52:55],a[120:123],a[40:43],v[52:55]\n"
SA"s17,s17,61440\n"
EX"v23,v23\n"
MF"v[52:55],a[124:127],a[44:47],v[52:55]\n"
RD"a[76:79],v227 offset:19456\n"
"v_readlane_b32 s44,v229,s42\n"
MF"v[60:63],a[124:127],a[60:63],v[60:63]\n"
EX"v28,v28\n"
MF"v[60:63],a[120:123],a[56:59],v[60:63]\n"
WT"lgkmcnt(1)\n"
BL"v225,s[8:11],s43 offen offset:0 sc1 lds\n"
MF"v[80:83],a[64:67],v[64:67],v[80:83]\n"
SA"s42,s42,1\n"
EX"v29,v29\n"
MF"v[112:115],a[64:67],v[68:71],v[112:115]\n"
SA"s41,s41,32768\n"
RD"a[80:83],v227 offset:20480\n"
PK"v18,v20,v21\n"
MF"v[144:147],a[64:67],v[72:75],v[144:147]\n"
"s_cmp_eq_u32 s41,163840\n"
EX"v30,v30\n"
MF"v[176:179],a[64:67],v[76:79],v[176:179]\n"
"s_cselect_b32 s41,0,s41\n"
BS"a[148:151],v224,s[8:11],s18 offen offset:0 sc1\n"
MF"v[84:87],a[68:71],v[64:67],v[84:87]\n"
EX"v31,v31\n"
MF"v[116:119],a[68:71],v[68:71],v[116:119]\n"
RD"a[84:87],v227 offset:21504\n"
"v_add_u32 v226,s41,v224\n"
PK"v19,v22,v23\n"
MF"v[148:151],a[68:71],v[72:75],v[148:151]\n"
EX"v32,v32\n"
MF"v[180:183],a[68:71],v[76:79],v[180:183]\n"
BL"v225,s[8:11],s43 offen offset:1024 sc1 lds\n"
MF"v[88:91],a[72:75],v[64:67],v[88:91]\n"
EX"v33,v33\n"
MF"v[120:123],a[72:75],v[68:71],v[120:123]\n"
RD"a[88:91],v227 offset:22528\n"
PK"v26,v28,v29\n"
MF"v[152:155],a[72:75],v[72:75],v[152:155]\n"
EX"v34,v34\n"
MF"v[184:187],a[72:75],v[76:79],v[184:187]\n"
WT"lgkmcnt(2)\n"
BS"a[152:155],v224,s[8:11],s18 offen offset:2048 sc1\n"
MF"v[92:95],a[76:79],v[64:67],v[92:95]\n"
EX"v35,v35\n"
MF"v[124:127],a[76:79],v[68:71],v[124:127]\n"
RD"a[92:95],v227 offset:23552\n"
PK"v27,v30,v31\n"
MF"v[156:159],a[76:79],v[72:75],v[156:159]\n"
SA"s18,s18,4096\n"
EX"v40,v40\n"
MF"v[188:191],a[76:79],v[76:79],v[188:191]\n"
BL"v225,s[8:11],s43 offen offset:2048 sc1 lds\n"
MF"v[96:99],a[80:83],v[64:67],v[96:99]\n"
EX"v41,v41\n"
MF"v[128:131],a[80:83],v[68:71],v[128:131]\n"
RD"a[96:99],v227 offset:24576\n"
MF"v[160:163],a[80:83],v[72:75],v[160:163]\n"
EX"v42,v42\n"
MF"v[192:195],a[80:83],v[76:79],v[192:195]\n"
WT"lgkmcnt(2)\n"
BS"a[156:159],v224,s[8:11],s18 offen offset:0 sc1\n"
MF"v[100:103],a[84:87],v[64:67],v[100:103]\n"
EX"v43,v43\n"
MF"v[132:135],a[84:87],v[68:71],v[132:135]\n"
RD"a[100:103],v227 offset:25600\n"
MF"v[164:167],a[84:87],v[72:75],v[164:167]\n"
EX"v48,v48\n"
MF"v[196:199],a[84:87],v[76:79],v[196:199]\n"
BL"v225,s[8:11],s43 offen offset:3072 sc1 lds\n"
MF"v[104:107],a[88:91],v[64:67],v[104:107]\n"
EX"v49,v49\n"
MF"v[136:139],a[88:91],v[68:71],v[136:139]\n"
RD"a[104:107],v227 offset:26624\n"
MF"v[168:171],a[88:91],v[72:75],v[168:171]\n"
EX"v50,v50\n"
MF"v[200:203],a[88:91],v[76:79],v[200:203]\n"
WT"lgkmcnt(2)\n"
BS"a[160:163],v224,s[8:11],s18 offen offset:2048 sc1\n"
MF"v[108:111],a[92:95],v[64:67],v[108:111]\n"
SA"m0,s40,16384\n"
EX"v51,v51\n"
MF"v[140:143],a[92:95],v[68:71],v[140:143]\n"
RD"a[108:111],v227 offset:27648\n"
MF"v[172:175],a[92:95],v[72:75],v[172:175]\n"
SA"s18,s18,4096\n"
EX"v56,v56\n"
MF"v[204:207],a[92:95],v[76:79],v[204:207]\n"
BL"v225,s[8:11],s44 offen offset:0 sc1 lds\n"
MF"a[132:135],a[128:131],v[64:67],a[132:135]\n"
SA"s40,s40,32768\n"
EX"v57,v57\n"
MF"a[136:139],a[128:131],v[68:71],a[136:139]\n"
"s_cmp_eq_u32 s40,s46\n"
RD"a[112:115],v227 offset:28672\n"
"s_cselect_b32 s40,s47,s40\n"
MF"a[140:143],a[128:131],v[72:75],a[140:143]\n"
EX"v58,v58\n"
MF"a[144:147],a[128:131],v[76:79],a[144:147]\n"
BS"a[164:167],v224,s[8:11],s18 offen offset:0 sc1\n"
MF"v[80:83],a[96:99],v[0:3],v[80:83]\n"
"s_lshr_b32 s21,s16,1\n"
EX"v59,v59\n"
MF"v[112:115],a[96:99],v[8:11],v[112:115]\n"
"s_sub_u32 s21,s21,s33\n"
RD"a[116:119],v227 offset:29696\n"
MF"v[144:147],a[96:99],v[16:19],v[144:147]\n"
"s_and_b32 s21,s21,3\n"
EX"v36,v36\n"
MF"v[176:179],a[96:99],v[24:27],v[176:179]\n"
WT"lgkmcnt(2)\n"
BL"v225,s[8:11],s44 offen offset:1024 sc1 lds\n"
MF"v[84:87],a[100:103],v[0:3],v[84:87]\n"
"s_lshl_b32 s21,s21,1\n"
EX"v37,v37\n"
MF"v[116:119],a[100:103],v[8:11],v[116:119]\n"
"s_and_b32 s22,s16,1\n"
RD"a[120:123],v227 offset:30720\n"
SA"s21,s21,s22\n"
MF"v[148:151],a[100:103],v[16:19],v[148:151]\n"
"s_lshl_b32 s21,s21,1\n"
EX"v38,v38\n"
MF"v[180:183],a[100:103],v[24:27],v[180:183]\n"
"s_and_b32 s22,s30,1\n"
MF"v[88:91],a[104:107],v[0:3],v[88:91]\n"
SA"s21,s21,s22\n"
EX"v39,v39\n"
MF"v[120:123],a[104:107],v[8:11],v[120:123]\n"
"s_mul_i32 s21,s21,10240\n"
RD"a[124:127],v227 offset:31744\n"
MF"v[152:155],a[104:107],v[16:19],v[152:155]\n"
"s_lshr_b32 s22,s30,1\n"
EX"v44,v44\n"
MF"v[184:187],a[104:107],v[24:27],v[184:187]\n"
"s_lshl_b32 s22,s22,10\n"
BL"v225,s[8:11],s44 offen offset:2048 sc1 lds\n"
MF"v[92:95],a[108:111],v[0:3],v[92:95]\n"
SA"s21,s21,s22\n"
EX"v45,v45\n"
MF"v[124:127],a[108:111],v[8:11],v[124:127]\n"
"s_mul_i32 s22,s35,163840\n"
MF"v[156:159],a[108:111],v[16:19],v[156:159]\n"
SA"s21,s21,s22\n"
EX"v46,v46\n"
MF"v[188:191],a[108:111],v[24:27],v[188:191]\n"
WT"lgkmcnt(1)\n"
SA"s18,s21,3428352\n"
MF"v[96:99],a[112:115],v[0:3],v[96:99]\n"
SA"s16,s16,1\n"
WT"lgkmcnt(0)\n"
"s_barrier\n"
MF"v[128:131],a[112:115],v[8:11],v[128:131]\n"
RD"a[64:67],v226 offset:0\n"
MF"v[160:163],a[112:115],v[16:19],v[160:163]\n"
EX"v47,v47\n"
MF"v[192:195],a[112:115],v[24:27],v[192:195]\n"
BL"v225,s[8:11],s44 offen offset:3072 sc1 lds\n"
MF"v[100:103],a[116:119],v[0:3],v[100:103]\n"
EX"v52,v52\n"
MF"v[132:135],a[116:119],v[8:11],v[132:135]\n"
RD"a[68:71],v226 offset:1024\n"
MF"v[164:167],a[116:119],v[16:19],v[164:167]\n"
EX"v53,v53\n"
MF"v[196:199],a[116:119],v[24:27],v[196:199]\n"
RD"a[72:75],v226 offset:2048\n"
MF"v[104:107],a[120:123],v[0:3],v[104:107]\n"
EX"v54,v54\n"
MF"v[136:139],a[120:123],v[8:11],v[136:139]\n"
RD"a[76:79],v226 offset:3072\n"
MF"v[168:171],a[120:123],v[16:19],v[168:171]\n"
EX"v55,v55\n"
MF"v[200:203],a[120:123],v[24:27],v[200:203]\n"
MF"v[108:111],a[124:127],v[0:3],v[108:111]\n"
EX"v60,v60\n"
MF"v[140:143],a[124:127],v[8:11],v[140:143]\n"
RD"a[80:83],v226 offset:4096\n"
MF"v[172:175],a[124:127],v[16:19],v[172:175]\n"
EX"v61,v61\n"
MF"v[204:207],a[124:127],v[24:27],v[204:207]\n"
RD"a[84:87],v226 offset:5120\n"
MF"a[132:135],a[128:131],v[0:3],a[132:135]\n"
EX"v62,v62\n"
MF"a[136:139],a[128:131],v[8:11],a[136:139]\n"
RD"a[88:91],v226 offset:6144\n"
MF"a[140:143],a[128:131],v[16:19],a[140:143]\n"
EX"v63,v63\n"
MF"a[144:147],a[128:131],v[24:27],a[144:147]\n"
"s_sub_u32 s20,s20,1\n"
"s_cbranch_scc0 LRT_%=\n"
"s_lshl_b32 s21,s35,8\n"
SA"s21,s21,81264640\n"
SA"s21,s21,s45\n"
WT"vmcnt(3)\n"
"s_barrier\n"
"buffer_store_dwordx2 v[232:233],v230,s[8:11],s21 offen sc1\n"
"v_cmp_eq_u64 vcc,s[28:29],v[244:245]\n"
"s_cmp_eq_u64 vcc,exec\n"
"s_cbranch_scc0 L1105_%=\n"
"L1106_%=:\n"
"s_mov_b32 s57,0\n"
"s_mov_b32 s56,0\n"
"s_mov_b32 s14,0\n"
"s_branch LP1_%=\n"
"LEND_%=:\n"
      : : [ka] "s"(__builtin_amdgcn_kernarg_segment_ptr()), [b] "s"(blockIdx.x), [t] "v"(threadIdx.x)
      : "v1", "v2", "v3", "v4", "v5", "v6", "v7", "v8", "v9", "v10", "v11", "v12", "v13", "v14", "v15", "v16", "v17", "v18", "v19", "v20", "v21", "v22", "v23", "v24", "v25", "v26", "v27", "v28", "v29", "v30", "v31", "v32", "v33", "v34", "v35", "v36", "v37", "v38", "v39", "v40", "v41", "v42", "v43", "v44", "v45", "v46", "v47", "v48", "v49", "v50", "v51", "v52", "v53", "v54", "v55", "v56", "v57", "v58", "v59", "v60", "v61", "v62", "v63", "v64", "v65", "v66", "v67", "v68", "v69", "v70", "v71", "v72", "v73", "v74", "v75", "v76", "v77", "v78", "v79", "v80", "v81", "v82", "v83", "v84", "v85", "v86", "v87", "v88", "v89", "v90", "v91", "v92", "v93", "v94", "v95", "v96", "v97", "v98", "v99", "v100", "v101", "v102", "v103", "v104", "v105", "v106", "v107", "v108", "v109", "v110", "v111", "v112", "v113", "v114", "v115", "v116", "v117", "v118", "v119", "v120", "v121", "v122", "v123", "v124", "v125", "v126", "v127", "v128", "v129", "v130", "v131", "v132", "v133", "v134", "v135", "v136", "v137", "v138", "v139", "v140", "v141", "v142", "v143", "v144", "v145", "v146", "v147", "v148", "v149", "v150", "v151", "v152", "v153", "v154", "v155", "v156", "v157", "v158", "v159", "v160", "v161", "v162", "v163", "v164", "v165", "v166", "v167", "v168", "v169", "v170", "v171", "v172", "v173", "v174", "v175", "v176", "v177", "v178", "v179", "v180", "v181", "v182", "v183", "v184", "v185", "v186", "v187", "v188", "v189", "v190", "v191", "v192", "v193", "v194", "v195", "v196", "v197", "v198", "v199", "v200", "v201", "v202", "v203", "v204", "v205", "v206", "v207", "v208", "v209", "v210", "v211", "v212", "v213", "v214", "v215", "v216", "v217", "v218", "v219", "v220", "v221", "v222", "v223", "v224", "v225", "v226", "v227", "v228", "v229", "v230", "v231", "v232", "v233", "v234", "v235", "v236", "v237", "v238", "v239", "v240", "v241", "v242", "v243", "v244", "v245", "v246", "v247", "v248", "v249", "v250", "v251", "v252", "v253", "v254", "v255", "a0", "a1", "a2", "a3", "a4", "a5", "a6", "a7", "a8", "a9", "a10", "a11", "a12", "a13", "a14", "a15", "a16", "a17", "a18", "a19", "a20", "a21", "a22", "a23", "a24", "a25", "a26", "a27", "a28", "a29", "a30", "a31", "a32", "a33", "a34", "a35", "a36", "a37", "a38", "a39", "a40", "a41", "a42", "a43", "a44", "a45", "a46", "a47", "a48", "a49", "a50", "a51", "a52", "a53", "a54", "a55", "a56", "a57", "a58", "a59", "a60", "a61", "a62", "a63", "a64", "a65", "a66", "a67", "a68", "a69", "a70", "a71", "a72", "a73", "a74", "a75", "a76", "a77", "a78", "a79", "a80", "a81", "a82", "a83", "a84", "a85", "a86", "a87", "a88", "a89", "a90", "a91", "a92", "a93", "a94", "a95", "a96", "a97", "a98", "a99", "a100", "a101", "a102", "a103", "a104", "a105", "a106", "a107", "a108", "a109", "a110", "a111", "a112", "a113", "a114", "a115", "a116", "a117", "a118", "a119", "a120", "a121", "a122", "a123", "a124", "a125", "a126", "a127", "a128", "a129", "a130", "a131", "a132", "a133", "a134", "a135", "a136", "a137", "a138", "a139", "a140", "a141", "a142", "a143", "a144", "a145", "a146", "a147", "a148", "a149", "a150", "a151", "a152", "a153", "a154", "a155", "a156", "a157", "a158", "a159", "a160", "a161", "a162", "a163", "a164", "a165", "a166", "a167", "a168", "a169", "a170", "a171", "a172", "a173", "a174", "a175", "a176", "a177", "a178", "a179", "a180", "a181", "a182", "a183", "a184", "a185", "a186", "a187", "a188", "a189", "a190", "a191", "a192", "a193", "a194", "a195", "a196", "a197", "a198", "a199", "a200", "a201", "a202", "a203", "a204", "a205", "a206", "a207", "a208", "a209", "a210", "a211", "a212", "a213", "a214", "a215", "a216", "a217", "a218", "a219", "a220", "a221", "a222", "a223", "a224", "a225", "a226", "a227", "a228", "a229", "a230", "a231", "a232", "a233", "a234", "a235", "a236", "a237", "a238", "a239", "a240", "a241", "a242", "a243", "a244", "a245", "a246", "a247", "a248", "a249", "a250", "a251", "a252", "a253", "a254", "a255", "s8", "s9", "s10", "s11", "s12", "s13", "s14", "s15", "s16", "s17", "s18", "s19", "s20", "s21", "s22", "s23", "s24", "s25", "s26", "s27", "s28", "s29", "s30", "s31", "s32", "s33", "s34", "s35", "s36", "s37", "s38", "s39", "s40", "s41", "s42", "s43", "s44", "s45", "s46", "s47", "s48", "s49", "s50", "s51", "s52", "s53", "s54", "s55", "s56", "s57", "s58", "s59", "s60", "s61", "s62", "s63", "s64", "s65", "s66", "s67", "s68", "s69", "s70", "s71", "s72", "s73", "s74", "s75", "s76", "s77", "s78", "s79", "s80", "s81", "s82", "s83", "s84", "s85", "s86", "s87", "s88", "s89", "s90", "s91", "s92", "s93", "s94", "s95", "s96", "s97", "s98", "s99", "scc", "vcc", "memory");
}

extern "C" void kernel_launch(void* const* d_in, const int* in_sizes, int n_in, void* d_out, int out_size, void* d_ws, size_t ws_size, hipStream_t stream) {
  (void)hipFuncSetAttribute((const void*)mega_kernel, hipFuncAttributeMaxDynamicSharedMemorySize, 163840);
  mega_kernel<<<256, 256, 163840, stream>>>((const bf16*)d_in[0], (const bf16*)d_in[1], (const bf16*)d_in[2], (const bf16*)d_in[3], (const bf16*)d_in[4], (const bf16*)d_in[5],
      (const bf16*)d_in[7], (const bf16*)d_in[8], (const bf16*)d_in[9], (const bf16*)d_in[10], (char*)d_ws + (size_t)0 * 1048576, (bf16*)d_out);
}
```

```cpp
#include <hip/hip_runtime.h>
#include <hip/hip_bf16.h>
typedef __hip_bfloat16 bf16;
#define MF "v_mfma_f32_16x16x32_bf16 "
#define RD "ds_read_b128 "
#define RT "ds_read_b64_tr_b16 "
#define BL "buffer_load_dwordx4 "
#define BS "buffer_store_dwordx4 "
#define PK "v_cvt_pk_bf16_f32 "
#define EX "v_exp_f32 "
#define WT "s_waitcnt "
#define MU "v_mul_f32 "
#define SA "s_add_u32 "

__global__ __launch_bounds__(256, 1) void mega_kernel(const bf16* x, const bf16* ckv, const bf16* wq, const bf16* wuk, const bf16* wuv, const bf16* wo, const bf16* w_r,
                                                      const bf16* wg, const bf16* wu, const bf16* wd, char* ws, bf16* out) {
  asm volatile(
"s_load_dwordx16 s[64:79],%[ka],0\n"
"s_load_dwordx8 s[80:87],%[ka],64\n"
"s_mov_b32 s31,%[b]\n"
"v_lshrrev_b32 v1,6,%[t]\n"
"v_and_b32 v2,63,%[t]\n"
"v_readfirstlane_b32 s30,v1\n"
"v_lshlrev_b32 v224,4,v2\n"
"s_getpc_b64 s[24:25]\n"
"LPC_%=:\n"
"s_and_b32 s25,s25,0xffff\n"
"s_mov_b32 s26,LEND_%=-LPC_%=\n"
"s_mov_b32 s27,0x20000\n"
"s_lshr_b32 s49,s31,3\n"
"s_lshl_b32 s49,s49,2\n"
SA"s49,s49,s30\n"
"s_lshl_b32 s49,s49,7\n"
"v_lshlrev_b32 v10,14,v2\n"
"buffer_load_dword v255,v10,s[24:27],s49 offen\n"
"s_and_b32 s32,s31,7\n"
"s_lshr_b32 s33,s31,6\n"
"s_bfe_u32 s34,s31,0x30003\n"
"s_lshl_b32 s35,s32,2\n"
SA"s35,s35,s33\n"
"s_mov_b32 s46,0x76543210\n"
"s_mov_b32 s47,0x32176540\n"
"s_bitcmp1_b32 s34,0\n"
"s_cselect_b32 s46,0x76543210,s46\n"
"s_cselect_b32 s47,0x32176540,s47\n"
"s_bitcmp1_b32 s34,1\n"
"s_cselect_b32 s46,s47,s46\n"
"s_mul_i32 s47,s32,0x11111111\n"
SA"s46,s46,s47\n"
"s_lshl_b32 s36,s30,12\n"
"s_mul_i32 s37,s30,24576\n"
SA"s37,s37,65536\n"
"v_lshrrev_b32 v3,3,v2\n"
"v_lshrrev_b32 v4,4,v2\n"
"v_and_b32 v5,7,v2\n"
"v_xor_b32 v6,v5,v4\n"
"v_lshlrev_b32 v6,4,v6\n"
"v_lshl_add_u32 v6,v3,10,v6\n"
"s_lshl_b32 s50,s30,15\n"
"v_add_u32 v234,s50,v6\n"
"v_xor_b32 v235,64,v234\n"
"v_add_u32 v235,7168,v235\n"
"v_add_u32 v236,14336,v234\n"
"v_add_u32 v237,14336,v235\n"
"v_and_b32 v7,1,v4\n"
"v_lshlrev_b32 v7,1,v7\n"
"v_xor_b32 v7,v7,v5\n"
"v_lshlrev_b32 v7,4,v7\n"
"v_lshl_add_u32 v238,v3,13,v7\n"
"v_xor_b32 v239,64,v238\n"
"v_add_u32 v239,64512,v239\n"
"v_add_u32 v240,129024,v238\n"
"v_add_u32 v241,129024,v239\n"
"v_and_b32 v8,31,v2\n"
"v_lshlrev_b32 v8,3,v8\n"
"v_lshrrev_b32 v9,5,v2\n"
"s_lshl_b32 s50,s31,2\n"
SA"s50,s50,s30\n"
"s_mul_i32 s50,s50,2560\n"
"v_mul_u32_u24 v9,1280,v9\n"
"v_add3_u32 v9,v9,v8,s50\n"
"s_lshl_b32 s50,s34,17\n"
"s_lshl_b32 s51,s35,8\n"
"s_and_b32 s52,s30,1\n"
"s_lshl_b32 s52,s52,7\n"
SA"s52,s52,s51\n"
"s_mov_b32 s14,0x120000\n"
"s_mov_b32 s15,0x20000\n"
"s_sub_u32 s18,4194304,s52\n"
"s_mov_b32 s19,0x20000\n"
WT"lgkmcnt(0)\n"
"s_load_dwordx2 s[28:29],s[84:85],0\n"
SA"s12,s66,s50\n"
"s_addc_u32 s13,s67,0\n"
"s_and_b32 s13,s13,0xffff\n"
"s_cmp_lt_u32 s30,2\n"
"s_cselect_b64 s[16:17],s[70:71],s[72:73]\n"
SA"s16,s16,s52\n"
"s_addc_u32 s17,s17,0\n"
"s_and_b32 s17,s17,0xffff\n"
"global_load_dwordx2 v[128:129],v9,s[64:65] offset:0\n"
"global_load_dwordx2 v[130:131],v9,s[64:65] offset:256\n"
"global_load_dwordx2 v[132:133],v9,s[64:65] offset:512\n"
"global_load_dwordx2 v[134:135],v9,s[64:65] offset:768\n"
"global_load_dwordx2 v[136:137],v9,s[64:65] offset:1024\n"
"global_load_dwordx2 v[138:139],v8,s[76:77] offset:0\n"
"global_load_dwordx2 v[140:141],v8,s[76:77] offset:256\n"
"global_load_dwordx2 v[142:143],v8,s[76:77] offset:512\n"
"global_load_dwordx2 v[144:145],v8,s[76:77] offset:768\n"
"global_load_dwordx2 v[146:147],v8,s[76:77] offset:1024\n"
SA"m0,s37,0\n"
"s_bfe_u32 s48,s46,0x30000\n"
"s_lshl_b32 s48,s48,19\n"
BL"v238,s[16:19],s48 offen offset:0 lds\n"
BL"v239,s[16:19],s48 offen offset:1024 lds\n"
BL"v240,s[16:19],s48 offen offset:2048 lds\n"
BL"v241,s[16:19],s48 offen offset:3072 lds\n"
SA"m0,s37,4096\n"
"s_bfe_u32 s48,s46,0x30000\n"
"s_lshl_b32 s48,s48,19\n"
"s_bitset1_b32 s48,18\n"
BL"v238,s[16:19],s48 offen offset:0 lds\n"
BL"v239,s[16:19],s48 offen offset:1024 lds\n"
BL"v240,s[16:19],s48 offen offset:2048 lds\n"
BL"v241,s[16:19],s48 offen offset:3072 lds\n"
"v_lshlrev_b32 v253,7,%[t]\n"
"s_mov_b32 s8,s84\n"
"s_and_b32 s9,s85,0xffff\n"
"s_mov_b32 s10,80478208\n"
"s_mov_b32 s11,0x20000\n"
SA"s20,s68,s51\n"
"s_addc_u32 s21,s69,0\n"
"s_and_b32 s21,s21,0xffff\n"
"s_sub_u32 s22,5242880,s51\n"
"s_mov_b32 s23,0x20000\n"
"v_and_b32 v11,15,v2\n"
"v_lshrrev_b32 v12,1,v11\n"
"v_xor_b32 v12,v12,v4\n"
"v_lshlrev_b32 v12,4,v12\n"
"v_lshl_add_u32 v242,v11,7,v12\n"
"v_xor_b32 v243,64,v242\n"
"v_lshrrev_b32 v12,2,v11\n"
"v_lshl_add_u32 v12,v4,3,v12\n"
"v_lshlrev_b32 v12,7,v12\n"
"v_and_b32 v13,3,v2\n"
"v_lshl_add_u32 v12,v13,3,v12\n"
"v_and_b32 v13,3,v3\n"
"v_lshl_add_u32 v12,v13,5,v12\n"
"v_add_u32 v246,s37,v12\n"
"v_xor_b32 v247,32,v246\n"
"v_xor_b32 v248,64,v246\n"
"v_xor_b32 v249,96,v246\n"
"v_mov_b32 v13,2130706432\n"
"v_cmp_eq_u32 vcc,0,v2\n"
"s_cmp_eq_u32 s30,0\n"
"s_cselect_b32 s45,0,2130706432\n"
"v_cndmask_b32_e64 v230,v13,0,vcc\n"
"s_lshl_b32 s39,s30,3\n"
"s_lshr_b32 s98,s46,s39\n"
"s_and_b32 s39,s98,7\n"
"s_lshl_b32 s39,s39,7\n"
"s_and_b32 s98,s98,0x70\n"
"s_lshl_b32 s98,s98,3\n"
"s_lshl_b32 s50,s30,15\n"
"s_sub_u32 s50,1048576,s50\n"
SA"s39,s39,s50\n"
SA"s98,s98,s50\n"
"s_mov_b32 s55,0x3e0293ee\n"
"s_cmp_lt_u32 s30,2\n"
"s_cbranch_scc0 LV_%=\n"
WT"vmcnt(17)\n"
"v_lshlrev_b32 v148,16,v128\n"
"v_and_b32 v149,0xffff0000,v128\n"
"v_lshlrev_b32 v150,16,v129\n"
"v_and_b32 v151,0xffff0000,v129\n"
WT"vmcnt(16)\n"
"v_lshlrev_b32 v152,16,v130\n"
"v_and_b32 v153,0xffff0000,v130\n"
"v_lshlrev_b32 v154,16,v131\n"
"v_and_b32 v155,0xffff0000,v131\n"
WT"vmcnt(15)\n"
"v_lshlrev_b32 v156,16,v132\n"
"v_and_b32 v157,0xffff0000,v132\n"
"v_lshlrev_b32 v158,16,v133\n"
"v_and_b32 v159,0xffff0000,v133\n"
WT"vmcnt(14)\n"
"v_lshlrev_b32 v160,16,v134\n"
"v_and_b32 v161,0xffff0000,v134\n"
"v_lshlrev_b32 v162,16,v135\n"
"v_and_b32 v163,0xffff0000,v135\n"
WT"vmcnt(13)\n"
"v_lshlrev_b32 v164,16,v136\n"
"v_and_b32 v165,0xffff0000,v136\n"
"v_lshlrev_b32 v166,16,v137\n"
"v_and_b32 v167,0xffff0000,v137\n"
"v_add_f32 v188,v148,v149\n"
"v_add_f32 v189,v150,v151\n"
"v_add_f32 v188,v188,v152\n"
"v_add_f32 v189,v189,v153\n"
"v_add_f32 v188,v188,v154\n"
"v_add_f32 v189,v189,v155\n"
"v_add_f32 v188,v188,v156\n"
"v_add_f32 v189,v189,v157\n"
"v_add_f32 v188,v188,v158\n"
"v_add_f32 v189,v189,v159\n"
"v_add_f32 v188,v188,v160\n"
"v_add_f32 v189,v189,v161\n"
"v_add_f32 v188,v188,v162\n"
"v_add_f32 v189,v189,v163\n"
"v_add_f32 v188,v188,v164\n"
"v_add_f32 v189,v189,v165\n"
"v_add_f32 v188,v188,v166\n"
"v_add_f32 v189,v189,v167\n"
"v_add_f32 v188,v188,v189\n"
"s_nop 1\n"
"v_add_f32_dpp v188,v188,v188 quad_perm:[1,0,3,2] row_mask:0xf bank_mask:0xf\n"
WT"vmcnt(12)\n"
"v_lshlrev_b32 v168,16,v138\n"
"v_and_b32 v169,0xffff0000,v138\n"
"v_add_f32_dpp v188,v188,v188 quad_perm:[2,3,0,1] row_mask:0xf bank_mask:0xf\n"
"v_lshlrev_b32 v170,16,v139\n"
"v_and_b32 v171,0xffff0000,v139\n"
"v_add_f32_dpp v188,v188,v188 row_half_mirror row_mask:0xf bank_mask:0xf\n"
WT"vmcnt(11)\n"
"v_lshlrev_b32 v172,16,v140\n"
SA"m0,s36,0\n"
"s_bfe_u32 s48,s46,0x30000\n"
"s_lshl_b32 s48,s48,7\n"
BL"v234,s[12:15],s48 offen offset:0 lds\n"
BL"v235,s[12:15],s48 offen offset:1024 lds\n"
BL"v236,s[12:15],s48 offen offset:2048 lds\n"
BL"v237,s[12:15],s48 offen offset:3072 lds\n"
"v_and_b32 v173,0xffff0000,v140\n"
"v_add_f32_dpp v188,v188,v188 row_mirror row_mask:0xf bank_mask:0xf\n"
"v_lshlrev_b32 v174,16,v141\n"
"v_and_b32 v175,0xffff0000,v141\n"
"v_mov_b32 v189,v188\n"
WT"vmcnt(14)\n"
"v_lshlrev_b32 v176,16,v142\n"
"v_and_b32 v177,0xffff0000,v142\n"
"v_permlane16_swap_b32 v189,v188\n"
"v_lshlrev_b32 v178,16,v143\n"
"v_and_b32 v179,0xffff0000,v143\n"
"v_add_f32 v188,v188,v189\n"
WT"vmcnt(13)\n"
"v_lshlrev_b32 v180,16,v144\n"
"v_and_b32 v181,0xffff0000,v144\n"
MU"v191,0x3acccccd,v188\n"
"v_lshlrev_b32 v182,16,v145\n"
"v_and_b32 v183,0xffff0000,v145\n"
PK"v128,v191,v191\n"
WT"vmcnt(12)\n"
"v_lshlrev_b32 v184,16,v146\n"
"v_and_b32 v185,0xffff0000,v146\n"
"v_lshlrev_b32 v128,16,v128\n"
"v_lshlrev_b32 v186,16,v147\n"
"v_and_b32 v187,0xffff0000,v147\n"
"v_sub_f32 v148,v148,v128\n"
"v_sub_f32 v149,v149,v128\n"
"v_sub_f32 v150,v150,v128\n"
"v_sub_f32 v151,v151,v128\n"
"v_sub_f32 v152,v152,v128\n"
"v_sub_f32 v153,v153,v128\n"
"v_sub_f32 v154,v154,v128\n"
"v_sub_f32 v155,v155,v128\n"
"v_sub_f32 v156,v156,v128\n"
"v_sub_f32 v157,v157,v128\n"
"v_sub_f32 v158,v158,v128\n"
"v_sub_f32 v159,v159,v128\n"
"v_sub_f32 v160,v160,v128\n"
"v_sub_f32 v161,v161,v128\n"
"v_sub_f32 v162,v162,v128\n"
"v_sub_f32 v163,v163,v128\n"
"v_sub_f32 v164,v164,v128\n"
"v_sub_f32 v165,v165,v128\n"
"v_sub_f32 v166,v166,v128\n"
"v_sub_f32 v167,v167,v128\n"
MU"v188,v148,v148\n"
MU"v189,v149,v149\n"
"v_fmac_f32 v188,v150,v150\n"
"v_fmac_f32 v189,v151,v151\n"
"v_fmac_f32 v188,v152,v152\n"
SA"m0,s37,8192\n"
"s_bfe_u32 s48,s46,0x30004\n"
"s_lshl_b32 s48,s48,19\n"
BL"v238,s[16:19],s48 offen offset:0 lds\n"
BL"v239,s[16:19],s48 offen offset:1024 lds\n"
BL"v240,s[16:19],s48 offen offset:2048 lds\n"
BL"v241,s[16:19],s48 offen offset:3072 lds\n"
"v_fmac_f32 v189,v153,v153\n"
"v_fmac_f32 v188,v154,v154\n"
"v_fmac_f32 v189,v155,v155\n"
"v_fmac_f32 v188,v156,v156\n"
"v_fmac_f32 v189,v157,v157\n"
"v_fmac_f32 v188,v158,v158\n"
"v_fmac_f32 v189,v159,v159\n"
"v_fmac_f32 v188,v160,v160\n"
"v_fmac_f32 v189,v161,v161\n"
"v_fmac_f32 v188,v162,v162\n"
"v_fmac_f32 v189,v163,v163\n"
"v_fmac_f32 v188,v164,v164\n"
"v_fmac_f32 v189,v165,v165\n"
"v_fmac_f32 v188,v166,v166\n"
"v_fmac_f32 v189,v167,v167\n"
"v_add_f32 v188,v188,v189\n"
PK"v149,v148,v149\n"
"v_lshlrev_b32 v148,16,v149\n"
"v_add_f32_dpp v188,v188,v188 quad_perm:[1,0,3,2] row_mask:0xf bank_mask:0xf\n"
"v_and_b32 v149,0xffff0000,v149\n"
PK"v151,v150,v151\n"
"v_add_f32_dpp v188,v188,v188 quad_perm:[2,3,0,1] row_mask:0xf bank_mask:0xf\n"
"v_lshlrev_b32 v150,16,v151\n"
"v_and_b32 v151,0xffff0000,v151\n"
"v_add_f32_dpp v188,v188,v188 row_half_mirror row_mask:0xf bank_mask:0xf\n"
PK"v153,v152,v153\n"
"v_lshlrev_b32 v152,16,v153\n"
"v_add_f32_dpp v188,v188,v188 row_mirror row_mask:0xf bank_mask:0xf\n"
"v_and_b32 v153,0xffff0000,v153\n"
PK"v155,v154,v155\n"
"v_mov_b32 v189,v188\n"
"v_lshlrev_b32 v154,16,v155\n"
"v_and_b32 v155,0xffff0000,v155\n"
"v_permlane16_swap_b32 v189,v188\n"
PK"v157,v156,v157\n"
"v_lshlrev_b32 v156,16,v157\n"
"v_add_f32 v188,v188,v189\n"
"v_and_b32 v157,0xffff0000,v157\n"
PK"v159,v158,v159\n"
"v_sub_f32 v191,v191,v128\n"
"v_lshlrev_b32 v158,16,v159\n"
"v_and_b32 v159,0xffff0000,v159\n"
MU"v188,0x3acccccd,v188\n"
PK"v161,v160,v161\n"
"v_lshlrev_b32 v160,16,v161\n"
"v_fma_f32 v188,-v191,v191,v188\n"
"v_and_b32 v161,0xffff0000,v161\n"
PK"v163,v162,v163\n"
SA"m0,s37,12288\n"
"s_bfe_u32 s48,s46,0x30004\n"
"s_lshl_b32 s48,s48,19\n"
"s_bitset1_b32 s48,18\n"
BL"v238,s[16:19],s48 offen offset:0 lds\n"
BL"v239,s[16:19],s48 offen offset:1024 lds\n"
BL"v240,s[16:19],s48 offen offset:2048 lds\n"
BL"v241,s[16:19],s48 offen offset:3072 lds\n"
"v_max_f32 v188,0,v188\n"
"v_lshlrev_b32 v162,16,v163\n"
"v_and_b32 v163,0xffff0000,v163\n"
PK"v188,v188,v188\n"
PK"v165,v164,v165\n"
"v_lshlrev_b32 v164,16,v165\n"
"v_lshlrev_b32 v188,16,v188\n"
"v_and_b32 v165,0xffff0000,v165\n"
PK"v167,v166,v167\n"
"v_add_f32 v188,0x3727c5ac,v188\n"
"v_lshlrev_b32 v166,16,v167\n"
"v_and_b32 v167,0xffff0000,v167\n"
PK"v188,v188,v188\n"
"v_bfe_u32 v130,v224,5,4\n"
"v_mul_u32_u24 v130,144,v130\n"
"v_lshlrev_b32 v188,16,v188\n"
"v_bfe_u32 v131,v224,4,1\n"
"v_lshl_add_u32 v130,v131,3,v130\n"
"v_sqrt_f32 v189,v188\n"
"v_lshrrev_b32 v131,9,v224\n"
"v_lshl_add_u32 v130,v131,4,v130\n"
"v_rcp_f32 v190,v189\n"
"s_lshl_b32 s52,s30,5\n"
"v_add_u32 v130,s52,v130\n"
"v_fma_f32 v188,-v189,v189,v188\n"
MU"v190,0.5,v190\n"
"v_fma_f32 v189,v188,v190,v189\n"
PK"v189,v189,v189\n"
"v_lshlrev_b32 v189,16,v189\n"
"v_rcp_f32 v129,v189\n"
"s_nop 0\n"
MU"v148,v148,v129\n"
MU"v149,v149,v129\n"
PK"v138,v148,v149\n"
MU"v150,v150,v129\n"
MU"v151,v151,v129\n"
PK"v139,v150,v151\n"
"ds_write_b64 v130,v[138:139] offset:49152\n"
MU"v152,v152,v129\n"
MU"v153,v153,v129\n"
PK"v140,v152,v153\n"
MU"v154,v154,v129\n"
MU"v155,v155,v129\n"
PK"v141,v154,v155\n"
"ds_write_b64 v130,v[140:141] offset:51456\n"
MU"v156,v156,v129\n"
MU"v157,v157,v129\n"
PK"v142,v156,v157\n"
SA"m0,s36,16384\n"
"s_bfe_u32 s48,s46,0x30004\n"
"s_lshl_b32 s48,s48,7\n"
BL"v234,s[12:15],s48 offen offset:0 lds\n"
BL"v235,s[12:15],s48 offen offset:1024 lds\n"
BL"v236,s[12:15],s48 offen offset:2048 lds\n"
BL"v237,s[12:15],s48 offen offset:3072 lds\n"
MU"v158,v158,v129\n"
MU"v159,v159,v129\n"
PK"v143,v158,v159\n"
"ds_write_b64 v130,v[142:143] offset:53760\n"
MU"v160,v160,v129\n"
MU"v161,v161,v129\n"
PK"v144,v160,v161\n"
MU"v162,v162,v129\n"
MU"v163,v163,v129\n"
PK"v145,v162,v163\n"
"ds_write_b64 v130,v[144:145] offset:56064\n"
MU"v164,v164,v129\n"
MU"v165,v165,v129\n"
PK"v146,v164,v165\n"
MU"v166,v166,v129\n"
MU"v167,v167,v129\n"
PK"v147,v166,v167\n"
"ds_write_b64 v130,v[146:147] offset:58368\n"
"v_lshlrev_b32 v148,16,v138\n"
"v_and_b32 v149,0xffff0000,v138\n"
MU"v188,v148,v168\n"
MU"v189,v149,v169\n"
"v_lshlrev_b32 v150,16,v139\n"
"v_and_b32 v151,0xffff0000,v139\n"
"v_fmac_f32 v188,v150,v170\n"
"v_fmac_f32 v189,v151,v171\n"
"v_lshlrev_b32 v152,16,v140\n"
"v_and_b32 v153,0xffff0000,v140\n"
"v_fmac_f32 v188,v152,v172\n"
"v_fmac_f32 v189,v153,v173\n"
"v_lshlrev_b32 v154,16,v141\n"
"v_and_b32 v155,0xffff0000,v141\n"
"v_fmac_f32 v188,v154,v174\n"
"v_fmac_f32 v189,v155,v175\n"
"v_lshlrev_b32 v156,16,v142\n"
"v_and_b32 v157,0xffff0000,v142\n"
"v_fmac_f32 v188,v156,v176\n"
"v_fmac_f32 v189,v157,v177\n"
"v_lshlrev_b32 v158,16,v143\n"
"v_and_b32 v159,0xffff0000,v143\n"
"v_fmac_f32 v188,v158,v178\n"
"v_fmac_f32 v189,v159,v179\n"
"v_lshlrev_b32 v160,16,v144\n"
"v_and_b32 v161,0xffff0000,v144\n"
"v_fmac_f32 v188,v160,v180\n"
"v_fmac_f32 v189,v161,v181\n"
"v_lshlrev_b32 v162,16,v145\n"
"v_and_b32 v163,0xffff0000,v145\n"
WT"lgkmcnt(0)\n"
SA"s28,s28,0x7f4a7c15\n"
"s_addc_u32 s29,s29,0x9e3779b9\n"
"v_mov_b32 v232,s28\n"
"v_mov_b32 v233,s29\n"
WT"vmcnt(20)\n"
RT"a[0:1],v246 offset:0\n"
RT"a[2:3],v246 offset:512\n"
RT"a[4:5],v247 offset:0\n"
RT"a[6:7],v247 offset:512\n"
RT"a[8:9],v248 offset:0\n"
RT"a[10:11],v248 offset:512\n"
RT"a[12:13],v249 offset:0\n"
RT"a[14:15],v249 offset:512\n"
WT"vmcnt(16)\n"
RT"a[16:17],v246 offset:4096\n"
RT"a[18:19],v246 offset:4608\n"
RT"a[20:21],v247 offset:4096\n"
RT"a[22:23],v247 offset:4608\n"
RT"a[24:25],v248 offset:4096\n"
RT"a[26:27],v248 offset:4608\n"
RT"a[28:29],v249 offset:4096\n"
RT"a[30:31],v249 offset:4608\n"
WT"vmcnt(12) lgkmcnt(15)\n"
"s_barrier\n"
RD"v[192:195],v242 offset:0\n"
RD"v[196:199],v243 offset:0\n"
RD"v[200:203],v242 offset:2048\n"
RD"v[204:207],v243 offset:2048\n"
RD"v[208:211],v242 offset:4096\n"
"v_lshrrev_b32 v131,7,v224\n"
WT"lgkmcnt(4)\n"
MF"v[0:3],a[0:3],v[192:195],0\n"
WT"lgkmcnt(3)\n"
RD"v[212:215],v243 offset:4096\n"
"v_lshl_add_u32 v131,v131,4,v224\n"
MF"v[0:3],a[16:19],v[196:199],v[0:3]\n"
SA"m0,s37,16384\n"
"s_bfe_u32 s48,s46,0x30008\n"
"s_lshl_b32 s48,s48,19\n"
MF"v[4:7],a[20:23],v[196:199],0\n"
"s_mul_i32 s52,s30,1152\n"
BL"v238,s[16:19],s48 offen offset:0 lds\n"
MF"v[4:7],a[4:7],v[192:195],v[4:7]\n"
"v_add_u32 v128,s52,v131\n"
"s_and_b32 s52,s30,1\n"
MF"v[8:11],a[8:11],v[192:195],0\n"
"s_mul_i32 s51,s52,1152\n"
RD"v[216:219],v242 offset:6144\n"
"v_add_u32 v131,s51,v131\n"
MF"v[8:11],a[24:27],v[196:199],v[8:11]\n"
"s_lshl_b32 s52,s52,10\n"
"v_fmac_f32 v188,v162,v182\n"
"v_fmac_f32 v189,v163,v183\n"
MF"v[12:15],a[28:31],v[196:199],0\n"
BL"v239,s[16:19],s48 offen offset:1024 lds\n"
MF"v[12:15],a[12:15],v[192:195],v[12:15]\n"
WT"lgkmcnt(1)\n"
RD"v[220:223],v243 offset:6144\n"
RD"v[132:135],v128 offset:49152\n"
MF"v[16:19],a[0:3],v[200:203],0\n"
WT"vmcnt(10)\n"
RT"a[32:33],v246 offset:8192\n"
RD"a[240:243],v128 offset:53760\n"
MF"v[16:19],a[16:19],v[204:207],v[16:19]\n"
"s_lshr_b32 s50,s31,1\n"
RD"a[244:247],v131 offset:58368\n"
"v_lshlrev_b32 v164,16,v146\n"
MF"v[20:23],a[20:23],v[204:207],0\n"
"s_mul_i32 s50,s50,20480\n"
"v_and_b32 v129,0x70,v224\n"
"v_and_b32 v165,0xffff0000,v146\n"
MF"v[20:23],a[4:7],v[200:203],v[20:23]\n"
"s_and_b32 s51,s31,1\n"
RD"v[192:195],v242 offset:8192\n"
RT"a[34:35],v246 offset:8704\n"
MF"v[24:27],a[8:11],v[200:203],0\n"
"s_lshl_b32 s51,s51,7\n"
BL"v240,s[16:19],s48 offen offset:2048 lds\n"
MF"v[24:27],a[24:27],v[204:207],v[24:27]\n"
SA"s50,s50,s51\n"
"v_and_b32 v130,0x380,v224\n"
"v_fmac_f32 v188,v164,v184\n"
MF"v[28:31],a[28:31],v[204:207],0\n"
SA"s50,s50,524288\n"
RT"a[36:37],v247 offset:8192\n"
"v_lshl_add_u32 v129,v130,1,v129\n"
MF"v[28:31],a[12:15],v[200:203],v[28:31]\n"
"s_lshl_b32 s52,s52,1\n"
RD"v[196:199],v243 offset:8192\n"
"v_fmac_f32 v189,v165,v185\n"
MF"v[32:35],a[0:3],v[208:211],0\n"
SA"s52,s52,s50\n"
BL"v241,s[16:19],s48 offen offset:3072 lds\n"
MF"v[32:35],a[16:19],v[212:215],v[32:35]\n"
SA"s52,s52,16384\n"
RT"a[38:39],v247 offset:8704\n"
"v_lshlrev_b32 v166,16,v147\n"
MF"v[36:39],a[20:23],v[212:215],0\n"
SA"m0,s37,20480\n"
RD"v[200:203],v242 offset:10240\n"
"v_and_b32 v167,0xffff0000,v147\n"
MF"v[36:39],a[4:7],v[208:211],v[36:39]\n"
"s_bfe_u32 s48,s46,0x30008\n"
"v_fmac_f32 v188,v166,v186\n"
"v_fmac_f32 v189,v167,v187\n"
MF"v[40:43],a[8:11],v[208:211],0\n"
"s_lshl_b32 s48,s48,19\n"
RT"a[40:41],v248 offset:8192\n"
"v_add_f32 v188,v188,v189\n"
MF"v[40:43],a[24:27],v[212:215],v[40:43]\n"
"s_bitset1_b32 s48,18\n"
"v_add_f32_dpp v188,v188,v188 quad_perm:[1,0,3,2] row_mask:0xf bank_mask:0xf\n"
"s_lshl_b32 s51,s30,11\n"
MF"v[44:47],a[28:31],v[212:215],0\n"
SA"s50,s50,s51\n"
BL"v238,s[16:19],s48 offen offset:0 lds\n"
MF"v[44:47],a[12:15],v[208:211],v[44:47]\n"
WT"lgkmcnt(3)\n"
RD"v[204:207],v243 offset:10240\n"
RT"a[42:43],v248 offset:8704\n"
MF"v[48:51],a[0:3],v[216:219],0\n"
SA"s51,s50,8192\n"
BS"v[132:135],v129,s[8:11],s50 offen sc1\n"
MF"v[48:51],a[16:19],v[220:223],v[48:51]\n"
"s_lshl_b32 s90,s31,2\n"
BL"v239,s[16:19],s48 offen offset:1024 lds\n"
MF"v[52:55],a[20:23],v[220:223],0\n"
SA"s90,s90,s30\n"
BS"a[240:243],v129,s[8:11],s51 offen sc1\n"
MF"v[52:55],a[4:7],v[216:219],v[52:55]\n"
"s_lshl_b32 s90,s90,1\n"
RD"v[208:211],v242 offset:12288\n"
RT"a[44:45],v249 offset:8192\n"
MF"v[56:59],a[8:11],v[216:219],0\n"
"s_sub_u32 s91,2047,s90\n"
BS"a[244:247],v129,s[8:11],s52 offen sc1\n"
MF"v[56:59],a[24:27],v[220:223],v[56:59]\n"
RD"v[212:215],v243 offset:12288\n"
RT"a[46:47],v249 offset:8704\n"
MF"v[60:63],a[28:31],v[220:223],0\n"
BL"v240,s[16:19],s48 offen offset:2048 lds\n"
MF"v[60:63],a[12:15],v[216:219],v[60:63]\n"
"v_add_f32_dpp v188,v188,v188 quad_perm:[2,3,0,1] row_mask:0xf bank_mask:0xf\n"
MF"v[64:67],a[0:3],v[192:195],0\n"
RD"v[216:219],v242 offset:14336\n"
"v_add_f32_dpp v188,v188,v188 row_half_mirror row_mask:0xf bank_mask:0xf\n"
MF"v[64:67],a[16:19],v[196:199],v[64:67]\n"
WT"vmcnt(14)\n"
RT"a[48:49],v246 offset:12288\n"
"v_add_f32_dpp v188,v188,v188 row_mirror row_mask:0xf bank_mask:0xf\n"
MF"v[68:71],a[20:23],v[196:199],0\n"
"s_lshl_b32 s90,s90,2\n"
BL"v241,s[16:19],s48 offen offset:3072 lds\n"
MF"v[68:71],a[4:7],v[192:195],v[68:71]\n"
SA"s90,s90,8192\n"
RT"a[50:51],v246 offset:12800\n"
"v_mov_b32 v189,v188\n"
MF"v[72:75],a[8:11],v[192:195],0\n"
SA"m0,s36,32768\n"
RD"v[220:223],v243 offset:14336\n"
"v_permlane16_swap_b32 v189,v188\n"
MF"v[72:75],a[24:27],v[196:199],v[72:75]\n"
"s_bfe_u32 s48,s46,0x30008\n"
"v_add_f32 v188,v188,v189\n"
PK"v188,v188,v188\n"
MF"v[76:79],a[28:31],v[196:199],0\n"
"s_lshl_b32 s48,s48,7\n"
RT"a[52:53],v247 offset:12288\n"
"v_lshlrev_b32 v188,16,v188\n"
MF"v[76:79],a[12:15],v[192:195],v[76:79]\n"
WT"lgkmcnt(4)\n"
BL"v234,s[12:15],s48 offen offset:0 lds\n"
MF"v[80:83],a[0:3],v[200:203],0\n"
"v_ashrrev_i32 v189,31,v188\n"
"v_or_b32 v189,0x80000000,v189\n"
MF"v[80:83],a[16:19],v[204:207],v[80:83]\n"
RT"a[54:55],v247 offset:12800\n"
"v_xor_b32 v188,v188,v189\n"
MF"v[84:87],a[20:23],v[204:207],0\n"
BL"v235,s[12:15],s48 offen offset:1024 lds\n"
MF"v[84:87],a[4:7],v[200:203],v[84:87]\n"
"v_and_b32 v188,0xffff0000,v188\n"
"v_lshrrev_b32 v189,9,v224\n"
MF"v[88:91],a[8:11],v[200:203],0\n"
RT"a[56:57],v248 offset:12288\n"
"v_sub_u32 v190,s91,v189\n"
MF"v[88:91],a[24:27],v[204:207],v[88:91]\n"
WT"vmcnt(13) lgkmcnt(3)\n"
"s_barrier\n"
MF"v[92:95],a[28:31],v[204:207],0\n"
RD"v[192:195],v242 offset:16384\n"
"v_or_b32 v188,v188,v190\n"
MF"v[92:95],a[12:15],v[200:203],v[92:95]\n"
RT"a[58:59],v248 offset:12800\n"
"v_and_b32 v190,0x1f0,v224\n"
MF"v[96:99],a[0:3],v[208:211],0\n"
BL"v236,s[12:15],s48 offen offset:2048 lds\n"
MF"v[96:99],a[16:19],v[212:215],v[96:99]\n"
"v_lshlrev_b32 v190,23,v190\n"
"v_lshl_add_u32 v190,v189,2,v190\n"
MF"v[100:103],a[20:23],v[212:215],0\n"
"buffer_store_dword v188,v190,s[8:11],s90 offen sc1\n"
MF"v[100:103],a[4:7],v[208:211],v[100:103]\n"
RD"v[196:199],v243 offset:16384\n"
RT"a[60:61],v249 offset:12288\n"
MF"v[104:107],a[8:11],v[208:211],0\n"
BL"v237,s[12:15],s48 offen offset:3072 lds\n"
MF"v[104:107],a[24:27],v[212:215],v[104:107]\n"
RT"a[62:63],v249 offset:12800\n"
MF"v[108:111],a[28:31],v[212:215],0\n"
SA"m0,s37,0\n"
RD"v[200:203],v242 offset:18432\n"
"s_bfe_u32 s48,s46,0x3000c\n"
MF"v[108:111],a[12:15],v[208:211],v[108:111]\n"
"s_lshl_b32 s48,s48,19\n"
MF"v[112:115],a[0:3],v[216:219],0\n"
BL"v238,s[16:19],s48 offen offset:0 lds\n"
MF"v[112:115],a[16:19],v[220:223],v[112:115]\n"
RD"v[204:207],v243 offset:18432\n"
MF"v[116:119],a[20:23],v[220:223],0\n"
MF"v[116:119],a[4:7],v[216:219],v[116:119]\n"
MF"v[120:123],a[8:11],v[216:219],0\n"
BL"v239,s[16:19],s48 offen offset:1024 lds\n"
MF"v[120:123],a[24:27],v[220:223],v[120:123]\n"
RD"v[208:211],v242 offset:20480\n"
MF"v[124:127],a[28:31],v[220:223],0\n"
MF"v[124:127],a[12:15],v[216:219],v[124:127]\n"
WT"lgkmcnt(2)\n"
MF"v[0:3],a[32:35],v[192:195],v[0:3]\n"
RD"v[212:215],v243 offset:20480\n"
MF"v[0:3],a[48:51],v[196:199],v[0:3]\n"
BL"v240,s[16:19],s48 offen offset:2048 lds\n"
MF"v[4:7],a[52:55],v[196:199],v[4:7]\n"
MF"v[4:7],a[36:39],v[192:195],v[4:7]\n"
MF"v[8:11],a[40:43],v[192:195],v[8:11]\n"
RD"v[216:219],v242 offset:22528\n"
MF"v[8:11],a[56:59],v[196:199],v[8:11]\n"
BL"v241,s[16:19],s48 offen offset:3072 lds\n"
MF"v[12:15],a[60:63],v[196:199],v[12:15]\n"
MF"v[12:15],a[44:47],v[192:195],v[12:15]\n"
RD"v[220:223],v243 offset:22528\n"
MF"v[16:19],a[32:35],v[200:203],v[16:19]\n"
WT"lgkmcnt(2)\n"
WT"vmcnt(16)\n"
RT"a[64:65],v246 offset:16384\n"
MF"v[16:19],a[48:51],v[204:207],v[16:19]\n"
SA"m0,s37,4096\n"
"s_bfe_u32 s48,s46,0x3000c\n"
"s_lshl_b32 s48,s48,19\n"
MF"v[20:23],a[52:55],v[204:207],v[20:23]\n"
"s_bitset1_b32 s48,18\n"
BL"v238,s[16:19],s48 offen offset:0 lds\n"
MF"v[20:23],a[36:39],v[200:203],v[20:23]\n"
RD"v[192:195],v242 offset:24576\n"
RT"a[66:67],v246 offset:16896\n"
MF"v[24:27],a[40:43],v[200:203],v[24:27]\n"
MF"v[24:27],a[56:59],v[204:207],v[24:27]\n"
MF"v[28:31],a[60:63],v[204:207],v[28:31]\n"
RT"a[68:69],v247 offset:16384\n"
MF"v[28:31],a[44:47],v[200:203],v[28:31]\n"
BL"v239,s[16:19],s48 offen offset:1024 lds\n"
MF"v[32:35],a[32:35],v[208:211],v[32:35]\n"
RD"v[196:199],v243 offset:24576\n"
MF"v[32:35],a[48:51],v[212:215],v[32:35]\n"
RT"a[70:71],v247 offset:16896\n"
MF"v[36:39],a[52:55],v[212:215],v[36:39]\n"
RD"v[200:203],v242 offset:26624\n"
MF"v[36:39],a[36:39],v[208:211],v[36:39]\n"
BL"v240,s[16:19],s48 offen offset:2048 lds\n"
MF"v[40:43],a[40:43],v[208:211],v[40:43]\n"
RT"a[72:73],v248 offset:16384\n"
MF"v[40:43],a[56:59],v[212:215],v[40:43]\n"
MF"v[44:47],a[60:63],v[212:215],v[44:47]\n"
RD"v[204:207],v243 offset:26624\n"
MF"v[44:47],a[44:47],v[208:211],v[44:47]\n"
WT"lgkmcnt(4)\n"
RT"a[74:75],v248 offset:16896\n"
MF"v[48:51],a[32:35],v[216:219],v[48:51]\n"
BL"v241,s[16:19],s48 offen offset:3072 lds\n"
MF"v[48:51],a[48:51],v[220:223],v[48:51]\n"
RD"v[208:211],v242 offset:28672\n"
MF"v[52:55],a[52:55],v[220:223],v[52:55]\n"
SA"m0,s36,0\n"
RT"a[76:77],v249 offset:16384\n"
"s_bfe_u32 s48,s46,0x3000c\n"
MF"v[52:55],a[36:39],v[216:219],v[52:55]\n"
"s_lshl_b32 s48,s48,7\n"
MF"v[56:59],a[40:43],v[216:219],v[56:59]\n"
BL"v234,s[12:15],s48 offen offset:0 lds\n"
MF"v[56:59],a[56:59],v[220:223],v[56:59]\n"
RD"v[212:215],v243 offset:28672\n"
RT"a[78:79],v249 offset:16896\n"
MF"v[60:63],a[60:63],v[220:223],v[60:63]\n"
MF"v[60:63],a[44:47],v[216:219],v[60:63]\n"
MF"v[64:67],a[32:35],v[192:195],v[64:67]\n"
RD"v[216:219],v242 offset:30720\n"
MF"v[64:67],a[48:51],v[196:199],v[64:67]\n"
WT"vmcnt(14)\n"
RT"a[80:81],v246 offset:20480\n"
MF"v[68:71],a[52:55],v[196:199],v[68:71]\n"
BL"v235,s[12:15],s48 offen offset:1024 lds\n"
MF"v[68:71],a[36:39],v[192:195],v[68:71]\n"
RT"a[82:83],v246 offset:20992\n"
MF"v[72:75],a[40:43],v[192:195],v[72:75]\n"
RD"v[220:223],v243 offset:30720\n"
MF"v[72:75],a[56:59],v[196:199],v[72:75]\n"
BL"v236,s[12:15],s48 offen offset:2048 lds\n"
MF"v[76:79],a[60:63],v[196:199],v[76:79]\n"
RT"a[84:85],v247 offset:20480\n"
MF"v[76:79],a[44:47],v[192:195],v[76:79]\n"
WT"lgkmcnt(4)\n"
MF"v[80:83],a[32:35],v[200:203],v[80:83]\n"
"s_lshl_b32 s51,s32,8\n"
"s_andn2_b32 s50,s31,7\n"
SA"s50,s50,s51\n"
MF"v[80:83],a[48:51],v[204:207],v[80:83]\n"
SA"s50,s50,256\n"
RT"a[86:87],v247 offset:20992\n"
SA"s50,s50,s45\n"
MF"v[84:87],a[52:55],v[204:207],v[84:87]\n"
BL"v237,s[12:15],s48 offen offset:3072 lds\n"
MF"v[84:87],a[36:39],v[200:203],v[84:87]\n"
MF"v[88:91],a[40:43],v[200:203],v[88:91]\n"
SA"m0,s37,8192\n"
RT"a[88:89],v248 offset:20480\n"
"s_bfe_u32 s48,s46,0x30010\n"
MF"v[88:91],a[56:59],v[204:207],v[88:91]\n"
"s_lshl_b32 s48,s48,19\n"
WT"vmcnt(12) lgkmcnt(3)\n"
"s_barrier\n"
MF"v[92:95],a[60:63],v[204:207],v[92:95]\n"
RD"v[192:195],v242 offset:32768\n"
MF"v[92:95],a[44:47],v[200:203],v[92:95]\n"
BL"v238,s[16:19],s48 offen offset:0 lds\n"
MF"v[96:99],a[32:35],v[208:211],v[96:99]\n"
"buffer_store_dwordx2 v[232:233],v230,s[8:11],s50 offen sc1\n"
MF"v[96:99],a[48:51],v[212:215],v[96:99]\n"
RT"a[90:91],v248 offset:20992\n"
MF"v[100:103],a[52:55],v[212:215],v[100:103]\n"
RD"v[196:199],v243 offset:32768\n"
RT"a[92:93],v249 offset:20480\n"
MF"v[100:103],a[36:39],v[208:211],v[100:103]\n"
BL"v239,s[16:19],s48 offen offset:1024 lds\n"
MF"v[104:107],a[40:43],v[208:211],v[104:107]\n"
MF"v[104:107],a[56:59],v[212:215],v[104:107]\n"
RT"a[94:95],v249 offset:20992\n"
MF"v[108:111],a[60:63],v[212:215],v[108:111]\n"
RD"v[200:203],v242 offset:34816\n"
MF"v[108:111],a[44:47],v[208:211],v[108:111]\n"
BL"v240,s[16:19],s48 offen offset:2048 lds\n"
MF"v[112:115],a[32:35],v[216:219],v[112:115]\n"
MF"v[112:115],a[48:51],v[220:223],v[112:115]\n"
RD"v[204:207],v243 offset:34816\n"
MF"v[116:119],a[52:55],v[220:223],v[116:119]\n"
MF"v[116:119],a[36:39],v[216:219],v[116:119]\n"
BL"v241,s[16:19],s48 offen offset:3072 lds\n"
MF"v[120:123],a[40:43],v[216:219],v[120:123]\n"
MF"v[120:123],a[56:59],v[220:223],v[120:123]\n"
RD"v[208:211],v242 offset:36864\n"
MF"v[124:127],a[60:63],v[220:223],v[124:127]\n"
SA"m0,s37,12288\n"
"s_bfe_u32 s48,s46,0x30010\n"
"s_lshl_b32 s48,s48,19\n"
MF"v[124:127],a[44:47],v[216:219],v[124:127]\n"
WT"lgkmcnt(2)\n"
"s_bitset1_b32 s48,18\n"
MF"v[0:3],a[64:67],v[192:195],v[0:3]\n"
RD"v[212:215],v243 offset:36864\n"
MF"v[0:3],a[80:83],v[196:199],v[0:3]\n"
BL"v238,s[16:19],s48 offen offset:0 lds\n"
MF"v[4:7],a[84:87],v[196:199],v[4:7]\n"
MF"v[4:7],a[68:71],v[192:195],v[4:7]\n"
MF"v[8:11],a[72:75],v[192:195],v[8:11]\n"
RD"v[216:219],v242 offset:38912\n"
MF"v[8:11],a[88:91],v[196:199],v[8:11]\n"
BL"v239,s[16:19],s48 offen offset:1024 lds\n"
MF"v[12:15],a[92:95],v[196:199],v[12:15]\n"
MF"v[12:15],a[76:79],v[192:195],v[12:15]\n"
RD"v[220:223],v243 offset:38912\n"
MF"v[16:19],a[64:67],v[200:203],v[16:19]\n"
WT"lgkmcnt(2)\n"
WT"vmcnt(15)\n"
RT"a[96:97],v246 offset:0\n"
MF"v[16:19],a[80:83],v[204:207],v[16:19]\n"
BL"v240,s[16:19],s48 offen offset:2048 lds\n"
MF"v[20:23],a[84:87],v[204:207],v[20:23]\n"
MF"v[20:23],a[68:71],v[200:203],v[20:23]\n"
RD"v[192:195],v242 offset:40960\n"
MF"v[24:27],a[72:75],v[200:203],v[24:27]\n"
RT"a[98:99],v246 offset:512\n"
MF"v[24:27],a[88:91],v[204:207],v[24:27]\n"
BL"v241,s[16:19],s48 offen offset:3072 lds\n"
MF"v[28:31],a[92:95],v[204:207],v[28:31]\n"
RT"a[100:101],v247 offset:0\n"
MF"v[28:31],a[76:79],v[200:203],v[28:31]\n"
RD"v[196:199],v243 offset:40960\n"
MF"v[32:35],a[64:67],v[208:211],v[32:35]\n"
SA"m0,s36,16384\n"
"s_bfe_u32 s48,s46,0x30010\n"
"s_lshl_b32 s48,s48,7\n"
MF"v[32:35],a[80:83],v[212:215],v[32:35]\n"
RT"a[102:103],v247 offset:512\n"
MF"v[36:39],a[84:87],v[212:215],v[36:39]\n"
RD"v[200:203],v242 offset:43008\n"
MF"v[36:39],a[68:71],v[208:211],v[36:39]\n"
BL"v234,s[12:15],s48 offen offset:0 lds\n"
MF"v[40:43],a[72:75],v[208:211],v[40:43]\n"
RT"a[104:105],v248 offset:0\n"
MF"v[40:43],a[88:91],v[212:215],v[40:43]\n"
MF"v[44:47],a[92:95],v[212:215],v[44:47]\n"
RD"v[204:207],v243 offset:43008\n"
MF"v[44:47],a[76:79],v[208:211],v[44:47]\n"
WT"lgkmcnt(4)\n"
BL"v235,s[12:15],s48 offen offset:1024 lds\n"
MF"v[48:51],a[64:67],v[216:219],v[48:51]\n"
RT"a[106:107],v248 offset:512\n"
MF"v[48:51],a[80:83],v[220:223],v[48:51]\n"
RD"v[208:211],v242 offset:45056\n"
MF"v[52:55],a[84:87],v[220:223],v[52:55]\n"
RT"a[108:109],v249 offset:0\n"
MF"v[52:55],a[68:71],v[216:219],v[52:55]\n"
BL"v236,s[12:15],s48 offen offset:2048 lds\n"
MF"v[56:59],a[72:75],v[216:219],v[56:59]\n"
MF"v[56:59],a[88:91],v[220:223],v[56:59]\n"
RD"v[212:215],v243 offset:45056\n"
MF"v[60:63],a[92:95],v[220:223],v[60:63]\n"
RT"a[110:111],v249 offset:512\n"
MF"v[60:63],a[76:79],v[216:219],v[60:63]\n"
BL"v237,s[12:15],s48 offen offset:3072 lds\n"
MF"v[64:67],a[64:67],v[192:195],v[64:67]\n"
RD"v[216:219],v242 offset:47104\n"
MF"v[64:67],a[80:83],v[196:199],v[64:67]\n"
WT"vmcnt(17)\n"
RT"a[112:113],v246 offset:4096\n"
MF"v[68:71],a[84:87],v[196:199],v[68:71]\n"
SA"m0,s37,16384\n"
"s_bfe_u32 s48,s46,0x30014\n"
"s_lshl_b32 s48,s48,19\n"
MF"v[68:71],a[68:71],v[192:195],v[68:71]\n"
RT"a[114:115],v246 offset:4608\n"
MF"v[72:75],a[72:75],v[192:195],v[72:75]\n"
RD"v[220:223],v243 offset:47104\n"
MF"v[72:75],a[88:91],v[196:199],v[72:75]\n"
BL"v238,s[16:19],s48 offen offset:0 lds\n"
MF"v[76:79],a[92:95],v[196:199],v[76:79]\n"
RT"a[116:117],v247 offset:4096\n"
MF"v[76:79],a[76:79],v[192:195],v[76:79]\n"
WT"lgkmcnt(4)\n"
MF"v[80:83],a[64:67],v[200:203],v[80:83]\n"
BL"v239,s[16:19],s48 offen offset:1024 lds\n"
MF"v[80:83],a[80:83],v[204:207],v[80:83]\n"
RT"a[118:119],v247 offset:4608\n"
MF"v[84:87],a[84:87],v[204:207],v[84:87]\n"
MF"v[84:87],a[68:71],v[200:203],v[84:87]\n"
MF"v[88:91],a[72:75],v[200:203],v[88:91]\n"
RT"a[120:121],v248 offset:4096\n"
MF"v[88:91],a[88:91],v[204:207],v[88:91]\n"
WT"vmcnt(15) lgkmcnt(3)\n"
"s_barrier\n"
MF"v[92:95],a[92:95],v[204:207],v[92:95]\n"
BL"v240,s[16:19],s48 offen offset:2048 lds\n"
MF"v[92:95],a[76:79],v[200:203],v[92:95]\n"
RD"v[192:195],v242 offset:0\n"
RT"a[122:123],v248 offset:4608\n"
MF"v[96:99],a[64:67],v[208:211],v[96:99]\n"
MF"v[96:99],a[80:83],v[212:215],v[96:99]\n"
BL"v241,s[16:19],s48 offen offset:3072 lds\n"
MF"v[100:103],a[84:87],v[212:215],v[100:103]\n"
RD"v[196:199],v243 offset:0\n"
RT"a[124:125],v249 offset:4096\n"
MF"v[100:103],a[68:71],v[208:211],v[100:103]\n"
MF"v[104:107],a[72:75],v[208:211],v[104:107]\n"
SA"m0,s37,20480\n"
"s_bfe_u32 s48,s46,0x30014\n"
"s_lshl_b32 s48,s48,19\n"
MF"v[104:107],a[88:91],v[212:215],v[104:107]\n"
"s_bitset1_b32 s48,18\n"
RT"a[126:127],v249 offset:4608\n"
MF"v[108:111],a[92:95],v[212:215],v[108:111]\n"
RD"v[200:203],v242 offset:2048\n"
MF"v[108:111],a[76:79],v[208:211],v[108:111]\n"
BL"v238,s[16:19],s48 offen offset:0 lds\n"
MF"v[112:115],a[64:67],v[216:219],v[112:115]\n"
MF"v[112:115],a[80:83],v[220:223],v[112:115]\n"
RD"v[204:207],v243 offset:2048\n"
MF"v[116:119],a[84:87],v[220:223],v[116:119]\n"
BL"v239,s[16:19],s48 offen offset:1024 lds\n"
MF"v[116:119],a[68:71],v[216:219],v[116:119]\n"
MF"v[120:123],a[72:75],v[216:219],v[120:123]\n"
MF"v[120:123],a[88:91],v[220:223],v[120:123]\n"
RD"v[208:211],v242 offset:4096\n"
MF"v[124:127],a[92:95],v[220:223],v[124:127]\n"
MF"v[124:127],a[76:79],v[216:219],v[124:127]\n"
WT"lgkmcnt(2)\n"
BL"v240,s[16:19],s48 offen offset:2048 lds\n"
MF"v[0:3],a[96:99],v[192:195],v[0:3]\n"
RD"v[212:215],v243 offset:4096\n"
MF"v[0:3],a[112:115],v[196:199],v[0:3]\n"
MF"v[4:7],a[116:119],v[196:199],v[4:7]\n"
MF"v[4:7],a[100:103],v[192:195],v[4:7]\n"
BL"v241,s[16:19],s48 offen offset:3072 lds\n"
MF"v[8:11],a[104:107],v[192:195],v[8:11]\n"
RD"v[216:219],v242 offset:6144\n"
MF"v[8:11],a[120:123],v[196:199],v[8:11]\n"
MF"v[12:15],a[124:127],v[196:199],v[12:15]\n"
SA"m0,s36,32768\n"
"s_bfe_u32 s48,s46,0x30014\n"
"s_lshl_b32 s48,s48,7\n"
MF"v[12:15],a[108:111],v[192:195],v[12:15]\n"
RD"v[220:223],v243 offset:6144\n"
MF"v[16:19],a[96:99],v[200:203],v[16:19]\n"
WT"lgkmcnt(2)\n"
WT"vmcnt(16)\n"
RT"a[128:129],v246 offset:8192\n"
MF"v[16:19],a[112:115],v[204:207],v[16:19]\n"
BL"v234,s[12:15],s48 offen offset:0 lds\n"
MF"v[20:23],a[116:119],v[204:207],v[20:23]\n"
MF"v[20:23],a[100:103],v[200:203],v[20:23]\n"
RD"v[192:195],v242 offset:8192\n"
MF"v[24:27],a[104:107],v[200:203],v[24:27]\n"
RT"a[130:131],v246 offset:8704\n"
MF"v[24:27],a[120:123],v[204:207],v[24:27]\n"
BL"v235,s[12:15],s48 offen offset:1024 lds\n"
MF"v[28:31],a[124:127],v[204:207],v[28:31]\n"
RT"a[132:133],v247 offset:8192\n"
MF"v[28:31],a[108:111],v[200:203],v[28:31]\n"
RD"v[196:199],v243 offset:8192\n"
MF"v[32:35],a[96:99],v[208:211],v[32:35]\n"
MF"v[32:35],a[112:115],v[212:215],v[32:35]\n"
RT"a[134:135],v247 offset:8704\n"
MF"v[36:39],a[116:119],v[212:215],v[36:39]\n"
BL"v236,s[12:15],s48 offen offset:2048 lds\n"
MF"v[36:39],a[100:103],v[208:211],v[36:39]\n"
RD"v[200:203],v242 offset:10240\n"
MF"v[40:43],a[104:107],v[208:211],v[40:43]\n"
RT"a[136:137],v248 offset:8192\n"
MF"v[40:43],a[120:123],v[212:215],v[40:43]\n"
BL"v237,s[12:15],s48 offen offset:3072 lds\n"
MF"v[44:47],a[124:127],v[212:215],v[44:47]\n"
RD"v[204:207],v243 offset:10240\n"
MF"v[44:47],a[108:111],v[208:211],v[44:47]\n"
WT"lgkmcnt(4)\n"
RT"a[138:139],v248 offset:8704\n"
MF"v[48:51],a[96:99],v[216:219],v[48:51]\n"
SA"m0,s37,0\n"
"s_bfe_u32 s48,s46,0x30018\n"
"s_lshl_b32 s48,s48,19\n"
MF"v[48:51],a[112:115],v[220:223],v[48:51]\n"
RD"v[208:211],v242 offset:12288\n"
MF"v[52:55],a[116:119],v[220:223],v[52:55]\n"
RT"a[140:141],v249 offset:8192\n"
MF"v[52:55],a[100:103],v[216:219],v[52:55]\n"
BL"v238,s[16:19],s48 offen offset:0 lds\n"
MF"v[56:59],a[104:107],v[216:219],v[56:59]\n"
MF"v[56:59],a[120:123],v[220:223],v[56:59]\n"
RD"v[212:215],v243 offset:12288\n"
MF"v[60:63],a[124:127],v[220:223],v[60:63]\n"
RT"a[142:143],v249 offset:8704\n"
MF"v[60:63],a[108:111],v[216:219],v[60:63]\n"
BL"v239,s[16:19],s48 offen offset:1024 lds\n"
MF"v[64:67],a[96:99],v[192:195],v[64:67]\n"
RD"v[216:219],v242 offset:14336\n"
MF"v[64:67],a[112:115],v[196:199],v[64:67]\n"
WT"vmcnt(18)\n"
RT"a[144:145],v246 offset:12288\n"
MF"v[68:71],a[116:119],v[196:199],v[68:71]\n"
MF"v[68:71],a[100:103],v[192:195],v[68:71]\n"
RT"a[146:147],v246 offset:12800\n"
MF"v[72:75],a[104:107],v[192:195],v[72:75]\n"
BL"v240,s[16:19],s48 offen offset:2048 lds\n"
MF"v[72:75],a[120:123],v[196:199],v[72:75]\n"
RD"v[220:223],v243 offset:14336\n"
MF"v[76:79],a[124:127],v[196:199],v[76:79]\n"
RT"a[148:149],v247 offset:12288\n"
MF"v[76:79],a[108:111],v[192:195],v[76:79]\n"
WT"lgkmcnt(4)\n"
BL"v241,s[16:19],s48 offen offset:3072 lds\n"
MF"v[80:83],a[96:99],v[200:203],v[80:83]\n"
MF"v[80:83],a[112:115],v[204:207],v[80:83]\n"
RT"a[150:151],v247 offset:12800\n"
MF"v[84:87],a[116:119],v[204:207],v[84:87]\n"
SA"m0,s37,4096\n"
"s_bfe_u32 s48,s46,0x30018\n"
"s_lshl_b32 s48,s48,19\n"
MF"v[84:87],a[100:103],v[200:203],v[84:87]\n"
"s_bitset1_b32 s48,18\n"
MF"v[88:91],a[104:107],v[200:203],v[88:91]\n"
RT"a[152:153],v248 offset:12288\n"
MF"v[88:91],a[120:123],v[204:207],v[88:91]\n"
BL"v238,s[16:19],s48 offen offset:0 lds\n"
MF"v[92:95],a[124:127],v[204:207],v[92:95]\n"
WT"vmcnt(17) lgkmcnt(3)\n"
"s_barrier\n"
MF"v[92:95],a[108:111],v[200:203],v[92:95]\n"
RD"v[192:195],v242 offset:16384\n"
RT"a[154:155],v248 offset:12800\n"
MF"v[96:99],a[96:99],v[208:211],v[96:99]\n"
BL"v239,s[16:19],s48 offen offset:1024 lds\n"
MF"v[96:99],a[112:115],v[212:215],v[96:99]\n"
MF"v[100:103],a[116:119],v[212:215],v[100:103]\n"
RD"v[196:199],v243 offset:16384\n"
MF"v[100:103],a[100:103],v[208:211],v[100:103]\n"
RT"a[156:157],v249 offset:12288\n"
MF"v[104:107],a[104:107],v[208:211],v[104:107]\n"
MF"v[104:107],a[120:123],v[212:215],v[104:107]\n"
RT"a[158:159],v249 offset:12800\n"
MF"v[108:111],a[124:127],v[212:215],v[108:111]\n"
BL"v240,s[16:19],s48 offen offset:2048 lds\n"
MF"v[108:111],a[108:111],v[208:211],v[108:111]\n"
RD"v[200:203],v242 offset:18432\n"
MF"v[112:115],a[96:99],v[216:219],v[112:115]\n"
MF"v[112:115],a[112:115],v[220:223],v[112:115]\n"
RD"v[204:207],v243 offset:18432\n"
MF"v[116:119],a[116:119],v[220:223],v[116:119]\n"
BL"v241,s[16:19],s48 offen offset:3072 lds\n"
MF"v[116:119],a[100:103],v[216:219],v[116:119]\n"
MF"v[120:123],a[104:107],v[216:219],v[120:123]\n"
SA"m0,s36,0\n"
"s_bfe_u32 s48,s46,0x30018\n"
"s_lshl_b32 s48,s48,7\n"
MF"v[120:123],a[120:123],v[220:223],v[120:123]\n"
RD"v[208:211],v242 offset:20480\n"
MF"v[124:127],a[124:127],v[220:223],v[124:127]\n"
BL"v234,s[12:15],s48 offen offset:0 lds\n"
MF"v[124:127],a[108:111],v[216:219],v[124:127]\n"
WT"lgkmcnt(2)\n"
MF"v[0:3],a[128:131],v[192:195],v[0:3]\n"
RD"v[212:215],v243 offset:20480\n"
MF"v[0:3],a[144:147],v[196:199],v[0:3]\n"
MF"v[4:7],a[148:151],v[196:199],v[4:7]\n"
BL"v235,s[12:15],s48 offen offset:1024 lds\n"
MF"v[4:7],a[132:135],v[192:195],v[4:7]\n"
MF"v[8:11],a[136:139],v[192:195],v[8:11]\n"
RD"v[216:219],v242 offset:22528\n"
MF"v[8:11],a[152:155],v[196:199],v[8:11]\n"
MF"v[12:15],a[156:159],v[196:199],v[12:15]\n"
MF"v[12:15],a[140:143],v[192:195],v[12:15]\n"
RD"v[220:223],v243 offset:22528\n"
MF"v[16:19],a[128:131],v[200:203],v[16:19]\n"
WT"lgkmcnt(2)\n"
BL"v236,s[12:15],s48 offen offset:2048 lds\n"
MF"v[16:19],a[144:147],v[204:207],v[16:19]\n"
WT"vmcnt(19)\n"
RT"a[160:161],v246 offset:16384\n"
MF"v[20:23],a[148:151],v[204:207],v[20:23]\n"
MF"v[20:23],a[132:135],v[200:203],v[20:23]\n"
RD"v[192:195],v242 offset:24576\n"
MF"v[24:27],a[136:139],v[200:203],v[24:27]\n"
RT"a[162:163],v246 offset:16896\n"
MF"v[24:27],a[152:155],v[204:207],v[24:27]\n"
BL"v237,s[12:15],s48 offen offset:3072 lds\n"
MF"v[28:31],a[156:159],v[204:207],v[28:31]\n"
SA"m0,s37,8192\n"
RT"a[164:165],v247 offset:16384\n"
"s_bfe_u32 s48,s46,0x3001c\n"
MF"v[28:31],a[140:143],v[200:203],v[28:31]\n"
"s_lshl_b32 s48,s48,19\n"
RD"v[196:199],v243 offset:24576\n"
MF"v[32:35],a[128:131],v[208:211],v[32:35]\n"
BL"v238,s[16:19],s48 offen offset:0 lds\n"
MF"v[32:35],a[144:147],v[212:215],v[32:35]\n"
RT"a[166:167],v247 offset:16896\n"
MF"v[36:39],a[148:151],v[212:215],v[36:39]\n"
RD"v[200:203],v242 offset:26624\n"
MF"v[36:39],a[132:135],v[208:211],v[36:39]\n"
MF"v[40:43],a[136:139],v[208:211],v[40:43]\n"
RT"a[168:169],v248 offset:16384\n"
MF"v[40:43],a[152:155],v[212:215],v[40:43]\n"
BL"v239,s[16:19],s48 offen offset:1024 lds\n"
MF"v[44:47],a[156:159],v[212:215],v[44:47]\n"
RD"v[204:207],v243 offset:26624\n"
MF"v[44:47],a[140:143],v[208:211],v[44:47]\n"
WT"lgkmcnt(4)\n"
RT"a[170:171],v248 offset:16896\n"
MF"v[48:51],a[128:131],v[216:219],v[48:51]\n"
MF"v[48:51],a[144:147],v[220:223],v[48:51]\n"
RD"v[208:211],v242 offset:28672\n"
MF"v[52:55],a[148:151],v[220:223],v[52:55]\n"
BL"v240,s[16:19],s48 offen offset:2048 lds\n"
MF"v[52:55],a[132:135],v[216:219],v[52:55]\n"
RT"a[172:173],v249 offset:16384\n"
MF"v[56:59],a[136:139],v[216:219],v[56:59]\n"
MF"v[56:59],a[152:155],v[220:223],v[56:59]\n"
RD"v[212:215],v243 offset:28672\n"
MF"v[60:63],a[156:159],v[220:223],v[60:63]\n"
RT"a[174:175],v249 offset:16896\n"
MF"v[60:63],a[140:143],v[216:219],v[60:63]\n"
BL"v241,s[16:19],s48 offen offset:3072 lds\n"
MF"v[64:67],a[128:131],v[192:195],v[64:67]\n"
SA"m0,s37,12288\n"
RD"v[216:219],v242 offset:30720\n"
"s_bfe_u32 s48,s46,0x3001c\n"
MF"v[64:67],a[144:147],v[196:199],v[64:67]\n"
"s_lshl_b32 s48,s48,19\n"
WT"vmcnt(20)\n"
RT"a[176:177],v246 offset:20480\n"
MF"v[68:71],a[148:151],v[196:199],v[68:71]\n"
"s_bitset1_b32 s48,18\n"
BL"v238,s[16:19],s48 offen offset:0 lds\n"
MF"v[68:71],a[132:135],v[192:195],v[68:71]\n"
RT"a[178:179],v246 offset:20992\n"
MF"v[72:75],a[136:139],v[192:195],v[72:75]\n"
RD"v[220:223],v243 offset:30720\n"
MF"v[72:75],a[152:155],v[196:199],v[72:75]\n"
MF"v[76:79],a[156:159],v[196:199],v[76:79]\n"
RT"a[180:181],v247 offset:20480\n"
MF"v[76:79],a[140:143],v[192:195],v[76:79]\n"
WT"lgkmcnt(4)\n"
BL"v239,s[16:19],s48 offen offset:1024 lds\n"
MF"v[80:83],a[128:131],v[200:203],v[80:83]\n"
MF"v[80:83],a[144:147],v[204:207],v[80:83]\n"
RT"a[182:183],v247 offset:20992\n"
MF"v[84:87],a[148:151],v[204:207],v[84:87]\n"
MF"v[84:87],a[132:135],v[200:203],v[84:87]\n"
BL"v240,s[16:19],s48 offen offset:2048 lds\n"
MF"v[88:91],a[136:139],v[200:203],v[88:91]\n"
RT"a[184:185],v248 offset:20480\n"
MF"v[88:91],a[152:155],v[204:207],v[88:91]\n"
WT"vmcnt(19) lgkmcnt(3)\n"
"s_barrier\n"
MF"v[92:95],a[156:159],v[204:207],v[92:95]\n"
RD"v[192:195],v242 offset:32768\n"
MF"v[92:95],a[140:143],v[200:203],v[92:95]\n"
RT"a[186:187],v248 offset:20992\n"
MF"v[96:99],a[128:131],v[208:211],v[96:99]\n"
BL"v241,s[16:19],s48 offen offset:3072 lds\n"
MF"v[96:99],a[144:147],v[212:215],v[96:99]\n"
MF"v[100:103],a[148:151],v[212:215],v[100:103]\n"
SA"m0,s36,16384\n"
RD"v[196:199],v243 offset:32768\n"
"s_bfe_u32 s48,s46,0x3001c\n"
MF"v[100:103],a[132:135],v[208:211],v[100:103]\n"
"s_lshl_b32 s48,s48,7\n"
RT"a[188:189],v249 offset:20480\n"
MF"v[104:107],a[136:139],v[208:211],v[104:107]\n"
BL"v234,s[12:15],s48 offen offset:0 lds\n"
MF"v[104:107],a[152:155],v[212:215],v[104:107]\n"
RT"a[190:191],v249 offset:20992\n"
MF"v[108:111],a[156:159],v[212:215],v[108:111]\n"
RD"v[200:203],v242 offset:34816\n"
MF"v[108:111],a[140:143],v[208:211],v[108:111]\n"
MF"v[112:115],a[128:131],v[216:219],v[112:115]\n"
BL"v235,s[12:15],s48 offen offset:1024 lds\n"
MF"v[112:115],a[144:147],v[220:223],v[112:115]\n"
RD"v[204:207],v243 offset:34816\n"
MF"v[116:119],a[148:151],v[220:223],v[116:119]\n"
MF"v[116:119],a[132:135],v[216:219],v[116:119]\n"
MF"v[120:123],a[136:139],v[216:219],v[120:123]\n"
MF"v[120:123],a[152:155],v[220:223],v[120:123]\n"
RD"v[208:211],v242 offset:36864\n"
MF"v[124:127],a[156:159],v[220:223],v[124:127]\n"
BL"v236,s[12:15],s48 offen offset:2048 lds\n"
MF"v[124:127],a[140:143],v[216:219],v[124:127]\n"
WT"lgkmcnt(2)\n"
MF"v[0:3],a[160:163],v[192:195],v[0:3]\n"
RD"v[212:215],v243 offset:36864\n"
MF"v[0:3],a[176:179],v[196:199],v[0:3]\n"
BL"v237,s[12:15],s48 offen offset:3072 lds\n"
MF"v[4:7],a[180:183],v[196:199],v[4:7]\n"
MF"v[4:7],a[164:167],v[192:195],v[4:7]\n"
MF"v[8:11],a[168:171],v[192:195],v[8:11]\n"
SA"m0,s37,16384\n"
RD"v[216:219],v242 offset:38912\n"
SA"s48,s39,0\n"
MF"v[8:11],a[184:187],v[196:199],v[8:11]\n"
MF"v[12:15],a[188:191],v[196:199],v[12:15]\n"
BL"v234,s[12:15],s48 offen offset:0 lds\n"
MF"v[12:15],a[172:175],v[192:195],v[12:15]\n"
RD"v[220:223],v243 offset:38912\n"
MF"v[16:19],a[160:163],v[200:203],v[16:19]\n"
WT"lgkmcnt(2)\n"
WT"vmcnt(21)\n"
RT"a[192:193],v246 offset:0\n"
MF"v[16:19],a[176:179],v[204:207],v[16:19]\n"
MF"v[20:23],a[180:183],v[204:207],v[20:23]\n"
BL"v235,s[12:15],s48 offen offset:1024 lds\n"
MF"v[20:23],a[164:167],v[200:203],v[20:23]\n"
RD"v[192:195],v242 offset:40960\n"
RT"a[194:195],v246 offset:512\n"
MF"v[24:27],a[168:171],v[200:203],v[24:27]\n"
MF"v[24:27],a[184:187],v[204:207],v[24:27]\n"
MF"v[28:31],a[188:191],v[204:207],v[28:31]\n"
RT"a[196:197],v247 offset:0\n"
MF"v[28:31],a[172:175],v[200:203],v[28:31]\n"
RD"v[196:199],v243 offset:40960\n"
MF"v[32:35],a[160:163],v[208:211],v[32:35]\n"
BL"v236,s[12:15],s48 offen offset:2048 lds\n"
MF"v[32:35],a[176:179],v[212:215],v[32:35]\n"
RT"a[198:199],v247 offset:512\n"
MF"v[36:39],a[180:183],v[212:215],v[36:39]\n"
RD"v[200:203],v242 offset:43008\n"
MF"v[36:39],a[164:167],v[208:211],v[36:39]\n"
BL"v237,s[12:15],s48 offen offset:3072 lds\n"
MF"v[40:43],a[168:171],v[208:211],v[40:43]\n"
RT"a[200:201],v248 offset:0\n"
MF"v[40:43],a[184:187],v[212:215],v[40:43]\n"
MF"v[44:47],a[188:191],v[212:215],v[44:47]\n"
SA"m0,s37,20480\n"
RD"v[204:207],v243 offset:43008\n"
SA"s48,s98,0\n"
MF"v[44:47],a[172:175],v[208:211],v[44:47]\n"
WT"lgkmcnt(4)\n"
RT"a[202:203],v248 offset:512\n"
"s_lshr_b32 s50,s30,1\n"
MF"v[48:51],a[160:163],v[216:219],v[48:51]\n"
"s_and_b32 s51,s30,1\n"
BL"v234,s[12:15],s48 offen offset:0 lds\n"
MF"v[48:51],a[176:179],v[220:223],v[48:51]\n"
"s_mul_i32 s38,s50,24576\n"
RD"v[208:211],v242 offset:45056\n"
MF"v[52:55],a[180:183],v[220:223],v[52:55]\n"
"s_lshl_b32 s60,s50,18\n"
RT"a[204:205],v249 offset:0\n"
MF"v[52:55],a[164:167],v[216:219],v[52:55]\n"
"s_lshl_b32 s50,s51,12\n"
MF"v[56:59],a[168:171],v[216:219],v[56:59]\n"
SA"s38,s38,s50\n"
BL"v235,s[12:15],s48 offen offset:1024 lds\n"
MF"v[56:59],a[184:187],v[220:223],v[56:59]\n"
"s_lshl_b32 s50,s51,7\n"
RD"v[212:215],v243 offset:45056\n"
RT"a[206:207],v249 offset:512\n"
MF"v[60:63],a[188:191],v[220:223],v[60:63]\n"
SA"s60,s60,s50\n"
MF"v[60:63],a[172:175],v[216:219],v[60:63]\n"
MF"v[64:67],a[160:163],v[192:195],v[64:67]\n"
RD"v[216:219],v242 offset:47104\n"
MF"v[64:67],a[176:179],v[196:199],v[64:67]\n"
"s_sub_u32 s99,s34,5\n"
WT"vmcnt(22)\n"
RT"a[208:209],v246 offset:4096\n"
MF"v[68:71],a[180:183],v[196:199],v[68:71]\n"
"s_cmp_lt_u32 s34,5\n"
BL"v236,s[12:15],s48 offen offset:2048 lds\n"
MF"v[68:71],a[164:167],v[192:195],v[68:71]\n"
"s_cselect_b32 s99,s34,s99\n"
RT"a[210:211],v246 offset:4608\n"
"s_mul_i32 s99,s99,1\n"
MF"v[72:75],a[168:171],v[192:195],v[72:75]\n"
"s_lshl_b32 s61,s99,20\n"
RD"v[220:223],v243 offset:47104\n"
MF"v[72:75],a[184:187],v[196:199],v[72:75]\n"
BL"v237,s[12:15],s48 offen offset:3072 lds\n"
MF"v[76:79],a[188:191],v[196:199],v[76:79]\n"
RT"a[212:213],v247 offset:4096\n"
MF"v[76:79],a[172:175],v[192:195],v[76:79]\n"
WT"lgkmcnt(4)\n"
MF"v[80:83],a[160:163],v[200:203],v[80:83]\n"
SA"m0,s37,0\n"
SA"s48,s39,32768\n"
MF"v[80:83],a[176:179],v[204:207],v[80:83]\n"
RT"a[214:215],v247 offset:4608\n"
MF"v[84:87],a[180:183],v[204:207],v[84:87]\n"
BL"v234,s[12:15],s48 offen offset:0 lds\n"
MF"v[84:87],a[164:167],v[200:203],v[84:87]\n"
MF"v[88:91],a[168:171],v[200:203],v[88:91]\n"
RT"a[216:217],v248 offset:4096\n"
MF"v[88:91],a[184:187],v[204:207],v[88:91]\n"
WT"vmcnt(21) lgkmcnt(3)\n"
"s_barrier\n"
MF"v[92:95],a[188:191],v[204:207],v[92:95]\n"
RD"v[192:195],v242 offset:0\n"
MF"v[92:95],a[172:175],v[200:203],v[92:95]\n"
BL"v235,s[12:15],s48 offen offset:1024 lds\n"
MF"v[96:99],a[160:163],v[208:211],v[96:99]\n"
RT"a[218:219],v248 offset:4608\n"
MF"v[96:99],a[176:179],v[212:215],v[96:99]\n"
MF"v[100:103],a[180:183],v[212:215],v[100:103]\n"
RD"v[196:199],v243 offset:0\n"
MF"v[100:103],a[164:167],v[208:211],v[100:103]\n"
RT"a[220:221],v249 offset:4096\n"
MF"v[104:107],a[168:171],v[208:211],v[104:107]\n"
BL"v236,s[12:15],s48 offen offset:2048 lds\n"
MF"v[104:107],a[184:187],v[212:215],v[104:107]\n"
RT"a[222:223],v249 offset:4608\n"
MF"v[108:111],a[188:191],v[212:215],v[108:111]\n"
RD"v[200:203],v242 offset:2048\n"
MF"v[108:111],a[172:175],v[208:211],v[108:111]\n"
BL"v237,s[12:15],s48 offen offset:3072 lds\n"
MF"v[112:115],a[160:163],v[216:219],v[112:115]\n"
MF"v[112:115],a[176:179],v[220:223],v[112:115]\n"
RD"v[204:207],v243 offset:2048\n"
MF"v[116:119],a[180:183],v[220:223],v[116:119]\n"
SA"m0,s37,4096\n"
SA"s48,s98,32768\n"
MF"v[116:119],a[164:167],v[216:219],v[116:119]\n"
MF"v[120:123],a[168:171],v[216:219],v[120:123]\n"
WT"lgkmcnt(2)\n"
BL"v234,s[12:15],s48 offen offset:0 lds\n"
MF"v[120:123],a[184:187],v[220:223],v[120:123]\n"
RD"v[208:211],v242 offset:4096\n"
MF"v[124:127],a[188:191],v[220:223],v[124:127]\n"
MF"v[124:127],a[172:175],v[216:219],v[124:127]\n"
MF"v[0:3],a[192:195],v[192:195],v[0:3]\n"
RD"v[212:215],v243 offset:4096\n"
MF"v[0:3],a[208:211],v[196:199],v[0:3]\n"
BL"v235,s[12:15],s48 offen offset:1024 lds\n"
MF"v[4:7],a[212:215],v[196:199],v[4:7]\n"
MF"v[4:7],a[196:199],v[192:195],v[4:7]\n"
MF"v[8:11],a[200:203],v[192:195],v[8:11]\n"
RD"v[216:219],v242 offset:6144\n"
MF"v[8:11],a[216:219],v[196:199],v[8:11]\n"
BL"v236,s[12:15],s48 offen offset:2048 lds\n"
MF"v[12:15],a[220:223],v[196:199],v[12:15]\n"
MF"v[12:15],a[204:207],v[192:195],v[12:15]\n"
WT"lgkmcnt(1)\n"
RD"v[220:223],v243 offset:6144\n"
MF"v[16:19],a[192:195],v[200:203],v[16:19]\n"
WT"vmcnt(23)\n"
RT"a[224:225],v246 offset:8192\n"
MF"v[16:19],a[208:211],v[204:207],v[16:19]\n"
BL"v237,s[12:15],s48 offen offset:3072 lds\n"
MF"v[20:23],a[212:215],v[204:207],v[20:23]\n"
MF"v[20:23],a[196:199],v[200:203],v[20:23]\n"
RD"v[192:195],v242 offset:8192\n"
MF"v[24:27],a[200:203],v[200:203],v[24:27]\n"
RT"a[226:227],v246 offset:8704\n"
MF"v[24:27],a[216:219],v[204:207],v[24:27]\n"
MF"v[28:31],a[220:223],v[204:207],v[28:31]\n"
RT"a[228:229],v247 offset:8192\n"
MF"v[28:31],a[204:207],v[200:203],v[28:31]\n"
RD"v[196:199],v243 offset:8192\n"
MF"v[32:35],a[192:195],v[208:211],v[32:35]\n"
MF"v[32:35],a[208:211],v[212:215],v[32:35]\n"
RT"a[230:231],v247 offset:8704\n"
MF"v[36:39],a[212:215],v[212:215],v[36:39]\n"
RD"v[200:203],v242 offset:10240\n"
MF"v[36:39],a[196:199],v[208:211],v[36:39]\n"
MF"v[40:43],a[200:203],v[208:211],v[40:43]\n"
RT"a[232:233],v248 offset:8192\n"
MF"v[40:43],a[216:219],v[212:215],v[40:43]\n"
MF"v[44:47],a[220:223],v[212:215],v[44:47]\n"
RD"v[204:207],v243 offset:10240\n"
MF"v[44:47],a[204:207],v[208:211],v[44:47]\n"
WT"lgkmcnt(4)\n"
RT"a[234:235],v248 offset:8704\n"
MF"v[48:51],a[192:195],v[216:219],v[48:51]\n"
MF"v[48:51],a[208:211],v[220:223],v[48:51]\n"
RD"v[208:211],v242 offset:12288\n"
MF"v[52:55],a[212:215],v[220:223],v[52:55]\n"
RT"a[236:237],v249 offset:8192\n"
MF"v[52:55],a[196:199],v[216:219],v[52:55]\n"
MF"v[56:59],a[200:203],v[216:219],v[56:59]\n"
MF"v[56:59],a[216:219],v[220:223],v[56:59]\n"
RD"v[212:215],v243 offset:12288\n"
MF"v[60:63],a[220:223],v[220:223],v[60:63]\n"
RT"a[238:239],v249 offset:8704\n"
MF"v[60:63],a[204:207],v[216:219],v[60:63]\n"
MF"v[64:67],a[192:195],v[192:195],v[64:67]\n"
RD"v[216:219],v242 offset:14336\n"
MF"v[64:67],a[208:211],v[196:199],v[64:67]\n"
SA"m0,s37,8192\n"
WT"vmcnt(20)\n"
RT"a[240:241],v246 offset:12288\n"
MF"v[68:71],a[212:215],v[196:199],v[68:71]\n"
SA"s48,s39,65536\n"
MF"v[68:71],a[196:199],v[192:195],v[68:71]\n"
RT"a[242:243],v246 offset:12800\n"
MF"v[72:75],a[200:203],v[192:195],v[72:75]\n"
WT"lgkmcnt(3)\n"
BL"v234,s[12:15],s48 offen offset:0 lds\n"
MF"v[72:75],a[216:219],v[196:199],v[72:75]\n"
RD"v[220:223],v243 offset:14336\n"
MF"v[76:79],a[220:223],v[196:199],v[76:79]\n"
RT"a[244:245],v247 offset:12288\n"
MF"v[76:79],a[204:207],v[192:195],v[76:79]\n"
BL"v235,s[12:15],s48 offen offset:1024 lds\n"
MF"v[80:83],a[192:195],v[200:203],v[80:83]\n"
MF"v[80:83],a[208:211],v[204:207],v[80:83]\n"
RT"a[246:247],v247 offset:12800\n"
MF"v[84:87],a[212:215],v[204:207],v[84:87]\n"
MF"v[84:87],a[196:199],v[200:203],v[84:87]\n"
MF"v[88:91],a[200:203],v[200:203],v[88:91]\n"
RT"a[248:249],v248 offset:12288\n"
MF"v[88:91],a[216:219],v[204:207],v[88:91]\n"
BL"v236,s[12:15],s48 offen offset:2048 lds\n"
MF"v[92:95],a[220:223],v[204:207],v[92:95]\n"
WT"vmcnt(19) lgkmcnt(3)\n"
"s_barrier\n"
MF"v[92:95],a[204:207],v[200:203],v[92:95]\n"
RD"v[192:195],v242 offset:16384\n"
RT"a[250:251],v248 offset:12800\n"
MF"v[96:99],a[192:195],v[208:211],v[96:99]\n"
BL"v237,s[12:15],s48 offen offset:3072 lds\n"
MF"v[96:99],a[208:211],v[212:215],v[96:99]\n"
MF"v[100:103],a[212:215],v[212:215],v[100:103]\n"
RD"v[196:199],v243 offset:16384\n"
MF"v[100:103],a[196:199],v[208:211],v[100:103]\n"
RT"a[252:253],v249 offset:12288\n"
MF"v[104:107],a[200:203],v[208:211],v[104:107]\n"
MF"v[104:107],a[216:219],v[212:215],v[104:107]\n"
RT"a[254:255],v249 offset:12800\n"
MF"v[108:111],a[220:223],v[212:215],v[108:111]\n"
RD"v[200:203],v242 offset:18432\n"
MF"v[108:111],a[204:207],v[208:211],v[108:111]\n"
MF"v[112:115],a[192:195],v[216:219],v[112:115]\n"
"s_lshl_b32 s50,s35,20\n"
MF"v[112:115],a[208:211],v[220:223],v[112:115]\n"
"s_lshl_b32 s51,s34,15\n"
RD"v[204:207],v243 offset:18432\n"
SA"m0,s37,12288\n"
MF"v[116:119],a[212:215],v[220:223],v[116:119]\n"
SA"s48,s98,65536\n"
SA"s50,s50,s51\n"
MF"v[116:119],a[196:199],v[216:219],v[116:119]\n"
WT"lgkmcnt(2)\n"
BL"v234,s[12:15],s48 offen offset:0 lds\n"
MF"v[120:123],a[200:203],v[216:219],v[120:123]\n"
"s_and_b32 s51,s30,1\n"
"s_lshl_b32 s51,s51,11\n"
MF"v[120:123],a[216:219],v[220:223],v[120:123]\n"
SA"s50,s50,s51\n"
RD"v[208:211],v242 offset:20480\n"
MF"v[124:127],a[220:223],v[220:223],v[124:127]\n"
SA"s50,s50,9175040\n"
MF"v[124:127],a[204:207],v[216:219],v[124:127]\n"
BL"v235,s[12:15],s48 offen offset:1024 lds\n"
MF"v[0:3],a[224:227],v[192:195],v[0:3]\n"
RD"v[212:215],v243 offset:20480\n"
"v_add_u32 v250,s50,v224\n"
MF"v[0:3],a[240:243],v[196:199],v[0:3]\n"
MF"v[4:7],a[244:247],v[196:199],v[4:7]\n"
MF"v[4:7],a[228:231],v[192:195],v[4:7]\n"
MF"v[8:11],a[232:235],v[192:195],v[8:11]\n"
RD"v[216:219],v242 offset:22528\n"
MF"v[8:11],a[248:251],v[196:199],v[8:11]\n"
BL"v236,s[12:15],s48 offen offset:2048 lds\n"
MF"v[12:15],a[252:255],v[196:199],v[12:15]\n"
MF"v[12:15],a[236:239],v[192:195],v[12:15]\n"
WT"lgkmcnt(1)\n"
RD"v[220:223],v243 offset:22528\n"
MF"v[16:19],a[224:227],v[200:203],v[16:19]\n"
BL"v237,s[12:15],s48 offen offset:3072 lds\n"
MF"v[16:19],a[240:243],v[204:207],v[16:19]\n"
MF"v[20:23],a[244:247],v[204:207],v[20:23]\n"
MF"v[20:23],a[228:231],v[200:203],v[20:23]\n"
RD"v[192:195],v242 offset:24576\n"
MF"v[24:27],a[232:235],v[200:203],v[24:27]\n"
MF"v[24:27],a[248:251],v[204:207],v[24:27]\n"
MF"v[28:31],a[252:255],v[204:207],v[28:31]\n"
MF"v[28:31],a[236:239],v[200:203],v[28:31]\n"
RD"v[196:199],v243 offset:24576\n"
MF"v[32:35],a[224:227],v[208:211],v[32:35]\n"
MF"v[32:35],a[240:243],v[212:215],v[32:35]\n"
MF"v[36:39],a[244:247],v[212:215],v[36:39]\n"
RD"v[200:203],v242 offset:26624\n"
MF"v[36:39],a[228:231],v[208:211],v[36:39]\n"
MF"v[40:43],a[232:235],v[208:211],v[40:43]\n"
MF"v[40:43],a[248:251],v[212:215],v[40:43]\n"
MF"v[44:47],a[252:255],v[212:215],v[44:47]\n"
RD"v[204:207],v243 offset:26624\n"
MF"v[44:47],a[236:239],v[208:211],v[44:47]\n"
WT"lgkmcnt(2)\n"
MF"v[48:51],a[224:227],v[216:219],v[48:51]\n"
MF"v[48:51],a[240:243],v[220:223],v[48:51]\n"
RD"v[208:211],v242 offset:28672\n"
MF"v[52:55],a[244:247],v[220:223],v[52:55]\n"
MF"v[52:55],a[228:231],v[216:219],v[52:55]\n"
MF"v[56:59],a[232:235],v[216:219],v[56:59]\n"
MF"v[56:59],a[248:251],v[220:223],v[56:59]\n"
RD"v[212:215],v243 offset:28672\n"
MF"v[60:63],a[252:255],v[220:223],v[60:63]\n"
MF"v[60:63],a[236:239],v[216:219],v[60:63]\n"
MF"v[64:67],a[224:227],v[192:195],v[64:67]\n"
RD"v[216:219],v242 offset:30720\n"
MF"v[64:67],a[240:243],v[196:199],v[64:67]\n"
MF"v[68:71],a[244:247],v[196:199],v[68:71]\n"
MF"v[68:71],a[228:231],v[192:195],v[68:71]\n"
MF"v[72:75],a[232:235],v[192:195],v[72:75]\n"
RD"v[220:223],v243 offset:30720\n"
MF"v[72:75],a[248:251],v[196:199],v[72:75]\n"
MF"v[76:79],a[252:255],v[196:199],v[76:79]\n"
MF"v[76:79],a[236:239],v[192:195],v[76:79]\n"
WT"lgkmcnt(1)\n"
MF"v[80:83],a[224:227],v[200:203],v[80:83]\n"
MF"v[80:83],a[240:243],v[204:207],v[80:83]\n"
MF"v[84:87],a[244:247],v[204:207],v[84:87]\n"
"v_add_u32 v244,114688,v242\n"
"v_add_u32 v245,114688,v243\n"
MF"v[84:87],a[228:231],v[200:203],v[84:87]\n"
"v_add_u32 v242,65536,v242\n"
"v_add_u32 v243,65536,v243\n"
MF"v[88:91],a[232:235],v[200:203],v[88:91]\n"
MF"v[88:91],a[248:251],v[204:207],v[88:91]\n"
WT"vmcnt(16) lgkmcnt(0)\n"
"s_barrier\n"
MF"v[92:95],a[252:255],v[204:207],v[92:95]\n"
RD"v[192:195],v242 offset:16384\n"
MF"v[92:95],a[236:239],v[200:203],v[92:95]\n"
MF"v[96:99],a[224:227],v[208:211],v[96:99]\n"
MF"v[96:99],a[240:243],v[212:215],v[96:99]\n"
MF"v[100:103],a[244:247],v[212:215],v[100:103]\n"
SA"m0,s36,49152\n"
RD"v[196:199],v243 offset:16384\n"
SA"s48,s60,s61\n"
MF"v[100:103],a[228:231],v[208:211],v[100:103]\n"
MF"v[104:107],a[232:235],v[208:211],v[104:107]\n"
BL"v238,s[20:23],s48 offen offset:0 lds\n"
MF"v[104:107],a[248:251],v[212:215],v[104:107]\n"
MF"v[108:111],a[252:255],v[212:215],v[108:111]\n"
RD"v[200:203],v242 offset:18432\n"
MF"v[108:111],a[236:239],v[208:211],v[108:111]\n"
MF"v[112:115],a[224:227],v[216:219],v[112:115]\n"
BL"v239,s[20:23],s48 offen offset:1024 lds\n"
MF"v[112:115],a[240:243],v[220:223],v[112:115]\n"
MF"v[116:119],a[244:247],v[220:223],v[116:119]\n"
RD"v[204:207],v243 offset:18432\n"
MF"v[116:119],a[228:231],v[216:219],v[116:119]\n"
MF"v[120:123],a[232:235],v[216:219],v[120:123]\n"
MF"v[120:123],a[248:251],v[220:223],v[120:123]\n"
BL"v240,s[20:23],s48 offen offset:2048 lds\n"
MF"v[124:127],a[252:255],v[220:223],v[124:127]\n"
SA"s61,s61,524288\n"
RD"v[208:211],v242 offset:20480\n"
MF"v[124:127],a[236:239],v[216:219],v[124:127]\n"
WT"lgkmcnt(2)\n"
MF"v[128:131],a[0:3],v[192:195],0\n"
MF"v[128:131],a[16:19],v[196:199],v[128:131]\n"
RD"v[212:215],v243 offset:20480\n"
MF"v[132:135],a[20:23],v[196:199],0\n"
BL"v241,s[20:23],s48 offen offset:3072 lds\n"
MF"v[132:135],a[4:7],v[192:195],v[132:135]\n"
MU"v0,s55,v0\n"
MU"v1,s55,v1\n"
MF"v[136:139],a[8:11],v[192:195],0\n"
MU"v2,s55,v2\n"
MU"v3,s55,v3\n"
MF"v[136:139],a[24:27],v[196:199],v[136:139]\n"
RD"v[216:219],v242 offset:22528\n"
MU"v4,s55,v4\n"
MF"v[140:143],a[28:31],v[196:199],0\n"
SA"m0,s36,32768\n"
MU"v5,s55,v5\n"
MU"v6,s55,v6\n"
MF"v[140:143],a[12:15],v[192:195],v[140:143]\n"
SA"s48,s60,s61\n"
MU"v7,s55,v7\n"
PK"v0,v0,v1\n"
MF"v[144:147],a[0:3],v[200:203],0\n"
WT"lgkmcnt(1)\n"
BL"v238,s[20:23],s48 offen offset:0 lds\n"
MF"v[144:147],a[16:19],v[204:207],v[144:147]\n"
RD"v[220:223],v243 offset:22528\n"
PK"v1,v2,v3\n"
MF"v[148:151],a[20:23],v[204:207],0\n"
PK"v2,v4,v5\n"
PK"v3,v6,v7\n"
MF"v[148:151],a[4:7],v[200:203],v[148:151]\n"
MF"v[152:155],a[8:11],v[200:203],0\n"
BL"v239,s[20:23],s48 offen offset:1024 lds\n"
MF"v[152:155],a[24:27],v[204:207],v[152:155]\n"
RD"v[192:195],v242 offset:40960\n"
MU"v8,s55,v8\n"
MF"v[156:159],a[28:31],v[204:207],0\n"
MU"v9,s55,v9\n"
MU"v10,s55,v10\n"
MF"v[156:159],a[12:15],v[200:203],v[156:159]\n"
MU"v11,s55,v11\n"
MU"v12,s55,v12\n"
MF"v[128:131],a[32:35],v[208:211],v[128:131]\n"
MU"v13,s55,v13\n"
MU"v14,s55,v14\n"
MF"v[128:131],a[48:51],v[212:215],v[128:131]\n"
RD"v[196:199],v243 offset:40960\n"
MU"v15,s55,v15\n"
MF"v[132:135],a[52:55],v[212:215],v[132:135]\n"
SA"s61,s61,524288\n"
BL"v240,s[20:23],s48 offen offset:2048 lds\n"
MF"v[132:135],a[36:39],v[208:211],v[132:135]\n"
"s_cmp_eq_u32 s61,5242880\n"
PK"v8,v8,v9\n"
PK"v9,v10,v11\n"
MF"v[136:139],a[40:43],v[208:211],v[136:139]\n"
"s_cselect_b32 s61,0,s61\n"
RD"v[200:203],v242 offset:43008\n"
PK"v10,v12,v13\n"
MF"v[136:139],a[56:59],v[212:215],v[136:139]\n"
BL"v241,s[20:23],s48 offen offset:3072 lds\n"
MF"v[140:143],a[60:63],v[212:215],v[140:143]\n"
PK"v11,v14,v15\n"
MU"v16,s55,v16\n"
MF"v[140:143],a[44:47],v[208:211],v[140:143]\n"
WT"lgkmcnt(2)\n"
MU"v17,s55,v17\n"
MU"v18,s55,v18\n"
MF"v[144:147],a[32:35],v[216:219],v[144:147]\n"
RD"v[204:207],v243 offset:43008\n"
MU"v19,s55,v19\n"
MF"v[144:147],a[48:51],v[220:223],v[144:147]\n"
MU"v20,s55,v20\n"
MU"v21,s55,v21\n"
MF"v[148:151],a[52:55],v[220:223],v[148:151]\n"
MU"v22,s55,v22\n"
MU"v23,s55,v23\n"
MF"v[148:151],a[36:39],v[216:219],v[148:151]\n"
PK"v16,v16,v17\n"
PK"v17,v18,v19\n"
MF"v[152:155],a[40:43],v[216:219],v[152:155]\n"
RD"v[208:211],v242 offset:45056\n"
PK"v18,v20,v21\n"
MF"v[152:155],a[56:59],v[220:223],v[152:155]\n"
"s_lshl_b32 s90,s31,2\n"
PK"v19,v22,v23\n"
"v_min_u32 v254,48,v224\n"
MF"v[156:159],a[60:63],v[220:223],v[156:159]\n"
SA"s90,s90,s30\n"
"v_lshlrev_b32 v254,3,v254\n"
MF"v[156:159],a[44:47],v[216:219],v[156:159]\n"
"s_mov_b32 s49,0\n"
"s_lshl_b32 s90,s90,9\n"
MF"v[128:131],a[64:67],v[192:195],v[128:131]\n"
WT"lgkmcnt(1)\n"
RD"v[212:215],v243 offset:45056\n"
MU"v24,s55,v24\n"
MF"v[128:131],a[80:83],v[196:199],v[128:131]\n"
BS"v[0:3],v250,s[8:11],s49 offen offset:0 sc1\n"
MF"v[132:135],a[84:87],v[196:199],v[132:135]\n"
"buffer_load_dword v255,v254,s[8:11],s90 offen sc1\n"
MF"v[132:135],a[68:71],v[192:195],v[132:135]\n"
RD"v[216:219],v242 offset:47104\n"
MU"v25,s55,v25\n"
MF"v[136:139],a[72:75],v[192:195],v[136:139]\n"
MU"v26,s55,v26\n"
MU"v27,s55,v27\n"
MF"v[136:139],a[88:91],v[196:199],v[136:139]\n"
MU"v28,s55,v28\n"
MU"v29,s55,v29\n"
MF"v[140:143],a[92:95],v[196:199],v[140:143]\n"
MU"v30,s55,v30\n"
MU"v31,s55,v31\n"
MF"v[140:143],a[76:79],v[192:195],v[140:143]\n"
RD"v[220:223],v243 offset:47104\n"
PK"v24,v24,v25\n"
MF"v[144:147],a[64:67],v[200:203],v[144:147]\n"
PK"v25,v26,v27\n"
PK"v26,v28,v29\n"
MF"v[144:147],a[80:83],v[204:207],v[144:147]\n"
PK"v27,v30,v31\n"
MF"v[148:151],a[84:87],v[204:207],v[148:151]\n"
MU"v32,s55,v32\n"
MU"v33,s55,v33\n"
MF"v[148:151],a[68:71],v[200:203],v[148:151]\n"
RD"v[192:195],v244 offset:16384\n"
MU"v34,s55,v34\n"
MF"v[152:155],a[72:75],v[200:203],v[152:155]\n"
MU"v35,s55,v35\n"
MU"v36,s55,v36\n"
MF"v[152:155],a[88:91],v[204:207],v[152:155]\n"
MU"v37,s55,v37\n"
MU"v38,s55,v38\n"
MF"v[156:159],a[92:95],v[204:207],v[156:159]\n"
MU"v39,s55,v39\n"
PK"v32,v32,v33\n"
MF"v[156:159],a[76:79],v[200:203],v[156:159]\n"
WT"lgkmcnt(1)\n"
RD"v[196:199],v245 offset:16384\n"
PK"v33,v34,v35\n"
MF"v[128:131],a[96:99],v[208:211],v[128:131]\n"
PK"v34,v36,v37\n"
PK"v35,v38,v39\n"
MF"v[128:131],a[112:115],v[212:215],v[128:131]\n"
MF"v[132:135],a[116:119],v[212:215],v[132:135]\n"
MF"v[132:135],a[100:103],v[208:211],v[132:135]\n"
RD"v[200:203],v244 offset:18432\n"
MF"v[136:139],a[104:107],v[208:211],v[136:139]\n"
MU"v40,s55,v40\n"
MU"v41,s55,v41\n"
MF"v[136:139],a[120:123],v[212:215],v[136:139]\n"
MU"v42,s55,v42\n"
MU"v43,s55,v43\n"
MF"v[140:143],a[124:127],v[212:215],v[140:143]\n"
RD"v[204:207],v245 offset:18432\n"
MU"v44,s55,v44\n"
MF"v[140:143],a[108:111],v[208:211],v[140:143]\n"
MU"v45,s55,v45\n"
MU"v46,s55,v46\n"
MF"v[144:147],a[96:99],v[216:219],v[144:147]\n"
MU"v47,s55,v47\n"
PK"v40,v40,v41\n"
MF"v[144:147],a[112:115],v[220:223],v[144:147]\n"
PK"v41,v42,v43\n"
PK"v42,v44,v45\n"
MF"v[148:151],a[116:119],v[220:223],v[148:151]\n"
RD"v[208:211],v244 offset:20480\n"
PK"v43,v46,v47\n"
MF"v[148:151],a[100:103],v[216:219],v[148:151]\n"
MF"v[152:155],a[104:107],v[216:219],v[152:155]\n"
MF"v[152:155],a[120:123],v[220:223],v[152:155]\n"
MF"v[156:159],a[124:127],v[220:223],v[156:159]\n"
RD"v[212:215],v245 offset:20480\n"
MU"v48,s55,v48\n"
MF"v[156:159],a[108:111],v[216:219],v[156:159]\n"
WT"lgkmcnt(2)\n"
MU"v49,s55,v49\n"
MU"v50,s55,v50\n"
MF"v[128:131],a[128:131],v[192:195],v[128:131]\n"
BS"v[8:11],v250,s[8:11],s49 offen offset:1024 sc1\n"
MF"v[128:131],a[144:147],v[196:199],v[128:131]\n"
MU"v51,s55,v51\n"
MU"v52,s55,v52\n"
MF"v[132:135],a[148:151],v[196:199],v[132:135]\n"
RD"v[216:219],v244 offset:22528\n"
MU"v53,s55,v53\n"
MF"v[132:135],a[132:135],v[192:195],v[132:135]\n"
MU"v54,s55,v54\n"
MU"v55,s55,v55\n"
MF"v[136:139],a[136:139],v[192:195],v[136:139]\n"
PK"v48,v48,v49\n"
PK"v49,v50,v51\n"
MF"v[136:139],a[152:155],v[196:199],v[136:139]\n"
RD"v[220:223],v245 offset:22528\n"
PK"v50,v52,v53\n"
MF"v[140:143],a[156:159],v[196:199],v[140:143]\n"
PK"v51,v54,v55\n"
MF"v[140:143],a[140:143],v[192:195],v[140:143]\n"
MF"v[144:147],a[128:131],v[200:203],v[144:147]\n"
MU"v56,s55,v56\n"
MU"v57,s55,v57\n"
MF"v[144:147],a[144:147],v[204:207],v[144:147]\n"
RD"v[192:195],v244 offset:40960\n"
MU"v58,s55,v58\n"
MF"v[148:151],a[148:151],v[204:207],v[148:151]\n"
MU"v59,s55,v59\n"
MU"v60,s55,v60\n"
MF"v[148:151],a[132:135],v[200:203],v[148:151]\n"
MU"v61,s55,v61\n"
MU"v62,s55,v62\n"
MF"v[152:155],a[136:139],v[200:203],v[152:155]\n"
MU"v63,s55,v63\n"
PK"v56,v56,v57\n"
MF"v[152:155],a[152:155],v[204:207],v[152:155]\n"
RD"v[196:199],v245 offset:40960\n"
PK"v57,v58,v59\n"
MF"v[156:159],a[156:159],v[204:207],v[156:159]\n"
PK"v58,v60,v61\n"
PK"v59,v62,v63\n"
MF"v[156:159],a[140:143],v[200:203],v[156:159]\n"
WT"lgkmcnt(2)\n"
MF"v[128:131],a[160:163],v[208:211],v[128:131]\n"
MF"v[128:131],a[176:179],v[212:215],v[128:131]\n"
RD"v[200:203],v244 offset:43008\n"
MF"v[132:135],a[180:183],v[212:215],v[132:135]\n"
MU"v64,s55,v64\n"
MU"v65,s55,v65\n"
MF"v[132:135],a[164:167],v[208:211],v[132:135]\n"
MU"v66,s55,v66\n"
MU"v67,s55,v67\n"
MF"v[136:139],a[168:171],v[208:211],v[136:139]\n"
MU"v68,s55,v68\n"
MU"v69,s55,v69\n"
MF"v[136:139],a[184:187],v[212:215],v[136:139]\n"
RD"v[204:207],v245 offset:43008\n"
MU"v70,s55,v70\n"
MF"v[140:143],a[188:191],v[212:215],v[140:143]\n"
MU"v71,s55,v71\n"
PK"v64,v64,v65\n"
MF"v[140:143],a[172:175],v[208:211],v[140:143]\n"
PK"v65,v66,v67\n"
PK"v66,v68,v69\n"
MF"v[144:147],a[160:163],v[216:219],v[144:147]\n"
RD"v[208:211],v244 offset:45056\n"
PK"v67,v70,v71\n"
MF"v[144:147],a[176:179],v[220:223],v[144:147]\n"
MF"v[148:151],a[180:183],v[220:223],v[148:151]\n"
MF"v[148:151],a[164:167],v[216:219],v[148:151]\n"
MF"v[152:155],a[168:171],v[216:219],v[152:155]\n"
RD"v[212:215],v245 offset:45056\n"
MU"v72,s55,v72\n"
MF"v[152:155],a[184:187],v[220:223],v[152:155]\n"
"s_mov_b32 s49,4096\n"
MU"v73,s55,v73\n"
MU"v74,s55,v74\n"
MF"v[156:159],a[188:191],v[220:223],v[156:159]\n"
MU"v75,s55,v75\n"
MU"v76,s55,v76\n"
MF"v[156:159],a[172:175],v[216:219],v[156:159]\n"
WT"lgkmcnt(1)\n"
BS"v[16:19],v250,s[8:11],s49 offen offset:0 sc1\n"
MF"v[128:131],a[192:195],v[192:195],v[128:131]\n"
RD"v[216:219],v244 offset:47104\n"
MU"v77,s55,v77\n"
MF"v[128:131],a[208:211],v[196:199],v[128:131]\n"
MU"v78,s55,v78\n"
MU"v79,s55,v79\n"
MF"v[132:135],a[212:215],v[196:199],v[132:135]\n"
PK"v72,v72,v73\n"
PK"v73,v74,v75\n"
MF"v[132:135],a[196:199],v[192:195],v[132:135]\n"
PK"v74,v76,v77\n"
PK"v75,v78,v79\n"
MF"v[136:139],a[200:203],v[192:195],v[136:139]\n"
RD"v[220:223],v245 offset:47104\n"
MF"v[136:139],a[216:219],v[196:199],v[136:139]\n"
MF"v[140:143],a[220:223],v[196:199],v[140:143]\n"
MU"v80,s55,v80\n"
MU"v81,s55,v81\n"
MF"v[140:143],a[204:207],v[192:195],v[140:143]\n"
MU"v82,s55,v82\n"
MU"v83,s55,v83\n"
MF"v[144:147],a[192:195],v[200:203],v[144:147]\n"
MU"v84,s55,v84\n"
MU"v85,s55,v85\n"
MF"v[144:147],a[208:211],v[204:207],v[144:147]\n"
MU"v86,s55,v86\n"
MU"v87,s55,v87\n"
MF"v[148:151],a[212:215],v[204:207],v[148:151]\n"
PK"v80,v80,v81\n"
PK"v81,v82,v83\n"
MF"v[148:151],a[196:199],v[200:203],v[148:151]\n"
PK"v82,v84,v85\n"
PK"v83,v86,v87\n"
MF"v[152:155],a[200:203],v[200:203],v[152:155]\n"
MF"v[152:155],a[216:219],v[204:207],v[152:155]\n"
WT"vmcnt(20) lgkmcnt(0)\n"
"s_barrier\n"
MF"v[156:159],a[220:223],v[204:207],v[156:159]\n"
RD"v[192:195],v242 offset:0\n"
MF"v[156:159],a[204:207],v[200:203],v[156:159]\n"
MF"v[128:131],a[224:227],v[208:211],v[128:131]\n"
MU"v88,s55,v88\n"
MU"v89,s55,v89\n"
MF"v[128:131],a[240:243],v[212:215],v[128:131]\n"
MU"v90,s55,v90\n"
MU"v91,s55,v91\n"
MF"v[132:135],a[244:247],v[212:215],v[132:135]\n"
RD"v[196:199],v243 offset:0\n"
MU"v92,s55,v92\n"
MF"v[132:135],a[228:231],v[208:211],v[132:135]\n"
MU"v93,s55,v93\n"
MU"v94,s55,v94\n"
MF"v[136:139],a[232:235],v[208:211],v[136:139]\n"
MU"v95,s55,v95\n"
PK"v88,v88,v89\n"
MF"v[136:139],a[248:251],v[212:215],v[136:139]\n"
PK"v89,v90,v91\n"
PK"v90,v92,v93\n"
MF"v[140:143],a[252:255],v[212:215],v[140:143]\n"
RD"v[200:203],v242 offset:2048\n"
PK"v91,v94,v95\n"
MF"v[140:143],a[236:239],v[208:211],v[140:143]\n"
MF"v[144:147],a[224:227],v[216:219],v[144:147]\n"
MF"v[144:147],a[240:243],v[220:223],v[144:147]\n"
MF"v[148:151],a[244:247],v[220:223],v[148:151]\n"
RD"v[204:207],v243 offset:2048\n"
MF"v[148:151],a[228:231],v[216:219],v[148:151]\n"
MF"v[152:155],a[232:235],v[216:219],v[152:155]\n"
MF"v[152:155],a[248:251],v[220:223],v[152:155]\n"
MF"v[156:159],a[252:255],v[220:223],v[156:159]\n"
RD"v[208:211],v242 offset:4096\n"
MF"v[156:159],a[236:239],v[216:219],v[156:159]\n"
WT"lgkmcnt(2)\n"
BS"v[24:27],v250,s[8:11],s49 offen offset:1024 sc1\n"
MF"v[36:39],a[0:3],v[192:195],0\n"
MF"v[36:39],a[16:19],v[196:199],v[36:39]\n"
RD"v[212:215],v243 offset:4096\n"
MF"v[68:71],a[20:23],v[196:199],0\n"
MF"v[68:71],a[4:7],v[192:195],v[68:71]\n"
MU"v128,s55,v128\n"
MU"v129,s55,v129\n"
MF"v[44:47],a[8:11],v[192:195],0\n"
MU"v130,s55,v130\n"
MU"v131,s55,v131\n"
MF"v[44:47],a[24:27],v[196:199],v[44:47]\n"
RD"v[216:219],v242 offset:6144\n"
MU"v132,s55,v132\n"
MF"v[76:79],a[28:31],v[196:199],0\n"
MU"v133,s55,v133\n"
MU"v134,s55,v134\n"
MF"v[76:79],a[12:15],v[192:195],v[76:79]\n"
MU"v135,s55,v135\n"
PK"v4,v128,v129\n"
MF"v[52:55],a[0:3],v[200:203],0\n"
WT"lgkmcnt(1)\n"
PK"v5,v130,v131\n"
PK"v6,v132,v133\n"
MF"v[52:55],a[16:19],v[204:207],v[52:55]\n"
RD"v[220:223],v243 offset:6144\n"
PK"v7,v134,v135\n"
MF"v[84:87],a[20:23],v[204:207],0\n"
MF"v[84:87],a[4:7],v[200:203],v[84:87]\n"
MF"v[60:63],a[8:11],v[200:203],0\n"
MF"v[60:63],a[24:27],v[204:207],v[60:63]\n"
RD"v[192:195],v242 offset:24576\n"
MU"v136,s55,v136\n"
MF"v[92:95],a[28:31],v[204:207],0\n"
MU"v137,s55,v137\n"
MU"v138,s55,v138\n"
MF"v[92:95],a[12:15],v[200:203],v[92:95]\n"
MU"v139,s55,v139\n"
MU"v140,s55,v140\n"
MF"v[36:39],a[32:35],v[208:211],v[36:39]\n"
MU"v141,s55,v141\n"
MU"v142,s55,v142\n"
MF"v[36:39],a[48:51],v[212:215],v[36:39]\n"
RD"v[196:199],v243 offset:24576\n"
MU"v143,s55,v143\n"
MF"v[68:71],a[52:55],v[212:215],v[68:71]\n"
PK"v12,v136,v137\n"
PK"v13,v138,v139\n"
MF"v[68:71],a[36:39],v[208:211],v[68:71]\n"
PK"v14,v140,v141\n"
PK"v15,v142,v143\n"
MF"v[44:47],a[40:43],v[208:211],v[44:47]\n"
RD"v[200:203],v242 offset:26624\n"
MF"v[44:47],a[56:59],v[212:215],v[44:47]\n"
MF"v[76:79],a[60:63],v[212:215],v[76:79]\n"
MF"v[76:79],a[44:47],v[208:211],v[76:79]\n"
WT"lgkmcnt(2)\n"
MU"v144,s55,v144\n"
MU"v145,s55,v145\n"
MF"v[52:55],a[32:35],v[216:219],v[52:55]\n"
RD"v[204:207],v243 offset:26624\n"
MU"v146,s55,v146\n"
MF"v[52:55],a[48:51],v[220:223],v[52:55]\n"
MU"v147,s55,v147\n"
MU"v148,s55,v148\n"
MF"v[84:87],a[52:55],v[220:223],v[84:87]\n"
MU"v149,s55,v149\n"
MU"v150,s55,v150\n"
MF"v[84:87],a[36:39],v[216:219],v[84:87]\n"
"s_mov_b32 s49,8192\n"
MU"v151,s55,v151\n"
PK"v20,v144,v145\n"
MF"v[60:63],a[40:43],v[216:219],v[60:63]\n"
RD"v[208:211],v242 offset:28672\n"
PK"v21,v146,v147\n"
MF"v[60:63],a[56:59],v[220:223],v[60:63]\n"
BS"v[32:35],v250,s[8:11],s49 offen offset:0 sc1\n"
MF"v[92:95],a[60:63],v[220:223],v[92:95]\n"
PK"v22,v148,v149\n"
PK"v23,v150,v151\n"
MF"v[92:95],a[44:47],v[216:219],v[92:95]\n"
MF"v[36:39],a[64:67],v[192:195],v[36:39]\n"
WT"lgkmcnt(1)\n"
RD"v[212:215],v243 offset:28672\n"
MF"v[36:39],a[80:83],v[196:199],v[36:39]\n"
MU"v152,s55,v152\n"
MU"v153,s55,v153\n"
MF"v[68:71],a[84:87],v[196:199],v[68:71]\n"
MU"v154,s55,v154\n"
MU"v155,s55,v155\n"
MF"v[68:71],a[68:71],v[192:195],v[68:71]\n"
RD"v[216:219],v242 offset:30720\n"
MU"v156,s55,v156\n"
MF"v[44:47],a[72:75],v[192:195],v[44:47]\n"
MU"v157,s55,v157\n"
MU"v158,s55,v158\n"
MF"v[44:47],a[88:91],v[196:199],v[44:47]\n"
MU"v159,s55,v159\n"
PK"v28,v152,v153\n"
MF"v[76:79],a[92:95],v[196:199],v[76:79]\n"
PK"v29,v154,v155\n"
PK"v30,v156,v157\n"
MF"v[76:79],a[76:79],v[192:195],v[76:79]\n"
RD"v[220:223],v243 offset:30720\n"
PK"v31,v158,v159\n"
MF"v[52:55],a[64:67],v[200:203],v[52:55]\n"
MF"v[52:55],a[80:83],v[204:207],v[52:55]\n"
MF"v[84:87],a[84:87],v[204:207],v[84:87]\n"
MF"v[84:87],a[68:71],v[200:203],v[84:87]\n"
RD"v[192:195],v244 offset:0\n"
MF"v[60:63],a[72:75],v[200:203],v[60:63]\n"
MF"v[60:63],a[88:91],v[204:207],v[60:63]\n"
MF"v[92:95],a[92:95],v[204:207],v[92:95]\n"
MF"v[92:95],a[76:79],v[200:203],v[92:95]\n"
WT"lgkmcnt(1)\n"
RD"v[196:199],v245 offset:0\n"
MF"v[36:39],a[96:99],v[208:211],v[36:39]\n"
MF"v[36:39],a[112:115],v[212:215],v[36:39]\n"
MF"v[68:71],a[116:119],v[212:215],v[68:71]\n"
MF"v[68:71],a[100:103],v[208:211],v[68:71]\n"
RD"v[200:203],v244 offset:2048\n"
MF"v[44:47],a[104:107],v[208:211],v[44:47]\n"
MF"v[44:47],a[120:123],v[212:215],v[44:47]\n"
MF"v[76:79],a[124:127],v[212:215],v[76:79]\n"
RD"v[204:207],v245 offset:2048\n"
MF"v[76:79],a[108:111],v[208:211],v[76:79]\n"
MF"v[52:55],a[96:99],v[216:219],v[52:55]\n"
MF"v[52:55],a[112:115],v[220:223],v[52:55]\n"
MF"v[84:87],a[116:119],v[220:223],v[84:87]\n"
SA"m0,s37,16384\n"
RD"v[208:211],v244 offset:4096\n"
SA"s48,s39,98304\n"
MF"v[84:87],a[100:103],v[216:219],v[84:87]\n"
MF"v[60:63],a[104:107],v[216:219],v[60:63]\n"
BS"v[40:43],v250,s[8:11],s49 offen offset:1024 sc1\n"
MF"v[60:63],a[120:123],v[220:223],v[60:63]\n"
BL"v234,s[12:15],s48 offen offset:0 lds\n"
MF"v[92:95],a[124:127],v[220:223],v[92:95]\n"
RD"v[212:215],v245 offset:4096\n"
MU"v96,s55,v96\n"
MF"v[92:95],a[108:111],v[216:219],v[92:95]\n"
WT"lgkmcnt(2)\n"
MU"v97,s55,v97\n"
MU"v98,s55,v98\n"
MF"v[36:39],a[128:131],v[192:195],v[36:39]\n"
MU"v99,s55,v99\n"
MU"v100,s55,v100\n"
MF"v[36:39],a[144:147],v[196:199],v[36:39]\n"
MU"v101,s55,v101\n"
MU"v102,s55,v102\n"
MF"v[68:71],a[148:151],v[196:199],v[68:71]\n"
RD"v[216:219],v244 offset:6144\n"
MU"v103,s55,v103\n"
MF"v[68:71],a[132:135],v[192:195],v[68:71]\n"
BL"v235,s[12:15],s48 offen offset:1024 lds\n"
MF"v[44:47],a[136:139],v[192:195],v[44:47]\n"
PK"v96,v96,v97\n"
PK"v97,v98,v99\n"
MF"v[44:47],a[152:155],v[196:199],v[44:47]\n"
RD"v[220:223],v245 offset:6144\n"
PK"v98,v100,v101\n"
MF"v[76:79],a[156:159],v[196:199],v[76:79]\n"
PK"v99,v102,v103\n"
MU"v104,s55,v104\n"
MF"v[76:79],a[140:143],v[192:195],v[76:79]\n"
MU"v105,s55,v105\n"
MU"v106,s55,v106\n"
MF"v[52:55],a[128:131],v[200:203],v[52:55]\n"
BL"v236,s[12:15],s48 offen offset:2048 lds\n"
MF"v[52:55],a[144:147],v[204:207],v[52:55]\n"
RD"v[192:195],v244 offset:24576\n"
MU"v107,s55,v107\n"
MF"v[84:87],a[148:151],v[204:207],v[84:87]\n"
MU"v108,s55,v108\n"
MU"v109,s55,v109\n"
MF"v[84:87],a[132:135],v[200:203],v[84:87]\n"
MU"v110,s55,v110\n"
MU"v111,s55,v111\n"
MF"v[60:63],a[136:139],v[200:203],v[60:63]\n"
PK"v104,v104,v105\n"
PK"v105,v106,v107\n"
MF"v[60:63],a[152:155],v[204:207],v[60:63]\n"
RD"v[196:199],v245 offset:24576\n"
PK"v106,v108,v109\n"
MF"v[92:95],a[156:159],v[204:207],v[92:95]\n"
BL"v237,s[12:15],s48 offen offset:3072 lds\n"
MF"v[92:95],a[140:143],v[200:203],v[92:95]\n"
WT"lgkmcnt(2)\n"
PK"v107,v110,v111\n"
MF"v[36:39],a[160:163],v[208:211],v[36:39]\n"
MU"v112,s55,v112\n"
MU"v113,s55,v113\n"
MF"v[36:39],a[176:179],v[212:215],v[36:39]\n"
RD"v[200:203],v244 offset:26624\n"
MU"v114,s55,v114\n"
MF"v[68:71],a[180:183],v[212:215],v[68:71]\n"
SA"m0,s37,20480\n"
MU"v115,s55,v115\n"
MU"v116,s55,v116\n"
MF"v[68:71],a[164:167],v[208:211],v[68:71]\n"
SA"s48,s98,98304\n"
MU"v117,s55,v117\n"
MU"v118,s55,v118\n"
MF"v[44:47],a[168:171],v[208:211],v[44:47]\n"
BL"v234,s[12:15],s48 offen offset:0 lds\n"
MF"v[44:47],a[184:187],v[212:215],v[44:47]\n"
RD"v[204:207],v245 offset:26624\n"
MU"v119,s55,v119\n"
MF"v[76:79],a[188:191],v[212:215],v[76:79]\n"
PK"v112,v112,v113\n"
PK"v113,v114,v115\n"
MF"v[76:79],a[172:175],v[208:211],v[76:79]\n"
PK"v114,v116,v117\n"
PK"v115,v118,v119\n"
MF"v[52:55],a[160:163],v[216:219],v[52:55]\n"
RD"v[208:211],v244 offset:28672\n"
MF"v[52:55],a[176:179],v[220:223],v[52:55]\n"
"s_mov_b32 s49,12288\n"
MF"v[84:87],a[180:183],v[220:223],v[84:87]\n"
BL"v235,s[12:15],s48 offen offset:1024 lds\n"
MF"v[84:87],a[164:167],v[216:219],v[84:87]\n"
BS"v[48:51],v250,s[8:11],s49 offen offset:0 sc1\n"
MF"v[60:63],a[168:171],v[216:219],v[60:63]\n"
RD"v[212:215],v245 offset:28672\n"
MU"v120,s55,v120\n"
MF"v[60:63],a[184:187],v[220:223],v[60:63]\n"
MU"v121,s55,v121\n"
MU"v122,s55,v122\n"
MF"v[92:95],a[188:191],v[220:223],v[92:95]\n"
MU"v123,s55,v123\n"
MU"v124,s55,v124\n"
MF"v[92:95],a[172:175],v[216:219],v[92:95]\n"
WT"lgkmcnt(1)\n"
MU"v125,s55,v125\n"
MU"v126,s55,v126\n"
MF"v[36:39],a[192:195],v[192:195],v[36:39]\n"
RD"v[216:219],v244 offset:30720\n"
MU"v127,s55,v127\n"
MF"v[36:39],a[208:211],v[196:199],v[36:39]\n"
BL"v236,s[12:15],s48 offen offset:2048 lds\n"
MF"v[68:71],a[212:215],v[196:199],v[68:71]\n"
PK"v120,v120,v121\n"
PK"v121,v122,v123\n"
MF"v[68:71],a[196:199],v[192:195],v[68:71]\n"
PK"v122,v124,v125\n"
PK"v123,v126,v127\n"
MF"v[44:47],a[200:203],v[192:195],v[44:47]\n"
RD"v[220:223],v245 offset:30720\n"
MF"v[44:47],a[216:219],v[196:199],v[44:47]\n"
MF"v[76:79],a[220:223],v[196:199],v[76:79]\n"
BL"v237,s[12:15],s48 offen offset:3072 lds\n"
MF"v[76:79],a[204:207],v[192:195],v[76:79]\n"
MF"v[52:55],a[192:195],v[200:203],v[52:55]\n"
MF"v[52:55],a[208:211],v[204:207],v[52:55]\n"
MF"v[84:87],a[212:215],v[204:207],v[84:87]\n"
MF"v[84:87],a[196:199],v[200:203],v[84:87]\n"
"v_lshrrev_b32 v251,2,v224\n"
MF"v[60:63],a[200:203],v[200:203],v[60:63]\n"
"v_and_b32 v251,28,v251\n"
MF"v[60:63],a[216:219],v[204:207],v[60:63]\n"
WT"vmcnt(24) lgkmcnt(0)\n"
"s_barrier\n"
MF"v[92:95],a[220:223],v[204:207],v[92:95]\n"
RD"v[192:195],v242 offset:8192\n"
"ds_read_b32 v234,v251 offset:16384\n"
MF"v[92:95],a[204:207],v[200:203],v[92:95]\n"
MF"v[36:39],a[224:227],v[208:211],v[36:39]\n"
MF"v[36:39],a[240:243],v[212:215],v[36:39]\n"
MF"v[68:71],a[244:247],v[212:215],v[68:71]\n"
RD"v[196:199],v243 offset:8192\n"
MF"v[68:71],a[228:231],v[208:211],v[68:71]\n"
MF"v[44:47],a[232:235],v[208:211],v[44:47]\n"
MF"v[44:47],a[248:251],v[212:215],v[44:47]\n"
MF"v[76:79],a[252:255],v[212:215],v[76:79]\n"
RD"v[200:203],v242 offset:10240\n"
MF"v[76:79],a[236:239],v[208:211],v[76:79]\n"
WT"lgkmcnt(2)\n"
"v_not_b32 v251,v234\n"
"v_bfe_u32 v252,v251,4,7\n"
MF"v[52:55],a[224:227],v[216:219],v[52:55]\n"
"v_mul_u32_u24 v252,20480,v252\n"
MF"v[52:55],a[240:243],v[220:223],v[52:55]\n"
"v_and_b32 v251,15,v251\n"
MF"v[84:87],a[244:247],v[220:223],v[84:87]\n"
RD"v[204:207],v243 offset:10240\n"
"v_lshl_add_u32 v251,v251,4,v252\n"
MF"v[84:87],a[228:231],v[216:219],v[84:87]\n"
BS"v[56:59],v250,s[8:11],s49 offen offset:1024 sc1\n"
MF"v[60:63],a[232:235],v[216:219],v[60:63]\n"
"v_and_b32 v252,0x380,v224\n"
"v_lshl_add_u32 v251,v252,1,v251\n"
MF"v[60:63],a[248:251],v[220:223],v[60:63]\n"
"v_add_u32 v252,v252,v224\n"
MF"v[92:95],a[252:255],v[220:223],v[92:95]\n"
"s_xor_b32 s92,s34,0\n"
RD"v[208:211],v242 offset:12288\n"
MF"v[92:95],a[236:239],v[216:219],v[92:95]\n"
"s_lshl_b32 s92,s92,2\n"
MF"v[128:131],a[0:3],v[192:195],0\n"
WT"lgkmcnt(2)\n"
SA"s92,s92,s30\n"
MF"v[128:131],a[16:19],v[196:199],v[128:131]\n"
"s_lshl_b32 s93,s92,11\n"
RD"v[212:215],v243 offset:12288\n"
MF"v[132:135],a[20:23],v[196:199],0\n"
"s_cmp_lt_u32 s92,10\n"
"s_cselect_b32 s93,s93,2130706432\n"
MF"v[132:135],a[4:7],v[192:195],v[132:135]\n"
SA"s92,s93,524288\n"
MU"v36,s55,v36\n"
MU"v37,s55,v37\n"
MF"v[136:139],a[8:11],v[192:195],0\n"
BL"v[100:103],v251,s[8:11],s92 offen sc1\n"
MF"v[136:139],a[24:27],v[196:199],v[136:139]\n"
"s_mul_i32 s92,s35,20480\n"
RD"v[216:219],v242 offset:14336\n"
MU"v38,s55,v38\n"
MF"v[140:143],a[28:31],v[196:199],0\n"
SA"s92,s92,s93\n"
MU"v39,s55,v39\n"
MU"v68,s55,v68\n"
MF"v[140:143],a[12:15],v[192:195],v[140:143]\n"
SA"s92,s92,3145728\n"
MU"v69,s55,v69\n"
MU"v70,s55,v70\n"
MF"v[144:147],a[0:3],v[200:203],0\n"
WT"lgkmcnt(1)\n"
MU"v71,s55,v71\n"
PK"v36,v36,v37\n"
MF"v[144:147],a[16:19],v[204:207],v[144:147]\n"
RD"v[220:223],v243 offset:14336\n"
PK"v37,v38,v39\n"
MF"v[148:151],a[20:23],v[204:207],0\n"
PK"v38,v68,v69\n"
PK"v39,v70,v71\n"
MF"v[148:151],a[4:7],v[200:203],v[148:151]\n"
MF"v[152:155],a[8:11],v[200:203],0\n"
MF"v[152:155],a[24:27],v[204:207],v[152:155]\n"
RD"v[192:195],v242 offset:32768\n"
MU"v44,s55,v44\n"
MF"v[156:159],a[28:31],v[204:207],0\n"
MU"v45,s55,v45\n"
MU"v46,s55,v46\n"
MF"v[156:159],a[12:15],v[200:203],v[156:159]\n"
MU"v47,s55,v47\n"
MU"v76,s55,v76\n"
MF"v[128:131],a[32:35],v[208:211],v[128:131]\n"
MU"v77,s55,v77\n"
MU"v78,s55,v78\n"
MF"v[128:131],a[48:51],v[212:215],v[128:131]\n"
RD"v[196:199],v243 offset:32768\n"
MU"v79,s55,v79\n"
MF"v[132:135],a[52:55],v[212:215],v[132:135]\n"
PK"v44,v44,v45\n"
PK"v45,v46,v47\n"
MF"v[132:135],a[36:39],v[208:211],v[132:135]\n"
PK"v46,v76,v77\n"
PK"v47,v78,v79\n"
MF"v[136:139],a[40:43],v[208:211],v[136:139]\n"
SA"m0,s36,0\n"
RD"v[200:203],v242 offset:34816\n"
SA"s48,s60,s61\n"
MF"v[136:139],a[56:59],v[212:215],v[136:139]\n"
MF"v[140:143],a[60:63],v[212:215],v[140:143]\n"
BL"v238,s[20:23],s48 offen offset:0 lds\n"
MF"v[140:143],a[44:47],v[208:211],v[140:143]\n"
WT"lgkmcnt(2)\n"
MU"v52,s55,v52\n"
MU"v53,s55,v53\n"
MF"v[144:147],a[32:35],v[216:219],v[144:147]\n"
"s_mov_b32 s49,16384\n"
RD"v[204:207],v243 offset:34816\n"
MU"v54,s55,v54\n"
MF"v[144:147],a[48:51],v[220:223],v[144:147]\n"
BS"v[64:67],v250,s[8:11],s49 offen offset:0 sc1\n"
MF"v[148:151],a[52:55],v[220:223],v[148:151]\n"
MU"v55,s55,v55\n"
MU"v84,s55,v84\n"
MF"v[148:151],a[36:39],v[216:219],v[148:151]\n"
MU"v85,s55,v85\n"
MU"v86,s55,v86\n"
MF"v[152:155],a[40:43],v[216:219],v[152:155]\n"
RD"v[208:211],v242 offset:36864\n"
MU"v87,s55,v87\n"
MF"v[152:155],a[56:59],v[220:223],v[152:155]\n"
PK"v52,v52,v53\n"
PK"v53,v54,v55\n"
MF"v[156:159],a[60:63],v[220:223],v[156:159]\n"
BL"v239,s[20:23],s48 offen offset:1024 lds\n"
MF"v[156:159],a[44:47],v[216:219],v[156:159]\n"
PK"v54,v84,v85\n"
PK"v55,v86,v87\n"
MF"v[128:131],a[64:67],v[192:195],v[128:131]\n"
WT"lgkmcnt(1)\n"
RD"v[212:215],v243 offset:36864\n"
MF"v[128:131],a[80:83],v[196:199],v[128:131]\n"
MU"v60,s55,v60\n"
MU"v61,s55,v61\n"
MF"v[132:135],a[84:87],v[196:199],v[132:135]\n"
MU"v62,s55,v62\n"
MU"v63,s55,v63\n"
MF"v[132:135],a[68:71],v[192:195],v[132:135]\n"
RD"v[216:219],v242 offset:38912\n"
MU"v92,s55,v92\n"
MF"v[136:139],a[72:75],v[192:195],v[136:139]\n"
MU"v93,s55,v93\n"
MU"v94,s55,v94\n"
MF"v[136:139],a[88:91],v[196:199],v[136:139]\n"
MU"v95,s55,v95\n"
PK"v60,v60,v61\n"
MF"v[140:143],a[92:95],v[196:199],v[140:143]\n"
BL"v240,s[20:23],s48 offen offset:2048 lds\n"
MF"v[140:143],a[76:79],v[192:195],v[140:143]\n"
RD"v[220:223],v243 offset:38912\n"
PK"v61,v62,v63\n"
MF"v[144:147],a[64:67],v[200:203],v[144:147]\n"
PK"v62,v92,v93\n"
PK"v63,v94,v95\n"
MF"v[144:147],a[80:83],v[204:207],v[144:147]\n"
MF"v[148:151],a[84:87],v[204:207],v[148:151]\n"
MF"v[148:151],a[68:71],v[200:203],v[148:151]\n"
RD"v[192:195],v244 offset:8192\n"
MF"v[152:155],a[72:75],v[200:203],v[152:155]\n"
MF"v[152:155],a[88:91],v[204:207],v[152:155]\n"
MF"v[156:159],a[92:95],v[204:207],v[156:159]\n"
SA"s61,s61,524288\n"
BL"v241,s[20:23],s48 offen offset:3072 lds\n"
MF"v[156:159],a[76:79],v[200:203],v[156:159]\n"
WT"lgkmcnt(1)\n"
RD"v[196:199],v245 offset:8192\n"
MF"v[128:131],a[96:99],v[208:211],v[128:131]\n"
MF"v[128:131],a[112:115],v[212:215],v[128:131]\n"
MF"v[132:135],a[116:119],v[212:215],v[132:135]\n"
MF"v[132:135],a[100:103],v[208:211],v[132:135]\n"
RD"v[200:203],v244 offset:10240\n"
MF"v[136:139],a[104:107],v[208:211],v[136:139]\n"
MF"v[136:139],a[120:123],v[212:215],v[136:139]\n"
MF"v[140:143],a[124:127],v[212:215],v[140:143]\n"
RD"v[204:207],v245 offset:10240\n"
MF"v[140:143],a[108:111],v[208:211],v[140:143]\n"
MF"v[144:147],a[96:99],v[216:219],v[144:147]\n"
BS"v[72:75],v250,s[8:11],s49 offen offset:1024 sc1\n"
MF"v[144:147],a[112:115],v[220:223],v[144:147]\n"
MF"v[148:151],a[116:119],v[220:223],v[148:151]\n"
RD"v[208:211],v244 offset:12288\n"
MF"v[148:151],a[100:103],v[216:219],v[148:151]\n"
MF"v[152:155],a[104:107],v[216:219],v[152:155]\n"
SA"m0,s38,65536\n"
SA"s48,s60,s61\n"
MF"v[152:155],a[120:123],v[220:223],v[152:155]\n"
MF"v[156:159],a[124:127],v[220:223],v[156:159]\n"
RD"v[212:215],v245 offset:12288\n"
MF"v[156:159],a[108:111],v[216:219],v[156:159]\n"
WT"lgkmcnt(2)\n"
BL"v238,s[20:23],s48 offen offset:0 lds\n"
MF"v[128:131],a[128:131],v[192:195],v[128:131]\n"
MF"v[128:131],a[144:147],v[196:199],v[128:131]\n"
MF"v[132:135],a[148:151],v[196:199],v[132:135]\n"
RD"v[216:219],v244 offset:14336\n"
MF"v[132:135],a[132:135],v[192:195],v[132:135]\n"
MF"v[136:139],a[136:139],v[192:195],v[136:139]\n"
MF"v[136:139],a[152:155],v[196:199],v[136:139]\n"
RD"v[220:223],v245 offset:14336\n"
MF"v[140:143],a[156:159],v[196:199],v[140:143]\n"
BL"v239,s[20:23],s48 offen offset:1024 lds\n"
MF"v[140:143],a[140:143],v[192:195],v[140:143]\n"
MF"v[144:147],a[128:131],v[200:203],v[144:147]\n"
MF"v[144:147],a[144:147],v[204:207],v[144:147]\n"
RD"v[192:195],v244 offset:32768\n"
MF"v[148:151],a[148:151],v[204:207],v[148:151]\n"
MF"v[148:151],a[132:135],v[200:203],v[148:151]\n"
MF"v[152:155],a[136:139],v[200:203],v[152:155]\n"
MF"v[152:155],a[152:155],v[204:207],v[152:155]\n"
RD"v[196:199],v245 offset:32768\n"
MF"v[156:159],a[156:159],v[204:207],v[156:159]\n"
BL"v240,s[20:23],s48 offen offset:2048 lds\n"
MF"v[156:159],a[140:143],v[200:203],v[156:159]\n"
WT"lgkmcnt(2)\n"
MF"v[128:131],a[160:163],v[208:211],v[128:131]\n"
MF"v[128:131],a[176:179],v[212:215],v[128:131]\n"
RD"v[200:203],v244 offset:34816\n"
MF"v[132:135],a[180:183],v[212:215],v[132:135]\n"
MF"v[132:135],a[164:167],v[208:211],v[132:135]\n"
MF"v[136:139],a[168:171],v[208:211],v[136:139]\n"
MF"v[136:139],a[184:187],v[212:215],v[136:139]\n"
"s_mov_b32 s49,20480\n"
RD"v[204:207],v245 offset:34816\n"
MF"v[140:143],a[188:191],v[212:215],v[140:143]\n"
SA"s61,s61,524288\n"
BL"v241,s[20:23],s48 offen offset:3072 lds\n"
MF"v[140:143],a[172:175],v[208:211],v[140:143]\n"
"s_cmp_eq_u32 s61,5242880\n"
BS"v[80:83],v250,s[8:11],s49 offen offset:0 sc1\n"
MF"v[144:147],a[160:163],v[216:219],v[144:147]\n"
"s_cselect_b32 s61,0,s61\n"
RD"v[208:211],v244 offset:36864\n"
MF"v[144:147],a[176:179],v[220:223],v[144:147]\n"
MF"v[148:151],a[180:183],v[220:223],v[148:151]\n"
MF"v[148:151],a[164:167],v[216:219],v[148:151]\n"
MF"v[152:155],a[168:171],v[216:219],v[152:155]\n"
SA"m0,s38,114688\n"
RD"v[212:215],v245 offset:36864\n"
SA"s48,s60,s61\n"
MF"v[152:155],a[184:187],v[220:223],v[152:155]\n"
MF"v[156:159],a[188:191],v[220:223],v[156:159]\n"
BL"v238,s[20:23],s48 offen offset:0 lds\n"
MF"v[156:159],a[172:175],v[216:219],v[156:159]\n"
WT"lgkmcnt(1)\n"
MF"v[128:131],a[192:195],v[192:195],v[128:131]\n"
RD"v[216:219],v244 offset:38912\n"
MF"v[128:131],a[208:211],v[196:199],v[128:131]\n"
MF"v[132:135],a[212:215],v[196:199],v[132:135]\n"
MF"v[132:135],a[196:199],v[192:195],v[132:135]\n"
MF"v[136:139],a[200:203],v[192:195],v[136:139]\n"
RD"v[220:223],v245 offset:38912\n"
MF"v[136:139],a[216:219],v[196:199],v[136:139]\n"
MF"v[140:143],a[220:223],v[196:199],v[140:143]\n"
BL"v239,s[20:23],s48 offen offset:1024 lds\n"
MF"v[140:143],a[204:207],v[192:195],v[140:143]\n"
MF"v[144:147],a[192:195],v[200:203],v[144:147]\n"
WT"vmcnt(13)\n"
BS"v[100:103],v252,s[8:11],s92 offen sc1\n"
MF"v[144:147],a[208:211],v[204:207],v[144:147]\n"
MF"v[148:151],a[212:215],v[204:207],v[148:151]\n"
MF"v[148:151],a[196:199],v[200:203],v[148:151]\n"
MF"v[152:155],a[200:203],v[200:203],v[152:155]\n"
MF"v[152:155],a[216:219],v[204:207],v[152:155]\n"
WT"lgkmcnt(0)\n"
"s_barrier\n"
MF"v[156:159],a[220:223],v[204:207],v[156:159]\n"
RD"v[192:195],v242 offset:16384\n"
MF"v[156:159],a[204:207],v[200:203],v[156:159]\n"
BL"v240,s[20:23],s48 offen offset:2048 lds\n"
MF"v[128:131],a[224:227],v[208:211],v[128:131]\n"
MF"v[128:131],a[240:243],v[212:215],v[128:131]\n"
MF"v[132:135],a[244:247],v[212:215],v[132:135]\n"
RD"v[196:199],v243 offset:16384\n"
MF"v[132:135],a[228:231],v[208:211],v[132:135]\n"
MF"v[136:139],a[232:235],v[208:211],v[136:139]\n"
MF"v[136:139],a[248:251],v[212:215],v[136:139]\n"
MF"v[140:143],a[252:255],v[212:215],v[140:143]\n"
SA"s61,s61,524288\n"
RD"v[200:203],v242 offset:18432\n"
MF"v[140:143],a[236:239],v[208:211],v[140:143]\n"
BS"v[88:91],v250,s[8:11],s49 offen offset:1024 sc1\n"
MF"v[144:147],a[224:227],v[216:219],v[144:147]\n"
"s_lshl_b32 s50,s34,3\n"
BL"v241,s[20:23],s48 offen offset:3072 lds\n"
MF"v[144:147],a[240:243],v[220:223],v[144:147]\n"
SA"s51,s50,s30\n"
MF"v[148:151],a[244:247],v[220:223],v[148:151]\n"
"s_mul_i32 s51,s51,20480\n"
RD"v[204:207],v243 offset:18432\n"
MF"v[148:151],a[228:231],v[216:219],v[148:151]\n"
SA"s56,s51,524288\n"
MF"v[152:155],a[232:235],v[216:219],v[152:155]\n"
SA"m0,s36,16384\n"
SA"s48,s60,s61\n"
SA"s57,s56,81920\n"
MF"v[152:155],a[248:251],v[220:223],v[152:155]\n"
"s_sub_u32 s51,s30,s50\n"
MF"v[156:159],a[252:255],v[220:223],v[156:159]\n"
SA"s51,s51,120\n"
RD"v[208:211],v242 offset:20480\n"
MF"v[156:159],a[236:239],v[216:219],v[156:159]\n"
WT"lgkmcnt(2)\n"
BL"v238,s[20:23],s48 offen offset:0 lds\n"
MF"v[0:3],a[0:3],v[192:195],0\n"
"s_mul_i32 s51,s51,20480\n"
SA"s58,s51,524288\n"
"s_lshl_b32 s99,s99,12\n"
MF"v[0:3],a[16:19],v[196:199],v[0:3]\n"
SA"s59,s58,81920\n"
RD"v[212:215],v243 offset:20480\n"
MF"v[8:11],a[20:23],v[196:199],0\n"
"v_add_u32 v235,s99,v224\n"
MF"v[8:11],a[4:7],v[192:195],v[8:11]\n"
MU"v128,s55,v128\n"
MU"v129,s55,v129\n"
MF"v[16:19],a[8:11],v[192:195],0\n"
MU"v130,s55,v130\n"
MU"v131,s55,v131\n"
MF"v[16:19],a[24:27],v[196:199],v[16:19]\n"
RD"v[216:219],v242 offset:22528\n"
MU"v132,s55,v132\n"
MF"v[24:27],a[28:31],v[196:199],0\n"
BL"v239,s[20:23],s48 offen offset:1024 lds\n"
MF"v[24:27],a[12:15],v[192:195],v[24:27]\n"
MU"v133,s55,v133\n"
MU"v134,s55,v134\n"
MF"v[32:35],a[0:3],v[200:203],0\n"
WT"lgkmcnt(1)\n"
MU"v135,s55,v135\n"
PK"v64,v128,v129\n"
MF"v[32:35],a[16:19],v[204:207],v[32:35]\n"
RD"v[220:223],v243 offset:22528\n"
PK"v65,v130,v131\n"
MF"v[40:43],a[20:23],v[204:207],0\n"
PK"v66,v132,v133\n"
PK"v67,v134,v135\n"
MF"v[40:43],a[4:7],v[200:203],v[40:43]\n"
MF"v[48:51],a[8:11],v[200:203],0\n"
MF"v[48:51],a[24:27],v[204:207],v[48:51]\n"
RD"v[192:195],v242 offset:40960\n"
MU"v136,s55,v136\n"
MF"v[56:59],a[28:31],v[204:207],0\n"
BL"v240,s[20:23],s48 offen offset:2048 lds\n"
MF"v[56:59],a[12:15],v[200:203],v[56:59]\n"
MU"v137,s55,v137\n"
MU"v138,s55,v138\n"
MF"v[0:3],a[32:35],v[208:211],v[0:3]\n"
BL"a[0:3],v235,s[8:11],s56 offen offset:0 sc1\n"
MF"v[0:3],a[48:51],v[212:215],v[0:3]\n"
RD"v[196:199],v243 offset:40960\n"
MU"v139,s55,v139\n"
MF"v[8:11],a[52:55],v[212:215],v[8:11]\n"
MU"v140,s55,v140\n"
MU"v141,s55,v141\n"
MF"v[8:11],a[36:39],v[208:211],v[8:11]\n"
"s_mov_b32 s49,24576\n"
MU"v142,s55,v142\n"
MU"v143,s55,v143\n"
MF"v[16:19],a[40:43],v[208:211],v[16:19]\n"
RD"v[200:203],v242 offset:43008\n"
PK"v68,v136,v137\n"
MF"v[16:19],a[56:59],v[212:215],v[16:19]\n"
BS"v[96:99],v250,s[8:11],s49 offen offset:0 sc1\n"
MF"v[24:27],a[60:63],v[212:215],v[24:27]\n"
SA"s61,s61,524288\n"
BL"v241,s[20:23],s48 offen offset:3072 lds\n"
MF"v[24:27],a[44:47],v[208:211],v[24:27]\n"
WT"lgkmcnt(2)\n"
BL"a[4:7],v235,s[8:11],s57 offen offset:0 sc1\n"
MF"v[32:35],a[32:35],v[216:219],v[32:35]\n"
"s_cmp_eq_u32 s61,5242880\n"
RD"v[204:207],v243 offset:43008\n"
PK"v69,v138,v139\n"
MF"v[32:35],a[48:51],v[220:223],v[32:35]\n"
"s_cselect_b32 s61,0,s61\n"
PK"v70,v140,v141\n"
PK"v71,v142,v143\n"
MF"v[40:43],a[52:55],v[220:223],v[40:43]\n"
MU"v144,s55,v144\n"
MU"v145,s55,v145\n"
MF"v[40:43],a[36:39],v[216:219],v[40:43]\n"
MU"v146,s55,v146\n"
MU"v147,s55,v147\n"
MF"v[48:51],a[40:43],v[216:219],v[48:51]\n"
RD"v[208:211],v242 offset:45056\n"
MU"v148,s55,v148\n"
MF"v[48:51],a[56:59],v[220:223],v[48:51]\n"
BL"a[8:11],v235,s[8:11],s58 offen offset:0 sc1\n"
MF"v[56:59],a[60:63],v[220:223],v[56:59]\n"
MU"v149,s55,v149\n"
MU"v150,s55,v150\n"
MF"v[56:59],a[44:47],v[216:219],v[56:59]\n"
MU"v151,s55,v151\n"
PK"v72,v144,v145\n"
MF"v[0:3],a[64:67],v[192:195],v[0:3]\n"
WT"lgkmcnt(1)\n"
RD"v[212:215],v243 offset:45056\n"
PK"v73,v146,v147\n"
MF"v[0:3],a[80:83],v[196:199],v[0:3]\n"
PK"v74,v148,v149\n"
PK"v75,v150,v151\n"
MF"v[8:11],a[84:87],v[196:199],v[8:11]\n"
BL"a[12:15],v235,s[8:11],s59 offen offset:0 sc1\n"
MF"v[8:11],a[68:71],v[192:195],v[8:11]\n"
RD"v[216:219],v242 offset:47104\n"
MU"v152,s55,v152\n"
MF"v[16:19],a[72:75],v[192:195],v[16:19]\n"
MU"v153,s55,v153\n"
MU"v154,s55,v154\n"
MF"v[16:19],a[88:91],v[196:199],v[16:19]\n"
MU"v155,s55,v155\n"
MU"v156,s55,v156\n"
MF"v[24:27],a[92:95],v[196:199],v[24:27]\n"
MU"v157,s55,v157\n"
MU"v158,s55,v158\n"
MF"v[24:27],a[76:79],v[192:195],v[24:27]\n"
RD"v[220:223],v243 offset:47104\n"
MU"v159,s55,v159\n"
MF"v[32:35],a[64:67],v[200:203],v[32:35]\n"
BL"a[16:19],v235,s[8:11],s56 offen offset:1024 sc1\n"
MF"v[32:35],a[80:83],v[204:207],v[32:35]\n"
PK"v76,v152,v153\n"
PK"v77,v154,v155\n"
MF"v[40:43],a[84:87],v[204:207],v[40:43]\n"
PK"v78,v156,v157\n"
PK"v79,v158,v159\n"
MF"v[40:43],a[68:71],v[200:203],v[40:43]\n"
RD"v[192:195],v244 offset:16384\n"
MF"v[48:51],a[72:75],v[200:203],v[48:51]\n"
MF"v[48:51],a[88:91],v[204:207],v[48:51]\n"
MF"v[56:59],a[92:95],v[204:207],v[56:59]\n"
BL"a[20:23],v235,s[8:11],s57 offen offset:1024 sc1\n"
MF"v[56:59],a[76:79],v[200:203],v[56:59]\n"
WT"lgkmcnt(1)\n"
RD"v[196:199],v245 offset:16384\n"
MF"v[0:3],a[96:99],v[208:211],v[0:3]\n"
MF"v[0:3],a[112:115],v[212:215],v[0:3]\n"
MF"v[8:11],a[116:119],v[212:215],v[8:11]\n"
MF"v[8:11],a[100:103],v[208:211],v[8:11]\n"
RD"v[200:203],v244 offset:18432\n"
MF"v[16:19],a[104:107],v[208:211],v[16:19]\n"
BS"v[104:107],v250,s[8:11],s49 offen offset:1024 sc1\n"
MF"v[16:19],a[120:123],v[212:215],v[16:19]\n"
BL"a[24:27],v235,s[8:11],s58 offen offset:1024 sc1\n"
MF"v[24:27],a[124:127],v[212:215],v[24:27]\n"
RD"v[204:207],v245 offset:18432\n"
MF"v[24:27],a[108:111],v[208:211],v[24:27]\n"
MF"v[32:35],a[96:99],v[216:219],v[32:35]\n"
MF"v[32:35],a[112:115],v[220:223],v[32:35]\n"
MF"v[40:43],a[116:119],v[220:223],v[40:43]\n"
RD"v[208:211],v244 offset:20480\n"
MF"v[40:43],a[100:103],v[216:219],v[40:43]\n"
BL"a[28:31],v235,s[8:11],s59 offen offset:1024 sc1\n"
MF"v[48:51],a[104:107],v[216:219],v[48:51]\n"
MF"v[48:51],a[120:123],v[220:223],v[48:51]\n"
MF"v[56:59],a[124:127],v[220:223],v[56:59]\n"
RD"v[212:215],v245 offset:20480\n"
MF"v[56:59],a[108:111],v[216:219],v[56:59]\n"
WT"lgkmcnt(2)\n"
MF"v[0:3],a[128:131],v[192:195],v[0:3]\n"
BL"a[32:35],v235,s[8:11],s56 offen offset:2048 sc1\n"
MF"v[0:3],a[144:147],v[196:199],v[0:3]\n"
MF"v[8:11],a[148:151],v[196:199],v[8:11]\n"
RD"v[216:219],v244 offset:22528\n"
MF"v[8:11],a[132:135],v[192:195],v[8:11]\n"
MF"v[16:19],a[136:139],v[192:195],v[16:19]\n"
MF"v[16:19],a[152:155],v[196:199],v[16:19]\n"
RD"v[220:223],v245 offset:22528\n"
MF"v[24:27],a[156:159],v[196:199],v[24:27]\n"
BL"a[36:39],v235,s[8:11],s57 offen offset:2048 sc1\n"
MF"v[24:27],a[140:143],v[192:195],v[24:27]\n"
MF"v[32:35],a[128:131],v[200:203],v[32:35]\n"
MF"v[32:35],a[144:147],v[204:207],v[32:35]\n"
RD"v[192:195],v244 offset:40960\n"
MF"v[40:43],a[148:151],v[204:207],v[40:43]\n"
MF"v[40:43],a[132:135],v[200:203],v[40:43]\n"
MF"v[48:51],a[136:139],v[200:203],v[48:51]\n"
BL"a[40:43],v235,s[8:11],s58 offen offset:2048 sc1\n"
MF"v[48:51],a[152:155],v[204:207],v[48:51]\n"
RD"v[196:199],v245 offset:40960\n"
MF"v[56:59],a[156:159],v[204:207],v[56:59]\n"
MF"v[56:59],a[140:143],v[200:203],v[56:59]\n"
WT"lgkmcnt(2)\n"
MF"v[0:3],a[160:163],v[208:211],v[0:3]\n"
"s_sub_u32 s53,s37,65536\n"
MF"v[0:3],a[176:179],v[212:215],v[0:3]\n"
"s_mov_b32 s49,28672\n"
RD"v[200:203],v244 offset:43008\n"
MF"v[8:11],a[180:183],v[212:215],v[8:11]\n"
BL"a[44:47],v235,s[8:11],s59 offen offset:2048 sc1\n"
MF"v[8:11],a[164:167],v[208:211],v[8:11]\n"
BS"v[112:115],v250,s[8:11],s49 offen offset:0 sc1\n"
MF"v[16:19],a[168:171],v[208:211],v[16:19]\n"
"v_subrev_u32 v246,s53,v246\n"
"v_subrev_u32 v247,s53,v247\n"
MF"v[16:19],a[184:187],v[212:215],v[16:19]\n"
RD"v[204:207],v245 offset:43008\n"
"v_subrev_u32 v248,s53,v248\n"
MF"v[24:27],a[188:191],v[212:215],v[24:27]\n"
"v_subrev_u32 v249,s53,v249\n"
"v_subrev_u32 v236,65536,v246\n"
MF"v[24:27],a[172:175],v[208:211],v[24:27]\n"
"v_subrev_u32 v237,65536,v247\n"
"v_subrev_u32 v228,65536,v248\n"
MF"v[32:35],a[160:163],v[216:219],v[32:35]\n"
RD"v[208:211],v244 offset:45056\n"
"v_subrev_u32 v229,65536,v249\n"
MF"v[32:35],a[176:179],v[220:223],v[32:35]\n"
BL"a[48:51],v235,s[8:11],s56 offen offset:3072 sc1\n"
MF"v[40:43],a[180:183],v[220:223],v[40:43]\n"
MF"v[40:43],a[164:167],v[216:219],v[40:43]\n"
MF"v[48:51],a[168:171],v[216:219],v[48:51]\n"
RD"v[212:215],v245 offset:45056\n"
MF"v[48:51],a[184:187],v[220:223],v[48:51]\n"
MF"v[56:59],a[188:191],v[220:223],v[56:59]\n"
BL"a[52:55],v235,s[8:11],s57 offen offset:3072 sc1\n"
MF"v[56:59],a[172:175],v[216:219],v[56:59]\n"
WT"lgkmcnt(1)\n"
MF"v[0:3],a[192:195],v[192:195],v[0:3]\n"
RD"v[216:219],v244 offset:47104\n"
MF"v[0:3],a[208:211],v[196:199],v[0:3]\n"
MF"v[8:11],a[212:215],v[196:199],v[8:11]\n"
MF"v[8:11],a[196:199],v[192:195],v[8:11]\n"
MF"v[16:19],a[200:203],v[192:195],v[16:19]\n"
RD"v[220:223],v245 offset:47104\n"
MF"v[16:19],a[216:219],v[196:199],v[16:19]\n"
BL"a[56:59],v235,s[8:11],s58 offen offset:3072 sc1\n"
MF"v[24:27],a[220:223],v[196:199],v[24:27]\n"
MF"v[24:27],a[204:207],v[192:195],v[24:27]\n"
MF"v[32:35],a[192:195],v[200:203],v[32:35]\n"
SA"s99,s99,4096\n"
MF"v[32:35],a[208:211],v[204:207],v[32:35]\n"
"s_cmp_eq_u32 s99,20480\n"
"s_cselect_b32 s99,0,s99\n"
MF"v[40:43],a[212:215],v[204:207],v[40:43]\n"
BL"a[60:63],v235,s[8:11],s59 offen offset:3072 sc1\n"
MF"v[40:43],a[196:199],v[200:203],v[40:43]\n"
MF"v[48:51],a[200:203],v[200:203],v[48:51]\n"
MF"v[48:51],a[216:219],v[204:207],v[48:51]\n"
WT"lgkmcnt(0)\n"
"s_barrier\n"
MF"v[56:59],a[220:223],v[204:207],v[56:59]\n"
RT"a[128:129],v236 offset:49152\n"
MF"v[56:59],a[204:207],v[200:203],v[56:59]\n"
"v_add_u32 v235,s99,v224\n"
MF"v[0:3],a[224:227],v[208:211],v[0:3]\n"
SA"m0,s38,73728\n"
RT"a[130:131],v236 offset:49664\n"
SA"s48,s60,s61\n"
MF"v[0:3],a[240:243],v[212:215],v[0:3]\n"
BL"a[64:67],v235,s[8:11],s56 offen offset:0 sc1\n"
MF"v[8:11],a[244:247],v[212:215],v[8:11]\n"
RT"a[132:133],v236 offset:57344\n"
"v_add_u32 v242,49152,v246\n"
MF"v[8:11],a[228:231],v[208:211],v[8:11]\n"
BS"v[120:123],v250,s[8:11],s49 offen offset:1024 sc1\n"
MF"v[16:19],a[232:235],v[208:211],v[16:19]\n"
BL"v238,s[20:23],s48 offen offset:0 lds\n"
MF"v[16:19],a[248:251],v[212:215],v[16:19]\n"
RT"a[134:135],v236 offset:57856\n"
"v_add_u32 v243,49152,v247\n"
MF"v[24:27],a[252:255],v[212:215],v[24:27]\n"
RT"a[136:137],v237 offset:49152\n"
"v_add_u32 v244,49152,v248\n"
MF"v[24:27],a[236:239],v[208:211],v[24:27]\n"
BL"a[68:71],v235,s[8:11],s57 offen offset:0 sc1\n"
MF"v[32:35],a[224:227],v[216:219],v[32:35]\n"
RT"a[138:139],v237 offset:49664\n"
"v_add_u32 v245,49152,v249\n"
MF"v[32:35],a[240:243],v[220:223],v[32:35]\n"
MF"v[40:43],a[244:247],v[220:223],v[40:43]\n"
RT"a[140:141],v237 offset:57344\n"
MF"v[40:43],a[228:231],v[216:219],v[40:43]\n"
BL"v239,s[20:23],s48 offen offset:1024 lds\n"
MF"v[48:51],a[232:235],v[216:219],v[48:51]\n"
RT"a[142:143],v237 offset:57856\n"
MF"v[48:51],a[248:251],v[220:223],v[48:51]\n"
BL"a[72:75],v235,s[8:11],s58 offen offset:0 sc1\n"
MF"v[56:59],a[252:255],v[220:223],v[56:59]\n"
RT"a[144:145],v228 offset:49152\n"
MF"v[56:59],a[236:239],v[216:219],v[56:59]\n"
WT"vmcnt(25) lgkmcnt(3)\n"
MF"v[80:83],a[128:131],a[0:3],0\n"
WT"vmcnt(19)\n"
RT"a[146:147],v228 offset:49664\n"
MF"v[80:83],a[132:135],a[16:19],v[80:83]\n"
WT"vmcnt(18)\n"
MF"v[88:91],a[132:135],a[20:23],0\n"
RT"a[148:149],v228 offset:57344\n"
MF"v[88:91],a[128:131],a[4:7],v[88:91]\n"
BL"v240,s[20:23],s48 offen offset:2048 lds\n"
MF"v[96:99],a[128:131],a[8:11],0\n"
WT"vmcnt(17)\n"
BL"a[76:79],v235,s[8:11],s59 offen offset:0 sc1\n"
MF"v[96:99],a[132:135],a[24:27],v[96:99]\n"
WT"vmcnt(17)\n"
RT"a[150:151],v228 offset:57856\n"
RT"a[152:153],v229 offset:49152\n"
MF"v[104:107],a[132:135],a[28:31],0\n"
MU"v0,s55,v0\n"
MU"v1,s55,v1\n"
MF"v[104:107],a[128:131],a[12:15],v[104:107]\n"
RT"a[154:155],v229 offset:49664\n"
MU"v2,s55,v2\n"
MF"v[84:87],a[136:139],a[0:3],0\n"
WT"lgkmcnt(4)\n"
MU"v3,s55,v3\n"
MU"v8,s55,v8\n"
MF"v[84:87],a[140:143],a[16:19],v[84:87]\n"
RT"a[156:157],v229 offset:57344\n"
MU"v9,s55,v9\n"
MF"v[92:95],a[140:143],a[20:23],0\n"
SA"s61,s61,524288\n"
BL"a[80:83],v235,s[8:11],s56 offen offset:1024 sc1\n"
MF"v[92:95],a[136:139],a[4:7],v[92:95]\n"
BL"v241,s[20:23],s48 offen offset:3072 lds\n"
MF"v[100:103],a[136:139],a[8:11],0\n"
RT"a[158:159],v229 offset:57856\n"
MU"v10,s55,v10\n"
MF"v[100:103],a[140:143],a[24:27],v[100:103]\n"
RT"a[160:161],v236 offset:53248\n"
MU"v11,s55,v11\n"
MF"v[108:111],a[140:143],a[28:31],0\n"
PK"v0,v0,v1\n"
PK"v1,v2,v3\n"
MF"v[108:111],a[136:139],a[12:15],v[108:111]\n"
RT"a[162:163],v236 offset:53760\n"
PK"v2,v8,v9\n"
MF"v[112:115],a[144:147],a[0:3],0\n"
WT"lgkmcnt(3)\n"
BL"a[84:87],v235,s[8:11],s57 offen offset:1024 sc1\n"
MF"v[112:115],a[148:151],a[16:19],v[112:115]\n"
RT"a[164:165],v236 offset:61440\n"
PK"v3,v10,v11\n"
MF"v[120:123],a[148:151],a[20:23],0\n"
MU"v16,s55,v16\n"
MU"v17,s55,v17\n"
MF"v[120:123],a[144:147],a[4:7],v[120:123]\n"
RT"a[166:167],v236 offset:61952\n"
MU"v18,s55,v18\n"
MF"v[128:131],a[144:147],a[8:11],0\n"
MU"v19,s55,v19\n"
MU"v24,s55,v24\n"
MF"v[128:131],a[148:151],a[24:27],v[128:131]\n"
RT"a[168:169],v237 offset:53248\n"
MU"v25,s55,v25\n"
MF"v[136:139],a[148:151],a[28:31],0\n"
MU"v26,s55,v26\n"
MU"v27,s55,v27\n"
MF"v[136:139],a[144:147],a[12:15],v[136:139]\n"
RT"a[170:171],v237 offset:53760\n"
PK"v16,v16,v17\n"
MF"v[116:119],a[152:155],a[0:3],0\n"
WT"lgkmcnt(3)\n"
BL"a[88:91],v235,s[8:11],s58 offen offset:1024 sc1\n"
MF"v[116:119],a[156:159],a[16:19],v[116:119]\n"
"s_mov_b32 s49,262144\n"
RT"a[172:173],v237 offset:61440\n"
PK"v17,v18,v19\n"
MF"v[124:127],a[156:159],a[20:23],0\n"
RT"a[174:175],v237 offset:61952\n"
PK"v18,v24,v25\n"
MF"v[124:127],a[152:155],a[4:7],v[124:127]\n"
BS"v[4:7],v250,s[8:11],s49 offen offset:0 sc1\n"
MF"v[132:135],a[152:155],a[8:11],0\n"
RT"a[176:177],v228 offset:53248\n"
PK"v19,v26,v27\n"
MF"v[132:135],a[156:159],a[24:27],v[132:135]\n"
MU"v32,s55,v32\n"
MU"v33,s55,v33\n"
MF"v[140:143],a[156:159],a[28:31],0\n"
RT"a[178:179],v228 offset:53760\n"
MU"v34,s55,v34\n"
MF"v[140:143],a[152:155],a[12:15],v[140:143]\n"
BL"a[92:95],v235,s[8:11],s59 offen offset:1024 sc1\n"
MF"v[144:147],a[160:163],a[0:3],0\n"
WT"lgkmcnt(3)\n"
RT"a[180:181],v228 offset:61440\n"
MU"v35,s55,v35\n"
MF"v[144:147],a[164:167],a[16:19],v[144:147]\n"
MU"v40,s55,v40\n"
MU"v41,s55,v41\n"
MF"v[152:155],a[164:167],a[20:23],0\n"
SA"m0,s38,122880\n"
RT"a[182:183],v228 offset:61952\n"
MU"v42,s55,v42\n"
MF"v[152:155],a[160:163],a[4:7],v[152:155]\n"
SA"s48,s60,s61\n"
MU"v43,s55,v43\n"
PK"v32,v32,v33\n"
MF"v[160:163],a[160:163],a[8:11],0\n"
"s_mov_b32 s88,4096\n"
RT"a[184:185],v229 offset:53248\n"
PK"v33,v34,v35\n"
MF"v[160:163],a[164:167],a[24:27],v[160:163]\n"
BL"v238,s[20:23],s48 offen offset:0 lds\n"
MF"v[168:171],a[164:167],a[28:31],0\n"
BL"a[96:99],v235,s[8:11],s56 offen offset:2048 sc1\n"
MF"v[168:171],a[160:163],a[12:15],v[168:171]\n"
RT"a[186:187],v229 offset:53760\n"
PK"v34,v40,v41\n"
MF"v[148:151],a[168:171],a[0:3],0\n"
WT"lgkmcnt(3)\n"
RT"a[188:189],v229 offset:61440\n"
PK"v35,v42,v43\n"
MF"v[148:151],a[172:175],a[16:19],v[148:151]\n"
MU"v48,s55,v48\n"
MU"v49,s55,v49\n"
MF"v[156:159],a[172:175],a[20:23],0\n"
RT"a[190:191],v229 offset:61952\n"
MU"v50,s55,v50\n"
MF"v[156:159],a[168:171],a[4:7],v[156:159]\n"
RT"a[128:129],v236 offset:32768\n"
MU"v51,s55,v51\n"
MF"v[164:167],a[168:171],a[8:11],0\n"
BL"v239,s[20:23],s48 offen offset:1024 lds\n"
MF"v[164:167],a[172:175],a[24:27],v[164:167]\n"
BL"a[100:103],v235,s[8:11],s57 offen offset:2048 sc1\n"
MF"v[172:175],a[172:175],a[28:31],0\n"
RT"a[130:131],v236 offset:33280\n"
MU"v56,s55,v56\n"
MF"v[172:175],a[168:171],a[12:15],v[172:175]\n"
RT"a[132:133],v236 offset:40960\n"
MU"v57,s55,v57\n"
MF"v[176:179],a[176:179],a[0:3],0\n"
WT"lgkmcnt(4)\n"
MU"v58,s55,v58\n"
MU"v59,s55,v59\n"
MF"v[176:179],a[180:183],a[16:19],v[176:179]\n"
RT"a[134:135],v236 offset:41472\n"
PK"v48,v48,v49\n"
MF"v[184:187],a[180:183],a[20:23],0\n"
PK"v49,v50,v51\n"
PK"v50,v56,v57\n"
MF"v[184:187],a[176:179],a[4:7],v[184:187]\n"
RT"a[136:137],v237 offset:32768\n"
PK"v51,v58,v59\n"
MF"v[192:195],a[176:179],a[8:11],0\n"
BL"a[104:107],v235,s[8:11],s58 offen offset:2048 sc1\n"
MF"v[192:195],a[180:183],a[24:27],v[192:195]\n"
BL"v240,s[20:23],s48 offen offset:2048 lds\n"
MF"v[200:203],a[180:183],a[28:31],0\n"
RT"a[138:139],v237 offset:33280\n"
MF"v[200:203],a[176:179],a[12:15],v[200:203]\n"
RT"a[140:141],v237 offset:40960\n"
MF"v[180:183],a[184:187],a[0:3],0\n"
WT"lgkmcnt(3)\n"
MF"v[180:183],a[188:191],a[16:19],v[180:183]\n"
RT"a[142:143],v237 offset:41472\n"
MF"v[188:191],a[188:191],a[20:23],0\n"
BL"a[108:111],v235,s[8:11],s59 offen offset:2048 sc1\n"
MF"v[188:191],a[184:187],a[4:7],v[188:191]\n"
RT"a[144:145],v228 offset:32768\n"
MF"v[196:199],a[184:187],a[8:11],0\n"
SA"s61,s61,524288\n"
BS"v[12:15],v250,s[8:11],s49 offen offset:1024 sc1\n"
MF"v[196:199],a[188:191],a[24:27],v[196:199]\n"
"s_cmp_eq_u32 s61,5242880\n"
BL"v241,s[20:23],s48 offen offset:3072 lds\n"
MF"v[204:207],a[188:191],a[28:31],0\n"
"s_cselect_b32 s61,0,s61\n"
RT"a[146:147],v228 offset:33280\n"
MF"v[204:207],a[184:187],a[12:15],v[204:207]\n"
WT"vmcnt(31)\n"
RT"a[148:149],v228 offset:40960\n"
MF"v[80:83],a[128:131],a[32:35],v[80:83]\n"
WT"vmcnt(26)\n"
MF"v[80:83],a[132:135],a[48:51],v[80:83]\n"
WT"vmcnt(25)\n"
RT"a[150:151],v228 offset:41472\n"
MF"v[88:91],a[132:135],a[52:55],v[88:91]\n"
BL"a[112:115],v235,s[8:11],s56 offen offset:3072 sc1\n"
MF"v[88:91],a[128:131],a[36:39],v[88:91]\n"
RT"a[152:153],v229 offset:32768\n"
MF"v[96:99],a[128:131],a[40:43],v[96:99]\n"
WT"vmcnt(25)\n"
RT"a[154:155],v229 offset:33280\n"
MF"v[96:99],a[132:135],a[56:59],v[96:99]\n"
WT"vmcnt(24)\n"
MF"v[104:107],a[132:135],a[60:63],v[104:107]\n"
RT"a[156:157],v229 offset:40960\n"
MF"v[104:107],a[128:131],a[44:47],v[104:107]\n"
WT"lgkmcnt(4)\n"
MF"v[84:87],a[136:139],a[32:35],v[84:87]\n"
RT"a[158:159],v229 offset:41472\n"
MF"v[84:87],a[140:143],a[48:51],v[84:87]\n"
BL"a[116:119],v235,s[8:11],s57 offen offset:3072 sc1\n"
MF"v[92:95],a[140:143],a[52:55],v[92:95]\n"
RT"a[160:161],v236 offset:36864\n"
MF"v[92:95],a[136:139],a[36:39],v[92:95]\n"
MF"v[100:103],a[136:139],a[40:43],v[100:103]\n"
RT"a[162:163],v236 offset:37376\n"
MF"v[100:103],a[140:143],a[56:59],v[100:103]\n"
MF"v[108:111],a[140:143],a[60:63],v[108:111]\n"
RT"a[164:165],v236 offset:45056\n"
MF"v[108:111],a[136:139],a[44:47],v[108:111]\n"
BL"a[120:123],v235,s[8:11],s58 offen offset:3072 sc1\n"
MF"v[112:115],a[144:147],a[32:35],v[112:115]\n"
WT"lgkmcnt(3)\n"
RT"a[166:167],v236 offset:45568\n"
MF"v[112:115],a[148:151],a[48:51],v[112:115]\n"
MF"v[120:123],a[148:151],a[52:55],v[120:123]\n"
RT"a[168:169],v237 offset:36864\n"
MF"v[120:123],a[144:147],a[36:39],v[120:123]\n"
MF"v[128:131],a[144:147],a[40:43],v[128:131]\n"
RT"a[170:171],v237 offset:37376\n"
MF"v[128:131],a[148:151],a[56:59],v[128:131]\n"
SA"s99,s99,4096\n"
RT"a[172:173],v237 offset:45056\n"
MF"v[136:139],a[148:151],a[60:63],v[136:139]\n"
"s_cmp_eq_u32 s99,20480\n"
BL"a[124:127],v235,s[8:11],s59 offen offset:3072 sc1\n"
MF"v[136:139],a[144:147],a[44:47],v[136:139]\n"
"s_cselect_b32 s99,0,s99\n"
RT"a[174:175],v237 offset:45568\n"
MF"v[116:119],a[152:155],a[32:35],v[116:119]\n"
MF"v[116:119],a[156:159],a[48:51],v[116:119]\n"
RT"a[176:177],v228 offset:36864\n"
MF"v[124:127],a[156:159],a[52:55],v[124:127]\n"
MF"v[124:127],a[152:155],a[36:39],v[124:127]\n"
RT"a[178:179],v228 offset:37376\n"
MF"v[132:135],a[152:155],a[40:43],v[132:135]\n"
"s_mov_b32 s49,266240\n"
"v_add_u32 v235,s99,v224\n"
MF"v[132:135],a[156:159],a[56:59],v[132:135]\n"
RT"a[180:181],v228 offset:45056\n"
MF"v[140:143],a[156:159],a[60:63],v[140:143]\n"
BL"a[0:3],v235,s[8:11],s56 offen offset:0 sc1\n"
MF"v[140:143],a[152:155],a[44:47],v[140:143]\n"
WT"lgkmcnt(3)\n"
BS"v[20:23],v250,s[8:11],s49 offen offset:0 sc1\n"
MF"v[144:147],a[160:163],a[32:35],v[144:147]\n"
RT"a[182:183],v228 offset:45568\n"
MF"v[144:147],a[164:167],a[48:51],v[144:147]\n"
RT"a[184:185],v229 offset:36864\n"
MF"v[152:155],a[164:167],a[52:55],v[152:155]\n"
MF"v[152:155],a[160:163],a[36:39],v[152:155]\n"
RT"a[186:187],v229 offset:37376\n"
MF"v[160:163],a[160:163],a[40:43],v[160:163]\n"
BL"a[4:7],v235,s[8:11],s57 offen offset:0 sc1\n"
MF"v[160:163],a[164:167],a[56:59],v[160:163]\n"
RT"a[188:189],v229 offset:45056\n"
MF"v[168:171],a[164:167],a[60:63],v[168:171]\n"
MF"v[168:171],a[160:163],a[44:47],v[168:171]\n"
RT"a[190:191],v229 offset:45568\n"
MF"v[148:151],a[168:171],a[32:35],v[148:151]\n"
"s_lshl_b32 s50,s35,3\n"
SA"s50,s50,s34\n"
"s_lshl_b32 s50,s50,3\n"
MF"v[148:151],a[172:175],a[48:51],v[148:151]\n"
SA"s50,s50,2304\n"
SA"s51,s50,s45\n"
MF"v[156:159],a[172:175],a[52:55],v[156:159]\n"
MF"v[156:159],a[168:171],a[36:39],v[156:159]\n"
BL"a[8:11],v235,s[8:11],s58 offen offset:0 sc1\n"
MF"v[164:167],a[168:171],a[40:43],v[164:167]\n"
MF"v[164:167],a[172:175],a[56:59],v[164:167]\n"
WT"vmcnt(29) lgkmcnt(0)\n"
"s_barrier\n"
MF"v[172:175],a[172:175],a[60:63],v[172:175]\n"
RT"a[128:129],v236 offset:0\n"
MF"v[172:175],a[168:171],a[44:47],v[172:175]\n"
"buffer_store_dwordx2 v[232:233],v230,s[8:11],s51 offen sc1\n"
MF"v[176:179],a[176:179],a[32:35],v[176:179]\n"
RT"a[130:131],v236 offset:512\n"
MF"v[176:179],a[180:183],a[48:51],v[176:179]\n"
MF"v[184:187],a[180:183],a[52:55],v[184:187]\n"
SA"m0,s38,81920\n"
RT"a[132:133],v236 offset:8192\n"
SA"s48,s60,s61\n"
MF"v[184:187],a[176:179],a[36:39],v[184:187]\n"
BL"a[12:15],v235,s[8:11],s59 offen offset:0 sc1\n"
MF"v[192:195],a[176:179],a[40:43],v[192:195]\n"
RT"a[134:135],v236 offset:8704\n"
MF"v[192:195],a[180:183],a[56:59],v[192:195]\n"
BL"v238,s[20:23],s48 offen offset:0 lds\n"
MF"v[200:203],a[180:183],a[60:63],v[200:203]\n"
RT"a[136:137],v237 offset:0\n"
MF"v[200:203],a[176:179],a[44:47],v[200:203]\n"
MF"v[180:183],a[184:187],a[32:35],v[180:183]\n"
RT"a[138:139],v237 offset:512\n"
MF"v[180:183],a[188:191],a[48:51],v[180:183]\n"
BL"a[16:19],v235,s[8:11],s56 offen offset:1024 sc1\n"
MF"v[188:191],a[188:191],a[52:55],v[188:191]\n"
RT"a[140:141],v237 offset:8192\n"
MF"v[188:191],a[184:187],a[36:39],v[188:191]\n"
MF"v[196:199],a[184:187],a[40:43],v[196:199]\n"
RT"a[142:143],v237 offset:8704\n"
MF"v[196:199],a[188:191],a[56:59],v[196:199]\n"
BL"v239,s[20:23],s48 offen offset:1024 lds\n"
MF"v[204:207],a[188:191],a[60:63],v[204:207]\n"
SA"s49,s88,262144\n"
RT"a[144:145],v228 offset:0\n"
MF"v[204:207],a[184:187],a[44:47],v[204:207]\n"
"s_branch LQ_%=\n"
"LV_%=:\n"
WT"vmcnt(17)\n"
"v_lshlrev_b32 v148,16,v128\n"
"v_and_b32 v149,0xffff0000,v128\n"
"v_lshlrev_b32 v150,16,v129\n"
"v_and_b32 v151,0xffff0000,v129\n"
WT"vmcnt(16)\n"
"v_lshlrev_b32 v152,16,v130\n"
"v_and_b32 v153,0xffff0000,v130\n"
"v_lshlrev_b32 v154,16,v131\n"
"v_and_b32 v155,0xffff0000,v131\n"
WT"vmcnt(15)\n"
"v_lshlrev_b32 v156,16,v132\n"
"v_and_b32 v157,0xffff0000,v132\n"
"v_lshlrev_b32 v158,16,v133\n"
"v_and_b32 v159,0xffff0000,v133\n"
WT"vmcnt(14)\n"
"v_lshlrev_b32 v160,16,v134\n"
"v_and_b32 v161,0xffff0000,v134\n"
"v_lshlrev_b32 v162,16,v135\n"
"v_and_b32 v163,0xffff0000,v135\n"
WT"vmcnt(13)\n"
"v_lshlrev_b32 v164,16,v136\n"
"v_and_b32 v165,0xffff0000,v136\n"
"v_lshlrev_b32 v166,16,v137\n"
"v_and_b32 v167,0xffff0000,v137\n"
"v_add_f32 v188,v148,v149\n"
"v_add_f32 v189,v150,v151\n"
"v_add_f32 v188,v188,v152\n"
"v_add_f32 v189,v189,v153\n"
"v_add_f32 v188,v188,v154\n"
"v_add_f32 v189,v189,v155\n"
"v_add_f32 v188,v188,v156\n"
"v_add_f32 v189,v189,v157\n"
"v_add_f32 v188,v188,v158\n"
"v_add_f32 v189,v189,v159\n"
"v_add_f32 v188,v188,v160\n"
"v_add_f32 v189,v189,v161\n"
"v_add_f32 v188,v188,v162\n"
"v_add_f32 v189,v189,v163\n"
"v_add_f32 v188,v188,v164\n"
"v_add_f32 v189,v189,v165\n"
"v_add_f32 v188,v188,v166\n"
"v_add_f32 v189,v189,v167\n"
"v_add_f32 v188,v188,v189\n"
"s_nop 1\n"
"v_add_f32_dpp v188,v188,v188 quad_perm:[1,0,3,2] row_mask:0xf bank_mask:0xf\n"
WT"vmcnt(12)\n"
"v_lshlrev_b32 v168,16,v138\n"
"v_and_b32 v169,0xffff0000,v138\n"
"v_add_f32_dpp v188,v188,v188 quad_perm:[2,3,0,1] row_mask:0xf bank_mask:0xf\n"
"v_lshlrev_b32 v170,16,v139\n"
"v_and_b32 v171,0xffff0000,v139\n"
"v_add_f32_dpp v188,v188,v188 row_half_mirror row_mask:0xf bank_mask:0xf\n"
WT"vmcnt(11)\n"
"v_lshlrev_b32 v172,16,v140\n"
SA"m0,s36,0\n"
"s_bfe_u32 s48,s46,0x30000\n"
"s_lshl_b32 s48,s48,7\n"
BL"v234,s[12:15],s48 offen offset:0 lds\n"
BL"v235,s[12:15],s48 offen offset:1024 lds\n"
BL"v236,s[12:15],s48 offen offset:2048 lds\n"
BL"v237,s[12:15],s48 offen offset:3072 lds\n"
"v_and_b32 v173,0xffff0000,v140\n"
"v_add_f32_dpp v188,v188,v188 row_mirror row_mask:0xf bank_mask:0xf\n"
"v_lshlrev_b32 v174,16,v141\n"
"v_and_b32 v175,0xffff0000,v141\n"
"v_mov_b32 v189,v188\n"
WT"vmcnt(14)\n"
"v_lshlrev_b32 v176,16,v142\n"
"v_and_b32 v177,0xffff0000,v142\n"
"v_permlane16_swap_b32 v189,v188\n"
"v_lshlrev_b32 v178,16,v143\n"
"v_and_b32 v179,0xffff0000,v143\n"
"v_add_f32 v188,v188,v189\n"
WT"vmcnt(13)\n"
"v_lshlrev_b32 v180,16,v144\n"
"v_and_b32 v181,0xffff0000,v144\n"
MU"v191,0x3acccccd,v188\n"
"v_lshlrev_b32 v182,16,v145\n"
"v_and_b32 v183,0xffff0000,v145\n"
PK"v128,v191,v191\n"
WT"vmcnt(12)\n"
"v_lshlrev_b32 v184,16,v146\n"
"v_and_b32 v185,0xffff0000,v146\n"
"v_lshlrev_b32 v128,16,v128\n"
"v_lshlrev_b32 v186,16,v147\n"
"v_and_b32 v187,0xffff0000,v147\n"
"v_sub_f32 v148,v148,v128\n"
"v_sub_f32 v149,v149,v128\n"
"v_sub_f32 v150,v150,v128\n"
"v_sub_f32 v151,v151,v128\n"
"v_sub_f32 v152,v152,v128\n"
"v_sub_f32 v153,v153,v128\n"
"v_sub_f32 v154,v154,v128\n"
"v_sub_f32 v155,v155,v128\n"
"v_sub_f32 v156,v156,v128\n"
"v_sub_f32 v157,v157,v128\n"
"v_sub_f32 v158,v158,v128\n"
"v_sub_f32 v159,v159,v128\n"
"v_sub_f32 v160,v160,v128\n"
"v_sub_f32 v161,v161,v128\n"
"v_sub_f32 v162,v162,v128\n"
"v_sub_f32 v163,v163,v128\n"
"v_sub_f32 v164,v164,v128\n"
"v_sub_f32 v165,v165,v128\n"
"v_sub_f32 v166,v166,v128\n"
"v_sub_f32 v167,v167,v128\n"
MU"v188,v148,v148\n"
MU"v189,v149,v149\n"
"v_fmac_f32 v188,v150,v150\n"
"v_fmac_f32 v189,v151,v151\n"
"v_fmac_f32 v188,v152,v152\n"
SA"m0,s37,8192\n"
"s_bfe_u32 s48,s46,0x30004\n"
"s_lshl_b32 s48,s48,19\n"
BL"v238,s[16:19],s48 offen offset:0 lds\n"
BL"v239,s[16:19],s48 offen offset:1024 lds\n"
BL"v240,s[16:19],s48 offen offset:2048 lds\n"
BL"v241,s[16:19],s48 offen offset:3072 lds\n"
"v_fmac_f32 v189,v153,v153\n"
"v_fmac_f32 v188,v154,v154\n"
"v_fmac_f32 v189,v155,v155\n"
"v_fmac_f32 v188,v156,v156\n"
"v_fmac_f32 v189,v157,v157\n"
"v_fmac_f32 v188,v158,v158\n"
"v_fmac_f32 v189,v159,v159\n"
"v_fmac_f32 v188,v160,v160\n"
"v_fmac_f32 v189,v161,v161\n"
"v_fmac_f32 v188,v162,v162\n"
"v_fmac_f32 v189,v163,v163\n"
"v_fmac_f32 v188,v164,v164\n"
"v_fmac_f32 v189,v165,v165\n"
"v_fmac_f32 v188,v166,v166\n"
"v_fmac_f32 v189,v167,v167\n"
"v_add_f32 v188,v188,v189\n"
PK"v149,v148,v149\n"
"v_lshlrev_b32 v148,16,v149\n"
"v_add_f32_dpp v188,v188,v188 quad_perm:[1,0,3,2] row_mask:0xf bank_mask:0xf\n"
"v_and_b32 v149,0xffff0000,v149\n"
PK"v151,v150,v151\n"
"v_add_f32_dpp v188,v188,v188 quad_perm:[2,3,0,1] row_mask:0xf bank_mask:0xf\n"
"v_lshlrev_b32 v150,16,v151\n"
"v_and_b32 v151,0xffff0000,v151\n"
"v_add_f32_dpp v188,v188,v188 row_half_mirror row_mask:0xf bank_mask:0xf\n"
PK"v153,v152,v153\n"
"v_lshlrev_b32 v152,16,v153\n"
"v_add_f32_dpp v188,v188,v188 row_mirror row_mask:0xf bank_mask:0xf\n"
"v_and_b32 v153,0xffff0000,v153\n"
PK"v155,v154,v155\n"
"v_mov_b32 v189,v188\n"
"v_lshlrev_b32 v154,16,v155\n"
"v_and_b32 v155,0xffff0000,v155\n"
"v_permlane16_swap_b32 v189,v188\n"
PK"v157,v156,v157\n"
"v_lshlrev_b32 v156,16,v157\n"
"v_add_f32 v188,v188,v189\n"
"v_and_b32 v157,0xffff0000,v157\n"
PK"v159,v158,v159\n"
"v_sub_f32 v191,v191,v128\n"
"v_lshlrev_b32 v158,16,v159\n"
"v_and_b32 v159,0xffff0000,v159\n"
MU"v188,0x3acccccd,v188\n"
PK"v161,v160,v161\n"
"v_lshlrev_b32 v160,16,v161\n"
"v_fma_f32 v188,-v191,v191,v188\n"
"v_and_b32 v161,0xffff0000,v161\n"
PK"v163,v162,v163\n"
SA"m0,s37,12288\n"
"s_bfe_u32 s48,s46,0x30004\n"
"s_lshl_b32 s48,s48,19\n"
"s_bitset1_b32 s48,18\n"
BL"v238,s[16:19],s48 offen offset:0 lds\n"
BL"v239,s[16:19],s48 offen offset:1024 lds\n"
BL"v240,s[16:19],s48 offen offset:2048 lds\n"
BL"v241,s[16:19],s48 offen offset:3072 lds\n"
"v_max_f32 v188,0,v188\n"
"v_lshlrev_b32 v162,16,v163\n"
"v_and_b32 v163,0xffff0000,v163\n"
PK"v188,v188,v188\n"
PK"v165,v164,v165\n"
"v_lshlrev_b32 v164,16,v165\n"
"v_lshlrev_b32 v188,16,v188\n"
"v_and_b32 v165,0xffff0000,v165\n"
PK"v167,v166,v167\n"
"v_add_f32 v188,0x3727c5ac,v188\n"
"v_lshlrev_b32 v166,16,v167\n"
"v_and_b32 v167,0xffff0000,v167\n"
PK"v188,v188,v188\n"
"v_bfe_u32 v130,v224,5,4\n"
"v_mul_u32_u24 v130,144,v130\n"
"v_lshlrev_b32 v188,16,v188\n"
"v_bfe_u32 v131,v224,4,1\n"
"v_lshl_add_u32 v130,v131,3,v130\n"
"v_sqrt_f32 v189,v188\n"
"v_lshrrev_b32 v131,9,v224\n"
"v_lshl_add_u32 v130,v131,4,v130\n"
"v_rcp_f32 v190,v189\n"
"s_lshl_b32 s52,s30,5\n"
"v_add_u32 v130,s52,v130\n"
"v_fma_f32 v188,-v189,v189,v188\n"
MU"v190,0.5,v190\n"
"v_fma_f32 v189,v188,v190,v189\n"
PK"v189,v189,v189\n"
"v_lshlrev_b32 v189,16,v189\n"
"v_rcp_f32 v129,v189\n"
"s_nop 0\n"
MU"v148,v148,v129\n"
MU"v149,v149,v129\n"
PK"v138,v148,v149\n"
MU"v150,v150,v129\n"
MU"v151,v151,v129\n"
PK"v139,v150,v151\n"
"ds_write_b64 v130,v[138:139] offset:49152\n"
MU"v152,v152,v129\n"
MU"v153,v153,v129\n"
PK"v140,v152,v153\n"
MU"v154,v154,v129\n"
MU"v155,v155,v129\n"
PK"v141,v154,v155\n"
"ds_write_b64 v130,v[140:141] offset:51456\n"
MU"v156,v156,v129\n"
MU"v157,v157,v129\n"
PK"v142,v156,v157\n"
SA"m0,s36,16384\n"
"s_bfe_u32 s48,s46,0x30004\n"
"s_lshl_b32 s48,s48,7\n"
BL"v234,s[12:15],s48 offen offset:0 lds\n"
BL"v235,s[12:15],s48 offen offset:1024 lds\n"
BL"v236,s[12:15],s48 offen offset:2048 lds\n"
BL"v237,s[12:15],s48 offen offset:3072 lds\n"
MU"v158,v158,v129\n"
MU"v159,v159,v129\n"
PK"v143,v158,v159\n"
"ds_write_b64 v130,v[142:143] offset:53760\n"
MU"v160,v160,v129\n"
MU"v161,v161,v129\n"
PK"v144,v160,v161\n"
MU"v162,v162,v129\n"
MU"v163,v163,v129\n"
PK"v145,v162,v163\n"
"ds_write_b64 v130,v[144:145] offset:56064\n"
MU"v164,v164,v129\n"
MU"v165,v165,v129\n"
PK"v146,v164,v165\n"
MU"v166,v166,v129\n"
MU"v167,v167,v129\n"
PK"v147,v166,v167\n"
"ds_write_b64 v130,v[146:147] offset:58368\n"
"v_lshlrev_b32 v148,16,v138\n"
"v_and_b32 v149,0xffff0000,v138\n"
MU"v188,v148,v168\n"
MU"v189,v149,v169\n"
"v_lshlrev_b32 v150,16,v139\n"
"v_and_b32 v151,0xffff0000,v139\n"
"v_fmac_f32 v188,v150,v170\n"
"v_fmac_f32 v189,v151,v171\n"
"v_lshlrev_b32 v152,16,v140\n"
"v_and_b32 v153,0xffff0000,v140\n"
"v_fmac_f32 v188,v152,v172\n"
"v_fmac_f32 v189,v153,v173\n"
"v_lshlrev_b32 v154,16,v141\n"
"v_and_b32 v155,0xffff0000,v141\n"
"v_fmac_f32 v188,v154,v174\n"
"v_fmac_f32 v189,v155,v175\n"
"v_lshlrev_b32 v156,16,v142\n"
"v_and_b32 v157,0xffff0000,v142\n"
"v_fmac_f32 v188,v156,v176\n"
"v_fmac_f32 v189,v157,v177\n"
"v_lshlrev_b32 v158,16,v143\n"
"v_and_b32 v159,0xffff0000,v143\n"
"v_fmac_f32 v188,v158,v178\n"
"v_fmac_f32 v189,v159,v179\n"
"v_lshlrev_b32 v160,16,v144\n"
"v_and_b32 v161,0xffff0000,v144\n"
"v_fmac_f32 v188,v160,v180\n"
"v_fmac_f32 v189,v161,v181\n"
"v_lshlrev_b32 v162,16,v145\n"
"v_and_b32 v163,0xffff0000,v145\n"
WT"lgkmcnt(0)\n"
SA"s28,s28,0x7f4a7c15\n"
"s_addc_u32 s29,s29,0x9e3779b9\n"
"v_mov_b32 v232,s28\n"
"v_mov_b32 v233,s29\n"
WT"vmcnt(20)\n"
RT"a[0:1],v246 offset:0\n"
RT"a[2:3],v246 offset:512\n"
RT"a[4:5],v247 offset:0\n"
RT"a[6:7],v247 offset:512\n"
RT"a[8:9],v248 offset:0\n"
RT"a[10:11],v248 offset:512\n"
RT"a[12:13],v249 offset:0\n"
RT"a[14:15],v249 offset:512\n"
WT"vmcnt(16)\n"
RT"a[16:17],v246 offset:4096\n"
RT"a[18:19],v246 offset:4608\n"
RT"a[20:21],v247 offset:4096\n"
RT"a[22:23],v247 offset:4608\n"
RT"a[24:25],v248 offset:4096\n"
RT"a[26:27],v248 offset:4608\n"
RT"a[28:29],v249 offset:4096\n"
RT"a[30:31],v249 offset:4608\n"
WT"vmcnt(12) lgkmcnt(15)\n"
"s_barrier\n"
RD"v[192:195],v242 offset:0\n"
RD"v[196:199],v243 offset:0\n"
RD"v[200:203],v242 offset:2048\n"
RD"v[204:207],v243 offset:2048\n"
RD"v[208:211],v242 offset:4096\n"
"v_lshrrev_b32 v131,7,v224\n"
WT"lgkmcnt(4)\n"
MF"v[0:3],v[192:195],a[0:3],0\n"
WT"lgkmcnt(3)\n"
RD"v[212:215],v243 offset:4096\n"
"v_lshl_add_u32 v131,v131,4,v224\n"
MF"v[0:3],v[196:199],a[16:19],v[0:3]\n"
SA"m0,s37,16384\n"
"s_bfe_u32 s48,s46,0x30008\n"
"s_lshl_b32 s48,s48,19\n"
MF"v[8:11],v[196:199],a[20:23],0\n"
"s_mul_i32 s52,s30,1152\n"
BL"v238,s[16:19],s48 offen offset:0 lds\n"
MF"v[8:11],v[192:195],a[4:7],v[8:11]\n"
"v_add_u32 v128,s52,v131\n"
"s_and_b32 s52,s30,1\n"
MF"v[16:19],v[192:195],a[8:11],0\n"
"s_mul_i32 s51,s52,1152\n"
RD"v[216:219],v242 offset:6144\n"
"v_add_u32 v131,s51,v131\n"
MF"v[16:19],v[196:199],a[24:27],v[16:19]\n"
"s_lshl_b32 s52,s52,10\n"
"v_fmac_f32 v188,v162,v182\n"
"v_fmac_f32 v189,v163,v183\n"
MF"v[24:27],v[196:199],a[28:31],0\n"
BL"v239,s[16:19],s48 offen offset:1024 lds\n"
MF"v[24:27],v[192:195],a[12:15],v[24:27]\n"
WT"lgkmcnt(1)\n"
RD"v[220:223],v243 offset:6144\n"
RD"v[132:135],v128 offset:49152\n"
MF"v[4:7],v[200:203],a[0:3],0\n"
WT"vmcnt(10)\n"
RT"a[32:33],v246 offset:8192\n"
RD"a[240:243],v128 offset:53760\n"
MF"v[4:7],v[204:207],a[16:19],v[4:7]\n"
"s_lshr_b32 s50,s31,1\n"
RD"a[244:247],v131 offset:58368\n"
"v_lshlrev_b32 v164,16,v146\n"
MF"v[12:15],v[204:207],a[20:23],0\n"
"s_mul_i32 s50,s50,20480\n"
"v_and_b32 v129,0x70,v224\n"
"v_and_b32 v165,0xffff0000,v146\n"
MF"v[12:15],v[200:203],a[4:7],v[12:15]\n"
"s_and_b32 s51,s31,1\n"
RD"v[192:195],v242 offset:8192\n"
RT"a[34:35],v246 offset:8704\n"
MF"v[20:23],v[200:203],a[8:11],0\n"
"s_lshl_b32 s51,s51,7\n"
BL"v240,s[16:19],s48 offen offset:2048 lds\n"
MF"v[20:23],v[204:207],a[24:27],v[20:23]\n"
SA"s50,s50,s51\n"
"v_and_b32 v130,0x380,v224\n"
"v_fmac_f32 v188,v164,v184\n"
MF"v[28:31],v[204:207],a[28:31],0\n"
SA"s50,s50,524288\n"
RT"a[36:37],v247 offset:8192\n"
"v_lshl_add_u32 v129,v130,1,v129\n"
MF"v[28:31],v[200:203],a[12:15],v[28:31]\n"
"s_lshl_b32 s52,s52,1\n"
RD"v[196:199],v243 offset:8192\n"
"v_fmac_f32 v189,v165,v185\n"
MF"v[32:35],v[208:211],a[0:3],0\n"
SA"s52,s52,s50\n"
BL"v241,s[16:19],s48 offen offset:3072 lds\n"
MF"v[32:35],v[212:215],a[16:19],v[32:35]\n"
SA"s52,s52,16384\n"
RT"a[38:39],v247 offset:8704\n"
"v_lshlrev_b32 v166,16,v147\n"
MF"v[40:43],v[212:215],a[20:23],0\n"
SA"m0,s37,20480\n"
RD"v[200:203],v242 offset:10240\n"
"v_and_b32 v167,0xffff0000,v147\n"
MF"v[40:43],v[208:211],a[4:7],v[40:43]\n"
"s_bfe_u32 s48,s46,0x30008\n"
"v_fmac_f32 v188,v166,v186\n"
"v_fmac_f32 v189,v167,v187\n"
MF"v[48:51],v[208:211],a[8:11],0\n"
"s_lshl_b32 s48,s48,19\n"
RT"a[40:41],v248 offset:8192\n"
"v_add_f32 v188,v188,v189\n"
MF"v[48:51],v[212:215],a[24:27],v[48:51]\n"
"s_bitset1_b32 s48,18\n"
"v_add_f32_dpp v188,v188,v188 quad_perm:[1,0,3,2] row_mask:0xf bank_mask:0xf\n"
"s_lshl_b32 s51,s30,11\n"
MF"v[56:59],v[212:215],a[28:31],0\n"
SA"s50,s50,s51\n"
BL"v238,s[16:19],s48 offen offset:0 lds\n"
MF"v[56:59],v[208:211],a[12:15],v[56:59]\n"
WT"lgkmcnt(3)\n"
RD"v[204:207],v243 offset:10240\n"
RT"a[42:43],v248 offset:8704\n"
MF"v[36:39],v[216:219],a[0:3],0\n"
SA"s51,s50,8192\n"
BS"v[132:135],v129,s[8:11],s50 offen sc1\n"
MF"v[36:39],v[220:223],a[16:19],v[36:39]\n"
"s_lshl_b32 s90,s31,2\n"
BL"v239,s[16:19],s48 offen offset:1024 lds\n"
MF"v[44:47],v[220:223],a[20:23],0\n"
SA"s90,s90,s30\n"
BS"a[240:243],v129,s[8:11],s51 offen sc1\n"
MF"v[44:47],v[216:219],a[4:7],v[44:47]\n"
"s_lshl_b32 s90,s90,1\n"
RD"v[208:211],v242 offset:12288\n"
RT"a[44:45],v249 offset:8192\n"
MF"v[52:55],v[216:219],a[8:11],0\n"
"s_sub_u32 s91,2047,s90\n"
BS"a[244:247],v129,s[8:11],s52 offen sc1\n"
MF"v[52:55],v[220:223],a[24:27],v[52:55]\n"
RD"v[212:215],v243 offset:12288\n"
RT"a[46:47],v249 offset:8704\n"
MF"v[60:63],v[220:223],a[28:31],0\n"
BL"v240,s[16:19],s48 offen offset:2048 lds\n"
MF"v[60:63],v[216:219],a[12:15],v[60:63]\n"
"v_add_f32_dpp v188,v188,v188 quad_perm:[2,3,0,1] row_mask:0xf bank_mask:0xf\n"
MF"v[64:67],v[192:195],a[0:3],0\n"
RD"v[216:219],v242 offset:14336\n"
"v_add_f32_dpp v188,v188,v188 row_half_mirror row_mask:0xf bank_mask:0xf\n"
MF"v[64:67],v[196:199],a[16:19],v[64:67]\n"
WT"vmcnt(14)\n"
RT"a[48:49],v246 offset:12288\n"
"v_add_f32_dpp v188,v188,v188 row_mirror row_mask:0xf bank_mask:0xf\n"
MF"v[72:75],v[196:199],a[20:23],0\n"
"s_lshl_b32 s90,s90,2\n"
BL"v241,s[16:19],s48 offen offset:3072 lds\n"
MF"v[72:75],v[192:195],a[4:7],v[72:75]\n"
SA"s90,s90,8192\n"
RT"a[50:51],v246 offset:12800\n"
"v_mov_b32 v189,v188\n"
MF"v[80:83],v[192:195],a[8:11],0\n"
SA"m0,s36,32768\n"
RD"v[220:223],v243 offset:14336\n"
"v_permlane16_swap_b32 v189,v188\n"
MF"v[80:83],v[196:199],a[24:27],v[80:83]\n"
"s_bfe_u32 s48,s46,0x30008\n"
"v_add_f32 v188,v188,v189\n"
PK"v188,v188,v188\n"
MF"v[88:91],v[196:199],a[28:31],0\n"
"s_lshl_b32 s48,s48,7\n"
RT"a[52:53],v247 offset:12288\n"
"v_lshlrev_b32 v188,16,v188\n"
MF"v[88:91],v[192:195],a[12:15],v[88:91]\n"
WT"lgkmcnt(4)\n"
BL"v234,s[12:15],s48 offen offset:0 lds\n"
MF"v[68:71],v[200:203],a[0:3],0\n"
"v_ashrrev_i32 v189,31,v188\n"
"v_or_b32 v189,0x80000000,v189\n"
MF"v[68:71],v[204:207],a[16:19],v[68:71]\n"
RT"a[54:55],v247 offset:12800\n"
"v_xor_b32 v188,v188,v189\n"
MF"v[76:79],v[204:207],a[20:23],0\n"
BL"v235,s[12:15],s48 offen offset:1024 lds\n"
MF"v[76:79],v[200:203],a[4:7],v[76:79]\n"
"v_and_b32 v188,0xffff0000,v188\n"
"v_lshrrev_b32 v189,9,v224\n"
MF"v[84:87],v[200:203],a[8:11],0\n"
RT"a[56:57],v248 offset:12288\n"
"v_sub_u32 v190,s91,v189\n"
MF"v[84:87],v[204:207],a[24:27],v[84:87]\n"
WT"vmcnt(13) lgkmcnt(3)\n"
"s_barrier\n"
MF"v[92:95],v[204:207],a[28:31],0\n"
RD"v[192:195],v242 offset:16384\n"
"v_or_b32 v188,v188,v190\n"
MF"v[92:95],v[200:203],a[12:15],v[92:95]\n"
RT"a[58:59],v248 offset:12800\n"
"v_and_b32 v190,0x1f0,v224\n"
MF"v[96:99],v[208:211],a[0:3],0\n"
BL"v236,s[12:15],s48 offen offset:2048 lds\n"
MF"v[96:99],v[212:215],a[16:19],v[96:99]\n"
"v_lshlrev_b32 v190,23,v190\n"
"v_lshl_add_u32 v190,v189,2,v190\n"
MF"v[104:107],v[212:215],a[20:23],0\n"
"buffer_store_dword v188,v190,s[8:11],s90 offen sc1\n"
MF"v[104:107],v[208:211],a[4:7],v[104:107]\n"
RD"v[196:199],v243 offset:16384\n"
RT"a[60:61],v249 offset:12288\n"
MF"v[112:115],v[208:211],a[8:11],0\n"
BL"v237,s[12:15],s48 offen offset:3072 lds\n"
MF"v[112:115],v[212:215],a[24:27],v[112:115]\n"
RT"a[62:63],v249 offset:12800\n"
MF"v[120:123],v[212:215],a[28:31],0\n"
SA"m0,s37,0\n"
RD"v[200:203],v242 offset:18432\n"
"s_bfe_u32 s48,s46,0x3000c\n"
MF"v[120:123],v[208:211],a[12:15],v[120:123]\n"
"s_lshl_b32 s48,s48,19\n"
MF"v[100:103],v[216:219],a[0:3],0\n"
BL"v238,s[16:19],s48 offen offset:0 lds\n"
MF"v[100:103],v[220:223],a[16:19],v[100:103]\n"
RD"v[204:207],v243 offset:18432\n"
MF"v[108:111],v[220:223],a[20:23],0\n"
MF"v[108:111],v[216:219],a[4:7],v[108:111]\n"
MF"v[116:119],v[216:219],a[8:11],0\n"
BL"v239,s[16:19],s48 offen offset:1024 lds\n"
MF"v[116:119],v[220:223],a[24:27],v[116:119]\n"
RD"v[208:211],v242 offset:20480\n"
MF"v[124:127],v[220:223],a[28:31],0\n"
MF"v[124:127],v[216:219],a[12:15],v[124:127]\n"
WT"lgkmcnt(2)\n"
MF"v[0:3],v[192:195],a[32:35],v[0:3]\n"
RD"v[212:215],v243 offset:20480\n"
MF"v[0:3],v[196:199],a[48:51],v[0:3]\n"
BL"v240,s[16:19],s48 offen offset:2048 lds\n"
MF"v[8:11],v[196:199],a[52:55],v[8:11]\n"
MF"v[8:11],v[192:195],a[36:39],v[8:11]\n"
MF"v[16:19],v[192:195],a[40:43],v[16:19]\n"
RD"v[216:219],v242 offset:22528\n"
MF"v[16:19],v[196:199],a[56:59],v[16:19]\n"
BL"v241,s[16:19],s48 offen offset:3072 lds\n"
MF"v[24:27],v[196:199],a[60:63],v[24:27]\n"
MF"v[24:27],v[192:195],a[44:47],v[24:27]\n"
RD"v[220:223],v243 offset:22528\n"
MF"v[4:7],v[200:203],a[32:35],v[4:7]\n"
WT"lgkmcnt(2)\n"
WT"vmcnt(16)\n"
RT"a[64:65],v246 offset:16384\n"
MF"v[4:7],v[204:207],a[48:51],v[4:7]\n"
SA"m0,s37,4096\n"
"s_bfe_u32 s48,s46,0x3000c\n"
"s_lshl_b32 s48,s48,19\n"
MF"v[12:15],v[204:207],a[52:55],v[12:15]\n"
"s_bitset1_b32 s48,18\n"
BL"v238,s[16:19],s48 offen offset:0 lds\n"
MF"v[12:15],v[200:203],a[36:39],v[12:15]\n"
RD"v[192:195],v242 offset:24576\n"
RT"a[66:67],v246 offset:16896\n"
MF"v[20:23],v[200:203],a[40:43],v[20:23]\n"
MF"v[20:23],v[204:207],a[56:59],v[20:23]\n"
MF"v[28:31],v[204:207],a[60:63],v[28:31]\n"
RT"a[68:69],v247 offset:16384\n"
MF"v[28:31],v[200:203],a[44:47],v[28:31]\n"
BL"v239,s[16:19],s48 offen offset:1024 lds\n"
MF"v[32:35],v[208:211],a[32:35],v[32:35]\n"
RD"v[196:199],v243 offset:24576\n"
MF"v[32:35],v[212:215],a[48:51],v[32:35]\n"
RT"a[70:71],v247 offset:16896\n"
MF"v[40:43],v[212:215],a[52:55],v[40:43]\n"
RD"v[200:203],v242 offset:26624\n"
MF"v[40:43],v[208:211],a[36:39],v[40:43]\n"
BL"v240,s[16:19],s48 offen offset:2048 lds\n"
MF"v[48:51],v[208:211],a[40:43],v[48:51]\n"
RT"a[72:73],v248 offset:16384\n"
MF"v[48:51],v[212:215],a[56:59],v[48:51]\n"
MF"v[56:59],v[212:215],a[60:63],v[56:59]\n"
RD"v[204:207],v243 offset:26624\n"
MF"v[56:59],v[208:211],a[44:47],v[56:59]\n"
WT"lgkmcnt(4)\n"
RT"a[74:75],v248 offset:16896\n"
MF"v[36:39],v[216:219],a[32:35],v[36:39]\n"
BL"v241,s[16:19],s48 offen offset:3072 lds\n"
MF"v[36:39],v[220:223],a[48:51],v[36:39]\n"
RD"v[208:211],v242 offset:28672\n"
MF"v[44:47],v[220:223],a[52:55],v[44:47]\n"
SA"m0,s36,0\n"
RT"a[76:77],v249 offset:16384\n"
"s_bfe_u32 s48,s46,0x3000c\n"
MF"v[44:47],v[216:219],a[36:39],v[44:47]\n"
"s_lshl_b32 s48,s48,7\n"
MF"v[52:55],v[216:219],a[40:43],v[52:55]\n"
BL"v234,s[12:15],s48 offen offset:0 lds\n"
MF"v[52:55],v[220:223],a[56:59],v[52:55]\n"
RD"v[212:215],v243 offset:28672\n"
RT"a[78:79],v249 offset:16896\n"
MF"v[60:63],v[220:223],a[60:63],v[60:63]\n"
MF"v[60:63],v[216:219],a[44:47],v[60:63]\n"
MF"v[64:67],v[192:195],a[32:35],v[64:67]\n"
RD"v[216:219],v242 offset:30720\n"
MF"v[64:67],v[196:199],a[48:51],v[64:67]\n"
WT"vmcnt(14)\n"
RT"a[80:81],v246 offset:20480\n"
MF"v[72:75],v[196:199],a[52:55],v[72:75]\n"
BL"v235,s[12:15],s48 offen offset:1024 lds\n"
MF"v[72:75],v[192:195],a[36:39],v[72:75]\n"
RT"a[82:83],v246 offset:20992\n"
MF"v[80:83],v[192:195],a[40:43],v[80:83]\n"
RD"v[220:223],v243 offset:30720\n"
MF"v[80:83],v[196:199],a[56:59],v[80:83]\n"
BL"v236,s[12:15],s48 offen offset:2048 lds\n"
MF"v[88:91],v[196:199],a[60:63],v[88:91]\n"
RT"a[84:85],v247 offset:20480\n"
MF"v[88:91],v[192:195],a[44:47],v[88:91]\n"
WT"lgkmcnt(4)\n"
MF"v[68:71],v[200:203],a[32:35],v[68:71]\n"
"s_lshl_b32 s51,s32,8\n"
"s_andn2_b32 s50,s31,7\n"
SA"s50,s50,s51\n"
MF"v[68:71],v[204:207],a[48:51],v[68:71]\n"
SA"s50,s50,256\n"
RT"a[86:87],v247 offset:20992\n"
SA"s50,s50,s45\n"
MF"v[76:79],v[204:207],a[52:55],v[76:79]\n"
BL"v237,s[12:15],s48 offen offset:3072 lds\n"
MF"v[76:79],v[200:203],a[36:39],v[76:79]\n"
MF"v[84:87],v[200:203],a[40:43],v[84:87]\n"
SA"m0,s37,8192\n"
RT"a[88:89],v248 offset:20480\n"
"s_bfe_u32 s48,s46,0x30010\n"
MF"v[84:87],v[204:207],a[56:59],v[84:87]\n"
"s_lshl_b32 s48,s48,19\n"
WT"vmcnt(12) lgkmcnt(3)\n"
"s_barrier\n"
MF"v[92:95],v[204:207],a[60:63],v[92:95]\n"
RD"v[192:195],v242 offset:32768\n"
MF"v[92:95],v[200:203],a[44:47],v[92:95]\n"
BL"v238,s[16:19],s48 offen offset:0 lds\n"
MF"v[96:99],v[208:211],a[32:35],v[96:99]\n"
"buffer_store_dwordx2 v[232:233],v230,s[8:11],s50 offen sc1\n"
MF"v[96:99],v[212:215],a[48:51],v[96:99]\n"
RT"a[90:91],v248 offset:20992\n"
MF"v[104:107],v[212:215],a[52:55],v[104:107]\n"
RD"v[196:199],v243 offset:32768\n"
RT"a[92:93],v249 offset:20480\n"
MF"v[104:107],v[208:211],a[36:39],v[104:107]\n"
BL"v239,s[16:19],s48 offen offset:1024 lds\n"
MF"v[112:115],v[208:211],a[40:43],v[112:115]\n"
MF"v[112:115],v[212:215],a[56:59],v[112:115]\n"
RT"a[94:95],v249 offset:20992\n"
MF"v[120:123],v[212:215],a[60:63],v[120:123]\n"
RD"v[200:203],v242 offset:34816\n"
MF"v[120:123],v[208:211],a[44:47],v[120:123]\n"
BL"v240,s[16:19],s48 offen offset:2048 lds\n"
MF"v[100:103],v[216:219],a[32:35],v[100:103]\n"
MF"v[100:103],v[220:223],a[48:51],v[100:103]\n"
RD"v[204:207],v243 offset:34816\n"
MF"v[108:111],v[220:223],a[52:55],v[108:111]\n"
MF"v[108:111],v[216:219],a[36:39],v[108:111]\n"
BL"v241,s[16:19],s48 offen offset:3072 lds\n"
MF"v[116:119],v[216:219],a[40:43],v[116:119]\n"
MF"v[116:119],v[220:223],a[56:59],v[116:119]\n"
RD"v[208:211],v242 offset:36864\n"
MF"v[124:127],v[220:223],a[60:63],v[124:127]\n"
SA"m0,s37,12288\n"
"s_bfe_u32 s48,s46,0x30010\n"
"s_lshl_b32 s48,s48,19\n"
MF"v[124:127],v[216:219],a[44:47],v[124:127]\n"
WT"lgkmcnt(2)\n"
"s_bitset1_b32 s48,18\n"
MF"v[0:3],v[192:195],a[64:67],v[0:3]\n"
RD"v[212:215],v243 offset:36864\n"
MF"v[0:3],v[196:199],a[80:83],v[0:3]\n"
BL"v238,s[16:19],s48 offen offset:0 lds\n"
MF"v[8:11],v[196:199],a[84:87],v[8:11]\n"
MF"v[8:11],v[192:195],a[68:71],v[8:11]\n"
MF"v[16:19],v[192:195],a[72:75],v[16:19]\n"
RD"v[216:219],v242 offset:38912\n"
MF"v[16:19],v[196:199],a[88:91],v[16:19]\n"
BL"v239,s[16:19],s48 offen offset:1024 lds\n"
MF"v[24:27],v[196:199],a[92:95],v[24:27]\n"
MF"v[24:27],v[192:195],a[76:79],v[24:27]\n"
RD"v[220:223],v243 offset:38912\n"
MF"v[4:7],v[200:203],a[64:67],v[4:7]\n"
WT"lgkmcnt(2)\n"
WT"vmcnt(15)\n"
RT"a[96:97],v246 offset:0\n"
MF"v[4:7],v[204:207],a[80:83],v[4:7]\n"
BL"v240,s[16:19],s48 offen offset:2048 lds\n"
MF"v[12:15],v[204:207],a[84:87],v[12:15]\n"
MF"v[12:15],v[200:203],a[68:71],v[12:15]\n"
RD"v[192:195],v242 offset:40960\n"
MF"v[20:23],v[200:203],a[72:75],v[20:23]\n"
RT"a[98:99],v246 offset:512\n"
MF"v[20:23],v[204:207],a[88:91],v[20:23]\n"
BL"v241,s[16:19],s48 offen offset:3072 lds\n"
MF"v[28:31],v[204:207],a[92:95],v[28:31]\n"
RT"a[100:101],v247 offset:0\n"
MF"v[28:31],v[200:203],a[76:79],v[28:31]\n"
RD"v[196:199],v243 offset:40960\n"
MF"v[32:35],v[208:211],a[64:67],v[32:35]\n"
SA"m0,s36,16384\n"
"s_bfe_u32 s48,s46,0x30010\n"
"s_lshl_b32 s48,s48,7\n"
MF"v[32:35],v[212:215],a[80:83],v[32:35]\n"
RT"a[102:103],v247 offset:512\n"
MF"v[40:43],v[212:215],a[84:87],v[40:43]\n"
RD"v[200:203],v242 offset:43008\n"
MF"v[40:43],v[208:211],a[68:71],v[40:43]\n"
BL"v234,s[12:15],s48 offen offset:0 lds\n"
MF"v[48:51],v[208:211],a[72:75],v[48:51]\n"
RT"a[104:105],v248 offset:0\n"
MF"v[48:51],v[212:215],a[88:91],v[48:51]\n"
MF"v[56:59],v[212:215],a[92:95],v[56:59]\n"
RD"v[204:207],v243 offset:43008\n"
MF"v[56:59],v[208:211],a[76:79],v[56:59]\n"
WT"lgkmcnt(4)\n"
BL"v235,s[12:15],s48 offen offset:1024 lds\n"
MF"v[36:39],v[216:219],a[64:67],v[36:39]\n"
RT"a[106:107],v248 offset:512\n"
MF"v[36:39],v[220:223],a[80:83],v[36:39]\n"
RD"v[208:211],v242 offset:45056\n"
MF"v[44:47],v[220:223],a[84:87],v[44:47]\n"
RT"a[108:109],v249 offset:0\n"
MF"v[44:47],v[216:219],a[68:71],v[44:47]\n"
BL"v236,s[12:15],s48 offen offset:2048 lds\n"
MF"v[52:55],v[216:219],a[72:75],v[52:55]\n"
MF"v[52:55],v[220:223],a[88:91],v[52:55]\n"
RD"v[212:215],v243 offset:45056\n"
MF"v[60:63],v[220:223],a[92:95],v[60:63]\n"
RT"a[110:111],v249 offset:512\n"
MF"v[60:63],v[216:219],a[76:79],v[60:63]\n"
BL"v237,s[12:15],s48 offen offset:3072 lds\n"
MF"v[64:67],v[192:195],a[64:67],v[64:67]\n"
RD"v[216:219],v242 offset:47104\n"
MF"v[64:67],v[196:199],a[80:83],v[64:67]\n"
WT"vmcnt(17)\n"
RT"a[112:113],v246 offset:4096\n"
MF"v[72:75],v[196:199],a[84:87],v[72:75]\n"
SA"m0,s37,16384\n"
"s_bfe_u32 s48,s46,0x30014\n"
"s_lshl_b32 s48,s48,19\n"
MF"v[72:75],v[192:195],a[68:71],v[72:75]\n"
RT"a[114:115],v246 offset:4608\n"
MF"v[80:83],v[192:195],a[72:75],v[80:83]\n"
RD"v[220:223],v243 offset:47104\n"
MF"v[80:83],v[196:199],a[88:91],v[80:83]\n"
BL"v238,s[16:19],s48 offen offset:0 lds\n"
MF"v[88:91],v[196:199],a[92:95],v[88:91]\n"
RT"a[116:117],v247 offset:4096\n"
MF"v[88:91],v[192:195],a[76:79],v[88:91]\n"
WT"lgkmcnt(4)\n"
MF"v[68:71],v[200:203],a[64:67],v[68:71]\n"
BL"v239,s[16:19],s48 offen offset:1024 lds\n"
MF"v[68:71],v[204:207],a[80:83],v[68:71]\n"
RT"a[118:119],v247 offset:4608\n"
MF"v[76:79],v[204:207],a[84:87],v[76:79]\n"
MF"v[76:79],v[200:203],a[68:71],v[76:79]\n"
MF"v[84:87],v[200:203],a[72:75],v[84:87]\n"
RT"a[120:121],v248 offset:4096\n"
MF"v[84:87],v[204:207],a[88:91],v[84:87]\n"
WT"vmcnt(15) lgkmcnt(3)\n"
"s_barrier\n"
MF"v[92:95],v[204:207],a[92:95],v[92:95]\n"
BL"v240,s[16:19],s48 offen offset:2048 lds\n"
MF"v[92:95],v[200:203],a[76:79],v[92:95]\n"
RD"v[192:195],v242 offset:0\n"
RT"a[122:123],v248 offset:4608\n"
MF"v[96:99],v[208:211],a[64:67],v[96:99]\n"
MF"v[96:99],v[212:215],a[80:83],v[96:99]\n"
BL"v241,s[16:19],s48 offen offset:3072 lds\n"
MF"v[104:107],v[212:215],a[84:87],v[104:107]\n"
RD"v[196:199],v243 offset:0\n"
RT"a[124:125],v249 offset:4096\n"
MF"v[104:107],v[208:211],a[68:71],v[104:107]\n"
MF"v[112:115],v[208:211],a[72:75],v[112:115]\n"
SA"m0,s37,20480\n"
"s_bfe_u32 s48,s46,0x30014\n"
"s_lshl_b32 s48,s48,19\n"
MF"v[112:115],v[212:215],a[88:91],v[112:115]\n"
"s_bitset1_b32 s48,18\n"
RT"a[126:127],v249 offset:4608\n"
MF"v[120:123],v[212:215],a[92:95],v[120:123]\n"
RD"v[200:203],v242 offset:2048\n"
MF"v[120:123],v[208:211],a[76:79],v[120:123]\n"
BL"v238,s[16:19],s48 offen offset:0 lds\n"
MF"v[100:103],v[216:219],a[64:67],v[100:103]\n"
MF"v[100:103],v[220:223],a[80:83],v[100:103]\n"
RD"v[204:207],v243 offset:2048\n"
MF"v[108:111],v[220:223],a[84:87],v[108:111]\n"
BL"v239,s[16:19],s48 offen offset:1024 lds\n"
MF"v[108:111],v[216:219],a[68:71],v[108:111]\n"
MF"v[116:119],v[216:219],a[72:75],v[116:119]\n"
MF"v[116:119],v[220:223],a[88:91],v[116:119]\n"
RD"v[208:211],v242 offset:4096\n"
MF"v[124:127],v[220:223],a[92:95],v[124:127]\n"
MF"v[124:127],v[216:219],a[76:79],v[124:127]\n"
WT"lgkmcnt(2)\n"
BL"v240,s[16:19],s48 offen offset:2048 lds\n"
MF"v[0:3],v[192:195],a[96:99],v[0:3]\n"
RD"v[212:215],v243 offset:4096\n"
MF"v[0:3],v[196:199],a[112:115],v[0:3]\n"
MF"v[8:11],v[196:199],a[116:119],v[8:11]\n"
MF"v[8:11],v[192:195],a[100:103],v[8:11]\n"
BL"v241,s[16:19],s48 offen offset:3072 lds\n"
MF"v[16:19],v[192:195],a[104:107],v[16:19]\n"
RD"v[216:219],v242 offset:6144\n"
MF"v[16:19],v[196:199],a[120:123],v[16:19]\n"
MF"v[24:27],v[196:199],a[124:127],v[24:27]\n"
SA"m0,s36,32768\n"
"s_bfe_u32 s48,s46,0x30014\n"
"s_lshl_b32 s48,s48,7\n"
MF"v[24:27],v[192:195],a[108:111],v[24:27]\n"
RD"v[220:223],v243 offset:6144\n"
MF"v[4:7],v[200:203],a[96:99],v[4:7]\n"
WT"lgkmcnt(2)\n"
WT"vmcnt(16)\n"
RT"a[128:129],v246 offset:8192\n"
MF"v[4:7],v[204:207],a[112:115],v[4:7]\n"
BL"v234,s[12:15],s48 offen offset:0 lds\n"
MF"v[12:15],v[204:207],a[116:119],v[12:15]\n"
MF"v[12:15],v[200:203],a[100:103],v[12:15]\n"
RD"v[192:195],v242 offset:8192\n"
MF"v[20:23],v[200:203],a[104:107],v[20:23]\n"
RT"a[130:131],v246 offset:8704\n"
MF"v[20:23],v[204:207],a[120:123],v[20:23]\n"
BL"v235,s[12:15],s48 offen offset:1024 lds\n"
MF"v[28:31],v[204:207],a[124:127],v[28:31]\n"
RT"a[132:133],v247 offset:8192\n"
MF"v[28:31],v[200:203],a[108:111],v[28:31]\n"
RD"v[196:199],v243 offset:8192\n"
MF"v[32:35],v[208:211],a[96:99],v[32:35]\n"
MF"v[32:35],v[212:215],a[112:115],v[32:35]\n"
RT"a[134:135],v247 offset:8704\n"
MF"v[40:43],v[212:215],a[116:119],v[40:43]\n"
BL"v236,s[12:15],s48 offen offset:2048 lds\n"
MF"v[40:43],v[208:211],a[100:103],v[40:43]\n"
RD"v[200:203],v242 offset:10240\n"
MF"v[48:51],v[208:211],a[104:107],v[48:51]\n"
RT"a[136:137],v248 offset:8192\n"
MF"v[48:51],v[212:215],a[120:123],v[48:51]\n"
BL"v237,s[12:15],s48 offen offset:3072 lds\n"
MF"v[56:59],v[212:215],a[124:127],v[56:59]\n"
RD"v[204:207],v243 offset:10240\n"
MF"v[56:59],v[208:211],a[108:111],v[56:59]\n"
WT"lgkmcnt(4)\n"
RT"a[138:139],v248 offset:8704\n"
MF"v[36:39],v[216:219],a[96:99],v[36:39]\n"
SA"m0,s37,0\n"
"s_bfe_u32 s48,s46,0x30018\n"
"s_lshl_b32 s48,s48,19\n"
MF"v[36:39],v[220:223],a[112:115],v[36:39]\n"
RD"v[208:211],v242 offset:12288\n"
MF"v[44:47],v[220:223],a[116:119],v[44:47]\n"
RT"a[140:141],v249 offset:8192\n"
MF"v[44:47],v[216:219],a[100:103],v[44:47]\n"
BL"v238,s[16:19],s48 offen offset:0 lds\n"
MF"v[52:55],v[216:219],a[104:107],v[52:55]\n"
MF"v[52:55],v[220:223],a[120:123],v[52:55]\n"
RD"v[212:215],v243 offset:12288\n"
MF"v[60:63],v[220:223],a[124:127],v[60:63]\n"
RT"a[142:143],v249 offset:8704\n"
MF"v[60:63],v[216:219],a[108:111],v[60:63]\n"
BL"v239,s[16:19],s48 offen offset:1024 lds\n"
MF"v[64:67],v[192:195],a[96:99],v[64:67]\n"
RD"v[216:219],v242 offset:14336\n"
MF"v[64:67],v[196:199],a[112:115],v[64:67]\n"
WT"vmcnt(18)\n"
RT"a[144:145],v246 offset:12288\n"
MF"v[72:75],v[196:199],a[116:119],v[72:75]\n"
MF"v[72:75],v[192:195],a[100:103],v[72:75]\n"
RT"a[146:147],v246 offset:12800\n"
MF"v[80:83],v[192:195],a[104:107],v[80:83]\n"
BL"v240,s[16:19],s48 offen offset:2048 lds\n"
MF"v[80:83],v[196:199],a[120:123],v[80:83]\n"
RD"v[220:223],v243 offset:14336\n"
MF"v[88:91],v[196:199],a[124:127],v[88:91]\n"
RT"a[148:149],v247 offset:12288\n"
MF"v[88:91],v[192:195],a[108:111],v[88:91]\n"
WT"lgkmcnt(4)\n"
BL"v241,s[16:19],s48 offen offset:3072 lds\n"
MF"v[68:71],v[200:203],a[96:99],v[68:71]\n"
MF"v[68:71],v[204:207],a[112:115],v[68:71]\n"
RT"a[150:151],v247 offset:12800\n"
MF"v[76:79],v[204:207],a[116:119],v[76:79]\n"
SA"m0,s37,4096\n"
"s_bfe_u32 s48,s46,0x30018\n"
"s_lshl_b32 s48,s48,19\n"
MF"v[76:79],v[200:203],a[100:103],v[76:79]\n"
"s_bitset1_b32 s48,18\n"
MF"v[84:87],v[200:203],a[104:107],v[84:87]\n"
RT"a[152:153],v248 offset:12288\n"
MF"v[84:87],v[204:207],a[120:123],v[84:87]\n"
BL"v238,s[16:19],s48 offen offset:0 lds\n"
MF"v[92:95],v[204:207],a[124:127],v[92:95]\n"
WT"vmcnt(17) lgkmcnt(3)\n"
"s_barrier\n"
MF"v[92:95],v[200:203],a[108:111],v[92:95]\n"
RD"v[192:195],v242 offset:16384\n"
RT"a[154:155],v248 offset:12800\n"
MF"v[96:99],v[208:211],a[96:99],v[96:99]\n"
BL"v239,s[16:19],s48 offen offset:1024 lds\n"
MF"v[96:99],v[212:215],a[112:115],v[96:99]\n"
MF"v[104:107],v[212:215],a[116:119],v[104:107]\n"
RD"v[196:199],v243 offset:16384\n"
MF"v[104:107],v[208:211],a[100:103],v[104:107]\n"
RT"a[156:157],v249 offset:12288\n"
MF"v[112:115],v[208:211],a[104:107],v[112:115]\n"
MF"v[112:115],v[212:215],a[120:123],v[112:115]\n"
RT"a[158:159],v249 offset:12800\n"
MF"v[120:123],v[212:215],a[124:127],v[120:123]\n"
BL"v240,s[16:19],s48 offen offset:2048 lds\n"
MF"v[120:123],v[208:211],a[108:111],v[120:123]\n"
RD"v[200:203],v242 offset:18432\n"
MF"v[100:103],v[216:219],a[96:99],v[100:103]\n"
MF"v[100:103],v[220:223],a[112:115],v[100:103]\n"
RD"v[204:207],v243 offset:18432\n"
MF"v[108:111],v[220:223],a[116:119],v[108:111]\n"
BL"v241,s[16:19],s48 offen offset:3072 lds\n"
MF"v[108:111],v[216:219],a[100:103],v[108:111]\n"
MF"v[116:119],v[216:219],a[104:107],v[116:119]\n"
SA"m0,s36,0\n"
"s_bfe_u32 s48,s46,0x30018\n"
"s_lshl_b32 s48,s48,7\n"
MF"v[116:119],v[220:223],a[120:123],v[116:119]\n"
RD"v[208:211],v242 offset:20480\n"
MF"v[124:127],v[220:223],a[124:127],v[124:127]\n"
BL"v234,s[12:15],s48 offen offset:0 lds\n"
MF"v[124:127],v[216:219],a[108:111],v[124:127]\n"
WT"lgkmcnt(2)\n"
MF"v[0:3],v[192:195],a[128:131],v[0:3]\n"
RD"v[212:215],v243 offset:20480\n"
MF"v[0:3],v[196:199],a[144:147],v[0:3]\n"
MF"v[8:11],v[196:199],a[148:151],v[8:11]\n"
BL"v235,s[12:15],s48 offen offset:1024 lds\n"
MF"v[8:11],v[192:195],a[132:135],v[8:11]\n"
MF"v[16:19],v[192:195],a[136:139],v[16:19]\n"
RD"v[216:219],v242 offset:22528\n"
MF"v[16:19],v[196:199],a[152:155],v[16:19]\n"
MF"v[24:27],v[196:199],a[156:159],v[24:27]\n"
MF"v[24:27],v[192:195],a[140:143],v[24:27]\n"
RD"v[220:223],v243 offset:22528\n"
MF"v[4:7],v[200:203],a[128:131],v[4:7]\n"
WT"lgkmcnt(2)\n"
BL"v236,s[12:15],s48 offen offset:2048 lds\n"
MF"v[4:7],v[204:207],a[144:147],v[4:7]\n"
WT"vmcnt(19)\n"
RT"a[160:161],v246 offset:16384\n"
MF"v[12:15],v[204:207],a[148:151],v[12:15]\n"
MF"v[12:15],v[200:203],a[132:135],v[12:15]\n"
RD"v[192:195],v242 offset:24576\n"
MF"v[20:23],v[200:203],a[136:139],v[20:23]\n"
RT"a[162:163],v246 offset:16896\n"
MF"v[20:23],v[204:207],a[152:155],v[20:23]\n"
BL"v237,s[12:15],s48 offen offset:3072 lds\n"
MF"v[28:31],v[204:207],a[156:159],v[28:31]\n"
SA"m0,s37,8192\n"
RT"a[164:165],v247 offset:16384\n"
"s_bfe_u32 s48,s46,0x3001c\n"
MF"v[28:31],v[200:203],a[140:143],v[28:31]\n"
"s_lshl_b32 s48,s48,19\n"
RD"v[196:199],v243 offset:24576\n"
MF"v[32:35],v[208:211],a[128:131],v[32:35]\n"
BL"v238,s[16:19],s48 offen offset:0 lds\n"
MF"v[32:35],v[212:215],a[144:147],v[32:35]\n"
RT"a[166:167],v247 offset:16896\n"
MF"v[40:43],v[212:215],a[148:151],v[40:43]\n"
RD"v[200:203],v242 offset:26624\n"
MF"v[40:43],v[208:211],a[132:135],v[40:43]\n"
MF"v[48:51],v[208:211],a[136:139],v[48:51]\n"
RT"a[168:169],v248 offset:16384\n"
MF"v[48:51],v[212:215],a[152:155],v[48:51]\n"
BL"v239,s[16:19],s48 offen offset:1024 lds\n"
MF"v[56:59],v[212:215],a[156:159],v[56:59]\n"
RD"v[204:207],v243 offset:26624\n"
MF"v[56:59],v[208:211],a[140:143],v[56:59]\n"
WT"lgkmcnt(4)\n"
RT"a[170:171],v248 offset:16896\n"
MF"v[36:39],v[216:219],a[128:131],v[36:39]\n"
MF"v[36:39],v[220:223],a[144:147],v[36:39]\n"
RD"v[208:211],v242 offset:28672\n"
MF"v[44:47],v[220:223],a[148:151],v[44:47]\n"
BL"v240,s[16:19],s48 offen offset:2048 lds\n"
MF"v[44:47],v[216:219],a[132:135],v[44:47]\n"
RT"a[172:173],v249 offset:16384\n"
MF"v[52:55],v[216:219],a[136:139],v[52:55]\n"
MF"v[52:55],v[220:223],a[152:155],v[52:55]\n"
RD"v[212:215],v243 offset:28672\n"
MF"v[60:63],v[220:223],a[156:159],v[60:63]\n"
RT"a[174:175],v249 offset:16896\n"
MF"v[60:63],v[216:219],a[140:143],v[60:63]\n"
BL"v241,s[16:19],s48 offen offset:3072 lds\n"
MF"v[64:67],v[192:195],a[128:131],v[64:67]\n"
SA"m0,s37,12288\n"
RD"v[216:219],v242 offset:30720\n"
"s_bfe_u32 s48,s46,0x3001c\n"
MF"v[64:67],v[196:199],a[144:147],v[64:67]\n"
"s_lshl_b32 s48,s48,19\n"
WT"vmcnt(20)\n"
RT"a[176:177],v246 offset:20480\n"
MF"v[72:75],v[196:199],a[148:151],v[72:75]\n"
"s_bitset1_b32 s48,18\n"
BL"v238,s[16:19],s48 offen offset:0 lds\n"
MF"v[72:75],v[192:195],a[132:135],v[72:75]\n"
RT"a[178:179],v246 offset:20992\n"
MF"v[80:83],v[192:195],a[136:139],v[80:83]\n"
RD"v[220:223],v243 offset:30720\n"
MF"v[80:83],v[196:199],a[152:155],v[80:83]\n"
MF"v[88:91],v[196:199],a[156:159],v[88:91]\n"
RT"a[180:181],v247 offset:20480\n"
MF"v[88:91],v[192:195],a[140:143],v[88:91]\n"
WT"lgkmcnt(4)\n"
BL"v239,s[16:19],s48 offen offset:1024 lds\n"
MF"v[68:71],v[200:203],a[128:131],v[68:71]\n"
MF"v[68:71],v[204:207],a[144:147],v[68:71]\n"
RT"a[182:183],v247 offset:20992\n"
MF"v[76:79],v[204:207],a[148:151],v[76:79]\n"
MF"v[76:79],v[200:203],a[132:135],v[76:79]\n"
BL"v240,s[16:19],s48 offen offset:2048 lds\n"
MF"v[84:87],v[200:203],a[136:139],v[84:87]\n"
RT"a[184:185],v248 offset:20480\n"
MF"v[84:87],v[204:207],a[152:155],v[84:87]\n"
WT"vmcnt(19) lgkmcnt(3)\n"
"s_barrier\n"
MF"v[92:95],v[204:207],a[156:159],v[92:95]\n"
RD"v[192:195],v242 offset:32768\n"
MF"v[92:95],v[200:203],a[140:143],v[92:95]\n"
RT"a[186:187],v248 offset:20992\n"
MF"v[96:99],v[208:211],a[128:131],v[96:99]\n"
BL"v241,s[16:19],s48 offen offset:3072 lds\n"
MF"v[96:99],v[212:215],a[144:147],v[96:99]\n"
MF"v[104:107],v[212:215],a[148:151],v[104:107]\n"
SA"m0,s36,16384\n"
RD"v[196:199],v243 offset:32768\n"
"s_bfe_u32 s48,s46,0x3001c\n"
MF"v[104:107],v[208:211],a[132:135],v[104:107]\n"
"s_lshl_b32 s48,s48,7\n"
RT"a[188:189],v249 offset:20480\n"
MF"v[112:115],v[208:211],a[136:139],v[112:115]\n"
BL"v234,s[12:15],s48 offen offset:0 lds\n"
MF"v[112:115],v[212:215],a[152:155],v[112:115]\n"
RT"a[190:191],v249 offset:20992\n"
MF"v[120:123],v[212:215],a[156:159],v[120:123]\n"
RD"v[200:203],v242 offset:34816\n"
MF"v[120:123],v[208:211],a[140:143],v[120:123]\n"
MF"v[100:103],v[216:219],a[128:131],v[100:103]\n"
BL"v235,s[12:15],s48 offen offset:1024 lds\n"
MF"v[100:103],v[220:223],a[144:147],v[100:103]\n"
RD"v[204:207],v243 offset:34816\n"
MF"v[108:111],v[220:223],a[148:151],v[108:111]\n"
MF"v[108:111],v[216:219],a[132:135],v[108:111]\n"
MF"v[116:119],v[216:219],a[136:139],v[116:119]\n"
MF"v[116:119],v[220:223],a[152:155],v[116:119]\n"
RD"v[208:211],v242 offset:36864\n"
MF"v[124:127],v[220:223],a[156:159],v[124:127]\n"
BL"v236,s[12:15],s48 offen offset:2048 lds\n"
MF"v[124:127],v[216:219],a[140:143],v[124:127]\n"
WT"lgkmcnt(2)\n"
MF"v[0:3],v[192:195],a[160:163],v[0:3]\n"
RD"v[212:215],v243 offset:36864\n"
MF"v[0:3],v[196:199],a[176:179],v[0:3]\n"
BL"v237,s[12:15],s48 offen offset:3072 lds\n"
MF"v[8:11],v[196:199],a[180:183],v[8:11]\n"
MF"v[8:11],v[192:195],a[164:167],v[8:11]\n"
MF"v[16:19],v[192:195],a[168:171],v[16:19]\n"
SA"m0,s37,16384\n"
RD"v[216:219],v242 offset:38912\n"
SA"s48,s39,0\n"
MF"v[16:19],v[196:199],a[184:187],v[16:19]\n"
MF"v[24:27],v[196:199],a[188:191],v[24:27]\n"
BL"v234,s[12:15],s48 offen offset:0 lds\n"
MF"v[24:27],v[192:195],a[172:175],v[24:27]\n"
RD"v[220:223],v243 offset:38912\n"
MF"v[4:7],v[200:203],a[160:163],v[4:7]\n"
WT"lgkmcnt(2)\n"
WT"vmcnt(21)\n"
RT"a[192:193],v246 offset:0\n"
MF"v[4:7],v[204:207],a[176:179],v[4:7]\n"
MF"v[12:15],v[204:207],a[180:183],v[12:15]\n"
BL"v235,s[12:15],s48 offen offset:1024 lds\n"
MF"v[12:15],v[200:203],a[164:167],v[12:15]\n"
RD"v[192:195],v242 offset:40960\n"
RT"a[194:195],v246 offset:512\n"
MF"v[20:23],v[200:203],a[168:171],v[20:23]\n"
MF"v[20:23],v[204:207],a[184:187],v[20:23]\n"
MF"v[28:31],v[204:207],a[188:191],v[28:31]\n"
RT"a[196:197],v247 offset:0\n"
MF"v[28:31],v[200:203],a[172:175],v[28:31]\n"
RD"v[196:199],v243 offset:40960\n"
MF"v[32:35],v[208:211],a[160:163],v[32:35]\n"
BL"v236,s[12:15],s48 offen offset:2048 lds\n"
MF"v[32:35],v[212:215],a[176:179],v[32:35]\n"
RT"a[198:199],v247 offset:512\n"
MF"v[40:43],v[212:215],a[180:183],v[40:43]\n"
RD"v[200:203],v242 offset:43008\n"
MF"v[40:43],v[208:211],a[164:167],v[40:43]\n"
BL"v237,s[12:15],s48 offen offset:3072 lds\n"
MF"v[48:51],v[208:211],a[168:171],v[48:51]\n"
RT"a[200:201],v248 offset:0\n"
MF"v[48:51],v[212:215],a[184:187],v[48:51]\n"
MF"v[56:59],v[212:215],a[188:191],v[56:59]\n"
SA"m0,s37,20480\n"
RD"v[204:207],v243 offset:43008\n"
SA"s48,s98,0\n"
MF"v[56:59],v[208:211],a[172:175],v[56:59]\n"
WT"lgkmcnt(4)\n"
RT"a[202:203],v248 offset:512\n"
"s_lshr_b32 s50,s30,1\n"
MF"v[36:39],v[216:219],a[160:163],v[36:39]\n"
"s_and_b32 s51,s30,1\n"
BL"v234,s[12:15],s48 offen offset:0 lds\n"
MF"v[36:39],v[220:223],a[176:179],v[36:39]\n"
"s_mul_i32 s38,s50,24576\n"
RD"v[208:211],v242 offset:45056\n"
MF"v[44:47],v[220:223],a[180:183],v[44:47]\n"
"s_lshl_b32 s60,s50,18\n"
RT"a[204:205],v249 offset:0\n"
MF"v[44:47],v[216:219],a[164:167],v[44:47]\n"
"s_lshl_b32 s50,s51,12\n"
MF"v[52:55],v[216:219],a[168:171],v[52:55]\n"
SA"s38,s38,s50\n"
BL"v235,s[12:15],s48 offen offset:1024 lds\n"
MF"v[52:55],v[220:223],a[184:187],v[52:55]\n"
"s_lshl_b32 s50,s51,7\n"
RD"v[212:215],v243 offset:45056\n"
RT"a[206:207],v249 offset:512\n"
MF"v[60:63],v[220:223],a[188:191],v[60:63]\n"
SA"s60,s60,s50\n"
MF"v[60:63],v[216:219],a[172:175],v[60:63]\n"
MF"v[64:67],v[192:195],a[160:163],v[64:67]\n"
RD"v[216:219],v242 offset:47104\n"
MF"v[64:67],v[196:199],a[176:179],v[64:67]\n"
"s_sub_u32 s99,s34,5\n"
WT"vmcnt(22)\n"
RT"a[208:209],v246 offset:4096\n"
MF"v[72:75],v[196:199],a[180:183],v[72:75]\n"
"s_cmp_lt_u32 s34,5\n"
BL"v236,s[12:15],s48 offen offset:2048 lds\n"
MF"v[72:75],v[192:195],a[164:167],v[72:75]\n"
"s_cselect_b32 s99,s34,s99\n"
RT"a[210:211],v246 offset:4608\n"
"s_mul_i32 s99,s99,1\n"
MF"v[80:83],v[192:195],a[168:171],v[80:83]\n"
"s_lshl_b32 s61,s99,20\n"
RD"v[220:223],v243 offset:47104\n"
MF"v[80:83],v[196:199],a[184:187],v[80:83]\n"
BL"v237,s[12:15],s48 offen offset:3072 lds\n"
MF"v[88:91],v[196:199],a[188:191],v[88:91]\n"
RT"a[212:213],v247 offset:4096\n"
MF"v[88:91],v[192:195],a[172:175],v[88:91]\n"
WT"lgkmcnt(4)\n"
MF"v[68:71],v[200:203],a[160:163],v[68:71]\n"
SA"m0,s37,0\n"
SA"s48,s39,32768\n"
MF"v[68:71],v[204:207],a[176:179],v[68:71]\n"
RT"a[214:215],v247 offset:4608\n"
MF"v[76:79],v[204:207],a[180:183],v[76:79]\n"
BL"v234,s[12:15],s48 offen offset:0 lds\n"
MF"v[76:79],v[200:203],a[164:167],v[76:79]\n"
MF"v[84:87],v[200:203],a[168:171],v[84:87]\n"
RT"a[216:217],v248 offset:4096\n"
MF"v[84:87],v[204:207],a[184:187],v[84:87]\n"
WT"vmcnt(21) lgkmcnt(3)\n"
"s_barrier\n"
MF"v[92:95],v[204:207],a[188:191],v[92:95]\n"
RD"v[192:195],v242 offset:0\n"
MF"v[92:95],v[200:203],a[172:175],v[92:95]\n"
BL"v235,s[12:15],s48 offen offset:1024 lds\n"
MF"v[96:99],v[208:211],a[160:163],v[96:99]\n"
RT"a[218:219],v248 offset:4608\n"
MF"v[96:99],v[212:215],a[176:179],v[96:99]\n"
MF"v[104:107],v[212:215],a[180:183],v[104:107]\n"
RD"v[196:199],v243 offset:0\n"
MF"v[104:107],v[208:211],a[164:167],v[104:107]\n"
RT"a[220:221],v249 offset:4096\n"
MF"v[112:115],v[208:211],a[168:171],v[112:115]\n"
BL"v236,s[12:15],s48 offen offset:2048 lds\n"
MF"v[112:115],v[212:215],a[184:187],v[112:115]\n"
RT"a[222:223],v249 offset:4608\n"
MF"v[120:123],v[212:215],a[188:191],v[120:123]\n"
RD"v[200:203],v242 offset:2048\n"
MF"v[120:123],v[208:211],a[172:175],v[120:123]\n"
BL"v237,s[12:15],s48 offen offset:3072 lds\n"
MF"v[100:103],v[216:219],a[160:163],v[100:103]\n"
MF"v[100:103],v[220:223],a[176:179],v[100:103]\n"
RD"v[204:207],v243 offset:2048\n"
MF"v[108:111],v[220:223],a[180:183],v[108:111]\n"
SA"m0,s37,4096\n"
SA"s48,s98,32768\n"
MF"v[108:111],v[216:219],a[164:167],v[108:111]\n"
MF"v[116:119],v[216:219],a[168:171],v[116:119]\n"
WT"lgkmcnt(2)\n"
BL"v234,s[12:15],s48 offen offset:0 lds\n"
MF"v[116:119],v[220:223],a[184:187],v[116:119]\n"
RD"v[208:211],v242 offset:4096\n"
MF"v[124:127],v[220:223],a[188:191],v[124:127]\n"
MF"v[124:127],v[216:219],a[172:175],v[124:127]\n"
MF"v[0:3],v[192:195],a[192:195],v[0:3]\n"
RD"v[212:215],v243 offset:4096\n"
MF"v[0:3],v[196:199],a[208:211],v[0:3]\n"
BL"v235,s[12:15],s48 offen offset:1024 lds\n"
MF"v[8:11],v[196:199],a[212:215],v[8:11]\n"
MF"v[8:11],v[192:195],a[196:199],v[8:11]\n"
MF"v[16:19],v[192:195],a[200:203],v[16:19]\n"
RD"v[216:219],v242 offset:6144\n"
MF"v[16:19],v[196:199],a[216:219],v[16:19]\n"
BL"v236,s[12:15],s48 offen offset:2048 lds\n"
MF"v[24:27],v[196:199],a[220:223],v[24:27]\n"
BL"v[160:163],v224,s[8:11],0 offen offset:256 sc1\n"
MF"v[24:27],v[192:195],a[204:207],v[24:27]\n"
WT"lgkmcnt(1)\n"
RD"v[220:223],v243 offset:6144\n"
MF"v[4:7],v[200:203],a[192:195],v[4:7]\n"
WT"vmcnt(24)\n"
RT"a[224:225],v246 offset:8192\n"
MF"v[4:7],v[204:207],a[208:211],v[4:7]\n"
BL"v[164:167],v224,s[8:11],0 offen offset:1280 sc1\n"
MF"v[12:15],v[204:207],a[212:215],v[12:15]\n"
BL"v237,s[12:15],s48 offen offset:3072 lds\n"
MF"v[12:15],v[200:203],a[196:199],v[12:15]\n"
RD"v[192:195],v242 offset:8192\n"
RT"a[226:227],v246 offset:8704\n"
MF"v[20:23],v[200:203],a[200:203],v[20:23]\n"
MF"v[20:23],v[204:207],a[216:219],v[20:23]\n"
MF"v[28:31],v[204:207],a[220:223],v[28:31]\n"
RT"a[228:229],v247 offset:8192\n"
MF"v[28:31],v[200:203],a[204:207],v[28:31]\n"
RD"v[196:199],v243 offset:8192\n"
MF"v[32:35],v[208:211],a[192:195],v[32:35]\n"
MF"v[32:35],v[212:215],a[208:211],v[32:35]\n"
RT"a[230:231],v247 offset:8704\n"
MF"v[40:43],v[212:215],a[212:215],v[40:43]\n"
RD"v[200:203],v242 offset:10240\n"
MF"v[40:43],v[208:211],a[196:199],v[40:43]\n"
MF"v[48:51],v[208:211],a[200:203],v[48:51]\n"
RT"a[232:233],v248 offset:8192\n"
MF"v[48:51],v[212:215],a[216:219],v[48:51]\n"
MF"v[56:59],v[212:215],a[220:223],v[56:59]\n"
RD"v[204:207],v243 offset:10240\n"
MF"v[56:59],v[208:211],a[204:207],v[56:59]\n"
WT"lgkmcnt(4)\n"
RT"a[234:235],v248 offset:8704\n"
MF"v[36:39],v[216:219],a[192:195],v[36:39]\n"
MF"v[36:39],v[220:223],a[208:211],v[36:39]\n"
RD"v[208:211],v242 offset:12288\n"
MF"v[44:47],v[220:223],a[212:215],v[44:47]\n"
RT"a[236:237],v249 offset:8192\n"
MF"v[44:47],v[216:219],a[196:199],v[44:47]\n"
MF"v[52:55],v[216:219],a[200:203],v[52:55]\n"
MF"v[52:55],v[220:223],a[216:219],v[52:55]\n"
RD"v[212:215],v243 offset:12288\n"
MF"v[60:63],v[220:223],a[220:223],v[60:63]\n"
RT"a[238:239],v249 offset:8704\n"
MF"v[60:63],v[216:219],a[204:207],v[60:63]\n"
MF"v[64:67],v[192:195],a[192:195],v[64:67]\n"
RD"v[216:219],v242 offset:14336\n"
MF"v[64:67],v[196:199],a[208:211],v[64:67]\n"
SA"m0,s37,8192\n"
WT"vmcnt(22)\n"
RT"a[240:241],v246 offset:12288\n"
MF"v[72:75],v[196:199],a[212:215],v[72:75]\n"
SA"s48,s39,65536\n"
MF"v[72:75],v[192:195],a[196:199],v[72:75]\n"
RT"a[242:243],v246 offset:12800\n"
MF"v[80:83],v[192:195],a[200:203],v[80:83]\n"
WT"lgkmcnt(3)\n"
BL"v234,s[12:15],s48 offen offset:0 lds\n"
MF"v[80:83],v[196:199],a[216:219],v[80:83]\n"
RD"v[220:223],v243 offset:14336\n"
MF"v[88:91],v[196:199],a[220:223],v[88:91]\n"
RT"a[244:245],v247 offset:12288\n"
MF"v[88:91],v[192:195],a[204:207],v[88:91]\n"
BL"v235,s[12:15],s48 offen offset:1024 lds\n"
MF"v[68:71],v[200:203],a[192:195],v[68:71]\n"
MF"v[68:71],v[204:207],a[208:211],v[68:71]\n"
RT"a[246:247],v247 offset:12800\n"
MF"v[76:79],v[204:207],a[212:215],v[76:79]\n"
MF"v[76:79],v[200:203],a[196:199],v[76:79]\n"
MF"v[84:87],v[200:203],a[200:203],v[84:87]\n"
RT"a[248:249],v248 offset:12288\n"
MF"v[84:87],v[204:207],a[216:219],v[84:87]\n"
BL"v236,s[12:15],s48 offen offset:2048 lds\n"
MF"v[92:95],v[204:207],a[220:223],v[92:95]\n"
WT"vmcnt(21) lgkmcnt(3)\n"
"s_barrier\n"
MF"v[92:95],v[200:203],a[204:207],v[92:95]\n"
RD"v[192:195],v242 offset:16384\n"
RT"a[250:251],v248 offset:12800\n"
MF"v[96:99],v[208:211],a[192:195],v[96:99]\n"
BL"v237,s[12:15],s48 offen offset:3072 lds\n"
MF"v[96:99],v[212:215],a[208:211],v[96:99]\n"
MF"v[104:107],v[212:215],a[212:215],v[104:107]\n"
RD"v[196:199],v243 offset:16384\n"
MF"v[104:107],v[208:211],a[196:199],v[104:107]\n"
RT"a[252:253],v249 offset:12288\n"
MF"v[112:115],v[208:211],a[200:203],v[112:115]\n"
MF"v[112:115],v[212:215],a[216:219],v[112:115]\n"
RT"a[254:255],v249 offset:12800\n"
MF"v[120:123],v[212:215],a[220:223],v[120:123]\n"
RD"v[200:203],v242 offset:18432\n"
MF"v[120:123],v[208:211],a[204:207],v[120:123]\n"
MF"v[100:103],v[216:219],a[192:195],v[100:103]\n"
"s_lshl_b32 s50,s35,20\n"
MF"v[100:103],v[220:223],a[208:211],v[100:103]\n"
"s_lshl_b32 s51,s34,15\n"
RD"v[204:207],v243 offset:18432\n"
SA"m0,s37,12288\n"
MF"v[108:111],v[220:223],a[212:215],v[108:111]\n"
SA"s48,s98,65536\n"
SA"s50,s50,s51\n"
MF"v[108:111],v[216:219],a[196:199],v[108:111]\n"
WT"lgkmcnt(2)\n"
BL"v234,s[12:15],s48 offen offset:0 lds\n"
MF"v[116:119],v[216:219],a[200:203],v[116:119]\n"
"s_and_b32 s51,s30,1\n"
"s_lshl_b32 s51,s51,12\n"
MF"v[116:119],v[220:223],a[216:219],v[116:119]\n"
SA"s50,s50,s51\n"
RD"v[208:211],v242 offset:20480\n"
MF"v[124:127],v[220:223],a[220:223],v[124:127]\n"
SA"s50,s50,9699328\n"
WT"vmcnt(6)\n"
"v_cmp_eq_u64 vcc,s[28:29],v[160:161]\n"
"v_cmp_eq_u64_e64 s[62:63],s[28:29],v[162:163]\n"
"s_and_b64 vcc,vcc,s[62:63]\n"
"v_cmp_eq_u64_e64 s[62:63],s[28:29],v[164:165]\n"
"s_and_b64 vcc,vcc,s[62:63]\n"
"v_cmp_eq_u64_e64 s[62:63],s[28:29],v[166:167]\n"
"s_and_b64 vcc,vcc,s[62:63]\n"
"s_cmp_eq_u64 vcc,exec\n"
"s_cbranch_scc0 L1004_%=\n"
"L1005_%=:\n"
MF"v[124:127],v[216:219],a[204:207],v[124:127]\n"
"s_mov_b32 s94,8192\n"
BL"v235,s[12:15],s48 offen offset:1024 lds\n"
MF"v[0:3],v[192:195],a[224:227],v[0:3]\n"
"s_mov_b32 s95,12288\n"
RD"v[212:215],v243 offset:20480\n"
"v_add_u32 v250,s50,v224\n"
MF"v[0:3],v[196:199],a[240:243],v[0:3]\n"
BL"v[160:163],v224,s[8:11],s94 offen offset:0 sc1\n"
MF"v[8:11],v[196:199],a[244:247],v[8:11]\n"
BL"v[164:167],v224,s[8:11],s94 offen offset:1024 sc1\n"
MF"v[8:11],v[192:195],a[228:231],v[8:11]\n"
MF"v[16:19],v[192:195],a[232:235],v[16:19]\n"
RD"v[216:219],v242 offset:22528\n"
MF"v[16:19],v[196:199],a[248:251],v[16:19]\n"
BL"v236,s[12:15],s48 offen offset:2048 lds\n"
MF"v[24:27],v[196:199],a[252:255],v[24:27]\n"
BL"v[168:171],v224,s[8:11],s94 offen offset:2048 sc1\n"
MF"v[24:27],v[192:195],a[236:239],v[24:27]\n"
WT"lgkmcnt(1)\n"
BL"v[172:175],v224,s[8:11],s94 offen offset:3072 sc1\n"
MF"v[4:7],v[200:203],a[224:227],v[4:7]\n"
RD"v[220:223],v243 offset:22528\n"
MF"v[4:7],v[204:207],a[240:243],v[4:7]\n"
BL"v237,s[12:15],s48 offen offset:3072 lds\n"
MF"v[12:15],v[204:207],a[244:247],v[12:15]\n"
BL"v[176:179],v224,s[8:11],s95 offen offset:0 sc1\n"
MF"v[12:15],v[200:203],a[228:231],v[12:15]\n"
BL"v[180:183],v224,s[8:11],s95 offen offset:1024 sc1\n"
MF"v[20:23],v[200:203],a[232:235],v[20:23]\n"
RD"v[192:195],v242 offset:24576\n"
MF"v[20:23],v[204:207],a[248:251],v[20:23]\n"
BL"v[184:187],v224,s[8:11],s95 offen offset:2048 sc1\n"
MF"v[28:31],v[204:207],a[252:255],v[28:31]\n"
BL"v[188:191],v224,s[8:11],s95 offen offset:3072 sc1\n"
MF"v[28:31],v[200:203],a[236:239],v[28:31]\n"
RD"v[196:199],v243 offset:24576\n"
MF"v[32:35],v[208:211],a[224:227],v[32:35]\n"
MF"v[32:35],v[212:215],a[240:243],v[32:35]\n"
MF"v[40:43],v[212:215],a[244:247],v[40:43]\n"
RD"v[200:203],v242 offset:26624\n"
MF"v[40:43],v[208:211],a[228:231],v[40:43]\n"
MF"v[48:51],v[208:211],a[232:235],v[48:51]\n"
MF"v[48:51],v[212:215],a[248:251],v[48:51]\n"
MF"v[56:59],v[212:215],a[252:255],v[56:59]\n"
RD"v[204:207],v243 offset:26624\n"
MF"v[56:59],v[208:211],a[236:239],v[56:59]\n"
WT"lgkmcnt(2)\n"
MF"v[36:39],v[216:219],a[224:227],v[36:39]\n"
MF"v[36:39],v[220:223],a[240:243],v[36:39]\n"
RD"v[208:211],v242 offset:28672\n"
MF"v[44:47],v[220:223],a[244:247],v[44:47]\n"
MF"v[44:47],v[216:219],a[228:231],v[44:47]\n"
MF"v[52:55],v[216:219],a[232:235],v[52:55]\n"
MF"v[52:55],v[220:223],a[248:251],v[52:55]\n"
RD"v[212:215],v243 offset:28672\n"
MF"v[60:63],v[220:223],a[252:255],v[60:63]\n"
MF"v[60:63],v[216:219],a[236:239],v[60:63]\n"
MF"v[64:67],v[192:195],a[224:227],v[64:67]\n"
RD"v[216:219],v242 offset:30720\n"
MF"v[64:67],v[196:199],a[240:243],v[64:67]\n"
MF"v[72:75],v[196:199],a[244:247],v[72:75]\n"
MF"v[72:75],v[192:195],a[228:231],v[72:75]\n"
MF"v[80:83],v[192:195],a[232:235],v[80:83]\n"
RD"v[220:223],v243 offset:30720\n"
MF"v[80:83],v[196:199],a[248:251],v[80:83]\n"
MF"v[88:91],v[196:199],a[252:255],v[88:91]\n"
MF"v[88:91],v[192:195],a[236:239],v[88:91]\n"
WT"lgkmcnt(1)\n"
MF"v[68:71],v[200:203],a[224:227],v[68:71]\n"
MF"v[68:71],v[204:207],a[240:243],v[68:71]\n"
MF"v[76:79],v[204:207],a[244:247],v[76:79]\n"
"v_add_u32 v244,114688,v242\n"
"v_add_u32 v245,114688,v243\n"
MF"v[76:79],v[200:203],a[228:231],v[76:79]\n"
"v_add_u32 v242,65536,v242\n"
"v_add_u32 v243,65536,v243\n"
MF"v[84:87],v[200:203],a[232:235],v[84:87]\n"
MF"v[84:87],v[204:207],a[248:251],v[84:87]\n"
WT"lgkmcnt(0)\n"
"s_barrier\n"
MF"v[92:95],v[204:207],a[252:255],v[92:95]\n"
RD"v[192:195],v242 offset:16384\n"
MF"v[92:95],v[200:203],a[236:239],v[92:95]\n"
MF"v[96:99],v[208:211],a[224:227],v[96:99]\n"
MF"v[96:99],v[212:215],a[240:243],v[96:99]\n"
MF"v[104:107],v[212:215],a[244:247],v[104:107]\n"
SA"m0,s36,49152\n"
RD"v[196:199],v243 offset:16384\n"
SA"s48,s60,s61\n"
MF"v[104:107],v[208:211],a[228:231],v[104:107]\n"
MF"v[112:115],v[208:211],a[232:235],v[112:115]\n"
BL"v238,s[20:23],s48 offen offset:0 lds\n"
MF"v[112:115],v[212:215],a[248:251],v[112:115]\n"
MF"v[120:123],v[212:215],a[252:255],v[120:123]\n"
RD"v[200:203],v242 offset:18432\n"
MF"v[120:123],v[208:211],a[236:239],v[120:123]\n"
MF"v[100:103],v[216:219],a[224:227],v[100:103]\n"
BL"v239,s[20:23],s48 offen offset:1024 lds\n"
MF"v[100:103],v[220:223],a[240:243],v[100:103]\n"
"s_cmp_eq_u32 s31,1\n"
"s_cselect_b32 s94,0,2130706432\n"
MF"v[108:111],v[220:223],a[244:247],v[108:111]\n"
"buffer_store_dwordx2 v[232:233],v230,s[8:11],s94 offen sc1\n"
MF"v[108:111],v[216:219],a[228:231],v[108:111]\n"
RD"v[204:207],v243 offset:18432\n"
"v_mov_b32 v227,0\n"
MF"v[116:119],v[216:219],a[232:235],v[116:119]\n"
WT"vmcnt(12)\n"
"v_mov_b32 v226,v160\n"
"v_mov_b32 v228,0\n"
MF"v[116:119],v[220:223],a[248:251],v[116:119]\n"
BL"v240,s[20:23],s48 offen offset:2048 lds\n"
MF"v[124:127],v[220:223],a[252:255],v[124:127]\n"
SA"s61,s61,524288\n"
RD"v[208:211],v242 offset:20480\n"
"v_med3_u32 v228,v227,v228,v161\n"
MF"v[124:127],v[216:219],a[236:239],v[124:127]\n"
WT"lgkmcnt(2)\n"
"v_med3_u32 v227,v226,v227,v161\n"
"v_max_u32 v226,v226,v161\n"
MF"v[128:131],v[192:195],a[0:3],0\n"
"v_med3_u32 v228,v227,v228,v162\n"
"v_med3_u32 v227,v226,v227,v162\n"
MF"v[128:131],v[196:199],a[16:19],v[128:131]\n"
RD"v[212:215],v243 offset:20480\n"
"v_max_u32 v226,v226,v162\n"
MF"v[136:139],v[196:199],a[20:23],0\n"
BL"v241,s[20:23],s48 offen offset:3072 lds\n"
MF"v[136:139],v[192:195],a[4:7],v[136:139]\n"
"v_med3_u32 v228,v227,v228,v163\n"
"v_med3_u32 v227,v226,v227,v163\n"
MF"v[144:147],v[192:195],a[8:11],0\n"
"v_max_u32 v226,v226,v163\n"
PK"v0,v0,v1\n"
MF"v[144:147],v[196:199],a[24:27],v[144:147]\n"
RD"v[216:219],v242 offset:22528\n"
PK"v1,v2,v3\n"
MF"v[152:155],v[196:199],a[28:31],0\n"
SA"m0,s36,32768\n"
WT"vmcnt(13)\n"
"v_med3_u32 v228,v227,v228,v164\n"
MF"v[152:155],v[192:195],a[12:15],v[152:155]\n"
SA"s48,s60,s61\n"
PK"v2,v4,v5\n"
PK"v3,v6,v7\n"
MF"v[132:135],v[200:203],a[0:3],0\n"
WT"lgkmcnt(1)\n"
BL"v238,s[20:23],s48 offen offset:0 lds\n"
MF"v[132:135],v[204:207],a[16:19],v[132:135]\n"
RD"v[220:223],v243 offset:22528\n"
"v_med3_u32 v227,v226,v227,v164\n"
MF"v[140:143],v[204:207],a[20:23],0\n"
"v_max_u32 v226,v226,v164\n"
"v_med3_u32 v228,v227,v228,v165\n"
MF"v[140:143],v[200:203],a[4:7],v[140:143]\n"
"v_med3_u32 v227,v226,v227,v165\n"
"v_max_u32 v226,v226,v165\n"
MF"v[148:151],v[200:203],a[8:11],0\n"
BL"v239,s[20:23],s48 offen offset:1024 lds\n"
MF"v[148:151],v[204:207],a[24:27],v[148:151]\n"
RD"v[192:195],v242 offset:40960\n"
"v_med3_u32 v228,v227,v228,v166\n"
MF"v[156:159],v[204:207],a[28:31],0\n"
PK"v8,v8,v9\n"
PK"v9,v10,v11\n"
MF"v[156:159],v[200:203],a[12:15],v[156:159]\n"
"v_med3_u32 v227,v226,v227,v166\n"
PK"v10,v12,v13\n"
MF"v[128:131],v[208:211],a[32:35],v[128:131]\n"
"s_mov_b32 s49,0\n"
PK"v11,v14,v15\n"
"v_max_u32 v226,v226,v166\n"
MF"v[128:131],v[212:215],a[48:51],v[128:131]\n"
RD"v[196:199],v243 offset:40960\n"
"v_med3_u32 v228,v227,v228,v167\n"
MF"v[136:139],v[212:215],a[52:55],v[136:139]\n"
SA"s61,s61,524288\n"
BL"v240,s[20:23],s48 offen offset:2048 lds\n"
MF"v[136:139],v[208:211],a[36:39],v[136:139]\n"
"s_cmp_eq_u32 s61,5242880\n"
BS"v[0:3],v250,s[8:11],s49 offen offset:0 sc1\n"
MF"v[144:147],v[208:211],a[40:43],v[144:147]\n"
"s_cselect_b32 s61,0,s61\n"
RD"v[200:203],v242 offset:43008\n"
"v_med3_u32 v227,v226,v227,v167\n"
MF"v[144:147],v[212:215],a[56:59],v[144:147]\n"
BL"v241,s[20:23],s48 offen offset:3072 lds\n"
MF"v[152:155],v[212:215],a[60:63],v[152:155]\n"
"v_max_u32 v226,v226,v167\n"
PK"v16,v16,v17\n"
MF"v[152:155],v[208:211],a[44:47],v[152:155]\n"
WT"lgkmcnt(2)\n"
WT"vmcnt(16)\n"
"v_med3_u32 v228,v227,v228,v168\n"
MF"v[132:135],v[216:219],a[32:35],v[132:135]\n"
RD"v[204:207],v243 offset:43008\n"
"v_med3_u32 v227,v226,v227,v168\n"
MF"v[132:135],v[220:223],a[48:51],v[132:135]\n"
PK"v17,v18,v19\n"
"v_max_u32 v226,v226,v168\n"
MF"v[140:143],v[220:223],a[52:55],v[140:143]\n"
"v_med3_u32 v228,v227,v228,v169\n"
PK"v18,v20,v21\n"
MF"v[140:143],v[216:219],a[36:39],v[140:143]\n"
PK"v19,v22,v23\n"
"v_med3_u32 v227,v226,v227,v169\n"
MF"v[148:151],v[216:219],a[40:43],v[148:151]\n"
RD"v[208:211],v242 offset:45056\n"
"v_max_u32 v226,v226,v169\n"
MF"v[148:151],v[220:223],a[56:59],v[148:151]\n"
"s_lshl_b32 s90,s31,2\n"
"v_med3_u32 v228,v227,v228,v170\n"
"v_med3_u32 v227,v226,v227,v170\n"
MF"v[156:159],v[220:223],a[60:63],v[156:159]\n"
SA"s90,s90,s30\n"
"v_max_u32 v226,v226,v170\n"
PK"v24,v24,v25\n"
MF"v[156:159],v[216:219],a[44:47],v[156:159]\n"
"s_lshl_b32 s90,s90,9\n"
PK"v25,v26,v27\n"
"v_med3_u32 v228,v227,v228,v171\n"
MF"v[128:131],v[192:195],a[64:67],v[128:131]\n"
WT"lgkmcnt(1)\n"
RD"v[212:215],v243 offset:45056\n"
"v_med3_u32 v227,v226,v227,v171\n"
MF"v[128:131],v[196:199],a[80:83],v[128:131]\n"
PK"v26,v28,v29\n"
PK"v27,v30,v31\n"
MF"v[136:139],v[196:199],a[84:87],v[136:139]\n"
"v_max_u32 v226,v226,v171\n"
PK"v32,v32,v33\n"
MF"v[136:139],v[192:195],a[68:71],v[136:139]\n"
RD"v[216:219],v242 offset:47104\n"
PK"v33,v34,v35\n"
MF"v[144:147],v[192:195],a[72:75],v[144:147]\n"
WT"vmcnt(15)\n"
"v_med3_u32 v228,v227,v228,v172\n"
"v_med3_u32 v227,v226,v227,v172\n"
MF"v[144:147],v[196:199],a[88:91],v[144:147]\n"
"v_max_u32 v226,v226,v172\n"
"v_med3_u32 v228,v227,v228,v173\n"
MF"v[152:155],v[196:199],a[92:95],v[152:155]\n"
"v_med3_u32 v227,v226,v227,v173\n"
"v_max_u32 v226,v226,v173\n"
MF"v[152:155],v[192:195],a[76:79],v[152:155]\n"
RD"v[220:223],v243 offset:47104\n"
PK"v34,v36,v37\n"
MF"v[132:135],v[200:203],a[64:67],v[132:135]\n"
PK"v35,v38,v39\n"
"v_med3_u32 v228,v227,v228,v174\n"
MF"v[132:135],v[204:207],a[80:83],v[132:135]\n"
"v_med3_u32 v227,v226,v227,v174\n"
"v_max_u32 v226,v226,v174\n"
MF"v[140:143],v[204:207],a[84:87],v[140:143]\n"
"v_med3_u32 v228,v227,v228,v175\n"
"v_med3_u32 v227,v226,v227,v175\n"
MF"v[140:143],v[200:203],a[68:71],v[140:143]\n"
RD"v[192:195],v244 offset:16384\n"
"v_max_u32 v226,v226,v175\n"
MF"v[148:151],v[200:203],a[72:75],v[148:151]\n"
PK"v40,v40,v41\n"
PK"v41,v42,v43\n"
MF"v[148:151],v[204:207],a[88:91],v[148:151]\n"
"v_min_u32 v254,48,v224\n"
PK"v42,v44,v45\n"
MF"v[156:159],v[204:207],a[92:95],v[156:159]\n"
WT"vmcnt(13)\n"
"v_med3_u32 v228,v227,v228,v176\n"
PK"v43,v46,v47\n"
MF"v[156:159],v[200:203],a[76:79],v[156:159]\n"
WT"lgkmcnt(1)\n"
RD"v[196:199],v245 offset:16384\n"
"v_lshlrev_b32 v254,3,v254\n"
MF"v[128:131],v[208:211],a[96:99],v[128:131]\n"
"buffer_load_dword v255,v254,s[8:11],s90 offen sc1\n"
MF"v[128:131],v[212:215],a[112:115],v[128:131]\n"
BS"v[8:11],v250,s[8:11],s49 offen offset:1024 sc1\n"
MF"v[136:139],v[212:215],a[116:119],v[136:139]\n"
"v_med3_u32 v227,v226,v227,v176\n"
"v_max_u32 v226,v226,v176\n"
MF"v[136:139],v[208:211],a[100:103],v[136:139]\n"
RD"v[200:203],v244 offset:18432\n"
"v_med3_u32 v228,v227,v228,v177\n"
MF"v[144:147],v[208:211],a[104:107],v[144:147]\n"
"v_med3_u32 v227,v226,v227,v177\n"
"v_max_u32 v226,v226,v177\n"
MF"v[144:147],v[212:215],a[120:123],v[144:147]\n"
"v_med3_u32 v228,v227,v228,v178\n"
PK"v48,v48,v49\n"
MF"v[152:155],v[212:215],a[124:127],v[152:155]\n"
RD"v[204:207],v245 offset:18432\n"
PK"v49,v50,v51\n"
MF"v[152:155],v[208:211],a[108:111],v[152:155]\n"
"v_med3_u32 v227,v226,v227,v178\n"
PK"v50,v52,v53\n"
MF"v[132:135],v[216:219],a[96:99],v[132:135]\n"
PK"v51,v54,v55\n"
"v_max_u32 v226,v226,v178\n"
MF"v[132:135],v[220:223],a[112:115],v[132:135]\n"
"v_med3_u32 v228,v227,v228,v179\n"
"v_med3_u32 v227,v226,v227,v179\n"
MF"v[140:143],v[220:223],a[116:119],v[140:143]\n"
RD"v[208:211],v244 offset:20480\n"
"v_max_u32 v226,v226,v179\n"
MF"v[140:143],v[216:219],a[100:103],v[140:143]\n"
WT"vmcnt(14)\n"
"v_med3_u32 v228,v227,v228,v180\n"
"v_med3_u32 v227,v226,v227,v180\n"
MF"v[148:151],v[216:219],a[104:107],v[148:151]\n"
PK"v56,v56,v57\n"
PK"v57,v58,v59\n"
MF"v[148:151],v[220:223],a[120:123],v[148:151]\n"
"v_max_u32 v226,v226,v180\n"
PK"v58,v60,v61\n"
MF"v[156:159],v[220:223],a[124:127],v[156:159]\n"
RD"v[212:215],v245 offset:20480\n"
PK"v59,v62,v63\n"
MF"v[156:159],v[216:219],a[108:111],v[156:159]\n"
WT"lgkmcnt(2)\n"
"v_med3_u32 v228,v227,v228,v181\n"
"v_med3_u32 v227,v226,v227,v181\n"
MF"v[128:131],v[192:195],a[128:131],v[128:131]\n"
"v_max_u32 v226,v226,v181\n"
"v_med3_u32 v228,v227,v228,v182\n"
MF"v[128:131],v[196:199],a[144:147],v[128:131]\n"
"v_med3_u32 v227,v226,v227,v182\n"
"v_max_u32 v226,v226,v182\n"
MF"v[136:139],v[196:199],a[148:151],v[136:139]\n"
RD"v[216:219],v244 offset:22528\n"
PK"v64,v64,v65\n"
MF"v[136:139],v[192:195],a[132:135],v[136:139]\n"
PK"v65,v66,v67\n"
"v_med3_u32 v228,v227,v228,v183\n"
MF"v[144:147],v[192:195],a[136:139],v[144:147]\n"
PK"v66,v68,v69\n"
PK"v67,v70,v71\n"
MF"v[144:147],v[196:199],a[152:155],v[144:147]\n"
RD"v[220:223],v245 offset:22528\n"
"v_med3_u32 v227,v226,v227,v183\n"
MF"v[152:155],v[196:199],a[156:159],v[152:155]\n"
"v_max_u32 v226,v226,v183\n"
PK"v72,v72,v73\n"
MF"v[152:155],v[192:195],a[140:143],v[152:155]\n"
WT"vmcnt(13)\n"
"v_med3_u32 v228,v227,v228,v184\n"
"v_med3_u32 v227,v226,v227,v184\n"
MF"v[132:135],v[200:203],a[128:131],v[132:135]\n"
"v_max_u32 v226,v226,v184\n"
"v_med3_u32 v228,v227,v228,v185\n"
MF"v[132:135],v[204:207],a[144:147],v[132:135]\n"
RD"v[192:195],v244 offset:40960\n"
PK"v73,v74,v75\n"
MF"v[140:143],v[204:207],a[148:151],v[140:143]\n"
"v_med3_u32 v227,v226,v227,v185\n"
"v_max_u32 v226,v226,v185\n"
MF"v[140:143],v[200:203],a[132:135],v[140:143]\n"
PK"v74,v76,v77\n"
PK"v75,v78,v79\n"
MF"v[148:151],v[200:203],a[136:139],v[148:151]\n"
"v_med3_u32 v228,v227,v228,v186\n"
"v_med3_u32 v227,v226,v227,v186\n"
MF"v[148:151],v[204:207],a[152:155],v[148:151]\n"
RD"v[196:199],v245 offset:40960\n"
"v_max_u32 v226,v226,v186\n"
MF"v[156:159],v[204:207],a[156:159],v[156:159]\n"
"v_med3_u32 v228,v227,v228,v187\n"
"v_med3_u32 v227,v226,v227,v187\n"
MF"v[156:159],v[200:203],a[140:143],v[156:159]\n"
WT"lgkmcnt(2)\n"
PK"v80,v80,v81\n"
PK"v81,v82,v83\n"
MF"v[128:131],v[208:211],a[160:163],v[128:131]\n"
BS"v[16:19],v250,s[8:11],s49 offen offset:2048 sc1\n"
MF"v[128:131],v[212:215],a[176:179],v[128:131]\n"
RD"v[200:203],v244 offset:43008\n"
"v_max_u32 v226,v226,v187\n"
MF"v[136:139],v[212:215],a[180:183],v[136:139]\n"
WT"vmcnt(13)\n"
"v_med3_u32 v228,v227,v228,v188\n"
PK"v82,v84,v85\n"
MF"v[136:139],v[208:211],a[164:167],v[136:139]\n"
PK"v83,v86,v87\n"
"v_med3_u32 v227,v226,v227,v188\n"
MF"v[144:147],v[208:211],a[168:171],v[144:147]\n"
"v_max_u32 v226,v226,v188\n"
"v_med3_u32 v228,v227,v228,v189\n"
MF"v[144:147],v[212:215],a[184:187],v[144:147]\n"
RD"v[204:207],v245 offset:43008\n"
"v_med3_u32 v227,v226,v227,v189\n"
MF"v[152:155],v[212:215],a[188:191],v[152:155]\n"
"v_max_u32 v226,v226,v189\n"
PK"v88,v88,v89\n"
MF"v[152:155],v[208:211],a[172:175],v[152:155]\n"
PK"v89,v90,v91\n"
"v_med3_u32 v228,v227,v228,v190\n"
MF"v[132:135],v[216:219],a[160:163],v[132:135]\n"
RD"v[208:211],v244 offset:45056\n"
"v_med3_u32 v227,v226,v227,v190\n"
MF"v[132:135],v[220:223],a[176:179],v[132:135]\n"
PK"v90,v92,v93\n"
PK"v91,v94,v95\n"
MF"v[140:143],v[220:223],a[180:183],v[140:143]\n"
"v_max_u32 v226,v226,v190\n"
"v_med3_u32 v228,v227,v228,v191\n"
MF"v[140:143],v[216:219],a[164:167],v[140:143]\n"
"v_med3_u32 v227,v226,v227,v191\n"
"v_max_u32 v226,v226,v191\n"
MF"v[148:151],v[216:219],a[168:171],v[148:151]\n"
RD"v[212:215],v245 offset:45056\n"
"v_mov_b32 v229,v226\n"
MF"v[148:151],v[220:223],a[184:187],v[148:151]\n"
MF"v[156:159],v[220:223],a[188:191],v[156:159]\n"
"v_max_u32_dpp v229,v229,v229 row_shr:1 row_mask:0xf bank_mask:0xf\n"
MF"v[156:159],v[216:219],a[172:175],v[156:159]\n"
WT"lgkmcnt(1)\n"
"v_max_u32_dpp v229,v229,v229 row_shr:2 row_mask:0xf bank_mask:0xf\n"
MF"v[128:131],v[192:195],a[192:195],v[128:131]\n"
RD"v[216:219],v244 offset:47104\n"
"v_max_u32_dpp v229,v229,v229 row_shr:4 row_mask:0xf bank_mask:0xf\n"
MF"v[128:131],v[196:199],a[208:211],v[128:131]\n"
MF"v[136:139],v[196:199],a[212:215],v[136:139]\n"
"v_max_u32_dpp v229,v229,v229 row_shr:8 row_mask:0xf bank_mask:0xf\n"
MF"v[136:139],v[192:195],a[196:199],v[136:139]\n"
MF"v[144:147],v[192:195],a[200:203],v[144:147]\n"
RD"v[220:223],v245 offset:47104\n"
"v_max_u32_dpp v229,v229,v229 row_bcast:15 row_mask:0xa bank_mask:0xf\n"
MF"v[144:147],v[196:199],a[216:219],v[144:147]\n"
MF"v[152:155],v[196:199],a[220:223],v[152:155]\n"
"v_max_u32_dpp v229,v229,v229 row_bcast:31 row_mask:0xc bank_mask:0xf\n"
MF"v[152:155],v[192:195],a[204:207],v[152:155]\n"
"v_readlane_b32 s62,v229,63\n"
MF"v[132:135],v[200:203],a[192:195],v[132:135]\n"
MF"v[132:135],v[204:207],a[208:211],v[132:135]\n"
"v_cmp_eq_u32 vcc,s62,v226\n"
"v_writelane_b32 v225,s62,0\n"
MF"v[140:143],v[204:207],a[212:215],v[140:143]\n"
"v_cndmask_b32 v226,v226,v227,vcc\n"
"v_cndmask_b32_e64 v227,v227,0,vcc\n"
MF"v[140:143],v[200:203],a[196:199],v[140:143]\n"
"v_mov_b32 v229,v226\n"
MF"v[148:151],v[200:203],a[200:203],v[148:151]\n"
MF"v[148:151],v[204:207],a[216:219],v[148:151]\n"
WT"lgkmcnt(0)\n"
"s_barrier\n"
MF"v[156:159],v[204:207],a[220:223],v[156:159]\n"
RD"v[192:195],v242 offset:0\n"
"v_max_u32_dpp v229,v229,v229 row_shr:1 row_mask:0xf bank_mask:0xf\n"
MF"v[156:159],v[200:203],a[204:207],v[156:159]\n"
BS"v[24:27],v250,s[8:11],s49 offen offset:3072 sc1\n"
MF"v[128:131],v[208:211],a[224:227],v[128:131]\n"
"v_max_u32_dpp v229,v229,v229 row_shr:2 row_mask:0xf bank_mask:0xf\n"
MF"v[128:131],v[212:215],a[240:243],v[128:131]\n"
MF"v[136:139],v[212:215],a[244:247],v[136:139]\n"
RD"v[196:199],v243 offset:0\n"
"v_max_u32_dpp v229,v229,v229 row_shr:4 row_mask:0xf bank_mask:0xf\n"
MF"v[136:139],v[208:211],a[228:231],v[136:139]\n"
MF"v[144:147],v[208:211],a[232:235],v[144:147]\n"
"v_max_u32_dpp v229,v229,v229 row_shr:8 row_mask:0xf bank_mask:0xf\n"
MF"v[144:147],v[212:215],a[248:251],v[144:147]\n"
MF"v[152:155],v[212:215],a[252:255],v[152:155]\n"
RD"v[200:203],v242 offset:2048\n"
"v_max_u32_dpp v229,v229,v229 row_bcast:15 row_mask:0xa bank_mask:0xf\n"
MF"v[152:155],v[208:211],a[236:239],v[152:155]\n"
MF"v[132:135],v[216:219],a[224:227],v[132:135]\n"
"v_max_u32_dpp v229,v229,v229 row_bcast:31 row_mask:0xc bank_mask:0xf\n"
MF"v[132:135],v[220:223],a[240:243],v[132:135]\n"
"v_readlane_b32 s62,v229,63\n"
MF"v[140:143],v[220:223],a[244:247],v[140:143]\n"
RD"v[204:207],v243 offset:2048\n"
"v_cmp_eq_u32 vcc,s62,v226\n"
MF"v[140:143],v[216:219],a[228:231],v[140:143]\n"
"v_writelane_b32 v225,s62,1\n"
"v_cndmask_b32 v226,v226,v227,vcc\n"
MF"v[148:151],v[216:219],a[232:235],v[148:151]\n"
"v_cndmask_b32_e64 v227,v227,0,vcc\n"
"v_mov_b32 v229,v226\n"
MF"v[148:151],v[220:223],a[248:251],v[148:151]\n"
MF"v[156:159],v[220:223],a[252:255],v[156:159]\n"
RD"v[208:211],v242 offset:4096\n"
"v_max_u32_dpp v229,v229,v229 row_shr:1 row_mask:0xf bank_mask:0xf\n"
MF"v[156:159],v[216:219],a[236:239],v[156:159]\n"
WT"lgkmcnt(2)\n"
"v_max_u32_dpp v229,v229,v229 row_shr:2 row_mask:0xf bank_mask:0xf\n"
MF"v[36:39],v[192:195],a[0:3],0\n"
MF"v[36:39],v[196:199],a[16:19],v[36:39]\n"
RD"v[212:215],v243 offset:4096\n"
"v_max_u32_dpp v229,v229,v229 row_shr:4 row_mask:0xf bank_mask:0xf\n"
MF"v[44:47],v[196:199],a[20:23],0\n"
MF"v[44:47],v[192:195],a[4:7],v[44:47]\n"
"v_max_u32_dpp v229,v229,v229 row_shr:8 row_mask:0xf bank_mask:0xf\n"
PK"v4,v128,v129\n"
MF"v[52:55],v[192:195],a[8:11],0\n"
"v_max_u32_dpp v229,v229,v229 row_bcast:15 row_mask:0xa bank_mask:0xf\n"
PK"v5,v130,v131\n"
MF"v[52:55],v[196:199],a[24:27],v[52:55]\n"
RD"v[216:219],v242 offset:6144\n"
"v_max_u32_dpp v229,v229,v229 row_bcast:31 row_mask:0xc bank_mask:0xf\n"
MF"v[60:63],v[196:199],a[28:31],0\n"
"v_readlane_b32 s62,v229,63\n"
PK"v6,v132,v133\n"
MF"v[60:63],v[192:195],a[12:15],v[60:63]\n"
"v_cmp_eq_u32 vcc,s62,v226\n"
"v_writelane_b32 v225,s62,2\n"
MF"v[68:71],v[200:203],a[0:3],0\n"
WT"lgkmcnt(1)\n"
"v_cndmask_b32 v226,v226,v227,vcc\n"
"v_cndmask_b32_e64 v227,v227,0,vcc\n"
MF"v[68:71],v[204:207],a[16:19],v[68:71]\n"
RD"v[220:223],v243 offset:6144\n"
"v_mov_b32 v229,v226\n"
MF"v[76:79],v[204:207],a[20:23],0\n"
PK"v7,v134,v135\n"
"v_max_u32_dpp v229,v229,v229 row_shr:1 row_mask:0xf bank_mask:0xf\n"
MF"v[76:79],v[200:203],a[4:7],v[76:79]\n"
PK"v12,v136,v137\n"
"v_max_u32_dpp v229,v229,v229 row_shr:2 row_mask:0xf bank_mask:0xf\n"
MF"v[84:87],v[200:203],a[8:11],0\n"
"s_mov_b32 s49,8192\n"
"v_max_u32_dpp v229,v229,v229 row_shr:4 row_mask:0xf bank_mask:0xf\n"
PK"v13,v138,v139\n"
MF"v[84:87],v[204:207],a[24:27],v[84:87]\n"
RD"v[192:195],v242 offset:24576\n"
"v_max_u32_dpp v229,v229,v229 row_shr:8 row_mask:0xf bank_mask:0xf\n"
MF"v[92:95],v[204:207],a[28:31],0\n"
BS"v[32:35],v250,s[8:11],s49 offen offset:0 sc1\n"
MF"v[92:95],v[200:203],a[12:15],v[92:95]\n"
"v_max_u32_dpp v229,v229,v229 row_bcast:15 row_mask:0xa bank_mask:0xf\n"
PK"v14,v140,v141\n"
MF"v[36:39],v[208:211],a[32:35],v[36:39]\n"
"v_max_u32_dpp v229,v229,v229 row_bcast:31 row_mask:0xc bank_mask:0xf\n"
PK"v15,v142,v143\n"
MF"v[36:39],v[212:215],a[48:51],v[36:39]\n"
RD"v[196:199],v243 offset:24576\n"
"v_readlane_b32 s62,v229,63\n"
MF"v[44:47],v[212:215],a[52:55],v[44:47]\n"
PK"v20,v144,v145\n"
"v_cmp_eq_u32 vcc,s62,v226\n"
MF"v[44:47],v[208:211],a[36:39],v[44:47]\n"
"v_writelane_b32 v225,s62,3\n"
"v_cndmask_b32 v226,v226,v227,vcc\n"
MF"v[52:55],v[208:211],a[40:43],v[52:55]\n"
RD"v[200:203],v242 offset:26624\n"
"v_cndmask_b32_e64 v227,v227,0,vcc\n"
MF"v[52:55],v[212:215],a[56:59],v[52:55]\n"
"v_mov_b32 v229,v226\n"
PK"v21,v146,v147\n"
MF"v[60:63],v[212:215],a[60:63],v[60:63]\n"
"v_max_u32_dpp v229,v229,v229 row_shr:1 row_mask:0xf bank_mask:0xf\n"
PK"v22,v148,v149\n"
MF"v[60:63],v[208:211],a[44:47],v[60:63]\n"
WT"lgkmcnt(2)\n"
"v_max_u32_dpp v229,v229,v229 row_shr:2 row_mask:0xf bank_mask:0xf\n"
PK"v23,v150,v151\n"
MF"v[68:71],v[216:219],a[32:35],v[68:71]\n"
RD"v[204:207],v243 offset:26624\n"
"v_max_u32_dpp v229,v229,v229 row_shr:4 row_mask:0xf bank_mask:0xf\n"
MF"v[68:71],v[220:223],a[48:51],v[68:71]\n"
PK"v28,v152,v153\n"
"v_max_u32_dpp v229,v229,v229 row_shr:8 row_mask:0xf bank_mask:0xf\n"
MF"v[76:79],v[220:223],a[52:55],v[76:79]\n"
PK"v29,v154,v155\n"
"v_max_u32_dpp v229,v229,v229 row_bcast:15 row_mask:0xa bank_mask:0xf\n"
MF"v[76:79],v[216:219],a[36:39],v[76:79]\n"
PK"v30,v156,v157\n"
"v_max_u32_dpp v229,v229,v229 row_bcast:31 row_mask:0xc bank_mask:0xf\n"
MF"v[84:87],v[216:219],a[40:43],v[84:87]\n"
RD"v[208:211],v242 offset:28672\n"
"v_readlane_b32 s62,v229,63\n"
MF"v[84:87],v[220:223],a[56:59],v[84:87]\n"
PK"v31,v158,v159\n"
"v_cmp_eq_u32 vcc,s62,v226\n"
MF"v[92:95],v[220:223],a[60:63],v[92:95]\n"
"v_writelane_b32 v225,s62,4\n"
"v_cndmask_b32 v226,v226,v227,vcc\n"
MF"v[92:95],v[216:219],a[44:47],v[92:95]\n"
"v_cndmask_b32_e64 v227,v227,0,vcc\n"
"v_mov_b32 v229,v226\n"
MF"v[36:39],v[192:195],a[64:67],v[36:39]\n"
WT"lgkmcnt(1)\n"
RD"v[212:215],v243 offset:28672\n"
"v_max_u32_dpp v229,v229,v229 row_shr:1 row_mask:0xf bank_mask:0xf\n"
MF"v[36:39],v[196:199],a[80:83],v[36:39]\n"
MF"v[44:47],v[196:199],a[84:87],v[44:47]\n"
"v_max_u32_dpp v229,v229,v229 row_shr:2 row_mask:0xf bank_mask:0xf\n"
MF"v[44:47],v[192:195],a[68:71],v[44:47]\n"
RD"v[216:219],v242 offset:30720\n"
"v_max_u32_dpp v229,v229,v229 row_shr:4 row_mask:0xf bank_mask:0xf\n"
MF"v[52:55],v[192:195],a[72:75],v[52:55]\n"
MF"v[52:55],v[196:199],a[88:91],v[52:55]\n"
"v_max_u32_dpp v229,v229,v229 row_shr:8 row_mask:0xf bank_mask:0xf\n"
MF"v[60:63],v[196:199],a[92:95],v[60:63]\n"
MF"v[60:63],v[192:195],a[76:79],v[60:63]\n"
RD"v[220:223],v243 offset:30720\n"
"v_max_u32_dpp v229,v229,v229 row_bcast:15 row_mask:0xa bank_mask:0xf\n"
MF"v[68:71],v[200:203],a[64:67],v[68:71]\n"
MF"v[68:71],v[204:207],a[80:83],v[68:71]\n"
"v_max_u32_dpp v229,v229,v229 row_bcast:31 row_mask:0xc bank_mask:0xf\n"
MF"v[76:79],v[204:207],a[84:87],v[76:79]\n"
"v_readlane_b32 s62,v229,63\n"
MF"v[76:79],v[200:203],a[68:71],v[76:79]\n"
RD"v[192:195],v244 offset:0\n"
"v_cmp_eq_u32 vcc,s62,v226\n"
MF"v[84:87],v[200:203],a[72:75],v[84:87]\n"
"v_writelane_b32 v225,s62,5\n"
"v_cndmask_b32 v226,v226,v227,vcc\n"
MF"v[84:87],v[204:207],a[88:91],v[84:87]\n"
BS"v[40:43],v250,s[8:11],s49 offen offset:1024 sc1\n"
MF"v[92:95],v[204:207],a[92:95],v[92:95]\n"
"v_cndmask_b32_e64 v227,v227,0,vcc\n"
"v_mov_b32 v229,v226\n"
MF"v[92:95],v[200:203],a[76:79],v[92:95]\n"
WT"lgkmcnt(1)\n"
RD"v[196:199],v245 offset:0\n"
"v_max_u32_dpp v229,v229,v229 row_shr:1 row_mask:0xf bank_mask:0xf\n"
MF"v[36:39],v[208:211],a[96:99],v[36:39]\n"
MF"v[36:39],v[212:215],a[112:115],v[36:39]\n"
"v_max_u32_dpp v229,v229,v229 row_shr:2 row_mask:0xf bank_mask:0xf\n"
MF"v[44:47],v[212:215],a[116:119],v[44:47]\n"
MF"v[44:47],v[208:211],a[100:103],v[44:47]\n"
RD"v[200:203],v244 offset:2048\n"
"v_max_u32_dpp v229,v229,v229 row_shr:4 row_mask:0xf bank_mask:0xf\n"
MF"v[52:55],v[208:211],a[104:107],v[52:55]\n"
MF"v[52:55],v[212:215],a[120:123],v[52:55]\n"
"v_max_u32_dpp v229,v229,v229 row_shr:8 row_mask:0xf bank_mask:0xf\n"
MF"v[60:63],v[212:215],a[124:127],v[60:63]\n"
RD"v[204:207],v245 offset:2048\n"
"v_max_u32_dpp v229,v229,v229 row_bcast:15 row_mask:0xa bank_mask:0xf\n"
MF"v[60:63],v[208:211],a[108:111],v[60:63]\n"
MF"v[68:71],v[216:219],a[96:99],v[68:71]\n"
"v_max_u32_dpp v229,v229,v229 row_bcast:31 row_mask:0xc bank_mask:0xf\n"
MF"v[68:71],v[220:223],a[112:115],v[68:71]\n"
"v_readlane_b32 s62,v229,63\n"
MF"v[76:79],v[220:223],a[116:119],v[76:79]\n"
SA"m0,s37,16384\n"
RD"v[208:211],v244 offset:4096\n"
"v_cmp_eq_u32 vcc,s62,v226\n"
MF"v[76:79],v[216:219],a[100:103],v[76:79]\n"
SA"s48,s39,98304\n"
"v_writelane_b32 v225,s62,6\n"
"v_cndmask_b32 v226,v226,v227,vcc\n"
MF"v[84:87],v[216:219],a[104:107],v[84:87]\n"
BL"v234,s[12:15],s48 offen offset:0 lds\n"
MF"v[84:87],v[220:223],a[120:123],v[84:87]\n"
"v_cndmask_b32_e64 v227,v227,0,vcc\n"
"v_mov_b32 v229,v226\n"
MF"v[92:95],v[220:223],a[124:127],v[92:95]\n"
RD"v[212:215],v245 offset:4096\n"
"v_max_u32_dpp v229,v229,v229 row_shr:1 row_mask:0xf bank_mask:0xf\n"
MF"v[92:95],v[216:219],a[108:111],v[92:95]\n"
WT"lgkmcnt(2)\n"
"v_max_u32_dpp v229,v229,v229 row_shr:2 row_mask:0xf bank_mask:0xf\n"
PK"v96,v96,v97\n"
MF"v[36:39],v[192:195],a[128:131],v[36:39]\n"
"v_max_u32_dpp v229,v229,v229 row_shr:4 row_mask:0xf bank_mask:0xf\n"
PK"v97,v98,v99\n"
MF"v[36:39],v[196:199],a[144:147],v[36:39]\n"
"v_max_u32_dpp v229,v229,v229 row_shr:8 row_mask:0xf bank_mask:0xf\n"
PK"v98,v100,v101\n"
MF"v[44:47],v[196:199],a[148:151],v[44:47]\n"
RD"v[216:219],v244 offset:6144\n"
"v_max_u32_dpp v229,v229,v229 row_bcast:15 row_mask:0xa bank_mask:0xf\n"
MF"v[44:47],v[192:195],a[132:135],v[44:47]\n"
BL"v235,s[12:15],s48 offen offset:1024 lds\n"
MF"v[52:55],v[192:195],a[136:139],v[52:55]\n"
"v_max_u32_dpp v229,v229,v229 row_bcast:31 row_mask:0xc bank_mask:0xf\n"
PK"v99,v102,v103\n"
MF"v[52:55],v[196:199],a[152:155],v[52:55]\n"
RD"v[220:223],v245 offset:6144\n"
"v_readlane_b32 s62,v229,63\n"
MF"v[60:63],v[196:199],a[156:159],v[60:63]\n"
"v_mov_b32 v229,v228\n"
"v_cmp_eq_u32 vcc,s62,v226\n"
MF"v[60:63],v[192:195],a[140:143],v[60:63]\n"
"v_writelane_b32 v225,s62,7\n"
"v_cndmask_b32 v226,v226,v227,vcc\n"
MF"v[68:71],v[200:203],a[128:131],v[68:71]\n"
BL"v236,s[12:15],s48 offen offset:2048 lds\n"
MF"v[68:71],v[204:207],a[144:147],v[68:71]\n"
RD"v[192:195],v244 offset:24576\n"
"v_cndmask_b32_e64 v227,v227,0,vcc\n"
MF"v[76:79],v[204:207],a[148:151],v[76:79]\n"
"v_max_u32_dpp v229,v229,v229 row_shr:1 row_mask:0xf bank_mask:0xf\n"
PK"v104,v104,v105\n"
MF"v[76:79],v[200:203],a[132:135],v[76:79]\n"
"v_max_u32_dpp v229,v229,v229 row_shr:2 row_mask:0xf bank_mask:0xf\n"
PK"v105,v106,v107\n"
MF"v[84:87],v[200:203],a[136:139],v[84:87]\n"
BS"v[48:51],v250,s[8:11],s49 offen offset:2048 sc1\n"
MF"v[84:87],v[204:207],a[152:155],v[84:87]\n"
RD"v[196:199],v245 offset:24576\n"
"v_max_u32_dpp v229,v229,v229 row_shr:4 row_mask:0xf bank_mask:0xf\n"
MF"v[92:95],v[204:207],a[156:159],v[92:95]\n"
BL"v237,s[12:15],s48 offen offset:3072 lds\n"
MF"v[92:95],v[200:203],a[140:143],v[92:95]\n"
WT"lgkmcnt(2)\n"
"v_max_u32_dpp v229,v229,v229 row_shr:8 row_mask:0xf bank_mask:0xf\n"
PK"v106,v108,v109\n"
MF"v[36:39],v[208:211],a[160:163],v[36:39]\n"
"v_max_u32_dpp v229,v229,v229 row_bcast:15 row_mask:0xa bank_mask:0xf\n"
PK"v107,v110,v111\n"
MF"v[36:39],v[212:215],a[176:179],v[36:39]\n"
RD"v[200:203],v244 offset:26624\n"
"v_max_u32_dpp v229,v229,v229 row_bcast:31 row_mask:0xc bank_mask:0xf\n"
MF"v[44:47],v[212:215],a[180:183],v[44:47]\n"
SA"m0,s37,20480\n"
"v_readlane_b32 s63,v229,63\n"
PK"v112,v112,v113\n"
MF"v[44:47],v[208:211],a[164:167],v[44:47]\n"
"s_cmp_gt_u32 s63,s62\n"
"s_cbranch_scc1 L1007_%=\n"
"L1008_%=:\n"
MF"v[52:55],v[208:211],a[168:171],v[52:55]\n"
SA"s48,s98,98304\n"
"v_lshrrev_b32 v229,2,v224\n"
PK"v113,v114,v115\n"
MF"v[52:55],v[212:215],a[184:187],v[52:55]\n"
"ds_write_b32 v229,v225 offset:16384\n"
MF"v[60:63],v[212:215],a[188:191],v[60:63]\n"
BL"v234,s[12:15],s48 offen offset:0 lds\n"
MF"v[60:63],v[208:211],a[172:175],v[60:63]\n"
RD"v[204:207],v245 offset:26624\n"
PK"v114,v116,v117\n"
MF"v[68:71],v[216:219],a[160:163],v[68:71]\n"
RD"v[208:211],v244 offset:28672\n"
PK"v115,v118,v119\n"
MF"v[68:71],v[220:223],a[176:179],v[68:71]\n"
PK"v120,v120,v121\n"
PK"v121,v122,v123\n"
MF"v[76:79],v[220:223],a[180:183],v[76:79]\n"
BL"v235,s[12:15],s48 offen offset:1024 lds\n"
MF"v[76:79],v[216:219],a[164:167],v[76:79]\n"
PK"v122,v124,v125\n"
PK"v123,v126,v127\n"
MF"v[84:87],v[216:219],a[168:171],v[84:87]\n"
RD"v[212:215],v245 offset:28672\n"
MF"v[84:87],v[220:223],a[184:187],v[84:87]\n"
MF"v[92:95],v[220:223],a[188:191],v[92:95]\n"
MF"v[92:95],v[216:219],a[172:175],v[92:95]\n"
WT"lgkmcnt(1)\n"
MF"v[36:39],v[192:195],a[192:195],v[36:39]\n"
RD"v[216:219],v244 offset:30720\n"
MF"v[36:39],v[196:199],a[208:211],v[36:39]\n"
BL"v236,s[12:15],s48 offen offset:2048 lds\n"
MF"v[44:47],v[196:199],a[212:215],v[44:47]\n"
MF"v[44:47],v[192:195],a[196:199],v[44:47]\n"
MF"v[52:55],v[192:195],a[200:203],v[52:55]\n"
RD"v[220:223],v245 offset:30720\n"
MF"v[52:55],v[196:199],a[216:219],v[52:55]\n"
MF"v[60:63],v[196:199],a[220:223],v[60:63]\n"
BL"v237,s[12:15],s48 offen offset:3072 lds\n"
MF"v[60:63],v[192:195],a[204:207],v[60:63]\n"
MF"v[68:71],v[200:203],a[192:195],v[68:71]\n"
MF"v[68:71],v[204:207],a[208:211],v[68:71]\n"
MF"v[76:79],v[204:207],a[212:215],v[76:79]\n"
MF"v[76:79],v[200:203],a[196:199],v[76:79]\n"
BS"v[56:59],v250,s[8:11],s49 offen offset:3072 sc1\n"
MF"v[84:87],v[200:203],a[200:203],v[84:87]\n"
"v_lshrrev_b32 v251,2,v224\n"
"v_and_b32 v251,28,v251\n"
MF"v[84:87],v[204:207],a[216:219],v[84:87]\n"
WT"lgkmcnt(0)\n"
"s_barrier\n"
MF"v[92:95],v[204:207],a[220:223],v[92:95]\n"
RD"v[192:195],v242 offset:8192\n"
"ds_read_b32 v234,v251 offset:16384\n"
MF"v[92:95],v[200:203],a[204:207],v[92:95]\n"
MF"v[36:39],v[208:211],a[224:227],v[36:39]\n"
MF"v[36:39],v[212:215],a[240:243],v[36:39]\n"
MF"v[44:47],v[212:215],a[244:247],v[44:47]\n"
RD"v[196:199],v243 offset:8192\n"
MF"v[44:47],v[208:211],a[228:231],v[44:47]\n"
MF"v[52:55],v[208:211],a[232:235],v[52:55]\n"
MF"v[52:55],v[212:215],a[248:251],v[52:55]\n"
MF"v[60:63],v[212:215],a[252:255],v[60:63]\n"
RD"v[200:203],v242 offset:10240\n"
MF"v[60:63],v[208:211],a[236:239],v[60:63]\n"
WT"lgkmcnt(2)\n"
"v_not_b32 v251,v234\n"
"v_bfe_u32 v252,v251,4,7\n"
MF"v[68:71],v[216:219],a[224:227],v[68:71]\n"
"v_mul_u32_u24 v252,20480,v252\n"
MF"v[68:71],v[220:223],a[240:243],v[68:71]\n"
"v_and_b32 v251,15,v251\n"
MF"v[76:79],v[220:223],a[244:247],v[76:79]\n"
RD"v[204:207],v243 offset:10240\n"
"v_lshl_add_u32 v251,v251,4,v252\n"
MF"v[76:79],v[216:219],a[228:231],v[76:79]\n"
"v_and_b32 v252,0x380,v224\n"
MF"v[84:87],v[216:219],a[232:235],v[84:87]\n"
"v_lshl_add_u32 v251,v252,1,v251\n"
MF"v[84:87],v[220:223],a[248:251],v[84:87]\n"
"v_add_u32 v252,v252,v224\n"
MF"v[92:95],v[220:223],a[252:255],v[92:95]\n"
"s_xor_b32 s92,s34,0\n"
RD"v[208:211],v242 offset:12288\n"
MF"v[92:95],v[216:219],a[236:239],v[92:95]\n"
"s_lshl_b32 s92,s92,2\n"
MF"v[128:131],v[192:195],a[0:3],0\n"
WT"lgkmcnt(2)\n"
SA"s92,s92,s30\n"
MF"v[128:131],v[196:199],a[16:19],v[128:131]\n"
"s_lshl_b32 s93,s92,11\n"
RD"v[212:215],v243 offset:12288\n"
MF"v[136:139],v[196:199],a[20:23],0\n"
"s_cmp_lt_u32 s92,10\n"
"s_cselect_b32 s93,s93,2130706432\n"
MF"v[136:139],v[192:195],a[4:7],v[136:139]\n"
SA"s92,s93,524288\n"
PK"v36,v36,v37\n"
PK"v37,v38,v39\n"
MF"v[144:147],v[192:195],a[8:11],0\n"
BL"v[100:103],v251,s[8:11],s92 offen sc1\n"
MF"v[144:147],v[196:199],a[24:27],v[144:147]\n"
"s_mul_i32 s92,s35,20480\n"
RD"v[216:219],v242 offset:14336\n"
PK"v38,v68,v69\n"
MF"v[152:155],v[196:199],a[28:31],0\n"
SA"s92,s92,s93\n"
PK"v39,v70,v71\n"
MF"v[152:155],v[192:195],a[12:15],v[152:155]\n"
SA"s92,s92,3145728\n"
MF"v[132:135],v[200:203],a[0:3],0\n"
WT"lgkmcnt(1)\n"
PK"v44,v44,v45\n"
PK"v45,v46,v47\n"
MF"v[132:135],v[204:207],a[16:19],v[132:135]\n"
"s_mov_b32 s49,16384\n"
RD"v[220:223],v243 offset:14336\n"
PK"v46,v76,v77\n"
MF"v[140:143],v[204:207],a[20:23],0\n"
BS"v[64:67],v250,s[8:11],s49 offen offset:0 sc1\n"
MF"v[140:143],v[200:203],a[4:7],v[140:143]\n"
PK"v47,v78,v79\n"
MF"v[148:151],v[200:203],a[8:11],0\n"
MF"v[148:151],v[204:207],a[24:27],v[148:151]\n"
RD"v[192:195],v242 offset:32768\n"
PK"v52,v52,v53\n"
MF"v[156:159],v[204:207],a[28:31],0\n"
PK"v53,v54,v55\n"
PK"v54,v84,v85\n"
MF"v[156:159],v[200:203],a[12:15],v[156:159]\n"
PK"v55,v86,v87\n"
MF"v[128:131],v[208:211],a[32:35],v[128:131]\n"
MF"v[128:131],v[212:215],a[48:51],v[128:131]\n"
RD"v[196:199],v243 offset:32768\n"
MF"v[136:139],v[212:215],a[52:55],v[136:139]\n"
PK"v60,v60,v61\n"
PK"v61,v62,v63\n"
MF"v[136:139],v[208:211],a[36:39],v[136:139]\n"
PK"v62,v92,v93\n"
PK"v63,v94,v95\n"
MF"v[144:147],v[208:211],a[40:43],v[144:147]\n"
SA"m0,s36,0\n"
RD"v[200:203],v242 offset:34816\n"
SA"s48,s60,s61\n"
MF"v[144:147],v[212:215],a[56:59],v[144:147]\n"
MF"v[152:155],v[212:215],a[60:63],v[152:155]\n"
BL"v238,s[20:23],s48 offen offset:0 lds\n"
MF"v[152:155],v[208:211],a[44:47],v[152:155]\n"
WT"lgkmcnt(2)\n"
MF"v[132:135],v[216:219],a[32:35],v[132:135]\n"
RD"v[204:207],v243 offset:34816\n"
MF"v[132:135],v[220:223],a[48:51],v[132:135]\n"
MF"v[140:143],v[220:223],a[52:55],v[140:143]\n"
MF"v[140:143],v[216:219],a[36:39],v[140:143]\n"
MF"v[148:151],v[216:219],a[40:43],v[148:151]\n"
RD"v[208:211],v242 offset:36864\n"
MF"v[148:151],v[220:223],a[56:59],v[148:151]\n"
MF"v[156:159],v[220:223],a[60:63],v[156:159]\n"
BL"v239,s[20:23],s48 offen offset:1024 lds\n"
MF"v[156:159],v[216:219],a[44:47],v[156:159]\n"
MF"v[128:131],v[192:195],a[64:67],v[128:131]\n"
WT"lgkmcnt(1)\n"
RD"v[212:215],v243 offset:36864\n"
MF"v[128:131],v[196:199],a[80:83],v[128:131]\n"
MF"v[136:139],v[196:199],a[84:87],v[136:139]\n"
MF"v[136:139],v[192:195],a[68:71],v[136:139]\n"
RD"v[216:219],v242 offset:38912\n"
MF"v[144:147],v[192:195],a[72:75],v[144:147]\n"
MF"v[144:147],v[196:199],a[88:91],v[144:147]\n"
MF"v[152:155],v[196:199],a[92:95],v[152:155]\n"
BL"v240,s[20:23],s48 offen offset:2048 lds\n"
MF"v[152:155],v[192:195],a[76:79],v[152:155]\n"
RD"v[220:223],v243 offset:38912\n"
MF"v[132:135],v[200:203],a[64:67],v[132:135]\n"
MF"v[132:135],v[204:207],a[80:83],v[132:135]\n"
BS"v[72:75],v250,s[8:11],s49 offen offset:1024 sc1\n"
MF"v[140:143],v[204:207],a[84:87],v[140:143]\n"
MF"v[140:143],v[200:203],a[68:71],v[140:143]\n"
RD"v[192:195],v244 offset:8192\n"
MF"v[148:151],v[200:203],a[72:75],v[148:151]\n"
MF"v[148:151],v[204:207],a[88:91],v[148:151]\n"
MF"v[156:159],v[204:207],a[92:95],v[156:159]\n"
SA"s61,s61,524288\n"
BL"v241,s[20:23],s48 offen offset:3072 lds\n"
MF"v[156:159],v[200:203],a[76:79],v[156:159]\n"
WT"lgkmcnt(1)\n"
RD"v[196:199],v245 offset:8192\n"
MF"v[128:131],v[208:211],a[96:99],v[128:131]\n"
MF"v[128:131],v[212:215],a[112:115],v[128:131]\n"
MF"v[136:139],v[212:215],a[116:119],v[136:139]\n"
MF"v[136:139],v[208:211],a[100:103],v[136:139]\n"
RD"v[200:203],v244 offset:10240\n"
MF"v[144:147],v[208:211],a[104:107],v[144:147]\n"
MF"v[144:147],v[212:215],a[120:123],v[144:147]\n"
MF"v[152:155],v[212:215],a[124:127],v[152:155]\n"
RD"v[204:207],v245 offset:10240\n"
MF"v[152:155],v[208:211],a[108:111],v[152:155]\n"
MF"v[132:135],v[216:219],a[96:99],v[132:135]\n"
MF"v[132:135],v[220:223],a[112:115],v[132:135]\n"
MF"v[140:143],v[220:223],a[116:119],v[140:143]\n"
RD"v[208:211],v244 offset:12288\n"
MF"v[140:143],v[216:219],a[100:103],v[140:143]\n"
MF"v[148:151],v[216:219],a[104:107],v[148:151]\n"
SA"m0,s38,65536\n"
SA"s48,s60,s61\n"
MF"v[148:151],v[220:223],a[120:123],v[148:151]\n"
MF"v[156:159],v[220:223],a[124:127],v[156:159]\n"
RD"v[212:215],v245 offset:12288\n"
MF"v[156:159],v[216:219],a[108:111],v[156:159]\n"
WT"lgkmcnt(2)\n"
BL"v238,s[20:23],s48 offen offset:0 lds\n"
MF"v[128:131],v[192:195],a[128:131],v[128:131]\n"
MF"v[128:131],v[196:199],a[144:147],v[128:131]\n"
MF"v[136:139],v[196:199],a[148:151],v[136:139]\n"
RD"v[216:219],v244 offset:14336\n"
MF"v[136:139],v[192:195],a[132:135],v[136:139]\n"
MF"v[144:147],v[192:195],a[136:139],v[144:147]\n"
MF"v[144:147],v[196:199],a[152:155],v[144:147]\n"
RD"v[220:223],v245 offset:14336\n"
MF"v[152:155],v[196:199],a[156:159],v[152:155]\n"
BL"v239,s[20:23],s48 offen offset:1024 lds\n"
MF"v[152:155],v[192:195],a[140:143],v[152:155]\n"
MF"v[132:135],v[200:203],a[128:131],v[132:135]\n"
BS"v[80:83],v250,s[8:11],s49 offen offset:2048 sc1\n"
MF"v[132:135],v[204:207],a[144:147],v[132:135]\n"
RD"v[192:195],v244 offset:32768\n"
MF"v[140:143],v[204:207],a[148:151],v[140:143]\n"
MF"v[140:143],v[200:203],a[132:135],v[140:143]\n"
MF"v[148:151],v[200:203],a[136:139],v[148:151]\n"
MF"v[148:151],v[204:207],a[152:155],v[148:151]\n"
RD"v[196:199],v245 offset:32768\n"
MF"v[156:159],v[204:207],a[156:159],v[156:159]\n"
BL"v240,s[20:23],s48 offen offset:2048 lds\n"
MF"v[156:159],v[200:203],a[140:143],v[156:159]\n"
WT"lgkmcnt(2)\n"
MF"v[128:131],v[208:211],a[160:163],v[128:131]\n"
MF"v[128:131],v[212:215],a[176:179],v[128:131]\n"
RD"v[200:203],v244 offset:34816\n"
MF"v[136:139],v[212:215],a[180:183],v[136:139]\n"
MF"v[136:139],v[208:211],a[164:167],v[136:139]\n"
MF"v[144:147],v[208:211],a[168:171],v[144:147]\n"
MF"v[144:147],v[212:215],a[184:187],v[144:147]\n"
RD"v[204:207],v245 offset:34816\n"
MF"v[152:155],v[212:215],a[188:191],v[152:155]\n"
SA"s61,s61,524288\n"
BL"v241,s[20:23],s48 offen offset:3072 lds\n"
MF"v[152:155],v[208:211],a[172:175],v[152:155]\n"
"s_cmp_eq_u32 s61,5242880\n"
"s_cselect_b32 s61,0,s61\n"
MF"v[132:135],v[216:219],a[160:163],v[132:135]\n"
RD"v[208:211],v244 offset:36864\n"
MF"v[132:135],v[220:223],a[176:179],v[132:135]\n"
MF"v[140:143],v[220:223],a[180:183],v[140:143]\n"
MF"v[140:143],v[216:219],a[164:167],v[140:143]\n"
MF"v[148:151],v[216:219],a[168:171],v[148:151]\n"
SA"m0,s38,114688\n"
RD"v[212:215],v245 offset:36864\n"
SA"s48,s60,s61\n"
MF"v[148:151],v[220:223],a[184:187],v[148:151]\n"
MF"v[156:159],v[220:223],a[188:191],v[156:159]\n"
BL"v238,s[20:23],s48 offen offset:0 lds\n"
MF"v[156:159],v[216:219],a[172:175],v[156:159]\n"
WT"lgkmcnt(1)\n"
MF"v[128:131],v[192:195],a[192:195],v[128:131]\n"
RD"v[216:219],v244 offset:38912\n"
MF"v[128:131],v[196:199],a[208:211],v[128:131]\n"
MF"v[136:139],v[196:199],a[212:215],v[136:139]\n"
MF"v[136:139],v[192:195],a[196:199],v[136:139]\n"
MF"v[144:147],v[192:195],a[200:203],v[144:147]\n"
RD"v[220:223],v245 offset:38912\n"
MF"v[144:147],v[196:199],a[216:219],v[144:147]\n"
MF"v[152:155],v[196:199],a[220:223],v[152:155]\n"
BL"v239,s[20:23],s48 offen offset:1024 lds\n"
MF"v[152:155],v[192:195],a[204:207],v[152:155]\n"
BS"v[88:91],v250,s[8:11],s49 offen offset:3072 sc1\n"
MF"v[132:135],v[200:203],a[192:195],v[132:135]\n"
WT"vmcnt(14)\n"
BS"v[100:103],v252,s[8:11],s92 offen sc1\n"
MF"v[132:135],v[204:207],a[208:211],v[132:135]\n"
MF"v[140:143],v[204:207],a[212:215],v[140:143]\n"
MF"v[140:143],v[200:203],a[196:199],v[140:143]\n"
MF"v[148:151],v[200:203],a[200:203],v[148:151]\n"
MF"v[148:151],v[204:207],a[216:219],v[148:151]\n"
WT"lgkmcnt(0)\n"
"s_barrier\n"
MF"v[156:159],v[204:207],a[220:223],v[156:159]\n"
RD"v[192:195],v242 offset:16384\n"
MF"v[156:159],v[200:203],a[204:207],v[156:159]\n"
BL"v240,s[20:23],s48 offen offset:2048 lds\n"
MF"v[128:131],v[208:211],a[224:227],v[128:131]\n"
MF"v[128:131],v[212:215],a[240:243],v[128:131]\n"
MF"v[136:139],v[212:215],a[244:247],v[136:139]\n"
RD"v[196:199],v243 offset:16384\n"
MF"v[136:139],v[208:211],a[228:231],v[136:139]\n"
MF"v[144:147],v[208:211],a[232:235],v[144:147]\n"
MF"v[144:147],v[212:215],a[248:251],v[144:147]\n"
MF"v[152:155],v[212:215],a[252:255],v[152:155]\n"
SA"s61,s61,524288\n"
RD"v[200:203],v242 offset:18432\n"
MF"v[152:155],v[208:211],a[236:239],v[152:155]\n"
BL"v241,s[20:23],s48 offen offset:3072 lds\n"
MF"v[132:135],v[216:219],a[224:227],v[132:135]\n"
"s_lshl_b32 s50,s34,3\n"
MF"v[132:135],v[220:223],a[240:243],v[132:135]\n"
SA"s51,s50,s30\n"
MF"v[140:143],v[220:223],a[244:247],v[140:143]\n"
"s_mul_i32 s51,s51,20480\n"
RD"v[204:207],v243 offset:18432\n"
MF"v[140:143],v[216:219],a[228:231],v[140:143]\n"
SA"s56,s51,524288\n"
MF"v[148:151],v[216:219],a[232:235],v[148:151]\n"
SA"m0,s36,16384\n"
SA"s48,s60,s61\n"
SA"s57,s56,81920\n"
MF"v[148:151],v[220:223],a[248:251],v[148:151]\n"
"s_sub_u32 s51,s30,s50\n"
MF"v[156:159],v[220:223],a[252:255],v[156:159]\n"
SA"s51,s51,120\n"
RD"v[208:211],v242 offset:20480\n"
MF"v[156:159],v[216:219],a[236:239],v[156:159]\n"
WT"lgkmcnt(2)\n"
BL"v238,s[20:23],s48 offen offset:0 lds\n"
MF"v[0:3],v[192:195],a[0:3],0\n"
"s_mul_i32 s51,s51,20480\n"
SA"s58,s51,524288\n"
"s_lshl_b32 s99,s99,12\n"
MF"v[0:3],v[196:199],a[16:19],v[0:3]\n"
SA"s59,s58,81920\n"
RD"v[212:215],v243 offset:20480\n"
MF"v[16:19],v[196:199],a[20:23],0\n"
"v_add_u32 v235,s99,v224\n"
MF"v[16:19],v[192:195],a[4:7],v[16:19]\n"
PK"v64,v128,v129\n"
PK"v65,v130,v131\n"
MF"v[32:35],v[192:195],a[8:11],0\n"
"s_mov_b32 s49,24576\n"
PK"v66,v132,v133\n"
PK"v67,v134,v135\n"
MF"v[32:35],v[196:199],a[24:27],v[32:35]\n"
RD"v[216:219],v242 offset:22528\n"
MF"v[48:51],v[196:199],a[28:31],0\n"
BS"v[96:99],v250,s[8:11],s49 offen offset:0 sc1\n"
MF"v[48:51],v[192:195],a[12:15],v[48:51]\n"
BL"v239,s[20:23],s48 offen offset:1024 lds\n"
MF"v[8:11],v[200:203],a[0:3],0\n"
WT"lgkmcnt(1)\n"
PK"v68,v136,v137\n"
PK"v69,v138,v139\n"
MF"v[8:11],v[204:207],a[16:19],v[8:11]\n"
RD"v[220:223],v243 offset:22528\n"
PK"v70,v140,v141\n"
MF"v[24:27],v[204:207],a[20:23],0\n"
PK"v71,v142,v143\n"
MF"v[24:27],v[200:203],a[4:7],v[24:27]\n"
MF"v[40:43],v[200:203],a[8:11],0\n"
MF"v[40:43],v[204:207],a[24:27],v[40:43]\n"
RD"v[192:195],v242 offset:40960\n"
PK"v72,v144,v145\n"
MF"v[56:59],v[204:207],a[28:31],0\n"
BL"v240,s[20:23],s48 offen offset:2048 lds\n"
MF"v[56:59],v[200:203],a[12:15],v[56:59]\n"
PK"v73,v146,v147\n"
PK"v74,v148,v149\n"
MF"v[0:3],v[208:211],a[32:35],v[0:3]\n"
BL"a[0:3],v235,s[8:11],s56 offen offset:0 sc1\n"
MF"v[0:3],v[212:215],a[48:51],v[0:3]\n"
RD"v[196:199],v243 offset:40960\n"
PK"v75,v150,v151\n"
MF"v[16:19],v[212:215],a[52:55],v[16:19]\n"
PK"v76,v152,v153\n"
PK"v77,v154,v155\n"
MF"v[16:19],v[208:211],a[36:39],v[16:19]\n"
PK"v78,v156,v157\n"
PK"v79,v158,v159\n"
MF"v[32:35],v[208:211],a[40:43],v[32:35]\n"
RD"v[200:203],v242 offset:43008\n"
MF"v[32:35],v[212:215],a[56:59],v[32:35]\n"
MF"v[48:51],v[212:215],a[60:63],v[48:51]\n"
SA"s61,s61,524288\n"
BL"v241,s[20:23],s48 offen offset:3072 lds\n"
MF"v[48:51],v[208:211],a[44:47],v[48:51]\n"
WT"lgkmcnt(2)\n"
BL"a[4:7],v235,s[8:11],s57 offen offset:0 sc1\n"
MF"v[8:11],v[216:219],a[32:35],v[8:11]\n"
"s_cmp_eq_u32 s61,5242880\n"
RD"v[204:207],v243 offset:43008\n"
"s_cselect_b32 s61,0,s61\n"
MF"v[8:11],v[220:223],a[48:51],v[8:11]\n"
MF"v[24:27],v[220:223],a[52:55],v[24:27]\n"
MF"v[24:27],v[216:219],a[36:39],v[24:27]\n"
MF"v[40:43],v[216:219],a[40:43],v[40:43]\n"
RD"v[208:211],v242 offset:45056\n"
MF"v[40:43],v[220:223],a[56:59],v[40:43]\n"
BL"a[8:11],v235,s[8:11],s58 offen offset:0 sc1\n"
MF"v[56:59],v[220:223],a[60:63],v[56:59]\n"
MF"v[56:59],v[216:219],a[44:47],v[56:59]\n"
MF"v[0:3],v[192:195],a[64:67],v[0:3]\n"
WT"lgkmcnt(1)\n"
RD"v[212:215],v243 offset:45056\n"
MF"v[0:3],v[196:199],a[80:83],v[0:3]\n"
MF"v[16:19],v[196:199],a[84:87],v[16:19]\n"
BL"a[12:15],v235,s[8:11],s59 offen offset:0 sc1\n"
MF"v[16:19],v[192:195],a[68:71],v[16:19]\n"
RD"v[216:219],v242 offset:47104\n"
MF"v[32:35],v[192:195],a[72:75],v[32:35]\n"
MF"v[32:35],v[196:199],a[88:91],v[32:35]\n"
BS"v[104:107],v250,s[8:11],s49 offen offset:1024 sc1\n"
MF"v[48:51],v[196:199],a[92:95],v[48:51]\n"
MF"v[48:51],v[192:195],a[76:79],v[48:51]\n"
RD"v[220:223],v243 offset:47104\n"
MF"v[8:11],v[200:203],a[64:67],v[8:11]\n"
BL"a[16:19],v235,s[8:11],s56 offen offset:1024 sc1\n"
MF"v[8:11],v[204:207],a[80:83],v[8:11]\n"
MF"v[24:27],v[204:207],a[84:87],v[24:27]\n"
MF"v[24:27],v[200:203],a[68:71],v[24:27]\n"
RD"v[192:195],v244 offset:16384\n"
MF"v[40:43],v[200:203],a[72:75],v[40:43]\n"
MF"v[40:43],v[204:207],a[88:91],v[40:43]\n"
MF"v[56:59],v[204:207],a[92:95],v[56:59]\n"
BL"a[20:23],v235,s[8:11],s57 offen offset:1024 sc1\n"
MF"v[56:59],v[200:203],a[76:79],v[56:59]\n"
WT"lgkmcnt(1)\n"
RD"v[196:199],v245 offset:16384\n"
MF"v[0:3],v[208:211],a[96:99],v[0:3]\n"
MF"v[0:3],v[212:215],a[112:115],v[0:3]\n"
MF"v[16:19],v[212:215],a[116:119],v[16:19]\n"
MF"v[16:19],v[208:211],a[100:103],v[16:19]\n"
RD"v[200:203],v244 offset:18432\n"
MF"v[32:35],v[208:211],a[104:107],v[32:35]\n"
BL"a[24:27],v235,s[8:11],s58 offen offset:1024 sc1\n"
MF"v[32:35],v[212:215],a[120:123],v[32:35]\n"
MF"v[48:51],v[212:215],a[124:127],v[48:51]\n"
RD"v[204:207],v245 offset:18432\n"
MF"v[48:51],v[208:211],a[108:111],v[48:51]\n"
MF"v[8:11],v[216:219],a[96:99],v[8:11]\n"
MF"v[8:11],v[220:223],a[112:115],v[8:11]\n"
MF"v[24:27],v[220:223],a[116:119],v[24:27]\n"
RD"v[208:211],v244 offset:20480\n"
MF"v[24:27],v[216:219],a[100:103],v[24:27]\n"
BL"a[28:31],v235,s[8:11],s59 offen offset:1024 sc1\n"
MF"v[40:43],v[216:219],a[104:107],v[40:43]\n"
MF"v[40:43],v[220:223],a[120:123],v[40:43]\n"
MF"v[56:59],v[220:223],a[124:127],v[56:59]\n"
RD"v[212:215],v245 offset:20480\n"
MF"v[56:59],v[216:219],a[108:111],v[56:59]\n"
WT"lgkmcnt(2)\n"
MF"v[0:3],v[192:195],a[128:131],v[0:3]\n"
BL"a[32:35],v235,s[8:11],s56 offen offset:2048 sc1\n"
MF"v[0:3],v[196:199],a[144:147],v[0:3]\n"
MF"v[16:19],v[196:199],a[148:151],v[16:19]\n"
RD"v[216:219],v244 offset:22528\n"
MF"v[16:19],v[192:195],a[132:135],v[16:19]\n"
MF"v[32:35],v[192:195],a[136:139],v[32:35]\n"
BS"v[112:115],v250,s[8:11],s49 offen offset:2048 sc1\n"
MF"v[32:35],v[196:199],a[152:155],v[32:35]\n"
RD"v[220:223],v245 offset:22528\n"
MF"v[48:51],v[196:199],a[156:159],v[48:51]\n"
BL"a[36:39],v235,s[8:11],s57 offen offset:2048 sc1\n"
MF"v[48:51],v[192:195],a[140:143],v[48:51]\n"
MF"v[8:11],v[200:203],a[128:131],v[8:11]\n"
MF"v[8:11],v[204:207],a[144:147],v[8:11]\n"
RD"v[192:195],v244 offset:40960\n"
MF"v[24:27],v[204:207],a[148:151],v[24:27]\n"
MF"v[24:27],v[200:203],a[132:135],v[24:27]\n"
MF"v[40:43],v[200:203],a[136:139],v[40:43]\n"
BL"a[40:43],v235,s[8:11],s58 offen offset:2048 sc1\n"
MF"v[40:43],v[204:207],a[152:155],v[40:43]\n"
RD"v[196:199],v245 offset:40960\n"
MF"v[56:59],v[204:207],a[156:159],v[56:59]\n"
MF"v[56:59],v[200:203],a[140:143],v[56:59]\n"
WT"lgkmcnt(2)\n"
MF"v[0:3],v[208:211],a[160:163],v[0:3]\n"
"s_sub_u32 s53,s37,65536\n"
MF"v[0:3],v[212:215],a[176:179],v[0:3]\n"
RD"v[200:203],v244 offset:43008\n"
MF"v[16:19],v[212:215],a[180:183],v[16:19]\n"
BL"a[44:47],v235,s[8:11],s59 offen offset:2048 sc1\n"
MF"v[16:19],v[208:211],a[164:167],v[16:19]\n"
"v_subrev_u32 v246,s53,v246\n"
"v_subrev_u32 v247,s53,v247\n"
MF"v[32:35],v[208:211],a[168:171],v[32:35]\n"
"v_subrev_u32 v248,s53,v248\n"
"v_subrev_u32 v249,s53,v249\n"
MF"v[32:35],v[212:215],a[184:187],v[32:35]\n"
RD"v[204:207],v245 offset:43008\n"
"v_subrev_u32 v236,65536,v246\n"
MF"v[48:51],v[212:215],a[188:191],v[48:51]\n"
"v_subrev_u32 v237,65536,v247\n"
"v_subrev_u32 v228,65536,v248\n"
MF"v[48:51],v[208:211],a[172:175],v[48:51]\n"
"v_subrev_u32 v229,65536,v249\n"
MF"v[8:11],v[216:219],a[160:163],v[8:11]\n"
RD"v[208:211],v244 offset:45056\n"
MF"v[8:11],v[220:223],a[176:179],v[8:11]\n"
BL"a[48:51],v235,s[8:11],s56 offen offset:3072 sc1\n"
MF"v[24:27],v[220:223],a[180:183],v[24:27]\n"
MF"v[24:27],v[216:219],a[164:167],v[24:27]\n"
MF"v[40:43],v[216:219],a[168:171],v[40:43]\n"
RD"v[212:215],v245 offset:45056\n"
MF"v[40:43],v[220:223],a[184:187],v[40:43]\n"
MF"v[56:59],v[220:223],a[188:191],v[56:59]\n"
BL"a[52:55],v235,s[8:11],s57 offen offset:3072 sc1\n"
MF"v[56:59],v[216:219],a[172:175],v[56:59]\n"
WT"lgkmcnt(1)\n"
MF"v[0:3],v[192:195],a[192:195],v[0:3]\n"
RD"v[216:219],v244 offset:47104\n"
MF"v[0:3],v[196:199],a[208:211],v[0:3]\n"
MF"v[16:19],v[196:199],a[212:215],v[16:19]\n"
MF"v[16:19],v[192:195],a[196:199],v[16:19]\n"
BS"v[120:123],v250,s[8:11],s49 offen offset:3072 sc1\n"
MF"v[32:35],v[192:195],a[200:203],v[32:35]\n"
RD"v[220:223],v245 offset:47104\n"
MF"v[32:35],v[196:199],a[216:219],v[32:35]\n"
BL"a[56:59],v235,s[8:11],s58 offen offset:3072 sc1\n"
MF"v[48:51],v[196:199],a[220:223],v[48:51]\n"
MF"v[48:51],v[192:195],a[204:207],v[48:51]\n"
MF"v[8:11],v[200:203],a[192:195],v[8:11]\n"
SA"s99,s99,4096\n"
MF"v[8:11],v[204:207],a[208:211],v[8:11]\n"
"s_cmp_eq_u32 s99,20480\n"
"s_cselect_b32 s99,0,s99\n"
MF"v[24:27],v[204:207],a[212:215],v[24:27]\n"
BL"a[60:63],v235,s[8:11],s59 offen offset:3072 sc1\n"
MF"v[24:27],v[200:203],a[196:199],v[24:27]\n"
MF"v[40:43],v[200:203],a[200:203],v[40:43]\n"
MF"v[40:43],v[204:207],a[216:219],v[40:43]\n"
WT"lgkmcnt(0)\n"
"s_barrier\n"
MF"v[56:59],v[204:207],a[220:223],v[56:59]\n"
RT"a[128:129],v236 offset:49152\n"
MF"v[56:59],v[200:203],a[204:207],v[56:59]\n"
"v_add_u32 v235,s99,v224\n"
MF"v[0:3],v[208:211],a[224:227],v[0:3]\n"
SA"m0,s38,73728\n"
RT"a[130:131],v236 offset:49664\n"
SA"s48,s60,s61\n"
MF"v[0:3],v[212:215],a[240:243],v[0:3]\n"
BL"a[64:67],v235,s[8:11],s56 offen offset:0 sc1\n"
MF"v[16:19],v[212:215],a[244:247],v[16:19]\n"
RT"a[132:133],v236 offset:57344\n"
"v_add_u32 v242,49152,v246\n"
MF"v[16:19],v[208:211],a[228:231],v[16:19]\n"
BL"v238,s[20:23],s48 offen offset:0 lds\n"
MF"v[32:35],v[208:211],a[232:235],v[32:35]\n"
RT"a[134:135],v236 offset:57856\n"
"v_add_u32 v243,49152,v247\n"
MF"v[32:35],v[212:215],a[248:251],v[32:35]\n"
"v_add_u32 v244,49152,v248\n"
"v_add_u32 v245,49152,v249\n"
MF"v[48:51],v[212:215],a[252:255],v[48:51]\n"
RT"a[136:137],v237 offset:49152\n"
MF"v[48:51],v[208:211],a[236:239],v[48:51]\n"
BL"a[68:71],v235,s[8:11],s57 offen offset:0 sc1\n"
MF"v[8:11],v[216:219],a[224:227],v[8:11]\n"
RT"a[138:139],v237 offset:49664\n"
MF"v[8:11],v[220:223],a[240:243],v[8:11]\n"
MF"v[24:27],v[220:223],a[244:247],v[24:27]\n"
RT"a[140:141],v237 offset:57344\n"
MF"v[24:27],v[216:219],a[228:231],v[24:27]\n"
BL"v239,s[20:23],s48 offen offset:1024 lds\n"
MF"v[40:43],v[216:219],a[232:235],v[40:43]\n"
RT"a[142:143],v237 offset:57856\n"
MF"v[40:43],v[220:223],a[248:251],v[40:43]\n"
BL"a[72:75],v235,s[8:11],s58 offen offset:0 sc1\n"
MF"v[56:59],v[220:223],a[252:255],v[56:59]\n"
RT"a[144:145],v228 offset:49152\n"
MF"v[56:59],v[216:219],a[236:239],v[56:59]\n"
WT"vmcnt(24) lgkmcnt(3)\n"
MF"v[80:83],a[128:131],a[0:3],0\n"
WT"vmcnt(18)\n"
RT"a[146:147],v228 offset:49664\n"
MF"v[80:83],a[132:135],a[16:19],v[80:83]\n"
WT"vmcnt(17)\n"
MF"v[88:91],a[132:135],a[20:23],0\n"
RT"a[148:149],v228 offset:57344\n"
MF"v[88:91],a[128:131],a[4:7],v[88:91]\n"
BL"v240,s[20:23],s48 offen offset:2048 lds\n"
MF"v[96:99],a[128:131],a[8:11],0\n"
WT"vmcnt(17)\n"
BL"a[76:79],v235,s[8:11],s59 offen offset:0 sc1\n"
MF"v[96:99],a[132:135],a[24:27],v[96:99]\n"
WT"vmcnt(17)\n"
RT"a[150:151],v228 offset:57856\n"
RT"a[152:153],v229 offset:49152\n"
MF"v[104:107],a[132:135],a[28:31],0\n"
PK"v0,v0,v1\n"
PK"v1,v2,v3\n"
MF"v[104:107],a[128:131],a[12:15],v[104:107]\n"
RT"a[154:155],v229 offset:49664\n"
PK"v2,v8,v9\n"
MF"v[84:87],a[136:139],a[0:3],0\n"
WT"lgkmcnt(4)\n"
PK"v3,v10,v11\n"
PK"v16,v16,v17\n"
MF"v[84:87],a[140:143],a[16:19],v[84:87]\n"
RT"a[156:157],v229 offset:57344\n"
PK"v17,v18,v19\n"
MF"v[92:95],a[140:143],a[20:23],0\n"
SA"s61,s61,524288\n"
BL"a[80:83],v235,s[8:11],s56 offen offset:1024 sc1\n"
MF"v[92:95],a[136:139],a[4:7],v[92:95]\n"
BL"v241,s[20:23],s48 offen offset:3072 lds\n"
MF"v[100:103],a[136:139],a[8:11],0\n"
RT"a[158:159],v229 offset:57856\n"
PK"v18,v24,v25\n"
MF"v[100:103],a[140:143],a[24:27],v[100:103]\n"
RT"a[160:161],v236 offset:53248\n"
PK"v19,v26,v27\n"
MF"v[108:111],a[140:143],a[28:31],0\n"
PK"v32,v32,v33\n"
PK"v33,v34,v35\n"
MF"v[108:111],a[136:139],a[12:15],v[108:111]\n"
RT"a[162:163],v236 offset:53760\n"
PK"v34,v40,v41\n"
MF"v[112:115],a[144:147],a[0:3],0\n"
WT"lgkmcnt(3)\n"
BL"a[84:87],v235,s[8:11],s57 offen offset:1024 sc1\n"
MF"v[112:115],a[148:151],a[16:19],v[112:115]\n"
RT"a[164:165],v236 offset:61440\n"
PK"v35,v42,v43\n"
MF"v[120:123],a[148:151],a[20:23],0\n"
PK"v48,v48,v49\n"
PK"v49,v50,v51\n"
MF"v[120:123],a[144:147],a[4:7],v[120:123]\n"
RT"a[166:167],v236 offset:61952\n"
PK"v50,v56,v57\n"
MF"v[128:131],a[144:147],a[8:11],0\n"
PK"v51,v58,v59\n"
MF"v[128:131],a[148:151],a[24:27],v[128:131]\n"
RT"a[168:169],v237 offset:53248\n"
MF"v[136:139],a[148:151],a[28:31],0\n"
MF"v[136:139],a[144:147],a[12:15],v[136:139]\n"
RT"a[170:171],v237 offset:53760\n"
MF"v[116:119],a[152:155],a[0:3],0\n"
WT"lgkmcnt(3)\n"
BL"a[88:91],v235,s[8:11],s58 offen offset:1024 sc1\n"
MF"v[116:119],a[156:159],a[16:19],v[116:119]\n"
"s_mov_b32 s49,262144\n"
RT"a[172:173],v237 offset:61440\n"
MF"v[124:127],a[156:159],a[20:23],0\n"
RT"a[174:175],v237 offset:61952\n"
MF"v[124:127],a[152:155],a[4:7],v[124:127]\n"
BS"v[4:7],v250,s[8:11],s49 offen offset:0 sc1\n"
MF"v[132:135],a[152:155],a[8:11],0\n"
RT"a[176:177],v228 offset:53248\n"
MF"v[132:135],a[156:159],a[24:27],v[132:135]\n"
MF"v[140:143],a[156:159],a[28:31],0\n"
RT"a[178:179],v228 offset:53760\n"
MF"v[140:143],a[152:155],a[12:15],v[140:143]\n"
BL"a[92:95],v235,s[8:11],s59 offen offset:1024 sc1\n"
MF"v[144:147],a[160:163],a[0:3],0\n"
WT"lgkmcnt(3)\n"
RT"a[180:181],v228 offset:61440\n"
MF"v[144:147],a[164:167],a[16:19],v[144:147]\n"
MF"v[152:155],a[164:167],a[20:23],0\n"
SA"m0,s38,122880\n"
RT"a[182:183],v228 offset:61952\n"
SA"s48,s60,s61\n"
MF"v[152:155],a[160:163],a[4:7],v[152:155]\n"
MF"v[160:163],a[160:163],a[8:11],0\n"
"s_mov_b32 s88,2048\n"
RT"a[184:185],v229 offset:53248\n"
MF"v[160:163],a[164:167],a[24:27],v[160:163]\n"
BL"v238,s[20:23],s48 offen offset:0 lds\n"
MF"v[168:171],a[164:167],a[28:31],0\n"
BL"a[96:99],v235,s[8:11],s56 offen offset:2048 sc1\n"
MF"v[168:171],a[160:163],a[12:15],v[168:171]\n"
RT"a[186:187],v229 offset:53760\n"
MF"v[148:151],a[168:171],a[0:3],0\n"
WT"lgkmcnt(3)\n"
RT"a[188:189],v229 offset:61440\n"
MF"v[148:151],a[172:175],a[16:19],v[148:151]\n"
MF"v[156:159],a[172:175],a[20:23],0\n"
RT"a[190:191],v229 offset:61952\n"
MF"v[156:159],a[168:171],a[4:7],v[156:159]\n"
RT"a[128:129],v236 offset:32768\n"
MF"v[164:167],a[168:171],a[8:11],0\n"
BL"v239,s[20:23],s48 offen offset:1024 lds\n"
MF"v[164:167],a[172:175],a[24:27],v[164:167]\n"
BL"a[100:103],v235,s[8:11],s57 offen offset:2048 sc1\n"
MF"v[172:175],a[172:175],a[28:31],0\n"
RT"a[130:131],v236 offset:33280\n"
MF"v[172:175],a[168:171],a[12:15],v[172:175]\n"
RT"a[132:133],v236 offset:40960\n"
MF"v[176:179],a[176:179],a[0:3],0\n"
WT"lgkmcnt(4)\n"
MF"v[176:179],a[180:183],a[16:19],v[176:179]\n"
RT"a[134:135],v236 offset:41472\n"
MF"v[184:187],a[180:183],a[20:23],0\n"
MF"v[184:187],a[176:179],a[4:7],v[184:187]\n"
RT"a[136:137],v237 offset:32768\n"
MF"v[192:195],a[176:179],a[8:11],0\n"
BL"a[104:107],v235,s[8:11],s58 offen offset:2048 sc1\n"
MF"v[192:195],a[180:183],a[24:27],v[192:195]\n"
BL"v240,s[20:23],s48 offen offset:2048 lds\n"
MF"v[200:203],a[180:183],a[28:31],0\n"
RT"a[138:139],v237 offset:33280\n"
MF"v[200:203],a[176:179],a[12:15],v[200:203]\n"
RT"a[140:141],v237 offset:40960\n"
MF"v[180:183],a[184:187],a[0:3],0\n"
WT"lgkmcnt(3)\n"
MF"v[180:183],a[188:191],a[16:19],v[180:183]\n"
RT"a[142:143],v237 offset:41472\n"
MF"v[188:191],a[188:191],a[20:23],0\n"
BL"a[108:111],v235,s[8:11],s59 offen offset:2048 sc1\n"
MF"v[188:191],a[184:187],a[4:7],v[188:191]\n"
RT"a[144:145],v228 offset:32768\n"
MF"v[196:199],a[184:187],a[8:11],0\n"
SA"s61,s61,524288\n"
BS"v[12:15],v250,s[8:11],s49 offen offset:1024 sc1\n"
MF"v[196:199],a[188:191],a[24:27],v[196:199]\n"
"s_cmp_eq_u32 s61,5242880\n"
BL"v241,s[20:23],s48 offen offset:3072 lds\n"
MF"v[204:207],a[188:191],a[28:31],0\n"
"s_cselect_b32 s61,0,s61\n"
RT"a[146:147],v228 offset:33280\n"
MF"v[204:207],a[184:187],a[12:15],v[204:207]\n"
WT"vmcnt(31)\n"
RT"a[148:149],v228 offset:40960\n"
MF"v[80:83],a[128:131],a[32:35],v[80:83]\n"
WT"vmcnt(26)\n"
MF"v[80:83],a[132:135],a[48:51],v[80:83]\n"
WT"vmcnt(25)\n"
RT"a[150:151],v228 offset:41472\n"
MF"v[88:91],a[132:135],a[52:55],v[88:91]\n"
BL"a[112:115],v235,s[8:11],s56 offen offset:3072 sc1\n"
MF"v[88:91],a[128:131],a[36:39],v[88:91]\n"
RT"a[152:153],v229 offset:32768\n"
MF"v[96:99],a[128:131],a[40:43],v[96:99]\n"
WT"vmcnt(24)\n"
RT"a[154:155],v229 offset:33280\n"
MF"v[96:99],a[132:135],a[56:59],v[96:99]\n"
WT"vmcnt(23)\n"
MF"v[104:107],a[132:135],a[60:63],v[104:107]\n"
RT"a[156:157],v229 offset:40960\n"
MF"v[104:107],a[128:131],a[44:47],v[104:107]\n"
WT"lgkmcnt(4)\n"
MF"v[84:87],a[136:139],a[32:35],v[84:87]\n"
RT"a[158:159],v229 offset:41472\n"
MF"v[84:87],a[140:143],a[48:51],v[84:87]\n"
BL"a[116:119],v235,s[8:11],s57 offen offset:3072 sc1\n"
MF"v[92:95],a[140:143],a[52:55],v[92:95]\n"
RT"a[160:161],v236 offset:36864\n"
MF"v[92:95],a[136:139],a[36:39],v[92:95]\n"
MF"v[100:103],a[136:139],a[40:43],v[100:103]\n"
RT"a[162:163],v236 offset:37376\n"
MF"v[100:103],a[140:143],a[56:59],v[100:103]\n"
MF"v[108:111],a[140:143],a[60:63],v[108:111]\n"
RT"a[164:165],v236 offset:45056\n"
MF"v[108:111],a[136:139],a[44:47],v[108:111]\n"
BL"a[120:123],v235,s[8:11],s58 offen offset:3072 sc1\n"
MF"v[112:115],a[144:147],a[32:35],v[112:115]\n"
WT"lgkmcnt(3)\n"
RT"a[166:167],v236 offset:45568\n"
MF"v[112:115],a[148:151],a[48:51],v[112:115]\n"
MF"v[120:123],a[148:151],a[52:55],v[120:123]\n"
RT"a[168:169],v237 offset:36864\n"
MF"v[120:123],a[144:147],a[36:39],v[120:123]\n"
MF"v[128:131],a[144:147],a[40:43],v[128:131]\n"
RT"a[170:171],v237 offset:37376\n"
MF"v[128:131],a[148:151],a[56:59],v[128:131]\n"
SA"s99,s99,4096\n"
RT"a[172:173],v237 offset:45056\n"
MF"v[136:139],a[148:151],a[60:63],v[136:139]\n"
"s_cmp_eq_u32 s99,20480\n"
BL"a[124:127],v235,s[8:11],s59 offen offset:3072 sc1\n"
MF"v[136:139],a[144:147],a[44:47],v[136:139]\n"
"s_cselect_b32 s99,0,s99\n"
RT"a[174:175],v237 offset:45568\n"
MF"v[116:119],a[152:155],a[32:35],v[116:119]\n"
MF"v[116:119],a[156:159],a[48:51],v[116:119]\n"
RT"a[176:177],v228 offset:36864\n"
MF"v[124:127],a[156:159],a[52:55],v[124:127]\n"
MF"v[124:127],a[152:155],a[36:39],v[124:127]\n"
RT"a[178:179],v228 offset:37376\n"
MF"v[132:135],a[152:155],a[40:43],v[132:135]\n"
"v_add_u32 v235,s99,v224\n"
MF"v[132:135],a[156:159],a[56:59],v[132:135]\n"
RT"a[180:181],v228 offset:45056\n"
MF"v[140:143],a[156:159],a[60:63],v[140:143]\n"
BL"a[0:3],v235,s[8:11],s56 offen offset:0 sc1\n"
MF"v[140:143],a[152:155],a[44:47],v[140:143]\n"
WT"lgkmcnt(3)\n"
BS"v[20:23],v250,s[8:11],s49 offen offset:2048 sc1\n"
MF"v[144:147],a[160:163],a[32:35],v[144:147]\n"
RT"a[182:183],v228 offset:45568\n"
MF"v[144:147],a[164:167],a[48:51],v[144:147]\n"
RT"a[184:185],v229 offset:36864\n"
MF"v[152:155],a[164:167],a[52:55],v[152:155]\n"
MF"v[152:155],a[160:163],a[36:39],v[152:155]\n"
RT"a[186:187],v229 offset:37376\n"
MF"v[160:163],a[160:163],a[40:43],v[160:163]\n"
BL"a[4:7],v235,s[8:11],s57 offen offset:0 sc1\n"
MF"v[160:163],a[164:167],a[56:59],v[160:163]\n"
RT"a[188:189],v229 offset:45056\n"
MF"v[168:171],a[164:167],a[60:63],v[168:171]\n"
MF"v[168:171],a[160:163],a[44:47],v[168:171]\n"
RT"a[190:191],v229 offset:45568\n"
MF"v[148:151],a[168:171],a[32:35],v[148:151]\n"
"s_lshl_b32 s50,s35,3\n"
SA"s50,s50,s34\n"
"s_lshl_b32 s50,s50,3\n"
MF"v[148:151],a[172:175],a[48:51],v[148:151]\n"
SA"s50,s50,2304\n"
SA"s51,s50,s45\n"
MF"v[156:159],a[172:175],a[52:55],v[156:159]\n"
MF"v[156:159],a[168:171],a[36:39],v[156:159]\n"
BL"a[8:11],v235,s[8:11],s58 offen offset:0 sc1\n"
MF"v[164:167],a[168:171],a[40:43],v[164:167]\n"
MF"v[164:167],a[172:175],a[56:59],v[164:167]\n"
WT"lgkmcnt(0)\n"
"s_barrier\n"
MF"v[172:175],a[172:175],a[60:63],v[172:175]\n"
RT"a[128:129],v236 offset:0\n"
MF"v[172:175],a[168:171],a[44:47],v[172:175]\n"
"buffer_store_dwordx2 v[232:233],v230,s[8:11],s51 offen sc1\n"
MF"v[176:179],a[176:179],a[32:35],v[176:179]\n"
RT"a[130:131],v236 offset:512\n"
MF"v[176:179],a[180:183],a[48:51],v[176:179]\n"
MF"v[184:187],a[180:183],a[52:55],v[184:187]\n"
SA"m0,s38,81920\n"
RT"a[132:133],v236 offset:8192\n"
SA"s48,s60,s61\n"
MF"v[184:187],a[176:179],a[36:39],v[184:187]\n"
BL"a[12:15],v235,s[8:11],s59 offen offset:0 sc1\n"
MF"v[192:195],a[176:179],a[40:43],v[192:195]\n"
RT"a[134:135],v236 offset:8704\n"
MF"v[192:195],a[180:183],a[56:59],v[192:195]\n"
BL"v238,s[20:23],s48 offen offset:0 lds\n"
MF"v[200:203],a[180:183],a[60:63],v[200:203]\n"
RT"a[136:137],v237 offset:0\n"
MF"v[200:203],a[176:179],a[44:47],v[200:203]\n"
MF"v[180:183],a[184:187],a[32:35],v[180:183]\n"
RT"a[138:139],v237 offset:512\n"
MF"v[180:183],a[188:191],a[48:51],v[180:183]\n"
BL"a[16:19],v235,s[8:11],s56 offen offset:1024 sc1\n"
MF"v[188:191],a[188:191],a[52:55],v[188:191]\n"
RT"a[140:141],v237 offset:8192\n"
MF"v[188:191],a[184:187],a[36:39],v[188:191]\n"
MF"v[196:199],a[184:187],a[40:43],v[196:199]\n"
RT"a[142:143],v237 offset:8704\n"
MF"v[196:199],a[188:191],a[56:59],v[196:199]\n"
BL"v239,s[20:23],s48 offen offset:1024 lds\n"
MF"v[204:207],a[188:191],a[60:63],v[204:207]\n"
SA"s49,s88,262144\n"
RT"a[144:145],v228 offset:0\n"
MF"v[204:207],a[184:187],a[44:47],v[204:207]\n"
"LQ_%=:\n"
WT"vmcnt(34) lgkmcnt(7)\n"
MF"v[80:83],a[128:131],a[64:67],v[80:83]\n"
WT"vmcnt(27) lgkmcnt(3)\n"
RT"a[146:147],v228 offset:512\n"
MF"v[80:83],a[132:135],a[80:83],v[80:83]\n"
WT"vmcnt(25)\n"
BS"v[28:31],v250,s[8:11],s49 offen offset:1024 sc1\n"
MF"v[88:91],a[132:135],a[84:87],v[88:91]\n"
BL"a[20:23],v235,s[8:11],s57 offen offset:1024 sc1\n"
MF"v[88:91],a[128:131],a[68:71],v[88:91]\n"
RT"a[148:149],v228 offset:8192\n"
MF"v[96:99],a[128:131],a[72:75],v[96:99]\n"
WT"vmcnt(26)\n"
RT"a[150:151],v228 offset:8704\n"
MF"v[96:99],a[132:135],a[88:91],v[96:99]\n"
WT"vmcnt(24)\n"
BL"v240,s[20:23],s48 offen offset:2048 lds\n"
MF"v[104:107],a[132:135],a[92:95],v[104:107]\n"
RT"a[152:153],v229 offset:0\n"
MF"v[104:107],a[128:131],a[76:79],v[104:107]\n"
RT"a[154:155],v229 offset:512\n"
MF"v[84:87],a[136:139],a[64:67],v[84:87]\n"
WT"lgkmcnt(4)\n"
BL"a[24:27],v235,s[8:11],s58 offen offset:1024 sc1\n"
MF"v[84:87],a[140:143],a[80:83],v[84:87]\n"
RT"a[156:157],v229 offset:8192\n"
MF"v[92:95],a[140:143],a[84:87],v[92:95]\n"
MF"v[92:95],a[136:139],a[68:71],v[92:95]\n"
RT"a[158:159],v229 offset:8704\n"
MF"v[100:103],a[136:139],a[72:75],v[100:103]\n"
SA"s61,s61,524288\n"
BL"v241,s[20:23],s48 offen offset:3072 lds\n"
MF"v[100:103],a[140:143],a[88:91],v[100:103]\n"
RT"a[160:161],v236 offset:4096\n"
MF"v[108:111],a[140:143],a[92:95],v[108:111]\n"
BL"a[28:31],v235,s[8:11],s59 offen offset:1024 sc1\n"
MF"v[108:111],a[136:139],a[76:79],v[108:111]\n"
RT"a[162:163],v236 offset:4608\n"
MF"v[112:115],a[144:147],a[64:67],v[112:115]\n"
WT"lgkmcnt(3)\n"
MF"v[112:115],a[148:151],a[80:83],v[112:115]\n"
RT"a[164:165],v236 offset:12288\n"
MF"v[120:123],a[148:151],a[84:87],v[120:123]\n"
MF"v[120:123],a[144:147],a[68:71],v[120:123]\n"
RT"a[166:167],v236 offset:12800\n"
MF"v[128:131],a[144:147],a[72:75],v[128:131]\n"
MF"v[128:131],a[148:151],a[88:91],v[128:131]\n"
RT"a[168:169],v237 offset:4096\n"
MF"v[136:139],a[148:151],a[92:95],v[136:139]\n"
BL"a[32:35],v235,s[8:11],s56 offen offset:2048 sc1\n"
MF"v[136:139],a[144:147],a[76:79],v[136:139]\n"
RT"a[170:171],v237 offset:4608\n"
MF"v[116:119],a[152:155],a[64:67],v[116:119]\n"
WT"lgkmcnt(3)\n"
RT"a[172:173],v237 offset:12288\n"
MF"v[116:119],a[156:159],a[80:83],v[116:119]\n"
MF"v[124:127],a[156:159],a[84:87],v[124:127]\n"
RT"a[174:175],v237 offset:12800\n"
MF"v[124:127],a[152:155],a[68:71],v[124:127]\n"
MF"v[132:135],a[152:155],a[72:75],v[132:135]\n"
RT"a[176:177],v228 offset:4096\n"
MF"v[132:135],a[156:159],a[88:91],v[132:135]\n"
BL"a[36:39],v235,s[8:11],s57 offen offset:2048 sc1\n"
MF"v[140:143],a[156:159],a[92:95],v[140:143]\n"
RT"a[178:179],v228 offset:4608\n"
MF"v[140:143],a[152:155],a[76:79],v[140:143]\n"
MF"v[144:147],a[160:163],a[64:67],v[144:147]\n"
WT"lgkmcnt(3)\n"
RT"a[180:181],v228 offset:12288\n"
"s_mov_b32 s49,286720\n"
MF"v[144:147],a[164:167],a[80:83],v[144:147]\n"
MF"v[152:155],a[164:167],a[84:87],v[152:155]\n"
RT"a[182:183],v228 offset:12800\n"
MF"v[152:155],a[160:163],a[68:71],v[152:155]\n"
BS"v[0:3],v250,s[8:11],s49 offen offset:0 sc1\n"
MF"v[160:163],a[160:163],a[72:75],v[160:163]\n"
BL"a[40:43],v235,s[8:11],s58 offen offset:2048 sc1\n"
MF"v[160:163],a[164:167],a[88:91],v[160:163]\n"
RT"a[184:185],v229 offset:4096\n"
MF"v[168:171],a[164:167],a[92:95],v[168:171]\n"
RT"a[186:187],v229 offset:4608\n"
MF"v[168:171],a[160:163],a[76:79],v[168:171]\n"
MF"v[148:151],a[168:171],a[64:67],v[148:151]\n"
WT"lgkmcnt(3)\n"
RT"a[188:189],v229 offset:12288\n"
MF"v[148:151],a[172:175],a[80:83],v[148:151]\n"
MF"v[156:159],a[172:175],a[84:87],v[156:159]\n"
RT"a[190:191],v229 offset:12800\n"
MF"v[156:159],a[168:171],a[68:71],v[156:159]\n"
BL"a[44:47],v235,s[8:11],s59 offen offset:2048 sc1\n"
MF"v[164:167],a[168:171],a[72:75],v[164:167]\n"
RT"a[128:129],v246 offset:0\n"
MF"v[164:167],a[172:175],a[88:91],v[164:167]\n"
RT"a[130:131],v246 offset:512\n"
MF"v[172:175],a[172:175],a[92:95],v[172:175]\n"
SA"m0,s38,131072\n"
SA"s48,s60,s61\n"
MF"v[172:175],a[168:171],a[76:79],v[172:175]\n"
RT"a[132:133],v246 offset:24576\n"
MF"v[176:179],a[176:179],a[64:67],v[176:179]\n"
WT"lgkmcnt(4)\n"
BL"v238,s[20:23],s48 offen offset:0 lds\n"
MF"v[176:179],a[180:183],a[80:83],v[176:179]\n"
RT"a[134:135],v246 offset:25088\n"
MF"v[184:187],a[180:183],a[84:87],v[184:187]\n"
BL"a[48:51],v235,s[8:11],s56 offen offset:3072 sc1\n"
MF"v[184:187],a[176:179],a[68:71],v[184:187]\n"
RT"a[136:137],v247 offset:0\n"
MF"v[192:195],a[176:179],a[72:75],v[192:195]\n"
MF"v[192:195],a[180:183],a[88:91],v[192:195]\n"
RT"a[138:139],v247 offset:512\n"
MF"v[200:203],a[180:183],a[92:95],v[200:203]\n"
MF"v[200:203],a[176:179],a[76:79],v[200:203]\n"
RT"a[140:141],v247 offset:24576\n"
MF"v[180:183],a[184:187],a[64:67],v[180:183]\n"
WT"lgkmcnt(3)\n"
BL"v239,s[20:23],s48 offen offset:1024 lds\n"
MF"v[180:183],a[188:191],a[80:83],v[180:183]\n"
BL"a[52:55],v235,s[8:11],s57 offen offset:3072 sc1\n"
MF"v[188:191],a[188:191],a[84:87],v[188:191]\n"
RT"a[142:143],v247 offset:25088\n"
MF"v[188:191],a[184:187],a[68:71],v[188:191]\n"
RT"a[144:145],v248 offset:0\n"
MF"v[196:199],a[184:187],a[72:75],v[196:199]\n"
MF"v[196:199],a[188:191],a[88:91],v[196:199]\n"
RT"a[146:147],v248 offset:512\n"
MF"v[204:207],a[188:191],a[92:95],v[204:207]\n"
MF"v[204:207],a[184:187],a[76:79],v[204:207]\n"
WT"vmcnt(35)\n"
RT"a[148:149],v248 offset:24576\n"
MF"v[80:83],a[128:131],a[96:99],v[80:83]\n"
WT"vmcnt(27)\n"
BL"a[56:59],v235,s[8:11],s58 offen offset:3072 sc1\n"
MF"v[80:83],a[132:135],a[112:115],v[80:83]\n"
WT"vmcnt(27)\n"
BL"v240,s[20:23],s48 offen offset:2048 lds\n"
MF"v[88:91],a[132:135],a[116:119],v[88:91]\n"
RT"a[150:151],v248 offset:25088\n"
RT"a[152:153],v249 offset:0\n"
MF"v[88:91],a[128:131],a[100:103],v[88:91]\n"
MF"v[96:99],a[128:131],a[104:107],v[96:99]\n"
WT"vmcnt(27)\n"
RT"a[154:155],v249 offset:512\n"
MF"v[96:99],a[132:135],a[120:123],v[96:99]\n"
WT"vmcnt(26)\n"
BS"v[16:19],v250,s[8:11],s49 offen offset:1024 sc1\n"
MF"v[104:107],a[132:135],a[124:127],v[104:107]\n"
SA"s99,s99,4096\n"
RT"a[156:157],v249 offset:24576\n"
"s_cmp_eq_u32 s99,20480\n"
MF"v[104:107],a[128:131],a[108:111],v[104:107]\n"
WT"lgkmcnt(4)\n"
BL"a[60:63],v235,s[8:11],s59 offen offset:3072 sc1\n"
MF"v[84:87],a[136:139],a[96:99],v[84:87]\n"
"s_cselect_b32 s99,0,s99\n"
RT"a[158:159],v249 offset:25088\n"
SA"s61,s61,524288\n"
MF"v[84:87],a[140:143],a[112:115],v[84:87]\n"
"s_cmp_eq_u32 s61,5242880\n"
BL"v241,s[20:23],s48 offen offset:3072 lds\n"
MF"v[92:95],a[140:143],a[116:119],v[92:95]\n"
"s_cselect_b32 s61,0,s61\n"
RT"a[160:161],v246 offset:4096\n"
MF"v[92:95],a[136:139],a[100:103],v[92:95]\n"
MF"v[100:103],a[136:139],a[104:107],v[100:103]\n"
RT"a[162:163],v246 offset:4608\n"
"v_add_u32 v235,s99,v224\n"
MF"v[100:103],a[140:143],a[120:123],v[100:103]\n"
BL"a[64:67],v235,s[8:11],s56 offen offset:0 sc1\n"
MF"v[108:111],a[140:143],a[124:127],v[108:111]\n"
RT"a[164:165],v246 offset:28672\n"
MF"v[108:111],a[136:139],a[108:111],v[108:111]\n"
MF"v[112:115],a[144:147],a[96:99],v[112:115]\n"
WT"lgkmcnt(3)\n"
RT"a[166:167],v246 offset:29184\n"
"v_add_u32 v225,s36,v224\n"
MF"v[112:115],a[148:151],a[112:115],v[112:115]\n"
"s_lshl_b32 s39,s35,20\n"
SA"s39,s39,9175040\n"
MF"v[120:123],a[148:151],a[116:119],v[120:123]\n"
RT"a[168:169],v247 offset:4096\n"
MF"v[120:123],a[144:147],a[100:103],v[120:123]\n"
MF"v[128:131],a[144:147],a[104:107],v[128:131]\n"
RT"a[170:171],v247 offset:4608\n"
MF"v[128:131],a[148:151],a[120:123],v[128:131]\n"
BL"a[68:71],v235,s[8:11],s57 offen offset:0 sc1\n"
MF"v[136:139],a[148:151],a[124:127],v[136:139]\n"
RT"a[172:173],v247 offset:28672\n"
MF"v[136:139],a[144:147],a[108:111],v[136:139]\n"
RT"a[174:175],v247 offset:29184\n"
MF"v[116:119],a[152:155],a[96:99],v[116:119]\n"
MF"v[116:119],a[156:159],a[112:115],v[116:119]\n"
RT"a[176:177],v248 offset:4096\n"
MF"v[124:127],a[156:159],a[116:119],v[124:127]\n"
MF"v[124:127],a[152:155],a[100:103],v[124:127]\n"
RT"a[178:179],v248 offset:4608\n"
MF"v[132:135],a[152:155],a[104:107],v[132:135]\n"
BL"a[72:75],v235,s[8:11],s58 offen offset:0 sc1\n"
MF"v[132:135],a[156:159],a[120:123],v[132:135]\n"
RT"a[180:181],v248 offset:28672\n"
MF"v[140:143],a[156:159],a[124:127],v[140:143]\n"
MF"v[140:143],a[152:155],a[108:111],v[140:143]\n"
WT"lgkmcnt(3)\n"
RT"a[182:183],v248 offset:29184\n"
MF"v[144:147],a[160:163],a[96:99],v[144:147]\n"
MF"v[144:147],a[164:167],a[112:115],v[144:147]\n"
RT"a[184:185],v249 offset:4096\n"
MF"v[152:155],a[164:167],a[116:119],v[152:155]\n"
BL"a[76:79],v235,s[8:11],s59 offen offset:0 sc1\n"
MF"v[152:155],a[160:163],a[100:103],v[152:155]\n"
RT"a[186:187],v249 offset:4608\n"
MF"v[160:163],a[160:163],a[104:107],v[160:163]\n"
SA"s49,s88,286720\n"
MF"v[160:163],a[164:167],a[120:123],v[160:163]\n"
RT"a[188:189],v249 offset:28672\n"
MF"v[168:171],a[164:167],a[124:127],v[168:171]\n"
BS"v[32:35],v250,s[8:11],s49 offen offset:0 sc1\n"
MF"v[168:171],a[160:163],a[108:111],v[168:171]\n"
RT"a[190:191],v249 offset:29184\n"
MF"v[148:151],a[168:171],a[96:99],v[148:151]\n"
MF"v[148:151],a[172:175],a[112:115],v[148:151]\n"
BL"a[80:83],v235,s[8:11],s56 offen offset:1024 sc1\n"
MF"v[156:159],a[172:175],a[116:119],v[156:159]\n"
MF"v[156:159],a[168:171],a[100:103],v[156:159]\n"
MF"v[164:167],a[168:171],a[104:107],v[164:167]\n"
MF"v[164:167],a[172:175],a[120:123],v[164:167]\n"
"s_barrier\n"
MF"v[172:175],a[172:175],a[124:127],v[172:175]\n"
RT"a[128:129],v242 offset:0\n"
MF"v[172:175],a[168:171],a[108:111],v[172:175]\n"
WT"lgkmcnt(1)\n"
MF"v[176:179],a[176:179],a[96:99],v[176:179]\n"
RT"a[130:131],v242 offset:512\n"
MF"v[176:179],a[180:183],a[112:115],v[176:179]\n"
BL"a[84:87],v235,s[8:11],s57 offen offset:1024 sc1\n"
MF"v[184:187],a[180:183],a[116:119],v[184:187]\n"
RT"a[132:133],v242 offset:24576\n"
MF"v[184:187],a[176:179],a[100:103],v[184:187]\n"
MF"v[192:195],a[176:179],a[104:107],v[192:195]\n"
RT"a[134:135],v242 offset:25088\n"
MF"v[192:195],a[180:183],a[120:123],v[192:195]\n"
MF"v[200:203],a[180:183],a[124:127],v[200:203]\n"
RT"a[136:137],v243 offset:0\n"
MF"v[200:203],a[176:179],a[108:111],v[200:203]\n"
BL"a[88:91],v235,s[8:11],s58 offen offset:1024 sc1\n"
MF"v[180:183],a[184:187],a[96:99],v[180:183]\n"
RT"a[138:139],v243 offset:512\n"
MF"v[180:183],a[188:191],a[112:115],v[180:183]\n"
MF"v[188:191],a[188:191],a[116:119],v[188:191]\n"
RT"a[140:141],v243 offset:24576\n"
MF"v[188:191],a[184:187],a[100:103],v[188:191]\n"
MF"v[196:199],a[184:187],a[104:107],v[196:199]\n"
RT"a[142:143],v243 offset:25088\n"
MF"v[196:199],a[188:191],a[120:123],v[196:199]\n"
MF"v[204:207],a[188:191],a[124:127],v[204:207]\n"
RT"a[144:145],v244 offset:0\n"
MF"v[204:207],a[184:187],a[108:111],v[204:207]\n"
WT"vmcnt(36) lgkmcnt(3)\n"
BL"a[92:95],v235,s[8:11],s59 offen offset:1024 sc1\n"
MF"v[80:83],a[128:131],a[0:3],v[80:83]\n"
WT"vmcnt(30)\n"
RT"a[146:147],v244 offset:512\n"
MF"v[80:83],a[132:135],a[16:19],v[80:83]\n"
WT"vmcnt(27)\n"
MF"v[88:91],a[132:135],a[20:23],v[88:91]\n"
RT"a[148:149],v244 offset:24576\n"
MF"v[88:91],a[128:131],a[4:7],v[88:91]\n"
MF"v[96:99],a[128:131],a[8:11],v[96:99]\n"
WT"vmcnt(25)\n"
RT"a[150:151],v244 offset:25088\n"
MF"v[96:99],a[132:135],a[24:27],v[96:99]\n"
WT"vmcnt(23)\n"
RT"a[152:153],v245 offset:0\n"
MF"v[104:107],a[132:135],a[28:31],v[104:107]\n"
BL"a[96:99],v235,s[8:11],s56 offen offset:2048 sc1\n"
MF"v[104:107],a[128:131],a[12:15],v[104:107]\n"
RT"a[154:155],v245 offset:512\n"
MF"v[84:87],a[136:139],a[0:3],v[84:87]\n"
WT"lgkmcnt(4)\n"
BS"v[48:51],v250,s[8:11],s49 offen offset:1024 sc1\n"
MF"v[84:87],a[140:143],a[16:19],v[84:87]\n"
RT"a[156:157],v245 offset:24576\n"
MF"v[92:95],a[140:143],a[20:23],v[92:95]\n"
MF"v[92:95],a[136:139],a[4:7],v[92:95]\n"
RT"a[158:159],v245 offset:25088\n"
MF"v[100:103],a[136:139],a[8:11],v[100:103]\n"
BL"a[100:103],v235,s[8:11],s57 offen offset:2048 sc1\n"
MF"v[100:103],a[140:143],a[24:27],v[100:103]\n"
RT"a[160:161],v242 offset:4096\n"
MF"v[108:111],a[140:143],a[28:31],v[108:111]\n"
MF"v[108:111],a[136:139],a[12:15],v[108:111]\n"
RT"a[162:163],v242 offset:4608\n"
MF"v[112:115],a[144:147],a[0:3],v[112:115]\n"
WT"lgkmcnt(3)\n"
MF"v[112:115],a[148:151],a[16:19],v[112:115]\n"
RT"a[164:165],v242 offset:28672\n"
MF"v[120:123],a[148:151],a[20:23],v[120:123]\n"
MF"v[120:123],a[144:147],a[4:7],v[120:123]\n"
RT"a[166:167],v242 offset:29184\n"
MF"v[128:131],a[144:147],a[8:11],v[128:131]\n"
BL"a[104:107],v235,s[8:11],s58 offen offset:2048 sc1\n"
MF"v[128:131],a[148:151],a[24:27],v[128:131]\n"
RT"a[168:169],v243 offset:4096\n"
MF"v[136:139],a[148:151],a[28:31],v[136:139]\n"
MF"v[136:139],a[144:147],a[12:15],v[136:139]\n"
RT"a[170:171],v243 offset:4608\n"
MF"v[116:119],a[152:155],a[0:3],v[116:119]\n"
WT"lgkmcnt(3)\n"
RT"a[172:173],v243 offset:28672\n"
MF"v[116:119],a[156:159],a[16:19],v[116:119]\n"
MF"v[124:127],a[156:159],a[20:23],v[124:127]\n"
RT"a[174:175],v243 offset:29184\n"
MF"v[124:127],a[152:155],a[4:7],v[124:127]\n"
BL"a[108:111],v235,s[8:11],s59 offen offset:2048 sc1\n"
MF"v[132:135],a[152:155],a[8:11],v[132:135]\n"
RT"a[176:177],v244 offset:4096\n"
MF"v[132:135],a[156:159],a[24:27],v[132:135]\n"
MF"v[140:143],a[156:159],a[28:31],v[140:143]\n"
RT"a[178:179],v244 offset:4608\n"
MF"v[140:143],a[152:155],a[12:15],v[140:143]\n"
MF"v[144:147],a[160:163],a[0:3],v[144:147]\n"
WT"lgkmcnt(3)\n"
RT"a[180:181],v244 offset:28672\n"
MF"v[144:147],a[164:167],a[16:19],v[144:147]\n"
BL"a[112:115],v235,s[8:11],s56 offen offset:3072 sc1\n"
MF"v[152:155],a[164:167],a[20:23],v[152:155]\n"
RT"a[182:183],v244 offset:29184\n"
MF"v[152:155],a[160:163],a[4:7],v[152:155]\n"
MF"v[160:163],a[160:163],a[8:11],v[160:163]\n"
RT"a[184:185],v245 offset:4096\n"
MF"v[160:163],a[164:167],a[24:27],v[160:163]\n"
MF"v[168:171],a[164:167],a[28:31],v[168:171]\n"
RT"a[186:187],v245 offset:4608\n"
MF"v[168:171],a[160:163],a[12:15],v[168:171]\n"
MF"v[148:151],a[168:171],a[0:3],v[148:151]\n"
WT"lgkmcnt(3)\n"
RT"a[188:189],v245 offset:28672\n"
"s_mov_b32 s49,270336\n"
MF"v[148:151],a[172:175],a[16:19],v[148:151]\n"
BL"a[116:119],v235,s[8:11],s57 offen offset:3072 sc1\n"
MF"v[156:159],a[172:175],a[20:23],v[156:159]\n"
RT"a[190:191],v245 offset:29184\n"
MF"v[156:159],a[168:171],a[4:7],v[156:159]\n"
BS"v[36:39],v250,s[8:11],s49 offen offset:0 sc1\n"
MF"v[164:167],a[168:171],a[8:11],v[164:167]\n"
RT"a[128:129],v236 offset:16384\n"
MF"v[164:167],a[172:175],a[24:27],v[164:167]\n"
RT"a[130:131],v236 offset:16896\n"
MF"v[172:175],a[172:175],a[28:31],v[172:175]\n"
MF"v[172:175],a[168:171],a[12:15],v[172:175]\n"
RT"a[132:133],v236 offset:24576\n"
MF"v[176:179],a[176:179],a[0:3],v[176:179]\n"
WT"lgkmcnt(4)\n"
BL"a[120:123],v235,s[8:11],s58 offen offset:3072 sc1\n"
MF"v[176:179],a[180:183],a[16:19],v[176:179]\n"
RT"a[134:135],v236 offset:25088\n"
MF"v[184:187],a[180:183],a[20:23],v[184:187]\n"
MF"v[184:187],a[176:179],a[4:7],v[184:187]\n"
RT"a[136:137],v237 offset:16384\n"
MF"v[192:195],a[176:179],a[8:11],v[192:195]\n"
MF"v[192:195],a[180:183],a[24:27],v[192:195]\n"
SA"s99,s99,4096\n"
RT"a[138:139],v237 offset:16896\n"
MF"v[200:203],a[180:183],a[28:31],v[200:203]\n"
"s_cmp_eq_u32 s99,20480\n"
BL"a[124:127],v235,s[8:11],s59 offen offset:3072 sc1\n"
MF"v[200:203],a[176:179],a[12:15],v[200:203]\n"
"s_cselect_b32 s99,0,s99\n"
RT"a[140:141],v237 offset:24576\n"
MF"v[180:183],a[184:187],a[0:3],v[180:183]\n"
WT"lgkmcnt(3)\n"
"v_lshrrev_b32 v252,1,v224\n"
MF"v[180:183],a[188:191],a[16:19],v[180:183]\n"
RT"a[142:143],v237 offset:25088\n"
"v_and_b32 v252,56,v252\n"
MF"v[188:191],a[188:191],a[20:23],v[188:191]\n"
"s_lshl_b32 s50,s35,6\n"
MF"v[188:191],a[184:187],a[4:7],v[188:191]\n"
SA"s50,s50,2304\n"
RT"a[144:145],v228 offset:16384\n"
MF"v[196:199],a[184:187],a[8:11],v[196:199]\n"
"buffer_load_dwordx2 v[208:209],v252,s[8:11],s50 offen sc1\n"
MF"v[196:199],a[188:191],a[24:27],v[196:199]\n"
RT"a[146:147],v228 offset:16896\n"
"v_add_u32 v235,s99,v224\n"
MF"v[204:207],a[188:191],a[28:31],v[204:207]\n"
BL"a[192:195],v235,s[8:11],s56 offen offset:0 sc1\n"
MF"v[204:207],a[184:187],a[12:15],v[204:207]\n"
WT"vmcnt(34)\n"
RT"a[148:149],v228 offset:24576\n"
MF"v[80:83],a[128:131],a[32:35],v[80:83]\n"
WT"vmcnt(28)\n"
MF"v[80:83],a[132:135],a[48:51],v[80:83]\n"
WT"vmcnt(26)\n"
RT"a[150:151],v228 offset:25088\n"
MF"v[88:91],a[132:135],a[52:55],v[88:91]\n"
RT"a[152:153],v229 offset:16384\n"
MF"v[88:91],a[128:131],a[36:39],v[88:91]\n"
MF"v[96:99],a[128:131],a[40:43],v[96:99]\n"
WT"vmcnt(25)\n"
RT"a[154:155],v229 offset:16896\n"
MF"v[96:99],a[132:135],a[56:59],v[96:99]\n"
WT"vmcnt(22)\n"
BL"a[196:199],v235,s[8:11],s57 offen offset:0 sc1\n"
MF"v[104:107],a[132:135],a[60:63],v[104:107]\n"
RT"a[156:157],v229 offset:24576\n"
MF"v[104:107],a[128:131],a[44:47],v[104:107]\n"
WT"lgkmcnt(4)\n"
MF"v[84:87],a[136:139],a[32:35],v[84:87]\n"
RT"a[158:159],v229 offset:25088\n"
MF"v[84:87],a[140:143],a[48:51],v[84:87]\n"
MF"v[92:95],a[140:143],a[52:55],v[92:95]\n"
RT"a[160:161],v236 offset:20480\n"
MF"v[92:95],a[136:139],a[36:39],v[92:95]\n"
BL"a[200:203],v235,s[8:11],s58 offen offset:0 sc1\n"
MF"v[100:103],a[136:139],a[40:43],v[100:103]\n"
RT"a[162:163],v236 offset:20992\n"
MF"v[100:103],a[140:143],a[56:59],v[100:103]\n"
BS"v[44:47],v250,s[8:11],s49 offen offset:1024 sc1\n"
MF"v[108:111],a[140:143],a[60:63],v[108:111]\n"
RT"a[164:165],v236 offset:28672\n"
MF"v[108:111],a[136:139],a[44:47],v[108:111]\n"
MF"v[112:115],a[144:147],a[32:35],v[112:115]\n"
WT"lgkmcnt(3)\n"
RT"a[166:167],v236 offset:29184\n"
MF"v[112:115],a[148:151],a[48:51],v[112:115]\n"
MF"v[120:123],a[148:151],a[52:55],v[120:123]\n"
RT"a[168:169],v237 offset:20480\n"
MF"v[120:123],a[144:147],a[36:39],v[120:123]\n"
BL"a[204:207],v235,s[8:11],s59 offen offset:0 sc1\n"
MF"v[128:131],a[144:147],a[40:43],v[128:131]\n"
RT"a[170:171],v237 offset:20992\n"
MF"v[128:131],a[148:151],a[56:59],v[128:131]\n"
RT"a[172:173],v237 offset:28672\n"
MF"v[136:139],a[148:151],a[60:63],v[136:139]\n"
MF"v[136:139],a[144:147],a[44:47],v[136:139]\n"
RT"a[174:175],v237 offset:29184\n"
MF"v[116:119],a[152:155],a[32:35],v[116:119]\n"
MF"v[116:119],a[156:159],a[48:51],v[116:119]\n"
RT"a[176:177],v228 offset:20480\n"
MF"v[124:127],a[156:159],a[52:55],v[124:127]\n"
BL"a[208:211],v235,s[8:11],s56 offen offset:1024 sc1\n"
MF"v[124:127],a[152:155],a[36:39],v[124:127]\n"
RT"a[178:179],v228 offset:20992\n"
MF"v[132:135],a[152:155],a[40:43],v[132:135]\n"
MF"v[132:135],a[156:159],a[56:59],v[132:135]\n"
RT"a[180:181],v228 offset:28672\n"
MF"v[140:143],a[156:159],a[60:63],v[140:143]\n"
MF"v[140:143],a[152:155],a[44:47],v[140:143]\n"
WT"lgkmcnt(3)\n"
RT"a[182:183],v228 offset:29184\n"
MF"v[144:147],a[160:163],a[32:35],v[144:147]\n"
BL"a[212:215],v235,s[8:11],s57 offen offset:1024 sc1\n"
MF"v[144:147],a[164:167],a[48:51],v[144:147]\n"
RT"a[184:185],v229 offset:20480\n"
MF"v[152:155],a[164:167],a[52:55],v[152:155]\n"
MF"v[152:155],a[160:163],a[36:39],v[152:155]\n"
RT"a[186:187],v229 offset:20992\n"
MF"v[160:163],a[160:163],a[40:43],v[160:163]\n"
MF"v[160:163],a[164:167],a[56:59],v[160:163]\n"
RT"a[188:189],v229 offset:28672\n"
MF"v[168:171],a[164:167],a[60:63],v[168:171]\n"
MF"v[168:171],a[160:163],a[44:47],v[168:171]\n"
RT"a[190:191],v229 offset:29184\n"
MF"v[148:151],a[168:171],a[32:35],v[148:151]\n"
BL"a[216:219],v235,s[8:11],s58 offen offset:1024 sc1\n"
MF"v[148:151],a[172:175],a[48:51],v[148:151]\n"
WT"vmcnt(8)\n"
"v_cmp_eq_u64 vcc,s[28:29],v[208:209]\n"
"s_cmp_eq_u64 vcc,exec\n"
"s_cbranch_scc0 L1010_%=\n"
"L1011_%=:\n"
MF"v[156:159],a[172:175],a[52:55],v[156:159]\n"
MF"v[156:159],a[168:171],a[36:39],v[156:159]\n"
MF"v[164:167],a[168:171],a[40:43],v[164:167]\n"
SA"s49,s88,270336\n"
MF"v[164:167],a[172:175],a[56:59],v[164:167]\n"
WT"lgkmcnt(0)\n"
"s_barrier\n"
MF"v[172:175],a[172:175],a[60:63],v[172:175]\n"
SA"m0,s36,0\n"
RT"a[128:129],v246 offset:8192\n"
SA"s48,s39,0\n"
MF"v[172:175],a[168:171],a[44:47],v[172:175]\n"
BS"v[52:55],v250,s[8:11],s49 offen offset:0 sc1\n"
MF"v[176:179],a[176:179],a[32:35],v[176:179]\n"
BL"a[220:223],v235,s[8:11],s59 offen offset:1024 sc1\n"
MF"v[176:179],a[180:183],a[48:51],v[176:179]\n"
RT"a[130:131],v246 offset:8704\n"
MF"v[184:187],a[180:183],a[52:55],v[184:187]\n"
BL"v225,s[8:11],s48 offen offset:0 sc1 lds\n"
MF"v[184:187],a[176:179],a[36:39],v[184:187]\n"
RT"a[132:133],v246 offset:32768\n"
"v_lshlrev_b32 v254,10,v224\n"
MF"v[192:195],a[176:179],a[40:43],v[192:195]\n"
"s_lshl_b32 s94,s34,1\n"
RT"a[134:135],v246 offset:33280\n"
"v_lshrrev_b32 v255,4,v224\n"
MF"v[192:195],a[180:183],a[56:59],v[192:195]\n"
SA"s95,s94,2\n"
BL"a[224:227],v235,s[8:11],s56 offen offset:2048 sc1\n"
MF"v[200:203],a[180:183],a[60:63],v[200:203]\n"
"s_sub_u32 s94,32,s94\n"
RT"a[136:137],v247 offset:8192\n"
MF"v[200:203],a[176:179],a[44:47],v[200:203]\n"
"v_add_u32 v228,s39,v254\n"
MF"v[180:183],a[184:187],a[32:35],v[180:183]\n"
RT"a[138:139],v247 offset:8704\n"
"v_and_b32 v254,1,v255\n"
MF"v[180:183],a[188:191],a[48:51],v[180:183]\n"
BL"v225,s[8:11],s48 offen offset:1024 sc1 lds\n"
MF"v[188:191],a[188:191],a[52:55],v[188:191]\n"
RT"a[140:141],v247 offset:32768\n"
"v_mul_u32_u24 v254,491520,v254\n"
MF"v[188:191],a[184:187],a[36:39],v[188:191]\n"
"v_cmp_gt_u32 vcc,s95,v255\n"
"v_mov_b32 v229,16384\n"
MF"v[196:199],a[184:187],a[40:43],v[196:199]\n"
RT"a[142:143],v247 offset:33280\n"
"v_mov_b32 v253,516096\n"
MF"v[196:199],a[188:191],a[56:59],v[196:199]\n"
BL"a[228:231],v235,s[8:11],s57 offen offset:2048 sc1\n"
MF"v[204:207],a[188:191],a[60:63],v[204:207]\n"
RT"a[144:145],v248 offset:8192\n"
"v_cndmask_b32_e64 v254,v254,0,vcc\n"
MF"v[204:207],a[184:187],a[44:47],v[204:207]\n"
WT"lgkmcnt(3)\n"
"v_cndmask_b32 v229,v229,v253,vcc\n"
"v_cmp_eq_u32 vcc,s94,v255\n"
MF"v[80:83],a[128:131],a[64:67],v[80:83]\n"
RT"a[146:147],v248 offset:8704\n"
"v_mov_b32 v253,507904\n"
MF"v[80:83],a[132:135],a[80:83],v[80:83]\n"
BL"v225,s[8:11],s48 offen offset:2048 sc1 lds\n"
MF"v[88:91],a[132:135],a[84:87],v[88:91]\n"
RT"a[148:149],v248 offset:32768\n"
"v_cndmask_b32 v254,v254,v253,vcc\n"
MF"v[88:91],a[128:131],a[68:71],v[88:91]\n"
BL"a[232:235],v235,s[8:11],s58 offen offset:2048 sc1\n"
MF"v[96:99],a[128:131],a[72:75],v[96:99]\n"
RT"a[150:151],v248 offset:33280\n"
"v_mov_b32 v253,1862270976\n"
MF"v[96:99],a[132:135],a[88:91],v[96:99]\n"
"s_mul_i32 s95,s35,163840\n"
RT"a[152:153],v249 offset:8192\n"
"v_cndmask_b32 v229,v229,v253,vcc\n"
MF"v[104:107],a[132:135],a[92:95],v[104:107]\n"
SA"s95,s95,3768320\n"
"v_add_u32 v228,v228,v254\n"
MF"v[104:107],a[128:131],a[76:79],v[104:107]\n"
RT"a[154:155],v249 offset:8704\n"
"v_subrev_u32 v254,s94,v255\n"
MF"v[84:87],a[136:139],a[64:67],v[84:87]\n"
WT"lgkmcnt(4)\n"
BL"v225,s[8:11],s48 offen offset:3072 sc1 lds\n"
MF"v[84:87],a[140:143],a[80:83],v[84:87]\n"
RT"a[156:157],v249 offset:32768\n"
"v_lshlrev_b32 v254,15,v254\n"
MF"v[92:95],a[140:143],a[84:87],v[92:95]\n"
BL"a[236:239],v235,s[8:11],s59 offen offset:2048 sc1\n"
MF"v[92:95],a[136:139],a[68:71],v[92:95]\n"
RT"a[158:159],v249 offset:33280\n"
"v_add_u32 v254,s95,v254\n"
MF"v[100:103],a[136:139],a[72:75],v[100:103]\n"
"v_cmp_lt_u32 vcc,s94,v255\n"
MF"v[100:103],a[140:143],a[88:91],v[100:103]\n"
"s_mov_b32 s47,s36\n"
RT"a[160:161],v246 offset:12288\n"
"v_cndmask_b32 v228,v228,v254,vcc\n"
MF"v[108:111],a[140:143],a[92:95],v[108:111]\n"
SA"m0,s36,16384\n"
"v_add_u32 v229,v229,v228\n"
SA"s48,s39,516096\n"
MF"v[108:111],a[136:139],a[76:79],v[108:111]\n"
"s_cmp_lt_u32 s30,2\n"
RT"a[162:163],v246 offset:12800\n"
"s_cselect_b32 s48,2130706432,s48\n"
MF"v[112:115],a[144:147],a[64:67],v[112:115]\n"
WT"lgkmcnt(3)\n"
BS"v[60:63],v250,s[8:11],s49 offen offset:1024 sc1\n"
MF"v[112:115],a[148:151],a[80:83],v[112:115]\n"
SA"s46,s36,163840\n"
BL"v225,s[8:11],s48 offen offset:0 sc1 lds\n"
MF"v[120:123],a[148:151],a[84:87],v[120:123]\n"
SA"s40,s36,65536\n"
RT"a[164:165],v246 offset:36864\n"
"v_mov_b32 v226,v224\n"
MF"v[120:123],a[144:147],a[68:71],v[120:123]\n"
"s_mov_b32 s41,0\n"
BL"a[240:243],v235,s[8:11],s56 offen offset:3072 sc1\n"
MF"v[128:131],a[144:147],a[72:75],v[128:131]\n"
"s_mov_b32 s42,2\n"
RT"a[166:167],v246 offset:37376\n"
"v_bfe_u32 v254,v224,4,4\n"
MF"v[128:131],a[148:151],a[88:91],v[128:131]\n"
RT"a[168:169],v247 offset:12288\n"
"v_lshrrev_b32 v255,8,v224\n"
MF"v[136:139],a[148:151],a[92:95],v[136:139]\n"
"s_lshl_b32 s94,s30,4\n"
"v_lshlrev_b32 v255,2,v255\n"
"v_sub_u32 v254,v254,v255\n"
MF"v[136:139],a[144:147],a[76:79],v[136:139]\n"
RT"a[170:171],v247 offset:12800\n"
"v_add_u32 v254,s94,v254\n"
MF"v[116:119],a[152:155],a[64:67],v[116:119]\n"
WT"lgkmcnt(4)\n"
RT"a[172:173],v247 offset:36864\n"
"v_mov_b32 v255,0xff800000\n"
MF"v[116:119],a[156:159],a[80:83],v[116:119]\n"
BL"a[244:247],v235,s[8:11],s57 offen offset:3072 sc1\n"
MF"v[124:127],a[156:159],a[84:87],v[124:127]\n"
BL"v225,s[8:11],s48 offen offset:1024 sc1 lds\n"
MF"v[124:127],a[152:155],a[68:71],v[124:127]\n"
RT"a[174:175],v247 offset:37376\n"
"v_cmp_gt_i32_e64 vcc,0,v254\n"
MF"v[132:135],a[152:155],a[72:75],v[132:135]\n"
RT"a[176:177],v248 offset:12288\n"
"v_cndmask_b32_e64 v208,0,v255,vcc\n"
MF"v[132:135],a[156:159],a[88:91],v[132:135]\n"
"v_cmp_gt_i32_e64 s[52:53],1,v254\n"
"v_cmp_gt_i32_e64 s[62:63],2,v254\n"
MF"v[140:143],a[156:159],a[92:95],v[140:143]\n"
RT"a[178:179],v248 offset:12800\n"
"v_cndmask_b32_e64 v209,0,v255,s[52:53]\n"
MF"v[140:143],a[152:155],a[76:79],v[140:143]\n"
BL"a[248:251],v235,s[8:11],s58 offen offset:3072 sc1\n"
MF"v[144:147],a[160:163],a[64:67],v[144:147]\n"
WT"lgkmcnt(3)\n"
RT"a[180:181],v248 offset:36864\n"
"v_cndmask_b32_e64 v210,0,v255,s[62:63]\n"
MF"v[144:147],a[164:167],a[80:83],v[144:147]\n"
BL"v225,s[8:11],s48 offen offset:2048 sc1 lds\n"
MF"v[152:155],a[164:167],a[84:87],v[152:155]\n"
RT"a[182:183],v248 offset:37376\n"
"v_cmp_gt_i32_e64 vcc,3,v254\n"
MF"v[152:155],a[160:163],a[68:71],v[152:155]\n"
"v_cmp_gt_i32_e64 s[52:53],16,v254\n"
"v_cndmask_b32_e64 v211,0,v255,vcc\n"
MF"v[160:163],a[160:163],a[72:75],v[160:163]\n"
RT"a[184:185],v249 offset:12288\n"
"v_cndmask_b32_e64 v212,0,v255,s[52:53]\n"
MF"v[160:163],a[164:167],a[88:91],v[160:163]\n"
"v_cmp_gt_i32_e64 s[62:63],17,v254\n"
"v_cmp_gt_i32_e64 vcc,18,v254\n"
MF"v[168:171],a[164:167],a[92:95],v[168:171]\n"
RT"a[186:187],v249 offset:12800\n"
"v_cndmask_b32_e64 v213,0,v255,s[62:63]\n"
MF"v[168:171],a[160:163],a[76:79],v[168:171]\n"
BL"a[252:255],v235,s[8:11],s59 offen offset:3072 sc1\n"
MF"v[148:151],a[168:171],a[64:67],v[148:151]\n"
WT"lgkmcnt(3)\n"
RT"a[188:189],v249 offset:36864\n"
"v_cndmask_b32_e64 v214,0,v255,vcc\n"
MF"v[148:151],a[172:175],a[80:83],v[148:151]\n"
BL"v225,s[8:11],s48 offen offset:3072 sc1 lds\n"
MF"v[156:159],a[172:175],a[84:87],v[156:159]\n"
RT"a[190:191],v249 offset:37376\n"
"v_cmp_gt_i32_e64 s[52:53],19,v254\n"
MF"v[156:159],a[168:171],a[68:71],v[156:159]\n"
RT"a[128:129],v242 offset:8192\n"
"v_cndmask_b32_e64 v215,0,v255,s[52:53]\n"
MF"v[164:167],a[168:171],a[72:75],v[164:167]\n"
"v_cmp_gt_i32_e64 s[62:63],32,v254\n"
"v_cmp_gt_i32_e64 vcc,33,v254\n"
MF"v[164:167],a[172:175],a[88:91],v[164:167]\n"
RT"a[130:131],v242 offset:8704\n"
"v_cndmask_b32_e64 v216,0,v255,s[62:63]\n"
MF"v[172:175],a[172:175],a[92:95],v[172:175]\n"
SA"m0,s36,32768\n"
"v_cndmask_b32_e64 v217,0,v255,vcc\n"
"v_cmp_gt_i32_e64 s[52:53],34,v254\n"
MF"v[172:175],a[168:171],a[76:79],v[172:175]\n"
SA"s48,s39,16384\n"
RT"a[132:133],v242 offset:32768\n"
"v_cndmask_b32_e64 v218,0,v255,s[52:53]\n"
MF"v[176:179],a[176:179],a[64:67],v[176:179]\n"
WT"lgkmcnt(4)\n"
BL"v225,s[8:11],s48 offen offset:0 sc1 lds\n"
MF"v[176:179],a[180:183],a[80:83],v[176:179]\n"
"s_mov_b32 s49,278528\n"
RT"a[134:135],v242 offset:33280\n"
"v_cmp_gt_i32_e64 s[62:63],35,v254\n"
MF"v[184:187],a[180:183],a[84:87],v[184:187]\n"
BS"v[64:67],v250,s[8:11],s49 offen offset:0 sc1\n"
MF"v[184:187],a[176:179],a[68:71],v[184:187]\n"
RT"a[136:137],v243 offset:8192\n"
"v_cndmask_b32_e64 v219,0,v255,s[62:63]\n"
MF"v[192:195],a[176:179],a[72:75],v[192:195]\n"
"s_lshl_b32 s59,s34,5\n"
"v_cmp_gt_i32_e64 vcc,48,v254\n"
"v_cmp_gt_i32_e64 s[52:53],49,v254\n"
MF"v[192:195],a[180:183],a[88:91],v[192:195]\n"
"s_sub_u32 s60,480,s59\n"
RT"a[138:139],v243 offset:8704\n"
"v_cndmask_b32_e64 v220,0,v255,vcc\n"
MF"v[200:203],a[180:183],a[92:95],v[200:203]\n"
SA"s59,s59,s35\n"
"v_cndmask_b32_e64 v221,0,v255,s[52:53]\n"
"v_cmp_gt_i32_e64 s[62:63],50,v254\n"
MF"v[200:203],a[176:179],a[76:79],v[200:203]\n"
SA"s60,s60,s35\n"
RT"a[140:141],v243 offset:32768\n"
"v_cndmask_b32_e64 v222,0,v255,s[62:63]\n"
MF"v[180:183],a[184:187],a[64:67],v[180:183]\n"
WT"lgkmcnt(3)\n"
BL"v225,s[8:11],s48 offen offset:1024 sc1 lds\n"
MF"v[180:183],a[188:191],a[80:83],v[180:183]\n"
"s_lshl_b32 s59,s59,15\n"
RT"a[142:143],v243 offset:33280\n"
"v_cmp_gt_i32_e64 vcc,51,v254\n"
MF"v[188:191],a[188:191],a[84:87],v[188:191]\n"
"s_lshl_b32 s60,s60,15\n"
"v_cndmask_b32_e64 v223,0,v255,vcc\n"
SA"s50,s36,42729472\n"
MF"v[188:191],a[184:187],a[68:71],v[188:191]\n"
SA"s59,s59,s50\n"
RT"a[144:145],v244 offset:8192\n"
SA"s60,s60,s50\n"
MF"v[196:199],a[184:187],a[72:75],v[196:199]\n"
"s_lshl_b32 s61,s34,3\n"
SA"s61,s61,s32\n"
"s_lshl_b32 s61,s61,2\n"
MF"v[196:199],a[188:191],a[88:91],v[196:199]\n"
SA"s61,s61,s30\n"
RT"a[146:147],v244 offset:8704\n"
"s_lshl_b32 s61,s61,2\n"
MF"v[204:207],a[188:191],a[92:95],v[204:207]\n"
SA"s61,s61,s33\n"
"s_lshl_b32 s61,s61,6\n"
MF"v[204:207],a[184:187],a[76:79],v[204:207]\n"
SA"s61,s61,393216\n"
RT"a[148:149],v244 offset:32768\n"
"s_lshl_b32 s98,s35,3\n"
MF"v[80:83],a[128:131],a[96:99],v[80:83]\n"
SA"s98,s98,s34\n"
BL"v225,s[8:11],s48 offen offset:2048 sc1 lds\n"
MF"v[80:83],a[132:135],a[112:115],v[80:83]\n"
"s_lshl_b32 s98,s98,3\n"
RT"a[150:151],v244 offset:33280\n"
MF"v[88:91],a[132:135],a[116:119],v[88:91]\n"
SA"s98,s98,4352\n"
RT"a[152:153],v245 offset:8192\n"
SA"s98,s98,s45\n"
MF"v[88:91],a[128:131],a[100:103],v[88:91]\n"
"s_lshl_b32 s50,s34,2\n"
MF"v[96:99],a[128:131],a[104:107],v[96:99]\n"
"s_mul_i32 s51,s33,0x1011100\n"
RT"a[154:155],v245 offset:8704\n"
SA"s51,s51,0xf48c00\n"
MF"v[96:99],a[132:135],a[120:123],v[96:99]\n"
"s_lshr_b32 s51,s51,s50\n"
MF"v[104:107],a[132:135],a[124:127],v[104:107]\n"
"s_and_b32 s51,s51,15\n"
RT"a[156:157],v245 offset:32768\n"
"s_lshr_b32 s50,s31,3\n"
MF"v[104:107],a[128:131],a[108:111],v[104:107]\n"
WT"lgkmcnt(4)\n"
"s_cmp_eq_u32 s50,6\n"
"s_cselect_b32 s51,15,s51\n"
MF"v[84:87],a[136:139],a[96:99],v[84:87]\n"
"s_cmp_eq_u32 s50,26\n"
RT"a[158:159],v245 offset:33280\n"
"s_cselect_b32 s51,0,s51\n"
MF"v[84:87],a[140:143],a[112:115],v[84:87]\n"
"s_mul_i32 s58,s32,14\n"
BL"v225,s[8:11],s48 offen offset:3072 sc1 lds\n"
MF"v[92:95],a[140:143],a[116:119],v[92:95]\n"
SA"s58,s58,s51\n"
RT"a[160:161],v242 offset:12288\n"
"s_sub_u32 s58,s58,1\n"
MF"v[92:95],a[136:139],a[100:103],v[92:95]\n"
"s_cmp_lt_u32 s58,108\n"
"s_cselect_b32 s90,s51,0\n"
"s_cmp_lg_u32 s90,0\n"
MF"v[100:103],a[136:139],a[104:107],v[100:103]\n"
"s_cselect_b32 s57,1,0\n"
RT"a[162:163],v242 offset:12800\n"
"s_cmp_eq_u32 s51,15\n"
MF"v[100:103],a[140:143],a[120:123],v[100:103]\n"
"s_cselect_b32 s57,2,s57\n"
"s_cmp_eq_u32 s50,6\n"
"s_cselect_b32 s90,4,s33\n"
MF"v[108:111],a[140:143],a[124:127],v[108:111]\n"
"s_mul_i32 s50,s32,5\n"
RT"a[164:165],v242 offset:36864\n"
SA"m0,s36,49152\n"
MF"v[108:111],a[136:139],a[108:111],v[108:111]\n"
SA"s48,s39,532480\n"
SA"s90,s90,s50\n"
"s_cmp_eq_u32 s57,2\n"
MF"v[112:115],a[144:147],a[96:99],v[112:115]\n"
WT"lgkmcnt(3)\n"
RT"a[166:167],v242 offset:37376\n"
"s_cselect_b32 s58,s90,s58\n"
MF"v[112:115],a[148:151],a[112:115],v[112:115]\n"
"s_lshl_b32 s56,s34,1\n"
BL"v225,s[8:11],s48 offen offset:0 sc1 lds\n"
MF"v[120:123],a[148:151],a[116:119],v[120:123]\n"
"s_sub_u32 s56,s56,2\n"
RT"a[168:169],v243 offset:12288\n"
"s_cmp_eq_u32 s34,7\n"
MF"v[120:123],a[144:147],a[100:103],v[120:123]\n"
"s_cselect_b32 s56,2,s56\n"
"s_cmp_eq_u32 s57,2\n"
"s_cselect_b32 s56,5,s56\n"
MF"v[128:131],a[144:147],a[104:107],v[128:131]\n"
"s_cmp_lt_u32 s30,2\n"
RT"a[170:171],v243 offset:12800\n"
"s_cselect_b64 s[24:25],s[78:79],s[80:81]\n"
MF"v[128:131],a[148:151],a[120:123],v[128:131]\n"
"s_cmp_eq_u32 s57,2\n"
BS"v[68:71],v250,s[8:11],s49 offen offset:1024 sc1\n"
MF"v[136:139],a[148:151],a[124:127],v[136:139]\n"
"s_cselect_b64 s[24:25],s[82:83],s[24:25]\n"
RT"a[172:173],v243 offset:36864\n"
"s_and_b32 s50,s30,1\n"
MF"v[136:139],a[144:147],a[108:111],v[136:139]\n"
"s_mul_i32 s50,s50,10\n"
RT"a[174:175],v243 offset:37376\n"
"s_mul_i32 s51,s30,14\n"
MF"v[116:119],a[152:155],a[96:99],v[116:119]\n"
"s_min_u32 s51,s51,41\n"
BL"v225,s[8:11],s48 offen offset:1024 sc1 lds\n"
MF"v[116:119],a[156:159],a[112:115],v[116:119]\n"
"s_cmp_eq_u32 s57,2\n"
RT"a[176:177],v244 offset:12288\n"
"s_cselect_b32 s50,s51,s50\n"
MF"v[124:127],a[156:159],a[116:119],v[124:127]\n"
"s_cmp_lt_u32 s30,2\n"
"s_cselect_b32 s51,14,13\n"
"s_cmp_lt_u32 s58,108\n"
MF"v[124:127],a[152:155],a[100:103],v[124:127]\n"
"s_cselect_b32 s92,10,0\n"
RT"a[178:179],v244 offset:12800\n"
"s_cmp_eq_u32 s57,2\n"
MF"v[132:135],a[152:155],a[104:107],v[132:135]\n"
"s_cselect_b32 s92,s51,s92\n"
"s_mov_b32 s51,3456\n"
"s_cmp_eq_u32 s57,2\n"
MF"v[132:135],a[156:159],a[120:123],v[132:135]\n"
"s_cselect_b32 s51,1280,s51\n"
RT"a[180:181],v244 offset:36864\n"
"s_lshl_b32 s62,s51,5\n"
MF"v[140:143],a[156:159],a[124:127],v[140:143]\n"
"s_mul_i32 s26,s62,s92\n"
"s_mul_i32 s90,s62,s50\n"
"s_lshl_b32 s91,s58,5\n"
MF"v[140:143],a[152:155],a[108:111],v[140:143]\n"
WT"lgkmcnt(3)\n"
RT"a[182:183],v244 offset:37376\n"
SA"s90,s90,s91\n"
MF"v[144:147],a[160:163],a[96:99],v[144:147]\n"
SA"s24,s24,s90\n"
BL"v225,s[8:11],s48 offen offset:2048 sc1 lds\n"
MF"v[144:147],a[164:167],a[112:115],v[144:147]\n"
"s_addc_u32 s25,s25,0\n"
RT"a[184:185],v245 offset:12288\n"
"s_and_b32 s25,s25,0xffff\n"
MF"v[152:155],a[164:167],a[116:119],v[152:155]\n"
"s_sub_u32 s90,s51,32\n"
"v_lshrrev_b32 v235,5,v224\n"
"v_mul_u32_u24 v235,s51,v235\n"
MF"v[152:155],a[160:163],a[100:103],v[152:155]\n"
"s_sub_u32 s26,s26,s90\n"
RT"a[186:187],v245 offset:12800\n"
"v_and_b32 v239,16,v224\n"
MF"v[160:163],a[160:163],a[104:107],v[160:163]\n"
"s_cselect_b32 s26,0,s26\n"
"v_add_u32 v235,v235,v239\n"
"s_mov_b32 s27,0x20000\n"
MF"v[160:163],a[164:167],a[120:123],v[160:163]\n"
"s_mul_i32 s63,s35,20480\n"
RT"a[188:189],v245 offset:36864\n"
SA"s63,s63,3145728\n"
MF"v[168:171],a[164:167],a[124:127],v[168:171]\n"
"s_cmp_eq_u32 s57,2\n"
"s_cselect_b32 s63,16384,s63\n"
MF"v[168:171],a[160:163],a[108:111],v[168:171]\n"
"s_lshl_b32 s50,s50,10\n"
RT"a[190:191],v245 offset:37376\n"
SA"s63,s63,s50\n"
MF"v[148:151],a[168:171],a[96:99],v[148:151]\n"
"s_mov_b32 s50,1065353216\n"
BL"v225,s[8:11],s48 offen offset:3072 sc1 lds\n"
MF"v[148:151],a[172:175],a[112:115],v[148:151]\n"
"s_cmp_lg_u32 s92,0\n"
"s_cselect_b32 s50,s45,s50\n"
"s_mov_b32 s93,16384\n"
MF"v[156:159],a[172:175],a[116:119],v[156:159]\n"
"s_cmp_eq_u32 s57,2\n"
"s_cselect_b32 s93,71680,s93\n"
MF"v[156:159],a[168:171],a[100:103],v[156:159]\n"
"s_lshl_b32 s51,s58,9\n"
MF"v[164:167],a[168:171],a[104:107],v[164:167]\n"
SA"s93,s93,s51\n"
SA"s93,s93,s50\n"
MF"v[164:167],a[172:175],a[120:123],v[164:167]\n"
"s_mov_b32 s94,6912\n"
"s_barrier\n"
MF"v[172:175],a[172:175],a[124:127],v[172:175]\n"
"s_cmp_eq_u32 s57,2\n"
RT"a[128:129],v246 offset:16384\n"
"s_cselect_b32 s94,6400,s94\n"
MF"v[172:175],a[168:171],a[108:111],v[172:175]\n"
WT"lgkmcnt(1)\n"
"s_lshl_b32 s51,s58,3\n"
SA"s94,s94,s51\n"
MF"v[176:179],a[176:179],a[96:99],v[176:179]\n"
SA"s94,s94,s50\n"
RT"a[130:131],v246 offset:16896\n"
"s_mov_b32 s52,0\n"
MF"v[176:179],a[180:183],a[112:115],v[176:179]\n"
"s_lshl_b32 s50,s35,6\n"
RT"a[132:133],v246 offset:40960\n"
MF"v[184:187],a[180:183],a[116:119],v[184:187]\n"
SA"s50,s50,2304\n"
MF"v[184:187],a[176:179],a[100:103],v[184:187]\n"
"s_cmp_eq_u32 s57,2\n"
RT"a[134:135],v246 offset:41472\n"
"s_cselect_b32 s50,6912,s50\n"
MF"v[192:195],a[176:179],a[104:107],v[192:195]\n"
"s_mov_b32 s51,48\n"
RT"a[136:137],v242 offset:16384\n"
SA"s49,s88,278528\n"
MF"v[192:195],a[180:183],a[120:123],v[192:195]\n"
"s_cmp_eq_u32 s57,2\n"
"s_cselect_b32 s51,848,s51\n"
"v_min_u32 v238,s51,v224\n"
MF"v[200:203],a[180:183],a[124:127],v[200:203]\n"
RT"a[138:139],v242 offset:16896\n"
"v_add_u32 v238,s50,v238\n"
MF"v[200:203],a[176:179],a[108:111],v[200:203]\n"
"s_mov_b32 s16,0\n"
BS"v[72:75],v250,s[8:11],s49 offen offset:0 sc1\n"
MF"v[180:183],a[184:187],a[96:99],v[180:183]\n"
"s_mov_b32 s20,8\n"
RT"a[140:141],v242 offset:40960\n"
"v_and_b32 v239,0x80,v224\n"
MF"v[180:183],a[188:191],a[112:115],v[180:183]\n"
"s_mov_b32 s18,2130706432\n"
RT"a[142:143],v242 offset:41472\n"
"v_lshl_add_u32 v236,v239,23,v224\n"
MF"v[188:191],a[188:191],a[116:119],v[188:191]\n"
"s_lshl_b32 s21,s32,4\n"
"s_lshr_b32 s22,s30,1\n"
MF"v[188:191],a[184:187],a[100:103],v[188:191]\n"
SA"s21,s21,s22\n"
RT"a[144:145],v247 offset:16384\n"
MF"v[196:199],a[184:187],a[104:107],v[196:199]\n"
"s_mul_i32 s21,s21,40960\n"
"s_lshl_b32 s22,s33,1\n"
MF"v[196:199],a[188:191],a[120:123],v[196:199]\n"
"s_and_b32 s23,s30,1\n"
RT"a[146:147],v247 offset:16896\n"
MF"v[204:207],a[188:191],a[124:127],v[204:207]\n"
SA"s22,s22,s23\n"
RT"a[148:149],v247 offset:40960\n"
"s_mul_i32 s22,s22,160\n"
MF"v[204:207],a[184:187],a[108:111],v[204:207]\n"
WT"vmcnt(37) lgkmcnt(4)\n"
SA"s17,s21,s22\n"
MF"v[80:83],a[128:131],a[192:195],v[80:83]\n"
WT"vmcnt(32)\n"
RT"a[150:151],v247 offset:41472\n"
"v_lshrrev_b32 v240,4,v224\n"
MF"v[80:83],a[132:135],a[208:211],v[80:83]\n"
WT"vmcnt(31)\n"
RT"a[152:153],v243 offset:16384\n"
"v_add_u32 v240,0,v240\n"
MF"v[88:91],a[132:135],a[212:215],v[88:91]\n"
"v_mul_u32_u24 v240,205,v240\n"
"v_lshrrev_b32 v240,11,v240\n"
MF"v[88:91],a[128:131],a[196:199],v[88:91]\n"
RT"a[154:155],v243 offset:16896\n"
"v_mul_u32_u24 v240,1120,v240\n"
MF"v[96:99],a[128:131],a[200:203],v[96:99]\n"
WT"vmcnt(30)\n"
RT"a[156:157],v243 offset:40960\n"
"v_add_u32 v240,v240,v224\n"
MF"v[96:99],a[132:135],a[216:219],v[96:99]\n"
WT"vmcnt(28)\n"
"v_lshrrev_b32 v241,4,v224\n"
"v_add_u32 v241,64,v241\n"
MF"v[104:107],a[132:135],a[220:223],v[104:107]\n"
RT"a[158:159],v243 offset:41472\n"
"v_mul_u32_u24 v241,205,v241\n"
MF"v[104:107],a[128:131],a[204:207],v[104:107]\n"
WT"vmcnt(26)\n"
"v_lshrrev_b32 v241,11,v241\n"
"v_mul_u32_u24 v241,1120,v241\n"
MF"v[80:83],a[136:139],a[224:227],v[80:83]\n"
WT"vmcnt(17) lgkmcnt(3)\n"
RT"a[160:161],v248 offset:16384\n"
"v_add_u32 v241,v241,v224\n"
MF"v[80:83],a[140:143],a[240:243],v[80:83]\n"
WT"vmcnt(16)\n"
"v_lshrrev_b32 v251,4,v224\n"
"v_add_u32 v251,128,v251\n"
MF"v[88:91],a[140:143],a[244:247],v[88:91]\n"
RT"a[162:163],v248 offset:16896\n"
"v_mul_u32_u24 v251,205,v251\n"
MF"v[88:91],a[136:139],a[228:231],v[88:91]\n"
RT"a[164:165],v248 offset:40960\n"
"v_lshrrev_b32 v251,11,v251\n"
MF"v[96:99],a[136:139],a[232:235],v[96:99]\n"
WT"vmcnt(14)\n"
"v_mul_u32_u24 v251,1120,v251\n"
"v_add_u32 v251,v251,v224\n"
MF"v[96:99],a[140:143],a[248:251],v[96:99]\n"
WT"vmcnt(12)\n"
RT"a[166:167],v248 offset:41472\n"
"v_and_b32 v237,512,v224\n"
MF"v[104:107],a[140:143],a[252:255],v[104:107]\n"
RT"a[168:169],v244 offset:16384\n"
"v_lshl_add_u32 v251,v237,20,v251\n"
MF"v[104:107],a[136:139],a[236:239],v[104:107]\n"
"v_lshrrev_b32 v252,6,v224\n"
"v_mul_u32_u24 v252,160,v252\n"
MF"v[84:87],a[144:147],a[192:195],v[84:87]\n"
RT"a[170:171],v244 offset:16896\n"
"v_bfe_u32 v237,v224,4,2\n"
MF"v[84:87],a[148:151],a[208:211],v[84:87]\n"
"s_cmp_eq_u32 s34,7\n"
RT"a[172:173],v244 offset:40960\n"
"v_lshl_add_u32 v252,v237,3,v252\n"
MF"v[92:95],a[148:151],a[212:215],v[92:95]\n"
"s_cselect_b32 s57,3,s57\n"
"v_lshrrev_b32 v254,4,v224\n"
"v_min_u32 v254,49,v254\n"
MF"v[92:95],a[144:147],a[196:199],v[92:95]\n"
RT"a[174:175],v244 offset:41472\n"
"v_mul_u32_u24 v253,52,v254\n"
MF"v[100:103],a[144:147],a[200:203],v[100:103]\n"
"v_lshrrev_b32 v253,8,v253\n"
MF"v[100:103],a[148:151],a[216:219],v[100:103]\n"
RT"a[176:177],v249 offset:16384\n"
"v_mul_u32_u24 v253,2901,v253\n"
MF"v[108:111],a[148:151],a[220:223],v[108:111]\n"
"v_mul_u32_u24 v254,111,v254\n"
"v_add_u32 v254,v254,v253\n"
MF"v[108:111],a[144:147],a[204:207],v[108:111]\n"
WT"lgkmcnt(3)\n"
RT"a[178:179],v249 offset:16896\n"
"v_and_b32 v254,-4,v254\n"
MF"v[84:87],a[152:155],a[224:227],v[84:87]\n"
RT"a[180:181],v249 offset:40960\n"
"v_lshrrev_b32 v253,5,v224\n"
MF"v[84:87],a[156:159],a[240:243],v[84:87]\n"
BS"v[76:79],v250,s[8:11],s49 offen offset:1024 sc1\n"
MF"v[92:95],a[156:159],a[244:247],v[92:95]\n"
RT"a[182:183],v249 offset:41472\n"
"v_min_u32 v253,13,v253\n"
MF"v[92:95],a[152:155],a[228:231],v[92:95]\n"
RT"a[184:185],v245 offset:16384\n"
"v_mul_u32_u24 v253,1280,v253\n"
MF"v[100:103],a[152:155],a[232:235],v[100:103]\n"
"v_bfe_u32 v237,v224,4,1\n"
"v_mul_u32_u24 v237,156,v237\n"
MF"v[100:103],a[156:159],a[248:251],v[100:103]\n"
"s_cmp_lt_u32 s30,2\n"
RT"a[186:187],v245 offset:16896\n"
"v_add_u32 v253,v253,v237\n"
MF"v[108:111],a[156:159],a[252:255],v[108:111]\n"
"s_cselect_b64 s[12:13],s[78:79],s[80:81]\n"
RT"a[188:189],v245 offset:40960\n"
"s_and_b32 s13,s13,0xffff\n"
MF"v[108:111],a[152:155],a[236:239],v[108:111]\n"
"s_mov_b32 s14,2211840\n"
"s_mov_b32 s15,0x20000\n"
MF"v[112:115],a[160:163],a[192:195],v[112:115]\n"
"s_lshr_b32 s90,s31,3\n"
RT"a[190:191],v245 offset:41472\n"
MF"v[112:115],a[164:167],a[208:211],v[112:115]\n"
"s_lshl_b32 s91,s90,1\n"
MF"v[120:123],a[164:167],a[212:215],v[120:123]\n"
"s_and_b32 s50,s30,1\n"
RT"a[128:129],v246 offset:20480\n"
PK"v80,v80,v81\n"
MF"v[120:123],a[160:163],a[196:199],v[120:123]\n"
SA"s91,s91,s50\n"
PK"v81,v82,v83\n"
PK"v82,v84,v85\n"
MF"v[128:131],a[160:163],a[200:203],v[128:131]\n"
"s_mul_i32 s91,s91,34560\n"
RT"a[130:131],v246 offset:20992\n"
PK"v83,v86,v87\n"
MF"v[128:131],a[164:167],a[216:219],v[128:131]\n"
"s_mul_i32 s50,s32,448\n"
RT"a[132:133],v246 offset:45056\n"
"v_accvgpr_write_b32 a0,v80\n"
MF"v[136:139],a[164:167],a[220:223],v[136:139]\n"
SA"s91,s91,s50\n"
"v_accvgpr_write_b32 a1,v81\n"
"v_accvgpr_write_b32 a2,v82\n"
MF"v[136:139],a[160:163],a[204:207],v[136:139]\n"
"buffer_load_dword v255,v254,s[12:15],s91 offen\n"
MF"v[112:115],a[168:171],a[224:227],v[112:115]\n"
WT"lgkmcnt(3)\n"
RT"a[134:135],v246 offset:45568\n"
RT"a[136:137],v242 offset:20480\n"
MF"v[112:115],a[172:175],a[240:243],v[112:115]\n"
"s_mov_b32 s12,s82\n"
"v_accvgpr_write_b32 a3,v83\n"
PK"v88,v88,v89\n"
MF"v[120:123],a[172:175],a[244:247],v[120:123]\n"
"s_and_b32 s13,s83,0xffff\n"
RT"a[138:139],v242 offset:20992\n"
PK"v89,v90,v91\n"
MF"v[120:123],a[168:171],a[228:231],v[120:123]\n"
"s_lshl_b32 s90,s90,2\n"
RT"a[140:141],v242 offset:45056\n"
PK"v90,v92,v93\n"
MF"v[128:131],a[168:171],a[232:235],v[128:131]\n"
SA"s90,s90,s30\n"
PK"v91,v94,v95\n"
"v_accvgpr_write_b32 a16,v88\n"
MF"v[128:131],a[172:175],a[248:251],v[128:131]\n"
"s_mul_i32 s90,s90,17920\n"
RT"a[142:143],v242 offset:45568\n"
"v_accvgpr_write_b32 a17,v89\n"
MF"v[136:139],a[172:175],a[252:255],v[136:139]\n"
"s_mul_i32 s50,s32,160\n"
"v_accvgpr_write_b32 a18,v90\n"
"v_accvgpr_write_b32 a19,v91\n"
MF"v[136:139],a[168:171],a[236:239],v[136:139]\n"
SA"s90,s90,s50\n"
RT"a[144:145],v247 offset:20480\n"
PK"v96,v96,v97\n"
MF"v[116:119],a[176:179],a[192:195],v[116:119]\n"
"buffer_load_dword v255,v253,s[12:15],s90 offen\n"
MF"v[116:119],a[180:183],a[208:211],v[116:119]\n"
RT"a[146:147],v247 offset:20992\n"
"v_min_u32 v253,624,v224\n"
MF"v[124:127],a[180:183],a[212:215],v[124:127]\n"
RT"a[148:149],v247 offset:45056\n"
"v_lshlrev_b32 v253,3,v253\n"
MF"v[124:127],a[176:179],a[196:199],v[124:127]\n"
PK"v97,v98,v99\n"
PK"v98,v100,v101\n"
MF"v[132:135],a[176:179],a[200:203],v[132:135]\n"
RT"a[150:151],v247 offset:45568\n"
PK"v99,v102,v103\n"
MF"v[132:135],a[180:183],a[216:219],v[132:135]\n"
RT"a[152:153],v243 offset:20480\n"
"v_accvgpr_write_b32 a32,v96\n"
MF"v[140:143],a[180:183],a[220:223],v[140:143]\n"
"v_accvgpr_write_b32 a33,v97\n"
"v_accvgpr_write_b32 a34,v98\n"
MF"v[140:143],a[176:179],a[204:207],v[140:143]\n"
RT"a[154:155],v243 offset:20992\n"
"v_accvgpr_write_b32 a35,v99\n"
MF"v[116:119],a[184:187],a[224:227],v[116:119]\n"
RT"a[156:157],v243 offset:45056\n"
PK"v104,v104,v105\n"
MF"v[116:119],a[188:191],a[240:243],v[116:119]\n"
PK"v105,v106,v107\n"
PK"v106,v108,v109\n"
MF"v[124:127],a[188:191],a[244:247],v[124:127]\n"
RT"a[158:159],v243 offset:45568\n"
PK"v107,v110,v111\n"
MF"v[124:127],a[184:187],a[228:231],v[124:127]\n"
"v_accvgpr_write_b32 a48,v104\n"
"v_accvgpr_write_b32 a49,v105\n"
MF"v[132:135],a[184:187],a[232:235],v[132:135]\n"
RT"a[160:161],v248 offset:20480\n"
"v_accvgpr_write_b32 a50,v106\n"
MF"v[132:135],a[188:191],a[248:251],v[132:135]\n"
"v_accvgpr_write_b32 a51,v107\n"
MF"v[140:143],a[188:191],a[252:255],v[140:143]\n"
RT"a[162:163],v248 offset:20992\n"
MF"v[140:143],a[184:187],a[236:239],v[140:143]\n"
WT"lgkmcnt(3)\n"
RT"a[164:165],v248 offset:45056\n"
MF"v[144:147],a[128:131],a[192:195],v[144:147]\n"
MF"v[144:147],a[132:135],a[208:211],v[144:147]\n"
RT"a[166:167],v248 offset:45568\n"
MF"v[152:155],a[132:135],a[212:215],v[152:155]\n"
RT"a[168:169],v244 offset:20480\n"
PK"v112,v112,v113\n"
MF"v[152:155],a[128:131],a[196:199],v[152:155]\n"
PK"v113,v114,v115\n"
PK"v114,v116,v117\n"
MF"v[160:163],a[128:131],a[200:203],v[160:163]\n"
RT"a[170:171],v244 offset:20992\n"
PK"v115,v118,v119\n"
MF"v[160:163],a[132:135],a[216:219],v[160:163]\n"
RT"a[172:173],v244 offset:45056\n"
"v_accvgpr_write_b32 a4,v112\n"
MF"v[168:171],a[132:135],a[220:223],v[168:171]\n"
"v_accvgpr_write_b32 a5,v113\n"
"v_accvgpr_write_b32 a6,v114\n"
MF"v[168:171],a[128:131],a[204:207],v[168:171]\n"
RT"a[174:175],v244 offset:45568\n"
"v_accvgpr_write_b32 a7,v115\n"
MF"v[144:147],a[136:139],a[224:227],v[144:147]\n"
PK"v120,v120,v121\n"
PK"v121,v122,v123\n"
MF"v[144:147],a[140:143],a[240:243],v[144:147]\n"
RT"a[176:177],v249 offset:20480\n"
PK"v122,v124,v125\n"
MF"v[152:155],a[140:143],a[244:247],v[152:155]\n"
PK"v123,v126,v127\n"
"v_accvgpr_write_b32 a20,v120\n"
MF"v[152:155],a[136:139],a[228:231],v[152:155]\n"
RT"a[178:179],v249 offset:20992\n"
"v_accvgpr_write_b32 a21,v121\n"
MF"v[160:163],a[136:139],a[232:235],v[160:163]\n"
RT"a[180:181],v249 offset:45056\n"
"v_accvgpr_write_b32 a22,v122\n"
MF"v[160:163],a[140:143],a[248:251],v[160:163]\n"
"v_accvgpr_write_b32 a23,v123\n"
MF"v[168:171],a[140:143],a[252:255],v[168:171]\n"
RT"a[182:183],v249 offset:45568\n"
PK"v128,v128,v129\n"
MF"v[168:171],a[136:139],a[236:239],v[168:171]\n"
RT"a[184:185],v245 offset:20480\n"
PK"v129,v130,v131\n"
MF"v[148:151],a[144:147],a[192:195],v[148:151]\n"
PK"v130,v132,v133\n"
PK"v131,v134,v135\n"
MF"v[148:151],a[148:151],a[208:211],v[148:151]\n"
RT"a[186:187],v245 offset:20992\n"
"v_accvgpr_write_b32 a36,v128\n"
MF"v[156:159],a[148:151],a[212:215],v[156:159]\n"
RT"a[188:189],v245 offset:45056\n"
"v_accvgpr_write_b32 a37,v129\n"
MF"v[156:159],a[144:147],a[196:199],v[156:159]\n"
"v_accvgpr_write_b32 a38,v130\n"
"v_accvgpr_write_b32 a39,v131\n"
MF"v[164:167],a[144:147],a[200:203],v[164:167]\n"
RT"a[190:191],v245 offset:45568\n"
PK"v136,v136,v137\n"
MF"v[164:167],a[148:151],a[216:219],v[164:167]\n"
PK"v137,v138,v139\n"
PK"v138,v140,v141\n"
MF"v[172:175],a[148:151],a[220:223],v[172:175]\n"
PK"v139,v142,v143\n"
"v_accvgpr_write_b32 a52,v136\n"
MF"v[172:175],a[144:147],a[204:207],v[172:175]\n"
"v_accvgpr_write_b32 a53,v137\n"
"v_accvgpr_write_b32 a54,v138\n"
MF"v[148:151],a[152:155],a[224:227],v[148:151]\n"
WT"lgkmcnt(2)\n"
"v_accvgpr_write_b32 a55,v139\n"
MF"v[148:151],a[156:159],a[240:243],v[148:151]\n"
MF"v[156:159],a[156:159],a[244:247],v[156:159]\n"
MF"v[156:159],a[152:155],a[228:231],v[156:159]\n"
MF"v[164:167],a[152:155],a[232:235],v[164:167]\n"
MF"v[164:167],a[156:159],a[248:251],v[164:167]\n"
MF"v[172:175],a[156:159],a[252:255],v[172:175]\n"
MF"v[172:175],a[152:155],a[236:239],v[172:175]\n"
MF"v[176:179],a[160:163],a[192:195],v[176:179]\n"
MF"v[176:179],a[164:167],a[208:211],v[176:179]\n"
MF"v[176:179],a[168:171],a[224:227],v[176:179]\n"
PK"v144,v144,v145\n"
PK"v145,v146,v147\n"
MF"v[176:179],a[172:175],a[240:243],v[176:179]\n"
PK"v146,v148,v149\n"
PK"v147,v150,v151\n"
MF"v[180:183],a[176:179],a[192:195],v[180:183]\n"
"v_mov_b32 v254,0x3f803f80\n"
"v_accvgpr_write_b32 a8,v144\n"
MF"v[180:183],a[180:183],a[208:211],v[180:183]\n"
"v_accvgpr_write_b32 a9,v145\n"
"v_accvgpr_write_b32 a128,v254\n"
MF"v[180:183],a[184:187],a[224:227],v[180:183]\n"
WT"lgkmcnt(0)\n"
"v_accvgpr_write_b32 a129,v254\n"
"v_accvgpr_write_b32 a130,v254\n"
MF"v[180:183],a[188:191],a[240:243],v[180:183]\n"
"v_accvgpr_write_b32 a131,v254\n"
"v_accvgpr_write_b32 a10,v146\n"
MF"v[184:187],a[160:163],a[196:199],v[184:187]\n"
"v_accvgpr_write_b32 a11,v147\n"
PK"v152,v152,v153\n"
MF"v[184:187],a[164:167],a[212:215],v[184:187]\n"
PK"v153,v154,v155\n"
PK"v154,v156,v157\n"
MF"v[184:187],a[168:171],a[228:231],v[184:187]\n"
PK"v155,v158,v159\n"
"v_accvgpr_write_b32 a24,v152\n"
MF"v[184:187],a[172:175],a[244:247],v[184:187]\n"
"v_accvgpr_write_b32 a25,v153\n"
PK"v176,v176,v177\n"
MF"v[188:191],a[176:179],a[196:199],v[188:191]\n"
PK"v177,v178,v179\n"
"v_accvgpr_write_b32 a26,v154\n"
MF"v[188:191],a[180:183],a[212:215],v[188:191]\n"
"v_accvgpr_write_b32 a27,v155\n"
PK"v178,v180,v181\n"
MF"v[188:191],a[184:187],a[228:231],v[188:191]\n"
PK"v179,v182,v183\n"
"v_accvgpr_write_b32 a12,v176\n"
MF"v[188:191],a[188:191],a[244:247],v[188:191]\n"
"v_accvgpr_write_b32 a13,v177\n"
"v_accvgpr_write_b32 a14,v178\n"
MF"v[192:195],a[160:163],a[200:203],v[192:195]\n"
"v_accvgpr_write_b32 a15,v179\n"
PK"v160,v160,v161\n"
MF"v[192:195],a[164:167],a[216:219],v[192:195]\n"
WT"vmcnt(14)\n"
"s_barrier\n"
MF"v[192:195],a[168:171],a[232:235],v[192:195]\n"
RD"a[64:67],v226 offset:0\n"
PK"v161,v162,v163\n"
MF"v[192:195],a[172:175],a[248:251],v[192:195]\n"
PK"v162,v164,v165\n"
PK"v163,v166,v167\n"
MF"v[196:199],a[176:179],a[200:203],v[196:199]\n"
RD"a[68:71],v226 offset:1024\n"
"v_accvgpr_write_b32 a40,v160\n"
MF"v[196:199],a[180:183],a[216:219],v[196:199]\n"
"v_accvgpr_write_b32 a41,v161\n"
"v_accvgpr_write_b32 a42,v162\n"
MF"v[196:199],a[184:187],a[232:235],v[196:199]\n"
RD"a[72:75],v226 offset:2048\n"
"v_accvgpr_write_b32 a43,v163\n"
MF"v[196:199],a[188:191],a[248:251],v[196:199]\n"
PK"v184,v184,v185\n"
PK"v185,v186,v187\n"
MF"v[200:203],a[160:163],a[204:207],v[200:203]\n"
RD"a[76:79],v226 offset:3072\n"
PK"v186,v188,v189\n"
MF"v[200:203],a[164:167],a[220:223],v[200:203]\n"
PK"v187,v190,v191\n"
PK"v168,v168,v169\n"
MF"v[200:203],a[168:171],a[236:239],v[200:203]\n"
RD"a[80:83],v226 offset:4096\n"
PK"v169,v170,v171\n"
MF"v[200:203],a[172:175],a[252:255],v[200:203]\n"
"v_accvgpr_write_b32 a28,v184\n"
"v_accvgpr_write_b32 a29,v185\n"
MF"v[204:207],a[176:179],a[204:207],v[204:207]\n"
RD"a[84:87],v226 offset:5120\n"
PK"v170,v172,v173\n"
MF"v[204:207],a[180:183],a[220:223],v[204:207]\n"
PK"v171,v174,v175\n"
"v_accvgpr_write_b32 a30,v186\n"
MF"v[204:207],a[184:187],a[236:239],v[204:207]\n"
"v_accvgpr_write_b32 a31,v187\n"
"v_accvgpr_write_b32 a56,v168\n"
MF"v[204:207],a[188:191],a[252:255],v[204:207]\n"
"v_accvgpr_write_b32 a57,v169\n"
"v_accvgpr_write_b32 a58,v170\n"
"v_accvgpr_write_b32 a59,v171\n"
RD"a[88:91],v226 offset:6144\n"
"s_cmp_eq_u32 s34,0\n"
"s_cbranch_scc1 LFA_%=\n"
WT"lgkmcnt(6)\n"
MF"v[0:3],a[64:67],a[0:3],0\n"
WT"lgkmcnt(3)\n"
PK"v192,v192,v193\n"
PK"v193,v194,v195\n"
MF"v[0:3],a[68:71],a[4:7],v[0:3]\n"
"s_mov_b32 m0,s40\n"
"v_readlane_b32 s43,v228,s42\n"
PK"v194,v196,v197\n"
MF"v[8:11],a[68:71],a[20:23],0\n"
PK"v195,v198,v199\n"
"v_readlane_b32 s44,v229,s42\n"
MF"v[8:11],a[64:67],a[16:19],v[8:11]\n"
SA"s42,s42,1\n"
BL"v225,s[8:11],s43 offen offset:0 sc1 lds\n"
MF"v[16:19],a[64:67],a[32:35],0\n"
"v_accvgpr_write_b32 a44,v192\n"
"v_accvgpr_write_b32 a45,v193\n"
MF"v[16:19],a[68:71],a[36:39],v[16:19]\n"
RD"a[92:95],v226 offset:7168\n"
"v_accvgpr_write_b32 a46,v194\n"
MF"v[24:27],a[68:71],a[52:55],0\n"
"v_accvgpr_write_b32 a47,v195\n"
PK"v200,v200,v201\n"
MF"v[24:27],a[64:67],a[48:51],v[24:27]\n"
PK"v201,v202,v203\n"
PK"v202,v204,v205\n"
"v_mov_b32 v227,v226\n"
MF"v[0:3],a[72:75],a[8:11],v[0:3]\n"
BL"v225,s[8:11],s43 offen offset:1024 sc1 lds\n"
PK"v203,v206,v207\n"
MF"v[0:3],a[76:79],a[12:15],v[0:3]\n"
"v_accvgpr_write_b32 a60,v200\n"
"v_accvgpr_write_b32 a61,v201\n"
MF"v[8:11],a[76:79],a[28:31],v[8:11]\n"
"s_mul_i32 s48,s32,655360\n"
"v_accvgpr_write_b32 a62,v202\n"
"v_accvgpr_write_b32 a63,v203\n"
MF"v[8:11],a[72:75],a[24:27],v[8:11]\n"
SA"s12,s74,s48\n"
"s_addc_u32 s13,s75,0\n"
MF"v[16:19],a[72:75],a[40:43],v[16:19]\n"
"s_and_b32 s13,s13,0xffff\n"
MF"v[16:19],a[76:79],a[44:47],v[16:19]\n"
"s_mov_b32 s14,655360\n"
RD"a[96:99],v226 offset:8192\n"
MF"v[24:27],a[76:79],a[60:63],v[24:27]\n"
"s_lshl_b32 s48,s33,3\n"
BL"v225,s[8:11],s43 offen offset:2048 sc1 lds\n"
MF"v[24:27],a[72:75],a[56:59],v[24:27]\n"
WT"lgkmcnt(1)\n"
EX"v0,v0\n"
MF"v[4:7],a[80:83],a[0:3],0\n"
"s_and_b32 s49,s34,2\n"
EX"v1,v1\n"
MF"v[4:7],a[84:87],a[4:7],v[4:7]\n"
"s_lshl_b32 s49,s49,1\n"
EX"v2,v2\n"
MF"v[12:15],a[84:87],a[20:23],0\n"
SA"s48,s48,s49\n"
RD"a[100:103],v226 offset:9216\n"
PK"v0,v0,v1\n"
MF"v[12:15],a[80:83],a[16:19],v[12:15]\n"
SA"s48,s48,s30\n"
EX"v3,v3\n"
MF"v[20:23],a[80:83],a[32:35],0\n"
"s_mul_i32 s48,s48,5120\n"
EX"v8,v8\n"
MF"v[20:23],a[84:87],a[36:39],v[20:23]\n"
"s_or_b32 s49,s34,2\n"
BL"v225,s[8:11],s43 offen offset:3072 sc1 lds\n"
MF"v[28:31],a[84:87],a[52:55],0\n"
"s_cmp_eq_u32 s49,7\n"
EX"v9,v9\n"
PK"v1,v2,v3\n"
MF"v[28:31],a[80:83],a[48:51],v[28:31]\n"
"s_cselect_b32 s55,s48,2130706432\n"
RD"a[104:107],v226 offset:10240\n"
PK"v8,v8,v9\n"
MF"v[4:7],a[88:91],a[8:11],v[4:7]\n"
SA"m0,s40,16384\n"
EX"v10,v10\n"
MF"v[4:7],a[92:95],a[12:15],v[4:7]\n"
EX"v11,v11\n"
MF"v[12:15],a[92:95],a[28:31],v[12:15]\n"
EX"v16,v16\n"
MF"v[12:15],a[88:91],a[24:27],v[12:15]\n"
SA"s40,s40,32768\n"
RD"a[108:111],v226 offset:11264\n"
PK"v9,v10,v11\n"
MF"v[20:23],a[88:91],a[40:43],v[20:23]\n"
"s_cmp_eq_u32 s40,s46\n"
BL"v225,s[8:11],s44 offen offset:0 sc1 lds\n"
EX"v17,v17\n"
MF"v[20:23],a[92:95],a[44:47],v[20:23]\n"
"s_cselect_b32 s40,s47,s40\n"
EX"v18,v18\n"
MF"v[28:31],a[92:95],a[60:63],v[28:31]\n"
EX"v19,v19\n"
PK"v16,v16,v17\n"
MF"v[28:31],a[88:91],a[56:59],v[28:31]\n"
WT"lgkmcnt(1)\n"
RD"a[112:115],v226 offset:12288\n"
PK"v17,v18,v19\n"
MF"v[32:35],a[96:99],a[0:3],0\n"
EX"v24,v24\n"
MF"v[32:35],a[100:103],a[4:7],v[32:35]\n"
EX"v25,v25\n"
MF"v[40:43],a[100:103],a[20:23],0\n"
BL"v225,s[8:11],s44 offen offset:1024 sc1 lds\n"
EX"v26,v26\n"
MF"v[40:43],a[96:99],a[16:19],v[40:43]\n"
RD"a[116:119],v226 offset:13312\n"
PK"v24,v24,v25\n"
MF"v[48:51],a[96:99],a[32:35],0\n"
EX"v27,v27\n"
MF"v[48:51],a[100:103],a[36:39],v[48:51]\n"
EX"v4,v4\n"
MF"v[56:59],a[100:103],a[52:55],0\n"
EX"v5,v5\n"
PK"v25,v26,v27\n"
MF"v[56:59],a[96:99],a[48:51],v[56:59]\n"
RD"a[120:123],v226 offset:14336\n"
PK"v2,v4,v5\n"
MF"v[32:35],a[104:107],a[8:11],v[32:35]\n"
WT"lgkmcnt(2)\n"
BL"v225,s[8:11],s44 offen offset:2048 sc1 lds\n"
EX"v6,v6\n"
MF"v[32:35],a[108:111],a[12:15],v[32:35]\n"
EX"v7,v7\n"
MF"v[40:43],a[108:111],a[28:31],v[40:43]\n"
EX"v12,v12\n"
MF"v[40:43],a[104:107],a[24:27],v[40:43]\n"
RD"a[124:127],v226 offset:15360\n"
"v_readlane_b32 s43,v228,s42\n"
PK"v3,v6,v7\n"
MF"v[48:51],a[104:107],a[40:43],v[48:51]\n"
EX"v13,v13\n"
MF"v[48:51],a[108:111],a[44:47],v[48:51]\n"
EX"v14,v14\n"
MF"v[56:59],a[108:111],a[60:63],v[56:59]\n"
BL"v225,s[8:11],s44 offen offset:3072 sc1 lds\n"
EX"v15,v15\n"
MF"v[56:59],a[104:107],a[56:59],v[56:59]\n"
"s_mov_b32 m0,s40\n"
RD"a[96:99],v227 offset:24576\n"
PK"v10,v12,v13\n"
MF"v[36:39],a[112:115],a[0:3],0\n"
WT"lgkmcnt(2)\n"
EX"v20,v20\n"
PK"v11,v14,v15\n"
MF"v[36:39],a[116:119],a[4:7],v[36:39]\n"
EX"v21,v21\n"
MF"v[44:47],a[116:119],a[20:23],0\n"
EX"v22,v22\n"
MF"v[44:47],a[112:115],a[16:19],v[44:47]\n"
RD"a[100:103],v227 offset:25600\n"
"v_readlane_b32 s44,v229,s42\n"
PK"v18,v20,v21\n"
MF"v[52:55],a[112:115],a[32:35],0\n"
SA"s42,s42,1\n"
BL"v225,s[8:11],s43 offen offset:0 sc1 lds\n"
MF"v[52:55],a[116:119],a[36:39],v[52:55]\n"
EX"v23,v23\n"
MF"v[60:63],a[116:119],a[52:55],0\n"
EX"v28,v28\n"
MF"v[60:63],a[112:115],a[48:51],v[60:63]\n"
RD"a[104:107],v227 offset:26624\n"
PK"v19,v22,v23\n"
MF"v[36:39],a[120:123],a[8:11],v[36:39]\n"
WT"lgkmcnt(2)\n"
EX"v29,v29\n"
MF"v[36:39],a[124:127],a[12:15],v[36:39]\n"
BL"v225,s[8:11],s43 offen offset:1024 sc1 lds\n"
MF"v[44:47],a[124:127],a[28:31],v[44:47]\n"
EX"v30,v30\n"
PK"v26,v28,v29\n"
MF"v[44:47],a[120:123],a[24:27],v[44:47]\n"
RD"a[108:111],v227 offset:27648\n"
MF"v[52:55],a[120:123],a[40:43],v[52:55]\n"
EX"v31,v31\n"
MF"v[52:55],a[124:127],a[44:47],v[52:55]\n"
EX"v32,v32\n"
MF"v[60:63],a[124:127],a[60:63],v[60:63]\n"
EX"v33,v33\n"
PK"v27,v30,v31\n"
MF"v[60:63],a[120:123],a[56:59],v[60:63]\n"
SA"s41,s41,32768\n"
BL"v225,s[8:11],s43 offen offset:2048 sc1 lds\n"
RD"a[112:115],v227 offset:28672\n"
MF"v[80:83],a[96:99],v[0:3],0\n"
"s_cmp_eq_u32 s41,163840\n"
EX"v34,v34\n"
MF"v[112:115],a[96:99],v[8:11],0\n"
"s_cselect_b32 s41,0,s41\n"
EX"v35,v35\n"
MF"v[144:147],a[96:99],v[16:19],0\n"
EX"v40,v40\n"
"v_add_u32 v226,s41,v224\n"
MF"v[176:179],a[96:99],v[24:27],0\n"
WT"lgkmcnt(1)\n"
RD"a[116:119],v227 offset:29696\n"
MF"v[84:87],a[100:103],v[0:3],0\n"
EX"v41,v41\n"
MF"v[116:119],a[100:103],v[8:11],0\n"
BL"v225,s[8:11],s43 offen offset:3072 sc1 lds\n"
MF"v[148:151],a[100:103],v[16:19],0\n"
SA"m0,s40,16384\n"
EX"v42,v42\n"
MF"v[180:183],a[100:103],v[24:27],0\n"
RD"a[120:123],v227 offset:30720\n"
MF"v[88:91],a[104:107],v[0:3],0\n"
EX"v43,v43\n"
MF"v[120:123],a[104:107],v[8:11],0\n"
EX"v48,v48\n"
MF"v[152:155],a[104:107],v[16:19],0\n"
SA"s40,s40,32768\n"
EX"v49,v49\n"
MF"v[184:187],a[104:107],v[24:27],0\n"
"s_cmp_eq_u32 s40,s46\n"
BL"v225,s[8:11],s44 offen offset:0 sc1 lds\n"
RD"a[124:127],v227 offset:31744\n"
MF"v[92:95],a[108:111],v[0:3],0\n"
"s_cselect_b32 s40,s47,s40\n"
EX"v50,v50\n"
MF"v[124:127],a[108:111],v[8:11],0\n"
EX"v51,v51\n"
MF"v[156:159],a[108:111],v[16:19],0\n"
EX"v56,v56\n"
MF"v[188:191],a[108:111],v[24:27],0\n"
WT"lgkmcnt(1)\n"
EX"v57,v57\n"
MF"v[96:99],a[112:115],v[0:3],0\n"
EX"v58,v58\n"
MF"v[128:131],a[112:115],v[8:11],0\n"
BL"v225,s[8:11],s44 offen offset:1024 sc1 lds\n"
MF"v[160:163],a[112:115],v[16:19],0\n"
EX"v59,v59\n"
MF"v[192:195],a[112:115],v[24:27],0\n"
EX"v36,v36\n"
MF"v[100:103],a[116:119],v[0:3],0\n"
WT"vmcnt(16) lgkmcnt(0)\n"
"s_barrier\n"
MF"v[132:135],a[116:119],v[8:11],0\n"
RD"a[64:67],v226 offset:0\n"
RD"a[68:71],v226 offset:1024\n"
"buffer_store_dwordx2 v[232:233],v230,s[8:11],s98 offen sc1\n"
MF"v[164:167],a[116:119],v[16:19],0\n"
EX"v37,v37\n"
MF"v[196:199],a[116:119],v[24:27],0\n"
BL"v225,s[8:11],s44 offen offset:2048 sc1 lds\n"
MF"v[104:107],a[120:123],v[0:3],0\n"
EX"v38,v38\n"
MF"v[136:139],a[120:123],v[8:11],0\n"
EX"v39,v39\n"
MF"v[168:171],a[120:123],v[16:19],0\n"
EX"v44,v44\n"
MF"v[200:203],a[120:123],v[24:27],0\n"
RD"a[72:75],v226 offset:2048\n"
RD"a[76:79],v226 offset:3072\n"
MF"v[108:111],a[124:127],v[0:3],0\n"
EX"v45,v45\n"
MF"v[140:143],a[124:127],v[8:11],0\n"
BL"v225,s[8:11],s44 offen offset:3072 sc1 lds\n"
MF"v[172:175],a[124:127],v[16:19],0\n"
EX"v46,v46\n"
MF"v[204:207],a[124:127],v[24:27],0\n"
RD"a[80:83],v226 offset:4096\n"
MF"a[132:135],a[128:131],v[0:3],0\n"
EX"v47,v47\n"
MF"a[136:139],a[128:131],v[8:11],0\n"
"buffer_load_dword v255,v253,s[12:15],s55 offen\n"
EX"v52,v52\n"
MF"a[140:143],a[128:131],v[16:19],0\n"
SA"s55,s55,163840\n"
EX"v53,v53\n"
MF"a[144:147],a[128:131],v[24:27],0\n"
RD"a[84:87],v226 offset:5120\n"
RD"a[88:91],v226 offset:6144\n"
EX"v54,v54\n"
EX"v55,v55\n"
EX"v60,v60\n"
EX"v61,v61\n"
EX"v62,v62\n"
EX"v63,v63\n"
"s_cmp_eq_u32 s57,1\n"
"s_cbranch_scc1 LEV_%=\n"
".p2align 6\n"
"LP1_%=:\n"
WT"lgkmcnt(6)\n"
MF"v[0:3],a[64:67],a[0:3],0\n"
WT"lgkmcnt(5)\n"
"v_readlane_b32 s43,v228,s42\n"
PK"v64,v32,v33\n"
MF"v[0:3],a[68:71],a[4:7],v[0:3]\n"
"s_mov_b32 m0,s40\n"
"v_readlane_b32 s44,v229,s42\n"
PK"v65,v34,v35\n"
MF"v[8:11],a[68:71],a[20:23],0\n"
PK"v66,v36,v37\n"
PK"v67,v38,v39\n"
MF"v[8:11],a[64:67],a[16:19],v[8:11]\n"
SA"s42,s42,1\n"
BL"v225,s[8:11],s43 offen offset:0 sc1 lds\n"
MF"v[16:19],a[64:67],a[32:35],0\n"
PK"v68,v40,v41\n"
PK"v69,v42,v43\n"
MF"v[16:19],a[68:71],a[36:39],v[16:19]\n"
RD"a[92:95],v226 offset:7168\n"
PK"v70,v44,v45\n"
MF"v[24:27],a[68:71],a[52:55],0\n"
PK"v71,v46,v47\n"
PK"v72,v48,v49\n"
MF"v[24:27],a[64:67],a[48:51],v[24:27]\n"
WT"lgkmcnt(1)\n"
BL"v225,s[8:11],s43 offen offset:1024 sc1 lds\n"
MF"v[0:3],a[72:75],a[8:11],v[0:3]\n"
PK"v73,v50,v51\n"
"v_mov_b32 v227,v226\n"
MF"v[0:3],a[76:79],a[12:15],v[0:3]\n"
PK"v74,v52,v53\n"
PK"v75,v54,v55\n"
MF"v[8:11],a[76:79],a[28:31],v[8:11]\n"
PK"v76,v56,v57\n"
PK"v77,v58,v59\n"
MF"v[8:11],a[72:75],a[24:27],v[8:11]\n"
PK"v78,v60,v61\n"
PK"v79,v62,v63\n"
MF"v[16:19],a[72:75],a[40:43],v[16:19]\n"
MF"v[16:19],a[76:79],a[44:47],v[16:19]\n"
RD"a[96:99],v226 offset:8192\n"
MF"v[24:27],a[76:79],a[60:63],v[24:27]\n"
EX"v0,v0\n"
MF"v[24:27],a[72:75],a[56:59],v[24:27]\n"
EX"v1,v1\n"
MF"v[4:7],a[80:83],a[0:3],0\n"
EX"v2,v2\n"
MF"v[4:7],a[84:87],a[4:7],v[4:7]\n"
RD"a[100:103],v226 offset:9216\n"
PK"v0,v0,v1\n"
MF"v[12:15],a[84:87],a[20:23],0\n"
EX"v3,v3\n"
MF"v[12:15],a[80:83],a[16:19],v[12:15]\n"
EX"v8,v8\n"
MF"v[20:23],a[80:83],a[32:35],0\n"
EX"v9,v9\n"
MF"v[20:23],a[84:87],a[36:39],v[20:23]\n"
RD"a[104:107],v226 offset:10240\n"
PK"v1,v2,v3\n"
MF"v[28:31],a[84:87],a[52:55],0\n"
EX"v10,v10\n"
MF"v[28:31],a[80:83],a[48:51],v[28:31]\n"
EX"v11,v11\n"
MF"v[4:7],a[88:91],a[8:11],v[4:7]\n"
WT"lgkmcnt(1)\n"
EX"v16,v16\n"
MF"v[4:7],a[92:95],a[12:15],v[4:7]\n"
RD"a[108:111],v226 offset:11264\n"
PK"v8,v8,v9\n"
MF"v[12:15],a[92:95],a[28:31],v[12:15]\n"
EX"v17,v17\n"
MF"v[12:15],a[88:91],a[24:27],v[12:15]\n"
EX"v18,v18\n"
MF"v[20:23],a[88:91],a[40:43],v[20:23]\n"
EX"v19,v19\n"
MF"v[20:23],a[92:95],a[44:47],v[20:23]\n"
RD"a[112:115],v226 offset:12288\n"
PK"v9,v10,v11\n"
MF"v[28:31],a[92:95],a[60:63],v[28:31]\n"
EX"v24,v24\n"
MF"v[28:31],a[88:91],a[56:59],v[28:31]\n"
EX"v25,v25\n"
MF"v[32:35],a[96:99],a[0:3],0\n"
EX"v26,v26\n"
MF"v[32:35],a[100:103],a[4:7],v[32:35]\n"
RD"a[116:119],v226 offset:13312\n"
PK"v16,v16,v17\n"
MF"v[40:43],a[100:103],a[20:23],0\n"
EX"v27,v27\n"
MF"v[40:43],a[96:99],a[16:19],v[40:43]\n"
EX"v4,v4\n"
MF"v[48:51],a[96:99],a[32:35],0\n"
EX"v5,v5\n"
MF"v[48:51],a[100:103],a[36:39],v[48:51]\n"
RD"a[120:123],v226 offset:14336\n"
PK"v17,v18,v19\n"
MF"v[56:59],a[100:103],a[52:55],0\n"
EX"v6,v6\n"
MF"v[56:59],a[96:99],a[48:51],v[56:59]\n"
WT"lgkmcnt(2)\n"
EX"v7,v7\n"
MF"v[32:35],a[104:107],a[8:11],v[32:35]\n"
EX"v12,v12\n"
MF"v[32:35],a[108:111],a[12:15],v[32:35]\n"
RD"a[124:127],v226 offset:15360\n"
PK"v24,v24,v25\n"
MF"v[40:43],a[108:111],a[28:31],v[40:43]\n"
EX"v13,v13\n"
MF"v[40:43],a[104:107],a[24:27],v[40:43]\n"
EX"v14,v14\n"
MF"v[48:51],a[104:107],a[40:43],v[48:51]\n"
EX"v15,v15\n"
MF"v[48:51],a[108:111],a[44:47],v[48:51]\n"
RD"a[64:67],v227 offset:16384\n"
PK"v25,v26,v27\n"
MF"v[56:59],a[108:111],a[60:63],v[56:59]\n"
EX"v20,v20\n"
MF"v[56:59],a[104:107],a[56:59],v[56:59]\n"
EX"v21,v21\n"
MF"v[36:39],a[112:115],a[0:3],0\n"
WT"lgkmcnt(1)\n"
EX"v22,v22\n"
MF"v[36:39],a[116:119],a[4:7],v[36:39]\n"
RD"a[68:71],v227 offset:24576\n"
PK"v2,v4,v5\n"
MF"v[44:47],a[116:119],a[20:23],0\n"
EX"v23,v23\n"
MF"v[44:47],a[112:115],a[16:19],v[44:47]\n"
EX"v28,v28\n"
MF"v[52:55],a[112:115],a[32:35],0\n"
EX"v29,v29\n"
MF"v[52:55],a[116:119],a[36:39],v[52:55]\n"
RD"a[72:75],v227 offset:17408\n"
PK"v3,v6,v7\n"
MF"v[60:63],a[116:119],a[52:55],0\n"
EX"v30,v30\n"
MF"v[60:63],a[112:115],a[48:51],v[60:63]\n"
EX"v31,v31\n"
MF"v[36:39],a[120:123],a[8:11],v[36:39]\n"
PK"v10,v12,v13\n"
PK"v11,v14,v15\n"
MF"v[36:39],a[124:127],a[12:15],v[36:39]\n"
RD"a[76:79],v227 offset:25600\n"
PK"v18,v20,v21\n"
MF"v[44:47],a[124:127],a[28:31],v[44:47]\n"
PK"v19,v22,v23\n"
PK"v26,v28,v29\n"
MF"v[44:47],a[120:123],a[24:27],v[44:47]\n"
PK"v27,v30,v31\n"
MF"v[52:55],a[120:123],a[40:43],v[52:55]\n"
MF"v[52:55],a[124:127],a[44:47],v[52:55]\n"
RD"a[80:83],v227 offset:18432\n"
MF"v[60:63],a[124:127],a[60:63],v[60:63]\n"
EX"v32,v32\n"
MF"v[60:63],a[120:123],a[56:59],v[60:63]\n"
WT"lgkmcnt(2)\n"
SA"s41,s41,32768\n"
"s_cmp_eq_u32 s41,163840\n"
MF"v[80:83],a[64:67],v[64:67],v[80:83]\n"
"s_cselect_b32 s41,0,s41\n"
EX"v33,v33\n"
MF"v[80:83],a[68:71],v[0:3],v[80:83]\n"
RD"a[84:87],v227 offset:26624\n"
"v_add_u32 v226,s41,v224\n"
MF"v[112:115],a[68:71],v[8:11],v[112:115]\n"
EX"v34,v34\n"
MF"v[112:115],a[64:67],v[68:71],v[112:115]\n"
MF"v[144:147],a[64:67],v[72:75],v[144:147]\n"
EX"v35,v35\n"
MF"v[144:147],a[68:71],v[16:19],v[144:147]\n"
RD"a[88:91],v227 offset:19456\n"
MF"v[176:179],a[68:71],v[24:27],v[176:179]\n"
EX"v40,v40\n"
MF"v[176:179],a[64:67],v[76:79],v[176:179]\n"
BL"v225,s[8:11],s43 offen offset:2048 sc1 lds\n"
MF"v[84:87],a[72:75],v[64:67],v[84:87]\n"
WT"lgkmcnt(1)\n"
EX"v41,v41\n"
MF"v[84:87],a[76:79],v[0:3],v[84:87]\n"
RD"a[92:95],v227 offset:27648\n"
MF"v[116:119],a[76:79],v[8:11],v[116:119]\n"
EX"v42,v42\n"
MF"v[116:119],a[72:75],v[68:71],v[116:119]\n"
MF"v[148:151],a[72:75],v[72:75],v[148:151]\n"
EX"v43,v43\n"
MF"v[148:151],a[76:79],v[16:19],v[148:151]\n"
RD"a[96:99],v227 offset:20480\n"
MF"v[180:183],a[76:79],v[24:27],v[180:183]\n"
EX"v48,v48\n"
MF"v[180:183],a[72:75],v[76:79],v[180:183]\n"
BL"v225,s[8:11],s43 offen offset:3072 sc1 lds\n"
MF"v[88:91],a[80:83],v[64:67],v[88:91]\n"
EX"v49,v49\n"
MF"v[88:91],a[84:87],v[0:3],v[88:91]\n"
RD"a[100:103],v227 offset:28672\n"
MF"v[120:123],a[84:87],v[8:11],v[120:123]\n"
EX"v50,v50\n"
MF"v[120:123],a[80:83],v[68:71],v[120:123]\n"
SA"m0,s40,16384\n"
MF"v[152:155],a[80:83],v[72:75],v[152:155]\n"
EX"v51,v51\n"
MF"v[152:155],a[84:87],v[16:19],v[152:155]\n"
RD"a[104:107],v227 offset:21504\n"
MF"v[184:187],a[84:87],v[24:27],v[184:187]\n"
EX"v56,v56\n"
MF"v[184:187],a[80:83],v[76:79],v[184:187]\n"
WT"lgkmcnt(2)\n"
BL"v225,s[8:11],s44 offen offset:0 sc1 lds\n"
MF"v[92:95],a[88:91],v[64:67],v[92:95]\n"
SA"s40,s40,32768\n"
EX"v57,v57\n"
MF"v[92:95],a[92:95],v[0:3],v[92:95]\n"
"s_cmp_eq_u32 s40,s46\n"
RD"a[108:111],v227 offset:29696\n"
"s_cselect_b32 s40,s47,s40\n"
MF"v[124:127],a[92:95],v[8:11],v[124:127]\n"
EX"v58,v58\n"
MF"v[124:127],a[88:91],v[68:71],v[124:127]\n"
MF"v[156:159],a[88:91],v[72:75],v[156:159]\n"
EX"v59,v59\n"
MF"v[156:159],a[92:95],v[16:19],v[156:159]\n"
RD"a[112:115],v227 offset:22528\n"
MF"v[188:191],a[92:95],v[24:27],v[188:191]\n"
EX"v36,v36\n"
MF"v[188:191],a[88:91],v[76:79],v[188:191]\n"
BL"v225,s[8:11],s44 offen offset:1024 sc1 lds\n"
MF"v[96:99],a[96:99],v[64:67],v[96:99]\n"
WT"lgkmcnt(1)\n"
EX"v37,v37\n"
MF"v[96:99],a[100:103],v[0:3],v[96:99]\n"
RD"a[116:119],v227 offset:30720\n"
MF"v[128:131],a[100:103],v[8:11],v[128:131]\n"
EX"v38,v38\n"
MF"v[128:131],a[96:99],v[68:71],v[128:131]\n"
MF"v[160:163],a[96:99],v[72:75],v[160:163]\n"
EX"v39,v39\n"
MF"v[160:163],a[100:103],v[16:19],v[160:163]\n"
RD"a[120:123],v227 offset:23552\n"
MF"v[192:195],a[100:103],v[24:27],v[192:195]\n"
EX"v44,v44\n"
MF"v[192:195],a[96:99],v[76:79],v[192:195]\n"
BL"v225,s[8:11],s44 offen offset:2048 sc1 lds\n"
MF"v[100:103],a[104:107],v[64:67],v[100:103]\n"
EX"v45,v45\n"
MF"v[100:103],a[108:111],v[0:3],v[100:103]\n"
RD"a[124:127],v227 offset:31744\n"
MF"v[132:135],a[108:111],v[8:11],v[132:135]\n"
EX"v46,v46\n"
MF"v[132:135],a[104:107],v[68:71],v[132:135]\n"
MF"v[164:167],a[104:107],v[72:75],v[164:167]\n"
EX"v47,v47\n"
MF"v[164:167],a[108:111],v[16:19],v[164:167]\n"
MF"v[196:199],a[108:111],v[24:27],v[196:199]\n"
EX"v52,v52\n"
MF"v[196:199],a[104:107],v[76:79],v[196:199]\n"
WT"lgkmcnt(1)\n"
BL"v225,s[8:11],s44 offen offset:3072 sc1 lds\n"
MF"v[104:107],a[112:115],v[64:67],v[104:107]\n"
EX"v53,v53\n"
MF"v[104:107],a[116:119],v[0:3],v[104:107]\n"
"buffer_load_dword v255,v253,s[12:15],s55 offen\n"
MF"v[136:139],a[116:119],v[8:11],v[136:139]\n"
EX"v54,v54\n"
MF"v[136:139],a[112:115],v[68:71],v[136:139]\n"
SA"s55,s55,163840\n"
MF"v[168:171],a[112:115],v[72:75],v[168:171]\n"
WT"vmcnt(17) lgkmcnt(0)\n"
"s_barrier\n"
MF"v[168:171],a[116:119],v[16:19],v[168:171]\n"
RD"a[64:67],v226 offset:0\n"
MF"v[200:203],a[116:119],v[24:27],v[200:203]\n"
EX"v55,v55\n"
MF"v[200:203],a[112:115],v[76:79],v[200:203]\n"
MF"v[108:111],a[120:123],v[64:67],v[108:111]\n"
EX"v60,v60\n"
MF"v[108:111],a[124:127],v[0:3],v[108:111]\n"
RD"a[68:71],v226 offset:1024\n"
MF"v[140:143],a[124:127],v[8:11],v[140:143]\n"
EX"v61,v61\n"
MF"v[140:143],a[120:123],v[68:71],v[140:143]\n"
RD"a[72:75],v226 offset:2048\n"
MF"v[172:175],a[120:123],v[72:75],v[172:175]\n"
EX"v62,v62\n"
MF"v[172:175],a[124:127],v[16:19],v[172:175]\n"
RD"a[76:79],v226 offset:3072\n"
MF"v[204:207],a[124:127],v[24:27],v[204:207]\n"
EX"v63,v63\n"
MF"v[204:207],a[120:123],v[76:79],v[204:207]\n"
MF"a[132:135],a[128:131],v[64:67],a[132:135]\n"
MF"a[132:135],a[128:131],v[0:3],a[132:135]\n"
RD"a[80:83],v226 offset:4096\n"
MF"a[136:139],a[128:131],v[8:11],a[136:139]\n"
MF"a[136:139],a[128:131],v[68:71],a[136:139]\n"
RD"a[84:87],v226 offset:5120\n"
MF"a[140:143],a[128:131],v[72:75],a[140:143]\n"
MF"a[140:143],a[128:131],v[16:19],a[140:143]\n"
RD"a[88:91],v226 offset:6144\n"
MF"a[144:147],a[128:131],v[24:27],a[144:147]\n"
MF"a[144:147],a[128:131],v[76:79],a[144:147]\n"
"s_sub_u32 s56,s56,1\n"
"s_cbranch_scc0 LP1_%=\n"
"s_cmp_eq_u32 s57,2\n"
"s_cbranch_scc1 LEV_%=\n"
"s_cmp_eq_u32 s57,3\n"
"s_cbranch_scc1 LRT_%=\n"
"LDA_%=:\n"
WT"lgkmcnt(6)\n"
MF"v[0:3],a[64:67],a[0:3],v[208:211]\n"
WT"lgkmcnt(5)\n"
"v_readlane_b32 s43,v228,s42\n"
PK"v64,v32,v33\n"
MF"v[0:3],a[68:71],a[4:7],v[0:3]\n"
"s_mov_b32 m0,s40\n"
"v_readlane_b32 s44,v229,s42\n"
PK"v65,v34,v35\n"
MF"v[8:11],a[68:71],a[20:23],0\n"
PK"v66,v36,v37\n"
PK"v67,v38,v39\n"
MF"v[8:11],a[64:67],a[16:19],v[8:11]\n"
SA"s42,s42,1\n"
BL"v225,s[8:11],s43 offen offset:0 sc1 lds\n"
MF"v[16:19],a[64:67],a[32:35],0\n"
PK"v68,v40,v41\n"
PK"v69,v42,v43\n"
MF"v[16:19],a[68:71],a[36:39],v[16:19]\n"
RD"a[92:95],v226 offset:7168\n"
PK"v70,v44,v45\n"
MF"v[24:27],a[68:71],a[52:55],0\n"
PK"v71,v46,v47\n"
PK"v72,v48,v49\n"
MF"v[24:27],a[64:67],a[48:51],v[24:27]\n"
WT"lgkmcnt(1)\n"
BL"v225,s[8:11],s43 offen offset:1024 sc1 lds\n"
MF"v[0:3],a[72:75],a[8:11],v[0:3]\n"
PK"v73,v50,v51\n"
"v_mov_b32 v227,v226\n"
MF"v[0:3],a[76:79],a[12:15],v[0:3]\n"
PK"v74,v52,v53\n"
PK"v75,v54,v55\n"
MF"v[8:11],a[76:79],a[28:31],v[8:11]\n"
PK"v76,v56,v57\n"
PK"v77,v58,v59\n"
MF"v[8:11],a[72:75],a[24:27],v[8:11]\n"
PK"v78,v60,v61\n"
PK"v79,v62,v63\n"
MF"v[16:19],a[72:75],a[40:43],v[16:19]\n"
MF"v[16:19],a[76:79],a[44:47],v[16:19]\n"
RD"a[96:99],v226 offset:8192\n"
MF"v[24:27],a[76:79],a[60:63],v[24:27]\n"
EX"v0,v0\n"
MF"v[24:27],a[72:75],a[56:59],v[24:27]\n"
EX"v1,v1\n"
MF"v[4:7],a[80:83],a[0:3],v[212:215]\n"
EX"v2,v2\n"
MF"v[4:7],a[84:87],a[4:7],v[4:7]\n"
RD"a[100:103],v226 offset:9216\n"
PK"v0,v0,v1\n"
MF"v[12:15],a[84:87],a[20:23],0\n"
EX"v3,v3\n"
MF"v[12:15],a[80:83],a[16:19],v[12:15]\n"
EX"v8,v8\n"
MF"v[20:23],a[80:83],a[32:35],0\n"
EX"v9,v9\n"
MF"v[20:23],a[84:87],a[36:39],v[20:23]\n"
RD"a[104:107],v226 offset:10240\n"
PK"v1,v2,v3\n"
MF"v[28:31],a[84:87],a[52:55],0\n"
EX"v10,v10\n"
MF"v[28:31],a[80:83],a[48:51],v[28:31]\n"
EX"v11,v11\n"
MF"v[4:7],a[88:91],a[8:11],v[4:7]\n"
WT"lgkmcnt(1)\n"
EX"v16,v16\n"
MF"v[4:7],a[92:95],a[12:15],v[4:7]\n"
RD"a[108:111],v226 offset:11264\n"
PK"v8,v8,v9\n"
MF"v[12:15],a[92:95],a[28:31],v[12:15]\n"
EX"v17,v17\n"
MF"v[12:15],a[88:91],a[24:27],v[12:15]\n"
EX"v18,v18\n"
MF"v[20:23],a[88:91],a[40:43],v[20:23]\n"
EX"v19,v19\n"
MF"v[20:23],a[92:95],a[44:47],v[20:23]\n"
RD"a[112:115],v226 offset:12288\n"
PK"v9,v10,v11\n"
MF"v[28:31],a[92:95],a[60:63],v[28:31]\n"
EX"v24,v24\n"
MF"v[28:31],a[88:91],a[56:59],v[28:31]\n"
EX"v25,v25\n"
MF"v[32:35],a[96:99],a[0:3],v[216:219]\n"
EX"v26,v26\n"
MF"v[32:35],a[100:103],a[4:7],v[32:35]\n"
RD"a[116:119],v226 offset:13312\n"
PK"v16,v16,v17\n"
MF"v[40:43],a[100:103],a[20:23],0\n"
EX"v27,v27\n"
MF"v[40:43],a[96:99],a[16:19],v[40:43]\n"
EX"v4,v4\n"
MF"v[48:51],a[96:99],a[32:35],0\n"
EX"v5,v5\n"
MF"v[48:51],a[100:103],a[36:39],v[48:51]\n"
RD"a[120:123],v226 offset:14336\n"
PK"v17,v18,v19\n"
MF"v[56:59],a[100:103],a[52:55],0\n"
EX"v6,v6\n"
MF"v[56:59],a[96:99],a[48:51],v[56:59]\n"
WT"lgkmcnt(2)\n"
EX"v7,v7\n"
MF"v[32:35],a[104:107],a[8:11],v[32:35]\n"
EX"v12,v12\n"
MF"v[32:35],a[108:111],a[12:15],v[32:35]\n"
RD"a[124:127],v226 offset:15360\n"
PK"v24,v24,v25\n"
MF"v[40:43],a[108:111],a[28:31],v[40:43]\n"
EX"v13,v13\n"
MF"v[40:43],a[104:107],a[24:27],v[40:43]\n"
EX"v14,v14\n"
MF"v[48:51],a[104:107],a[40:43],v[48:51]\n"
EX"v15,v15\n"
MF"v[48:51],a[108:111],a[44:47],v[48:51]\n"
RD"a[64:67],v227 offset:16384\n"
PK"v25,v26,v27\n"
MF"v[56:59],a[108:111],a[60:63],v[56:59]\n"
EX"v20,v20\n"
MF"v[56:59],a[104:107],a[56:59],v[56:59]\n"
EX"v21,v21\n"
MF"v[36:39],a[112:115],a[0:3],v[220:223]\n"
WT"lgkmcnt(1)\n"
EX"v22,v22\n"
MF"v[36:39],a[116:119],a[4:7],v[36:39]\n"
RD"a[68:71],v227 offset:24576\n"
PK"v2,v4,v5\n"
MF"v[44:47],a[116:119],a[20:23],0\n"
EX"v23,v23\n"
MF"v[44:47],a[112:115],a[16:19],v[44:47]\n"
EX"v28,v28\n"
MF"v[52:55],a[112:115],a[32:35],0\n"
EX"v29,v29\n"
MF"v[52:55],a[116:119],a[36:39],v[52:55]\n"
RD"a[72:75],v227 offset:17408\n"
PK"v3,v6,v7\n"
MF"v[60:63],a[116:119],a[52:55],0\n"
EX"v30,v30\n"
MF"v[60:63],a[112:115],a[48:51],v[60:63]\n"
EX"v31,v31\n"
MF"v[36:39],a[120:123],a[8:11],v[36:39]\n"
PK"v10,v12,v13\n"
PK"v11,v14,v15\n"
MF"v[36:39],a[124:127],a[12:15],v[36:39]\n"
RD"a[76:79],v227 offset:25600\n"
PK"v18,v20,v21\n"
MF"v[44:47],a[124:127],a[28:31],v[44:47]\n"
PK"v19,v22,v23\n"
PK"v26,v28,v29\n"
MF"v[44:47],a[120:123],a[24:27],v[44:47]\n"
PK"v27,v30,v31\n"
MF"v[52:55],a[120:123],a[40:43],v[52:55]\n"
MF"v[52:55],a[124:127],a[44:47],v[52:55]\n"
RD"a[80:83],v227 offset:18432\n"
MF"v[60:63],a[124:127],a[60:63],v[60:63]\n"
EX"v32,v32\n"
MF"v[60:63],a[120:123],a[56:59],v[60:63]\n"
WT"lgkmcnt(2)\n"
SA"s41,s41,32768\n"
"s_cmp_eq_u32 s41,163840\n"
MF"v[80:83],a[64:67],v[64:67],v[80:83]\n"
"s_cselect_b32 s41,0,s41\n"
EX"v33,v33\n"
MF"v[80:83],a[68:71],v[0:3],v[80:83]\n"
RD"a[84:87],v227 offset:26624\n"
"v_add_u32 v226,s41,v224\n"
MF"v[112:115],a[68:71],v[8:11],v[112:115]\n"
EX"v34,v34\n"
MF"v[112:115],a[64:67],v[68:71],v[112:115]\n"
MF"v[144:147],a[64:67],v[72:75],v[144:147]\n"
EX"v35,v35\n"
MF"v[144:147],a[68:71],v[16:19],v[144:147]\n"
RD"a[88:91],v227 offset:19456\n"
MF"v[176:179],a[68:71],v[24:27],v[176:179]\n"
EX"v40,v40\n"
MF"v[176:179],a[64:67],v[76:79],v[176:179]\n"
BL"v225,s[8:11],s43 offen offset:2048 sc1 lds\n"
MF"v[84:87],a[72:75],v[64:67],v[84:87]\n"
WT"lgkmcnt(1)\n"
EX"v41,v41\n"
MF"v[84:87],a[76:79],v[0:3],v[84:87]\n"
RD"a[92:95],v227 offset:27648\n"
MF"v[116:119],a[76:79],v[8:11],v[116:119]\n"
EX"v42,v42\n"
MF"v[116:119],a[72:75],v[68:71],v[116:119]\n"
MF"v[148:151],a[72:75],v[72:75],v[148:151]\n"
EX"v43,v43\n"
MF"v[148:151],a[76:79],v[16:19],v[148:151]\n"
RD"a[96:99],v227 offset:20480\n"
MF"v[180:183],a[76:79],v[24:27],v[180:183]\n"
EX"v48,v48\n"
MF"v[180:183],a[72:75],v[76:79],v[180:183]\n"
BL"v225,s[8:11],s43 offen offset:3072 sc1 lds\n"
MF"v[88:91],a[80:83],v[64:67],v[88:91]\n"
EX"v49,v49\n"
MF"v[88:91],a[84:87],v[0:3],v[88:91]\n"
RD"a[100:103],v227 offset:28672\n"
MF"v[120:123],a[84:87],v[8:11],v[120:123]\n"
EX"v50,v50\n"
MF"v[120:123],a[80:83],v[68:71],v[120:123]\n"
SA"m0,s40,16384\n"
MF"v[152:155],a[80:83],v[72:75],v[152:155]\n"
EX"v51,v51\n"
MF"v[152:155],a[84:87],v[16:19],v[152:155]\n"
RD"a[104:107],v227 offset:21504\n"
MF"v[184:187],a[84:87],v[24:27],v[184:187]\n"
EX"v56,v56\n"
MF"v[184:187],a[80:83],v[76:79],v[184:187]\n"
WT"lgkmcnt(2)\n"
BL"v225,s[8:11],s44 offen offset:0 sc1 lds\n"
MF"v[92:95],a[88:91],v[64:67],v[92:95]\n"
SA"s40,s40,32768\n"
EX"v57,v57\n"
MF"v[92:95],a[92:95],v[0:3],v[92:95]\n"
"s_cmp_eq_u32 s40,s46\n"
RD"a[108:111],v227 offset:29696\n"
"s_cselect_b32 s40,s47,s40\n"
MF"v[124:127],a[92:95],v[8:11],v[124:127]\n"
EX"v58,v58\n"
MF"v[124:127],a[88:91],v[68:71],v[124:127]\n"
MF"v[156:159],a[88:91],v[72:75],v[156:159]\n"
EX"v59,v59\n"
MF"v[156:159],a[92:95],v[16:19],v[156:159]\n"
RD"a[112:115],v227 offset:22528\n"
MF"v[188:191],a[92:95],v[24:27],v[188:191]\n"
EX"v36,v36\n"
MF"v[188:191],a[88:91],v[76:79],v[188:191]\n"
BL"v225,s[8:11],s44 offen offset:1024 sc1 lds\n"
MF"v[96:99],a[96:99],v[64:67],v[96:99]\n"
WT"lgkmcnt(1)\n"
EX"v37,v37\n"
MF"v[96:99],a[100:103],v[0:3],v[96:99]\n"
RD"a[116:119],v227 offset:30720\n"
MF"v[128:131],a[100:103],v[8:11],v[128:131]\n"
EX"v38,v38\n"
MF"v[128:131],a[96:99],v[68:71],v[128:131]\n"
MF"v[160:163],a[96:99],v[72:75],v[160:163]\n"
EX"v39,v39\n"
MF"v[160:163],a[100:103],v[16:19],v[160:163]\n"
RD"a[120:123],v227 offset:23552\n"
MF"v[192:195],a[100:103],v[24:27],v[192:195]\n"
EX"v44,v44\n"
MF"v[192:195],a[96:99],v[76:79],v[192:195]\n"
BL"v225,s[8:11],s44 offen offset:2048 sc1 lds\n"
MF"v[100:103],a[104:107],v[64:67],v[100:103]\n"
EX"v45,v45\n"
MF"v[100:103],a[108:111],v[0:3],v[100:103]\n"
RD"a[124:127],v227 offset:31744\n"
MF"v[132:135],a[108:111],v[8:11],v[132:135]\n"
EX"v46,v46\n"
MF"v[132:135],a[104:107],v[68:71],v[132:135]\n"
MF"v[164:167],a[104:107],v[72:75],v[164:167]\n"
EX"v47,v47\n"
MF"v[164:167],a[108:111],v[16:19],v[164:167]\n"
MF"v[196:199],a[108:111],v[24:27],v[196:199]\n"
EX"v52,v52\n"
MF"v[196:199],a[104:107],v[76:79],v[196:199]\n"
WT"lgkmcnt(1)\n"
BL"v225,s[8:11],s44 offen offset:3072 sc1 lds\n"
MF"v[104:107],a[112:115],v[64:67],v[104:107]\n"
EX"v53,v53\n"
MF"v[104:107],a[116:119],v[0:3],v[104:107]\n"
MF"v[136:139],a[116:119],v[8:11],v[136:139]\n"
EX"v54,v54\n"
MF"v[136:139],a[112:115],v[68:71],v[136:139]\n"
MF"v[168:171],a[112:115],v[72:75],v[168:171]\n"
WT"vmcnt(16) lgkmcnt(0)\n"
"s_barrier\n"
MF"v[168:171],a[116:119],v[16:19],v[168:171]\n"
RD"a[64:67],v226 offset:0\n"
MF"v[200:203],a[116:119],v[24:27],v[200:203]\n"
EX"v55,v55\n"
MF"v[200:203],a[112:115],v[76:79],v[200:203]\n"
MF"v[108:111],a[120:123],v[64:67],v[108:111]\n"
EX"v60,v60\n"
MF"v[108:111],a[124:127],v[0:3],v[108:111]\n"
RD"a[68:71],v226 offset:1024\n"
MF"v[140:143],a[124:127],v[8:11],v[140:143]\n"
EX"v61,v61\n"
MF"v[140:143],a[120:123],v[68:71],v[140:143]\n"
RD"a[72:75],v226 offset:2048\n"
MF"v[172:175],a[120:123],v[72:75],v[172:175]\n"
EX"v62,v62\n"
MF"v[172:175],a[124:127],v[16:19],v[172:175]\n"
RD"a[76:79],v226 offset:3072\n"
MF"v[204:207],a[124:127],v[24:27],v[204:207]\n"
EX"v63,v63\n"
MF"v[204:207],a[120:123],v[76:79],v[204:207]\n"
MF"a[132:135],a[128:131],v[64:67],a[132:135]\n"
MF"a[132:135],a[128:131],v[0:3],a[132:135]\n"
RD"a[80:83],v226 offset:4096\n"
MF"a[136:139],a[128:131],v[8:11],a[136:139]\n"
MF"a[136:139],a[128:131],v[68:71],a[136:139]\n"
RD"a[84:87],v226 offset:5120\n"
MF"a[140:143],a[128:131],v[72:75],a[140:143]\n"
MF"a[140:143],a[128:131],v[16:19],a[140:143]\n"
RD"a[88:91],v226 offset:6144\n"
MF"a[144:147],a[128:131],v[24:27],a[144:147]\n"
MF"a[144:147],a[128:131],v[76:79],a[144:147]\n"
"LDB_%=:\n"
WT"lgkmcnt(6)\n"
MF"v[8:11],a[64:67],a[16:19],v[208:211]\n"
WT"lgkmcnt(5)\n"
"v_readlane_b32 s43,v228,s42\n"
PK"v64,v32,v33\n"
MF"v[8:11],a[68:71],a[20:23],v[8:11]\n"
RD"a[92:95],v226 offset:7168\n"
PK"v65,v34,v35\n"
MF"v[16:19],a[68:71],a[36:39],0\n"
PK"v66,v36,v37\n"
PK"v67,v38,v39\n"
MF"v[16:19],a[64:67],a[32:35],v[16:19]\n"
"s_mov_b32 m0,s40\n"
PK"v68,v40,v41\n"
PK"v69,v42,v43\n"
MF"v[24:27],a[64:67],a[48:51],0\n"
PK"v70,v44,v45\n"
PK"v71,v46,v47\n"
MF"v[24:27],a[68:71],a[52:55],v[24:27]\n"
WT"lgkmcnt(1)\n"
RD"a[96:99],v226 offset:8192\n"
"v_readlane_b32 s44,v229,s42\n"
MF"v[8:11],a[72:75],a[24:27],v[8:11]\n"
PK"v72,v48,v49\n"
PK"v73,v50,v51\n"
MF"v[8:11],a[76:79],a[28:31],v[8:11]\n"
SA"s42,s42,1\n"
BL"v225,s[8:11],s43 offen offset:0 sc1 lds\n"
"v_mov_b32 v227,v226\n"
MF"v[16:19],a[76:79],a[44:47],v[16:19]\n"
PK"v74,v52,v53\n"
PK"v75,v54,v55\n"
MF"v[16:19],a[72:75],a[40:43],v[16:19]\n"
RD"a[100:103],v226 offset:9216\n"
PK"v76,v56,v57\n"
PK"v77,v58,v59\n"
MF"v[24:27],a[72:75],a[56:59],v[24:27]\n"
PK"v78,v60,v61\n"
PK"v79,v62,v63\n"
MF"v[24:27],a[76:79],a[60:63],v[24:27]\n"
RD"a[104:107],v226 offset:10240\n"
MF"v[12:15],a[80:83],a[16:19],v[212:215]\n"
EX"v8,v8\n"
MF"v[12:15],a[84:87],a[20:23],v[12:15]\n"
RD"a[108:111],v226 offset:11264\n"
"v_lshrrev_b32 v235,7,v224\n"
MF"v[20:23],a[84:87],a[36:39],0\n"
EX"v9,v9\n"
MF"v[20:23],a[80:83],a[32:35],v[20:23]\n"
BL"v225,s[8:11],s43 offen offset:1024 sc1 lds\n"
MF"v[28:31],a[80:83],a[48:51],0\n"
EX"v10,v10\n"
"v_lshrrev_b32 v230,1,v224\n"
MF"v[28:31],a[84:87],a[52:55],v[28:31]\n"
RD"a[112:115],v226 offset:12288\n"
"v_lshl_add_u32 v230,v235,27,v230\n"
MF"v[12:15],a[88:91],a[24:27],v[12:15]\n"
WT"lgkmcnt(2)\n"
EX"v11,v11\n"
PK"v8,v8,v9\n"
MF"v[12:15],a[92:95],a[28:31],v[12:15]\n"
RD"a[116:119],v226 offset:13312\n"
PK"v9,v10,v11\n"
MF"v[20:23],a[92:95],a[44:47],v[20:23]\n"
"s_lshl_b32 s48,s35,8\n"
EX"v16,v16\n"
MF"v[20:23],a[88:91],a[40:43],v[20:23]\n"
MF"v[28:31],a[88:91],a[56:59],v[28:31]\n"
SA"s48,s48,92160\n"
EX"v17,v17\n"
MF"v[28:31],a[92:95],a[60:63],v[28:31]\n"
BL"v225,s[8:11],s43 offen offset:2048 sc1 lds\n"
MF"v[40:43],a[96:99],a[16:19],v[216:219]\n"
"s_andn2_b32 s49,s61,255\n"
EX"v18,v18\n"
MF"v[40:43],a[100:103],a[20:23],v[40:43]\n"
RD"a[120:123],v226 offset:14336\n"
PK"v16,v16,v17\n"
MF"v[48:51],a[100:103],a[36:39],0\n"
EX"v19,v19\n"
MF"v[48:51],a[96:99],a[32:35],v[48:51]\n"
RD"a[124:127],v226 offset:15360\n"
"v_bfe_u32 v236,v224,4,2\n"
MF"v[56:59],a[96:99],a[48:51],0\n"
EX"v24,v24\n"
MF"v[56:59],a[100:103],a[52:55],v[56:59]\n"
PK"v17,v18,v19\n"
"v_lshl_add_u32 v236,v236,6,s49\n"
MF"v[40:43],a[104:107],a[24:27],v[40:43]\n"
WT"lgkmcnt(2)\n"
EX"v25,v25\n"
MF"v[40:43],a[108:111],a[28:31],v[40:43]\n"
BL"v225,s[8:11],s43 offen offset:3072 sc1 lds\n"
MF"v[48:51],a[108:111],a[44:47],v[48:51]\n"
EX"v26,v26\n"
MF"v[48:51],a[104:107],a[40:43],v[48:51]\n"
RD"a[64:67],v227 offset:16384\n"
"v_mov_b32 v237,s48\n"
MF"v[56:59],a[104:107],a[56:59],v[56:59]\n"
EX"v27,v27\n"
PK"v24,v24,v25\n"
MF"v[56:59],a[108:111],a[60:63],v[56:59]\n"
SA"m0,s40,16384\n"
"v_cmp_lt_u32 vcc,511,v224\n"
PK"v25,v26,v27\n"
MF"v[44:47],a[112:115],a[16:19],v[220:223]\n"
EX"v12,v12\n"
MF"v[44:47],a[116:119],a[20:23],v[44:47]\n"
RD"a[68:71],v227 offset:17408\n"
"v_cndmask_b32 v236,v236,v237,vcc\n"
MF"v[52:55],a[116:119],a[36:39],0\n"
EX"v13,v13\n"
MF"v[52:55],a[112:115],a[32:35],v[52:55]\n"
BL"v225,s[8:11],s44 offen offset:0 sc1 lds\n"
MF"v[60:63],a[112:115],a[48:51],0\n"
SA"s40,s40,32768\n"
EX"v14,v14\n"
MF"v[60:63],a[116:119],a[52:55],v[60:63]\n"
WT"lgkmcnt(1)\n"
RD"a[72:75],v227 offset:18432\n"
PK"v10,v12,v13\n"
MF"v[44:47],a[120:123],a[24:27],v[44:47]\n"
"s_cmp_eq_u32 s40,s46\n"
EX"v15,v15\n"
MF"v[44:47],a[124:127],a[28:31],v[44:47]\n"
"s_cselect_b32 s40,s47,s40\n"
PK"v11,v14,v15\n"
MF"v[52:55],a[124:127],a[44:47],v[52:55]\n"
EX"v20,v20\n"
MF"v[52:55],a[120:123],a[40:43],v[52:55]\n"
RD"a[76:79],v227 offset:19456\n"
MF"v[60:63],a[120:123],a[56:59],v[60:63]\n"
EX"v21,v21\n"
MF"v[60:63],a[124:127],a[60:63],v[60:63]\n"
SA"s41,s41,32768\n"
BL"v225,s[8:11],s44 offen offset:1024 sc1 lds\n"
MF"v[80:83],a[64:67],v[64:67],v[80:83]\n"
"s_cmp_eq_u32 s41,163840\n"
EX"v22,v22\n"
MF"v[112:115],a[64:67],v[68:71],v[112:115]\n"
"s_cselect_b32 s41,0,s41\n"
RD"a[80:83],v227 offset:20480\n"
"v_add_u32 v226,s41,v224\n"
MF"v[144:147],a[64:67],v[72:75],v[144:147]\n"
EX"v23,v23\n"
PK"v18,v20,v21\n"
MF"v[176:179],a[64:67],v[76:79],v[176:179]\n"
WT"lgkmcnt(2)\n"
PK"v19,v22,v23\n"
MF"v[84:87],a[68:71],v[64:67],v[84:87]\n"
EX"v28,v28\n"
MF"v[116:119],a[68:71],v[68:71],v[116:119]\n"
RD"a[84:87],v227 offset:21504\n"
MF"v[148:151],a[68:71],v[72:75],v[148:151]\n"
EX"v29,v29\n"
MF"v[180:183],a[68:71],v[76:79],v[180:183]\n"
BL"v225,s[8:11],s44 offen offset:2048 sc1 lds\n"
MF"v[88:91],a[72:75],v[64:67],v[88:91]\n"
EX"v30,v30\n"
MF"v[120:123],a[72:75],v[68:71],v[120:123]\n"
RD"a[88:91],v227 offset:22528\n"
PK"v26,v28,v29\n"
MF"v[152:155],a[72:75],v[72:75],v[152:155]\n"
EX"v31,v31\n"
MF"v[184:187],a[72:75],v[76:79],v[184:187]\n"
WT"lgkmcnt(2)\n"
PK"v27,v30,v31\n"
MF"v[92:95],a[76:79],v[64:67],v[92:95]\n"
EX"v40,v40\n"
MF"v[124:127],a[76:79],v[68:71],v[124:127]\n"
RD"a[92:95],v227 offset:23552\n"
MF"v[156:159],a[76:79],v[72:75],v[156:159]\n"
EX"v41,v41\n"
MF"v[188:191],a[76:79],v[76:79],v[188:191]\n"
BL"v225,s[8:11],s44 offen offset:3072 sc1 lds\n"
MF"v[96:99],a[80:83],v[64:67],v[96:99]\n"
EX"v42,v42\n"
MF"v[128:131],a[80:83],v[68:71],v[128:131]\n"
RD"a[96:99],v227 offset:24576\n"
MF"v[160:163],a[80:83],v[72:75],v[160:163]\n"
EX"v43,v43\n"
MF"v[192:195],a[80:83],v[76:79],v[192:195]\n"
WT"lgkmcnt(2)\n"
RD"a[100:103],v227 offset:25600\n"
MF"v[100:103],a[84:87],v[64:67],v[100:103]\n"
EX"v48,v48\n"
MF"v[132:135],a[84:87],v[68:71],v[132:135]\n"
MF"v[164:167],a[84:87],v[72:75],v[164:167]\n"
EX"v49,v49\n"
MF"v[196:199],a[84:87],v[76:79],v[196:199]\n"
MF"v[104:107],a[88:91],v[64:67],v[104:107]\n"
EX"v50,v50\n"
MF"v[136:139],a[88:91],v[68:71],v[136:139]\n"
RD"a[104:107],v227 offset:26624\n"
MF"v[168:171],a[88:91],v[72:75],v[168:171]\n"
EX"v51,v51\n"
MF"v[200:203],a[88:91],v[76:79],v[200:203]\n"
WT"lgkmcnt(1)\n"
RD"a[108:111],v227 offset:27648\n"
MF"v[108:111],a[92:95],v[64:67],v[108:111]\n"
EX"v44,v44\n"
MF"v[140:143],a[92:95],v[68:71],v[140:143]\n"
RD"a[112:115],v227 offset:28672\n"
MF"v[172:175],a[92:95],v[72:75],v[172:175]\n"
EX"v45,v45\n"
MF"v[204:207],a[92:95],v[76:79],v[204:207]\n"
MF"a[132:135],a[128:131],v[64:67],a[132:135]\n"
EX"v46,v46\n"
MF"a[136:139],a[128:131],v[68:71],a[136:139]\n"
RD"a[116:119],v227 offset:29696\n"
PK"v68,v40,v41\n"
PK"v69,v42,v43\n"
MF"a[140:143],a[128:131],v[72:75],a[140:143]\n"
EX"v47,v47\n"
PK"v72,v48,v49\n"
MF"a[144:147],a[128:131],v[76:79],a[144:147]\n"
RD"a[120:123],v227 offset:30720\n"
PK"v73,v50,v51\n"
PK"v70,v44,v45\n"
MF"v[112:115],a[96:99],v[8:11],v[112:115]\n"
EX"v52,v52\n"
MF"v[144:147],a[96:99],v[16:19],v[144:147]\n"
RD"a[124:127],v227 offset:31744\n"
PK"v71,v46,v47\n"
MF"v[176:179],a[96:99],v[24:27],v[176:179]\n"
EX"v53,v53\n"
MF"v[116:119],a[100:103],v[8:11],v[116:119]\n"
"v_accvgpr_read_b32 v251,a132\n"
"v_subrev_u32 v253,0x2b800000,v251\n"
MF"v[148:151],a[100:103],v[16:19],v[148:151]\n"
"v_cmp_le_u32 vcc,0x34000000,v253\n"
"s_cmp_lg_u64 vcc,0\n"
"s_cbranch_scc1 L1014_%=\n"
"L1013_%=:\n"
MF"v[180:183],a[100:103],v[24:27],v[180:183]\n"
WT"lgkmcnt(2)\n"
EX"v54,v54\n"
MF"v[120:123],a[104:107],v[8:11],v[120:123]\n"
"v_rcp_f32 v251,v251\n"
PK"v74,v52,v53\n"
MF"v[152:155],a[104:107],v[16:19],v[152:155]\n"
EX"v55,v55\n"
MF"v[184:187],a[104:107],v[24:27],v[184:187]\n"
WT"vmcnt(16) lgkmcnt(0)\n"
"s_barrier\n"
MF"v[124:127],a[108:111],v[8:11],v[124:127]\n"
RD"a[64:67],v226 offset:0\n"
MU"v80,v251,v80\n"
MU"v81,v251,v81\n"
MF"v[156:159],a[108:111],v[16:19],v[156:159]\n"
MU"v82,v251,v82\n"
MU"v83,v251,v83\n"
PK"v75,v54,v55\n"
MF"v[188:191],a[108:111],v[24:27],v[188:191]\n"
MU"v84,v251,v84\n"
MU"v85,v251,v85\n"
MU"v86,v251,v86\n"
MF"v[128:131],a[112:115],v[8:11],v[128:131]\n"
MU"v87,v251,v87\n"
PK"v80,v80,v81\n"
PK"v81,v82,v83\n"
MF"v[160:163],a[112:115],v[16:19],v[160:163]\n"
RD"a[68:71],v226 offset:1024\n"
PK"v82,v84,v85\n"
PK"v83,v86,v87\n"
MF"v[192:195],a[112:115],v[24:27],v[192:195]\n"
BS"v[80:83],v224,s[8:11],s59 offen offset:0 sc1\n"
MU"v88,v251,v88\n"
MF"v[132:135],a[116:119],v[8:11],v[132:135]\n"
RD"a[72:75],v226 offset:2048\n"
MU"v89,v251,v89\n"
MU"v90,v251,v90\n"
MF"v[164:167],a[116:119],v[16:19],v[164:167]\n"
MU"v91,v251,v91\n"
MU"v92,v251,v92\n"
MU"v93,v251,v93\n"
MF"v[196:199],a[116:119],v[24:27],v[196:199]\n"
RD"a[76:79],v226 offset:3072\n"
MU"v94,v251,v94\n"
MU"v95,v251,v95\n"
MF"v[136:139],a[120:123],v[8:11],v[136:139]\n"
PK"v88,v88,v89\n"
PK"v89,v90,v91\n"
MF"v[168:171],a[120:123],v[16:19],v[168:171]\n"
PK"v90,v92,v93\n"
PK"v91,v94,v95\n"
MF"v[200:203],a[120:123],v[24:27],v[200:203]\n"
BS"v[88:91],v224,s[8:11],s59 offen offset:1024 sc1\n"
MU"v96,v251,v96\n"
MF"v[140:143],a[124:127],v[8:11],v[140:143]\n"
RD"a[80:83],v226 offset:4096\n"
MU"v97,v251,v97\n"
MU"v98,v251,v98\n"
MF"v[172:175],a[124:127],v[16:19],v[172:175]\n"
MU"v99,v251,v99\n"
MU"v100,v251,v100\n"
MU"v101,v251,v101\n"
MF"v[204:207],a[124:127],v[24:27],v[204:207]\n"
MU"v104,v251,v104\n"
MU"v105,v251,v105\n"
MU"v102,v251,v102\n"
MF"a[136:139],a[128:131],v[8:11],a[136:139]\n"
PK"v96,v96,v97\n"
MU"v106,v251,v106\n"
MU"v103,v251,v103\n"
MF"a[140:143],a[128:131],v[16:19],a[140:143]\n"
MU"v107,v251,v107\n"
PK"v97,v98,v99\n"
PK"v98,v100,v101\n"
MF"a[144:147],a[128:131],v[24:27],a[144:147]\n"
MU"v108,v251,v108\n"
MU"v109,v251,v109\n"
PK"v99,v102,v103\n"
BS"v[96:99],v224,s[8:11],s59 offen offset:2048 sc1\n"
MU"v110,v251,v110\n"
MU"v111,v251,v111\n"
PK"v104,v104,v105\n"
PK"v105,v106,v107\n"
PK"v106,v108,v109\n"
PK"v107,v110,v111\n"
BS"v[104:107],v224,s[8:11],s59 offen offset:3072 sc1\n"
RD"a[84:87],v226 offset:5120\n"
RD"a[88:91],v226 offset:6144\n"
"s_cmp_eq_u32 s34,7\n"
"s_cbranch_scc1 LD2_%=\n"
WT"lgkmcnt(6)\n"
MF"v[0:3],a[64:67],a[32:35],0\n"
WT"lgkmcnt(5)\n"
EX"v56,v56\n"
MF"v[0:3],a[68:71],a[36:39],v[0:3]\n"
"s_mov_b32 m0,s40\n"
EX"v57,v57\n"
MF"v[8:11],a[68:71],a[52:55],0\n"
"s_lshl_b32 s50,s35,6\n"
RD"a[92:95],v226 offset:7168\n"
"v_readlane_b32 s43,v228,s42\n"
MF"v[8:11],a[64:67],a[48:51],v[8:11]\n"
WT"lgkmcnt(5)\n"
EX"v58,v58\n"
MF"v[0:3],a[72:75],a[40:43],v[0:3]\n"
WT"lgkmcnt(1)\n"
BL"v225,s[8:11],s43 offen offset:0 sc1 lds\n"
"v_readlane_b32 s44,v229,s42\n"
MF"v[0:3],a[76:79],a[44:47],v[0:3]\n"
SA"s50,s50,4352\n"
RD"a[96:99],v226 offset:8192\n"
RD"a[100:103],v226 offset:9216\n"
PK"v76,v56,v57\n"
MF"v[8:11],a[76:79],a[60:63],v[8:11]\n"
SA"s42,s42,1\n"
EX"v59,v59\n"
MF"v[8:11],a[72:75],a[56:59],v[8:11]\n"
SA"s41,s41,32768\n"
RD"a[104:107],v226 offset:10240\n"
"v_lshrrev_b32 v237,1,v224\n"
MF"v[4:7],a[80:83],a[32:35],0\n"
"s_cmp_eq_u32 s41,163840\n"
EX"v60,v60\n"
"v_and_b32 v237,56,v237\n"
MF"v[4:7],a[84:87],a[36:39],v[4:7]\n"
"s_cselect_b32 s41,0,s41\n"
EX"v61,v61\n"
"v_add_u32 v227,s41,v224\n"
MF"v[12:15],a[84:87],a[52:55],0\n"
RD"a[108:111],v226 offset:11264\n"
PK"v77,v58,v59\n"
MF"v[12:15],a[80:83],a[48:51],v[12:15]\n"
BL"v225,s[8:11],s43 offen offset:1024 sc1 lds\n"
MF"v[4:7],a[88:91],a[40:43],v[4:7]\n"
WT"lgkmcnt(2)\n"
EX"v62,v62\n"
MF"v[4:7],a[92:95],a[44:47],v[4:7]\n"
RD"a[112:115],v226 offset:12288\n"
RD"a[116:119],v226 offset:13312\n"
PK"v78,v60,v61\n"
MF"v[12:15],a[92:95],a[60:63],v[12:15]\n"
EX"v63,v63\n"
MF"v[12:15],a[88:91],a[56:59],v[12:15]\n"
RD"a[120:123],v226 offset:14336\n"
PK"v79,v62,v63\n"
MF"v[16:19],a[96:99],a[32:35],0\n"
EX"v0,v0\n"
MF"v[16:19],a[100:103],a[36:39],v[16:19]\n"
EX"v1,v1\n"
MF"v[24:27],a[100:103],a[52:55],0\n"
RD"a[124:127],v226 offset:15360\n"
PK"v0,v0,v1\n"
MF"v[24:27],a[96:99],a[48:51],v[24:27]\n"
WT"lgkmcnt(4)\n"
BL"v225,s[8:11],s43 offen offset:2048 sc1 lds\n"
MF"v[16:19],a[104:107],a[40:43],v[16:19]\n"
"s_lshl_b32 s56,s34,1\n"
EX"v2,v2\n"
MF"v[16:19],a[108:111],a[44:47],v[16:19]\n"
"s_sub_u32 s56,12,s56\n"
RD"a[64:67],v226 offset:16384\n"
RD"a[68:71],v226 offset:17408\n"
MF"v[24:27],a[108:111],a[60:63],v[24:27]\n"
"s_sub_u32 s22,s56,2\n"
EX"v3,v3\n"
MF"v[24:27],a[104:107],a[56:59],v[24:27]\n"
WT"lgkmcnt(3)\n"
RD"a[72:75],v226 offset:18432\n"
PK"v1,v2,v3\n"
MF"v[20:23],a[112:115],a[32:35],0\n"
"s_min_u32 s56,s56,1\n"
EX"v8,v8\n"
MF"v[20:23],a[116:119],a[36:39],v[20:23]\n"
"s_mov_b32 s57,s61\n"
EX"v9,v9\n"
MF"v[28:31],a[116:119],a[52:55],0\n"
BL"v225,s[8:11],s43 offen offset:3072 sc1 lds\n"
MF"v[28:31],a[112:115],a[48:51],v[28:31]\n"
RD"a[76:79],v226 offset:19456\n"
RD"a[80:83],v226 offset:20480\n"
MF"v[20:23],a[120:123],a[40:43],v[20:23]\n"
WT"lgkmcnt(3)\n"
EX"v10,v10\n"
PK"v8,v8,v9\n"
MF"v[20:23],a[124:127],a[44:47],v[20:23]\n"
SA"m0,s40,16384\n"
RD"a[84:87],v226 offset:21504\n"
RD"a[88:91],v226 offset:22528\n"
EX"v11,v11\n"
MF"v[28:31],a[124:127],a[60:63],v[28:31]\n"
EX"v4,v4\n"
MF"v[28:31],a[120:123],a[56:59],v[28:31]\n"
"buffer_load_dwordx2 v[238:239],v237,s[8:11],s50 offen sc1\n"
PK"v9,v10,v11\n"
MF"v[32:35],a[64:67],a[32:35],0\n"
EX"v5,v5\n"
MF"v[32:35],a[68:71],a[36:39],v[32:35]\n"
BL"v225,s[8:11],s44 offen offset:0 sc1 lds\n"
MF"v[40:43],a[68:71],a[52:55],0\n"
SA"s40,s40,32768\n"
RD"a[92:95],v226 offset:23552\n"
PK"v2,v4,v5\n"
MF"v[40:43],a[64:67],a[48:51],v[40:43]\n"
WT"lgkmcnt(3)\n"
EX"v6,v6\n"
MF"v[32:35],a[72:75],a[40:43],v[32:35]\n"
"s_cmp_eq_u32 s40,s46\n"
RD"a[96:99],v226 offset:24576\n"
RD"a[100:103],v226 offset:25600\n"
MF"v[32:35],a[76:79],a[44:47],v[32:35]\n"
"s_cselect_b32 s40,s47,s40\n"
EX"v7,v7\n"
MF"v[40:43],a[76:79],a[60:63],v[40:43]\n"
RD"a[104:107],v226 offset:26624\n"
PK"v3,v6,v7\n"
MF"v[40:43],a[72:75],a[56:59],v[40:43]\n"
EX"v12,v12\n"
MF"v[36:39],a[80:83],a[32:35],0\n"
WT"lgkmcnt(4)\n"
EX"v13,v13\n"
MF"v[36:39],a[84:87],a[36:39],v[36:39]\n"
BL"v225,s[8:11],s44 offen offset:1024 sc1 lds\n"
MF"v[44:47],a[84:87],a[52:55],0\n"
EX"v14,v14\n"
MF"v[44:47],a[80:83],a[48:51],v[44:47]\n"
RD"a[108:111],v226 offset:27648\n"
RD"a[112:115],v226 offset:28672\n"
PK"v10,v12,v13\n"
MF"v[36:39],a[88:91],a[40:43],v[36:39]\n"
WT"lgkmcnt(3)\n"
EX"v15,v15\n"
MF"v[36:39],a[92:95],a[44:47],v[36:39]\n"
RD"a[116:119],v226 offset:29696\n"
RD"a[120:123],v226 offset:30720\n"
MF"v[44:47],a[92:95],a[60:63],v[44:47]\n"
EX"v16,v16\n"
PK"v11,v14,v15\n"
MF"v[44:47],a[88:91],a[56:59],v[44:47]\n"
EX"v17,v17\n"
MF"v[48:51],a[96:99],a[32:35],0\n"
BL"v225,s[8:11],s44 offen offset:2048 sc1 lds\n"
MF"v[48:51],a[100:103],a[36:39],v[48:51]\n"
EX"v18,v18\n"
MF"v[56:59],a[100:103],a[52:55],0\n"
RD"a[124:127],v226 offset:31744\n"
PK"v16,v16,v17\n"
MF"v[56:59],a[96:99],a[48:51],v[56:59]\n"
WT"lgkmcnt(3)\n"
EX"v19,v19\n"
MF"v[48:51],a[104:107],a[40:43],v[48:51]\n"
WT"vmcnt(16)\n"
"s_barrier\n"
MF"v[48:51],a[108:111],a[44:47],v[48:51]\n"
RD"a[96:99],v227 offset:8192\n"
RD"a[100:103],v227 offset:9216\n"
MF"v[56:59],a[108:111],a[60:63],v[56:59]\n"
EX"v24,v24\n"
PK"v17,v18,v19\n"
MF"v[56:59],a[104:107],a[56:59],v[56:59]\n"
EX"v25,v25\n"
MF"v[52:55],a[112:115],a[32:35],0\n"
WT"lgkmcnt(3)\n"
BL"v225,s[8:11],s44 offen offset:3072 sc1 lds\n"
MF"v[52:55],a[116:119],a[36:39],v[52:55]\n"
EX"v26,v26\n"
PK"v24,v24,v25\n"
MF"v[60:63],a[116:119],a[52:55],0\n"
EX"v27,v27\n"
"v_readlane_b32 s43,v228,s42\n"
MF"v[60:63],a[112:115],a[48:51],v[60:63]\n"
"s_mov_b32 m0,s40\n"
RD"a[104:107],v227 offset:10240\n"
RD"a[108:111],v227 offset:11264\n"
MF"v[52:55],a[120:123],a[40:43],v[52:55]\n"
WT"lgkmcnt(2)\n"
EX"v20,v20\n"
MF"v[52:55],a[124:127],a[44:47],v[52:55]\n"
EX"v21,v21\n"
PK"v25,v26,v27\n"
MF"v[60:63],a[124:127],a[60:63],v[60:63]\n"
RD"a[112:115],v227 offset:12288\n"
"v_readlane_b32 s44,v229,s42\n"
MF"v[60:63],a[120:123],a[56:59],v[60:63]\n"
SA"s41,s41,32768\n"
EX"v22,v22\n"
MF"v[112:115],a[96:99],v[68:71],v[112:115]\n"
"s_cmp_eq_u32 s41,163840\n"
BL"v225,s[8:11],s43 offen offset:0 sc1 lds\n"
MF"v[144:147],a[96:99],v[72:75],v[144:147]\n"
"s_cselect_b32 s41,0,s41\n"
EX"v23,v23\n"
PK"v18,v20,v21\n"
MF"v[176:179],a[96:99],v[76:79],v[176:179]\n"
SA"s42,s42,1\n"
RD"a[116:119],v227 offset:13312\n"
"v_add_u32 v226,s41,v224\n"
MF"v[116:119],a[100:103],v[68:71],v[116:119]\n"
EX"v28,v28\n"
MF"v[148:151],a[100:103],v[72:75],v[148:151]\n"
RD"a[120:123],v227 offset:14336\n"
PK"v19,v22,v23\n"
MF"v[180:183],a[100:103],v[76:79],v[180:183]\n"
WT"lgkmcnt(2)\n"
EX"v29,v29\n"
MF"v[120:123],a[104:107],v[68:71],v[120:123]\n"
EX"v30,v30\n"
MF"v[152:155],a[104:107],v[72:75],v[152:155]\n"
BL"v225,s[8:11],s43 offen offset:1024 sc1 lds\n"
MF"v[184:187],a[104:107],v[76:79],v[184:187]\n"
EX"v31,v31\n"
PK"v26,v28,v29\n"
MF"v[124:127],a[108:111],v[68:71],v[124:127]\n"
RD"a[124:127],v227 offset:15360\n"
PK"v27,v30,v31\n"
MF"v[156:159],a[108:111],v[72:75],v[156:159]\n"
EX"v32,v32\n"
MF"v[188:191],a[108:111],v[76:79],v[188:191]\n"
MF"v[128:131],a[112:115],v[68:71],v[128:131]\n"
EX"v33,v33\n"
MF"v[160:163],a[112:115],v[72:75],v[160:163]\n"
RD"a[64:67],v227 offset:16384\n"
PK"v64,v32,v33\n"
MF"v[192:195],a[112:115],v[76:79],v[192:195]\n"
WT"lgkmcnt(2)\n"
BL"v225,s[8:11],s43 offen offset:2048 sc1 lds\n"
MF"v[132:135],a[116:119],v[68:71],v[132:135]\n"
EX"v34,v34\n"
MF"v[164:167],a[116:119],v[72:75],v[164:167]\n"
EX"v35,v35\n"
MF"v[196:199],a[116:119],v[76:79],v[196:199]\n"
EX"v40,v40\n"
MF"v[136:139],a[120:123],v[68:71],v[136:139]\n"
RD"a[68:71],v227 offset:24576\n"
PK"v65,v34,v35\n"
MF"v[168:171],a[120:123],v[72:75],v[168:171]\n"
EX"v41,v41\n"
MF"v[200:203],a[120:123],v[76:79],v[200:203]\n"
WT"lgkmcnt(1)\n"
RD"a[72:75],v227 offset:17408\n"
RD"a[76:79],v227 offset:25600\n"
MF"v[140:143],a[124:127],v[68:71],v[140:143]\n"
WT"vmcnt(7)\n"
"v_cmp_eq_u64 vcc,s[28:29],v[238:239]\n"
"s_cmp_eq_u64 vcc,exec\n"
"s_cbranch_scc0 L1019_%=\n"
"L1020_%=:\n"
MF"v[172:175],a[124:127],v[72:75],v[172:175]\n"
EX"v42,v42\n"
MF"v[204:207],a[124:127],v[76:79],v[204:207]\n"
RD"a[80:83],v227 offset:18432\n"
MF"a[136:139],a[128:131],v[68:71],a[136:139]\n"
BL"v225,s[8:11],s43 offen offset:3072 sc1 lds\n"
PK"v68,v40,v41\n"
MF"a[140:143],a[128:131],v[72:75],a[140:143]\n"
SA"m0,s40,16384\n"
EX"v43,v43\n"
MF"a[144:147],a[128:131],v[76:79],a[144:147]\n"
EX"v36,v36\n"
MF"v[144:147],a[64:67],v[0:3],v[144:147]\n"
WT"lgkmcnt(1)\n"
RD"a[84:87],v227 offset:26624\n"
RD"a[88:91],v227 offset:19456\n"
PK"v69,v42,v43\n"
MF"v[144:147],a[68:71],v[16:19],v[144:147]\n"
EX"v37,v37\n"
MF"v[176:179],a[68:71],v[24:27],v[176:179]\n"
SA"s51,s59,16384\n"
RD"a[92:95],v227 offset:27648\n"
RD"a[96:99],v227 offset:20480\n"
MF"v[176:179],a[64:67],v[8:11],v[176:179]\n"
SA"s40,s40,32768\n"
EX"v38,v38\n"
MF"v[148:151],a[72:75],v[0:3],v[148:151]\n"
"s_cmp_eq_u32 s40,s46\n"
BL"v225,s[8:11],s44 offen offset:0 sc1 lds\n"
PK"v66,v36,v37\n"
MF"v[148:151],a[76:79],v[16:19],v[148:151]\n"
"s_cselect_b32 s40,s47,s40\n"
EX"v39,v39\n"
"v_accvgpr_read_b32 v252,a136\n"
MF"v[180:183],a[76:79],v[24:27],v[180:183]\n"
EX"v44,v44\n"
"v_subrev_u32 v253,0x2b800000,v252\n"
MF"v[180:183],a[72:75],v[8:11],v[180:183]\n"
WT"lgkmcnt(2)\n"
"v_cmp_le_u32 vcc,0x34000000,v253\n"
"s_cmp_lg_u64 vcc,0\n"
"s_cbranch_scc1 L1023_%=\n"
"L1022_%=:\n"
MF"v[152:155],a[80:83],v[0:3],v[152:155]\n"
"v_rcp_f32 v252,v252\n"
PK"v67,v38,v39\n"
MF"v[152:155],a[84:87],v[16:19],v[152:155]\n"
RD"a[100:103],v227 offset:28672\n"
RD"a[104:107],v227 offset:21504\n"
EX"v45,v45\n"
MF"v[184:187],a[84:87],v[24:27],v[184:187]\n"
RD"a[108:111],v227 offset:29696\n"
RD"a[112:115],v227 offset:22528\n"
MU"v112,v252,v112\n"
MF"v[184:187],a[80:83],v[8:11],v[184:187]\n"
EX"v46,v46\n"
MU"v113,v252,v113\n"
MF"v[156:159],a[88:91],v[0:3],v[156:159]\n"
WT"lgkmcnt(4)\n"
BL"v225,s[8:11],s44 offen offset:1024 sc1 lds\n"
MU"v114,v252,v114\n"
MF"v[156:159],a[92:95],v[16:19],v[156:159]\n"
EX"v47,v47\n"
MU"v115,v252,v115\n"
MF"v[188:191],a[92:95],v[24:27],v[188:191]\n"
EX"v48,v48\n"
MU"v116,v252,v116\n"
MF"v[188:191],a[88:91],v[8:11],v[188:191]\n"
EX"v49,v49\n"
PK"v70,v44,v45\n"
MF"v[160:163],a[96:99],v[0:3],v[160:163]\n"
WT"lgkmcnt(2)\n"
RD"a[116:119],v227 offset:30720\n"
RD"a[120:123],v227 offset:23552\n"
MU"v117,v252,v117\n"
MF"v[160:163],a[100:103],v[16:19],v[160:163]\n"
EX"v50,v50\n"
MU"v118,v252,v118\n"
MF"v[192:195],a[100:103],v[24:27],v[192:195]\n"
RD"a[124:127],v227 offset:31744\n"
MU"v119,v252,v119\n"
PK"v112,v112,v113\n"
MF"v[192:195],a[96:99],v[8:11],v[192:195]\n"
BL"v225,s[8:11],s44 offen offset:2048 sc1 lds\n"
PK"v113,v114,v115\n"
MF"v[164:167],a[104:107],v[0:3],v[164:167]\n"
WT"lgkmcnt(3)\n"
EX"v51,v51\n"
PK"v114,v116,v117\n"
MF"v[164:167],a[108:111],v[16:19],v[164:167]\n"
EX"v52,v52\n"
PK"v115,v118,v119\n"
MF"v[196:199],a[108:111],v[24:27],v[196:199]\n"
BS"v[112:115],v224,s[8:11],s51 offen offset:0 sc1\n"
PK"v71,v46,v47\n"
MF"v[196:199],a[104:107],v[8:11],v[196:199]\n"
"s_barrier\n"
MF"v[168:171],a[112:115],v[0:3],v[168:171]\n"
WT"lgkmcnt(1)\n"
EX"v53,v53\n"
MU"v120,v252,v120\n"
MF"v[168:171],a[116:119],v[16:19],v[168:171]\n"
RD"a[64:67],v226 offset:0\n"
RD"a[68:71],v226 offset:1024\n"
MU"v121,v252,v121\n"
MF"v[200:203],a[116:119],v[24:27],v[200:203]\n"
EX"v54,v54\n"
MU"v122,v252,v122\n"
MF"v[200:203],a[112:115],v[8:11],v[200:203]\n"
BL"v225,s[8:11],s44 offen offset:3072 sc1 lds\n"
MU"v123,v252,v123\n"
MF"v[172:175],a[120:123],v[0:3],v[172:175]\n"
WT"lgkmcnt(2)\n"
EX"v55,v55\n"
MU"v124,v252,v124\n"
MF"v[172:175],a[124:127],v[16:19],v[172:175]\n"
RD"a[72:75],v226 offset:2048\n"
PK"v72,v48,v49\n"
MU"v125,v252,v125\n"
MF"v[204:207],a[124:127],v[24:27],v[204:207]\n"
RD"a[76:79],v226 offset:3072\n"
MU"v126,v252,v126\n"
MU"v127,v252,v127\n"
MF"v[204:207],a[120:123],v[8:11],v[204:207]\n"
PK"v120,v120,v121\n"
PK"v121,v122,v123\n"
PK"v122,v124,v125\n"
MF"a[140:143],a[128:131],v[0:3],a[140:143]\n"
MU"v128,v252,v128\n"
MU"v129,v252,v129\n"
MU"v136,v252,v136\n"
MF"a[140:143],a[128:131],v[16:19],a[140:143]\n"
MU"v137,v252,v137\n"
MU"v130,v252,v130\n"
MU"v131,v252,v131\n"
MF"a[144:147],a[128:131],v[24:27],a[144:147]\n"
PK"v128,v128,v129\n"
MU"v138,v252,v138\n"
MU"v139,v252,v139\n"
MF"a[144:147],a[128:131],v[8:11],a[144:147]\n"
PK"v136,v136,v137\n"
MU"v132,v252,v132\n"
MU"v133,v252,v133\n"
PK"v73,v50,v51\n"
PK"v74,v52,v53\n"
PK"v75,v54,v55\n"
PK"v123,v126,v127\n"
BS"v[120:123],v224,s[8:11],s51 offen offset:1024 sc1\n"
MU"v134,v252,v134\n"
MU"v135,v252,v135\n"
PK"v129,v130,v131\n"
PK"v130,v132,v133\n"
PK"v131,v134,v135\n"
BS"v[128:131],v224,s[8:11],s51 offen offset:2048 sc1\n"
MU"v140,v252,v140\n"
MU"v141,v252,v141\n"
MU"v142,v252,v142\n"
MU"v143,v252,v143\n"
PK"v137,v138,v139\n"
PK"v138,v140,v141\n"
PK"v139,v142,v143\n"
BS"v[136:139],v224,s[8:11],s51 offen offset:3072 sc1\n"
RD"a[80:83],v226 offset:4096\n"
RD"a[84:87],v226 offset:5120\n"
RD"a[88:91],v226 offset:6144\n"
".p2align 6\n"
"LP2_%=:\n"
WT"lgkmcnt(6)\n"
MF"v[0:3],a[64:67],a[32:35],0\n"
WT"lgkmcnt(5)\n"
EX"v56,v56\n"
MF"v[0:3],a[68:71],a[36:39],v[0:3]\n"
RD"a[92:95],v226 offset:7168\n"
"v_readlane_b32 s43,v228,s42\n"
MF"v[8:11],a[68:71],a[52:55],0\n"
EX"v57,v57\n"
MF"v[8:11],a[64:67],a[48:51],v[8:11]\n"
WT"lgkmcnt(5)\n"
RD"a[96:99],v226 offset:8192\n"
RD"a[100:103],v226 offset:9216\n"
MF"v[0:3],a[72:75],a[40:43],v[0:3]\n"
WT"lgkmcnt(3)\n"
EX"v58,v58\n"
MF"v[0:3],a[76:79],a[44:47],v[0:3]\n"
"s_mov_b32 m0,s40\n"
RD"a[104:107],v226 offset:10240\n"
"v_readlane_b32 s44,v229,s42\n"
MF"v[8:11],a[76:79],a[60:63],v[8:11]\n"
SA"s41,s41,32768\n"
EX"v59,v59\n"
MF"v[8:11],a[72:75],a[56:59],v[8:11]\n"
"s_cmp_eq_u32 s41,163840\n"
BL"v225,s[8:11],s43 offen offset:0 sc1 lds\n"
MF"v[4:7],a[80:83],a[32:35],0\n"
"s_cselect_b32 s41,0,s41\n"
EX"v60,v60\n"
MF"v[4:7],a[84:87],a[36:39],v[4:7]\n"
SA"s42,s42,1\n"
RD"a[108:111],v226 offset:11264\n"
"v_add_u32 v227,s41,v224\n"
MF"v[12:15],a[84:87],a[52:55],0\n"
EX"v61,v61\n"
MF"v[12:15],a[80:83],a[48:51],v[12:15]\n"
RD"a[112:115],v226 offset:12288\n"
RD"a[116:119],v226 offset:13312\n"
MF"v[4:7],a[88:91],a[40:43],v[4:7]\n"
WT"lgkmcnt(3)\n"
EX"v62,v62\n"
MF"v[4:7],a[92:95],a[44:47],v[4:7]\n"
RD"a[120:123],v226 offset:14336\n"
PK"v76,v56,v57\n"
MF"v[12:15],a[92:95],a[60:63],v[12:15]\n"
EX"v63,v63\n"
MF"v[12:15],a[88:91],a[56:59],v[12:15]\n"
BL"v225,s[8:11],s43 offen offset:1024 sc1 lds\n"
MF"v[16:19],a[96:99],a[32:35],0\n"
EX"v0,v0\n"
MF"v[16:19],a[100:103],a[36:39],v[16:19]\n"
RD"a[124:127],v226 offset:15360\n"
PK"v77,v58,v59\n"
MF"v[24:27],a[100:103],a[52:55],0\n"
EX"v1,v1\n"
MF"v[24:27],a[96:99],a[48:51],v[24:27]\n"
RD"a[64:67],v226 offset:16384\n"
RD"a[68:71],v226 offset:17408\n"
MF"v[16:19],a[104:107],a[40:43],v[16:19]\n"
WT"lgkmcnt(3)\n"
EX"v2,v2\n"
MF"v[16:19],a[108:111],a[44:47],v[16:19]\n"
RD"a[72:75],v226 offset:18432\n"
PK"v78,v60,v61\n"
MF"v[24:27],a[108:111],a[60:63],v[24:27]\n"
EX"v3,v3\n"
MF"v[24:27],a[104:107],a[56:59],v[24:27]\n"
BL"v225,s[8:11],s43 offen offset:2048 sc1 lds\n"
MF"v[20:23],a[112:115],a[32:35],0\n"
EX"v8,v8\n"
MF"v[20:23],a[116:119],a[36:39],v[20:23]\n"
RD"a[76:79],v226 offset:19456\n"
PK"v79,v62,v63\n"
MF"v[28:31],a[116:119],a[52:55],0\n"
EX"v9,v9\n"
MF"v[28:31],a[112:115],a[48:51],v[28:31]\n"
RD"a[80:83],v226 offset:20480\n"
RD"a[84:87],v226 offset:21504\n"
MF"v[20:23],a[120:123],a[40:43],v[20:23]\n"
WT"lgkmcnt(3)\n"
EX"v10,v10\n"
MF"v[20:23],a[124:127],a[44:47],v[20:23]\n"
RD"a[88:91],v226 offset:22528\n"
PK"v0,v0,v1\n"
MF"v[28:31],a[124:127],a[60:63],v[28:31]\n"
EX"v11,v11\n"
MF"v[28:31],a[120:123],a[56:59],v[28:31]\n"
BL"v225,s[8:11],s43 offen offset:3072 sc1 lds\n"
MF"v[32:35],a[64:67],a[32:35],0\n"
EX"v4,v4\n"
MF"v[32:35],a[68:71],a[36:39],v[32:35]\n"
RD"a[92:95],v226 offset:23552\n"
PK"v1,v2,v3\n"
MF"v[40:43],a[68:71],a[52:55],0\n"
EX"v5,v5\n"
MF"v[40:43],a[64:67],a[48:51],v[40:43]\n"
SA"m0,s40,16384\n"
RD"a[96:99],v226 offset:24576\n"
RD"a[100:103],v226 offset:25600\n"
MF"v[32:35],a[72:75],a[40:43],v[32:35]\n"
WT"lgkmcnt(3)\n"
EX"v6,v6\n"
MF"v[32:35],a[76:79],a[44:47],v[32:35]\n"
RD"a[104:107],v226 offset:26624\n"
PK"v8,v8,v9\n"
MF"v[40:43],a[76:79],a[60:63],v[40:43]\n"
EX"v7,v7\n"
MF"v[40:43],a[72:75],a[56:59],v[40:43]\n"
BL"v225,s[8:11],s44 offen offset:0 sc1 lds\n"
MF"v[36:39],a[80:83],a[32:35],0\n"
SA"s40,s40,32768\n"
EX"v12,v12\n"
MF"v[36:39],a[84:87],a[36:39],v[36:39]\n"
"s_cmp_eq_u32 s40,s46\n"
RD"a[108:111],v226 offset:27648\n"
PK"v9,v10,v11\n"
MF"v[44:47],a[84:87],a[52:55],0\n"
"s_cselect_b32 s40,s47,s40\n"
EX"v13,v13\n"
MF"v[44:47],a[80:83],a[48:51],v[44:47]\n"
RD"a[112:115],v226 offset:28672\n"
RD"a[116:119],v226 offset:29696\n"
MF"v[36:39],a[88:91],a[40:43],v[36:39]\n"
WT"lgkmcnt(3)\n"
EX"v14,v14\n"
MF"v[36:39],a[92:95],a[44:47],v[36:39]\n"
RD"a[120:123],v226 offset:30720\n"
PK"v2,v4,v5\n"
MF"v[44:47],a[92:95],a[60:63],v[44:47]\n"
EX"v15,v15\n"
MF"v[44:47],a[88:91],a[56:59],v[44:47]\n"
BL"v225,s[8:11],s44 offen offset:1024 sc1 lds\n"
MF"v[48:51],a[96:99],a[32:35],0\n"
EX"v16,v16\n"
MF"v[48:51],a[100:103],a[36:39],v[48:51]\n"
RD"a[124:127],v226 offset:31744\n"
PK"v3,v6,v7\n"
MF"v[56:59],a[100:103],a[52:55],0\n"
WT"vmcnt(14)\n"
"s_barrier\n"
MF"v[56:59],a[96:99],a[48:51],v[56:59]\n"
RD"a[64:67],v227 offset:0\n"
RD"a[68:71],v227 offset:8192\n"
MF"v[48:51],a[104:107],a[40:43],v[48:51]\n"
WT"lgkmcnt(3)\n"
EX"v17,v17\n"
MF"v[48:51],a[108:111],a[44:47],v[48:51]\n"
RD"a[72:75],v227 offset:1024\n"
PK"v10,v12,v13\n"
MF"v[56:59],a[108:111],a[60:63],v[56:59]\n"
EX"v18,v18\n"
MF"v[56:59],a[104:107],a[56:59],v[56:59]\n"
BL"v225,s[8:11],s44 offen offset:2048 sc1 lds\n"
MF"v[52:55],a[112:115],a[32:35],0\n"
EX"v19,v19\n"
MF"v[52:55],a[116:119],a[36:39],v[52:55]\n"
RD"a[76:79],v227 offset:9216\n"
PK"v11,v14,v15\n"
MF"v[60:63],a[116:119],a[52:55],0\n"
EX"v24,v24\n"
MF"v[60:63],a[112:115],a[48:51],v[60:63]\n"
RD"a[80:83],v227 offset:2048\n"
RD"a[84:87],v227 offset:10240\n"
MF"v[52:55],a[120:123],a[40:43],v[52:55]\n"
WT"lgkmcnt(3)\n"
EX"v25,v25\n"
MF"v[52:55],a[124:127],a[44:47],v[52:55]\n"
RD"a[88:91],v227 offset:3072\n"
PK"v16,v16,v17\n"
MF"v[60:63],a[124:127],a[60:63],v[60:63]\n"
EX"v26,v26\n"
MF"v[60:63],a[120:123],a[56:59],v[60:63]\n"
SA"s41,s41,32768\n"
BL"v225,s[8:11],s44 offen offset:3072 sc1 lds\n"
MF"v[144:147],a[64:67],v[64:67],v[144:147]\n"
"s_cmp_eq_u32 s41,163840\n"
EX"v27,v27\n"
MF"v[144:147],a[68:71],v[72:75],v[144:147]\n"
"s_cselect_b32 s41,0,s41\n"
RD"a[92:95],v227 offset:11264\n"
"v_add_u32 v226,s41,v224\n"
MF"v[176:179],a[68:71],v[76:79],v[176:179]\n"
EX"v20,v20\n"
MF"v[176:179],a[64:67],v[68:71],v[176:179]\n"
"s_mov_b32 m0,s40\n"
RD"a[96:99],v227 offset:4096\n"
RD"a[100:103],v227 offset:12288\n"
MF"v[148:151],a[72:75],v[64:67],v[148:151]\n"
WT"lgkmcnt(3)\n"
EX"v21,v21\n"
MF"v[148:151],a[76:79],v[72:75],v[148:151]\n"
RD"a[104:107],v227 offset:5120\n"
"v_readlane_b32 s43,v228,s42\n"
MF"v[180:183],a[76:79],v[76:79],v[180:183]\n"
EX"v22,v22\n"
MF"v[180:183],a[72:75],v[68:71],v[180:183]\n"
"v_readlane_b32 s44,v229,s42\n"
PK"v17,v18,v19\n"
MF"v[152:155],a[80:83],v[64:67],v[152:155]\n"
SA"s42,s42,1\n"
BL"v225,s[8:11],s43 offen offset:0 sc1 lds\n"
MF"v[152:155],a[84:87],v[72:75],v[152:155]\n"
EX"v23,v23\n"
MF"v[184:187],a[84:87],v[76:79],v[184:187]\n"
RD"a[108:111],v227 offset:13312\n"
PK"v24,v24,v25\n"
MF"v[184:187],a[80:83],v[68:71],v[184:187]\n"
EX"v28,v28\n"
MF"v[156:159],a[88:91],v[64:67],v[156:159]\n"
WT"lgkmcnt(1)\n"
RD"a[112:115],v227 offset:6144\n"
RD"a[116:119],v227 offset:14336\n"
MF"v[156:159],a[92:95],v[72:75],v[156:159]\n"
EX"v29,v29\n"
MF"v[188:191],a[92:95],v[76:79],v[188:191]\n"
RD"a[120:123],v227 offset:7168\n"
PK"v25,v26,v27\n"
MF"v[188:191],a[88:91],v[68:71],v[188:191]\n"
EX"v30,v30\n"
MF"v[160:163],a[96:99],v[64:67],v[160:163]\n"
BL"v225,s[8:11],s43 offen offset:1024 sc1 lds\n"
MF"v[160:163],a[100:103],v[72:75],v[160:163]\n"
EX"v31,v31\n"
MF"v[192:195],a[100:103],v[76:79],v[192:195]\n"
RD"a[124:127],v227 offset:15360\n"
PK"v18,v20,v21\n"
MF"v[192:195],a[96:99],v[68:71],v[192:195]\n"
EX"v32,v32\n"
MF"v[164:167],a[104:107],v[64:67],v[164:167]\n"
WT"lgkmcnt(2)\n"
EX"v33,v33\n"
MF"v[164:167],a[108:111],v[72:75],v[164:167]\n"
RD"a[64:67],v227 offset:16384\n"
PK"v19,v22,v23\n"
MF"v[196:199],a[108:111],v[76:79],v[196:199]\n"
EX"v34,v34\n"
MF"v[196:199],a[104:107],v[68:71],v[196:199]\n"
BL"v225,s[8:11],s43 offen offset:2048 sc1 lds\n"
MF"v[168:171],a[112:115],v[64:67],v[168:171]\n"
EX"v35,v35\n"
MF"v[168:171],a[116:119],v[72:75],v[168:171]\n"
RD"a[68:71],v227 offset:24576\n"
PK"v26,v28,v29\n"
MF"v[200:203],a[116:119],v[76:79],v[200:203]\n"
EX"v40,v40\n"
MF"v[200:203],a[112:115],v[68:71],v[200:203]\n"
WT"lgkmcnt(2)\n"
RD"a[72:75],v227 offset:17408\n"
RD"a[76:79],v227 offset:25600\n"
MF"v[172:175],a[120:123],v[64:67],v[172:175]\n"
EX"v41,v41\n"
MF"v[172:175],a[124:127],v[72:75],v[172:175]\n"
RD"a[80:83],v227 offset:18432\n"
PK"v27,v30,v31\n"
MF"v[204:207],a[124:127],v[76:79],v[204:207]\n"
EX"v42,v42\n"
MF"v[204:207],a[120:123],v[68:71],v[204:207]\n"
BL"v225,s[8:11],s43 offen offset:3072 sc1 lds\n"
MF"a[140:143],a[128:131],v[64:67],a[140:143]\n"
EX"v43,v43\n"
MF"a[140:143],a[128:131],v[72:75],a[140:143]\n"
RD"a[84:87],v227 offset:26624\n"
PK"v64,v32,v33\n"
MF"a[144:147],a[128:131],v[76:79],a[144:147]\n"
EX"v36,v36\n"
MF"a[144:147],a[128:131],v[68:71],a[144:147]\n"
WT"lgkmcnt(2)\n"
RD"a[88:91],v227 offset:19456\n"
RD"a[92:95],v227 offset:27648\n"
MF"v[144:147],a[64:67],v[0:3],v[144:147]\n"
SA"m0,s40,16384\n"
EX"v37,v37\n"
MF"v[144:147],a[68:71],v[16:19],v[144:147]\n"
"buffer_load_dwordx2 v[242:243],v236,s[8:11],0 offen sc1\n"
MF"v[176:179],a[68:71],v[24:27],v[176:179]\n"
RD"a[96:99],v227 offset:20480\n"
PK"v65,v34,v35\n"
MF"v[176:179],a[64:67],v[8:11],v[176:179]\n"
EX"v38,v38\n"
MF"v[148:151],a[72:75],v[0:3],v[148:151]\n"
SA"s40,s40,32768\n"
BL"v225,s[8:11],s44 offen offset:0 sc1 lds\n"
MF"v[148:151],a[76:79],v[16:19],v[148:151]\n"
"s_cmp_eq_u32 s40,s46\n"
EX"v39,v39\n"
MF"v[180:183],a[76:79],v[24:27],v[180:183]\n"
"s_cselect_b32 s40,s47,s40\n"
RD"a[100:103],v227 offset:28672\n"
PK"v68,v40,v41\n"
MF"v[180:183],a[72:75],v[8:11],v[180:183]\n"
WT"lgkmcnt(2)\n"
EX"v44,v44\n"
MF"v[152:155],a[80:83],v[0:3],v[152:155]\n"
RD"a[104:107],v227 offset:21504\n"
RD"a[108:111],v227 offset:29696\n"
MF"v[152:155],a[84:87],v[16:19],v[152:155]\n"
EX"v45,v45\n"
MF"v[184:187],a[84:87],v[24:27],v[184:187]\n"
RD"a[112:115],v227 offset:22528\n"
PK"v69,v42,v43\n"
MF"v[184:187],a[80:83],v[8:11],v[184:187]\n"
EX"v46,v46\n"
MF"v[156:159],a[88:91],v[0:3],v[156:159]\n"
BL"v225,s[8:11],s44 offen offset:1024 sc1 lds\n"
MF"v[156:159],a[92:95],v[16:19],v[156:159]\n"
EX"v47,v47\n"
MF"v[188:191],a[92:95],v[24:27],v[188:191]\n"
RD"a[116:119],v227 offset:30720\n"
PK"v66,v36,v37\n"
MF"v[188:191],a[88:91],v[8:11],v[188:191]\n"
WT"lgkmcnt(2)\n"
EX"v48,v48\n"
MF"v[160:163],a[96:99],v[0:3],v[160:163]\n"
RD"a[120:123],v227 offset:23552\n"
RD"a[124:127],v227 offset:31744\n"
MF"v[160:163],a[100:103],v[16:19],v[160:163]\n"
EX"v49,v49\n"
MF"v[192:195],a[100:103],v[24:27],v[192:195]\n"
EX"v50,v50\n"
MF"v[192:195],a[96:99],v[8:11],v[192:195]\n"
BL"v225,s[8:11],s44 offen offset:2048 sc1 lds\n"
MF"v[164:167],a[104:107],v[0:3],v[164:167]\n"
WT"vmcnt(16)\n"
"s_barrier\n"
MF"v[164:167],a[108:111],v[16:19],v[164:167]\n"
RD"a[64:67],v226 offset:0\n"
PK"v67,v38,v39\n"
MF"v[196:199],a[108:111],v[24:27],v[196:199]\n"
EX"v51,v51\n"
MF"v[196:199],a[104:107],v[8:11],v[196:199]\n"
WT"lgkmcnt(1)\n"
RD"a[68:71],v226 offset:1024\n"
PK"v70,v44,v45\n"
MF"v[168:171],a[112:115],v[0:3],v[168:171]\n"
EX"v52,v52\n"
MF"v[168:171],a[116:119],v[16:19],v[168:171]\n"
RD"a[72:75],v226 offset:2048\n"
PK"v71,v46,v47\n"
MF"v[200:203],a[116:119],v[24:27],v[200:203]\n"
EX"v53,v53\n"
MF"v[200:203],a[112:115],v[8:11],v[200:203]\n"
BL"v225,s[8:11],s44 offen offset:3072 sc1 lds\n"
MF"v[172:175],a[120:123],v[0:3],v[172:175]\n"
EX"v54,v54\n"
MF"v[172:175],a[124:127],v[16:19],v[172:175]\n"
"buffer_store_dwordx2 v[232:233],v230,s[8:11],s57 offen sc1\n"
MF"v[204:207],a[124:127],v[24:27],v[204:207]\n"
"s_mov_b32 s57,2130706432\n"
RD"a[76:79],v226 offset:3072\n"
PK"v72,v48,v49\n"
MF"v[204:207],a[120:123],v[8:11],v[204:207]\n"
EX"v55,v55\n"
MF"a[140:143],a[128:131],v[0:3],a[140:143]\n"
RD"a[80:83],v226 offset:4096\n"
RD"a[84:87],v226 offset:5120\n"
MF"a[140:143],a[128:131],v[16:19],a[140:143]\n"
RD"a[88:91],v226 offset:6144\n"
PK"v73,v50,v51\n"
MF"a[144:147],a[128:131],v[24:27],a[144:147]\n"
PK"v74,v52,v53\n"
PK"v75,v54,v55\n"
MF"a[144:147],a[128:131],v[8:11],a[144:147]\n"
"s_sub_u32 s56,s56,1\n"
"s_cbranch_scc0 LP2_%=\n"
"s_cmp_lt_i32 s22,0\n"
"s_cbranch_scc1 LE2_%=\n"
WT"lgkmcnt(6)\n"
MF"v[0:3],a[64:67],a[32:35],0\n"
WT"lgkmcnt(5)\n"
WT"vmcnt(5)\n"
"v_cmp_eq_u64 vcc,s[28:29],v[242:243]\n"
"s_or_b32 vcc_hi,vcc_hi,-1\n"
"s_cmp_eq_u64 vcc,exec\n"
"s_cbranch_scc0 L1028_%=\n"
"L1029_%=:\n"
MF"v[0:3],a[68:71],a[36:39],v[0:3]\n"
SA"s48,s59,32768\n"
EX"v56,v56\n"
MF"v[8:11],a[68:71],a[52:55],0\n"
"s_and_b32 s48,s48,0x18000\n"
RD"a[92:95],v226 offset:7168\n"
"v_readlane_b32 s43,v228,s42\n"
MF"v[8:11],a[64:67],a[48:51],v[8:11]\n"
WT"lgkmcnt(5)\n"
EX"v57,v57\n"
MF"v[0:3],a[72:75],a[40:43],v[0:3]\n"
WT"lgkmcnt(1)\n"
RD"a[96:99],v226 offset:8192\n"
RD"a[100:103],v226 offset:9216\n"
MF"v[0:3],a[76:79],a[44:47],v[0:3]\n"
"s_andn2_b32 s49,s59,0x18000\n"
EX"v58,v58\n"
MF"v[8:11],a[76:79],a[60:63],v[8:11]\n"
"s_or_b32 s52,s48,s49\n"
RD"a[104:107],v226 offset:10240\n"
"v_readlane_b32 s44,v229,s42\n"
MF"v[8:11],a[72:75],a[56:59],v[8:11]\n"
SA"s53,s52,16384\n"
BL"a[0:3],v224,s[8:11],s52 offen offset:0 sc1\n"
MF"v[4:7],a[80:83],a[32:35],0\n"
"s_mov_b32 m0,s40\n"
EX"v59,v59\n"
MF"v[4:7],a[84:87],a[36:39],v[4:7]\n"
SA"s41,s41,32768\n"
BL"v225,s[8:11],s43 offen offset:0 sc1 lds\n"
MF"v[12:15],a[84:87],a[52:55],0\n"
"s_cmp_eq_u32 s41,163840\n"
EX"v60,v60\n"
MF"v[12:15],a[80:83],a[48:51],v[12:15]\n"
"s_cselect_b32 s41,0,s41\n"
RD"a[108:111],v226 offset:11264\n"
RD"a[112:115],v226 offset:12288\n"
PK"v76,v56,v57\n"
MF"v[4:7],a[88:91],a[40:43],v[4:7]\n"
WT"lgkmcnt(3)\n"
BL"a[4:7],v224,s[8:11],s53 offen offset:0 sc1\n"
EX"v61,v61\n"
MF"v[4:7],a[92:95],a[44:47],v[4:7]\n"
SA"s42,s42,1\n"
RD"a[116:119],v226 offset:13312\n"
RD"a[120:123],v226 offset:14336\n"
"v_add_u32 v227,s41,v224\n"
MF"v[12:15],a[92:95],a[60:63],v[12:15]\n"
EX"v62,v62\n"
MF"v[12:15],a[88:91],a[56:59],v[12:15]\n"
EX"v63,v63\n"
PK"v77,v58,v59\n"
MF"v[16:19],a[96:99],a[32:35],0\n"
BL"a[8:11],v224,s[8:11],s52 offen offset:1024 sc1\n"
MF"v[16:19],a[100:103],a[36:39],v[16:19]\n"
BL"v225,s[8:11],s43 offen offset:1024 sc1 lds\n"
MF"v[24:27],a[100:103],a[52:55],0\n"
EX"v0,v0\n"
MF"v[24:27],a[96:99],a[48:51],v[24:27]\n"
WT"lgkmcnt(2)\n"
RD"a[124:127],v226 offset:15360\n"
RD"a[64:67],v226 offset:16384\n"
PK"v78,v60,v61\n"
MF"v[16:19],a[104:107],a[40:43],v[16:19]\n"
EX"v1,v1\n"
MF"v[16:19],a[108:111],a[44:47],v[16:19]\n"
RD"a[68:71],v226 offset:17408\n"
RD"a[72:75],v226 offset:18432\n"
MF"v[24:27],a[108:111],a[60:63],v[24:27]\n"
BL"a[12:15],v224,s[8:11],s53 offen offset:1024 sc1\n"
EX"v2,v2\n"
MF"v[24:27],a[104:107],a[56:59],v[24:27]\n"
EX"v3,v3\n"
PK"v79,v62,v63\n"
MF"v[20:23],a[112:115],a[32:35],0\n"
WT"lgkmcnt(4)\n"
BL"v225,s[8:11],s43 offen offset:2048 sc1 lds\n"
MF"v[20:23],a[116:119],a[36:39],v[20:23]\n"
EX"v8,v8\n"
MF"v[28:31],a[116:119],a[52:55],0\n"
RD"a[76:79],v226 offset:19456\n"
PK"v0,v0,v1\n"
MF"v[28:31],a[112:115],a[48:51],v[28:31]\n"
BL"a[16:19],v224,s[8:11],s52 offen offset:2048 sc1\n"
MF"v[20:23],a[120:123],a[40:43],v[20:23]\n"
WT"lgkmcnt(1)\n"
EX"v9,v9\n"
MF"v[20:23],a[124:127],a[44:47],v[20:23]\n"
RD"a[80:83],v226 offset:20480\n"
RD"a[84:87],v226 offset:21504\n"
MF"v[28:31],a[124:127],a[60:63],v[28:31]\n"
EX"v10,v10\n"
MF"v[28:31],a[120:123],a[56:59],v[28:31]\n"
RD"a[88:91],v226 offset:22528\n"
PK"v1,v2,v3\n"
MF"v[32:35],a[64:67],a[32:35],0\n"
EX"v11,v11\n"
MF"v[32:35],a[68:71],a[36:39],v[32:35]\n"
BL"a[20:23],v224,s[8:11],s53 offen offset:2048 sc1\n"
BL"v225,s[8:11],s43 offen offset:3072 sc1 lds\n"
MF"v[40:43],a[68:71],a[52:55],0\n"
EX"v4,v4\n"
MF"v[40:43],a[64:67],a[48:51],v[40:43]\n"
SA"m0,s40,16384\n"
RD"a[92:95],v226 offset:23552\n"
RD"a[96:99],v226 offset:24576\n"
PK"v8,v8,v9\n"
MF"v[32:35],a[72:75],a[40:43],v[32:35]\n"
WT"lgkmcnt(3)\n"
EX"v5,v5\n"
MF"v[32:35],a[76:79],a[44:47],v[32:35]\n"
RD"a[100:103],v226 offset:25600\n"
RD"a[104:107],v226 offset:26624\n"
MF"v[40:43],a[76:79],a[60:63],v[40:43]\n"
BL"a[24:27],v224,s[8:11],s52 offen offset:3072 sc1\n"
EX"v6,v6\n"
MF"v[40:43],a[72:75],a[56:59],v[40:43]\n"
EX"v7,v7\n"
PK"v9,v10,v11\n"
MF"v[36:39],a[80:83],a[32:35],0\n"
SA"s40,s40,32768\n"
BL"v225,s[8:11],s44 offen offset:0 sc1 lds\n"
MF"v[36:39],a[84:87],a[36:39],v[36:39]\n"
"s_cmp_eq_u32 s40,s46\n"
EX"v12,v12\n"
MF"v[44:47],a[84:87],a[52:55],0\n"
"s_cselect_b32 s40,s47,s40\n"
BL"a[28:31],v224,s[8:11],s53 offen offset:3072 sc1\n"
MF"v[44:47],a[80:83],a[48:51],v[44:47]\n"
WT"lgkmcnt(2)\n"
RD"a[108:111],v226 offset:27648\n"
RD"a[112:115],v226 offset:28672\n"
PK"v2,v4,v5\n"
MF"v[36:39],a[88:91],a[40:43],v[36:39]\n"
SA"s48,s59,65536\n"
EX"v13,v13\n"
MF"v[36:39],a[92:95],a[44:47],v[36:39]\n"
"s_and_b32 s48,s48,0x18000\n"
RD"a[116:119],v226 offset:29696\n"
RD"a[120:123],v226 offset:30720\n"
MF"v[44:47],a[92:95],a[60:63],v[44:47]\n"
"s_andn2_b32 s49,s59,0x18000\n"
EX"v14,v14\n"
MF"v[44:47],a[88:91],a[56:59],v[44:47]\n"
"s_or_b32 s52,s48,s49\n"
EX"v15,v15\n"
PK"v3,v6,v7\n"
MF"v[48:51],a[96:99],a[32:35],0\n"
WT"lgkmcnt(4)\n"
BL"a[180:183],v224,s[8:11],s52 offen offset:0 sc1\n"
MF"v[48:51],a[100:103],a[36:39],v[48:51]\n"
SA"s53,s52,16384\n"
BL"v225,s[8:11],s44 offen offset:1024 sc1 lds\n"
MF"v[56:59],a[100:103],a[52:55],0\n"
EX"v16,v16\n"
MF"v[56:59],a[96:99],a[48:51],v[56:59]\n"
RD"a[124:127],v226 offset:31744\n"
PK"v10,v12,v13\n"
MF"v[48:51],a[104:107],a[40:43],v[48:51]\n"
WT"lgkmcnt(1)\n"
"s_barrier\n"
MF"v[48:51],a[108:111],a[44:47],v[48:51]\n"
RD"a[64:67],v227 offset:0\n"
RD"a[68:71],v227 offset:8192\n"
MF"v[56:59],a[108:111],a[60:63],v[56:59]\n"
BL"a[184:187],v224,s[8:11],s53 offen offset:0 sc1\n"
EX"v17,v17\n"
MF"v[56:59],a[104:107],a[56:59],v[56:59]\n"
RD"a[72:75],v227 offset:1024\n"
PK"v11,v14,v15\n"
MF"v[52:55],a[112:115],a[32:35],0\n"
EX"v18,v18\n"
MF"v[52:55],a[116:119],a[36:39],v[52:55]\n"
BL"v225,s[8:11],s44 offen offset:2048 sc1 lds\n"
MF"v[60:63],a[116:119],a[52:55],0\n"
EX"v19,v19\n"
MF"v[60:63],a[112:115],a[48:51],v[60:63]\n"
BL"a[188:191],v224,s[8:11],s52 offen offset:1024 sc1\n"
RD"a[76:79],v227 offset:9216\n"
MF"v[52:55],a[120:123],a[40:43],v[52:55]\n"
WT"lgkmcnt(2)\n"
EX"v24,v24\n"
MF"v[52:55],a[124:127],a[44:47],v[52:55]\n"
RD"a[80:83],v227 offset:2048\n"
RD"a[84:87],v227 offset:10240\n"
PK"v16,v16,v17\n"
MF"v[60:63],a[124:127],a[60:63],v[60:63]\n"
EX"v25,v25\n"
MF"v[60:63],a[120:123],a[56:59],v[60:63]\n"
SA"s41,s41,32768\n"
RD"a[88:91],v227 offset:3072\n"
"v_readlane_b32 s43,v228,s42\n"
MF"v[144:147],a[64:67],v[64:67],v[144:147]\n"
"s_cmp_eq_u32 s41,163840\n"
BL"a[192:195],v224,s[8:11],s53 offen offset:1024 sc1\n"
EX"v26,v26\n"
MF"v[144:147],a[68:71],v[72:75],v[144:147]\n"
"s_cselect_b32 s41,0,s41\n"
BL"v225,s[8:11],s44 offen offset:3072 sc1 lds\n"
PK"v17,v18,v19\n"
MF"v[176:179],a[68:71],v[76:79],v[176:179]\n"
EX"v27,v27\n"
MF"v[176:179],a[64:67],v[68:71],v[176:179]\n"
WT"lgkmcnt(3)\n"
RD"a[92:95],v227 offset:11264\n"
RD"a[96:99],v227 offset:4096\n"
MF"v[148:151],a[72:75],v[64:67],v[148:151]\n"
"s_mov_b32 m0,s40\n"
EX"v20,v20\n"
MF"v[148:151],a[76:79],v[72:75],v[148:151]\n"
RD"a[100:103],v227 offset:12288\n"
RD"a[104:107],v227 offset:5120\n"
PK"v24,v24,v25\n"
MF"v[180:183],a[76:79],v[76:79],v[180:183]\n"
BL"a[196:199],v224,s[8:11],s52 offen offset:2048 sc1\n"
EX"v21,v21\n"
MF"v[180:183],a[72:75],v[68:71],v[180:183]\n"
WT"lgkmcnt(4)\n"
EX"v22,v22\n"
"v_add_u32 v226,s41,v224\n"
MF"v[152:155],a[80:83],v[64:67],v[152:155]\n"
BL"v225,s[8:11],s43 offen offset:0 sc1 lds\n"
MF"v[152:155],a[84:87],v[72:75],v[152:155]\n"
EX"v23,v23\n"
PK"v25,v26,v27\n"
MF"v[184:187],a[84:87],v[76:79],v[184:187]\n"
BL"a[200:203],v224,s[8:11],s53 offen offset:2048 sc1\n"
MF"v[184:187],a[80:83],v[68:71],v[184:187]\n"
RD"a[108:111],v227 offset:13312\n"
RD"a[112:115],v227 offset:6144\n"
MF"v[156:159],a[88:91],v[64:67],v[156:159]\n"
WT"lgkmcnt(2)\n"
EX"v28,v28\n"
MF"v[156:159],a[92:95],v[72:75],v[156:159]\n"
RD"a[116:119],v227 offset:14336\n"
RD"a[120:123],v227 offset:7168\n"
"v_readlane_b32 s44,v229,s42\n"
MF"v[188:191],a[92:95],v[76:79],v[188:191]\n"
SA"s42,s42,1\n"
EX"v29,v29\n"
PK"v18,v20,v21\n"
MF"v[188:191],a[88:91],v[68:71],v[188:191]\n"
BL"a[204:207],v224,s[8:11],s52 offen offset:3072 sc1\n"
MF"v[160:163],a[96:99],v[64:67],v[160:163]\n"
EX"v30,v30\n"
MF"v[160:163],a[100:103],v[72:75],v[160:163]\n"
BL"v225,s[8:11],s43 offen offset:1024 sc1 lds\n"
PK"v19,v22,v23\n"
MF"v[192:195],a[100:103],v[76:79],v[192:195]\n"
EX"v31,v31\n"
MF"v[192:195],a[96:99],v[68:71],v[192:195]\n"
RD"a[124:127],v227 offset:15360\n"
PK"v26,v28,v29\n"
MF"v[164:167],a[104:107],v[64:67],v[164:167]\n"
WT"lgkmcnt(1)\n"
EX"v32,v32\n"
MF"v[164:167],a[108:111],v[72:75],v[164:167]\n"
SA"s48,s59,98304\n"
BL"a[208:211],v224,s[8:11],s53 offen offset:3072 sc1\n"
MF"v[196:199],a[108:111],v[76:79],v[196:199]\n"
"s_and_b32 s48,s48,0x18000\n"
EX"v33,v33\n"
MF"v[196:199],a[104:107],v[68:71],v[196:199]\n"
"s_andn2_b32 s49,s59,0x18000\n"
RD"a[64:67],v227 offset:16384\n"
PK"v27,v30,v31\n"
MF"v[168:171],a[112:115],v[64:67],v[168:171]\n"
"s_or_b32 s52,s48,s49\n"
EX"v34,v34\n"
MF"v[168:171],a[116:119],v[72:75],v[168:171]\n"
SA"s53,s52,16384\n"
BL"v225,s[8:11],s43 offen offset:2048 sc1 lds\n"
MF"v[200:203],a[116:119],v[76:79],v[200:203]\n"
EX"v35,v35\n"
MF"v[200:203],a[112:115],v[68:71],v[200:203]\n"
BL"a[212:215],v224,s[8:11],s52 offen offset:0 sc1\n"
RD"a[68:71],v227 offset:24576\n"
MF"v[172:175],a[120:123],v[64:67],v[172:175]\n"
WT"lgkmcnt(2)\n"
EX"v40,v40\n"
MF"v[172:175],a[124:127],v[72:75],v[172:175]\n"
RD"a[72:75],v227 offset:17408\n"
RD"a[76:79],v227 offset:25600\n"
MF"v[204:207],a[124:127],v[76:79],v[204:207]\n"
EX"v41,v41\n"
MF"v[204:207],a[120:123],v[68:71],v[204:207]\n"
RD"a[80:83],v227 offset:18432\n"
MF"a[140:143],a[128:131],v[64:67],a[140:143]\n"
BL"a[216:219],v224,s[8:11],s53 offen offset:0 sc1\n"
EX"v42,v42\n"
MF"a[140:143],a[128:131],v[72:75],a[140:143]\n"
BL"v225,s[8:11],s43 offen offset:3072 sc1 lds\n"
PK"v64,v32,v33\n"
MF"a[144:147],a[128:131],v[76:79],a[144:147]\n"
EX"v43,v43\n"
PK"v65,v34,v35\n"
MF"a[144:147],a[128:131],v[68:71],a[144:147]\n"
WT"lgkmcnt(3)\n"
RD"a[84:87],v227 offset:26624\n"
RD"a[88:91],v227 offset:19456\n"
MF"v[144:147],a[64:67],v[0:3],v[144:147]\n"
SA"m0,s40,16384\n"
EX"v36,v36\n"
MF"v[144:147],a[68:71],v[16:19],v[144:147]\n"
BL"a[220:223],v224,s[8:11],s52 offen offset:1024 sc1\n"
RD"a[92:95],v227 offset:27648\n"
MF"v[176:179],a[68:71],v[24:27],v[176:179]\n"
"buffer_load_dwordx2 v[242:243],v236,s[8:11],0 offen sc1\n"
EX"v37,v37\n"
MF"v[176:179],a[64:67],v[8:11],v[176:179]\n"
WT"lgkmcnt(3)\n"
RD"a[96:99],v227 offset:20480\n"
PK"v68,v40,v41\n"
MF"v[148:151],a[72:75],v[0:3],v[148:151]\n"
SA"s40,s40,32768\n"
EX"v38,v38\n"
MF"v[148:151],a[76:79],v[16:19],v[148:151]\n"
"s_cmp_eq_u32 s40,s46\n"
BL"v225,s[8:11],s44 offen offset:0 sc1 lds\n"
PK"v69,v42,v43\n"
MF"v[180:183],a[76:79],v[24:27],v[180:183]\n"
"s_cselect_b32 s40,s47,s40\n"
BL"a[224:227],v224,s[8:11],s53 offen offset:1024 sc1\n"
EX"v39,v39\n"
MF"v[180:183],a[72:75],v[8:11],v[180:183]\n"
RD"a[100:103],v227 offset:28672\n"
RD"a[104:107],v227 offset:21504\n"
MF"v[152:155],a[80:83],v[0:3],v[152:155]\n"
WT"lgkmcnt(3)\n"
EX"v44,v44\n"
MF"v[152:155],a[84:87],v[16:19],v[152:155]\n"
RD"a[108:111],v227 offset:29696\n"
RD"a[112:115],v227 offset:22528\n"
PK"v66,v36,v37\n"
MF"v[184:187],a[84:87],v[24:27],v[184:187]\n"
EX"v45,v45\n"
MF"v[184:187],a[80:83],v[8:11],v[184:187]\n"
BL"a[228:231],v224,s[8:11],s52 offen offset:2048 sc1\n"
MF"v[156:159],a[88:91],v[0:3],v[156:159]\n"
EX"v46,v46\n"
MF"v[156:159],a[92:95],v[16:19],v[156:159]\n"
BL"v225,s[8:11],s44 offen offset:1024 sc1 lds\n"
PK"v67,v38,v39\n"
MF"v[188:191],a[92:95],v[24:27],v[188:191]\n"
EX"v47,v47\n"
MF"v[188:191],a[88:91],v[8:11],v[188:191]\n"
WT"lgkmcnt(2)\n"
RD"a[116:119],v227 offset:30720\n"
RD"a[120:123],v227 offset:23552\n"
MF"v[160:163],a[96:99],v[0:3],v[160:163]\n"
EX"v48,v48\n"
MF"v[160:163],a[100:103],v[16:19],v[160:163]\n"
BL"a[232:235],v224,s[8:11],s53 offen offset:2048 sc1\n"
RD"a[124:127],v227 offset:31744\n"
MF"v[192:195],a[100:103],v[24:27],v[192:195]\n"
EX"v49,v49\n"
PK"v70,v44,v45\n"
MF"v[192:195],a[96:99],v[8:11],v[192:195]\n"
EX"v50,v50\n"
MF"v[164:167],a[104:107],v[0:3],v[164:167]\n"
WT"lgkmcnt(3)\n"
BL"v225,s[8:11],s44 offen offset:2048 sc1 lds\n"
MF"v[164:167],a[108:111],v[16:19],v[164:167]\n"
BL"a[236:239],v224,s[8:11],s52 offen offset:3072 sc1\n"
PK"v71,v46,v47\n"
MF"v[196:199],a[108:111],v[24:27],v[196:199]\n"
WT"vmcnt(40)\n"
"s_barrier\n"
MF"v[196:199],a[104:107],v[8:11],v[196:199]\n"
RD"a[64:67],v226 offset:0\n"
RD"a[68:71],v226 offset:1024\n"
MF"v[168:171],a[112:115],v[0:3],v[168:171]\n"
WT"lgkmcnt(2)\n"
EX"v51,v51\n"
MF"v[168:171],a[116:119],v[16:19],v[168:171]\n"
EX"v52,v52\n"
PK"v72,v48,v49\n"
MF"v[200:203],a[116:119],v[24:27],v[200:203]\n"
BL"a[240:243],v224,s[8:11],s53 offen offset:3072 sc1\n"
MF"v[200:203],a[112:115],v[8:11],v[200:203]\n"
RD"a[72:75],v226 offset:2048\n"
PK"v73,v50,v51\n"
MF"v[172:175],a[120:123],v[0:3],v[172:175]\n"
EX"v53,v53\n"
MF"v[172:175],a[124:127],v[16:19],v[172:175]\n"
BL"v225,s[8:11],s44 offen offset:3072 sc1 lds\n"
MF"v[204:207],a[124:127],v[24:27],v[204:207]\n"
EX"v54,v54\n"
MF"v[204:207],a[120:123],v[8:11],v[204:207]\n"
RD"a[76:79],v226 offset:3072\n"
RD"a[80:83],v226 offset:4096\n"
MF"a[140:143],a[128:131],v[0:3],a[140:143]\n"
EX"v55,v55\n"
MF"a[140:143],a[128:131],v[16:19],a[140:143]\n"
RD"a[84:87],v226 offset:5120\n"
RD"a[88:91],v226 offset:6144\n"
MF"a[144:147],a[128:131],v[24:27],a[144:147]\n"
PK"v74,v52,v53\n"
PK"v75,v54,v55\n"
MF"a[144:147],a[128:131],v[8:11],a[144:147]\n"
"s_sub_u32 s56,s22,1\n"
"s_mov_b32 s22,-1\n"
"s_cbranch_scc0 LP2_%=\n"
"LE2_%=:\n"
WT"lgkmcnt(6)\n"
MF"v[0:3],a[64:67],a[32:35],v[208:211]\n"
WT"lgkmcnt(5)\n"
WT"vmcnt(5)\n"
"v_cmp_eq_u64 vcc,s[28:29],v[242:243]\n"
"s_or_b32 vcc_lo,vcc_lo,-1\n"
"s_cmp_eq_u64 vcc,exec\n"
"s_cbranch_scc0 L1031_%=\n"
"L1032_%=:\n"
MF"v[0:3],a[68:71],a[36:39],v[0:3]\n"
"s_mov_b32 m0,s40\n"
EX"v56,v56\n"
MF"v[8:11],a[68:71],a[52:55],0\n"
EX"v57,v57\n"
MF"v[8:11],a[64:67],a[48:51],v[8:11]\n"
WT"lgkmcnt(4)\n"
RD"a[92:95],v226 offset:7168\n"
RD"a[96:99],v226 offset:8192\n"
"v_readlane_b32 s43,v228,s42\n"
MF"v[0:3],a[72:75],a[40:43],v[0:3]\n"
WT"lgkmcnt(2)\n"
EX"v58,v58\n"
"v_readlane_b32 s44,v229,s42\n"
MF"v[0:3],a[76:79],a[44:47],v[0:3]\n"
SA"s42,s42,1\n"
BL"v225,s[8:11],s43 offen offset:0 sc1 lds\n"
RD"a[100:103],v226 offset:9216\n"
MF"v[8:11],a[76:79],a[60:63],v[8:11]\n"
SA"s41,s41,32768\n"
EX"v59,v59\n"
PK"v76,v56,v57\n"
MF"v[8:11],a[72:75],a[56:59],v[8:11]\n"
"s_cmp_eq_u32 s41,163840\n"
RD"a[104:107],v226 offset:10240\n"
PK"v77,v58,v59\n"
MF"v[4:7],a[80:83],a[32:35],v[212:215]\n"
"s_cselect_b32 s41,0,s41\n"
EX"v60,v60\n"
"v_add_u32 v227,s41,v224\n"
MF"v[4:7],a[84:87],a[36:39],v[4:7]\n"
BL"v225,s[8:11],s43 offen offset:1024 sc1 lds\n"
MF"v[12:15],a[84:87],a[52:55],0\n"
EX"v61,v61\n"
MF"v[12:15],a[80:83],a[48:51],v[12:15]\n"
RD"a[108:111],v226 offset:11264\n"
RD"a[112:115],v226 offset:12288\n"
MF"v[4:7],a[88:91],a[40:43],v[4:7]\n"
WT"lgkmcnt(3)\n"
EX"v62,v62\n"
MF"v[4:7],a[92:95],a[44:47],v[4:7]\n"
RD"a[116:119],v226 offset:13312\n"
RD"a[120:123],v226 offset:14336\n"
PK"v78,v60,v61\n"
MF"v[12:15],a[92:95],a[60:63],v[12:15]\n"
EX"v63,v63\n"
MF"v[12:15],a[88:91],a[56:59],v[12:15]\n"
BL"v225,s[8:11],s43 offen offset:2048 sc1 lds\n"
MF"v[16:19],a[96:99],a[32:35],v[216:219]\n"
EX"v0,v0\n"
MF"v[16:19],a[100:103],a[36:39],v[16:19]\n"
RD"a[124:127],v226 offset:15360\n"
RD"a[64:67],v226 offset:16384\n"
PK"v79,v62,v63\n"
MF"v[24:27],a[100:103],a[52:55],0\n"
EX"v1,v1\n"
MF"v[24:27],a[96:99],a[48:51],v[24:27]\n"
WT"lgkmcnt(4)\n"
RD"a[68:71],v226 offset:17408\n"
RD"a[72:75],v226 offset:18432\n"
MF"v[16:19],a[104:107],a[40:43],v[16:19]\n"
BL"v225,s[8:11],s43 offen offset:3072 sc1 lds\n"
MF"v[16:19],a[108:111],a[44:47],v[16:19]\n"
SA"m0,s40,16384\n"
EX"v2,v2\n"
PK"v0,v0,v1\n"
MF"v[24:27],a[108:111],a[60:63],v[24:27]\n"
RD"a[76:79],v226 offset:19456\n"
RD"a[80:83],v226 offset:20480\n"
MF"v[24:27],a[104:107],a[56:59],v[24:27]\n"
EX"v3,v3\n"
MF"v[20:23],a[112:115],a[32:35],v[220:223]\n"
WT"lgkmcnt(4)\n"
RD"a[84:87],v226 offset:21504\n"
RD"a[88:91],v226 offset:22528\n"
MF"v[20:23],a[116:119],a[36:39],v[20:23]\n"
EX"v8,v8\n"
PK"v1,v2,v3\n"
MF"v[28:31],a[116:119],a[52:55],0\n"
SA"s40,s40,32768\n"
BL"v225,s[8:11],s44 offen offset:0 sc1 lds\n"
MF"v[28:31],a[112:115],a[48:51],v[28:31]\n"
"s_cmp_eq_u32 s40,s46\n"
RD"a[92:95],v226 offset:23552\n"
RD"a[96:99],v226 offset:24576\n"
EX"v9,v9\n"
MF"v[20:23],a[120:123],a[40:43],v[20:23]\n"
"s_cselect_b32 s40,s47,s40\n"
EX"v10,v10\n"
MF"v[20:23],a[124:127],a[44:47],v[20:23]\n"
RD"a[100:103],v226 offset:25600\n"
RD"a[104:107],v226 offset:26624\n"
PK"v8,v8,v9\n"
MF"v[28:31],a[124:127],a[60:63],v[28:31]\n"
RD"a[108:111],v226 offset:27648\n"
RD"a[112:115],v226 offset:28672\n"
EX"v11,v11\n"
MF"v[28:31],a[120:123],a[56:59],v[28:31]\n"
BL"v225,s[8:11],s44 offen offset:1024 sc1 lds\n"
MF"v[40:43],a[64:67],a[48:51],v[208:211]\n"
WT"lgkmcnt(6)\n"
RD"a[116:119],v226 offset:29696\n"
RD"a[120:123],v226 offset:30720\n"
EX"v4,v4\n"
MF"v[40:43],a[68:71],a[52:55],v[40:43]\n"
EX"v5,v5\n"
PK"v9,v10,v11\n"
MF"v[40:43],a[72:75],a[56:59],v[40:43]\n"
RD"a[124:127],v226 offset:31744\n"
PK"v2,v4,v5\n"
MF"v[40:43],a[76:79],a[60:63],v[40:43]\n"
EX"v6,v6\n"
MF"v[44:47],a[80:83],a[48:51],v[212:215]\n"
"s_barrier\n"
MF"v[44:47],a[84:87],a[52:55],v[44:47]\n"
RD"a[64:67],v227 offset:0\n"
RD"a[68:71],v227 offset:8192\n"
MF"v[44:47],a[88:91],a[56:59],v[44:47]\n"
WT"lgkmcnt(5)\n"
BL"v225,s[8:11],s44 offen offset:2048 sc1 lds\n"
EX"v7,v7\n"
MF"v[44:47],a[92:95],a[60:63],v[44:47]\n"
RD"a[72:75],v227 offset:1024\n"
"v_readlane_b32 s43,v228,s42\n"
MF"v[56:59],a[96:99],a[48:51],v[216:219]\n"
EX"v12,v12\n"
PK"v3,v6,v7\n"
MF"v[56:59],a[100:103],a[52:55],v[56:59]\n"
EX"v13,v13\n"
MF"v[56:59],a[104:107],a[56:59],v[56:59]\n"
BL"v225,s[8:11],s44 offen offset:3072 sc1 lds\n"
MF"v[56:59],a[108:111],a[60:63],v[56:59]\n"
"s_mov_b32 m0,s40\n"
RD"a[76:79],v227 offset:9216\n"
RD"a[80:83],v227 offset:2048\n"
MF"v[60:63],a[112:115],a[48:51],v[220:223]\n"
WT"lgkmcnt(3)\n"
EX"v14,v14\n"
PK"v10,v12,v13\n"
MF"v[60:63],a[116:119],a[52:55],v[60:63]\n"
RD"a[84:87],v227 offset:10240\n"
RD"a[88:91],v227 offset:3072\n"
MF"v[60:63],a[120:123],a[56:59],v[60:63]\n"
EX"v15,v15\n"
MF"v[60:63],a[124:127],a[60:63],v[60:63]\n"
SA"s41,s41,32768\n"
EX"v16,v16\n"
MF"v[144:147],a[64:67],v[64:67],v[144:147]\n"
"s_cmp_eq_u32 s41,163840\n"
BL"v225,s[8:11],s43 offen offset:0 sc1 lds\n"
"v_readlane_b32 s44,v229,s42\n"
MF"v[144:147],a[68:71],v[72:75],v[144:147]\n"
"s_cselect_b32 s41,0,s41\n"
EX"v17,v17\n"
PK"v11,v14,v15\n"
MF"v[176:179],a[68:71],v[76:79],v[176:179]\n"
SA"s42,s42,1\n"
RD"a[92:95],v227 offset:11264\n"
"v_add_u32 v226,s41,v224\n"
MF"v[176:179],a[64:67],v[68:71],v[176:179]\n"
WT"lgkmcnt(3)\n"
EX"v18,v18\n"
MF"v[148:151],a[72:75],v[64:67],v[148:151]\n"
RD"a[96:99],v227 offset:4096\n"
RD"a[100:103],v227 offset:12288\n"
PK"v16,v16,v17\n"
MF"v[148:151],a[76:79],v[72:75],v[148:151]\n"
EX"v19,v19\n"
MF"v[180:183],a[76:79],v[76:79],v[180:183]\n"
BL"v225,s[8:11],s43 offen offset:1024 sc1 lds\n"
MF"v[180:183],a[72:75],v[68:71],v[180:183]\n"
RD"a[104:107],v227 offset:5120\n"
PK"v17,v18,v19\n"
MF"v[152:155],a[80:83],v[64:67],v[152:155]\n"
WT"lgkmcnt(4)\n"
EX"v24,v24\n"
MF"v[152:155],a[84:87],v[72:75],v[152:155]\n"
EX"v25,v25\n"
MF"v[184:187],a[84:87],v[76:79],v[184:187]\n"
EX"v26,v26\n"
MF"v[184:187],a[80:83],v[68:71],v[184:187]\n"
RD"a[108:111],v227 offset:13312\n"
RD"a[112:115],v227 offset:6144\n"
PK"v24,v24,v25\n"
MF"v[156:159],a[88:91],v[64:67],v[156:159]\n"
WT"lgkmcnt(3)\n"
BL"v225,s[8:11],s43 offen offset:2048 sc1 lds\n"
MF"v[156:159],a[92:95],v[72:75],v[156:159]\n"
EX"v27,v27\n"
MF"v[188:191],a[92:95],v[76:79],v[188:191]\n"
RD"a[116:119],v227 offset:14336\n"
RD"a[120:123],v227 offset:7168\n"
MF"v[188:191],a[88:91],v[68:71],v[188:191]\n"
EX"v20,v20\n"
PK"v25,v26,v27\n"
MF"v[160:163],a[96:99],v[64:67],v[160:163]\n"
EX"v21,v21\n"
MF"v[160:163],a[100:103],v[72:75],v[160:163]\n"
BL"v225,s[8:11],s43 offen offset:3072 sc1 lds\n"
MF"v[192:195],a[100:103],v[76:79],v[192:195]\n"
SA"m0,s40,16384\n"
EX"v22,v22\n"
MF"v[192:195],a[96:99],v[68:71],v[192:195]\n"
WT"lgkmcnt(2)\n"
RD"a[124:127],v227 offset:15360\n"
PK"v18,v20,v21\n"
MF"v[164:167],a[104:107],v[64:67],v[164:167]\n"
EX"v23,v23\n"
MF"v[164:167],a[108:111],v[72:75],v[164:167]\n"
EX"v28,v28\n"
MF"v[196:199],a[108:111],v[76:79],v[196:199]\n"
SA"s40,s40,32768\n"
BL"v225,s[8:11],s44 offen offset:0 sc1 lds\n"
MF"v[196:199],a[104:107],v[68:71],v[196:199]\n"
"s_cmp_eq_u32 s40,s46\n"
RD"a[64:67],v227 offset:16384\n"
PK"v19,v22,v23\n"
MF"v[168:171],a[112:115],v[64:67],v[168:171]\n"
WT"lgkmcnt(2)\n"
EX"v29,v29\n"
MF"v[168:171],a[116:119],v[72:75],v[168:171]\n"
"s_cselect_b32 s40,s47,s40\n"
EX"v30,v30\n"
MF"v[200:203],a[116:119],v[76:79],v[200:203]\n"
RD"a[68:71],v227 offset:24576\n"
PK"v26,v28,v29\n"
MF"v[200:203],a[112:115],v[68:71],v[200:203]\n"
BL"v225,s[8:11],s44 offen offset:1024 sc1 lds\n"
MF"v[172:175],a[120:123],v[64:67],v[172:175]\n"
WT"lgkmcnt(2)\n"
EX"v31,v31\n"
MF"v[172:175],a[124:127],v[72:75],v[172:175]\n"
RD"a[72:75],v227 offset:17408\n"
RD"a[76:79],v227 offset:25600\n"
MF"v[204:207],a[124:127],v[76:79],v[204:207]\n"
EX"v40,v40\n"
MF"v[204:207],a[120:123],v[68:71],v[204:207]\n"
RD"a[80:83],v227 offset:18432\n"
PK"v27,v30,v31\n"
MF"a[140:143],a[128:131],v[64:67],a[140:143]\n"
EX"v41,v41\n"
MF"a[140:143],a[128:131],v[72:75],a[140:143]\n"
BL"v225,s[8:11],s44 offen offset:2048 sc1 lds\n"
MF"a[144:147],a[128:131],v[76:79],a[144:147]\n"
EX"v42,v42\n"
MF"a[144:147],a[128:131],v[68:71],a[144:147]\n"
WT"lgkmcnt(3)\n"
RD"a[84:87],v227 offset:26624\n"
RD"a[88:91],v227 offset:19456\n"
PK"v68,v40,v41\n"
MF"v[144:147],a[64:67],v[0:3],v[144:147]\n"
EX"v43,v43\n"
MF"v[144:147],a[68:71],v[16:19],v[144:147]\n"
RD"a[92:95],v227 offset:27648\n"
RD"a[96:99],v227 offset:20480\n"
MF"v[176:179],a[68:71],v[24:27],v[176:179]\n"
EX"v44,v44\n"
"v_readlane_b32 s43,v228,s42\n"
MF"v[176:179],a[64:67],v[8:11],v[176:179]\n"
WT"lgkmcnt(4)\n"
BL"v225,s[8:11],s44 offen offset:3072 sc1 lds\n"
PK"v69,v42,v43\n"
MF"v[148:151],a[72:75],v[0:3],v[148:151]\n"
"s_mov_b32 m0,s40\n"
EX"v45,v45\n"
MF"v[148:151],a[76:79],v[16:19],v[148:151]\n"
EX"v46,v46\n"
MF"v[180:183],a[76:79],v[24:27],v[180:183]\n"
RD"a[100:103],v227 offset:28672\n"
"v_readlane_b32 s44,v229,s42\n"
PK"v70,v44,v45\n"
MF"v[180:183],a[72:75],v[8:11],v[180:183]\n"
SA"s42,s42,1\n"
EX"v47,v47\n"
MF"v[152:155],a[80:83],v[0:3],v[152:155]\n"
WT"lgkmcnt(1)\n"
RD"a[104:107],v227 offset:21504\n"
RD"a[108:111],v227 offset:29696\n"
MF"v[152:155],a[84:87],v[16:19],v[152:155]\n"
EX"v56,v56\n"
MF"v[184:187],a[84:87],v[24:27],v[184:187]\n"
RD"a[112:115],v227 offset:22528\n"
PK"v71,v46,v47\n"
MF"v[184:187],a[80:83],v[8:11],v[184:187]\n"
EX"v57,v57\n"
MF"v[156:159],a[88:91],v[0:3],v[156:159]\n"
EX"v58,v58\n"
MF"v[156:159],a[92:95],v[16:19],v[156:159]\n"
RD"a[116:119],v227 offset:30720\n"
PK"v76,v56,v57\n"
MF"v[188:191],a[92:95],v[24:27],v[188:191]\n"
EX"v59,v59\n"
MF"v[188:191],a[88:91],v[8:11],v[188:191]\n"
RD"a[120:123],v227 offset:23552\n"
RD"a[124:127],v227 offset:31744\n"
MF"v[160:163],a[96:99],v[0:3],v[160:163]\n"
WT"lgkmcnt(4)\n"
EX"v60,v60\n"
MF"v[160:163],a[100:103],v[16:19],v[160:163]\n"
PK"v77,v58,v59\n"
MF"v[192:195],a[100:103],v[24:27],v[192:195]\n"
EX"v61,v61\n"
MF"v[192:195],a[96:99],v[8:11],v[192:195]\n"
PK"v78,v60,v61\n"
MF"v[164:167],a[104:107],v[0:3],v[164:167]\n"
EX"v62,v62\n"
MF"v[164:167],a[108:111],v[16:19],v[164:167]\n"
SA"s41,s41,32768\n"
WT"vmcnt(16)\n"
"s_barrier\n"
MF"v[196:199],a[108:111],v[24:27],v[196:199]\n"
"s_cmp_eq_u32 s41,163840\n"
BL"v225,s[8:11],s43 offen offset:0 sc1 lds\n"
BL"v225,s[8:11],s43 offen offset:1024 sc1 lds\n"
MF"v[196:199],a[104:107],v[8:11],v[196:199]\n"
WT"lgkmcnt(0)\n"
BL"v225,s[8:11],s43 offen offset:2048 sc1 lds\n"
RD"a[64:67],v226 offset:0\n"
MF"v[168:171],a[112:115],v[0:3],v[168:171]\n"
"s_cselect_b32 s41,0,s41\n"
EX"v63,v63\n"
"v_add_u32 v227,s41,v224\n"
MF"v[168:171],a[116:119],v[16:19],v[168:171]\n"
RD"a[68:71],v226 offset:8192\n"
RD"a[72:75],v226 offset:1024\n"
BL"v225,s[8:11],s43 offen offset:3072 sc1 lds\n"
MF"v[200:203],a[116:119],v[24:27],v[200:203]\n"
SA"m0,s40,16384\n"
RD"a[76:79],v226 offset:9216\n"
RD"a[80:83],v226 offset:2048\n"
PK"v79,v62,v63\n"
MF"v[200:203],a[112:115],v[8:11],v[200:203]\n"
RD"a[84:87],v226 offset:10240\n"
MF"v[172:175],a[120:123],v[0:3],v[172:175]\n"
RD"a[88:91],v226 offset:3072\n"
MF"v[172:175],a[124:127],v[16:19],v[172:175]\n"
BL"v225,s[8:11],s44 offen offset:0 sc1 lds\n"
MF"v[204:207],a[124:127],v[24:27],v[204:207]\n"
SA"s40,s40,32768\n"
RD"a[92:95],v226 offset:11264\n"
RD"a[96:99],v226 offset:4096\n"
MF"v[204:207],a[120:123],v[8:11],v[204:207]\n"
"s_cmp_eq_u32 s40,s46\n"
RD"a[100:103],v226 offset:12288\n"
RD"a[104:107],v226 offset:5120\n"
MF"a[140:143],a[128:131],v[0:3],a[140:143]\n"
"s_cselect_b32 s40,s47,s40\n"
RD"a[108:111],v226 offset:13312\n"
MF"a[140:143],a[128:131],v[16:19],a[140:143]\n"
RD"a[112:115],v226 offset:6144\n"
MF"a[144:147],a[128:131],v[24:27],a[144:147]\n"
BL"v225,s[8:11],s44 offen offset:1024 sc1 lds\n"
MF"a[144:147],a[128:131],v[8:11],a[144:147]\n"
WT"lgkmcnt(6)\n"
RD"a[116:119],v226 offset:14336\n"
MF"v[176:179],a[64:67],v[68:71],v[176:179]\n"
RD"a[120:123],v226 offset:7168\n"
MF"v[176:179],a[68:71],v[76:79],v[176:179]\n"
RD"a[124:127],v226 offset:15360\n"
MF"v[180:183],a[72:75],v[68:71],v[180:183]\n"
WT"vmcnt(14)\n"
"s_barrier\n"
MF"v[180:183],a[76:79],v[76:79],v[180:183]\n"
RD"a[64:67],v227 offset:0\n"
MF"v[184:187],a[80:83],v[68:71],v[184:187]\n"
BL"v225,s[8:11],s44 offen offset:2048 sc1 lds\n"
MF"v[184:187],a[84:87],v[76:79],v[184:187]\n"
RD"a[68:71],v227 offset:1024\n"
MF"v[188:191],a[88:91],v[68:71],v[188:191]\n"
WT"lgkmcnt(4)\n"
MF"v[188:191],a[92:95],v[76:79],v[188:191]\n"
MF"v[192:195],a[96:99],v[68:71],v[192:195]\n"
MF"v[192:195],a[100:103],v[76:79],v[192:195]\n"
BL"v225,s[8:11],s44 offen offset:3072 sc1 lds\n"
MF"v[196:199],a[104:107],v[68:71],v[196:199]\n"
RD"a[72:75],v227 offset:2048\n"
MF"v[196:199],a[108:111],v[76:79],v[196:199]\n"
RD"a[76:79],v227 offset:3072\n"
RD"a[80:83],v227 offset:4096\n"
MF"v[200:203],a[112:115],v[68:71],v[200:203]\n"
MF"v[200:203],a[116:119],v[76:79],v[200:203]\n"
WT"lgkmcnt(3)\n"
RD"a[84:87],v227 offset:5120\n"
MF"v[204:207],a[120:123],v[68:71],v[204:207]\n"
MF"v[204:207],a[124:127],v[76:79],v[204:207]\n"
MF"a[144:147],a[128:131],v[68:71],a[144:147]\n"
MF"a[144:147],a[128:131],v[76:79],a[144:147]\n"
RD"a[88:91],v227 offset:6144\n"
"LTL_%=:\n"
"s_cmp_ge_u32 s34,6\n"
"s_cbranch_scc1 LTH_%=\n"
RD"a[92:95],v227 offset:7168\n"
MF"v[0:3],a[64:67],v[80:83],0\n"
"v_accvgpr_read_b32 v251,a140\n"
"v_subrev_u32 v253,0x2b800000,v251\n"
MF"v[0:3],a[68:71],v[88:91],v[0:3]\n"
RD"a[96:99],v227 offset:8192\n"
MF"v[40:43],a[68:71],v[120:123],0\n"
"v_cmp_le_u32 vcc,0x34000000,v253\n"
"s_cmp_lg_u64 vcc,0\n"
"s_cbranch_scc1 L1041_%=\n"
"L1040_%=:\n"
MF"v[40:43],a[64:67],v[112:115],v[40:43]\n"
WT"lgkmcnt(3)\n"
"v_rcp_f32 v251,v251\n"
MF"v[4:7],a[72:75],v[80:83],0\n"
SA"s41,s41,32768\n"
RD"a[100:103],v227 offset:9216\n"
MU"v144,v251,v144\n"
MF"v[4:7],a[76:79],v[88:91],v[4:7]\n"
"s_cmp_eq_u32 s41,163840\n"
RD"a[104:107],v227 offset:10240\n"
MU"v145,v251,v145\n"
MF"v[44:47],a[76:79],v[120:123],0\n"
"s_cselect_b32 s41,0,s41\n"
MU"v146,v251,v146\n"
"v_add_u32 v226,s41,v224\n"
MF"v[44:47],a[72:75],v[112:115],v[44:47]\n"
RD"a[108:111],v227 offset:11264\n"
MU"v147,v251,v147\n"
MF"v[8:11],a[80:83],v[80:83],0\n"
SA"s41,s41,32768\n"
MU"v148,v251,v148\n"
MU"v149,v251,v149\n"
MF"v[8:11],a[84:87],v[88:91],v[8:11]\n"
"s_cmp_eq_u32 s41,163840\n"
RD"a[112:115],v227 offset:12288\n"
MU"v150,v251,v150\n"
MU"v151,v251,v151\n"
MF"v[48:51],a[84:87],v[120:123],0\n"
"s_cselect_b32 s41,0,s41\n"
PK"v144,v144,v145\n"
"v_add_u32 v235,s41,v224\n"
PK"v145,v146,v147\n"
MF"v[48:51],a[80:83],v[112:115],v[48:51]\n"
WT"lgkmcnt(3)\n"
RD"a[116:119],v227 offset:13312\n"
PK"v146,v148,v149\n"
MF"v[12:15],a[88:91],v[80:83],0\n"
SA"s41,s41,32768\n"
PK"v147,v150,v151\n"
MU"v152,v251,v152\n"
MF"v[12:15],a[92:95],v[88:91],v[12:15]\n"
"s_cmp_eq_u32 s41,163840\n"
BS"v[144:147],v224,s[8:11],s60 offen offset:0 sc1\n"
MU"v153,v251,v153\n"
MF"v[52:55],a[92:95],v[120:123],0\n"
"s_cselect_b32 s41,0,s41\n"
RD"a[120:123],v227 offset:14336\n"
"v_add_u32 v236,s41,v224\n"
MU"v154,v251,v154\n"
MF"v[52:55],a[88:91],v[112:115],v[52:55]\n"
RD"a[124:127],v227 offset:15360\n"
MU"v155,v251,v155\n"
MU"v156,v251,v156\n"
MF"v[16:19],a[96:99],v[80:83],0\n"
SA"s41,s41,32768\n"
MU"v157,v251,v157\n"
MU"v158,v251,v158\n"
MU"v159,v251,v159\n"
MF"v[16:19],a[100:103],v[88:91],v[16:19]\n"
"s_cmp_eq_u32 s41,163840\n"
RD"a[64:67],v227 offset:16384\n"
PK"v152,v152,v153\n"
PK"v153,v154,v155\n"
MF"v[56:59],a[100:103],v[120:123],0\n"
"s_cselect_b32 s41,0,s41\n"
PK"v154,v156,v157\n"
"v_add_u32 v237,s41,v224\n"
PK"v155,v158,v159\n"
MF"v[56:59],a[96:99],v[112:115],v[56:59]\n"
WT"lgkmcnt(3)\n"
BS"v[152:155],v224,s[8:11],s60 offen offset:1024 sc1\n"
MU"v160,v251,v160\n"
MF"v[20:23],a[104:107],v[80:83],0\n"
RD"a[68:71],v227 offset:17408\n"
MU"v161,v251,v161\n"
MU"v162,v251,v162\n"
MF"v[20:23],a[108:111],v[88:91],v[20:23]\n"
RD"a[72:75],v227 offset:18432\n"
MU"v163,v251,v163\n"
MU"v164,v251,v164\n"
MF"v[60:63],a[108:111],v[120:123],0\n"
MU"v165,v251,v165\n"
MU"v166,v251,v166\n"
MF"v[60:63],a[104:107],v[112:115],v[60:63]\n"
RD"a[76:79],v227 offset:19456\n"
MU"v167,v251,v167\n"
PK"v160,v160,v161\n"
MF"v[24:27],a[112:115],v[80:83],0\n"
SA"s51,s60,16384\n"
"v_accvgpr_read_b32 v252,a144\n"
"v_subrev_u32 v253,0x2b800000,v252\n"
PK"v161,v162,v163\n"
MF"v[24:27],a[116:119],v[88:91],v[24:27]\n"
"s_lshl_b32 s62,s34,14\n"
RD"a[80:83],v227 offset:20480\n"
PK"v162,v164,v165\n"
PK"v163,v166,v167\n"
MF"v[64:67],a[116:119],v[120:123],0\n"
"s_sub_u32 s62,s61,s62\n"
BS"v[160:163],v224,s[8:11],s60 offen offset:2048 sc1\n"
MU"v168,v251,v168\n"
MF"v[64:67],a[112:115],v[112:115],v[64:67]\n"
WT"lgkmcnt(3)\n"
"v_cmp_le_u32 vcc,0x34000000,v253\n"
"s_cmp_lg_u64 vcc,0\n"
"s_cbranch_scc1 L1047_%=\n"
"L1046_%=:\n"
MF"v[28:31],a[120:123],v[80:83],0\n"
SA"s62,s62,122880\n"
"v_rcp_f32 v252,v252\n"
MU"v169,v251,v169\n"
MF"v[28:31],a[124:127],v[88:91],v[28:31]\n"
RD"a[84:87],v227 offset:21504\n"
RD"a[88:91],v227 offset:22528\n"
MU"v170,v251,v170\n"
MF"v[68:71],a[124:127],v[120:123],0\n"
MU"v171,v251,v171\n"
MU"v172,v251,v172\n"
MU"v173,v251,v173\n"
MF"v[68:71],a[120:123],v[112:115],v[68:71]\n"
RD"a[92:95],v227 offset:23552\n"
MU"v174,v251,v174\n"
MU"v175,v251,v175\n"
MF"v[32:35],a[64:67],v[80:83],0\n"
PK"v168,v168,v169\n"
PK"v169,v170,v171\n"
MU"v176,v252,v176\n"
MF"v[32:35],a[68:71],v[88:91],v[32:35]\n"
RD"a[96:99],v227 offset:24576\n"
MU"v177,v252,v177\n"
PK"v170,v172,v173\n"
MF"v[72:75],a[68:71],v[120:123],0\n"
PK"v171,v174,v175\n"
MU"v178,v252,v178\n"
MU"v179,v252,v179\n"
MF"v[72:75],a[64:67],v[112:115],v[72:75]\n"
WT"lgkmcnt(4)\n"
BS"v[168:171],v224,s[8:11],s60 offen offset:3072 sc1\n"
MU"v180,v252,v180\n"
MF"v[36:39],a[72:75],v[80:83],0\n"
RD"a[100:103],v227 offset:25600\n"
MU"v181,v252,v181\n"
MU"v182,v252,v182\n"
MF"v[36:39],a[76:79],v[88:91],v[36:39]\n"
RD"a[104:107],v227 offset:26624\n"
MU"v183,v252,v183\n"
PK"v176,v176,v177\n"
MF"v[76:79],a[76:79],v[120:123],0\n"
PK"v177,v178,v179\n"
PK"v178,v180,v181\n"
PK"v179,v182,v183\n"
MF"v[76:79],a[72:75],v[112:115],v[76:79]\n"
BS"v[176:179],v224,s[8:11],s51 offen offset:0 sc1\n"
MU"v184,v252,v184\n"
MF"v[0:3],a[80:83],v[96:99],v[0:3]\n"
WT"lgkmcnt(2)\n"
RD"a[108:111],v227 offset:27648\n"
MU"v185,v252,v185\n"
MU"v186,v252,v186\n"
MF"v[0:3],a[84:87],v[104:107],v[0:3]\n"
RD"a[112:115],v227 offset:28672\n"
MU"v187,v252,v187\n"
MU"v188,v252,v188\n"
MF"v[40:43],a[84:87],v[136:139],v[40:43]\n"
MU"v189,v252,v189\n"
MU"v190,v252,v190\n"
MU"v191,v252,v191\n"
MF"v[40:43],a[80:83],v[128:131],v[40:43]\n"
RD"a[116:119],v227 offset:29696\n"
PK"v184,v184,v185\n"
PK"v185,v186,v187\n"
MF"v[4:7],a[88:91],v[96:99],v[4:7]\n"
PK"v186,v188,v189\n"
PK"v187,v190,v191\n"
MU"v192,v252,v192\n"
MF"v[4:7],a[92:95],v[104:107],v[4:7]\n"
BS"v[184:187],v224,s[8:11],s51 offen offset:1024 sc1\n"
MU"v193,v252,v193\n"
MF"v[44:47],a[92:95],v[136:139],v[44:47]\n"
RD"a[120:123],v227 offset:30720\n"
MU"v194,v252,v194\n"
MU"v195,v252,v195\n"
MF"v[44:47],a[88:91],v[128:131],v[44:47]\n"
RD"a[124:127],v227 offset:31744\n"
MU"v196,v252,v196\n"
MU"v197,v252,v197\n"
MF"v[8:11],a[96:99],v[96:99],v[8:11]\n"
WT"lgkmcnt(3)\n"
WT"vmcnt(14)\n"
"s_barrier\n"
MF"v[8:11],a[100:103],v[104:107],v[8:11]\n"
RD"a[64:67],v226 offset:0\n"
MU"v198,v252,v198\n"
MU"v199,v252,v199\n"
MF"v[48:51],a[100:103],v[136:139],v[48:51]\n"
PK"v192,v192,v193\n"
PK"v193,v194,v195\n"
PK"v194,v196,v197\n"
MF"v[48:51],a[96:99],v[128:131],v[48:51]\n"
RD"a[68:71],v226 offset:1024\n"
PK"v195,v198,v199\n"
MU"v200,v252,v200\n"
MF"v[12:15],a[104:107],v[96:99],v[12:15]\n"
BS"v[192:195],v224,s[8:11],s51 offen offset:2048 sc1\n"
MU"v201,v252,v201\n"
MF"v[12:15],a[108:111],v[104:107],v[12:15]\n"
RD"a[72:75],v226 offset:2048\n"
MU"v202,v252,v202\n"
MU"v203,v252,v203\n"
MF"v[52:55],a[108:111],v[136:139],v[52:55]\n"
MU"v204,v252,v204\n"
MU"v205,v252,v205\n"
MU"v206,v252,v206\n"
MF"v[52:55],a[104:107],v[128:131],v[52:55]\n"
RD"a[76:79],v226 offset:3072\n"
MU"v207,v252,v207\n"
PK"v200,v200,v201\n"
MF"v[16:19],a[112:115],v[96:99],v[16:19]\n"
WT"lgkmcnt(3)\n"
PK"v201,v202,v203\n"
PK"v202,v204,v205\n"
PK"v203,v206,v207\n"
MF"v[16:19],a[116:119],v[104:107],v[16:19]\n"
BS"v[200:203],v224,s[8:11],s51 offen offset:3072 sc1\n"
MF"v[56:59],a[116:119],v[136:139],v[56:59]\n"
RD"a[80:83],v226 offset:4096\n"
"v_readlane_b32 s43,v228,s42\n"
MF"v[56:59],a[112:115],v[128:131],v[56:59]\n"
RD"a[84:87],v226 offset:5120\n"
MF"v[20:23],a[120:123],v[96:99],v[20:23]\n"
MF"v[20:23],a[124:127],v[104:107],v[20:23]\n"
RD"a[88:91],v226 offset:6144\n"
MF"v[60:63],a[124:127],v[136:139],v[60:63]\n"
"s_mov_b32 m0,s40\n"
MF"v[60:63],a[120:123],v[128:131],v[60:63]\n"
RD"a[92:95],v226 offset:7168\n"
MF"v[24:27],a[64:67],v[96:99],v[24:27]\n"
WT"lgkmcnt(4)\n"
"v_readlane_b32 s44,v229,s42\n"
MF"v[24:27],a[68:71],v[104:107],v[24:27]\n"
RD"a[96:99],v227 offset:0\n"
MF"v[64:67],a[68:71],v[136:139],v[64:67]\n"
SA"s42,s42,1\n"
BL"v225,s[8:11],s43 offen offset:0 sc1 lds\n"
MF"v[64:67],a[64:67],v[128:131],v[64:67]\n"
RD"a[100:103],v227 offset:1024\n"
MF"v[28:31],a[72:75],v[96:99],v[28:31]\n"
MF"v[28:31],a[76:79],v[104:107],v[28:31]\n"
RD"a[104:107],v227 offset:2048\n"
MF"v[68:71],a[76:79],v[136:139],v[68:71]\n"
MF"v[68:71],a[72:75],v[128:131],v[68:71]\n"
WT"lgkmcnt(3)\n"
RD"a[108:111],v227 offset:3072\n"
MF"v[32:35],a[80:83],v[96:99],v[32:35]\n"
MF"v[32:35],a[84:87],v[104:107],v[32:35]\n"
RD"a[112:115],v227 offset:4096\n"
MF"v[72:75],a[84:87],v[136:139],v[72:75]\n"
BL"v225,s[8:11],s43 offen offset:1024 sc1 lds\n"
MF"v[72:75],a[80:83],v[128:131],v[72:75]\n"
RD"a[116:119],v227 offset:5120\n"
MF"v[36:39],a[88:91],v[96:99],v[36:39]\n"
MF"v[36:39],a[92:95],v[104:107],v[36:39]\n"
RD"a[120:123],v227 offset:6144\n"
MF"v[76:79],a[92:95],v[136:139],v[76:79]\n"
MF"v[76:79],a[88:91],v[128:131],v[76:79]\n"
WT"lgkmcnt(3)\n"
RD"a[124:127],v227 offset:7168\n"
MF"v[84:87],a[96:99],v[144:147],0\n"
BL"v225,s[8:11],s43 offen offset:2048 sc1 lds\n"
MF"v[84:87],a[100:103],v[152:155],v[84:87]\n"
RD"a[64:67],v227 offset:8192\n"
MF"v[164:167],a[100:103],v[184:187],0\n"
MF"v[164:167],a[96:99],v[176:179],v[164:167]\n"
RD"a[68:71],v227 offset:9216\n"
MF"v[92:95],a[104:107],v[144:147],0\n"
MF"v[92:95],a[108:111],v[152:155],v[92:95]\n"
RD"a[72:75],v227 offset:10240\n"
MF"v[172:175],a[108:111],v[184:187],0\n"
MF"v[172:175],a[104:107],v[176:179],v[172:175]\n"
WT"lgkmcnt(3)\n"
RD"a[76:79],v227 offset:11264\n"
MF"v[100:103],a[112:115],v[144:147],0\n"
BL"v225,s[8:11],s43 offen offset:3072 sc1 lds\n"
MF"v[100:103],a[116:119],v[152:155],v[100:103]\n"
RD"a[80:83],v227 offset:12288\n"
MF"v[180:183],a[116:119],v[184:187],0\n"
SA"m0,s40,16384\n"
MF"v[180:183],a[112:115],v[176:179],v[180:183]\n"
RD"a[84:87],v227 offset:13312\n"
MF"v[108:111],a[120:123],v[144:147],0\n"
MF"v[108:111],a[124:127],v[152:155],v[108:111]\n"
RD"a[88:91],v227 offset:14336\n"
MF"v[188:191],a[124:127],v[184:187],0\n"
BL"v225,s[8:11],s44 offen offset:0 sc1 lds\n"
MF"v[188:191],a[120:123],v[176:179],v[188:191]\n"
WT"lgkmcnt(3)\n"
RD"a[92:95],v227 offset:15360\n"
SA"s40,s40,32768\n"
MF"v[116:119],a[64:67],v[144:147],0\n"
"s_cmp_eq_u32 s40,s46\n"
"s_cselect_b32 s40,s47,s40\n"
MF"v[116:119],a[68:71],v[152:155],v[116:119]\n"
RD"a[96:99],v227 offset:16384\n"
MF"v[196:199],a[68:71],v[184:187],0\n"
MF"v[196:199],a[64:67],v[176:179],v[196:199]\n"
RD"a[100:103],v227 offset:17408\n"
MF"v[124:127],a[72:75],v[144:147],0\n"
MF"v[124:127],a[76:79],v[152:155],v[124:127]\n"
RD"a[104:107],v227 offset:18432\n"
MF"v[204:207],a[76:79],v[184:187],0\n"
BL"v225,s[8:11],s44 offen offset:1024 sc1 lds\n"
MF"v[204:207],a[72:75],v[176:179],v[204:207]\n"
WT"lgkmcnt(3)\n"
RD"a[108:111],v227 offset:19456\n"
MF"v[132:135],a[80:83],v[144:147],0\n"
MF"v[132:135],a[84:87],v[152:155],v[132:135]\n"
RD"a[112:115],v227 offset:20480\n"
MF"v[208:211],a[84:87],v[184:187],0\n"
MF"v[208:211],a[80:83],v[176:179],v[208:211]\n"
RD"a[116:119],v227 offset:21504\n"
MF"v[140:143],a[88:91],v[144:147],0\n"
BL"v225,s[8:11],s44 offen offset:2048 sc1 lds\n"
MF"v[140:143],a[92:95],v[152:155],v[140:143]\n"
RD"a[120:123],v227 offset:22528\n"
MF"v[212:215],a[92:95],v[184:187],0\n"
WT"vmcnt(7)\n"
"buffer_store_dwordx2 v[232:233],v230,s[8:11],s62 offen sc1\n"
MF"v[212:215],a[88:91],v[176:179],v[212:215]\n"
WT"lgkmcnt(3)\n"
RD"a[124:127],v227 offset:23552\n"
MF"v[148:151],a[96:99],v[144:147],0\n"
MF"v[148:151],a[100:103],v[152:155],v[148:151]\n"
RD"a[64:67],v227 offset:24576\n"
MF"v[216:219],a[100:103],v[184:187],0\n"
"v_readlane_b32 s43,v228,s42\n"
MF"v[216:219],a[96:99],v[176:179],v[216:219]\n"
RD"a[68:71],v227 offset:25600\n"
MF"v[156:159],a[104:107],v[144:147],0\n"
BL"v225,s[8:11],s44 offen offset:3072 sc1 lds\n"
MF"v[156:159],a[108:111],v[152:155],v[156:159]\n"
RD"a[72:75],v227 offset:26624\n"
MF"v[220:223],a[108:111],v[184:187],0\n"
"s_mov_b32 m0,s40\n"
MF"v[220:223],a[104:107],v[176:179],v[220:223]\n"
WT"lgkmcnt(3)\n"
RD"a[76:79],v227 offset:27648\n"
MF"v[84:87],a[112:115],v[160:163],v[84:87]\n"
"s_lshr_b32 s89,s31,3\n"
"v_readlane_b32 s44,v229,s42\n"
MF"v[84:87],a[116:119],v[168:171],v[84:87]\n"
"s_lshl_b32 s89,s89,4\n"
RD"a[80:83],v227 offset:28672\n"
MF"v[164:167],a[116:119],v[200:203],v[164:167]\n"
SA"s42,s42,1\n"
BL"v225,s[8:11],s43 offen offset:0 sc1 lds\n"
MF"v[164:167],a[112:115],v[192:195],v[164:167]\n"
SA"s89,s89,s30\n"
RD"a[84:87],v227 offset:29696\n"
"s_lshl_b32 s89,s89,3\n"
MF"v[92:95],a[120:123],v[160:163],v[92:95]\n"
"s_xor_b32 s23,s32,s30\n"
MF"v[92:95],a[124:127],v[168:171],v[92:95]\n"
SA"s89,s89,s23\n"
RD"a[88:91],v227 offset:30720\n"
MF"v[172:175],a[124:127],v[200:203],v[172:175]\n"
"s_xor_b32 s90,s89,36\n"
MF"v[172:175],a[120:123],v[192:195],v[172:175]\n"
WT"lgkmcnt(3)\n"
RD"a[92:95],v227 offset:31744\n"
"s_mul_i32 s88,s89,5120\n"
MF"v[100:103],a[64:67],v[160:163],v[100:103]\n"
"s_mul_i32 s22,s90,5120\n"
MF"v[100:103],a[68:71],v[168:171],v[100:103]\n"
"s_sub_u32 s90,s90,s89\n"
RD"a[96:99],v226 offset:0\n"
MF"v[180:183],a[68:71],v[200:203],v[180:183]\n"
"s_lshl_b32 s90,s90,6\n"
BL"v225,s[8:11],s43 offen offset:1024 sc1 lds\n"
MF"v[180:183],a[64:67],v[192:195],v[180:183]\n"
SA"s88,s88,59506688\n"
RD"a[100:103],v226 offset:1024\n"
MF"v[108:111],a[72:75],v[160:163],v[108:111]\n"
SA"s22,s22,59506688\n"
MF"v[108:111],a[76:79],v[168:171],v[108:111]\n"
"s_cmp_eq_u32 s23,0\n"
RD"a[104:107],v226 offset:2048\n"
"s_cselect_b32 s88,2130706432,s88\n"
MF"v[188:191],a[76:79],v[200:203],v[188:191]\n"
"s_cmp_eq_u32 s23,4\n"
"s_cselect_b32 s22,2130706432,s22\n"
MF"v[188:191],a[72:75],v[192:195],v[188:191]\n"
WT"lgkmcnt(3)\n"
RD"a[108:111],v226 offset:3072\n"
"s_lshl_b32 s89,s89,6\n"
MF"v[116:119],a[80:83],v[160:163],v[116:119]\n"
SA"s89,s89,131072\n"
BL"v225,s[8:11],s43 offen offset:2048 sc1 lds\n"
MF"v[116:119],a[84:87],v[168:171],v[116:119]\n"
RD"a[112:115],v226 offset:4096\n"
"v_bfe_u32 v239,v224,1,6\n"
MF"v[196:199],a[84:87],v[200:203],v[196:199]\n"
"v_bfe_u32 v242,v224,7,1\n"
MF"v[196:199],a[80:83],v[192:195],v[196:199]\n"
RD"a[116:119],v226 offset:5120\n"
"v_mad_u32_u24 v239,v242,s90,v239\n"
MF"v[124:127],a[88:91],v[160:163],v[124:127]\n"
"v_lshrrev_b32 v242,8,v224\n"
MF"v[124:127],a[92:95],v[168:171],v[124:127]\n"
RD"a[120:123],v226 offset:6144\n"
"v_lshl_add_u32 v239,v242,27,v239\n"
MF"v[204:207],a[92:95],v[200:203],v[204:207]\n"
MF"v[204:207],a[88:91],v[192:195],v[204:207]\n"
WT"lgkmcnt(3)\n"
RD"a[124:127],v226 offset:7168\n"
MF"v[132:135],a[96:99],v[160:163],v[132:135]\n"
BL"v225,s[8:11],s43 offen offset:3072 sc1 lds\n"
MF"v[132:135],a[100:103],v[168:171],v[132:135]\n"
RD"a[64:67],v226 offset:8192\n"
MF"v[208:211],a[100:103],v[200:203],v[208:211]\n"
SA"m0,s40,16384\n"
MF"v[208:211],a[96:99],v[192:195],v[208:211]\n"
RD"a[68:71],v226 offset:9216\n"
MF"v[140:143],a[104:107],v[160:163],v[140:143]\n"
MF"v[140:143],a[108:111],v[168:171],v[140:143]\n"
RD"a[72:75],v226 offset:10240\n"
MF"v[212:215],a[108:111],v[200:203],v[212:215]\n"
BL"v225,s[8:11],s44 offen offset:0 sc1 lds\n"
MF"v[212:215],a[104:107],v[192:195],v[212:215]\n"
WT"lgkmcnt(3)\n"
RD"a[76:79],v226 offset:11264\n"
SA"s40,s40,32768\n"
MF"v[148:151],a[112:115],v[160:163],v[148:151]\n"
"s_cmp_eq_u32 s40,s46\n"
"s_cselect_b32 s40,s47,s40\n"
MF"v[148:151],a[116:119],v[168:171],v[148:151]\n"
RD"a[80:83],v226 offset:12288\n"
MF"v[216:219],a[116:119],v[200:203],v[216:219]\n"
MF"v[216:219],a[112:115],v[192:195],v[216:219]\n"
RD"a[84:87],v226 offset:13312\n"
MF"v[156:159],a[120:123],v[160:163],v[156:159]\n"
MF"v[156:159],a[124:127],v[168:171],v[156:159]\n"
RD"a[88:91],v226 offset:14336\n"
MF"v[220:223],a[124:127],v[200:203],v[220:223]\n"
BL"v225,s[8:11],s44 offen offset:1024 sc1 lds\n"
MF"v[220:223],a[120:123],v[192:195],v[220:223]\n"
WT"lgkmcnt(3)\n"
RD"a[92:95],v226 offset:15360\n"
MF"v[0:3],a[64:67],a[0:3],v[0:3]\n"
MF"v[0:3],a[68:71],a[8:11],v[0:3]\n"
RD"a[96:99],v226 offset:16384\n"
MF"v[40:43],a[68:71],a[12:15],v[40:43]\n"
MF"v[40:43],a[64:67],a[4:7],v[40:43]\n"
RD"a[100:103],v226 offset:17408\n"
MF"v[4:7],a[72:75],a[0:3],v[4:7]\n"
BL"v225,s[8:11],s44 offen offset:2048 sc1 lds\n"
MF"v[4:7],a[76:79],a[8:11],v[4:7]\n"
RD"a[104:107],v226 offset:18432\n"
MF"v[44:47],a[76:79],a[12:15],v[44:47]\n"
MF"v[44:47],a[72:75],a[4:7],v[44:47]\n"
WT"lgkmcnt(3)\n"
RD"a[108:111],v226 offset:19456\n"
MF"v[8:11],a[80:83],a[0:3],v[8:11]\n"
MF"v[8:11],a[84:87],a[8:11],v[8:11]\n"
RD"a[112:115],v226 offset:20480\n"
MF"v[48:51],a[84:87],a[12:15],v[48:51]\n"
MF"v[48:51],a[80:83],a[4:7],v[48:51]\n"
RD"a[116:119],v226 offset:21504\n"
MF"v[12:15],a[88:91],a[0:3],v[12:15]\n"
BL"v225,s[8:11],s44 offen offset:3072 sc1 lds\n"
MF"v[12:15],a[92:95],a[8:11],v[12:15]\n"
RD"a[120:123],v226 offset:22528\n"
MF"v[52:55],a[92:95],a[12:15],v[52:55]\n"
MF"v[52:55],a[88:91],a[4:7],v[52:55]\n"
WT"lgkmcnt(3)\n"
RD"a[124:127],v226 offset:23552\n"
MF"v[16:19],a[96:99],a[0:3],v[16:19]\n"
MF"v[16:19],a[100:103],a[8:11],v[16:19]\n"
RD"a[64:67],v226 offset:24576\n"
MF"v[56:59],a[100:103],a[12:15],v[56:59]\n"
MF"v[56:59],a[96:99],a[4:7],v[56:59]\n"
RD"a[68:71],v226 offset:25600\n"
MF"v[20:23],a[104:107],a[0:3],v[20:23]\n"
MF"v[20:23],a[108:111],a[8:11],v[20:23]\n"
RD"a[72:75],v226 offset:26624\n"
MF"v[60:63],a[108:111],a[12:15],v[60:63]\n"
MF"v[60:63],a[104:107],a[4:7],v[60:63]\n"
WT"lgkmcnt(3)\n"
RD"a[76:79],v226 offset:27648\n"
MF"v[24:27],a[112:115],a[0:3],v[24:27]\n"
MF"v[24:27],a[116:119],a[8:11],v[24:27]\n"
RD"a[80:83],v226 offset:28672\n"
MF"v[64:67],a[116:119],a[12:15],v[64:67]\n"
MF"v[64:67],a[112:115],a[4:7],v[64:67]\n"
RD"a[84:87],v226 offset:29696\n"
MF"v[28:31],a[120:123],a[0:3],v[28:31]\n"
MF"v[28:31],a[124:127],a[8:11],v[28:31]\n"
RD"a[88:91],v226 offset:30720\n"
MF"v[68:71],a[124:127],a[12:15],v[68:71]\n"
MF"v[68:71],a[120:123],a[4:7],v[68:71]\n"
WT"lgkmcnt(3)\n"
RD"a[92:95],v226 offset:31744\n"
MF"v[32:35],a[64:67],a[0:3],v[32:35]\n"
"s_barrier\n"
MF"v[32:35],a[68:71],a[8:11],v[32:35]\n"
RD"a[96:99],v235 offset:0\n"
MF"v[72:75],a[68:71],a[12:15],v[72:75]\n"
MF"v[72:75],a[64:67],a[4:7],v[72:75]\n"
RD"a[100:103],v235 offset:1024\n"
MF"v[36:39],a[72:75],a[0:3],v[36:39]\n"
MF"v[36:39],a[76:79],a[8:11],v[36:39]\n"
RD"a[104:107],v235 offset:2048\n"
MF"v[76:79],a[76:79],a[12:15],v[76:79]\n"
MF"v[76:79],a[72:75],a[4:7],v[76:79]\n"
WT"lgkmcnt(3)\n"
RD"a[108:111],v235 offset:3072\n"
MF"v[0:3],a[80:83],a[16:19],v[0:3]\n"
MF"v[0:3],a[84:87],a[24:27],v[0:3]\n"
RD"a[112:115],v235 offset:4096\n"
MF"v[40:43],a[84:87],a[28:31],v[40:43]\n"
MF"v[40:43],a[80:83],a[20:23],v[40:43]\n"
RD"a[116:119],v235 offset:5120\n"
MF"v[4:7],a[88:91],a[16:19],v[4:7]\n"
MF"v[4:7],a[92:95],a[24:27],v[4:7]\n"
RD"a[120:123],v235 offset:6144\n"
MF"v[44:47],a[92:95],a[28:31],v[44:47]\n"
MF"v[44:47],a[88:91],a[20:23],v[44:47]\n"
WT"lgkmcnt(3)\n"
RD"a[124:127],v235 offset:7168\n"
MF"v[8:11],a[96:99],a[16:19],v[8:11]\n"
MF"v[8:11],a[100:103],a[24:27],v[8:11]\n"
RD"a[64:67],v235 offset:8192\n"
MF"v[48:51],a[100:103],a[28:31],v[48:51]\n"
MF"v[48:51],a[96:99],a[20:23],v[48:51]\n"
RD"a[68:71],v235 offset:9216\n"
MF"v[12:15],a[104:107],a[16:19],v[12:15]\n"
MF"v[12:15],a[108:111],a[24:27],v[12:15]\n"
RD"a[72:75],v235 offset:10240\n"
"v_bfe_u32 v238,v224,4,2\n"
MF"v[52:55],a[108:111],a[28:31],v[52:55]\n"
"s_andn2_b32 s63,s62,255\n"
"v_lshlrev_b32 v238,6,v238\n"
MF"v[52:55],a[104:107],a[20:23],v[52:55]\n"
WT"lgkmcnt(3)\n"
RD"a[76:79],v235 offset:11264\n"
MF"v[16:19],a[112:115],a[16:19],v[16:19]\n"
"buffer_load_dwordx2 v[240:241],v238,s[8:11],s63 offen sc1\n"
MF"v[16:19],a[116:119],a[24:27],v[16:19]\n"
RD"a[80:83],v235 offset:12288\n"
MF"v[56:59],a[116:119],a[28:31],v[56:59]\n"
MF"v[56:59],a[112:115],a[20:23],v[56:59]\n"
RD"a[84:87],v235 offset:13312\n"
MF"v[20:23],a[120:123],a[16:19],v[20:23]\n"
MF"v[20:23],a[124:127],a[24:27],v[20:23]\n"
RD"a[88:91],v235 offset:14336\n"
MF"v[60:63],a[124:127],a[28:31],v[60:63]\n"
MF"v[60:63],a[120:123],a[20:23],v[60:63]\n"
RD"a[92:95],v235 offset:15360\n"
"LTJ_%=:\n"
WT"lgkmcnt(7)\n"
MF"v[24:27],a[64:67],a[16:19],v[24:27]\n"
WT"lgkmcnt(3)\n"
MF"v[24:27],a[68:71],a[24:27],v[24:27]\n"
RD"a[96:99],v235 offset:16384\n"
MF"v[64:67],a[68:71],a[28:31],v[64:67]\n"
MF"v[64:67],a[64:67],a[20:23],v[64:67]\n"
RD"a[100:103],v235 offset:17408\n"
MF"v[28:31],a[72:75],a[16:19],v[28:31]\n"
MF"v[28:31],a[76:79],a[24:27],v[28:31]\n"
RD"a[104:107],v235 offset:18432\n"
MF"v[68:71],a[76:79],a[28:31],v[68:71]\n"
MF"v[68:71],a[72:75],a[20:23],v[68:71]\n"
RD"a[108:111],v235 offset:19456\n"
MF"v[32:35],a[80:83],a[16:19],v[32:35]\n"
WT"lgkmcnt(3)\n"
WT"vmcnt(1)\n"
"s_barrier\n"
MF"v[32:35],a[84:87],a[24:27],v[32:35]\n"
RD"a[112:115],v236 offset:4096\n"
MF"v[72:75],a[84:87],a[28:31],v[72:75]\n"
MF"v[72:75],a[80:83],a[20:23],v[72:75]\n"
RD"a[116:119],v236 offset:5120\n"
MF"v[36:39],a[88:91],a[16:19],v[36:39]\n"
MF"v[36:39],a[92:95],a[24:27],v[36:39]\n"
RD"a[120:123],v236 offset:6144\n"
MF"v[76:79],a[92:95],a[28:31],v[76:79]\n"
MF"v[76:79],a[88:91],a[20:23],v[76:79]\n"
RD"a[124:127],v236 offset:7168\n"
MF"v[0:3],a[96:99],a[180:183],v[0:3]\n"
WT"lgkmcnt(3)\n"
MF"v[0:3],a[100:103],a[188:191],v[0:3]\n"
RD"a[64:67],v236 offset:24576\n"
MF"v[40:43],a[100:103],a[192:195],v[40:43]\n"
MF"v[40:43],a[96:99],a[184:187],v[40:43]\n"
RD"a[68:71],v236 offset:25600\n"
MF"v[4:7],a[104:107],a[180:183],v[4:7]\n"
MF"v[4:7],a[108:111],a[188:191],v[4:7]\n"
RD"a[72:75],v236 offset:26624\n"
MF"v[44:47],a[108:111],a[192:195],v[44:47]\n"
MF"v[44:47],a[104:107],a[184:187],v[44:47]\n"
RD"a[76:79],v236 offset:27648\n"
MF"v[0:3],a[112:115],a[196:199],v[0:3]\n"
WT"lgkmcnt(3)\n"
MF"v[0:3],a[116:119],a[204:207],v[0:3]\n"
RD"a[80:83],v237 offset:12288\n"
MF"v[40:43],a[116:119],a[208:211],v[40:43]\n"
MF"v[40:43],a[112:115],a[200:203],v[40:43]\n"
RD"a[84:87],v237 offset:13312\n"
MF"v[4:7],a[120:123],a[196:199],v[4:7]\n"
MF"v[4:7],a[124:127],a[204:207],v[4:7]\n"
SA"s48,s60,32768\n"
RD"a[88:91],v237 offset:14336\n"
MF"v[44:47],a[124:127],a[208:211],v[44:47]\n"
"s_and_b32 s48,s48,0x18000\n"
MF"v[44:47],a[120:123],a[200:203],v[44:47]\n"
"s_andn2_b32 s49,s60,0x18000\n"
RD"a[92:95],v237 offset:15360\n"
MF"v[0:3],a[64:67],a[212:215],v[0:3]\n"
WT"lgkmcnt(3)\n"
WT"vmcnt(0)\n"
"v_cmp_eq_u64 vcc,s[28:29],v[240:241]\n"
"s_cmp_eq_u64 vcc,exec\n"
"s_cbranch_scc0 L1083_%=\n"
"L1084_%=:\n"
MF"v[0:3],a[68:71],a[220:223],v[0:3]\n"
"s_or_b32 s52,s48,s49\n"
RD"a[96:99],v235 offset:20480\n"
SA"s53,s52,16384\n"
MF"v[40:43],a[68:71],a[224:227],v[40:43]\n"
BL"a[244:247],v224,s[8:11],s52 offen offset:0 sc1\n"
MF"v[40:43],a[64:67],a[216:219],v[40:43]\n"
RD"a[100:103],v235 offset:21504\n"
MF"v[4:7],a[72:75],a[212:215],v[4:7]\n"
MF"v[4:7],a[76:79],a[220:223],v[4:7]\n"
RD"a[104:107],v235 offset:22528\n"
MF"v[44:47],a[76:79],a[224:227],v[44:47]\n"
"v_bfe_u32 v243,v224,6,2\n"
MF"v[44:47],a[72:75],a[216:219],v[44:47]\n"
RD"a[108:111],v235 offset:23552\n"
"v_bfe_u32 v244,v224,4,2\n"
MF"v[0:3],a[80:83],a[228:231],v[0:3]\n"
WT"lgkmcnt(3)\n"
BL"a[248:251],v224,s[8:11],s53 offen offset:0 sc1\n"
MF"v[0:3],a[84:87],a[236:239],v[0:3]\n"
RD"a[112:115],v236 offset:8192\n"
"v_lshrrev_b32 v245,8,v224\n"
MF"v[40:43],a[84:87],a[240:243],v[40:43]\n"
"v_lshlrev_b32 v245,3,v245\n"
"v_sub_u32 v244,v244,v245\n"
MF"v[40:43],a[80:83],a[232:235],v[40:43]\n"
RD"a[116:119],v236 offset:9216\n"
"v_and_b32 v245,16,v224\n"
MF"v[4:7],a[88:91],a[228:231],v[4:7]\n"
"v_mov_b32 v246,0x3f80\n"
MF"v[4:7],a[92:95],a[236:239],v[4:7]\n"
RD"a[120:123],v236 offset:10240\n"
"v_lshlrev_b32 v245,v245,v246\n"
MF"v[44:47],a[92:95],a[240:243],v[44:47]\n"
BL"a[252:255],v224,s[8:11],s52 offen offset:1024 sc1\n"
MF"v[44:47],a[88:91],a[232:235],v[44:47]\n"
RD"a[124:127],v236 offset:11264\n"
"v_lshlrev_b32 v246,4,v243\n"
MF"v[8:11],a[96:99],a[180:183],v[8:11]\n"
WT"lgkmcnt(3)\n"
"v_and_b32 v246,16,v246\n"
"v_and_b32 v247,2,v243\n"
MF"v[8:11],a[100:103],a[188:191],v[8:11]\n"
SA"s90,s88,0\n"
RD"a[64:67],v236 offset:28672\n"
PK"v0,v0,v1\n"
MF"v[48:51],a[100:103],a[192:195],v[48:51]\n"
BL"a[128:131],v224,s[8:11],s53 offen offset:1024 sc1\n"
MF"v[48:51],a[96:99],a[184:187],v[48:51]\n"
RD"a[68:71],v236 offset:29696\n"
PK"v1,v2,v3\n"
MF"v[12:15],a[104:107],a[180:183],v[12:15]\n"
"v_lshl_add_u32 v246,v247,1,v246\n"
PK"v2,v4,v5\n"
MF"v[12:15],a[108:111],a[188:191],v[12:15]\n"
RD"a[72:75],v236 offset:30720\n"
PK"v3,v6,v7\n"
"v_add3_u32 v246,v246,v244,0\n"
MF"v[52:55],a[108:111],a[192:195],v[52:55]\n"
BS"v[0:3],v224,s[8:11],s90 offen sc1\n"
"v_ashrrev_i32 v246,1,v246\n"
MF"v[52:55],a[104:107],a[184:187],v[52:55]\n"
RD"a[76:79],v236 offset:31744\n"
"v_cmp_eq_u32_e64 vcc,0,v246\n"
MF"v[8:11],a[112:115],a[196:199],v[8:11]\n"
WT"lgkmcnt(3)\n"
BL"a[132:135],v224,s[8:11],s52 offen offset:2048 sc1\n"
"v_cndmask_b32_e64 v80,0,v245,vcc\n"
MF"v[8:11],a[116:119],a[204:207],v[8:11]\n"
SA"s91,s22,0\n"
RD"a[80:83],v237 offset:16384\n"
"v_cmp_eq_u32_e64 s[92:93],1,v246\n"
MF"v[48:51],a[116:119],a[208:211],v[48:51]\n"
"v_cmp_eq_u32_e64 s[94:95],2,v246\n"
"v_cndmask_b32_e64 v81,0,v245,s[92:93]\n"
MF"v[48:51],a[112:115],a[200:203],v[48:51]\n"
RD"a[84:87],v237 offset:17408\n"
PK"v40,v40,v41\n"
MF"v[12:15],a[120:123],a[196:199],v[12:15]\n"
PK"v41,v42,v43\n"
"v_cndmask_b32_e64 v82,0,v245,s[94:95]\n"
MF"v[12:15],a[124:127],a[204:207],v[12:15]\n"
RD"a[88:91],v237 offset:18432\n"
PK"v42,v44,v45\n"
PK"v43,v46,v47\n"
MF"v[52:55],a[124:127],a[208:211],v[52:55]\n"
BS"v[40:43],v224,s[8:11],s91 offen sc1\n"
"v_cmp_eq_u32_e64 vcc,3,v246\n"
MF"v[52:55],a[120:123],a[200:203],v[52:55]\n"
BL"a[136:139],v224,s[8:11],s53 offen offset:2048 sc1\n"
"v_cndmask_b32_e64 v83,0,v245,vcc\n"
MF"v[8:11],a[64:67],a[212:215],v[8:11]\n"
WT"lgkmcnt(2)\n"
RD"a[92:95],v237 offset:19456\n"
"v_lshlrev_b32 v246,4,v243\n"
"v_and_b32 v246,16,v246\n"
MF"v[8:11],a[68:71],a[220:223],v[8:11]\n"
RD"a[96:99],v235 offset:24576\n"
"v_and_b32 v247,2,v243\n"
MF"v[48:51],a[68:71],a[224:227],v[48:51]\n"
"v_lshl_add_u32 v246,v247,1,v246\n"
"v_add3_u32 v246,v246,v244,8\n"
MF"v[48:51],a[64:67],a[216:219],v[48:51]\n"
RD"a[100:103],v235 offset:25600\n"
"v_ashrrev_i32 v246,1,v246\n"
MF"v[12:15],a[72:75],a[212:215],v[12:15]\n"
BL"a[140:143],v224,s[8:11],s52 offen offset:3072 sc1\n"
MF"v[12:15],a[76:79],a[220:223],v[12:15]\n"
RD"a[104:107],v235 offset:26624\n"
"v_cmp_eq_u32_e64 vcc,0,v246\n"
MF"v[52:55],a[76:79],a[224:227],v[52:55]\n"
"v_cmp_eq_u32_e64 s[92:93],1,v246\n"
"v_cndmask_b32_e64 v88,0,v245,vcc\n"
MF"v[52:55],a[72:75],a[216:219],v[52:55]\n"
RD"a[108:111],v235 offset:27648\n"
"v_cndmask_b32_e64 v89,0,v245,s[92:93]\n"
MF"v[8:11],a[80:83],a[228:231],v[8:11]\n"
WT"lgkmcnt(3)\n"
BL"a[144:147],v224,s[8:11],s53 offen offset:3072 sc1\n"
MF"v[8:11],a[84:87],a[236:239],v[8:11]\n"
SA"s48,s60,65536\n"
RD"a[112:115],v236 offset:12288\n"
"v_cmp_eq_u32_e64 s[94:95],2,v246\n"
MF"v[48:51],a[84:87],a[240:243],v[48:51]\n"
"s_and_b32 s48,s48,0x18000\n"
"v_cndmask_b32_e64 v90,0,v245,s[94:95]\n"
"v_cmp_eq_u32_e64 vcc,3,v246\n"
MF"v[48:51],a[80:83],a[232:235],v[48:51]\n"
"s_andn2_b32 s49,s60,0x18000\n"
RD"a[116:119],v236 offset:13312\n"
"v_cndmask_b32_e64 v91,0,v245,vcc\n"
MF"v[12:15],a[88:91],a[228:231],v[12:15]\n"
"s_or_b32 s52,s48,s49\n"
"v_lshl_add_u32 v246,v243,3,v244\n"
"v_add_u32 v246,0,v246\n"
MF"v[12:15],a[92:95],a[236:239],v[12:15]\n"
SA"s53,s52,16384\n"
RD"a[120:123],v236 offset:14336\n"
"v_ashrrev_i32 v246,1,v246\n"
MF"v[52:55],a[92:95],a[240:243],v[52:55]\n"
BL"a[148:151],v224,s[8:11],s52 offen offset:0 sc1\n"
MF"v[52:55],a[88:91],a[232:235],v[52:55]\n"
RD"a[124:127],v236 offset:15360\n"
"v_cmp_eq_u32_e64 vcc,0,v246\n"
MF"v[16:19],a[96:99],a[180:183],v[16:19]\n"
WT"lgkmcnt(3)\n"
"v_cndmask_b32_e64 v96,0,v245,vcc\n"
"v_cmp_eq_u32_e64 s[92:93],1,v246\n"
MF"v[16:19],a[100:103],a[188:191],v[16:19]\n"
SA"s90,s88,1024\n"
RD"a[64:67],v237 offset:0\n"
"v_cndmask_b32_e64 v97,0,v245,s[92:93]\n"
MF"v[56:59],a[100:103],a[192:195],v[56:59]\n"
PK"v8,v8,v9\n"
PK"v9,v10,v11\n"
MF"v[56:59],a[96:99],a[184:187],v[56:59]\n"
RD"a[68:71],v237 offset:1024\n"
"v_cmp_eq_u32_e64 s[94:95],2,v246\n"
MF"v[20:23],a[104:107],a[180:183],v[20:23]\n"
BL"a[152:155],v224,s[8:11],s53 offen offset:0 sc1\n"
MF"v[20:23],a[108:111],a[188:191],v[20:23]\n"
RD"a[72:75],v237 offset:2048\n"
PK"v10,v12,v13\n"
PK"v11,v14,v15\n"
MF"v[60:63],a[108:111],a[192:195],v[60:63]\n"
BS"v[8:11],v224,s[8:11],s90 offen sc1\n"
"v_cndmask_b32_e64 v98,0,v245,s[94:95]\n"
MF"v[60:63],a[104:107],a[184:187],v[60:63]\n"
RD"a[76:79],v237 offset:3072\n"
"v_cmp_eq_u32_e64 vcc,3,v246\n"
MF"v[16:19],a[112:115],a[196:199],v[16:19]\n"
WT"lgkmcnt(3)\n"
"v_cndmask_b32_e64 v99,0,v245,vcc\n"
"v_lshl_add_u32 v246,v243,3,v244\n"
MF"v[16:19],a[116:119],a[204:207],v[16:19]\n"
SA"s91,s22,1024\n"
RD"a[80:83],v237 offset:20480\n"
"v_add_u32 v246,4,v246\n"
MF"v[56:59],a[116:119],a[208:211],v[56:59]\n"
BL"a[156:159],v224,s[8:11],s52 offen offset:1024 sc1\n"
"v_ashrrev_i32 v246,1,v246\n"
MF"v[56:59],a[112:115],a[200:203],v[56:59]\n"
RD"a[84:87],v237 offset:21504\n"
"v_cmp_eq_u32_e64 vcc,0,v246\n"
MF"v[20:23],a[120:123],a[196:199],v[20:23]\n"
PK"v48,v48,v49\n"
PK"v49,v50,v51\n"
"v_cndmask_b32_e64 v104,0,v245,vcc\n"
MF"v[20:23],a[124:127],a[204:207],v[20:23]\n"
RD"a[88:91],v237 offset:22528\n"
PK"v50,v52,v53\n"
PK"v51,v54,v55\n"
MF"v[60:63],a[124:127],a[208:211],v[60:63]\n"
BS"v[48:51],v224,s[8:11],s91 offen sc1\n"
"v_cmp_eq_u32_e64 s[92:93],1,v246\n"
MF"v[60:63],a[120:123],a[200:203],v[60:63]\n"
BL"a[160:163],v224,s[8:11],s53 offen offset:1024 sc1\n"
"v_cndmask_b32_e64 v105,0,v245,s[92:93]\n"
MF"v[16:19],a[64:67],a[212:215],v[16:19]\n"
WT"lgkmcnt(2)\n"
RD"a[92:95],v237 offset:23552\n"
"v_cmp_eq_u32_e64 s[94:95],2,v246\n"
MF"v[16:19],a[68:71],a[220:223],v[16:19]\n"
"s_mov_b32 s12,s64\n"
RD"a[96:99],v235 offset:28672\n"
"v_cndmask_b32_e64 v106,0,v245,s[94:95]\n"
"v_cmp_eq_u32_e64 vcc,3,v246\n"
MF"v[56:59],a[68:71],a[224:227],v[56:59]\n"
"s_and_b32 s13,s65,0xffff\n"
"v_cndmask_b32_e64 v107,0,v245,vcc\n"
"s_mov_b32 s14,2621440\n"
MF"v[56:59],a[64:67],a[216:219],v[56:59]\n"
"s_mov_b32 s15,0x20000\n"
RD"a[100:103],v235 offset:29696\n"
MF"v[20:23],a[72:75],a[212:215],v[20:23]\n"
"s_mov_b32 s16,s86\n"
BL"a[164:167],v224,s[8:11],s52 offen offset:2048 sc1\n"
MF"v[20:23],a[76:79],a[220:223],v[20:23]\n"
"s_and_b32 s17,s87,0xffff\n"
RD"a[104:107],v235 offset:30720\n"
MF"v[60:63],a[76:79],a[224:227],v[60:63]\n"
"s_mov_b32 s18,2621440\n"
MF"v[60:63],a[72:75],a[216:219],v[60:63]\n"
"s_mov_b32 s19,0x20000\n"
RD"a[108:111],v235 offset:31744\n"
MF"v[16:19],a[80:83],a[228:231],v[16:19]\n"
WT"lgkmcnt(3)\n"
"v_bfe_u32 v243,v224,4,4\n"
MF"v[16:19],a[84:87],a[236:239],v[16:19]\n"
"s_lshl_b32 s92,s34,3\n"
RD"a[112:115],v236 offset:16384\n"
MF"v[56:59],a[84:87],a[240:243],v[56:59]\n"
SA"s93,s92,s32\n"
BL"a[168:171],v224,s[8:11],s53 offen offset:2048 sc1\n"
MF"v[56:59],a[80:83],a[232:235],v[56:59]\n"
"s_sub_u32 s92,s32,s92\n"
RD"a[116:119],v236 offset:17408\n"
MF"v[20:23],a[88:91],a[228:231],v[20:23]\n"
SA"s92,s92,120\n"
MF"v[20:23],a[92:95],a[236:239],v[20:23]\n"
RD"a[120:123],v236 offset:18432\n"
"v_lshl_add_u32 v244,s93,4,v243\n"
MF"v[60:63],a[92:95],a[240:243],v[60:63]\n"
"v_lshl_add_u32 v245,s92,4,v243\n"
MF"v[60:63],a[88:91],a[232:235],v[60:63]\n"
"s_mul_i32 s93,s93,20480\n"
RD"a[124:127],v236 offset:19456\n"
MF"v[24:27],a[96:99],a[180:183],v[24:27]\n"
WT"lgkmcnt(3)\n"
BL"a[172:175],v224,s[8:11],s52 offen offset:3072 sc1\n"
MF"v[24:27],a[100:103],a[188:191],v[24:27]\n"
"s_mul_i32 s92,s92,20480\n"
RD"a[64:67],v237 offset:4096\n"
PK"v16,v16,v17\n"
MF"v[64:67],a[100:103],a[192:195],v[64:67]\n"
SA"s90,s88,2048\n"
PK"v17,v18,v19\n"
PK"v18,v20,v21\n"
MF"v[64:67],a[96:99],a[184:187],v[64:67]\n"
SA"s93,s93,452608\n"
RD"a[68:71],v237 offset:5120\n"
PK"v19,v22,v23\n"
MF"v[28:31],a[104:107],a[180:183],v[28:31]\n"
SA"s92,s92,452608\n"
BL"a[176:179],v224,s[8:11],s53 offen offset:3072 sc1\n"
MF"v[28:31],a[108:111],a[188:191],v[28:31]\n"
SA"s48,s60,98304\n"
BS"v[16:19],v224,s[8:11],s90 offen sc1\n"
MF"v[68:71],a[108:111],a[192:195],v[68:71]\n"
"s_and_b32 s48,s48,0x18000\n"
RD"a[72:75],v237 offset:6144\n"
"v_add_u32 v178,s93,v224\n"
MF"v[68:71],a[104:107],a[184:187],v[68:71]\n"
"s_andn2_b32 s49,s60,0x18000\n"
RD"a[76:79],v237 offset:7168\n"
"v_add_u32 v179,s92,v224\n"
MF"v[24:27],a[112:115],a[196:199],v[24:27]\n"
WT"lgkmcnt(3)\n"
"v_lshrrev_b32 v243,4,v224\n"
"v_and_b32 v243,48,v243\n"
MF"v[24:27],a[116:119],a[204:207],v[24:27]\n"
"s_or_b32 s52,s48,s49\n"
RD"a[80:83],v237 offset:24576\n"
"v_mul_u32_u24 v176,1280,v244\n"
MF"v[64:67],a[116:119],a[208:211],v[64:67]\n"
SA"s53,s52,16384\n"
BL"a[32:35],v224,s[8:11],s52 offen offset:0 sc1\n"
MF"v[64:67],a[112:115],a[200:203],v[64:67]\n"
SA"s91,s22,2048\n"
RD"a[84:87],v237 offset:25600\n"
"v_mul_u32_u24 v177,1280,v245\n"
MF"v[28:31],a[120:123],a[196:199],v[28:31]\n"
PK"v56,v56,v57\n"
PK"v57,v58,v59\n"
"v_add_u32 v176,v176,v243\n"
MF"v[28:31],a[124:127],a[204:207],v[28:31]\n"
RD"a[88:91],v237 offset:26624\n"
PK"v58,v60,v61\n"
PK"v59,v62,v63\n"
MF"v[68:71],a[124:127],a[208:211],v[68:71]\n"
BS"v[56:59],v224,s[8:11],s91 offen sc1\n"
"v_add_u32 v177,v177,v243\n"
MF"v[68:71],a[120:123],a[200:203],v[68:71]\n"
RD"a[92:95],v237 offset:27648\n"
"v_and_b32 v243,0x300,v224\n"
"v_sub_u32 v244,2047,v244\n"
MF"v[24:27],a[64:67],a[212:215],v[24:27]\n"
WT"lgkmcnt(3)\n"
BL"a[36:39],v224,s[8:11],s53 offen offset:0 sc1\n"
"v_sub_u32 v245,2047,v245\n"
MF"v[24:27],a[68:71],a[220:223],v[24:27]\n"
RD"a[96:99],v236 offset:0\n"
"v_readlane_b32 s92,v234,0\n"
MF"v[64:67],a[68:71],a[224:227],v[64:67]\n"
"s_and_b32 s92,s92,0xffff\n"
"v_cmp_eq_u32 vcc,s92,v244\n"
"v_cmp_eq_u32_e64 s[94:95],s92,v245\n"
MF"v[64:67],a[64:67],a[216:219],v[64:67]\n"
RD"a[100:103],v236 offset:1024\n"
"v_cndmask_b32 v178,v178,v243,vcc\n"
MF"v[28:31],a[72:75],a[212:215],v[28:31]\n"
"v_cndmask_b32_e64 v179,v179,v243,s[94:95]\n"
"v_add_u32 v243,16,v243\n"
MF"v[28:31],a[76:79],a[220:223],v[28:31]\n"
RD"a[104:107],v236 offset:2048\n"
"v_readlane_b32 s92,v234,1\n"
MF"v[68:71],a[76:79],a[224:227],v[68:71]\n"
"s_and_b32 s92,s92,0xffff\n"
BL"a[40:43],v224,s[8:11],s52 offen offset:1024 sc1\n"
MF"v[68:71],a[72:75],a[216:219],v[68:71]\n"
RD"a[108:111],v236 offset:3072\n"
"v_cmp_eq_u32 vcc,s92,v244\n"
MF"v[24:27],a[80:83],a[228:231],v[24:27]\n"
WT"lgkmcnt(3)\n"
"v_cmp_eq_u32_e64 s[94:95],s92,v245\n"
MF"v[24:27],a[84:87],a[236:239],v[24:27]\n"
RD"a[112:115],v236 offset:20480\n"
"v_cndmask_b32 v178,v178,v243,vcc\n"
MF"v[64:67],a[84:87],a[240:243],v[64:67]\n"
BL"a[44:47],v224,s[8:11],s53 offen offset:1024 sc1\n"
MF"v[64:67],a[80:83],a[232:235],v[64:67]\n"
RD"a[116:119],v236 offset:21504\n"
"v_cndmask_b32_e64 v179,v179,v243,s[94:95]\n"
MF"v[28:31],a[88:91],a[228:231],v[28:31]\n"
"v_add_u32 v243,16,v243\n"
"v_readlane_b32 s92,v234,2\n"
MF"v[28:31],a[92:95],a[236:239],v[28:31]\n"
"s_and_b32 s92,s92,0xffff\n"
RD"a[120:123],v236 offset:22528\n"
MF"v[68:71],a[92:95],a[240:243],v[68:71]\n"
"v_cmp_eq_u32 vcc,s92,v244\n"
MF"v[68:71],a[88:91],a[232:235],v[68:71]\n"
RD"a[124:127],v236 offset:23552\n"
"v_cmp_eq_u32_e64 s[94:95],s92,v245\n"
MF"v[32:35],a[96:99],a[180:183],v[32:35]\n"
WT"lgkmcnt(3)\n"
BL"a[48:51],v224,s[8:11],s52 offen offset:2048 sc1\n"
MF"v[32:35],a[100:103],a[188:191],v[32:35]\n"
SA"s90,s88,3072\n"
RD"a[64:67],v237 offset:8192\n"
"v_cndmask_b32 v178,v178,v243,vcc\n"
MF"v[72:75],a[100:103],a[192:195],v[72:75]\n"
PK"v24,v24,v25\n"
PK"v25,v26,v27\n"
MF"v[72:75],a[96:99],a[184:187],v[72:75]\n"
RD"a[68:71],v237 offset:9216\n"
"v_cndmask_b32_e64 v179,v179,v243,s[94:95]\n"
MF"v[36:39],a[104:107],a[180:183],v[36:39]\n"
PK"v26,v28,v29\n"
PK"v27,v30,v31\n"
MF"v[36:39],a[108:111],a[188:191],v[36:39]\n"
BS"v[24:27],v224,s[8:11],s90 offen sc1\n"
"v_add_u32 v243,16,v243\n"
MF"v[76:79],a[108:111],a[192:195],v[76:79]\n"
RD"a[72:75],v237 offset:10240\n"
"v_readlane_b32 s92,v234,3\n"
MF"v[76:79],a[104:107],a[184:187],v[76:79]\n"
"s_and_b32 s92,s92,0xffff\n"
BL"a[52:55],v224,s[8:11],s53 offen offset:2048 sc1\n"
MF"v[32:35],a[112:115],a[196:199],v[32:35]\n"
WT"lgkmcnt(2)\n"
RD"a[76:79],v237 offset:11264\n"
"v_cmp_eq_u32 vcc,s92,v244\n"
MF"v[32:35],a[116:119],a[204:207],v[32:35]\n"
SA"s91,s22,3072\n"
RD"a[80:83],v237 offset:28672\n"
"v_cmp_eq_u32_e64 s[94:95],s92,v245\n"
MF"v[72:75],a[116:119],a[208:211],v[72:75]\n"
"v_cndmask_b32 v178,v178,v243,vcc\n"
"v_cndmask_b32_e64 v179,v179,v243,s[94:95]\n"
MF"v[72:75],a[112:115],a[200:203],v[72:75]\n"
RD"a[84:87],v237 offset:29696\n"
PK"v64,v64,v65\n"
MF"v[36:39],a[120:123],a[196:199],v[36:39]\n"
BL"a[56:59],v224,s[8:11],s52 offen offset:3072 sc1\n"
PK"v65,v66,v67\n"
MF"v[36:39],a[124:127],a[204:207],v[36:39]\n"
RD"a[88:91],v237 offset:30720\n"
"v_add_u32 v243,16,v243\n"
PK"v66,v68,v69\n"
MF"v[76:79],a[124:127],a[208:211],v[76:79]\n"
PK"v67,v70,v71\n"
"v_readlane_b32 s92,v234,4\n"
MF"v[76:79],a[120:123],a[200:203],v[76:79]\n"
"s_and_b32 s92,s92,0xffff\n"
BS"v[64:67],v224,s[8:11],s91 offen sc1\n"
"v_cmp_eq_u32 vcc,s92,v244\n"
MF"v[32:35],a[64:67],a[212:215],v[32:35]\n"
WT"lgkmcnt(2)\n"
RD"a[92:95],v237 offset:31744\n"
"v_cmp_eq_u32_e64 s[94:95],s92,v245\n"
MF"v[32:35],a[68:71],a[220:223],v[32:35]\n"
BL"a[60:63],v224,s[8:11],s53 offen offset:3072 sc1\n"
"v_cndmask_b32 v178,v178,v243,vcc\n"
MF"v[72:75],a[68:71],a[224:227],v[72:75]\n"
RD"a[96:99],v226 offset:8192\n"
"v_cndmask_b32_e64 v179,v179,v243,s[94:95]\n"
MF"v[72:75],a[64:67],a[216:219],v[72:75]\n"
RD"a[100:103],v226 offset:9216\n"
"v_add_u32 v243,16,v243\n"
MF"v[36:39],a[72:75],a[212:215],v[36:39]\n"
"v_readlane_b32 s92,v234,5\n"
"s_and_b32 s92,s92,0xffff\n"
MF"v[36:39],a[76:79],a[220:223],v[36:39]\n"
RD"a[104:107],v226 offset:10240\n"
"v_cmp_eq_u32 vcc,s92,v244\n"
MF"v[76:79],a[76:79],a[224:227],v[76:79]\n"
"v_cmp_eq_u32_e64 s[94:95],s92,v245\n"
"v_cndmask_b32 v178,v178,v243,vcc\n"
MF"v[76:79],a[72:75],a[216:219],v[76:79]\n"
RD"a[108:111],v226 offset:11264\n"
"v_cndmask_b32_e64 v179,v179,v243,s[94:95]\n"
MF"v[32:35],a[80:83],a[228:231],v[32:35]\n"
WT"lgkmcnt(4)\n"
"v_add_u32 v243,16,v243\n"
"v_readlane_b32 s92,v234,6\n"
MF"v[32:35],a[84:87],a[236:239],v[32:35]\n"
"s_and_b32 s92,s92,0xffff\n"
RD"a[112:115],v226 offset:28672\n"
MF"v[72:75],a[84:87],a[240:243],v[72:75]\n"
"v_cmp_eq_u32 vcc,s92,v244\n"
MF"v[72:75],a[80:83],a[232:235],v[72:75]\n"
RD"a[116:119],v226 offset:29696\n"
"v_cmp_eq_u32_e64 s[94:95],s92,v245\n"
MF"v[36:39],a[88:91],a[228:231],v[36:39]\n"
"v_cndmask_b32 v178,v178,v243,vcc\n"
MF"v[36:39],a[92:95],a[236:239],v[36:39]\n"
RD"a[120:123],v226 offset:30720\n"
"v_cndmask_b32_e64 v179,v179,v243,s[94:95]\n"
MF"v[76:79],a[92:95],a[240:243],v[76:79]\n"
"v_add_u32 v243,16,v243\n"
MF"v[76:79],a[88:91],a[232:235],v[76:79]\n"
WT"vmcnt(31) lgkmcnt(3)\n"
RD"a[124:127],v226 offset:31744\n"
"v_readlane_b32 s92,v234,7\n"
MF"v[84:87],a[96:99],a[244:247],v[84:87]\n"
WT"vmcnt(29)\n"
"s_and_b32 s92,s92,0xffff\n"
MF"v[84:87],a[100:103],a[252:255],v[84:87]\n"
WT"vmcnt(28)\n"
RD"a[64:67],v235 offset:16384\n"
PK"v32,v32,v33\n"
MF"v[164:167],a[100:103],a[128:131],v[164:167]\n"
SA"s90,s88,4096\n"
PK"v33,v34,v35\n"
"v_cmp_eq_u32 vcc,s92,v244\n"
MF"v[164:167],a[96:99],a[248:251],v[164:167]\n"
RD"a[68:71],v235 offset:17408\n"
PK"v34,v36,v37\n"
MF"v[92:95],a[104:107],a[244:247],v[92:95]\n"
PK"v35,v38,v39\n"
"v_cmp_eq_u32_e64 s[94:95],s92,v245\n"
MF"v[92:95],a[108:111],a[252:255],v[92:95]\n"
"s_mul_i32 s92,s33,5\n"
BS"v[32:35],v224,s[8:11],s90 offen sc1\n"
MF"v[172:175],a[108:111],a[128:131],v[172:175]\n"
SA"s93,s92,s30\n"
RD"a[72:75],v235 offset:18432\n"
"v_cndmask_b32 v178,v178,v243,vcc\n"
MF"v[172:175],a[104:107],a[248:251],v[172:175]\n"
WT"vmcnt(27) lgkmcnt(3)\n"
RD"a[76:79],v235 offset:19456\n"
"v_cndmask_b32_e64 v179,v179,v243,s[94:95]\n"
MF"v[84:87],a[112:115],a[132:135],v[84:87]\n"
WT"vmcnt(24)\n"
SA"s92,s92,4\n"
"s_lshl_b32 s38,s93,6\n"
MF"v[84:87],a[116:119],a[140:143],v[84:87]\n"
WT"vmcnt(23)\n"
RD"a[80:83],v236 offset:4096\n"
PK"v72,v72,v73\n"
MF"v[164:167],a[116:119],a[144:147],v[164:167]\n"
SA"s91,s22,4096\n"
PK"v73,v74,v75\n"
PK"v74,v76,v77\n"
MF"v[164:167],a[112:115],a[136:139],v[164:167]\n"
"s_lshl_b32 s55,s92,6\n"
RD"a[84:87],v236 offset:5120\n"
PK"v75,v78,v79\n"
MF"v[92:95],a[120:123],a[132:135],v[92:95]\n"
SA"s55,s55,s45\n"
BS"v[72:75],v224,s[8:11],s91 offen sc1\n"
MF"v[92:95],a[124:127],a[140:143],v[92:95]\n"
"s_lshl_b32 s36,s93,10\n"
RD"a[88:91],v236 offset:6144\n"
"s_lshl_b32 s37,s92,10\n"
MF"v[172:175],a[124:127],a[144:147],v[172:175]\n"
SA"s36,s36,71680\n"
SA"s37,s37,71680\n"
MF"v[172:175],a[120:123],a[136:139],v[172:175]\n"
WT"vmcnt(23) lgkmcnt(3)\n"
RD"a[92:95],v236 offset:7168\n"
SA"s37,s37,s45\n"
MF"v[84:87],a[64:67],a[148:151],v[84:87]\n"
WT"vmcnt(20)\n"
"v_lshrrev_b32 v243,1,v224\n"
MF"v[84:87],a[68:71],a[156:159],v[84:87]\n"
WT"vmcnt(18)\n"
RD"a[96:99],v236 offset:24576\n"
"v_min_u32 v244,312,v243\n"
MF"v[164:167],a[68:71],a[160:163],v[164:167]\n"
"v_and_b32 v243,56,v243\n"
MF"v[164:167],a[64:67],a[152:155],v[164:167]\n"
RD"a[100:103],v236 offset:25600\n"
"v_lshlrev_b32 v243,3,v243\n"
MF"v[92:95],a[72:75],a[148:151],v[92:95]\n"
"s_mov_b32 s92,6400\n"
MF"v[92:95],a[76:79],a[156:159],v[92:95]\n"
RD"a[104:107],v236 offset:26624\n"
MF"v[172:175],a[76:79],a[160:163],v[172:175]\n"
MF"v[172:175],a[72:75],a[152:155],v[172:175]\n"
WT"vmcnt(17) lgkmcnt(3)\n"
RD"a[108:111],v236 offset:27648\n"
MF"v[84:87],a[80:83],a[164:167],v[84:87]\n"
WT"vmcnt(15)\n"
MF"v[84:87],a[84:87],a[172:175],v[84:87]\n"
WT"vmcnt(14)\n"
RD"a[112:115],v237 offset:12288\n"
MF"v[164:167],a[84:87],a[176:179],v[164:167]\n"
MF"v[164:167],a[80:83],a[168:171],v[164:167]\n"
RD"a[116:119],v237 offset:13312\n"
MF"v[92:95],a[88:91],a[164:167],v[92:95]\n"
MF"v[92:95],a[92:95],a[172:175],v[92:95]\n"
RD"a[120:123],v237 offset:14336\n"
MF"v[172:175],a[92:95],a[176:179],v[172:175]\n"
MF"v[172:175],a[88:91],a[168:171],v[172:175]\n"
WT"vmcnt(12) lgkmcnt(3)\n"
RD"a[124:127],v237 offset:15360\n"
MF"v[84:87],a[96:99],a[32:35],v[84:87]\n"
WT"vmcnt(9)\n"
MF"v[84:87],a[100:103],a[40:43],v[84:87]\n"
WT"vmcnt(8)\n"
RD"a[64:67],v226 offset:12288\n"
MF"v[164:167],a[100:103],a[44:47],v[164:167]\n"
MF"v[164:167],a[96:99],a[36:39],v[164:167]\n"
RD"a[68:71],v226 offset:13312\n"
MF"v[92:95],a[104:107],a[32:35],v[92:95]\n"
MF"v[92:95],a[108:111],a[40:43],v[92:95]\n"
RD"a[72:75],v226 offset:14336\n"
MF"v[172:175],a[108:111],a[44:47],v[172:175]\n"
MF"v[172:175],a[104:107],a[36:39],v[172:175]\n"
WT"vmcnt(7) lgkmcnt(3)\n"
RD"a[76:79],v226 offset:15360\n"
MF"v[84:87],a[112:115],a[48:51],v[84:87]\n"
WT"vmcnt(4)\n"
"buffer_load_dwordx2 v[240:241],v244,s[8:11],s92 offen sc1\n"
MF"v[84:87],a[116:119],a[56:59],v[84:87]\n"
WT"vmcnt(3)\n"
RD"a[80:83],v235 offset:0\n"
MF"v[164:167],a[116:119],a[60:63],v[164:167]\n"
MF"v[164:167],a[112:115],a[52:55],v[164:167]\n"
RD"a[84:87],v235 offset:1024\n"
MF"v[92:95],a[120:123],a[48:51],v[92:95]\n"
MF"v[92:95],a[124:127],a[56:59],v[92:95]\n"
RD"a[88:91],v235 offset:2048\n"
MF"v[172:175],a[124:127],a[60:63],v[172:175]\n"
MF"v[172:175],a[120:123],a[52:55],v[172:175]\n"
WT"lgkmcnt(3)\n"
RD"a[92:95],v235 offset:3072\n"
MF"v[100:103],a[64:67],a[244:247],v[100:103]\n"
MF"v[100:103],a[68:71],a[252:255],v[100:103]\n"
SA"s90,s88,327680\n"
RD"a[96:99],v235 offset:20480\n"
PK"v84,v84,v85\n"
MF"v[180:183],a[68:71],a[128:131],v[180:183]\n"
PK"v85,v86,v87\n"
PK"v86,v92,v93\n"
MF"v[180:183],a[64:67],a[248:251],v[180:183]\n"
RD"a[100:103],v235 offset:21504\n"
PK"v87,v94,v95\n"
MF"v[108:111],a[72:75],a[244:247],v[108:111]\n"
BS"v[84:87],v224,s[8:11],s90 offen sc1\n"
MF"v[108:111],a[76:79],a[252:255],v[108:111]\n"
RD"a[104:107],v235 offset:22528\n"
MF"v[188:191],a[76:79],a[128:131],v[188:191]\n"
MF"v[188:191],a[72:75],a[248:251],v[188:191]\n"
WT"lgkmcnt(3)\n"
RD"a[108:111],v235 offset:23552\n"
MF"v[100:103],a[80:83],a[132:135],v[100:103]\n"
MF"v[100:103],a[84:87],a[140:143],v[100:103]\n"
SA"s91,s22,327680\n"
RD"a[112:115],v236 offset:8192\n"
PK"v164,v164,v165\n"
MF"v[180:183],a[84:87],a[144:147],v[180:183]\n"
PK"v165,v166,v167\n"
PK"v166,v172,v173\n"
MF"v[180:183],a[80:83],a[136:139],v[180:183]\n"
RD"a[116:119],v236 offset:9216\n"
PK"v167,v174,v175\n"
MF"v[108:111],a[88:91],a[132:135],v[108:111]\n"
BS"v[164:167],v224,s[8:11],s91 offen sc1\n"
MF"v[108:111],a[92:95],a[140:143],v[108:111]\n"
RD"a[120:123],v236 offset:10240\n"
MF"v[188:191],a[92:95],a[144:147],v[188:191]\n"
MF"v[188:191],a[88:91],a[136:139],v[188:191]\n"
WT"lgkmcnt(3)\n"
RD"a[124:127],v236 offset:11264\n"
MF"v[100:103],a[96:99],a[148:151],v[100:103]\n"
MF"v[100:103],a[100:103],a[156:159],v[100:103]\n"
RD"a[64:67],v236 offset:28672\n"
MF"v[180:183],a[100:103],a[160:163],v[180:183]\n"
WT"vmcnt(3)\n"
"buffer_store_dwordx2 v[232:233],v239,s[8:11],s89 offen sc1\n"
MF"v[180:183],a[96:99],a[152:155],v[180:183]\n"
RD"a[68:71],v236 offset:29696\n"
MF"v[108:111],a[104:107],a[148:151],v[108:111]\n"
MF"v[108:111],a[108:111],a[156:159],v[108:111]\n"
RD"a[72:75],v236 offset:30720\n"
MF"v[188:191],a[108:111],a[160:163],v[188:191]\n"
MF"v[188:191],a[104:107],a[152:155],v[188:191]\n"
WT"lgkmcnt(3)\n"
RD"a[76:79],v236 offset:31744\n"
MF"v[100:103],a[112:115],a[164:167],v[100:103]\n"
MF"v[100:103],a[116:119],a[172:175],v[100:103]\n"
RD"a[80:83],v237 offset:16384\n"
MF"v[180:183],a[116:119],a[176:179],v[180:183]\n"
MF"v[180:183],a[112:115],a[168:171],v[180:183]\n"
RD"a[84:87],v237 offset:17408\n"
MF"v[108:111],a[120:123],a[164:167],v[108:111]\n"
MF"v[108:111],a[124:127],a[172:175],v[108:111]\n"
RD"a[88:91],v237 offset:18432\n"
MF"v[188:191],a[124:127],a[176:179],v[188:191]\n"
MF"v[188:191],a[120:123],a[168:171],v[188:191]\n"
WT"lgkmcnt(3)\n"
RD"a[92:95],v237 offset:19456\n"
MF"v[100:103],a[64:67],a[32:35],v[100:103]\n"
MF"v[100:103],a[68:71],a[40:43],v[100:103]\n"
RD"a[96:99],v226 offset:16384\n"
MF"v[180:183],a[68:71],a[44:47],v[180:183]\n"
MF"v[180:183],a[64:67],a[36:39],v[180:183]\n"
RD"a[100:103],v226 offset:17408\n"
MF"v[108:111],a[72:75],a[32:35],v[108:111]\n"
MF"v[108:111],a[76:79],a[40:43],v[108:111]\n"
RD"a[104:107],v226 offset:18432\n"
MF"v[188:191],a[76:79],a[44:47],v[188:191]\n"
MF"v[188:191],a[72:75],a[36:39],v[188:191]\n"
WT"lgkmcnt(3)\n"
RD"a[108:111],v226 offset:19456\n"
MF"v[100:103],a[80:83],a[48:51],v[100:103]\n"
MF"v[100:103],a[84:87],a[56:59],v[100:103]\n"
RD"a[112:115],v235 offset:4096\n"
MF"v[180:183],a[84:87],a[60:63],v[180:183]\n"
MF"v[180:183],a[80:83],a[52:55],v[180:183]\n"
RD"a[116:119],v235 offset:5120\n"
MF"v[108:111],a[88:91],a[48:51],v[108:111]\n"
MF"v[108:111],a[92:95],a[56:59],v[108:111]\n"
RD"a[120:123],v235 offset:6144\n"
MF"v[188:191],a[92:95],a[60:63],v[188:191]\n"
MF"v[188:191],a[88:91],a[52:55],v[188:191]\n"
WT"lgkmcnt(3)\n"
RD"a[124:127],v235 offset:7168\n"
MF"v[116:119],a[96:99],a[244:247],v[116:119]\n"
MF"v[116:119],a[100:103],a[252:255],v[116:119]\n"
SA"s90,s88,328704\n"
RD"a[64:67],v235 offset:24576\n"
PK"v100,v100,v101\n"
MF"v[196:199],a[100:103],a[128:131],v[196:199]\n"
PK"v101,v102,v103\n"
PK"v102,v108,v109\n"
MF"v[196:199],a[96:99],a[248:251],v[196:199]\n"
RD"a[68:71],v235 offset:25600\n"
PK"v103,v110,v111\n"
MF"v[124:127],a[104:107],a[244:247],v[124:127]\n"
BS"v[100:103],v224,s[8:11],s90 offen sc1\n"
MF"v[124:127],a[108:111],a[252:255],v[124:127]\n"
WT"vmcnt(4)\n"
"v_cmp_eq_u64 vcc,s[28:29],v[240:241]\n"
"s_cmp_eq_u64 vcc,exec\n"
"s_cbranch_scc0 L1086_%=\n"
"L1087_%=:\n"
MF"v[204:207],a[108:111],a[128:131],v[204:207]\n"
RD"a[72:75],v235 offset:26624\n"
MF"v[204:207],a[104:107],a[248:251],v[204:207]\n"
WT"lgkmcnt(3)\n"
RD"a[76:79],v235 offset:27648\n"
MF"v[116:119],a[112:115],a[132:135],v[116:119]\n"
BL"v[112:115],v176,s[12:15],s38 offen\n"
MF"v[116:119],a[116:119],a[140:143],v[116:119]\n"
SA"s91,s22,328704\n"
RD"a[80:83],v236 offset:12288\n"
PK"v180,v180,v181\n"
MF"v[196:199],a[116:119],a[144:147],v[196:199]\n"
BL"v[144:147],v178,s[8:11],s36 offen sc1\n"
MF"v[196:199],a[112:115],a[136:139],v[196:199]\n"
RD"a[84:87],v236 offset:13312\n"
PK"v181,v182,v183\n"
MF"v[124:127],a[120:123],a[132:135],v[124:127]\n"
PK"v182,v188,v189\n"
PK"v183,v190,v191\n"
MF"v[124:127],a[124:127],a[140:143],v[124:127]\n"
BS"v[180:183],v224,s[8:11],s91 offen sc1\n"
MF"v[204:207],a[124:127],a[144:147],v[204:207]\n"
RD"a[88:91],v236 offset:14336\n"
MF"v[204:207],a[120:123],a[136:139],v[204:207]\n"
WT"lgkmcnt(3)\n"
BL"v[120:123],v176,s[12:15],s55 offen\n"
MF"v[116:119],a[64:67],a[148:151],v[116:119]\n"
RD"a[92:95],v236 offset:15360\n"
MF"v[116:119],a[68:71],a[156:159],v[116:119]\n"
BL"v[152:155],v178,s[8:11],s37 offen sc1\n"
MF"v[196:199],a[68:71],a[160:163],v[196:199]\n"
RD"a[96:99],v237 offset:0\n"
MF"v[196:199],a[64:67],a[152:155],v[196:199]\n"
RD"a[100:103],v237 offset:1024\n"
MF"v[124:127],a[72:75],a[148:151],v[124:127]\n"
BL"v[128:131],v177,s[12:15],s38 offen\n"
MF"v[124:127],a[76:79],a[156:159],v[124:127]\n"
RD"a[104:107],v237 offset:2048\n"
MF"v[204:207],a[76:79],a[160:163],v[204:207]\n"
BL"v[160:163],v179,s[8:11],s36 offen sc1\n"
MF"v[204:207],a[72:75],a[152:155],v[204:207]\n"
WT"lgkmcnt(3)\n"
RD"a[108:111],v237 offset:3072\n"
MF"v[116:119],a[80:83],a[164:167],v[116:119]\n"
MF"v[116:119],a[84:87],a[172:175],v[116:119]\n"
RD"a[112:115],v237 offset:20480\n"
MF"v[196:199],a[84:87],a[176:179],v[196:199]\n"
BL"v[136:139],v177,s[12:15],s55 offen\n"
MF"v[196:199],a[80:83],a[168:171],v[196:199]\n"
RD"a[116:119],v237 offset:21504\n"
MF"v[124:127],a[88:91],a[164:167],v[124:127]\n"
BL"v[168:171],v179,s[8:11],s37 offen sc1\n"
MF"v[124:127],a[92:95],a[172:175],v[124:127]\n"
RD"a[120:123],v237 offset:22528\n"
MF"v[204:207],a[92:95],a[176:179],v[204:207]\n"
MF"v[204:207],a[88:91],a[168:171],v[204:207]\n"
WT"lgkmcnt(3)\n"
RD"a[124:127],v237 offset:23552\n"
MF"v[116:119],a[96:99],a[32:35],v[116:119]\n"
MF"v[116:119],a[100:103],a[40:43],v[116:119]\n"
RD"a[64:67],v226 offset:20480\n"
MF"v[196:199],a[100:103],a[44:47],v[196:199]\n"
"s_lshr_b32 s93,s31,3\n"
MF"v[196:199],a[96:99],a[36:39],v[196:199]\n"
"s_lshl_b32 s93,s93,4\n"
RD"a[68:71],v226 offset:21504\n"
MF"v[124:127],a[104:107],a[32:35],v[124:127]\n"
SA"s93,s93,s32\n"
MF"v[124:127],a[108:111],a[40:43],v[124:127]\n"
"s_mul_i32 s94,s93,40960\n"
RD"a[72:75],v226 offset:22528\n"
MF"v[204:207],a[108:111],a[44:47],v[204:207]\n"
"s_lshl_b32 s93,s93,9\n"
MF"v[204:207],a[104:107],a[36:39],v[204:207]\n"
WT"lgkmcnt(3)\n"
RD"a[76:79],v226 offset:23552\n"
SA"s93,s93,131072\n"
MF"v[116:119],a[112:115],a[48:51],v[116:119]\n"
SA"s94,s94,59506688\n"
MF"v[116:119],a[116:119],a[56:59],v[116:119]\n"
"s_lshl_b32 s92,s30,10\n"
RD"a[80:83],v235 offset:8192\n"
MF"v[196:199],a[116:119],a[60:63],v[196:199]\n"
"v_add_u32 v244,s92,v224\n"
MF"v[196:199],a[112:115],a[52:55],v[196:199]\n"
SA"s92,s45,4096\n"
RD"a[84:87],v235 offset:9216\n"
MF"v[124:127],a[120:123],a[48:51],v[124:127]\n"
"v_add_u32 v245,s92,v224\n"
MF"v[124:127],a[124:127],a[56:59],v[124:127]\n"
RD"a[88:91],v235 offset:10240\n"
MF"v[204:207],a[124:127],a[60:63],v[204:207]\n"
"v_and_b32 v246,128,v224\n"
MF"v[204:207],a[120:123],a[52:55],v[204:207]\n"
WT"lgkmcnt(3)\n"
RD"a[92:95],v235 offset:11264\n"
"v_lshl_add_u32 v243,v246,5,v243\n"
MF"v[132:135],a[64:67],a[244:247],v[132:135]\n"
"v_lshrrev_b32 v246,8,v224\n"
MF"v[132:135],a[68:71],a[252:255],v[132:135]\n"
SA"s90,s88,329728\n"
RD"a[96:99],v235 offset:28672\n"
PK"v116,v116,v117\n"
MF"v[208:211],a[68:71],a[128:131],v[208:211]\n"
"s_mov_b32 s95,0xffff0101\n"
PK"v117,v118,v119\n"
"v_lshl_add_u32 v243,v246,27,v243\n"
MF"v[208:211],a[64:67],a[248:251],v[208:211]\n"
"s_lshl_b32 s92,s30,10\n"
RD"a[100:103],v235 offset:29696\n"
PK"v118,v124,v125\n"
MF"v[140:143],a[72:75],a[244:247],v[140:143]\n"
PK"v119,v126,v127\n"
"v_add_u32 v247,s92,v227\n"
MF"v[140:143],a[76:79],a[252:255],v[140:143]\n"
BS"v[116:119],v224,s[8:11],s90 offen sc1\n"
MF"v[212:215],a[76:79],a[128:131],v[212:215]\n"
RD"a[104:107],v235 offset:30720\n"
MF"v[212:215],a[72:75],a[248:251],v[212:215]\n"
WT"lgkmcnt(3)\n"
"buffer_load_dwordx2 v[240:241],v243,s[8:11],s93 offen sc1\n"
MF"v[132:135],a[80:83],a[132:135],v[132:135]\n"
RD"a[108:111],v235 offset:31744\n"
MF"v[132:135],a[84:87],a[140:143],v[132:135]\n"
SA"s91,s22,329728\n"
RD"a[112:115],v236 offset:16384\n"
PK"v196,v196,v197\n"
MF"v[208:211],a[84:87],a[144:147],v[208:211]\n"
PK"v197,v198,v199\n"
PK"v198,v204,v205\n"
MF"v[208:211],a[80:83],a[136:139],v[208:211]\n"
RD"a[116:119],v236 offset:17408\n"
PK"v199,v206,v207\n"
MF"v[140:143],a[88:91],a[132:135],v[140:143]\n"
BS"v[196:199],v224,s[8:11],s91 offen sc1\n"
MF"v[140:143],a[92:95],a[140:143],v[140:143]\n"
RD"a[120:123],v236 offset:18432\n"
MF"v[212:215],a[92:95],a[144:147],v[212:215]\n"
MF"v[212:215],a[88:91],a[136:139],v[212:215]\n"
WT"lgkmcnt(3)\n"
RD"a[124:127],v236 offset:19456\n"
MF"v[132:135],a[96:99],a[148:151],v[132:135]\n"
MF"v[132:135],a[100:103],a[156:159],v[132:135]\n"
RD"a[64:67],v237 offset:4096\n"
MF"v[208:211],a[100:103],a[160:163],v[208:211]\n"
MF"v[208:211],a[96:99],a[152:155],v[208:211]\n"
RD"a[68:71],v237 offset:5120\n"
MF"v[140:143],a[104:107],a[148:151],v[140:143]\n"
MF"v[140:143],a[108:111],a[156:159],v[140:143]\n"
RD"a[72:75],v237 offset:6144\n"
MF"v[212:215],a[108:111],a[160:163],v[212:215]\n"
MF"v[212:215],a[104:107],a[152:155],v[212:215]\n"
WT"lgkmcnt(3)\n"
RD"a[76:79],v237 offset:7168\n"
MF"v[132:135],a[112:115],a[164:167],v[132:135]\n"
MF"v[132:135],a[116:119],a[172:175],v[132:135]\n"
RD"a[80:83],v237 offset:24576\n"
MF"v[208:211],a[116:119],a[176:179],v[208:211]\n"
MF"v[208:211],a[112:115],a[168:171],v[208:211]\n"
RD"a[84:87],v237 offset:25600\n"
MF"v[140:143],a[120:123],a[164:167],v[140:143]\n"
MF"v[140:143],a[124:127],a[172:175],v[140:143]\n"
RD"a[88:91],v237 offset:26624\n"
MF"v[212:215],a[124:127],a[176:179],v[212:215]\n"
MF"v[212:215],a[120:123],a[168:171],v[212:215]\n"
WT"lgkmcnt(3)\n"
RD"a[92:95],v237 offset:27648\n"
MF"v[132:135],a[64:67],a[32:35],v[132:135]\n"
MF"v[132:135],a[68:71],a[40:43],v[132:135]\n"
RD"a[96:99],v226 offset:24576\n"
MF"v[208:211],a[68:71],a[44:47],v[208:211]\n"
MF"v[208:211],a[64:67],a[36:39],v[208:211]\n"
RD"a[100:103],v226 offset:25600\n"
MF"v[140:143],a[72:75],a[32:35],v[140:143]\n"
MF"v[140:143],a[76:79],a[40:43],v[140:143]\n"
RD"a[104:107],v226 offset:26624\n"
MF"v[212:215],a[76:79],a[44:47],v[212:215]\n"
MF"v[212:215],a[72:75],a[36:39],v[212:215]\n"
WT"lgkmcnt(3)\n"
RD"a[108:111],v226 offset:27648\n"
MF"v[132:135],a[80:83],a[48:51],v[132:135]\n"
MF"v[132:135],a[84:87],a[56:59],v[132:135]\n"
RD"a[112:115],v235 offset:12288\n"
MF"v[208:211],a[84:87],a[60:63],v[208:211]\n"
WT"vmcnt(1)\n"
"v_cmp_eq_u64 vcc,s[28:29],v[240:241]\n"
MF"v[208:211],a[80:83],a[52:55],v[208:211]\n"
"s_and_b32 s50,vcc_lo,0xff\n"
RD"a[116:119],v235 offset:13312\n"
"s_or_b32 s95,s95,s50\n"
MF"v[140:143],a[88:91],a[48:51],v[140:143]\n"
"s_bitcmp1_b32 s95,1\n"
"s_cselect_b32 s51,s94,1065353216\n"
MF"v[140:143],a[92:95],a[56:59],v[140:143]\n"
SA"s92,s51,5120\n"
RD"a[120:123],v235 offset:14336\n"
MF"v[212:215],a[92:95],a[60:63],v[212:215]\n"
BL"a[184:187],v244,s[8:11],s92 offen sc1\n"
MF"v[212:215],a[88:91],a[52:55],v[212:215]\n"
WT"lgkmcnt(3)\n"
RD"a[124:127],v235 offset:15360\n"
MF"v[148:151],a[96:99],a[244:247],v[148:151]\n"
BL"a[216:219],v245,s[8:11],s92 offen sc1\n"
MF"v[148:151],a[100:103],a[252:255],v[148:151]\n"
SA"s90,s88,330752\n"
RD"a[64:67],v236 offset:0\n"
PK"v132,v132,v133\n"
MF"v[216:219],a[100:103],a[128:131],v[216:219]\n"
"s_bitcmp1_b32 s95,2\n"
PK"v133,v134,v135\n"
PK"v134,v140,v141\n"
MF"v[216:219],a[96:99],a[248:251],v[216:219]\n"
"s_cselect_b32 s51,s94,1065353216\n"
RD"a[68:71],v236 offset:1024\n"
PK"v135,v142,v143\n"
MF"v[156:159],a[104:107],a[244:247],v[156:159]\n"
SA"s92,s51,10240\n"
BS"v[132:135],v224,s[8:11],s90 offen sc1\n"
MF"v[156:159],a[108:111],a[252:255],v[156:159]\n"
BL"a[188:191],v244,s[8:11],s92 offen sc1\n"
MF"v[220:223],a[108:111],a[128:131],v[220:223]\n"
RD"a[72:75],v236 offset:2048\n"
MF"v[220:223],a[104:107],a[248:251],v[220:223]\n"
WT"lgkmcnt(3)\n"
BL"a[220:223],v245,s[8:11],s92 offen sc1\n"
MF"v[148:151],a[112:115],a[132:135],v[148:151]\n"
"s_bitcmp1_b32 s95,3\n"
RD"a[76:79],v236 offset:3072\n"
"s_cselect_b32 s51,s94,1065353216\n"
MF"v[148:151],a[116:119],a[140:143],v[148:151]\n"
SA"s91,s22,330752\n"
RD"a[80:83],v236 offset:20480\n"
PK"v208,v208,v209\n"
MF"v[216:219],a[116:119],a[144:147],v[216:219]\n"
SA"s92,s51,15360\n"
PK"v209,v210,v211\n"
PK"v210,v212,v213\n"
MF"v[216:219],a[112:115],a[136:139],v[216:219]\n"
BL"a[192:195],v244,s[8:11],s92 offen sc1\n"
MF"v[156:159],a[120:123],a[132:135],v[156:159]\n"
RD"a[84:87],v236 offset:21504\n"
PK"v211,v214,v215\n"
MF"v[156:159],a[124:127],a[140:143],v[156:159]\n"
BS"v[208:211],v224,s[8:11],s91 offen sc1\n"
MF"v[220:223],a[124:127],a[144:147],v[220:223]\n"
"s_bitcmp1_b32 s95,4\n"
BL"a[224:227],v245,s[8:11],s92 offen sc1\n"
MF"v[220:223],a[120:123],a[136:139],v[220:223]\n"
WT"lgkmcnt(2)\n"
RD"a[88:91],v236 offset:22528\n"
RD"a[92:95],v236 offset:23552\n"
MF"v[148:151],a[64:67],a[148:151],v[148:151]\n"
"s_cselect_b32 s51,s94,1065353216\n"
SA"s92,s51,20480\n"
MF"v[148:151],a[68:71],a[156:159],v[148:151]\n"
BL"a[196:199],v244,s[8:11],s92 offen sc1\n"
MF"v[216:219],a[68:71],a[160:163],v[216:219]\n"
RD"a[96:99],v237 offset:8192\n"
MF"v[216:219],a[64:67],a[152:155],v[216:219]\n"
BL"a[228:231],v245,s[8:11],s92 offen sc1\n"
MF"v[156:159],a[72:75],a[148:151],v[156:159]\n"
"s_bitcmp1_b32 s95,5\n"
RD"a[100:103],v237 offset:9216\n"
"s_cselect_b32 s51,s94,1065353216\n"
MF"v[156:159],a[76:79],a[156:159],v[156:159]\n"
SA"s92,s51,25600\n"
RD"a[104:107],v237 offset:10240\n"
MF"v[220:223],a[76:79],a[160:163],v[220:223]\n"
BL"a[200:203],v244,s[8:11],s92 offen sc1\n"
MF"v[220:223],a[72:75],a[152:155],v[220:223]\n"
WT"lgkmcnt(3)\n"
RD"a[108:111],v237 offset:11264\n"
MF"v[148:151],a[80:83],a[164:167],v[148:151]\n"
BL"a[232:235],v245,s[8:11],s92 offen sc1\n"
MF"v[148:151],a[84:87],a[172:175],v[148:151]\n"
RD"a[112:115],v237 offset:28672\n"
MF"v[216:219],a[84:87],a[176:179],v[216:219]\n"
"s_bitcmp1_b32 s95,6\n"
"s_cselect_b32 s51,s94,1065353216\n"
MF"v[216:219],a[80:83],a[168:171],v[216:219]\n"
SA"s92,s51,30720\n"
RD"a[116:119],v237 offset:29696\n"
MF"v[156:159],a[88:91],a[164:167],v[156:159]\n"
BL"a[204:207],v244,s[8:11],s92 offen sc1\n"
MF"v[156:159],a[92:95],a[172:175],v[156:159]\n"
RD"a[120:123],v237 offset:30720\n"
MF"v[220:223],a[92:95],a[176:179],v[220:223]\n"
BL"a[236:239],v245,s[8:11],s92 offen sc1\n"
MF"v[220:223],a[88:91],a[168:171],v[220:223]\n"
WT"lgkmcnt(3)\n"
RD"a[124:127],v237 offset:31744\n"
MF"v[148:151],a[96:99],a[32:35],v[148:151]\n"
"s_bitcmp1_b32 s95,7\n"
"s_cselect_b32 s51,s94,1065353216\n"
MF"v[148:151],a[100:103],a[40:43],v[148:151]\n"
SA"s92,s51,35840\n"
MF"v[216:219],a[100:103],a[44:47],v[216:219]\n"
BL"a[208:211],v244,s[8:11],s92 offen sc1\n"
MF"v[216:219],a[96:99],a[36:39],v[216:219]\n"
MF"v[156:159],a[104:107],a[32:35],v[156:159]\n"
BL"a[240:243],v245,s[8:11],s92 offen sc1\n"
MF"v[156:159],a[108:111],a[40:43],v[156:159]\n"
MF"v[220:223],a[108:111],a[44:47],v[220:223]\n"
MF"v[220:223],a[104:107],a[36:39],v[220:223]\n"
WT"lgkmcnt(0)\n"
MF"v[148:151],a[112:115],a[48:51],v[148:151]\n"
MF"v[148:151],a[116:119],a[56:59],v[148:151]\n"
MF"v[216:219],a[116:119],a[60:63],v[216:219]\n"
MF"v[216:219],a[112:115],a[52:55],v[216:219]\n"
MF"v[156:159],a[120:123],a[48:51],v[156:159]\n"
MF"v[156:159],a[124:127],a[56:59],v[156:159]\n"
MF"v[220:223],a[124:127],a[60:63],v[220:223]\n"
MF"v[220:223],a[120:123],a[52:55],v[220:223]\n"
"s_nop 5\n"
PK"v148,v148,v149\n"
PK"v149,v150,v151\n"
PK"v150,v156,v157\n"
PK"v151,v158,v159\n"
SA"s90,s88,331776\n"
BS"v[148:151],v224,s[8:11],s90 offen sc1\n"
PK"v216,v216,v217\n"
PK"v217,v218,v219\n"
PK"v218,v220,v221\n"
PK"v219,v222,v223\n"
SA"s91,s22,331776\n"
BS"v[216:219],v224,s[8:11],s91 offen sc1\n"
"s_cmp_eq_u32 s23,0\n"
"s_cbranch_scc0 L1089_%=\n"
"ds_write_b128 v227,v[0:3] offset:0\n"
"ds_write_b128 v227,v[8:11] offset:1024\n"
"ds_write_b128 v227,v[16:19] offset:2048\n"
"ds_write_b128 v227,v[24:27] offset:3072\n"
"ds_write_b128 v227,v[32:35] offset:4096\n"
"ds_write_b128 v227,v[84:87] offset:5120\n"
"ds_write_b128 v227,v[100:103] offset:6144\n"
"ds_write_b128 v227,v[116:119] offset:7168\n"
"ds_write_b128 v227,v[132:135] offset:8192\n"
"ds_write_b128 v227,v[148:151] offset:9216\n"
"L1089_%=:\n"
"s_cmp_eq_u32 s23,4\n"
"s_cbranch_scc0 L1090_%=\n"
"ds_write_b128 v227,v[40:43] offset:0\n"
"ds_write_b128 v227,v[48:51] offset:1024\n"
"ds_write_b128 v227,v[56:59] offset:2048\n"
"ds_write_b128 v227,v[64:67] offset:3072\n"
"ds_write_b128 v227,v[72:75] offset:4096\n"
"ds_write_b128 v227,v[164:167] offset:5120\n"
"ds_write_b128 v227,v[180:183] offset:6144\n"
"ds_write_b128 v227,v[196:199] offset:7168\n"
"ds_write_b128 v227,v[208:211] offset:8192\n"
"ds_write_b128 v227,v[216:219] offset:9216\n"
"L1090_%=:\n"
MF"v[4:7],v[96:99],v[112:115],0\n"
MF"v[12:15],v[104:107],v[112:115],0\n"
MF"v[20:23],v[96:99],v[120:123],0\n"
MF"v[28:31],v[104:107],v[120:123],0\n"
MF"v[4:7],v[96:99],v[144:147],v[4:7]\n"
MF"v[12:15],v[104:107],v[144:147],v[12:15]\n"
MF"v[20:23],v[96:99],v[152:155],v[20:23]\n"
MF"v[28:31],v[104:107],v[152:155],v[28:31]\n"
MF"v[36:39],v[96:99],v[128:131],0\n"
MF"v[44:47],v[104:107],v[128:131],0\n"
MF"v[52:55],v[96:99],v[136:139],0\n"
MF"v[60:63],v[104:107],v[136:139],0\n"
MF"v[36:39],v[96:99],v[160:163],v[36:39]\n"
MF"v[44:47],v[104:107],v[160:163],v[44:47]\n"
MF"v[52:55],v[96:99],v[168:171],v[52:55]\n"
MF"v[60:63],v[104:107],v[168:171],v[60:63]\n"
SA"s89,s89,4096\n"
"s_mov_b32 s54,1048576\n"
WT"vmcnt(0)\n"
"buffer_store_dwordx2 v[232:233],v239,s[8:11],s89 offen sc1\n"
"buffer_load_dwordx2 v[240:241],v243,s[8:11],s93 offen sc1\n"
WT"lgkmcnt(0)\n"
"s_barrier\n"
RD"a[180:183],v247 offset:0\n"
RD"a[212:215],v227 offset:4096\n"
RD"a[64:67],v247 offset:5120\n"
RD"a[96:99],v227 offset:9216\n"
"s_getpc_b64 s[52:53]\n"
"L1097_%=:\n"
SA"s52,s52,L1096_%=-L1097_%=-32\n"
"s_addc_u32 s53,s53,0\n"
"s_mov_b32 s89,0\n"
WT"lgkmcnt(0)\n"
MF"v[4:7],v[80:83],a[180:183],v[4:7]\n"
MF"v[12:15],v[88:91],a[180:183],v[12:15]\n"
MF"v[20:23],v[80:83],a[212:215],v[20:23]\n"
MF"v[28:31],v[88:91],a[212:215],v[28:31]\n"
MF"v[36:39],v[80:83],a[64:67],v[36:39]\n"
MF"v[44:47],v[88:91],a[64:67],v[44:47]\n"
MF"v[52:55],v[80:83],a[96:99],v[52:55]\n"
MF"v[60:63],v[88:91],a[96:99],v[60:63]\n"
"s_branch L1092_%=\n"
"L1094_%=:\n"
"s_sleep 3\n"
"buffer_load_dwordx2 v[240:241],v243,s[8:11],s93 offen sc1\n"
"L1091_%=:\n"
WT"vmcnt(0)\n"
"v_cmp_eq_u64 vcc,s[28:29],v[240:241]\n"
"s_sub_u32 s54,s54,1\n"
"s_cselect_b32 s90,-1,vcc_lo\n"
"s_andn2_b32 s90,s90,s95\n"
"s_cbranch_scc0 L1094_%=\n"
"s_or_b32 s95,s95,s90\n"
"L1098_%=:\n"
"s_ff1_i32_b32 s50,s90\n"
"s_bitset0_b32 s90,s50\n"
"s_lshl_b32 s50,s50,5\n"
SA"s50,s50,s52\n"
"s_addc_u32 s51,s53,0\n"
"s_setpc_b64 s[50:51]\n"
"L1096_%=:\n"
SA"s92,s94,5120\n"
BL"a[184:187],v244,s[8:11],s92 offen sc1\n"
BL"a[216:219],v245,s[8:11],s92 offen sc1\n"
"s_branch L1093_%=\n"
"s_nop 0\n"
SA"s92,s94,10240\n"
BL"a[188:191],v244,s[8:11],s92 offen sc1\n"
BL"a[220:223],v245,s[8:11],s92 offen sc1\n"
"s_branch L1093_%=\n"
"s_nop 0\n"
SA"s92,s94,15360\n"
BL"a[192:195],v244,s[8:11],s92 offen sc1\n"
BL"a[224:227],v245,s[8:11],s92 offen sc1\n"
"s_branch L1093_%=\n"
"s_nop 0\n"
SA"s92,s94,20480\n"
BL"a[196:199],v244,s[8:11],s92 offen sc1\n"
BL"a[228:231],v245,s[8:11],s92 offen sc1\n"
"s_branch L1093_%=\n"
"s_nop 0\n"
SA"s92,s94,25600\n"
BL"a[200:203],v244,s[8:11],s92 offen sc1\n"
BL"a[232:235],v245,s[8:11],s92 offen sc1\n"
"s_branch L1093_%=\n"
"s_nop 0\n"
SA"s92,s94,30720\n"
BL"a[204:207],v244,s[8:11],s92 offen sc1\n"
BL"a[236:239],v245,s[8:11],s92 offen sc1\n"
"s_branch L1093_%=\n"
"s_nop 0\n"
SA"s92,s94,35840\n"
BL"a[208:211],v244,s[8:11],s92 offen sc1\n"
BL"a[240:243],v245,s[8:11],s92 offen sc1\n"
"s_branch L1093_%=\n"
"s_nop 0\n"
"s_nop 0\n"
"s_nop 0\n"
"s_nop 0\n"
"s_nop 0\n"
"s_nop 0\n"
"s_nop 0\n"
"s_nop 0\n"
"s_nop 0\n"
SA"s92,s94,332800\n"
BL"a[68:71],v244,s[8:11],s92 offen sc1\n"
BL"a[100:103],v245,s[8:11],s92 offen sc1\n"
"s_branch L1093_%=\n"
"s_nop 0\n"
SA"s92,s94,337920\n"
BL"a[72:75],v244,s[8:11],s92 offen sc1\n"
BL"a[104:107],v245,s[8:11],s92 offen sc1\n"
"s_branch L1093_%=\n"
"s_nop 0\n"
SA"s92,s94,343040\n"
BL"a[76:79],v244,s[8:11],s92 offen sc1\n"
BL"a[108:111],v245,s[8:11],s92 offen sc1\n"
"s_branch L1093_%=\n"
"s_nop 0\n"
SA"s92,s94,348160\n"
BL"a[80:83],v244,s[8:11],s92 offen sc1\n"
BL"a[112:115],v245,s[8:11],s92 offen sc1\n"
"s_branch L1093_%=\n"
"s_nop 0\n"
SA"s92,s94,353280\n"
BL"a[84:87],v244,s[8:11],s92 offen sc1\n"
BL"a[116:119],v245,s[8:11],s92 offen sc1\n"
"s_branch L1093_%=\n"
"s_nop 0\n"
SA"s92,s94,358400\n"
BL"a[88:91],v244,s[8:11],s92 offen sc1\n"
BL"a[120:123],v245,s[8:11],s92 offen sc1\n"
"s_branch L1093_%=\n"
"s_nop 0\n"
SA"s92,s94,363520\n"
BL"a[92:95],v244,s[8:11],s92 offen sc1\n"
BL"a[124:127],v245,s[8:11],s92 offen sc1\n"
"s_branch L1093_%=\n"
"s_nop 0\n"
"L1093_%=:\n"
"s_cmp_lg_u32 s90,0\n"
"s_cbranch_scc1 L1098_%=\n"
"s_and_b32 s92,s95,s89\n"
"s_cmp_eq_u32 s92,-1\n"
"s_cbranch_scc1 L1095_%=\n"
"buffer_load_dwordx2 v[240:241],v243,s[8:11],s93 offen sc1\n"
WT"vmcnt(1)\n"
"L1092_%=:\n"
"s_or_b32 s92,s95,0xff00\n"
"s_andn2_b32 s92,s92,s89\n"
"s_cmp_eq_u32 s92,-1\n"
"s_cbranch_scc0 L1091_%=\n"
MF"v[4:7],v[80:83],a[184:187],v[4:7]\n"
MF"v[12:15],v[88:91],a[184:187],v[12:15]\n"
MF"v[4:7],v[80:83],a[188:191],v[4:7]\n"
MF"v[12:15],v[88:91],a[188:191],v[12:15]\n"
MF"v[4:7],v[80:83],a[192:195],v[4:7]\n"
MF"v[12:15],v[88:91],a[192:195],v[12:15]\n"
MF"v[4:7],v[80:83],a[196:199],v[4:7]\n"
MF"v[12:15],v[88:91],a[196:199],v[12:15]\n"
MF"v[4:7],v[80:83],a[200:203],v[4:7]\n"
MF"v[12:15],v[88:91],a[200:203],v[12:15]\n"
MF"v[4:7],v[80:83],a[204:207],v[4:7]\n"
MF"v[12:15],v[88:91],a[204:207],v[12:15]\n"
MF"v[4:7],v[80:83],a[208:211],v[4:7]\n"
MF"v[12:15],v[88:91],a[208:211],v[12:15]\n"
MF"v[20:23],v[80:83],a[216:219],v[20:23]\n"
MF"v[28:31],v[88:91],a[216:219],v[28:31]\n"
MF"v[20:23],v[80:83],a[220:223],v[20:23]\n"
MF"v[28:31],v[88:91],a[220:223],v[28:31]\n"
MF"v[20:23],v[80:83],a[224:227],v[20:23]\n"
MF"v[28:31],v[88:91],a[224:227],v[28:31]\n"
MF"v[20:23],v[80:83],a[228:231],v[20:23]\n"
MF"v[28:31],v[88:91],a[228:231],v[28:31]\n"
PK"v4,v4,v5\n"
PK"v5,v6,v7\n"
PK"v6,v12,v13\n"
PK"v7,v14,v15\n"
BS"v[4:7],v176,s[16:19],s38 offen\n"
MF"v[20:23],v[80:83],a[232:235],v[20:23]\n"
MF"v[28:31],v[88:91],a[232:235],v[28:31]\n"
MF"v[20:23],v[80:83],a[236:239],v[20:23]\n"
MF"v[28:31],v[88:91],a[236:239],v[28:31]\n"
MF"v[20:23],v[80:83],a[240:243],v[20:23]\n"
MF"v[28:31],v[88:91],a[240:243],v[28:31]\n"
"s_nop 7\n"
PK"v20,v20,v21\n"
PK"v21,v22,v23\n"
PK"v22,v28,v29\n"
PK"v23,v30,v31\n"
BS"v[20:23],v176,s[16:19],s55 offen\n"
"s_mov_b32 s89,-1\n"
"s_cmp_eq_u32 s95,-1\n"
"s_cbranch_scc0 L1091_%=\n"
"L1095_%=:\n"
WT"vmcnt(0)\n"
MF"v[36:39],v[80:83],a[68:71],v[36:39]\n"
MF"v[44:47],v[88:91],a[68:71],v[44:47]\n"
MF"v[36:39],v[80:83],a[72:75],v[36:39]\n"
MF"v[44:47],v[88:91],a[72:75],v[44:47]\n"
MF"v[36:39],v[80:83],a[76:79],v[36:39]\n"
MF"v[44:47],v[88:91],a[76:79],v[44:47]\n"
MF"v[36:39],v[80:83],a[80:83],v[36:39]\n"
MF"v[44:47],v[88:91],a[80:83],v[44:47]\n"
MF"v[36:39],v[80:83],a[84:87],v[36:39]\n"
MF"v[44:47],v[88:91],a[84:87],v[44:47]\n"
MF"v[36:39],v[80:83],a[88:91],v[36:39]\n"
MF"v[44:47],v[88:91],a[88:91],v[44:47]\n"
MF"v[36:39],v[80:83],a[92:95],v[36:39]\n"
MF"v[44:47],v[88:91],a[92:95],v[44:47]\n"
MF"v[52:55],v[80:83],a[100:103],v[52:55]\n"
MF"v[60:63],v[88:91],a[100:103],v[60:63]\n"
MF"v[52:55],v[80:83],a[104:107],v[52:55]\n"
MF"v[60:63],v[88:91],a[104:107],v[60:63]\n"
MF"v[52:55],v[80:83],a[108:111],v[52:55]\n"
MF"v[60:63],v[88:91],a[108:111],v[60:63]\n"
MF"v[52:55],v[80:83],a[112:115],v[52:55]\n"
MF"v[60:63],v[88:91],a[112:115],v[60:63]\n"
PK"v36,v36,v37\n"
PK"v37,v38,v39\n"
PK"v38,v44,v45\n"
PK"v39,v46,v47\n"
BS"v[36:39],v177,s[16:19],s38 offen\n"
MF"v[52:55],v[80:83],a[116:119],v[52:55]\n"
MF"v[60:63],v[88:91],a[116:119],v[60:63]\n"
MF"v[52:55],v[80:83],a[120:123],v[52:55]\n"
MF"v[60:63],v[88:91],a[120:123],v[60:63]\n"
MF"v[52:55],v[80:83],a[124:127],v[52:55]\n"
MF"v[60:63],v[88:91],a[124:127],v[60:63]\n"
"s_nop 7\n"
PK"v52,v52,v53\n"
PK"v53,v54,v55\n"
PK"v54,v60,v61\n"
PK"v55,v62,v63\n"
BS"v[52:55],v177,s[16:19],s55 offen\n"
WT"vmcnt(0) lgkmcnt(0)\n"
"s_endpgm\n"
"L1004_%=:\n"
"s_mov_b32 s54,0\n"
"L1006_%=:\n"
"s_sleep 2\n"
BL"v[160:163],v224,s[8:11],0 offen offset:256 sc1\n"
BL"v[164:167],v224,s[8:11],0 offen offset:1280 sc1\n"
WT"vmcnt(0)\n"
SA"s54,s54,1\n"
"v_cmp_eq_u64 vcc,s[28:29],v[160:161]\n"
"v_cmp_eq_u64_e64 s[62:63],s[28:29],v[162:163]\n"
"s_and_b64 vcc,vcc,s[62:63]\n"
"v_cmp_eq_u64_e64 s[62:63],s[28:29],v[164:165]\n"
"s_and_b64 vcc,vcc,s[62:63]\n"
"v_cmp_eq_u64_e64 s[62:63],s[28:29],v[166:167]\n"
"s_and_b64 vcc,vcc,s[62:63]\n"
"s_cmp_eq_u64 vcc,exec\n"
"s_cbranch_scc1 L1005_%=\n"
"s_cmp_lt_u32 s54,1048576\n"
"s_cbranch_scc1 L1006_%=\n"
"s_branch L1005_%=\n"
"L1007_%=:\n"
"s_mov_b32 s54,0\n"
"L1009_%=:\n"
"v_max3_u32 v226,v160,v161,v162\n"
"v_max3_u32 v226,v226,v163,v164\n"
"v_max3_u32 v226,v226,v165,v166\n"
"v_max3_u32 v226,v226,v167,v168\n"
"v_max3_u32 v226,v226,v169,v170\n"
"v_max3_u32 v226,v226,v171,v172\n"
"v_max3_u32 v226,v226,v173,v174\n"
"v_max3_u32 v226,v226,v175,v176\n"
"v_max3_u32 v226,v226,v177,v178\n"
"v_max3_u32 v226,v226,v179,v180\n"
"v_max3_u32 v226,v226,v181,v182\n"
"v_max3_u32 v226,v226,v183,v184\n"
"v_max3_u32 v226,v226,v185,v186\n"
"v_max3_u32 v226,v226,v187,v188\n"
"v_max3_u32 v226,v226,v189,v190\n"
"v_max_u32 v226,v226,v191\n"
"v_mov_b32 v229,v226\n"
"s_nop 1\n"
"v_max_u32_dpp v229,v229,v229 row_shr:1 row_mask:0xf bank_mask:0xf\n"
"s_nop 1\n"
"v_max_u32_dpp v229,v229,v229 row_shr:2 row_mask:0xf bank_mask:0xf\n"
"s_nop 1\n"
"v_max_u32_dpp v229,v229,v229 row_shr:4 row_mask:0xf bank_mask:0xf\n"
"s_nop 1\n"
"v_max_u32_dpp v229,v229,v229 row_shr:8 row_mask:0xf bank_mask:0xf\n"
"s_nop 1\n"
"v_max_u32_dpp v229,v229,v229 row_bcast:15 row_mask:0xa bank_mask:0xf\n"
"s_nop 1\n"
"v_max_u32_dpp v229,v229,v229 row_bcast:31 row_mask:0xc bank_mask:0xf\n"
"s_nop 1\n"
"v_readlane_b32 s62,v229,63\n"
"s_nop 1\n"
"v_cmp_eq_u32 vcc,s62,v160\n"
"s_nop 1\n"
"v_cndmask_b32_e64 v160,v160,0,vcc\n"
"v_cmp_eq_u32 vcc,s62,v161\n"
"s_nop 1\n"
"v_cndmask_b32_e64 v161,v161,0,vcc\n"
"v_cmp_eq_u32 vcc,s62,v162\n"
"s_nop 1\n"
"v_cndmask_b32_e64 v162,v162,0,vcc\n"
"v_cmp_eq_u32 vcc,s62,v163\n"
"s_nop 1\n"
"v_cndmask_b32_e64 v163,v163,0,vcc\n"
"v_cmp_eq_u32 vcc,s62,v164\n"
"s_nop 1\n"
"v_cndmask_b32_e64 v164,v164,0,vcc\n"
"v_cmp_eq_u32 vcc,s62,v165\n"
"s_nop 1\n"
"v_cndmask_b32_e64 v165,v165,0,vcc\n"
"v_cmp_eq_u32 vcc,s62,v166\n"
"s_nop 1\n"
"v_cndmask_b32_e64 v166,v166,0,vcc\n"
"v_cmp_eq_u32 vcc,s62,v167\n"
"s_nop 1\n"
"v_cndmask_b32_e64 v167,v167,0,vcc\n"
"v_cmp_eq_u32 vcc,s62,v168\n"
"s_nop 1\n"
"v_cndmask_b32_e64 v168,v168,0,vcc\n"
"v_cmp_eq_u32 vcc,s62,v169\n"
"s_nop 1\n"
"v_cndmask_b32_e64 v169,v169,0,vcc\n"
"v_cmp_eq_u32 vcc,s62,v170\n"
"s_nop 1\n"
"v_cndmask_b32_e64 v170,v170,0,vcc\n"
"v_cmp_eq_u32 vcc,s62,v171\n"
"s_nop 1\n"
"v_cndmask_b32_e64 v171,v171,0,vcc\n"
"v_cmp_eq_u32 vcc,s62,v172\n"
"s_nop 1\n"
"v_cndmask_b32_e64 v172,v172,0,vcc\n"
"v_cmp_eq_u32 vcc,s62,v173\n"
"s_nop 1\n"
"v_cndmask_b32_e64 v173,v173,0,vcc\n"
"v_cmp_eq_u32 vcc,s62,v174\n"
"s_nop 1\n"
"v_cndmask_b32_e64 v174,v174,0,vcc\n"
"v_cmp_eq_u32 vcc,s62,v175\n"
"s_nop 1\n"
"v_cndmask_b32_e64 v175,v175,0,vcc\n"
"v_cmp_eq_u32 vcc,s62,v176\n"
"s_nop 1\n"
"v_cndmask_b32_e64 v176,v176,0,vcc\n"
"v_cmp_eq_u32 vcc,s62,v177\n"
"s_nop 1\n"
"v_cndmask_b32_e64 v177,v177,0,vcc\n"
"v_cmp_eq_u32 vcc,s62,v178\n"
"s_nop 1\n"
"v_cndmask_b32_e64 v178,v178,0,vcc\n"
"v_cmp_eq_u32 vcc,s62,v179\n"
"s_nop 1\n"
"v_cndmask_b32_e64 v179,v179,0,vcc\n"
"v_cmp_eq_u32 vcc,s62,v180\n"
"s_nop 1\n"
"v_cndmask_b32_e64 v180,v180,0,vcc\n"
"v_cmp_eq_u32 vcc,s62,v181\n"
"s_nop 1\n"
"v_cndmask_b32_e64 v181,v181,0,vcc\n"
"v_cmp_eq_u32 vcc,s62,v182\n"
"s_nop 1\n"
"v_cndmask_b32_e64 v182,v182,0,vcc\n"
"v_cmp_eq_u32 vcc,s62,v183\n"
"s_nop 1\n"
"v_cndmask_b32_e64 v183,v183,0,vcc\n"
"v_cmp_eq_u32 vcc,s62,v184\n"
"s_nop 1\n"
"v_cndmask_b32_e64 v184,v184,0,vcc\n"
"v_cmp_eq_u32 vcc,s62,v185\n"
"s_nop 1\n"
"v_cndmask_b32_e64 v185,v185,0,vcc\n"
"v_cmp_eq_u32 vcc,s62,v186\n"
"s_nop 1\n"
"v_cndmask_b32_e64 v186,v186,0,vcc\n"
"v_cmp_eq_u32 vcc,s62,v187\n"
"s_nop 1\n"
"v_cndmask_b32_e64 v187,v187,0,vcc\n"
"v_cmp_eq_u32 vcc,s62,v188\n"
"s_nop 1\n"
"v_cndmask_b32_e64 v188,v188,0,vcc\n"
"v_cmp_eq_u32 vcc,s62,v189\n"
"s_nop 1\n"
"v_cndmask_b32_e64 v189,v189,0,vcc\n"
"v_cmp_eq_u32 vcc,s62,v190\n"
"s_nop 1\n"
"v_cndmask_b32_e64 v190,v190,0,vcc\n"
"v_cmp_eq_u32 vcc,s62,v191\n"
"s_nop 1\n"
"v_cndmask_b32_e64 v191,v191,0,vcc\n"
"v_mov_b32 v229,s62\n"
"s_lshl_b64 exec,1,s54\n"
"v_mov_b32 v225,v229\n"
"s_mov_b64 exec,-1\n"
SA"s54,s54,1\n"
"s_cmp_lt_u32 s54,8\n"
"s_cbranch_scc1 L1009_%=\n"
"s_nop 4\n"
"s_branch L1008_%=\n"
"L1010_%=:\n"
"s_mov_b32 s54,0\n"
"L1012_%=:\n"
"s_sleep 2\n"
"buffer_load_dwordx2 v[208:209],v252,s[8:11],s50 offen sc1\n"
WT"vmcnt(0)\n"
SA"s54,s54,1\n"
"v_cmp_eq_u64 vcc,s[28:29],v[208:209]\n"
"s_cmp_eq_u64 vcc,exec\n"
"s_cbranch_scc1 L1011_%=\n"
"s_cmp_lt_u32 s54,1048576\n"
"s_cbranch_scc1 L1012_%=\n"
"s_branch L1011_%=\n"
"L1014_%=:\n"
"s_nop 15\n"
WT"vmcnt(0)\n"
"s_lshl_b32 s89,s34,3\n"
SA"s89,s89,s30\n"
SA"s89,s89,0\n"
"s_lshl_b32 s91,s89,4\n"
"s_lshr_b32 s89,s89,1\n"
"v_bfe_u32 v249,v224,4,4\n"
"v_lshrrev_b32 v254,8,v224\n"
"v_lshlrev_b32 v254,2,v254\n"
"v_sub_u32 v249,v249,v254\n"
"v_add_u32 v249,s91,v249\n"
"v_mov_b32 v255,0xff800000\n"
"v_mov_b32 v248,v255\n"
"s_mov_b32 s90,0\n"
"s_mov_b32 s88,0\n"
"v_mov_b32 v80,0\n"
"v_mov_b32 v81,0\n"
"v_mov_b32 v82,0\n"
"v_mov_b32 v83,0\n"
"v_mov_b32 v84,0\n"
"v_mov_b32 v85,0\n"
"v_mov_b32 v86,0\n"
"v_mov_b32 v87,0\n"
"v_mov_b32 v88,0\n"
"v_mov_b32 v89,0\n"
"v_mov_b32 v90,0\n"
"v_mov_b32 v91,0\n"
"v_mov_b32 v92,0\n"
"v_mov_b32 v93,0\n"
"v_mov_b32 v94,0\n"
"v_mov_b32 v95,0\n"
"v_mov_b32 v96,0\n"
"v_mov_b32 v97,0\n"
"v_mov_b32 v98,0\n"
"v_mov_b32 v99,0\n"
"v_mov_b32 v100,0\n"
"v_mov_b32 v101,0\n"
"v_mov_b32 v102,0\n"
"v_mov_b32 v103,0\n"
"v_mov_b32 v104,0\n"
"v_mov_b32 v105,0\n"
"v_mov_b32 v106,0\n"
"v_mov_b32 v107,0\n"
"v_mov_b32 v108,0\n"
"v_mov_b32 v109,0\n"
"v_mov_b32 v110,0\n"
"v_mov_b32 v111,0\n"
"v_accvgpr_write_b32 a132,0\n"
"v_accvgpr_write_b32 a133,0\n"
"v_accvgpr_write_b32 a134,0\n"
"v_accvgpr_write_b32 a135,0\n"
"L1015_%=:\n"
"s_lshl_b32 s91,s88,13\n"
SA"s91,s91,s39\n"
SA"s91,s91,0\n"
BL"a[148:151],v224,s[8:11],s91 offen offset:0 sc1\n"
BL"a[152:155],v224,s[8:11],s91 offen offset:1024 sc1\n"
BL"a[156:159],v224,s[8:11],s91 offen offset:2048 sc1\n"
BL"a[160:163],v224,s[8:11],s91 offen offset:3072 sc1\n"
SA"s91,s91,4096\n"
BL"a[164:167],v224,s[8:11],s91 offen offset:0 sc1\n"
BL"a[168:171],v224,s[8:11],s91 offen offset:1024 sc1\n"
BL"a[172:175],v224,s[8:11],s91 offen offset:2048 sc1\n"
BL"a[176:179],v224,s[8:11],s91 offen offset:3072 sc1\n"
WT"vmcnt(0)\n"
MF"v[240:243],a[148:151],a[0:3],0\n"
MF"v[240:243],a[152:155],a[4:7],v[240:243]\n"
MF"v[240:243],a[156:159],a[8:11],v[240:243]\n"
MF"v[240:243],a[160:163],a[12:15],v[240:243]\n"
MF"v[244:247],a[164:167],a[0:3],0\n"
MF"v[244:247],a[168:171],a[4:7],v[244:247]\n"
MF"v[244:247],a[172:175],a[8:11],v[244:247]\n"
MF"v[244:247],a[176:179],a[12:15],v[244:247]\n"
"s_lshl_b32 s91,s88,5\n"
"v_subrev_u32 v250,s91,v249\n"
"s_nop 7\n"
"v_cmp_gt_i32 vcc,0,v250\n"
"s_nop 1\n"
"v_cndmask_b32 v240,v240,v255,vcc\n"
"v_cmp_gt_i32 vcc,1,v250\n"
"s_nop 1\n"
"v_cndmask_b32 v241,v241,v255,vcc\n"
"v_cmp_gt_i32 vcc,2,v250\n"
"s_nop 1\n"
"v_cndmask_b32 v242,v242,v255,vcc\n"
"v_cmp_gt_i32 vcc,3,v250\n"
"s_nop 1\n"
"v_cndmask_b32 v243,v243,v255,vcc\n"
"v_cmp_gt_i32 vcc,16,v250\n"
"s_nop 1\n"
"v_cndmask_b32 v244,v244,v255,vcc\n"
"v_cmp_gt_i32 vcc,17,v250\n"
"s_nop 1\n"
"v_cndmask_b32 v245,v245,v255,vcc\n"
"v_cmp_gt_i32 vcc,18,v250\n"
"s_nop 1\n"
"v_cndmask_b32 v246,v246,v255,vcc\n"
"v_cmp_gt_i32 vcc,19,v250\n"
"s_nop 1\n"
"v_cndmask_b32 v247,v247,v255,vcc\n"
"s_cmp_lg_u32 s90,0\n"
"s_cbranch_scc1 L1016_%=\n"
"v_max3_f32 v248,v248,v240,v241\n"
"v_max3_f32 v248,v248,v242,v243\n"
"v_max3_f32 v248,v248,v244,v245\n"
"v_max3_f32 v248,v248,v246,v247\n"
"s_branch L1017_%=\n"
"L1016_%=:\n"
"s_lshl_b32 s91,s88,13\n"
SA"s91,s91,s39\n"
SA"s91,s91,524288\n"
BL"a[148:151],v224,s[8:11],s91 offen offset:0 sc1\n"
BL"a[152:155],v224,s[8:11],s91 offen offset:1024 sc1\n"
BL"a[156:159],v224,s[8:11],s91 offen offset:2048 sc1\n"
BL"a[160:163],v224,s[8:11],s91 offen offset:3072 sc1\n"
SA"s91,s91,4096\n"
BL"a[164:167],v224,s[8:11],s91 offen offset:0 sc1\n"
BL"a[168:171],v224,s[8:11],s91 offen offset:1024 sc1\n"
BL"a[172:175],v224,s[8:11],s91 offen offset:2048 sc1\n"
BL"a[176:179],v224,s[8:11],s91 offen offset:3072 sc1\n"
WT"vmcnt(0)\n"
"v_sub_f32 v240,v240,v248\n"
"v_sub_f32 v241,v241,v248\n"
"v_sub_f32 v242,v242,v248\n"
"v_sub_f32 v243,v243,v248\n"
"v_sub_f32 v244,v244,v248\n"
"v_sub_f32 v245,v245,v248\n"
"v_sub_f32 v246,v246,v248\n"
"v_sub_f32 v247,v247,v248\n"
EX"v240,v240\n"
EX"v241,v241\n"
EX"v242,v242\n"
EX"v243,v243\n"
EX"v244,v244\n"
EX"v245,v245\n"
EX"v246,v246\n"
EX"v247,v247\n"
"s_nop 0\n"
PK"v240,v240,v241\n"
PK"v241,v242,v243\n"
PK"v242,v244,v245\n"
PK"v243,v246,v247\n"
"s_nop 1\n"
MF"v[80:83],a[148:151],v[240:243],v[80:83]\n"
MF"v[84:87],a[152:155],v[240:243],v[84:87]\n"
MF"v[88:91],a[156:159],v[240:243],v[88:91]\n"
MF"v[92:95],a[160:163],v[240:243],v[92:95]\n"
MF"v[96:99],a[164:167],v[240:243],v[96:99]\n"
MF"v[100:103],a[168:171],v[240:243],v[100:103]\n"
MF"v[104:107],a[172:175],v[240:243],v[104:107]\n"
MF"v[108:111],a[176:179],v[240:243],v[108:111]\n"
MF"a[132:135],a[128:131],v[240:243],a[132:135]\n"
"s_nop 7\n"
"L1017_%=:\n"
SA"s88,s88,1\n"
"s_cmp_le_u32 s88,s89\n"
"s_cbranch_scc1 L1015_%=\n"
"s_cmp_lg_u32 s90,0\n"
"s_cbranch_scc1 L1018_%=\n"
"v_mov_b32 v254,v248\n"
"s_nop 1\n"
"v_permlane16_swap_b32 v254,v248\n"
"s_nop 1\n"
"v_max_f32 v248,v248,v254\n"
"v_mov_b32 v254,v248\n"
"s_nop 1\n"
"v_permlane32_swap_b32 v254,v248\n"
"s_nop 1\n"
"v_max_f32 v248,v248,v254\n"
"s_mov_b32 s90,1\n"
"s_mov_b32 s88,0\n"
"s_branch L1015_%=\n"
"L1018_%=:\n"
"s_nop 7\n"
"v_accvgpr_read_b32 v251,a132\n"
"s_nop 1\n"
"s_branch L1013_%=\n"
"L1019_%=:\n"
"s_mov_b32 s54,0\n"
"L1021_%=:\n"
"s_sleep 2\n"
"buffer_load_dwordx2 v[238:239],v237,s[8:11],s50 offen sc1\n"
WT"vmcnt(0)\n"
SA"s54,s54,1\n"
"v_cmp_eq_u64 vcc,s[28:29],v[238:239]\n"
"s_cmp_eq_u64 vcc,exec\n"
"s_cbranch_scc1 L1020_%=\n"
"s_cmp_lt_u32 s54,1048576\n"
"s_cbranch_scc1 L1021_%=\n"
"s_branch L1020_%=\n"
"L1023_%=:\n"
"s_nop 15\n"
WT"vmcnt(0)\n"
"s_lshl_b32 s89,s34,3\n"
SA"s89,s89,s30\n"
SA"s89,s89,4\n"
"s_lshl_b32 s91,s89,4\n"
"s_lshr_b32 s89,s89,1\n"
"v_bfe_u32 v249,v224,4,4\n"
"v_lshrrev_b32 v254,8,v224\n"
"v_lshlrev_b32 v254,2,v254\n"
"v_sub_u32 v249,v249,v254\n"
"v_add_u32 v249,s91,v249\n"
"v_mov_b32 v255,0xff800000\n"
"v_mov_b32 v248,v255\n"
"s_mov_b32 s90,0\n"
"s_mov_b32 s88,0\n"
"v_mov_b32 v112,0\n"
"v_mov_b32 v113,0\n"
"v_mov_b32 v114,0\n"
"v_mov_b32 v115,0\n"
"v_mov_b32 v116,0\n"
"v_mov_b32 v117,0\n"
"v_mov_b32 v118,0\n"
"v_mov_b32 v119,0\n"
"v_mov_b32 v120,0\n"
"v_mov_b32 v121,0\n"
"v_mov_b32 v122,0\n"
"v_mov_b32 v123,0\n"
"v_mov_b32 v124,0\n"
"v_mov_b32 v125,0\n"
"v_mov_b32 v126,0\n"
"v_mov_b32 v127,0\n"
"v_mov_b32 v128,0\n"
"v_mov_b32 v129,0\n"
"v_mov_b32 v130,0\n"
"v_mov_b32 v131,0\n"
"v_mov_b32 v132,0\n"
"v_mov_b32 v133,0\n"
"v_mov_b32 v134,0\n"
"v_mov_b32 v135,0\n"
"v_mov_b32 v136,0\n"
"v_mov_b32 v137,0\n"
"v_mov_b32 v138,0\n"
"v_mov_b32 v139,0\n"
"v_mov_b32 v140,0\n"
"v_mov_b32 v141,0\n"
"v_mov_b32 v142,0\n"
"v_mov_b32 v143,0\n"
"v_accvgpr_write_b32 a136,0\n"
"v_accvgpr_write_b32 a137,0\n"
"v_accvgpr_write_b32 a138,0\n"
"v_accvgpr_write_b32 a139,0\n"
"L1024_%=:\n"
"s_lshl_b32 s91,s88,13\n"
SA"s91,s91,s39\n"
SA"s91,s91,0\n"
BL"a[148:151],v224,s[8:11],s91 offen offset:0 sc1\n"
BL"a[152:155],v224,s[8:11],s91 offen offset:1024 sc1\n"
BL"a[156:159],v224,s[8:11],s91 offen offset:2048 sc1\n"
BL"a[160:163],v224,s[8:11],s91 offen offset:3072 sc1\n"
SA"s91,s91,4096\n"
BL"a[164:167],v224,s[8:11],s91 offen offset:0 sc1\n"
BL"a[168:171],v224,s[8:11],s91 offen offset:1024 sc1\n"
BL"a[172:175],v224,s[8:11],s91 offen offset:2048 sc1\n"
BL"a[176:179],v224,s[8:11],s91 offen offset:3072 sc1\n"
WT"vmcnt(0)\n"
MF"v[240:243],a[148:151],a[16:19],0\n"
MF"v[240:243],a[152:155],a[20:23],v[240:243]\n"
MF"v[240:243],a[156:159],a[24:27],v[240:243]\n"
MF"v[240:243],a[160:163],a[28:31],v[240:243]\n"
MF"v[244:247],a[164:167],a[16:19],0\n"
MF"v[244:247],a[168:171],a[20:23],v[244:247]\n"
MF"v[244:247],a[172:175],a[24:27],v[244:247]\n"
MF"v[244:247],a[176:179],a[28:31],v[244:247]\n"
"s_lshl_b32 s91,s88,5\n"
"v_subrev_u32 v250,s91,v249\n"
"s_nop 7\n"
"v_cmp_gt_i32 vcc,0,v250\n"
"s_nop 1\n"
"v_cndmask_b32 v240,v240,v255,vcc\n"
"v_cmp_gt_i32 vcc,1,v250\n"
"s_nop 1\n"
"v_cndmask_b32 v241,v241,v255,vcc\n"
"v_cmp_gt_i32 vcc,2,v250\n"
"s_nop 1\n"
"v_cndmask_b32 v242,v242,v255,vcc\n"
"v_cmp_gt_i32 vcc,3,v250\n"
"s_nop 1\n"
"v_cndmask_b32 v243,v243,v255,vcc\n"
"v_cmp_gt_i32 vcc,16,v250\n"
"s_nop 1\n"
"v_cndmask_b32 v244,v244,v255,vcc\n"
"v_cmp_gt_i32 vcc,17,v250\n"
"s_nop 1\n"
"v_cndmask_b32 v245,v245,v255,vcc\n"
"v_cmp_gt_i32 vcc,18,v250\n"
"s_nop 1\n"
"v_cndmask_b32 v246,v246,v255,vcc\n"
"v_cmp_gt_i32 vcc,19,v250\n"
"s_nop 1\n"
"v_cndmask_b32 v247,v247,v255,vcc\n"
"s_cmp_lg_u32 s90,0\n"
"s_cbranch_scc1 L1025_%=\n"
"v_max3_f32 v248,v248,v240,v241\n"
"v_max3_f32 v248,v248,v242,v243\n"
"v_max3_f32 v248,v248,v244,v245\n"
"v_max3_f32 v248,v248,v246,v247\n"
"s_branch L1026_%=\n"
"L1025_%=:\n"
"s_lshl_b32 s91,s88,13\n"
SA"s91,s91,s39\n"
SA"s91,s91,524288\n"
BL"a[148:151],v224,s[8:11],s91 offen offset:0 sc1\n"
BL"a[152:155],v224,s[8:11],s91 offen offset:1024 sc1\n"
BL"a[156:159],v224,s[8:11],s91 offen offset:2048 sc1\n"
BL"a[160:163],v224,s[8:11],s91 offen offset:3072 sc1\n"
SA"s91,s91,4096\n"
BL"a[164:167],v224,s[8:11],s91 offen offset:0 sc1\n"
BL"a[168:171],v224,s[8:11],s91 offen offset:1024 sc1\n"
BL"a[172:175],v224,s[8:11],s91 offen offset:2048 sc1\n"
BL"a[176:179],v224,s[8:11],s91 offen offset:3072 sc1\n"
WT"vmcnt(0)\n"
"v_sub_f32 v240,v240,v248\n"
"v_sub_f32 v241,v241,v248\n"
"v_sub_f32 v242,v242,v248\n"
"v_sub_f32 v243,v243,v248\n"
"v_sub_f32 v244,v244,v248\n"
"v_sub_f32 v245,v245,v248\n"
"v_sub_f32 v246,v246,v248\n"
"v_sub_f32 v247,v247,v248\n"
EX"v240,v240\n"
EX"v241,v241\n"
EX"v242,v242\n"
EX"v243,v243\n"
EX"v244,v244\n"
EX"v245,v245\n"
EX"v246,v246\n"
EX"v247,v247\n"
"s_nop 0\n"
PK"v240,v240,v241\n"
PK"v241,v242,v243\n"
PK"v242,v244,v245\n"
PK"v243,v246,v247\n"
"s_nop 1\n"
MF"v[112:115],a[148:151],v[240:243],v[112:115]\n"
MF"v[116:119],a[152:155],v[240:243],v[116:119]\n"
MF"v[120:123],a[156:159],v[240:243],v[120:123]\n"
MF"v[124:127],a[160:163],v[240:243],v[124:127]\n"
MF"v[128:131],a[164:167],v[240:243],v[128:131]\n"
MF"v[132:135],a[168:171],v[240:243],v[132:135]\n"
MF"v[136:139],a[172:175],v[240:243],v[136:139]\n"
MF"v[140:143],a[176:179],v[240:243],v[140:143]\n"
MF"a[136:139],a[128:131],v[240:243],a[136:139]\n"
"s_nop 7\n"
"L1026_%=:\n"
SA"s88,s88,1\n"
"s_cmp_le_u32 s88,s89\n"
"s_cbranch_scc1 L1024_%=\n"
"s_cmp_lg_u32 s90,0\n"
"s_cbranch_scc1 L1027_%=\n"
"v_mov_b32 v254,v248\n"
"s_nop 1\n"
"v_permlane16_swap_b32 v254,v248\n"
"s_nop 1\n"
"v_max_f32 v248,v248,v254\n"
"v_mov_b32 v254,v248\n"
"s_nop 1\n"
"v_permlane32_swap_b32 v254,v248\n"
"s_nop 1\n"
"v_max_f32 v248,v248,v254\n"
"s_mov_b32 s90,1\n"
"s_mov_b32 s88,0\n"
"s_branch L1024_%=\n"
"L1027_%=:\n"
"s_nop 7\n"
"v_accvgpr_read_b32 v252,a136\n"
"s_nop 1\n"
"s_branch L1022_%=\n"
"L1028_%=:\n"
"s_mov_b32 s54,0\n"
"L1030_%=:\n"
"s_sleep 2\n"
"buffer_load_dwordx2 v[242:243],v236,s[8:11],0 offen sc1\n"
WT"vmcnt(0)\n"
SA"s54,s54,1\n"
"v_cmp_eq_u64 vcc,s[28:29],v[242:243]\n"
"s_or_b32 vcc_hi,vcc_hi,-1\n"
"s_cmp_eq_u64 vcc,exec\n"
"s_cbranch_scc1 L1029_%=\n"
"s_cmp_lt_u32 s54,1048576\n"
"s_cbranch_scc1 L1030_%=\n"
"s_branch L1029_%=\n"
"L1031_%=:\n"
"s_mov_b32 s54,0\n"
"L1033_%=:\n"
"s_sleep 2\n"
"buffer_load_dwordx2 v[242:243],v236,s[8:11],0 offen sc1\n"
WT"vmcnt(0)\n"
SA"s54,s54,1\n"
"v_cmp_eq_u64 vcc,s[28:29],v[242:243]\n"
"s_or_b32 vcc_lo,vcc_lo,-1\n"
"s_cmp_eq_u64 vcc,exec\n"
"s_cbranch_scc1 L1032_%=\n"
"s_cmp_lt_u32 s54,1048576\n"
"s_cbranch_scc1 L1033_%=\n"
"s_branch L1032_%=\n"
"L1035_%=:\n"
"s_nop 15\n"
WT"vmcnt(0)\n"
"s_lshl_b32 s89,s34,3\n"
SA"s89,s89,s30\n"
SA"s89,s89,4\n"
"s_lshl_b32 s91,s89,4\n"
"s_lshr_b32 s89,s89,1\n"
"v_bfe_u32 v249,v224,4,4\n"
"v_lshrrev_b32 v254,8,v224\n"
"v_lshlrev_b32 v254,2,v254\n"
"v_sub_u32 v249,v249,v254\n"
"v_add_u32 v249,s91,v249\n"
"v_mov_b32 v255,0xff800000\n"
"v_mov_b32 v248,v255\n"
"s_mov_b32 s90,0\n"
"s_mov_b32 s88,0\n"
"v_mov_b32 v112,0\n"
"v_mov_b32 v113,0\n"
"v_mov_b32 v114,0\n"
"v_mov_b32 v115,0\n"
"v_mov_b32 v116,0\n"
"v_mov_b32 v117,0\n"
"v_mov_b32 v118,0\n"
"v_mov_b32 v119,0\n"
"v_mov_b32 v120,0\n"
"v_mov_b32 v121,0\n"
"v_mov_b32 v122,0\n"
"v_mov_b32 v123,0\n"
"v_mov_b32 v124,0\n"
"v_mov_b32 v125,0\n"
"v_mov_b32 v126,0\n"
"v_mov_b32 v127,0\n"
"v_mov_b32 v128,0\n"
"v_mov_b32 v129,0\n"
"v_mov_b32 v130,0\n"
"v_mov_b32 v131,0\n"
"v_mov_b32 v132,0\n"
"v_mov_b32 v133,0\n"
"v_mov_b32 v134,0\n"
"v_mov_b32 v135,0\n"
"v_mov_b32 v136,0\n"
"v_mov_b32 v137,0\n"
"v_mov_b32 v138,0\n"
"v_mov_b32 v139,0\n"
"v_mov_b32 v140,0\n"
"v_mov_b32 v141,0\n"
"v_mov_b32 v142,0\n"
"v_mov_b32 v143,0\n"
"v_accvgpr_write_b32 a136,0\n"
"v_accvgpr_write_b32 a137,0\n"
"v_accvgpr_write_b32 a138,0\n"
"v_accvgpr_write_b32 a139,0\n"
"L1036_%=:\n"
"s_lshl_b32 s91,s88,13\n"
SA"s91,s91,s39\n"
SA"s91,s91,0\n"
BL"a[148:151],v224,s[8:11],s91 offen offset:0 sc1\n"
BL"a[152:155],v224,s[8:11],s91 offen offset:1024 sc1\n"
BL"a[156:159],v224,s[8:11],s91 offen offset:2048 sc1\n"
BL"a[160:163],v224,s[8:11],s91 offen offset:3072 sc1\n"
SA"s91,s91,4096\n"
BL"a[164:167],v224,s[8:11],s91 offen offset:0 sc1\n"
BL"a[168:171],v224,s[8:11],s91 offen offset:1024 sc1\n"
BL"a[172:175],v224,s[8:11],s91 offen offset:2048 sc1\n"
BL"a[176:179],v224,s[8:11],s91 offen offset:3072 sc1\n"
WT"vmcnt(0)\n"
MF"v[240:243],a[148:151],a[16:19],0\n"
MF"v[240:243],a[152:155],a[20:23],v[240:243]\n"
MF"v[240:243],a[156:159],a[24:27],v[240:243]\n"
MF"v[240:243],a[160:163],a[28:31],v[240:243]\n"
MF"v[244:247],a[164:167],a[16:19],0\n"
MF"v[244:247],a[168:171],a[20:23],v[244:247]\n"
MF"v[244:247],a[172:175],a[24:27],v[244:247]\n"
MF"v[244:247],a[176:179],a[28:31],v[244:247]\n"
"s_lshl_b32 s91,s88,5\n"
"v_subrev_u32 v250,s91,v249\n"
"s_nop 7\n"
"v_cmp_gt_i32 vcc,0,v250\n"
"s_nop 1\n"
"v_cndmask_b32 v240,v240,v255,vcc\n"
"v_cmp_gt_i32 vcc,1,v250\n"
"s_nop 1\n"
"v_cndmask_b32 v241,v241,v255,vcc\n"
"v_cmp_gt_i32 vcc,2,v250\n"
"s_nop 1\n"
"v_cndmask_b32 v242,v242,v255,vcc\n"
"v_cmp_gt_i32 vcc,3,v250\n"
"s_nop 1\n"
"v_cndmask_b32 v243,v243,v255,vcc\n"
"v_cmp_gt_i32 vcc,16,v250\n"
"s_nop 1\n"
"v_cndmask_b32 v244,v244,v255,vcc\n"
"v_cmp_gt_i32 vcc,17,v250\n"
"s_nop 1\n"
"v_cndmask_b32 v245,v245,v255,vcc\n"
"v_cmp_gt_i32 vcc,18,v250\n"
"s_nop 1\n"
"v_cndmask_b32 v246,v246,v255,vcc\n"
"v_cmp_gt_i32 vcc,19,v250\n"
"s_nop 1\n"
"v_cndmask_b32 v247,v247,v255,vcc\n"
"s_cmp_lg_u32 s90,0\n"
"s_cbranch_scc1 L1037_%=\n"
"v_max3_f32 v248,v248,v240,v241\n"
"v_max3_f32 v248,v248,v242,v243\n"
"v_max3_f32 v248,v248,v244,v245\n"
"v_max3_f32 v248,v248,v246,v247\n"
"s_branch L1038_%=\n"
"L1037_%=:\n"
"s_lshl_b32 s91,s88,13\n"
SA"s91,s91,s39\n"
SA"s91,s91,524288\n"
BL"a[148:151],v224,s[8:11],s91 offen offset:0 sc1\n"
BL"a[152:155],v224,s[8:11],s91 offen offset:1024 sc1\n"
BL"a[156:159],v224,s[8:11],s91 offen offset:2048 sc1\n"
BL"a[160:163],v224,s[8:11],s91 offen offset:3072 sc1\n"
SA"s91,s91,4096\n"
BL"a[164:167],v224,s[8:11],s91 offen offset:0 sc1\n"
BL"a[168:171],v224,s[8:11],s91 offen offset:1024 sc1\n"
BL"a[172:175],v224,s[8:11],s91 offen offset:2048 sc1\n"
BL"a[176:179],v224,s[8:11],s91 offen offset:3072 sc1\n"
WT"vmcnt(0)\n"
"v_sub_f32 v240,v240,v248\n"
"v_sub_f32 v241,v241,v248\n"
"v_sub_f32 v242,v242,v248\n"
"v_sub_f32 v243,v243,v248\n"
"v_sub_f32 v244,v244,v248\n"
"v_sub_f32 v245,v245,v248\n"
"v_sub_f32 v246,v246,v248\n"
"v_sub_f32 v247,v247,v248\n"
EX"v240,v240\n"
EX"v241,v241\n"
EX"v242,v242\n"
EX"v243,v243\n"
EX"v244,v244\n"
EX"v245,v245\n"
EX"v246,v246\n"
EX"v247,v247\n"
"s_nop 0\n"
PK"v240,v240,v241\n"
PK"v241,v242,v243\n"
PK"v242,v244,v245\n"
PK"v243,v246,v247\n"
"s_nop 1\n"
MF"v[112:115],a[148:151],v[240:243],v[112:115]\n"
MF"v[116:119],a[152:155],v[240:243],v[116:119]\n"
MF"v[120:123],a[156:159],v[240:243],v[120:123]\n"
MF"v[124:127],a[160:163],v[240:243],v[124:127]\n"
MF"v[128:131],a[164:167],v[240:243],v[128:131]\n"
MF"v[132:135],a[168:171],v[240:243],v[132:135]\n"
MF"v[136:139],a[172:175],v[240:243],v[136:139]\n"
MF"v[140:143],a[176:179],v[240:243],v[140:143]\n"
MF"a[136:139],a[128:131],v[240:243],a[136:139]\n"
"s_nop 7\n"
"L1038_%=:\n"
SA"s88,s88,1\n"
"s_cmp_le_u32 s88,s89\n"
"s_cbranch_scc1 L1036_%=\n"
"s_cmp_lg_u32 s90,0\n"
"s_cbranch_scc1 L1039_%=\n"
"v_mov_b32 v254,v248\n"
"s_nop 1\n"
"v_permlane16_swap_b32 v254,v248\n"
"s_nop 1\n"
"v_max_f32 v248,v248,v254\n"
"v_mov_b32 v254,v248\n"
"s_nop 1\n"
"v_permlane32_swap_b32 v254,v248\n"
"s_nop 1\n"
"v_max_f32 v248,v248,v254\n"
"s_mov_b32 s90,1\n"
"s_mov_b32 s88,0\n"
"s_branch L1036_%=\n"
"L1039_%=:\n"
"s_nop 7\n"
"v_accvgpr_read_b32 v252,a136\n"
"s_nop 1\n"
"s_branch L1034_%=\n"
"L1041_%=:\n"
"s_nop 15\n"
WT"vmcnt(0)\n"
"s_lshl_b32 s89,s34,3\n"
"s_sub_u32 s89,120,s89\n"
SA"s89,s89,s30\n"
SA"s89,s89,0\n"
"s_lshl_b32 s91,s89,4\n"
"s_lshr_b32 s89,s89,1\n"
"v_bfe_u32 v249,v224,4,4\n"
"v_lshrrev_b32 v254,8,v224\n"
"v_lshlrev_b32 v254,2,v254\n"
"v_sub_u32 v249,v249,v254\n"
"v_add_u32 v249,s91,v249\n"
"v_mov_b32 v255,0xff800000\n"
"v_mov_b32 v248,v255\n"
"s_mov_b32 s90,0\n"
"s_mov_b32 s88,0\n"
"v_mov_b32 v144,0\n"
"v_mov_b32 v145,0\n"
"v_mov_b32 v146,0\n"
"v_mov_b32 v147,0\n"
"v_mov_b32 v148,0\n"
"v_mov_b32 v149,0\n"
"v_mov_b32 v150,0\n"
"v_mov_b32 v151,0\n"
"v_mov_b32 v152,0\n"
"v_mov_b32 v153,0\n"
"v_mov_b32 v154,0\n"
"v_mov_b32 v155,0\n"
"v_mov_b32 v156,0\n"
"v_mov_b32 v157,0\n"
"v_mov_b32 v158,0\n"
"v_mov_b32 v159,0\n"
"v_mov_b32 v160,0\n"
"v_mov_b32 v161,0\n"
"v_mov_b32 v162,0\n"
"v_mov_b32 v163,0\n"
"v_mov_b32 v164,0\n"
"v_mov_b32 v165,0\n"
"v_mov_b32 v166,0\n"
"v_mov_b32 v167,0\n"
"v_mov_b32 v168,0\n"
"v_mov_b32 v169,0\n"
"v_mov_b32 v170,0\n"
"v_mov_b32 v171,0\n"
"v_mov_b32 v172,0\n"
"v_mov_b32 v173,0\n"
"v_mov_b32 v174,0\n"
"v_mov_b32 v175,0\n"
"v_accvgpr_write_b32 a140,0\n"
"v_accvgpr_write_b32 a141,0\n"
"v_accvgpr_write_b32 a142,0\n"
"v_accvgpr_write_b32 a143,0\n"
"L1042_%=:\n"
"s_lshl_b32 s91,s88,13\n"
SA"s91,s91,s39\n"
SA"s91,s91,0\n"
BL"a[148:151],v224,s[8:11],s91 offen offset:0 sc1\n"
BL"a[152:155],v224,s[8:11],s91 offen offset:1024 sc1\n"
BL"a[156:159],v224,s[8:11],s91 offen offset:2048 sc1\n"
BL"a[160:163],v224,s[8:11],s91 offen offset:3072 sc1\n"
SA"s91,s91,4096\n"
BL"a[164:167],v224,s[8:11],s91 offen offset:0 sc1\n"
BL"a[168:171],v224,s[8:11],s91 offen offset:1024 sc1\n"
BL"a[172:175],v224,s[8:11],s91 offen offset:2048 sc1\n"
BL"a[176:179],v224,s[8:11],s91 offen offset:3072 sc1\n"
WT"vmcnt(0)\n"
MF"v[240:243],a[148:151],a[32:35],0\n"
MF"v[240:243],a[152:155],a[36:39],v[240:243]\n"
MF"v[240:243],a[156:159],a[40:43],v[240:243]\n"
MF"v[240:243],a[160:163],a[44:47],v[240:243]\n"
MF"v[244:247],a[164:167],a[32:35],0\n"
MF"v[244:247],a[168:171],a[36:39],v[244:247]\n"
MF"v[244:247],a[172:175],a[40:43],v[244:247]\n"
MF"v[244:247],a[176:179],a[44:47],v[244:247]\n"
"s_lshl_b32 s91,s88,5\n"
"v_subrev_u32 v250,s91,v249\n"
"s_nop 7\n"
"v_cmp_gt_i32 vcc,0,v250\n"
"s_nop 1\n"
"v_cndmask_b32 v240,v240,v255,vcc\n"
"v_cmp_gt_i32 vcc,1,v250\n"
"s_nop 1\n"
"v_cndmask_b32 v241,v241,v255,vcc\n"
"v_cmp_gt_i32 vcc,2,v250\n"
"s_nop 1\n"
"v_cndmask_b32 v242,v242,v255,vcc\n"
"v_cmp_gt_i32 vcc,3,v250\n"
"s_nop 1\n"
"v_cndmask_b32 v243,v243,v255,vcc\n"
"v_cmp_gt_i32 vcc,16,v250\n"
"s_nop 1\n"
"v_cndmask_b32 v244,v244,v255,vcc\n"
"v_cmp_gt_i32 vcc,17,v250\n"
"s_nop 1\n"
"v_cndmask_b32 v245,v245,v255,vcc\n"
"v_cmp_gt_i32 vcc,18,v250\n"
"s_nop 1\n"
"v_cndmask_b32 v246,v246,v255,vcc\n"
"v_cmp_gt_i32 vcc,19,v250\n"
"s_nop 1\n"
"v_cndmask_b32 v247,v247,v255,vcc\n"
"s_cmp_lg_u32 s90,0\n"
"s_cbranch_scc1 L1043_%=\n"
"v_max3_f32 v248,v248,v240,v241\n"
"v_max3_f32 v248,v248,v242,v243\n"
"v_max3_f32 v248,v248,v244,v245\n"
"v_max3_f32 v248,v248,v246,v247\n"
"s_branch L1044_%=\n"
"L1043_%=:\n"
"s_lshl_b32 s91,s88,13\n"
SA"s91,s91,s39\n"
SA"s91,s91,524288\n"
BL"a[148:151],v224,s[8:11],s91 offen offset:0 sc1\n"
BL"a[152:155],v224,s[8:11],s91 offen offset:1024 sc1\n"
BL"a[156:159],v224,s[8:11],s91 offen offset:2048 sc1\n"
BL"a[160:163],v224,s[8:11],s91 offen offset:3072 sc1\n"
SA"s91,s91,4096\n"
BL"a[164:167],v224,s[8:11],s91 offen offset:0 sc1\n"
BL"a[168:171],v224,s[8:11],s91 offen offset:1024 sc1\n"
BL"a[172:175],v224,s[8:11],s91 offen offset:2048 sc1\n"
BL"a[176:179],v224,s[8:11],s91 offen offset:3072 sc1\n"
WT"vmcnt(0)\n"
"v_sub_f32 v240,v240,v248\n"
"v_sub_f32 v241,v241,v248\n"
"v_sub_f32 v242,v242,v248\n"
"v_sub_f32 v243,v243,v248\n"
"v_sub_f32 v244,v244,v248\n"
"v_sub_f32 v245,v245,v248\n"
"v_sub_f32 v246,v246,v248\n"
"v_sub_f32 v247,v247,v248\n"
EX"v240,v240\n"
EX"v241,v241\n"
EX"v242,v242\n"
EX"v243,v243\n"
EX"v244,v244\n"
EX"v245,v245\n"
EX"v246,v246\n"
EX"v247,v247\n"
"s_nop 0\n"
PK"v240,v240,v241\n"
PK"v241,v242,v243\n"
PK"v242,v244,v245\n"
PK"v243,v246,v247\n"
"s_nop 1\n"
MF"v[144:147],a[148:151],v[240:243],v[144:147]\n"
MF"v[148:151],a[152:155],v[240:243],v[148:151]\n"
MF"v[152:155],a[156:159],v[240:243],v[152:155]\n"
MF"v[156:159],a[160:163],v[240:243],v[156:159]\n"
MF"v[160:163],a[164:167],v[240:243],v[160:163]\n"
MF"v[164:167],a[168:171],v[240:243],v[164:167]\n"
MF"v[168:171],a[172:175],v[240:243],v[168:171]\n"
MF"v[172:175],a[176:179],v[240:243],v[172:175]\n"
MF"a[140:143],a[128:131],v[240:243],a[140:143]\n"
"s_nop 7\n"
"L1044_%=:\n"
SA"s88,s88,1\n"
"s_cmp_le_u32 s88,s89\n"
"s_cbranch_scc1 L1042_%=\n"
"s_cmp_lg_u32 s90,0\n"
"s_cbranch_scc1 L1045_%=\n"
"v_mov_b32 v254,v248\n"
"s_nop 1\n"
"v_permlane16_swap_b32 v254,v248\n"
"s_nop 1\n"
"v_max_f32 v248,v248,v254\n"
"v_mov_b32 v254,v248\n"
"s_nop 1\n"
"v_permlane32_swap_b32 v254,v248\n"
"s_nop 1\n"
"v_max_f32 v248,v248,v254\n"
"s_mov_b32 s90,1\n"
"s_mov_b32 s88,0\n"
"s_branch L1042_%=\n"
"L1045_%=:\n"
"s_nop 7\n"
"v_accvgpr_read_b32 v251,a140\n"
"s_nop 1\n"
"s_branch L1040_%=\n"
"L1047_%=:\n"
"s_nop 15\n"
WT"vmcnt(0)\n"
"s_lshl_b32 s89,s34,3\n"
"s_sub_u32 s89,120,s89\n"
SA"s89,s89,s30\n"
SA"s89,s89,4\n"
"s_lshl_b32 s91,s89,4\n"
"s_lshr_b32 s89,s89,1\n"
"v_bfe_u32 v249,v224,4,4\n"
"v_lshrrev_b32 v254,8,v224\n"
"v_lshlrev_b32 v254,2,v254\n"
"v_sub_u32 v249,v249,v254\n"
"v_add_u32 v249,s91,v249\n"
"v_mov_b32 v255,0xff800000\n"
"v_mov_b32 v248,v255\n"
"s_mov_b32 s90,0\n"
"s_mov_b32 s88,0\n"
"v_mov_b32 v176,0\n"
"v_mov_b32 v177,0\n"
"v_mov_b32 v178,0\n"
"v_mov_b32 v179,0\n"
"v_mov_b32 v180,0\n"
"v_mov_b32 v181,0\n"
"v_mov_b32 v182,0\n"
"v_mov_b32 v183,0\n"
"v_mov_b32 v184,0\n"
"v_mov_b32 v185,0\n"
"v_mov_b32 v186,0\n"
"v_mov_b32 v187,0\n"
"v_mov_b32 v188,0\n"
"v_mov_b32 v189,0\n"
"v_mov_b32 v190,0\n"
"v_mov_b32 v191,0\n"
"v_mov_b32 v192,0\n"
"v_mov_b32 v193,0\n"
"v_mov_b32 v194,0\n"
"v_mov_b32 v195,0\n"
"v_mov_b32 v196,0\n"
"v_mov_b32 v197,0\n"
"v_mov_b32 v198,0\n"
"v_mov_b32 v199,0\n"
"v_mov_b32 v200,0\n"
"v_mov_b32 v201,0\n"
"v_mov_b32 v202,0\n"
"v_mov_b32 v203,0\n"
"v_mov_b32 v204,0\n"
"v_mov_b32 v205,0\n"
"v_mov_b32 v206,0\n"
"v_mov_b32 v207,0\n"
"v_accvgpr_write_b32 a144,0\n"
"v_accvgpr_write_b32 a145,0\n"
"v_accvgpr_write_b32 a146,0\n"
"v_accvgpr_write_b32 a147,0\n"
"L1048_%=:\n"
"s_lshl_b32 s91,s88,13\n"
SA"s91,s91,s39\n"
SA"s91,s91,0\n"
BL"a[148:151],v224,s[8:11],s91 offen offset:0 sc1\n"
BL"a[152:155],v224,s[8:11],s91 offen offset:1024 sc1\n"
BL"a[156:159],v224,s[8:11],s91 offen offset:2048 sc1\n"
BL"a[160:163],v224,s[8:11],s91 offen offset:3072 sc1\n"
SA"s91,s91,4096\n"
BL"a[164:167],v224,s[8:11],s91 offen offset:0 sc1\n"
BL"a[168:171],v224,s[8:11],s91 offen offset:1024 sc1\n"
BL"a[172:175],v224,s[8:11],s91 offen offset:2048 sc1\n"
BL"a[176:179],v224,s[8:11],s91 offen offset:3072 sc1\n"
WT"vmcnt(0)\n"
MF"v[240:243],a[148:151],a[48:51],0\n"
MF"v[240:243],a[152:155],a[52:55],v[240:243]\n"
MF"v[240:243],a[156:159],a[56:59],v[240:243]\n"
MF"v[240:243],a[160:163],a[60:63],v[240:243]\n"
MF"v[244:247],a[164:167],a[48:51],0\n"
MF"v[244:247],a[168:171],a[52:55],v[244:247]\n"
MF"v[244:247],a[172:175],a[56:59],v[244:247]\n"
MF"v[244:247],a[176:179],a[60:63],v[244:247]\n"
"s_lshl_b32 s91,s88,5\n"
"v_subrev_u32 v250,s91,v249\n"
"s_nop 7\n"
"v_cmp_gt_i32 vcc,0,v250\n"
"s_nop 1\n"
"v_cndmask_b32 v240,v240,v255,vcc\n"
"v_cmp_gt_i32 vcc,1,v250\n"
"s_nop 1\n"
"v_cndmask_b32 v241,v241,v255,vcc\n"
"v_cmp_gt_i32 vcc,2,v250\n"
"s_nop 1\n"
"v_cndmask_b32 v242,v242,v255,vcc\n"
"v_cmp_gt_i32 vcc,3,v250\n"
"s_nop 1\n"
"v_cndmask_b32 v243,v243,v255,vcc\n"
"v_cmp_gt_i32 vcc,16,v250\n"
"s_nop 1\n"
"v_cndmask_b32 v244,v244,v255,vcc\n"
"v_cmp_gt_i32 vcc,17,v250\n"
"s_nop 1\n"
"v_cndmask_b32 v245,v245,v255,vcc\n"
"v_cmp_gt_i32 vcc,18,v250\n"
"s_nop 1\n"
"v_cndmask_b32 v246,v246,v255,vcc\n"
"v_cmp_gt_i32 vcc,19,v250\n"
"s_nop 1\n"
"v_cndmask_b32 v247,v247,v255,vcc\n"
"s_cmp_lg_u32 s90,0\n"
"s_cbranch_scc1 L1049_%=\n"
"v_max3_f32 v248,v248,v240,v241\n"
"v_max3_f32 v248,v248,v242,v243\n"
"v_max3_f32 v248,v248,v244,v245\n"
"v_max3_f32 v248,v248,v246,v247\n"
"s_branch L1050_%=\n"
"L1049_%=:\n"
"s_lshl_b32 s91,s88,13\n"
SA"s91,s91,s39\n"
SA"s91,s91,524288\n"
BL"a[148:151],v224,s[8:11],s91 offen offset:0 sc1\n"
BL"a[152:155],v224,s[8:11],s91 offen offset:1024 sc1\n"
BL"a[156:159],v224,s[8:11],s91 offen offset:2048 sc1\n"
BL"a[160:163],v224,s[8:11],s91 offen offset:3072 sc1\n"
SA"s91,s91,4096\n"
BL"a[164:167],v224,s[8:11],s91 offen offset:0 sc1\n"
BL"a[168:171],v224,s[8:11],s91 offen offset:1024 sc1\n"
BL"a[172:175],v224,s[8:11],s91 offen offset:2048 sc1\n"
BL"a[176:179],v224,s[8:11],s91 offen offset:3072 sc1\n"
WT"vmcnt(0)\n"
"v_sub_f32 v240,v240,v248\n"
"v_sub_f32 v241,v241,v248\n"
"v_sub_f32 v242,v242,v248\n"
"v_sub_f32 v243,v243,v248\n"
"v_sub_f32 v244,v244,v248\n"
"v_sub_f32 v245,v245,v248\n"
"v_sub_f32 v246,v246,v248\n"
"v_sub_f32 v247,v247,v248\n"
EX"v240,v240\n"
EX"v241,v241\n"
EX"v242,v242\n"
EX"v243,v243\n"
EX"v244,v244\n"
EX"v245,v245\n"
EX"v246,v246\n"
EX"v247,v247\n"
"s_nop 0\n"
PK"v240,v240,v241\n"
PK"v241,v242,v243\n"
PK"v242,v244,v245\n"
PK"v243,v246,v247\n"
"s_nop 1\n"
MF"v[176:179],a[148:151],v[240:243],v[176:179]\n"
MF"v[180:183],a[152:155],v[240:243],v[180:183]\n"
MF"v[184:187],a[156:159],v[240:243],v[184:187]\n"
MF"v[188:191],a[160:163],v[240:243],v[188:191]\n"
MF"v[192:195],a[164:167],v[240:243],v[192:195]\n"
MF"v[196:199],a[168:171],v[240:243],v[196:199]\n"
MF"v[200:203],a[172:175],v[240:243],v[200:203]\n"
MF"v[204:207],a[176:179],v[240:243],v[204:207]\n"
MF"a[144:147],a[128:131],v[240:243],a[144:147]\n"
"s_nop 7\n"
"L1050_%=:\n"
SA"s88,s88,1\n"
"s_cmp_le_u32 s88,s89\n"
"s_cbranch_scc1 L1048_%=\n"
"s_cmp_lg_u32 s90,0\n"
"s_cbranch_scc1 L1051_%=\n"
"v_mov_b32 v254,v248\n"
"s_nop 1\n"
"v_permlane16_swap_b32 v254,v248\n"
"s_nop 1\n"
"v_max_f32 v248,v248,v254\n"
"v_mov_b32 v254,v248\n"
"s_nop 1\n"
"v_permlane32_swap_b32 v254,v248\n"
"s_nop 1\n"
"v_max_f32 v248,v248,v254\n"
"s_mov_b32 s90,1\n"
"s_mov_b32 s88,0\n"
"s_branch L1048_%=\n"
"L1051_%=:\n"
"s_nop 7\n"
"v_accvgpr_read_b32 v252,a144\n"
"s_nop 1\n"
"s_branch L1046_%=\n"
"L1069_%=:\n"
"s_nop 15\n"
WT"vmcnt(0)\n"
"s_lshl_b32 s89,s34,3\n"
"s_sub_u32 s89,120,s89\n"
SA"s89,s89,s30\n"
SA"s89,s89,0\n"
"s_lshl_b32 s91,s89,4\n"
"s_lshr_b32 s89,s89,1\n"
"v_bfe_u32 v249,v224,4,4\n"
"v_lshrrev_b32 v254,8,v224\n"
"v_lshlrev_b32 v254,2,v254\n"
"v_sub_u32 v249,v249,v254\n"
"v_add_u32 v249,s91,v249\n"
"v_mov_b32 v255,0xff800000\n"
"v_mov_b32 v248,v255\n"
"s_mov_b32 s90,0\n"
"s_mov_b32 s88,0\n"
"v_mov_b32 v144,0\n"
"v_mov_b32 v145,0\n"
"v_mov_b32 v146,0\n"
"v_mov_b32 v147,0\n"
"v_mov_b32 v148,0\n"
"v_mov_b32 v149,0\n"
"v_mov_b32 v150,0\n"
"v_mov_b32 v151,0\n"
"v_mov_b32 v152,0\n"
"v_mov_b32 v153,0\n"
"v_mov_b32 v154,0\n"
"v_mov_b32 v155,0\n"
"v_mov_b32 v156,0\n"
"v_mov_b32 v157,0\n"
"v_mov_b32 v158,0\n"
"v_mov_b32 v159,0\n"
"v_mov_b32 v160,0\n"
"v_mov_b32 v161,0\n"
"v_mov_b32 v162,0\n"
"v_mov_b32 v163,0\n"
"v_mov_b32 v164,0\n"
"v_mov_b32 v165,0\n"
"v_mov_b32 v166,0\n"
"v_mov_b32 v167,0\n"
"v_mov_b32 v168,0\n"
"v_mov_b32 v169,0\n"
"v_mov_b32 v170,0\n"
"v_mov_b32 v171,0\n"
"v_mov_b32 v172,0\n"
"v_mov_b32 v173,0\n"
"v_mov_b32 v174,0\n"
"v_mov_b32 v175,0\n"
"v_accvgpr_write_b32 a140,0\n"
"v_accvgpr_write_b32 a141,0\n"
"v_accvgpr_write_b32 a142,0\n"
"v_accvgpr_write_b32 a143,0\n"
"L1070_%=:\n"
"s_lshl_b32 s91,s88,13\n"
SA"s91,s91,s39\n"
SA"s91,s91,0\n"
BL"a[148:151],v224,s[8:11],s91 offen offset:0 sc1\n"
BL"a[152:155],v224,s[8:11],s91 offen offset:1024 sc1\n"
BL"a[156:159],v224,s[8:11],s91 offen offset:2048 sc1\n"
BL"a[160:163],v224,s[8:11],s91 offen offset:3072 sc1\n"
SA"s91,s91,4096\n"
BL"a[164:167],v224,s[8:11],s91 offen offset:0 sc1\n"
BL"a[168:171],v224,s[8:11],s91 offen offset:1024 sc1\n"
BL"a[172:175],v224,s[8:11],s91 offen offset:2048 sc1\n"
BL"a[176:179],v224,s[8:11],s91 offen offset:3072 sc1\n"
WT"vmcnt(0)\n"
MF"v[240:243],a[148:151],a[32:35],0\n"
MF"v[240:243],a[152:155],a[36:39],v[240:243]\n"
MF"v[240:243],a[156:159],a[40:43],v[240:243]\n"
MF"v[240:243],a[160:163],a[44:47],v[240:243]\n"
MF"v[244:247],a[164:167],a[32:35],0\n"
MF"v[244:247],a[168:171],a[36:39],v[244:247]\n"
MF"v[244:247],a[172:175],a[40:43],v[244:247]\n"
MF"v[244:247],a[176:179],a[44:47],v[244:247]\n"
"s_lshl_b32 s91,s88,5\n"
"v_subrev_u32 v250,s91,v249\n"
"s_nop 7\n"
"v_cmp_gt_i32 vcc,0,v250\n"
"s_nop 1\n"
"v_cndmask_b32 v240,v240,v255,vcc\n"
"v_cmp_gt_i32 vcc,1,v250\n"
"s_nop 1\n"
"v_cndmask_b32 v241,v241,v255,vcc\n"
"v_cmp_gt_i32 vcc,2,v250\n"
"s_nop 1\n"
"v_cndmask_b32 v242,v242,v255,vcc\n"
"v_cmp_gt_i32 vcc,3,v250\n"
"s_nop 1\n"
"v_cndmask_b32 v243,v243,v255,vcc\n"
"v_cmp_gt_i32 vcc,16,v250\n"
"s_nop 1\n"
"v_cndmask_b32 v244,v244,v255,vcc\n"
"v_cmp_gt_i32 vcc,17,v250\n"
"s_nop 1\n"
"v_cndmask_b32 v245,v245,v255,vcc\n"
"v_cmp_gt_i32 vcc,18,v250\n"
"s_nop 1\n"
"v_cndmask_b32 v246,v246,v255,vcc\n"
"v_cmp_gt_i32 vcc,19,v250\n"
"s_nop 1\n"
"v_cndmask_b32 v247,v247,v255,vcc\n"
"s_cmp_lg_u32 s90,0\n"
"s_cbranch_scc1 L1071_%=\n"
"v_max3_f32 v248,v248,v240,v241\n"
"v_max3_f32 v248,v248,v242,v243\n"
"v_max3_f32 v248,v248,v244,v245\n"
"v_max3_f32 v248,v248,v246,v247\n"
"s_branch L1072_%=\n"
"L1071_%=:\n"
"s_lshl_b32 s91,s88,13\n"
SA"s91,s91,s39\n"
SA"s91,s91,524288\n"
BL"a[148:151],v224,s[8:11],s91 offen offset:0 sc1\n"
BL"a[152:155],v224,s[8:11],s91 offen offset:1024 sc1\n"
BL"a[156:159],v224,s[8:11],s91 offen offset:2048 sc1\n"
BL"a[160:163],v224,s[8:11],s91 offen offset:3072 sc1\n"
SA"s91,s91,4096\n"
BL"a[164:167],v224,s[8:11],s91 offen offset:0 sc1\n"
BL"a[168:171],v224,s[8:11],s91 offen offset:1024 sc1\n"
BL"a[172:175],v224,s[8:11],s91 offen offset:2048 sc1\n"
BL"a[176:179],v224,s[8:11],s91 offen offset:3072 sc1\n"
WT"vmcnt(0)\n"
"v_sub_f32 v240,v240,v248\n"
"v_sub_f32 v241,v241,v248\n"
"v_sub_f32 v242,v242,v248\n"
"v_sub_f32 v243,v243,v248\n"
"v_sub_f32 v244,v244,v248\n"
"v_sub_f32 v245,v245,v248\n"
"v_sub_f32 v246,v246,v248\n"
"v_sub_f32 v247,v247,v248\n"
EX"v240,v240\n"
EX"v241,v241\n"
EX"v242,v242\n"
EX"v243,v243\n"
EX"v244,v244\n"
EX"v245,v245\n"
EX"v246,v246\n"
EX"v247,v247\n"
"s_nop 0\n"
PK"v240,v240,v241\n"
PK"v241,v242,v243\n"
PK"v242,v244,v245\n"
PK"v243,v246,v247\n"
"s_nop 1\n"
MF"v[144:147],a[148:151],v[240:243],v[144:147]\n"
MF"v[148:151],a[152:155],v[240:243],v[148:151]\n"
MF"v[152:155],a[156:159],v[240:243],v[152:155]\n"
MF"v[156:159],a[160:163],v[240:243],v[156:159]\n"
MF"v[160:163],a[164:167],v[240:243],v[160:163]\n"
MF"v[164:167],a[168:171],v[240:243],v[164:167]\n"
MF"v[168:171],a[172:175],v[240:243],v[168:171]\n"
MF"v[172:175],a[176:179],v[240:243],v[172:175]\n"
MF"a[140:143],a[128:131],v[240:243],a[140:143]\n"
"s_nop 7\n"
"L1072_%=:\n"
SA"s88,s88,1\n"
"s_cmp_le_u32 s88,s89\n"
"s_cbranch_scc1 L1070_%=\n"
"s_cmp_lg_u32 s90,0\n"
"s_cbranch_scc1 L1073_%=\n"
"v_mov_b32 v254,v248\n"
"s_nop 1\n"
"v_permlane16_swap_b32 v254,v248\n"
"s_nop 1\n"
"v_max_f32 v248,v248,v254\n"
"v_mov_b32 v254,v248\n"
"s_nop 1\n"
"v_permlane32_swap_b32 v254,v248\n"
"s_nop 1\n"
"v_max_f32 v248,v248,v254\n"
"s_mov_b32 s90,1\n"
"s_mov_b32 s88,0\n"
"s_branch L1070_%=\n"
"L1073_%=:\n"
"s_nop 7\n"
"v_accvgpr_read_b32 v251,a140\n"
"s_nop 1\n"
"s_branch L1068_%=\n"
"L1075_%=:\n"
"s_nop 15\n"
WT"vmcnt(0)\n"
"s_lshl_b32 s89,s34,3\n"
"s_sub_u32 s89,120,s89\n"
SA"s89,s89,s30\n"
SA"s89,s89,4\n"
"s_lshl_b32 s91,s89,4\n"
"s_lshr_b32 s89,s89,1\n"
"v_bfe_u32 v249,v224,4,4\n"
"v_lshrrev_b32 v254,8,v224\n"
"v_lshlrev_b32 v254,2,v254\n"
"v_sub_u32 v249,v249,v254\n"
"v_add_u32 v249,s91,v249\n"
"v_mov_b32 v255,0xff800000\n"
"v_mov_b32 v248,v255\n"
"s_mov_b32 s90,0\n"
"s_mov_b32 s88,0\n"
"v_mov_b32 v176,0\n"
"v_mov_b32 v177,0\n"
"v_mov_b32 v178,0\n"
"v_mov_b32 v179,0\n"
"v_mov_b32 v180,0\n"
"v_mov_b32 v181,0\n"
"v_mov_b32 v182,0\n"
"v_mov_b32 v183,0\n"
"v_mov_b32 v184,0\n"
"v_mov_b32 v185,0\n"
"v_mov_b32 v186,0\n"
"v_mov_b32 v187,0\n"
"v_mov_b32 v188,0\n"
"v_mov_b32 v189,0\n"
"v_mov_b32 v190,0\n"
"v_mov_b32 v191,0\n"
"v_mov_b32 v192,0\n"
"v_mov_b32 v193,0\n"
"v_mov_b32 v194,0\n"
"v_mov_b32 v195,0\n"
"v_mov_b32 v196,0\n"
"v_mov_b32 v197,0\n"
"v_mov_b32 v198,0\n"
"v_mov_b32 v199,0\n"
"v_mov_b32 v200,0\n"
"v_mov_b32 v201,0\n"
"v_mov_b32 v202,0\n"
"v_mov_b32 v203,0\n"
"v_mov_b32 v204,0\n"
"v_mov_b32 v205,0\n"
"v_mov_b32 v206,0\n"
"v_mov_b32 v207,0\n"
"v_accvgpr_write_b32 a144,0\n"
"v_accvgpr_write_b32 a145,0\n"
"v_accvgpr_write_b32 a146,0\n"
"v_accvgpr_write_b32 a147,0\n"
"L1076_%=:\n"
"s_lshl_b32 s91,s88,13\n"
SA"s91,s91,s39\n"
SA"s91,s91,0\n"
BL"a[148:151],v224,s[8:11],s91 offen offset:0 sc1\n"
BL"a[152:155],v224,s[8:11],s91 offen offset:1024 sc1\n"
BL"a[156:159],v224,s[8:11],s91 offen offset:2048 sc1\n"
BL"a[160:163],v224,s[8:11],s91 offen offset:3072 sc1\n"
SA"s91,s91,4096\n"
BL"a[164:167],v224,s[8:11],s91 offen offset:0 sc1\n"
BL"a[168:171],v224,s[8:11],s91 offen offset:1024 sc1\n"
BL"a[172:175],v224,s[8:11],s91 offen offset:2048 sc1\n"
BL"a[176:179],v224,s[8:11],s91 offen offset:3072 sc1\n"
WT"vmcnt(0)\n"
MF"v[240:243],a[148:151],a[48:51],0\n"
MF"v[240:243],a[152:155],a[52:55],v[240:243]\n"
MF"v[240:243],a[156:159],a[56:59],v[240:243]\n"
MF"v[240:243],a[160:163],a[60:63],v[240:243]\n"
MF"v[244:247],a[164:167],a[48:51],0\n"
MF"v[244:247],a[168:171],a[52:55],v[244:247]\n"
MF"v[244:247],a[172:175],a[56:59],v[244:247]\n"
MF"v[244:247],a[176:179],a[60:63],v[244:247]\n"
"s_lshl_b32 s91,s88,5\n"
"v_subrev_u32 v250,s91,v249\n"
"s_nop 7\n"
"v_cmp_gt_i32 vcc,0,v250\n"
"s_nop 1\n"
"v_cndmask_b32 v240,v240,v255,vcc\n"
"v_cmp_gt_i32 vcc,1,v250\n"
"s_nop 1\n"
"v_cndmask_b32 v241,v241,v255,vcc\n"
"v_cmp_gt_i32 vcc,2,v250\n"
"s_nop 1\n"
"v_cndmask_b32 v242,v242,v255,vcc\n"
"v_cmp_gt_i32 vcc,3,v250\n"
"s_nop 1\n"
"v_cndmask_b32 v243,v243,v255,vcc\n"
"v_cmp_gt_i32 vcc,16,v250\n"
"s_nop 1\n"
"v_cndmask_b32 v244,v244,v255,vcc\n"
"v_cmp_gt_i32 vcc,17,v250\n"
"s_nop 1\n"
"v_cndmask_b32 v245,v245,v255,vcc\n"
"v_cmp_gt_i32 vcc,18,v250\n"
"s_nop 1\n"
"v_cndmask_b32 v246,v246,v255,vcc\n"
"v_cmp_gt_i32 vcc,19,v250\n"
"s_nop 1\n"
"v_cndmask_b32 v247,v247,v255,vcc\n"
"s_cmp_lg_u32 s90,0\n"
"s_cbranch_scc1 L1077_%=\n"
"v_max3_f32 v248,v248,v240,v241\n"
"v_max3_f32 v248,v248,v242,v243\n"
"v_max3_f32 v248,v248,v244,v245\n"
"v_max3_f32 v248,v248,v246,v247\n"
"s_branch L1078_%=\n"
"L1077_%=:\n"
"s_lshl_b32 s91,s88,13\n"
SA"s91,s91,s39\n"
SA"s91,s91,524288\n"
BL"a[148:151],v224,s[8:11],s91 offen offset:0 sc1\n"
BL"a[152:155],v224,s[8:11],s91 offen offset:1024 sc1\n"
BL"a[156:159],v224,s[8:11],s91 offen offset:2048 sc1\n"
BL"a[160:163],v224,s[8:11],s91 offen offset:3072 sc1\n"
SA"s91,s91,4096\n"
BL"a[164:167],v224,s[8:11],s91 offen offset:0 sc1\n"
BL"a[168:171],v224,s[8:11],s91 offen offset:1024 sc1\n"
BL"a[172:175],v224,s[8:11],s91 offen offset:2048 sc1\n"
BL"a[176:179],v224,s[8:11],s91 offen offset:3072 sc1\n"
WT"vmcnt(0)\n"
"v_sub_f32 v240,v240,v248\n"
"v_sub_f32 v241,v241,v248\n"
"v_sub_f32 v242,v242,v248\n"
"v_sub_f32 v243,v243,v248\n"
"v_sub_f32 v244,v244,v248\n"
"v_sub_f32 v245,v245,v248\n"
"v_sub_f32 v246,v246,v248\n"
"v_sub_f32 v247,v247,v248\n"
EX"v240,v240\n"
EX"v241,v241\n"
EX"v242,v242\n"
EX"v243,v243\n"
EX"v244,v244\n"
EX"v245,v245\n"
EX"v246,v246\n"
EX"v247,v247\n"
"s_nop 0\n"
PK"v240,v240,v241\n"
PK"v241,v242,v243\n"
PK"v242,v244,v245\n"
PK"v243,v246,v247\n"
"s_nop 1\n"
MF"v[176:179],a[148:151],v[240:243],v[176:179]\n"
MF"v[180:183],a[152:155],v[240:243],v[180:183]\n"
MF"v[184:187],a[156:159],v[240:243],v[184:187]\n"
MF"v[188:191],a[160:163],v[240:243],v[188:191]\n"
MF"v[192:195],a[164:167],v[240:243],v[192:195]\n"
MF"v[196:199],a[168:171],v[240:243],v[196:199]\n"
MF"v[200:203],a[172:175],v[240:243],v[200:203]\n"
MF"v[204:207],a[176:179],v[240:243],v[204:207]\n"
MF"a[144:147],a[128:131],v[240:243],a[144:147]\n"
"s_nop 7\n"
"L1078_%=:\n"
SA"s88,s88,1\n"
"s_cmp_le_u32 s88,s89\n"
"s_cbranch_scc1 L1076_%=\n"
"s_cmp_lg_u32 s90,0\n"
"s_cbranch_scc1 L1079_%=\n"
"v_mov_b32 v254,v248\n"
"s_nop 1\n"
"v_permlane16_swap_b32 v254,v248\n"
"s_nop 1\n"
"v_max_f32 v248,v248,v254\n"
"v_mov_b32 v254,v248\n"
"s_nop 1\n"
"v_permlane32_swap_b32 v254,v248\n"
"s_nop 1\n"
"v_max_f32 v248,v248,v254\n"
"s_mov_b32 s90,1\n"
"s_mov_b32 s88,0\n"
"s_branch L1076_%=\n"
"L1079_%=:\n"
"s_nop 7\n"
"v_accvgpr_read_b32 v252,a144\n"
"s_nop 1\n"
"s_branch L1074_%=\n"
"L1080_%=:\n"
"s_mov_b32 s54,0\n"
"L1082_%=:\n"
"s_sleep 2\n"
"buffer_load_dwordx2 v[240:241],v238,s[8:11],s63 offen sc1\n"
WT"vmcnt(0)\n"
SA"s54,s54,1\n"
"v_cmp_eq_u64 vcc,s[28:29],v[240:241]\n"
"s_cmp_eq_u64 vcc,exec\n"
"s_cbranch_scc1 L1081_%=\n"
"s_cmp_lt_u32 s54,1048576\n"
"s_cbranch_scc1 L1082_%=\n"
"s_branch L1081_%=\n"
"L1083_%=:\n"
"s_mov_b32 s54,0\n"
"L1085_%=:\n"
"s_sleep 2\n"
"buffer_load_dwordx2 v[240:241],v238,s[8:11],s63 offen sc1\n"
WT"vmcnt(0)\n"
SA"s54,s54,1\n"
"v_cmp_eq_u64 vcc,s[28:29],v[240:241]\n"
"s_cmp_eq_u64 vcc,exec\n"
"s_cbranch_scc1 L1084_%=\n"
"s_cmp_lt_u32 s54,1048576\n"
"s_cbranch_scc1 L1085_%=\n"
"s_branch L1084_%=\n"
"L1086_%=:\n"
"s_mov_b32 s54,0\n"
"L1088_%=:\n"
"s_sleep 2\n"
"buffer_load_dwordx2 v[240:241],v244,s[8:11],s92 offen sc1\n"
WT"vmcnt(0)\n"
SA"s54,s54,1\n"
"v_cmp_eq_u64 vcc,s[28:29],v[240:241]\n"
"s_cmp_eq_u64 vcc,exec\n"
"s_cbranch_scc1 L1087_%=\n"
"s_cmp_lt_u32 s54,1048576\n"
"s_cbranch_scc1 L1088_%=\n"
"s_branch L1087_%=\n"
"LTH_%=:\n"
RD"a[92:95],v227 offset:7168\n"
MF"v[0:3],a[64:67],v[80:83],0\n"
WT"lgkmcnt(4)\n"
"v_accvgpr_read_b32 v251,a140\n"
"v_subrev_u32 v253,0x2b800000,v251\n"
MF"v[0:3],a[68:71],v[88:91],v[0:3]\n"
RD"a[96:99],v227 offset:8192\n"
MF"v[40:43],a[68:71],v[120:123],0\n"
"v_cmp_le_u32 vcc,0x34000000,v253\n"
"s_cmp_lg_u64 vcc,0\n"
"s_cbranch_scc1 L1069_%=\n"
"L1068_%=:\n"
MF"v[40:43],a[64:67],v[112:115],v[40:43]\n"
"v_rcp_f32 v251,v251\n"
MF"v[4:7],a[72:75],v[80:83],0\n"
SA"s41,s41,32768\n"
RD"a[100:103],v227 offset:9216\n"
MU"v144,v251,v144\n"
MF"v[4:7],a[76:79],v[88:91],v[4:7]\n"
"s_cmp_eq_u32 s41,163840\n"
RD"a[104:107],v227 offset:10240\n"
MU"v145,v251,v145\n"
MF"v[44:47],a[76:79],v[120:123],0\n"
"s_cselect_b32 s41,0,s41\n"
MU"v146,v251,v146\n"
"v_add_u32 v226,s41,v224\n"
MF"v[44:47],a[72:75],v[112:115],v[44:47]\n"
WT"lgkmcnt(3)\n"
RD"a[108:111],v227 offset:11264\n"
MU"v147,v251,v147\n"
MF"v[8:11],a[80:83],v[80:83],0\n"
SA"s41,s41,32768\n"
MU"v148,v251,v148\n"
MU"v149,v251,v149\n"
MF"v[8:11],a[84:87],v[88:91],v[8:11]\n"
"s_cmp_eq_u32 s41,163840\n"
RD"a[112:115],v227 offset:12288\n"
MU"v150,v251,v150\n"
MU"v151,v251,v151\n"
MF"v[48:51],a[84:87],v[120:123],0\n"
"s_cselect_b32 s41,0,s41\n"
PK"v144,v144,v145\n"
"v_add_u32 v235,s41,v224\n"
PK"v145,v146,v147\n"
MF"v[48:51],a[80:83],v[112:115],v[48:51]\n"
RD"a[116:119],v227 offset:13312\n"
PK"v146,v148,v149\n"
MF"v[12:15],a[88:91],v[80:83],0\n"
SA"s41,s41,32768\n"
PK"v147,v150,v151\n"
MU"v152,v251,v152\n"
MF"v[12:15],a[92:95],v[88:91],v[12:15]\n"
"s_cmp_eq_u32 s41,163840\n"
BS"v[144:147],v224,s[8:11],s60 offen offset:0 sc1\n"
MU"v153,v251,v153\n"
MF"v[52:55],a[92:95],v[120:123],0\n"
"s_cselect_b32 s41,0,s41\n"
RD"a[120:123],v227 offset:14336\n"
"v_add_u32 v236,s41,v224\n"
MU"v154,v251,v154\n"
MF"v[52:55],a[88:91],v[112:115],v[52:55]\n"
WT"lgkmcnt(3)\n"
RD"a[124:127],v227 offset:15360\n"
MU"v155,v251,v155\n"
MU"v156,v251,v156\n"
MF"v[16:19],a[96:99],v[80:83],0\n"
SA"s41,s41,32768\n"
MU"v157,v251,v157\n"
MU"v158,v251,v158\n"
MU"v159,v251,v159\n"
MF"v[16:19],a[100:103],v[88:91],v[16:19]\n"
"s_cmp_eq_u32 s41,163840\n"
RD"a[64:67],v227 offset:16384\n"
PK"v152,v152,v153\n"
PK"v153,v154,v155\n"
MF"v[56:59],a[100:103],v[120:123],0\n"
"s_cselect_b32 s41,0,s41\n"
PK"v154,v156,v157\n"
"v_add_u32 v237,s41,v224\n"
PK"v155,v158,v159\n"
MF"v[56:59],a[96:99],v[112:115],v[56:59]\n"
BS"v[152:155],v224,s[8:11],s60 offen offset:1024 sc1\n"
MU"v160,v251,v160\n"
MF"v[20:23],a[104:107],v[80:83],0\n"
RD"a[68:71],v227 offset:17408\n"
MU"v161,v251,v161\n"
MU"v162,v251,v162\n"
MF"v[20:23],a[108:111],v[88:91],v[20:23]\n"
WT"vmcnt(2)\n"
"buffer_store_dwordx2 v[232:233],v230,s[8:11],s61 offen sc1\n"
MU"v163,v251,v163\n"
MF"v[60:63],a[108:111],v[120:123],0\n"
RD"a[72:75],v227 offset:18432\n"
MU"v164,v251,v164\n"
MU"v165,v251,v165\n"
MF"v[60:63],a[104:107],v[112:115],v[60:63]\n"
WT"lgkmcnt(3)\n"
RD"a[76:79],v227 offset:19456\n"
MU"v166,v251,v166\n"
MU"v167,v251,v167\n"
MF"v[24:27],a[112:115],v[80:83],0\n"
SA"s51,s60,16384\n"
"v_accvgpr_read_b32 v252,a144\n"
"v_subrev_u32 v253,0x2b800000,v252\n"
PK"v160,v160,v161\n"
MF"v[24:27],a[116:119],v[88:91],v[24:27]\n"
"s_lshl_b32 s62,s34,14\n"
RD"a[80:83],v227 offset:20480\n"
PK"v161,v162,v163\n"
PK"v162,v164,v165\n"
MF"v[64:67],a[116:119],v[120:123],0\n"
"s_sub_u32 s62,s61,s62\n"
"v_cmp_le_u32 vcc,0x34000000,v253\n"
"s_cmp_lg_u64 vcc,0\n"
"s_cbranch_scc1 L1075_%=\n"
"L1074_%=:\n"
MF"v[64:67],a[112:115],v[112:115],v[64:67]\n"
SA"s62,s62,122880\n"
"v_rcp_f32 v252,v252\n"
PK"v163,v166,v167\n"
MF"v[28:31],a[120:123],v[80:83],0\n"
BS"v[160:163],v224,s[8:11],s60 offen offset:2048 sc1\n"
MU"v168,v251,v168\n"
MF"v[28:31],a[124:127],v[88:91],v[28:31]\n"
RD"a[84:87],v227 offset:21504\n"
RD"a[88:91],v227 offset:22528\n"
MU"v169,v251,v169\n"
MF"v[68:71],a[124:127],v[120:123],0\n"
MU"v170,v251,v170\n"
MU"v171,v251,v171\n"
MU"v172,v251,v172\n"
MF"v[68:71],a[120:123],v[112:115],v[68:71]\n"
WT"lgkmcnt(3)\n"
RD"a[92:95],v227 offset:23552\n"
MU"v173,v251,v173\n"
MU"v174,v251,v174\n"
MF"v[32:35],a[64:67],v[80:83],0\n"
MU"v175,v251,v175\n"
PK"v168,v168,v169\n"
PK"v169,v170,v171\n"
MF"v[32:35],a[68:71],v[88:91],v[32:35]\n"
RD"a[96:99],v227 offset:24576\n"
MU"v176,v252,v176\n"
MU"v177,v252,v177\n"
MF"v[72:75],a[68:71],v[120:123],0\n"
PK"v170,v172,v173\n"
PK"v171,v174,v175\n"
MU"v178,v252,v178\n"
MF"v[72:75],a[64:67],v[112:115],v[72:75]\n"
BS"v[168:171],v224,s[8:11],s60 offen offset:3072 sc1\n"
MU"v179,v252,v179\n"
MF"v[36:39],a[72:75],v[80:83],0\n"
RD"a[100:103],v227 offset:25600\n"
MU"v180,v252,v180\n"
MU"v181,v252,v181\n"
MF"v[36:39],a[76:79],v[88:91],v[36:39]\n"
RD"a[104:107],v227 offset:26624\n"
MU"v182,v252,v182\n"
MU"v183,v252,v183\n"
MF"v[76:79],a[76:79],v[120:123],0\n"
PK"v176,v176,v177\n"
PK"v177,v178,v179\n"
PK"v178,v180,v181\n"
MF"v[76:79],a[72:75],v[112:115],v[76:79]\n"
WT"lgkmcnt(3)\n"
RD"a[108:111],v227 offset:27648\n"
PK"v179,v182,v183\n"
MU"v184,v252,v184\n"
MF"v[0:3],a[80:83],v[96:99],v[0:3]\n"
BS"v[176:179],v224,s[8:11],s51 offen offset:0 sc1\n"
MU"v185,v252,v185\n"
MF"v[0:3],a[84:87],v[104:107],v[0:3]\n"
RD"a[112:115],v227 offset:28672\n"
MU"v186,v252,v186\n"
MU"v187,v252,v187\n"
MF"v[40:43],a[84:87],v[136:139],v[40:43]\n"
MU"v188,v252,v188\n"
MU"v189,v252,v189\n"
MU"v190,v252,v190\n"
MF"v[40:43],a[80:83],v[128:131],v[40:43]\n"
RD"a[116:119],v227 offset:29696\n"
MU"v191,v252,v191\n"
PK"v184,v184,v185\n"
MF"v[4:7],a[88:91],v[96:99],v[4:7]\n"
PK"v185,v186,v187\n"
PK"v186,v188,v189\n"
PK"v187,v190,v191\n"
MF"v[4:7],a[92:95],v[104:107],v[4:7]\n"
BS"v[184:187],v224,s[8:11],s51 offen offset:1024 sc1\n"
MU"v192,v252,v192\n"
MF"v[44:47],a[92:95],v[136:139],v[44:47]\n"
RD"a[120:123],v227 offset:30720\n"
MU"v193,v252,v193\n"
MU"v194,v252,v194\n"
MF"v[44:47],a[88:91],v[128:131],v[44:47]\n"
WT"lgkmcnt(3)\n"
RD"a[124:127],v227 offset:31744\n"
"v_bfe_u32 v238,v224,4,2\n"
MU"v195,v252,v195\n"
MF"v[8:11],a[96:99],v[96:99],v[8:11]\n"
"s_andn2_b32 s63,s61,255\n"
"s_barrier\n"
MF"v[8:11],a[100:103],v[104:107],v[8:11]\n"
RD"a[64:67],v226 offset:0\n"
"v_lshlrev_b32 v238,6,v238\n"
MU"v196,v252,v196\n"
MF"v[48:51],a[100:103],v[136:139],v[48:51]\n"
"buffer_load_dwordx2 v[240:241],v238,s[8:11],s63 offen sc1\n"
MU"v197,v252,v197\n"
MF"v[48:51],a[96:99],v[128:131],v[48:51]\n"
RD"a[68:71],v226 offset:1024\n"
MU"v198,v252,v198\n"
MU"v199,v252,v199\n"
MF"v[12:15],a[104:107],v[96:99],v[12:15]\n"
PK"v192,v192,v193\n"
PK"v193,v194,v195\n"
PK"v194,v196,v197\n"
MF"v[12:15],a[108:111],v[104:107],v[12:15]\n"
RD"a[72:75],v226 offset:2048\n"
PK"v195,v198,v199\n"
MU"v200,v252,v200\n"
MF"v[52:55],a[108:111],v[136:139],v[52:55]\n"
BS"v[192:195],v224,s[8:11],s51 offen offset:2048 sc1\n"
MU"v201,v252,v201\n"
MF"v[52:55],a[104:107],v[128:131],v[52:55]\n"
WT"lgkmcnt(3)\n"
RD"a[76:79],v226 offset:3072\n"
MU"v202,v252,v202\n"
MU"v203,v252,v203\n"
MF"v[16:19],a[112:115],v[96:99],v[16:19]\n"
MU"v204,v252,v204\n"
MU"v205,v252,v205\n"
MU"v206,v252,v206\n"
MF"v[16:19],a[116:119],v[104:107],v[16:19]\n"
RD"a[80:83],v226 offset:4096\n"
MU"v207,v252,v207\n"
PK"v200,v200,v201\n"
MF"v[56:59],a[116:119],v[136:139],v[56:59]\n"
"v_readlane_b32 s43,v228,s42\n"
PK"v201,v202,v203\n"
PK"v202,v204,v205\n"
MF"v[56:59],a[112:115],v[128:131],v[56:59]\n"
RD"a[84:87],v226 offset:5120\n"
PK"v203,v206,v207\n"
MF"v[20:23],a[120:123],v[96:99],v[20:23]\n"
BS"v[200:203],v224,s[8:11],s51 offen offset:3072 sc1\n"
MF"v[20:23],a[124:127],v[104:107],v[20:23]\n"
RD"a[88:91],v226 offset:6144\n"
MF"v[60:63],a[124:127],v[136:139],v[60:63]\n"
"s_mov_b32 m0,s40\n"
MF"v[60:63],a[120:123],v[128:131],v[60:63]\n"
WT"lgkmcnt(3)\n"
RD"a[92:95],v226 offset:7168\n"
MF"v[24:27],a[64:67],v[96:99],v[24:27]\n"
"v_readlane_b32 s44,v229,s42\n"
MF"v[24:27],a[68:71],v[104:107],v[24:27]\n"
RD"a[96:99],v227 offset:0\n"
MF"v[64:67],a[68:71],v[136:139],v[64:67]\n"
SA"s42,s42,1\n"
BL"v225,s[8:11],s43 offen offset:0 sc1 lds\n"
MF"v[64:67],a[64:67],v[128:131],v[64:67]\n"
RD"a[100:103],v227 offset:1024\n"
MF"v[28:31],a[72:75],v[96:99],v[28:31]\n"
MF"v[28:31],a[76:79],v[104:107],v[28:31]\n"
RD"a[104:107],v227 offset:2048\n"
MF"v[68:71],a[76:79],v[136:139],v[68:71]\n"
MF"v[68:71],a[72:75],v[128:131],v[68:71]\n"
WT"lgkmcnt(3)\n"
RD"a[108:111],v227 offset:3072\n"
MF"v[32:35],a[80:83],v[96:99],v[32:35]\n"
BL"v225,s[8:11],s43 offen offset:1024 sc1 lds\n"
MF"v[32:35],a[84:87],v[104:107],v[32:35]\n"
RD"a[112:115],v227 offset:4096\n"
MF"v[72:75],a[84:87],v[136:139],v[72:75]\n"
MF"v[72:75],a[80:83],v[128:131],v[72:75]\n"
RD"a[116:119],v227 offset:5120\n"
MF"v[36:39],a[88:91],v[96:99],v[36:39]\n"
MF"v[36:39],a[92:95],v[104:107],v[36:39]\n"
RD"a[120:123],v227 offset:6144\n"
MF"v[76:79],a[92:95],v[136:139],v[76:79]\n"
BL"v225,s[8:11],s43 offen offset:2048 sc1 lds\n"
MF"v[76:79],a[88:91],v[128:131],v[76:79]\n"
WT"lgkmcnt(3)\n"
RD"a[124:127],v227 offset:7168\n"
MF"v[84:87],a[96:99],v[144:147],0\n"
MF"v[84:87],a[100:103],v[152:155],v[84:87]\n"
RD"a[64:67],v227 offset:8192\n"
MF"v[164:167],a[100:103],v[184:187],0\n"
MF"v[164:167],a[96:99],v[176:179],v[164:167]\n"
RD"a[68:71],v227 offset:9216\n"
MF"v[92:95],a[104:107],v[144:147],0\n"
BL"v225,s[8:11],s43 offen offset:3072 sc1 lds\n"
MF"v[92:95],a[108:111],v[152:155],v[92:95]\n"
RD"a[72:75],v227 offset:10240\n"
MF"v[172:175],a[108:111],v[184:187],0\n"
SA"m0,s40,16384\n"
MF"v[172:175],a[104:107],v[176:179],v[172:175]\n"
WT"lgkmcnt(3)\n"
RD"a[76:79],v227 offset:11264\n"
MF"v[100:103],a[112:115],v[144:147],0\n"
MF"v[100:103],a[116:119],v[152:155],v[100:103]\n"
SA"s48,s59,32768\n"
RD"a[80:83],v227 offset:12288\n"
MF"v[180:183],a[116:119],v[184:187],0\n"
"s_and_b32 s48,s48,0x18000\n"
BL"v225,s[8:11],s44 offen offset:0 sc1 lds\n"
MF"v[180:183],a[112:115],v[176:179],v[180:183]\n"
SA"s40,s40,32768\n"
RD"a[84:87],v227 offset:13312\n"
"s_andn2_b32 s49,s59,0x18000\n"
MF"v[108:111],a[120:123],v[144:147],0\n"
"s_cmp_eq_u32 s40,s46\n"
"s_cselect_b32 s40,s47,s40\n"
WT"vmcnt(7)\n"
"v_cmp_eq_u64 vcc,s[28:29],v[240:241]\n"
"s_cmp_eq_u64 vcc,exec\n"
"s_cbranch_scc0 L1080_%=\n"
"L1081_%=:\n"
MF"v[108:111],a[124:127],v[152:155],v[108:111]\n"
"s_or_b32 s52,s48,s49\n"
RD"a[88:91],v227 offset:14336\n"
SA"s53,s52,16384\n"
MF"v[188:191],a[124:127],v[184:187],0\n"
BL"a[0:3],v224,s[8:11],s52 offen offset:0 sc1\n"
MF"v[188:191],a[120:123],v[176:179],v[188:191]\n"
WT"lgkmcnt(3)\n"
RD"a[92:95],v227 offset:15360\n"
MF"v[116:119],a[64:67],v[144:147],0\n"
BL"v225,s[8:11],s44 offen offset:1024 sc1 lds\n"
MF"v[116:119],a[68:71],v[152:155],v[116:119]\n"
RD"a[96:99],v227 offset:16384\n"
MF"v[196:199],a[68:71],v[184:187],0\n"
BL"a[4:7],v224,s[8:11],s53 offen offset:0 sc1\n"
MF"v[196:199],a[64:67],v[176:179],v[196:199]\n"
RD"a[100:103],v227 offset:17408\n"
MF"v[124:127],a[72:75],v[144:147],0\n"
MF"v[124:127],a[76:79],v[152:155],v[124:127]\n"
RD"a[104:107],v227 offset:18432\n"
MF"v[204:207],a[76:79],v[184:187],0\n"
BL"v225,s[8:11],s44 offen offset:2048 sc1 lds\n"
MF"v[204:207],a[72:75],v[176:179],v[204:207]\n"
WT"lgkmcnt(3)\n"
RD"a[108:111],v227 offset:19456\n"
MF"v[132:135],a[80:83],v[144:147],0\n"
BL"a[8:11],v224,s[8:11],s52 offen offset:1024 sc1\n"
MF"v[132:135],a[84:87],v[152:155],v[132:135]\n"
RD"a[112:115],v227 offset:20480\n"
MF"v[208:211],a[84:87],v[184:187],0\n"
"v_readlane_b32 s43,v228,s42\n"
MF"v[208:211],a[80:83],v[176:179],v[208:211]\n"
RD"a[116:119],v227 offset:21504\n"
MF"v[140:143],a[88:91],v[144:147],0\n"
BL"a[12:15],v224,s[8:11],s53 offen offset:1024 sc1\n"
MF"v[140:143],a[92:95],v[152:155],v[140:143]\n"
BL"v225,s[8:11],s44 offen offset:3072 sc1 lds\n"
MF"v[212:215],a[92:95],v[184:187],0\n"
"s_mov_b32 m0,s40\n"
RD"a[120:123],v227 offset:22528\n"
MF"v[212:215],a[88:91],v[176:179],v[212:215]\n"
WT"lgkmcnt(3)\n"
RD"a[124:127],v227 offset:23552\n"
MF"v[148:151],a[96:99],v[144:147],0\n"
WT"vmcnt(12)\n"
"buffer_store_dwordx2 v[232:233],v230,s[8:11],s62 offen sc1\n"
MF"v[148:151],a[100:103],v[152:155],v[148:151]\n"
BL"a[16:19],v224,s[8:11],s52 offen offset:2048 sc1\n"
MF"v[216:219],a[100:103],v[184:187],0\n"
RD"a[64:67],v227 offset:24576\n"
"v_readlane_b32 s44,v229,s42\n"
MF"v[216:219],a[96:99],v[176:179],v[216:219]\n"
SA"s42,s42,1\n"
BL"v225,s[8:11],s43 offen offset:0 sc1 lds\n"
MF"v[156:159],a[104:107],v[144:147],0\n"
RD"a[68:71],v227 offset:25600\n"
MF"v[156:159],a[108:111],v[152:155],v[156:159]\n"
BL"a[20:23],v224,s[8:11],s53 offen offset:2048 sc1\n"
MF"v[220:223],a[108:111],v[184:187],0\n"
RD"a[72:75],v227 offset:26624\n"
MF"v[220:223],a[104:107],v[176:179],v[220:223]\n"
WT"lgkmcnt(3)\n"
RD"a[76:79],v227 offset:27648\n"
MF"v[84:87],a[112:115],v[160:163],v[84:87]\n"
"s_lshr_b32 s89,s31,3\n"
BL"v225,s[8:11],s43 offen offset:1024 sc1 lds\n"
MF"v[84:87],a[116:119],v[168:171],v[84:87]\n"
"s_lshl_b32 s89,s89,4\n"
RD"a[80:83],v227 offset:28672\n"
MF"v[164:167],a[116:119],v[200:203],v[164:167]\n"
SA"s89,s89,s30\n"
BL"a[24:27],v224,s[8:11],s52 offen offset:3072 sc1\n"
MF"v[164:167],a[112:115],v[192:195],v[164:167]\n"
"s_lshl_b32 s89,s89,3\n"
RD"a[84:87],v227 offset:29696\n"
MF"v[92:95],a[120:123],v[160:163],v[92:95]\n"
"s_xor_b32 s23,s32,s30\n"
MF"v[92:95],a[124:127],v[168:171],v[92:95]\n"
SA"s48,s59,65536\n"
RD"a[88:91],v227 offset:30720\n"
SA"s89,s89,s23\n"
MF"v[172:175],a[124:127],v[200:203],v[172:175]\n"
"s_and_b32 s48,s48,0x18000\n"
BL"a[28:31],v224,s[8:11],s53 offen offset:3072 sc1\n"
MF"v[172:175],a[120:123],v[192:195],v[172:175]\n"
WT"lgkmcnt(3)\n"
BL"v225,s[8:11],s43 offen offset:2048 sc1 lds\n"
MF"v[100:103],a[64:67],v[160:163],v[100:103]\n"
"s_xor_b32 s90,s89,36\n"
RD"a[92:95],v227 offset:31744\n"
"s_andn2_b32 s49,s59,0x18000\n"
MF"v[100:103],a[68:71],v[168:171],v[100:103]\n"
"s_mul_i32 s88,s89,5120\n"
RD"a[96:99],v226 offset:0\n"
"s_or_b32 s52,s48,s49\n"
MF"v[180:183],a[68:71],v[200:203],v[180:183]\n"
SA"s53,s52,16384\n"
BL"a[180:183],v224,s[8:11],s52 offen offset:0 sc1\n"
MF"v[180:183],a[64:67],v[192:195],v[180:183]\n"
"s_mul_i32 s22,s90,5120\n"
RD"a[100:103],v226 offset:1024\n"
"s_sub_u32 s90,s90,s89\n"
MF"v[108:111],a[72:75],v[160:163],v[108:111]\n"
"s_lshl_b32 s90,s90,6\n"
BL"v225,s[8:11],s43 offen offset:3072 sc1 lds\n"
MF"v[108:111],a[76:79],v[168:171],v[108:111]\n"
SA"s88,s88,59506688\n"
RD"a[104:107],v226 offset:2048\n"
SA"s22,s22,59506688\n"
MF"v[188:191],a[76:79],v[200:203],v[188:191]\n"
"s_cmp_eq_u32 s23,0\n"
BL"a[184:187],v224,s[8:11],s53 offen offset:0 sc1\n"
MF"v[188:191],a[72:75],v[192:195],v[188:191]\n"
WT"lgkmcnt(3)\n"
RD"a[108:111],v226 offset:3072\n"
"s_cselect_b32 s88,2130706432,s88\n"
MF"v[116:119],a[80:83],v[160:163],v[116:119]\n"
SA"m0,s40,16384\n"
"s_cmp_eq_u32 s23,4\n"
"s_cselect_b32 s22,2130706432,s22\n"
MF"v[116:119],a[84:87],v[168:171],v[116:119]\n"
"s_lshl_b32 s89,s89,6\n"
RD"a[112:115],v226 offset:4096\n"
"v_bfe_u32 v239,v224,1,6\n"
MF"v[196:199],a[84:87],v[200:203],v[196:199]\n"
SA"s89,s89,131072\n"
BL"v225,s[8:11],s44 offen offset:0 sc1 lds\n"
MF"v[196:199],a[80:83],v[192:195],v[196:199]\n"
SA"s40,s40,32768\n"
RD"a[116:119],v226 offset:5120\n"
"v_bfe_u32 v242,v224,7,1\n"
MF"v[124:127],a[88:91],v[160:163],v[124:127]\n"
"s_cmp_eq_u32 s40,s46\n"
BL"a[188:191],v224,s[8:11],s52 offen offset:1024 sc1\n"
MF"v[124:127],a[92:95],v[168:171],v[124:127]\n"
"s_cselect_b32 s40,s47,s40\n"
RD"a[120:123],v226 offset:6144\n"
"v_mad_u32_u24 v239,v242,s90,v239\n"
MF"v[204:207],a[92:95],v[200:203],v[204:207]\n"
"v_lshrrev_b32 v242,8,v224\n"
"v_lshl_add_u32 v239,v242,27,v239\n"
MF"v[204:207],a[88:91],v[192:195],v[204:207]\n"
WT"lgkmcnt(3)\n"
RD"a[124:127],v226 offset:7168\n"
MF"v[132:135],a[96:99],v[160:163],v[132:135]\n"
BL"a[192:195],v224,s[8:11],s53 offen offset:1024 sc1\n"
MF"v[132:135],a[100:103],v[168:171],v[132:135]\n"
BL"v225,s[8:11],s44 offen offset:1024 sc1 lds\n"
MF"v[208:211],a[100:103],v[200:203],v[208:211]\n"
RD"a[64:67],v226 offset:8192\n"
MF"v[208:211],a[96:99],v[192:195],v[208:211]\n"
RD"a[68:71],v226 offset:9216\n"
MF"v[140:143],a[104:107],v[160:163],v[140:143]\n"
BL"a[196:199],v224,s[8:11],s52 offen offset:2048 sc1\n"
MF"v[140:143],a[108:111],v[168:171],v[140:143]\n"
RD"a[72:75],v226 offset:10240\n"
MF"v[212:215],a[108:111],v[200:203],v[212:215]\n"
BL"v225,s[8:11],s44 offen offset:2048 sc1 lds\n"
MF"v[212:215],a[104:107],v[192:195],v[212:215]\n"
WT"lgkmcnt(3)\n"
RD"a[76:79],v226 offset:11264\n"
MF"v[148:151],a[112:115],v[160:163],v[148:151]\n"
BL"a[200:203],v224,s[8:11],s53 offen offset:2048 sc1\n"
MF"v[148:151],a[116:119],v[168:171],v[148:151]\n"
RD"a[80:83],v226 offset:12288\n"
MF"v[216:219],a[116:119],v[200:203],v[216:219]\n"
MF"v[216:219],a[112:115],v[192:195],v[216:219]\n"
RD"a[84:87],v226 offset:13312\n"
MF"v[156:159],a[120:123],v[160:163],v[156:159]\n"
BL"v225,s[8:11],s44 offen offset:3072 sc1 lds\n"
MF"v[156:159],a[124:127],v[168:171],v[156:159]\n"
RD"a[88:91],v226 offset:14336\n"
MF"v[220:223],a[124:127],v[200:203],v[220:223]\n"
BL"a[204:207],v224,s[8:11],s52 offen offset:3072 sc1\n"
MF"v[220:223],a[120:123],v[192:195],v[220:223]\n"
WT"vmcnt(26) lgkmcnt(3)\n"
RD"a[92:95],v226 offset:15360\n"
MF"v[0:3],a[64:67],a[0:3],v[0:3]\n"
WT"vmcnt(22)\n"
MF"v[0:3],a[68:71],a[8:11],v[0:3]\n"
WT"vmcnt(21)\n"
RD"a[96:99],v226 offset:16384\n"
SA"s48,s59,98304\n"
MF"v[40:43],a[68:71],a[12:15],v[40:43]\n"
"s_and_b32 s48,s48,0x18000\n"
BL"a[208:211],v224,s[8:11],s53 offen offset:3072 sc1\n"
MF"v[40:43],a[64:67],a[4:7],v[40:43]\n"
"s_andn2_b32 s49,s59,0x18000\n"
RD"a[100:103],v226 offset:17408\n"
MF"v[4:7],a[72:75],a[0:3],v[4:7]\n"
"s_or_b32 s52,s48,s49\n"
SA"s53,s52,16384\n"
MF"v[4:7],a[76:79],a[8:11],v[4:7]\n"
RD"a[104:107],v226 offset:18432\n"
MF"v[44:47],a[76:79],a[12:15],v[44:47]\n"
BL"a[212:215],v224,s[8:11],s52 offen offset:0 sc1\n"
MF"v[44:47],a[72:75],a[4:7],v[44:47]\n"
WT"lgkmcnt(3)\n"
RD"a[108:111],v226 offset:19456\n"
MF"v[8:11],a[80:83],a[0:3],v[8:11]\n"
MF"v[8:11],a[84:87],a[8:11],v[8:11]\n"
RD"a[112:115],v226 offset:20480\n"
MF"v[48:51],a[84:87],a[12:15],v[48:51]\n"
BL"a[216:219],v224,s[8:11],s53 offen offset:0 sc1\n"
MF"v[48:51],a[80:83],a[4:7],v[48:51]\n"
RD"a[116:119],v226 offset:21504\n"
MF"v[12:15],a[88:91],a[0:3],v[12:15]\n"
MF"v[12:15],a[92:95],a[8:11],v[12:15]\n"
RD"a[120:123],v226 offset:22528\n"
MF"v[52:55],a[92:95],a[12:15],v[52:55]\n"
MF"v[52:55],a[88:91],a[4:7],v[52:55]\n"
WT"lgkmcnt(3)\n"
RD"a[124:127],v226 offset:23552\n"
MF"v[16:19],a[96:99],a[0:3],v[16:19]\n"
BL"a[220:223],v224,s[8:11],s52 offen offset:1024 sc1\n"
MF"v[16:19],a[100:103],a[8:11],v[16:19]\n"
RD"a[64:67],v226 offset:24576\n"
MF"v[56:59],a[100:103],a[12:15],v[56:59]\n"
MF"v[56:59],a[96:99],a[4:7],v[56:59]\n"
RD"a[68:71],v226 offset:25600\n"
MF"v[20:23],a[104:107],a[0:3],v[20:23]\n"
BL"a[224:227],v224,s[8:11],s53 offen offset:1024 sc1\n"
MF"v[20:23],a[108:111],a[8:11],v[20:23]\n"
RD"a[72:75],v226 offset:26624\n"
MF"v[60:63],a[108:111],a[12:15],v[60:63]\n"
MF"v[60:63],a[104:107],a[4:7],v[60:63]\n"
WT"lgkmcnt(3)\n"
RD"a[76:79],v226 offset:27648\n"
MF"v[24:27],a[112:115],a[0:3],v[24:27]\n"
BL"a[228:231],v224,s[8:11],s52 offen offset:2048 sc1\n"
MF"v[24:27],a[116:119],a[8:11],v[24:27]\n"
RD"a[80:83],v226 offset:28672\n"
MF"v[64:67],a[116:119],a[12:15],v[64:67]\n"
MF"v[64:67],a[112:115],a[4:7],v[64:67]\n"
RD"a[84:87],v226 offset:29696\n"
MF"v[28:31],a[120:123],a[0:3],v[28:31]\n"
BL"a[232:235],v224,s[8:11],s53 offen offset:2048 sc1\n"
MF"v[28:31],a[124:127],a[8:11],v[28:31]\n"
RD"a[88:91],v226 offset:30720\n"
MF"v[68:71],a[124:127],a[12:15],v[68:71]\n"
MF"v[68:71],a[120:123],a[4:7],v[68:71]\n"
WT"lgkmcnt(3)\n"
RD"a[92:95],v226 offset:31744\n"
MF"v[32:35],a[64:67],a[0:3],v[32:35]\n"
"s_barrier\n"
MF"v[32:35],a[68:71],a[8:11],v[32:35]\n"
RD"a[96:99],v235 offset:0\n"
MF"v[72:75],a[68:71],a[12:15],v[72:75]\n"
BL"a[236:239],v224,s[8:11],s52 offen offset:3072 sc1\n"
MF"v[72:75],a[64:67],a[4:7],v[72:75]\n"
RD"a[100:103],v235 offset:1024\n"
MF"v[36:39],a[72:75],a[0:3],v[36:39]\n"
MF"v[36:39],a[76:79],a[8:11],v[36:39]\n"
RD"a[104:107],v235 offset:2048\n"
MF"v[76:79],a[76:79],a[12:15],v[76:79]\n"
BL"a[240:243],v224,s[8:11],s53 offen offset:3072 sc1\n"
MF"v[76:79],a[72:75],a[4:7],v[76:79]\n"
WT"vmcnt(27) lgkmcnt(3)\n"
RD"a[108:111],v235 offset:3072\n"
MF"v[0:3],a[80:83],a[16:19],v[0:3]\n"
WT"vmcnt(23)\n"
MF"v[0:3],a[84:87],a[24:27],v[0:3]\n"
WT"vmcnt(22)\n"
RD"a[112:115],v235 offset:4096\n"
MF"v[40:43],a[84:87],a[28:31],v[40:43]\n"
MF"v[40:43],a[80:83],a[20:23],v[40:43]\n"
RD"a[116:119],v235 offset:5120\n"
MF"v[4:7],a[88:91],a[16:19],v[4:7]\n"
MF"v[4:7],a[92:95],a[24:27],v[4:7]\n"
RD"a[120:123],v235 offset:6144\n"
MF"v[44:47],a[92:95],a[28:31],v[44:47]\n"
MF"v[44:47],a[88:91],a[20:23],v[44:47]\n"
WT"lgkmcnt(3)\n"
RD"a[124:127],v235 offset:7168\n"
MF"v[8:11],a[96:99],a[16:19],v[8:11]\n"
MF"v[8:11],a[100:103],a[24:27],v[8:11]\n"
RD"a[64:67],v235 offset:8192\n"
MF"v[48:51],a[100:103],a[28:31],v[48:51]\n"
MF"v[48:51],a[96:99],a[20:23],v[48:51]\n"
RD"a[68:71],v235 offset:9216\n"
MF"v[12:15],a[104:107],a[16:19],v[12:15]\n"
MF"v[12:15],a[108:111],a[24:27],v[12:15]\n"
RD"a[72:75],v235 offset:10240\n"
"v_bfe_u32 v238,v224,4,2\n"
MF"v[52:55],a[108:111],a[28:31],v[52:55]\n"
"s_andn2_b32 s63,s62,255\n"
"v_lshlrev_b32 v238,6,v238\n"
MF"v[52:55],a[104:107],a[20:23],v[52:55]\n"
WT"lgkmcnt(3)\n"
RD"a[76:79],v235 offset:11264\n"
MF"v[16:19],a[112:115],a[16:19],v[16:19]\n"
"buffer_load_dwordx2 v[240:241],v238,s[8:11],s63 offen sc1\n"
MF"v[16:19],a[116:119],a[24:27],v[16:19]\n"
RD"a[80:83],v235 offset:12288\n"
MF"v[56:59],a[116:119],a[28:31],v[56:59]\n"
MF"v[56:59],a[112:115],a[20:23],v[56:59]\n"
RD"a[84:87],v235 offset:13312\n"
MF"v[20:23],a[120:123],a[16:19],v[20:23]\n"
MF"v[20:23],a[124:127],a[24:27],v[20:23]\n"
RD"a[88:91],v235 offset:14336\n"
MF"v[60:63],a[124:127],a[28:31],v[60:63]\n"
MF"v[60:63],a[120:123],a[20:23],v[60:63]\n"
RD"a[92:95],v235 offset:15360\n"
"s_branch LTJ_%=\n"
"L1099_%=:\n"
"s_mov_b32 s54,0\n"
"L1101_%=:\n"
"s_sleep 2\n"
BL"v[240:243],v238,s[8:11],0 offen sc1\n"
WT"vmcnt(0)\n"
SA"s54,s54,1\n"
"v_cmp_eq_u64 vcc,s[28:29],v[240:241]\n"
"v_cmp_eq_u64_e64 s[50:51],s[28:29],v[242:243]\n"
"s_and_b64 vcc,vcc,s[50:51]\n"
"s_cmp_eq_u64 vcc,exec\n"
"s_cbranch_scc1 L1100_%=\n"
"s_cmp_lt_u32 s54,1048576\n"
"s_cbranch_scc1 L1101_%=\n"
"s_branch L1100_%=\n"
"L1102_%=:\n"
"s_mov_b32 s54,0\n"
"L1104_%=:\n"
"s_sleep 2\n"
"buffer_load_dwordx2 v[240:241],v238,s[8:11],s50 offen sc1\n"
WT"vmcnt(0)\n"
SA"s54,s54,1\n"
"v_cmp_eq_u64 vcc,s[28:29],v[240:241]\n"
"s_cmp_eq_u64 vcc,exec\n"
"s_cbranch_scc1 L1103_%=\n"
"s_cmp_lt_u32 s54,1048576\n"
"s_cbranch_scc1 L1104_%=\n"
"s_branch L1103_%=\n"
"L1105_%=:\n"
"s_mov_b32 s54,0\n"
"L1107_%=:\n"
"s_sleep 2\n"
"buffer_load_dwordx2 v[244:245],v242,s[8:11],s23 offen sc1\n"
WT"vmcnt(0)\n"
SA"s54,s54,1\n"
"v_cmp_eq_u64 vcc,s[28:29],v[244:245]\n"
"s_cmp_eq_u64 vcc,exec\n"
"s_cbranch_scc1 L1106_%=\n"
"s_cmp_lt_u32 s54,1048576\n"
"s_cbranch_scc1 L1107_%=\n"
"s_branch L1106_%=\n"
"LFA_%=:\n"
WT"lgkmcnt(6)\n"
MF"v[0:3],a[64:67],a[0:3],v[208:211]\n"
WT"lgkmcnt(3)\n"
PK"v192,v192,v193\n"
PK"v193,v194,v195\n"
MF"v[0:3],a[68:71],a[4:7],v[0:3]\n"
"s_mov_b32 m0,s40\n"
"v_readlane_b32 s43,v228,s42\n"
PK"v194,v196,v197\n"
MF"v[8:11],a[68:71],a[20:23],0\n"
PK"v195,v198,v199\n"
"v_readlane_b32 s44,v229,s42\n"
MF"v[8:11],a[64:67],a[16:19],v[8:11]\n"
SA"s42,s42,1\n"
BL"v225,s[8:11],s43 offen offset:0 sc1 lds\n"
MF"v[16:19],a[64:67],a[32:35],0\n"
"v_accvgpr_write_b32 a44,v192\n"
"v_accvgpr_write_b32 a45,v193\n"
MF"v[16:19],a[68:71],a[36:39],v[16:19]\n"
RD"a[92:95],v226 offset:7168\n"
"v_accvgpr_write_b32 a46,v194\n"
MF"v[24:27],a[68:71],a[52:55],0\n"
"v_accvgpr_write_b32 a47,v195\n"
PK"v200,v200,v201\n"
MF"v[24:27],a[64:67],a[48:51],v[24:27]\n"
PK"v201,v202,v203\n"
PK"v202,v204,v205\n"
"v_mov_b32 v227,v226\n"
MF"v[0:3],a[72:75],a[8:11],v[0:3]\n"
BL"v225,s[8:11],s43 offen offset:1024 sc1 lds\n"
PK"v203,v206,v207\n"
MF"v[0:3],a[76:79],a[12:15],v[0:3]\n"
"v_accvgpr_write_b32 a60,v200\n"
"v_accvgpr_write_b32 a61,v201\n"
MF"v[8:11],a[76:79],a[28:31],v[8:11]\n"
"s_mul_i32 s48,s32,655360\n"
"v_accvgpr_write_b32 a62,v202\n"
"v_accvgpr_write_b32 a63,v203\n"
MF"v[8:11],a[72:75],a[24:27],v[8:11]\n"
SA"s12,s74,s48\n"
"s_addc_u32 s13,s75,0\n"
MF"v[16:19],a[72:75],a[40:43],v[16:19]\n"
"s_and_b32 s13,s13,0xffff\n"
MF"v[16:19],a[76:79],a[44:47],v[16:19]\n"
"s_mov_b32 s14,655360\n"
RD"a[96:99],v226 offset:8192\n"
MF"v[24:27],a[76:79],a[60:63],v[24:27]\n"
"s_lshl_b32 s48,s33,3\n"
BL"v225,s[8:11],s43 offen offset:2048 sc1 lds\n"
MF"v[24:27],a[72:75],a[56:59],v[24:27]\n"
WT"lgkmcnt(1)\n"
EX"v0,v0\n"
MF"v[4:7],a[80:83],a[0:3],v[212:215]\n"
"s_and_b32 s49,s34,2\n"
EX"v1,v1\n"
MF"v[4:7],a[84:87],a[4:7],v[4:7]\n"
"s_lshl_b32 s49,s49,1\n"
EX"v2,v2\n"
MF"v[12:15],a[84:87],a[20:23],0\n"
SA"s48,s48,s49\n"
RD"a[100:103],v226 offset:9216\n"
PK"v0,v0,v1\n"
MF"v[12:15],a[80:83],a[16:19],v[12:15]\n"
SA"s48,s48,s30\n"
EX"v3,v3\n"
MF"v[20:23],a[80:83],a[32:35],0\n"
"s_mul_i32 s48,s48,5120\n"
EX"v8,v8\n"
MF"v[20:23],a[84:87],a[36:39],v[20:23]\n"
"s_or_b32 s49,s34,2\n"
BL"v225,s[8:11],s43 offen offset:3072 sc1 lds\n"
MF"v[28:31],a[84:87],a[52:55],0\n"
"s_cmp_eq_u32 s49,7\n"
EX"v9,v9\n"
PK"v1,v2,v3\n"
MF"v[28:31],a[80:83],a[48:51],v[28:31]\n"
"s_cselect_b32 s55,s48,2130706432\n"
RD"a[104:107],v226 offset:10240\n"
PK"v8,v8,v9\n"
MF"v[4:7],a[88:91],a[8:11],v[4:7]\n"
SA"m0,s40,16384\n"
EX"v10,v10\n"
MF"v[4:7],a[92:95],a[12:15],v[4:7]\n"
EX"v11,v11\n"
MF"v[12:15],a[92:95],a[28:31],v[12:15]\n"
EX"v16,v16\n"
MF"v[12:15],a[88:91],a[24:27],v[12:15]\n"
SA"s40,s40,32768\n"
RD"a[108:111],v226 offset:11264\n"
PK"v9,v10,v11\n"
MF"v[20:23],a[88:91],a[40:43],v[20:23]\n"
"s_cmp_eq_u32 s40,s46\n"
BL"v225,s[8:11],s44 offen offset:0 sc1 lds\n"
EX"v17,v17\n"
MF"v[20:23],a[92:95],a[44:47],v[20:23]\n"
"s_cselect_b32 s40,s47,s40\n"
EX"v18,v18\n"
MF"v[28:31],a[92:95],a[60:63],v[28:31]\n"
EX"v19,v19\n"
PK"v16,v16,v17\n"
MF"v[28:31],a[88:91],a[56:59],v[28:31]\n"
WT"lgkmcnt(1)\n"
RD"a[112:115],v226 offset:12288\n"
PK"v17,v18,v19\n"
MF"v[32:35],a[96:99],a[0:3],v[216:219]\n"
EX"v24,v24\n"
MF"v[32:35],a[100:103],a[4:7],v[32:35]\n"
EX"v25,v25\n"
MF"v[40:43],a[100:103],a[20:23],0\n"
BL"v225,s[8:11],s44 offen offset:1024 sc1 lds\n"
EX"v26,v26\n"
MF"v[40:43],a[96:99],a[16:19],v[40:43]\n"
RD"a[116:119],v226 offset:13312\n"
PK"v24,v24,v25\n"
MF"v[48:51],a[96:99],a[32:35],0\n"
EX"v27,v27\n"
MF"v[48:51],a[100:103],a[36:39],v[48:51]\n"
EX"v4,v4\n"
MF"v[56:59],a[100:103],a[52:55],0\n"
EX"v5,v5\n"
PK"v25,v26,v27\n"
MF"v[56:59],a[96:99],a[48:51],v[56:59]\n"
RD"a[120:123],v226 offset:14336\n"
PK"v2,v4,v5\n"
MF"v[32:35],a[104:107],a[8:11],v[32:35]\n"
WT"lgkmcnt(2)\n"
BL"v225,s[8:11],s44 offen offset:2048 sc1 lds\n"
EX"v6,v6\n"
MF"v[32:35],a[108:111],a[12:15],v[32:35]\n"
EX"v7,v7\n"
MF"v[40:43],a[108:111],a[28:31],v[40:43]\n"
EX"v12,v12\n"
MF"v[40:43],a[104:107],a[24:27],v[40:43]\n"
RD"a[124:127],v226 offset:15360\n"
"v_readlane_b32 s43,v228,s42\n"
PK"v3,v6,v7\n"
MF"v[48:51],a[104:107],a[40:43],v[48:51]\n"
EX"v13,v13\n"
MF"v[48:51],a[108:111],a[44:47],v[48:51]\n"
EX"v14,v14\n"
MF"v[56:59],a[108:111],a[60:63],v[56:59]\n"
BL"v225,s[8:11],s44 offen offset:3072 sc1 lds\n"
EX"v15,v15\n"
MF"v[56:59],a[104:107],a[56:59],v[56:59]\n"
"s_mov_b32 m0,s40\n"
RD"a[96:99],v227 offset:24576\n"
PK"v10,v12,v13\n"
MF"v[36:39],a[112:115],a[0:3],v[220:223]\n"
WT"lgkmcnt(2)\n"
EX"v20,v20\n"
PK"v11,v14,v15\n"
MF"v[36:39],a[116:119],a[4:7],v[36:39]\n"
EX"v21,v21\n"
MF"v[44:47],a[116:119],a[20:23],0\n"
EX"v22,v22\n"
MF"v[44:47],a[112:115],a[16:19],v[44:47]\n"
RD"a[100:103],v227 offset:25600\n"
"v_readlane_b32 s44,v229,s42\n"
PK"v18,v20,v21\n"
MF"v[52:55],a[112:115],a[32:35],0\n"
SA"s42,s42,1\n"
BL"v225,s[8:11],s43 offen offset:0 sc1 lds\n"
MF"v[52:55],a[116:119],a[36:39],v[52:55]\n"
EX"v23,v23\n"
MF"v[60:63],a[116:119],a[52:55],0\n"
EX"v28,v28\n"
MF"v[60:63],a[112:115],a[48:51],v[60:63]\n"
RD"a[104:107],v227 offset:26624\n"
PK"v19,v22,v23\n"
MF"v[36:39],a[120:123],a[8:11],v[36:39]\n"
WT"lgkmcnt(2)\n"
EX"v29,v29\n"
MF"v[36:39],a[124:127],a[12:15],v[36:39]\n"
BL"v225,s[8:11],s43 offen offset:1024 sc1 lds\n"
MF"v[44:47],a[124:127],a[28:31],v[44:47]\n"
EX"v30,v30\n"
PK"v26,v28,v29\n"
MF"v[44:47],a[120:123],a[24:27],v[44:47]\n"
RD"a[108:111],v227 offset:27648\n"
MF"v[52:55],a[120:123],a[40:43],v[52:55]\n"
EX"v31,v31\n"
MF"v[52:55],a[124:127],a[44:47],v[52:55]\n"
EX"v32,v32\n"
MF"v[60:63],a[124:127],a[60:63],v[60:63]\n"
EX"v33,v33\n"
PK"v27,v30,v31\n"
MF"v[60:63],a[120:123],a[56:59],v[60:63]\n"
SA"s41,s41,32768\n"
BL"v225,s[8:11],s43 offen offset:2048 sc1 lds\n"
RD"a[112:115],v227 offset:28672\n"
MF"v[80:83],a[96:99],v[0:3],0\n"
"s_cmp_eq_u32 s41,163840\n"
EX"v34,v34\n"
MF"v[112:115],a[96:99],v[8:11],0\n"
"s_cselect_b32 s41,0,s41\n"
EX"v35,v35\n"
MF"v[144:147],a[96:99],v[16:19],0\n"
EX"v40,v40\n"
"v_add_u32 v226,s41,v224\n"
MF"v[176:179],a[96:99],v[24:27],0\n"
WT"lgkmcnt(1)\n"
RD"a[116:119],v227 offset:29696\n"
MF"v[84:87],a[100:103],v[0:3],0\n"
EX"v41,v41\n"
MF"v[116:119],a[100:103],v[8:11],0\n"
BL"v225,s[8:11],s43 offen offset:3072 sc1 lds\n"
MF"v[148:151],a[100:103],v[16:19],0\n"
SA"m0,s40,16384\n"
EX"v42,v42\n"
MF"v[180:183],a[100:103],v[24:27],0\n"
RD"a[120:123],v227 offset:30720\n"
MF"v[88:91],a[104:107],v[0:3],0\n"
EX"v43,v43\n"
MF"v[120:123],a[104:107],v[8:11],0\n"
EX"v48,v48\n"
MF"v[152:155],a[104:107],v[16:19],0\n"
SA"s40,s40,32768\n"
EX"v49,v49\n"
MF"v[184:187],a[104:107],v[24:27],0\n"
"s_cmp_eq_u32 s40,s46\n"
BL"v225,s[8:11],s44 offen offset:0 sc1 lds\n"
RD"a[124:127],v227 offset:31744\n"
MF"v[92:95],a[108:111],v[0:3],0\n"
"s_cselect_b32 s40,s47,s40\n"
EX"v50,v50\n"
MF"v[124:127],a[108:111],v[8:11],0\n"
EX"v51,v51\n"
MF"v[156:159],a[108:111],v[16:19],0\n"
EX"v56,v56\n"
MF"v[188:191],a[108:111],v[24:27],0\n"
WT"lgkmcnt(1)\n"
EX"v57,v57\n"
MF"v[96:99],a[112:115],v[0:3],0\n"
EX"v58,v58\n"
MF"v[128:131],a[112:115],v[8:11],0\n"
BL"v225,s[8:11],s44 offen offset:1024 sc1 lds\n"
MF"v[160:163],a[112:115],v[16:19],0\n"
EX"v59,v59\n"
MF"v[192:195],a[112:115],v[24:27],0\n"
EX"v36,v36\n"
MF"v[100:103],a[116:119],v[0:3],0\n"
WT"vmcnt(16) lgkmcnt(0)\n"
"s_barrier\n"
MF"v[132:135],a[116:119],v[8:11],0\n"
RD"a[64:67],v226 offset:0\n"
RD"a[68:71],v226 offset:1024\n"
"buffer_store_dwordx2 v[232:233],v230,s[8:11],s98 offen sc1\n"
MF"v[164:167],a[116:119],v[16:19],0\n"
EX"v37,v37\n"
MF"v[196:199],a[116:119],v[24:27],0\n"
BL"v225,s[8:11],s44 offen offset:2048 sc1 lds\n"
MF"v[104:107],a[120:123],v[0:3],0\n"
EX"v38,v38\n"
MF"v[136:139],a[120:123],v[8:11],0\n"
EX"v39,v39\n"
MF"v[168:171],a[120:123],v[16:19],0\n"
EX"v44,v44\n"
MF"v[200:203],a[120:123],v[24:27],0\n"
RD"a[72:75],v226 offset:2048\n"
RD"a[76:79],v226 offset:3072\n"
MF"v[108:111],a[124:127],v[0:3],0\n"
EX"v45,v45\n"
MF"v[140:143],a[124:127],v[8:11],0\n"
BL"v225,s[8:11],s44 offen offset:3072 sc1 lds\n"
MF"v[172:175],a[124:127],v[16:19],0\n"
EX"v46,v46\n"
MF"v[204:207],a[124:127],v[24:27],0\n"
RD"a[80:83],v226 offset:4096\n"
MF"a[132:135],a[128:131],v[0:3],0\n"
EX"v47,v47\n"
MF"a[136:139],a[128:131],v[8:11],0\n"
"buffer_load_dword v255,v253,s[12:15],s55 offen\n"
EX"v52,v52\n"
MF"a[140:143],a[128:131],v[16:19],0\n"
SA"s55,s55,163840\n"
EX"v53,v53\n"
MF"a[144:147],a[128:131],v[24:27],0\n"
RD"a[84:87],v226 offset:5120\n"
RD"a[88:91],v226 offset:6144\n"
EX"v54,v54\n"
EX"v55,v55\n"
EX"v60,v60\n"
EX"v61,v61\n"
EX"v62,v62\n"
EX"v63,v63\n"
"s_branch LDB_%=\n"
"LD2_%=:\n"
WT"lgkmcnt(6)\n"
MF"v[0:3],a[64:67],a[32:35],v[208:211]\n"
WT"lgkmcnt(3)\n"
EX"v56,v56\n"
MF"v[0:3],a[68:71],a[36:39],v[0:3]\n"
"s_mov_b32 m0,s40\n"
EX"v57,v57\n"
MF"v[8:11],a[68:71],a[52:55],0\n"
RD"a[92:95],v226 offset:7168\n"
"v_readlane_b32 s43,v228,s42\n"
MF"v[8:11],a[64:67],a[48:51],v[8:11]\n"
EX"v58,v58\n"
MF"v[0:3],a[72:75],a[40:43],v[0:3]\n"
SA"s41,s41,32768\n"
RD"a[96:99],v226 offset:8192\n"
RD"a[100:103],v226 offset:9216\n"
"v_readlane_b32 s44,v229,s42\n"
MF"v[0:3],a[76:79],a[44:47],v[0:3]\n"
SA"s42,s42,1\n"
BL"v225,s[8:11],s43 offen offset:0 sc1 lds\n"
PK"v76,v56,v57\n"
MF"v[8:11],a[76:79],a[60:63],v[8:11]\n"
"s_cmp_eq_u32 s41,163840\n"
EX"v59,v59\n"
MF"v[8:11],a[72:75],a[56:59],v[8:11]\n"
WT"lgkmcnt(3)\n"
RD"a[104:107],v226 offset:10240\n"
PK"v77,v58,v59\n"
MF"v[4:7],a[80:83],a[32:35],v[212:215]\n"
"s_cselect_b32 s41,0,s41\n"
EX"v60,v60\n"
"v_add_u32 v227,s41,v224\n"
MF"v[4:7],a[84:87],a[36:39],v[4:7]\n"
EX"v61,v61\n"
MF"v[12:15],a[84:87],a[52:55],0\n"
BL"v225,s[8:11],s43 offen offset:1024 sc1 lds\n"
MF"v[12:15],a[80:83],a[48:51],v[12:15]\n"
RD"a[108:111],v226 offset:11264\n"
RD"a[112:115],v226 offset:12288\n"
MF"v[4:7],a[88:91],a[40:43],v[4:7]\n"
WT"lgkmcnt(3)\n"
EX"v62,v62\n"
PK"v78,v60,v61\n"
MF"v[4:7],a[92:95],a[44:47],v[4:7]\n"
RD"a[116:119],v226 offset:13312\n"
RD"a[120:123],v226 offset:14336\n"
MF"v[12:15],a[92:95],a[60:63],v[12:15]\n"
EX"v63,v63\n"
MF"v[12:15],a[88:91],a[56:59],v[12:15]\n"
BL"v225,s[8:11],s43 offen offset:2048 sc1 lds\n"
MF"v[16:19],a[96:99],a[32:35],v[216:219]\n"
EX"v0,v0\n"
PK"v79,v62,v63\n"
MF"v[16:19],a[100:103],a[36:39],v[16:19]\n"
EX"v1,v1\n"
MF"v[24:27],a[100:103],a[52:55],0\n"
RD"a[124:127],v226 offset:15360\n"
RD"a[64:67],v226 offset:16384\n"
MF"v[24:27],a[96:99],a[48:51],v[24:27]\n"
WT"lgkmcnt(4)\n"
EX"v2,v2\n"
PK"v0,v0,v1\n"
MF"v[16:19],a[104:107],a[40:43],v[16:19]\n"
BL"v225,s[8:11],s43 offen offset:3072 sc1 lds\n"
RD"a[68:71],v226 offset:17408\n"
MF"v[16:19],a[108:111],a[44:47],v[16:19]\n"
SA"m0,s40,16384\n"
RD"a[72:75],v226 offset:18432\n"
RD"a[76:79],v226 offset:19456\n"
EX"v3,v3\n"
MF"v[24:27],a[108:111],a[60:63],v[24:27]\n"
RD"a[80:83],v226 offset:20480\n"
RD"a[84:87],v226 offset:21504\n"
MF"v[24:27],a[104:107],a[56:59],v[24:27]\n"
EX"v8,v8\n"
PK"v1,v2,v3\n"
MF"v[20:23],a[112:115],a[32:35],v[220:223]\n"
WT"lgkmcnt(7)\n"
EX"v9,v9\n"
MF"v[20:23],a[116:119],a[36:39],v[20:23]\n"
SA"s40,s40,32768\n"
BL"v225,s[8:11],s44 offen offset:0 sc1 lds\n"
MF"v[28:31],a[116:119],a[52:55],0\n"
"s_cmp_eq_u32 s40,s46\n"
RD"a[88:91],v226 offset:22528\n"
RD"a[92:95],v226 offset:23552\n"
EX"v10,v10\n"
MF"v[28:31],a[112:115],a[48:51],v[28:31]\n"
"s_cselect_b32 s40,s47,s40\n"
RD"a[96:99],v226 offset:24576\n"
RD"a[100:103],v226 offset:25600\n"
PK"v8,v8,v9\n"
MF"v[20:23],a[120:123],a[40:43],v[20:23]\n"
WT"lgkmcnt(6)\n"
EX"v11,v11\n"
MF"v[20:23],a[124:127],a[44:47],v[20:23]\n"
RD"a[104:107],v226 offset:26624\n"
RD"a[108:111],v226 offset:27648\n"
MF"v[28:31],a[124:127],a[60:63],v[28:31]\n"
BL"v225,s[8:11],s44 offen offset:1024 sc1 lds\n"
EX"v4,v4\n"
MF"v[28:31],a[120:123],a[56:59],v[28:31]\n"
RD"a[112:115],v226 offset:28672\n"
RD"a[116:119],v226 offset:29696\n"
PK"v9,v10,v11\n"
MF"v[40:43],a[64:67],a[48:51],v[208:211]\n"
EX"v5,v5\n"
MF"v[40:43],a[68:71],a[52:55],v[40:43]\n"
EX"v6,v6\n"
MF"v[40:43],a[72:75],a[56:59],v[40:43]\n"
RD"a[120:123],v226 offset:30720\n"
RD"a[124:127],v226 offset:31744\n"
PK"v2,v4,v5\n"
MF"v[40:43],a[76:79],a[60:63],v[40:43]\n"
WT"lgkmcnt(6)\n"
BL"v225,s[8:11],s44 offen offset:2048 sc1 lds\n"
EX"v7,v7\n"
MF"v[44:47],a[80:83],a[48:51],v[212:215]\n"
WT"vmcnt(19)\n"
"s_barrier\n"
MF"v[44:47],a[84:87],a[52:55],v[44:47]\n"
EX"v12,v12\n"
PK"v3,v6,v7\n"
MF"v[44:47],a[88:91],a[56:59],v[44:47]\n"
EX"v13,v13\n"
MF"v[44:47],a[92:95],a[60:63],v[44:47]\n"
BL"v225,s[8:11],s44 offen offset:3072 sc1 lds\n"
"v_readlane_b32 s43,v228,s42\n"
MF"v[56:59],a[96:99],a[48:51],v[216:219]\n"
"s_mov_b32 m0,s40\n"
RD"a[96:99],v227 offset:8192\n"
PK"v10,v12,v13\n"
EX"v14,v14\n"
MF"v[56:59],a[100:103],a[52:55],v[56:59]\n"
WT"lgkmcnt(1)\n"
RD"a[100:103],v227 offset:9216\n"
MF"v[56:59],a[104:107],a[56:59],v[56:59]\n"
EX"v15,v15\n"
MF"v[56:59],a[108:111],a[60:63],v[56:59]\n"
RD"a[104:107],v227 offset:10240\n"
RD"a[108:111],v227 offset:11264\n"
MF"v[60:63],a[112:115],a[48:51],v[220:223]\n"
EX"v16,v16\n"
PK"v11,v14,v15\n"
MF"v[60:63],a[116:119],a[52:55],v[60:63]\n"
EX"v17,v17\n"
"v_readlane_b32 s44,v229,s42\n"
MF"v[60:63],a[120:123],a[56:59],v[60:63]\n"
SA"s42,s42,1\n"
BL"v225,s[8:11],s43 offen offset:0 sc1 lds\n"
MF"v[60:63],a[124:127],a[60:63],v[60:63]\n"
WT"lgkmcnt(3)\n"
EX"v18,v18\n"
MF"v[112:115],a[96:99],v[68:71],v[112:115]\n"
SA"s41,s41,32768\n"
RD"a[112:115],v227 offset:12288\n"
PK"v16,v16,v17\n"
MF"v[144:147],a[96:99],v[72:75],v[144:147]\n"
"s_cmp_eq_u32 s41,163840\n"
EX"v19,v19\n"
MF"v[176:179],a[96:99],v[76:79],v[176:179]\n"
WT"lgkmcnt(1)\n"
EX"v24,v24\n"
MF"v[116:119],a[100:103],v[68:71],v[116:119]\n"
"s_cselect_b32 s41,0,s41\n"
BL"v225,s[8:11],s43 offen offset:1024 sc1 lds\n"
RD"a[116:119],v227 offset:13312\n"
MF"v[148:151],a[100:103],v[72:75],v[148:151]\n"
EX"v25,v25\n"
"v_add_u32 v226,s41,v224\n"
MF"v[180:183],a[100:103],v[76:79],v[180:183]\n"
RD"a[120:123],v227 offset:14336\n"
PK"v17,v18,v19\n"
MF"v[120:123],a[104:107],v[68:71],v[120:123]\n"
EX"v26,v26\n"
MF"v[152:155],a[104:107],v[72:75],v[152:155]\n"
EX"v27,v27\n"
PK"v24,v24,v25\n"
MF"v[184:187],a[104:107],v[76:79],v[184:187]\n"
BL"v225,s[8:11],s43 offen offset:2048 sc1 lds\n"
MF"v[124:127],a[108:111],v[68:71],v[124:127]\n"
EX"v20,v20\n"
MF"v[156:159],a[108:111],v[72:75],v[156:159]\n"
RD"a[124:127],v227 offset:15360\n"
PK"v25,v26,v27\n"
MF"v[188:191],a[108:111],v[76:79],v[188:191]\n"
WT"lgkmcnt(2)\n"
EX"v21,v21\n"
MF"v[128:131],a[112:115],v[68:71],v[128:131]\n"
BL"v225,s[8:11],s43 offen offset:3072 sc1 lds\n"
MF"v[160:163],a[112:115],v[72:75],v[160:163]\n"
SA"m0,s40,16384\n"
EX"v22,v22\n"
MF"v[192:195],a[112:115],v[76:79],v[192:195]\n"
EX"v23,v23\n"
PK"v18,v20,v21\n"
MF"v[132:135],a[116:119],v[68:71],v[132:135]\n"
RD"a[64:67],v227 offset:16384\n"
PK"v19,v22,v23\n"
MF"v[164:167],a[116:119],v[72:75],v[164:167]\n"
EX"v28,v28\n"
MF"v[196:199],a[116:119],v[76:79],v[196:199]\n"
WT"lgkmcnt(1)\n"
BL"v225,s[8:11],s44 offen offset:0 sc1 lds\n"
MF"v[136:139],a[120:123],v[68:71],v[136:139]\n"
SA"s40,s40,32768\n"
EX"v29,v29\n"
MF"v[168:171],a[120:123],v[72:75],v[168:171]\n"
"s_cmp_eq_u32 s40,s46\n"
RD"a[68:71],v227 offset:24576\n"
RD"a[72:75],v227 offset:17408\n"
MF"v[200:203],a[120:123],v[76:79],v[200:203]\n"
"s_cselect_b32 s40,s47,s40\n"
EX"v30,v30\n"
MF"v[140:143],a[124:127],v[68:71],v[140:143]\n"
RD"a[76:79],v227 offset:25600\n"
RD"a[80:83],v227 offset:18432\n"
PK"v26,v28,v29\n"
MF"v[172:175],a[124:127],v[72:75],v[172:175]\n"
EX"v31,v31\n"
MF"v[204:207],a[124:127],v[76:79],v[204:207]\n"
BL"v225,s[8:11],s44 offen offset:1024 sc1 lds\n"
MF"a[136:139],a[128:131],v[68:71],a[136:139]\n"
EX"v40,v40\n"
PK"v27,v30,v31\n"
MF"a[140:143],a[128:131],v[72:75],a[140:143]\n"
EX"v41,v41\n"
MF"a[144:147],a[128:131],v[76:79],a[144:147]\n"
WT"lgkmcnt(2)\n"
RD"a[84:87],v227 offset:26624\n"
PK"v68,v40,v41\n"
MF"v[144:147],a[64:67],v[0:3],v[144:147]\n"
BL"v225,s[8:11],s44 offen offset:2048 sc1 lds\n"
MF"v[144:147],a[68:71],v[16:19],v[144:147]\n"
EX"v42,v42\n"
MF"v[176:179],a[68:71],v[24:27],v[176:179]\n"
SA"s51,s59,16384\n"
RD"a[88:91],v227 offset:19456\n"
RD"a[92:95],v227 offset:27648\n"
MF"v[176:179],a[64:67],v[8:11],v[176:179]\n"
EX"v43,v43\n"
MF"v[148:151],a[72:75],v[0:3],v[148:151]\n"
WT"lgkmcnt(3)\n"
EX"v44,v44\n"
MF"v[148:151],a[76:79],v[16:19],v[148:151]\n"
RD"a[96:99],v227 offset:20480\n"
"v_accvgpr_read_b32 v252,a136\n"
"v_subrev_u32 v253,0x2b800000,v252\n"
MF"v[180:183],a[76:79],v[24:27],v[180:183]\n"
"v_cmp_le_u32 vcc,0x34000000,v253\n"
"s_cmp_lg_u64 vcc,0\n"
"s_cbranch_scc1 L1035_%=\n"
"L1034_%=:\n"
MF"v[180:183],a[72:75],v[8:11],v[180:183]\n"
BL"v225,s[8:11],s44 offen offset:3072 sc1 lds\n"
EX"v45,v45\n"
MF"v[152:155],a[80:83],v[0:3],v[152:155]\n"
WT"lgkmcnt(3)\n"
"v_rcp_f32 v252,v252\n"
EX"v46,v46\n"
MF"v[152:155],a[84:87],v[16:19],v[152:155]\n"
"s_mov_b32 m0,s40\n"
RD"a[100:103],v227 offset:28672\n"
RD"a[104:107],v227 offset:21504\n"
"v_readlane_b32 s43,v228,s42\n"
MF"v[184:187],a[84:87],v[24:27],v[184:187]\n"
EX"v47,v47\n"
PK"v69,v42,v43\n"
MF"v[184:187],a[80:83],v[8:11],v[184:187]\n"
WT"lgkmcnt(3)\n"
RD"a[108:111],v227 offset:29696\n"
RD"a[112:115],v227 offset:22528\n"
EX"v56,v56\n"
MF"v[156:159],a[88:91],v[0:3],v[156:159]\n"
EX"v57,v57\n"
"v_readlane_b32 s44,v229,s42\n"
MF"v[156:159],a[92:95],v[16:19],v[156:159]\n"
SA"s42,s42,1\n"
EX"v58,v58\n"
PK"v70,v44,v45\n"
MF"v[188:191],a[92:95],v[24:27],v[188:191]\n"
RD"a[116:119],v227 offset:30720\n"
MU"v112,v252,v112\n"
MU"v113,v252,v113\n"
MF"v[188:191],a[88:91],v[8:11],v[188:191]\n"
WT"lgkmcnt(5)\n"
EX"v59,v59\n"
MU"v114,v252,v114\n"
MF"v[160:163],a[96:99],v[0:3],v[160:163]\n"
WT"lgkmcnt(3)\n"
EX"v60,v60\n"
MU"v115,v252,v115\n"
MF"v[160:163],a[100:103],v[16:19],v[160:163]\n"
RD"a[120:123],v227 offset:23552\n"
RD"a[124:127],v227 offset:31744\n"
MU"v116,v252,v116\n"
MF"v[192:195],a[100:103],v[24:27],v[192:195]\n"
EX"v61,v61\n"
MU"v117,v252,v117\n"
MF"v[192:195],a[96:99],v[8:11],v[192:195]\n"
MU"v118,v252,v118\n"
MU"v119,v252,v119\n"
PK"v71,v46,v47\n"
MF"v[164:167],a[104:107],v[0:3],v[164:167]\n"
WT"lgkmcnt(3)\n"
EX"v62,v62\n"
PK"v112,v112,v113\n"
MF"v[164:167],a[108:111],v[16:19],v[164:167]\n"
SA"s41,s41,32768\n"
WT"vmcnt(20)\n"
"s_barrier\n"
MF"v[196:199],a[108:111],v[24:27],v[196:199]\n"
"s_cmp_eq_u32 s41,163840\n"
BL"v225,s[8:11],s43 offen offset:0 sc1 lds\n"
BL"v225,s[8:11],s43 offen offset:1024 sc1 lds\n"
MF"v[196:199],a[104:107],v[8:11],v[196:199]\n"
"s_cselect_b32 s41,0,s41\n"
BL"v225,s[8:11],s43 offen offset:2048 sc1 lds\n"
RD"a[64:67],v226 offset:0\n"
MF"v[168:171],a[112:115],v[0:3],v[168:171]\n"
WT"lgkmcnt(1)\n"
EX"v63,v63\n"
"v_add_u32 v227,s41,v224\n"
MF"v[168:171],a[116:119],v[16:19],v[168:171]\n"
RD"a[68:71],v226 offset:8192\n"
RD"a[72:75],v226 offset:1024\n"
PK"v113,v114,v115\n"
MF"v[200:203],a[116:119],v[24:27],v[200:203]\n"
RD"a[76:79],v226 offset:9216\n"
RD"a[80:83],v226 offset:2048\n"
PK"v114,v116,v117\n"
MF"v[200:203],a[112:115],v[8:11],v[200:203]\n"
BL"v225,s[8:11],s43 offen offset:3072 sc1 lds\n"
PK"v115,v118,v119\n"
MF"v[172:175],a[120:123],v[0:3],v[172:175]\n"
SA"m0,s40,16384\n"
BS"v[112:115],v224,s[8:11],s51 offen offset:0 sc1\n"
RD"a[84:87],v226 offset:10240\n"
MF"v[172:175],a[124:127],v[16:19],v[172:175]\n"
RD"a[88:91],v226 offset:3072\n"
RD"a[92:95],v226 offset:11264\n"
MU"v120,v252,v120\n"
MF"v[204:207],a[124:127],v[24:27],v[204:207]\n"
BL"v225,s[8:11],s44 offen offset:0 sc1 lds\n"
MU"v121,v252,v121\n"
MF"v[204:207],a[120:123],v[8:11],v[204:207]\n"
SA"s40,s40,32768\n"
RD"a[96:99],v226 offset:4096\n"
RD"a[100:103],v226 offset:12288\n"
PK"v76,v56,v57\n"
MF"a[140:143],a[128:131],v[0:3],a[140:143]\n"
"s_cmp_eq_u32 s40,s46\n"
RD"a[104:107],v226 offset:5120\n"
RD"a[108:111],v226 offset:13312\n"
MU"v122,v252,v122\n"
MF"a[140:143],a[128:131],v[16:19],a[140:143]\n"
"s_cselect_b32 s40,s47,s40\n"
RD"a[112:115],v226 offset:6144\n"
MU"v123,v252,v123\n"
MU"v124,v252,v124\n"
MF"a[144:147],a[128:131],v[24:27],a[144:147]\n"
PK"v77,v58,v59\n"
PK"v78,v60,v61\n"
PK"v79,v62,v63\n"
MF"a[144:147],a[128:131],v[8:11],a[144:147]\n"
WT"lgkmcnt(7)\n"
RD"a[116:119],v226 offset:14336\n"
RD"a[120:123],v226 offset:7168\n"
MU"v125,v252,v125\n"
MF"v[176:179],a[64:67],v[68:71],v[176:179]\n"
BL"v225,s[8:11],s44 offen offset:1024 sc1 lds\n"
MU"v126,v252,v126\n"
MF"v[176:179],a[68:71],v[76:79],v[176:179]\n"
RD"a[124:127],v226 offset:15360\n"
MU"v127,v252,v127\n"
PK"v120,v120,v121\n"
MF"v[180:183],a[72:75],v[68:71],v[180:183]\n"
WT"vmcnt(15)\n"
"s_barrier\n"
MF"v[180:183],a[76:79],v[76:79],v[180:183]\n"
RD"a[64:67],v227 offset:0\n"
PK"v121,v122,v123\n"
PK"v122,v124,v125\n"
MF"v[184:187],a[80:83],v[68:71],v[184:187]\n"
BL"v225,s[8:11],s44 offen offset:2048 sc1 lds\n"
PK"v123,v126,v127\n"
MF"v[184:187],a[84:87],v[76:79],v[184:187]\n"
WT"lgkmcnt(4)\n"
BS"v[120:123],v224,s[8:11],s51 offen offset:1024 sc1\n"
MU"v128,v252,v128\n"
MF"v[188:191],a[88:91],v[68:71],v[188:191]\n"
RD"a[68:71],v227 offset:1024\n"
RD"a[72:75],v227 offset:2048\n"
MU"v129,v252,v129\n"
MF"v[188:191],a[92:95],v[76:79],v[188:191]\n"
MU"v130,v252,v130\n"
MU"v131,v252,v131\n"
MU"v132,v252,v132\n"
MF"v[192:195],a[96:99],v[68:71],v[192:195]\n"
MU"v133,v252,v133\n"
MU"v134,v252,v134\n"
MU"v135,v252,v135\n"
MF"v[192:195],a[100:103],v[76:79],v[192:195]\n"
BL"v225,s[8:11],s44 offen offset:3072 sc1 lds\n"
PK"v128,v128,v129\n"
MF"v[196:199],a[104:107],v[68:71],v[196:199]\n"
RD"a[76:79],v227 offset:3072\n"
PK"v129,v130,v131\n"
PK"v130,v132,v133\n"
MF"v[196:199],a[108:111],v[76:79],v[196:199]\n"
PK"v131,v134,v135\n"
MU"v136,v252,v136\n"
MU"v137,v252,v137\n"
MF"v[200:203],a[112:115],v[68:71],v[200:203]\n"
WT"lgkmcnt(3)\n"
BS"v[128:131],v224,s[8:11],s51 offen offset:2048 sc1\n"
MU"v138,v252,v138\n"
MF"v[200:203],a[116:119],v[76:79],v[200:203]\n"
RD"a[80:83],v227 offset:4096\n"
RD"a[84:87],v227 offset:5120\n"
MU"v139,v252,v139\n"
MF"v[204:207],a[120:123],v[68:71],v[204:207]\n"
RD"a[88:91],v227 offset:6144\n"
MU"v140,v252,v140\n"
MU"v141,v252,v141\n"
MF"v[204:207],a[124:127],v[76:79],v[204:207]\n"
PK"v136,v136,v137\n"
MU"v142,v252,v142\n"
MU"v143,v252,v143\n"
MF"a[144:147],a[128:131],v[68:71],a[144:147]\n"
PK"v137,v138,v139\n"
PK"v138,v140,v141\n"
PK"v139,v142,v143\n"
MF"a[144:147],a[128:131],v[76:79],a[144:147]\n"
BS"v[136:139],v224,s[8:11],s51 offen offset:3072 sc1\n"
"s_branch LTL_%=\n"
"LEV_%=:\n"
"s_mov_b32 s14,0\n"
WT"lgkmcnt(6)\n"
MF"v[0:3],a[64:67],a[0:3],0\n"
WT"lgkmcnt(5)\n"
PK"v64,v32,v33\n"
PK"v65,v34,v35\n"
MF"v[0:3],a[68:71],a[4:7],v[0:3]\n"
SA"s95,s40,32768\n"
PK"v66,v36,v37\n"
PK"v67,v38,v39\n"
MF"v[8:11],a[68:71],a[20:23],0\n"
"s_cmp_eq_u32 s95,s46\n"
PK"v68,v40,v41\n"
PK"v69,v42,v43\n"
MF"v[8:11],a[64:67],a[16:19],v[8:11]\n"
"s_cselect_b32 s95,s47,s95\n"
PK"v70,v44,v45\n"
PK"v71,v46,v47\n"
MF"v[16:19],a[64:67],a[32:35],0\n"
SA"m0,s95,0\n"
PK"v72,v48,v49\n"
PK"v73,v50,v51\n"
MF"v[16:19],a[68:71],a[36:39],v[16:19]\n"
RD"a[92:95],v226 offset:7168\n"
"v_mov_b32 v227,v226\n"
MF"v[24:27],a[68:71],a[52:55],0\n"
PK"v74,v52,v53\n"
PK"v75,v54,v55\n"
MF"v[24:27],a[64:67],a[48:51],v[24:27]\n"
WT"lgkmcnt(1)\n"
BL"v235,s[24:27],s52 offen lds\n"
MF"v[0:3],a[72:75],a[8:11],v[0:3]\n"
SA"s52,s52,s62\n"
PK"v76,v56,v57\n"
PK"v77,v58,v59\n"
MF"v[0:3],a[76:79],a[12:15],v[0:3]\n"
PK"v78,v60,v61\n"
PK"v79,v62,v63\n"
MF"v[8:11],a[76:79],a[28:31],v[8:11]\n"
MF"v[8:11],a[72:75],a[24:27],v[8:11]\n"
MF"v[16:19],a[72:75],a[40:43],v[16:19]\n"
SA"m0,s95,1024\n"
MF"v[16:19],a[76:79],a[44:47],v[16:19]\n"
RD"a[96:99],v226 offset:8192\n"
MF"v[24:27],a[76:79],a[60:63],v[24:27]\n"
EX"v0,v0\n"
MF"v[24:27],a[72:75],a[56:59],v[24:27]\n"
BL"v235,s[24:27],s52 offen lds\n"
MF"v[4:7],a[80:83],a[0:3],0\n"
SA"s52,s52,s62\n"
EX"v1,v1\n"
MF"v[4:7],a[84:87],a[4:7],v[4:7]\n"
RD"a[100:103],v226 offset:9216\n"
PK"v0,v0,v1\n"
MF"v[12:15],a[84:87],a[20:23],0\n"
EX"v2,v2\n"
MF"v[12:15],a[80:83],a[16:19],v[12:15]\n"
EX"v3,v3\n"
MF"v[20:23],a[80:83],a[32:35],0\n"
SA"m0,s95,2048\n"
EX"v8,v8\n"
MF"v[20:23],a[84:87],a[36:39],v[20:23]\n"
RD"a[104:107],v226 offset:10240\n"
PK"v1,v2,v3\n"
MF"v[28:31],a[84:87],a[52:55],0\n"
EX"v9,v9\n"
MF"v[28:31],a[80:83],a[48:51],v[28:31]\n"
BL"v235,s[24:27],s52 offen lds\n"
MF"v[4:7],a[88:91],a[8:11],v[4:7]\n"
WT"lgkmcnt(1)\n"
EX"v10,v10\n"
MF"v[4:7],a[92:95],a[12:15],v[4:7]\n"
SA"s52,s52,s62\n"
RD"a[108:111],v226 offset:11264\n"
PK"v8,v8,v9\n"
MF"v[12:15],a[92:95],a[28:31],v[12:15]\n"
EX"v11,v11\n"
MF"v[12:15],a[88:91],a[24:27],v[12:15]\n"
EX"v16,v16\n"
MF"v[20:23],a[88:91],a[40:43],v[20:23]\n"
SA"m0,s95,3072\n"
EX"v17,v17\n"
MF"v[20:23],a[92:95],a[44:47],v[20:23]\n"
RD"a[112:115],v226 offset:12288\n"
PK"v9,v10,v11\n"
MF"v[28:31],a[92:95],a[60:63],v[28:31]\n"
EX"v18,v18\n"
MF"v[28:31],a[88:91],a[56:59],v[28:31]\n"
BL"v235,s[24:27],s52 offen lds\n"
MF"v[32:35],a[96:99],a[0:3],0\n"
SA"s52,s52,s62\n"
EX"v19,v19\n"
MF"v[32:35],a[100:103],a[4:7],v[32:35]\n"
RD"a[116:119],v226 offset:13312\n"
PK"v16,v16,v17\n"
MF"v[40:43],a[100:103],a[20:23],0\n"
EX"v24,v24\n"
MF"v[40:43],a[96:99],a[16:19],v[40:43]\n"
EX"v25,v25\n"
MF"v[48:51],a[96:99],a[32:35],0\n"
SA"m0,s95,16384\n"
EX"v26,v26\n"
MF"v[48:51],a[100:103],a[36:39],v[48:51]\n"
RD"a[120:123],v226 offset:14336\n"
PK"v17,v18,v19\n"
MF"v[56:59],a[100:103],a[52:55],0\n"
EX"v27,v27\n"
MF"v[56:59],a[96:99],a[48:51],v[56:59]\n"
WT"lgkmcnt(2)\n"
BL"v235,s[24:27],s52 offen lds\n"
MF"v[32:35],a[104:107],a[8:11],v[32:35]\n"
SA"s52,s52,s62\n"
EX"v4,v4\n"
MF"v[32:35],a[108:111],a[12:15],v[32:35]\n"
RD"a[124:127],v226 offset:15360\n"
PK"v24,v24,v25\n"
MF"v[40:43],a[108:111],a[28:31],v[40:43]\n"
EX"v5,v5\n"
MF"v[40:43],a[104:107],a[24:27],v[40:43]\n"
EX"v6,v6\n"
MF"v[48:51],a[104:107],a[40:43],v[48:51]\n"
SA"m0,s95,17408\n"
EX"v7,v7\n"
MF"v[48:51],a[108:111],a[44:47],v[48:51]\n"
PK"v25,v26,v27\n"
PK"v2,v4,v5\n"
MF"v[56:59],a[108:111],a[60:63],v[56:59]\n"
EX"v12,v12\n"
MF"v[56:59],a[104:107],a[56:59],v[56:59]\n"
BL"v235,s[24:27],s52 offen lds\n"
MF"v[36:39],a[112:115],a[0:3],0\n"
WT"lgkmcnt(0)\n"
EX"v13,v13\n"
MF"v[36:39],a[116:119],a[4:7],v[36:39]\n"
SA"s52,s52,s62\n"
RD"a[64:67],v227 offset:16384\n"
PK"v3,v6,v7\n"
MF"v[44:47],a[116:119],a[20:23],0\n"
EX"v14,v14\n"
MF"v[44:47],a[112:115],a[16:19],v[44:47]\n"
PK"v10,v12,v13\n"
MF"v[52:55],a[112:115],a[32:35],0\n"
SA"m0,s95,18432\n"
EX"v15,v15\n"
MF"v[52:55],a[116:119],a[36:39],v[52:55]\n"
RD"a[68:71],v227 offset:17408\n"
PK"v11,v14,v15\n"
MF"v[60:63],a[116:119],a[52:55],0\n"
EX"v20,v20\n"
MF"v[60:63],a[112:115],a[48:51],v[60:63]\n"
BL"v235,s[24:27],s52 offen lds\n"
MF"v[36:39],a[120:123],a[8:11],v[36:39]\n"
SA"s52,s52,s62\n"
EX"v21,v21\n"
MF"v[36:39],a[124:127],a[12:15],v[36:39]\n"
RD"a[72:75],v227 offset:18432\n"
"v_readlane_b32 s43,v228,s42\n"
MF"v[44:47],a[124:127],a[28:31],v[44:47]\n"
EX"v22,v22\n"
MF"v[44:47],a[120:123],a[24:27],v[44:47]\n"
"s_mov_b32 m0,s40\n"
PK"v18,v20,v21\n"
MF"v[52:55],a[120:123],a[40:43],v[52:55]\n"
EX"v23,v23\n"
MF"v[52:55],a[124:127],a[44:47],v[52:55]\n"
RD"a[76:79],v227 offset:19456\n"
"v_readlane_b32 s44,v229,s42\n"
MF"v[60:63],a[124:127],a[60:63],v[60:63]\n"
EX"v28,v28\n"
MF"v[60:63],a[120:123],a[56:59],v[60:63]\n"
WT"lgkmcnt(2)\n"
BL"v225,s[8:11],s43 offen offset:0 sc1 lds\n"
MF"v[80:83],a[64:67],v[64:67],v[80:83]\n"
SA"s42,s42,1\n"
EX"v29,v29\n"
MF"v[112:115],a[64:67],v[68:71],v[112:115]\n"
SA"s41,s41,32768\n"
RD"a[80:83],v227 offset:20480\n"
PK"v19,v22,v23\n"
MF"v[144:147],a[64:67],v[72:75],v[144:147]\n"
"s_cmp_eq_u32 s41,163840\n"
EX"v30,v30\n"
MF"v[176:179],a[64:67],v[76:79],v[176:179]\n"
"s_cselect_b32 s41,0,s41\n"
PK"v26,v28,v29\n"
"v_add_u32 v226,s41,v224\n"
MF"v[84:87],a[68:71],v[64:67],v[84:87]\n"
EX"v31,v31\n"
MF"v[116:119],a[68:71],v[68:71],v[116:119]\n"
RD"a[84:87],v227 offset:21504\n"
PK"v27,v30,v31\n"
MF"v[148:151],a[68:71],v[72:75],v[148:151]\n"
EX"v32,v32\n"
MF"v[180:183],a[68:71],v[76:79],v[180:183]\n"
WT"lgkmcnt(2)\n"
BL"v225,s[8:11],s43 offen offset:1024 sc1 lds\n"
MF"v[88:91],a[72:75],v[64:67],v[88:91]\n"
EX"v33,v33\n"
MF"v[120:123],a[72:75],v[68:71],v[120:123]\n"
RD"a[88:91],v227 offset:22528\n"
MF"v[152:155],a[72:75],v[72:75],v[152:155]\n"
EX"v34,v34\n"
MF"v[184:187],a[72:75],v[76:79],v[184:187]\n"
MF"v[92:95],a[76:79],v[64:67],v[92:95]\n"
EX"v35,v35\n"
MF"v[124:127],a[76:79],v[68:71],v[124:127]\n"
RD"a[92:95],v227 offset:23552\n"
MF"v[156:159],a[76:79],v[72:75],v[156:159]\n"
EX"v40,v40\n"
MF"v[188:191],a[76:79],v[76:79],v[188:191]\n"
WT"lgkmcnt(2)\n"
BL"v225,s[8:11],s43 offen offset:2048 sc1 lds\n"
MF"v[96:99],a[80:83],v[64:67],v[96:99]\n"
EX"v41,v41\n"
MF"v[128:131],a[80:83],v[68:71],v[128:131]\n"
RD"a[96:99],v227 offset:24576\n"
MF"v[160:163],a[80:83],v[72:75],v[160:163]\n"
EX"v42,v42\n"
MF"v[192:195],a[80:83],v[76:79],v[192:195]\n"
MF"v[100:103],a[84:87],v[64:67],v[100:103]\n"
EX"v43,v43\n"
MF"v[132:135],a[84:87],v[68:71],v[132:135]\n"
RD"a[100:103],v227 offset:25600\n"
MF"v[164:167],a[84:87],v[72:75],v[164:167]\n"
EX"v48,v48\n"
MF"v[196:199],a[84:87],v[76:79],v[196:199]\n"
WT"lgkmcnt(2)\n"
BL"v225,s[8:11],s43 offen offset:3072 sc1 lds\n"
MF"v[104:107],a[88:91],v[64:67],v[104:107]\n"
EX"v49,v49\n"
MF"v[136:139],a[88:91],v[68:71],v[136:139]\n"
RD"a[104:107],v227 offset:26624\n"
MF"v[168:171],a[88:91],v[72:75],v[168:171]\n"
EX"v50,v50\n"
MF"v[200:203],a[88:91],v[76:79],v[200:203]\n"
SA"m0,s40,16384\n"
MF"v[108:111],a[92:95],v[64:67],v[108:111]\n"
EX"v51,v51\n"
MF"v[140:143],a[92:95],v[68:71],v[140:143]\n"
RD"a[108:111],v227 offset:27648\n"
MF"v[172:175],a[92:95],v[72:75],v[172:175]\n"
EX"v56,v56\n"
MF"v[204:207],a[92:95],v[76:79],v[204:207]\n"
BL"v225,s[8:11],s44 offen offset:0 sc1 lds\n"
MF"a[132:135],a[128:131],v[64:67],a[132:135]\n"
SA"s40,s40,32768\n"
EX"v57,v57\n"
MF"a[136:139],a[128:131],v[68:71],a[136:139]\n"
"s_cmp_eq_u32 s40,s46\n"
RD"a[112:115],v227 offset:28672\n"
"s_cselect_b32 s40,s47,s40\n"
MF"a[140:143],a[128:131],v[72:75],a[140:143]\n"
EX"v58,v58\n"
MF"a[144:147],a[128:131],v[76:79],a[144:147]\n"
WT"lgkmcnt(2)\n"
BL"v[240:243],v238,s[8:11],0 offen sc1\n"
MF"v[80:83],a[96:99],v[0:3],v[80:83]\n"
EX"v59,v59\n"
MF"v[112:115],a[96:99],v[8:11],v[112:115]\n"
RD"a[116:119],v227 offset:29696\n"
MF"v[144:147],a[96:99],v[16:19],v[144:147]\n"
EX"v36,v36\n"
MF"v[176:179],a[96:99],v[24:27],v[176:179]\n"
BL"v225,s[8:11],s44 offen offset:1024 sc1 lds\n"
MF"v[84:87],a[100:103],v[0:3],v[84:87]\n"
EX"v37,v37\n"
MF"v[116:119],a[100:103],v[8:11],v[116:119]\n"
RD"a[120:123],v227 offset:30720\n"
MF"v[148:151],a[100:103],v[16:19],v[148:151]\n"
EX"v38,v38\n"
MF"v[180:183],a[100:103],v[24:27],v[180:183]\n"
MF"v[88:91],a[104:107],v[0:3],v[88:91]\n"
EX"v39,v39\n"
MF"v[120:123],a[104:107],v[8:11],v[120:123]\n"
RD"a[124:127],v227 offset:31744\n"
MF"v[152:155],a[104:107],v[16:19],v[152:155]\n"
EX"v44,v44\n"
MF"v[184:187],a[104:107],v[24:27],v[184:187]\n"
WT"lgkmcnt(2)\n"
BL"v225,s[8:11],s44 offen offset:2048 sc1 lds\n"
MF"v[92:95],a[108:111],v[0:3],v[92:95]\n"
EX"v45,v45\n"
MF"v[124:127],a[108:111],v[8:11],v[124:127]\n"
MF"v[156:159],a[108:111],v[16:19],v[156:159]\n"
EX"v46,v46\n"
MF"v[188:191],a[108:111],v[24:27],v[188:191]\n"
MF"v[96:99],a[112:115],v[0:3],v[96:99]\n"
WT"vmcnt(23) lgkmcnt(0)\n"
"s_barrier\n"
MF"v[128:131],a[112:115],v[8:11],v[128:131]\n"
RD"a[64:67],v226 offset:0\n"
MF"v[160:163],a[112:115],v[16:19],v[160:163]\n"
EX"v47,v47\n"
MF"v[192:195],a[112:115],v[24:27],v[192:195]\n"
BL"v225,s[8:11],s44 offen offset:3072 sc1 lds\n"
MF"v[100:103],a[116:119],v[0:3],v[100:103]\n"
EX"v52,v52\n"
MF"v[132:135],a[116:119],v[8:11],v[132:135]\n"
RD"a[68:71],v226 offset:1024\n"
MF"v[164:167],a[116:119],v[16:19],v[164:167]\n"
EX"v53,v53\n"
MF"v[196:199],a[116:119],v[24:27],v[196:199]\n"
RD"a[72:75],v226 offset:2048\n"
MF"v[104:107],a[120:123],v[0:3],v[104:107]\n"
EX"v54,v54\n"
MF"v[136:139],a[120:123],v[8:11],v[136:139]\n"
RD"a[76:79],v226 offset:3072\n"
MF"v[168:171],a[120:123],v[16:19],v[168:171]\n"
EX"v55,v55\n"
MF"v[200:203],a[120:123],v[24:27],v[200:203]\n"
MF"v[108:111],a[124:127],v[0:3],v[108:111]\n"
EX"v60,v60\n"
MF"v[140:143],a[124:127],v[8:11],v[140:143]\n"
RD"a[80:83],v226 offset:4096\n"
MF"v[172:175],a[124:127],v[16:19],v[172:175]\n"
EX"v61,v61\n"
MF"v[204:207],a[124:127],v[24:27],v[204:207]\n"
RD"a[84:87],v226 offset:5120\n"
MF"a[132:135],a[128:131],v[0:3],a[132:135]\n"
EX"v62,v62\n"
MF"a[136:139],a[128:131],v[8:11],a[136:139]\n"
RD"a[88:91],v226 offset:6144\n"
MF"a[140:143],a[128:131],v[16:19],a[140:143]\n"
EX"v63,v63\n"
MF"a[144:147],a[128:131],v[24:27],a[144:147]\n"
WT"lgkmcnt(6)\n"
MF"v[0:3],a[64:67],a[0:3],0\n"
WT"lgkmcnt(2)\n"
"v_lshrrev_b32 v237,1,v224\n"
PK"v64,v32,v33\n"
MF"v[0:3],a[68:71],a[4:7],v[0:3]\n"
SA"s95,s40,32768\n"
"v_and_b32 v237,0x78,v237\n"
PK"v65,v34,v35\n"
MF"v[8:11],a[68:71],a[20:23],0\n"
"s_cmp_eq_u32 s95,s46\n"
"v_and_b32 v239,0x300,v224\n"
PK"v66,v36,v37\n"
MF"v[8:11],a[64:67],a[16:19],v[8:11]\n"
"s_cselect_b32 s95,s47,s95\n"
"v_add3_u32 v237,v237,v239,s40\n"
PK"v67,v38,v39\n"
MF"v[16:19],a[64:67],a[32:35],0\n"
SA"m0,s95,0\n"
PK"v68,v40,v41\n"
PK"v69,v42,v43\n"
MF"v[16:19],a[68:71],a[36:39],v[16:19]\n"
RD"a[92:95],v226 offset:7168\n"
PK"v70,v44,v45\n"
MF"v[24:27],a[68:71],a[52:55],0\n"
PK"v71,v46,v47\n"
PK"v72,v48,v49\n"
MF"v[24:27],a[64:67],a[48:51],v[24:27]\n"
BL"v235,s[24:27],s52 offen lds\n"
MF"v[0:3],a[72:75],a[8:11],v[0:3]\n"
SA"s52,s52,s62\n"
PK"v73,v50,v51\n"
"v_mov_b32 v227,v226\n"
MF"v[0:3],a[76:79],a[12:15],v[0:3]\n"
PK"v74,v52,v53\n"
PK"v75,v54,v55\n"
MF"v[8:11],a[76:79],a[28:31],v[8:11]\n"
PK"v76,v56,v57\n"
PK"v77,v58,v59\n"
MF"v[8:11],a[72:75],a[24:27],v[8:11]\n"
WT"vmcnt(4)\n"
"v_cmp_eq_u64 vcc,s[28:29],v[240:241]\n"
"v_cmp_eq_u64_e64 s[50:51],s[28:29],v[242:243]\n"
"s_and_b64 vcc,vcc,s[50:51]\n"
"s_cmp_eq_u64 vcc,exec\n"
"s_cbranch_scc0 L1099_%=\n"
"L1100_%=:\n"
MF"v[16:19],a[72:75],a[40:43],v[16:19]\n"
SA"m0,s95,1024\n"
PK"v78,v60,v61\n"
PK"v79,v62,v63\n"
MF"v[16:19],a[76:79],a[44:47],v[16:19]\n"
RD"a[96:99],v226 offset:8192\n"
MF"v[24:27],a[76:79],a[60:63],v[24:27]\n"
EX"v0,v0\n"
MF"v[24:27],a[72:75],a[56:59],v[24:27]\n"
BL"v235,s[24:27],s52 offen lds\n"
MF"v[4:7],a[80:83],a[0:3],0\n"
WT"lgkmcnt(1)\n"
EX"v1,v1\n"
MF"v[4:7],a[84:87],a[4:7],v[4:7]\n"
SA"s52,s52,s62\n"
RD"a[100:103],v226 offset:9216\n"
PK"v0,v0,v1\n"
MF"v[12:15],a[84:87],a[20:23],0\n"
EX"v2,v2\n"
MF"v[12:15],a[80:83],a[16:19],v[12:15]\n"
EX"v3,v3\n"
MF"v[20:23],a[80:83],a[32:35],0\n"
SA"m0,s95,2048\n"
EX"v8,v8\n"
MF"v[20:23],a[84:87],a[36:39],v[20:23]\n"
RD"a[104:107],v226 offset:10240\n"
PK"v1,v2,v3\n"
MF"v[28:31],a[84:87],a[52:55],0\n"
EX"v9,v9\n"
MF"v[28:31],a[80:83],a[48:51],v[28:31]\n"
BL"v235,s[24:27],s52 offen lds\n"
MF"v[4:7],a[88:91],a[8:11],v[4:7]\n"
SA"s52,s52,s62\n"
EX"v10,v10\n"
MF"v[4:7],a[92:95],a[12:15],v[4:7]\n"
RD"a[108:111],v226 offset:11264\n"
PK"v8,v8,v9\n"
MF"v[12:15],a[92:95],a[28:31],v[12:15]\n"
EX"v11,v11\n"
MF"v[12:15],a[88:91],a[24:27],v[12:15]\n"
RT"a[148:149],v237 offset:0\n"
RT"a[150:151],v237 offset:128\n"
MF"v[20:23],a[88:91],a[40:43],v[20:23]\n"
SA"m0,s95,3072\n"
EX"v16,v16\n"
MF"v[20:23],a[92:95],a[44:47],v[20:23]\n"
EX"v17,v17\n"
MF"v[28:31],a[92:95],a[60:63],v[28:31]\n"
RD"a[112:115],v226 offset:12288\n"
PK"v9,v10,v11\n"
MF"v[28:31],a[88:91],a[56:59],v[28:31]\n"
WT"lgkmcnt(4)\n"
EX"v18,v18\n"
MF"v[32:35],a[96:99],a[0:3],0\n"
RT"a[152:153],v237 offset:1024\n"
RT"a[154:155],v237 offset:1152\n"
MF"v[32:35],a[100:103],a[4:7],v[32:35]\n"
BL"v235,s[24:27],s52 offen lds\n"
MF"v[40:43],a[100:103],a[20:23],0\n"
SA"s52,s52,s62\n"
EX"v19,v19\n"
MF"v[40:43],a[96:99],a[16:19],v[40:43]\n"
RD"a[116:119],v226 offset:13312\n"
RT"a[156:157],v237 offset:2048\n"
MF"v[48:51],a[96:99],a[32:35],0\n"
SA"m0,s95,16384\n"
EX"v24,v24\n"
MF"v[48:51],a[100:103],a[36:39],v[48:51]\n"
RT"a[158:159],v237 offset:2176\n"
RD"a[120:123],v226 offset:14336\n"
EX"v25,v25\n"
MF"v[56:59],a[100:103],a[52:55],0\n"
EX"v26,v26\n"
PK"v16,v16,v17\n"
MF"v[56:59],a[96:99],a[48:51],v[56:59]\n"
EX"v27,v27\n"
MF"v[32:35],a[104:107],a[8:11],v[32:35]\n"
WT"lgkmcnt(4)\n"
RT"a[160:161],v237 offset:3072\n"
RT"a[162:163],v237 offset:3200\n"
MF"v[32:35],a[108:111],a[12:15],v[32:35]\n"
BL"v235,s[24:27],s52 offen lds\n"
PK"v17,v18,v19\n"
MF"v[40:43],a[108:111],a[28:31],v[40:43]\n"
SA"s52,s52,s62\n"
EX"v4,v4\n"
MF"v[40:43],a[104:107],a[24:27],v[40:43]\n"
RD"a[124:127],v226 offset:15360\n"
RT"a[164:165],v237 offset:16384\n"
MF"v[48:51],a[104:107],a[40:43],v[48:51]\n"
SA"m0,s95,17408\n"
EX"v5,v5\n"
PK"v24,v24,v25\n"
MF"v[48:51],a[108:111],a[44:47],v[48:51]\n"
RT"a[166:167],v237 offset:16512\n"
PK"v25,v26,v27\n"
EX"v6,v6\n"
MF"v[56:59],a[108:111],a[60:63],v[56:59]\n"
EX"v7,v7\n"
MF"v[56:59],a[104:107],a[56:59],v[56:59]\n"
EX"v12,v12\n"
MF"v[36:39],a[112:115],a[0:3],0\n"
WT"lgkmcnt(3)\n"
RT"a[168:169],v237 offset:17408\n"
RT"a[170:171],v237 offset:17536\n"
MF"v[36:39],a[116:119],a[4:7],v[36:39]\n"
BL"v235,s[24:27],s52 offen lds\n"
MF"v[44:47],a[116:119],a[20:23],0\n"
SA"s52,s52,s62\n"
EX"v13,v13\n"
MF"v[44:47],a[112:115],a[16:19],v[44:47]\n"
RD"a[64:67],v227 offset:16384\n"
RT"a[172:173],v237 offset:18432\n"
PK"v2,v4,v5\n"
MF"v[52:55],a[112:115],a[32:35],0\n"
SA"m0,s95,18432\n"
EX"v14,v14\n"
MF"v[52:55],a[116:119],a[36:39],v[52:55]\n"
RT"a[174:175],v237 offset:18560\n"
RD"a[68:71],v227 offset:17408\n"
PK"v3,v6,v7\n"
MF"v[60:63],a[116:119],a[52:55],0\n"
EX"v15,v15\n"
MF"v[60:63],a[112:115],a[48:51],v[60:63]\n"
EX"v20,v20\n"
MF"v[36:39],a[120:123],a[8:11],v[36:39]\n"
WT"lgkmcnt(4)\n"
BL"v235,s[24:27],s52 offen lds\n"
MF"v[36:39],a[124:127],a[12:15],v[36:39]\n"
SA"s52,s52,s62\n"
EX"v21,v21\n"
PK"v10,v12,v13\n"
MF"v[44:47],a[124:127],a[28:31],v[44:47]\n"
"s_cmp_gt_u32 s92,0\n"
RD"a[72:75],v227 offset:18432\n"
"v_readlane_b32 s43,v228,s42\n"
MF"v[44:47],a[120:123],a[24:27],v[44:47]\n"
"s_cselect_b32 s63,s63,1065353216\n"
EX"v22,v22\n"
MF"v[52:55],a[120:123],a[40:43],v[52:55]\n"
"s_mov_b32 m0,s40\n"
BL"a[204:207],v236,s[8:11],s63 offen offset:0 sc1\n"
MF"v[52:55],a[124:127],a[44:47],v[52:55]\n"
EX"v23,v23\n"
PK"v11,v14,v15\n"
MF"v[60:63],a[124:127],a[60:63],v[60:63]\n"
RD"a[76:79],v227 offset:19456\n"
"v_readlane_b32 s44,v229,s42\n"
MF"v[60:63],a[120:123],a[56:59],v[60:63]\n"
WT"lgkmcnt(2)\n"
EX"v28,v28\n"
MF"v[80:83],a[64:67],v[64:67],v[80:83]\n"
SA"s42,s42,1\n"
BL"v225,s[8:11],s43 offen offset:0 sc1 lds\n"
MF"v[112:115],a[64:67],v[68:71],v[112:115]\n"
SA"s41,s41,32768\n"
EX"v29,v29\n"
PK"v18,v20,v21\n"
MF"v[144:147],a[64:67],v[72:75],v[144:147]\n"
"s_cmp_eq_u32 s41,163840\n"
RD"a[80:83],v227 offset:20480\n"
PK"v19,v22,v23\n"
MF"v[176:179],a[64:67],v[76:79],v[176:179]\n"
"s_cselect_b32 s41,0,s41\n"
EX"v30,v30\n"
MF"v[84:87],a[68:71],v[64:67],v[84:87]\n"
BL"a[208:211],v236,s[8:11],s63 offen offset:1024 sc1\n"
MF"v[116:119],a[68:71],v[68:71],v[116:119]\n"
EX"v31,v31\n"
MF"v[148:151],a[68:71],v[72:75],v[148:151]\n"
RD"a[84:87],v227 offset:21504\n"
"v_add_u32 v226,s41,v224\n"
MF"v[180:183],a[68:71],v[76:79],v[180:183]\n"
WT"lgkmcnt(2)\n"
EX"v32,v32\n"
MF"v[88:91],a[72:75],v[64:67],v[88:91]\n"
BL"v225,s[8:11],s43 offen offset:1024 sc1 lds\n"
MF"v[120:123],a[72:75],v[68:71],v[120:123]\n"
EX"v33,v33\n"
PK"v26,v28,v29\n"
MF"v[152:155],a[72:75],v[72:75],v[152:155]\n"
RD"a[88:91],v227 offset:22528\n"
PK"v27,v30,v31\n"
MF"v[184:187],a[72:75],v[76:79],v[184:187]\n"
EX"v34,v34\n"
MF"v[92:95],a[76:79],v[64:67],v[92:95]\n"
BL"a[212:215],v236,s[8:11],s63 offen offset:2048 sc1\n"
MF"v[124:127],a[76:79],v[68:71],v[124:127]\n"
EX"v35,v35\n"
MF"v[156:159],a[76:79],v[72:75],v[156:159]\n"
RD"a[92:95],v227 offset:23552\n"
MF"v[188:191],a[76:79],v[76:79],v[188:191]\n"
WT"lgkmcnt(2)\n"
EX"v40,v40\n"
MF"v[96:99],a[80:83],v[64:67],v[96:99]\n"
BL"v225,s[8:11],s43 offen offset:2048 sc1 lds\n"
MF"v[128:131],a[80:83],v[68:71],v[128:131]\n"
EX"v41,v41\n"
MF"v[160:163],a[80:83],v[72:75],v[160:163]\n"
RD"a[96:99],v227 offset:24576\n"
MF"v[192:195],a[80:83],v[76:79],v[192:195]\n"
EX"v42,v42\n"
MF"v[100:103],a[84:87],v[64:67],v[100:103]\n"
BL"a[216:219],v236,s[8:11],s63 offen offset:3072 sc1\n"
MF"v[132:135],a[84:87],v[68:71],v[132:135]\n"
EX"v43,v43\n"
MF"v[164:167],a[84:87],v[72:75],v[164:167]\n"
RD"a[100:103],v227 offset:25600\n"
MF"v[196:199],a[84:87],v[76:79],v[196:199]\n"
WT"lgkmcnt(2)\n"
EX"v48,v48\n"
MF"v[104:107],a[88:91],v[64:67],v[104:107]\n"
SA"s63,s63,4096\n"
BL"v225,s[8:11],s43 offen offset:3072 sc1 lds\n"
MF"v[136:139],a[88:91],v[68:71],v[136:139]\n"
EX"v49,v49\n"
MF"v[168:171],a[88:91],v[72:75],v[168:171]\n"
RD"a[104:107],v227 offset:26624\n"
MF"v[200:203],a[88:91],v[76:79],v[200:203]\n"
SA"m0,s40,16384\n"
EX"v50,v50\n"
MF"v[108:111],a[92:95],v[64:67],v[108:111]\n"
BL"a[220:223],v236,s[8:11],s63 offen offset:0 sc1\n"
MF"v[140:143],a[92:95],v[68:71],v[140:143]\n"
EX"v51,v51\n"
MF"v[172:175],a[92:95],v[72:75],v[172:175]\n"
RD"a[108:111],v227 offset:27648\n"
MF"v[204:207],a[92:95],v[76:79],v[204:207]\n"
EX"v56,v56\n"
MF"a[132:135],a[128:131],v[64:67],a[132:135]\n"
SA"s40,s40,32768\n"
BL"v225,s[8:11],s44 offen offset:0 sc1 lds\n"
MF"a[136:139],a[128:131],v[68:71],a[136:139]\n"
"s_cmp_eq_u32 s40,s46\n"
EX"v57,v57\n"
MF"a[140:143],a[128:131],v[72:75],a[140:143]\n"
"s_cselect_b32 s40,s47,s40\n"
RD"a[112:115],v227 offset:28672\n"
MF"a[144:147],a[128:131],v[76:79],a[144:147]\n"
WT"lgkmcnt(2)\n"
EX"v58,v58\n"
MF"v[80:83],a[96:99],v[0:3],v[80:83]\n"
BL"a[224:227],v236,s[8:11],s63 offen offset:1024 sc1\n"
MF"v[112:115],a[96:99],v[8:11],v[112:115]\n"
EX"v59,v59\n"
MF"v[144:147],a[96:99],v[16:19],v[144:147]\n"
RD"a[116:119],v227 offset:29696\n"
MF"v[176:179],a[96:99],v[24:27],v[176:179]\n"
EX"v36,v36\n"
MF"v[84:87],a[100:103],v[0:3],v[84:87]\n"
BL"v225,s[8:11],s44 offen offset:1024 sc1 lds\n"
MF"v[116:119],a[100:103],v[8:11],v[116:119]\n"
EX"v37,v37\n"
MF"v[148:151],a[100:103],v[16:19],v[148:151]\n"
RD"a[120:123],v227 offset:30720\n"
MF"v[180:183],a[100:103],v[24:27],v[180:183]\n"
EX"v38,v38\n"
MF"v[88:91],a[104:107],v[0:3],v[88:91]\n"
BL"a[228:231],v236,s[8:11],s63 offen offset:2048 sc1\n"
MF"v[120:123],a[104:107],v[8:11],v[120:123]\n"
EX"v39,v39\n"
MF"v[152:155],a[104:107],v[16:19],v[152:155]\n"
RD"a[124:127],v227 offset:31744\n"
MF"v[184:187],a[104:107],v[24:27],v[184:187]\n"
WT"lgkmcnt(2)\n"
EX"v44,v44\n"
MF"v[92:95],a[108:111],v[0:3],v[92:95]\n"
BL"v225,s[8:11],s44 offen offset:2048 sc1 lds\n"
MF"v[124:127],a[108:111],v[8:11],v[124:127]\n"
EX"v45,v45\n"
MF"v[156:159],a[108:111],v[16:19],v[156:159]\n"
EX"v46,v46\n"
MF"v[188:191],a[108:111],v[24:27],v[188:191]\n"
BL"a[232:235],v236,s[8:11],s63 offen offset:3072 sc1\n"
MF"v[96:99],a[112:115],v[0:3],v[96:99]\n"
WT"lgkmcnt(0)\n"
"s_barrier\n"
MF"v[128:131],a[112:115],v[8:11],v[128:131]\n"
RD"a[64:67],v226 offset:0\n"
MF"v[160:163],a[112:115],v[16:19],v[160:163]\n"
EX"v47,v47\n"
MF"v[192:195],a[112:115],v[24:27],v[192:195]\n"
SA"s63,s63,4096\n"
BL"v225,s[8:11],s44 offen offset:3072 sc1 lds\n"
MF"v[100:103],a[116:119],v[0:3],v[100:103]\n"
EX"v52,v52\n"
MF"v[132:135],a[116:119],v[8:11],v[132:135]\n"
RD"a[68:71],v226 offset:1024\n"
MF"v[164:167],a[116:119],v[16:19],v[164:167]\n"
EX"v53,v53\n"
MF"v[196:199],a[116:119],v[24:27],v[196:199]\n"
BL"a[236:239],v236,s[8:11],s63 offen offset:0 sc1\n"
MF"v[104:107],a[120:123],v[0:3],v[104:107]\n"
RD"a[72:75],v226 offset:2048\n"
MF"v[136:139],a[120:123],v[8:11],v[136:139]\n"
EX"v54,v54\n"
MF"v[168:171],a[120:123],v[16:19],v[168:171]\n"
RD"a[76:79],v226 offset:3072\n"
MF"v[200:203],a[120:123],v[24:27],v[200:203]\n"
EX"v55,v55\n"
MF"v[108:111],a[124:127],v[0:3],v[108:111]\n"
EX"v60,v60\n"
MF"v[140:143],a[124:127],v[8:11],v[140:143]\n"
RD"a[80:83],v226 offset:4096\n"
MF"v[172:175],a[124:127],v[16:19],v[172:175]\n"
EX"v61,v61\n"
MF"v[204:207],a[124:127],v[24:27],v[204:207]\n"
RD"a[84:87],v226 offset:5120\n"
MF"a[132:135],a[128:131],v[0:3],a[132:135]\n"
EX"v62,v62\n"
MF"a[136:139],a[128:131],v[8:11],a[136:139]\n"
RD"a[88:91],v226 offset:6144\n"
MF"a[140:143],a[128:131],v[16:19],a[140:143]\n"
EX"v63,v63\n"
MF"a[144:147],a[128:131],v[24:27],a[144:147]\n"
WT"lgkmcnt(6)\n"
MF"v[0:3],a[64:67],a[0:3],0\n"
WT"lgkmcnt(2)\n"
"v_lshrrev_b32 v237,1,v224\n"
PK"v64,v32,v33\n"
MF"v[0:3],a[68:71],a[4:7],v[0:3]\n"
SA"s95,s40,32768\n"
"v_and_b32 v237,0x78,v237\n"
PK"v65,v34,v35\n"
MF"v[8:11],a[68:71],a[20:23],0\n"
"s_cmp_eq_u32 s95,s46\n"
"v_and_b32 v239,0x300,v224\n"
PK"v66,v36,v37\n"
MF"v[8:11],a[64:67],a[16:19],v[8:11]\n"
"s_cselect_b32 s95,s47,s95\n"
BL"a[240:243],v236,s[8:11],s63 offen offset:1024 sc1\n"
MF"v[16:19],a[64:67],a[32:35],0\n"
"s_sub_u32 s95,s95,s47\n"
"v_add3_u32 v237,v237,v239,s40\n"
PK"v67,v38,v39\n"
MF"v[16:19],a[68:71],a[36:39],v[16:19]\n"
"s_lshr_b32 s53,s47,2\n"
RD"a[92:95],v226 offset:7168\n"
PK"v68,v40,v41\n"
MF"v[24:27],a[68:71],a[52:55],0\n"
"s_cmp_gt_u32 s92,10\n"
PK"v69,v42,v43\n"
PK"v70,v44,v45\n"
MF"v[24:27],a[64:67],a[48:51],v[24:27]\n"
"s_cselect_b32 s63,s63,1065353216\n"
PK"v71,v46,v47\n"
"v_add_u32 v235,s95,v224\n"
MF"v[0:3],a[72:75],a[8:11],v[0:3]\n"
"s_lshl_b32 s50,s35,6\n"
BL"a[244:247],v236,s[8:11],s63 offen offset:2048 sc1\n"
MF"v[0:3],a[76:79],a[12:15],v[0:3]\n"
SA"s50,s50,4352\n"
PK"v72,v48,v49\n"
PK"v73,v50,v51\n"
MF"v[8:11],a[76:79],a[28:31],v[8:11]\n"
"v_mov_b32 v227,v226\n"
PK"v74,v52,v53\n"
MF"v[8:11],a[72:75],a[24:27],v[8:11]\n"
BL"a[248:251],v236,s[8:11],s63 offen offset:3072 sc1\n"
MF"v[16:19],a[72:75],a[40:43],v[16:19]\n"
SA"s63,s63,4096\n"
PK"v75,v54,v55\n"
PK"v76,v56,v57\n"
MF"v[16:19],a[76:79],a[44:47],v[16:19]\n"
RD"a[96:99],v226 offset:8192\n"
PK"v77,v58,v59\n"
MF"v[24:27],a[76:79],a[60:63],v[24:27]\n"
EX"v0,v0\n"
MF"v[24:27],a[72:75],a[56:59],v[24:27]\n"
BL"a[252:255],v236,s[8:11],s63 offen offset:0 sc1\n"
PK"v78,v60,v61\n"
MF"v[4:7],a[80:83],a[0:3],0\n"
WT"lgkmcnt(1)\n"
EX"v1,v1\n"
PK"v79,v62,v63\n"
MF"v[4:7],a[84:87],a[4:7],v[4:7]\n"
"s_cmp_gt_u32 s92,13\n"
RD"a[100:103],v226 offset:9216\n"
"v_lshrrev_b32 v238,1,v224\n"
"v_and_b32 v238,56,v238\n"
MF"v[12:15],a[84:87],a[20:23],0\n"
"s_cselect_b32 s63,s63,1065353216\n"
EX"v2,v2\n"
MF"v[12:15],a[80:83],a[16:19],v[12:15]\n"
BL"v[244:247],v236,s[8:11],s63 offen offset:1024 sc1\n"
"v_add_u32 v236,s53,v235\n"
MF"v[20:23],a[80:83],a[32:35],0\n"
EX"v3,v3\n"
MF"v[20:23],a[84:87],a[36:39],v[20:23]\n"
EX"v8,v8\n"
MF"v[28:31],a[84:87],a[52:55],0\n"
RD"a[104:107],v226 offset:10240\n"
PK"v0,v0,v1\n"
MF"v[28:31],a[80:83],a[48:51],v[28:31]\n"
EX"v9,v9\n"
MF"v[4:7],a[88:91],a[8:11],v[4:7]\n"
"buffer_load_dwordx2 v[240:241],v238,s[8:11],s50 offen sc1\n"
MF"v[4:7],a[92:95],a[12:15],v[4:7]\n"
EX"v10,v10\n"
MF"v[12:15],a[92:95],a[28:31],v[12:15]\n"
RD"a[108:111],v226 offset:11264\n"
PK"v1,v2,v3\n"
MF"v[12:15],a[88:91],a[24:27],v[12:15]\n"
EX"v11,v11\n"
MF"v[20:23],a[88:91],a[40:43],v[20:23]\n"
WT"vmcnt(23)\n"
RT"a[176:177],v237 offset:0\n"
RT"a[178:179],v237 offset:128\n"
MF"v[20:23],a[92:95],a[44:47],v[20:23]\n"
EX"v16,v16\n"
MF"v[28:31],a[92:95],a[60:63],v[28:31]\n"
EX"v17,v17\n"
MF"v[28:31],a[88:91],a[56:59],v[28:31]\n"
WT"lgkmcnt(3)\n"
RD"a[112:115],v226 offset:12288\n"
RT"a[180:181],v237 offset:1024\n"
PK"v8,v8,v9\n"
MF"v[32:35],a[96:99],a[0:3],0\n"
EX"v18,v18\n"
MF"v[32:35],a[100:103],a[4:7],v[32:35]\n"
RT"a[182:183],v237 offset:1152\n"
RD"a[116:119],v226 offset:13312\n"
MF"v[40:43],a[100:103],a[20:23],0\n"
EX"v19,v19\n"
MF"v[40:43],a[96:99],a[16:19],v[40:43]\n"
EX"v24,v24\n"
PK"v9,v10,v11\n"
MF"v[48:51],a[96:99],a[32:35],0\n"
RT"a[184:185],v237 offset:2048\n"
RT"a[186:187],v237 offset:2176\n"
MF"v[48:51],a[100:103],a[36:39],v[48:51]\n"
EX"v25,v25\n"
PK"v16,v16,v17\n"
MF"v[56:59],a[100:103],a[52:55],0\n"
EX"v26,v26\n"
MF"v[56:59],a[96:99],a[48:51],v[56:59]\n"
RD"a[120:123],v226 offset:14336\n"
RT"a[188:189],v237 offset:3072\n"
MF"v[32:35],a[104:107],a[8:11],v[32:35]\n"
WT"lgkmcnt(4)\n"
EX"v27,v27\n"
MF"v[32:35],a[108:111],a[12:15],v[32:35]\n"
RT"a[190:191],v237 offset:3200\n"
RD"a[124:127],v226 offset:15360\n"
PK"v17,v18,v19\n"
MF"v[40:43],a[108:111],a[28:31],v[40:43]\n"
EX"v4,v4\n"
MF"v[40:43],a[104:107],a[24:27],v[40:43]\n"
EX"v5,v5\n"
MF"v[48:51],a[104:107],a[40:43],v[48:51]\n"
RT"a[192:193],v237 offset:16384\n"
RT"a[194:195],v237 offset:16512\n"
PK"v24,v24,v25\n"
MF"v[48:51],a[108:111],a[44:47],v[48:51]\n"
EX"v6,v6\n"
MF"v[56:59],a[108:111],a[60:63],v[56:59]\n"
EX"v7,v7\n"
MF"v[56:59],a[104:107],a[56:59],v[56:59]\n"
EX"v12,v12\n"
PK"v25,v26,v27\n"
MF"v[36:39],a[112:115],a[0:3],0\n"
RT"a[196:197],v237 offset:17408\n"
RT"a[198:199],v237 offset:17536\n"
MF"v[36:39],a[116:119],a[4:7],v[36:39]\n"
EX"v13,v13\n"
MF"v[44:47],a[116:119],a[20:23],0\n"
RD"a[64:67],v227 offset:16384\n"
PK"v2,v4,v5\n"
MF"v[44:47],a[112:115],a[16:19],v[44:47]\n"
EX"v14,v14\n"
MF"v[52:55],a[112:115],a[32:35],0\n"
RT"a[200:201],v237 offset:18432\n"
RT"a[202:203],v237 offset:18560\n"
MF"v[52:55],a[116:119],a[36:39],v[52:55]\n"
EX"v15,v15\n"
PK"v3,v6,v7\n"
MF"v[60:63],a[116:119],a[52:55],0\n"
RD"a[68:71],v227 offset:17408\n"
PK"v10,v12,v13\n"
MF"v[60:63],a[112:115],a[48:51],v[60:63]\n"
WT"lgkmcnt(4)\n"
EX"v20,v20\n"
MF"v[36:39],a[120:123],a[8:11],v[36:39]\n"
EX"v21,v21\n"
MF"v[36:39],a[124:127],a[12:15],v[36:39]\n"
RD"a[72:75],v227 offset:18432\n"
"v_readlane_b32 s43,v228,s42\n"
MF"v[44:47],a[124:127],a[28:31],v[44:47]\n"
EX"v22,v22\n"
MF"v[44:47],a[120:123],a[24:27],v[44:47]\n"
"s_mov_b32 m0,s40\n"
PK"v11,v14,v15\n"
PK"v18,v20,v21\n"
MF"v[52:55],a[120:123],a[40:43],v[52:55]\n"
EX"v23,v23\n"
MF"v[52:55],a[124:127],a[44:47],v[52:55]\n"
RD"a[76:79],v227 offset:19456\n"
"v_readlane_b32 s44,v229,s42\n"
MF"v[60:63],a[124:127],a[60:63],v[60:63]\n"
EX"v28,v28\n"
MF"v[60:63],a[120:123],a[56:59],v[60:63]\n"
WT"lgkmcnt(2)\n"
BL"v225,s[8:11],s43 offen offset:0 sc1 lds\n"
MF"v[80:83],a[64:67],v[64:67],v[80:83]\n"
SA"s42,s42,1\n"
EX"v29,v29\n"
MF"v[112:115],a[64:67],v[68:71],v[112:115]\n"
SA"s41,s41,32768\n"
RD"a[80:83],v227 offset:20480\n"
PK"v19,v22,v23\n"
MF"v[144:147],a[64:67],v[72:75],v[144:147]\n"
"s_cmp_eq_u32 s41,163840\n"
EX"v30,v30\n"
MF"v[176:179],a[64:67],v[76:79],v[176:179]\n"
"s_cselect_b32 s41,0,s41\n"
PK"v26,v28,v29\n"
"v_add_u32 v226,s41,v224\n"
MF"v[84:87],a[68:71],v[64:67],v[84:87]\n"
EX"v31,v31\n"
MF"v[116:119],a[68:71],v[68:71],v[116:119]\n"
RD"a[84:87],v227 offset:21504\n"
PK"v27,v30,v31\n"
MF"v[148:151],a[68:71],v[72:75],v[148:151]\n"
WT"vmcnt(1)\n"
"v_cmp_eq_u64 vcc,s[28:29],v[240:241]\n"
"s_cmp_eq_u64 vcc,exec\n"
"s_cbranch_scc0 L1102_%=\n"
"L1103_%=:\n"
MF"v[180:183],a[68:71],v[76:79],v[180:183]\n"
WT"lgkmcnt(2)\n"
EX"v32,v32\n"
MF"v[88:91],a[72:75],v[64:67],v[88:91]\n"
BL"v225,s[8:11],s43 offen offset:1024 sc1 lds\n"
MF"v[120:123],a[72:75],v[68:71],v[120:123]\n"
EX"v33,v33\n"
MF"v[152:155],a[72:75],v[72:75],v[152:155]\n"
RD"a[88:91],v227 offset:22528\n"
MF"v[184:187],a[72:75],v[76:79],v[184:187]\n"
EX"v34,v34\n"
MF"v[92:95],a[76:79],v[64:67],v[92:95]\n"
EX"v35,v35\n"
MF"v[124:127],a[76:79],v[68:71],v[124:127]\n"
RD"a[92:95],v227 offset:23552\n"
MF"v[156:159],a[76:79],v[72:75],v[156:159]\n"
EX"v40,v40\n"
MF"v[188:191],a[76:79],v[76:79],v[188:191]\n"
WT"lgkmcnt(2)\n"
BL"v225,s[8:11],s43 offen offset:2048 sc1 lds\n"
MF"v[96:99],a[80:83],v[64:67],v[96:99]\n"
EX"v41,v41\n"
MF"v[128:131],a[80:83],v[68:71],v[128:131]\n"
RD"a[96:99],v227 offset:24576\n"
MF"v[160:163],a[80:83],v[72:75],v[160:163]\n"
EX"v42,v42\n"
MF"v[192:195],a[80:83],v[76:79],v[192:195]\n"
MF"v[100:103],a[84:87],v[64:67],v[100:103]\n"
EX"v43,v43\n"
MF"v[132:135],a[84:87],v[68:71],v[132:135]\n"
RD"a[100:103],v227 offset:25600\n"
MF"v[164:167],a[84:87],v[72:75],v[164:167]\n"
EX"v48,v48\n"
MF"v[196:199],a[84:87],v[76:79],v[196:199]\n"
WT"lgkmcnt(2)\n"
BL"v225,s[8:11],s43 offen offset:3072 sc1 lds\n"
MF"v[104:107],a[88:91],v[64:67],v[104:107]\n"
EX"v49,v49\n"
MF"v[136:139],a[88:91],v[68:71],v[136:139]\n"
RD"a[104:107],v227 offset:26624\n"
MF"v[168:171],a[88:91],v[72:75],v[168:171]\n"
EX"v50,v50\n"
MF"v[200:203],a[88:91],v[76:79],v[200:203]\n"
SA"m0,s40,16384\n"
MF"v[108:111],a[92:95],v[64:67],v[108:111]\n"
EX"v51,v51\n"
MF"v[140:143],a[92:95],v[68:71],v[140:143]\n"
RD"a[108:111],v227 offset:27648\n"
MF"v[172:175],a[92:95],v[72:75],v[172:175]\n"
EX"v56,v56\n"
MF"v[204:207],a[92:95],v[76:79],v[204:207]\n"
BL"v225,s[8:11],s44 offen offset:0 sc1 lds\n"
MF"a[132:135],a[128:131],v[64:67],a[132:135]\n"
SA"s40,s40,32768\n"
EX"v57,v57\n"
MF"a[136:139],a[128:131],v[68:71],a[136:139]\n"
"s_cmp_eq_u32 s40,s46\n"
RD"a[112:115],v227 offset:28672\n"
"s_cselect_b32 s40,s47,s40\n"
MF"a[140:143],a[128:131],v[72:75],a[140:143]\n"
EX"v58,v58\n"
MF"a[144:147],a[128:131],v[76:79],a[144:147]\n"
MF"v[240:243],a[148:151],a[204:207],0\n"
EX"v59,v59\n"
MF"v[240:243],a[152:155],a[208:211],v[240:243]\n"
RD"a[116:119],v227 offset:29696\n"
MF"v[240:243],a[156:159],a[212:215],v[240:243]\n"
EX"v36,v36\n"
MF"v[240:243],a[160:163],a[216:219],v[240:243]\n"
BL"v225,s[8:11],s44 offen offset:1024 sc1 lds\n"
MF"v[240:243],a[164:167],a[220:223],v[240:243]\n"
EX"v37,v37\n"
MF"v[240:243],a[168:171],a[224:227],v[240:243]\n"
RD"a[120:123],v227 offset:30720\n"
MF"v[240:243],a[172:175],a[228:231],v[240:243]\n"
EX"v38,v38\n"
MF"v[240:243],a[176:179],a[232:235],v[240:243]\n"
MF"v[240:243],a[180:183],a[236:239],v[240:243]\n"
EX"v39,v39\n"
MF"v[240:243],a[184:187],a[240:243],v[240:243]\n"
RD"a[124:127],v227 offset:31744\n"
MF"v[240:243],a[188:191],a[244:247],v[240:243]\n"
EX"v44,v44\n"
MF"v[240:243],a[192:195],a[248:251],v[240:243]\n"
BL"v225,s[8:11],s44 offen offset:2048 sc1 lds\n"
MF"v[240:243],a[196:199],a[252:255],v[240:243]\n"
EX"v45,v45\n"
MF"v[240:243],a[200:203],v[244:247],v[240:243]\n"
WT"lgkmcnt(1)\n"
MF"v[80:83],a[96:99],v[0:3],v[80:83]\n"
EX"v46,v46\n"
MF"v[112:115],a[96:99],v[8:11],v[112:115]\n"
MF"v[144:147],a[96:99],v[16:19],v[144:147]\n"
MF"v[176:179],a[96:99],v[24:27],v[176:179]\n"
MF"v[84:87],a[100:103],v[0:3],v[84:87]\n"
EX"v47,v47\n"
MF"v[116:119],a[100:103],v[8:11],v[116:119]\n"
"ds_write_b128 v236,v[240:243]\n"
MF"v[148:151],a[100:103],v[16:19],v[148:151]\n"
BL"v225,s[8:11],s44 offen offset:3072 sc1 lds\n"
MF"v[180:183],a[100:103],v[24:27],v[180:183]\n"
EX"v52,v52\n"
MF"v[88:91],a[104:107],v[0:3],v[88:91]\n"
EX"v53,v53\n"
MF"v[120:123],a[104:107],v[8:11],v[120:123]\n"
MF"v[152:155],a[104:107],v[16:19],v[152:155]\n"
EX"v54,v54\n"
MF"v[184:187],a[104:107],v[24:27],v[184:187]\n"
MF"v[92:95],a[108:111],v[0:3],v[92:95]\n"
EX"v55,v55\n"
MF"v[124:127],a[108:111],v[8:11],v[124:127]\n"
MF"v[156:159],a[108:111],v[16:19],v[156:159]\n"
EX"v60,v60\n"
MF"v[188:191],a[108:111],v[24:27],v[188:191]\n"
MF"v[96:99],a[112:115],v[0:3],v[96:99]\n"
EX"v61,v61\n"
MF"v[128:131],a[112:115],v[8:11],v[128:131]\n"
WT"lgkmcnt(0)\n"
"s_barrier\n"
MF"v[160:163],a[112:115],v[16:19],v[160:163]\n"
RD"a[64:67],v226 offset:0\n"
RD"v[240:243],v235 offset:0\n"
MF"v[192:195],a[112:115],v[24:27],v[192:195]\n"
EX"v62,v62\n"
MF"v[100:103],a[116:119],v[0:3],v[100:103]\n"
EX"v63,v63\n"
MF"v[132:135],a[116:119],v[8:11],v[132:135]\n"
RD"v[244:247],v235 offset:1024\n"
RD"a[68:71],v226 offset:1024\n"
MF"v[164:167],a[116:119],v[16:19],v[164:167]\n"
RD"v[248:251],v235 offset:2048\n"
MF"v[196:199],a[116:119],v[24:27],v[196:199]\n"
RD"a[72:75],v226 offset:2048\n"
MF"v[104:107],a[120:123],v[0:3],v[104:107]\n"
RD"v[252:255],v235 offset:3072\n"
MF"v[136:139],a[120:123],v[8:11],v[136:139]\n"
RD"a[76:79],v226 offset:3072\n"
MF"v[168:171],a[120:123],v[16:19],v[168:171]\n"
MF"v[200:203],a[120:123],v[24:27],v[200:203]\n"
MF"v[108:111],a[124:127],v[0:3],v[108:111]\n"
MF"v[140:143],a[124:127],v[8:11],v[140:143]\n"
RD"a[80:83],v226 offset:4096\n"
MF"v[172:175],a[124:127],v[16:19],v[172:175]\n"
MF"v[204:207],a[124:127],v[24:27],v[204:207]\n"
RD"a[84:87],v226 offset:5120\n"
MF"a[132:135],a[128:131],v[0:3],a[132:135]\n"
MF"a[136:139],a[128:131],v[8:11],a[136:139]\n"
RD"a[88:91],v226 offset:6144\n"
MF"a[140:143],a[128:131],v[16:19],a[140:143]\n"
MF"a[144:147],a[128:131],v[24:27],a[144:147]\n"
"s_cmp_eq_u32 s57,2\n"
"s_cbranch_scc1 LMD_%=\n"
"s_cmp_eq_u32 s34,2\n"
"s_cbranch_scc1 LMA_%=\n"
WT"lgkmcnt(10)\n"
MF"v[0:3],a[64:67],a[0:3],0\n"
WT"lgkmcnt(2)\n"
"v_add_f32 v240,v240,v244\n"
PK"v64,v32,v33\n"
MF"v[0:3],a[68:71],a[4:7],v[0:3]\n"
PK"v65,v34,v35\n"
"v_add_f32 v248,v248,v252\n"
MF"v[8:11],a[68:71],a[20:23],0\n"
PK"v66,v36,v37\n"
PK"v67,v38,v39\n"
MF"v[8:11],a[64:67],a[16:19],v[8:11]\n"
"v_add_f32 v241,v241,v245\n"
PK"v68,v40,v41\n"
MF"v[16:19],a[64:67],a[32:35],0\n"
PK"v69,v42,v43\n"
"v_add_f32 v249,v249,v253\n"
MF"v[16:19],a[68:71],a[36:39],v[16:19]\n"
RD"a[92:95],v226 offset:7168\n"
PK"v70,v44,v45\n"
MF"v[24:27],a[68:71],a[52:55],0\n"
PK"v71,v46,v47\n"
"v_add_f32 v242,v242,v246\n"
MF"v[24:27],a[64:67],a[48:51],v[24:27]\n"
PK"v72,v48,v49\n"
PK"v73,v50,v51\n"
MF"v[0:3],a[72:75],a[8:11],v[0:3]\n"
"v_mov_b32 v227,v226\n"
"v_add_f32 v250,v250,v254\n"
MF"v[0:3],a[76:79],a[12:15],v[0:3]\n"
PK"v74,v52,v53\n"
PK"v75,v54,v55\n"
MF"v[8:11],a[76:79],a[28:31],v[8:11]\n"
"v_add_f32 v243,v243,v247\n"
PK"v76,v56,v57\n"
MF"v[8:11],a[72:75],a[24:27],v[8:11]\n"
PK"v77,v58,v59\n"
"v_add_f32 v251,v251,v255\n"
MF"v[16:19],a[72:75],a[40:43],v[16:19]\n"
PK"v78,v60,v61\n"
PK"v79,v62,v63\n"
MF"v[16:19],a[76:79],a[44:47],v[16:19]\n"
RD"a[96:99],v226 offset:8192\n"
PK"v240,v240,v240\n"
MF"v[24:27],a[76:79],a[60:63],v[24:27]\n"
EX"v0,v0\n"
MF"v[24:27],a[72:75],a[56:59],v[24:27]\n"
"v_lshlrev_b32 v240,16,v240\n"
PK"v241,v241,v241\n"
MF"v[4:7],a[80:83],a[0:3],0\n"
WT"lgkmcnt(1)\n"
EX"v1,v1\n"
MF"v[4:7],a[84:87],a[4:7],v[4:7]\n"
RD"a[100:103],v226 offset:9216\n"
"v_lshlrev_b32 v241,16,v241\n"
MF"v[12:15],a[84:87],a[20:23],0\n"
EX"v2,v2\n"
MF"v[12:15],a[80:83],a[16:19],v[12:15]\n"
EX"v3,v3\n"
MF"v[20:23],a[80:83],a[32:35],0\n"
EX"v8,v8\n"
PK"v242,v242,v242\n"
MF"v[20:23],a[84:87],a[36:39],v[20:23]\n"
RD"a[104:107],v226 offset:10240\n"
"v_lshlrev_b32 v242,16,v242\n"
MF"v[28:31],a[84:87],a[52:55],0\n"
EX"v9,v9\n"
PK"v243,v243,v243\n"
MF"v[28:31],a[80:83],a[48:51],v[28:31]\n"
"v_lshlrev_b32 v243,16,v243\n"
MU"v244,0xbfb8aa3b,v240\n"
EX"v244,v244\n"
MF"v[4:7],a[88:91],a[8:11],v[4:7]\n"
EX"v10,v10\n"
MF"v[4:7],a[92:95],a[12:15],v[4:7]\n"
RD"a[108:111],v226 offset:11264\n"
MU"v245,0xbfb8aa3b,v241\n"
EX"v245,v245\n"
MF"v[12:15],a[92:95],a[28:31],v[12:15]\n"
EX"v11,v11\n"
PK"v0,v0,v1\n"
MF"v[12:15],a[88:91],a[24:27],v[12:15]\n"
EX"v16,v16\n"
MU"v246,0xbfb8aa3b,v242\n"
MF"v[20:23],a[88:91],a[40:43],v[20:23]\n"
EX"v246,v246\n"
MU"v247,0xbfb8aa3b,v243\n"
MF"v[20:23],a[92:95],a[44:47],v[20:23]\n"
EX"v17,v17\n"
PK"v1,v2,v3\n"
MF"v[28:31],a[92:95],a[60:63],v[28:31]\n"
EX"v247,v247\n"
PK"v248,v248,v248\n"
MF"v[28:31],a[88:91],a[56:59],v[28:31]\n"
WT"lgkmcnt(1)\n"
RD"a[112:115],v226 offset:12288\n"
"v_lshlrev_b32 v248,16,v248\n"
PK"v8,v8,v9\n"
MF"v[32:35],a[96:99],a[0:3],0\n"
EX"v18,v18\n"
PK"v249,v249,v249\n"
MF"v[32:35],a[100:103],a[4:7],v[32:35]\n"
EX"v19,v19\n"
"v_lshlrev_b32 v249,16,v249\n"
MF"v[40:43],a[100:103],a[20:23],0\n"
RD"a[116:119],v226 offset:13312\n"
PK"v250,v250,v250\n"
MF"v[40:43],a[96:99],a[16:19],v[40:43]\n"
EX"v24,v24\n"
PK"v9,v10,v11\n"
MF"v[48:51],a[96:99],a[32:35],0\n"
EX"v25,v25\n"
"v_lshlrev_b32 v250,16,v250\n"
MF"v[48:51],a[100:103],a[36:39],v[48:51]\n"
EX"v26,v26\n"
PK"v16,v16,v17\n"
MF"v[56:59],a[100:103],a[52:55],0\n"
RD"a[120:123],v226 offset:14336\n"
PK"v251,v251,v251\n"
MF"v[56:59],a[96:99],a[48:51],v[56:59]\n"
EX"v27,v27\n"
"v_lshlrev_b32 v251,16,v251\n"
MF"v[32:35],a[104:107],a[8:11],v[32:35]\n"
WT"lgkmcnt(2)\n"
EX"v4,v4\n"
"v_add_f32 v244,1.0,v244\n"
MF"v[32:35],a[108:111],a[12:15],v[32:35]\n"
"v_rcp_f32 v244,v244\n"
"v_add_f32 v245,1.0,v245\n"
MF"v[40:43],a[108:111],a[28:31],v[40:43]\n"
"v_rcp_f32 v245,v245\n"
PK"v17,v18,v19\n"
MF"v[40:43],a[104:107],a[24:27],v[40:43]\n"
RD"a[124:127],v226 offset:15360\n"
"v_add_f32 v246,1.0,v246\n"
"v_rcp_f32 v246,v246\n"
MF"v[48:51],a[104:107],a[40:43],v[48:51]\n"
EX"v5,v5\n"
"v_add_f32 v247,1.0,v247\n"
MF"v[48:51],a[108:111],a[44:47],v[48:51]\n"
"v_rcp_f32 v247,v247\n"
EX"v6,v6\n"
MF"v[56:59],a[108:111],a[60:63],v[56:59]\n"
EX"v7,v7\n"
PK"v24,v24,v25\n"
MF"v[56:59],a[104:107],a[56:59],v[56:59]\n"
EX"v12,v12\n"
PK"v25,v26,v27\n"
MF"v[36:39],a[112:115],a[0:3],0\n"
WT"lgkmcnt(1)\n"
EX"v13,v13\n"
MU"v240,v240,v244\n"
MF"v[36:39],a[116:119],a[4:7],v[36:39]\n"
RD"a[64:67],v227 offset:16384\n"
MU"v241,v241,v245\n"
MF"v[44:47],a[116:119],a[20:23],0\n"
EX"v14,v14\n"
MU"v242,v242,v246\n"
MF"v[44:47],a[112:115],a[16:19],v[44:47]\n"
MU"v243,v243,v247\n"
PK"v2,v4,v5\n"
MF"v[52:55],a[112:115],a[32:35],0\n"
EX"v15,v15\n"
PK"v240,v240,v240\n"
MF"v[52:55],a[116:119],a[36:39],v[52:55]\n"
RD"a[68:71],v227 offset:17408\n"
"v_lshlrev_b32 v240,16,v240\n"
PK"v3,v6,v7\n"
MF"v[60:63],a[116:119],a[52:55],0\n"
EX"v20,v20\n"
PK"v241,v241,v241\n"
MF"v[60:63],a[112:115],a[48:51],v[60:63]\n"
"v_readlane_b32 s43,v228,s42\n"
"v_lshlrev_b32 v241,16,v241\n"
MF"v[36:39],a[120:123],a[8:11],v[36:39]\n"
WT"lgkmcnt(1)\n"
EX"v21,v21\n"
PK"v242,v242,v242\n"
MF"v[36:39],a[124:127],a[12:15],v[36:39]\n"
RD"a[72:75],v227 offset:18432\n"
"v_lshlrev_b32 v242,16,v242\n"
PK"v10,v12,v13\n"
MF"v[44:47],a[124:127],a[28:31],v[44:47]\n"
EX"v22,v22\n"
PK"v243,v243,v243\n"
MF"v[44:47],a[120:123],a[24:27],v[44:47]\n"
"s_mov_b32 m0,s40\n"
"v_lshlrev_b32 v243,16,v243\n"
MU"v240,v240,v248\n"
MF"v[52:55],a[120:123],a[40:43],v[52:55]\n"
EX"v23,v23\n"
MF"v[52:55],a[124:127],a[44:47],v[52:55]\n"
RD"a[76:79],v227 offset:19456\n"
"v_readlane_b32 s44,v229,s42\n"
MU"v241,v241,v249\n"
MF"v[60:63],a[124:127],a[60:63],v[60:63]\n"
EX"v28,v28\n"
PK"v11,v14,v15\n"
MF"v[60:63],a[120:123],a[56:59],v[60:63]\n"
SA"s42,s42,1\n"
BL"v225,s[8:11],s43 offen offset:0 sc1 lds\n"
MU"v242,v242,v250\n"
MF"v[80:83],a[64:67],v[64:67],v[80:83]\n"
SA"s41,s41,32768\n"
EX"v29,v29\n"
MU"v243,v243,v251\n"
MF"v[112:115],a[64:67],v[68:71],v[112:115]\n"
"s_cmp_eq_u32 s41,163840\n"
RD"a[80:83],v227 offset:20480\n"
PK"v240,v240,v241\n"
PK"v241,v242,v243\n"
MF"v[144:147],a[64:67],v[72:75],v[144:147]\n"
"s_cselect_b32 s41,0,s41\n"
EX"v30,v30\n"
PK"v18,v20,v21\n"
MF"v[176:179],a[64:67],v[76:79],v[176:179]\n"
WT"lgkmcnt(2)\n"
"v_add_u32 v226,s41,v224\n"
"v_lshrrev_b32 v239,9,v224\n"
"v_lshlrev_b32 v239,8,v239\n"
MF"v[84:87],a[68:71],v[64:67],v[84:87]\n"
EX"v31,v31\n"
"v_bfe_u32 v237,v224,8,1\n"
MF"v[116:119],a[68:71],v[68:71],v[116:119]\n"
RD"a[84:87],v227 offset:21504\n"
"v_lshl_add_u32 v239,v237,3,v239\n"
PK"v19,v22,v23\n"
MF"v[148:151],a[68:71],v[72:75],v[148:151]\n"
EX"v32,v32\n"
"v_and_b32 v237,0x80,v224\n"
MF"v[180:183],a[68:71],v[76:79],v[180:183]\n"
BL"v225,s[8:11],s43 offen offset:1024 sc1 lds\n"
"v_lshl_add_u32 v239,v237,23,v239\n"
MF"v[88:91],a[72:75],v[64:67],v[88:91]\n"
EX"v33,v33\n"
"v_and_b32 v237,0x70,v224\n"
MF"v[120:123],a[72:75],v[68:71],v[120:123]\n"
RD"a[88:91],v227 offset:22528\n"
"v_add_u32 v239,v239,v237\n"
PK"v26,v28,v29\n"
MF"v[152:155],a[72:75],v[72:75],v[152:155]\n"
"buffer_store_dwordx2 v[240:241],v239,s[8:11],s93 offen sc1\n"
MF"v[184:187],a[72:75],v[76:79],v[184:187]\n"
WT"lgkmcnt(2)\n"
EX"v34,v34\n"
MF"v[92:95],a[76:79],v[64:67],v[92:95]\n"
EX"v35,v35\n"
MF"v[124:127],a[76:79],v[68:71],v[124:127]\n"
RD"a[92:95],v227 offset:23552\n"
PK"v27,v30,v31\n"
MF"v[156:159],a[76:79],v[72:75],v[156:159]\n"
EX"v40,v40\n"
MF"v[188:191],a[76:79],v[76:79],v[188:191]\n"
BL"v225,s[8:11],s43 offen offset:2048 sc1 lds\n"
MF"v[96:99],a[80:83],v[64:67],v[96:99]\n"
EX"v41,v41\n"
MF"v[128:131],a[80:83],v[68:71],v[128:131]\n"
RD"a[96:99],v227 offset:24576\n"
MF"v[160:163],a[80:83],v[72:75],v[160:163]\n"
EX"v42,v42\n"
MF"v[192:195],a[80:83],v[76:79],v[192:195]\n"
WT"lgkmcnt(2)\n"
MF"v[100:103],a[84:87],v[64:67],v[100:103]\n"
EX"v43,v43\n"
MF"v[132:135],a[84:87],v[68:71],v[132:135]\n"
RD"a[100:103],v227 offset:25600\n"
MF"v[164:167],a[84:87],v[72:75],v[164:167]\n"
EX"v48,v48\n"
MF"v[196:199],a[84:87],v[76:79],v[196:199]\n"
BL"v225,s[8:11],s43 offen offset:3072 sc1 lds\n"
MF"v[104:107],a[88:91],v[64:67],v[104:107]\n"
EX"v49,v49\n"
MF"v[136:139],a[88:91],v[68:71],v[136:139]\n"
RD"a[104:107],v227 offset:26624\n"
MF"v[168:171],a[88:91],v[72:75],v[168:171]\n"
EX"v50,v50\n"
MF"v[200:203],a[88:91],v[76:79],v[200:203]\n"
WT"lgkmcnt(2)\n"
SA"m0,s40,16384\n"
MF"v[108:111],a[92:95],v[64:67],v[108:111]\n"
EX"v51,v51\n"
MF"v[140:143],a[92:95],v[68:71],v[140:143]\n"
RD"a[108:111],v227 offset:27648\n"
MF"v[172:175],a[92:95],v[72:75],v[172:175]\n"
EX"v56,v56\n"
MF"v[204:207],a[92:95],v[76:79],v[204:207]\n"
BL"v225,s[8:11],s44 offen offset:0 sc1 lds\n"
MF"a[132:135],a[128:131],v[64:67],a[132:135]\n"
SA"s40,s40,32768\n"
EX"v57,v57\n"
MF"a[136:139],a[128:131],v[68:71],a[136:139]\n"
"s_cmp_eq_u32 s40,s46\n"
RD"a[112:115],v227 offset:28672\n"
"s_cselect_b32 s40,s47,s40\n"
MF"a[140:143],a[128:131],v[72:75],a[140:143]\n"
EX"v58,v58\n"
MF"a[144:147],a[128:131],v[76:79],a[144:147]\n"
MF"v[80:83],a[96:99],v[0:3],v[80:83]\n"
EX"v59,v59\n"
MF"v[112:115],a[96:99],v[8:11],v[112:115]\n"
RD"a[116:119],v227 offset:29696\n"
MF"v[144:147],a[96:99],v[16:19],v[144:147]\n"
EX"v36,v36\n"
MF"v[176:179],a[96:99],v[24:27],v[176:179]\n"
WT"lgkmcnt(2)\n"
BL"v225,s[8:11],s44 offen offset:1024 sc1 lds\n"
MF"v[84:87],a[100:103],v[0:3],v[84:87]\n"
EX"v37,v37\n"
MF"v[116:119],a[100:103],v[8:11],v[116:119]\n"
RD"a[120:123],v227 offset:30720\n"
MF"v[148:151],a[100:103],v[16:19],v[148:151]\n"
EX"v38,v38\n"
MF"v[180:183],a[100:103],v[24:27],v[180:183]\n"
MF"v[88:91],a[104:107],v[0:3],v[88:91]\n"
EX"v39,v39\n"
MF"v[120:123],a[104:107],v[8:11],v[120:123]\n"
RD"a[124:127],v227 offset:31744\n"
MF"v[152:155],a[104:107],v[16:19],v[152:155]\n"
EX"v44,v44\n"
MF"v[184:187],a[104:107],v[24:27],v[184:187]\n"
BL"v225,s[8:11],s44 offen offset:2048 sc1 lds\n"
MF"v[92:95],a[108:111],v[0:3],v[92:95]\n"
EX"v45,v45\n"
MF"v[124:127],a[108:111],v[8:11],v[124:127]\n"
MF"v[156:159],a[108:111],v[16:19],v[156:159]\n"
EX"v46,v46\n"
MF"v[188:191],a[108:111],v[24:27],v[188:191]\n"
WT"lgkmcnt(1)\n"
MF"v[96:99],a[112:115],v[0:3],v[96:99]\n"
WT"lgkmcnt(0)\n"
"s_barrier\n"
MF"v[128:131],a[112:115],v[8:11],v[128:131]\n"
RD"a[64:67],v226 offset:0\n"
MF"v[160:163],a[112:115],v[16:19],v[160:163]\n"
EX"v47,v47\n"
MF"v[192:195],a[112:115],v[24:27],v[192:195]\n"
BL"v225,s[8:11],s44 offen offset:3072 sc1 lds\n"
MF"v[100:103],a[116:119],v[0:3],v[100:103]\n"
EX"v52,v52\n"
MF"v[132:135],a[116:119],v[8:11],v[132:135]\n"
RD"a[68:71],v226 offset:1024\n"
MF"v[164:167],a[116:119],v[16:19],v[164:167]\n"
EX"v53,v53\n"
MF"v[196:199],a[116:119],v[24:27],v[196:199]\n"
RD"a[72:75],v226 offset:2048\n"
MF"v[104:107],a[120:123],v[0:3],v[104:107]\n"
EX"v54,v54\n"
MF"v[136:139],a[120:123],v[8:11],v[136:139]\n"
RD"a[76:79],v226 offset:3072\n"
MF"v[168:171],a[120:123],v[16:19],v[168:171]\n"
EX"v55,v55\n"
MF"v[200:203],a[120:123],v[24:27],v[200:203]\n"
MF"v[108:111],a[124:127],v[0:3],v[108:111]\n"
WT"vmcnt(6)\n"
"buffer_store_dwordx2 v[232:233],v230,s[8:11],s94 offen sc1\n"
MF"v[140:143],a[124:127],v[8:11],v[140:143]\n"
EX"v60,v60\n"
MF"v[172:175],a[124:127],v[16:19],v[172:175]\n"
RD"a[80:83],v226 offset:4096\n"
MF"v[204:207],a[124:127],v[24:27],v[204:207]\n"
EX"v61,v61\n"
MF"a[132:135],a[128:131],v[0:3],a[132:135]\n"
RD"a[84:87],v226 offset:5120\n"
MF"a[136:139],a[128:131],v[8:11],a[136:139]\n"
EX"v62,v62\n"
MF"a[140:143],a[128:131],v[16:19],a[140:143]\n"
RD"a[88:91],v226 offset:6144\n"
MF"a[144:147],a[128:131],v[24:27],a[144:147]\n"
EX"v63,v63\n"
"s_mov_b32 s57,0\n"
"s_lshl_b32 s56,s34,1\n"
"s_sub_u32 s56,s56,6\n"
"s_branch LP1_%=\n"
"LMA_%=:\n"
WT"lgkmcnt(10)\n"
MF"v[0:3],a[64:67],a[0:3],v[208:211]\n"
WT"lgkmcnt(2)\n"
"v_add_f32 v240,v240,v244\n"
PK"v64,v32,v33\n"
MF"v[0:3],a[68:71],a[4:7],v[0:3]\n"
PK"v65,v34,v35\n"
"v_add_f32 v248,v248,v252\n"
MF"v[8:11],a[68:71],a[20:23],0\n"
PK"v66,v36,v37\n"
PK"v67,v38,v39\n"
MF"v[8:11],a[64:67],a[16:19],v[8:11]\n"
"v_add_f32 v241,v241,v245\n"
PK"v68,v40,v41\n"
MF"v[16:19],a[64:67],a[32:35],0\n"
PK"v69,v42,v43\n"
"v_add_f32 v249,v249,v253\n"
MF"v[16:19],a[68:71],a[36:39],v[16:19]\n"
RD"a[92:95],v226 offset:7168\n"
PK"v70,v44,v45\n"
MF"v[24:27],a[68:71],a[52:55],0\n"
PK"v71,v46,v47\n"
"v_add_f32 v242,v242,v246\n"
MF"v[24:27],a[64:67],a[48:51],v[24:27]\n"
PK"v72,v48,v49\n"
PK"v73,v50,v51\n"
MF"v[0:3],a[72:75],a[8:11],v[0:3]\n"
"v_mov_b32 v227,v226\n"
"v_add_f32 v250,v250,v254\n"
MF"v[0:3],a[76:79],a[12:15],v[0:3]\n"
PK"v74,v52,v53\n"
PK"v75,v54,v55\n"
MF"v[8:11],a[76:79],a[28:31],v[8:11]\n"
"v_add_f32 v243,v243,v247\n"
PK"v76,v56,v57\n"
MF"v[8:11],a[72:75],a[24:27],v[8:11]\n"
PK"v77,v58,v59\n"
"v_add_f32 v251,v251,v255\n"
MF"v[16:19],a[72:75],a[40:43],v[16:19]\n"
PK"v78,v60,v61\n"
PK"v79,v62,v63\n"
MF"v[16:19],a[76:79],a[44:47],v[16:19]\n"
RD"a[96:99],v226 offset:8192\n"
PK"v240,v240,v240\n"
MF"v[24:27],a[76:79],a[60:63],v[24:27]\n"
EX"v0,v0\n"
MF"v[24:27],a[72:75],a[56:59],v[24:27]\n"
"v_lshlrev_b32 v240,16,v240\n"
PK"v241,v241,v241\n"
MF"v[4:7],a[80:83],a[0:3],v[212:215]\n"
WT"lgkmcnt(1)\n"
EX"v1,v1\n"
MF"v[4:7],a[84:87],a[4:7],v[4:7]\n"
RD"a[100:103],v226 offset:9216\n"
"v_lshlrev_b32 v241,16,v241\n"
MF"v[12:15],a[84:87],a[20:23],0\n"
EX"v2,v2\n"
MF"v[12:15],a[80:83],a[16:19],v[12:15]\n"
EX"v3,v3\n"
MF"v[20:23],a[80:83],a[32:35],0\n"
EX"v8,v8\n"
PK"v242,v242,v242\n"
MF"v[20:23],a[84:87],a[36:39],v[20:23]\n"
RD"a[104:107],v226 offset:10240\n"
"v_lshlrev_b32 v242,16,v242\n"
MF"v[28:31],a[84:87],a[52:55],0\n"
EX"v9,v9\n"
PK"v243,v243,v243\n"
MF"v[28:31],a[80:83],a[48:51],v[28:31]\n"
"v_lshlrev_b32 v243,16,v243\n"
MU"v244,0xbfb8aa3b,v240\n"
EX"v244,v244\n"
MF"v[4:7],a[88:91],a[8:11],v[4:7]\n"
EX"v10,v10\n"
MF"v[4:7],a[92:95],a[12:15],v[4:7]\n"
RD"a[108:111],v226 offset:11264\n"
MU"v245,0xbfb8aa3b,v241\n"
EX"v245,v245\n"
MF"v[12:15],a[92:95],a[28:31],v[12:15]\n"
EX"v11,v11\n"
PK"v0,v0,v1\n"
MF"v[12:15],a[88:91],a[24:27],v[12:15]\n"
EX"v16,v16\n"
MU"v246,0xbfb8aa3b,v242\n"
MF"v[20:23],a[88:91],a[40:43],v[20:23]\n"
EX"v246,v246\n"
MU"v247,0xbfb8aa3b,v243\n"
MF"v[20:23],a[92:95],a[44:47],v[20:23]\n"
EX"v17,v17\n"
PK"v1,v2,v3\n"
MF"v[28:31],a[92:95],a[60:63],v[28:31]\n"
EX"v247,v247\n"
PK"v248,v248,v248\n"
MF"v[28:31],a[88:91],a[56:59],v[28:31]\n"
WT"lgkmcnt(1)\n"
RD"a[112:115],v226 offset:12288\n"
"v_lshlrev_b32 v248,16,v248\n"
PK"v8,v8,v9\n"
MF"v[32:35],a[96:99],a[0:3],v[216:219]\n"
EX"v18,v18\n"
PK"v249,v249,v249\n"
MF"v[32:35],a[100:103],a[4:7],v[32:35]\n"
EX"v19,v19\n"
"v_lshlrev_b32 v249,16,v249\n"
MF"v[40:43],a[100:103],a[20:23],0\n"
RD"a[116:119],v226 offset:13312\n"
PK"v250,v250,v250\n"
MF"v[40:43],a[96:99],a[16:19],v[40:43]\n"
EX"v24,v24\n"
PK"v9,v10,v11\n"
MF"v[48:51],a[96:99],a[32:35],0\n"
EX"v25,v25\n"
"v_lshlrev_b32 v250,16,v250\n"
MF"v[48:51],a[100:103],a[36:39],v[48:51]\n"
EX"v26,v26\n"
PK"v16,v16,v17\n"
MF"v[56:59],a[100:103],a[52:55],0\n"
RD"a[120:123],v226 offset:14336\n"
PK"v251,v251,v251\n"
MF"v[56:59],a[96:99],a[48:51],v[56:59]\n"
EX"v27,v27\n"
"v_lshlrev_b32 v251,16,v251\n"
MF"v[32:35],a[104:107],a[8:11],v[32:35]\n"
WT"lgkmcnt(2)\n"
EX"v4,v4\n"
"v_add_f32 v244,1.0,v244\n"
MF"v[32:35],a[108:111],a[12:15],v[32:35]\n"
"v_rcp_f32 v244,v244\n"
"v_add_f32 v245,1.0,v245\n"
MF"v[40:43],a[108:111],a[28:31],v[40:43]\n"
"v_rcp_f32 v245,v245\n"
PK"v17,v18,v19\n"
MF"v[40:43],a[104:107],a[24:27],v[40:43]\n"
RD"a[124:127],v226 offset:15360\n"
"v_add_f32 v246,1.0,v246\n"
"v_rcp_f32 v246,v246\n"
MF"v[48:51],a[104:107],a[40:43],v[48:51]\n"
EX"v5,v5\n"
"v_add_f32 v247,1.0,v247\n"
MF"v[48:51],a[108:111],a[44:47],v[48:51]\n"
"v_rcp_f32 v247,v247\n"
EX"v6,v6\n"
MF"v[56:59],a[108:111],a[60:63],v[56:59]\n"
EX"v7,v7\n"
PK"v24,v24,v25\n"
MF"v[56:59],a[104:107],a[56:59],v[56:59]\n"
EX"v12,v12\n"
PK"v25,v26,v27\n"
MF"v[36:39],a[112:115],a[0:3],v[220:223]\n"
WT"lgkmcnt(1)\n"
EX"v13,v13\n"
MU"v240,v240,v244\n"
MF"v[36:39],a[116:119],a[4:7],v[36:39]\n"
RD"a[64:67],v227 offset:16384\n"
MU"v241,v241,v245\n"
MF"v[44:47],a[116:119],a[20:23],0\n"
EX"v14,v14\n"
MU"v242,v242,v246\n"
MF"v[44:47],a[112:115],a[16:19],v[44:47]\n"
MU"v243,v243,v247\n"
PK"v2,v4,v5\n"
MF"v[52:55],a[112:115],a[32:35],0\n"
EX"v15,v15\n"
PK"v240,v240,v240\n"
MF"v[52:55],a[116:119],a[36:39],v[52:55]\n"
RD"a[68:71],v227 offset:17408\n"
"v_lshlrev_b32 v240,16,v240\n"
PK"v3,v6,v7\n"
MF"v[60:63],a[116:119],a[52:55],0\n"
EX"v20,v20\n"
PK"v241,v241,v241\n"
MF"v[60:63],a[112:115],a[48:51],v[60:63]\n"
"v_readlane_b32 s43,v228,s42\n"
"v_lshlrev_b32 v241,16,v241\n"
MF"v[36:39],a[120:123],a[8:11],v[36:39]\n"
WT"lgkmcnt(1)\n"
EX"v21,v21\n"
PK"v242,v242,v242\n"
MF"v[36:39],a[124:127],a[12:15],v[36:39]\n"
RD"a[72:75],v227 offset:18432\n"
"v_lshlrev_b32 v242,16,v242\n"
PK"v10,v12,v13\n"
MF"v[44:47],a[124:127],a[28:31],v[44:47]\n"
EX"v22,v22\n"
PK"v243,v243,v243\n"
MF"v[44:47],a[120:123],a[24:27],v[44:47]\n"
"s_mov_b32 m0,s40\n"
"v_lshlrev_b32 v243,16,v243\n"
MU"v240,v240,v248\n"
MF"v[52:55],a[120:123],a[40:43],v[52:55]\n"
EX"v23,v23\n"
MF"v[52:55],a[124:127],a[44:47],v[52:55]\n"
RD"a[76:79],v227 offset:19456\n"
"v_readlane_b32 s44,v229,s42\n"
MU"v241,v241,v249\n"
MF"v[60:63],a[124:127],a[60:63],v[60:63]\n"
EX"v28,v28\n"
PK"v11,v14,v15\n"
MF"v[60:63],a[120:123],a[56:59],v[60:63]\n"
SA"s42,s42,1\n"
BL"v225,s[8:11],s43 offen offset:0 sc1 lds\n"
MU"v242,v242,v250\n"
MF"v[80:83],a[64:67],v[64:67],v[80:83]\n"
SA"s41,s41,32768\n"
EX"v29,v29\n"
MU"v243,v243,v251\n"
MF"v[112:115],a[64:67],v[68:71],v[112:115]\n"
"s_cmp_eq_u32 s41,163840\n"
RD"a[80:83],v227 offset:20480\n"
PK"v240,v240,v241\n"
PK"v241,v242,v243\n"
MF"v[144:147],a[64:67],v[72:75],v[144:147]\n"
"s_cselect_b32 s41,0,s41\n"
EX"v30,v30\n"
PK"v18,v20,v21\n"
MF"v[176:179],a[64:67],v[76:79],v[176:179]\n"
WT"lgkmcnt(2)\n"
"v_add_u32 v226,s41,v224\n"
"v_lshrrev_b32 v239,9,v224\n"
"v_lshlrev_b32 v239,8,v239\n"
MF"v[84:87],a[68:71],v[64:67],v[84:87]\n"
EX"v31,v31\n"
"v_bfe_u32 v237,v224,8,1\n"
MF"v[116:119],a[68:71],v[68:71],v[116:119]\n"
RD"a[84:87],v227 offset:21504\n"
"v_lshl_add_u32 v239,v237,3,v239\n"
PK"v19,v22,v23\n"
MF"v[148:151],a[68:71],v[72:75],v[148:151]\n"
EX"v32,v32\n"
"v_and_b32 v237,0x80,v224\n"
MF"v[180:183],a[68:71],v[76:79],v[180:183]\n"
BL"v225,s[8:11],s43 offen offset:1024 sc1 lds\n"
"v_lshl_add_u32 v239,v237,23,v239\n"
MF"v[88:91],a[72:75],v[64:67],v[88:91]\n"
EX"v33,v33\n"
"v_and_b32 v237,0x70,v224\n"
MF"v[120:123],a[72:75],v[68:71],v[120:123]\n"
RD"a[88:91],v227 offset:22528\n"
"v_add_u32 v239,v239,v237\n"
PK"v26,v28,v29\n"
MF"v[152:155],a[72:75],v[72:75],v[152:155]\n"
"buffer_store_dwordx2 v[240:241],v239,s[8:11],s93 offen sc1\n"
MF"v[184:187],a[72:75],v[76:79],v[184:187]\n"
WT"lgkmcnt(2)\n"
EX"v34,v34\n"
MF"v[92:95],a[76:79],v[64:67],v[92:95]\n"
EX"v35,v35\n"
MF"v[124:127],a[76:79],v[68:71],v[124:127]\n"
RD"a[92:95],v227 offset:23552\n"
PK"v27,v30,v31\n"
MF"v[156:159],a[76:79],v[72:75],v[156:159]\n"
EX"v40,v40\n"
MF"v[188:191],a[76:79],v[76:79],v[188:191]\n"
BL"v225,s[8:11],s43 offen offset:2048 sc1 lds\n"
MF"v[96:99],a[80:83],v[64:67],v[96:99]\n"
EX"v41,v41\n"
MF"v[128:131],a[80:83],v[68:71],v[128:131]\n"
RD"a[96:99],v227 offset:24576\n"
MF"v[160:163],a[80:83],v[72:75],v[160:163]\n"
EX"v42,v42\n"
MF"v[192:195],a[80:83],v[76:79],v[192:195]\n"
WT"lgkmcnt(2)\n"
MF"v[100:103],a[84:87],v[64:67],v[100:103]\n"
EX"v43,v43\n"
MF"v[132:135],a[84:87],v[68:71],v[132:135]\n"
RD"a[100:103],v227 offset:25600\n"
MF"v[164:167],a[84:87],v[72:75],v[164:167]\n"
EX"v48,v48\n"
MF"v[196:199],a[84:87],v[76:79],v[196:199]\n"
BL"v225,s[8:11],s43 offen offset:3072 sc1 lds\n"
MF"v[104:107],a[88:91],v[64:67],v[104:107]\n"
EX"v49,v49\n"
MF"v[136:139],a[88:91],v[68:71],v[136:139]\n"
RD"a[104:107],v227 offset:26624\n"
MF"v[168:171],a[88:91],v[72:75],v[168:171]\n"
EX"v50,v50\n"
MF"v[200:203],a[88:91],v[76:79],v[200:203]\n"
WT"lgkmcnt(2)\n"
SA"m0,s40,16384\n"
MF"v[108:111],a[92:95],v[64:67],v[108:111]\n"
EX"v51,v51\n"
MF"v[140:143],a[92:95],v[68:71],v[140:143]\n"
RD"a[108:111],v227 offset:27648\n"
MF"v[172:175],a[92:95],v[72:75],v[172:175]\n"
EX"v56,v56\n"
MF"v[204:207],a[92:95],v[76:79],v[204:207]\n"
BL"v225,s[8:11],s44 offen offset:0 sc1 lds\n"
MF"a[132:135],a[128:131],v[64:67],a[132:135]\n"
SA"s40,s40,32768\n"
EX"v57,v57\n"
MF"a[136:139],a[128:131],v[68:71],a[136:139]\n"
"s_cmp_eq_u32 s40,s46\n"
RD"a[112:115],v227 offset:28672\n"
"s_cselect_b32 s40,s47,s40\n"
MF"a[140:143],a[128:131],v[72:75],a[140:143]\n"
EX"v58,v58\n"
MF"a[144:147],a[128:131],v[76:79],a[144:147]\n"
MF"v[80:83],a[96:99],v[0:3],v[80:83]\n"
EX"v59,v59\n"
MF"v[112:115],a[96:99],v[8:11],v[112:115]\n"
RD"a[116:119],v227 offset:29696\n"
MF"v[144:147],a[96:99],v[16:19],v[144:147]\n"
EX"v36,v36\n"
MF"v[176:179],a[96:99],v[24:27],v[176:179]\n"
WT"lgkmcnt(2)\n"
BL"v225,s[8:11],s44 offen offset:1024 sc1 lds\n"
MF"v[84:87],a[100:103],v[0:3],v[84:87]\n"
EX"v37,v37\n"
MF"v[116:119],a[100:103],v[8:11],v[116:119]\n"
RD"a[120:123],v227 offset:30720\n"
MF"v[148:151],a[100:103],v[16:19],v[148:151]\n"
EX"v38,v38\n"
MF"v[180:183],a[100:103],v[24:27],v[180:183]\n"
MF"v[88:91],a[104:107],v[0:3],v[88:91]\n"
EX"v39,v39\n"
MF"v[120:123],a[104:107],v[8:11],v[120:123]\n"
RD"a[124:127],v227 offset:31744\n"
MF"v[152:155],a[104:107],v[16:19],v[152:155]\n"
EX"v44,v44\n"
MF"v[184:187],a[104:107],v[24:27],v[184:187]\n"
BL"v225,s[8:11],s44 offen offset:2048 sc1 lds\n"
MF"v[92:95],a[108:111],v[0:3],v[92:95]\n"
EX"v45,v45\n"
MF"v[124:127],a[108:111],v[8:11],v[124:127]\n"
MF"v[156:159],a[108:111],v[16:19],v[156:159]\n"
EX"v46,v46\n"
MF"v[188:191],a[108:111],v[24:27],v[188:191]\n"
WT"lgkmcnt(1)\n"
MF"v[96:99],a[112:115],v[0:3],v[96:99]\n"
WT"lgkmcnt(0)\n"
"s_barrier\n"
MF"v[128:131],a[112:115],v[8:11],v[128:131]\n"
RD"a[64:67],v226 offset:0\n"
MF"v[160:163],a[112:115],v[16:19],v[160:163]\n"
EX"v47,v47\n"
MF"v[192:195],a[112:115],v[24:27],v[192:195]\n"
BL"v225,s[8:11],s44 offen offset:3072 sc1 lds\n"
MF"v[100:103],a[116:119],v[0:3],v[100:103]\n"
EX"v52,v52\n"
MF"v[132:135],a[116:119],v[8:11],v[132:135]\n"
RD"a[68:71],v226 offset:1024\n"
MF"v[164:167],a[116:119],v[16:19],v[164:167]\n"
EX"v53,v53\n"
MF"v[196:199],a[116:119],v[24:27],v[196:199]\n"
RD"a[72:75],v226 offset:2048\n"
MF"v[104:107],a[120:123],v[0:3],v[104:107]\n"
EX"v54,v54\n"
MF"v[136:139],a[120:123],v[8:11],v[136:139]\n"
RD"a[76:79],v226 offset:3072\n"
MF"v[168:171],a[120:123],v[16:19],v[168:171]\n"
EX"v55,v55\n"
MF"v[200:203],a[120:123],v[24:27],v[200:203]\n"
MF"v[108:111],a[124:127],v[0:3],v[108:111]\n"
WT"vmcnt(6)\n"
"buffer_store_dwordx2 v[232:233],v230,s[8:11],s94 offen sc1\n"
MF"v[140:143],a[124:127],v[8:11],v[140:143]\n"
EX"v60,v60\n"
MF"v[172:175],a[124:127],v[16:19],v[172:175]\n"
RD"a[80:83],v226 offset:4096\n"
MF"v[204:207],a[124:127],v[24:27],v[204:207]\n"
EX"v61,v61\n"
MF"a[132:135],a[128:131],v[0:3],a[132:135]\n"
RD"a[84:87],v226 offset:5120\n"
MF"a[136:139],a[128:131],v[8:11],a[136:139]\n"
EX"v62,v62\n"
MF"a[140:143],a[128:131],v[16:19],a[140:143]\n"
RD"a[88:91],v226 offset:6144\n"
MF"a[144:147],a[128:131],v[24:27],a[144:147]\n"
EX"v63,v63\n"
"s_branch LDB_%=\n"
"LMD_%=:\n"
"s_cmp_eq_u32 s34,6\n"
"s_cbranch_scc1 LME_%=\n"
WT"lgkmcnt(10)\n"
MF"v[0:3],a[64:67],a[0:3],v[208:211]\n"
WT"lgkmcnt(2)\n"
"v_lshrrev_b32 v239,9,v224\n"
"v_lshlrev_b32 v239,8,v239\n"
MF"v[0:3],a[68:71],a[4:7],v[0:3]\n"
PK"v64,v32,v33\n"
PK"v65,v34,v35\n"
MF"v[8:11],a[68:71],a[20:23],0\n"
"v_bfe_u32 v237,v224,8,1\n"
PK"v66,v36,v37\n"
MF"v[8:11],a[64:67],a[16:19],v[8:11]\n"
PK"v67,v38,v39\n"
"v_lshl_add_u32 v239,v237,3,v239\n"
MF"v[16:19],a[64:67],a[32:35],0\n"
PK"v68,v40,v41\n"
PK"v69,v42,v43\n"
MF"v[16:19],a[68:71],a[36:39],v[16:19]\n"
RD"a[92:95],v226 offset:7168\n"
"v_and_b32 v237,0x80,v224\n"
MF"v[24:27],a[68:71],a[52:55],0\n"
PK"v70,v44,v45\n"
PK"v71,v46,v47\n"
MF"v[24:27],a[64:67],a[48:51],v[24:27]\n"
"v_lshl_add_u32 v239,v237,23,v239\n"
"v_not_b32 v236,v234\n"
MF"v[0:3],a[72:75],a[8:11],v[0:3]\n"
PK"v72,v48,v49\n"
PK"v73,v50,v51\n"
MF"v[0:3],a[76:79],a[12:15],v[0:3]\n"
SA"s53,s93,452608\n"
"v_mov_b32 v227,v226\n"
"v_and_b32 v237,15,v236\n"
MF"v[8:11],a[76:79],a[28:31],v[8:11]\n"
PK"v74,v52,v53\n"
PK"v75,v54,v55\n"
MF"v[8:11],a[72:75],a[24:27],v[8:11]\n"
"v_bfe_u32 v236,v236,4,7\n"
PK"v76,v56,v57\n"
MF"v[16:19],a[72:75],a[40:43],v[16:19]\n"
PK"v77,v58,v59\n"
"v_mul_u32_u24 v236,20480,v236\n"
MF"v[16:19],a[76:79],a[44:47],v[16:19]\n"
RD"a[96:99],v226 offset:8192\n"
PK"v78,v60,v61\n"
MF"v[24:27],a[76:79],a[60:63],v[24:27]\n"
EX"v0,v0\n"
MF"v[24:27],a[72:75],a[56:59],v[24:27]\n"
PK"v79,v62,v63\n"
"v_lshl_add_u32 v236,v237,4,v236\n"
MF"v[4:7],a[80:83],a[0:3],v[212:215]\n"
WT"lgkmcnt(1)\n"
EX"v1,v1\n"
"v_add_u32 v236,v236,v239\n"
MF"v[4:7],a[84:87],a[4:7],v[4:7]\n"
RD"a[100:103],v226 offset:9216\n"
"v_and_b32 v237,0x70,v224\n"
MF"v[12:15],a[84:87],a[20:23],0\n"
EX"v2,v2\n"
MF"v[12:15],a[80:83],a[16:19],v[12:15]\n"
EX"v3,v3\n"
"v_add_u32 v239,v239,v237\n"
MF"v[20:23],a[80:83],a[32:35],0\n"
EX"v8,v8\n"
"v_add_f32 v240,v240,v244\n"
MF"v[20:23],a[84:87],a[36:39],v[20:23]\n"
RD"a[104:107],v226 offset:10240\n"
"v_add_f32 v248,v248,v252\n"
"v_add_f32 v241,v241,v245\n"
MF"v[28:31],a[84:87],a[52:55],0\n"
EX"v9,v9\n"
"v_add_f32 v249,v249,v253\n"
MF"v[28:31],a[80:83],a[48:51],v[28:31]\n"
"v_add_f32 v242,v242,v246\n"
"v_add_f32 v250,v250,v254\n"
MF"v[4:7],a[88:91],a[8:11],v[4:7]\n"
EX"v10,v10\n"
"v_add_f32 v243,v243,v247\n"
MF"v[4:7],a[92:95],a[12:15],v[4:7]\n"
RD"a[108:111],v226 offset:11264\n"
"v_add_f32 v251,v251,v255\n"
PK"v0,v0,v1\n"
MF"v[12:15],a[92:95],a[28:31],v[12:15]\n"
"buffer_load_dwordx2 v[254:255],v236,s[8:11],s53 offen sc1\n"
"v_ashrrev_i32 v235,31,v234\n"
MF"v[12:15],a[88:91],a[24:27],v[12:15]\n"
EX"v11,v11\n"
"v_not_b32 v235,v235\n"
MF"v[20:23],a[88:91],a[40:43],v[20:23]\n"
EX"v16,v16\n"
"v_or_b32 v235,0x80000000,v235\n"
MF"v[20:23],a[92:95],a[44:47],v[20:23]\n"
EX"v17,v17\n"
PK"v1,v2,v3\n"
MF"v[28:31],a[92:95],a[60:63],v[28:31]\n"
RD"a[112:115],v226 offset:12288\n"
"v_xor_b32 v235,v235,v234\n"
"v_and_b32 v235,0xffff0000,v235\n"
MF"v[28:31],a[88:91],a[56:59],v[28:31]\n"
WT"lgkmcnt(2)\n"
EX"v18,v18\n"
MU"v235,0xbfb8aa3b,v235\n"
MF"v[32:35],a[96:99],a[0:3],v[216:219]\n"
EX"v235,v235\n"
PK"v8,v8,v9\n"
MF"v[32:35],a[100:103],a[4:7],v[32:35]\n"
EX"v19,v19\n"
"v_add_f32 v235,1.0,v235\n"
MF"v[40:43],a[100:103],a[20:23],0\n"
"v_rcp_f32 v235,v235\n"
MF"v[40:43],a[96:99],a[16:19],v[40:43]\n"
RD"a[116:119],v226 offset:13312\n"
PK"v235,v235,v235\n"
PK"v9,v10,v11\n"
MF"v[48:51],a[96:99],a[32:35],0\n"
EX"v24,v24\n"
"v_lshlrev_b32 v235,16,v235\n"
MF"v[48:51],a[100:103],a[36:39],v[48:51]\n"
EX"v25,v25\n"
"v_add_f32 v240,v240,v248\n"
MF"v[56:59],a[100:103],a[52:55],0\n"
EX"v26,v26\n"
PK"v16,v16,v17\n"
MF"v[56:59],a[96:99],a[48:51],v[56:59]\n"
RD"a[120:123],v226 offset:14336\n"
"v_add_f32 v241,v241,v249\n"
"v_add_f32 v242,v242,v250\n"
MF"v[32:35],a[104:107],a[8:11],v[32:35]\n"
WT"lgkmcnt(2)\n"
EX"v27,v27\n"
"v_add_f32 v243,v243,v251\n"
MF"v[32:35],a[108:111],a[12:15],v[32:35]\n"
EX"v4,v4\n"
PK"v240,v240,v240\n"
MF"v[40:43],a[108:111],a[28:31],v[40:43]\n"
RD"a[124:127],v226 offset:15360\n"
PK"v17,v18,v19\n"
"v_lshlrev_b32 v240,16,v240\n"
MF"v[40:43],a[104:107],a[24:27],v[40:43]\n"
EX"v5,v5\n"
MU"v240,v240,v235\n"
MF"v[48:51],a[104:107],a[40:43],v[48:51]\n"
EX"v6,v6\n"
PK"v240,v240,v240\n"
MF"v[48:51],a[108:111],a[44:47],v[48:51]\n"
EX"v7,v7\n"
PK"v24,v24,v25\n"
MF"v[56:59],a[108:111],a[60:63],v[56:59]\n"
EX"v12,v12\n"
"v_lshlrev_b32 v240,16,v240\n"
MF"v[56:59],a[104:107],a[56:59],v[56:59]\n"
PK"v241,v241,v241\n"
"v_lshlrev_b32 v241,16,v241\n"
PK"v25,v26,v27\n"
MF"v[36:39],a[112:115],a[0:3],v[220:223]\n"
WT"lgkmcnt(1)\n"
EX"v13,v13\n"
MU"v241,v241,v235\n"
MF"v[36:39],a[116:119],a[4:7],v[36:39]\n"
RD"a[64:67],v227 offset:16384\n"
PK"v241,v241,v241\n"
MF"v[44:47],a[116:119],a[20:23],0\n"
EX"v14,v14\n"
"v_lshlrev_b32 v241,16,v241\n"
MF"v[44:47],a[112:115],a[16:19],v[44:47]\n"
PK"v242,v242,v242\n"
PK"v2,v4,v5\n"
MF"v[52:55],a[112:115],a[32:35],0\n"
EX"v15,v15\n"
"v_lshlrev_b32 v242,16,v242\n"
MF"v[52:55],a[116:119],a[36:39],v[52:55]\n"
RD"a[68:71],v227 offset:17408\n"
MU"v242,v242,v235\n"
PK"v3,v6,v7\n"
MF"v[60:63],a[116:119],a[52:55],0\n"
EX"v20,v20\n"
PK"v242,v242,v242\n"
MF"v[60:63],a[112:115],a[48:51],v[60:63]\n"
"v_readlane_b32 s43,v228,s42\n"
"v_lshlrev_b32 v242,16,v242\n"
MF"v[36:39],a[120:123],a[8:11],v[36:39]\n"
WT"lgkmcnt(1)\n"
EX"v21,v21\n"
PK"v243,v243,v243\n"
MF"v[36:39],a[124:127],a[12:15],v[36:39]\n"
RD"a[72:75],v227 offset:18432\n"
"v_lshlrev_b32 v243,16,v243\n"
PK"v10,v12,v13\n"
MF"v[44:47],a[124:127],a[28:31],v[44:47]\n"
EX"v22,v22\n"
MU"v243,v243,v235\n"
MF"v[44:47],a[120:123],a[24:27],v[44:47]\n"
"s_mov_b32 m0,s40\n"
PK"v243,v243,v243\n"
"v_lshlrev_b32 v243,16,v243\n"
MF"v[52:55],a[120:123],a[40:43],v[52:55]\n"
EX"v23,v23\n"
MF"v[52:55],a[124:127],a[44:47],v[52:55]\n"
RD"a[76:79],v227 offset:19456\n"
"v_readlane_b32 s44,v229,s42\n"
PK"v11,v14,v15\n"
MF"v[60:63],a[124:127],a[60:63],v[60:63]\n"
EX"v28,v28\n"
MF"v[60:63],a[120:123],a[56:59],v[60:63]\n"
SA"s42,s42,1\n"
BL"v225,s[8:11],s43 offen offset:0 sc1 lds\n"
MF"v[80:83],a[64:67],v[64:67],v[80:83]\n"
SA"s41,s41,32768\n"
EX"v29,v29\n"
MF"v[112:115],a[64:67],v[68:71],v[112:115]\n"
"s_cmp_eq_u32 s41,163840\n"
RD"a[80:83],v227 offset:20480\n"
PK"v18,v20,v21\n"
MF"v[144:147],a[64:67],v[72:75],v[144:147]\n"
"s_cselect_b32 s41,0,s41\n"
EX"v30,v30\n"
MF"v[176:179],a[64:67],v[76:79],v[176:179]\n"
WT"lgkmcnt(2)\n"
"v_add_u32 v226,s41,v224\n"
PK"v19,v22,v23\n"
MF"v[84:87],a[68:71],v[64:67],v[84:87]\n"
EX"v31,v31\n"
MF"v[116:119],a[68:71],v[68:71],v[116:119]\n"
RD"a[84:87],v227 offset:21504\n"
PK"v26,v28,v29\n"
MF"v[148:151],a[68:71],v[72:75],v[148:151]\n"
EX"v32,v32\n"
MF"v[180:183],a[68:71],v[76:79],v[180:183]\n"
BL"v225,s[8:11],s43 offen offset:1024 sc1 lds\n"
MF"v[88:91],a[72:75],v[64:67],v[88:91]\n"
EX"v33,v33\n"
MF"v[120:123],a[72:75],v[68:71],v[120:123]\n"
RD"a[88:91],v227 offset:22528\n"
PK"v27,v30,v31\n"
MF"v[152:155],a[72:75],v[72:75],v[152:155]\n"
EX"v34,v34\n"
MF"v[184:187],a[72:75],v[76:79],v[184:187]\n"
WT"lgkmcnt(2)\n"
WT"vmcnt(2)\n"
"v_lshlrev_b32 v244,16,v254\n"
"v_and_b32 v245,0xffff0000,v254\n"
MF"v[92:95],a[76:79],v[64:67],v[92:95]\n"
EX"v35,v35\n"
"v_lshlrev_b32 v246,16,v255\n"
MF"v[124:127],a[76:79],v[68:71],v[124:127]\n"
RD"a[92:95],v227 offset:23552\n"
"v_and_b32 v247,0xffff0000,v255\n"
"v_add_f32 v240,v240,v244\n"
MF"v[156:159],a[76:79],v[72:75],v[156:159]\n"
EX"v40,v40\n"
"v_add_f32 v241,v241,v245\n"
MF"v[188:191],a[76:79],v[76:79],v[188:191]\n"
BL"v225,s[8:11],s43 offen offset:2048 sc1 lds\n"
"v_add_f32 v242,v242,v246\n"
MF"v[96:99],a[80:83],v[64:67],v[96:99]\n"
EX"v41,v41\n"
"v_add_f32 v243,v243,v247\n"
MF"v[128:131],a[80:83],v[68:71],v[128:131]\n"
RD"a[96:99],v227 offset:24576\n"
PK"v240,v240,v241\n"
PK"v241,v242,v243\n"
MF"v[160:163],a[80:83],v[72:75],v[160:163]\n"
"buffer_store_dwordx2 v[240:241],v239,s[8:11],s93 offen sc1\n"
MF"v[192:195],a[80:83],v[76:79],v[192:195]\n"
WT"lgkmcnt(2)\n"
EX"v42,v42\n"
MF"v[100:103],a[84:87],v[64:67],v[100:103]\n"
EX"v43,v43\n"
MF"v[132:135],a[84:87],v[68:71],v[132:135]\n"
RD"a[100:103],v227 offset:25600\n"
MF"v[164:167],a[84:87],v[72:75],v[164:167]\n"
EX"v48,v48\n"
MF"v[196:199],a[84:87],v[76:79],v[196:199]\n"
BL"v225,s[8:11],s43 offen offset:3072 sc1 lds\n"
MF"v[104:107],a[88:91],v[64:67],v[104:107]\n"
EX"v49,v49\n"
MF"v[136:139],a[88:91],v[68:71],v[136:139]\n"
RD"a[104:107],v227 offset:26624\n"
MF"v[168:171],a[88:91],v[72:75],v[168:171]\n"
EX"v50,v50\n"
MF"v[200:203],a[88:91],v[76:79],v[200:203]\n"
WT"lgkmcnt(2)\n"
SA"m0,s40,16384\n"
MF"v[108:111],a[92:95],v[64:67],v[108:111]\n"
EX"v51,v51\n"
MF"v[140:143],a[92:95],v[68:71],v[140:143]\n"
RD"a[108:111],v227 offset:27648\n"
MF"v[172:175],a[92:95],v[72:75],v[172:175]\n"
EX"v56,v56\n"
MF"v[204:207],a[92:95],v[76:79],v[204:207]\n"
BL"v225,s[8:11],s44 offen offset:0 sc1 lds\n"
MF"a[132:135],a[128:131],v[64:67],a[132:135]\n"
SA"s40,s40,32768\n"
EX"v57,v57\n"
MF"a[136:139],a[128:131],v[68:71],a[136:139]\n"
"s_cmp_eq_u32 s40,s46\n"
RD"a[112:115],v227 offset:28672\n"
"s_cselect_b32 s40,s47,s40\n"
MF"a[140:143],a[128:131],v[72:75],a[140:143]\n"
EX"v58,v58\n"
MF"a[144:147],a[128:131],v[76:79],a[144:147]\n"
MF"v[80:83],a[96:99],v[0:3],v[80:83]\n"
EX"v59,v59\n"
MF"v[112:115],a[96:99],v[8:11],v[112:115]\n"
RD"a[116:119],v227 offset:29696\n"
MF"v[144:147],a[96:99],v[16:19],v[144:147]\n"
EX"v36,v36\n"
MF"v[176:179],a[96:99],v[24:27],v[176:179]\n"
WT"lgkmcnt(2)\n"
BL"v225,s[8:11],s44 offen offset:1024 sc1 lds\n"
MF"v[84:87],a[100:103],v[0:3],v[84:87]\n"
EX"v37,v37\n"
MF"v[116:119],a[100:103],v[8:11],v[116:119]\n"
RD"a[120:123],v227 offset:30720\n"
MF"v[148:151],a[100:103],v[16:19],v[148:151]\n"
EX"v38,v38\n"
MF"v[180:183],a[100:103],v[24:27],v[180:183]\n"
MF"v[88:91],a[104:107],v[0:3],v[88:91]\n"
EX"v39,v39\n"
MF"v[120:123],a[104:107],v[8:11],v[120:123]\n"
RD"a[124:127],v227 offset:31744\n"
MF"v[152:155],a[104:107],v[16:19],v[152:155]\n"
EX"v44,v44\n"
MF"v[184:187],a[104:107],v[24:27],v[184:187]\n"
BL"v225,s[8:11],s44 offen offset:2048 sc1 lds\n"
MF"v[92:95],a[108:111],v[0:3],v[92:95]\n"
EX"v45,v45\n"
MF"v[124:127],a[108:111],v[8:11],v[124:127]\n"
MF"v[156:159],a[108:111],v[16:19],v[156:159]\n"
EX"v46,v46\n"
MF"v[188:191],a[108:111],v[24:27],v[188:191]\n"
WT"lgkmcnt(1)\n"
MF"v[96:99],a[112:115],v[0:3],v[96:99]\n"
WT"lgkmcnt(0)\n"
"s_barrier\n"
MF"v[128:131],a[112:115],v[8:11],v[128:131]\n"
RD"a[64:67],v226 offset:0\n"
MF"v[160:163],a[112:115],v[16:19],v[160:163]\n"
EX"v47,v47\n"
MF"v[192:195],a[112:115],v[24:27],v[192:195]\n"
BL"v225,s[8:11],s44 offen offset:3072 sc1 lds\n"
MF"v[100:103],a[116:119],v[0:3],v[100:103]\n"
EX"v52,v52\n"
MF"v[132:135],a[116:119],v[8:11],v[132:135]\n"
RD"a[68:71],v226 offset:1024\n"
MF"v[164:167],a[116:119],v[16:19],v[164:167]\n"
EX"v53,v53\n"
MF"v[196:199],a[116:119],v[24:27],v[196:199]\n"
RD"a[72:75],v226 offset:2048\n"
MF"v[104:107],a[120:123],v[0:3],v[104:107]\n"
EX"v54,v54\n"
MF"v[136:139],a[120:123],v[8:11],v[136:139]\n"
RD"a[76:79],v226 offset:3072\n"
MF"v[168:171],a[120:123],v[16:19],v[168:171]\n"
EX"v55,v55\n"
MF"v[200:203],a[120:123],v[24:27],v[200:203]\n"
MF"v[108:111],a[124:127],v[0:3],v[108:111]\n"
WT"vmcnt(5)\n"
"buffer_store_dwordx2 v[232:233],v230,s[8:11],s94 offen sc1\n"
MF"v[140:143],a[124:127],v[8:11],v[140:143]\n"
EX"v60,v60\n"
MF"v[172:175],a[124:127],v[16:19],v[172:175]\n"
RD"a[80:83],v226 offset:4096\n"
MF"v[204:207],a[124:127],v[24:27],v[204:207]\n"
EX"v61,v61\n"
MF"a[132:135],a[128:131],v[0:3],a[132:135]\n"
RD"a[84:87],v226 offset:5120\n"
MF"a[136:139],a[128:131],v[8:11],a[136:139]\n"
EX"v62,v62\n"
MF"a[140:143],a[128:131],v[16:19],a[140:143]\n"
RD"a[88:91],v226 offset:6144\n"
MF"a[144:147],a[128:131],v[24:27],a[144:147]\n"
EX"v63,v63\n"
"s_branch LDB_%=\n"
"LME_%=:\n"
WT"lgkmcnt(10)\n"
MF"v[0:3],a[64:67],a[0:3],0\n"
WT"lgkmcnt(2)\n"
"v_lshrrev_b32 v239,9,v224\n"
"v_lshlrev_b32 v239,8,v239\n"
MF"v[0:3],a[68:71],a[4:7],v[0:3]\n"
PK"v64,v32,v33\n"
PK"v65,v34,v35\n"
MF"v[8:11],a[68:71],a[20:23],0\n"
"v_bfe_u32 v237,v224,8,1\n"
PK"v66,v36,v37\n"
MF"v[8:11],a[64:67],a[16:19],v[8:11]\n"
PK"v67,v38,v39\n"
"v_lshl_add_u32 v239,v237,3,v239\n"
MF"v[16:19],a[64:67],a[32:35],0\n"
PK"v68,v40,v41\n"
PK"v69,v42,v43\n"
MF"v[16:19],a[68:71],a[36:39],v[16:19]\n"
RD"a[92:95],v226 offset:7168\n"
"v_and_b32 v237,0x80,v224\n"
MF"v[24:27],a[68:71],a[52:55],0\n"
PK"v70,v44,v45\n"
PK"v71,v46,v47\n"
MF"v[24:27],a[64:67],a[48:51],v[24:27]\n"
"v_lshl_add_u32 v239,v237,23,v239\n"
"v_not_b32 v236,v234\n"
MF"v[0:3],a[72:75],a[8:11],v[0:3]\n"
PK"v72,v48,v49\n"
PK"v73,v50,v51\n"
MF"v[0:3],a[76:79],a[12:15],v[0:3]\n"
SA"s53,s93,452608\n"
"v_mov_b32 v227,v226\n"
"v_and_b32 v237,15,v236\n"
MF"v[8:11],a[76:79],a[28:31],v[8:11]\n"
PK"v74,v52,v53\n"
PK"v75,v54,v55\n"
MF"v[8:11],a[72:75],a[24:27],v[8:11]\n"
"v_bfe_u32 v236,v236,4,7\n"
PK"v76,v56,v57\n"
MF"v[16:19],a[72:75],a[40:43],v[16:19]\n"
PK"v77,v58,v59\n"
"v_mul_u32_u24 v236,20480,v236\n"
MF"v[16:19],a[76:79],a[44:47],v[16:19]\n"
RD"a[96:99],v226 offset:8192\n"
PK"v78,v60,v61\n"
MF"v[24:27],a[76:79],a[60:63],v[24:27]\n"
EX"v0,v0\n"
MF"v[24:27],a[72:75],a[56:59],v[24:27]\n"
PK"v79,v62,v63\n"
"v_lshl_add_u32 v236,v237,4,v236\n"
MF"v[4:7],a[80:83],a[0:3],0\n"
WT"lgkmcnt(1)\n"
EX"v1,v1\n"
"v_add_u32 v236,v236,v239\n"
MF"v[4:7],a[84:87],a[4:7],v[4:7]\n"
RD"a[100:103],v226 offset:9216\n"
"v_and_b32 v237,0x70,v224\n"
MF"v[12:15],a[84:87],a[20:23],0\n"
EX"v2,v2\n"
MF"v[12:15],a[80:83],a[16:19],v[12:15]\n"
EX"v3,v3\n"
"v_add_u32 v239,v239,v237\n"
MF"v[20:23],a[80:83],a[32:35],0\n"
EX"v8,v8\n"
"v_add_f32 v240,v240,v244\n"
MF"v[20:23],a[84:87],a[36:39],v[20:23]\n"
RD"a[104:107],v226 offset:10240\n"
"v_add_f32 v248,v248,v252\n"
"v_add_f32 v241,v241,v245\n"
MF"v[28:31],a[84:87],a[52:55],0\n"
EX"v9,v9\n"
"v_add_f32 v249,v249,v253\n"
MF"v[28:31],a[80:83],a[48:51],v[28:31]\n"
"v_add_f32 v242,v242,v246\n"
"v_add_f32 v250,v250,v254\n"
MF"v[4:7],a[88:91],a[8:11],v[4:7]\n"
EX"v10,v10\n"
"v_add_f32 v243,v243,v247\n"
MF"v[4:7],a[92:95],a[12:15],v[4:7]\n"
RD"a[108:111],v226 offset:11264\n"
"v_add_f32 v251,v251,v255\n"
PK"v0,v0,v1\n"
MF"v[12:15],a[92:95],a[28:31],v[12:15]\n"
"buffer_load_dwordx2 v[254:255],v236,s[8:11],s53 offen sc1\n"
"v_ashrrev_i32 v235,31,v234\n"
MF"v[12:15],a[88:91],a[24:27],v[12:15]\n"
EX"v11,v11\n"
"v_not_b32 v235,v235\n"
MF"v[20:23],a[88:91],a[40:43],v[20:23]\n"
EX"v16,v16\n"
"v_or_b32 v235,0x80000000,v235\n"
MF"v[20:23],a[92:95],a[44:47],v[20:23]\n"
EX"v17,v17\n"
PK"v1,v2,v3\n"
MF"v[28:31],a[92:95],a[60:63],v[28:31]\n"
RD"a[112:115],v226 offset:12288\n"
"v_xor_b32 v235,v235,v234\n"
"v_and_b32 v235,0xffff0000,v235\n"
MF"v[28:31],a[88:91],a[56:59],v[28:31]\n"
WT"lgkmcnt(2)\n"
EX"v18,v18\n"
MU"v235,0xbfb8aa3b,v235\n"
MF"v[32:35],a[96:99],a[0:3],0\n"
EX"v235,v235\n"
PK"v8,v8,v9\n"
MF"v[32:35],a[100:103],a[4:7],v[32:35]\n"
EX"v19,v19\n"
"v_add_f32 v235,1.0,v235\n"
MF"v[40:43],a[100:103],a[20:23],0\n"
"v_rcp_f32 v235,v235\n"
MF"v[40:43],a[96:99],a[16:19],v[40:43]\n"
RD"a[116:119],v226 offset:13312\n"
PK"v235,v235,v235\n"
PK"v9,v10,v11\n"
MF"v[48:51],a[96:99],a[32:35],0\n"
EX"v24,v24\n"
"v_lshlrev_b32 v235,16,v235\n"
MF"v[48:51],a[100:103],a[36:39],v[48:51]\n"
EX"v25,v25\n"
"v_add_f32 v240,v240,v248\n"
MF"v[56:59],a[100:103],a[52:55],0\n"
EX"v26,v26\n"
PK"v16,v16,v17\n"
MF"v[56:59],a[96:99],a[48:51],v[56:59]\n"
RD"a[120:123],v226 offset:14336\n"
"v_add_f32 v241,v241,v249\n"
"v_add_f32 v242,v242,v250\n"
MF"v[32:35],a[104:107],a[8:11],v[32:35]\n"
WT"lgkmcnt(2)\n"
EX"v27,v27\n"
"v_add_f32 v243,v243,v251\n"
MF"v[32:35],a[108:111],a[12:15],v[32:35]\n"
EX"v4,v4\n"
PK"v240,v240,v240\n"
MF"v[40:43],a[108:111],a[28:31],v[40:43]\n"
RD"a[124:127],v226 offset:15360\n"
PK"v17,v18,v19\n"
"v_lshlrev_b32 v240,16,v240\n"
MF"v[40:43],a[104:107],a[24:27],v[40:43]\n"
EX"v5,v5\n"
MU"v240,v240,v235\n"
MF"v[48:51],a[104:107],a[40:43],v[48:51]\n"
EX"v6,v6\n"
PK"v240,v240,v240\n"
MF"v[48:51],a[108:111],a[44:47],v[48:51]\n"
EX"v7,v7\n"
PK"v24,v24,v25\n"
MF"v[56:59],a[108:111],a[60:63],v[56:59]\n"
EX"v12,v12\n"
"v_lshlrev_b32 v240,16,v240\n"
MF"v[56:59],a[104:107],a[56:59],v[56:59]\n"
PK"v241,v241,v241\n"
"v_lshlrev_b32 v241,16,v241\n"
PK"v25,v26,v27\n"
MF"v[36:39],a[112:115],a[0:3],0\n"
WT"lgkmcnt(1)\n"
EX"v13,v13\n"
MU"v241,v241,v235\n"
MF"v[36:39],a[116:119],a[4:7],v[36:39]\n"
RD"a[64:67],v227 offset:16384\n"
PK"v241,v241,v241\n"
MF"v[44:47],a[116:119],a[20:23],0\n"
EX"v14,v14\n"
"v_lshlrev_b32 v241,16,v241\n"
MF"v[44:47],a[112:115],a[16:19],v[44:47]\n"
PK"v242,v242,v242\n"
PK"v2,v4,v5\n"
MF"v[52:55],a[112:115],a[32:35],0\n"
EX"v15,v15\n"
"v_lshlrev_b32 v242,16,v242\n"
MF"v[52:55],a[116:119],a[36:39],v[52:55]\n"
RD"a[68:71],v227 offset:17408\n"
MU"v242,v242,v235\n"
PK"v3,v6,v7\n"
MF"v[60:63],a[116:119],a[52:55],0\n"
EX"v20,v20\n"
PK"v242,v242,v242\n"
MF"v[60:63],a[112:115],a[48:51],v[60:63]\n"
"v_readlane_b32 s43,v228,s42\n"
"v_lshlrev_b32 v242,16,v242\n"
MF"v[36:39],a[120:123],a[8:11],v[36:39]\n"
WT"lgkmcnt(1)\n"
EX"v21,v21\n"
PK"v243,v243,v243\n"
MF"v[36:39],a[124:127],a[12:15],v[36:39]\n"
RD"a[72:75],v227 offset:18432\n"
"v_lshlrev_b32 v243,16,v243\n"
PK"v10,v12,v13\n"
MF"v[44:47],a[124:127],a[28:31],v[44:47]\n"
EX"v22,v22\n"
MU"v243,v243,v235\n"
MF"v[44:47],a[120:123],a[24:27],v[44:47]\n"
"s_mov_b32 m0,s40\n"
PK"v243,v243,v243\n"
"v_lshlrev_b32 v243,16,v243\n"
MF"v[52:55],a[120:123],a[40:43],v[52:55]\n"
EX"v23,v23\n"
MF"v[52:55],a[124:127],a[44:47],v[52:55]\n"
RD"a[76:79],v227 offset:19456\n"
"v_readlane_b32 s44,v229,s42\n"
PK"v11,v14,v15\n"
MF"v[60:63],a[124:127],a[60:63],v[60:63]\n"
EX"v28,v28\n"
MF"v[60:63],a[120:123],a[56:59],v[60:63]\n"
SA"s42,s42,1\n"
BL"v225,s[8:11],s43 offen offset:0 sc1 lds\n"
MF"v[80:83],a[64:67],v[64:67],v[80:83]\n"
SA"s41,s41,32768\n"
EX"v29,v29\n"
MF"v[112:115],a[64:67],v[68:71],v[112:115]\n"
"s_cmp_eq_u32 s41,163840\n"
RD"a[80:83],v227 offset:20480\n"
PK"v18,v20,v21\n"
MF"v[144:147],a[64:67],v[72:75],v[144:147]\n"
"s_cselect_b32 s41,0,s41\n"
EX"v30,v30\n"
MF"v[176:179],a[64:67],v[76:79],v[176:179]\n"
WT"lgkmcnt(2)\n"
"v_add_u32 v226,s41,v224\n"
PK"v19,v22,v23\n"
MF"v[84:87],a[68:71],v[64:67],v[84:87]\n"
EX"v31,v31\n"
MF"v[116:119],a[68:71],v[68:71],v[116:119]\n"
RD"a[84:87],v227 offset:21504\n"
PK"v26,v28,v29\n"
MF"v[148:151],a[68:71],v[72:75],v[148:151]\n"
EX"v32,v32\n"
MF"v[180:183],a[68:71],v[76:79],v[180:183]\n"
BL"v225,s[8:11],s43 offen offset:1024 sc1 lds\n"
MF"v[88:91],a[72:75],v[64:67],v[88:91]\n"
EX"v33,v33\n"
MF"v[120:123],a[72:75],v[68:71],v[120:123]\n"
RD"a[88:91],v227 offset:22528\n"
PK"v27,v30,v31\n"
MF"v[152:155],a[72:75],v[72:75],v[152:155]\n"
EX"v34,v34\n"
MF"v[184:187],a[72:75],v[76:79],v[184:187]\n"
WT"lgkmcnt(2)\n"
WT"vmcnt(2)\n"
"v_lshlrev_b32 v244,16,v254\n"
"v_and_b32 v245,0xffff0000,v254\n"
MF"v[92:95],a[76:79],v[64:67],v[92:95]\n"
EX"v35,v35\n"
"v_lshlrev_b32 v246,16,v255\n"
MF"v[124:127],a[76:79],v[68:71],v[124:127]\n"
RD"a[92:95],v227 offset:23552\n"
"v_and_b32 v247,0xffff0000,v255\n"
"v_add_f32 v240,v240,v244\n"
MF"v[156:159],a[76:79],v[72:75],v[156:159]\n"
EX"v40,v40\n"
"v_add_f32 v241,v241,v245\n"
MF"v[188:191],a[76:79],v[76:79],v[188:191]\n"
BL"v225,s[8:11],s43 offen offset:2048 sc1 lds\n"
"v_add_f32 v242,v242,v246\n"
MF"v[96:99],a[80:83],v[64:67],v[96:99]\n"
EX"v41,v41\n"
"v_add_f32 v243,v243,v247\n"
MF"v[128:131],a[80:83],v[68:71],v[128:131]\n"
RD"a[96:99],v227 offset:24576\n"
PK"v240,v240,v241\n"
PK"v241,v242,v243\n"
MF"v[160:163],a[80:83],v[72:75],v[160:163]\n"
"buffer_store_dwordx2 v[240:241],v239,s[8:11],s93 offen sc1\n"
MF"v[192:195],a[80:83],v[76:79],v[192:195]\n"
WT"lgkmcnt(2)\n"
EX"v42,v42\n"
MF"v[100:103],a[84:87],v[64:67],v[100:103]\n"
EX"v43,v43\n"
MF"v[132:135],a[84:87],v[68:71],v[132:135]\n"
RD"a[100:103],v227 offset:25600\n"
MF"v[164:167],a[84:87],v[72:75],v[164:167]\n"
EX"v48,v48\n"
MF"v[196:199],a[84:87],v[76:79],v[196:199]\n"
BL"v225,s[8:11],s43 offen offset:3072 sc1 lds\n"
MF"v[104:107],a[88:91],v[64:67],v[104:107]\n"
EX"v49,v49\n"
MF"v[136:139],a[88:91],v[68:71],v[136:139]\n"
RD"a[104:107],v227 offset:26624\n"
MF"v[168:171],a[88:91],v[72:75],v[168:171]\n"
EX"v50,v50\n"
MF"v[200:203],a[88:91],v[76:79],v[200:203]\n"
WT"lgkmcnt(2)\n"
SA"m0,s40,16384\n"
MF"v[108:111],a[92:95],v[64:67],v[108:111]\n"
EX"v51,v51\n"
MF"v[140:143],a[92:95],v[68:71],v[140:143]\n"
RD"a[108:111],v227 offset:27648\n"
MF"v[172:175],a[92:95],v[72:75],v[172:175]\n"
EX"v56,v56\n"
MF"v[204:207],a[92:95],v[76:79],v[204:207]\n"
BL"v225,s[8:11],s44 offen offset:0 sc1 lds\n"
MF"a[132:135],a[128:131],v[64:67],a[132:135]\n"
SA"s40,s40,32768\n"
EX"v57,v57\n"
MF"a[136:139],a[128:131],v[68:71],a[136:139]\n"
"s_cmp_eq_u32 s40,s46\n"
RD"a[112:115],v227 offset:28672\n"
"s_cselect_b32 s40,s47,s40\n"
MF"a[140:143],a[128:131],v[72:75],a[140:143]\n"
EX"v58,v58\n"
MF"a[144:147],a[128:131],v[76:79],a[144:147]\n"
MF"v[80:83],a[96:99],v[0:3],v[80:83]\n"
EX"v59,v59\n"
MF"v[112:115],a[96:99],v[8:11],v[112:115]\n"
RD"a[116:119],v227 offset:29696\n"
MF"v[144:147],a[96:99],v[16:19],v[144:147]\n"
EX"v36,v36\n"
MF"v[176:179],a[96:99],v[24:27],v[176:179]\n"
WT"lgkmcnt(2)\n"
BL"v225,s[8:11],s44 offen offset:1024 sc1 lds\n"
MF"v[84:87],a[100:103],v[0:3],v[84:87]\n"
EX"v37,v37\n"
MF"v[116:119],a[100:103],v[8:11],v[116:119]\n"
RD"a[120:123],v227 offset:30720\n"
MF"v[148:151],a[100:103],v[16:19],v[148:151]\n"
EX"v38,v38\n"
MF"v[180:183],a[100:103],v[24:27],v[180:183]\n"
MF"v[88:91],a[104:107],v[0:3],v[88:91]\n"
EX"v39,v39\n"
MF"v[120:123],a[104:107],v[8:11],v[120:123]\n"
RD"a[124:127],v227 offset:31744\n"
MF"v[152:155],a[104:107],v[16:19],v[152:155]\n"
EX"v44,v44\n"
MF"v[184:187],a[104:107],v[24:27],v[184:187]\n"
BL"v225,s[8:11],s44 offen offset:2048 sc1 lds\n"
MF"v[92:95],a[108:111],v[0:3],v[92:95]\n"
EX"v45,v45\n"
MF"v[124:127],a[108:111],v[8:11],v[124:127]\n"
MF"v[156:159],a[108:111],v[16:19],v[156:159]\n"
EX"v46,v46\n"
MF"v[188:191],a[108:111],v[24:27],v[188:191]\n"
WT"lgkmcnt(1)\n"
MF"v[96:99],a[112:115],v[0:3],v[96:99]\n"
WT"lgkmcnt(0)\n"
"s_barrier\n"
MF"v[128:131],a[112:115],v[8:11],v[128:131]\n"
RD"a[64:67],v226 offset:0\n"
MF"v[160:163],a[112:115],v[16:19],v[160:163]\n"
EX"v47,v47\n"
MF"v[192:195],a[112:115],v[24:27],v[192:195]\n"
BL"v225,s[8:11],s44 offen offset:3072 sc1 lds\n"
MF"v[100:103],a[116:119],v[0:3],v[100:103]\n"
EX"v52,v52\n"
MF"v[132:135],a[116:119],v[8:11],v[132:135]\n"
RD"a[68:71],v226 offset:1024\n"
MF"v[164:167],a[116:119],v[16:19],v[164:167]\n"
EX"v53,v53\n"
MF"v[196:199],a[116:119],v[24:27],v[196:199]\n"
RD"a[72:75],v226 offset:2048\n"
MF"v[104:107],a[120:123],v[0:3],v[104:107]\n"
EX"v54,v54\n"
MF"v[136:139],a[120:123],v[8:11],v[136:139]\n"
RD"a[76:79],v226 offset:3072\n"
MF"v[168:171],a[120:123],v[16:19],v[168:171]\n"
EX"v55,v55\n"
MF"v[200:203],a[120:123],v[24:27],v[200:203]\n"
MF"v[108:111],a[124:127],v[0:3],v[108:111]\n"
WT"vmcnt(5)\n"
"buffer_store_dwordx2 v[232:233],v230,s[8:11],s94 offen sc1\n"
MF"v[140:143],a[124:127],v[8:11],v[140:143]\n"
EX"v60,v60\n"
MF"v[172:175],a[124:127],v[16:19],v[172:175]\n"
RD"a[80:83],v226 offset:4096\n"
MF"v[204:207],a[124:127],v[24:27],v[204:207]\n"
EX"v61,v61\n"
MF"a[132:135],a[128:131],v[0:3],a[132:135]\n"
RD"a[84:87],v226 offset:5120\n"
MF"a[136:139],a[128:131],v[8:11],a[136:139]\n"
EX"v62,v62\n"
MF"a[140:143],a[128:131],v[16:19],a[140:143]\n"
RD"a[88:91],v226 offset:6144\n"
MF"a[144:147],a[128:131],v[24:27],a[144:147]\n"
EX"v63,v63\n"
"s_mov_b32 s57,0\n"
"s_lshl_b32 s56,s34,1\n"
"s_sub_u32 s56,s56,12\n"
"s_branch LP1_%=\n"
"LRT_%=:\n"
WT"lgkmcnt(6)\n"
MF"v[0:3],a[64:67],a[0:3],0\n"
WT"lgkmcnt(5)\n"
"v_lshrrev_b32 v242,1,v224\n"
PK"v64,v32,v33\n"
MF"v[0:3],a[68:71],a[4:7],v[0:3]\n"
"s_mov_b32 s12,s74\n"
PK"v65,v34,v35\n"
"v_and_b32 v242,56,v242\n"
MF"v[8:11],a[68:71],a[20:23],0\n"
"s_and_b32 s13,s75,0xffff\n"
PK"v66,v36,v37\n"
PK"v67,v38,v39\n"
MF"v[8:11],a[64:67],a[16:19],v[8:11]\n"
"s_mov_b32 s14,5242880\n"
PK"v68,v40,v41\n"
PK"v69,v42,v43\n"
MF"v[16:19],a[64:67],a[32:35],0\n"
"s_lshl_b32 s23,s35,6\n"
PK"v70,v44,v45\n"
PK"v71,v46,v47\n"
MF"v[16:19],a[68:71],a[36:39],v[16:19]\n"
"s_mov_b32 s15,0x20000\n"
RD"a[92:95],v226 offset:7168\n"
"v_add_u32 v243,s40,v252\n"
MF"v[24:27],a[68:71],a[52:55],0\n"
SA"s23,s23,4352\n"
PK"v72,v48,v49\n"
PK"v73,v50,v51\n"
MF"v[24:27],a[64:67],a[48:51],v[24:27]\n"
WT"lgkmcnt(1)\n"
"buffer_load_dwordx2 v[244:245],v242,s[8:11],s23 offen sc1\n"
MF"v[0:3],a[72:75],a[8:11],v[0:3]\n"
SA"s19,s40,32768\n"
"v_mov_b32 v227,v226\n"
PK"v74,v52,v53\n"
MF"v[0:3],a[76:79],a[12:15],v[0:3]\n"
"s_cmp_eq_u32 s19,s46\n"
PK"v75,v54,v55\n"
PK"v76,v56,v57\n"
MF"v[8:11],a[76:79],a[28:31],v[8:11]\n"
"s_cselect_b32 s19,s47,s19\n"
PK"v77,v58,v59\n"
PK"v78,v60,v61\n"
MF"v[8:11],a[72:75],a[24:27],v[8:11]\n"
"s_cmp_eq_u32 s20,0\n"
PK"v79,v62,v63\n"
"s_cselect_b32 s17,2130706432,s17\n"
MF"v[16:19],a[72:75],a[40:43],v[16:19]\n"
SA"m0,s19,0\n"
MF"v[16:19],a[76:79],a[44:47],v[16:19]\n"
BL"v240,s[12:15],s17 offen offset:0 lds\n"
MF"v[24:27],a[76:79],a[60:63],v[24:27]\n"
RD"a[96:99],v226 offset:8192\n"
MF"v[24:27],a[72:75],a[56:59],v[24:27]\n"
EX"v0,v0\n"
MF"v[4:7],a[80:83],a[0:3],0\n"
BL"v241,s[12:15],s17 offen offset:1024 lds\n"
MF"v[4:7],a[84:87],a[4:7],v[4:7]\n"
EX"v1,v1\n"
MF"v[12:15],a[84:87],a[20:23],0\n"
RD"a[100:103],v226 offset:9216\n"
PK"v0,v0,v1\n"
MF"v[12:15],a[80:83],a[16:19],v[12:15]\n"
EX"v2,v2\n"
MF"v[20:23],a[80:83],a[32:35],0\n"
BL"v251,s[12:15],s17 offen offset:2048 lds\n"
MF"v[20:23],a[84:87],a[36:39],v[20:23]\n"
SA"m0,s19,16384\n"
EX"v3,v3\n"
MF"v[28:31],a[84:87],a[52:55],0\n"
SA"s17,s17,20480\n"
EX"v8,v8\n"
MF"v[28:31],a[80:83],a[48:51],v[28:31]\n"
RD"a[104:107],v226 offset:10240\n"
PK"v1,v2,v3\n"
MF"v[4:7],a[88:91],a[8:11],v[4:7]\n"
WT"lgkmcnt(2)\n"
EX"v9,v9\n"
MF"v[4:7],a[92:95],a[12:15],v[4:7]\n"
BL"v240,s[12:15],s17 offen offset:0 lds\n"
MF"v[12:15],a[92:95],a[28:31],v[12:15]\n"
EX"v10,v10\n"
MF"v[12:15],a[88:91],a[24:27],v[12:15]\n"
RD"a[108:111],v226 offset:11264\n"
PK"v8,v8,v9\n"
MF"v[20:23],a[88:91],a[40:43],v[20:23]\n"
EX"v11,v11\n"
MF"v[20:23],a[92:95],a[44:47],v[20:23]\n"
WT"vmcnt(13)\n"
RT"a[148:149],v243 offset:0\n"
RT"a[150:151],v243 offset:16384\n"
EX"v16,v16\n"
MF"v[28:31],a[92:95],a[60:63],v[28:31]\n"
EX"v17,v17\n"
MF"v[28:31],a[88:91],a[56:59],v[28:31]\n"
RD"a[112:115],v226 offset:12288\n"
PK"v9,v10,v11\n"
MF"v[32:35],a[96:99],a[0:3],0\n"
WT"lgkmcnt(4)\n"
EX"v18,v18\n"
MF"v[32:35],a[100:103],a[4:7],v[32:35]\n"
BL"v241,s[12:15],s17 offen offset:1024 lds\n"
MF"v[40:43],a[100:103],a[20:23],0\n"
EX"v19,v19\n"
MF"v[40:43],a[96:99],a[16:19],v[40:43]\n"
RD"a[116:119],v226 offset:13312\n"
RT"a[152:153],v243 offset:32\n"
MF"v[48:51],a[96:99],a[32:35],0\n"
EX"v24,v24\n"
MF"v[48:51],a[100:103],a[36:39],v[48:51]\n"
RT"a[154:155],v243 offset:16416\n"
RD"a[120:123],v226 offset:14336\n"
EX"v25,v25\n"
MF"v[56:59],a[100:103],a[52:55],0\n"
EX"v26,v26\n"
PK"v16,v16,v17\n"
MF"v[56:59],a[96:99],a[48:51],v[56:59]\n"
EX"v27,v27\n"
MF"v[32:35],a[104:107],a[8:11],v[32:35]\n"
WT"lgkmcnt(4)\n"
BL"v251,s[12:15],s17 offen offset:2048 lds\n"
MF"v[32:35],a[108:111],a[12:15],v[32:35]\n"
EX"v4,v4\n"
PK"v17,v18,v19\n"
MF"v[40:43],a[108:111],a[28:31],v[40:43]\n"
RD"a[124:127],v226 offset:15360\n"
PK"v24,v24,v25\n"
MF"v[40:43],a[104:107],a[24:27],v[40:43]\n"
EX"v5,v5\n"
MF"v[48:51],a[104:107],a[40:43],v[48:51]\n"
RT"a[156:157],v243 offset:64\n"
RT"a[158:159],v243 offset:16448\n"
MF"v[48:51],a[108:111],a[44:47],v[48:51]\n"
EX"v6,v6\n"
MF"v[56:59],a[108:111],a[60:63],v[56:59]\n"
EX"v7,v7\n"
MF"v[56:59],a[104:107],a[56:59],v[56:59]\n"
EX"v12,v12\n"
PK"v25,v26,v27\n"
MF"v[36:39],a[112:115],a[0:3],0\n"
WT"lgkmcnt(3)\n"
EX"v13,v13\n"
MF"v[36:39],a[116:119],a[4:7],v[36:39]\n"
RD"a[64:67],v227 offset:16384\n"
PK"v2,v4,v5\n"
MF"v[44:47],a[116:119],a[20:23],0\n"
EX"v14,v14\n"
MF"v[44:47],a[112:115],a[16:19],v[44:47]\n"
RT"a[160:161],v243 offset:96\n"
RT"a[162:163],v243 offset:16480\n"
MF"v[52:55],a[112:115],a[32:35],0\n"
EX"v15,v15\n"
MF"v[52:55],a[116:119],a[36:39],v[52:55]\n"
RD"a[68:71],v227 offset:17408\n"
PK"v3,v6,v7\n"
MF"v[60:63],a[116:119],a[52:55],0\n"
EX"v20,v20\n"
MF"v[60:63],a[112:115],a[48:51],v[60:63]\n"
"v_readlane_b32 s43,v228,s42\n"
PK"v10,v12,v13\n"
MF"v[36:39],a[120:123],a[8:11],v[36:39]\n"
WT"lgkmcnt(3)\n"
EX"v21,v21\n"
MF"v[36:39],a[124:127],a[12:15],v[36:39]\n"
RD"a[72:75],v227 offset:18432\n"
PK"v11,v14,v15\n"
MF"v[44:47],a[124:127],a[28:31],v[44:47]\n"
EX"v22,v22\n"
MF"v[44:47],a[120:123],a[24:27],v[44:47]\n"
"s_mov_b32 m0,s40\n"
RT"a[164:165],v243 offset:128\n"
RT"a[166:167],v243 offset:16512\n"
MF"v[52:55],a[120:123],a[40:43],v[52:55]\n"
SA"s17,s17,61440\n"
EX"v23,v23\n"
MF"v[52:55],a[124:127],a[44:47],v[52:55]\n"
RD"a[76:79],v227 offset:19456\n"
"v_readlane_b32 s44,v229,s42\n"
MF"v[60:63],a[124:127],a[60:63],v[60:63]\n"
EX"v28,v28\n"
MF"v[60:63],a[120:123],a[56:59],v[60:63]\n"
WT"lgkmcnt(1)\n"
BL"v225,s[8:11],s43 offen offset:0 sc1 lds\n"
MF"v[80:83],a[64:67],v[64:67],v[80:83]\n"
SA"s42,s42,1\n"
EX"v29,v29\n"
MF"v[112:115],a[64:67],v[68:71],v[112:115]\n"
SA"s41,s41,32768\n"
RD"a[80:83],v227 offset:20480\n"
PK"v18,v20,v21\n"
MF"v[144:147],a[64:67],v[72:75],v[144:147]\n"
"s_cmp_eq_u32 s41,163840\n"
EX"v30,v30\n"
MF"v[176:179],a[64:67],v[76:79],v[176:179]\n"
"s_cselect_b32 s41,0,s41\n"
BS"a[148:151],v224,s[8:11],s18 offen offset:0 sc1\n"
MF"v[84:87],a[68:71],v[64:67],v[84:87]\n"
EX"v31,v31\n"
MF"v[116:119],a[68:71],v[68:71],v[116:119]\n"
RD"a[84:87],v227 offset:21504\n"
"v_add_u32 v226,s41,v224\n"
PK"v19,v22,v23\n"
MF"v[148:151],a[68:71],v[72:75],v[148:151]\n"
EX"v32,v32\n"
MF"v[180:183],a[68:71],v[76:79],v[180:183]\n"
BL"v225,s[8:11],s43 offen offset:1024 sc1 lds\n"
MF"v[88:91],a[72:75],v[64:67],v[88:91]\n"
EX"v33,v33\n"
MF"v[120:123],a[72:75],v[68:71],v[120:123]\n"
RD"a[88:91],v227 offset:22528\n"
PK"v26,v28,v29\n"
MF"v[152:155],a[72:75],v[72:75],v[152:155]\n"
EX"v34,v34\n"
MF"v[184:187],a[72:75],v[76:79],v[184:187]\n"
WT"lgkmcnt(2)\n"
BS"a[152:155],v224,s[8:11],s18 offen offset:2048 sc1\n"
MF"v[92:95],a[76:79],v[64:67],v[92:95]\n"
EX"v35,v35\n"
MF"v[124:127],a[76:79],v[68:71],v[124:127]\n"
RD"a[92:95],v227 offset:23552\n"
PK"v27,v30,v31\n"
MF"v[156:159],a[76:79],v[72:75],v[156:159]\n"
SA"s18,s18,4096\n"
EX"v40,v40\n"
MF"v[188:191],a[76:79],v[76:79],v[188:191]\n"
BL"v225,s[8:11],s43 offen offset:2048 sc1 lds\n"
MF"v[96:99],a[80:83],v[64:67],v[96:99]\n"
EX"v41,v41\n"
MF"v[128:131],a[80:83],v[68:71],v[128:131]\n"
RD"a[96:99],v227 offset:24576\n"
MF"v[160:163],a[80:83],v[72:75],v[160:163]\n"
EX"v42,v42\n"
MF"v[192:195],a[80:83],v[76:79],v[192:195]\n"
WT"lgkmcnt(2)\n"
BS"a[156:159],v224,s[8:11],s18 offen offset:0 sc1\n"
MF"v[100:103],a[84:87],v[64:67],v[100:103]\n"
EX"v43,v43\n"
MF"v[132:135],a[84:87],v[68:71],v[132:135]\n"
RD"a[100:103],v227 offset:25600\n"
MF"v[164:167],a[84:87],v[72:75],v[164:167]\n"
EX"v48,v48\n"
MF"v[196:199],a[84:87],v[76:79],v[196:199]\n"
BL"v225,s[8:11],s43 offen offset:3072 sc1 lds\n"
MF"v[104:107],a[88:91],v[64:67],v[104:107]\n"
EX"v49,v49\n"
MF"v[136:139],a[88:91],v[68:71],v[136:139]\n"
RD"a[104:107],v227 offset:26624\n"
MF"v[168:171],a[88:91],v[72:75],v[168:171]\n"
EX"v50,v50\n"
MF"v[200:203],a[88:91],v[76:79],v[200:203]\n"
WT"lgkmcnt(2)\n"
BS"a[160:163],v224,s[8:11],s18 offen offset:2048 sc1\n"
MF"v[108:111],a[92:95],v[64:67],v[108:111]\n"
SA"m0,s40,16384\n"
EX"v51,v51\n"
MF"v[140:143],a[92:95],v[68:71],v[140:143]\n"
RD"a[108:111],v227 offset:27648\n"
MF"v[172:175],a[92:95],v[72:75],v[172:175]\n"
SA"s18,s18,4096\n"
EX"v56,v56\n"
MF"v[204:207],a[92:95],v[76:79],v[204:207]\n"
BL"v225,s[8:11],s44 offen offset:0 sc1 lds\n"
MF"a[132:135],a[128:131],v[64:67],a[132:135]\n"
SA"s40,s40,32768\n"
EX"v57,v57\n"
MF"a[136:139],a[128:131],v[68:71],a[136:139]\n"
"s_cmp_eq_u32 s40,s46\n"
RD"a[112:115],v227 offset:28672\n"
"s_cselect_b32 s40,s47,s40\n"
MF"a[140:143],a[128:131],v[72:75],a[140:143]\n"
EX"v58,v58\n"
MF"a[144:147],a[128:131],v[76:79],a[144:147]\n"
BS"a[164:167],v224,s[8:11],s18 offen offset:0 sc1\n"
MF"v[80:83],a[96:99],v[0:3],v[80:83]\n"
"s_lshr_b32 s21,s16,1\n"
EX"v59,v59\n"
MF"v[112:115],a[96:99],v[8:11],v[112:115]\n"
"s_sub_u32 s21,s21,s33\n"
RD"a[116:119],v227 offset:29696\n"
MF"v[144:147],a[96:99],v[16:19],v[144:147]\n"
"s_and_b32 s21,s21,3\n"
EX"v36,v36\n"
MF"v[176:179],a[96:99],v[24:27],v[176:179]\n"
WT"lgkmcnt(2)\n"
BL"v225,s[8:11],s44 offen offset:1024 sc1 lds\n"
MF"v[84:87],a[100:103],v[0:3],v[84:87]\n"
"s_lshl_b32 s21,s21,1\n"
EX"v37,v37\n"
MF"v[116:119],a[100:103],v[8:11],v[116:119]\n"
"s_and_b32 s22,s16,1\n"
RD"a[120:123],v227 offset:30720\n"
SA"s21,s21,s22\n"
MF"v[148:151],a[100:103],v[16:19],v[148:151]\n"
"s_lshl_b32 s21,s21,1\n"
EX"v38,v38\n"
MF"v[180:183],a[100:103],v[24:27],v[180:183]\n"
"s_and_b32 s22,s30,1\n"
MF"v[88:91],a[104:107],v[0:3],v[88:91]\n"
SA"s21,s21,s22\n"
EX"v39,v39\n"
MF"v[120:123],a[104:107],v[8:11],v[120:123]\n"
"s_mul_i32 s21,s21,10240\n"
RD"a[124:127],v227 offset:31744\n"
MF"v[152:155],a[104:107],v[16:19],v[152:155]\n"
"s_lshr_b32 s22,s30,1\n"
EX"v44,v44\n"
MF"v[184:187],a[104:107],v[24:27],v[184:187]\n"
"s_lshl_b32 s22,s22,10\n"
BL"v225,s[8:11],s44 offen offset:2048 sc1 lds\n"
MF"v[92:95],a[108:111],v[0:3],v[92:95]\n"
SA"s21,s21,s22\n"
EX"v45,v45\n"
MF"v[124:127],a[108:111],v[8:11],v[124:127]\n"
"s_mul_i32 s22,s35,163840\n"
MF"v[156:159],a[108:111],v[16:19],v[156:159]\n"
SA"s21,s21,s22\n"
EX"v46,v46\n"
MF"v[188:191],a[108:111],v[24:27],v[188:191]\n"
WT"lgkmcnt(1)\n"
SA"s18,s21,3801088\n"
MF"v[96:99],a[112:115],v[0:3],v[96:99]\n"
SA"s16,s16,1\n"
WT"lgkmcnt(0)\n"
"s_barrier\n"
MF"v[128:131],a[112:115],v[8:11],v[128:131]\n"
RD"a[64:67],v226 offset:0\n"
MF"v[160:163],a[112:115],v[16:19],v[160:163]\n"
EX"v47,v47\n"
MF"v[192:195],a[112:115],v[24:27],v[192:195]\n"
BL"v225,s[8:11],s44 offen offset:3072 sc1 lds\n"
MF"v[100:103],a[116:119],v[0:3],v[100:103]\n"
EX"v52,v52\n"
MF"v[132:135],a[116:119],v[8:11],v[132:135]\n"
RD"a[68:71],v226 offset:1024\n"
MF"v[164:167],a[116:119],v[16:19],v[164:167]\n"
EX"v53,v53\n"
MF"v[196:199],a[116:119],v[24:27],v[196:199]\n"
RD"a[72:75],v226 offset:2048\n"
MF"v[104:107],a[120:123],v[0:3],v[104:107]\n"
EX"v54,v54\n"
MF"v[136:139],a[120:123],v[8:11],v[136:139]\n"
RD"a[76:79],v226 offset:3072\n"
MF"v[168:171],a[120:123],v[16:19],v[168:171]\n"
EX"v55,v55\n"
MF"v[200:203],a[120:123],v[24:27],v[200:203]\n"
MF"v[108:111],a[124:127],v[0:3],v[108:111]\n"
EX"v60,v60\n"
MF"v[140:143],a[124:127],v[8:11],v[140:143]\n"
RD"a[80:83],v226 offset:4096\n"
MF"v[172:175],a[124:127],v[16:19],v[172:175]\n"
EX"v61,v61\n"
MF"v[204:207],a[124:127],v[24:27],v[204:207]\n"
RD"a[84:87],v226 offset:5120\n"
MF"a[132:135],a[128:131],v[0:3],a[132:135]\n"
EX"v62,v62\n"
MF"a[136:139],a[128:131],v[8:11],a[136:139]\n"
RD"a[88:91],v226 offset:6144\n"
MF"a[140:143],a[128:131],v[16:19],a[140:143]\n"
EX"v63,v63\n"
MF"a[144:147],a[128:131],v[24:27],a[144:147]\n"
"s_sub_u32 s20,s20,1\n"
"s_cbranch_scc0 LRT_%=\n"
"s_lshl_b32 s21,s35,8\n"
SA"s21,s21,92160\n"
SA"s21,s21,s45\n"
WT"vmcnt(3)\n"
"s_barrier\n"
"buffer_store_dwordx2 v[232:233],v230,s[8:11],s21 offen sc1\n"
"v_cmp_eq_u64 vcc,s[28:29],v[244:245]\n"
"s_cmp_eq_u64 vcc,exec\n"
"s_cbranch_scc0 L1105_%=\n"
"L1106_%=:\n"
"s_mov_b32 s57,0\n"
"s_mov_b32 s56,0\n"
"s_mov_b32 s14,0\n"
"s_branch LP1_%=\n"
"LEND_%=:\n"
      : : [ka] "s"(__builtin_amdgcn_kernarg_segment_ptr()), [b] "s"(blockIdx.x), [t] "v"(threadIdx.x)
      : "v1", "v2", "v3", "v4", "v5", "v6", "v7", "v8", "v9", "v10", "v11", "v12", "v13", "v14", "v15", "v16", "v17", "v18", "v19", "v20", "v21", "v22", "v23", "v24", "v25", "v26", "v27", "v28", "v29", "v30", "v31", "v32", "v33", "v34", "v35", "v36", "v37", "v38", "v39", "v40", "v41", "v42", "v43", "v44", "v45", "v46", "v47", "v48", "v49", "v50", "v51", "v52", "v53", "v54", "v55", "v56", "v57", "v58", "v59", "v60", "v61", "v62", "v63", "v64", "v65", "v66", "v67", "v68", "v69", "v70", "v71", "v72", "v73", "v74", "v75", "v76", "v77", "v78", "v79", "v80", "v81", "v82", "v83", "v84", "v85", "v86", "v87", "v88", "v89", "v90", "v91", "v92", "v93", "v94", "v95", "v96", "v97", "v98", "v99", "v100", "v101", "v102", "v103", "v104", "v105", "v106", "v107", "v108", "v109", "v110", "v111", "v112", "v113", "v114", "v115", "v116", "v117", "v118", "v119", "v120", "v121", "v122", "v123", "v124", "v125", "v126", "v127", "v128", "v129", "v130", "v131", "v132", "v133", "v134", "v135", "v136", "v137", "v138", "v139", "v140", "v141", "v142", "v143", "v144", "v145", "v146", "v147", "v148", "v149", "v150", "v151", "v152", "v153", "v154", "v155", "v156", "v157", "v158", "v159", "v160", "v161", "v162", "v163", "v164", "v165", "v166", "v167", "v168", "v169", "v170", "v171", "v172", "v173", "v174", "v175", "v176", "v177", "v178", "v179", "v180", "v181", "v182", "v183", "v184", "v185", "v186", "v187", "v188", "v189", "v190", "v191", "v192", "v193", "v194", "v195", "v196", "v197", "v198", "v199", "v200", "v201", "v202", "v203", "v204", "v205", "v206", "v207", "v208", "v209", "v210", "v211", "v212", "v213", "v214", "v215", "v216", "v217", "v218", "v219", "v220", "v221", "v222", "v223", "v224", "v225", "v226", "v227", "v228", "v229", "v230", "v231", "v232", "v233", "v234", "v235", "v236", "v237", "v238", "v239", "v240", "v241", "v242", "v243", "v244", "v245", "v246", "v247", "v248", "v249", "v250", "v251", "v252", "v253", "v254", "v255", "a0", "a1", "a2", "a3", "a4", "a5", "a6", "a7", "a8", "a9", "a10", "a11", "a12", "a13", "a14", "a15", "a16", "a17", "a18", "a19", "a20", "a21", "a22", "a23", "a24", "a25", "a26", "a27", "a28", "a29", "a30", "a31", "a32", "a33", "a34", "a35", "a36", "a37", "a38", "a39", "a40", "a41", "a42", "a43", "a44", "a45", "a46", "a47", "a48", "a49", "a50", "a51", "a52", "a53", "a54", "a55", "a56", "a57", "a58", "a59", "a60", "a61", "a62", "a63", "a64", "a65", "a66", "a67", "a68", "a69", "a70", "a71", "a72", "a73", "a74", "a75", "a76", "a77", "a78", "a79", "a80", "a81", "a82", "a83", "a84", "a85", "a86", "a87", "a88", "a89", "a90", "a91", "a92", "a93", "a94", "a95", "a96", "a97", "a98", "a99", "a100", "a101", "a102", "a103", "a104", "a105", "a106", "a107", "a108", "a109", "a110", "a111", "a112", "a113", "a114", "a115", "a116", "a117", "a118", "a119", "a120", "a121", "a122", "a123", "a124", "a125", "a126", "a127", "a128", "a129", "a130", "a131", "a132", "a133", "a134", "a135", "a136", "a137", "a138", "a139", "a140", "a141", "a142", "a143", "a144", "a145", "a146", "a147", "a148", "a149", "a150", "a151", "a152", "a153", "a154", "a155", "a156", "a157", "a158", "a159", "a160", "a161", "a162", "a163", "a164", "a165", "a166", "a167", "a168", "a169", "a170", "a171", "a172", "a173", "a174", "a175", "a176", "a177", "a178", "a179", "a180", "a181", "a182", "a183", "a184", "a185", "a186", "a187", "a188", "a189", "a190", "a191", "a192", "a193", "a194", "a195", "a196", "a197", "a198", "a199", "a200", "a201", "a202", "a203", "a204", "a205", "a206", "a207", "a208", "a209", "a210", "a211", "a212", "a213", "a214", "a215", "a216", "a217", "a218", "a219", "a220", "a221", "a222", "a223", "a224", "a225", "a226", "a227", "a228", "a229", "a230", "a231", "a232", "a233", "a234", "a235", "a236", "a237", "a238", "a239", "a240", "a241", "a242", "a243", "a244", "a245", "a246", "a247", "a248", "a249", "a250", "a251", "a252", "a253", "a254", "a255", "s8", "s9", "s10", "s11", "s12", "s13", "s14", "s15", "s16", "s17", "s18", "s19", "s20", "s21", "s22", "s23", "s24", "s25", "s26", "s27", "s28", "s29", "s30", "s31", "s32", "s33", "s34", "s35", "s36", "s37", "s38", "s39", "s40", "s41", "s42", "s43", "s44", "s45", "s46", "s47", "s48", "s49", "s50", "s51", "s52", "s53", "s54", "s55", "s56", "s57", "s58", "s59", "s60", "s61", "s62", "s63", "s64", "s65", "s66", "s67", "s68", "s69", "s70", "s71", "s72", "s73", "s74", "s75", "s76", "s77", "s78", "s79", "s80", "s81", "s82", "s83", "s84", "s85", "s86", "s87", "s88", "s89", "s90", "s91", "s92", "s93", "s94", "s95", "s96", "s97", "s98", "s99", "scc", "vcc", "memory");
}

extern "C" void kernel_launch(void* const* d_in, const int* in_sizes, int n_in, void* d_out, int out_size, void* d_ws, size_t ws_size, hipStream_t stream) {
  (void)hipFuncSetAttribute((const void*)mega_kernel, hipFuncAttributeMaxDynamicSharedMemorySize, 163840);
  mega_kernel<<<256, 256, 163840, stream>>>((const bf16*)d_in[0], (const bf16*)d_in[1], (const bf16*)d_in[2], (const bf16*)d_in[3], (const bf16*)d_in[4], (const bf16*)d_in[5],
      (const bf16*)d_in[7], (const bf16*)d_in[8], (const bf16*)d_in[9], (const bf16*)d_in[10], (char*)d_ws + (size_t)0 * 1048576, (bf16*)d_out);
}
```

```cpp
#include <hip/hip_runtime.h>
#include <hip/hip_bf16.h>
typedef __hip_bfloat16 bf16;
#define MF "v_mfma_f32_16x16x32_bf16 "
#define RD "ds_read_b128 "
#define RT "ds_read_b64_tr_b16 "
#define BL "buffer_load_dwordx4 "
#define BS "buffer_store_dwordx4 "
#define PK "v_cvt_pk_bf16_f32 "
#define EX "v_exp_f32 "
#define WT "s_waitcnt "
#define MU "v_mul_f32 "
#define SA "s_add_u32 "

__global__ __launch_bounds__(256, 1) void mega_kernel(const bf16* x, const bf16* ckv, const bf16* wq, const bf16* wuk, const bf16* wuv, const bf16* wo, const bf16* w_r,
                                                      const bf16* wg, const bf16* wu, const bf16* wd, char* ws, bf16* out) {
  asm volatile(
"s_load_dwordx16 s[64:79],%[ka],0\n"
"s_load_dwordx8 s[80:87],%[ka],64\n"
"s_mov_b32 s31,%[b]\n"
"v_lshrrev_b32 v1,6,%[t]\n"
"v_and_b32 v2,63,%[t]\n"
"v_readfirstlane_b32 s30,v1\n"
"v_lshlrev_b32 v224,4,v2\n"
"s_getpc_b64 s[24:25]\n"
"LPC_%=:\n"
"s_and_b32 s25,s25,0xffff\n"
"s_mov_b32 s26,LEND_%=-LPC_%=\n"
"s_mov_b32 s27,0x20000\n"
"s_lshr_b32 s49,s31,3\n"
"s_lshl_b32 s49,s49,2\n"
SA"s49,s49,s30\n"
"s_lshl_b32 s49,s49,7\n"
"v_lshlrev_b32 v10,14,v2\n"
"buffer_load_dword v255,v10,s[24:27],s49 offen\n"
"s_and_b32 s32,s31,7\n"
"s_lshr_b32 s33,s31,6\n"
"s_bfe_u32 s34,s31,0x30003\n"
"s_lshl_b32 s35,s32,2\n"
SA"s35,s35,s33\n"
"s_mov_b32 s46,0x65327410\n"
"s_mov_b32 s47,0x54217630\n"
"s_bitcmp1_b32 s34,0\n"
"s_cselect_b32 s46,0x64317520,s46\n"
"s_cselect_b32 s47,0x64327510,s47\n"
"s_bitcmp1_b32 s34,1\n"
"s_cselect_b32 s46,s47,s46\n"
"s_mul_i32 s47,s32,0x11111111\n"
SA"s46,s46,s47\n"
"s_lshl_b32 s36,s30,12\n"
"s_mul_i32 s37,s30,24576\n"
SA"s37,s37,65536\n"
"v_lshrrev_b32 v3,3,v2\n"
"v_lshrrev_b32 v4,4,v2\n"
"v_and_b32 v5,7,v2\n"
"v_xor_b32 v6,v5,v4\n"
"v_lshlrev_b32 v6,4,v6\n"
"v_lshl_add_u32 v6,v3,10,v6\n"
"s_lshl_b32 s50,s30,15\n"
"v_add_u32 v234,s50,v6\n"
"v_xor_b32 v235,64,v234\n"
"v_add_u32 v235,7168,v235\n"
"v_add_u32 v236,14336,v234\n"
"v_add_u32 v237,14336,v235\n"
"v_and_b32 v7,1,v4\n"
"v_lshlrev_b32 v7,1,v7\n"
"v_xor_b32 v7,v7,v5\n"
"v_lshlrev_b32 v7,4,v7\n"
"v_lshl_add_u32 v238,v3,13,v7\n"
"v_xor_b32 v239,64,v238\n"
"v_add_u32 v239,64512,v239\n"
"v_add_u32 v240,129024,v238\n"
"v_add_u32 v241,129024,v239\n"
"v_and_b32 v8,31,v2\n"
"v_lshlrev_b32 v8,3,v8\n"
"v_lshrrev_b32 v9,5,v2\n"
"s_lshl_b32 s50,s31,2\n"
SA"s50,s50,s30\n"
"s_mul_i32 s50,s50,2560\n"
"v_mul_u32_u24 v9,1280,v9\n"
"v_add3_u32 v9,v9,v8,s50\n"
"s_lshl_b32 s50,s34,17\n"
"s_lshl_b32 s51,s35,8\n"
"s_and_b32 s52,s30,1\n"
"s_lshl_b32 s52,s52,7\n"
SA"s52,s52,s51\n"
"s_mov_b32 s14,0x120000\n"
"s_mov_b32 s15,0x20000\n"
"s_sub_u32 s18,4194304,s52\n"
"s_mov_b32 s19,0x20000\n"
WT"lgkmcnt(0)\n"
"s_load_dwordx2 s[28:29],s[84:85],0\n"
SA"s12,s66,s50\n"
"s_addc_u32 s13,s67,0\n"
"s_and_b32 s13,s13,0xffff\n"
"s_cmp_lt_u32 s30,2\n"
"s_cselect_b64 s[16:17],s[70:71],s[72:73]\n"
SA"s16,s16,s52\n"
"s_addc_u32 s17,s17,0\n"
"s_and_b32 s17,s17,0xffff\n"
"global_load_dwordx2 v[128:129],v9,s[64:65] offset:0\n"
"global_load_dwordx2 v[130:131],v9,s[64:65] offset:256\n"
"global_load_dwordx2 v[132:133],v9,s[64:65] offset:512\n"
"global_load_dwordx2 v[134:135],v9,s[64:65] offset:768\n"
"global_load_dwordx2 v[136:137],v9,s[64:65] offset:1024\n"
"global_load_dwordx2 v[138:139],v8,s[76:77] offset:0\n"
"global_load_dwordx2 v[140:141],v8,s[76:77] offset:256\n"
"global_load_dwordx2 v[142:143],v8,s[76:77] offset:512\n"
"global_load_dwordx2 v[144:145],v8,s[76:77] offset:768\n"
"global_load_dwordx2 v[146:147],v8,s[76:77] offset:1024\n"
SA"m0,s37,0\n"
"s_bfe_u32 s48,s46,0x30000\n"
"s_lshl_b32 s48,s48,19\n"
BL"v238,s[16:19],s48 offen offset:0 lds\n"
BL"v239,s[16:19],s48 offen offset:1024 lds\n"
BL"v240,s[16:19],s48 offen offset:2048 lds\n"
BL"v241,s[16:19],s48 offen offset:3072 lds\n"
SA"m0,s37,4096\n"
"s_bfe_u32 s48,s46,0x30000\n"
"s_lshl_b32 s48,s48,19\n"
"s_bitset1_b32 s48,18\n"
BL"v238,s[16:19],s48 offen offset:0 lds\n"
BL"v239,s[16:19],s48 offen offset:1024 lds\n"
BL"v240,s[16:19],s48 offen offset:2048 lds\n"
BL"v241,s[16:19],s48 offen offset:3072 lds\n"
"v_lshlrev_b32 v253,7,%[t]\n"
"s_mov_b32 s8,s84\n"
"s_and_b32 s9,s85,0xffff\n"
"s_mov_b32 s10,80478208\n"
"s_mov_b32 s11,0x20000\n"
SA"s20,s68,s51\n"
"s_addc_u32 s21,s69,0\n"
"s_and_b32 s21,s21,0xffff\n"
"s_sub_u32 s22,5242880,s51\n"
"s_mov_b32 s23,0x20000\n"
"v_and_b32 v11,15,v2\n"
"v_lshrrev_b32 v12,1,v11\n"
"v_xor_b32 v12,v12,v4\n"
"v_lshlrev_b32 v12,4,v12\n"
"v_lshl_add_u32 v242,v11,7,v12\n"
"v_xor_b32 v243,64,v242\n"
"v_lshrrev_b32 v12,2,v11\n"
"v_lshl_add_u32 v12,v4,3,v12\n"
"v_lshlrev_b32 v12,7,v12\n"
"v_and_b32 v13,3,v2\n"
"v_lshl_add_u32 v12,v13,3,v12\n"
"v_and_b32 v13,3,v3\n"
"v_lshl_add_u32 v12,v13,5,v12\n"
"v_add_u32 v246,s37,v12\n"
"v_xor_b32 v247,32,v246\n"
"v_xor_b32 v248,64,v246\n"
"v_xor_b32 v249,96,v246\n"
"v_mov_b32 v13,2130706432\n"
"v_cmp_eq_u32 vcc,0,v2\n"
"s_cmp_eq_u32 s30,0\n"
"s_cselect_b32 s45,0,2130706432\n"
"v_cndmask_b32_e64 v230,v13,0,vcc\n"
"s_lshl_b32 s39,s30,3\n"
"s_lshr_b32 s98,s46,s39\n"
"s_and_b32 s39,s98,7\n"
"s_lshl_b32 s39,s39,7\n"
"s_and_b32 s98,s98,0x70\n"
"s_lshl_b32 s98,s98,3\n"
"s_lshl_b32 s50,s30,15\n"
"s_sub_u32 s50,1048576,s50\n"
SA"s39,s39,s50\n"
SA"s98,s98,s50\n"
"s_mov_b32 s55,0x3e0293ee\n"
"s_cmp_lt_u32 s30,2\n"
"s_cbranch_scc0 LV_%=\n"
WT"vmcnt(17)\n"
"v_lshlrev_b32 v148,16,v128\n"
"v_and_b32 v149,0xffff0000,v128\n"
"v_lshlrev_b32 v150,16,v129\n"
"v_and_b32 v151,0xffff0000,v129\n"
WT"vmcnt(16)\n"
"v_lshlrev_b32 v152,16,v130\n"
"v_and_b32 v153,0xffff0000,v130\n"
"v_lshlrev_b32 v154,16,v131\n"
"v_and_b32 v155,0xffff0000,v131\n"
WT"vmcnt(15)\n"
"v_lshlrev_b32 v156,16,v132\n"
"v_and_b32 v157,0xffff0000,v132\n"
"v_lshlrev_b32 v158,16,v133\n"
"v_and_b32 v159,0xffff0000,v133\n"
WT"vmcnt(14)\n"
"v_lshlrev_b32 v160,16,v134\n"
"v_and_b32 v161,0xffff0000,v134\n"
"v_lshlrev_b32 v162,16,v135\n"
"v_and_b32 v163,0xffff0000,v135\n"
WT"vmcnt(13)\n"
"v_lshlrev_b32 v164,16,v136\n"
"v_and_b32 v165,0xffff0000,v136\n"
"v_lshlrev_b32 v166,16,v137\n"
"v_and_b32 v167,0xffff0000,v137\n"
"v_add_f32 v188,v148,v149\n"
"v_add_f32 v189,v150,v151\n"
"v_add_f32 v188,v188,v152\n"
"v_add_f32 v189,v189,v153\n"
"v_add_f32 v188,v188,v154\n"
"v_add_f32 v189,v189,v155\n"
"v_add_f32 v188,v188,v156\n"
"v_add_f32 v189,v189,v157\n"
"v_add_f32 v188,v188,v158\n"
"v_add_f32 v189,v189,v159\n"
"v_add_f32 v188,v188,v160\n"
"v_add_f32 v189,v189,v161\n"
"v_add_f32 v188,v188,v162\n"
"v_add_f32 v189,v189,v163\n"
"v_add_f32 v188,v188,v164\n"
"v_add_f32 v189,v189,v165\n"
"v_add_f32 v188,v188,v166\n"
"v_add_f32 v189,v189,v167\n"
"v_add_f32 v188,v188,v189\n"
"s_nop 1\n"
"v_add_f32_dpp v188,v188,v188 quad_perm:[1,0,3,2] row_mask:0xf bank_mask:0xf\n"
WT"vmcnt(12)\n"
"v_lshlrev_b32 v168,16,v138\n"
"v_and_b32 v169,0xffff0000,v138\n"
"v_add_f32_dpp v188,v188,v188 quad_perm:[2,3,0,1] row_mask:0xf bank_mask:0xf\n"
"v_lshlrev_b32 v170,16,v139\n"
"v_and_b32 v171,0xffff0000,v139\n"
"v_add_f32_dpp v188,v188,v188 row_half_mirror row_mask:0xf bank_mask:0xf\n"
WT"vmcnt(11)\n"
"v_lshlrev_b32 v172,16,v140\n"
"v_and_b32 v173,0xffff0000,v140\n"
"v_add_f32_dpp v188,v188,v188 row_mirror row_mask:0xf bank_mask:0xf\n"
"v_lshlrev_b32 v174,16,v141\n"
"v_and_b32 v175,0xffff0000,v141\n"
"v_mov_b32 v189,v188\n"
WT"vmcnt(10)\n"
"v_lshlrev_b32 v176,16,v142\n"
"v_and_b32 v177,0xffff0000,v142\n"
"v_permlane16_swap_b32 v189,v188\n"
"v_lshlrev_b32 v178,16,v143\n"
"v_and_b32 v179,0xffff0000,v143\n"
"v_add_f32 v188,v188,v189\n"
WT"vmcnt(9)\n"
"v_lshlrev_b32 v180,16,v144\n"
SA"m0,s36,0\n"
"s_bfe_u32 s48,s46,0x30000\n"
"s_lshl_b32 s48,s48,7\n"
BL"v234,s[12:15],s48 offen offset:0 lds\n"
BL"v235,s[12:15],s48 offen offset:1024 lds\n"
BL"v236,s[12:15],s48 offen offset:2048 lds\n"
BL"v237,s[12:15],s48 offen offset:3072 lds\n"
"v_and_b32 v181,0xffff0000,v144\n"
MU"v191,0x3acccccd,v188\n"
"v_lshlrev_b32 v182,16,v145\n"
"v_and_b32 v183,0xffff0000,v145\n"
PK"v128,v191,v191\n"
WT"vmcnt(12)\n"
"v_lshlrev_b32 v184,16,v146\n"
"v_and_b32 v185,0xffff0000,v146\n"
"v_lshlrev_b32 v128,16,v128\n"
"v_lshlrev_b32 v186,16,v147\n"
"v_and_b32 v187,0xffff0000,v147\n"
"v_sub_f32 v148,v148,v128\n"
"v_sub_f32 v149,v149,v128\n"
"v_sub_f32 v150,v150,v128\n"
"v_sub_f32 v151,v151,v128\n"
"v_sub_f32 v152,v152,v128\n"
"v_sub_f32 v153,v153,v128\n"
"v_sub_f32 v154,v154,v128\n"
"v_sub_f32 v155,v155,v128\n"
"v_sub_f32 v156,v156,v128\n"
"v_sub_f32 v157,v157,v128\n"
"v_sub_f32 v158,v158,v128\n"
"v_sub_f32 v159,v159,v128\n"
"v_sub_f32 v160,v160,v128\n"
"v_sub_f32 v161,v161,v128\n"
"v_sub_f32 v162,v162,v128\n"
"v_sub_f32 v163,v163,v128\n"
"v_sub_f32 v164,v164,v128\n"
"v_sub_f32 v165,v165,v128\n"
"v_sub_f32 v166,v166,v128\n"
"v_sub_f32 v167,v167,v128\n"
MU"v188,v148,v148\n"
MU"v189,v149,v149\n"
"v_fmac_f32 v188,v150,v150\n"
"v_fmac_f32 v189,v151,v151\n"
"v_fmac_f32 v188,v152,v152\n"
"v_fmac_f32 v189,v153,v153\n"
"v_fmac_f32 v188,v154,v154\n"
"v_fmac_f32 v189,v155,v155\n"
"v_fmac_f32 v188,v156,v156\n"
"v_fmac_f32 v189,v157,v157\n"
"v_fmac_f32 v188,v158,v158\n"
"v_fmac_f32 v189,v159,v159\n"
"v_fmac_f32 v188,v160,v160\n"
"v_fmac_f32 v189,v161,v161\n"
"v_fmac_f32 v188,v162,v162\n"
"v_fmac_f32 v189,v163,v163\n"
"v_fmac_f32 v188,v164,v164\n"
"v_fmac_f32 v189,v165,v165\n"
"v_fmac_f32 v188,v166,v166\n"
"v_fmac_f32 v189,v167,v167\n"
"v_add_f32 v188,v188,v189\n"
PK"v149,v148,v149\n"
"v_lshlrev_b32 v148,16,v149\n"
"v_add_f32_dpp v188,v188,v188 quad_perm:[1,0,3,2] row_mask:0xf bank_mask:0xf\n"
"v_and_b32 v149,0xffff0000,v149\n"
PK"v151,v150,v151\n"
"v_add_f32_dpp v188,v188,v188 quad_perm:[2,3,0,1] row_mask:0xf bank_mask:0xf\n"
"v_lshlrev_b32 v150,16,v151\n"
"v_and_b32 v151,0xffff0000,v151\n"
SA"m0,s37,8192\n"
"s_bfe_u32 s48,s46,0x30004\n"
"s_lshl_b32 s48,s48,19\n"
BL"v238,s[16:19],s48 offen offset:0 lds\n"
BL"v239,s[16:19],s48 offen offset:1024 lds\n"
BL"v240,s[16:19],s48 offen offset:2048 lds\n"
BL"v241,s[16:19],s48 offen offset:3072 lds\n"
"v_add_f32_dpp v188,v188,v188 row_half_mirror row_mask:0xf bank_mask:0xf\n"
PK"v153,v152,v153\n"
"v_lshlrev_b32 v152,16,v153\n"
"v_add_f32_dpp v188,v188,v188 row_mirror row_mask:0xf bank_mask:0xf\n"
"v_and_b32 v153,0xffff0000,v153\n"
PK"v155,v154,v155\n"
"v_mov_b32 v189,v188\n"
"v_lshlrev_b32 v154,16,v155\n"
"v_and_b32 v155,0xffff0000,v155\n"
"v_permlane16_swap_b32 v189,v188\n"
PK"v157,v156,v157\n"
"v_lshlrev_b32 v156,16,v157\n"
"v_add_f32 v188,v188,v189\n"
"v_and_b32 v157,0xffff0000,v157\n"
PK"v159,v158,v159\n"
"v_sub_f32 v191,v191,v128\n"
"v_lshlrev_b32 v158,16,v159\n"
"v_and_b32 v159,0xffff0000,v159\n"
MU"v188,0x3acccccd,v188\n"
PK"v161,v160,v161\n"
"v_lshlrev_b32 v160,16,v161\n"
"v_fma_f32 v188,-v191,v191,v188\n"
"v_and_b32 v161,0xffff0000,v161\n"
PK"v163,v162,v163\n"
"v_max_f32 v188,0,v188\n"
"v_lshlrev_b32 v162,16,v163\n"
"v_and_b32 v163,0xffff0000,v163\n"
PK"v188,v188,v188\n"
PK"v165,v164,v165\n"
"v_lshlrev_b32 v164,16,v165\n"
"v_lshlrev_b32 v188,16,v188\n"
"v_and_b32 v165,0xffff0000,v165\n"
PK"v167,v166,v167\n"
"v_add_f32 v188,0x3727c5ac,v188\n"
"v_lshlrev_b32 v166,16,v167\n"
"v_and_b32 v167,0xffff0000,v167\n"
PK"v188,v188,v188\n"
"v_bfe_u32 v130,v224,5,4\n"
"v_mul_u32_u24 v130,144,v130\n"
"v_lshlrev_b32 v188,16,v188\n"
"v_bfe_u32 v131,v224,4,1\n"
"v_lshl_add_u32 v130,v131,3,v130\n"
"v_sqrt_f32 v189,v188\n"
"v_lshrrev_b32 v131,9,v224\n"
"v_lshl_add_u32 v130,v131,4,v130\n"
"v_rcp_f32 v190,v189\n"
"s_lshl_b32 s52,s30,5\n"
"v_add_u32 v130,s52,v130\n"
"v_fma_f32 v188,-v189,v189,v188\n"
MU"v190,0.5,v190\n"
"v_fma_f32 v189,v188,v190,v189\n"
PK"v189,v189,v189\n"
"v_lshlrev_b32 v189,16,v189\n"
"v_rcp_f32 v129,v189\n"
"s_nop 0\n"
MU"v148,v148,v129\n"
MU"v149,v149,v129\n"
PK"v138,v148,v149\n"
MU"v150,v150,v129\n"
MU"v151,v151,v129\n"
SA"m0,s37,12288\n"
"s_bfe_u32 s48,s46,0x30004\n"
"s_lshl_b32 s48,s48,19\n"
"s_bitset1_b32 s48,18\n"
BL"v238,s[16:19],s48 offen offset:0 lds\n"
BL"v239,s[16:19],s48 offen offset:1024 lds\n"
BL"v240,s[16:19],s48 offen offset:2048 lds\n"
BL"v241,s[16:19],s48 offen offset:3072 lds\n"
PK"v139,v150,v151\n"
"ds_write_b64 v130,v[138:139] offset:49152\n"
MU"v152,v152,v129\n"
MU"v153,v153,v129\n"
PK"v140,v152,v153\n"
MU"v154,v154,v129\n"
MU"v155,v155,v129\n"
PK"v141,v154,v155\n"
"ds_write_b64 v130,v[140:141] offset:51456\n"
MU"v156,v156,v129\n"
MU"v157,v157,v129\n"
PK"v142,v156,v157\n"
MU"v158,v158,v129\n"
MU"v159,v159,v129\n"
PK"v143,v158,v159\n"
"ds_write_b64 v130,v[142:143] offset:53760\n"
MU"v160,v160,v129\n"
MU"v161,v161,v129\n"
PK"v144,v160,v161\n"
MU"v162,v162,v129\n"
MU"v163,v163,v129\n"
PK"v145,v162,v163\n"
"ds_write_b64 v130,v[144:145] offset:56064\n"
MU"v164,v164,v129\n"
MU"v165,v165,v129\n"
PK"v146,v164,v165\n"
MU"v166,v166,v129\n"
MU"v167,v167,v129\n"
PK"v147,v166,v167\n"
"ds_write_b64 v130,v[146:147] offset:58368\n"
"v_lshlrev_b32 v148,16,v138\n"
"v_and_b32 v149,0xffff0000,v138\n"
MU"v188,v148,v168\n"
MU"v189,v149,v169\n"
"v_lshlrev_b32 v150,16,v139\n"
"v_and_b32 v151,0xffff0000,v139\n"
"v_fmac_f32 v188,v150,v170\n"
"v_fmac_f32 v189,v151,v171\n"
"v_lshlrev_b32 v152,16,v140\n"
"v_and_b32 v153,0xffff0000,v140\n"
"v_fmac_f32 v188,v152,v172\n"
"v_fmac_f32 v189,v153,v173\n"
"v_lshlrev_b32 v154,16,v141\n"
"v_and_b32 v155,0xffff0000,v141\n"
"v_fmac_f32 v188,v154,v174\n"
"v_fmac_f32 v189,v155,v175\n"
"v_lshlrev_b32 v156,16,v142\n"
"v_and_b32 v157,0xffff0000,v142\n"
"v_fmac_f32 v188,v156,v176\n"
"v_fmac_f32 v189,v157,v177\n"
"v_lshlrev_b32 v158,16,v143\n"
"v_and_b32 v159,0xffff0000,v143\n"
"v_fmac_f32 v188,v158,v178\n"
"v_fmac_f32 v189,v159,v179\n"
"v_lshlrev_b32 v160,16,v144\n"
"v_and_b32 v161,0xffff0000,v144\n"
"v_fmac_f32 v188,v160,v180\n"
"v_fmac_f32 v189,v161,v181\n"
"v_lshlrev_b32 v162,16,v145\n"
"v_and_b32 v163,0xffff0000,v145\n"
WT"lgkmcnt(0)\n"
SA"s28,s28,0x7f4a7c15\n"
"s_addc_u32 s29,s29,0x9e3779b9\n"
"v_mov_b32 v232,s28\n"
"v_mov_b32 v233,s29\n"
WT"vmcnt(16)\n"
RT"a[0:1],v246 offset:0\n"
RT"a[2:3],v246 offset:512\n"
RT"a[4:5],v247 offset:0\n"
RT"a[6:7],v247 offset:512\n"
RT"a[8:9],v248 offset:0\n"
RT"a[10:11],v248 offset:512\n"
RT"a[12:13],v249 offset:0\n"
RT"a[14:15],v249 offset:512\n"
WT"vmcnt(12)\n"
RT"a[16:17],v246 offset:4096\n"
RT"a[18:19],v246 offset:4608\n"
RT"a[20:21],v247 offset:4096\n"
RT"a[22:23],v247 offset:4608\n"
RT"a[24:25],v248 offset:4096\n"
RT"a[26:27],v248 offset:4608\n"
RT"a[28:29],v249 offset:4096\n"
RT"a[30:31],v249 offset:4608\n"
WT"vmcnt(8) lgkmcnt(15)\n"
"s_barrier\n"
RD"v[192:195],v242 offset:0\n"
RD"v[196:199],v243 offset:0\n"
RD"v[200:203],v242 offset:2048\n"
RD"v[204:207],v243 offset:2048\n"
RD"v[208:211],v242 offset:4096\n"
"v_lshrrev_b32 v131,7,v224\n"
WT"lgkmcnt(4)\n"
MF"v[0:3],a[0:3],v[192:195],0\n"
WT"lgkmcnt(3)\n"
RD"v[212:215],v243 offset:4096\n"
"v_lshl_add_u32 v131,v131,4,v224\n"
MF"v[0:3],a[16:19],v[196:199],v[0:3]\n"
SA"m0,s36,16384\n"
"s_bfe_u32 s48,s46,0x30004\n"
"s_lshl_b32 s48,s48,7\n"
MF"v[4:7],a[20:23],v[196:199],0\n"
"s_mul_i32 s52,s30,1152\n"
BL"v234,s[12:15],s48 offen offset:0 lds\n"
MF"v[4:7],a[4:7],v[192:195],v[4:7]\n"
"v_add_u32 v128,s52,v131\n"
"s_and_b32 s52,s30,1\n"
MF"v[8:11],a[8:11],v[192:195],0\n"
"s_mul_i32 s51,s52,1152\n"
RD"v[216:219],v242 offset:6144\n"
"v_add_u32 v131,s51,v131\n"
MF"v[8:11],a[24:27],v[196:199],v[8:11]\n"
"s_lshl_b32 s52,s52,10\n"
"v_fmac_f32 v188,v162,v182\n"
"v_fmac_f32 v189,v163,v183\n"
MF"v[12:15],a[28:31],v[196:199],0\n"
BL"v235,s[12:15],s48 offen offset:1024 lds\n"
MF"v[12:15],a[12:15],v[192:195],v[12:15]\n"
WT"lgkmcnt(1)\n"
RD"v[220:223],v243 offset:6144\n"
RD"v[132:135],v128 offset:49152\n"
MF"v[16:19],a[0:3],v[200:203],0\n"
WT"vmcnt(6)\n"
RT"a[32:33],v246 offset:8192\n"
RD"a[240:243],v128 offset:53760\n"
MF"v[16:19],a[16:19],v[204:207],v[16:19]\n"
"s_lshr_b32 s50,s31,1\n"
RD"a[244:247],v131 offset:58368\n"
"v_lshlrev_b32 v164,16,v146\n"
MF"v[20:23],a[20:23],v[204:207],0\n"
"s_mul_i32 s50,s50,20480\n"
"v_and_b32 v129,0x70,v224\n"
"v_and_b32 v165,0xffff0000,v146\n"
MF"v[20:23],a[4:7],v[200:203],v[20:23]\n"
"s_and_b32 s51,s31,1\n"
RD"v[192:195],v242 offset:8192\n"
RT"a[34:35],v246 offset:8704\n"
MF"v[24:27],a[8:11],v[200:203],0\n"
"s_lshl_b32 s51,s51,7\n"
BL"v236,s[12:15],s48 offen offset:2048 lds\n"
MF"v[24:27],a[24:27],v[204:207],v[24:27]\n"
SA"s50,s50,s51\n"
"v_and_b32 v130,0x380,v224\n"
"v_fmac_f32 v188,v164,v184\n"
MF"v[28:31],a[28:31],v[204:207],0\n"
SA"s50,s50,524288\n"
RT"a[36:37],v247 offset:8192\n"
"v_lshl_add_u32 v129,v130,1,v129\n"
MF"v[28:31],a[12:15],v[200:203],v[28:31]\n"
"s_lshl_b32 s52,s52,1\n"
RD"v[196:199],v243 offset:8192\n"
"v_fmac_f32 v189,v165,v185\n"
MF"v[32:35],a[0:3],v[208:211],0\n"
SA"s52,s52,s50\n"
BL"v237,s[12:15],s48 offen offset:3072 lds\n"
MF"v[32:35],a[16:19],v[212:215],v[32:35]\n"
SA"s52,s52,16384\n"
RT"a[38:39],v247 offset:8704\n"
"v_lshlrev_b32 v166,16,v147\n"
MF"v[36:39],a[20:23],v[212:215],0\n"
SA"m0,s37,16384\n"
RD"v[200:203],v242 offset:10240\n"
"v_and_b32 v167,0xffff0000,v147\n"
MF"v[36:39],a[4:7],v[208:211],v[36:39]\n"
"s_bfe_u32 s48,s46,0x30008\n"
"v_fmac_f32 v188,v166,v186\n"
"v_fmac_f32 v189,v167,v187\n"
MF"v[40:43],a[8:11],v[208:211],0\n"
"s_lshl_b32 s48,s48,19\n"
RT"a[40:41],v248 offset:8192\n"
"v_add_f32 v188,v188,v189\n"
MF"v[40:43],a[24:27],v[212:215],v[40:43]\n"
"s_lshl_b32 s51,s30,11\n"
BL"v238,s[16:19],s48 offen offset:0 lds\n"
MF"v[44:47],a[28:31],v[212:215],0\n"
SA"s50,s50,s51\n"
RD"v[204:207],v243 offset:10240\n"
"v_add_f32_dpp v188,v188,v188 quad_perm:[1,0,3,2] row_mask:0xf bank_mask:0xf\n"
MF"v[44:47],a[12:15],v[208:211],v[44:47]\n"
WT"lgkmcnt(4)\n"
BS"v[132:135],v129,s[8:11],s50 offen sc1\n"
MF"v[48:51],a[0:3],v[216:219],0\n"
SA"s51,s50,8192\n"
RT"a[42:43],v248 offset:8704\n"
"v_add_f32_dpp v188,v188,v188 quad_perm:[2,3,0,1] row_mask:0xf bank_mask:0xf\n"
MF"v[48:51],a[16:19],v[220:223],v[48:51]\n"
"s_lshl_b32 s90,s31,2\n"
BL"v239,s[16:19],s48 offen offset:1024 lds\n"
MF"v[52:55],a[20:23],v[220:223],0\n"
SA"s90,s90,s30\n"
BS"a[240:243],v129,s[8:11],s51 offen sc1\n"
MF"v[52:55],a[4:7],v[216:219],v[52:55]\n"
"s_lshl_b32 s90,s90,1\n"
RD"v[208:211],v242 offset:12288\n"
RT"a[44:45],v249 offset:8192\n"
MF"v[56:59],a[8:11],v[216:219],0\n"
"s_sub_u32 s91,2047,s90\n"
BS"a[244:247],v129,s[8:11],s52 offen sc1\n"
MF"v[56:59],a[24:27],v[220:223],v[56:59]\n"
RD"v[212:215],v243 offset:12288\n"
RT"a[46:47],v249 offset:8704\n"
MF"v[60:63],a[28:31],v[220:223],0\n"
BL"v240,s[16:19],s48 offen offset:2048 lds\n"
MF"v[60:63],a[12:15],v[216:219],v[60:63]\n"
"v_add_f32_dpp v188,v188,v188 row_half_mirror row_mask:0xf bank_mask:0xf\n"
MF"v[64:67],a[0:3],v[192:195],0\n"
RD"v[216:219],v242 offset:14336\n"
"v_add_f32_dpp v188,v188,v188 row_mirror row_mask:0xf bank_mask:0xf\n"
MF"v[64:67],a[16:19],v[196:199],v[64:67]\n"
WT"vmcnt(10)\n"
RT"a[48:49],v246 offset:12288\n"
"v_mov_b32 v189,v188\n"
MF"v[68:71],a[20:23],v[196:199],0\n"
"s_lshl_b32 s90,s90,2\n"
BL"v241,s[16:19],s48 offen offset:3072 lds\n"
MF"v[68:71],a[4:7],v[192:195],v[68:71]\n"
SA"s90,s90,8192\n"
RT"a[50:51],v246 offset:12800\n"
"v_permlane16_swap_b32 v189,v188\n"
MF"v[72:75],a[8:11],v[192:195],0\n"
SA"m0,s37,20480\n"
RD"v[220:223],v243 offset:14336\n"
"v_add_f32 v188,v188,v189\n"
MF"v[72:75],a[24:27],v[196:199],v[72:75]\n"
"s_bfe_u32 s48,s46,0x30008\n"
PK"v188,v188,v188\n"
"v_lshlrev_b32 v188,16,v188\n"
MF"v[76:79],a[28:31],v[196:199],0\n"
"s_lshl_b32 s48,s48,19\n"
RT"a[52:53],v247 offset:12288\n"
"v_ashrrev_i32 v189,31,v188\n"
MF"v[76:79],a[12:15],v[192:195],v[76:79]\n"
WT"lgkmcnt(4)\n"
"v_or_b32 v189,0x80000000,v189\n"
"v_xor_b32 v188,v188,v189\n"
MF"v[80:83],a[0:3],v[200:203],0\n"
"s_bitset1_b32 s48,18\n"
"v_and_b32 v188,0xffff0000,v188\n"
"v_lshrrev_b32 v189,9,v224\n"
MF"v[80:83],a[16:19],v[204:207],v[80:83]\n"
BL"v238,s[16:19],s48 offen offset:0 lds\n"
MF"v[84:87],a[20:23],v[204:207],0\n"
RT"a[54:55],v247 offset:12800\n"
"v_sub_u32 v190,s91,v189\n"
MF"v[84:87],a[4:7],v[200:203],v[84:87]\n"
BL"v239,s[16:19],s48 offen offset:1024 lds\n"
MF"v[88:91],a[8:11],v[200:203],0\n"
RT"a[56:57],v248 offset:12288\n"
"v_or_b32 v188,v188,v190\n"
MF"v[88:91],a[24:27],v[204:207],v[88:91]\n"
WT"vmcnt(9) lgkmcnt(3)\n"
"s_barrier\n"
MF"v[92:95],a[28:31],v[204:207],0\n"
RD"v[192:195],v242 offset:16384\n"
"v_and_b32 v190,0x1f0,v224\n"
MF"v[92:95],a[12:15],v[200:203],v[92:95]\n"
RT"a[58:59],v248 offset:12800\n"
"v_lshlrev_b32 v190,23,v190\n"
MF"v[96:99],a[0:3],v[208:211],0\n"
BL"v240,s[16:19],s48 offen offset:2048 lds\n"
MF"v[96:99],a[16:19],v[212:215],v[96:99]\n"
"v_lshl_add_u32 v190,v189,2,v190\n"
MF"v[100:103],a[20:23],v[212:215],0\n"
"buffer_store_dword v188,v190,s[8:11],s90 offen sc1\n"
MF"v[100:103],a[4:7],v[208:211],v[100:103]\n"
RD"v[196:199],v243 offset:16384\n"
RT"a[60:61],v249 offset:12288\n"
MF"v[104:107],a[8:11],v[208:211],0\n"
BL"v241,s[16:19],s48 offen offset:3072 lds\n"
MF"v[104:107],a[24:27],v[212:215],v[104:107]\n"
RT"a[62:63],v249 offset:12800\n"
MF"v[108:111],a[28:31],v[212:215],0\n"
SA"m0,s36,32768\n"
RD"v[200:203],v242 offset:18432\n"
"s_bfe_u32 s48,s46,0x30008\n"
MF"v[108:111],a[12:15],v[208:211],v[108:111]\n"
"s_lshl_b32 s48,s48,7\n"
MF"v[112:115],a[0:3],v[216:219],0\n"
BL"v234,s[12:15],s48 offen offset:0 lds\n"
MF"v[112:115],a[16:19],v[220:223],v[112:115]\n"
RD"v[204:207],v243 offset:18432\n"
MF"v[116:119],a[20:23],v[220:223],0\n"
MF"v[116:119],a[4:7],v[216:219],v[116:119]\n"
MF"v[120:123],a[8:11],v[216:219],0\n"
BL"v235,s[12:15],s48 offen offset:1024 lds\n"
MF"v[120:123],a[24:27],v[220:223],v[120:123]\n"
RD"v[208:211],v242 offset:20480\n"
MF"v[124:127],a[28:31],v[220:223],0\n"
MF"v[124:127],a[12:15],v[216:219],v[124:127]\n"
WT"lgkmcnt(2)\n"
MF"v[0:3],a[32:35],v[192:195],v[0:3]\n"
RD"v[212:215],v243 offset:20480\n"
MF"v[0:3],a[48:51],v[196:199],v[0:3]\n"
BL"v236,s[12:15],s48 offen offset:2048 lds\n"
MF"v[4:7],a[52:55],v[196:199],v[4:7]\n"
MF"v[4:7],a[36:39],v[192:195],v[4:7]\n"
MF"v[8:11],a[40:43],v[192:195],v[8:11]\n"
RD"v[216:219],v242 offset:22528\n"
MF"v[8:11],a[56:59],v[196:199],v[8:11]\n"
BL"v237,s[12:15],s48 offen offset:3072 lds\n"
MF"v[12:15],a[60:63],v[196:199],v[12:15]\n"
MF"v[12:15],a[44:47],v[192:195],v[12:15]\n"
RD"v[220:223],v243 offset:22528\n"
MF"v[16:19],a[32:35],v[200:203],v[16:19]\n"
WT"lgkmcnt(2)\n"
WT"vmcnt(9)\n"
RT"a[64:65],v246 offset:16384\n"
MF"v[16:19],a[48:51],v[204:207],v[16:19]\n"
SA"m0,s37,0\n"
"s_bfe_u32 s48,s46,0x3000c\n"
"s_lshl_b32 s48,s48,19\n"
MF"v[20:23],a[52:55],v[204:207],v[20:23]\n"
BL"v238,s[16:19],s48 offen offset:0 lds\n"
MF"v[20:23],a[36:39],v[200:203],v[20:23]\n"
RD"v[192:195],v242 offset:24576\n"
RT"a[66:67],v246 offset:16896\n"
MF"v[24:27],a[40:43],v[200:203],v[24:27]\n"
MF"v[24:27],a[56:59],v[204:207],v[24:27]\n"
MF"v[28:31],a[60:63],v[204:207],v[28:31]\n"
RT"a[68:69],v247 offset:16384\n"
MF"v[28:31],a[44:47],v[200:203],v[28:31]\n"
BL"v239,s[16:19],s48 offen offset:1024 lds\n"
MF"v[32:35],a[32:35],v[208:211],v[32:35]\n"
RD"v[196:199],v243 offset:24576\n"
MF"v[32:35],a[48:51],v[212:215],v[32:35]\n"
RT"a[70:71],v247 offset:16896\n"
MF"v[36:39],a[52:55],v[212:215],v[36:39]\n"
RD"v[200:203],v242 offset:26624\n"
MF"v[36:39],a[36:39],v[208:211],v[36:39]\n"
BL"v240,s[16:19],s48 offen offset:2048 lds\n"
MF"v[40:43],a[40:43],v[208:211],v[40:43]\n"
RT"a[72:73],v248 offset:16384\n"
MF"v[40:43],a[56:59],v[212:215],v[40:43]\n"
MF"v[44:47],a[60:63],v[212:215],v[44:47]\n"
RD"v[204:207],v243 offset:26624\n"
MF"v[44:47],a[44:47],v[208:211],v[44:47]\n"
WT"lgkmcnt(4)\n"
RT"a[74:75],v248 offset:16896\n"
MF"v[48:51],a[32:35],v[216:219],v[48:51]\n"
BL"v241,s[16:19],s48 offen offset:3072 lds\n"
MF"v[48:51],a[48:51],v[220:223],v[48:51]\n"
RD"v[208:211],v242 offset:28672\n"
MF"v[52:55],a[52:55],v[220:223],v[52:55]\n"
SA"m0,s37,4096\n"
RT"a[76:77],v249 offset:16384\n"
"s_bfe_u32 s48,s46,0x3000c\n"
MF"v[52:55],a[36:39],v[216:219],v[52:55]\n"
"s_lshl_b32 s48,s48,19\n"
"s_bitset1_b32 s48,18\n"
MF"v[56:59],a[40:43],v[216:219],v[56:59]\n"
BL"v238,s[16:19],s48 offen offset:0 lds\n"
MF"v[56:59],a[56:59],v[220:223],v[56:59]\n"
RD"v[212:215],v243 offset:28672\n"
RT"a[78:79],v249 offset:16896\n"
MF"v[60:63],a[60:63],v[220:223],v[60:63]\n"
MF"v[60:63],a[44:47],v[216:219],v[60:63]\n"
MF"v[64:67],a[32:35],v[192:195],v[64:67]\n"
RD"v[216:219],v242 offset:30720\n"
MF"v[64:67],a[48:51],v[196:199],v[64:67]\n"
WT"vmcnt(9)\n"
RT"a[80:81],v246 offset:20480\n"
MF"v[68:71],a[52:55],v[196:199],v[68:71]\n"
BL"v239,s[16:19],s48 offen offset:1024 lds\n"
MF"v[68:71],a[36:39],v[192:195],v[68:71]\n"
RT"a[82:83],v246 offset:20992\n"
MF"v[72:75],a[40:43],v[192:195],v[72:75]\n"
RD"v[220:223],v243 offset:30720\n"
MF"v[72:75],a[56:59],v[196:199],v[72:75]\n"
BL"v240,s[16:19],s48 offen offset:2048 lds\n"
MF"v[76:79],a[60:63],v[196:199],v[76:79]\n"
RT"a[84:85],v247 offset:20480\n"
MF"v[76:79],a[44:47],v[192:195],v[76:79]\n"
WT"lgkmcnt(4)\n"
MF"v[80:83],a[32:35],v[200:203],v[80:83]\n"
"s_lshl_b32 s51,s32,8\n"
"s_andn2_b32 s50,s31,7\n"
SA"s50,s50,s51\n"
MF"v[80:83],a[48:51],v[204:207],v[80:83]\n"
SA"s50,s50,256\n"
RT"a[86:87],v247 offset:20992\n"
SA"s50,s50,s45\n"
MF"v[84:87],a[52:55],v[204:207],v[84:87]\n"
BL"v241,s[16:19],s48 offen offset:3072 lds\n"
MF"v[84:87],a[36:39],v[200:203],v[84:87]\n"
MF"v[88:91],a[40:43],v[200:203],v[88:91]\n"
SA"m0,s36,0\n"
RT"a[88:89],v248 offset:20480\n"
"s_bfe_u32 s48,s46,0x3000c\n"
MF"v[88:91],a[56:59],v[204:207],v[88:91]\n"
"s_lshl_b32 s48,s48,7\n"
WT"vmcnt(8) lgkmcnt(3)\n"
"s_barrier\n"
MF"v[92:95],a[60:63],v[204:207],v[92:95]\n"
RD"v[192:195],v242 offset:32768\n"
MF"v[92:95],a[44:47],v[200:203],v[92:95]\n"
BL"v234,s[12:15],s48 offen offset:0 lds\n"
MF"v[96:99],a[32:35],v[208:211],v[96:99]\n"
"buffer_store_dwordx2 v[232:233],v230,s[8:11],s50 offen sc1\n"
MF"v[96:99],a[48:51],v[212:215],v[96:99]\n"
RT"a[90:91],v248 offset:20992\n"
MF"v[100:103],a[52:55],v[212:215],v[100:103]\n"
RD"v[196:199],v243 offset:32768\n"
RT"a[92:93],v249 offset:20480\n"
MF"v[100:103],a[36:39],v[208:211],v[100:103]\n"
BL"v235,s[12:15],s48 offen offset:1024 lds\n"
MF"v[104:107],a[40:43],v[208:211],v[104:107]\n"
MF"v[104:107],a[56:59],v[212:215],v[104:107]\n"
RT"a[94:95],v249 offset:20992\n"
MF"v[108:111],a[60:63],v[212:215],v[108:111]\n"
RD"v[200:203],v242 offset:34816\n"
MF"v[108:111],a[44:47],v[208:211],v[108:111]\n"
BL"v236,s[12:15],s48 offen offset:2048 lds\n"
MF"v[112:115],a[32:35],v[216:219],v[112:115]\n"
MF"v[112:115],a[48:51],v[220:223],v[112:115]\n"
RD"v[204:207],v243 offset:34816\n"
MF"v[116:119],a[52:55],v[220:223],v[116:119]\n"
MF"v[116:119],a[36:39],v[216:219],v[116:119]\n"
BL"v237,s[12:15],s48 offen offset:3072 lds\n"
MF"v[120:123],a[40:43],v[216:219],v[120:123]\n"
MF"v[120:123],a[56:59],v[220:223],v[120:123]\n"
RD"v[208:211],v242 offset:36864\n"
MF"v[124:127],a[60:63],v[220:223],v[124:127]\n"
SA"m0,s37,8192\n"
"s_bfe_u32 s48,s46,0x30010\n"
"s_lshl_b32 s48,s48,19\n"
MF"v[124:127],a[44:47],v[216:219],v[124:127]\n"
WT"lgkmcnt(2)\n"
MF"v[0:3],a[64:67],v[192:195],v[0:3]\n"
RD"v[212:215],v243 offset:36864\n"
MF"v[0:3],a[80:83],v[196:199],v[0:3]\n"
BL"v238,s[16:19],s48 offen offset:0 lds\n"
MF"v[4:7],a[84:87],v[196:199],v[4:7]\n"
MF"v[4:7],a[68:71],v[192:195],v[4:7]\n"
MF"v[8:11],a[72:75],v[192:195],v[8:11]\n"
RD"v[216:219],v242 offset:38912\n"
MF"v[8:11],a[88:91],v[196:199],v[8:11]\n"
BL"v239,s[16:19],s48 offen offset:1024 lds\n"
MF"v[12:15],a[92:95],v[196:199],v[12:15]\n"
MF"v[12:15],a[76:79],v[192:195],v[12:15]\n"
RD"v[220:223],v243 offset:38912\n"
MF"v[16:19],a[64:67],v[200:203],v[16:19]\n"
WT"lgkmcnt(2)\n"
WT"vmcnt(11)\n"
RT"a[96:97],v246 offset:0\n"
MF"v[16:19],a[80:83],v[204:207],v[16:19]\n"
BL"v240,s[16:19],s48 offen offset:2048 lds\n"
MF"v[20:23],a[84:87],v[204:207],v[20:23]\n"
MF"v[20:23],a[68:71],v[200:203],v[20:23]\n"
RD"v[192:195],v242 offset:40960\n"
MF"v[24:27],a[72:75],v[200:203],v[24:27]\n"
RT"a[98:99],v246 offset:512\n"
MF"v[24:27],a[88:91],v[204:207],v[24:27]\n"
BL"v241,s[16:19],s48 offen offset:3072 lds\n"
MF"v[28:31],a[92:95],v[204:207],v[28:31]\n"
RT"a[100:101],v247 offset:0\n"
MF"v[28:31],a[76:79],v[200:203],v[28:31]\n"
RD"v[196:199],v243 offset:40960\n"
MF"v[32:35],a[64:67],v[208:211],v[32:35]\n"
SA"m0,s37,12288\n"
"s_bfe_u32 s48,s46,0x30010\n"
"s_lshl_b32 s48,s48,19\n"
MF"v[32:35],a[80:83],v[212:215],v[32:35]\n"
"s_bitset1_b32 s48,18\n"
RT"a[102:103],v247 offset:512\n"
MF"v[36:39],a[84:87],v[212:215],v[36:39]\n"
RD"v[200:203],v242 offset:43008\n"
MF"v[36:39],a[68:71],v[208:211],v[36:39]\n"
BL"v238,s[16:19],s48 offen offset:0 lds\n"
MF"v[40:43],a[72:75],v[208:211],v[40:43]\n"
RT"a[104:105],v248 offset:0\n"
MF"v[40:43],a[88:91],v[212:215],v[40:43]\n"
MF"v[44:47],a[92:95],v[212:215],v[44:47]\n"
RD"v[204:207],v243 offset:43008\n"
MF"v[44:47],a[76:79],v[208:211],v[44:47]\n"
WT"lgkmcnt(4)\n"
BL"v239,s[16:19],s48 offen offset:1024 lds\n"
MF"v[48:51],a[64:67],v[216:219],v[48:51]\n"
RT"a[106:107],v248 offset:512\n"
MF"v[48:51],a[80:83],v[220:223],v[48:51]\n"
RD"v[208:211],v242 offset:45056\n"
MF"v[52:55],a[84:87],v[220:223],v[52:55]\n"
RT"a[108:109],v249 offset:0\n"
MF"v[52:55],a[68:71],v[216:219],v[52:55]\n"
BL"v240,s[16:19],s48 offen offset:2048 lds\n"
MF"v[56:59],a[72:75],v[216:219],v[56:59]\n"
MF"v[56:59],a[88:91],v[220:223],v[56:59]\n"
RD"v[212:215],v243 offset:45056\n"
MF"v[60:63],a[92:95],v[220:223],v[60:63]\n"
RT"a[110:111],v249 offset:512\n"
MF"v[60:63],a[76:79],v[216:219],v[60:63]\n"
BL"v241,s[16:19],s48 offen offset:3072 lds\n"
MF"v[64:67],a[64:67],v[192:195],v[64:67]\n"
RD"v[216:219],v242 offset:47104\n"
MF"v[64:67],a[80:83],v[196:199],v[64:67]\n"
WT"vmcnt(13)\n"
RT"a[112:113],v246 offset:4096\n"
MF"v[68:71],a[84:87],v[196:199],v[68:71]\n"
SA"m0,s36,16384\n"
"s_bfe_u32 s48,s46,0x30010\n"
"s_lshl_b32 s48,s48,7\n"
MF"v[68:71],a[68:71],v[192:195],v[68:71]\n"
RT"a[114:115],v246 offset:4608\n"
MF"v[72:75],a[72:75],v[192:195],v[72:75]\n"
RD"v[220:223],v243 offset:47104\n"
MF"v[72:75],a[88:91],v[196:199],v[72:75]\n"
BL"v234,s[12:15],s48 offen offset:0 lds\n"
MF"v[76:79],a[92:95],v[196:199],v[76:79]\n"
RT"a[116:117],v247 offset:4096\n"
MF"v[76:79],a[76:79],v[192:195],v[76:79]\n"
WT"lgkmcnt(4)\n"
MF"v[80:83],a[64:67],v[200:203],v[80:83]\n"
BL"v235,s[12:15],s48 offen offset:1024 lds\n"
MF"v[80:83],a[80:83],v[204:207],v[80:83]\n"
RT"a[118:119],v247 offset:4608\n"
MF"v[84:87],a[84:87],v[204:207],v[84:87]\n"
MF"v[84:87],a[68:71],v[200:203],v[84:87]\n"
MF"v[88:91],a[72:75],v[200:203],v[88:91]\n"
RT"a[120:121],v248 offset:4096\n"
MF"v[88:91],a[88:91],v[204:207],v[88:91]\n"
WT"vmcnt(10) lgkmcnt(3)\n"
"s_barrier\n"
MF"v[92:95],a[92:95],v[204:207],v[92:95]\n"
BL"v236,s[12:15],s48 offen offset:2048 lds\n"
MF"v[92:95],a[76:79],v[200:203],v[92:95]\n"
RD"v[192:195],v242 offset:0\n"
RT"a[122:123],v248 offset:4608\n"
MF"v[96:99],a[64:67],v[208:211],v[96:99]\n"
MF"v[96:99],a[80:83],v[212:215],v[96:99]\n"
BL"v237,s[12:15],s48 offen offset:3072 lds\n"
MF"v[100:103],a[84:87],v[212:215],v[100:103]\n"
RD"v[196:199],v243 offset:0\n"
RT"a[124:125],v249 offset:4096\n"
MF"v[100:103],a[68:71],v[208:211],v[100:103]\n"
MF"v[104:107],a[72:75],v[208:211],v[104:107]\n"
SA"m0,s37,16384\n"
"s_bfe_u32 s48,s46,0x30014\n"
"s_lshl_b32 s48,s48,19\n"
MF"v[104:107],a[88:91],v[212:215],v[104:107]\n"
RT"a[126:127],v249 offset:4608\n"
MF"v[108:111],a[92:95],v[212:215],v[108:111]\n"
RD"v[200:203],v242 offset:2048\n"
MF"v[108:111],a[76:79],v[208:211],v[108:111]\n"
BL"v238,s[16:19],s48 offen offset:0 lds\n"
MF"v[112:115],a[64:67],v[216:219],v[112:115]\n"
MF"v[112:115],a[80:83],v[220:223],v[112:115]\n"
RD"v[204:207],v243 offset:2048\n"
MF"v[116:119],a[84:87],v[220:223],v[116:119]\n"
BL"v239,s[16:19],s48 offen offset:1024 lds\n"
MF"v[116:119],a[68:71],v[216:219],v[116:119]\n"
MF"v[120:123],a[72:75],v[216:219],v[120:123]\n"
MF"v[120:123],a[88:91],v[220:223],v[120:123]\n"
RD"v[208:211],v242 offset:4096\n"
MF"v[124:127],a[92:95],v[220:223],v[124:127]\n"
MF"v[124:127],a[76:79],v[216:219],v[124:127]\n"
WT"lgkmcnt(2)\n"
BL"v240,s[16:19],s48 offen offset:2048 lds\n"
MF"v[0:3],a[96:99],v[192:195],v[0:3]\n"
RD"v[212:215],v243 offset:4096\n"
MF"v[0:3],a[112:115],v[196:199],v[0:3]\n"
MF"v[4:7],a[116:119],v[196:199],v[4:7]\n"
MF"v[4:7],a[100:103],v[192:195],v[4:7]\n"
BL"v241,s[16:19],s48 offen offset:3072 lds\n"
MF"v[8:11],a[104:107],v[192:195],v[8:11]\n"
RD"v[216:219],v242 offset:6144\n"
MF"v[8:11],a[120:123],v[196:199],v[8:11]\n"
MF"v[12:15],a[124:127],v[196:199],v[12:15]\n"
SA"m0,s37,20480\n"
"s_bfe_u32 s48,s46,0x30014\n"
"s_lshl_b32 s48,s48,19\n"
MF"v[12:15],a[108:111],v[192:195],v[12:15]\n"
"s_bitset1_b32 s48,18\n"
RD"v[220:223],v243 offset:6144\n"
MF"v[16:19],a[96:99],v[200:203],v[16:19]\n"
WT"lgkmcnt(2)\n"
WT"vmcnt(12)\n"
RT"a[128:129],v246 offset:8192\n"
MF"v[16:19],a[112:115],v[204:207],v[16:19]\n"
BL"v238,s[16:19],s48 offen offset:0 lds\n"
MF"v[20:23],a[116:119],v[204:207],v[20:23]\n"
MF"v[20:23],a[100:103],v[200:203],v[20:23]\n"
RD"v[192:195],v242 offset:8192\n"
MF"v[24:27],a[104:107],v[200:203],v[24:27]\n"
RT"a[130:131],v246 offset:8704\n"
MF"v[24:27],a[120:123],v[204:207],v[24:27]\n"
BL"v239,s[16:19],s48 offen offset:1024 lds\n"
MF"v[28:31],a[124:127],v[204:207],v[28:31]\n"
RT"a[132:133],v247 offset:8192\n"
MF"v[28:31],a[108:111],v[200:203],v[28:31]\n"
RD"v[196:199],v243 offset:8192\n"
MF"v[32:35],a[96:99],v[208:211],v[32:35]\n"
MF"v[32:35],a[112:115],v[212:215],v[32:35]\n"
RT"a[134:135],v247 offset:8704\n"
MF"v[36:39],a[116:119],v[212:215],v[36:39]\n"
BL"v240,s[16:19],s48 offen offset:2048 lds\n"
MF"v[36:39],a[100:103],v[208:211],v[36:39]\n"
RD"v[200:203],v242 offset:10240\n"
MF"v[40:43],a[104:107],v[208:211],v[40:43]\n"
RT"a[136:137],v248 offset:8192\n"
MF"v[40:43],a[120:123],v[212:215],v[40:43]\n"
BL"v241,s[16:19],s48 offen offset:3072 lds\n"
MF"v[44:47],a[124:127],v[212:215],v[44:47]\n"
RD"v[204:207],v243 offset:10240\n"
MF"v[44:47],a[108:111],v[208:211],v[44:47]\n"
WT"lgkmcnt(4)\n"
RT"a[138:139],v248 offset:8704\n"
MF"v[48:51],a[96:99],v[216:219],v[48:51]\n"
SA"m0,s36,32768\n"
"s_bfe_u32 s48,s46,0x30014\n"
"s_lshl_b32 s48,s48,7\n"
MF"v[48:51],a[112:115],v[220:223],v[48:51]\n"
RD"v[208:211],v242 offset:12288\n"
MF"v[52:55],a[116:119],v[220:223],v[52:55]\n"
RT"a[140:141],v249 offset:8192\n"
MF"v[52:55],a[100:103],v[216:219],v[52:55]\n"
BL"v234,s[12:15],s48 offen offset:0 lds\n"
MF"v[56:59],a[104:107],v[216:219],v[56:59]\n"
MF"v[56:59],a[120:123],v[220:223],v[56:59]\n"
RD"v[212:215],v243 offset:12288\n"
MF"v[60:63],a[124:127],v[220:223],v[60:63]\n"
RT"a[142:143],v249 offset:8704\n"
MF"v[60:63],a[108:111],v[216:219],v[60:63]\n"
BL"v235,s[12:15],s48 offen offset:1024 lds\n"
MF"v[64:67],a[96:99],v[192:195],v[64:67]\n"
RD"v[216:219],v242 offset:14336\n"
MF"v[64:67],a[112:115],v[196:199],v[64:67]\n"
WT"vmcnt(14)\n"
RT"a[144:145],v246 offset:12288\n"
MF"v[68:71],a[116:119],v[196:199],v[68:71]\n"
MF"v[68:71],a[100:103],v[192:195],v[68:71]\n"
RT"a[146:147],v246 offset:12800\n"
MF"v[72:75],a[104:107],v[192:195],v[72:75]\n"
BL"v236,s[12:15],s48 offen offset:2048 lds\n"
MF"v[72:75],a[120:123],v[196:199],v[72:75]\n"
RD"v[220:223],v243 offset:14336\n"
MF"v[76:79],a[124:127],v[196:199],v[76:79]\n"
RT"a[148:149],v247 offset:12288\n"
MF"v[76:79],a[108:111],v[192:195],v[76:79]\n"
WT"lgkmcnt(4)\n"
BL"v237,s[12:15],s48 offen offset:3072 lds\n"
MF"v[80:83],a[96:99],v[200:203],v[80:83]\n"
MF"v[80:83],a[112:115],v[204:207],v[80:83]\n"
RT"a[150:151],v247 offset:12800\n"
MF"v[84:87],a[116:119],v[204:207],v[84:87]\n"
SA"m0,s37,0\n"
"s_bfe_u32 s48,s46,0x30018\n"
"s_lshl_b32 s48,s48,19\n"
MF"v[84:87],a[100:103],v[200:203],v[84:87]\n"
MF"v[88:91],a[104:107],v[200:203],v[88:91]\n"
RT"a[152:153],v248 offset:12288\n"
MF"v[88:91],a[120:123],v[204:207],v[88:91]\n"
BL"v238,s[16:19],s48 offen offset:0 lds\n"
MF"v[92:95],a[124:127],v[204:207],v[92:95]\n"
WT"vmcnt(13) lgkmcnt(3)\n"
"s_barrier\n"
MF"v[92:95],a[108:111],v[200:203],v[92:95]\n"
RD"v[192:195],v242 offset:16384\n"
RT"a[154:155],v248 offset:12800\n"
MF"v[96:99],a[96:99],v[208:211],v[96:99]\n"
BL"v239,s[16:19],s48 offen offset:1024 lds\n"
MF"v[96:99],a[112:115],v[212:215],v[96:99]\n"
MF"v[100:103],a[116:119],v[212:215],v[100:103]\n"
RD"v[196:199],v243 offset:16384\n"
MF"v[100:103],a[100:103],v[208:211],v[100:103]\n"
RT"a[156:157],v249 offset:12288\n"
MF"v[104:107],a[104:107],v[208:211],v[104:107]\n"
MF"v[104:107],a[120:123],v[212:215],v[104:107]\n"
RT"a[158:159],v249 offset:12800\n"
MF"v[108:111],a[124:127],v[212:215],v[108:111]\n"
BL"v240,s[16:19],s48 offen offset:2048 lds\n"
MF"v[108:111],a[108:111],v[208:211],v[108:111]\n"
RD"v[200:203],v242 offset:18432\n"
MF"v[112:115],a[96:99],v[216:219],v[112:115]\n"
MF"v[112:115],a[112:115],v[220:223],v[112:115]\n"
RD"v[204:207],v243 offset:18432\n"
MF"v[116:119],a[116:119],v[220:223],v[116:119]\n"
BL"v241,s[16:19],s48 offen offset:3072 lds\n"
MF"v[116:119],a[100:103],v[216:219],v[116:119]\n"
MF"v[120:123],a[104:107],v[216:219],v[120:123]\n"
SA"m0,s37,4096\n"
"s_bfe_u32 s48,s46,0x30018\n"
"s_lshl_b32 s48,s48,19\n"
MF"v[120:123],a[120:123],v[220:223],v[120:123]\n"
"s_bitset1_b32 s48,18\n"
RD"v[208:211],v242 offset:20480\n"
MF"v[124:127],a[124:127],v[220:223],v[124:127]\n"
BL"v238,s[16:19],s48 offen offset:0 lds\n"
MF"v[124:127],a[108:111],v[216:219],v[124:127]\n"
WT"lgkmcnt(2)\n"
MF"v[0:3],a[128:131],v[192:195],v[0:3]\n"
RD"v[212:215],v243 offset:20480\n"
MF"v[0:3],a[144:147],v[196:199],v[0:3]\n"
MF"v[4:7],a[148:151],v[196:199],v[4:7]\n"
BL"v239,s[16:19],s48 offen offset:1024 lds\n"
MF"v[4:7],a[132:135],v[192:195],v[4:7]\n"
MF"v[8:11],a[136:139],v[192:195],v[8:11]\n"
RD"v[216:219],v242 offset:22528\n"
MF"v[8:11],a[152:155],v[196:199],v[8:11]\n"
MF"v[12:15],a[156:159],v[196:199],v[12:15]\n"
MF"v[12:15],a[140:143],v[192:195],v[12:15]\n"
RD"v[220:223],v243 offset:22528\n"
MF"v[16:19],a[128:131],v[200:203],v[16:19]\n"
WT"lgkmcnt(2)\n"
BL"v240,s[16:19],s48 offen offset:2048 lds\n"
MF"v[16:19],a[144:147],v[204:207],v[16:19]\n"
WT"vmcnt(15)\n"
RT"a[160:161],v246 offset:16384\n"
MF"v[20:23],a[148:151],v[204:207],v[20:23]\n"
MF"v[20:23],a[132:135],v[200:203],v[20:23]\n"
RD"v[192:195],v242 offset:24576\n"
MF"v[24:27],a[136:139],v[200:203],v[24:27]\n"
RT"a[162:163],v246 offset:16896\n"
MF"v[24:27],a[152:155],v[204:207],v[24:27]\n"
BL"v241,s[16:19],s48 offen offset:3072 lds\n"
MF"v[28:31],a[156:159],v[204:207],v[28:31]\n"
SA"m0,s36,0\n"
RT"a[164:165],v247 offset:16384\n"
"s_bfe_u32 s48,s46,0x30018\n"
MF"v[28:31],a[140:143],v[200:203],v[28:31]\n"
"s_lshl_b32 s48,s48,7\n"
RD"v[196:199],v243 offset:24576\n"
MF"v[32:35],a[128:131],v[208:211],v[32:35]\n"
BL"v234,s[12:15],s48 offen offset:0 lds\n"
MF"v[32:35],a[144:147],v[212:215],v[32:35]\n"
RT"a[166:167],v247 offset:16896\n"
MF"v[36:39],a[148:151],v[212:215],v[36:39]\n"
RD"v[200:203],v242 offset:26624\n"
MF"v[36:39],a[132:135],v[208:211],v[36:39]\n"
MF"v[40:43],a[136:139],v[208:211],v[40:43]\n"
RT"a[168:169],v248 offset:16384\n"
MF"v[40:43],a[152:155],v[212:215],v[40:43]\n"
BL"v235,s[12:15],s48 offen offset:1024 lds\n"
MF"v[44:47],a[156:159],v[212:215],v[44:47]\n"
RD"v[204:207],v243 offset:26624\n"
MF"v[44:47],a[140:143],v[208:211],v[44:47]\n"
WT"lgkmcnt(4)\n"
RT"a[170:171],v248 offset:16896\n"
MF"v[48:51],a[128:131],v[216:219],v[48:51]\n"
MF"v[48:51],a[144:147],v[220:223],v[48:51]\n"
RD"v[208:211],v242 offset:28672\n"
MF"v[52:55],a[148:151],v[220:223],v[52:55]\n"
BL"v236,s[12:15],s48 offen offset:2048 lds\n"
MF"v[52:55],a[132:135],v[216:219],v[52:55]\n"
RT"a[172:173],v249 offset:16384\n"
MF"v[56:59],a[136:139],v[216:219],v[56:59]\n"
MF"v[56:59],a[152:155],v[220:223],v[56:59]\n"
RD"v[212:215],v243 offset:28672\n"
MF"v[60:63],a[156:159],v[220:223],v[60:63]\n"
RT"a[174:175],v249 offset:16896\n"
MF"v[60:63],a[140:143],v[216:219],v[60:63]\n"
BL"v237,s[12:15],s48 offen offset:3072 lds\n"
MF"v[64:67],a[128:131],v[192:195],v[64:67]\n"
SA"m0,s37,8192\n"
RD"v[216:219],v242 offset:30720\n"
"s_bfe_u32 s48,s46,0x3001c\n"
MF"v[64:67],a[144:147],v[196:199],v[64:67]\n"
"s_lshl_b32 s48,s48,19\n"
WT"vmcnt(16)\n"
RT"a[176:177],v246 offset:20480\n"
MF"v[68:71],a[148:151],v[196:199],v[68:71]\n"
BL"v238,s[16:19],s48 offen offset:0 lds\n"
MF"v[68:71],a[132:135],v[192:195],v[68:71]\n"
RT"a[178:179],v246 offset:20992\n"
MF"v[72:75],a[136:139],v[192:195],v[72:75]\n"
RD"v[220:223],v243 offset:30720\n"
MF"v[72:75],a[152:155],v[196:199],v[72:75]\n"
MF"v[76:79],a[156:159],v[196:199],v[76:79]\n"
RT"a[180:181],v247 offset:20480\n"
MF"v[76:79],a[140:143],v[192:195],v[76:79]\n"
WT"lgkmcnt(4)\n"
BL"v239,s[16:19],s48 offen offset:1024 lds\n"
MF"v[80:83],a[128:131],v[200:203],v[80:83]\n"
MF"v[80:83],a[144:147],v[204:207],v[80:83]\n"
RT"a[182:183],v247 offset:20992\n"
MF"v[84:87],a[148:151],v[204:207],v[84:87]\n"
MF"v[84:87],a[132:135],v[200:203],v[84:87]\n"
BL"v240,s[16:19],s48 offen offset:2048 lds\n"
MF"v[88:91],a[136:139],v[200:203],v[88:91]\n"
RT"a[184:185],v248 offset:20480\n"
MF"v[88:91],a[152:155],v[204:207],v[88:91]\n"
WT"vmcnt(15) lgkmcnt(3)\n"
"s_barrier\n"
MF"v[92:95],a[156:159],v[204:207],v[92:95]\n"
RD"v[192:195],v242 offset:32768\n"
MF"v[92:95],a[140:143],v[200:203],v[92:95]\n"
RT"a[186:187],v248 offset:20992\n"
MF"v[96:99],a[128:131],v[208:211],v[96:99]\n"
BL"v241,s[16:19],s48 offen offset:3072 lds\n"
MF"v[96:99],a[144:147],v[212:215],v[96:99]\n"
MF"v[100:103],a[148:151],v[212:215],v[100:103]\n"
SA"m0,s37,12288\n"
RD"v[196:199],v243 offset:32768\n"
"s_bfe_u32 s48,s46,0x3001c\n"
MF"v[100:103],a[132:135],v[208:211],v[100:103]\n"
"s_lshl_b32 s48,s48,19\n"
RT"a[188:189],v249 offset:20480\n"
"s_bitset1_b32 s48,18\n"
MF"v[104:107],a[136:139],v[208:211],v[104:107]\n"
BL"v238,s[16:19],s48 offen offset:0 lds\n"
MF"v[104:107],a[152:155],v[212:215],v[104:107]\n"
RT"a[190:191],v249 offset:20992\n"
MF"v[108:111],a[156:159],v[212:215],v[108:111]\n"
RD"v[200:203],v242 offset:34816\n"
MF"v[108:111],a[140:143],v[208:211],v[108:111]\n"
MF"v[112:115],a[128:131],v[216:219],v[112:115]\n"
BL"v239,s[16:19],s48 offen offset:1024 lds\n"
MF"v[112:115],a[144:147],v[220:223],v[112:115]\n"
RD"v[204:207],v243 offset:34816\n"
MF"v[116:119],a[148:151],v[220:223],v[116:119]\n"
MF"v[116:119],a[132:135],v[216:219],v[116:119]\n"
MF"v[120:123],a[136:139],v[216:219],v[120:123]\n"
MF"v[120:123],a[152:155],v[220:223],v[120:123]\n"
RD"v[208:211],v242 offset:36864\n"
MF"v[124:127],a[156:159],v[220:223],v[124:127]\n"
BL"v240,s[16:19],s48 offen offset:2048 lds\n"
MF"v[124:127],a[140:143],v[216:219],v[124:127]\n"
WT"lgkmcnt(2)\n"
MF"v[0:3],a[160:163],v[192:195],v[0:3]\n"
RD"v[212:215],v243 offset:36864\n"
MF"v[0:3],a[176:179],v[196:199],v[0:3]\n"
BL"v241,s[16:19],s48 offen offset:3072 lds\n"
MF"v[4:7],a[180:183],v[196:199],v[4:7]\n"
MF"v[4:7],a[164:167],v[192:195],v[4:7]\n"
MF"v[8:11],a[168:171],v[192:195],v[8:11]\n"
SA"m0,s36,16384\n"
RD"v[216:219],v242 offset:38912\n"
"s_bfe_u32 s48,s46,0x3001c\n"
MF"v[8:11],a[184:187],v[196:199],v[8:11]\n"
"s_lshl_b32 s48,s48,7\n"
MF"v[12:15],a[188:191],v[196:199],v[12:15]\n"
BL"v234,s[12:15],s48 offen offset:0 lds\n"
MF"v[12:15],a[172:175],v[192:195],v[12:15]\n"
RD"v[220:223],v243 offset:38912\n"
MF"v[16:19],a[160:163],v[200:203],v[16:19]\n"
WT"lgkmcnt(2)\n"
WT"vmcnt(17)\n"
RT"a[192:193],v246 offset:0\n"
MF"v[16:19],a[176:179],v[204:207],v[16:19]\n"
MF"v[20:23],a[180:183],v[204:207],v[20:23]\n"
BL"v235,s[12:15],s48 offen offset:1024 lds\n"
MF"v[20:23],a[164:167],v[200:203],v[20:23]\n"
RD"v[192:195],v242 offset:40960\n"
RT"a[194:195],v246 offset:512\n"
MF"v[24:27],a[168:171],v[200:203],v[24:27]\n"
MF"v[24:27],a[184:187],v[204:207],v[24:27]\n"
MF"v[28:31],a[188:191],v[204:207],v[28:31]\n"
RT"a[196:197],v247 offset:0\n"
MF"v[28:31],a[172:175],v[200:203],v[28:31]\n"
RD"v[196:199],v243 offset:40960\n"
MF"v[32:35],a[160:163],v[208:211],v[32:35]\n"
BL"v236,s[12:15],s48 offen offset:2048 lds\n"
MF"v[32:35],a[176:179],v[212:215],v[32:35]\n"
RT"a[198:199],v247 offset:512\n"
MF"v[36:39],a[180:183],v[212:215],v[36:39]\n"
RD"v[200:203],v242 offset:43008\n"
MF"v[36:39],a[164:167],v[208:211],v[36:39]\n"
BL"v237,s[12:15],s48 offen offset:3072 lds\n"
MF"v[40:43],a[168:171],v[208:211],v[40:43]\n"
RT"a[200:201],v248 offset:0\n"
MF"v[40:43],a[184:187],v[212:215],v[40:43]\n"
MF"v[44:47],a[188:191],v[212:215],v[44:47]\n"
SA"m0,s37,16384\n"
RD"v[204:207],v243 offset:43008\n"
SA"s48,s39,0\n"
MF"v[44:47],a[172:175],v[208:211],v[44:47]\n"
WT"lgkmcnt(4)\n"
RT"a[202:203],v248 offset:512\n"
"s_lshr_b32 s50,s30,1\n"
MF"v[48:51],a[160:163],v[216:219],v[48:51]\n"
"s_and_b32 s51,s30,1\n"
BL"v234,s[12:15],s48 offen offset:0 lds\n"
MF"v[48:51],a[176:179],v[220:223],v[48:51]\n"
"s_mul_i32 s38,s50,24576\n"
RD"v[208:211],v242 offset:45056\n"
MF"v[52:55],a[180:183],v[220:223],v[52:55]\n"
"s_lshl_b32 s60,s50,18\n"
RT"a[204:205],v249 offset:0\n"
MF"v[52:55],a[164:167],v[216:219],v[52:55]\n"
"s_lshl_b32 s50,s51,12\n"
MF"v[56:59],a[168:171],v[216:219],v[56:59]\n"
SA"s38,s38,s50\n"
BL"v235,s[12:15],s48 offen offset:1024 lds\n"
MF"v[56:59],a[184:187],v[220:223],v[56:59]\n"
"s_lshl_b32 s50,s51,7\n"
RD"v[212:215],v243 offset:45056\n"
RT"a[206:207],v249 offset:512\n"
MF"v[60:63],a[188:191],v[220:223],v[60:63]\n"
SA"s60,s60,s50\n"
MF"v[60:63],a[172:175],v[216:219],v[60:63]\n"
MF"v[64:67],a[160:163],v[192:195],v[64:67]\n"
RD"v[216:219],v242 offset:47104\n"
MF"v[64:67],a[176:179],v[196:199],v[64:67]\n"
"s_sub_u32 s99,s34,5\n"
WT"vmcnt(18)\n"
RT"a[208:209],v246 offset:4096\n"
MF"v[68:71],a[180:183],v[196:199],v[68:71]\n"
"s_cmp_lt_u32 s34,5\n"
BL"v236,s[12:15],s48 offen offset:2048 lds\n"
MF"v[68:71],a[164:167],v[192:195],v[68:71]\n"
"s_cselect_b32 s99,s34,s99\n"
RT"a[210:211],v246 offset:4608\n"
"s_mul_i32 s99,s99,1\n"
MF"v[72:75],a[168:171],v[192:195],v[72:75]\n"
"s_lshl_b32 s61,s99,20\n"
RD"v[220:223],v243 offset:47104\n"
MF"v[72:75],a[184:187],v[196:199],v[72:75]\n"
BL"v237,s[12:15],s48 offen offset:3072 lds\n"
MF"v[76:79],a[188:191],v[196:199],v[76:79]\n"
RT"a[212:213],v247 offset:4096\n"
MF"v[76:79],a[172:175],v[192:195],v[76:79]\n"
WT"lgkmcnt(4)\n"
MF"v[80:83],a[160:163],v[200:203],v[80:83]\n"
SA"m0,s37,20480\n"
SA"s48,s98,0\n"
MF"v[80:83],a[176:179],v[204:207],v[80:83]\n"
RT"a[214:215],v247 offset:4608\n"
MF"v[84:87],a[180:183],v[204:207],v[84:87]\n"
BL"v234,s[12:15],s48 offen offset:0 lds\n"
MF"v[84:87],a[164:167],v[200:203],v[84:87]\n"
MF"v[88:91],a[168:171],v[200:203],v[88:91]\n"
RT"a[216:217],v248 offset:4096\n"
MF"v[88:91],a[184:187],v[204:207],v[88:91]\n"
WT"vmcnt(17) lgkmcnt(3)\n"
"s_barrier\n"
MF"v[92:95],a[188:191],v[204:207],v[92:95]\n"
RD"v[192:195],v242 offset:0\n"
MF"v[92:95],a[172:175],v[200:203],v[92:95]\n"
BL"v235,s[12:15],s48 offen offset:1024 lds\n"
MF"v[96:99],a[160:163],v[208:211],v[96:99]\n"
RT"a[218:219],v248 offset:4608\n"
MF"v[96:99],a[176:179],v[212:215],v[96:99]\n"
MF"v[100:103],a[180:183],v[212:215],v[100:103]\n"
RD"v[196:199],v243 offset:0\n"
MF"v[100:103],a[164:167],v[208:211],v[100:103]\n"
RT"a[220:221],v249 offset:4096\n"
MF"v[104:107],a[168:171],v[208:211],v[104:107]\n"
BL"v236,s[12:15],s48 offen offset:2048 lds\n"
MF"v[104:107],a[184:187],v[212:215],v[104:107]\n"
RT"a[222:223],v249 offset:4608\n"
MF"v[108:111],a[188:191],v[212:215],v[108:111]\n"
RD"v[200:203],v242 offset:2048\n"
MF"v[108:111],a[172:175],v[208:211],v[108:111]\n"
BL"v237,s[12:15],s48 offen offset:3072 lds\n"
MF"v[112:115],a[160:163],v[216:219],v[112:115]\n"
MF"v[112:115],a[176:179],v[220:223],v[112:115]\n"
RD"v[204:207],v243 offset:2048\n"
MF"v[116:119],a[180:183],v[220:223],v[116:119]\n"
SA"m0,s37,0\n"
SA"s48,s39,32768\n"
MF"v[116:119],a[164:167],v[216:219],v[116:119]\n"
MF"v[120:123],a[168:171],v[216:219],v[120:123]\n"
BL"v234,s[12:15],s48 offen offset:0 lds\n"
MF"v[120:123],a[184:187],v[220:223],v[120:123]\n"
RD"v[208:211],v242 offset:4096\n"
MF"v[124:127],a[188:191],v[220:223],v[124:127]\n"
MF"v[124:127],a[172:175],v[216:219],v[124:127]\n"
WT"lgkmcnt(2)\n"
MF"v[0:3],a[192:195],v[192:195],v[0:3]\n"
RD"v[212:215],v243 offset:4096\n"
MF"v[0:3],a[208:211],v[196:199],v[0:3]\n"
BL"v235,s[12:15],s48 offen offset:1024 lds\n"
MF"v[4:7],a[212:215],v[196:199],v[4:7]\n"
MF"v[4:7],a[196:199],v[192:195],v[4:7]\n"
MF"v[8:11],a[200:203],v[192:195],v[8:11]\n"
RD"v[216:219],v242 offset:6144\n"
MF"v[8:11],a[216:219],v[196:199],v[8:11]\n"
BL"v236,s[12:15],s48 offen offset:2048 lds\n"
MF"v[12:15],a[220:223],v[196:199],v[12:15]\n"
MF"v[12:15],a[204:207],v[192:195],v[12:15]\n"
RD"v[220:223],v243 offset:6144\n"
MF"v[16:19],a[192:195],v[200:203],v[16:19]\n"
WT"lgkmcnt(2)\n"
WT"vmcnt(19)\n"
RT"a[224:225],v246 offset:8192\n"
MF"v[16:19],a[208:211],v[204:207],v[16:19]\n"
BL"v237,s[12:15],s48 offen offset:3072 lds\n"
MF"v[20:23],a[212:215],v[204:207],v[20:23]\n"
MF"v[20:23],a[196:199],v[200:203],v[20:23]\n"
RD"v[192:195],v242 offset:8192\n"
MF"v[24:27],a[200:203],v[200:203],v[24:27]\n"
SA"m0,s37,4096\n"
RT"a[226:227],v246 offset:8704\n"
SA"s48,s98,32768\n"
MF"v[24:27],a[216:219],v[204:207],v[24:27]\n"
MF"v[28:31],a[220:223],v[204:207],v[28:31]\n"
RT"a[228:229],v247 offset:8192\n"
MF"v[28:31],a[204:207],v[200:203],v[28:31]\n"
BL"v234,s[12:15],s48 offen offset:0 lds\n"
MF"v[32:35],a[192:195],v[208:211],v[32:35]\n"
RD"v[196:199],v243 offset:8192\n"
MF"v[32:35],a[208:211],v[212:215],v[32:35]\n"
RT"a[230:231],v247 offset:8704\n"
MF"v[36:39],a[212:215],v[212:215],v[36:39]\n"
RD"v[200:203],v242 offset:10240\n"
MF"v[36:39],a[196:199],v[208:211],v[36:39]\n"
BL"v235,s[12:15],s48 offen offset:1024 lds\n"
MF"v[40:43],a[200:203],v[208:211],v[40:43]\n"
RT"a[232:233],v248 offset:8192\n"
MF"v[40:43],a[216:219],v[212:215],v[40:43]\n"
MF"v[44:47],a[220:223],v[212:215],v[44:47]\n"
RD"v[204:207],v243 offset:10240\n"
MF"v[44:47],a[204:207],v[208:211],v[44:47]\n"
WT"lgkmcnt(4)\n"
RT"a[234:235],v248 offset:8704\n"
MF"v[48:51],a[192:195],v[216:219],v[48:51]\n"
BL"v236,s[12:15],s48 offen offset:2048 lds\n"
MF"v[48:51],a[208:211],v[220:223],v[48:51]\n"
RD"v[208:211],v242 offset:12288\n"
MF"v[52:55],a[212:215],v[220:223],v[52:55]\n"
RT"a[236:237],v249 offset:8192\n"
MF"v[52:55],a[196:199],v[216:219],v[52:55]\n"
BL"v237,s[12:15],s48 offen offset:3072 lds\n"
MF"v[56:59],a[200:203],v[216:219],v[56:59]\n"
MF"v[56:59],a[216:219],v[220:223],v[56:59]\n"
RD"v[212:215],v243 offset:12288\n"
MF"v[60:63],a[220:223],v[220:223],v[60:63]\n"
RT"a[238:239],v249 offset:8704\n"
MF"v[60:63],a[204:207],v[216:219],v[60:63]\n"
MF"v[64:67],a[192:195],v[192:195],v[64:67]\n"
RD"v[216:219],v242 offset:14336\n"
MF"v[64:67],a[208:211],v[196:199],v[64:67]\n"
SA"m0,s37,8192\n"
WT"vmcnt(20)\n"
RT"a[240:241],v246 offset:12288\n"
MF"v[68:71],a[212:215],v[196:199],v[68:71]\n"
SA"s48,s39,65536\n"
MF"v[68:71],a[196:199],v[192:195],v[68:71]\n"
RT"a[242:243],v246 offset:12800\n"
MF"v[72:75],a[200:203],v[192:195],v[72:75]\n"
WT"lgkmcnt(3)\n"
BL"v234,s[12:15],s48 offen offset:0 lds\n"
MF"v[72:75],a[216:219],v[196:199],v[72:75]\n"
RD"v[220:223],v243 offset:14336\n"
MF"v[76:79],a[220:223],v[196:199],v[76:79]\n"
RT"a[244:245],v247 offset:12288\n"
MF"v[76:79],a[204:207],v[192:195],v[76:79]\n"
BL"v235,s[12:15],s48 offen offset:1024 lds\n"
MF"v[80:83],a[192:195],v[200:203],v[80:83]\n"
MF"v[80:83],a[208:211],v[204:207],v[80:83]\n"
RT"a[246:247],v247 offset:12800\n"
MF"v[84:87],a[212:215],v[204:207],v[84:87]\n"
MF"v[84:87],a[196:199],v[200:203],v[84:87]\n"
MF"v[88:91],a[200:203],v[200:203],v[88:91]\n"
RT"a[248:249],v248 offset:12288\n"
MF"v[88:91],a[216:219],v[204:207],v[88:91]\n"
BL"v236,s[12:15],s48 offen offset:2048 lds\n"
MF"v[92:95],a[220:223],v[204:207],v[92:95]\n"
WT"vmcnt(19) lgkmcnt(3)\n"
"s_barrier\n"
MF"v[92:95],a[204:207],v[200:203],v[92:95]\n"
RD"v[192:195],v242 offset:16384\n"
RT"a[250:251],v248 offset:12800\n"
MF"v[96:99],a[192:195],v[208:211],v[96:99]\n"
BL"v237,s[12:15],s48 offen offset:3072 lds\n"
MF"v[96:99],a[208:211],v[212:215],v[96:99]\n"
MF"v[100:103],a[212:215],v[212:215],v[100:103]\n"
RD"v[196:199],v243 offset:16384\n"
MF"v[100:103],a[196:199],v[208:211],v[100:103]\n"
RT"a[252:253],v249 offset:12288\n"
MF"v[104:107],a[200:203],v[208:211],v[104:107]\n"
MF"v[104:107],a[216:219],v[212:215],v[104:107]\n"
RT"a[254:255],v249 offset:12800\n"
MF"v[108:111],a[220:223],v[212:215],v[108:111]\n"
RD"v[200:203],v242 offset:18432\n"
MF"v[108:111],a[204:207],v[208:211],v[108:111]\n"
MF"v[112:115],a[192:195],v[216:219],v[112:115]\n"
"s_lshl_b32 s50,s35,20\n"
MF"v[112:115],a[208:211],v[220:223],v[112:115]\n"
"s_lshl_b32 s51,s34,15\n"
RD"v[204:207],v243 offset:18432\n"
SA"m0,s37,12288\n"
MF"v[116:119],a[212:215],v[220:223],v[116:119]\n"
SA"s48,s98,65536\n"
SA"s50,s50,s51\n"
MF"v[116:119],a[196:199],v[216:219],v[116:119]\n"
WT"lgkmcnt(2)\n"
BL"v234,s[12:15],s48 offen offset:0 lds\n"
MF"v[120:123],a[200:203],v[216:219],v[120:123]\n"
"s_and_b32 s51,s30,1\n"
"s_lshl_b32 s51,s51,11\n"
MF"v[120:123],a[216:219],v[220:223],v[120:123]\n"
SA"s50,s50,s51\n"
RD"v[208:211],v242 offset:20480\n"
MF"v[124:127],a[220:223],v[220:223],v[124:127]\n"
SA"s50,s50,9175040\n"
MF"v[124:127],a[204:207],v[216:219],v[124:127]\n"
BL"v235,s[12:15],s48 offen offset:1024 lds\n"
MF"v[0:3],a[224:227],v[192:195],v[0:3]\n"
RD"v[212:215],v243 offset:20480\n"
"v_add_u32 v250,s50,v224\n"
MF"v[0:3],a[240:243],v[196:199],v[0:3]\n"
MF"v[4:7],a[244:247],v[196:199],v[4:7]\n"
MF"v[4:7],a[228:231],v[192:195],v[4:7]\n"
MF"v[8:11],a[232:235],v[192:195],v[8:11]\n"
RD"v[216:219],v242 offset:22528\n"
MF"v[8:11],a[248:251],v[196:199],v[8:11]\n"
BL"v236,s[12:15],s48 offen offset:2048 lds\n"
MF"v[12:15],a[252:255],v[196:199],v[12:15]\n"
MF"v[12:15],a[236:239],v[192:195],v[12:15]\n"
WT"lgkmcnt(1)\n"
RD"v[220:223],v243 offset:22528\n"
MF"v[16:19],a[224:227],v[200:203],v[16:19]\n"
BL"v237,s[12:15],s48 offen offset:3072 lds\n"
MF"v[16:19],a[240:243],v[204:207],v[16:19]\n"
MF"v[20:23],a[244:247],v[204:207],v[20:23]\n"
MF"v[20:23],a[228:231],v[200:203],v[20:23]\n"
RD"v[192:195],v242 offset:24576\n"
MF"v[24:27],a[232:235],v[200:203],v[24:27]\n"
MF"v[24:27],a[248:251],v[204:207],v[24:27]\n"
MF"v[28:31],a[252:255],v[204:207],v[28:31]\n"
MF"v[28:31],a[236:239],v[200:203],v[28:31]\n"
RD"v[196:199],v243 offset:24576\n"
MF"v[32:35],a[224:227],v[208:211],v[32:35]\n"
MF"v[32:35],a[240:243],v[212:215],v[32:35]\n"
MF"v[36:39],a[244:247],v[212:215],v[36:39]\n"
RD"v[200:203],v242 offset:26624\n"
MF"v[36:39],a[228:231],v[208:211],v[36:39]\n"
MF"v[40:43],a[232:235],v[208:211],v[40:43]\n"
MF"v[40:43],a[248:251],v[212:215],v[40:43]\n"
MF"v[44:47],a[252:255],v[212:215],v[44:47]\n"
RD"v[204:207],v243 offset:26624\n"
MF"v[44:47],a[236:239],v[208:211],v[44:47]\n"
WT"lgkmcnt(2)\n"
MF"v[48:51],a[224:227],v[216:219],v[48:51]\n"
MF"v[48:51],a[240:243],v[220:223],v[48:51]\n"
RD"v[208:211],v242 offset:28672\n"
MF"v[52:55],a[244:247],v[220:223],v[52:55]\n"
MF"v[52:55],a[228:231],v[216:219],v[52:55]\n"
MF"v[56:59],a[232:235],v[216:219],v[56:59]\n"
MF"v[56:59],a[248:251],v[220:223],v[56:59]\n"
RD"v[212:215],v243 offset:28672\n"
MF"v[60:63],a[252:255],v[220:223],v[60:63]\n"
MF"v[60:63],a[236:239],v[216:219],v[60:63]\n"
MF"v[64:67],a[224:227],v[192:195],v[64:67]\n"
RD"v[216:219],v242 offset:30720\n"
MF"v[64:67],a[240:243],v[196:199],v[64:67]\n"
MF"v[68:71],a[244:247],v[196:199],v[68:71]\n"
MF"v[68:71],a[228:231],v[192:195],v[68:71]\n"
MF"v[72:75],a[232:235],v[192:195],v[72:75]\n"
RD"v[220:223],v243 offset:30720\n"
MF"v[72:75],a[248:251],v[196:199],v[72:75]\n"
MF"v[76:79],a[252:255],v[196:199],v[76:79]\n"
MF"v[76:79],a[236:239],v[192:195],v[76:79]\n"
WT"lgkmcnt(1)\n"
MF"v[80:83],a[224:227],v[200:203],v[80:83]\n"
MF"v[80:83],a[240:243],v[204:207],v[80:83]\n"
MF"v[84:87],a[244:247],v[204:207],v[84:87]\n"
"v_add_u32 v244,114688,v242\n"
"v_add_u32 v245,114688,v243\n"
MF"v[84:87],a[228:231],v[200:203],v[84:87]\n"
"v_add_u32 v242,65536,v242\n"
"v_add_u32 v243,65536,v243\n"
MF"v[88:91],a[232:235],v[200:203],v[88:91]\n"
MF"v[88:91],a[248:251],v[204:207],v[88:91]\n"
WT"vmcnt(16) lgkmcnt(0)\n"
"s_barrier\n"
MF"v[92:95],a[252:255],v[204:207],v[92:95]\n"
RD"v[192:195],v242 offset:16384\n"
MF"v[92:95],a[236:239],v[200:203],v[92:95]\n"
MF"v[96:99],a[224:227],v[208:211],v[96:99]\n"
MF"v[96:99],a[240:243],v[212:215],v[96:99]\n"
MF"v[100:103],a[244:247],v[212:215],v[100:103]\n"
SA"m0,s36,49152\n"
RD"v[196:199],v243 offset:16384\n"
SA"s48,s60,s61\n"
MF"v[100:103],a[228:231],v[208:211],v[100:103]\n"
MF"v[104:107],a[232:235],v[208:211],v[104:107]\n"
BL"v238,s[20:23],s48 offen offset:0 lds\n"
MF"v[104:107],a[248:251],v[212:215],v[104:107]\n"
MF"v[108:111],a[252:255],v[212:215],v[108:111]\n"
RD"v[200:203],v242 offset:18432\n"
MF"v[108:111],a[236:239],v[208:211],v[108:111]\n"
MF"v[112:115],a[224:227],v[216:219],v[112:115]\n"
BL"v239,s[20:23],s48 offen offset:1024 lds\n"
MF"v[112:115],a[240:243],v[220:223],v[112:115]\n"
MF"v[116:119],a[244:247],v[220:223],v[116:119]\n"
RD"v[204:207],v243 offset:18432\n"
MF"v[116:119],a[228:231],v[216:219],v[116:119]\n"
MF"v[120:123],a[232:235],v[216:219],v[120:123]\n"
MF"v[120:123],a[248:251],v[220:223],v[120:123]\n"
BL"v240,s[20:23],s48 offen offset:2048 lds\n"
MF"v[124:127],a[252:255],v[220:223],v[124:127]\n"
SA"s61,s61,524288\n"
RD"v[208:211],v242 offset:20480\n"
MF"v[124:127],a[236:239],v[216:219],v[124:127]\n"
WT"lgkmcnt(2)\n"
MF"v[128:131],a[0:3],v[192:195],0\n"
MF"v[128:131],a[16:19],v[196:199],v[128:131]\n"
RD"v[212:215],v243 offset:20480\n"
MF"v[132:135],a[20:23],v[196:199],0\n"
BL"v241,s[20:23],s48 offen offset:3072 lds\n"
MF"v[132:135],a[4:7],v[192:195],v[132:135]\n"
MU"v0,s55,v0\n"
MU"v1,s55,v1\n"
MF"v[136:139],a[8:11],v[192:195],0\n"
MU"v2,s55,v2\n"
MU"v3,s55,v3\n"
MF"v[136:139],a[24:27],v[196:199],v[136:139]\n"
RD"v[216:219],v242 offset:22528\n"
MU"v4,s55,v4\n"
MF"v[140:143],a[28:31],v[196:199],0\n"
SA"m0,s36,32768\n"
MU"v5,s55,v5\n"
MU"v6,s55,v6\n"
MF"v[140:143],a[12:15],v[192:195],v[140:143]\n"
SA"s48,s60,s61\n"
MU"v7,s55,v7\n"
PK"v0,v0,v1\n"
MF"v[144:147],a[0:3],v[200:203],0\n"
WT"lgkmcnt(1)\n"
BL"v238,s[20:23],s48 offen offset:0 lds\n"
MF"v[144:147],a[16:19],v[204:207],v[144:147]\n"
RD"v[220:223],v243 offset:22528\n"
PK"v1,v2,v3\n"
MF"v[148:151],a[20:23],v[204:207],0\n"
PK"v2,v4,v5\n"
PK"v3,v6,v7\n"
MF"v[148:151],a[4:7],v[200:203],v[148:151]\n"
MF"v[152:155],a[8:11],v[200:203],0\n"
BL"v239,s[20:23],s48 offen offset:1024 lds\n"
MF"v[152:155],a[24:27],v[204:207],v[152:155]\n"
RD"v[192:195],v242 offset:40960\n"
MU"v8,s55,v8\n"
MF"v[156:159],a[28:31],v[204:207],0\n"
MU"v9,s55,v9\n"
MU"v10,s55,v10\n"
MF"v[156:159],a[12:15],v[200:203],v[156:159]\n"
MU"v11,s55,v11\n"
MU"v12,s55,v12\n"
MF"v[128:131],a[32:35],v[208:211],v[128:131]\n"
MU"v13,s55,v13\n"
MU"v14,s55,v14\n"
MF"v[128:131],a[48:51],v[212:215],v[128:131]\n"
RD"v[196:199],v243 offset:40960\n"
MU"v15,s55,v15\n"
MF"v[132:135],a[52:55],v[212:215],v[132:135]\n"
SA"s61,s61,524288\n"
BL"v240,s[20:23],s48 offen offset:2048 lds\n"
MF"v[132:135],a[36:39],v[208:211],v[132:135]\n"
"s_cmp_eq_u32 s61,5242880\n"
PK"v8,v8,v9\n"
PK"v9,v10,v11\n"
MF"v[136:139],a[40:43],v[208:211],v[136:139]\n"
"s_cselect_b32 s61,0,s61\n"
RD"v[200:203],v242 offset:43008\n"
PK"v10,v12,v13\n"
MF"v[136:139],a[56:59],v[212:215],v[136:139]\n"
BL"v241,s[20:23],s48 offen offset:3072 lds\n"
MF"v[140:143],a[60:63],v[212:215],v[140:143]\n"
PK"v11,v14,v15\n"
MU"v16,s55,v16\n"
MF"v[140:143],a[44:47],v[208:211],v[140:143]\n"
WT"lgkmcnt(2)\n"
MU"v17,s55,v17\n"
MU"v18,s55,v18\n"
MF"v[144:147],a[32:35],v[216:219],v[144:147]\n"
RD"v[204:207],v243 offset:43008\n"
MU"v19,s55,v19\n"
MF"v[144:147],a[48:51],v[220:223],v[144:147]\n"
MU"v20,s55,v20\n"
MU"v21,s55,v21\n"
MF"v[148:151],a[52:55],v[220:223],v[148:151]\n"
MU"v22,s55,v22\n"
MU"v23,s55,v23\n"
MF"v[148:151],a[36:39],v[216:219],v[148:151]\n"
PK"v16,v16,v17\n"
PK"v17,v18,v19\n"
MF"v[152:155],a[40:43],v[216:219],v[152:155]\n"
RD"v[208:211],v242 offset:45056\n"
PK"v18,v20,v21\n"
MF"v[152:155],a[56:59],v[220:223],v[152:155]\n"
"s_lshl_b32 s90,s31,2\n"
PK"v19,v22,v23\n"
"v_min_u32 v254,48,v224\n"
MF"v[156:159],a[60:63],v[220:223],v[156:159]\n"
SA"s90,s90,s30\n"
"v_lshlrev_b32 v254,3,v254\n"
MF"v[156:159],a[44:47],v[216:219],v[156:159]\n"
"s_mov_b32 s49,0\n"
"s_lshl_b32 s90,s90,9\n"
MF"v[128:131],a[64:67],v[192:195],v[128:131]\n"
WT"lgkmcnt(1)\n"
RD"v[212:215],v243 offset:45056\n"
MU"v24,s55,v24\n"
MF"v[128:131],a[80:83],v[196:199],v[128:131]\n"
BS"v[0:3],v250,s[8:11],s49 offen offset:0 sc1\n"
MF"v[132:135],a[84:87],v[196:199],v[132:135]\n"
"buffer_load_dword v255,v254,s[8:11],s90 offen sc1\n"
MF"v[132:135],a[68:71],v[192:195],v[132:135]\n"
RD"v[216:219],v242 offset:47104\n"
MU"v25,s55,v25\n"
MF"v[136:139],a[72:75],v[192:195],v[136:139]\n"
MU"v26,s55,v26\n"
MU"v27,s55,v27\n"
MF"v[136:139],a[88:91],v[196:199],v[136:139]\n"
MU"v28,s55,v28\n"
MU"v29,s55,v29\n"
MF"v[140:143],a[92:95],v[196:199],v[140:143]\n"
MU"v30,s55,v30\n"
MU"v31,s55,v31\n"
MF"v[140:143],a[76:79],v[192:195],v[140:143]\n"
RD"v[220:223],v243 offset:47104\n"
PK"v24,v24,v25\n"
MF"v[144:147],a[64:67],v[200:203],v[144:147]\n"
PK"v25,v26,v27\n"
PK"v26,v28,v29\n"
MF"v[144:147],a[80:83],v[204:207],v[144:147]\n"
PK"v27,v30,v31\n"
MF"v[148:151],a[84:87],v[204:207],v[148:151]\n"
MU"v32,s55,v32\n"
MU"v33,s55,v33\n"
MF"v[148:151],a[68:71],v[200:203],v[148:151]\n"
RD"v[192:195],v244 offset:16384\n"
MU"v34,s55,v34\n"
MF"v[152:155],a[72:75],v[200:203],v[152:155]\n"
MU"v35,s55,v35\n"
MU"v36,s55,v36\n"
MF"v[152:155],a[88:91],v[204:207],v[152:155]\n"
MU"v37,s55,v37\n"
MU"v38,s55,v38\n"
MF"v[156:159],a[92:95],v[204:207],v[156:159]\n"
MU"v39,s55,v39\n"
PK"v32,v32,v33\n"
MF"v[156:159],a[76:79],v[200:203],v[156:159]\n"
WT"lgkmcnt(1)\n"
RD"v[196:199],v245 offset:16384\n"
PK"v33,v34,v35\n"
MF"v[128:131],a[96:99],v[208:211],v[128:131]\n"
PK"v34,v36,v37\n"
PK"v35,v38,v39\n"
MF"v[128:131],a[112:115],v[212:215],v[128:131]\n"
MF"v[132:135],a[116:119],v[212:215],v[132:135]\n"
MF"v[132:135],a[100:103],v[208:211],v[132:135]\n"
RD"v[200:203],v244 offset:18432\n"
MF"v[136:139],a[104:107],v[208:211],v[136:139]\n"
MU"v40,s55,v40\n"
MU"v41,s55,v41\n"
MF"v[136:139],a[120:123],v[212:215],v[136:139]\n"
MU"v42,s55,v42\n"
MU"v43,s55,v43\n"
MF"v[140:143],a[124:127],v[212:215],v[140:143]\n"
RD"v[204:207],v245 offset:18432\n"
MU"v44,s55,v44\n"
MF"v[140:143],a[108:111],v[208:211],v[140:143]\n"
MU"v45,s55,v45\n"
MU"v46,s55,v46\n"
MF"v[144:147],a[96:99],v[216:219],v[144:147]\n"
MU"v47,s55,v47\n"
PK"v40,v40,v41\n"
MF"v[144:147],a[112:115],v[220:223],v[144:147]\n"
PK"v41,v42,v43\n"
PK"v42,v44,v45\n"
MF"v[148:151],a[116:119],v[220:223],v[148:151]\n"
RD"v[208:211],v244 offset:20480\n"
PK"v43,v46,v47\n"
MF"v[148:151],a[100:103],v[216:219],v[148:151]\n"
MF"v[152:155],a[104:107],v[216:219],v[152:155]\n"
MF"v[152:155],a[120:123],v[220:223],v[152:155]\n"
MF"v[156:159],a[124:127],v[220:223],v[156:159]\n"
RD"v[212:215],v245 offset:20480\n"
MU"v48,s55,v48\n"
MF"v[156:159],a[108:111],v[216:219],v[156:159]\n"
WT"lgkmcnt(2)\n"
MU"v49,s55,v49\n"
MU"v50,s55,v50\n"
MF"v[128:131],a[128:131],v[192:195],v[128:131]\n"
BS"v[8:11],v250,s[8:11],s49 offen offset:1024 sc1\n"
MF"v[128:131],a[144:147],v[196:199],v[128:131]\n"
MU"v51,s55,v51\n"
MU"v52,s55,v52\n"
MF"v[132:135],a[148:151],v[196:199],v[132:135]\n"
RD"v[216:219],v244 offset:22528\n"
MU"v53,s55,v53\n"
MF"v[132:135],a[132:135],v[192:195],v[132:135]\n"
MU"v54,s55,v54\n"
MU"v55,s55,v55\n"
MF"v[136:139],a[136:139],v[192:195],v[136:139]\n"
PK"v48,v48,v49\n"
PK"v49,v50,v51\n"
MF"v[136:139],a[152:155],v[196:199],v[136:139]\n"
RD"v[220:223],v245 offset:22528\n"
PK"v50,v52,v53\n"
MF"v[140:143],a[156:159],v[196:199],v[140:143]\n"
PK"v51,v54,v55\n"
MF"v[140:143],a[140:143],v[192:195],v[140:143]\n"
MF"v[144:147],a[128:131],v[200:203],v[144:147]\n"
MU"v56,s55,v56\n"
MU"v57,s55,v57\n"
MF"v[144:147],a[144:147],v[204:207],v[144:147]\n"
RD"v[192:195],v244 offset:40960\n"
MU"v58,s55,v58\n"
MF"v[148:151],a[148:151],v[204:207],v[148:151]\n"
MU"v59,s55,v59\n"
MU"v60,s55,v60\n"
MF"v[148:151],a[132:135],v[200:203],v[148:151]\n"
MU"v61,s55,v61\n"
MU"v62,s55,v62\n"
MF"v[152:155],a[136:139],v[200:203],v[152:155]\n"
MU"v63,s55,v63\n"
PK"v56,v56,v57\n"
MF"v[152:155],a[152:155],v[204:207],v[152:155]\n"
RD"v[196:199],v245 offset:40960\n"
PK"v57,v58,v59\n"
MF"v[156:159],a[156:159],v[204:207],v[156:159]\n"
PK"v58,v60,v61\n"
PK"v59,v62,v63\n"
MF"v[156:159],a[140:143],v[200:203],v[156:159]\n"
WT"lgkmcnt(2)\n"
MF"v[128:131],a[160:163],v[208:211],v[128:131]\n"
MF"v[128:131],a[176:179],v[212:215],v[128:131]\n"
RD"v[200:203],v244 offset:43008\n"
MF"v[132:135],a[180:183],v[212:215],v[132:135]\n"
MU"v64,s55,v64\n"
MU"v65,s55,v65\n"
MF"v[132:135],a[164:167],v[208:211],v[132:135]\n"
MU"v66,s55,v66\n"
MU"v67,s55,v67\n"
MF"v[136:139],a[168:171],v[208:211],v[136:139]\n"
MU"v68,s55,v68\n"
MU"v69,s55,v69\n"
MF"v[136:139],a[184:187],v[212:215],v[136:139]\n"
RD"v[204:207],v245 offset:43008\n"
MU"v70,s55,v70\n"
MF"v[140:143],a[188:191],v[212:215],v[140:143]\n"
MU"v71,s55,v71\n"
PK"v64,v64,v65\n"
MF"v[140:143],a[172:175],v[208:211],v[140:143]\n"
PK"v65,v66,v67\n"
PK"v66,v68,v69\n"
MF"v[144:147],a[160:163],v[216:219],v[144:147]\n"
RD"v[208:211],v244 offset:45056\n"
PK"v67,v70,v71\n"
MF"v[144:147],a[176:179],v[220:223],v[144:147]\n"
MF"v[148:151],a[180:183],v[220:223],v[148:151]\n"
MF"v[148:151],a[164:167],v[216:219],v[148:151]\n"
MF"v[152:155],a[168:171],v[216:219],v[152:155]\n"
RD"v[212:215],v245 offset:45056\n"
MU"v72,s55,v72\n"
MF"v[152:155],a[184:187],v[220:223],v[152:155]\n"
"s_mov_b32 s49,4096\n"
MU"v73,s55,v73\n"
MU"v74,s55,v74\n"
MF"v[156:159],a[188:191],v[220:223],v[156:159]\n"
MU"v75,s55,v75\n"
MU"v76,s55,v76\n"
MF"v[156:159],a[172:175],v[216:219],v[156:159]\n"
WT"lgkmcnt(1)\n"
BS"v[16:19],v250,s[8:11],s49 offen offset:0 sc1\n"
MF"v[128:131],a[192:195],v[192:195],v[128:131]\n"
RD"v[216:219],v244 offset:47104\n"
MU"v77,s55,v77\n"
MF"v[128:131],a[208:211],v[196:199],v[128:131]\n"
MU"v78,s55,v78\n"
MU"v79,s55,v79\n"
MF"v[132:135],a[212:215],v[196:199],v[132:135]\n"
PK"v72,v72,v73\n"
PK"v73,v74,v75\n"
MF"v[132:135],a[196:199],v[192:195],v[132:135]\n"
PK"v74,v76,v77\n"
PK"v75,v78,v79\n"
MF"v[136:139],a[200:203],v[192:195],v[136:139]\n"
RD"v[220:223],v245 offset:47104\n"
MF"v[136:139],a[216:219],v[196:199],v[136:139]\n"
MF"v[140:143],a[220:223],v[196:199],v[140:143]\n"
MU"v80,s55,v80\n"
MU"v81,s55,v81\n"
MF"v[140:143],a[204:207],v[192:195],v[140:143]\n"
MU"v82,s55,v82\n"
MU"v83,s55,v83\n"
MF"v[144:147],a[192:195],v[200:203],v[144:147]\n"
MU"v84,s55,v84\n"
MU"v85,s55,v85\n"
MF"v[144:147],a[208:211],v[204:207],v[144:147]\n"
MU"v86,s55,v86\n"
MU"v87,s55,v87\n"
MF"v[148:151],a[212:215],v[204:207],v[148:151]\n"
PK"v80,v80,v81\n"
PK"v81,v82,v83\n"
MF"v[148:151],a[196:199],v[200:203],v[148:151]\n"
PK"v82,v84,v85\n"
PK"v83,v86,v87\n"
MF"v[152:155],a[200:203],v[200:203],v[152:155]\n"
MF"v[152:155],a[216:219],v[204:207],v[152:155]\n"
WT"vmcnt(20) lgkmcnt(0)\n"
"s_barrier\n"
MF"v[156:159],a[220:223],v[204:207],v[156:159]\n"
RD"v[192:195],v242 offset:0\n"
MF"v[156:159],a[204:207],v[200:203],v[156:159]\n"
MF"v[128:131],a[224:227],v[208:211],v[128:131]\n"
MU"v88,s55,v88\n"
MU"v89,s55,v89\n"
MF"v[128:131],a[240:243],v[212:215],v[128:131]\n"
MU"v90,s55,v90\n"
MU"v91,s55,v91\n"
MF"v[132:135],a[244:247],v[212:215],v[132:135]\n"
RD"v[196:199],v243 offset:0\n"
MU"v92,s55,v92\n"
MF"v[132:135],a[228:231],v[208:211],v[132:135]\n"
MU"v93,s55,v93\n"
MU"v94,s55,v94\n"
MF"v[136:139],a[232:235],v[208:211],v[136:139]\n"
MU"v95,s55,v95\n"
PK"v88,v88,v89\n"
MF"v[136:139],a[248:251],v[212:215],v[136:139]\n"
PK"v89,v90,v91\n"
PK"v90,v92,v93\n"
MF"v[140:143],a[252:255],v[212:215],v[140:143]\n"
RD"v[200:203],v242 offset:2048\n"
PK"v91,v94,v95\n"
MF"v[140:143],a[236:239],v[208:211],v[140:143]\n"
MF"v[144:147],a[224:227],v[216:219],v[144:147]\n"
MF"v[144:147],a[240:243],v[220:223],v[144:147]\n"
MF"v[148:151],a[244:247],v[220:223],v[148:151]\n"
RD"v[204:207],v243 offset:2048\n"
MF"v[148:151],a[228:231],v[216:219],v[148:151]\n"
MF"v[152:155],a[232:235],v[216:219],v[152:155]\n"
MF"v[152:155],a[248:251],v[220:223],v[152:155]\n"
MF"v[156:159],a[252:255],v[220:223],v[156:159]\n"
RD"v[208:211],v242 offset:4096\n"
MF"v[156:159],a[236:239],v[216:219],v[156:159]\n"
WT"lgkmcnt(2)\n"
BS"v[24:27],v250,s[8:11],s49 offen offset:1024 sc1\n"
MF"v[36:39],a[0:3],v[192:195],0\n"
MF"v[36:39],a[16:19],v[196:199],v[36:39]\n"
RD"v[212:215],v243 offset:4096\n"
MF"v[68:71],a[20:23],v[196:199],0\n"
MF"v[68:71],a[4:7],v[192:195],v[68:71]\n"
MU"v128,s55,v128\n"
MU"v129,s55,v129\n"
MF"v[44:47],a[8:11],v[192:195],0\n"
MU"v130,s55,v130\n"
MU"v131,s55,v131\n"
MF"v[44:47],a[24:27],v[196:199],v[44:47]\n"
RD"v[216:219],v242 offset:6144\n"
MU"v132,s55,v132\n"
MF"v[76:79],a[28:31],v[196:199],0\n"
MU"v133,s55,v133\n"
MU"v134,s55,v134\n"
MF"v[76:79],a[12:15],v[192:195],v[76:79]\n"
MU"v135,s55,v135\n"
PK"v4,v128,v129\n"
MF"v[52:55],a[0:3],v[200:203],0\n"
WT"lgkmcnt(1)\n"
PK"v5,v130,v131\n"
PK"v6,v132,v133\n"
MF"v[52:55],a[16:19],v[204:207],v[52:55]\n"
RD"v[220:223],v243 offset:6144\n"
PK"v7,v134,v135\n"
MF"v[84:87],a[20:23],v[204:207],0\n"
MF"v[84:87],a[4:7],v[200:203],v[84:87]\n"
MF"v[60:63],a[8:11],v[200:203],0\n"
MF"v[60:63],a[24:27],v[204:207],v[60:63]\n"
RD"v[192:195],v242 offset:24576\n"
MU"v136,s55,v136\n"
MF"v[92:95],a[28:31],v[204:207],0\n"
MU"v137,s55,v137\n"
MU"v138,s55,v138\n"
MF"v[92:95],a[12:15],v[200:203],v[92:95]\n"
MU"v139,s55,v139\n"
MU"v140,s55,v140\n"
MF"v[36:39],a[32:35],v[208:211],v[36:39]\n"
MU"v141,s55,v141\n"
MU"v142,s55,v142\n"
MF"v[36:39],a[48:51],v[212:215],v[36:39]\n"
RD"v[196:199],v243 offset:24576\n"
MU"v143,s55,v143\n"
MF"v[68:71],a[52:55],v[212:215],v[68:71]\n"
PK"v12,v136,v137\n"
PK"v13,v138,v139\n"
MF"v[68:71],a[36:39],v[208:211],v[68:71]\n"
PK"v14,v140,v141\n"
PK"v15,v142,v143\n"
MF"v[44:47],a[40:43],v[208:211],v[44:47]\n"
RD"v[200:203],v242 offset:26624\n"
MF"v[44:47],a[56:59],v[212:215],v[44:47]\n"
MF"v[76:79],a[60:63],v[212:215],v[76:79]\n"
MF"v[76:79],a[44:47],v[208:211],v[76:79]\n"
WT"lgkmcnt(2)\n"
MU"v144,s55,v144\n"
MU"v145,s55,v145\n"
MF"v[52:55],a[32:35],v[216:219],v[52:55]\n"
RD"v[204:207],v243 offset:26624\n"
MU"v146,s55,v146\n"
MF"v[52:55],a[48:51],v[220:223],v[52:55]\n"
MU"v147,s55,v147\n"
MU"v148,s55,v148\n"
MF"v[84:87],a[52:55],v[220:223],v[84:87]\n"
MU"v149,s55,v149\n"
MU"v150,s55,v150\n"
MF"v[84:87],a[36:39],v[216:219],v[84:87]\n"
"s_mov_b32 s49,8192\n"
MU"v151,s55,v151\n"
PK"v20,v144,v145\n"
MF"v[60:63],a[40:43],v[216:219],v[60:63]\n"
RD"v[208:211],v242 offset:28672\n"
PK"v21,v146,v147\n"
MF"v[60:63],a[56:59],v[220:223],v[60:63]\n"
BS"v[32:35],v250,s[8:11],s49 offen offset:0 sc1\n"
MF"v[92:95],a[60:63],v[220:223],v[92:95]\n"
PK"v22,v148,v149\n"
PK"v23,v150,v151\n"
MF"v[92:95],a[44:47],v[216:219],v[92:95]\n"
MF"v[36:39],a[64:67],v[192:195],v[36:39]\n"
WT"lgkmcnt(1)\n"
RD"v[212:215],v243 offset:28672\n"
MF"v[36:39],a[80:83],v[196:199],v[36:39]\n"
MU"v152,s55,v152\n"
MU"v153,s55,v153\n"
MF"v[68:71],a[84:87],v[196:199],v[68:71]\n"
MU"v154,s55,v154\n"
MU"v155,s55,v155\n"
MF"v[68:71],a[68:71],v[192:195],v[68:71]\n"
RD"v[216:219],v242 offset:30720\n"
MU"v156,s55,v156\n"
MF"v[44:47],a[72:75],v[192:195],v[44:47]\n"
MU"v157,s55,v157\n"
MU"v158,s55,v158\n"
MF"v[44:47],a[88:91],v[196:199],v[44:47]\n"
MU"v159,s55,v159\n"
PK"v28,v152,v153\n"
MF"v[76:79],a[92:95],v[196:199],v[76:79]\n"
PK"v29,v154,v155\n"
PK"v30,v156,v157\n"
MF"v[76:79],a[76:79],v[192:195],v[76:79]\n"
RD"v[220:223],v243 offset:30720\n"
PK"v31,v158,v159\n"
MF"v[52:55],a[64:67],v[200:203],v[52:55]\n"
MF"v[52:55],a[80:83],v[204:207],v[52:55]\n"
MF"v[84:87],a[84:87],v[204:207],v[84:87]\n"
MF"v[84:87],a[68:71],v[200:203],v[84:87]\n"
RD"v[192:195],v244 offset:0\n"
MF"v[60:63],a[72:75],v[200:203],v[60:63]\n"
MF"v[60:63],a[88:91],v[204:207],v[60:63]\n"
MF"v[92:95],a[92:95],v[204:207],v[92:95]\n"
MF"v[92:95],a[76:79],v[200:203],v[92:95]\n"
WT"lgkmcnt(1)\n"
RD"v[196:199],v245 offset:0\n"
MF"v[36:39],a[96:99],v[208:211],v[36:39]\n"
MF"v[36:39],a[112:115],v[212:215],v[36:39]\n"
MF"v[68:71],a[116:119],v[212:215],v[68:71]\n"
MF"v[68:71],a[100:103],v[208:211],v[68:71]\n"
RD"v[200:203],v244 offset:2048\n"
MF"v[44:47],a[104:107],v[208:211],v[44:47]\n"
MF"v[44:47],a[120:123],v[212:215],v[44:47]\n"
MF"v[76:79],a[124:127],v[212:215],v[76:79]\n"
RD"v[204:207],v245 offset:2048\n"
MF"v[76:79],a[108:111],v[208:211],v[76:79]\n"
MF"v[52:55],a[96:99],v[216:219],v[52:55]\n"
MF"v[52:55],a[112:115],v[220:223],v[52:55]\n"
MF"v[84:87],a[116:119],v[220:223],v[84:87]\n"
SA"m0,s37,16384\n"
RD"v[208:211],v244 offset:4096\n"
SA"s48,s39,98304\n"
MF"v[84:87],a[100:103],v[216:219],v[84:87]\n"
MF"v[60:63],a[104:107],v[216:219],v[60:63]\n"
BS"v[40:43],v250,s[8:11],s49 offen offset:1024 sc1\n"
MF"v[60:63],a[120:123],v[220:223],v[60:63]\n"
BL"v234,s[12:15],s48 offen offset:0 lds\n"
MF"v[92:95],a[124:127],v[220:223],v[92:95]\n"
RD"v[212:215],v245 offset:4096\n"
MU"v96,s55,v96\n"
MF"v[92:95],a[108:111],v[216:219],v[92:95]\n"
WT"lgkmcnt(2)\n"
MU"v97,s55,v97\n"
MU"v98,s55,v98\n"
MF"v[36:39],a[128:131],v[192:195],v[36:39]\n"
MU"v99,s55,v99\n"
MU"v100,s55,v100\n"
MF"v[36:39],a[144:147],v[196:199],v[36:39]\n"
MU"v101,s55,v101\n"
MU"v102,s55,v102\n"
MF"v[68:71],a[148:151],v[196:199],v[68:71]\n"
RD"v[216:219],v244 offset:6144\n"
MU"v103,s55,v103\n"
MF"v[68:71],a[132:135],v[192:195],v[68:71]\n"
BL"v235,s[12:15],s48 offen offset:1024 lds\n"
MF"v[44:47],a[136:139],v[192:195],v[44:47]\n"
PK"v96,v96,v97\n"
PK"v97,v98,v99\n"
MF"v[44:47],a[152:155],v[196:199],v[44:47]\n"
RD"v[220:223],v245 offset:6144\n"
PK"v98,v100,v101\n"
MF"v[76:79],a[156:159],v[196:199],v[76:79]\n"
PK"v99,v102,v103\n"
MU"v104,s55,v104\n"
MF"v[76:79],a[140:143],v[192:195],v[76:79]\n"
MU"v105,s55,v105\n"
MU"v106,s55,v106\n"
MF"v[52:55],a[128:131],v[200:203],v[52:55]\n"
BL"v236,s[12:15],s48 offen offset:2048 lds\n"
MF"v[52:55],a[144:147],v[204:207],v[52:55]\n"
RD"v[192:195],v244 offset:24576\n"
MU"v107,s55,v107\n"
MF"v[84:87],a[148:151],v[204:207],v[84:87]\n"
MU"v108,s55,v108\n"
MU"v109,s55,v109\n"
MF"v[84:87],a[132:135],v[200:203],v[84:87]\n"
MU"v110,s55,v110\n"
MU"v111,s55,v111\n"
MF"v[60:63],a[136:139],v[200:203],v[60:63]\n"
PK"v104,v104,v105\n"
PK"v105,v106,v107\n"
MF"v[60:63],a[152:155],v[204:207],v[60:63]\n"
RD"v[196:199],v245 offset:24576\n"
PK"v106,v108,v109\n"
MF"v[92:95],a[156:159],v[204:207],v[92:95]\n"
BL"v237,s[12:15],s48 offen offset:3072 lds\n"
MF"v[92:95],a[140:143],v[200:203],v[92:95]\n"
WT"lgkmcnt(2)\n"
PK"v107,v110,v111\n"
MF"v[36:39],a[160:163],v[208:211],v[36:39]\n"
MU"v112,s55,v112\n"
MU"v113,s55,v113\n"
MF"v[36:39],a[176:179],v[212:215],v[36:39]\n"
RD"v[200:203],v244 offset:26624\n"
MU"v114,s55,v114\n"
MF"v[68:71],a[180:183],v[212:215],v[68:71]\n"
SA"m0,s37,20480\n"
MU"v115,s55,v115\n"
MU"v116,s55,v116\n"
MF"v[68:71],a[164:167],v[208:211],v[68:71]\n"
SA"s48,s98,98304\n"
MU"v117,s55,v117\n"
MU"v118,s55,v118\n"
MF"v[44:47],a[168:171],v[208:211],v[44:47]\n"
BL"v234,s[12:15],s48 offen offset:0 lds\n"
MF"v[44:47],a[184:187],v[212:215],v[44:47]\n"
RD"v[204:207],v245 offset:26624\n"
MU"v119,s55,v119\n"
MF"v[76:79],a[188:191],v[212:215],v[76:79]\n"
PK"v112,v112,v113\n"
PK"v113,v114,v115\n"
MF"v[76:79],a[172:175],v[208:211],v[76:79]\n"
PK"v114,v116,v117\n"
PK"v115,v118,v119\n"
MF"v[52:55],a[160:163],v[216:219],v[52:55]\n"
RD"v[208:211],v244 offset:28672\n"
MF"v[52:55],a[176:179],v[220:223],v[52:55]\n"
"s_mov_b32 s49,12288\n"
MF"v[84:87],a[180:183],v[220:223],v[84:87]\n"
BL"v235,s[12:15],s48 offen offset:1024 lds\n"
MF"v[84:87],a[164:167],v[216:219],v[84:87]\n"
BS"v[48:51],v250,s[8:11],s49 offen offset:0 sc1\n"
MF"v[60:63],a[168:171],v[216:219],v[60:63]\n"
RD"v[212:215],v245 offset:28672\n"
MU"v120,s55,v120\n"
MF"v[60:63],a[184:187],v[220:223],v[60:63]\n"
MU"v121,s55,v121\n"
MU"v122,s55,v122\n"
MF"v[92:95],a[188:191],v[220:223],v[92:95]\n"
MU"v123,s55,v123\n"
MU"v124,s55,v124\n"
MF"v[92:95],a[172:175],v[216:219],v[92:95]\n"
WT"lgkmcnt(1)\n"
MU"v125,s55,v125\n"
MU"v126,s55,v126\n"
MF"v[36:39],a[192:195],v[192:195],v[36:39]\n"
RD"v[216:219],v244 offset:30720\n"
MU"v127,s55,v127\n"
MF"v[36:39],a[208:211],v[196:199],v[36:39]\n"
BL"v236,s[12:15],s48 offen offset:2048 lds\n"
MF"v[68:71],a[212:215],v[196:199],v[68:71]\n"
PK"v120,v120,v121\n"
PK"v121,v122,v123\n"
MF"v[68:71],a[196:199],v[192:195],v[68:71]\n"
PK"v122,v124,v125\n"
PK"v123,v126,v127\n"
MF"v[44:47],a[200:203],v[192:195],v[44:47]\n"
RD"v[220:223],v245 offset:30720\n"
MF"v[44:47],a[216:219],v[196:199],v[44:47]\n"
MF"v[76:79],a[220:223],v[196:199],v[76:79]\n"
BL"v237,s[12:15],s48 offen offset:3072 lds\n"
MF"v[76:79],a[204:207],v[192:195],v[76:79]\n"
MF"v[52:55],a[192:195],v[200:203],v[52:55]\n"
MF"v[52:55],a[208:211],v[204:207],v[52:55]\n"
MF"v[84:87],a[212:215],v[204:207],v[84:87]\n"
MF"v[84:87],a[196:199],v[200:203],v[84:87]\n"
"v_lshrrev_b32 v251,2,v224\n"
MF"v[60:63],a[200:203],v[200:203],v[60:63]\n"
"v_and_b32 v251,28,v251\n"
MF"v[60:63],a[216:219],v[204:207],v[60:63]\n"
WT"vmcnt(24) lgkmcnt(0)\n"
"s_barrier\n"
MF"v[92:95],a[220:223],v[204:207],v[92:95]\n"
RD"v[192:195],v242 offset:8192\n"
"ds_read_b32 v234,v251 offset:16384\n"
MF"v[92:95],a[204:207],v[200:203],v[92:95]\n"
MF"v[36:39],a[224:227],v[208:211],v[36:39]\n"
MF"v[36:39],a[240:243],v[212:215],v[36:39]\n"
MF"v[68:71],a[244:247],v[212:215],v[68:71]\n"
RD"v[196:199],v243 offset:8192\n"
MF"v[68:71],a[228:231],v[208:211],v[68:71]\n"
MF"v[44:47],a[232:235],v[208:211],v[44:47]\n"
MF"v[44:47],a[248:251],v[212:215],v[44:47]\n"
MF"v[76:79],a[252:255],v[212:215],v[76:79]\n"
RD"v[200:203],v242 offset:10240\n"
MF"v[76:79],a[236:239],v[208:211],v[76:79]\n"
WT"lgkmcnt(2)\n"
"v_not_b32 v251,v234\n"
"v_bfe_u32 v252,v251,4,7\n"
MF"v[52:55],a[224:227],v[216:219],v[52:55]\n"
"v_mul_u32_u24 v252,20480,v252\n"
MF"v[52:55],a[240:243],v[220:223],v[52:55]\n"
"v_and_b32 v251,15,v251\n"
MF"v[84:87],a[244:247],v[220:223],v[84:87]\n"
RD"v[204:207],v243 offset:10240\n"
"v_lshl_add_u32 v251,v251,4,v252\n"
MF"v[84:87],a[228:231],v[216:219],v[84:87]\n"
BS"v[56:59],v250,s[8:11],s49 offen offset:1024 sc1\n"
MF"v[60:63],a[232:235],v[216:219],v[60:63]\n"
"v_and_b32 v252,0x380,v224\n"
"v_lshl_add_u32 v251,v252,1,v251\n"
MF"v[60:63],a[248:251],v[220:223],v[60:63]\n"
"v_add_u32 v252,v252,v224\n"
MF"v[92:95],a[252:255],v[220:223],v[92:95]\n"
"s_xor_b32 s92,s34,0\n"
RD"v[208:211],v242 offset:12288\n"
MF"v[92:95],a[236:239],v[216:219],v[92:95]\n"
"s_lshl_b32 s92,s92,2\n"
MF"v[128:131],a[0:3],v[192:195],0\n"
WT"lgkmcnt(2)\n"
SA"s92,s92,s30\n"
MF"v[128:131],a[16:19],v[196:199],v[128:131]\n"
"s_lshl_b32 s93,s92,11\n"
RD"v[212:215],v243 offset:12288\n"
MF"v[132:135],a[20:23],v[196:199],0\n"
"s_cmp_lt_u32 s92,10\n"
"s_cselect_b32 s93,s93,2130706432\n"
MF"v[132:135],a[4:7],v[192:195],v[132:135]\n"
SA"s92,s93,524288\n"
MU"v36,s55,v36\n"
MU"v37,s55,v37\n"
MF"v[136:139],a[8:11],v[192:195],0\n"
BL"v[100:103],v251,s[8:11],s92 offen sc1\n"
MF"v[136:139],a[24:27],v[196:199],v[136:139]\n"
"s_mul_i32 s92,s35,20480\n"
RD"v[216:219],v242 offset:14336\n"
MU"v38,s55,v38\n"
MF"v[140:143],a[28:31],v[196:199],0\n"
SA"s92,s92,s93\n"
MU"v39,s55,v39\n"
MU"v68,s55,v68\n"
MF"v[140:143],a[12:15],v[192:195],v[140:143]\n"
SA"s92,s92,3145728\n"
MU"v69,s55,v69\n"
MU"v70,s55,v70\n"
MF"v[144:147],a[0:3],v[200:203],0\n"
WT"lgkmcnt(1)\n"
MU"v71,s55,v71\n"
PK"v36,v36,v37\n"
MF"v[144:147],a[16:19],v[204:207],v[144:147]\n"
RD"v[220:223],v243 offset:14336\n"
PK"v37,v38,v39\n"
MF"v[148:151],a[20:23],v[204:207],0\n"
PK"v38,v68,v69\n"
PK"v39,v70,v71\n"
MF"v[148:151],a[4:7],v[200:203],v[148:151]\n"
MF"v[152:155],a[8:11],v[200:203],0\n"
MF"v[152:155],a[24:27],v[204:207],v[152:155]\n"
RD"v[192:195],v242 offset:32768\n"
MU"v44,s55,v44\n"
MF"v[156:159],a[28:31],v[204:207],0\n"
MU"v45,s55,v45\n"
MU"v46,s55,v46\n"
MF"v[156:159],a[12:15],v[200:203],v[156:159]\n"
MU"v47,s55,v47\n"
MU"v76,s55,v76\n"
MF"v[128:131],a[32:35],v[208:211],v[128:131]\n"
MU"v77,s55,v77\n"
MU"v78,s55,v78\n"
MF"v[128:131],a[48:51],v[212:215],v[128:131]\n"
RD"v[196:199],v243 offset:32768\n"
MU"v79,s55,v79\n"
MF"v[132:135],a[52:55],v[212:215],v[132:135]\n"
PK"v44,v44,v45\n"
PK"v45,v46,v47\n"
MF"v[132:135],a[36:39],v[208:211],v[132:135]\n"
PK"v46,v76,v77\n"
PK"v47,v78,v79\n"
MF"v[136:139],a[40:43],v[208:211],v[136:139]\n"
SA"m0,s36,0\n"
RD"v[200:203],v242 offset:34816\n"
SA"s48,s60,s61\n"
MF"v[136:139],a[56:59],v[212:215],v[136:139]\n"
MF"v[140:143],a[60:63],v[212:215],v[140:143]\n"
BL"v238,s[20:23],s48 offen offset:0 lds\n"
MF"v[140:143],a[44:47],v[208:211],v[140:143]\n"
WT"lgkmcnt(2)\n"
MU"v52,s55,v52\n"
MU"v53,s55,v53\n"
MF"v[144:147],a[32:35],v[216:219],v[144:147]\n"
"s_mov_b32 s49,16384\n"
RD"v[204:207],v243 offset:34816\n"
MU"v54,s55,v54\n"
MF"v[144:147],a[48:51],v[220:223],v[144:147]\n"
BS"v[64:67],v250,s[8:11],s49 offen offset:0 sc1\n"
MF"v[148:151],a[52:55],v[220:223],v[148:151]\n"
MU"v55,s55,v55\n"
MU"v84,s55,v84\n"
MF"v[148:151],a[36:39],v[216:219],v[148:151]\n"
MU"v85,s55,v85\n"
MU"v86,s55,v86\n"
MF"v[152:155],a[40:43],v[216:219],v[152:155]\n"
RD"v[208:211],v242 offset:36864\n"
MU"v87,s55,v87\n"
MF"v[152:155],a[56:59],v[220:223],v[152:155]\n"
PK"v52,v52,v53\n"
PK"v53,v54,v55\n"
MF"v[156:159],a[60:63],v[220:223],v[156:159]\n"
BL"v239,s[20:23],s48 offen offset:1024 lds\n"
MF"v[156:159],a[44:47],v[216:219],v[156:159]\n"
PK"v54,v84,v85\n"
PK"v55,v86,v87\n"
MF"v[128:131],a[64:67],v[192:195],v[128:131]\n"
WT"lgkmcnt(1)\n"
RD"v[212:215],v243 offset:36864\n"
MF"v[128:131],a[80:83],v[196:199],v[128:131]\n"
MU"v60,s55,v60\n"
MU"v61,s55,v61\n"
MF"v[132:135],a[84:87],v[196:199],v[132:135]\n"
MU"v62,s55,v62\n"
MU"v63,s55,v63\n"
MF"v[132:135],a[68:71],v[192:195],v[132:135]\n"
RD"v[216:219],v242 offset:38912\n"
MU"v92,s55,v92\n"
MF"v[136:139],a[72:75],v[192:195],v[136:139]\n"
MU"v93,s55,v93\n"
MU"v94,s55,v94\n"
MF"v[136:139],a[88:91],v[196:199],v[136:139]\n"
MU"v95,s55,v95\n"
PK"v60,v60,v61\n"
MF"v[140:143],a[92:95],v[196:199],v[140:143]\n"
BL"v240,s[20:23],s48 offen offset:2048 lds\n"
MF"v[140:143],a[76:79],v[192:195],v[140:143]\n"
RD"v[220:223],v243 offset:38912\n"
PK"v61,v62,v63\n"
MF"v[144:147],a[64:67],v[200:203],v[144:147]\n"
PK"v62,v92,v93\n"
PK"v63,v94,v95\n"
MF"v[144:147],a[80:83],v[204:207],v[144:147]\n"
MF"v[148:151],a[84:87],v[204:207],v[148:151]\n"
MF"v[148:151],a[68:71],v[200:203],v[148:151]\n"
RD"v[192:195],v244 offset:8192\n"
MF"v[152:155],a[72:75],v[200:203],v[152:155]\n"
MF"v[152:155],a[88:91],v[204:207],v[152:155]\n"
MF"v[156:159],a[92:95],v[204:207],v[156:159]\n"
SA"s61,s61,524288\n"
BL"v241,s[20:23],s48 offen offset:3072 lds\n"
MF"v[156:159],a[76:79],v[200:203],v[156:159]\n"
WT"lgkmcnt(1)\n"
RD"v[196:199],v245 offset:8192\n"
MF"v[128:131],a[96:99],v[208:211],v[128:131]\n"
MF"v[128:131],a[112:115],v[212:215],v[128:131]\n"
MF"v[132:135],a[116:119],v[212:215],v[132:135]\n"
MF"v[132:135],a[100:103],v[208:211],v[132:135]\n"
RD"v[200:203],v244 offset:10240\n"
MF"v[136:139],a[104:107],v[208:211],v[136:139]\n"
MF"v[136:139],a[120:123],v[212:215],v[136:139]\n"
MF"v[140:143],a[124:127],v[212:215],v[140:143]\n"
RD"v[204:207],v245 offset:10240\n"
MF"v[140:143],a[108:111],v[208:211],v[140:143]\n"
MF"v[144:147],a[96:99],v[216:219],v[144:147]\n"
BS"v[72:75],v250,s[8:11],s49 offen offset:1024 sc1\n"
MF"v[144:147],a[112:115],v[220:223],v[144:147]\n"
MF"v[148:151],a[116:119],v[220:223],v[148:151]\n"
RD"v[208:211],v244 offset:12288\n"
MF"v[148:151],a[100:103],v[216:219],v[148:151]\n"
MF"v[152:155],a[104:107],v[216:219],v[152:155]\n"
SA"m0,s38,65536\n"
SA"s48,s60,s61\n"
MF"v[152:155],a[120:123],v[220:223],v[152:155]\n"
MF"v[156:159],a[124:127],v[220:223],v[156:159]\n"
RD"v[212:215],v245 offset:12288\n"
MF"v[156:159],a[108:111],v[216:219],v[156:159]\n"
WT"lgkmcnt(2)\n"
BL"v238,s[20:23],s48 offen offset:0 lds\n"
MF"v[128:131],a[128:131],v[192:195],v[128:131]\n"
MF"v[128:131],a[144:147],v[196:199],v[128:131]\n"
MF"v[132:135],a[148:151],v[196:199],v[132:135]\n"
RD"v[216:219],v244 offset:14336\n"
MF"v[132:135],a[132:135],v[192:195],v[132:135]\n"
MF"v[136:139],a[136:139],v[192:195],v[136:139]\n"
MF"v[136:139],a[152:155],v[196:199],v[136:139]\n"
RD"v[220:223],v245 offset:14336\n"
MF"v[140:143],a[156:159],v[196:199],v[140:143]\n"
BL"v239,s[20:23],s48 offen offset:1024 lds\n"
MF"v[140:143],a[140:143],v[192:195],v[140:143]\n"
MF"v[144:147],a[128:131],v[200:203],v[144:147]\n"
MF"v[144:147],a[144:147],v[204:207],v[144:147]\n"
RD"v[192:195],v244 offset:32768\n"
MF"v[148:151],a[148:151],v[204:207],v[148:151]\n"
MF"v[148:151],a[132:135],v[200:203],v[148:151]\n"
MF"v[152:155],a[136:139],v[200:203],v[152:155]\n"
MF"v[152:155],a[152:155],v[204:207],v[152:155]\n"
RD"v[196:199],v245 offset:32768\n"
MF"v[156:159],a[156:159],v[204:207],v[156:159]\n"
BL"v240,s[20:23],s48 offen offset:2048 lds\n"
MF"v[156:159],a[140:143],v[200:203],v[156:159]\n"
WT"lgkmcnt(2)\n"
MF"v[128:131],a[160:163],v[208:211],v[128:131]\n"
MF"v[128:131],a[176:179],v[212:215],v[128:131]\n"
RD"v[200:203],v244 offset:34816\n"
MF"v[132:135],a[180:183],v[212:215],v[132:135]\n"
MF"v[132:135],a[164:167],v[208:211],v[132:135]\n"
MF"v[136:139],a[168:171],v[208:211],v[136:139]\n"
MF"v[136:139],a[184:187],v[212:215],v[136:139]\n"
"s_mov_b32 s49,20480\n"
RD"v[204:207],v245 offset:34816\n"
MF"v[140:143],a[188:191],v[212:215],v[140:143]\n"
SA"s61,s61,524288\n"
BL"v241,s[20:23],s48 offen offset:3072 lds\n"
MF"v[140:143],a[172:175],v[208:211],v[140:143]\n"
"s_cmp_eq_u32 s61,5242880\n"
BS"v[80:83],v250,s[8:11],s49 offen offset:0 sc1\n"
MF"v[144:147],a[160:163],v[216:219],v[144:147]\n"
"s_cselect_b32 s61,0,s61\n"
RD"v[208:211],v244 offset:36864\n"
MF"v[144:147],a[176:179],v[220:223],v[144:147]\n"
MF"v[148:151],a[180:183],v[220:223],v[148:151]\n"
MF"v[148:151],a[164:167],v[216:219],v[148:151]\n"
MF"v[152:155],a[168:171],v[216:219],v[152:155]\n"
SA"m0,s38,114688\n"
RD"v[212:215],v245 offset:36864\n"
SA"s48,s60,s61\n"
MF"v[152:155],a[184:187],v[220:223],v[152:155]\n"
MF"v[156:159],a[188:191],v[220:223],v[156:159]\n"
BL"v238,s[20:23],s48 offen offset:0 lds\n"
MF"v[156:159],a[172:175],v[216:219],v[156:159]\n"
WT"lgkmcnt(1)\n"
MF"v[128:131],a[192:195],v[192:195],v[128:131]\n"
RD"v[216:219],v244 offset:38912\n"
MF"v[128:131],a[208:211],v[196:199],v[128:131]\n"
MF"v[132:135],a[212:215],v[196:199],v[132:135]\n"
MF"v[132:135],a[196:199],v[192:195],v[132:135]\n"
MF"v[136:139],a[200:203],v[192:195],v[136:139]\n"
RD"v[220:223],v245 offset:38912\n"
MF"v[136:139],a[216:219],v[196:199],v[136:139]\n"
MF"v[140:143],a[220:223],v[196:199],v[140:143]\n"
BL"v239,s[20:23],s48 offen offset:1024 lds\n"
MF"v[140:143],a[204:207],v[192:195],v[140:143]\n"
MF"v[144:147],a[192:195],v[200:203],v[144:147]\n"
WT"vmcnt(13)\n"
BS"v[100:103],v252,s[8:11],s92 offen sc1\n"
MF"v[144:147],a[208:211],v[204:207],v[144:147]\n"
MF"v[148:151],a[212:215],v[204:207],v[148:151]\n"
MF"v[148:151],a[196:199],v[200:203],v[148:151]\n"
MF"v[152:155],a[200:203],v[200:203],v[152:155]\n"
MF"v[152:155],a[216:219],v[204:207],v[152:155]\n"
WT"lgkmcnt(0)\n"
"s_barrier\n"
MF"v[156:159],a[220:223],v[204:207],v[156:159]\n"
RD"v[192:195],v242 offset:16384\n"
MF"v[156:159],a[204:207],v[200:203],v[156:159]\n"
BL"v240,s[20:23],s48 offen offset:2048 lds\n"
MF"v[128:131],a[224:227],v[208:211],v[128:131]\n"
MF"v[128:131],a[240:243],v[212:215],v[128:131]\n"
MF"v[132:135],a[244:247],v[212:215],v[132:135]\n"
RD"v[196:199],v243 offset:16384\n"
MF"v[132:135],a[228:231],v[208:211],v[132:135]\n"
MF"v[136:139],a[232:235],v[208:211],v[136:139]\n"
MF"v[136:139],a[248:251],v[212:215],v[136:139]\n"
MF"v[140:143],a[252:255],v[212:215],v[140:143]\n"
SA"s61,s61,524288\n"
RD"v[200:203],v242 offset:18432\n"
MF"v[140:143],a[236:239],v[208:211],v[140:143]\n"
BS"v[88:91],v250,s[8:11],s49 offen offset:1024 sc1\n"
MF"v[144:147],a[224:227],v[216:219],v[144:147]\n"
"s_lshl_b32 s50,s34,3\n"
BL"v241,s[20:23],s48 offen offset:3072 lds\n"
MF"v[144:147],a[240:243],v[220:223],v[144:147]\n"
SA"s51,s50,s30\n"
MF"v[148:151],a[244:247],v[220:223],v[148:151]\n"
"s_mul_i32 s51,s51,20480\n"
RD"v[204:207],v243 offset:18432\n"
MF"v[148:151],a[228:231],v[216:219],v[148:151]\n"
SA"s56,s51,524288\n"
MF"v[152:155],a[232:235],v[216:219],v[152:155]\n"
SA"m0,s36,16384\n"
SA"s48,s60,s61\n"
SA"s57,s56,81920\n"
MF"v[152:155],a[248:251],v[220:223],v[152:155]\n"
"s_sub_u32 s51,s30,s50\n"
MF"v[156:159],a[252:255],v[220:223],v[156:159]\n"
SA"s51,s51,120\n"
RD"v[208:211],v242 offset:20480\n"
MF"v[156:159],a[236:239],v[216:219],v[156:159]\n"
WT"lgkmcnt(2)\n"
BL"v238,s[20:23],s48 offen offset:0 lds\n"
MF"v[0:3],a[0:3],v[192:195],0\n"
"s_mul_i32 s51,s51,20480\n"
SA"s58,s51,524288\n"
"s_lshl_b32 s99,s99,12\n"
MF"v[0:3],a[16:19],v[196:199],v[0:3]\n"
SA"s59,s58,81920\n"
RD"v[212:215],v243 offset:20480\n"
MF"v[8:11],a[20:23],v[196:199],0\n"
"v_add_u32 v235,s99,v224\n"
MF"v[8:11],a[4:7],v[192:195],v[8:11]\n"
MU"v128,s55,v128\n"
MU"v129,s55,v129\n"
MF"v[16:19],a[8:11],v[192:195],0\n"
MU"v130,s55,v130\n"
MU"v131,s55,v131\n"
MF"v[16:19],a[24:27],v[196:199],v[16:19]\n"
RD"v[216:219],v242 offset:22528\n"
MU"v132,s55,v132\n"
MF"v[24:27],a[28:31],v[196:199],0\n"
BL"v239,s[20:23],s48 offen offset:1024 lds\n"
MF"v[24:27],a[12:15],v[192:195],v[24:27]\n"
MU"v133,s55,v133\n"
MU"v134,s55,v134\n"
MF"v[32:35],a[0:3],v[200:203],0\n"
WT"lgkmcnt(1)\n"
MU"v135,s55,v135\n"
PK"v64,v128,v129\n"
MF"v[32:35],a[16:19],v[204:207],v[32:35]\n"
RD"v[220:223],v243 offset:22528\n"
PK"v65,v130,v131\n"
MF"v[40:43],a[20:23],v[204:207],0\n"
PK"v66,v132,v133\n"
PK"v67,v134,v135\n"
MF"v[40:43],a[4:7],v[200:203],v[40:43]\n"
MF"v[48:51],a[8:11],v[200:203],0\n"
MF"v[48:51],a[24:27],v[204:207],v[48:51]\n"
RD"v[192:195],v242 offset:40960\n"
MU"v136,s55,v136\n"
MF"v[56:59],a[28:31],v[204:207],0\n"
BL"v240,s[20:23],s48 offen offset:2048 lds\n"
MF"v[56:59],a[12:15],v[200:203],v[56:59]\n"
MU"v137,s55,v137\n"
MU"v138,s55,v138\n"
MF"v[0:3],a[32:35],v[208:211],v[0:3]\n"
BL"a[0:3],v235,s[8:11],s56 offen offset:0 sc1\n"
MF"v[0:3],a[48:51],v[212:215],v[0:3]\n"
RD"v[196:199],v243 offset:40960\n"
MU"v139,s55,v139\n"
MF"v[8:11],a[52:55],v[212:215],v[8:11]\n"
MU"v140,s55,v140\n"
MU"v141,s55,v141\n"
MF"v[8:11],a[36:39],v[208:211],v[8:11]\n"
"s_mov_b32 s49,24576\n"
MU"v142,s55,v142\n"
MU"v143,s55,v143\n"
MF"v[16:19],a[40:43],v[208:211],v[16:19]\n"
RD"v[200:203],v242 offset:43008\n"
PK"v68,v136,v137\n"
MF"v[16:19],a[56:59],v[212:215],v[16:19]\n"
BS"v[96:99],v250,s[8:11],s49 offen offset:0 sc1\n"
MF"v[24:27],a[60:63],v[212:215],v[24:27]\n"
SA"s61,s61,524288\n"
BL"v241,s[20:23],s48 offen offset:3072 lds\n"
MF"v[24:27],a[44:47],v[208:211],v[24:27]\n"
WT"lgkmcnt(2)\n"
BL"a[4:7],v235,s[8:11],s57 offen offset:0 sc1\n"
MF"v[32:35],a[32:35],v[216:219],v[32:35]\n"
"s_cmp_eq_u32 s61,5242880\n"
RD"v[204:207],v243 offset:43008\n"
PK"v69,v138,v139\n"
MF"v[32:35],a[48:51],v[220:223],v[32:35]\n"
"s_cselect_b32 s61,0,s61\n"
PK"v70,v140,v141\n"
PK"v71,v142,v143\n"
MF"v[40:43],a[52:55],v[220:223],v[40:43]\n"
MU"v144,s55,v144\n"
MU"v145,s55,v145\n"
MF"v[40:43],a[36:39],v[216:219],v[40:43]\n"
MU"v146,s55,v146\n"
MU"v147,s55,v147\n"
MF"v[48:51],a[40:43],v[216:219],v[48:51]\n"
RD"v[208:211],v242 offset:45056\n"
MU"v148,s55,v148\n"
MF"v[48:51],a[56:59],v[220:223],v[48:51]\n"
BL"a[8:11],v235,s[8:11],s58 offen offset:0 sc1\n"
MF"v[56:59],a[60:63],v[220:223],v[56:59]\n"
MU"v149,s55,v149\n"
MU"v150,s55,v150\n"
MF"v[56:59],a[44:47],v[216:219],v[56:59]\n"
MU"v151,s55,v151\n"
PK"v72,v144,v145\n"
MF"v[0:3],a[64:67],v[192:195],v[0:3]\n"
WT"lgkmcnt(1)\n"
RD"v[212:215],v243 offset:45056\n"
PK"v73,v146,v147\n"
MF"v[0:3],a[80:83],v[196:199],v[0:3]\n"
PK"v74,v148,v149\n"
PK"v75,v150,v151\n"
MF"v[8:11],a[84:87],v[196:199],v[8:11]\n"
BL"a[12:15],v235,s[8:11],s59 offen offset:0 sc1\n"
MF"v[8:11],a[68:71],v[192:195],v[8:11]\n"
RD"v[216:219],v242 offset:47104\n"
MU"v152,s55,v152\n"
MF"v[16:19],a[72:75],v[192:195],v[16:19]\n"
MU"v153,s55,v153\n"
MU"v154,s55,v154\n"
MF"v[16:19],a[88:91],v[196:199],v[16:19]\n"
MU"v155,s55,v155\n"
MU"v156,s55,v156\n"
MF"v[24:27],a[92:95],v[196:199],v[24:27]\n"
MU"v157,s55,v157\n"
MU"v158,s55,v158\n"
MF"v[24:27],a[76:79],v[192:195],v[24:27]\n"
RD"v[220:223],v243 offset:47104\n"
MU"v159,s55,v159\n"
MF"v[32:35],a[64:67],v[200:203],v[32:35]\n"
BL"a[16:19],v235,s[8:11],s56 offen offset:1024 sc1\n"
MF"v[32:35],a[80:83],v[204:207],v[32:35]\n"
PK"v76,v152,v153\n"
PK"v77,v154,v155\n"
MF"v[40:43],a[84:87],v[204:207],v[40:43]\n"
PK"v78,v156,v157\n"
PK"v79,v158,v159\n"
MF"v[40:43],a[68:71],v[200:203],v[40:43]\n"
RD"v[192:195],v244 offset:16384\n"
MF"v[48:51],a[72:75],v[200:203],v[48:51]\n"
MF"v[48:51],a[88:91],v[204:207],v[48:51]\n"
MF"v[56:59],a[92:95],v[204:207],v[56:59]\n"
BL"a[20:23],v235,s[8:11],s57 offen offset:1024 sc1\n"
MF"v[56:59],a[76:79],v[200:203],v[56:59]\n"
WT"lgkmcnt(1)\n"
RD"v[196:199],v245 offset:16384\n"
MF"v[0:3],a[96:99],v[208:211],v[0:3]\n"
MF"v[0:3],a[112:115],v[212:215],v[0:3]\n"
MF"v[8:11],a[116:119],v[212:215],v[8:11]\n"
MF"v[8:11],a[100:103],v[208:211],v[8:11]\n"
RD"v[200:203],v244 offset:18432\n"
MF"v[16:19],a[104:107],v[208:211],v[16:19]\n"
BS"v[104:107],v250,s[8:11],s49 offen offset:1024 sc1\n"
MF"v[16:19],a[120:123],v[212:215],v[16:19]\n"
BL"a[24:27],v235,s[8:11],s58 offen offset:1024 sc1\n"
MF"v[24:27],a[124:127],v[212:215],v[24:27]\n"
RD"v[204:207],v245 offset:18432\n"
MF"v[24:27],a[108:111],v[208:211],v[24:27]\n"
MF"v[32:35],a[96:99],v[216:219],v[32:35]\n"
MF"v[32:35],a[112:115],v[220:223],v[32:35]\n"
MF"v[40:43],a[116:119],v[220:223],v[40:43]\n"
RD"v[208:211],v244 offset:20480\n"
MF"v[40:43],a[100:103],v[216:219],v[40:43]\n"
BL"a[28:31],v235,s[8:11],s59 offen offset:1024 sc1\n"
MF"v[48:51],a[104:107],v[216:219],v[48:51]\n"
MF"v[48:51],a[120:123],v[220:223],v[48:51]\n"
MF"v[56:59],a[124:127],v[220:223],v[56:59]\n"
RD"v[212:215],v245 offset:20480\n"
MF"v[56:59],a[108:111],v[216:219],v[56:59]\n"
WT"lgkmcnt(2)\n"
MF"v[0:3],a[128:131],v[192:195],v[0:3]\n"
BL"a[32:35],v235,s[8:11],s56 offen offset:2048 sc1\n"
MF"v[0:3],a[144:147],v[196:199],v[0:3]\n"
MF"v[8:11],a[148:151],v[196:199],v[8:11]\n"
RD"v[216:219],v244 offset:22528\n"
MF"v[8:11],a[132:135],v[192:195],v[8:11]\n"
MF"v[16:19],a[136:139],v[192:195],v[16:19]\n"
MF"v[16:19],a[152:155],v[196:199],v[16:19]\n"
RD"v[220:223],v245 offset:22528\n"
MF"v[24:27],a[156:159],v[196:199],v[24:27]\n"
BL"a[36:39],v235,s[8:11],s57 offen offset:2048 sc1\n"
MF"v[24:27],a[140:143],v[192:195],v[24:27]\n"
MF"v[32:35],a[128:131],v[200:203],v[32:35]\n"
MF"v[32:35],a[144:147],v[204:207],v[32:35]\n"
RD"v[192:195],v244 offset:40960\n"
MF"v[40:43],a[148:151],v[204:207],v[40:43]\n"
MF"v[40:43],a[132:135],v[200:203],v[40:43]\n"
MF"v[48:51],a[136:139],v[200:203],v[48:51]\n"
BL"a[40:43],v235,s[8:11],s58 offen offset:2048 sc1\n"
MF"v[48:51],a[152:155],v[204:207],v[48:51]\n"
RD"v[196:199],v245 offset:40960\n"
MF"v[56:59],a[156:159],v[204:207],v[56:59]\n"
MF"v[56:59],a[140:143],v[200:203],v[56:59]\n"
WT"lgkmcnt(2)\n"
MF"v[0:3],a[160:163],v[208:211],v[0:3]\n"
"s_sub_u32 s53,s37,65536\n"
MF"v[0:3],a[176:179],v[212:215],v[0:3]\n"
"s_mov_b32 s49,28672\n"
RD"v[200:203],v244 offset:43008\n"
MF"v[8:11],a[180:183],v[212:215],v[8:11]\n"
BL"a[44:47],v235,s[8:11],s59 offen offset:2048 sc1\n"
MF"v[8:11],a[164:167],v[208:211],v[8:11]\n"
BS"v[112:115],v250,s[8:11],s49 offen offset:0 sc1\n"
MF"v[16:19],a[168:171],v[208:211],v[16:19]\n"
"v_subrev_u32 v246,s53,v246\n"
"v_subrev_u32 v247,s53,v247\n"
MF"v[16:19],a[184:187],v[212:215],v[16:19]\n"
RD"v[204:207],v245 offset:43008\n"
"v_subrev_u32 v248,s53,v248\n"
MF"v[24:27],a[188:191],v[212:215],v[24:27]\n"
"v_subrev_u32 v249,s53,v249\n"
"v_subrev_u32 v236,65536,v246\n"
MF"v[24:27],a[172:175],v[208:211],v[24:27]\n"
"v_subrev_u32 v237,65536,v247\n"
"v_subrev_u32 v228,65536,v248\n"
MF"v[32:35],a[160:163],v[216:219],v[32:35]\n"
RD"v[208:211],v244 offset:45056\n"
"v_subrev_u32 v229,65536,v249\n"
MF"v[32:35],a[176:179],v[220:223],v[32:35]\n"
BL"a[48:51],v235,s[8:11],s56 offen offset:3072 sc1\n"
MF"v[40:43],a[180:183],v[220:223],v[40:43]\n"
MF"v[40:43],a[164:167],v[216:219],v[40:43]\n"
MF"v[48:51],a[168:171],v[216:219],v[48:51]\n"
RD"v[212:215],v245 offset:45056\n"
MF"v[48:51],a[184:187],v[220:223],v[48:51]\n"
MF"v[56:59],a[188:191],v[220:223],v[56:59]\n"
BL"a[52:55],v235,s[8:11],s57 offen offset:3072 sc1\n"
MF"v[56:59],a[172:175],v[216:219],v[56:59]\n"
WT"lgkmcnt(1)\n"
MF"v[0:3],a[192:195],v[192:195],v[0:3]\n"
RD"v[216:219],v244 offset:47104\n"
MF"v[0:3],a[208:211],v[196:199],v[0:3]\n"
MF"v[8:11],a[212:215],v[196:199],v[8:11]\n"
MF"v[8:11],a[196:199],v[192:195],v[8:11]\n"
MF"v[16:19],a[200:203],v[192:195],v[16:19]\n"
RD"v[220:223],v245 offset:47104\n"
MF"v[16:19],a[216:219],v[196:199],v[16:19]\n"
BL"a[56:59],v235,s[8:11],s58 offen offset:3072 sc1\n"
MF"v[24:27],a[220:223],v[196:199],v[24:27]\n"
MF"v[24:27],a[204:207],v[192:195],v[24:27]\n"
MF"v[32:35],a[192:195],v[200:203],v[32:35]\n"
SA"s99,s99,4096\n"
MF"v[32:35],a[208:211],v[204:207],v[32:35]\n"
"s_cmp_eq_u32 s99,20480\n"
"s_cselect_b32 s99,0,s99\n"
MF"v[40:43],a[212:215],v[204:207],v[40:43]\n"
BL"a[60:63],v235,s[8:11],s59 offen offset:3072 sc1\n"
MF"v[40:43],a[196:199],v[200:203],v[40:43]\n"
MF"v[48:51],a[200:203],v[200:203],v[48:51]\n"
MF"v[48:51],a[216:219],v[204:207],v[48:51]\n"
WT"lgkmcnt(0)\n"
"s_barrier\n"
MF"v[56:59],a[220:223],v[204:207],v[56:59]\n"
RT"a[128:129],v236 offset:49152\n"
MF"v[56:59],a[204:207],v[200:203],v[56:59]\n"
"v_add_u32 v235,s99,v224\n"
MF"v[0:3],a[224:227],v[208:211],v[0:3]\n"
SA"m0,s38,73728\n"
RT"a[130:131],v236 offset:49664\n"
SA"s48,s60,s61\n"
MF"v[0:3],a[240:243],v[212:215],v[0:3]\n"
BL"a[64:67],v235,s[8:11],s56 offen offset:0 sc1\n"
MF"v[8:11],a[244:247],v[212:215],v[8:11]\n"
RT"a[132:133],v236 offset:57344\n"
"v_add_u32 v242,49152,v246\n"
MF"v[8:11],a[228:231],v[208:211],v[8:11]\n"
BS"v[120:123],v250,s[8:11],s49 offen offset:1024 sc1\n"
MF"v[16:19],a[232:235],v[208:211],v[16:19]\n"
BL"v238,s[20:23],s48 offen offset:0 lds\n"
MF"v[16:19],a[248:251],v[212:215],v[16:19]\n"
RT"a[134:135],v236 offset:57856\n"
"v_add_u32 v243,49152,v247\n"
MF"v[24:27],a[252:255],v[212:215],v[24:27]\n"
RT"a[136:137],v237 offset:49152\n"
"v_add_u32 v244,49152,v248\n"
MF"v[24:27],a[236:239],v[208:211],v[24:27]\n"
BL"a[68:71],v235,s[8:11],s57 offen offset:0 sc1\n"
MF"v[32:35],a[224:227],v[216:219],v[32:35]\n"
RT"a[138:139],v237 offset:49664\n"
"v_add_u32 v245,49152,v249\n"
MF"v[32:35],a[240:243],v[220:223],v[32:35]\n"
MF"v[40:43],a[244:247],v[220:223],v[40:43]\n"
RT"a[140:141],v237 offset:57344\n"
MF"v[40:43],a[228:231],v[216:219],v[40:43]\n"
BL"v239,s[20:23],s48 offen offset:1024 lds\n"
MF"v[48:51],a[232:235],v[216:219],v[48:51]\n"
RT"a[142:143],v237 offset:57856\n"
MF"v[48:51],a[248:251],v[220:223],v[48:51]\n"
BL"a[72:75],v235,s[8:11],s58 offen offset:0 sc1\n"
MF"v[56:59],a[252:255],v[220:223],v[56:59]\n"
RT"a[144:145],v228 offset:49152\n"
MF"v[56:59],a[236:239],v[216:219],v[56:59]\n"
WT"vmcnt(25) lgkmcnt(3)\n"
MF"v[80:83],a[128:131],a[0:3],0\n"
WT"vmcnt(19)\n"
RT"a[146:147],v228 offset:49664\n"
MF"v[80:83],a[132:135],a[16:19],v[80:83]\n"
WT"vmcnt(18)\n"
MF"v[88:91],a[132:135],a[20:23],0\n"
RT"a[148:149],v228 offset:57344\n"
MF"v[88:91],a[128:131],a[4:7],v[88:91]\n"
BL"v240,s[20:23],s48 offen offset:2048 lds\n"
MF"v[96:99],a[128:131],a[8:11],0\n"
WT"vmcnt(17)\n"
BL"a[76:79],v235,s[8:11],s59 offen offset:0 sc1\n"
MF"v[96:99],a[132:135],a[24:27],v[96:99]\n"
WT"vmcnt(17)\n"
RT"a[150:151],v228 offset:57856\n"
RT"a[152:153],v229 offset:49152\n"
MF"v[104:107],a[132:135],a[28:31],0\n"
MU"v0,s55,v0\n"
MU"v1,s55,v1\n"
MF"v[104:107],a[128:131],a[12:15],v[104:107]\n"
RT"a[154:155],v229 offset:49664\n"
MU"v2,s55,v2\n"
MF"v[84:87],a[136:139],a[0:3],0\n"
WT"lgkmcnt(4)\n"
MU"v3,s55,v3\n"
MU"v8,s55,v8\n"
MF"v[84:87],a[140:143],a[16:19],v[84:87]\n"
RT"a[156:157],v229 offset:57344\n"
MU"v9,s55,v9\n"
MF"v[92:95],a[140:143],a[20:23],0\n"
SA"s61,s61,524288\n"
BL"a[80:83],v235,s[8:11],s56 offen offset:1024 sc1\n"
MF"v[92:95],a[136:139],a[4:7],v[92:95]\n"
BL"v241,s[20:23],s48 offen offset:3072 lds\n"
MF"v[100:103],a[136:139],a[8:11],0\n"
RT"a[158:159],v229 offset:57856\n"
MU"v10,s55,v10\n"
MF"v[100:103],a[140:143],a[24:27],v[100:103]\n"
RT"a[160:161],v236 offset:53248\n"
MU"v11,s55,v11\n"
MF"v[108:111],a[140:143],a[28:31],0\n"
PK"v0,v0,v1\n"
PK"v1,v2,v3\n"
MF"v[108:111],a[136:139],a[12:15],v[108:111]\n"
RT"a[162:163],v236 offset:53760\n"
PK"v2,v8,v9\n"
MF"v[112:115],a[144:147],a[0:3],0\n"
WT"lgkmcnt(3)\n"
BL"a[84:87],v235,s[8:11],s57 offen offset:1024 sc1\n"
MF"v[112:115],a[148:151],a[16:19],v[112:115]\n"
RT"a[164:165],v236 offset:61440\n"
PK"v3,v10,v11\n"
MF"v[120:123],a[148:151],a[20:23],0\n"
MU"v16,s55,v16\n"
MU"v17,s55,v17\n"
MF"v[120:123],a[144:147],a[4:7],v[120:123]\n"
RT"a[166:167],v236 offset:61952\n"
MU"v18,s55,v18\n"
MF"v[128:131],a[144:147],a[8:11],0\n"
MU"v19,s55,v19\n"
MU"v24,s55,v24\n"
MF"v[128:131],a[148:151],a[24:27],v[128:131]\n"
RT"a[168:169],v237 offset:53248\n"
MU"v25,s55,v25\n"
MF"v[136:139],a[148:151],a[28:31],0\n"
MU"v26,s55,v26\n"
MU"v27,s55,v27\n"
MF"v[136:139],a[144:147],a[12:15],v[136:139]\n"
RT"a[170:171],v237 offset:53760\n"
PK"v16,v16,v17\n"
MF"v[116:119],a[152:155],a[0:3],0\n"
WT"lgkmcnt(3)\n"
BL"a[88:91],v235,s[8:11],s58 offen offset:1024 sc1\n"
MF"v[116:119],a[156:159],a[16:19],v[116:119]\n"
"s_mov_b32 s49,262144\n"
RT"a[172:173],v237 offset:61440\n"
PK"v17,v18,v19\n"
MF"v[124:127],a[156:159],a[20:23],0\n"
RT"a[174:175],v237 offset:61952\n"
PK"v18,v24,v25\n"
MF"v[124:127],a[152:155],a[4:7],v[124:127]\n"
BS"v[4:7],v250,s[8:11],s49 offen offset:0 sc1\n"
MF"v[132:135],a[152:155],a[8:11],0\n"
RT"a[176:177],v228 offset:53248\n"
PK"v19,v26,v27\n"
MF"v[132:135],a[156:159],a[24:27],v[132:135]\n"
MU"v32,s55,v32\n"
MU"v33,s55,v33\n"
MF"v[140:143],a[156:159],a[28:31],0\n"
RT"a[178:179],v228 offset:53760\n"
MU"v34,s55,v34\n"
MF"v[140:143],a[152:155],a[12:15],v[140:143]\n"
BL"a[92:95],v235,s[8:11],s59 offen offset:1024 sc1\n"
MF"v[144:147],a[160:163],a[0:3],0\n"
WT"lgkmcnt(3)\n"
RT"a[180:181],v228 offset:61440\n"
MU"v35,s55,v35\n"
MF"v[144:147],a[164:167],a[16:19],v[144:147]\n"
MU"v40,s55,v40\n"
MU"v41,s55,v41\n"
MF"v[152:155],a[164:167],a[20:23],0\n"
SA"m0,s38,122880\n"
RT"a[182:183],v228 offset:61952\n"
MU"v42,s55,v42\n"
MF"v[152:155],a[160:163],a[4:7],v[152:155]\n"
SA"s48,s60,s61\n"
MU"v43,s55,v43\n"
PK"v32,v32,v33\n"
MF"v[160:163],a[160:163],a[8:11],0\n"
"s_mov_b32 s88,4096\n"
RT"a[184:185],v229 offset:53248\n"
PK"v33,v34,v35\n"
MF"v[160:163],a[164:167],a[24:27],v[160:163]\n"
BL"v238,s[20:23],s48 offen offset:0 lds\n"
MF"v[168:171],a[164:167],a[28:31],0\n"
BL"a[96:99],v235,s[8:11],s56 offen offset:2048 sc1\n"
MF"v[168:171],a[160:163],a[12:15],v[168:171]\n"
RT"a[186:187],v229 offset:53760\n"
PK"v34,v40,v41\n"
MF"v[148:151],a[168:171],a[0:3],0\n"
WT"lgkmcnt(3)\n"
RT"a[188:189],v229 offset:61440\n"
PK"v35,v42,v43\n"
MF"v[148:151],a[172:175],a[16:19],v[148:151]\n"
MU"v48,s55,v48\n"
MU"v49,s55,v49\n"
MF"v[156:159],a[172:175],a[20:23],0\n"
RT"a[190:191],v229 offset:61952\n"
MU"v50,s55,v50\n"
MF"v[156:159],a[168:171],a[4:7],v[156:159]\n"
RT"a[128:129],v236 offset:32768\n"
MU"v51,s55,v51\n"
MF"v[164:167],a[168:171],a[8:11],0\n"
BL"v239,s[20:23],s48 offen offset:1024 lds\n"
MF"v[164:167],a[172:175],a[24:27],v[164:167]\n"
BL"a[100:103],v235,s[8:11],s57 offen offset:2048 sc1\n"
MF"v[172:175],a[172:175],a[28:31],0\n"
RT"a[130:131],v236 offset:33280\n"
MU"v56,s55,v56\n"
MF"v[172:175],a[168:171],a[12:15],v[172:175]\n"
RT"a[132:133],v236 offset:40960\n"
MU"v57,s55,v57\n"
MF"v[176:179],a[176:179],a[0:3],0\n"
WT"lgkmcnt(4)\n"
MU"v58,s55,v58\n"
MU"v59,s55,v59\n"
MF"v[176:179],a[180:183],a[16:19],v[176:179]\n"
RT"a[134:135],v236 offset:41472\n"
PK"v48,v48,v49\n"
MF"v[184:187],a[180:183],a[20:23],0\n"
PK"v49,v50,v51\n"
PK"v50,v56,v57\n"
MF"v[184:187],a[176:179],a[4:7],v[184:187]\n"
RT"a[136:137],v237 offset:32768\n"
PK"v51,v58,v59\n"
MF"v[192:195],a[176:179],a[8:11],0\n"
BL"a[104:107],v235,s[8:11],s58 offen offset:2048 sc1\n"
MF"v[192:195],a[180:183],a[24:27],v[192:195]\n"
BL"v240,s[20:23],s48 offen offset:2048 lds\n"
MF"v[200:203],a[180:183],a[28:31],0\n"
RT"a[138:139],v237 offset:33280\n"
MF"v[200:203],a[176:179],a[12:15],v[200:203]\n"
RT"a[140:141],v237 offset:40960\n"
MF"v[180:183],a[184:187],a[0:3],0\n"
WT"lgkmcnt(3)\n"
MF"v[180:183],a[188:191],a[16:19],v[180:183]\n"
RT"a[142:143],v237 offset:41472\n"
MF"v[188:191],a[188:191],a[20:23],0\n"
BL"a[108:111],v235,s[8:11],s59 offen offset:2048 sc1\n"
MF"v[188:191],a[184:187],a[4:7],v[188:191]\n"
RT"a[144:145],v228 offset:32768\n"
MF"v[196:199],a[184:187],a[8:11],0\n"
SA"s61,s61,524288\n"
BS"v[12:15],v250,s[8:11],s49 offen offset:1024 sc1\n"
MF"v[196:199],a[188:191],a[24:27],v[196:199]\n"
"s_cmp_eq_u32 s61,5242880\n"
BL"v241,s[20:23],s48 offen offset:3072 lds\n"
MF"v[204:207],a[188:191],a[28:31],0\n"
"s_cselect_b32 s61,0,s61\n"
RT"a[146:147],v228 offset:33280\n"
MF"v[204:207],a[184:187],a[12:15],v[204:207]\n"
WT"vmcnt(31)\n"
RT"a[148:149],v228 offset:40960\n"
MF"v[80:83],a[128:131],a[32:35],v[80:83]\n"
WT"vmcnt(26)\n"
MF"v[80:83],a[132:135],a[48:51],v[80:83]\n"
WT"vmcnt(25)\n"
RT"a[150:151],v228 offset:41472\n"
MF"v[88:91],a[132:135],a[52:55],v[88:91]\n"
BL"a[112:115],v235,s[8:11],s56 offen offset:3072 sc1\n"
MF"v[88:91],a[128:131],a[36:39],v[88:91]\n"
RT"a[152:153],v229 offset:32768\n"
MF"v[96:99],a[128:131],a[40:43],v[96:99]\n"
WT"vmcnt(25)\n"
RT"a[154:155],v229 offset:33280\n"
MF"v[96:99],a[132:135],a[56:59],v[96:99]\n"
WT"vmcnt(24)\n"
MF"v[104:107],a[132:135],a[60:63],v[104:107]\n"
RT"a[156:157],v229 offset:40960\n"
MF"v[104:107],a[128:131],a[44:47],v[104:107]\n"
WT"lgkmcnt(4)\n"
MF"v[84:87],a[136:139],a[32:35],v[84:87]\n"
RT"a[158:159],v229 offset:41472\n"
MF"v[84:87],a[140:143],a[48:51],v[84:87]\n"
BL"a[116:119],v235,s[8:11],s57 offen offset:3072 sc1\n"
MF"v[92:95],a[140:143],a[52:55],v[92:95]\n"
RT"a[160:161],v236 offset:36864\n"
MF"v[92:95],a[136:139],a[36:39],v[92:95]\n"
MF"v[100:103],a[136:139],a[40:43],v[100:103]\n"
RT"a[162:163],v236 offset:37376\n"
MF"v[100:103],a[140:143],a[56:59],v[100:103]\n"
MF"v[108:111],a[140:143],a[60:63],v[108:111]\n"
RT"a[164:165],v236 offset:45056\n"
MF"v[108:111],a[136:139],a[44:47],v[108:111]\n"
BL"a[120:123],v235,s[8:11],s58 offen offset:3072 sc1\n"
MF"v[112:115],a[144:147],a[32:35],v[112:115]\n"
WT"lgkmcnt(3)\n"
RT"a[166:167],v236 offset:45568\n"
MF"v[112:115],a[148:151],a[48:51],v[112:115]\n"
MF"v[120:123],a[148:151],a[52:55],v[120:123]\n"
RT"a[168:169],v237 offset:36864\n"
MF"v[120:123],a[144:147],a[36:39],v[120:123]\n"
MF"v[128:131],a[144:147],a[40:43],v[128:131]\n"
RT"a[170:171],v237 offset:37376\n"
MF"v[128:131],a[148:151],a[56:59],v[128:131]\n"
SA"s99,s99,4096\n"
RT"a[172:173],v237 offset:45056\n"
MF"v[136:139],a[148:151],a[60:63],v[136:139]\n"
"s_cmp_eq_u32 s99,20480\n"
BL"a[124:127],v235,s[8:11],s59 offen offset:3072 sc1\n"
MF"v[136:139],a[144:147],a[44:47],v[136:139]\n"
"s_cselect_b32 s99,0,s99\n"
RT"a[174:175],v237 offset:45568\n"
MF"v[116:119],a[152:155],a[32:35],v[116:119]\n"
MF"v[116:119],a[156:159],a[48:51],v[116:119]\n"
RT"a[176:177],v228 offset:36864\n"
MF"v[124:127],a[156:159],a[52:55],v[124:127]\n"
MF"v[124:127],a[152:155],a[36:39],v[124:127]\n"
RT"a[178:179],v228 offset:37376\n"
MF"v[132:135],a[152:155],a[40:43],v[132:135]\n"
"s_mov_b32 s49,266240\n"
"v_add_u32 v235,s99,v224\n"
MF"v[132:135],a[156:159],a[56:59],v[132:135]\n"
RT"a[180:181],v228 offset:45056\n"
MF"v[140:143],a[156:159],a[60:63],v[140:143]\n"
BL"a[0:3],v235,s[8:11],s56 offen offset:0 sc1\n"
MF"v[140:143],a[152:155],a[44:47],v[140:143]\n"
WT"lgkmcnt(3)\n"
BS"v[20:23],v250,s[8:11],s49 offen offset:0 sc1\n"
MF"v[144:147],a[160:163],a[32:35],v[144:147]\n"
RT"a[182:183],v228 offset:45568\n"
MF"v[144:147],a[164:167],a[48:51],v[144:147]\n"
RT"a[184:185],v229 offset:36864\n"
MF"v[152:155],a[164:167],a[52:55],v[152:155]\n"
MF"v[152:155],a[160:163],a[36:39],v[152:155]\n"
RT"a[186:187],v229 offset:37376\n"
MF"v[160:163],a[160:163],a[40:43],v[160:163]\n"
BL"a[4:7],v235,s[8:11],s57 offen offset:0 sc1\n"
MF"v[160:163],a[164:167],a[56:59],v[160:163]\n"
RT"a[188:189],v229 offset:45056\n"
MF"v[168:171],a[164:167],a[60:63],v[168:171]\n"
MF"v[168:171],a[160:163],a[44:47],v[168:171]\n"
RT"a[190:191],v229 offset:45568\n"
MF"v[148:151],a[168:171],a[32:35],v[148:151]\n"
"s_lshl_b32 s50,s35,3\n"
SA"s50,s50,s34\n"
"s_lshl_b32 s50,s50,3\n"
MF"v[148:151],a[172:175],a[48:51],v[148:151]\n"
SA"s50,s50,2304\n"
SA"s51,s50,s45\n"
MF"v[156:159],a[172:175],a[52:55],v[156:159]\n"
MF"v[156:159],a[168:171],a[36:39],v[156:159]\n"
BL"a[8:11],v235,s[8:11],s58 offen offset:0 sc1\n"
MF"v[164:167],a[168:171],a[40:43],v[164:167]\n"
MF"v[164:167],a[172:175],a[56:59],v[164:167]\n"
WT"vmcnt(29) lgkmcnt(0)\n"
"s_barrier\n"
MF"v[172:175],a[172:175],a[60:63],v[172:175]\n"
RT"a[128:129],v236 offset:0\n"
MF"v[172:175],a[168:171],a[44:47],v[172:175]\n"
"buffer_store_dwordx2 v[232:233],v230,s[8:11],s51 offen sc1\n"
MF"v[176:179],a[176:179],a[32:35],v[176:179]\n"
RT"a[130:131],v236 offset:512\n"
MF"v[176:179],a[180:183],a[48:51],v[176:179]\n"
MF"v[184:187],a[180:183],a[52:55],v[184:187]\n"
SA"m0,s38,81920\n"
RT"a[132:133],v236 offset:8192\n"
SA"s48,s60,s61\n"
MF"v[184:187],a[176:179],a[36:39],v[184:187]\n"
BL"a[12:15],v235,s[8:11],s59 offen offset:0 sc1\n"
MF"v[192:195],a[176:179],a[40:43],v[192:195]\n"
RT"a[134:135],v236 offset:8704\n"
MF"v[192:195],a[180:183],a[56:59],v[192:195]\n"
BL"v238,s[20:23],s48 offen offset:0 lds\n"
MF"v[200:203],a[180:183],a[60:63],v[200:203]\n"
RT"a[136:137],v237 offset:0\n"
MF"v[200:203],a[176:179],a[44:47],v[200:203]\n"
MF"v[180:183],a[184:187],a[32:35],v[180:183]\n"
RT"a[138:139],v237 offset:512\n"
MF"v[180:183],a[188:191],a[48:51],v[180:183]\n"
BL"a[16:19],v235,s[8:11],s56 offen offset:1024 sc1\n"
MF"v[188:191],a[188:191],a[52:55],v[188:191]\n"
RT"a[140:141],v237 offset:8192\n"
MF"v[188:191],a[184:187],a[36:39],v[188:191]\n"
MF"v[196:199],a[184:187],a[40:43],v[196:199]\n"
RT"a[142:143],v237 offset:8704\n"
MF"v[196:199],a[188:191],a[56:59],v[196:199]\n"
BL"v239,s[20:23],s48 offen offset:1024 lds\n"
MF"v[204:207],a[188:191],a[60:63],v[204:207]\n"
SA"s49,s88,262144\n"
RT"a[144:145],v228 offset:0\n"
MF"v[204:207],a[184:187],a[44:47],v[204:207]\n"
"s_branch LQ_%=\n"
"LV_%=:\n"
WT"vmcnt(17)\n"
"v_lshlrev_b32 v148,16,v128\n"
"v_and_b32 v149,0xffff0000,v128\n"
"v_lshlrev_b32 v150,16,v129\n"
"v_and_b32 v151,0xffff0000,v129\n"
WT"vmcnt(16)\n"
"v_lshlrev_b32 v152,16,v130\n"
"v_and_b32 v153,0xffff0000,v130\n"
"v_lshlrev_b32 v154,16,v131\n"
"v_and_b32 v155,0xffff0000,v131\n"
WT"vmcnt(15)\n"
"v_lshlrev_b32 v156,16,v132\n"
"v_and_b32 v157,0xffff0000,v132\n"
"v_lshlrev_b32 v158,16,v133\n"
"v_and_b32 v159,0xffff0000,v133\n"
WT"vmcnt(14)\n"
"v_lshlrev_b32 v160,16,v134\n"
"v_and_b32 v161,0xffff0000,v134\n"
"v_lshlrev_b32 v162,16,v135\n"
"v_and_b32 v163,0xffff0000,v135\n"
WT"vmcnt(13)\n"
"v_lshlrev_b32 v164,16,v136\n"
"v_and_b32 v165,0xffff0000,v136\n"
"v_lshlrev_b32 v166,16,v137\n"
"v_and_b32 v167,0xffff0000,v137\n"
"v_add_f32 v188,v148,v149\n"
"v_add_f32 v189,v150,v151\n"
"v_add_f32 v188,v188,v152\n"
"v_add_f32 v189,v189,v153\n"
"v_add_f32 v188,v188,v154\n"
"v_add_f32 v189,v189,v155\n"
"v_add_f32 v188,v188,v156\n"
"v_add_f32 v189,v189,v157\n"
"v_add_f32 v188,v188,v158\n"
"v_add_f32 v189,v189,v159\n"
"v_add_f32 v188,v188,v160\n"
"v_add_f32 v189,v189,v161\n"
"v_add_f32 v188,v188,v162\n"
"v_add_f32 v189,v189,v163\n"
"v_add_f32 v188,v188,v164\n"
"v_add_f32 v189,v189,v165\n"
"v_add_f32 v188,v188,v166\n"
"v_add_f32 v189,v189,v167\n"
"v_add_f32 v188,v188,v189\n"
"s_nop 1\n"
"v_add_f32_dpp v188,v188,v188 quad_perm:[1,0,3,2] row_mask:0xf bank_mask:0xf\n"
WT"vmcnt(12)\n"
"v_lshlrev_b32 v168,16,v138\n"
"v_and_b32 v169,0xffff0000,v138\n"
"v_add_f32_dpp v188,v188,v188 quad_perm:[2,3,0,1] row_mask:0xf bank_mask:0xf\n"
"v_lshlrev_b32 v170,16,v139\n"
"v_and_b32 v171,0xffff0000,v139\n"
"v_add_f32_dpp v188,v188,v188 row_half_mirror row_mask:0xf bank_mask:0xf\n"
WT"vmcnt(11)\n"
"v_lshlrev_b32 v172,16,v140\n"
"v_and_b32 v173,0xffff0000,v140\n"
"v_add_f32_dpp v188,v188,v188 row_mirror row_mask:0xf bank_mask:0xf\n"
"v_lshlrev_b32 v174,16,v141\n"
"v_and_b32 v175,0xffff0000,v141\n"
"v_mov_b32 v189,v188\n"
WT"vmcnt(10)\n"
"v_lshlrev_b32 v176,16,v142\n"
"v_and_b32 v177,0xffff0000,v142\n"
"v_permlane16_swap_b32 v189,v188\n"
"v_lshlrev_b32 v178,16,v143\n"
"v_and_b32 v179,0xffff0000,v143\n"
"v_add_f32 v188,v188,v189\n"
WT"vmcnt(9)\n"
"v_lshlrev_b32 v180,16,v144\n"
SA"m0,s36,0\n"
"s_bfe_u32 s48,s46,0x30000\n"
"s_lshl_b32 s48,s48,7\n"
BL"v234,s[12:15],s48 offen offset:0 lds\n"
BL"v235,s[12:15],s48 offen offset:1024 lds\n"
BL"v236,s[12:15],s48 offen offset:2048 lds\n"
BL"v237,s[12:15],s48 offen offset:3072 lds\n"
"v_and_b32 v181,0xffff0000,v144\n"
MU"v191,0x3acccccd,v188\n"
"v_lshlrev_b32 v182,16,v145\n"
"v_and_b32 v183,0xffff0000,v145\n"
PK"v128,v191,v191\n"
WT"vmcnt(12)\n"
"v_lshlrev_b32 v184,16,v146\n"
"v_and_b32 v185,0xffff0000,v146\n"
"v_lshlrev_b32 v128,16,v128\n"
"v_lshlrev_b32 v186,16,v147\n"
"v_and_b32 v187,0xffff0000,v147\n"
"v_sub_f32 v148,v148,v128\n"
"v_sub_f32 v149,v149,v128\n"
"v_sub_f32 v150,v150,v128\n"
"v_sub_f32 v151,v151,v128\n"
"v_sub_f32 v152,v152,v128\n"
"v_sub_f32 v153,v153,v128\n"
"v_sub_f32 v154,v154,v128\n"
"v_sub_f32 v155,v155,v128\n"
"v_sub_f32 v156,v156,v128\n"
"v_sub_f32 v157,v157,v128\n"
"v_sub_f32 v158,v158,v128\n"
"v_sub_f32 v159,v159,v128\n"
"v_sub_f32 v160,v160,v128\n"
"v_sub_f32 v161,v161,v128\n"
"v_sub_f32 v162,v162,v128\n"
"v_sub_f32 v163,v163,v128\n"
"v_sub_f32 v164,v164,v128\n"
"v_sub_f32 v165,v165,v128\n"
"v_sub_f32 v166,v166,v128\n"
"v_sub_f32 v167,v167,v128\n"
MU"v188,v148,v148\n"
MU"v189,v149,v149\n"
"v_fmac_f32 v188,v150,v150\n"
"v_fmac_f32 v189,v151,v151\n"
"v_fmac_f32 v188,v152,v152\n"
"v_fmac_f32 v189,v153,v153\n"
"v_fmac_f32 v188,v154,v154\n"
"v_fmac_f32 v189,v155,v155\n"
"v_fmac_f32 v188,v156,v156\n"
"v_fmac_f32 v189,v157,v157\n"
"v_fmac_f32 v188,v158,v158\n"
"v_fmac_f32 v189,v159,v159\n"
"v_fmac_f32 v188,v160,v160\n"
"v_fmac_f32 v189,v161,v161\n"
"v_fmac_f32 v188,v162,v162\n"
"v_fmac_f32 v189,v163,v163\n"
"v_fmac_f32 v188,v164,v164\n"
"v_fmac_f32 v189,v165,v165\n"
"v_fmac_f32 v188,v166,v166\n"
"v_fmac_f32 v189,v167,v167\n"
"v_add_f32 v188,v188,v189\n"
PK"v149,v148,v149\n"
"v_lshlrev_b32 v148,16,v149\n"
"v_add_f32_dpp v188,v188,v188 quad_perm:[1,0,3,2] row_mask:0xf bank_mask:0xf\n"
"v_and_b32 v149,0xffff0000,v149\n"
PK"v151,v150,v151\n"
"v_add_f32_dpp v188,v188,v188 quad_perm:[2,3,0,1] row_mask:0xf bank_mask:0xf\n"
"v_lshlrev_b32 v150,16,v151\n"
"v_and_b32 v151,0xffff0000,v151\n"
SA"m0,s37,8192\n"
"s_bfe_u32 s48,s46,0x30004\n"
"s_lshl_b32 s48,s48,19\n"
BL"v238,s[16:19],s48 offen offset:0 lds\n"
BL"v239,s[16:19],s48 offen offset:1024 lds\n"
BL"v240,s[16:19],s48 offen offset:2048 lds\n"
BL"v241,s[16:19],s48 offen offset:3072 lds\n"
"v_add_f32_dpp v188,v188,v188 row_half_mirror row_mask:0xf bank_mask:0xf\n"
PK"v153,v152,v153\n"
"v_lshlrev_b32 v152,16,v153\n"
"v_add_f32_dpp v188,v188,v188 row_mirror row_mask:0xf bank_mask:0xf\n"
"v_and_b32 v153,0xffff0000,v153\n"
PK"v155,v154,v155\n"
"v_mov_b32 v189,v188\n"
"v_lshlrev_b32 v154,16,v155\n"
"v_and_b32 v155,0xffff0000,v155\n"
"v_permlane16_swap_b32 v189,v188\n"
PK"v157,v156,v157\n"
"v_lshlrev_b32 v156,16,v157\n"
"v_add_f32 v188,v188,v189\n"
"v_and_b32 v157,0xffff0000,v157\n"
PK"v159,v158,v159\n"
"v_sub_f32 v191,v191,v128\n"
"v_lshlrev_b32 v158,16,v159\n"
"v_and_b32 v159,0xffff0000,v159\n"
MU"v188,0x3acccccd,v188\n"
PK"v161,v160,v161\n"
"v_lshlrev_b32 v160,16,v161\n"
"v_fma_f32 v188,-v191,v191,v188\n"
"v_and_b32 v161,0xffff0000,v161\n"
PK"v163,v162,v163\n"
"v_max_f32 v188,0,v188\n"
"v_lshlrev_b32 v162,16,v163\n"
"v_and_b32 v163,0xffff0000,v163\n"
PK"v188,v188,v188\n"
PK"v165,v164,v165\n"
"v_lshlrev_b32 v164,16,v165\n"
"v_lshlrev_b32 v188,16,v188\n"
"v_and_b32 v165,0xffff0000,v165\n"
PK"v167,v166,v167\n"
"v_add_f32 v188,0x3727c5ac,v188\n"
"v_lshlrev_b32 v166,16,v167\n"
"v_and_b32 v167,0xffff0000,v167\n"
PK"v188,v188,v188\n"
"v_bfe_u32 v130,v224,5,4\n"
"v_mul_u32_u24 v130,144,v130\n"
"v_lshlrev_b32 v188,16,v188\n"
"v_bfe_u32 v131,v224,4,1\n"
"v_lshl_add_u32 v130,v131,3,v130\n"
"v_sqrt_f32 v189,v188\n"
"v_lshrrev_b32 v131,9,v224\n"
"v_lshl_add_u32 v130,v131,4,v130\n"
"v_rcp_f32 v190,v189\n"
"s_lshl_b32 s52,s30,5\n"
"v_add_u32 v130,s52,v130\n"
"v_fma_f32 v188,-v189,v189,v188\n"
MU"v190,0.5,v190\n"
"v_fma_f32 v189,v188,v190,v189\n"
PK"v189,v189,v189\n"
"v_lshlrev_b32 v189,16,v189\n"
"v_rcp_f32 v129,v189\n"
"s_nop 0\n"
MU"v148,v148,v129\n"
MU"v149,v149,v129\n"
PK"v138,v148,v149\n"
MU"v150,v150,v129\n"
MU"v151,v151,v129\n"
SA"m0,s37,12288\n"
"s_bfe_u32 s48,s46,0x30004\n"
"s_lshl_b32 s48,s48,19\n"
"s_bitset1_b32 s48,18\n"
BL"v238,s[16:19],s48 offen offset:0 lds\n"
BL"v239,s[16:19],s48 offen offset:1024 lds\n"
BL"v240,s[16:19],s48 offen offset:2048 lds\n"
BL"v241,s[16:19],s48 offen offset:3072 lds\n"
PK"v139,v150,v151\n"
"ds_write_b64 v130,v[138:139] offset:49152\n"
MU"v152,v152,v129\n"
MU"v153,v153,v129\n"
PK"v140,v152,v153\n"
MU"v154,v154,v129\n"
MU"v155,v155,v129\n"
PK"v141,v154,v155\n"
"ds_write_b64 v130,v[140:141] offset:51456\n"
MU"v156,v156,v129\n"
MU"v157,v157,v129\n"
PK"v142,v156,v157\n"
MU"v158,v158,v129\n"
MU"v159,v159,v129\n"
PK"v143,v158,v159\n"
"ds_write_b64 v130,v[142:143] offset:53760\n"
MU"v160,v160,v129\n"
MU"v161,v161,v129\n"
PK"v144,v160,v161\n"
MU"v162,v162,v129\n"
MU"v163,v163,v129\n"
PK"v145,v162,v163\n"
"ds_write_b64 v130,v[144:145] offset:56064\n"
MU"v164,v164,v129\n"
MU"v165,v165,v129\n"
PK"v146,v164,v165\n"
MU"v166,v166,v129\n"
MU"v167,v167,v129\n"
PK"v147,v166,v167\n"
"ds_write_b64 v130,v[146:147] offset:58368\n"
"v_lshlrev_b32 v148,16,v138\n"
"v_and_b32 v149,0xffff0000,v138\n"
MU"v188,v148,v168\n"
MU"v189,v149,v169\n"
"v_lshlrev_b32 v150,16,v139\n"
"v_and_b32 v151,0xffff0000,v139\n"
"v_fmac_f32 v188,v150,v170\n"
"v_fmac_f32 v189,v151,v171\n"
"v_lshlrev_b32 v152,16,v140\n"
"v_and_b32 v153,0xffff0000,v140\n"
"v_fmac_f32 v188,v152,v172\n"
"v_fmac_f32 v189,v153,v173\n"
"v_lshlrev_b32 v154,16,v141\n"
"v_and_b32 v155,0xffff0000,v141\n"
"v_fmac_f32 v188,v154,v174\n"
"v_fmac_f32 v189,v155,v175\n"
"v_lshlrev_b32 v156,16,v142\n"
"v_and_b32 v157,0xffff0000,v142\n"
"v_fmac_f32 v188,v156,v176\n"
"v_fmac_f32 v189,v157,v177\n"
"v_lshlrev_b32 v158,16,v143\n"
"v_and_b32 v159,0xffff0000,v143\n"
"v_fmac_f32 v188,v158,v178\n"
"v_fmac_f32 v189,v159,v179\n"
"v_lshlrev_b32 v160,16,v144\n"
"v_and_b32 v161,0xffff0000,v144\n"
"v_fmac_f32 v188,v160,v180\n"
"v_fmac_f32 v189,v161,v181\n"
"v_lshlrev_b32 v162,16,v145\n"
"v_and_b32 v163,0xffff0000,v145\n"
WT"lgkmcnt(0)\n"
SA"s28,s28,0x7f4a7c15\n"
"s_addc_u32 s29,s29,0x9e3779b9\n"
"v_mov_b32 v232,s28\n"
"v_mov_b32 v233,s29\n"
WT"vmcnt(16)\n"
RT"a[0:1],v246 offset:0\n"
RT"a[2:3],v246 offset:512\n"
RT"a[4:5],v247 offset:0\n"
RT"a[6:7],v247 offset:512\n"
RT"a[8:9],v248 offset:0\n"
RT"a[10:11],v248 offset:512\n"
RT"a[12:13],v249 offset:0\n"
RT"a[14:15],v249 offset:512\n"
WT"vmcnt(12)\n"
RT"a[16:17],v246 offset:4096\n"
RT"a[18:19],v246 offset:4608\n"
RT"a[20:21],v247 offset:4096\n"
RT"a[22:23],v247 offset:4608\n"
RT"a[24:25],v248 offset:4096\n"
RT"a[26:27],v248 offset:4608\n"
RT"a[28:29],v249 offset:4096\n"
RT"a[30:31],v249 offset:4608\n"
WT"vmcnt(8) lgkmcnt(15)\n"
"s_barrier\n"
RD"v[192:195],v242 offset:0\n"
RD"v[196:199],v243 offset:0\n"
RD"v[200:203],v242 offset:2048\n"
RD"v[204:207],v243 offset:2048\n"
RD"v[208:211],v242 offset:4096\n"
"v_lshrrev_b32 v131,7,v224\n"
WT"lgkmcnt(4)\n"
MF"v[0:3],v[192:195],a[0:3],0\n"
WT"lgkmcnt(3)\n"
RD"v[212:215],v243 offset:4096\n"
"v_lshl_add_u32 v131,v131,4,v224\n"
MF"v[0:3],v[196:199],a[16:19],v[0:3]\n"
SA"m0,s36,16384\n"
"s_bfe_u32 s48,s46,0x30004\n"
"s_lshl_b32 s48,s48,7\n"
MF"v[8:11],v[196:199],a[20:23],0\n"
"s_mul_i32 s52,s30,1152\n"
BL"v234,s[12:15],s48 offen offset:0 lds\n"
MF"v[8:11],v[192:195],a[4:7],v[8:11]\n"
"v_add_u32 v128,s52,v131\n"
"s_and_b32 s52,s30,1\n"
MF"v[16:19],v[192:195],a[8:11],0\n"
"s_mul_i32 s51,s52,1152\n"
RD"v[216:219],v242 offset:6144\n"
"v_add_u32 v131,s51,v131\n"
MF"v[16:19],v[196:199],a[24:27],v[16:19]\n"
"s_lshl_b32 s52,s52,10\n"
"v_fmac_f32 v188,v162,v182\n"
"v_fmac_f32 v189,v163,v183\n"
MF"v[24:27],v[196:199],a[28:31],0\n"
BL"v235,s[12:15],s48 offen offset:1024 lds\n"
MF"v[24:27],v[192:195],a[12:15],v[24:27]\n"
WT"lgkmcnt(1)\n"
RD"v[220:223],v243 offset:6144\n"
RD"v[132:135],v128 offset:49152\n"
MF"v[4:7],v[200:203],a[0:3],0\n"
WT"vmcnt(6)\n"
RT"a[32:33],v246 offset:8192\n"
RD"a[240:243],v128 offset:53760\n"
MF"v[4:7],v[204:207],a[16:19],v[4:7]\n"
"s_lshr_b32 s50,s31,1\n"
RD"a[244:247],v131 offset:58368\n"
"v_lshlrev_b32 v164,16,v146\n"
MF"v[12:15],v[204:207],a[20:23],0\n"
"s_mul_i32 s50,s50,20480\n"
"v_and_b32 v129,0x70,v224\n"
"v_and_b32 v165,0xffff0000,v146\n"
MF"v[12:15],v[200:203],a[4:7],v[12:15]\n"
"s_and_b32 s51,s31,1\n"
RD"v[192:195],v242 offset:8192\n"
RT"a[34:35],v246 offset:8704\n"
MF"v[20:23],v[200:203],a[8:11],0\n"
"s_lshl_b32 s51,s51,7\n"
BL"v236,s[12:15],s48 offen offset:2048 lds\n"
MF"v[20:23],v[204:207],a[24:27],v[20:23]\n"
SA"s50,s50,s51\n"
"v_and_b32 v130,0x380,v224\n"
"v_fmac_f32 v188,v164,v184\n"
MF"v[28:31],v[204:207],a[28:31],0\n"
SA"s50,s50,524288\n"
RT"a[36:37],v247 offset:8192\n"
"v_lshl_add_u32 v129,v130,1,v129\n"
MF"v[28:31],v[200:203],a[12:15],v[28:31]\n"
"s_lshl_b32 s52,s52,1\n"
RD"v[196:199],v243 offset:8192\n"
"v_fmac_f32 v189,v165,v185\n"
MF"v[32:35],v[208:211],a[0:3],0\n"
SA"s52,s52,s50\n"
BL"v237,s[12:15],s48 offen offset:3072 lds\n"
MF"v[32:35],v[212:215],a[16:19],v[32:35]\n"
SA"s52,s52,16384\n"
RT"a[38:39],v247 offset:8704\n"
"v_lshlrev_b32 v166,16,v147\n"
MF"v[40:43],v[212:215],a[20:23],0\n"
SA"m0,s37,16384\n"
RD"v[200:203],v242 offset:10240\n"
"v_and_b32 v167,0xffff0000,v147\n"
MF"v[40:43],v[208:211],a[4:7],v[40:43]\n"
"s_bfe_u32 s48,s46,0x30008\n"
"v_fmac_f32 v188,v166,v186\n"
"v_fmac_f32 v189,v167,v187\n"
MF"v[48:51],v[208:211],a[8:11],0\n"
"s_lshl_b32 s48,s48,19\n"
RT"a[40:41],v248 offset:8192\n"
"v_add_f32 v188,v188,v189\n"
MF"v[48:51],v[212:215],a[24:27],v[48:51]\n"
"s_lshl_b32 s51,s30,11\n"
BL"v238,s[16:19],s48 offen offset:0 lds\n"
MF"v[56:59],v[212:215],a[28:31],0\n"
SA"s50,s50,s51\n"
RD"v[204:207],v243 offset:10240\n"
"v_add_f32_dpp v188,v188,v188 quad_perm:[1,0,3,2] row_mask:0xf bank_mask:0xf\n"
MF"v[56:59],v[208:211],a[12:15],v[56:59]\n"
WT"lgkmcnt(4)\n"
BS"v[132:135],v129,s[8:11],s50 offen sc1\n"
MF"v[36:39],v[216:219],a[0:3],0\n"
SA"s51,s50,8192\n"
RT"a[42:43],v248 offset:8704\n"
"v_add_f32_dpp v188,v188,v188 quad_perm:[2,3,0,1] row_mask:0xf bank_mask:0xf\n"
MF"v[36:39],v[220:223],a[16:19],v[36:39]\n"
"s_lshl_b32 s90,s31,2\n"
BL"v239,s[16:19],s48 offen offset:1024 lds\n"
MF"v[44:47],v[220:223],a[20:23],0\n"
SA"s90,s90,s30\n"
BS"a[240:243],v129,s[8:11],s51 offen sc1\n"
MF"v[44:47],v[216:219],a[4:7],v[44:47]\n"
"s_lshl_b32 s90,s90,1\n"
RD"v[208:211],v242 offset:12288\n"
RT"a[44:45],v249 offset:8192\n"
MF"v[52:55],v[216:219],a[8:11],0\n"
"s_sub_u32 s91,2047,s90\n"
BS"a[244:247],v129,s[8:11],s52 offen sc1\n"
MF"v[52:55],v[220:223],a[24:27],v[52:55]\n"
RD"v[212:215],v243 offset:12288\n"
RT"a[46:47],v249 offset:8704\n"
MF"v[60:63],v[220:223],a[28:31],0\n"
BL"v240,s[16:19],s48 offen offset:2048 lds\n"
MF"v[60:63],v[216:219],a[12:15],v[60:63]\n"
"v_add_f32_dpp v188,v188,v188 row_half_mirror row_mask:0xf bank_mask:0xf\n"
MF"v[64:67],v[192:195],a[0:3],0\n"
RD"v[216:219],v242 offset:14336\n"
"v_add_f32_dpp v188,v188,v188 row_mirror row_mask:0xf bank_mask:0xf\n"
MF"v[64:67],v[196:199],a[16:19],v[64:67]\n"
WT"vmcnt(10)\n"
RT"a[48:49],v246 offset:12288\n"
"v_mov_b32 v189,v188\n"
MF"v[72:75],v[196:199],a[20:23],0\n"
"s_lshl_b32 s90,s90,2\n"
BL"v241,s[16:19],s48 offen offset:3072 lds\n"
MF"v[72:75],v[192:195],a[4:7],v[72:75]\n"
SA"s90,s90,8192\n"
RT"a[50:51],v246 offset:12800\n"
"v_permlane16_swap_b32 v189,v188\n"
MF"v[80:83],v[192:195],a[8:11],0\n"
SA"m0,s37,20480\n"
RD"v[220:223],v243 offset:14336\n"
"v_add_f32 v188,v188,v189\n"
MF"v[80:83],v[196:199],a[24:27],v[80:83]\n"
"s_bfe_u32 s48,s46,0x30008\n"
PK"v188,v188,v188\n"
"v_lshlrev_b32 v188,16,v188\n"
MF"v[88:91],v[196:199],a[28:31],0\n"
"s_lshl_b32 s48,s48,19\n"
RT"a[52:53],v247 offset:12288\n"
"v_ashrrev_i32 v189,31,v188\n"
MF"v[88:91],v[192:195],a[12:15],v[88:91]\n"
WT"lgkmcnt(4)\n"
"v_or_b32 v189,0x80000000,v189\n"
"v_xor_b32 v188,v188,v189\n"
MF"v[68:71],v[200:203],a[0:3],0\n"
"s_bitset1_b32 s48,18\n"
"v_and_b32 v188,0xffff0000,v188\n"
"v_lshrrev_b32 v189,9,v224\n"
MF"v[68:71],v[204:207],a[16:19],v[68:71]\n"
BL"v238,s[16:19],s48 offen offset:0 lds\n"
MF"v[76:79],v[204:207],a[20:23],0\n"
RT"a[54:55],v247 offset:12800\n"
"v_sub_u32 v190,s91,v189\n"
MF"v[76:79],v[200:203],a[4:7],v[76:79]\n"
BL"v239,s[16:19],s48 offen offset:1024 lds\n"
MF"v[84:87],v[200:203],a[8:11],0\n"
RT"a[56:57],v248 offset:12288\n"
"v_or_b32 v188,v188,v190\n"
MF"v[84:87],v[204:207],a[24:27],v[84:87]\n"
WT"vmcnt(9) lgkmcnt(3)\n"
"s_barrier\n"
MF"v[92:95],v[204:207],a[28:31],0\n"
RD"v[192:195],v242 offset:16384\n"
"v_and_b32 v190,0x1f0,v224\n"
MF"v[92:95],v[200:203],a[12:15],v[92:95]\n"
RT"a[58:59],v248 offset:12800\n"
"v_lshlrev_b32 v190,23,v190\n"
MF"v[96:99],v[208:211],a[0:3],0\n"
BL"v240,s[16:19],s48 offen offset:2048 lds\n"
MF"v[96:99],v[212:215],a[16:19],v[96:99]\n"
"v_lshl_add_u32 v190,v189,2,v190\n"
MF"v[104:107],v[212:215],a[20:23],0\n"
"buffer_store_dword v188,v190,s[8:11],s90 offen sc1\n"
MF"v[104:107],v[208:211],a[4:7],v[104:107]\n"
RD"v[196:199],v243 offset:16384\n"
RT"a[60:61],v249 offset:12288\n"
MF"v[112:115],v[208:211],a[8:11],0\n"
BL"v241,s[16:19],s48 offen offset:3072 lds\n"
MF"v[112:115],v[212:215],a[24:27],v[112:115]\n"
RT"a[62:63],v249 offset:12800\n"
MF"v[120:123],v[212:215],a[28:31],0\n"
SA"m0,s36,32768\n"
RD"v[200:203],v242 offset:18432\n"
"s_bfe_u32 s48,s46,0x30008\n"
MF"v[120:123],v[208:211],a[12:15],v[120:123]\n"
"s_lshl_b32 s48,s48,7\n"
MF"v[100:103],v[216:219],a[0:3],0\n"
BL"v234,s[12:15],s48 offen offset:0 lds\n"
MF"v[100:103],v[220:223],a[16:19],v[100:103]\n"
RD"v[204:207],v243 offset:18432\n"
MF"v[108:111],v[220:223],a[20:23],0\n"
MF"v[108:111],v[216:219],a[4:7],v[108:111]\n"
MF"v[116:119],v[216:219],a[8:11],0\n"
BL"v235,s[12:15],s48 offen offset:1024 lds\n"
MF"v[116:119],v[220:223],a[24:27],v[116:119]\n"
RD"v[208:211],v242 offset:20480\n"
MF"v[124:127],v[220:223],a[28:31],0\n"
MF"v[124:127],v[216:219],a[12:15],v[124:127]\n"
WT"lgkmcnt(2)\n"
MF"v[0:3],v[192:195],a[32:35],v[0:3]\n"
RD"v[212:215],v243 offset:20480\n"
MF"v[0:3],v[196:199],a[48:51],v[0:3]\n"
BL"v236,s[12:15],s48 offen offset:2048 lds\n"
MF"v[8:11],v[196:199],a[52:55],v[8:11]\n"
MF"v[8:11],v[192:195],a[36:39],v[8:11]\n"
MF"v[16:19],v[192:195],a[40:43],v[16:19]\n"
RD"v[216:219],v242 offset:22528\n"
MF"v[16:19],v[196:199],a[56:59],v[16:19]\n"
BL"v237,s[12:15],s48 offen offset:3072 lds\n"
MF"v[24:27],v[196:199],a[60:63],v[24:27]\n"
MF"v[24:27],v[192:195],a[44:47],v[24:27]\n"
RD"v[220:223],v243 offset:22528\n"
MF"v[4:7],v[200:203],a[32:35],v[4:7]\n"
WT"lgkmcnt(2)\n"
WT"vmcnt(9)\n"
RT"a[64:65],v246 offset:16384\n"
MF"v[4:7],v[204:207],a[48:51],v[4:7]\n"
SA"m0,s37,0\n"
"s_bfe_u32 s48,s46,0x3000c\n"
"s_lshl_b32 s48,s48,19\n"
MF"v[12:15],v[204:207],a[52:55],v[12:15]\n"
BL"v238,s[16:19],s48 offen offset:0 lds\n"
MF"v[12:15],v[200:203],a[36:39],v[12:15]\n"
RD"v[192:195],v242 offset:24576\n"
RT"a[66:67],v246 offset:16896\n"
MF"v[20:23],v[200:203],a[40:43],v[20:23]\n"
MF"v[20:23],v[204:207],a[56:59],v[20:23]\n"
MF"v[28:31],v[204:207],a[60:63],v[28:31]\n"
RT"a[68:69],v247 offset:16384\n"
MF"v[28:31],v[200:203],a[44:47],v[28:31]\n"
BL"v239,s[16:19],s48 offen offset:1024 lds\n"
MF"v[32:35],v[208:211],a[32:35],v[32:35]\n"
RD"v[196:199],v243 offset:24576\n"
MF"v[32:35],v[212:215],a[48:51],v[32:35]\n"
RT"a[70:71],v247 offset:16896\n"
MF"v[40:43],v[212:215],a[52:55],v[40:43]\n"
RD"v[200:203],v242 offset:26624\n"
MF"v[40:43],v[208:211],a[36:39],v[40:43]\n"
BL"v240,s[16:19],s48 offen offset:2048 lds\n"
MF"v[48:51],v[208:211],a[40:43],v[48:51]\n"
RT"a[72:73],v248 offset:16384\n"
MF"v[48:51],v[212:215],a[56:59],v[48:51]\n"
MF"v[56:59],v[212:215],a[60:63],v[56:59]\n"
RD"v[204:207],v243 offset:26624\n"
MF"v[56:59],v[208:211],a[44:47],v[56:59]\n"
WT"lgkmcnt(4)\n"
RT"a[74:75],v248 offset:16896\n"
MF"v[36:39],v[216:219],a[32:35],v[36:39]\n"
BL"v241,s[16:19],s48 offen offset:3072 lds\n"
MF"v[36:39],v[220:223],a[48:51],v[36:39]\n"
RD"v[208:211],v242 offset:28672\n"
MF"v[44:47],v[220:223],a[52:55],v[44:47]\n"
SA"m0,s37,4096\n"
RT"a[76:77],v249 offset:16384\n"
"s_bfe_u32 s48,s46,0x3000c\n"
MF"v[44:47],v[216:219],a[36:39],v[44:47]\n"
"s_lshl_b32 s48,s48,19\n"
"s_bitset1_b32 s48,18\n"
MF"v[52:55],v[216:219],a[40:43],v[52:55]\n"
BL"v238,s[16:19],s48 offen offset:0 lds\n"
MF"v[52:55],v[220:223],a[56:59],v[52:55]\n"
RD"v[212:215],v243 offset:28672\n"
RT"a[78:79],v249 offset:16896\n"
MF"v[60:63],v[220:223],a[60:63],v[60:63]\n"
MF"v[60:63],v[216:219],a[44:47],v[60:63]\n"
MF"v[64:67],v[192:195],a[32:35],v[64:67]\n"
RD"v[216:219],v242 offset:30720\n"
MF"v[64:67],v[196:199],a[48:51],v[64:67]\n"
WT"vmcnt(9)\n"
RT"a[80:81],v246 offset:20480\n"
MF"v[72:75],v[196:199],a[52:55],v[72:75]\n"
BL"v239,s[16:19],s48 offen offset:1024 lds\n"
MF"v[72:75],v[192:195],a[36:39],v[72:75]\n"
RT"a[82:83],v246 offset:20992\n"
MF"v[80:83],v[192:195],a[40:43],v[80:83]\n"
RD"v[220:223],v243 offset:30720\n"
MF"v[80:83],v[196:199],a[56:59],v[80:83]\n"
BL"v240,s[16:19],s48 offen offset:2048 lds\n"
MF"v[88:91],v[196:199],a[60:63],v[88:91]\n"
RT"a[84:85],v247 offset:20480\n"
MF"v[88:91],v[192:195],a[44:47],v[88:91]\n"
WT"lgkmcnt(4)\n"
MF"v[68:71],v[200:203],a[32:35],v[68:71]\n"
"s_lshl_b32 s51,s32,8\n"
"s_andn2_b32 s50,s31,7\n"
SA"s50,s50,s51\n"
MF"v[68:71],v[204:207],a[48:51],v[68:71]\n"
SA"s50,s50,256\n"
RT"a[86:87],v247 offset:20992\n"
SA"s50,s50,s45\n"
MF"v[76:79],v[204:207],a[52:55],v[76:79]\n"
BL"v241,s[16:19],s48 offen offset:3072 lds\n"
MF"v[76:79],v[200:203],a[36:39],v[76:79]\n"
MF"v[84:87],v[200:203],a[40:43],v[84:87]\n"
SA"m0,s36,0\n"
RT"a[88:89],v248 offset:20480\n"
"s_bfe_u32 s48,s46,0x3000c\n"
MF"v[84:87],v[204:207],a[56:59],v[84:87]\n"
"s_lshl_b32 s48,s48,7\n"
WT"vmcnt(8) lgkmcnt(3)\n"
"s_barrier\n"
MF"v[92:95],v[204:207],a[60:63],v[92:95]\n"
RD"v[192:195],v242 offset:32768\n"
MF"v[92:95],v[200:203],a[44:47],v[92:95]\n"
BL"v234,s[12:15],s48 offen offset:0 lds\n"
MF"v[96:99],v[208:211],a[32:35],v[96:99]\n"
"buffer_store_dwordx2 v[232:233],v230,s[8:11],s50 offen sc1\n"
MF"v[96:99],v[212:215],a[48:51],v[96:99]\n"
RT"a[90:91],v248 offset:20992\n"
MF"v[104:107],v[212:215],a[52:55],v[104:107]\n"
RD"v[196:199],v243 offset:32768\n"
RT"a[92:93],v249 offset:20480\n"
MF"v[104:107],v[208:211],a[36:39],v[104:107]\n"
BL"v235,s[12:15],s48 offen offset:1024 lds\n"
MF"v[112:115],v[208:211],a[40:43],v[112:115]\n"
MF"v[112:115],v[212:215],a[56:59],v[112:115]\n"
RT"a[94:95],v249 offset:20992\n"
MF"v[120:123],v[212:215],a[60:63],v[120:123]\n"
RD"v[200:203],v242 offset:34816\n"
MF"v[120:123],v[208:211],a[44:47],v[120:123]\n"
BL"v236,s[12:15],s48 offen offset:2048 lds\n"
MF"v[100:103],v[216:219],a[32:35],v[100:103]\n"
MF"v[100:103],v[220:223],a[48:51],v[100:103]\n"
RD"v[204:207],v243 offset:34816\n"
MF"v[108:111],v[220:223],a[52:55],v[108:111]\n"
MF"v[108:111],v[216:219],a[36:39],v[108:111]\n"
BL"v237,s[12:15],s48 offen offset:3072 lds\n"
MF"v[116:119],v[216:219],a[40:43],v[116:119]\n"
MF"v[116:119],v[220:223],a[56:59],v[116:119]\n"
RD"v[208:211],v242 offset:36864\n"
MF"v[124:127],v[220:223],a[60:63],v[124:127]\n"
SA"m0,s37,8192\n"
"s_bfe_u32 s48,s46,0x30010\n"
"s_lshl_b32 s48,s48,19\n"
MF"v[124:127],v[216:219],a[44:47],v[124:127]\n"
WT"lgkmcnt(2)\n"
MF"v[0:3],v[192:195],a[64:67],v[0:3]\n"
RD"v[212:215],v243 offset:36864\n"
MF"v[0:3],v[196:199],a[80:83],v[0:3]\n"
BL"v238,s[16:19],s48 offen offset:0 lds\n"
MF"v[8:11],v[196:199],a[84:87],v[8:11]\n"
MF"v[8:11],v[192:195],a[68:71],v[8:11]\n"
MF"v[16:19],v[192:195],a[72:75],v[16:19]\n"
RD"v[216:219],v242 offset:38912\n"
MF"v[16:19],v[196:199],a[88:91],v[16:19]\n"
BL"v239,s[16:19],s48 offen offset:1024 lds\n"
MF"v[24:27],v[196:199],a[92:95],v[24:27]\n"
MF"v[24:27],v[192:195],a[76:79],v[24:27]\n"
RD"v[220:223],v243 offset:38912\n"
MF"v[4:7],v[200:203],a[64:67],v[4:7]\n"
WT"lgkmcnt(2)\n"
WT"vmcnt(11)\n"
RT"a[96:97],v246 offset:0\n"
MF"v[4:7],v[204:207],a[80:83],v[4:7]\n"
BL"v240,s[16:19],s48 offen offset:2048 lds\n"
MF"v[12:15],v[204:207],a[84:87],v[12:15]\n"
MF"v[12:15],v[200:203],a[68:71],v[12:15]\n"
RD"v[192:195],v242 offset:40960\n"
MF"v[20:23],v[200:203],a[72:75],v[20:23]\n"
RT"a[98:99],v246 offset:512\n"
MF"v[20:23],v[204:207],a[88:91],v[20:23]\n"
BL"v241,s[16:19],s48 offen offset:3072 lds\n"
MF"v[28:31],v[204:207],a[92:95],v[28:31]\n"
RT"a[100:101],v247 offset:0\n"
MF"v[28:31],v[200:203],a[76:79],v[28:31]\n"
RD"v[196:199],v243 offset:40960\n"
MF"v[32:35],v[208:211],a[64:67],v[32:35]\n"
SA"m0,s37,12288\n"
"s_bfe_u32 s48,s46,0x30010\n"
"s_lshl_b32 s48,s48,19\n"
MF"v[32:35],v[212:215],a[80:83],v[32:35]\n"
"s_bitset1_b32 s48,18\n"
RT"a[102:103],v247 offset:512\n"
MF"v[40:43],v[212:215],a[84:87],v[40:43]\n"
RD"v[200:203],v242 offset:43008\n"
MF"v[40:43],v[208:211],a[68:71],v[40:43]\n"
BL"v238,s[16:19],s48 offen offset:0 lds\n"
MF"v[48:51],v[208:211],a[72:75],v[48:51]\n"
RT"a[104:105],v248 offset:0\n"
MF"v[48:51],v[212:215],a[88:91],v[48:51]\n"
MF"v[56:59],v[212:215],a[92:95],v[56:59]\n"
RD"v[204:207],v243 offset:43008\n"
MF"v[56:59],v[208:211],a[76:79],v[56:59]\n"
WT"lgkmcnt(4)\n"
BL"v239,s[16:19],s48 offen offset:1024 lds\n"
MF"v[36:39],v[216:219],a[64:67],v[36:39]\n"
RT"a[106:107],v248 offset:512\n"
MF"v[36:39],v[220:223],a[80:83],v[36:39]\n"
RD"v[208:211],v242 offset:45056\n"
MF"v[44:47],v[220:223],a[84:87],v[44:47]\n"
RT"a[108:109],v249 offset:0\n"
MF"v[44:47],v[216:219],a[68:71],v[44:47]\n"
BL"v240,s[16:19],s48 offen offset:2048 lds\n"
MF"v[52:55],v[216:219],a[72:75],v[52:55]\n"
MF"v[52:55],v[220:223],a[88:91],v[52:55]\n"
RD"v[212:215],v243 offset:45056\n"
MF"v[60:63],v[220:223],a[92:95],v[60:63]\n"
RT"a[110:111],v249 offset:512\n"
MF"v[60:63],v[216:219],a[76:79],v[60:63]\n"
BL"v241,s[16:19],s48 offen offset:3072 lds\n"
MF"v[64:67],v[192:195],a[64:67],v[64:67]\n"
RD"v[216:219],v242 offset:47104\n"
MF"v[64:67],v[196:199],a[80:83],v[64:67]\n"
WT"vmcnt(13)\n"
RT"a[112:113],v246 offset:4096\n"
MF"v[72:75],v[196:199],a[84:87],v[72:75]\n"
SA"m0,s36,16384\n"
"s_bfe_u32 s48,s46,0x30010\n"
"s_lshl_b32 s48,s48,7\n"
MF"v[72:75],v[192:195],a[68:71],v[72:75]\n"
RT"a[114:115],v246 offset:4608\n"
MF"v[80:83],v[192:195],a[72:75],v[80:83]\n"
RD"v[220:223],v243 offset:47104\n"
MF"v[80:83],v[196:199],a[88:91],v[80:83]\n"
BL"v234,s[12:15],s48 offen offset:0 lds\n"
MF"v[88:91],v[196:199],a[92:95],v[88:91]\n"
RT"a[116:117],v247 offset:4096\n"
MF"v[88:91],v[192:195],a[76:79],v[88:91]\n"
WT"lgkmcnt(4)\n"
MF"v[68:71],v[200:203],a[64:67],v[68:71]\n"
BL"v235,s[12:15],s48 offen offset:1024 lds\n"
MF"v[68:71],v[204:207],a[80:83],v[68:71]\n"
RT"a[118:119],v247 offset:4608\n"
MF"v[76:79],v[204:207],a[84:87],v[76:79]\n"
MF"v[76:79],v[200:203],a[68:71],v[76:79]\n"
MF"v[84:87],v[200:203],a[72:75],v[84:87]\n"
RT"a[120:121],v248 offset:4096\n"
MF"v[84:87],v[204:207],a[88:91],v[84:87]\n"
WT"vmcnt(10) lgkmcnt(3)\n"
"s_barrier\n"
MF"v[92:95],v[204:207],a[92:95],v[92:95]\n"
BL"v236,s[12:15],s48 offen offset:2048 lds\n"
MF"v[92:95],v[200:203],a[76:79],v[92:95]\n"
RD"v[192:195],v242 offset:0\n"
RT"a[122:123],v248 offset:4608\n"
MF"v[96:99],v[208:211],a[64:67],v[96:99]\n"
MF"v[96:99],v[212:215],a[80:83],v[96:99]\n"
BL"v237,s[12:15],s48 offen offset:3072 lds\n"
MF"v[104:107],v[212:215],a[84:87],v[104:107]\n"
RD"v[196:199],v243 offset:0\n"
RT"a[124:125],v249 offset:4096\n"
MF"v[104:107],v[208:211],a[68:71],v[104:107]\n"
MF"v[112:115],v[208:211],a[72:75],v[112:115]\n"
SA"m0,s37,16384\n"
"s_bfe_u32 s48,s46,0x30014\n"
"s_lshl_b32 s48,s48,19\n"
MF"v[112:115],v[212:215],a[88:91],v[112:115]\n"
RT"a[126:127],v249 offset:4608\n"
MF"v[120:123],v[212:215],a[92:95],v[120:123]\n"
RD"v[200:203],v242 offset:2048\n"
MF"v[120:123],v[208:211],a[76:79],v[120:123]\n"
BL"v238,s[16:19],s48 offen offset:0 lds\n"
MF"v[100:103],v[216:219],a[64:67],v[100:103]\n"
MF"v[100:103],v[220:223],a[80:83],v[100:103]\n"
RD"v[204:207],v243 offset:2048\n"
MF"v[108:111],v[220:223],a[84:87],v[108:111]\n"
BL"v239,s[16:19],s48 offen offset:1024 lds\n"
MF"v[108:111],v[216:219],a[68:71],v[108:111]\n"
MF"v[116:119],v[216:219],a[72:75],v[116:119]\n"
MF"v[116:119],v[220:223],a[88:91],v[116:119]\n"
RD"v[208:211],v242 offset:4096\n"
MF"v[124:127],v[220:223],a[92:95],v[124:127]\n"
MF"v[124:127],v[216:219],a[76:79],v[124:127]\n"
WT"lgkmcnt(2)\n"
BL"v240,s[16:19],s48 offen offset:2048 lds\n"
MF"v[0:3],v[192:195],a[96:99],v[0:3]\n"
RD"v[212:215],v243 offset:4096\n"
MF"v[0:3],v[196:199],a[112:115],v[0:3]\n"
MF"v[8:11],v[196:199],a[116:119],v[8:11]\n"
MF"v[8:11],v[192:195],a[100:103],v[8:11]\n"
BL"v241,s[16:19],s48 offen offset:3072 lds\n"
MF"v[16:19],v[192:195],a[104:107],v[16:19]\n"
RD"v[216:219],v242 offset:6144\n"
MF"v[16:19],v[196:199],a[120:123],v[16:19]\n"
MF"v[24:27],v[196:199],a[124:127],v[24:27]\n"
SA"m0,s37,20480\n"
"s_bfe_u32 s48,s46,0x30014\n"
"s_lshl_b32 s48,s48,19\n"
MF"v[24:27],v[192:195],a[108:111],v[24:27]\n"
"s_bitset1_b32 s48,18\n"
RD"v[220:223],v243 offset:6144\n"
MF"v[4:7],v[200:203],a[96:99],v[4:7]\n"
WT"lgkmcnt(2)\n"
WT"vmcnt(12)\n"
RT"a[128:129],v246 offset:8192\n"
MF"v[4:7],v[204:207],a[112:115],v[4:7]\n"
BL"v238,s[16:19],s48 offen offset:0 lds\n"
MF"v[12:15],v[204:207],a[116:119],v[12:15]\n"
MF"v[12:15],v[200:203],a[100:103],v[12:15]\n"
RD"v[192:195],v242 offset:8192\n"
MF"v[20:23],v[200:203],a[104:107],v[20:23]\n"
RT"a[130:131],v246 offset:8704\n"
MF"v[20:23],v[204:207],a[120:123],v[20:23]\n"
BL"v239,s[16:19],s48 offen offset:1024 lds\n"
MF"v[28:31],v[204:207],a[124:127],v[28:31]\n"
RT"a[132:133],v247 offset:8192\n"
MF"v[28:31],v[200:203],a[108:111],v[28:31]\n"
RD"v[196:199],v243 offset:8192\n"
MF"v[32:35],v[208:211],a[96:99],v[32:35]\n"
MF"v[32:35],v[212:215],a[112:115],v[32:35]\n"
RT"a[134:135],v247 offset:8704\n"
MF"v[40:43],v[212:215],a[116:119],v[40:43]\n"
BL"v240,s[16:19],s48 offen offset:2048 lds\n"
MF"v[40:43],v[208:211],a[100:103],v[40:43]\n"
RD"v[200:203],v242 offset:10240\n"
MF"v[48:51],v[208:211],a[104:107],v[48:51]\n"
RT"a[136:137],v248 offset:8192\n"
MF"v[48:51],v[212:215],a[120:123],v[48:51]\n"
BL"v241,s[16:19],s48 offen offset:3072 lds\n"
MF"v[56:59],v[212:215],a[124:127],v[56:59]\n"
RD"v[204:207],v243 offset:10240\n"
MF"v[56:59],v[208:211],a[108:111],v[56:59]\n"
WT"lgkmcnt(4)\n"
RT"a[138:139],v248 offset:8704\n"
MF"v[36:39],v[216:219],a[96:99],v[36:39]\n"
SA"m0,s36,32768\n"
"s_bfe_u32 s48,s46,0x30014\n"
"s_lshl_b32 s48,s48,7\n"
MF"v[36:39],v[220:223],a[112:115],v[36:39]\n"
RD"v[208:211],v242 offset:12288\n"
MF"v[44:47],v[220:223],a[116:119],v[44:47]\n"
RT"a[140:141],v249 offset:8192\n"
MF"v[44:47],v[216:219],a[100:103],v[44:47]\n"
BL"v234,s[12:15],s48 offen offset:0 lds\n"
MF"v[52:55],v[216:219],a[104:107],v[52:55]\n"
MF"v[52:55],v[220:223],a[120:123],v[52:55]\n"
RD"v[212:215],v243 offset:12288\n"
MF"v[60:63],v[220:223],a[124:127],v[60:63]\n"
RT"a[142:143],v249 offset:8704\n"
MF"v[60:63],v[216:219],a[108:111],v[60:63]\n"
BL"v235,s[12:15],s48 offen offset:1024 lds\n"
MF"v[64:67],v[192:195],a[96:99],v[64:67]\n"
RD"v[216:219],v242 offset:14336\n"
MF"v[64:67],v[196:199],a[112:115],v[64:67]\n"
WT"vmcnt(14)\n"
RT"a[144:145],v246 offset:12288\n"
MF"v[72:75],v[196:199],a[116:119],v[72:75]\n"
MF"v[72:75],v[192:195],a[100:103],v[72:75]\n"
RT"a[146:147],v246 offset:12800\n"
MF"v[80:83],v[192:195],a[104:107],v[80:83]\n"
BL"v236,s[12:15],s48 offen offset:2048 lds\n"
MF"v[80:83],v[196:199],a[120:123],v[80:83]\n"
RD"v[220:223],v243 offset:14336\n"
MF"v[88:91],v[196:199],a[124:127],v[88:91]\n"
RT"a[148:149],v247 offset:12288\n"
MF"v[88:91],v[192:195],a[108:111],v[88:91]\n"
WT"lgkmcnt(4)\n"
BL"v237,s[12:15],s48 offen offset:3072 lds\n"
MF"v[68:71],v[200:203],a[96:99],v[68:71]\n"
MF"v[68:71],v[204:207],a[112:115],v[68:71]\n"
RT"a[150:151],v247 offset:12800\n"
MF"v[76:79],v[204:207],a[116:119],v[76:79]\n"
SA"m0,s37,0\n"
"s_bfe_u32 s48,s46,0x30018\n"
"s_lshl_b32 s48,s48,19\n"
MF"v[76:79],v[200:203],a[100:103],v[76:79]\n"
MF"v[84:87],v[200:203],a[104:107],v[84:87]\n"
RT"a[152:153],v248 offset:12288\n"
MF"v[84:87],v[204:207],a[120:123],v[84:87]\n"
BL"v238,s[16:19],s48 offen offset:0 lds\n"
MF"v[92:95],v[204:207],a[124:127],v[92:95]\n"
WT"vmcnt(13) lgkmcnt(3)\n"
"s_barrier\n"
MF"v[92:95],v[200:203],a[108:111],v[92:95]\n"
RD"v[192:195],v242 offset:16384\n"
RT"a[154:155],v248 offset:12800\n"
MF"v[96:99],v[208:211],a[96:99],v[96:99]\n"
BL"v239,s[16:19],s48 offen offset:1024 lds\n"
MF"v[96:99],v[212:215],a[112:115],v[96:99]\n"
MF"v[104:107],v[212:215],a[116:119],v[104:107]\n"
RD"v[196:199],v243 offset:16384\n"
MF"v[104:107],v[208:211],a[100:103],v[104:107]\n"
RT"a[156:157],v249 offset:12288\n"
MF"v[112:115],v[208:211],a[104:107],v[112:115]\n"
MF"v[112:115],v[212:215],a[120:123],v[112:115]\n"
RT"a[158:159],v249 offset:12800\n"
MF"v[120:123],v[212:215],a[124:127],v[120:123]\n"
BL"v240,s[16:19],s48 offen offset:2048 lds\n"
MF"v[120:123],v[208:211],a[108:111],v[120:123]\n"
RD"v[200:203],v242 offset:18432\n"
MF"v[100:103],v[216:219],a[96:99],v[100:103]\n"
MF"v[100:103],v[220:223],a[112:115],v[100:103]\n"
RD"v[204:207],v243 offset:18432\n"
MF"v[108:111],v[220:223],a[116:119],v[108:111]\n"
BL"v241,s[16:19],s48 offen offset:3072 lds\n"
MF"v[108:111],v[216:219],a[100:103],v[108:111]\n"
MF"v[116:119],v[216:219],a[104:107],v[116:119]\n"
SA"m0,s37,4096\n"
"s_bfe_u32 s48,s46,0x30018\n"
"s_lshl_b32 s48,s48,19\n"
MF"v[116:119],v[220:223],a[120:123],v[116:119]\n"
"s_bitset1_b32 s48,18\n"
RD"v[208:211],v242 offset:20480\n"
MF"v[124:127],v[220:223],a[124:127],v[124:127]\n"
BL"v238,s[16:19],s48 offen offset:0 lds\n"
MF"v[124:127],v[216:219],a[108:111],v[124:127]\n"
WT"lgkmcnt(2)\n"
MF"v[0:3],v[192:195],a[128:131],v[0:3]\n"
RD"v[212:215],v243 offset:20480\n"
MF"v[0:3],v[196:199],a[144:147],v[0:3]\n"
MF"v[8:11],v[196:199],a[148:151],v[8:11]\n"
BL"v239,s[16:19],s48 offen offset:1024 lds\n"
MF"v[8:11],v[192:195],a[132:135],v[8:11]\n"
MF"v[16:19],v[192:195],a[136:139],v[16:19]\n"
RD"v[216:219],v242 offset:22528\n"
MF"v[16:19],v[196:199],a[152:155],v[16:19]\n"
MF"v[24:27],v[196:199],a[156:159],v[24:27]\n"
MF"v[24:27],v[192:195],a[140:143],v[24:27]\n"
RD"v[220:223],v243 offset:22528\n"
MF"v[4:7],v[200:203],a[128:131],v[4:7]\n"
WT"lgkmcnt(2)\n"
BL"v240,s[16:19],s48 offen offset:2048 lds\n"
MF"v[4:7],v[204:207],a[144:147],v[4:7]\n"
WT"vmcnt(15)\n"
RT"a[160:161],v246 offset:16384\n"
MF"v[12:15],v[204:207],a[148:151],v[12:15]\n"
MF"v[12:15],v[200:203],a[132:135],v[12:15]\n"
RD"v[192:195],v242 offset:24576\n"
MF"v[20:23],v[200:203],a[136:139],v[20:23]\n"
RT"a[162:163],v246 offset:16896\n"
MF"v[20:23],v[204:207],a[152:155],v[20:23]\n"
BL"v241,s[16:19],s48 offen offset:3072 lds\n"
MF"v[28:31],v[204:207],a[156:159],v[28:31]\n"
SA"m0,s36,0\n"
RT"a[164:165],v247 offset:16384\n"
"s_bfe_u32 s48,s46,0x30018\n"
MF"v[28:31],v[200:203],a[140:143],v[28:31]\n"
"s_lshl_b32 s48,s48,7\n"
RD"v[196:199],v243 offset:24576\n"
MF"v[32:35],v[208:211],a[128:131],v[32:35]\n"
BL"v234,s[12:15],s48 offen offset:0 lds\n"
MF"v[32:35],v[212:215],a[144:147],v[32:35]\n"
RT"a[166:167],v247 offset:16896\n"
MF"v[40:43],v[212:215],a[148:151],v[40:43]\n"
RD"v[200:203],v242 offset:26624\n"
MF"v[40:43],v[208:211],a[132:135],v[40:43]\n"
MF"v[48:51],v[208:211],a[136:139],v[48:51]\n"
RT"a[168:169],v248 offset:16384\n"
MF"v[48:51],v[212:215],a[152:155],v[48:51]\n"
BL"v235,s[12:15],s48 offen offset:1024 lds\n"
MF"v[56:59],v[212:215],a[156:159],v[56:59]\n"
RD"v[204:207],v243 offset:26624\n"
MF"v[56:59],v[208:211],a[140:143],v[56:59]\n"
WT"lgkmcnt(4)\n"
RT"a[170:171],v248 offset:16896\n"
MF"v[36:39],v[216:219],a[128:131],v[36:39]\n"
MF"v[36:39],v[220:223],a[144:147],v[36:39]\n"
RD"v[208:211],v242 offset:28672\n"
MF"v[44:47],v[220:223],a[148:151],v[44:47]\n"
BL"v236,s[12:15],s48 offen offset:2048 lds\n"
MF"v[44:47],v[216:219],a[132:135],v[44:47]\n"
RT"a[172:173],v249 offset:16384\n"
MF"v[52:55],v[216:219],a[136:139],v[52:55]\n"
MF"v[52:55],v[220:223],a[152:155],v[52:55]\n"
RD"v[212:215],v243 offset:28672\n"
MF"v[60:63],v[220:223],a[156:159],v[60:63]\n"
RT"a[174:175],v249 offset:16896\n"
MF"v[60:63],v[216:219],a[140:143],v[60:63]\n"
BL"v237,s[12:15],s48 offen offset:3072 lds\n"
MF"v[64:67],v[192:195],a[128:131],v[64:67]\n"
SA"m0,s37,8192\n"
RD"v[216:219],v242 offset:30720\n"
"s_bfe_u32 s48,s46,0x3001c\n"
MF"v[64:67],v[196:199],a[144:147],v[64:67]\n"
"s_lshl_b32 s48,s48,19\n"
WT"vmcnt(16)\n"
RT"a[176:177],v246 offset:20480\n"
MF"v[72:75],v[196:199],a[148:151],v[72:75]\n"
BL"v238,s[16:19],s48 offen offset:0 lds\n"
MF"v[72:75],v[192:195],a[132:135],v[72:75]\n"
RT"a[178:179],v246 offset:20992\n"
MF"v[80:83],v[192:195],a[136:139],v[80:83]\n"
RD"v[220:223],v243 offset:30720\n"
MF"v[80:83],v[196:199],a[152:155],v[80:83]\n"
MF"v[88:91],v[196:199],a[156:159],v[88:91]\n"
RT"a[180:181],v247 offset:20480\n"
MF"v[88:91],v[192:195],a[140:143],v[88:91]\n"
WT"lgkmcnt(4)\n"
BL"v239,s[16:19],s48 offen offset:1024 lds\n"
MF"v[68:71],v[200:203],a[128:131],v[68:71]\n"
MF"v[68:71],v[204:207],a[144:147],v[68:71]\n"
RT"a[182:183],v247 offset:20992\n"
MF"v[76:79],v[204:207],a[148:151],v[76:79]\n"
MF"v[76:79],v[200:203],a[132:135],v[76:79]\n"
BL"v240,s[16:19],s48 offen offset:2048 lds\n"
MF"v[84:87],v[200:203],a[136:139],v[84:87]\n"
RT"a[184:185],v248 offset:20480\n"
MF"v[84:87],v[204:207],a[152:155],v[84:87]\n"
WT"vmcnt(15) lgkmcnt(3)\n"
"s_barrier\n"
MF"v[92:95],v[204:207],a[156:159],v[92:95]\n"
RD"v[192:195],v242 offset:32768\n"
MF"v[92:95],v[200:203],a[140:143],v[92:95]\n"
RT"a[186:187],v248 offset:20992\n"
MF"v[96:99],v[208:211],a[128:131],v[96:99]\n"
BL"v241,s[16:19],s48 offen offset:3072 lds\n"
MF"v[96:99],v[212:215],a[144:147],v[96:99]\n"
MF"v[104:107],v[212:215],a[148:151],v[104:107]\n"
SA"m0,s37,12288\n"
RD"v[196:199],v243 offset:32768\n"
"s_bfe_u32 s48,s46,0x3001c\n"
MF"v[104:107],v[208:211],a[132:135],v[104:107]\n"
"s_lshl_b32 s48,s48,19\n"
RT"a[188:189],v249 offset:20480\n"
"s_bitset1_b32 s48,18\n"
MF"v[112:115],v[208:211],a[136:139],v[112:115]\n"
BL"v238,s[16:19],s48 offen offset:0 lds\n"
MF"v[112:115],v[212:215],a[152:155],v[112:115]\n"
RT"a[190:191],v249 offset:20992\n"
MF"v[120:123],v[212:215],a[156:159],v[120:123]\n"
RD"v[200:203],v242 offset:34816\n"
MF"v[120:123],v[208:211],a[140:143],v[120:123]\n"
MF"v[100:103],v[216:219],a[128:131],v[100:103]\n"
BL"v239,s[16:19],s48 offen offset:1024 lds\n"
MF"v[100:103],v[220:223],a[144:147],v[100:103]\n"
RD"v[204:207],v243 offset:34816\n"
MF"v[108:111],v[220:223],a[148:151],v[108:111]\n"
MF"v[108:111],v[216:219],a[132:135],v[108:111]\n"
MF"v[116:119],v[216:219],a[136:139],v[116:119]\n"
MF"v[116:119],v[220:223],a[152:155],v[116:119]\n"
RD"v[208:211],v242 offset:36864\n"
MF"v[124:127],v[220:223],a[156:159],v[124:127]\n"
BL"v240,s[16:19],s48 offen offset:2048 lds\n"
MF"v[124:127],v[216:219],a[140:143],v[124:127]\n"
WT"lgkmcnt(2)\n"
MF"v[0:3],v[192:195],a[160:163],v[0:3]\n"
RD"v[212:215],v243 offset:36864\n"
MF"v[0:3],v[196:199],a[176:179],v[0:3]\n"
BL"v241,s[16:19],s48 offen offset:3072 lds\n"
MF"v[8:11],v[196:199],a[180:183],v[8:11]\n"
MF"v[8:11],v[192:195],a[164:167],v[8:11]\n"
MF"v[16:19],v[192:195],a[168:171],v[16:19]\n"
SA"m0,s36,16384\n"
RD"v[216:219],v242 offset:38912\n"
"s_bfe_u32 s48,s46,0x3001c\n"
MF"v[16:19],v[196:199],a[184:187],v[16:19]\n"
"s_lshl_b32 s48,s48,7\n"
MF"v[24:27],v[196:199],a[188:191],v[24:27]\n"
BL"v234,s[12:15],s48 offen offset:0 lds\n"
MF"v[24:27],v[192:195],a[172:175],v[24:27]\n"
RD"v[220:223],v243 offset:38912\n"
MF"v[4:7],v[200:203],a[160:163],v[4:7]\n"
WT"lgkmcnt(2)\n"
WT"vmcnt(17)\n"
RT"a[192:193],v246 offset:0\n"
MF"v[4:7],v[204:207],a[176:179],v[4:7]\n"
MF"v[12:15],v[204:207],a[180:183],v[12:15]\n"
BL"v235,s[12:15],s48 offen offset:1024 lds\n"
MF"v[12:15],v[200:203],a[164:167],v[12:15]\n"
RD"v[192:195],v242 offset:40960\n"
RT"a[194:195],v246 offset:512\n"
MF"v[20:23],v[200:203],a[168:171],v[20:23]\n"
MF"v[20:23],v[204:207],a[184:187],v[20:23]\n"
MF"v[28:31],v[204:207],a[188:191],v[28:31]\n"
RT"a[196:197],v247 offset:0\n"
MF"v[28:31],v[200:203],a[172:175],v[28:31]\n"
RD"v[196:199],v243 offset:40960\n"
MF"v[32:35],v[208:211],a[160:163],v[32:35]\n"
BL"v236,s[12:15],s48 offen offset:2048 lds\n"
MF"v[32:35],v[212:215],a[176:179],v[32:35]\n"
RT"a[198:199],v247 offset:512\n"
MF"v[40:43],v[212:215],a[180:183],v[40:43]\n"
RD"v[200:203],v242 offset:43008\n"
MF"v[40:43],v[208:211],a[164:167],v[40:43]\n"
BL"v237,s[12:15],s48 offen offset:3072 lds\n"
MF"v[48:51],v[208:211],a[168:171],v[48:51]\n"
RT"a[200:201],v248 offset:0\n"
MF"v[48:51],v[212:215],a[184:187],v[48:51]\n"
MF"v[56:59],v[212:215],a[188:191],v[56:59]\n"
SA"m0,s37,16384\n"
RD"v[204:207],v243 offset:43008\n"
SA"s48,s39,0\n"
MF"v[56:59],v[208:211],a[172:175],v[56:59]\n"
WT"lgkmcnt(4)\n"
RT"a[202:203],v248 offset:512\n"
"s_lshr_b32 s50,s30,1\n"
MF"v[36:39],v[216:219],a[160:163],v[36:39]\n"
"s_and_b32 s51,s30,1\n"
BL"v234,s[12:15],s48 offen offset:0 lds\n"
MF"v[36:39],v[220:223],a[176:179],v[36:39]\n"
"s_mul_i32 s38,s50,24576\n"
RD"v[208:211],v242 offset:45056\n"
MF"v[44:47],v[220:223],a[180:183],v[44:47]\n"
"s_lshl_b32 s60,s50,18\n"
RT"a[204:205],v249 offset:0\n"
MF"v[44:47],v[216:219],a[164:167],v[44:47]\n"
"s_lshl_b32 s50,s51,12\n"
MF"v[52:55],v[216:219],a[168:171],v[52:55]\n"
SA"s38,s38,s50\n"
BL"v235,s[12:15],s48 offen offset:1024 lds\n"
MF"v[52:55],v[220:223],a[184:187],v[52:55]\n"
"s_lshl_b32 s50,s51,7\n"
RD"v[212:215],v243 offset:45056\n"
RT"a[206:207],v249 offset:512\n"
MF"v[60:63],v[220:223],a[188:191],v[60:63]\n"
SA"s60,s60,s50\n"
MF"v[60:63],v[216:219],a[172:175],v[60:63]\n"
MF"v[64:67],v[192:195],a[160:163],v[64:67]\n"
RD"v[216:219],v242 offset:47104\n"
MF"v[64:67],v[196:199],a[176:179],v[64:67]\n"
"s_sub_u32 s99,s34,5\n"
WT"vmcnt(18)\n"
RT"a[208:209],v246 offset:4096\n"
MF"v[72:75],v[196:199],a[180:183],v[72:75]\n"
"s_cmp_lt_u32 s34,5\n"
BL"v236,s[12:15],s48 offen offset:2048 lds\n"
MF"v[72:75],v[192:195],a[164:167],v[72:75]\n"
"s_cselect_b32 s99,s34,s99\n"
RT"a[210:211],v246 offset:4608\n"
"s_mul_i32 s99,s99,1\n"
MF"v[80:83],v[192:195],a[168:171],v[80:83]\n"
"s_lshl_b32 s61,s99,20\n"
RD"v[220:223],v243 offset:47104\n"
MF"v[80:83],v[196:199],a[184:187],v[80:83]\n"
BL"v237,s[12:15],s48 offen offset:3072 lds\n"
MF"v[88:91],v[196:199],a[188:191],v[88:91]\n"
RT"a[212:213],v247 offset:4096\n"
MF"v[88:91],v[192:195],a[172:175],v[88:91]\n"
WT"lgkmcnt(4)\n"
MF"v[68:71],v[200:203],a[160:163],v[68:71]\n"
SA"m0,s37,20480\n"
SA"s48,s98,0\n"
MF"v[68:71],v[204:207],a[176:179],v[68:71]\n"
RT"a[214:215],v247 offset:4608\n"
MF"v[76:79],v[204:207],a[180:183],v[76:79]\n"
BL"v234,s[12:15],s48 offen offset:0 lds\n"
MF"v[76:79],v[200:203],a[164:167],v[76:79]\n"
MF"v[84:87],v[200:203],a[168:171],v[84:87]\n"
RT"a[216:217],v248 offset:4096\n"
MF"v[84:87],v[204:207],a[184:187],v[84:87]\n"
WT"vmcnt(17) lgkmcnt(3)\n"
"s_barrier\n"
MF"v[92:95],v[204:207],a[188:191],v[92:95]\n"
RD"v[192:195],v242 offset:0\n"
MF"v[92:95],v[200:203],a[172:175],v[92:95]\n"
BL"v235,s[12:15],s48 offen offset:1024 lds\n"
MF"v[96:99],v[208:211],a[160:163],v[96:99]\n"
RT"a[218:219],v248 offset:4608\n"
MF"v[96:99],v[212:215],a[176:179],v[96:99]\n"
MF"v[104:107],v[212:215],a[180:183],v[104:107]\n"
RD"v[196:199],v243 offset:0\n"
MF"v[104:107],v[208:211],a[164:167],v[104:107]\n"
RT"a[220:221],v249 offset:4096\n"
MF"v[112:115],v[208:211],a[168:171],v[112:115]\n"
BL"v236,s[12:15],s48 offen offset:2048 lds\n"
MF"v[112:115],v[212:215],a[184:187],v[112:115]\n"
RT"a[222:223],v249 offset:4608\n"
MF"v[120:123],v[212:215],a[188:191],v[120:123]\n"
RD"v[200:203],v242 offset:2048\n"
MF"v[120:123],v[208:211],a[172:175],v[120:123]\n"
BL"v237,s[12:15],s48 offen offset:3072 lds\n"
MF"v[100:103],v[216:219],a[160:163],v[100:103]\n"
MF"v[100:103],v[220:223],a[176:179],v[100:103]\n"
RD"v[204:207],v243 offset:2048\n"
MF"v[108:111],v[220:223],a[180:183],v[108:111]\n"
SA"m0,s37,0\n"
SA"s48,s39,32768\n"
MF"v[108:111],v[216:219],a[164:167],v[108:111]\n"
MF"v[116:119],v[216:219],a[168:171],v[116:119]\n"
BL"v234,s[12:15],s48 offen offset:0 lds\n"
MF"v[116:119],v[220:223],a[184:187],v[116:119]\n"
RD"v[208:211],v242 offset:4096\n"
MF"v[124:127],v[220:223],a[188:191],v[124:127]\n"
MF"v[124:127],v[216:219],a[172:175],v[124:127]\n"
WT"lgkmcnt(2)\n"
MF"v[0:3],v[192:195],a[192:195],v[0:3]\n"
RD"v[212:215],v243 offset:4096\n"
MF"v[0:3],v[196:199],a[208:211],v[0:3]\n"
BL"v235,s[12:15],s48 offen offset:1024 lds\n"
MF"v[8:11],v[196:199],a[212:215],v[8:11]\n"
MF"v[8:11],v[192:195],a[196:199],v[8:11]\n"
MF"v[16:19],v[192:195],a[200:203],v[16:19]\n"
RD"v[216:219],v242 offset:6144\n"
MF"v[16:19],v[196:199],a[216:219],v[16:19]\n"
BL"v236,s[12:15],s48 offen offset:2048 lds\n"
MF"v[24:27],v[196:199],a[220:223],v[24:27]\n"
BL"v[160:163],v224,s[8:11],0 offen offset:256 sc1\n"
MF"v[24:27],v[192:195],a[204:207],v[24:27]\n"
RD"v[220:223],v243 offset:6144\n"
MF"v[4:7],v[200:203],a[192:195],v[4:7]\n"
WT"lgkmcnt(2)\n"
WT"vmcnt(20)\n"
RT"a[224:225],v246 offset:8192\n"
MF"v[4:7],v[204:207],a[208:211],v[4:7]\n"
BL"v[164:167],v224,s[8:11],0 offen offset:1280 sc1\n"
MF"v[12:15],v[204:207],a[212:215],v[12:15]\n"
BL"v237,s[12:15],s48 offen offset:3072 lds\n"
MF"v[12:15],v[200:203],a[196:199],v[12:15]\n"
RD"v[192:195],v242 offset:8192\n"
RT"a[226:227],v246 offset:8704\n"
MF"v[20:23],v[200:203],a[200:203],v[20:23]\n"
SA"m0,s37,4096\n"
SA"s48,s98,32768\n"
MF"v[20:23],v[204:207],a[216:219],v[20:23]\n"
MF"v[28:31],v[204:207],a[220:223],v[28:31]\n"
RT"a[228:229],v247 offset:8192\n"
MF"v[28:31],v[200:203],a[204:207],v[28:31]\n"
BL"v234,s[12:15],s48 offen offset:0 lds\n"
MF"v[32:35],v[208:211],a[192:195],v[32:35]\n"
RD"v[196:199],v243 offset:8192\n"
MF"v[32:35],v[212:215],a[208:211],v[32:35]\n"
RT"a[230:231],v247 offset:8704\n"
MF"v[40:43],v[212:215],a[212:215],v[40:43]\n"
RD"v[200:203],v242 offset:10240\n"
MF"v[40:43],v[208:211],a[196:199],v[40:43]\n"
BL"v235,s[12:15],s48 offen offset:1024 lds\n"
MF"v[48:51],v[208:211],a[200:203],v[48:51]\n"
RT"a[232:233],v248 offset:8192\n"
MF"v[48:51],v[212:215],a[216:219],v[48:51]\n"
MF"v[56:59],v[212:215],a[220:223],v[56:59]\n"
RD"v[204:207],v243 offset:10240\n"
MF"v[56:59],v[208:211],a[204:207],v[56:59]\n"
WT"lgkmcnt(4)\n"
RT"a[234:235],v248 offset:8704\n"
MF"v[36:39],v[216:219],a[192:195],v[36:39]\n"
BL"v236,s[12:15],s48 offen offset:2048 lds\n"
MF"v[36:39],v[220:223],a[208:211],v[36:39]\n"
RD"v[208:211],v242 offset:12288\n"
MF"v[44:47],v[220:223],a[212:215],v[44:47]\n"
RT"a[236:237],v249 offset:8192\n"
MF"v[44:47],v[216:219],a[196:199],v[44:47]\n"
BL"v237,s[12:15],s48 offen offset:3072 lds\n"
MF"v[52:55],v[216:219],a[200:203],v[52:55]\n"
MF"v[52:55],v[220:223],a[216:219],v[52:55]\n"
RD"v[212:215],v243 offset:12288\n"
MF"v[60:63],v[220:223],a[220:223],v[60:63]\n"
RT"a[238:239],v249 offset:8704\n"
MF"v[60:63],v[216:219],a[204:207],v[60:63]\n"
MF"v[64:67],v[192:195],a[192:195],v[64:67]\n"
RD"v[216:219],v242 offset:14336\n"
MF"v[64:67],v[196:199],a[208:211],v[64:67]\n"
SA"m0,s37,8192\n"
WT"vmcnt(22)\n"
RT"a[240:241],v246 offset:12288\n"
MF"v[72:75],v[196:199],a[212:215],v[72:75]\n"
SA"s48,s39,65536\n"
MF"v[72:75],v[192:195],a[196:199],v[72:75]\n"
RT"a[242:243],v246 offset:12800\n"
MF"v[80:83],v[192:195],a[200:203],v[80:83]\n"
WT"lgkmcnt(3)\n"
BL"v234,s[12:15],s48 offen offset:0 lds\n"
MF"v[80:83],v[196:199],a[216:219],v[80:83]\n"
RD"v[220:223],v243 offset:14336\n"
MF"v[88:91],v[196:199],a[220:223],v[88:91]\n"
RT"a[244:245],v247 offset:12288\n"
MF"v[88:91],v[192:195],a[204:207],v[88:91]\n"
BL"v235,s[12:15],s48 offen offset:1024 lds\n"
MF"v[68:71],v[200:203],a[192:195],v[68:71]\n"
MF"v[68:71],v[204:207],a[208:211],v[68:71]\n"
RT"a[246:247],v247 offset:12800\n"
MF"v[76:79],v[204:207],a[212:215],v[76:79]\n"
MF"v[76:79],v[200:203],a[196:199],v[76:79]\n"
MF"v[84:87],v[200:203],a[200:203],v[84:87]\n"
RT"a[248:249],v248 offset:12288\n"
MF"v[84:87],v[204:207],a[216:219],v[84:87]\n"
BL"v236,s[12:15],s48 offen offset:2048 lds\n"
MF"v[92:95],v[204:207],a[220:223],v[92:95]\n"
WT"vmcnt(21) lgkmcnt(3)\n"
"s_barrier\n"
MF"v[92:95],v[200:203],a[204:207],v[92:95]\n"
RD"v[192:195],v242 offset:16384\n"
RT"a[250:251],v248 offset:12800\n"
MF"v[96:99],v[208:211],a[192:195],v[96:99]\n"
BL"v237,s[12:15],s48 offen offset:3072 lds\n"
MF"v[96:99],v[212:215],a[208:211],v[96:99]\n"
MF"v[104:107],v[212:215],a[212:215],v[104:107]\n"
RD"v[196:199],v243 offset:16384\n"
MF"v[104:107],v[208:211],a[196:199],v[104:107]\n"
RT"a[252:253],v249 offset:12288\n"
MF"v[112:115],v[208:211],a[200:203],v[112:115]\n"
MF"v[112:115],v[212:215],a[216:219],v[112:115]\n"
RT"a[254:255],v249 offset:12800\n"
MF"v[120:123],v[212:215],a[220:223],v[120:123]\n"
RD"v[200:203],v242 offset:18432\n"
MF"v[120:123],v[208:211],a[204:207],v[120:123]\n"
MF"v[100:103],v[216:219],a[192:195],v[100:103]\n"
"s_lshl_b32 s50,s35,20\n"
MF"v[100:103],v[220:223],a[208:211],v[100:103]\n"
"s_lshl_b32 s51,s34,15\n"
RD"v[204:207],v243 offset:18432\n"
SA"m0,s37,12288\n"
MF"v[108:111],v[220:223],a[212:215],v[108:111]\n"
SA"s48,s98,65536\n"
SA"s50,s50,s51\n"
MF"v[108:111],v[216:219],a[196:199],v[108:111]\n"
WT"lgkmcnt(2)\n"
BL"v234,s[12:15],s48 offen offset:0 lds\n"
MF"v[116:119],v[216:219],a[200:203],v[116:119]\n"
"s_and_b32 s51,s30,1\n"
"s_lshl_b32 s51,s51,12\n"
MF"v[116:119],v[220:223],a[216:219],v[116:119]\n"
SA"s50,s50,s51\n"
RD"v[208:211],v242 offset:20480\n"
MF"v[124:127],v[220:223],a[220:223],v[124:127]\n"
SA"s50,s50,9699328\n"
WT"vmcnt(10)\n"
"v_cmp_eq_u64 vcc,s[28:29],v[160:161]\n"
"v_cmp_eq_u64_e64 s[62:63],s[28:29],v[162:163]\n"
"s_and_b64 vcc,vcc,s[62:63]\n"
"v_cmp_eq_u64_e64 s[62:63],s[28:29],v[164:165]\n"
"s_and_b64 vcc,vcc,s[62:63]\n"
"v_cmp_eq_u64_e64 s[62:63],s[28:29],v[166:167]\n"
"s_and_b64 vcc,vcc,s[62:63]\n"
"s_cmp_eq_u64 vcc,exec\n"
"s_cbranch_scc0 L1004_%=\n"
"L1005_%=:\n"
MF"v[124:127],v[216:219],a[204:207],v[124:127]\n"
"s_mov_b32 s94,8192\n"
BL"v235,s[12:15],s48 offen offset:1024 lds\n"
MF"v[0:3],v[192:195],a[224:227],v[0:3]\n"
"s_mov_b32 s95,12288\n"
RD"v[212:215],v243 offset:20480\n"
"v_add_u32 v250,s50,v224\n"
MF"v[0:3],v[196:199],a[240:243],v[0:3]\n"
BL"v[160:163],v224,s[8:11],s94 offen offset:0 sc1\n"
MF"v[8:11],v[196:199],a[244:247],v[8:11]\n"
BL"v[164:167],v224,s[8:11],s94 offen offset:1024 sc1\n"
MF"v[8:11],v[192:195],a[228:231],v[8:11]\n"
MF"v[16:19],v[192:195],a[232:235],v[16:19]\n"
RD"v[216:219],v242 offset:22528\n"
MF"v[16:19],v[196:199],a[248:251],v[16:19]\n"
BL"v236,s[12:15],s48 offen offset:2048 lds\n"
MF"v[24:27],v[196:199],a[252:255],v[24:27]\n"
BL"v[168:171],v224,s[8:11],s94 offen offset:2048 sc1\n"
MF"v[24:27],v[192:195],a[236:239],v[24:27]\n"
WT"lgkmcnt(1)\n"
BL"v[172:175],v224,s[8:11],s94 offen offset:3072 sc1\n"
MF"v[4:7],v[200:203],a[224:227],v[4:7]\n"
RD"v[220:223],v243 offset:22528\n"
MF"v[4:7],v[204:207],a[240:243],v[4:7]\n"
BL"v237,s[12:15],s48 offen offset:3072 lds\n"
MF"v[12:15],v[204:207],a[244:247],v[12:15]\n"
BL"v[176:179],v224,s[8:11],s95 offen offset:0 sc1\n"
MF"v[12:15],v[200:203],a[228:231],v[12:15]\n"
BL"v[180:183],v224,s[8:11],s95 offen offset:1024 sc1\n"
MF"v[20:23],v[200:203],a[232:235],v[20:23]\n"
RD"v[192:195],v242 offset:24576\n"
MF"v[20:23],v[204:207],a[248:251],v[20:23]\n"
BL"v[184:187],v224,s[8:11],s95 offen offset:2048 sc1\n"
MF"v[28:31],v[204:207],a[252:255],v[28:31]\n"
BL"v[188:191],v224,s[8:11],s95 offen offset:3072 sc1\n"
MF"v[28:31],v[200:203],a[236:239],v[28:31]\n"
RD"v[196:199],v243 offset:24576\n"
MF"v[32:35],v[208:211],a[224:227],v[32:35]\n"
MF"v[32:35],v[212:215],a[240:243],v[32:35]\n"
MF"v[40:43],v[212:215],a[244:247],v[40:43]\n"
RD"v[200:203],v242 offset:26624\n"
MF"v[40:43],v[208:211],a[228:231],v[40:43]\n"
MF"v[48:51],v[208:211],a[232:235],v[48:51]\n"
MF"v[48:51],v[212:215],a[248:251],v[48:51]\n"
MF"v[56:59],v[212:215],a[252:255],v[56:59]\n"
RD"v[204:207],v243 offset:26624\n"
MF"v[56:59],v[208:211],a[236:239],v[56:59]\n"
WT"lgkmcnt(2)\n"
MF"v[36:39],v[216:219],a[224:227],v[36:39]\n"
MF"v[36:39],v[220:223],a[240:243],v[36:39]\n"
RD"v[208:211],v242 offset:28672\n"
MF"v[44:47],v[220:223],a[244:247],v[44:47]\n"
MF"v[44:47],v[216:219],a[228:231],v[44:47]\n"
MF"v[52:55],v[216:219],a[232:235],v[52:55]\n"
MF"v[52:55],v[220:223],a[248:251],v[52:55]\n"
RD"v[212:215],v243 offset:28672\n"
MF"v[60:63],v[220:223],a[252:255],v[60:63]\n"
MF"v[60:63],v[216:219],a[236:239],v[60:63]\n"
MF"v[64:67],v[192:195],a[224:227],v[64:67]\n"
RD"v[216:219],v242 offset:30720\n"
MF"v[64:67],v[196:199],a[240:243],v[64:67]\n"
MF"v[72:75],v[196:199],a[244:247],v[72:75]\n"
MF"v[72:75],v[192:195],a[228:231],v[72:75]\n"
MF"v[80:83],v[192:195],a[232:235],v[80:83]\n"
RD"v[220:223],v243 offset:30720\n"
MF"v[80:83],v[196:199],a[248:251],v[80:83]\n"
MF"v[88:91],v[196:199],a[252:255],v[88:91]\n"
MF"v[88:91],v[192:195],a[236:239],v[88:91]\n"
WT"lgkmcnt(1)\n"
MF"v[68:71],v[200:203],a[224:227],v[68:71]\n"
MF"v[68:71],v[204:207],a[240:243],v[68:71]\n"
MF"v[76:79],v[204:207],a[244:247],v[76:79]\n"
"v_add_u32 v244,114688,v242\n"
"v_add_u32 v245,114688,v243\n"
MF"v[76:79],v[200:203],a[228:231],v[76:79]\n"
"v_add_u32 v242,65536,v242\n"
"v_add_u32 v243,65536,v243\n"
MF"v[84:87],v[200:203],a[232:235],v[84:87]\n"
MF"v[84:87],v[204:207],a[248:251],v[84:87]\n"
WT"lgkmcnt(0)\n"
"s_barrier\n"
MF"v[92:95],v[204:207],a[252:255],v[92:95]\n"
RD"v[192:195],v242 offset:16384\n"
MF"v[92:95],v[200:203],a[236:239],v[92:95]\n"
MF"v[96:99],v[208:211],a[224:227],v[96:99]\n"
MF"v[96:99],v[212:215],a[240:243],v[96:99]\n"
MF"v[104:107],v[212:215],a[244:247],v[104:107]\n"
SA"m0,s36,49152\n"
RD"v[196:199],v243 offset:16384\n"
SA"s48,s60,s61\n"
MF"v[104:107],v[208:211],a[228:231],v[104:107]\n"
MF"v[112:115],v[208:211],a[232:235],v[112:115]\n"
BL"v238,s[20:23],s48 offen offset:0 lds\n"
MF"v[112:115],v[212:215],a[248:251],v[112:115]\n"
MF"v[120:123],v[212:215],a[252:255],v[120:123]\n"
RD"v[200:203],v242 offset:18432\n"
MF"v[120:123],v[208:211],a[236:239],v[120:123]\n"
MF"v[100:103],v[216:219],a[224:227],v[100:103]\n"
BL"v239,s[20:23],s48 offen offset:1024 lds\n"
MF"v[100:103],v[220:223],a[240:243],v[100:103]\n"
"s_cmp_eq_u32 s31,1\n"
"s_cselect_b32 s94,0,2130706432\n"
MF"v[108:111],v[220:223],a[244:247],v[108:111]\n"
"buffer_store_dwordx2 v[232:233],v230,s[8:11],s94 offen sc1\n"
MF"v[108:111],v[216:219],a[228:231],v[108:111]\n"
RD"v[204:207],v243 offset:18432\n"
"v_mov_b32 v227,0\n"
MF"v[116:119],v[216:219],a[232:235],v[116:119]\n"
WT"vmcnt(12)\n"
"v_mov_b32 v226,v160\n"
"v_mov_b32 v228,0\n"
MF"v[116:119],v[220:223],a[248:251],v[116:119]\n"
BL"v240,s[20:23],s48 offen offset:2048 lds\n"
MF"v[124:127],v[220:223],a[252:255],v[124:127]\n"
SA"s61,s61,524288\n"
RD"v[208:211],v242 offset:20480\n"
"v_med3_u32 v228,v227,v228,v161\n"
MF"v[124:127],v[216:219],a[236:239],v[124:127]\n"
WT"lgkmcnt(2)\n"
"v_med3_u32 v227,v226,v227,v161\n"
"v_max_u32 v226,v226,v161\n"
MF"v[128:131],v[192:195],a[0:3],0\n"
"v_med3_u32 v228,v227,v228,v162\n"
"v_med3_u32 v227,v226,v227,v162\n"
MF"v[128:131],v[196:199],a[16:19],v[128:131]\n"
RD"v[212:215],v243 offset:20480\n"
"v_max_u32 v226,v226,v162\n"
MF"v[136:139],v[196:199],a[20:23],0\n"
BL"v241,s[20:23],s48 offen offset:3072 lds\n"
MF"v[136:139],v[192:195],a[4:7],v[136:139]\n"
"v_med3_u32 v228,v227,v228,v163\n"
"v_med3_u32 v227,v226,v227,v163\n"
MF"v[144:147],v[192:195],a[8:11],0\n"
"v_max_u32 v226,v226,v163\n"
PK"v0,v0,v1\n"
MF"v[144:147],v[196:199],a[24:27],v[144:147]\n"
RD"v[216:219],v242 offset:22528\n"
PK"v1,v2,v3\n"
MF"v[152:155],v[196:199],a[28:31],0\n"
SA"m0,s36,32768\n"
WT"vmcnt(13)\n"
"v_med3_u32 v228,v227,v228,v164\n"
MF"v[152:155],v[192:195],a[12:15],v[152:155]\n"
SA"s48,s60,s61\n"
PK"v2,v4,v5\n"
PK"v3,v6,v7\n"
MF"v[132:135],v[200:203],a[0:3],0\n"
WT"lgkmcnt(1)\n"
BL"v238,s[20:23],s48 offen offset:0 lds\n"
MF"v[132:135],v[204:207],a[16:19],v[132:135]\n"
RD"v[220:223],v243 offset:22528\n"
"v_med3_u32 v227,v226,v227,v164\n"
MF"v[140:143],v[204:207],a[20:23],0\n"
"v_max_u32 v226,v226,v164\n"
"v_med3_u32 v228,v227,v228,v165\n"
MF"v[140:143],v[200:203],a[4:7],v[140:143]\n"
"v_med3_u32 v227,v226,v227,v165\n"
"v_max_u32 v226,v226,v165\n"
MF"v[148:151],v[200:203],a[8:11],0\n"
BL"v239,s[20:23],s48 offen offset:1024 lds\n"
MF"v[148:151],v[204:207],a[24:27],v[148:151]\n"
RD"v[192:195],v242 offset:40960\n"
"v_med3_u32 v228,v227,v228,v166\n"
MF"v[156:159],v[204:207],a[28:31],0\n"
PK"v8,v8,v9\n"
PK"v9,v10,v11\n"
MF"v[156:159],v[200:203],a[12:15],v[156:159]\n"
"v_med3_u32 v227,v226,v227,v166\n"
PK"v10,v12,v13\n"
MF"v[128:131],v[208:211],a[32:35],v[128:131]\n"
"s_mov_b32 s49,0\n"
PK"v11,v14,v15\n"
"v_max_u32 v226,v226,v166\n"
MF"v[128:131],v[212:215],a[48:51],v[128:131]\n"
RD"v[196:199],v243 offset:40960\n"
"v_med3_u32 v228,v227,v228,v167\n"
MF"v[136:139],v[212:215],a[52:55],v[136:139]\n"
SA"s61,s61,524288\n"
BL"v240,s[20:23],s48 offen offset:2048 lds\n"
MF"v[136:139],v[208:211],a[36:39],v[136:139]\n"
"s_cmp_eq_u32 s61,5242880\n"
BS"v[0:3],v250,s[8:11],s49 offen offset:0 sc1\n"
MF"v[144:147],v[208:211],a[40:43],v[144:147]\n"
"s_cselect_b32 s61,0,s61\n"
RD"v[200:203],v242 offset:43008\n"
"v_med3_u32 v227,v226,v227,v167\n"
MF"v[144:147],v[212:215],a[56:59],v[144:147]\n"
BL"v241,s[20:23],s48 offen offset:3072 lds\n"
MF"v[152:155],v[212:215],a[60:63],v[152:155]\n"
"v_max_u32 v226,v226,v167\n"
PK"v16,v16,v17\n"
MF"v[152:155],v[208:211],a[44:47],v[152:155]\n"
WT"lgkmcnt(2)\n"
WT"vmcnt(16)\n"
"v_med3_u32 v228,v227,v228,v168\n"
MF"v[132:135],v[216:219],a[32:35],v[132:135]\n"
RD"v[204:207],v243 offset:43008\n"
"v_med3_u32 v227,v226,v227,v168\n"
MF"v[132:135],v[220:223],a[48:51],v[132:135]\n"
PK"v17,v18,v19\n"
"v_max_u32 v226,v226,v168\n"
MF"v[140:143],v[220:223],a[52:55],v[140:143]\n"
"v_med3_u32 v228,v227,v228,v169\n"
PK"v18,v20,v21\n"
MF"v[140:143],v[216:219],a[36:39],v[140:143]\n"
PK"v19,v22,v23\n"
"v_med3_u32 v227,v226,v227,v169\n"
MF"v[148:151],v[216:219],a[40:43],v[148:151]\n"
RD"v[208:211],v242 offset:45056\n"
"v_max_u32 v226,v226,v169\n"
MF"v[148:151],v[220:223],a[56:59],v[148:151]\n"
"s_lshl_b32 s90,s31,2\n"
"v_med3_u32 v228,v227,v228,v170\n"
"v_med3_u32 v227,v226,v227,v170\n"
MF"v[156:159],v[220:223],a[60:63],v[156:159]\n"
SA"s90,s90,s30\n"
"v_max_u32 v226,v226,v170\n"
PK"v24,v24,v25\n"
MF"v[156:159],v[216:219],a[44:47],v[156:159]\n"
"s_lshl_b32 s90,s90,9\n"
PK"v25,v26,v27\n"
"v_med3_u32 v228,v227,v228,v171\n"
MF"v[128:131],v[192:195],a[64:67],v[128:131]\n"
WT"lgkmcnt(1)\n"
RD"v[212:215],v243 offset:45056\n"
"v_med3_u32 v227,v226,v227,v171\n"
MF"v[128:131],v[196:199],a[80:83],v[128:131]\n"
PK"v26,v28,v29\n"
PK"v27,v30,v31\n"
MF"v[136:139],v[196:199],a[84:87],v[136:139]\n"
"v_max_u32 v226,v226,v171\n"
PK"v32,v32,v33\n"
MF"v[136:139],v[192:195],a[68:71],v[136:139]\n"
RD"v[216:219],v242 offset:47104\n"
PK"v33,v34,v35\n"
MF"v[144:147],v[192:195],a[72:75],v[144:147]\n"
WT"vmcnt(15)\n"
"v_med3_u32 v228,v227,v228,v172\n"
"v_med3_u32 v227,v226,v227,v172\n"
MF"v[144:147],v[196:199],a[88:91],v[144:147]\n"
"v_max_u32 v226,v226,v172\n"
"v_med3_u32 v228,v227,v228,v173\n"
MF"v[152:155],v[196:199],a[92:95],v[152:155]\n"
"v_med3_u32 v227,v226,v227,v173\n"
"v_max_u32 v226,v226,v173\n"
MF"v[152:155],v[192:195],a[76:79],v[152:155]\n"
RD"v[220:223],v243 offset:47104\n"
PK"v34,v36,v37\n"
MF"v[132:135],v[200:203],a[64:67],v[132:135]\n"
PK"v35,v38,v39\n"
"v_med3_u32 v228,v227,v228,v174\n"
MF"v[132:135],v[204:207],a[80:83],v[132:135]\n"
"v_med3_u32 v227,v226,v227,v174\n"
"v_max_u32 v226,v226,v174\n"
MF"v[140:143],v[204:207],a[84:87],v[140:143]\n"
"v_med3_u32 v228,v227,v228,v175\n"
"v_med3_u32 v227,v226,v227,v175\n"
MF"v[140:143],v[200:203],a[68:71],v[140:143]\n"
RD"v[192:195],v244 offset:16384\n"
"v_max_u32 v226,v226,v175\n"
MF"v[148:151],v[200:203],a[72:75],v[148:151]\n"
PK"v40,v40,v41\n"
PK"v41,v42,v43\n"
MF"v[148:151],v[204:207],a[88:91],v[148:151]\n"
"v_min_u32 v254,48,v224\n"
PK"v42,v44,v45\n"
MF"v[156:159],v[204:207],a[92:95],v[156:159]\n"
WT"vmcnt(13)\n"
"v_med3_u32 v228,v227,v228,v176\n"
PK"v43,v46,v47\n"
MF"v[156:159],v[200:203],a[76:79],v[156:159]\n"
WT"lgkmcnt(1)\n"
RD"v[196:199],v245 offset:16384\n"
"v_lshlrev_b32 v254,3,v254\n"
MF"v[128:131],v[208:211],a[96:99],v[128:131]\n"
"buffer_load_dword v255,v254,s[8:11],s90 offen sc1\n"
MF"v[128:131],v[212:215],a[112:115],v[128:131]\n"
BS"v[8:11],v250,s[8:11],s49 offen offset:1024 sc1\n"
MF"v[136:139],v[212:215],a[116:119],v[136:139]\n"
"v_med3_u32 v227,v226,v227,v176\n"
"v_max_u32 v226,v226,v176\n"
MF"v[136:139],v[208:211],a[100:103],v[136:139]\n"
RD"v[200:203],v244 offset:18432\n"
"v_med3_u32 v228,v227,v228,v177\n"
MF"v[144:147],v[208:211],a[104:107],v[144:147]\n"
"v_med3_u32 v227,v226,v227,v177\n"
"v_max_u32 v226,v226,v177\n"
MF"v[144:147],v[212:215],a[120:123],v[144:147]\n"
"v_med3_u32 v228,v227,v228,v178\n"
PK"v48,v48,v49\n"
MF"v[152:155],v[212:215],a[124:127],v[152:155]\n"
RD"v[204:207],v245 offset:18432\n"
PK"v49,v50,v51\n"
MF"v[152:155],v[208:211],a[108:111],v[152:155]\n"
"v_med3_u32 v227,v226,v227,v178\n"
PK"v50,v52,v53\n"
MF"v[132:135],v[216:219],a[96:99],v[132:135]\n"
PK"v51,v54,v55\n"
"v_max_u32 v226,v226,v178\n"
MF"v[132:135],v[220:223],a[112:115],v[132:135]\n"
"v_med3_u32 v228,v227,v228,v179\n"
"v_med3_u32 v227,v226,v227,v179\n"
MF"v[140:143],v[220:223],a[116:119],v[140:143]\n"
RD"v[208:211],v244 offset:20480\n"
"v_max_u32 v226,v226,v179\n"
MF"v[140:143],v[216:219],a[100:103],v[140:143]\n"
WT"vmcnt(14)\n"
"v_med3_u32 v228,v227,v228,v180\n"
"v_med3_u32 v227,v226,v227,v180\n"
MF"v[148:151],v[216:219],a[104:107],v[148:151]\n"
PK"v56,v56,v57\n"
PK"v57,v58,v59\n"
MF"v[148:151],v[220:223],a[120:123],v[148:151]\n"
"v_max_u32 v226,v226,v180\n"
PK"v58,v60,v61\n"
MF"v[156:159],v[220:223],a[124:127],v[156:159]\n"
RD"v[212:215],v245 offset:20480\n"
PK"v59,v62,v63\n"
MF"v[156:159],v[216:219],a[108:111],v[156:159]\n"
WT"lgkmcnt(2)\n"
"v_med3_u32 v228,v227,v228,v181\n"
"v_med3_u32 v227,v226,v227,v181\n"
MF"v[128:131],v[192:195],a[128:131],v[128:131]\n"
"v_max_u32 v226,v226,v181\n"
"v_med3_u32 v228,v227,v228,v182\n"
MF"v[128:131],v[196:199],a[144:147],v[128:131]\n"
"v_med3_u32 v227,v226,v227,v182\n"
"v_max_u32 v226,v226,v182\n"
MF"v[136:139],v[196:199],a[148:151],v[136:139]\n"
RD"v[216:219],v244 offset:22528\n"
PK"v64,v64,v65\n"
MF"v[136:139],v[192:195],a[132:135],v[136:139]\n"
PK"v65,v66,v67\n"
"v_med3_u32 v228,v227,v228,v183\n"
MF"v[144:147],v[192:195],a[136:139],v[144:147]\n"
PK"v66,v68,v69\n"
PK"v67,v70,v71\n"
MF"v[144:147],v[196:199],a[152:155],v[144:147]\n"
RD"v[220:223],v245 offset:22528\n"
"v_med3_u32 v227,v226,v227,v183\n"
MF"v[152:155],v[196:199],a[156:159],v[152:155]\n"
"v_max_u32 v226,v226,v183\n"
PK"v72,v72,v73\n"
MF"v[152:155],v[192:195],a[140:143],v[152:155]\n"
WT"vmcnt(13)\n"
"v_med3_u32 v228,v227,v228,v184\n"
"v_med3_u32 v227,v226,v227,v184\n"
MF"v[132:135],v[200:203],a[128:131],v[132:135]\n"
"v_max_u32 v226,v226,v184\n"
"v_med3_u32 v228,v227,v228,v185\n"
MF"v[132:135],v[204:207],a[144:147],v[132:135]\n"
RD"v[192:195],v244 offset:40960\n"
PK"v73,v74,v75\n"
MF"v[140:143],v[204:207],a[148:151],v[140:143]\n"
"v_med3_u32 v227,v226,v227,v185\n"
"v_max_u32 v226,v226,v185\n"
MF"v[140:143],v[200:203],a[132:135],v[140:143]\n"
PK"v74,v76,v77\n"
PK"v75,v78,v79\n"
MF"v[148:151],v[200:203],a[136:139],v[148:151]\n"
"v_med3_u32 v228,v227,v228,v186\n"
"v_med3_u32 v227,v226,v227,v186\n"
MF"v[148:151],v[204:207],a[152:155],v[148:151]\n"
RD"v[196:199],v245 offset:40960\n"
"v_max_u32 v226,v226,v186\n"
MF"v[156:159],v[204:207],a[156:159],v[156:159]\n"
"v_med3_u32 v228,v227,v228,v187\n"
"v_med3_u32 v227,v226,v227,v187\n"
MF"v[156:159],v[200:203],a[140:143],v[156:159]\n"
WT"lgkmcnt(2)\n"
PK"v80,v80,v81\n"
PK"v81,v82,v83\n"
MF"v[128:131],v[208:211],a[160:163],v[128:131]\n"
BS"v[16:19],v250,s[8:11],s49 offen offset:2048 sc1\n"
MF"v[128:131],v[212:215],a[176:179],v[128:131]\n"
RD"v[200:203],v244 offset:43008\n"
"v_max_u32 v226,v226,v187\n"
MF"v[136:139],v[212:215],a[180:183],v[136:139]\n"
WT"vmcnt(13)\n"
"v_med3_u32 v228,v227,v228,v188\n"
PK"v82,v84,v85\n"
MF"v[136:139],v[208:211],a[164:167],v[136:139]\n"
PK"v83,v86,v87\n"
"v_med3_u32 v227,v226,v227,v188\n"
MF"v[144:147],v[208:211],a[168:171],v[144:147]\n"
"v_max_u32 v226,v226,v188\n"
"v_med3_u32 v228,v227,v228,v189\n"
MF"v[144:147],v[212:215],a[184:187],v[144:147]\n"
RD"v[204:207],v245 offset:43008\n"
"v_med3_u32 v227,v226,v227,v189\n"
MF"v[152:155],v[212:215],a[188:191],v[152:155]\n"
"v_max_u32 v226,v226,v189\n"
PK"v88,v88,v89\n"
MF"v[152:155],v[208:211],a[172:175],v[152:155]\n"
PK"v89,v90,v91\n"
"v_med3_u32 v228,v227,v228,v190\n"
MF"v[132:135],v[216:219],a[160:163],v[132:135]\n"
RD"v[208:211],v244 offset:45056\n"
"v_med3_u32 v227,v226,v227,v190\n"
MF"v[132:135],v[220:223],a[176:179],v[132:135]\n"
PK"v90,v92,v93\n"
PK"v91,v94,v95\n"
MF"v[140:143],v[220:223],a[180:183],v[140:143]\n"
"v_max_u32 v226,v226,v190\n"
"v_med3_u32 v228,v227,v228,v191\n"
MF"v[140:143],v[216:219],a[164:167],v[140:143]\n"
"v_med3_u32 v227,v226,v227,v191\n"
"v_max_u32 v226,v226,v191\n"
MF"v[148:151],v[216:219],a[168:171],v[148:151]\n"
RD"v[212:215],v245 offset:45056\n"
"v_mov_b32 v229,v226\n"
MF"v[148:151],v[220:223],a[184:187],v[148:151]\n"
MF"v[156:159],v[220:223],a[188:191],v[156:159]\n"
"v_max_u32_dpp v229,v229,v229 row_shr:1 row_mask:0xf bank_mask:0xf\n"
MF"v[156:159],v[216:219],a[172:175],v[156:159]\n"
WT"lgkmcnt(1)\n"
"v_max_u32_dpp v229,v229,v229 row_shr:2 row_mask:0xf bank_mask:0xf\n"
MF"v[128:131],v[192:195],a[192:195],v[128:131]\n"
RD"v[216:219],v244 offset:47104\n"
"v_max_u32_dpp v229,v229,v229 row_shr:4 row_mask:0xf bank_mask:0xf\n"
MF"v[128:131],v[196:199],a[208:211],v[128:131]\n"
MF"v[136:139],v[196:199],a[212:215],v[136:139]\n"
"v_max_u32_dpp v229,v229,v229 row_shr:8 row_mask:0xf bank_mask:0xf\n"
MF"v[136:139],v[192:195],a[196:199],v[136:139]\n"
MF"v[144:147],v[192:195],a[200:203],v[144:147]\n"
RD"v[220:223],v245 offset:47104\n"
"v_max_u32_dpp v229,v229,v229 row_bcast:15 row_mask:0xa bank_mask:0xf\n"
MF"v[144:147],v[196:199],a[216:219],v[144:147]\n"
MF"v[152:155],v[196:199],a[220:223],v[152:155]\n"
"v_max_u32_dpp v229,v229,v229 row_bcast:31 row_mask:0xc bank_mask:0xf\n"
MF"v[152:155],v[192:195],a[204:207],v[152:155]\n"
"v_readlane_b32 s62,v229,63\n"
MF"v[132:135],v[200:203],a[192:195],v[132:135]\n"
MF"v[132:135],v[204:207],a[208:211],v[132:135]\n"
"v_cmp_eq_u32 vcc,s62,v226\n"
"v_writelane_b32 v225,s62,0\n"
MF"v[140:143],v[204:207],a[212:215],v[140:143]\n"
"v_cndmask_b32 v226,v226,v227,vcc\n"
"v_cndmask_b32_e64 v227,v227,0,vcc\n"
MF"v[140:143],v[200:203],a[196:199],v[140:143]\n"
"v_mov_b32 v229,v226\n"
MF"v[148:151],v[200:203],a[200:203],v[148:151]\n"
MF"v[148:151],v[204:207],a[216:219],v[148:151]\n"
WT"lgkmcnt(0)\n"
"s_barrier\n"
MF"v[156:159],v[204:207],a[220:223],v[156:159]\n"
RD"v[192:195],v242 offset:0\n"
"v_max_u32_dpp v229,v229,v229 row_shr:1 row_mask:0xf bank_mask:0xf\n"
MF"v[156:159],v[200:203],a[204:207],v[156:159]\n"
BS"v[24:27],v250,s[8:11],s49 offen offset:3072 sc1\n"
MF"v[128:131],v[208:211],a[224:227],v[128:131]\n"
"v_max_u32_dpp v229,v229,v229 row_shr:2 row_mask:0xf bank_mask:0xf\n"
MF"v[128:131],v[212:215],a[240:243],v[128:131]\n"
MF"v[136:139],v[212:215],a[244:247],v[136:139]\n"
RD"v[196:199],v243 offset:0\n"
"v_max_u32_dpp v229,v229,v229 row_shr:4 row_mask:0xf bank_mask:0xf\n"
MF"v[136:139],v[208:211],a[228:231],v[136:139]\n"
MF"v[144:147],v[208:211],a[232:235],v[144:147]\n"
"v_max_u32_dpp v229,v229,v229 row_shr:8 row_mask:0xf bank_mask:0xf\n"
MF"v[144:147],v[212:215],a[248:251],v[144:147]\n"
MF"v[152:155],v[212:215],a[252:255],v[152:155]\n"
RD"v[200:203],v242 offset:2048\n"
"v_max_u32_dpp v229,v229,v229 row_bcast:15 row_mask:0xa bank_mask:0xf\n"
MF"v[152:155],v[208:211],a[236:239],v[152:155]\n"
MF"v[132:135],v[216:219],a[224:227],v[132:135]\n"
"v_max_u32_dpp v229,v229,v229 row_bcast:31 row_mask:0xc bank_mask:0xf\n"
MF"v[132:135],v[220:223],a[240:243],v[132:135]\n"
"v_readlane_b32 s62,v229,63\n"
MF"v[140:143],v[220:223],a[244:247],v[140:143]\n"
RD"v[204:207],v243 offset:2048\n"
"v_cmp_eq_u32 vcc,s62,v226\n"
MF"v[140:143],v[216:219],a[228:231],v[140:143]\n"
"v_writelane_b32 v225,s62,1\n"
"v_cndmask_b32 v226,v226,v227,vcc\n"
MF"v[148:151],v[216:219],a[232:235],v[148:151]\n"
"v_cndmask_b32_e64 v227,v227,0,vcc\n"
"v_mov_b32 v229,v226\n"
MF"v[148:151],v[220:223],a[248:251],v[148:151]\n"
MF"v[156:159],v[220:223],a[252:255],v[156:159]\n"
RD"v[208:211],v242 offset:4096\n"
"v_max_u32_dpp v229,v229,v229 row_shr:1 row_mask:0xf bank_mask:0xf\n"
MF"v[156:159],v[216:219],a[236:239],v[156:159]\n"
WT"lgkmcnt(2)\n"
"v_max_u32_dpp v229,v229,v229 row_shr:2 row_mask:0xf bank_mask:0xf\n"
MF"v[36:39],v[192:195],a[0:3],0\n"
MF"v[36:39],v[196:199],a[16:19],v[36:39]\n"
RD"v[212:215],v243 offset:4096\n"
"v_max_u32_dpp v229,v229,v229 row_shr:4 row_mask:0xf bank_mask:0xf\n"
MF"v[44:47],v[196:199],a[20:23],0\n"
MF"v[44:47],v[192:195],a[4:7],v[44:47]\n"
"v_max_u32_dpp v229,v229,v229 row_shr:8 row_mask:0xf bank_mask:0xf\n"
PK"v4,v128,v129\n"
MF"v[52:55],v[192:195],a[8:11],0\n"
"v_max_u32_dpp v229,v229,v229 row_bcast:15 row_mask:0xa bank_mask:0xf\n"
PK"v5,v130,v131\n"
MF"v[52:55],v[196:199],a[24:27],v[52:55]\n"
RD"v[216:219],v242 offset:6144\n"
"v_max_u32_dpp v229,v229,v229 row_bcast:31 row_mask:0xc bank_mask:0xf\n"
MF"v[60:63],v[196:199],a[28:31],0\n"
"v_readlane_b32 s62,v229,63\n"
PK"v6,v132,v133\n"
MF"v[60:63],v[192:195],a[12:15],v[60:63]\n"
"v_cmp_eq_u32 vcc,s62,v226\n"
"v_writelane_b32 v225,s62,2\n"
MF"v[68:71],v[200:203],a[0:3],0\n"
WT"lgkmcnt(1)\n"
"v_cndmask_b32 v226,v226,v227,vcc\n"
"v_cndmask_b32_e64 v227,v227,0,vcc\n"
MF"v[68:71],v[204:207],a[16:19],v[68:71]\n"
RD"v[220:223],v243 offset:6144\n"
"v_mov_b32 v229,v226\n"
MF"v[76:79],v[204:207],a[20:23],0\n"
PK"v7,v134,v135\n"
"v_max_u32_dpp v229,v229,v229 row_shr:1 row_mask:0xf bank_mask:0xf\n"
MF"v[76:79],v[200:203],a[4:7],v[76:79]\n"
PK"v12,v136,v137\n"
"v_max_u32_dpp v229,v229,v229 row_shr:2 row_mask:0xf bank_mask:0xf\n"
MF"v[84:87],v[200:203],a[8:11],0\n"
"s_mov_b32 s49,8192\n"
"v_max_u32_dpp v229,v229,v229 row_shr:4 row_mask:0xf bank_mask:0xf\n"
PK"v13,v138,v139\n"
MF"v[84:87],v[204:207],a[24:27],v[84:87]\n"
RD"v[192:195],v242 offset:24576\n"
"v_max_u32_dpp v229,v229,v229 row_shr:8 row_mask:0xf bank_mask:0xf\n"
MF"v[92:95],v[204:207],a[28:31],0\n"
BS"v[32:35],v250,s[8:11],s49 offen offset:0 sc1\n"
MF"v[92:95],v[200:203],a[12:15],v[92:95]\n"
"v_max_u32_dpp v229,v229,v229 row_bcast:15 row_mask:0xa bank_mask:0xf\n"
PK"v14,v140,v141\n"
MF"v[36:39],v[208:211],a[32:35],v[36:39]\n"
"v_max_u32_dpp v229,v229,v229 row_bcast:31 row_mask:0xc bank_mask:0xf\n"
PK"v15,v142,v143\n"
MF"v[36:39],v[212:215],a[48:51],v[36:39]\n"
RD"v[196:199],v243 offset:24576\n"
"v_readlane_b32 s62,v229,63\n"
MF"v[44:47],v[212:215],a[52:55],v[44:47]\n"
PK"v20,v144,v145\n"
"v_cmp_eq_u32 vcc,s62,v226\n"
MF"v[44:47],v[208:211],a[36:39],v[44:47]\n"
"v_writelane_b32 v225,s62,3\n"
"v_cndmask_b32 v226,v226,v227,vcc\n"
MF"v[52:55],v[208:211],a[40:43],v[52:55]\n"
RD"v[200:203],v242 offset:26624\n"
"v_cndmask_b32_e64 v227,v227,0,vcc\n"
MF"v[52:55],v[212:215],a[56:59],v[52:55]\n"
"v_mov_b32 v229,v226\n"
PK"v21,v146,v147\n"
MF"v[60:63],v[212:215],a[60:63],v[60:63]\n"
"v_max_u32_dpp v229,v229,v229 row_shr:1 row_mask:0xf bank_mask:0xf\n"
PK"v22,v148,v149\n"
MF"v[60:63],v[208:211],a[44:47],v[60:63]\n"
WT"lgkmcnt(2)\n"
"v_max_u32_dpp v229,v229,v229 row_shr:2 row_mask:0xf bank_mask:0xf\n"
PK"v23,v150,v151\n"
MF"v[68:71],v[216:219],a[32:35],v[68:71]\n"
RD"v[204:207],v243 offset:26624\n"
"v_max_u32_dpp v229,v229,v229 row_shr:4 row_mask:0xf bank_mask:0xf\n"
MF"v[68:71],v[220:223],a[48:51],v[68:71]\n"
PK"v28,v152,v153\n"
"v_max_u32_dpp v229,v229,v229 row_shr:8 row_mask:0xf bank_mask:0xf\n"
MF"v[76:79],v[220:223],a[52:55],v[76:79]\n"
PK"v29,v154,v155\n"
"v_max_u32_dpp v229,v229,v229 row_bcast:15 row_mask:0xa bank_mask:0xf\n"
MF"v[76:79],v[216:219],a[36:39],v[76:79]\n"
PK"v30,v156,v157\n"
"v_max_u32_dpp v229,v229,v229 row_bcast:31 row_mask:0xc bank_mask:0xf\n"
MF"v[84:87],v[216:219],a[40:43],v[84:87]\n"
RD"v[208:211],v242 offset:28672\n"
"v_readlane_b32 s62,v229,63\n"
MF"v[84:87],v[220:223],a[56:59],v[84:87]\n"
PK"v31,v158,v159\n"
"v_cmp_eq_u32 vcc,s62,v226\n"
MF"v[92:95],v[220:223],a[60:63],v[92:95]\n"
"v_writelane_b32 v225,s62,4\n"
"v_cndmask_b32 v226,v226,v227,vcc\n"
MF"v[92:95],v[216:219],a[44:47],v[92:95]\n"
"v_cndmask_b32_e64 v227,v227,0,vcc\n"
"v_mov_b32 v229,v226\n"
MF"v[36:39],v[192:195],a[64:67],v[36:39]\n"
WT"lgkmcnt(1)\n"
RD"v[212:215],v243 offset:28672\n"
"v_max_u32_dpp v229,v229,v229 row_shr:1 row_mask:0xf bank_mask:0xf\n"
MF"v[36:39],v[196:199],a[80:83],v[36:39]\n"
MF"v[44:47],v[196:199],a[84:87],v[44:47]\n"
"v_max_u32_dpp v229,v229,v229 row_shr:2 row_mask:0xf bank_mask:0xf\n"
MF"v[44:47],v[192:195],a[68:71],v[44:47]\n"
RD"v[216:219],v242 offset:30720\n"
"v_max_u32_dpp v229,v229,v229 row_shr:4 row_mask:0xf bank_mask:0xf\n"
MF"v[52:55],v[192:195],a[72:75],v[52:55]\n"
MF"v[52:55],v[196:199],a[88:91],v[52:55]\n"
"v_max_u32_dpp v229,v229,v229 row_shr:8 row_mask:0xf bank_mask:0xf\n"
MF"v[60:63],v[196:199],a[92:95],v[60:63]\n"
MF"v[60:63],v[192:195],a[76:79],v[60:63]\n"
RD"v[220:223],v243 offset:30720\n"
"v_max_u32_dpp v229,v229,v229 row_bcast:15 row_mask:0xa bank_mask:0xf\n"
MF"v[68:71],v[200:203],a[64:67],v[68:71]\n"
MF"v[68:71],v[204:207],a[80:83],v[68:71]\n"
"v_max_u32_dpp v229,v229,v229 row_bcast:31 row_mask:0xc bank_mask:0xf\n"
MF"v[76:79],v[204:207],a[84:87],v[76:79]\n"
"v_readlane_b32 s62,v229,63\n"
MF"v[76:79],v[200:203],a[68:71],v[76:79]\n"
RD"v[192:195],v244 offset:0\n"
"v_cmp_eq_u32 vcc,s62,v226\n"
MF"v[84:87],v[200:203],a[72:75],v[84:87]\n"
"v_writelane_b32 v225,s62,5\n"
"v_cndmask_b32 v226,v226,v227,vcc\n"
MF"v[84:87],v[204:207],a[88:91],v[84:87]\n"
BS"v[40:43],v250,s[8:11],s49 offen offset:1024 sc1\n"
MF"v[92:95],v[204:207],a[92:95],v[92:95]\n"
"v_cndmask_b32_e64 v227,v227,0,vcc\n"
"v_mov_b32 v229,v226\n"
MF"v[92:95],v[200:203],a[76:79],v[92:95]\n"
WT"lgkmcnt(1)\n"
RD"v[196:199],v245 offset:0\n"
"v_max_u32_dpp v229,v229,v229 row_shr:1 row_mask:0xf bank_mask:0xf\n"
MF"v[36:39],v[208:211],a[96:99],v[36:39]\n"
MF"v[36:39],v[212:215],a[112:115],v[36:39]\n"
"v_max_u32_dpp v229,v229,v229 row_shr:2 row_mask:0xf bank_mask:0xf\n"
MF"v[44:47],v[212:215],a[116:119],v[44:47]\n"
MF"v[44:47],v[208:211],a[100:103],v[44:47]\n"
RD"v[200:203],v244 offset:2048\n"
"v_max_u32_dpp v229,v229,v229 row_shr:4 row_mask:0xf bank_mask:0xf\n"
MF"v[52:55],v[208:211],a[104:107],v[52:55]\n"
MF"v[52:55],v[212:215],a[120:123],v[52:55]\n"
"v_max_u32_dpp v229,v229,v229 row_shr:8 row_mask:0xf bank_mask:0xf\n"
MF"v[60:63],v[212:215],a[124:127],v[60:63]\n"
RD"v[204:207],v245 offset:2048\n"
"v_max_u32_dpp v229,v229,v229 row_bcast:15 row_mask:0xa bank_mask:0xf\n"
MF"v[60:63],v[208:211],a[108:111],v[60:63]\n"
MF"v[68:71],v[216:219],a[96:99],v[68:71]\n"
"v_max_u32_dpp v229,v229,v229 row_bcast:31 row_mask:0xc bank_mask:0xf\n"
MF"v[68:71],v[220:223],a[112:115],v[68:71]\n"
"v_readlane_b32 s62,v229,63\n"
MF"v[76:79],v[220:223],a[116:119],v[76:79]\n"
SA"m0,s37,16384\n"
RD"v[208:211],v244 offset:4096\n"
"v_cmp_eq_u32 vcc,s62,v226\n"
MF"v[76:79],v[216:219],a[100:103],v[76:79]\n"
SA"s48,s39,98304\n"
"v_writelane_b32 v225,s62,6\n"
"v_cndmask_b32 v226,v226,v227,vcc\n"
MF"v[84:87],v[216:219],a[104:107],v[84:87]\n"
BL"v234,s[12:15],s48 offen offset:0 lds\n"
MF"v[84:87],v[220:223],a[120:123],v[84:87]\n"
"v_cndmask_b32_e64 v227,v227,0,vcc\n"
"v_mov_b32 v229,v226\n"
MF"v[92:95],v[220:223],a[124:127],v[92:95]\n"
RD"v[212:215],v245 offset:4096\n"
"v_max_u32_dpp v229,v229,v229 row_shr:1 row_mask:0xf bank_mask:0xf\n"
MF"v[92:95],v[216:219],a[108:111],v[92:95]\n"
WT"lgkmcnt(2)\n"
"v_max_u32_dpp v229,v229,v229 row_shr:2 row_mask:0xf bank_mask:0xf\n"
PK"v96,v96,v97\n"
MF"v[36:39],v[192:195],a[128:131],v[36:39]\n"
"v_max_u32_dpp v229,v229,v229 row_shr:4 row_mask:0xf bank_mask:0xf\n"
PK"v97,v98,v99\n"
MF"v[36:39],v[196:199],a[144:147],v[36:39]\n"
"v_max_u32_dpp v229,v229,v229 row_shr:8 row_mask:0xf bank_mask:0xf\n"
PK"v98,v100,v101\n"
MF"v[44:47],v[196:199],a[148:151],v[44:47]\n"
RD"v[216:219],v244 offset:6144\n"
"v_max_u32_dpp v229,v229,v229 row_bcast:15 row_mask:0xa bank_mask:0xf\n"
MF"v[44:47],v[192:195],a[132:135],v[44:47]\n"
BL"v235,s[12:15],s48 offen offset:1024 lds\n"
MF"v[52:55],v[192:195],a[136:139],v[52:55]\n"
"v_max_u32_dpp v229,v229,v229 row_bcast:31 row_mask:0xc bank_mask:0xf\n"
PK"v99,v102,v103\n"
MF"v[52:55],v[196:199],a[152:155],v[52:55]\n"
RD"v[220:223],v245 offset:6144\n"
"v_readlane_b32 s62,v229,63\n"
MF"v[60:63],v[196:199],a[156:159],v[60:63]\n"
"v_mov_b32 v229,v228\n"
"v_cmp_eq_u32 vcc,s62,v226\n"
MF"v[60:63],v[192:195],a[140:143],v[60:63]\n"
"v_writelane_b32 v225,s62,7\n"
"v_cndmask_b32 v226,v226,v227,vcc\n"
MF"v[68:71],v[200:203],a[128:131],v[68:71]\n"
BL"v236,s[12:15],s48 offen offset:2048 lds\n"
MF"v[68:71],v[204:207],a[144:147],v[68:71]\n"
RD"v[192:195],v244 offset:24576\n"
"v_cndmask_b32_e64 v227,v227,0,vcc\n"
MF"v[76:79],v[204:207],a[148:151],v[76:79]\n"
"v_max_u32_dpp v229,v229,v229 row_shr:1 row_mask:0xf bank_mask:0xf\n"
PK"v104,v104,v105\n"
MF"v[76:79],v[200:203],a[132:135],v[76:79]\n"
"v_max_u32_dpp v229,v229,v229 row_shr:2 row_mask:0xf bank_mask:0xf\n"
PK"v105,v106,v107\n"
MF"v[84:87],v[200:203],a[136:139],v[84:87]\n"
BS"v[48:51],v250,s[8:11],s49 offen offset:2048 sc1\n"
MF"v[84:87],v[204:207],a[152:155],v[84:87]\n"
RD"v[196:199],v245 offset:24576\n"
"v_max_u32_dpp v229,v229,v229 row_shr:4 row_mask:0xf bank_mask:0xf\n"
MF"v[92:95],v[204:207],a[156:159],v[92:95]\n"
BL"v237,s[12:15],s48 offen offset:3072 lds\n"
MF"v[92:95],v[200:203],a[140:143],v[92:95]\n"
WT"lgkmcnt(2)\n"
"v_max_u32_dpp v229,v229,v229 row_shr:8 row_mask:0xf bank_mask:0xf\n"
PK"v106,v108,v109\n"
MF"v[36:39],v[208:211],a[160:163],v[36:39]\n"
"v_max_u32_dpp v229,v229,v229 row_bcast:15 row_mask:0xa bank_mask:0xf\n"
PK"v107,v110,v111\n"
MF"v[36:39],v[212:215],a[176:179],v[36:39]\n"
RD"v[200:203],v244 offset:26624\n"
"v_max_u32_dpp v229,v229,v229 row_bcast:31 row_mask:0xc bank_mask:0xf\n"
MF"v[44:47],v[212:215],a[180:183],v[44:47]\n"
SA"m0,s37,20480\n"
"v_readlane_b32 s63,v229,63\n"
PK"v112,v112,v113\n"
MF"v[44:47],v[208:211],a[164:167],v[44:47]\n"
"s_cmp_gt_u32 s63,s62\n"
"s_cbranch_scc1 L1007_%=\n"
"L1008_%=:\n"
MF"v[52:55],v[208:211],a[168:171],v[52:55]\n"
SA"s48,s98,98304\n"
"v_lshrrev_b32 v229,2,v224\n"
PK"v113,v114,v115\n"
MF"v[52:55],v[212:215],a[184:187],v[52:55]\n"
"ds_write_b32 v229,v225 offset:16384\n"
MF"v[60:63],v[212:215],a[188:191],v[60:63]\n"
BL"v234,s[12:15],s48 offen offset:0 lds\n"
MF"v[60:63],v[208:211],a[172:175],v[60:63]\n"
RD"v[204:207],v245 offset:26624\n"
PK"v114,v116,v117\n"
MF"v[68:71],v[216:219],a[160:163],v[68:71]\n"
RD"v[208:211],v244 offset:28672\n"
PK"v115,v118,v119\n"
MF"v[68:71],v[220:223],a[176:179],v[68:71]\n"
PK"v120,v120,v121\n"
PK"v121,v122,v123\n"
MF"v[76:79],v[220:223],a[180:183],v[76:79]\n"
BL"v235,s[12:15],s48 offen offset:1024 lds\n"
MF"v[76:79],v[216:219],a[164:167],v[76:79]\n"
PK"v122,v124,v125\n"
PK"v123,v126,v127\n"
MF"v[84:87],v[216:219],a[168:171],v[84:87]\n"
RD"v[212:215],v245 offset:28672\n"
MF"v[84:87],v[220:223],a[184:187],v[84:87]\n"
MF"v[92:95],v[220:223],a[188:191],v[92:95]\n"
MF"v[92:95],v[216:219],a[172:175],v[92:95]\n"
WT"lgkmcnt(1)\n"
MF"v[36:39],v[192:195],a[192:195],v[36:39]\n"
RD"v[216:219],v244 offset:30720\n"
MF"v[36:39],v[196:199],a[208:211],v[36:39]\n"
BL"v236,s[12:15],s48 offen offset:2048 lds\n"
MF"v[44:47],v[196:199],a[212:215],v[44:47]\n"
MF"v[44:47],v[192:195],a[196:199],v[44:47]\n"
MF"v[52:55],v[192:195],a[200:203],v[52:55]\n"
RD"v[220:223],v245 offset:30720\n"
MF"v[52:55],v[196:199],a[216:219],v[52:55]\n"
MF"v[60:63],v[196:199],a[220:223],v[60:63]\n"
BL"v237,s[12:15],s48 offen offset:3072 lds\n"
MF"v[60:63],v[192:195],a[204:207],v[60:63]\n"
MF"v[68:71],v[200:203],a[192:195],v[68:71]\n"
MF"v[68:71],v[204:207],a[208:211],v[68:71]\n"
MF"v[76:79],v[204:207],a[212:215],v[76:79]\n"
MF"v[76:79],v[200:203],a[196:199],v[76:79]\n"
BS"v[56:59],v250,s[8:11],s49 offen offset:3072 sc1\n"
MF"v[84:87],v[200:203],a[200:203],v[84:87]\n"
"v_lshrrev_b32 v251,2,v224\n"
"v_and_b32 v251,28,v251\n"
MF"v[84:87],v[204:207],a[216:219],v[84:87]\n"
WT"lgkmcnt(0)\n"
"s_barrier\n"
MF"v[92:95],v[204:207],a[220:223],v[92:95]\n"
RD"v[192:195],v242 offset:8192\n"
"ds_read_b32 v234,v251 offset:16384\n"
MF"v[92:95],v[200:203],a[204:207],v[92:95]\n"
MF"v[36:39],v[208:211],a[224:227],v[36:39]\n"
MF"v[36:39],v[212:215],a[240:243],v[36:39]\n"
MF"v[44:47],v[212:215],a[244:247],v[44:47]\n"
RD"v[196:199],v243 offset:8192\n"
MF"v[44:47],v[208:211],a[228:231],v[44:47]\n"
MF"v[52:55],v[208:211],a[232:235],v[52:55]\n"
MF"v[52:55],v[212:215],a[248:251],v[52:55]\n"
MF"v[60:63],v[212:215],a[252:255],v[60:63]\n"
RD"v[200:203],v242 offset:10240\n"
MF"v[60:63],v[208:211],a[236:239],v[60:63]\n"
WT"lgkmcnt(2)\n"
"v_not_b32 v251,v234\n"
"v_bfe_u32 v252,v251,4,7\n"
MF"v[68:71],v[216:219],a[224:227],v[68:71]\n"
"v_mul_u32_u24 v252,20480,v252\n"
MF"v[68:71],v[220:223],a[240:243],v[68:71]\n"
"v_and_b32 v251,15,v251\n"
MF"v[76:79],v[220:223],a[244:247],v[76:79]\n"
RD"v[204:207],v243 offset:10240\n"
"v_lshl_add_u32 v251,v251,4,v252\n"
MF"v[76:79],v[216:219],a[228:231],v[76:79]\n"
"v_and_b32 v252,0x380,v224\n"
MF"v[84:87],v[216:219],a[232:235],v[84:87]\n"
"v_lshl_add_u32 v251,v252,1,v251\n"
MF"v[84:87],v[220:223],a[248:251],v[84:87]\n"
"v_add_u32 v252,v252,v224\n"
MF"v[92:95],v[220:223],a[252:255],v[92:95]\n"
"s_xor_b32 s92,s34,0\n"
RD"v[208:211],v242 offset:12288\n"
MF"v[92:95],v[216:219],a[236:239],v[92:95]\n"
"s_lshl_b32 s92,s92,2\n"
MF"v[128:131],v[192:195],a[0:3],0\n"
WT"lgkmcnt(2)\n"
SA"s92,s92,s30\n"
MF"v[128:131],v[196:199],a[16:19],v[128:131]\n"
"s_lshl_b32 s93,s92,11\n"
RD"v[212:215],v243 offset:12288\n"
MF"v[136:139],v[196:199],a[20:23],0\n"
"s_cmp_lt_u32 s92,10\n"
"s_cselect_b32 s93,s93,2130706432\n"
MF"v[136:139],v[192:195],a[4:7],v[136:139]\n"
SA"s92,s93,524288\n"
PK"v36,v36,v37\n"
PK"v37,v38,v39\n"
MF"v[144:147],v[192:195],a[8:11],0\n"
BL"v[100:103],v251,s[8:11],s92 offen sc1\n"
MF"v[144:147],v[196:199],a[24:27],v[144:147]\n"
"s_mul_i32 s92,s35,20480\n"
RD"v[216:219],v242 offset:14336\n"
PK"v38,v68,v69\n"
MF"v[152:155],v[196:199],a[28:31],0\n"
SA"s92,s92,s93\n"
PK"v39,v70,v71\n"
MF"v[152:155],v[192:195],a[12:15],v[152:155]\n"
SA"s92,s92,3145728\n"
MF"v[132:135],v[200:203],a[0:3],0\n"
WT"lgkmcnt(1)\n"
PK"v44,v44,v45\n"
PK"v45,v46,v47\n"
MF"v[132:135],v[204:207],a[16:19],v[132:135]\n"
"s_mov_b32 s49,16384\n"
RD"v[220:223],v243 offset:14336\n"
PK"v46,v76,v77\n"
MF"v[140:143],v[204:207],a[20:23],0\n"
BS"v[64:67],v250,s[8:11],s49 offen offset:0 sc1\n"
MF"v[140:143],v[200:203],a[4:7],v[140:143]\n"
PK"v47,v78,v79\n"
MF"v[148:151],v[200:203],a[8:11],0\n"
MF"v[148:151],v[204:207],a[24:27],v[148:151]\n"
RD"v[192:195],v242 offset:32768\n"
PK"v52,v52,v53\n"
MF"v[156:159],v[204:207],a[28:31],0\n"
PK"v53,v54,v55\n"
PK"v54,v84,v85\n"
MF"v[156:159],v[200:203],a[12:15],v[156:159]\n"
PK"v55,v86,v87\n"
MF"v[128:131],v[208:211],a[32:35],v[128:131]\n"
MF"v[128:131],v[212:215],a[48:51],v[128:131]\n"
RD"v[196:199],v243 offset:32768\n"
MF"v[136:139],v[212:215],a[52:55],v[136:139]\n"
PK"v60,v60,v61\n"
PK"v61,v62,v63\n"
MF"v[136:139],v[208:211],a[36:39],v[136:139]\n"
PK"v62,v92,v93\n"
PK"v63,v94,v95\n"
MF"v[144:147],v[208:211],a[40:43],v[144:147]\n"
SA"m0,s36,0\n"
RD"v[200:203],v242 offset:34816\n"
SA"s48,s60,s61\n"
MF"v[144:147],v[212:215],a[56:59],v[144:147]\n"
MF"v[152:155],v[212:215],a[60:63],v[152:155]\n"
BL"v238,s[20:23],s48 offen offset:0 lds\n"
MF"v[152:155],v[208:211],a[44:47],v[152:155]\n"
WT"lgkmcnt(2)\n"
MF"v[132:135],v[216:219],a[32:35],v[132:135]\n"
RD"v[204:207],v243 offset:34816\n"
MF"v[132:135],v[220:223],a[48:51],v[132:135]\n"
MF"v[140:143],v[220:223],a[52:55],v[140:143]\n"
MF"v[140:143],v[216:219],a[36:39],v[140:143]\n"
MF"v[148:151],v[216:219],a[40:43],v[148:151]\n"
RD"v[208:211],v242 offset:36864\n"
MF"v[148:151],v[220:223],a[56:59],v[148:151]\n"
MF"v[156:159],v[220:223],a[60:63],v[156:159]\n"
BL"v239,s[20:23],s48 offen offset:1024 lds\n"
MF"v[156:159],v[216:219],a[44:47],v[156:159]\n"
MF"v[128:131],v[192:195],a[64:67],v[128:131]\n"
WT"lgkmcnt(1)\n"
RD"v[212:215],v243 offset:36864\n"
MF"v[128:131],v[196:199],a[80:83],v[128:131]\n"
MF"v[136:139],v[196:199],a[84:87],v[136:139]\n"
MF"v[136:139],v[192:195],a[68:71],v[136:139]\n"
RD"v[216:219],v242 offset:38912\n"
MF"v[144:147],v[192:195],a[72:75],v[144:147]\n"
MF"v[144:147],v[196:199],a[88:91],v[144:147]\n"
MF"v[152:155],v[196:199],a[92:95],v[152:155]\n"
BL"v240,s[20:23],s48 offen offset:2048 lds\n"
MF"v[152:155],v[192:195],a[76:79],v[152:155]\n"
RD"v[220:223],v243 offset:38912\n"
MF"v[132:135],v[200:203],a[64:67],v[132:135]\n"
MF"v[132:135],v[204:207],a[80:83],v[132:135]\n"
BS"v[72:75],v250,s[8:11],s49 offen offset:1024 sc1\n"
MF"v[140:143],v[204:207],a[84:87],v[140:143]\n"
MF"v[140:143],v[200:203],a[68:71],v[140:143]\n"
RD"v[192:195],v244 offset:8192\n"
MF"v[148:151],v[200:203],a[72:75],v[148:151]\n"
MF"v[148:151],v[204:207],a[88:91],v[148:151]\n"
MF"v[156:159],v[204:207],a[92:95],v[156:159]\n"
SA"s61,s61,524288\n"
BL"v241,s[20:23],s48 offen offset:3072 lds\n"
MF"v[156:159],v[200:203],a[76:79],v[156:159]\n"
WT"lgkmcnt(1)\n"
RD"v[196:199],v245 offset:8192\n"
MF"v[128:131],v[208:211],a[96:99],v[128:131]\n"
MF"v[128:131],v[212:215],a[112:115],v[128:131]\n"
MF"v[136:139],v[212:215],a[116:119],v[136:139]\n"
MF"v[136:139],v[208:211],a[100:103],v[136:139]\n"
RD"v[200:203],v244 offset:10240\n"
MF"v[144:147],v[208:211],a[104:107],v[144:147]\n"
MF"v[144:147],v[212:215],a[120:123],v[144:147]\n"
MF"v[152:155],v[212:215],a[124:127],v[152:155]\n"
RD"v[204:207],v245 offset:10240\n"
MF"v[152:155],v[208:211],a[108:111],v[152:155]\n"
MF"v[132:135],v[216:219],a[96:99],v[132:135]\n"
MF"v[132:135],v[220:223],a[112:115],v[132:135]\n"
MF"v[140:143],v[220:223],a[116:119],v[140:143]\n"
RD"v[208:211],v244 offset:12288\n"
MF"v[140:143],v[216:219],a[100:103],v[140:143]\n"
MF"v[148:151],v[216:219],a[104:107],v[148:151]\n"
SA"m0,s38,65536\n"
SA"s48,s60,s61\n"
MF"v[148:151],v[220:223],a[120:123],v[148:151]\n"
MF"v[156:159],v[220:223],a[124:127],v[156:159]\n"
RD"v[212:215],v245 offset:12288\n"
MF"v[156:159],v[216:219],a[108:111],v[156:159]\n"
WT"lgkmcnt(2)\n"
BL"v238,s[20:23],s48 offen offset:0 lds\n"
MF"v[128:131],v[192:195],a[128:131],v[128:131]\n"
MF"v[128:131],v[196:199],a[144:147],v[128:131]\n"
MF"v[136:139],v[196:199],a[148:151],v[136:139]\n"
RD"v[216:219],v244 offset:14336\n"
MF"v[136:139],v[192:195],a[132:135],v[136:139]\n"
MF"v[144:147],v[192:195],a[136:139],v[144:147]\n"
MF"v[144:147],v[196:199],a[152:155],v[144:147]\n"
RD"v[220:223],v245 offset:14336\n"
MF"v[152:155],v[196:199],a[156:159],v[152:155]\n"
BL"v239,s[20:23],s48 offen offset:1024 lds\n"
MF"v[152:155],v[192:195],a[140:143],v[152:155]\n"
MF"v[132:135],v[200:203],a[128:131],v[132:135]\n"
BS"v[80:83],v250,s[8:11],s49 offen offset:2048 sc1\n"
MF"v[132:135],v[204:207],a[144:147],v[132:135]\n"
RD"v[192:195],v244 offset:32768\n"
MF"v[140:143],v[204:207],a[148:151],v[140:143]\n"
MF"v[140:143],v[200:203],a[132:135],v[140:143]\n"
MF"v[148:151],v[200:203],a[136:139],v[148:151]\n"
MF"v[148:151],v[204:207],a[152:155],v[148:151]\n"
RD"v[196:199],v245 offset:32768\n"
MF"v[156:159],v[204:207],a[156:159],v[156:159]\n"
BL"v240,s[20:23],s48 offen offset:2048 lds\n"
MF"v[156:159],v[200:203],a[140:143],v[156:159]\n"
WT"lgkmcnt(2)\n"
MF"v[128:131],v[208:211],a[160:163],v[128:131]\n"
MF"v[128:131],v[212:215],a[176:179],v[128:131]\n"
RD"v[200:203],v244 offset:34816\n"
MF"v[136:139],v[212:215],a[180:183],v[136:139]\n"
MF"v[136:139],v[208:211],a[164:167],v[136:139]\n"
MF"v[144:147],v[208:211],a[168:171],v[144:147]\n"
MF"v[144:147],v[212:215],a[184:187],v[144:147]\n"
RD"v[204:207],v245 offset:34816\n"
MF"v[152:155],v[212:215],a[188:191],v[152:155]\n"
SA"s61,s61,524288\n"
BL"v241,s[20:23],s48 offen offset:3072 lds\n"
MF"v[152:155],v[208:211],a[172:175],v[152:155]\n"
"s_cmp_eq_u32 s61,5242880\n"
"s_cselect_b32 s61,0,s61\n"
MF"v[132:135],v[216:219],a[160:163],v[132:135]\n"
RD"v[208:211],v244 offset:36864\n"
MF"v[132:135],v[220:223],a[176:179],v[132:135]\n"
MF"v[140:143],v[220:223],a[180:183],v[140:143]\n"
MF"v[140:143],v[216:219],a[164:167],v[140:143]\n"
MF"v[148:151],v[216:219],a[168:171],v[148:151]\n"
SA"m0,s38,114688\n"
RD"v[212:215],v245 offset:36864\n"
SA"s48,s60,s61\n"
MF"v[148:151],v[220:223],a[184:187],v[148:151]\n"
MF"v[156:159],v[220:223],a[188:191],v[156:159]\n"
BL"v238,s[20:23],s48 offen offset:0 lds\n"
MF"v[156:159],v[216:219],a[172:175],v[156:159]\n"
WT"lgkmcnt(1)\n"
MF"v[128:131],v[192:195],a[192:195],v[128:131]\n"
RD"v[216:219],v244 offset:38912\n"
MF"v[128:131],v[196:199],a[208:211],v[128:131]\n"
MF"v[136:139],v[196:199],a[212:215],v[136:139]\n"
MF"v[136:139],v[192:195],a[196:199],v[136:139]\n"
MF"v[144:147],v[192:195],a[200:203],v[144:147]\n"
RD"v[220:223],v245 offset:38912\n"
MF"v[144:147],v[196:199],a[216:219],v[144:147]\n"
MF"v[152:155],v[196:199],a[220:223],v[152:155]\n"
BL"v239,s[20:23],s48 offen offset:1024 lds\n"
MF"v[152:155],v[192:195],a[204:207],v[152:155]\n"
BS"v[88:91],v250,s[8:11],s49 offen offset:3072 sc1\n"
MF"v[132:135],v[200:203],a[192:195],v[132:135]\n"
WT"vmcnt(14)\n"
BS"v[100:103],v252,s[8:11],s92 offen sc1\n"
MF"v[132:135],v[204:207],a[208:211],v[132:135]\n"
MF"v[140:143],v[204:207],a[212:215],v[140:143]\n"
MF"v[140:143],v[200:203],a[196:199],v[140:143]\n"
MF"v[148:151],v[200:203],a[200:203],v[148:151]\n"
MF"v[148:151],v[204:207],a[216:219],v[148:151]\n"
WT"lgkmcnt(0)\n"
"s_barrier\n"
MF"v[156:159],v[204:207],a[220:223],v[156:159]\n"
RD"v[192:195],v242 offset:16384\n"
MF"v[156:159],v[200:203],a[204:207],v[156:159]\n"
BL"v240,s[20:23],s48 offen offset:2048 lds\n"
MF"v[128:131],v[208:211],a[224:227],v[128:131]\n"
MF"v[128:131],v[212:215],a[240:243],v[128:131]\n"
MF"v[136:139],v[212:215],a[244:247],v[136:139]\n"
RD"v[196:199],v243 offset:16384\n"
MF"v[136:139],v[208:211],a[228:231],v[136:139]\n"
MF"v[144:147],v[208:211],a[232:235],v[144:147]\n"
MF"v[144:147],v[212:215],a[248:251],v[144:147]\n"
MF"v[152:155],v[212:215],a[252:255],v[152:155]\n"
SA"s61,s61,524288\n"
RD"v[200:203],v242 offset:18432\n"
MF"v[152:155],v[208:211],a[236:239],v[152:155]\n"
BL"v241,s[20:23],s48 offen offset:3072 lds\n"
MF"v[132:135],v[216:219],a[224:227],v[132:135]\n"
"s_lshl_b32 s50,s34,3\n"
MF"v[132:135],v[220:223],a[240:243],v[132:135]\n"
SA"s51,s50,s30\n"
MF"v[140:143],v[220:223],a[244:247],v[140:143]\n"
"s_mul_i32 s51,s51,20480\n"
RD"v[204:207],v243 offset:18432\n"
MF"v[140:143],v[216:219],a[228:231],v[140:143]\n"
SA"s56,s51,524288\n"
MF"v[148:151],v[216:219],a[232:235],v[148:151]\n"
SA"m0,s36,16384\n"
SA"s48,s60,s61\n"
SA"s57,s56,81920\n"
MF"v[148:151],v[220:223],a[248:251],v[148:151]\n"
"s_sub_u32 s51,s30,s50\n"
MF"v[156:159],v[220:223],a[252:255],v[156:159]\n"
SA"s51,s51,120\n"
RD"v[208:211],v242 offset:20480\n"
MF"v[156:159],v[216:219],a[236:239],v[156:159]\n"
WT"lgkmcnt(2)\n"
BL"v238,s[20:23],s48 offen offset:0 lds\n"
MF"v[0:3],v[192:195],a[0:3],0\n"
"s_mul_i32 s51,s51,20480\n"
SA"s58,s51,524288\n"
"s_lshl_b32 s99,s99,12\n"
MF"v[0:3],v[196:199],a[16:19],v[0:3]\n"
SA"s59,s58,81920\n"
RD"v[212:215],v243 offset:20480\n"
MF"v[16:19],v[196:199],a[20:23],0\n"
"v_add_u32 v235,s99,v224\n"
MF"v[16:19],v[192:195],a[4:7],v[16:19]\n"
PK"v64,v128,v129\n"
PK"v65,v130,v131\n"
MF"v[32:35],v[192:195],a[8:11],0\n"
"s_mov_b32 s49,24576\n"
PK"v66,v132,v133\n"
PK"v67,v134,v135\n"
MF"v[32:35],v[196:199],a[24:27],v[32:35]\n"
RD"v[216:219],v242 offset:22528\n"
MF"v[48:51],v[196:199],a[28:31],0\n"
BS"v[96:99],v250,s[8:11],s49 offen offset:0 sc1\n"
MF"v[48:51],v[192:195],a[12:15],v[48:51]\n"
BL"v239,s[20:23],s48 offen offset:1024 lds\n"
MF"v[8:11],v[200:203],a[0:3],0\n"
WT"lgkmcnt(1)\n"
PK"v68,v136,v137\n"
PK"v69,v138,v139\n"
MF"v[8:11],v[204:207],a[16:19],v[8:11]\n"
RD"v[220:223],v243 offset:22528\n"
PK"v70,v140,v141\n"
MF"v[24:27],v[204:207],a[20:23],0\n"
PK"v71,v142,v143\n"
MF"v[24:27],v[200:203],a[4:7],v[24:27]\n"
MF"v[40:43],v[200:203],a[8:11],0\n"
MF"v[40:43],v[204:207],a[24:27],v[40:43]\n"
RD"v[192:195],v242 offset:40960\n"
PK"v72,v144,v145\n"
MF"v[56:59],v[204:207],a[28:31],0\n"
BL"v240,s[20:23],s48 offen offset:2048 lds\n"
MF"v[56:59],v[200:203],a[12:15],v[56:59]\n"
PK"v73,v146,v147\n"
PK"v74,v148,v149\n"
MF"v[0:3],v[208:211],a[32:35],v[0:3]\n"
BL"a[0:3],v235,s[8:11],s56 offen offset:0 sc1\n"
MF"v[0:3],v[212:215],a[48:51],v[0:3]\n"
RD"v[196:199],v243 offset:40960\n"
PK"v75,v150,v151\n"
MF"v[16:19],v[212:215],a[52:55],v[16:19]\n"
PK"v76,v152,v153\n"
PK"v77,v154,v155\n"
MF"v[16:19],v[208:211],a[36:39],v[16:19]\n"
PK"v78,v156,v157\n"
PK"v79,v158,v159\n"
MF"v[32:35],v[208:211],a[40:43],v[32:35]\n"
RD"v[200:203],v242 offset:43008\n"
MF"v[32:35],v[212:215],a[56:59],v[32:35]\n"
MF"v[48:51],v[212:215],a[60:63],v[48:51]\n"
SA"s61,s61,524288\n"
BL"v241,s[20:23],s48 offen offset:3072 lds\n"
MF"v[48:51],v[208:211],a[44:47],v[48:51]\n"
WT"lgkmcnt(2)\n"
BL"a[4:7],v235,s[8:11],s57 offen offset:0 sc1\n"
MF"v[8:11],v[216:219],a[32:35],v[8:11]\n"
"s_cmp_eq_u32 s61,5242880\n"
RD"v[204:207],v243 offset:43008\n"
"s_cselect_b32 s61,0,s61\n"
MF"v[8:11],v[220:223],a[48:51],v[8:11]\n"
MF"v[24:27],v[220:223],a[52:55],v[24:27]\n"
MF"v[24:27],v[216:219],a[36:39],v[24:27]\n"
MF"v[40:43],v[216:219],a[40:43],v[40:43]\n"
RD"v[208:211],v242 offset:45056\n"
MF"v[40:43],v[220:223],a[56:59],v[40:43]\n"
BL"a[8:11],v235,s[8:11],s58 offen offset:0 sc1\n"
MF"v[56:59],v[220:223],a[60:63],v[56:59]\n"
MF"v[56:59],v[216:219],a[44:47],v[56:59]\n"
MF"v[0:3],v[192:195],a[64:67],v[0:3]\n"
WT"lgkmcnt(1)\n"
RD"v[212:215],v243 offset:45056\n"
MF"v[0:3],v[196:199],a[80:83],v[0:3]\n"
MF"v[16:19],v[196:199],a[84:87],v[16:19]\n"
BL"a[12:15],v235,s[8:11],s59 offen offset:0 sc1\n"
MF"v[16:19],v[192:195],a[68:71],v[16:19]\n"
RD"v[216:219],v242 offset:47104\n"
MF"v[32:35],v[192:195],a[72:75],v[32:35]\n"
MF"v[32:35],v[196:199],a[88:91],v[32:35]\n"
BS"v[104:107],v250,s[8:11],s49 offen offset:1024 sc1\n"
MF"v[48:51],v[196:199],a[92:95],v[48:51]\n"
MF"v[48:51],v[192:195],a[76:79],v[48:51]\n"
RD"v[220:223],v243 offset:47104\n"
MF"v[8:11],v[200:203],a[64:67],v[8:11]\n"
BL"a[16:19],v235,s[8:11],s56 offen offset:1024 sc1\n"
MF"v[8:11],v[204:207],a[80:83],v[8:11]\n"
MF"v[24:27],v[204:207],a[84:87],v[24:27]\n"
MF"v[24:27],v[200:203],a[68:71],v[24:27]\n"
RD"v[192:195],v244 offset:16384\n"
MF"v[40:43],v[200:203],a[72:75],v[40:43]\n"
MF"v[40:43],v[204:207],a[88:91],v[40:43]\n"
MF"v[56:59],v[204:207],a[92:95],v[56:59]\n"
BL"a[20:23],v235,s[8:11],s57 offen offset:1024 sc1\n"
MF"v[56:59],v[200:203],a[76:79],v[56:59]\n"
WT"lgkmcnt(1)\n"
RD"v[196:199],v245 offset:16384\n"
MF"v[0:3],v[208:211],a[96:99],v[0:3]\n"
MF"v[0:3],v[212:215],a[112:115],v[0:3]\n"
MF"v[16:19],v[212:215],a[116:119],v[16:19]\n"
MF"v[16:19],v[208:211],a[100:103],v[16:19]\n"
RD"v[200:203],v244 offset:18432\n"
MF"v[32:35],v[208:211],a[104:107],v[32:35]\n"
BL"a[24:27],v235,s[8:11],s58 offen offset:1024 sc1\n"
MF"v[32:35],v[212:215],a[120:123],v[32:35]\n"
MF"v[48:51],v[212:215],a[124:127],v[48:51]\n"
RD"v[204:207],v245 offset:18432\n"
MF"v[48:51],v[208:211],a[108:111],v[48:51]\n"
MF"v[8:11],v[216:219],a[96:99],v[8:11]\n"
MF"v[8:11],v[220:223],a[112:115],v[8:11]\n"
MF"v[24:27],v[220:223],a[116:119],v[24:27]\n"
RD"v[208:211],v244 offset:20480\n"
MF"v[24:27],v[216:219],a[100:103],v[24:27]\n"
BL"a[28:31],v235,s[8:11],s59 offen offset:1024 sc1\n"
MF"v[40:43],v[216:219],a[104:107],v[40:43]\n"
MF"v[40:43],v[220:223],a[120:123],v[40:43]\n"
MF"v[56:59],v[220:223],a[124:127],v[56:59]\n"
RD"v[212:215],v245 offset:20480\n"
MF"v[56:59],v[216:219],a[108:111],v[56:59]\n"
WT"lgkmcnt(2)\n"
MF"v[0:3],v[192:195],a[128:131],v[0:3]\n"
BL"a[32:35],v235,s[8:11],s56 offen offset:2048 sc1\n"
MF"v[0:3],v[196:199],a[144:147],v[0:3]\n"
MF"v[16:19],v[196:199],a[148:151],v[16:19]\n"
RD"v[216:219],v244 offset:22528\n"
MF"v[16:19],v[192:195],a[132:135],v[16:19]\n"
MF"v[32:35],v[192:195],a[136:139],v[32:35]\n"
BS"v[112:115],v250,s[8:11],s49 offen offset:2048 sc1\n"
MF"v[32:35],v[196:199],a[152:155],v[32:35]\n"
RD"v[220:223],v245 offset:22528\n"
MF"v[48:51],v[196:199],a[156:159],v[48:51]\n"
BL"a[36:39],v235,s[8:11],s57 offen offset:2048 sc1\n"
MF"v[48:51],v[192:195],a[140:143],v[48:51]\n"
MF"v[8:11],v[200:203],a[128:131],v[8:11]\n"
MF"v[8:11],v[204:207],a[144:147],v[8:11]\n"
RD"v[192:195],v244 offset:40960\n"
MF"v[24:27],v[204:207],a[148:151],v[24:27]\n"
MF"v[24:27],v[200:203],a[132:135],v[24:27]\n"
MF"v[40:43],v[200:203],a[136:139],v[40:43]\n"
BL"a[40:43],v235,s[8:11],s58 offen offset:2048 sc1\n"
MF"v[40:43],v[204:207],a[152:155],v[40:43]\n"
RD"v[196:199],v245 offset:40960\n"
MF"v[56:59],v[204:207],a[156:159],v[56:59]\n"
MF"v[56:59],v[200:203],a[140:143],v[56:59]\n"
WT"lgkmcnt(2)\n"
MF"v[0:3],v[208:211],a[160:163],v[0:3]\n"
"s_sub_u32 s53,s37,65536\n"
MF"v[0:3],v[212:215],a[176:179],v[0:3]\n"
RD"v[200:203],v244 offset:43008\n"
MF"v[16:19],v[212:215],a[180:183],v[16:19]\n"
BL"a[44:47],v235,s[8:11],s59 offen offset:2048 sc1\n"
MF"v[16:19],v[208:211],a[164:167],v[16:19]\n"
"v_subrev_u32 v246,s53,v246\n"
"v_subrev_u32 v247,s53,v247\n"
MF"v[32:35],v[208:211],a[168:171],v[32:35]\n"
"v_subrev_u32 v248,s53,v248\n"
"v_subrev_u32 v249,s53,v249\n"
MF"v[32:35],v[212:215],a[184:187],v[32:35]\n"
RD"v[204:207],v245 offset:43008\n"
"v_subrev_u32 v236,65536,v246\n"
MF"v[48:51],v[212:215],a[188:191],v[48:51]\n"
"v_subrev_u32 v237,65536,v247\n"
"v_subrev_u32 v228,65536,v248\n"
MF"v[48:51],v[208:211],a[172:175],v[48:51]\n"
"v_subrev_u32 v229,65536,v249\n"
MF"v[8:11],v[216:219],a[160:163],v[8:11]\n"
RD"v[208:211],v244 offset:45056\n"
MF"v[8:11],v[220:223],a[176:179],v[8:11]\n"
BL"a[48:51],v235,s[8:11],s56 offen offset:3072 sc1\n"
MF"v[24:27],v[220:223],a[180:183],v[24:27]\n"
MF"v[24:27],v[216:219],a[164:167],v[24:27]\n"
MF"v[40:43],v[216:219],a[168:171],v[40:43]\n"
RD"v[212:215],v245 offset:45056\n"
MF"v[40:43],v[220:223],a[184:187],v[40:43]\n"
MF"v[56:59],v[220:223],a[188:191],v[56:59]\n"
BL"a[52:55],v235,s[8:11],s57 offen offset:3072 sc1\n"
MF"v[56:59],v[216:219],a[172:175],v[56:59]\n"
WT"lgkmcnt(1)\n"
MF"v[0:3],v[192:195],a[192:195],v[0:3]\n"
RD"v[216:219],v244 offset:47104\n"
MF"v[0:3],v[196:199],a[208:211],v[0:3]\n"
MF"v[16:19],v[196:199],a[212:215],v[16:19]\n"
MF"v[16:19],v[192:195],a[196:199],v[16:19]\n"
BS"v[120:123],v250,s[8:11],s49 offen offset:3072 sc1\n"
MF"v[32:35],v[192:195],a[200:203],v[32:35]\n"
RD"v[220:223],v245 offset:47104\n"
MF"v[32:35],v[196:199],a[216:219],v[32:35]\n"
BL"a[56:59],v235,s[8:11],s58 offen offset:3072 sc1\n"
MF"v[48:51],v[196:199],a[220:223],v[48:51]\n"
MF"v[48:51],v[192:195],a[204:207],v[48:51]\n"
MF"v[8:11],v[200:203],a[192:195],v[8:11]\n"
SA"s99,s99,4096\n"
MF"v[8:11],v[204:207],a[208:211],v[8:11]\n"
"s_cmp_eq_u32 s99,20480\n"
"s_cselect_b32 s99,0,s99\n"
MF"v[24:27],v[204:207],a[212:215],v[24:27]\n"
BL"a[60:63],v235,s[8:11],s59 offen offset:3072 sc1\n"
MF"v[24:27],v[200:203],a[196:199],v[24:27]\n"
MF"v[40:43],v[200:203],a[200:203],v[40:43]\n"
MF"v[40:43],v[204:207],a[216:219],v[40:43]\n"
WT"lgkmcnt(0)\n"
"s_barrier\n"
MF"v[56:59],v[204:207],a[220:223],v[56:59]\n"
RT"a[128:129],v236 offset:49152\n"
MF"v[56:59],v[200:203],a[204:207],v[56:59]\n"
"v_add_u32 v235,s99,v224\n"
MF"v[0:3],v[208:211],a[224:227],v[0:3]\n"
SA"m0,s38,73728\n"
RT"a[130:131],v236 offset:49664\n"
SA"s48,s60,s61\n"
MF"v[0:3],v[212:215],a[240:243],v[0:3]\n"
BL"a[64:67],v235,s[8:11],s56 offen offset:0 sc1\n"
MF"v[16:19],v[212:215],a[244:247],v[16:19]\n"
RT"a[132:133],v236 offset:57344\n"
"v_add_u32 v242,49152,v246\n"
MF"v[16:19],v[208:211],a[228:231],v[16:19]\n"
BL"v238,s[20:23],s48 offen offset:0 lds\n"
MF"v[32:35],v[208:211],a[232:235],v[32:35]\n"
RT"a[134:135],v236 offset:57856\n"
"v_add_u32 v243,49152,v247\n"
MF"v[32:35],v[212:215],a[248:251],v[32:35]\n"
"v_add_u32 v244,49152,v248\n"
"v_add_u32 v245,49152,v249\n"
MF"v[48:51],v[212:215],a[252:255],v[48:51]\n"
RT"a[136:137],v237 offset:49152\n"
MF"v[48:51],v[208:211],a[236:239],v[48:51]\n"
BL"a[68:71],v235,s[8:11],s57 offen offset:0 sc1\n"
MF"v[8:11],v[216:219],a[224:227],v[8:11]\n"
RT"a[138:139],v237 offset:49664\n"
MF"v[8:11],v[220:223],a[240:243],v[8:11]\n"
MF"v[24:27],v[220:223],a[244:247],v[24:27]\n"
RT"a[140:141],v237 offset:57344\n"
MF"v[24:27],v[216:219],a[228:231],v[24:27]\n"
BL"v239,s[20:23],s48 offen offset:1024 lds\n"
MF"v[40:43],v[216:219],a[232:235],v[40:43]\n"
RT"a[142:143],v237 offset:57856\n"
MF"v[40:43],v[220:223],a[248:251],v[40:43]\n"
BL"a[72:75],v235,s[8:11],s58 offen offset:0 sc1\n"
MF"v[56:59],v[220:223],a[252:255],v[56:59]\n"
RT"a[144:145],v228 offset:49152\n"
MF"v[56:59],v[216:219],a[236:239],v[56:59]\n"
WT"vmcnt(24) lgkmcnt(3)\n"
MF"v[80:83],a[128:131],a[0:3],0\n"
WT"vmcnt(18)\n"
RT"a[146:147],v228 offset:49664\n"
MF"v[80:83],a[132:135],a[16:19],v[80:83]\n"
WT"vmcnt(17)\n"
MF"v[88:91],a[132:135],a[20:23],0\n"
RT"a[148:149],v228 offset:57344\n"
MF"v[88:91],a[128:131],a[4:7],v[88:91]\n"
BL"v240,s[20:23],s48 offen offset:2048 lds\n"
MF"v[96:99],a[128:131],a[8:11],0\n"
WT"vmcnt(17)\n"
BL"a[76:79],v235,s[8:11],s59 offen offset:0 sc1\n"
MF"v[96:99],a[132:135],a[24:27],v[96:99]\n"
WT"vmcnt(17)\n"
RT"a[150:151],v228 offset:57856\n"
RT"a[152:153],v229 offset:49152\n"
MF"v[104:107],a[132:135],a[28:31],0\n"
PK"v0,v0,v1\n"
PK"v1,v2,v3\n"
MF"v[104:107],a[128:131],a[12:15],v[104:107]\n"
RT"a[154:155],v229 offset:49664\n"
PK"v2,v8,v9\n"
MF"v[84:87],a[136:139],a[0:3],0\n"
WT"lgkmcnt(4)\n"
PK"v3,v10,v11\n"
PK"v16,v16,v17\n"
MF"v[84:87],a[140:143],a[16:19],v[84:87]\n"
RT"a[156:157],v229 offset:57344\n"
PK"v17,v18,v19\n"
MF"v[92:95],a[140:143],a[20:23],0\n"
SA"s61,s61,524288\n"
BL"a[80:83],v235,s[8:11],s56 offen offset:1024 sc1\n"
MF"v[92:95],a[136:139],a[4:7],v[92:95]\n"
BL"v241,s[20:23],s48 offen offset:3072 lds\n"
MF"v[100:103],a[136:139],a[8:11],0\n"
RT"a[158:159],v229 offset:57856\n"
PK"v18,v24,v25\n"
MF"v[100:103],a[140:143],a[24:27],v[100:103]\n"
RT"a[160:161],v236 offset:53248\n"
PK"v19,v26,v27\n"
MF"v[108:111],a[140:143],a[28:31],0\n"
PK"v32,v32,v33\n"
PK"v33,v34,v35\n"
MF"v[108:111],a[136:139],a[12:15],v[108:111]\n"
RT"a[162:163],v236 offset:53760\n"
PK"v34,v40,v41\n"
MF"v[112:115],a[144:147],a[0:3],0\n"
WT"lgkmcnt(3)\n"
BL"a[84:87],v235,s[8:11],s57 offen offset:1024 sc1\n"
MF"v[112:115],a[148:151],a[16:19],v[112:115]\n"
RT"a[164:165],v236 offset:61440\n"
PK"v35,v42,v43\n"
MF"v[120:123],a[148:151],a[20:23],0\n"
PK"v48,v48,v49\n"
PK"v49,v50,v51\n"
MF"v[120:123],a[144:147],a[4:7],v[120:123]\n"
RT"a[166:167],v236 offset:61952\n"
PK"v50,v56,v57\n"
MF"v[128:131],a[144:147],a[8:11],0\n"
PK"v51,v58,v59\n"
MF"v[128:131],a[148:151],a[24:27],v[128:131]\n"
RT"a[168:169],v237 offset:53248\n"
MF"v[136:139],a[148:151],a[28:31],0\n"
MF"v[136:139],a[144:147],a[12:15],v[136:139]\n"
RT"a[170:171],v237 offset:53760\n"
MF"v[116:119],a[152:155],a[0:3],0\n"
WT"lgkmcnt(3)\n"
BL"a[88:91],v235,s[8:11],s58 offen offset:1024 sc1\n"
MF"v[116:119],a[156:159],a[16:19],v[116:119]\n"
"s_mov_b32 s49,262144\n"
RT"a[172:173],v237 offset:61440\n"
MF"v[124:127],a[156:159],a[20:23],0\n"
RT"a[174:175],v237 offset:61952\n"
MF"v[124:127],a[152:155],a[4:7],v[124:127]\n"
BS"v[4:7],v250,s[8:11],s49 offen offset:0 sc1\n"
MF"v[132:135],a[152:155],a[8:11],0\n"
RT"a[176:177],v228 offset:53248\n"
MF"v[132:135],a[156:159],a[24:27],v[132:135]\n"
MF"v[140:143],a[156:159],a[28:31],0\n"
RT"a[178:179],v228 offset:53760\n"
MF"v[140:143],a[152:155],a[12:15],v[140:143]\n"
BL"a[92:95],v235,s[8:11],s59 offen offset:1024 sc1\n"
MF"v[144:147],a[160:163],a[0:3],0\n"
WT"lgkmcnt(3)\n"
RT"a[180:181],v228 offset:61440\n"
MF"v[144:147],a[164:167],a[16:19],v[144:147]\n"
MF"v[152:155],a[164:167],a[20:23],0\n"
SA"m0,s38,122880\n"
RT"a[182:183],v228 offset:61952\n"
SA"s48,s60,s61\n"
MF"v[152:155],a[160:163],a[4:7],v[152:155]\n"
MF"v[160:163],a[160:163],a[8:11],0\n"
"s_mov_b32 s88,2048\n"
RT"a[184:185],v229 offset:53248\n"
MF"v[160:163],a[164:167],a[24:27],v[160:163]\n"
BL"v238,s[20:23],s48 offen offset:0 lds\n"
MF"v[168:171],a[164:167],a[28:31],0\n"
BL"a[96:99],v235,s[8:11],s56 offen offset:2048 sc1\n"
MF"v[168:171],a[160:163],a[12:15],v[168:171]\n"
RT"a[186:187],v229 offset:53760\n"
MF"v[148:151],a[168:171],a[0:3],0\n"
WT"lgkmcnt(3)\n"
RT"a[188:189],v229 offset:61440\n"
MF"v[148:151],a[172:175],a[16:19],v[148:151]\n"
MF"v[156:159],a[172:175],a[20:23],0\n"
RT"a[190:191],v229 offset:61952\n"
MF"v[156:159],a[168:171],a[4:7],v[156:159]\n"
RT"a[128:129],v236 offset:32768\n"
MF"v[164:167],a[168:171],a[8:11],0\n"
BL"v239,s[20:23],s48 offen offset:1024 lds\n"
MF"v[164:167],a[172:175],a[24:27],v[164:167]\n"
BL"a[100:103],v235,s[8:11],s57 offen offset:2048 sc1\n"
MF"v[172:175],a[172:175],a[28:31],0\n"
RT"a[130:131],v236 offset:33280\n"
MF"v[172:175],a[168:171],a[12:15],v[172:175]\n"
RT"a[132:133],v236 offset:40960\n"
MF"v[176:179],a[176:179],a[0:3],0\n"
WT"lgkmcnt(4)\n"
MF"v[176:179],a[180:183],a[16:19],v[176:179]\n"
RT"a[134:135],v236 offset:41472\n"
MF"v[184:187],a[180:183],a[20:23],0\n"
MF"v[184:187],a[176:179],a[4:7],v[184:187]\n"
RT"a[136:137],v237 offset:32768\n"
MF"v[192:195],a[176:179],a[8:11],0\n"
BL"a[104:107],v235,s[8:11],s58 offen offset:2048 sc1\n"
MF"v[192:195],a[180:183],a[24:27],v[192:195]\n"
BL"v240,s[20:23],s48 offen offset:2048 lds\n"
MF"v[200:203],a[180:183],a[28:31],0\n"
RT"a[138:139],v237 offset:33280\n"
MF"v[200:203],a[176:179],a[12:15],v[200:203]\n"
RT"a[140:141],v237 offset:40960\n"
MF"v[180:183],a[184:187],a[0:3],0\n"
WT"lgkmcnt(3)\n"
MF"v[180:183],a[188:191],a[16:19],v[180:183]\n"
RT"a[142:143],v237 offset:41472\n"
MF"v[188:191],a[188:191],a[20:23],0\n"
BL"a[108:111],v235,s[8:11],s59 offen offset:2048 sc1\n"
MF"v[188:191],a[184:187],a[4:7],v[188:191]\n"
RT"a[144:145],v228 offset:32768\n"
MF"v[196:199],a[184:187],a[8:11],0\n"
SA"s61,s61,524288\n"
BS"v[12:15],v250,s[8:11],s49 offen offset:1024 sc1\n"
MF"v[196:199],a[188:191],a[24:27],v[196:199]\n"
"s_cmp_eq_u32 s61,5242880\n"
BL"v241,s[20:23],s48 offen offset:3072 lds\n"
MF"v[204:207],a[188:191],a[28:31],0\n"
"s_cselect_b32 s61,0,s61\n"
RT"a[146:147],v228 offset:33280\n"
MF"v[204:207],a[184:187],a[12:15],v[204:207]\n"
WT"vmcnt(31)\n"
RT"a[148:149],v228 offset:40960\n"
MF"v[80:83],a[128:131],a[32:35],v[80:83]\n"
WT"vmcnt(26)\n"
MF"v[80:83],a[132:135],a[48:51],v[80:83]\n"
WT"vmcnt(25)\n"
RT"a[150:151],v228 offset:41472\n"
MF"v[88:91],a[132:135],a[52:55],v[88:91]\n"
BL"a[112:115],v235,s[8:11],s56 offen offset:3072 sc1\n"
MF"v[88:91],a[128:131],a[36:39],v[88:91]\n"
RT"a[152:153],v229 offset:32768\n"
MF"v[96:99],a[128:131],a[40:43],v[96:99]\n"
WT"vmcnt(24)\n"
RT"a[154:155],v229 offset:33280\n"
MF"v[96:99],a[132:135],a[56:59],v[96:99]\n"
WT"vmcnt(23)\n"
MF"v[104:107],a[132:135],a[60:63],v[104:107]\n"
RT"a[156:157],v229 offset:40960\n"
MF"v[104:107],a[128:131],a[44:47],v[104:107]\n"
WT"lgkmcnt(4)\n"
MF"v[84:87],a[136:139],a[32:35],v[84:87]\n"
RT"a[158:159],v229 offset:41472\n"
MF"v[84:87],a[140:143],a[48:51],v[84:87]\n"
BL"a[116:119],v235,s[8:11],s57 offen offset:3072 sc1\n"
MF"v[92:95],a[140:143],a[52:55],v[92:95]\n"
RT"a[160:161],v236 offset:36864\n"
MF"v[92:95],a[136:139],a[36:39],v[92:95]\n"
MF"v[100:103],a[136:139],a[40:43],v[100:103]\n"
RT"a[162:163],v236 offset:37376\n"
MF"v[100:103],a[140:143],a[56:59],v[100:103]\n"
MF"v[108:111],a[140:143],a[60:63],v[108:111]\n"
RT"a[164:165],v236 offset:45056\n"
MF"v[108:111],a[136:139],a[44:47],v[108:111]\n"
BL"a[120:123],v235,s[8:11],s58 offen offset:3072 sc1\n"
MF"v[112:115],a[144:147],a[32:35],v[112:115]\n"
WT"lgkmcnt(3)\n"
RT"a[166:167],v236 offset:45568\n"
MF"v[112:115],a[148:151],a[48:51],v[112:115]\n"
MF"v[120:123],a[148:151],a[52:55],v[120:123]\n"
RT"a[168:169],v237 offset:36864\n"
MF"v[120:123],a[144:147],a[36:39],v[120:123]\n"
MF"v[128:131],a[144:147],a[40:43],v[128:131]\n"
RT"a[170:171],v237 offset:37376\n"
MF"v[128:131],a[148:151],a[56:59],v[128:131]\n"
SA"s99,s99,4096\n"
RT"a[172:173],v237 offset:45056\n"
MF"v[136:139],a[148:151],a[60:63],v[136:139]\n"
"s_cmp_eq_u32 s99,20480\n"
BL"a[124:127],v235,s[8:11],s59 offen offset:3072 sc1\n"
MF"v[136:139],a[144:147],a[44:47],v[136:139]\n"
"s_cselect_b32 s99,0,s99\n"
RT"a[174:175],v237 offset:45568\n"
MF"v[116:119],a[152:155],a[32:35],v[116:119]\n"
MF"v[116:119],a[156:159],a[48:51],v[116:119]\n"
RT"a[176:177],v228 offset:36864\n"
MF"v[124:127],a[156:159],a[52:55],v[124:127]\n"
MF"v[124:127],a[152:155],a[36:39],v[124:127]\n"
RT"a[178:179],v228 offset:37376\n"
MF"v[132:135],a[152:155],a[40:43],v[132:135]\n"
"v_add_u32 v235,s99,v224\n"
MF"v[132:135],a[156:159],a[56:59],v[132:135]\n"
RT"a[180:181],v228 offset:45056\n"
MF"v[140:143],a[156:159],a[60:63],v[140:143]\n"
BL"a[0:3],v235,s[8:11],s56 offen offset:0 sc1\n"
MF"v[140:143],a[152:155],a[44:47],v[140:143]\n"
WT"lgkmcnt(3)\n"
BS"v[20:23],v250,s[8:11],s49 offen offset:2048 sc1\n"
MF"v[144:147],a[160:163],a[32:35],v[144:147]\n"
RT"a[182:183],v228 offset:45568\n"
MF"v[144:147],a[164:167],a[48:51],v[144:147]\n"
RT"a[184:185],v229 offset:36864\n"
MF"v[152:155],a[164:167],a[52:55],v[152:155]\n"
MF"v[152:155],a[160:163],a[36:39],v[152:155]\n"
RT"a[186:187],v229 offset:37376\n"
MF"v[160:163],a[160:163],a[40:43],v[160:163]\n"
BL"a[4:7],v235,s[8:11],s57 offen offset:0 sc1\n"
MF"v[160:163],a[164:167],a[56:59],v[160:163]\n"
RT"a[188:189],v229 offset:45056\n"
MF"v[168:171],a[164:167],a[60:63],v[168:171]\n"
MF"v[168:171],a[160:163],a[44:47],v[168:171]\n"
RT"a[190:191],v229 offset:45568\n"
MF"v[148:151],a[168:171],a[32:35],v[148:151]\n"
"s_lshl_b32 s50,s35,3\n"
SA"s50,s50,s34\n"
"s_lshl_b32 s50,s50,3\n"
MF"v[148:151],a[172:175],a[48:51],v[148:151]\n"
SA"s50,s50,2304\n"
SA"s51,s50,s45\n"
MF"v[156:159],a[172:175],a[52:55],v[156:159]\n"
MF"v[156:159],a[168:171],a[36:39],v[156:159]\n"
BL"a[8:11],v235,s[8:11],s58 offen offset:0 sc1\n"
MF"v[164:167],a[168:171],a[40:43],v[164:167]\n"
MF"v[164:167],a[172:175],a[56:59],v[164:167]\n"
WT"lgkmcnt(0)\n"
"s_barrier\n"
MF"v[172:175],a[172:175],a[60:63],v[172:175]\n"
RT"a[128:129],v236 offset:0\n"
MF"v[172:175],a[168:171],a[44:47],v[172:175]\n"
"buffer_store_dwordx2 v[232:233],v230,s[8:11],s51 offen sc1\n"
MF"v[176:179],a[176:179],a[32:35],v[176:179]\n"
RT"a[130:131],v236 offset:512\n"
MF"v[176:179],a[180:183],a[48:51],v[176:179]\n"
MF"v[184:187],a[180:183],a[52:55],v[184:187]\n"
SA"m0,s38,81920\n"
RT"a[132:133],v236 offset:8192\n"
SA"s48,s60,s61\n"
MF"v[184:187],a[176:179],a[36:39],v[184:187]\n"
BL"a[12:15],v235,s[8:11],s59 offen offset:0 sc1\n"
MF"v[192:195],a[176:179],a[40:43],v[192:195]\n"
RT"a[134:135],v236 offset:8704\n"
MF"v[192:195],a[180:183],a[56:59],v[192:195]\n"
BL"v238,s[20:23],s48 offen offset:0 lds\n"
MF"v[200:203],a[180:183],a[60:63],v[200:203]\n"
RT"a[136:137],v237 offset:0\n"
MF"v[200:203],a[176:179],a[44:47],v[200:203]\n"
MF"v[180:183],a[184:187],a[32:35],v[180:183]\n"
RT"a[138:139],v237 offset:512\n"
MF"v[180:183],a[188:191],a[48:51],v[180:183]\n"
BL"a[16:19],v235,s[8:11],s56 offen offset:1024 sc1\n"
MF"v[188:191],a[188:191],a[52:55],v[188:191]\n"
RT"a[140:141],v237 offset:8192\n"
MF"v[188:191],a[184:187],a[36:39],v[188:191]\n"
MF"v[196:199],a[184:187],a[40:43],v[196:199]\n"
RT"a[142:143],v237 offset:8704\n"
MF"v[196:199],a[188:191],a[56:59],v[196:199]\n"
BL"v239,s[20:23],s48 offen offset:1024 lds\n"
MF"v[204:207],a[188:191],a[60:63],v[204:207]\n"
SA"s49,s88,262144\n"
RT"a[144:145],v228 offset:0\n"
MF"v[204:207],a[184:187],a[44:47],v[204:207]\n"
"LQ_%=:\n"
WT"vmcnt(34) lgkmcnt(7)\n"
MF"v[80:83],a[128:131],a[64:67],v[80:83]\n"
WT"vmcnt(27) lgkmcnt(3)\n"
RT"a[146:147],v228 offset:512\n"
MF"v[80:83],a[132:135],a[80:83],v[80:83]\n"
WT"vmcnt(25)\n"
BS"v[28:31],v250,s[8:11],s49 offen offset:1024 sc1\n"
MF"v[88:91],a[132:135],a[84:87],v[88:91]\n"
BL"a[20:23],v235,s[8:11],s57 offen offset:1024 sc1\n"
MF"v[88:91],a[128:131],a[68:71],v[88:91]\n"
RT"a[148:149],v228 offset:8192\n"
MF"v[96:99],a[128:131],a[72:75],v[96:99]\n"
WT"vmcnt(26)\n"
RT"a[150:151],v228 offset:8704\n"
MF"v[96:99],a[132:135],a[88:91],v[96:99]\n"
WT"vmcnt(24)\n"
BL"v240,s[20:23],s48 offen offset:2048 lds\n"
MF"v[104:107],a[132:135],a[92:95],v[104:107]\n"
RT"a[152:153],v229 offset:0\n"
MF"v[104:107],a[128:131],a[76:79],v[104:107]\n"
RT"a[154:155],v229 offset:512\n"
MF"v[84:87],a[136:139],a[64:67],v[84:87]\n"
WT"lgkmcnt(4)\n"
BL"a[24:27],v235,s[8:11],s58 offen offset:1024 sc1\n"
MF"v[84:87],a[140:143],a[80:83],v[84:87]\n"
RT"a[156:157],v229 offset:8192\n"
MF"v[92:95],a[140:143],a[84:87],v[92:95]\n"
MF"v[92:95],a[136:139],a[68:71],v[92:95]\n"
RT"a[158:159],v229 offset:8704\n"
MF"v[100:103],a[136:139],a[72:75],v[100:103]\n"
SA"s61,s61,524288\n"
BL"v241,s[20:23],s48 offen offset:3072 lds\n"
MF"v[100:103],a[140:143],a[88:91],v[100:103]\n"
RT"a[160:161],v236 offset:4096\n"
MF"v[108:111],a[140:143],a[92:95],v[108:111]\n"
BL"a[28:31],v235,s[8:11],s59 offen offset:1024 sc1\n"
MF"v[108:111],a[136:139],a[76:79],v[108:111]\n"
RT"a[162:163],v236 offset:4608\n"
MF"v[112:115],a[144:147],a[64:67],v[112:115]\n"
WT"lgkmcnt(3)\n"
MF"v[112:115],a[148:151],a[80:83],v[112:115]\n"
RT"a[164:165],v236 offset:12288\n"
MF"v[120:123],a[148:151],a[84:87],v[120:123]\n"
MF"v[120:123],a[144:147],a[68:71],v[120:123]\n"
RT"a[166:167],v236 offset:12800\n"
MF"v[128:131],a[144:147],a[72:75],v[128:131]\n"
MF"v[128:131],a[148:151],a[88:91],v[128:131]\n"
RT"a[168:169],v237 offset:4096\n"
MF"v[136:139],a[148:151],a[92:95],v[136:139]\n"
BL"a[32:35],v235,s[8:11],s56 offen offset:2048 sc1\n"
MF"v[136:139],a[144:147],a[76:79],v[136:139]\n"
RT"a[170:171],v237 offset:4608\n"
MF"v[116:119],a[152:155],a[64:67],v[116:119]\n"
WT"lgkmcnt(3)\n"
RT"a[172:173],v237 offset:12288\n"
MF"v[116:119],a[156:159],a[80:83],v[116:119]\n"
MF"v[124:127],a[156:159],a[84:87],v[124:127]\n"
RT"a[174:175],v237 offset:12800\n"
MF"v[124:127],a[152:155],a[68:71],v[124:127]\n"
MF"v[132:135],a[152:155],a[72:75],v[132:135]\n"
RT"a[176:177],v228 offset:4096\n"
MF"v[132:135],a[156:159],a[88:91],v[132:135]\n"
BL"a[36:39],v235,s[8:11],s57 offen offset:2048 sc1\n"
MF"v[140:143],a[156:159],a[92:95],v[140:143]\n"
RT"a[178:179],v228 offset:4608\n"
MF"v[140:143],a[152:155],a[76:79],v[140:143]\n"
MF"v[144:147],a[160:163],a[64:67],v[144:147]\n"
WT"lgkmcnt(3)\n"
RT"a[180:181],v228 offset:12288\n"
"s_mov_b32 s49,286720\n"
MF"v[144:147],a[164:167],a[80:83],v[144:147]\n"
MF"v[152:155],a[164:167],a[84:87],v[152:155]\n"
RT"a[182:183],v228 offset:12800\n"
MF"v[152:155],a[160:163],a[68:71],v[152:155]\n"
BS"v[0:3],v250,s[8:11],s49 offen offset:0 sc1\n"
MF"v[160:163],a[160:163],a[72:75],v[160:163]\n"
BL"a[40:43],v235,s[8:11],s58 offen offset:2048 sc1\n"
MF"v[160:163],a[164:167],a[88:91],v[160:163]\n"
RT"a[184:185],v229 offset:4096\n"
MF"v[168:171],a[164:167],a[92:95],v[168:171]\n"
RT"a[186:187],v229 offset:4608\n"
MF"v[168:171],a[160:163],a[76:79],v[168:171]\n"
MF"v[148:151],a[168:171],a[64:67],v[148:151]\n"
WT"lgkmcnt(3)\n"
RT"a[188:189],v229 offset:12288\n"
MF"v[148:151],a[172:175],a[80:83],v[148:151]\n"
MF"v[156:159],a[172:175],a[84:87],v[156:159]\n"
RT"a[190:191],v229 offset:12800\n"
MF"v[156:159],a[168:171],a[68:71],v[156:159]\n"
BL"a[44:47],v235,s[8:11],s59 offen offset:2048 sc1\n"
MF"v[164:167],a[168:171],a[72:75],v[164:167]\n"
RT"a[128:129],v246 offset:0\n"
MF"v[164:167],a[172:175],a[88:91],v[164:167]\n"
RT"a[130:131],v246 offset:512\n"
MF"v[172:175],a[172:175],a[92:95],v[172:175]\n"
SA"m0,s38,131072\n"
SA"s48,s60,s61\n"
MF"v[172:175],a[168:171],a[76:79],v[172:175]\n"
RT"a[132:133],v246 offset:24576\n"
MF"v[176:179],a[176:179],a[64:67],v[176:179]\n"
WT"lgkmcnt(4)\n"
BL"v238,s[20:23],s48 offen offset:0 lds\n"
MF"v[176:179],a[180:183],a[80:83],v[176:179]\n"
RT"a[134:135],v246 offset:25088\n"
MF"v[184:187],a[180:183],a[84:87],v[184:187]\n"
BL"a[48:51],v235,s[8:11],s56 offen offset:3072 sc1\n"
MF"v[184:187],a[176:179],a[68:71],v[184:187]\n"
RT"a[136:137],v247 offset:0\n"
MF"v[192:195],a[176:179],a[72:75],v[192:195]\n"
MF"v[192:195],a[180:183],a[88:91],v[192:195]\n"
RT"a[138:139],v247 offset:512\n"
MF"v[200:203],a[180:183],a[92:95],v[200:203]\n"
MF"v[200:203],a[176:179],a[76:79],v[200:203]\n"
RT"a[140:141],v247 offset:24576\n"
MF"v[180:183],a[184:187],a[64:67],v[180:183]\n"
WT"lgkmcnt(3)\n"
BL"v239,s[20:23],s48 offen offset:1024 lds\n"
MF"v[180:183],a[188:191],a[80:83],v[180:183]\n"
BL"a[52:55],v235,s[8:11],s57 offen offset:3072 sc1\n"
MF"v[188:191],a[188:191],a[84:87],v[188:191]\n"
RT"a[142:143],v247 offset:25088\n"
MF"v[188:191],a[184:187],a[68:71],v[188:191]\n"
RT"a[144:145],v248 offset:0\n"
MF"v[196:199],a[184:187],a[72:75],v[196:199]\n"
MF"v[196:199],a[188:191],a[88:91],v[196:199]\n"
RT"a[146:147],v248 offset:512\n"
MF"v[204:207],a[188:191],a[92:95],v[204:207]\n"
MF"v[204:207],a[184:187],a[76:79],v[204:207]\n"
WT"vmcnt(35)\n"
RT"a[148:149],v248 offset:24576\n"
MF"v[80:83],a[128:131],a[96:99],v[80:83]\n"
WT"vmcnt(27)\n"
BL"a[56:59],v235,s[8:11],s58 offen offset:3072 sc1\n"
MF"v[80:83],a[132:135],a[112:115],v[80:83]\n"
WT"vmcnt(27)\n"
BL"v240,s[20:23],s48 offen offset:2048 lds\n"
MF"v[88:91],a[132:135],a[116:119],v[88:91]\n"
RT"a[150:151],v248 offset:25088\n"
RT"a[152:153],v249 offset:0\n"
MF"v[88:91],a[128:131],a[100:103],v[88:91]\n"
MF"v[96:99],a[128:131],a[104:107],v[96:99]\n"
WT"vmcnt(27)\n"
RT"a[154:155],v249 offset:512\n"
MF"v[96:99],a[132:135],a[120:123],v[96:99]\n"
WT"vmcnt(26)\n"
BS"v[16:19],v250,s[8:11],s49 offen offset:1024 sc1\n"
MF"v[104:107],a[132:135],a[124:127],v[104:107]\n"
SA"s99,s99,4096\n"
RT"a[156:157],v249 offset:24576\n"
"s_cmp_eq_u32 s99,20480\n"
MF"v[104:107],a[128:131],a[108:111],v[104:107]\n"
WT"lgkmcnt(4)\n"
BL"a[60:63],v235,s[8:11],s59 offen offset:3072 sc1\n"
MF"v[84:87],a[136:139],a[96:99],v[84:87]\n"
"s_cselect_b32 s99,0,s99\n"
RT"a[158:159],v249 offset:25088\n"
SA"s61,s61,524288\n"
MF"v[84:87],a[140:143],a[112:115],v[84:87]\n"
"s_cmp_eq_u32 s61,5242880\n"
BL"v241,s[20:23],s48 offen offset:3072 lds\n"
MF"v[92:95],a[140:143],a[116:119],v[92:95]\n"
"s_cselect_b32 s61,0,s61\n"
RT"a[160:161],v246 offset:4096\n"
MF"v[92:95],a[136:139],a[100:103],v[92:95]\n"
MF"v[100:103],a[136:139],a[104:107],v[100:103]\n"
RT"a[162:163],v246 offset:4608\n"
"v_add_u32 v235,s99,v224\n"
MF"v[100:103],a[140:143],a[120:123],v[100:103]\n"
BL"a[64:67],v235,s[8:11],s56 offen offset:0 sc1\n"
MF"v[108:111],a[140:143],a[124:127],v[108:111]\n"
RT"a[164:165],v246 offset:28672\n"
MF"v[108:111],a[136:139],a[108:111],v[108:111]\n"
MF"v[112:115],a[144:147],a[96:99],v[112:115]\n"
WT"lgkmcnt(3)\n"
RT"a[166:167],v246 offset:29184\n"
"v_add_u32 v225,s36,v224\n"
MF"v[112:115],a[148:151],a[112:115],v[112:115]\n"
"s_lshl_b32 s39,s35,20\n"
SA"s39,s39,9175040\n"
MF"v[120:123],a[148:151],a[116:119],v[120:123]\n"
RT"a[168:169],v247 offset:4096\n"
MF"v[120:123],a[144:147],a[100:103],v[120:123]\n"
MF"v[128:131],a[144:147],a[104:107],v[128:131]\n"
RT"a[170:171],v247 offset:4608\n"
MF"v[128:131],a[148:151],a[120:123],v[128:131]\n"
BL"a[68:71],v235,s[8:11],s57 offen offset:0 sc1\n"
MF"v[136:139],a[148:151],a[124:127],v[136:139]\n"
RT"a[172:173],v247 offset:28672\n"
MF"v[136:139],a[144:147],a[108:111],v[136:139]\n"
RT"a[174:175],v247 offset:29184\n"
MF"v[116:119],a[152:155],a[96:99],v[116:119]\n"
MF"v[116:119],a[156:159],a[112:115],v[116:119]\n"
RT"a[176:177],v248 offset:4096\n"
MF"v[124:127],a[156:159],a[116:119],v[124:127]\n"
MF"v[124:127],a[152:155],a[100:103],v[124:127]\n"
RT"a[178:179],v248 offset:4608\n"
MF"v[132:135],a[152:155],a[104:107],v[132:135]\n"
BL"a[72:75],v235,s[8:11],s58 offen offset:0 sc1\n"
MF"v[132:135],a[156:159],a[120:123],v[132:135]\n"
RT"a[180:181],v248 offset:28672\n"
MF"v[140:143],a[156:159],a[124:127],v[140:143]\n"
MF"v[140:143],a[152:155],a[108:111],v[140:143]\n"
WT"lgkmcnt(3)\n"
RT"a[182:183],v248 offset:29184\n"
MF"v[144:147],a[160:163],a[96:99],v[144:147]\n"
MF"v[144:147],a[164:167],a[112:115],v[144:147]\n"
RT"a[184:185],v249 offset:4096\n"
MF"v[152:155],a[164:167],a[116:119],v[152:155]\n"
BL"a[76:79],v235,s[8:11],s59 offen offset:0 sc1\n"
MF"v[152:155],a[160:163],a[100:103],v[152:155]\n"
RT"a[186:187],v249 offset:4608\n"
MF"v[160:163],a[160:163],a[104:107],v[160:163]\n"
SA"s49,s88,286720\n"
MF"v[160:163],a[164:167],a[120:123],v[160:163]\n"
RT"a[188:189],v249 offset:28672\n"
MF"v[168:171],a[164:167],a[124:127],v[168:171]\n"
BS"v[32:35],v250,s[8:11],s49 offen offset:0 sc1\n"
MF"v[168:171],a[160:163],a[108:111],v[168:171]\n"
RT"a[190:191],v249 offset:29184\n"
MF"v[148:151],a[168:171],a[96:99],v[148:151]\n"
MF"v[148:151],a[172:175],a[112:115],v[148:151]\n"
BL"a[80:83],v235,s[8:11],s56 offen offset:1024 sc1\n"
MF"v[156:159],a[172:175],a[116:119],v[156:159]\n"
MF"v[156:159],a[168:171],a[100:103],v[156:159]\n"
MF"v[164:167],a[168:171],a[104:107],v[164:167]\n"
MF"v[164:167],a[172:175],a[120:123],v[164:167]\n"
"s_barrier\n"
MF"v[172:175],a[172:175],a[124:127],v[172:175]\n"
RT"a[128:129],v242 offset:0\n"
MF"v[172:175],a[168:171],a[108:111],v[172:175]\n"
WT"lgkmcnt(1)\n"
MF"v[176:179],a[176:179],a[96:99],v[176:179]\n"
RT"a[130:131],v242 offset:512\n"
MF"v[176:179],a[180:183],a[112:115],v[176:179]\n"
BL"a[84:87],v235,s[8:11],s57 offen offset:1024 sc1\n"
MF"v[184:187],a[180:183],a[116:119],v[184:187]\n"
RT"a[132:133],v242 offset:24576\n"
MF"v[184:187],a[176:179],a[100:103],v[184:187]\n"
MF"v[192:195],a[176:179],a[104:107],v[192:195]\n"
RT"a[134:135],v242 offset:25088\n"
MF"v[192:195],a[180:183],a[120:123],v[192:195]\n"
MF"v[200:203],a[180:183],a[124:127],v[200:203]\n"
RT"a[136:137],v243 offset:0\n"
MF"v[200:203],a[176:179],a[108:111],v[200:203]\n"
BL"a[88:91],v235,s[8:11],s58 offen offset:1024 sc1\n"
MF"v[180:183],a[184:187],a[96:99],v[180:183]\n"
RT"a[138:139],v243 offset:512\n"
MF"v[180:183],a[188:191],a[112:115],v[180:183]\n"
MF"v[188:191],a[188:191],a[116:119],v[188:191]\n"
RT"a[140:141],v243 offset:24576\n"
MF"v[188:191],a[184:187],a[100:103],v[188:191]\n"
MF"v[196:199],a[184:187],a[104:107],v[196:199]\n"
RT"a[142:143],v243 offset:25088\n"
MF"v[196:199],a[188:191],a[120:123],v[196:199]\n"
MF"v[204:207],a[188:191],a[124:127],v[204:207]\n"
RT"a[144:145],v244 offset:0\n"
MF"v[204:207],a[184:187],a[108:111],v[204:207]\n"
WT"vmcnt(36) lgkmcnt(3)\n"
BL"a[92:95],v235,s[8:11],s59 offen offset:1024 sc1\n"
MF"v[80:83],a[128:131],a[0:3],v[80:83]\n"
WT"vmcnt(30)\n"
RT"a[146:147],v244 offset:512\n"
MF"v[80:83],a[132:135],a[16:19],v[80:83]\n"
WT"vmcnt(27)\n"
MF"v[88:91],a[132:135],a[20:23],v[88:91]\n"
RT"a[148:149],v244 offset:24576\n"
MF"v[88:91],a[128:131],a[4:7],v[88:91]\n"
MF"v[96:99],a[128:131],a[8:11],v[96:99]\n"
WT"vmcnt(25)\n"
RT"a[150:151],v244 offset:25088\n"
MF"v[96:99],a[132:135],a[24:27],v[96:99]\n"
WT"vmcnt(23)\n"
RT"a[152:153],v245 offset:0\n"
MF"v[104:107],a[132:135],a[28:31],v[104:107]\n"
BL"a[96:99],v235,s[8:11],s56 offen offset:2048 sc1\n"
MF"v[104:107],a[128:131],a[12:15],v[104:107]\n"
RT"a[154:155],v245 offset:512\n"
MF"v[84:87],a[136:139],a[0:3],v[84:87]\n"
WT"lgkmcnt(4)\n"
BS"v[48:51],v250,s[8:11],s49 offen offset:1024 sc1\n"
MF"v[84:87],a[140:143],a[16:19],v[84:87]\n"
RT"a[156:157],v245 offset:24576\n"
MF"v[92:95],a[140:143],a[20:23],v[92:95]\n"
MF"v[92:95],a[136:139],a[4:7],v[92:95]\n"
RT"a[158:159],v245 offset:25088\n"
MF"v[100:103],a[136:139],a[8:11],v[100:103]\n"
BL"a[100:103],v235,s[8:11],s57 offen offset:2048 sc1\n"
MF"v[100:103],a[140:143],a[24:27],v[100:103]\n"
RT"a[160:161],v242 offset:4096\n"
MF"v[108:111],a[140:143],a[28:31],v[108:111]\n"
MF"v[108:111],a[136:139],a[12:15],v[108:111]\n"
RT"a[162:163],v242 offset:4608\n"
MF"v[112:115],a[144:147],a[0:3],v[112:115]\n"
WT"lgkmcnt(3)\n"
MF"v[112:115],a[148:151],a[16:19],v[112:115]\n"
RT"a[164:165],v242 offset:28672\n"
MF"v[120:123],a[148:151],a[20:23],v[120:123]\n"
MF"v[120:123],a[144:147],a[4:7],v[120:123]\n"
RT"a[166:167],v242 offset:29184\n"
MF"v[128:131],a[144:147],a[8:11],v[128:131]\n"
BL"a[104:107],v235,s[8:11],s58 offen offset:2048 sc1\n"
MF"v[128:131],a[148:151],a[24:27],v[128:131]\n"
RT"a[168:169],v243 offset:4096\n"
MF"v[136:139],a[148:151],a[28:31],v[136:139]\n"
MF"v[136:139],a[144:147],a[12:15],v[136:139]\n"
RT"a[170:171],v243 offset:4608\n"
MF"v[116:119],a[152:155],a[0:3],v[116:119]\n"
WT"lgkmcnt(3)\n"
RT"a[172:173],v243 offset:28672\n"
MF"v[116:119],a[156:159],a[16:19],v[116:119]\n"
MF"v[124:127],a[156:159],a[20:23],v[124:127]\n"
RT"a[174:175],v243 offset:29184\n"
MF"v[124:127],a[152:155],a[4:7],v[124:127]\n"
BL"a[108:111],v235,s[8:11],s59 offen offset:2048 sc1\n"
MF"v[132:135],a[152:155],a[8:11],v[132:135]\n"
RT"a[176:177],v244 offset:4096\n"
MF"v[132:135],a[156:159],a[24:27],v[132:135]\n"
MF"v[140:143],a[156:159],a[28:31],v[140:143]\n"
RT"a[178:179],v244 offset:4608\n"
MF"v[140:143],a[152:155],a[12:15],v[140:143]\n"
MF"v[144:147],a[160:163],a[0:3],v[144:147]\n"
WT"lgkmcnt(3)\n"
RT"a[180:181],v244 offset:28672\n"
MF"v[144:147],a[164:167],a[16:19],v[144:147]\n"
BL"a[112:115],v235,s[8:11],s56 offen offset:3072 sc1\n"
MF"v[152:155],a[164:167],a[20:23],v[152:155]\n"
RT"a[182:183],v244 offset:29184\n"
MF"v[152:155],a[160:163],a[4:7],v[152:155]\n"
MF"v[160:163],a[160:163],a[8:11],v[160:163]\n"
RT"a[184:185],v245 offset:4096\n"
MF"v[160:163],a[164:167],a[24:27],v[160:163]\n"
MF"v[168:171],a[164:167],a[28:31],v[168:171]\n"
RT"a[186:187],v245 offset:4608\n"
MF"v[168:171],a[160:163],a[12:15],v[168:171]\n"
MF"v[148:151],a[168:171],a[0:3],v[148:151]\n"
WT"lgkmcnt(3)\n"
RT"a[188:189],v245 offset:28672\n"
"s_mov_b32 s49,270336\n"
MF"v[148:151],a[172:175],a[16:19],v[148:151]\n"
BL"a[116:119],v235,s[8:11],s57 offen offset:3072 sc1\n"
MF"v[156:159],a[172:175],a[20:23],v[156:159]\n"
RT"a[190:191],v245 offset:29184\n"
MF"v[156:159],a[168:171],a[4:7],v[156:159]\n"
BS"v[36:39],v250,s[8:11],s49 offen offset:0 sc1\n"
MF"v[164:167],a[168:171],a[8:11],v[164:167]\n"
RT"a[128:129],v236 offset:16384\n"
MF"v[164:167],a[172:175],a[24:27],v[164:167]\n"
RT"a[130:131],v236 offset:16896\n"
MF"v[172:175],a[172:175],a[28:31],v[172:175]\n"
MF"v[172:175],a[168:171],a[12:15],v[172:175]\n"
RT"a[132:133],v236 offset:24576\n"
MF"v[176:179],a[176:179],a[0:3],v[176:179]\n"
WT"lgkmcnt(4)\n"
BL"a[120:123],v235,s[8:11],s58 offen offset:3072 sc1\n"
MF"v[176:179],a[180:183],a[16:19],v[176:179]\n"
RT"a[134:135],v236 offset:25088\n"
MF"v[184:187],a[180:183],a[20:23],v[184:187]\n"
MF"v[184:187],a[176:179],a[4:7],v[184:187]\n"
RT"a[136:137],v237 offset:16384\n"
MF"v[192:195],a[176:179],a[8:11],v[192:195]\n"
MF"v[192:195],a[180:183],a[24:27],v[192:195]\n"
SA"s99,s99,4096\n"
RT"a[138:139],v237 offset:16896\n"
MF"v[200:203],a[180:183],a[28:31],v[200:203]\n"
"s_cmp_eq_u32 s99,20480\n"
BL"a[124:127],v235,s[8:11],s59 offen offset:3072 sc1\n"
MF"v[200:203],a[176:179],a[12:15],v[200:203]\n"
"s_cselect_b32 s99,0,s99\n"
RT"a[140:141],v237 offset:24576\n"
MF"v[180:183],a[184:187],a[0:3],v[180:183]\n"
WT"lgkmcnt(3)\n"
"v_lshrrev_b32 v252,1,v224\n"
MF"v[180:183],a[188:191],a[16:19],v[180:183]\n"
RT"a[142:143],v237 offset:25088\n"
"v_and_b32 v252,56,v252\n"
MF"v[188:191],a[188:191],a[20:23],v[188:191]\n"
"s_lshl_b32 s50,s35,6\n"
MF"v[188:191],a[184:187],a[4:7],v[188:191]\n"
SA"s50,s50,2304\n"
RT"a[144:145],v228 offset:16384\n"
MF"v[196:199],a[184:187],a[8:11],v[196:199]\n"
"buffer_load_dwordx2 v[208:209],v252,s[8:11],s50 offen sc1\n"
MF"v[196:199],a[188:191],a[24:27],v[196:199]\n"
RT"a[146:147],v228 offset:16896\n"
"v_add_u32 v235,s99,v224\n"
MF"v[204:207],a[188:191],a[28:31],v[204:207]\n"
BL"a[192:195],v235,s[8:11],s56 offen offset:0 sc1\n"
MF"v[204:207],a[184:187],a[12:15],v[204:207]\n"
WT"vmcnt(34)\n"
RT"a[148:149],v228 offset:24576\n"
MF"v[80:83],a[128:131],a[32:35],v[80:83]\n"
WT"vmcnt(28)\n"
MF"v[80:83],a[132:135],a[48:51],v[80:83]\n"
WT"vmcnt(26)\n"
RT"a[150:151],v228 offset:25088\n"
MF"v[88:91],a[132:135],a[52:55],v[88:91]\n"
RT"a[152:153],v229 offset:16384\n"
MF"v[88:91],a[128:131],a[36:39],v[88:91]\n"
MF"v[96:99],a[128:131],a[40:43],v[96:99]\n"
WT"vmcnt(25)\n"
RT"a[154:155],v229 offset:16896\n"
MF"v[96:99],a[132:135],a[56:59],v[96:99]\n"
WT"vmcnt(22)\n"
BL"a[196:199],v235,s[8:11],s57 offen offset:0 sc1\n"
MF"v[104:107],a[132:135],a[60:63],v[104:107]\n"
RT"a[156:157],v229 offset:24576\n"
MF"v[104:107],a[128:131],a[44:47],v[104:107]\n"
WT"lgkmcnt(4)\n"
MF"v[84:87],a[136:139],a[32:35],v[84:87]\n"
RT"a[158:159],v229 offset:25088\n"
MF"v[84:87],a[140:143],a[48:51],v[84:87]\n"
MF"v[92:95],a[140:143],a[52:55],v[92:95]\n"
RT"a[160:161],v236 offset:20480\n"
MF"v[92:95],a[136:139],a[36:39],v[92:95]\n"
BL"a[200:203],v235,s[8:11],s58 offen offset:0 sc1\n"
MF"v[100:103],a[136:139],a[40:43],v[100:103]\n"
RT"a[162:163],v236 offset:20992\n"
MF"v[100:103],a[140:143],a[56:59],v[100:103]\n"
BS"v[44:47],v250,s[8:11],s49 offen offset:1024 sc1\n"
MF"v[108:111],a[140:143],a[60:63],v[108:111]\n"
RT"a[164:165],v236 offset:28672\n"
MF"v[108:111],a[136:139],a[44:47],v[108:111]\n"
MF"v[112:115],a[144:147],a[32:35],v[112:115]\n"
WT"lgkmcnt(3)\n"
RT"a[166:167],v236 offset:29184\n"
MF"v[112:115],a[148:151],a[48:51],v[112:115]\n"
MF"v[120:123],a[148:151],a[52:55],v[120:123]\n"
RT"a[168:169],v237 offset:20480\n"
MF"v[120:123],a[144:147],a[36:39],v[120:123]\n"
BL"a[204:207],v235,s[8:11],s59 offen offset:0 sc1\n"
MF"v[128:131],a[144:147],a[40:43],v[128:131]\n"
RT"a[170:171],v237 offset:20992\n"
MF"v[128:131],a[148:151],a[56:59],v[128:131]\n"
RT"a[172:173],v237 offset:28672\n"
MF"v[136:139],a[148:151],a[60:63],v[136:139]\n"
MF"v[136:139],a[144:147],a[44:47],v[136:139]\n"
RT"a[174:175],v237 offset:29184\n"
MF"v[116:119],a[152:155],a[32:35],v[116:119]\n"
MF"v[116:119],a[156:159],a[48:51],v[116:119]\n"
RT"a[176:177],v228 offset:20480\n"
MF"v[124:127],a[156:159],a[52:55],v[124:127]\n"
BL"a[208:211],v235,s[8:11],s56 offen offset:1024 sc1\n"
MF"v[124:127],a[152:155],a[36:39],v[124:127]\n"
RT"a[178:179],v228 offset:20992\n"
MF"v[132:135],a[152:155],a[40:43],v[132:135]\n"
MF"v[132:135],a[156:159],a[56:59],v[132:135]\n"
RT"a[180:181],v228 offset:28672\n"
MF"v[140:143],a[156:159],a[60:63],v[140:143]\n"
MF"v[140:143],a[152:155],a[44:47],v[140:143]\n"
WT"lgkmcnt(3)\n"
RT"a[182:183],v228 offset:29184\n"
MF"v[144:147],a[160:163],a[32:35],v[144:147]\n"
BL"a[212:215],v235,s[8:11],s57 offen offset:1024 sc1\n"
MF"v[144:147],a[164:167],a[48:51],v[144:147]\n"
RT"a[184:185],v229 offset:20480\n"
MF"v[152:155],a[164:167],a[52:55],v[152:155]\n"
MF"v[152:155],a[160:163],a[36:39],v[152:155]\n"
RT"a[186:187],v229 offset:20992\n"
MF"v[160:163],a[160:163],a[40:43],v[160:163]\n"
MF"v[160:163],a[164:167],a[56:59],v[160:163]\n"
RT"a[188:189],v229 offset:28672\n"
MF"v[168:171],a[164:167],a[60:63],v[168:171]\n"
MF"v[168:171],a[160:163],a[44:47],v[168:171]\n"
RT"a[190:191],v229 offset:29184\n"
MF"v[148:151],a[168:171],a[32:35],v[148:151]\n"
BL"a[216:219],v235,s[8:11],s58 offen offset:1024 sc1\n"
MF"v[148:151],a[172:175],a[48:51],v[148:151]\n"
WT"vmcnt(8)\n"
"v_cmp_eq_u64 vcc,s[28:29],v[208:209]\n"
"s_cmp_eq_u64 vcc,exec\n"
"s_cbranch_scc0 L1010_%=\n"
"L1011_%=:\n"
MF"v[156:159],a[172:175],a[52:55],v[156:159]\n"
MF"v[156:159],a[168:171],a[36:39],v[156:159]\n"
MF"v[164:167],a[168:171],a[40:43],v[164:167]\n"
SA"s49,s88,270336\n"
MF"v[164:167],a[172:175],a[56:59],v[164:167]\n"
WT"lgkmcnt(0)\n"
"s_barrier\n"
MF"v[172:175],a[172:175],a[60:63],v[172:175]\n"
SA"m0,s36,0\n"
RT"a[128:129],v246 offset:8192\n"
SA"s48,s39,0\n"
MF"v[172:175],a[168:171],a[44:47],v[172:175]\n"
BS"v[52:55],v250,s[8:11],s49 offen offset:0 sc1\n"
MF"v[176:179],a[176:179],a[32:35],v[176:179]\n"
BL"a[220:223],v235,s[8:11],s59 offen offset:1024 sc1\n"
MF"v[176:179],a[180:183],a[48:51],v[176:179]\n"
RT"a[130:131],v246 offset:8704\n"
MF"v[184:187],a[180:183],a[52:55],v[184:187]\n"
BL"v225,s[8:11],s48 offen offset:0 sc1 lds\n"
MF"v[184:187],a[176:179],a[36:39],v[184:187]\n"
RT"a[132:133],v246 offset:32768\n"
"v_lshlrev_b32 v254,10,v224\n"
MF"v[192:195],a[176:179],a[40:43],v[192:195]\n"
"s_lshl_b32 s94,s34,1\n"
RT"a[134:135],v246 offset:33280\n"
"v_lshrrev_b32 v255,4,v224\n"
MF"v[192:195],a[180:183],a[56:59],v[192:195]\n"
SA"s95,s94,2\n"
BL"a[224:227],v235,s[8:11],s56 offen offset:2048 sc1\n"
MF"v[200:203],a[180:183],a[60:63],v[200:203]\n"
"s_sub_u32 s94,32,s94\n"
RT"a[136:137],v247 offset:8192\n"
MF"v[200:203],a[176:179],a[44:47],v[200:203]\n"
"v_add_u32 v228,s39,v254\n"
MF"v[180:183],a[184:187],a[32:35],v[180:183]\n"
RT"a[138:139],v247 offset:8704\n"
"v_and_b32 v254,1,v255\n"
MF"v[180:183],a[188:191],a[48:51],v[180:183]\n"
BL"v225,s[8:11],s48 offen offset:1024 sc1 lds\n"
MF"v[188:191],a[188:191],a[52:55],v[188:191]\n"
RT"a[140:141],v247 offset:32768\n"
"v_mul_u32_u24 v254,491520,v254\n"
MF"v[188:191],a[184:187],a[36:39],v[188:191]\n"
"v_cmp_gt_u32 vcc,s95,v255\n"
"v_mov_b32 v229,16384\n"
MF"v[196:199],a[184:187],a[40:43],v[196:199]\n"
RT"a[142:143],v247 offset:33280\n"
"v_mov_b32 v253,516096\n"
MF"v[196:199],a[188:191],a[56:59],v[196:199]\n"
BL"a[228:231],v235,s[8:11],s57 offen offset:2048 sc1\n"
MF"v[204:207],a[188:191],a[60:63],v[204:207]\n"
RT"a[144:145],v248 offset:8192\n"
"v_cndmask_b32_e64 v254,v254,0,vcc\n"
MF"v[204:207],a[184:187],a[44:47],v[204:207]\n"
WT"lgkmcnt(3)\n"
"v_cndmask_b32 v229,v229,v253,vcc\n"
"v_cmp_eq_u32 vcc,s94,v255\n"
MF"v[80:83],a[128:131],a[64:67],v[80:83]\n"
RT"a[146:147],v248 offset:8704\n"
"v_mov_b32 v253,507904\n"
MF"v[80:83],a[132:135],a[80:83],v[80:83]\n"
BL"v225,s[8:11],s48 offen offset:2048 sc1 lds\n"
MF"v[88:91],a[132:135],a[84:87],v[88:91]\n"
RT"a[148:149],v248 offset:32768\n"
"v_cndmask_b32 v254,v254,v253,vcc\n"
MF"v[88:91],a[128:131],a[68:71],v[88:91]\n"
BL"a[232:235],v235,s[8:11],s58 offen offset:2048 sc1\n"
MF"v[96:99],a[128:131],a[72:75],v[96:99]\n"
RT"a[150:151],v248 offset:33280\n"
"v_mov_b32 v253,1862270976\n"
MF"v[96:99],a[132:135],a[88:91],v[96:99]\n"
"s_mul_i32 s95,s35,163840\n"
RT"a[152:153],v249 offset:8192\n"
"v_cndmask_b32 v229,v229,v253,vcc\n"
MF"v[104:107],a[132:135],a[92:95],v[104:107]\n"
SA"s95,s95,3768320\n"
"v_add_u32 v228,v228,v254\n"
MF"v[104:107],a[128:131],a[76:79],v[104:107]\n"
RT"a[154:155],v249 offset:8704\n"
"v_subrev_u32 v254,s94,v255\n"
MF"v[84:87],a[136:139],a[64:67],v[84:87]\n"
WT"lgkmcnt(4)\n"
BL"v225,s[8:11],s48 offen offset:3072 sc1 lds\n"
MF"v[84:87],a[140:143],a[80:83],v[84:87]\n"
RT"a[156:157],v249 offset:32768\n"
"v_lshlrev_b32 v254,15,v254\n"
MF"v[92:95],a[140:143],a[84:87],v[92:95]\n"
BL"a[236:239],v235,s[8:11],s59 offen offset:2048 sc1\n"
MF"v[92:95],a[136:139],a[68:71],v[92:95]\n"
RT"a[158:159],v249 offset:33280\n"
"v_add_u32 v254,s95,v254\n"
MF"v[100:103],a[136:139],a[72:75],v[100:103]\n"
"v_cmp_lt_u32 vcc,s94,v255\n"
MF"v[100:103],a[140:143],a[88:91],v[100:103]\n"
"s_mov_b32 s47,s36\n"
RT"a[160:161],v246 offset:12288\n"
"v_cndmask_b32 v228,v228,v254,vcc\n"
MF"v[108:111],a[140:143],a[92:95],v[108:111]\n"
SA"m0,s36,16384\n"
"v_add_u32 v229,v229,v228\n"
SA"s48,s39,516096\n"
MF"v[108:111],a[136:139],a[76:79],v[108:111]\n"
"s_cmp_lt_u32 s30,2\n"
RT"a[162:163],v246 offset:12800\n"
"s_cselect_b32 s48,2130706432,s48\n"
MF"v[112:115],a[144:147],a[64:67],v[112:115]\n"
WT"lgkmcnt(3)\n"
BS"v[60:63],v250,s[8:11],s49 offen offset:1024 sc1\n"
MF"v[112:115],a[148:151],a[80:83],v[112:115]\n"
SA"s46,s36,163840\n"
BL"v225,s[8:11],s48 offen offset:0 sc1 lds\n"
MF"v[120:123],a[148:151],a[84:87],v[120:123]\n"
SA"s40,s36,65536\n"
RT"a[164:165],v246 offset:36864\n"
"v_mov_b32 v226,v224\n"
MF"v[120:123],a[144:147],a[68:71],v[120:123]\n"
"s_mov_b32 s41,0\n"
BL"a[240:243],v235,s[8:11],s56 offen offset:3072 sc1\n"
MF"v[128:131],a[144:147],a[72:75],v[128:131]\n"
"s_mov_b32 s42,2\n"
RT"a[166:167],v246 offset:37376\n"
"v_bfe_u32 v254,v224,4,4\n"
MF"v[128:131],a[148:151],a[88:91],v[128:131]\n"
RT"a[168:169],v247 offset:12288\n"
"v_lshrrev_b32 v255,8,v224\n"
MF"v[136:139],a[148:151],a[92:95],v[136:139]\n"
"s_lshl_b32 s94,s30,4\n"
"v_lshlrev_b32 v255,2,v255\n"
"v_sub_u32 v254,v254,v255\n"
MF"v[136:139],a[144:147],a[76:79],v[136:139]\n"
RT"a[170:171],v247 offset:12800\n"
"v_add_u32 v254,s94,v254\n"
MF"v[116:119],a[152:155],a[64:67],v[116:119]\n"
WT"lgkmcnt(4)\n"
RT"a[172:173],v247 offset:36864\n"
"v_mov_b32 v255,0xff800000\n"
MF"v[116:119],a[156:159],a[80:83],v[116:119]\n"
BL"a[244:247],v235,s[8:11],s57 offen offset:3072 sc1\n"
MF"v[124:127],a[156:159],a[84:87],v[124:127]\n"
BL"v225,s[8:11],s48 offen offset:1024 sc1 lds\n"
MF"v[124:127],a[152:155],a[68:71],v[124:127]\n"
RT"a[174:175],v247 offset:37376\n"
"v_cmp_gt_i32_e64 vcc,0,v254\n"
MF"v[132:135],a[152:155],a[72:75],v[132:135]\n"
RT"a[176:177],v248 offset:12288\n"
"v_cndmask_b32_e64 v208,0,v255,vcc\n"
MF"v[132:135],a[156:159],a[88:91],v[132:135]\n"
"v_cmp_gt_i32_e64 s[52:53],1,v254\n"
"v_cmp_gt_i32_e64 s[62:63],2,v254\n"
MF"v[140:143],a[156:159],a[92:95],v[140:143]\n"
RT"a[178:179],v248 offset:12800\n"
"v_cndmask_b32_e64 v209,0,v255,s[52:53]\n"
MF"v[140:143],a[152:155],a[76:79],v[140:143]\n"
BL"a[248:251],v235,s[8:11],s58 offen offset:3072 sc1\n"
MF"v[144:147],a[160:163],a[64:67],v[144:147]\n"
WT"lgkmcnt(3)\n"
RT"a[180:181],v248 offset:36864\n"
"v_cndmask_b32_e64 v210,0,v255,s[62:63]\n"
MF"v[144:147],a[164:167],a[80:83],v[144:147]\n"
BL"v225,s[8:11],s48 offen offset:2048 sc1 lds\n"
MF"v[152:155],a[164:167],a[84:87],v[152:155]\n"
RT"a[182:183],v248 offset:37376\n"
"v_cmp_gt_i32_e64 vcc,3,v254\n"
MF"v[152:155],a[160:163],a[68:71],v[152:155]\n"
"v_cmp_gt_i32_e64 s[52:53],16,v254\n"
"v_cndmask_b32_e64 v211,0,v255,vcc\n"
MF"v[160:163],a[160:163],a[72:75],v[160:163]\n"
RT"a[184:185],v249 offset:12288\n"
"v_cndmask_b32_e64 v212,0,v255,s[52:53]\n"
MF"v[160:163],a[164:167],a[88:91],v[160:163]\n"
"v_cmp_gt_i32_e64 s[62:63],17,v254\n"
"v_cmp_gt_i32_e64 vcc,18,v254\n"
MF"v[168:171],a[164:167],a[92:95],v[168:171]\n"
RT"a[186:187],v249 offset:12800\n"
"v_cndmask_b32_e64 v213,0,v255,s[62:63]\n"
MF"v[168:171],a[160:163],a[76:79],v[168:171]\n"
BL"a[252:255],v235,s[8:11],s59 offen offset:3072 sc1\n"
MF"v[148:151],a[168:171],a[64:67],v[148:151]\n"
WT"lgkmcnt(3)\n"
RT"a[188:189],v249 offset:36864\n"
"v_cndmask_b32_e64 v214,0,v255,vcc\n"
MF"v[148:151],a[172:175],a[80:83],v[148:151]\n"
BL"v225,s[8:11],s48 offen offset:3072 sc1 lds\n"
MF"v[156:159],a[172:175],a[84:87],v[156:159]\n"
RT"a[190:191],v249 offset:37376\n"
"v_cmp_gt_i32_e64 s[52:53],19,v254\n"
MF"v[156:159],a[168:171],a[68:71],v[156:159]\n"
RT"a[128:129],v242 offset:8192\n"
"v_cndmask_b32_e64 v215,0,v255,s[52:53]\n"
MF"v[164:167],a[168:171],a[72:75],v[164:167]\n"
"v_cmp_gt_i32_e64 s[62:63],32,v254\n"
"v_cmp_gt_i32_e64 vcc,33,v254\n"
MF"v[164:167],a[172:175],a[88:91],v[164:167]\n"
RT"a[130:131],v242 offset:8704\n"
"v_cndmask_b32_e64 v216,0,v255,s[62:63]\n"
MF"v[172:175],a[172:175],a[92:95],v[172:175]\n"
SA"m0,s36,32768\n"
"v_cndmask_b32_e64 v217,0,v255,vcc\n"
"v_cmp_gt_i32_e64 s[52:53],34,v254\n"
MF"v[172:175],a[168:171],a[76:79],v[172:175]\n"
SA"s48,s39,16384\n"
RT"a[132:133],v242 offset:32768\n"
"v_cndmask_b32_e64 v218,0,v255,s[52:53]\n"
MF"v[176:179],a[176:179],a[64:67],v[176:179]\n"
WT"lgkmcnt(4)\n"
BL"v225,s[8:11],s48 offen offset:0 sc1 lds\n"
MF"v[176:179],a[180:183],a[80:83],v[176:179]\n"
"s_mov_b32 s49,278528\n"
RT"a[134:135],v242 offset:33280\n"
"v_cmp_gt_i32_e64 s[62:63],35,v254\n"
MF"v[184:187],a[180:183],a[84:87],v[184:187]\n"
BS"v[64:67],v250,s[8:11],s49 offen offset:0 sc1\n"
MF"v[184:187],a[176:179],a[68:71],v[184:187]\n"
RT"a[136:137],v243 offset:8192\n"
"v_cndmask_b32_e64 v219,0,v255,s[62:63]\n"
MF"v[192:195],a[176:179],a[72:75],v[192:195]\n"
"s_lshl_b32 s59,s34,5\n"
"v_cmp_gt_i32_e64 vcc,48,v254\n"
"v_cmp_gt_i32_e64 s[52:53],49,v254\n"
MF"v[192:195],a[180:183],a[88:91],v[192:195]\n"
"s_sub_u32 s60,480,s59\n"
RT"a[138:139],v243 offset:8704\n"
"v_cndmask_b32_e64 v220,0,v255,vcc\n"
MF"v[200:203],a[180:183],a[92:95],v[200:203]\n"
SA"s59,s59,s35\n"
"v_cndmask_b32_e64 v221,0,v255,s[52:53]\n"
"v_cmp_gt_i32_e64 s[62:63],50,v254\n"
MF"v[200:203],a[176:179],a[76:79],v[200:203]\n"
SA"s60,s60,s35\n"
RT"a[140:141],v243 offset:32768\n"
"v_cndmask_b32_e64 v222,0,v255,s[62:63]\n"
MF"v[180:183],a[184:187],a[64:67],v[180:183]\n"
WT"lgkmcnt(3)\n"
BL"v225,s[8:11],s48 offen offset:1024 sc1 lds\n"
MF"v[180:183],a[188:191],a[80:83],v[180:183]\n"
"s_lshl_b32 s59,s59,15\n"
RT"a[142:143],v243 offset:33280\n"
"v_cmp_gt_i32_e64 vcc,51,v254\n"
MF"v[188:191],a[188:191],a[84:87],v[188:191]\n"
"s_lshl_b32 s60,s60,15\n"
"v_cndmask_b32_e64 v223,0,v255,vcc\n"
SA"s50,s36,42729472\n"
MF"v[188:191],a[184:187],a[68:71],v[188:191]\n"
SA"s59,s59,s50\n"
RT"a[144:145],v244 offset:8192\n"
SA"s60,s60,s50\n"
MF"v[196:199],a[184:187],a[72:75],v[196:199]\n"
"s_lshl_b32 s61,s34,3\n"
SA"s61,s61,s32\n"
"s_lshl_b32 s61,s61,2\n"
MF"v[196:199],a[188:191],a[88:91],v[196:199]\n"
SA"s61,s61,s30\n"
RT"a[146:147],v244 offset:8704\n"
"s_lshl_b32 s61,s61,2\n"
MF"v[204:207],a[188:191],a[92:95],v[204:207]\n"
SA"s61,s61,s33\n"
"s_lshl_b32 s61,s61,6\n"
MF"v[204:207],a[184:187],a[76:79],v[204:207]\n"
SA"s61,s61,393216\n"
RT"a[148:149],v244 offset:32768\n"
"s_lshl_b32 s98,s35,3\n"
MF"v[80:83],a[128:131],a[96:99],v[80:83]\n"
SA"s98,s98,s34\n"
BL"v225,s[8:11],s48 offen offset:2048 sc1 lds\n"
MF"v[80:83],a[132:135],a[112:115],v[80:83]\n"
"s_lshl_b32 s98,s98,3\n"
RT"a[150:151],v244 offset:33280\n"
MF"v[88:91],a[132:135],a[116:119],v[88:91]\n"
SA"s98,s98,4352\n"
RT"a[152:153],v245 offset:8192\n"
SA"s98,s98,s45\n"
MF"v[88:91],a[128:131],a[100:103],v[88:91]\n"
"s_lshl_b32 s50,s34,2\n"
MF"v[96:99],a[128:131],a[104:107],v[96:99]\n"
"s_mul_i32 s51,s33,0x1011100\n"
RT"a[154:155],v245 offset:8704\n"
SA"s51,s51,0xf48c00\n"
MF"v[96:99],a[132:135],a[120:123],v[96:99]\n"
"s_lshr_b32 s51,s51,s50\n"
MF"v[104:107],a[132:135],a[124:127],v[104:107]\n"
"s_and_b32 s51,s51,15\n"
RT"a[156:157],v245 offset:32768\n"
"s_lshr_b32 s50,s31,3\n"
MF"v[104:107],a[128:131],a[108:111],v[104:107]\n"
WT"lgkmcnt(4)\n"
"s_cmp_eq_u32 s50,6\n"
"s_cselect_b32 s51,15,s51\n"
MF"v[84:87],a[136:139],a[96:99],v[84:87]\n"
"s_cmp_eq_u32 s50,26\n"
RT"a[158:159],v245 offset:33280\n"
"s_cselect_b32 s51,0,s51\n"
MF"v[84:87],a[140:143],a[112:115],v[84:87]\n"
"s_mul_i32 s58,s32,14\n"
BL"v225,s[8:11],s48 offen offset:3072 sc1 lds\n"
MF"v[92:95],a[140:143],a[116:119],v[92:95]\n"
SA"s58,s58,s51\n"
RT"a[160:161],v242 offset:12288\n"
"s_sub_u32 s58,s58,1\n"
MF"v[92:95],a[136:139],a[100:103],v[92:95]\n"
"s_cmp_lt_u32 s58,108\n"
"s_cselect_b32 s90,s51,0\n"
"s_cmp_lg_u32 s90,0\n"
MF"v[100:103],a[136:139],a[104:107],v[100:103]\n"
"s_cselect_b32 s57,1,0\n"
RT"a[162:163],v242 offset:12800\n"
"s_cmp_eq_u32 s51,15\n"
MF"v[100:103],a[140:143],a[120:123],v[100:103]\n"
"s_cselect_b32 s57,2,s57\n"
"s_cmp_eq_u32 s50,6\n"
"s_cselect_b32 s90,4,s33\n"
MF"v[108:111],a[140:143],a[124:127],v[108:111]\n"
"s_mul_i32 s50,s32,5\n"
RT"a[164:165],v242 offset:36864\n"
SA"m0,s36,49152\n"
MF"v[108:111],a[136:139],a[108:111],v[108:111]\n"
SA"s48,s39,532480\n"
SA"s90,s90,s50\n"
"s_cmp_eq_u32 s57,2\n"
MF"v[112:115],a[144:147],a[96:99],v[112:115]\n"
WT"lgkmcnt(3)\n"
RT"a[166:167],v242 offset:37376\n"
"s_cselect_b32 s58,s90,s58\n"
MF"v[112:115],a[148:151],a[112:115],v[112:115]\n"
"s_lshl_b32 s56,s34,1\n"
BL"v225,s[8:11],s48 offen offset:0 sc1 lds\n"
MF"v[120:123],a[148:151],a[116:119],v[120:123]\n"
"s_sub_u32 s56,s56,2\n"
RT"a[168:169],v243 offset:12288\n"
"s_cmp_eq_u32 s34,7\n"
MF"v[120:123],a[144:147],a[100:103],v[120:123]\n"
"s_cselect_b32 s56,2,s56\n"
"s_cmp_eq_u32 s57,2\n"
"s_cselect_b32 s56,5,s56\n"
MF"v[128:131],a[144:147],a[104:107],v[128:131]\n"
"s_cmp_lt_u32 s30,2\n"
RT"a[170:171],v243 offset:12800\n"
"s_cselect_b64 s[24:25],s[78:79],s[80:81]\n"
MF"v[128:131],a[148:151],a[120:123],v[128:131]\n"
"s_cmp_eq_u32 s57,2\n"
BS"v[68:71],v250,s[8:11],s49 offen offset:1024 sc1\n"
MF"v[136:139],a[148:151],a[124:127],v[136:139]\n"
"s_cselect_b64 s[24:25],s[82:83],s[24:25]\n"
RT"a[172:173],v243 offset:36864\n"
"s_and_b32 s50,s30,1\n"
MF"v[136:139],a[144:147],a[108:111],v[136:139]\n"
"s_mul_i32 s50,s50,10\n"
RT"a[174:175],v243 offset:37376\n"
"s_mul_i32 s51,s30,14\n"
MF"v[116:119],a[152:155],a[96:99],v[116:119]\n"
"s_min_u32 s51,s51,41\n"
BL"v225,s[8:11],s48 offen offset:1024 sc1 lds\n"
MF"v[116:119],a[156:159],a[112:115],v[116:119]\n"
"s_cmp_eq_u32 s57,2\n"
RT"a[176:177],v244 offset:12288\n"
"s_cselect_b32 s50,s51,s50\n"
MF"v[124:127],a[156:159],a[116:119],v[124:127]\n"
"s_cmp_lt_u32 s30,2\n"
"s_cselect_b32 s51,14,13\n"
"s_cmp_lt_u32 s58,108\n"
MF"v[124:127],a[152:155],a[100:103],v[124:127]\n"
"s_cselect_b32 s92,10,0\n"
RT"a[178:179],v244 offset:12800\n"
"s_cmp_eq_u32 s57,2\n"
MF"v[132:135],a[152:155],a[104:107],v[132:135]\n"
"s_cselect_b32 s92,s51,s92\n"
"s_mov_b32 s51,3456\n"
"s_cmp_eq_u32 s57,2\n"
MF"v[132:135],a[156:159],a[120:123],v[132:135]\n"
"s_cselect_b32 s51,1280,s51\n"
RT"a[180:181],v244 offset:36864\n"
"s_lshl_b32 s62,s51,5\n"
MF"v[140:143],a[156:159],a[124:127],v[140:143]\n"
"s_mul_i32 s26,s62,s92\n"
"s_mul_i32 s90,s62,s50\n"
"s_lshl_b32 s91,s58,5\n"
MF"v[140:143],a[152:155],a[108:111],v[140:143]\n"
WT"lgkmcnt(3)\n"
RT"a[182:183],v244 offset:37376\n"
SA"s90,s90,s91\n"
MF"v[144:147],a[160:163],a[96:99],v[144:147]\n"
SA"s24,s24,s90\n"
BL"v225,s[8:11],s48 offen offset:2048 sc1 lds\n"
MF"v[144:147],a[164:167],a[112:115],v[144:147]\n"
"s_addc_u32 s25,s25,0\n"
RT"a[184:185],v245 offset:12288\n"
"s_and_b32 s25,s25,0xffff\n"
MF"v[152:155],a[164:167],a[116:119],v[152:155]\n"
"s_sub_u32 s90,s51,32\n"
"v_lshrrev_b32 v235,5,v224\n"
"v_mul_u32_u24 v235,s51,v235\n"
MF"v[152:155],a[160:163],a[100:103],v[152:155]\n"
"s_sub_u32 s26,s26,s90\n"
RT"a[186:187],v245 offset:12800\n"
"v_and_b32 v239,16,v224\n"
MF"v[160:163],a[160:163],a[104:107],v[160:163]\n"
"s_cselect_b32 s26,0,s26\n"
"v_add_u32 v235,v235,v239\n"
"s_mov_b32 s27,0x20000\n"
MF"v[160:163],a[164:167],a[120:123],v[160:163]\n"
"s_mul_i32 s63,s35,20480\n"
RT"a[188:189],v245 offset:36864\n"
SA"s63,s63,3145728\n"
MF"v[168:171],a[164:167],a[124:127],v[168:171]\n"
"s_cmp_eq_u32 s57,2\n"
"s_cselect_b32 s63,16384,s63\n"
MF"v[168:171],a[160:163],a[108:111],v[168:171]\n"
"s_lshl_b32 s50,s50,10\n"
RT"a[190:191],v245 offset:37376\n"
SA"s63,s63,s50\n"
MF"v[148:151],a[168:171],a[96:99],v[148:151]\n"
"s_mov_b32 s50,1065353216\n"
BL"v225,s[8:11],s48 offen offset:3072 sc1 lds\n"
MF"v[148:151],a[172:175],a[112:115],v[148:151]\n"
"s_cmp_lg_u32 s92,0\n"
"s_cselect_b32 s50,s45,s50\n"
"s_mov_b32 s93,16384\n"
MF"v[156:159],a[172:175],a[116:119],v[156:159]\n"
"s_cmp_eq_u32 s57,2\n"
"s_cselect_b32 s93,71680,s93\n"
MF"v[156:159],a[168:171],a[100:103],v[156:159]\n"
"s_lshl_b32 s51,s58,9\n"
MF"v[164:167],a[168:171],a[104:107],v[164:167]\n"
SA"s93,s93,s51\n"
SA"s93,s93,s50\n"
MF"v[164:167],a[172:175],a[120:123],v[164:167]\n"
"s_mov_b32 s94,6912\n"
"s_barrier\n"
MF"v[172:175],a[172:175],a[124:127],v[172:175]\n"
"s_cmp_eq_u32 s57,2\n"
RT"a[128:129],v246 offset:16384\n"
"s_cselect_b32 s94,6400,s94\n"
MF"v[172:175],a[168:171],a[108:111],v[172:175]\n"
WT"lgkmcnt(1)\n"
"s_lshl_b32 s51,s58,3\n"
SA"s94,s94,s51\n"
MF"v[176:179],a[176:179],a[96:99],v[176:179]\n"
SA"s94,s94,s50\n"
RT"a[130:131],v246 offset:16896\n"
"s_mov_b32 s52,0\n"
MF"v[176:179],a[180:183],a[112:115],v[176:179]\n"
"s_lshl_b32 s50,s35,6\n"
RT"a[132:133],v246 offset:40960\n"
MF"v[184:187],a[180:183],a[116:119],v[184:187]\n"
SA"s50,s50,2304\n"
MF"v[184:187],a[176:179],a[100:103],v[184:187]\n"
"s_cmp_eq_u32 s57,2\n"
RT"a[134:135],v246 offset:41472\n"
"s_cselect_b32 s50,6912,s50\n"
MF"v[192:195],a[176:179],a[104:107],v[192:195]\n"
"s_mov_b32 s51,48\n"
RT"a[136:137],v242 offset:16384\n"
SA"s49,s88,278528\n"
MF"v[192:195],a[180:183],a[120:123],v[192:195]\n"
"s_cmp_eq_u32 s57,2\n"
"s_cselect_b32 s51,848,s51\n"
"v_min_u32 v238,s51,v224\n"
MF"v[200:203],a[180:183],a[124:127],v[200:203]\n"
RT"a[138:139],v242 offset:16896\n"
"v_add_u32 v238,s50,v238\n"
MF"v[200:203],a[176:179],a[108:111],v[200:203]\n"
"s_mov_b32 s16,0\n"
BS"v[72:75],v250,s[8:11],s49 offen offset:0 sc1\n"
MF"v[180:183],a[184:187],a[96:99],v[180:183]\n"
"s_mov_b32 s20,8\n"
RT"a[140:141],v242 offset:40960\n"
"v_and_b32 v239,0x80,v224\n"
MF"v[180:183],a[188:191],a[112:115],v[180:183]\n"
"s_mov_b32 s18,2130706432\n"
RT"a[142:143],v242 offset:41472\n"
"v_lshl_add_u32 v236,v239,23,v224\n"
MF"v[188:191],a[188:191],a[116:119],v[188:191]\n"
"s_lshl_b32 s21,s32,4\n"
"s_lshr_b32 s22,s30,1\n"
MF"v[188:191],a[184:187],a[100:103],v[188:191]\n"
SA"s21,s21,s22\n"
RT"a[144:145],v247 offset:16384\n"
MF"v[196:199],a[184:187],a[104:107],v[196:199]\n"
"s_mul_i32 s21,s21,40960\n"
"s_lshl_b32 s22,s33,1\n"
MF"v[196:199],a[188:191],a[120:123],v[196:199]\n"
"s_and_b32 s23,s30,1\n"
RT"a[146:147],v247 offset:16896\n"
MF"v[204:207],a[188:191],a[124:127],v[204:207]\n"
SA"s22,s22,s23\n"
RT"a[148:149],v247 offset:40960\n"
"s_mul_i32 s22,s22,160\n"
MF"v[204:207],a[184:187],a[108:111],v[204:207]\n"
WT"vmcnt(37) lgkmcnt(4)\n"
SA"s17,s21,s22\n"
MF"v[80:83],a[128:131],a[192:195],v[80:83]\n"
WT"vmcnt(32)\n"
RT"a[150:151],v247 offset:41472\n"
"v_lshrrev_b32 v240,4,v224\n"
MF"v[80:83],a[132:135],a[208:211],v[80:83]\n"
WT"vmcnt(31)\n"
RT"a[152:153],v243 offset:16384\n"
"v_add_u32 v240,0,v240\n"
MF"v[88:91],a[132:135],a[212:215],v[88:91]\n"
"v_mul_u32_u24 v240,205,v240\n"
"v_lshrrev_b32 v240,11,v240\n"
MF"v[88:91],a[128:131],a[196:199],v[88:91]\n"
RT"a[154:155],v243 offset:16896\n"
"v_mul_u32_u24 v240,1120,v240\n"
MF"v[96:99],a[128:131],a[200:203],v[96:99]\n"
WT"vmcnt(30)\n"
RT"a[156:157],v243 offset:40960\n"
"v_add_u32 v240,v240,v224\n"
MF"v[96:99],a[132:135],a[216:219],v[96:99]\n"
WT"vmcnt(28)\n"
"v_lshrrev_b32 v241,4,v224\n"
"v_add_u32 v241,64,v241\n"
MF"v[104:107],a[132:135],a[220:223],v[104:107]\n"
RT"a[158:159],v243 offset:41472\n"
"v_mul_u32_u24 v241,205,v241\n"
MF"v[104:107],a[128:131],a[204:207],v[104:107]\n"
WT"vmcnt(26)\n"
"v_lshrrev_b32 v241,11,v241\n"
"v_mul_u32_u24 v241,1120,v241\n"
MF"v[80:83],a[136:139],a[224:227],v[80:83]\n"
WT"vmcnt(17) lgkmcnt(3)\n"
RT"a[160:161],v248 offset:16384\n"
"v_add_u32 v241,v241,v224\n"
MF"v[80:83],a[140:143],a[240:243],v[80:83]\n"
WT"vmcnt(16)\n"
"v_lshrrev_b32 v251,4,v224\n"
"v_add_u32 v251,128,v251\n"
MF"v[88:91],a[140:143],a[244:247],v[88:91]\n"
RT"a[162:163],v248 offset:16896\n"
"v_mul_u32_u24 v251,205,v251\n"
MF"v[88:91],a[136:139],a[228:231],v[88:91]\n"
RT"a[164:165],v248 offset:40960\n"
"v_lshrrev_b32 v251,11,v251\n"
MF"v[96:99],a[136:139],a[232:235],v[96:99]\n"
WT"vmcnt(14)\n"
"v_mul_u32_u24 v251,1120,v251\n"
"v_add_u32 v251,v251,v224\n"
MF"v[96:99],a[140:143],a[248:251],v[96:99]\n"
WT"vmcnt(12)\n"
RT"a[166:167],v248 offset:41472\n"
"v_and_b32 v237,512,v224\n"
MF"v[104:107],a[140:143],a[252:255],v[104:107]\n"
RT"a[168:169],v244 offset:16384\n"
"v_lshl_add_u32 v251,v237,20,v251\n"
MF"v[104:107],a[136:139],a[236:239],v[104:107]\n"
"v_lshrrev_b32 v252,6,v224\n"
"v_mul_u32_u24 v252,160,v252\n"
MF"v[84:87],a[144:147],a[192:195],v[84:87]\n"
RT"a[170:171],v244 offset:16896\n"
"v_bfe_u32 v237,v224,4,2\n"
MF"v[84:87],a[148:151],a[208:211],v[84:87]\n"
"s_cmp_eq_u32 s34,7\n"
RT"a[172:173],v244 offset:40960\n"
"v_lshl_add_u32 v252,v237,3,v252\n"
MF"v[92:95],a[148:151],a[212:215],v[92:95]\n"
"s_cselect_b32 s57,3,s57\n"
"v_lshrrev_b32 v254,4,v224\n"
"v_min_u32 v254,49,v254\n"
MF"v[92:95],a[144:147],a[196:199],v[92:95]\n"
RT"a[174:175],v244 offset:41472\n"
"v_mul_u32_u24 v253,52,v254\n"
MF"v[100:103],a[144:147],a[200:203],v[100:103]\n"
"v_lshrrev_b32 v253,8,v253\n"
MF"v[100:103],a[148:151],a[216:219],v[100:103]\n"
RT"a[176:177],v249 offset:16384\n"
"v_mul_u32_u24 v253,2901,v253\n"
MF"v[108:111],a[148:151],a[220:223],v[108:111]\n"
"v_mul_u32_u24 v254,111,v254\n"
"v_add_u32 v254,v254,v253\n"
MF"v[108:111],a[144:147],a[204:207],v[108:111]\n"
WT"lgkmcnt(3)\n"
RT"a[178:179],v249 offset:16896\n"
"v_and_b32 v254,-4,v254\n"
MF"v[84:87],a[152:155],a[224:227],v[84:87]\n"
RT"a[180:181],v249 offset:40960\n"
"v_lshrrev_b32 v253,5,v224\n"
MF"v[84:87],a[156:159],a[240:243],v[84:87]\n"
BS"v[76:79],v250,s[8:11],s49 offen offset:1024 sc1\n"
MF"v[92:95],a[156:159],a[244:247],v[92:95]\n"
RT"a[182:183],v249 offset:41472\n"
"v_min_u32 v253,13,v253\n"
MF"v[92:95],a[152:155],a[228:231],v[92:95]\n"
RT"a[184:185],v245 offset:16384\n"
"v_mul_u32_u24 v253,1280,v253\n"
MF"v[100:103],a[152:155],a[232:235],v[100:103]\n"
"v_bfe_u32 v237,v224,4,1\n"
"v_mul_u32_u24 v237,156,v237\n"
MF"v[100:103],a[156:159],a[248:251],v[100:103]\n"
"s_cmp_lt_u32 s30,2\n"
RT"a[186:187],v245 offset:16896\n"
"v_add_u32 v253,v253,v237\n"
MF"v[108:111],a[156:159],a[252:255],v[108:111]\n"
"s_cselect_b64 s[12:13],s[78:79],s[80:81]\n"
RT"a[188:189],v245 offset:40960\n"
"s_and_b32 s13,s13,0xffff\n"
MF"v[108:111],a[152:155],a[236:239],v[108:111]\n"
"s_mov_b32 s14,2211840\n"
"s_mov_b32 s15,0x20000\n"
MF"v[112:115],a[160:163],a[192:195],v[112:115]\n"
"s_lshr_b32 s90,s31,3\n"
RT"a[190:191],v245 offset:41472\n"
MF"v[112:115],a[164:167],a[208:211],v[112:115]\n"
"s_lshl_b32 s91,s90,1\n"
MF"v[120:123],a[164:167],a[212:215],v[120:123]\n"
"s_and_b32 s50,s30,1\n"
RT"a[128:129],v246 offset:20480\n"
PK"v80,v80,v81\n"
MF"v[120:123],a[160:163],a[196:199],v[120:123]\n"
SA"s91,s91,s50\n"
PK"v81,v82,v83\n"
PK"v82,v84,v85\n"
MF"v[128:131],a[160:163],a[200:203],v[128:131]\n"
"s_mul_i32 s91,s91,34560\n"
RT"a[130:131],v246 offset:20992\n"
PK"v83,v86,v87\n"
MF"v[128:131],a[164:167],a[216:219],v[128:131]\n"
"s_mul_i32 s50,s32,448\n"
RT"a[132:133],v246 offset:45056\n"
"v_accvgpr_write_b32 a0,v80\n"
MF"v[136:139],a[164:167],a[220:223],v[136:139]\n"
SA"s91,s91,s50\n"
"v_accvgpr_write_b32 a1,v81\n"
"v_accvgpr_write_b32 a2,v82\n"
MF"v[136:139],a[160:163],a[204:207],v[136:139]\n"
"buffer_load_dword v255,v254,s[12:15],s91 offen\n"
MF"v[112:115],a[168:171],a[224:227],v[112:115]\n"
WT"lgkmcnt(3)\n"
RT"a[134:135],v246 offset:45568\n"
RT"a[136:137],v242 offset:20480\n"
MF"v[112:115],a[172:175],a[240:243],v[112:115]\n"
"s_mov_b32 s12,s82\n"
"v_accvgpr_write_b32 a3,v83\n"
PK"v88,v88,v89\n"
MF"v[120:123],a[172:175],a[244:247],v[120:123]\n"
"s_and_b32 s13,s83,0xffff\n"
RT"a[138:139],v242 offset:20992\n"
PK"v89,v90,v91\n"
MF"v[120:123],a[168:171],a[228:231],v[120:123]\n"
"s_lshl_b32 s90,s90,2\n"
RT"a[140:141],v242 offset:45056\n"
PK"v90,v92,v93\n"
MF"v[128:131],a[168:171],a[232:235],v[128:131]\n"
SA"s90,s90,s30\n"
PK"v91,v94,v95\n"
"v_accvgpr_write_b32 a16,v88\n"
MF"v[128:131],a[172:175],a[248:251],v[128:131]\n"
"s_mul_i32 s90,s90,17920\n"
RT"a[142:143],v242 offset:45568\n"
"v_accvgpr_write_b32 a17,v89\n"
MF"v[136:139],a[172:175],a[252:255],v[136:139]\n"
"s_mul_i32 s50,s32,160\n"
"v_accvgpr_write_b32 a18,v90\n"
"v_accvgpr_write_b32 a19,v91\n"
MF"v[136:139],a[168:171],a[236:239],v[136:139]\n"
SA"s90,s90,s50\n"
RT"a[144:145],v247 offset:20480\n"
PK"v96,v96,v97\n"
MF"v[116:119],a[176:179],a[192:195],v[116:119]\n"
"buffer_load_dword v255,v253,s[12:15],s90 offen\n"
MF"v[116:119],a[180:183],a[208:211],v[116:119]\n"
RT"a[146:147],v247 offset:20992\n"
"v_min_u32 v253,624,v224\n"
MF"v[124:127],a[180:183],a[212:215],v[124:127]\n"
RT"a[148:149],v247 offset:45056\n"
"v_lshlrev_b32 v253,3,v253\n"
MF"v[124:127],a[176:179],a[196:199],v[124:127]\n"
PK"v97,v98,v99\n"
PK"v98,v100,v101\n"
MF"v[132:135],a[176:179],a[200:203],v[132:135]\n"
RT"a[150:151],v247 offset:45568\n"
PK"v99,v102,v103\n"
MF"v[132:135],a[180:183],a[216:219],v[132:135]\n"
RT"a[152:153],v243 offset:20480\n"
"v_accvgpr_write_b32 a32,v96\n"
MF"v[140:143],a[180:183],a[220:223],v[140:143]\n"
"v_accvgpr_write_b32 a33,v97\n"
"v_accvgpr_write_b32 a34,v98\n"
MF"v[140:143],a[176:179],a[204:207],v[140:143]\n"
RT"a[154:155],v243 offset:20992\n"
"v_accvgpr_write_b32 a35,v99\n"
MF"v[116:119],a[184:187],a[224:227],v[116:119]\n"
RT"a[156:157],v243 offset:45056\n"
PK"v104,v104,v105\n"
MF"v[116:119],a[188:191],a[240:243],v[116:119]\n"
PK"v105,v106,v107\n"
PK"v106,v108,v109\n"
MF"v[124:127],a[188:191],a[244:247],v[124:127]\n"
RT"a[158:159],v243 offset:45568\n"
PK"v107,v110,v111\n"
MF"v[124:127],a[184:187],a[228:231],v[124:127]\n"
"v_accvgpr_write_b32 a48,v104\n"
"v_accvgpr_write_b32 a49,v105\n"
MF"v[132:135],a[184:187],a[232:235],v[132:135]\n"
RT"a[160:161],v248 offset:20480\n"
"v_accvgpr_write_b32 a50,v106\n"
MF"v[132:135],a[188:191],a[248:251],v[132:135]\n"
"v_accvgpr_write_b32 a51,v107\n"
MF"v[140:143],a[188:191],a[252:255],v[140:143]\n"
RT"a[162:163],v248 offset:20992\n"
MF"v[140:143],a[184:187],a[236:239],v[140:143]\n"
WT"lgkmcnt(3)\n"
RT"a[164:165],v248 offset:45056\n"
MF"v[144:147],a[128:131],a[192:195],v[144:147]\n"
MF"v[144:147],a[132:135],a[208:211],v[144:147]\n"
RT"a[166:167],v248 offset:45568\n"
MF"v[152:155],a[132:135],a[212:215],v[152:155]\n"
RT"a[168:169],v244 offset:20480\n"
PK"v112,v112,v113\n"
MF"v[152:155],a[128:131],a[196:199],v[152:155]\n"
PK"v113,v114,v115\n"
PK"v114,v116,v117\n"
MF"v[160:163],a[128:131],a[200:203],v[160:163]\n"
RT"a[170:171],v244 offset:20992\n"
PK"v115,v118,v119\n"
MF"v[160:163],a[132:135],a[216:219],v[160:163]\n"
RT"a[172:173],v244 offset:45056\n"
"v_accvgpr_write_b32 a4,v112\n"
MF"v[168:171],a[132:135],a[220:223],v[168:171]\n"
"v_accvgpr_write_b32 a5,v113\n"
"v_accvgpr_write_b32 a6,v114\n"
MF"v[168:171],a[128:131],a[204:207],v[168:171]\n"
RT"a[174:175],v244 offset:45568\n"
"v_accvgpr_write_b32 a7,v115\n"
MF"v[144:147],a[136:139],a[224:227],v[144:147]\n"
PK"v120,v120,v121\n"
PK"v121,v122,v123\n"
MF"v[144:147],a[140:143],a[240:243],v[144:147]\n"
RT"a[176:177],v249 offset:20480\n"
PK"v122,v124,v125\n"
MF"v[152:155],a[140:143],a[244:247],v[152:155]\n"
PK"v123,v126,v127\n"
"v_accvgpr_write_b32 a20,v120\n"
MF"v[152:155],a[136:139],a[228:231],v[152:155]\n"
RT"a[178:179],v249 offset:20992\n"
"v_accvgpr_write_b32 a21,v121\n"
MF"v[160:163],a[136:139],a[232:235],v[160:163]\n"
RT"a[180:181],v249 offset:45056\n"
"v_accvgpr_write_b32 a22,v122\n"
MF"v[160:163],a[140:143],a[248:251],v[160:163]\n"
"v_accvgpr_write_b32 a23,v123\n"
MF"v[168:171],a[140:143],a[252:255],v[168:171]\n"
RT"a[182:183],v249 offset:45568\n"
PK"v128,v128,v129\n"
MF"v[168:171],a[136:139],a[236:239],v[168:171]\n"
RT"a[184:185],v245 offset:20480\n"
PK"v129,v130,v131\n"
MF"v[148:151],a[144:147],a[192:195],v[148:151]\n"
PK"v130,v132,v133\n"
PK"v131,v134,v135\n"
MF"v[148:151],a[148:151],a[208:211],v[148:151]\n"
RT"a[186:187],v245 offset:20992\n"
"v_accvgpr_write_b32 a36,v128\n"
MF"v[156:159],a[148:151],a[212:215],v[156:159]\n"
RT"a[188:189],v245 offset:45056\n"
"v_accvgpr_write_b32 a37,v129\n"
MF"v[156:159],a[144:147],a[196:199],v[156:159]\n"
"v_accvgpr_write_b32 a38,v130\n"
"v_accvgpr_write_b32 a39,v131\n"
MF"v[164:167],a[144:147],a[200:203],v[164:167]\n"
RT"a[190:191],v245 offset:45568\n"
PK"v136,v136,v137\n"
MF"v[164:167],a[148:151],a[216:219],v[164:167]\n"
PK"v137,v138,v139\n"
PK"v138,v140,v141\n"
MF"v[172:175],a[148:151],a[220:223],v[172:175]\n"
PK"v139,v142,v143\n"
"v_accvgpr_write_b32 a52,v136\n"
MF"v[172:175],a[144:147],a[204:207],v[172:175]\n"
"v_accvgpr_write_b32 a53,v137\n"
"v_accvgpr_write_b32 a54,v138\n"
MF"v[148:151],a[152:155],a[224:227],v[148:151]\n"
WT"lgkmcnt(2)\n"
"v_accvgpr_write_b32 a55,v139\n"
MF"v[148:151],a[156:159],a[240:243],v[148:151]\n"
MF"v[156:159],a[156:159],a[244:247],v[156:159]\n"
MF"v[156:159],a[152:155],a[228:231],v[156:159]\n"
MF"v[164:167],a[152:155],a[232:235],v[164:167]\n"
MF"v[164:167],a[156:159],a[248:251],v[164:167]\n"
MF"v[172:175],a[156:159],a[252:255],v[172:175]\n"
MF"v[172:175],a[152:155],a[236:239],v[172:175]\n"
MF"v[176:179],a[160:163],a[192:195],v[176:179]\n"
MF"v[176:179],a[164:167],a[208:211],v[176:179]\n"
MF"v[176:179],a[168:171],a[224:227],v[176:179]\n"
PK"v144,v144,v145\n"
PK"v145,v146,v147\n"
MF"v[176:179],a[172:175],a[240:243],v[176:179]\n"
PK"v146,v148,v149\n"
PK"v147,v150,v151\n"
MF"v[180:183],a[176:179],a[192:195],v[180:183]\n"
"v_mov_b32 v254,0x3f803f80\n"
"v_accvgpr_write_b32 a8,v144\n"
MF"v[180:183],a[180:183],a[208:211],v[180:183]\n"
"v_accvgpr_write_b32 a9,v145\n"
"v_accvgpr_write_b32 a128,v254\n"
MF"v[180:183],a[184:187],a[224:227],v[180:183]\n"
WT"lgkmcnt(0)\n"
"v_accvgpr_write_b32 a129,v254\n"
"v_accvgpr_write_b32 a130,v254\n"
MF"v[180:183],a[188:191],a[240:243],v[180:183]\n"
"v_accvgpr_write_b32 a131,v254\n"
"v_accvgpr_write_b32 a10,v146\n"
MF"v[184:187],a[160:163],a[196:199],v[184:187]\n"
"v_accvgpr_write_b32 a11,v147\n"
PK"v152,v152,v153\n"
MF"v[184:187],a[164:167],a[212:215],v[184:187]\n"
PK"v153,v154,v155\n"
PK"v154,v156,v157\n"
MF"v[184:187],a[168:171],a[228:231],v[184:187]\n"
PK"v155,v158,v159\n"
"v_accvgpr_write_b32 a24,v152\n"
MF"v[184:187],a[172:175],a[244:247],v[184:187]\n"
"v_accvgpr_write_b32 a25,v153\n"
PK"v176,v176,v177\n"
MF"v[188:191],a[176:179],a[196:199],v[188:191]\n"
PK"v177,v178,v179\n"
"v_accvgpr_write_b32 a26,v154\n"
MF"v[188:191],a[180:183],a[212:215],v[188:191]\n"
"v_accvgpr_write_b32 a27,v155\n"
PK"v178,v180,v181\n"
MF"v[188:191],a[184:187],a[228:231],v[188:191]\n"
PK"v179,v182,v183\n"
"v_accvgpr_write_b32 a12,v176\n"
MF"v[188:191],a[188:191],a[244:247],v[188:191]\n"
"v_accvgpr_write_b32 a13,v177\n"
"v_accvgpr_write_b32 a14,v178\n"
MF"v[192:195],a[160:163],a[200:203],v[192:195]\n"
"v_accvgpr_write_b32 a15,v179\n"
PK"v160,v160,v161\n"
MF"v[192:195],a[164:167],a[216:219],v[192:195]\n"
WT"vmcnt(14)\n"
"s_barrier\n"
MF"v[192:195],a[168:171],a[232:235],v[192:195]\n"
RD"a[64:67],v226 offset:0\n"
PK"v161,v162,v163\n"
MF"v[192:195],a[172:175],a[248:251],v[192:195]\n"
PK"v162,v164,v165\n"
PK"v163,v166,v167\n"
MF"v[196:199],a[176:179],a[200:203],v[196:199]\n"
RD"a[68:71],v226 offset:1024\n"
"v_accvgpr_write_b32 a40,v160\n"
MF"v[196:199],a[180:183],a[216:219],v[196:199]\n"
"v_accvgpr_write_b32 a41,v161\n"
"v_accvgpr_write_b32 a42,v162\n"
MF"v[196:199],a[184:187],a[232:235],v[196:199]\n"
RD"a[72:75],v226 offset:2048\n"
"v_accvgpr_write_b32 a43,v163\n"
MF"v[196:199],a[188:191],a[248:251],v[196:199]\n"
PK"v184,v184,v185\n"
PK"v185,v186,v187\n"
MF"v[200:203],a[160:163],a[204:207],v[200:203]\n"
RD"a[76:79],v226 offset:3072\n"
PK"v186,v188,v189\n"
MF"v[200:203],a[164:167],a[220:223],v[200:203]\n"
PK"v187,v190,v191\n"
PK"v168,v168,v169\n"
MF"v[200:203],a[168:171],a[236:239],v[200:203]\n"
RD"a[80:83],v226 offset:4096\n"
PK"v169,v170,v171\n"
MF"v[200:203],a[172:175],a[252:255],v[200:203]\n"
"v_accvgpr_write_b32 a28,v184\n"
"v_accvgpr_write_b32 a29,v185\n"
MF"v[204:207],a[176:179],a[204:207],v[204:207]\n"
RD"a[84:87],v226 offset:5120\n"
PK"v170,v172,v173\n"
MF"v[204:207],a[180:183],a[220:223],v[204:207]\n"
PK"v171,v174,v175\n"
"v_accvgpr_write_b32 a30,v186\n"
MF"v[204:207],a[184:187],a[236:239],v[204:207]\n"
"v_accvgpr_write_b32 a31,v187\n"
"v_accvgpr_write_b32 a56,v168\n"
MF"v[204:207],a[188:191],a[252:255],v[204:207]\n"
"v_accvgpr_write_b32 a57,v169\n"
"v_accvgpr_write_b32 a58,v170\n"
"v_accvgpr_write_b32 a59,v171\n"
RD"a[88:91],v226 offset:6144\n"
"s_cmp_eq_u32 s34,0\n"
"s_cbranch_scc1 LFA_%=\n"
WT"lgkmcnt(6)\n"
MF"v[0:3],a[64:67],a[0:3],0\n"
WT"lgkmcnt(3)\n"
PK"v192,v192,v193\n"
PK"v193,v194,v195\n"
MF"v[0:3],a[68:71],a[4:7],v[0:3]\n"
"s_mov_b32 m0,s40\n"
"v_readlane_b32 s43,v228,s42\n"
PK"v194,v196,v197\n"
MF"v[8:11],a[68:71],a[20:23],0\n"
PK"v195,v198,v199\n"
"v_readlane_b32 s44,v229,s42\n"
MF"v[8:11],a[64:67],a[16:19],v[8:11]\n"
SA"s42,s42,1\n"
BL"v225,s[8:11],s43 offen offset:0 sc1 lds\n"
MF"v[16:19],a[64:67],a[32:35],0\n"
"v_accvgpr_write_b32 a44,v192\n"
"v_accvgpr_write_b32 a45,v193\n"
MF"v[16:19],a[68:71],a[36:39],v[16:19]\n"
RD"a[92:95],v226 offset:7168\n"
"v_accvgpr_write_b32 a46,v194\n"
MF"v[24:27],a[68:71],a[52:55],0\n"
"v_accvgpr_write_b32 a47,v195\n"
PK"v200,v200,v201\n"
MF"v[24:27],a[64:67],a[48:51],v[24:27]\n"
PK"v201,v202,v203\n"
PK"v202,v204,v205\n"
"v_mov_b32 v227,v226\n"
MF"v[0:3],a[72:75],a[8:11],v[0:3]\n"
BL"v225,s[8:11],s43 offen offset:1024 sc1 lds\n"
PK"v203,v206,v207\n"
MF"v[0:3],a[76:79],a[12:15],v[0:3]\n"
"v_accvgpr_write_b32 a60,v200\n"
"v_accvgpr_write_b32 a61,v201\n"
MF"v[8:11],a[76:79],a[28:31],v[8:11]\n"
"s_mul_i32 s48,s32,655360\n"
"v_accvgpr_write_b32 a62,v202\n"
"v_accvgpr_write_b32 a63,v203\n"
MF"v[8:11],a[72:75],a[24:27],v[8:11]\n"
SA"s12,s74,s48\n"
"s_addc_u32 s13,s75,0\n"
MF"v[16:19],a[72:75],a[40:43],v[16:19]\n"
"s_and_b32 s13,s13,0xffff\n"
MF"v[16:19],a[76:79],a[44:47],v[16:19]\n"
"s_mov_b32 s14,655360\n"
RD"a[96:99],v226 offset:8192\n"
MF"v[24:27],a[76:79],a[60:63],v[24:27]\n"
"s_lshl_b32 s48,s33,3\n"
BL"v225,s[8:11],s43 offen offset:2048 sc1 lds\n"
MF"v[24:27],a[72:75],a[56:59],v[24:27]\n"
WT"lgkmcnt(1)\n"
EX"v0,v0\n"
MF"v[4:7],a[80:83],a[0:3],0\n"
"s_and_b32 s49,s34,2\n"
EX"v1,v1\n"
MF"v[4:7],a[84:87],a[4:7],v[4:7]\n"
"s_lshl_b32 s49,s49,1\n"
EX"v2,v2\n"
MF"v[12:15],a[84:87],a[20:23],0\n"
SA"s48,s48,s49\n"
RD"a[100:103],v226 offset:9216\n"
PK"v0,v0,v1\n"
MF"v[12:15],a[80:83],a[16:19],v[12:15]\n"
SA"s48,s48,s30\n"
EX"v3,v3\n"
MF"v[20:23],a[80:83],a[32:35],0\n"
"s_mul_i32 s48,s48,5120\n"
EX"v8,v8\n"
MF"v[20:23],a[84:87],a[36:39],v[20:23]\n"
"s_or_b32 s49,s34,2\n"
BL"v225,s[8:11],s43 offen offset:3072 sc1 lds\n"
MF"v[28:31],a[84:87],a[52:55],0\n"
"s_cmp_eq_u32 s49,7\n"
EX"v9,v9\n"
PK"v1,v2,v3\n"
MF"v[28:31],a[80:83],a[48:51],v[28:31]\n"
"s_cselect_b32 s55,s48,2130706432\n"
RD"a[104:107],v226 offset:10240\n"
PK"v8,v8,v9\n"
MF"v[4:7],a[88:91],a[8:11],v[4:7]\n"
SA"m0,s40,16384\n"
EX"v10,v10\n"
MF"v[4:7],a[92:95],a[12:15],v[4:7]\n"
EX"v11,v11\n"
MF"v[12:15],a[92:95],a[28:31],v[12:15]\n"
EX"v16,v16\n"
MF"v[12:15],a[88:91],a[24:27],v[12:15]\n"
SA"s40,s40,32768\n"
RD"a[108:111],v226 offset:11264\n"
PK"v9,v10,v11\n"
MF"v[20:23],a[88:91],a[40:43],v[20:23]\n"
"s_cmp_eq_u32 s40,s46\n"
BL"v225,s[8:11],s44 offen offset:0 sc1 lds\n"
EX"v17,v17\n"
MF"v[20:23],a[92:95],a[44:47],v[20:23]\n"
"s_cselect_b32 s40,s47,s40\n"
EX"v18,v18\n"
MF"v[28:31],a[92:95],a[60:63],v[28:31]\n"
EX"v19,v19\n"
PK"v16,v16,v17\n"
MF"v[28:31],a[88:91],a[56:59],v[28:31]\n"
WT"lgkmcnt(1)\n"
RD"a[112:115],v226 offset:12288\n"
PK"v17,v18,v19\n"
MF"v[32:35],a[96:99],a[0:3],0\n"
EX"v24,v24\n"
MF"v[32:35],a[100:103],a[4:7],v[32:35]\n"
EX"v25,v25\n"
MF"v[40:43],a[100:103],a[20:23],0\n"
BL"v225,s[8:11],s44 offen offset:1024 sc1 lds\n"
EX"v26,v26\n"
MF"v[40:43],a[96:99],a[16:19],v[40:43]\n"
RD"a[116:119],v226 offset:13312\n"
PK"v24,v24,v25\n"
MF"v[48:51],a[96:99],a[32:35],0\n"
EX"v27,v27\n"
MF"v[48:51],a[100:103],a[36:39],v[48:51]\n"
EX"v4,v4\n"
MF"v[56:59],a[100:103],a[52:55],0\n"
EX"v5,v5\n"
PK"v25,v26,v27\n"
MF"v[56:59],a[96:99],a[48:51],v[56:59]\n"
RD"a[120:123],v226 offset:14336\n"
PK"v2,v4,v5\n"
MF"v[32:35],a[104:107],a[8:11],v[32:35]\n"
WT"lgkmcnt(2)\n"
BL"v225,s[8:11],s44 offen offset:2048 sc1 lds\n"
EX"v6,v6\n"
MF"v[32:35],a[108:111],a[12:15],v[32:35]\n"
EX"v7,v7\n"
MF"v[40:43],a[108:111],a[28:31],v[40:43]\n"
EX"v12,v12\n"
MF"v[40:43],a[104:107],a[24:27],v[40:43]\n"
RD"a[124:127],v226 offset:15360\n"
"v_readlane_b32 s43,v228,s42\n"
PK"v3,v6,v7\n"
MF"v[48:51],a[104:107],a[40:43],v[48:51]\n"
EX"v13,v13\n"
MF"v[48:51],a[108:111],a[44:47],v[48:51]\n"
EX"v14,v14\n"
MF"v[56:59],a[108:111],a[60:63],v[56:59]\n"
BL"v225,s[8:11],s44 offen offset:3072 sc1 lds\n"
EX"v15,v15\n"
MF"v[56:59],a[104:107],a[56:59],v[56:59]\n"
"s_mov_b32 m0,s40\n"
RD"a[96:99],v227 offset:24576\n"
PK"v10,v12,v13\n"
MF"v[36:39],a[112:115],a[0:3],0\n"
WT"lgkmcnt(2)\n"
EX"v20,v20\n"
PK"v11,v14,v15\n"
MF"v[36:39],a[116:119],a[4:7],v[36:39]\n"
EX"v21,v21\n"
MF"v[44:47],a[116:119],a[20:23],0\n"
EX"v22,v22\n"
MF"v[44:47],a[112:115],a[16:19],v[44:47]\n"
RD"a[100:103],v227 offset:25600\n"
"v_readlane_b32 s44,v229,s42\n"
PK"v18,v20,v21\n"
MF"v[52:55],a[112:115],a[32:35],0\n"
SA"s42,s42,1\n"
BL"v225,s[8:11],s43 offen offset:0 sc1 lds\n"
MF"v[52:55],a[116:119],a[36:39],v[52:55]\n"
EX"v23,v23\n"
MF"v[60:63],a[116:119],a[52:55],0\n"
EX"v28,v28\n"
MF"v[60:63],a[112:115],a[48:51],v[60:63]\n"
RD"a[104:107],v227 offset:26624\n"
PK"v19,v22,v23\n"
MF"v[36:39],a[120:123],a[8:11],v[36:39]\n"
WT"lgkmcnt(2)\n"
EX"v29,v29\n"
MF"v[36:39],a[124:127],a[12:15],v[36:39]\n"
BL"v225,s[8:11],s43 offen offset:1024 sc1 lds\n"
MF"v[44:47],a[124:127],a[28:31],v[44:47]\n"
EX"v30,v30\n"
PK"v26,v28,v29\n"
MF"v[44:47],a[120:123],a[24:27],v[44:47]\n"
RD"a[108:111],v227 offset:27648\n"
MF"v[52:55],a[120:123],a[40:43],v[52:55]\n"
EX"v31,v31\n"
MF"v[52:55],a[124:127],a[44:47],v[52:55]\n"
EX"v32,v32\n"
MF"v[60:63],a[124:127],a[60:63],v[60:63]\n"
EX"v33,v33\n"
PK"v27,v30,v31\n"
MF"v[60:63],a[120:123],a[56:59],v[60:63]\n"
SA"s41,s41,32768\n"
BL"v225,s[8:11],s43 offen offset:2048 sc1 lds\n"
RD"a[112:115],v227 offset:28672\n"
MF"v[80:83],a[96:99],v[0:3],0\n"
"s_cmp_eq_u32 s41,163840\n"
EX"v34,v34\n"
MF"v[112:115],a[96:99],v[8:11],0\n"
"s_cselect_b32 s41,0,s41\n"
EX"v35,v35\n"
MF"v[144:147],a[96:99],v[16:19],0\n"
EX"v40,v40\n"
"v_add_u32 v226,s41,v224\n"
MF"v[176:179],a[96:99],v[24:27],0\n"
WT"lgkmcnt(1)\n"
RD"a[116:119],v227 offset:29696\n"
MF"v[84:87],a[100:103],v[0:3],0\n"
EX"v41,v41\n"
MF"v[116:119],a[100:103],v[8:11],0\n"
BL"v225,s[8:11],s43 offen offset:3072 sc1 lds\n"
MF"v[148:151],a[100:103],v[16:19],0\n"
SA"m0,s40,16384\n"
EX"v42,v42\n"
MF"v[180:183],a[100:103],v[24:27],0\n"
RD"a[120:123],v227 offset:30720\n"
MF"v[88:91],a[104:107],v[0:3],0\n"
EX"v43,v43\n"
MF"v[120:123],a[104:107],v[8:11],0\n"
EX"v48,v48\n"
MF"v[152:155],a[104:107],v[16:19],0\n"
SA"s40,s40,32768\n"
EX"v49,v49\n"
MF"v[184:187],a[104:107],v[24:27],0\n"
"s_cmp_eq_u32 s40,s46\n"
BL"v225,s[8:11],s44 offen offset:0 sc1 lds\n"
RD"a[124:127],v227 offset:31744\n"
MF"v[92:95],a[108:111],v[0:3],0\n"
"s_cselect_b32 s40,s47,s40\n"
EX"v50,v50\n"
MF"v[124:127],a[108:111],v[8:11],0\n"
EX"v51,v51\n"
MF"v[156:159],a[108:111],v[16:19],0\n"
EX"v56,v56\n"
MF"v[188:191],a[108:111],v[24:27],0\n"
WT"lgkmcnt(1)\n"
EX"v57,v57\n"
MF"v[96:99],a[112:115],v[0:3],0\n"
EX"v58,v58\n"
MF"v[128:131],a[112:115],v[8:11],0\n"
BL"v225,s[8:11],s44 offen offset:1024 sc1 lds\n"
MF"v[160:163],a[112:115],v[16:19],0\n"
EX"v59,v59\n"
MF"v[192:195],a[112:115],v[24:27],0\n"
EX"v36,v36\n"
MF"v[100:103],a[116:119],v[0:3],0\n"
WT"vmcnt(16) lgkmcnt(0)\n"
"s_barrier\n"
MF"v[132:135],a[116:119],v[8:11],0\n"
RD"a[64:67],v226 offset:0\n"
RD"a[68:71],v226 offset:1024\n"
"buffer_store_dwordx2 v[232:233],v230,s[8:11],s98 offen sc1\n"
MF"v[164:167],a[116:119],v[16:19],0\n"
EX"v37,v37\n"
MF"v[196:199],a[116:119],v[24:27],0\n"
BL"v225,s[8:11],s44 offen offset:2048 sc1 lds\n"
MF"v[104:107],a[120:123],v[0:3],0\n"
EX"v38,v38\n"
MF"v[136:139],a[120:123],v[8:11],0\n"
EX"v39,v39\n"
MF"v[168:171],a[120:123],v[16:19],0\n"
EX"v44,v44\n"
MF"v[200:203],a[120:123],v[24:27],0\n"
RD"a[72:75],v226 offset:2048\n"
RD"a[76:79],v226 offset:3072\n"
MF"v[108:111],a[124:127],v[0:3],0\n"
EX"v45,v45\n"
MF"v[140:143],a[124:127],v[8:11],0\n"
BL"v225,s[8:11],s44 offen offset:3072 sc1 lds\n"
MF"v[172:175],a[124:127],v[16:19],0\n"
EX"v46,v46\n"
MF"v[204:207],a[124:127],v[24:27],0\n"
RD"a[80:83],v226 offset:4096\n"
MF"a[132:135],a[128:131],v[0:3],0\n"
EX"v47,v47\n"
MF"a[136:139],a[128:131],v[8:11],0\n"
"buffer_load_dword v255,v253,s[12:15],s55 offen\n"
EX"v52,v52\n"
MF"a[140:143],a[128:131],v[16:19],0\n"
SA"s55,s55,163840\n"
EX"v53,v53\n"
MF"a[144:147],a[128:131],v[24:27],0\n"
RD"a[84:87],v226 offset:5120\n"
RD"a[88:91],v226 offset:6144\n"
EX"v54,v54\n"
EX"v55,v55\n"
EX"v60,v60\n"
EX"v61,v61\n"
EX"v62,v62\n"
EX"v63,v63\n"
"s_cmp_eq_u32 s57,1\n"
"s_cbranch_scc1 LEV_%=\n"
".p2align 6\n"
"LP1_%=:\n"
WT"lgkmcnt(6)\n"
MF"v[0:3],a[64:67],a[0:3],0\n"
WT"lgkmcnt(5)\n"
"v_readlane_b32 s43,v228,s42\n"
PK"v64,v32,v33\n"
MF"v[0:3],a[68:71],a[4:7],v[0:3]\n"
"s_mov_b32 m0,s40\n"
"v_readlane_b32 s44,v229,s42\n"
PK"v65,v34,v35\n"
MF"v[8:11],a[68:71],a[20:23],0\n"
PK"v66,v36,v37\n"
PK"v67,v38,v39\n"
MF"v[8:11],a[64:67],a[16:19],v[8:11]\n"
SA"s42,s42,1\n"
BL"v225,s[8:11],s43 offen offset:0 sc1 lds\n"
MF"v[16:19],a[64:67],a[32:35],0\n"
PK"v68,v40,v41\n"
PK"v69,v42,v43\n"
MF"v[16:19],a[68:71],a[36:39],v[16:19]\n"
RD"a[92:95],v226 offset:7168\n"
PK"v70,v44,v45\n"
MF"v[24:27],a[68:71],a[52:55],0\n"
PK"v71,v46,v47\n"
PK"v72,v48,v49\n"
MF"v[24:27],a[64:67],a[48:51],v[24:27]\n"
WT"lgkmcnt(1)\n"
BL"v225,s[8:11],s43 offen offset:1024 sc1 lds\n"
MF"v[0:3],a[72:75],a[8:11],v[0:3]\n"
PK"v73,v50,v51\n"
"v_mov_b32 v227,v226\n"
MF"v[0:3],a[76:79],a[12:15],v[0:3]\n"
PK"v74,v52,v53\n"
PK"v75,v54,v55\n"
MF"v[8:11],a[76:79],a[28:31],v[8:11]\n"
PK"v76,v56,v57\n"
PK"v77,v58,v59\n"
MF"v[8:11],a[72:75],a[24:27],v[8:11]\n"
PK"v78,v60,v61\n"
PK"v79,v62,v63\n"
MF"v[16:19],a[72:75],a[40:43],v[16:19]\n"
MF"v[16:19],a[76:79],a[44:47],v[16:19]\n"
RD"a[96:99],v226 offset:8192\n"
MF"v[24:27],a[76:79],a[60:63],v[24:27]\n"
EX"v0,v0\n"
MF"v[24:27],a[72:75],a[56:59],v[24:27]\n"
EX"v1,v1\n"
MF"v[4:7],a[80:83],a[0:3],0\n"
EX"v2,v2\n"
MF"v[4:7],a[84:87],a[4:7],v[4:7]\n"
RD"a[100:103],v226 offset:9216\n"
PK"v0,v0,v1\n"
MF"v[12:15],a[84:87],a[20:23],0\n"
EX"v3,v3\n"
MF"v[12:15],a[80:83],a[16:19],v[12:15]\n"
EX"v8,v8\n"
MF"v[20:23],a[80:83],a[32:35],0\n"
EX"v9,v9\n"
MF"v[20:23],a[84:87],a[36:39],v[20:23]\n"
RD"a[104:107],v226 offset:10240\n"
PK"v1,v2,v3\n"
MF"v[28:31],a[84:87],a[52:55],0\n"
EX"v10,v10\n"
MF"v[28:31],a[80:83],a[48:51],v[28:31]\n"
EX"v11,v11\n"
MF"v[4:7],a[88:91],a[8:11],v[4:7]\n"
WT"lgkmcnt(1)\n"
EX"v16,v16\n"
MF"v[4:7],a[92:95],a[12:15],v[4:7]\n"
RD"a[108:111],v226 offset:11264\n"
PK"v8,v8,v9\n"
MF"v[12:15],a[92:95],a[28:31],v[12:15]\n"
EX"v17,v17\n"
MF"v[12:15],a[88:91],a[24:27],v[12:15]\n"
EX"v18,v18\n"
MF"v[20:23],a[88:91],a[40:43],v[20:23]\n"
EX"v19,v19\n"
MF"v[20:23],a[92:95],a[44:47],v[20:23]\n"
RD"a[112:115],v226 offset:12288\n"
PK"v9,v10,v11\n"
MF"v[28:31],a[92:95],a[60:63],v[28:31]\n"
EX"v24,v24\n"
MF"v[28:31],a[88:91],a[56:59],v[28:31]\n"
EX"v25,v25\n"
MF"v[32:35],a[96:99],a[0:3],0\n"
EX"v26,v26\n"
MF"v[32:35],a[100:103],a[4:7],v[32:35]\n"
RD"a[116:119],v226 offset:13312\n"
PK"v16,v16,v17\n"
MF"v[40:43],a[100:103],a[20:23],0\n"
EX"v27,v27\n"
MF"v[40:43],a[96:99],a[16:19],v[40:43]\n"
EX"v4,v4\n"
MF"v[48:51],a[96:99],a[32:35],0\n"
EX"v5,v5\n"
MF"v[48:51],a[100:103],a[36:39],v[48:51]\n"
RD"a[120:123],v226 offset:14336\n"
PK"v17,v18,v19\n"
MF"v[56:59],a[100:103],a[52:55],0\n"
EX"v6,v6\n"
MF"v[56:59],a[96:99],a[48:51],v[56:59]\n"
WT"lgkmcnt(2)\n"
EX"v7,v7\n"
MF"v[32:35],a[104:107],a[8:11],v[32:35]\n"
EX"v12,v12\n"
MF"v[32:35],a[108:111],a[12:15],v[32:35]\n"
RD"a[124:127],v226 offset:15360\n"
PK"v24,v24,v25\n"
MF"v[40:43],a[108:111],a[28:31],v[40:43]\n"
EX"v13,v13\n"
MF"v[40:43],a[104:107],a[24:27],v[40:43]\n"
EX"v14,v14\n"
MF"v[48:51],a[104:107],a[40:43],v[48:51]\n"
EX"v15,v15\n"
MF"v[48:51],a[108:111],a[44:47],v[48:51]\n"
RD"a[64:67],v227 offset:16384\n"
PK"v25,v26,v27\n"
MF"v[56:59],a[108:111],a[60:63],v[56:59]\n"
EX"v20,v20\n"
MF"v[56:59],a[104:107],a[56:59],v[56:59]\n"
EX"v21,v21\n"
MF"v[36:39],a[112:115],a[0:3],0\n"
WT"lgkmcnt(1)\n"
EX"v22,v22\n"
MF"v[36:39],a[116:119],a[4:7],v[36:39]\n"
RD"a[68:71],v227 offset:24576\n"
PK"v2,v4,v5\n"
MF"v[44:47],a[116:119],a[20:23],0\n"
EX"v23,v23\n"
MF"v[44:47],a[112:115],a[16:19],v[44:47]\n"
EX"v28,v28\n"
MF"v[52:55],a[112:115],a[32:35],0\n"
EX"v29,v29\n"
MF"v[52:55],a[116:119],a[36:39],v[52:55]\n"
RD"a[72:75],v227 offset:17408\n"
PK"v3,v6,v7\n"
MF"v[60:63],a[116:119],a[52:55],0\n"
EX"v30,v30\n"
MF"v[60:63],a[112:115],a[48:51],v[60:63]\n"
EX"v31,v31\n"
MF"v[36:39],a[120:123],a[8:11],v[36:39]\n"
PK"v10,v12,v13\n"
PK"v11,v14,v15\n"
MF"v[36:39],a[124:127],a[12:15],v[36:39]\n"
RD"a[76:79],v227 offset:25600\n"
PK"v18,v20,v21\n"
MF"v[44:47],a[124:127],a[28:31],v[44:47]\n"
PK"v19,v22,v23\n"
PK"v26,v28,v29\n"
MF"v[44:47],a[120:123],a[24:27],v[44:47]\n"
PK"v27,v30,v31\n"
MF"v[52:55],a[120:123],a[40:43],v[52:55]\n"
MF"v[52:55],a[124:127],a[44:47],v[52:55]\n"
RD"a[80:83],v227 offset:18432\n"
MF"v[60:63],a[124:127],a[60:63],v[60:63]\n"
EX"v32,v32\n"
MF"v[60:63],a[120:123],a[56:59],v[60:63]\n"
WT"lgkmcnt(2)\n"
SA"s41,s41,32768\n"
"s_cmp_eq_u32 s41,163840\n"
MF"v[80:83],a[64:67],v[64:67],v[80:83]\n"
"s_cselect_b32 s41,0,s41\n"
EX"v33,v33\n"
MF"v[80:83],a[68:71],v[0:3],v[80:83]\n"
RD"a[84:87],v227 offset:26624\n"
"v_add_u32 v226,s41,v224\n"
MF"v[112:115],a[68:71],v[8:11],v[112:115]\n"
EX"v34,v34\n"
MF"v[112:115],a[64:67],v[68:71],v[112:115]\n"
MF"v[144:147],a[64:67],v[72:75],v[144:147]\n"
EX"v35,v35\n"
MF"v[144:147],a[68:71],v[16:19],v[144:147]\n"
RD"a[88:91],v227 offset:19456\n"
MF"v[176:179],a[68:71],v[24:27],v[176:179]\n"
EX"v40,v40\n"
MF"v[176:179],a[64:67],v[76:79],v[176:179]\n"
BL"v225,s[8:11],s43 offen offset:2048 sc1 lds\n"
MF"v[84:87],a[72:75],v[64:67],v[84:87]\n"
WT"lgkmcnt(1)\n"
EX"v41,v41\n"
MF"v[84:87],a[76:79],v[0:3],v[84:87]\n"
RD"a[92:95],v227 offset:27648\n"
MF"v[116:119],a[76:79],v[8:11],v[116:119]\n"
EX"v42,v42\n"
MF"v[116:119],a[72:75],v[68:71],v[116:119]\n"
MF"v[148:151],a[72:75],v[72:75],v[148:151]\n"
EX"v43,v43\n"
MF"v[148:151],a[76:79],v[16:19],v[148:151]\n"
RD"a[96:99],v227 offset:20480\n"
MF"v[180:183],a[76:79],v[24:27],v[180:183]\n"
EX"v48,v48\n"
MF"v[180:183],a[72:75],v[76:79],v[180:183]\n"
BL"v225,s[8:11],s43 offen offset:3072 sc1 lds\n"
MF"v[88:91],a[80:83],v[64:67],v[88:91]\n"
EX"v49,v49\n"
MF"v[88:91],a[84:87],v[0:3],v[88:91]\n"
RD"a[100:103],v227 offset:28672\n"
MF"v[120:123],a[84:87],v[8:11],v[120:123]\n"
EX"v50,v50\n"
MF"v[120:123],a[80:83],v[68:71],v[120:123]\n"
SA"m0,s40,16384\n"
MF"v[152:155],a[80:83],v[72:75],v[152:155]\n"
EX"v51,v51\n"
MF"v[152:155],a[84:87],v[16:19],v[152:155]\n"
RD"a[104:107],v227 offset:21504\n"
MF"v[184:187],a[84:87],v[24:27],v[184:187]\n"
EX"v56,v56\n"
MF"v[184:187],a[80:83],v[76:79],v[184:187]\n"
WT"lgkmcnt(2)\n"
BL"v225,s[8:11],s44 offen offset:0 sc1 lds\n"
MF"v[92:95],a[88:91],v[64:67],v[92:95]\n"
SA"s40,s40,32768\n"
EX"v57,v57\n"
MF"v[92:95],a[92:95],v[0:3],v[92:95]\n"
"s_cmp_eq_u32 s40,s46\n"
RD"a[108:111],v227 offset:29696\n"
"s_cselect_b32 s40,s47,s40\n"
MF"v[124:127],a[92:95],v[8:11],v[124:127]\n"
EX"v58,v58\n"
MF"v[124:127],a[88:91],v[68:71],v[124:127]\n"
MF"v[156:159],a[88:91],v[72:75],v[156:159]\n"
EX"v59,v59\n"
MF"v[156:159],a[92:95],v[16:19],v[156:159]\n"
RD"a[112:115],v227 offset:22528\n"
MF"v[188:191],a[92:95],v[24:27],v[188:191]\n"
EX"v36,v36\n"
MF"v[188:191],a[88:91],v[76:79],v[188:191]\n"
BL"v225,s[8:11],s44 offen offset:1024 sc1 lds\n"
MF"v[96:99],a[96:99],v[64:67],v[96:99]\n"
WT"lgkmcnt(1)\n"
EX"v37,v37\n"
MF"v[96:99],a[100:103],v[0:3],v[96:99]\n"
RD"a[116:119],v227 offset:30720\n"
MF"v[128:131],a[100:103],v[8:11],v[128:131]\n"
EX"v38,v38\n"
MF"v[128:131],a[96:99],v[68:71],v[128:131]\n"
MF"v[160:163],a[96:99],v[72:75],v[160:163]\n"
EX"v39,v39\n"
MF"v[160:163],a[100:103],v[16:19],v[160:163]\n"
RD"a[120:123],v227 offset:23552\n"
MF"v[192:195],a[100:103],v[24:27],v[192:195]\n"
EX"v44,v44\n"
MF"v[192:195],a[96:99],v[76:79],v[192:195]\n"
BL"v225,s[8:11],s44 offen offset:2048 sc1 lds\n"
MF"v[100:103],a[104:107],v[64:67],v[100:103]\n"
EX"v45,v45\n"
MF"v[100:103],a[108:111],v[0:3],v[100:103]\n"
RD"a[124:127],v227 offset:31744\n"
MF"v[132:135],a[108:111],v[8:11],v[132:135]\n"
EX"v46,v46\n"
MF"v[132:135],a[104:107],v[68:71],v[132:135]\n"
MF"v[164:167],a[104:107],v[72:75],v[164:167]\n"
EX"v47,v47\n"
MF"v[164:167],a[108:111],v[16:19],v[164:167]\n"
MF"v[196:199],a[108:111],v[24:27],v[196:199]\n"
EX"v52,v52\n"
MF"v[196:199],a[104:107],v[76:79],v[196:199]\n"
WT"lgkmcnt(1)\n"
BL"v225,s[8:11],s44 offen offset:3072 sc1 lds\n"
MF"v[104:107],a[112:115],v[64:67],v[104:107]\n"
EX"v53,v53\n"
MF"v[104:107],a[116:119],v[0:3],v[104:107]\n"
"buffer_load_dword v255,v253,s[12:15],s55 offen\n"
MF"v[136:139],a[116:119],v[8:11],v[136:139]\n"
EX"v54,v54\n"
MF"v[136:139],a[112:115],v[68:71],v[136:139]\n"
SA"s55,s55,163840\n"
MF"v[168:171],a[112:115],v[72:75],v[168:171]\n"
WT"vmcnt(17) lgkmcnt(0)\n"
"s_barrier\n"
MF"v[168:171],a[116:119],v[16:19],v[168:171]\n"
RD"a[64:67],v226 offset:0\n"
MF"v[200:203],a[116:119],v[24:27],v[200:203]\n"
EX"v55,v55\n"
MF"v[200:203],a[112:115],v[76:79],v[200:203]\n"
MF"v[108:111],a[120:123],v[64:67],v[108:111]\n"
EX"v60,v60\n"
MF"v[108:111],a[124:127],v[0:3],v[108:111]\n"
RD"a[68:71],v226 offset:1024\n"
MF"v[140:143],a[124:127],v[8:11],v[140:143]\n"
EX"v61,v61\n"
MF"v[140:143],a[120:123],v[68:71],v[140:143]\n"
RD"a[72:75],v226 offset:2048\n"
MF"v[172:175],a[120:123],v[72:75],v[172:175]\n"
EX"v62,v62\n"
MF"v[172:175],a[124:127],v[16:19],v[172:175]\n"
RD"a[76:79],v226 offset:3072\n"
MF"v[204:207],a[124:127],v[24:27],v[204:207]\n"
EX"v63,v63\n"
MF"v[204:207],a[120:123],v[76:79],v[204:207]\n"
MF"a[132:135],a[128:131],v[64:67],a[132:135]\n"
MF"a[132:135],a[128:131],v[0:3],a[132:135]\n"
RD"a[80:83],v226 offset:4096\n"
MF"a[136:139],a[128:131],v[8:11],a[136:139]\n"
MF"a[136:139],a[128:131],v[68:71],a[136:139]\n"
RD"a[84:87],v226 offset:5120\n"
MF"a[140:143],a[128:131],v[72:75],a[140:143]\n"
MF"a[140:143],a[128:131],v[16:19],a[140:143]\n"
RD"a[88:91],v226 offset:6144\n"
MF"a[144:147],a[128:131],v[24:27],a[144:147]\n"
MF"a[144:147],a[128:131],v[76:79],a[144:147]\n"
"s_sub_u32 s56,s56,1\n"
"s_cbranch_scc0 LP1_%=\n"
"s_cmp_eq_u32 s57,2\n"
"s_cbranch_scc1 LEV_%=\n"
"s_cmp_eq_u32 s57,3\n"
"s_cbranch_scc1 LRT_%=\n"
"LDA_%=:\n"
WT"lgkmcnt(6)\n"
MF"v[0:3],a[64:67],a[0:3],v[208:211]\n"
WT"lgkmcnt(5)\n"
"v_readlane_b32 s43,v228,s42\n"
PK"v64,v32,v33\n"
MF"v[0:3],a[68:71],a[4:7],v[0:3]\n"
"s_mov_b32 m0,s40\n"
"v_readlane_b32 s44,v229,s42\n"
PK"v65,v34,v35\n"
MF"v[8:11],a[68:71],a[20:23],0\n"
PK"v66,v36,v37\n"
PK"v67,v38,v39\n"
MF"v[8:11],a[64:67],a[16:19],v[8:11]\n"
SA"s42,s42,1\n"
BL"v225,s[8:11],s43 offen offset:0 sc1 lds\n"
MF"v[16:19],a[64:67],a[32:35],0\n"
PK"v68,v40,v41\n"
PK"v69,v42,v43\n"
MF"v[16:19],a[68:71],a[36:39],v[16:19]\n"
RD"a[92:95],v226 offset:7168\n"
PK"v70,v44,v45\n"
MF"v[24:27],a[68:71],a[52:55],0\n"
PK"v71,v46,v47\n"
PK"v72,v48,v49\n"
MF"v[24:27],a[64:67],a[48:51],v[24:27]\n"
WT"lgkmcnt(1)\n"
BL"v225,s[8:11],s43 offen offset:1024 sc1 lds\n"
MF"v[0:3],a[72:75],a[8:11],v[0:3]\n"
PK"v73,v50,v51\n"
"v_mov_b32 v227,v226\n"
MF"v[0:3],a[76:79],a[12:15],v[0:3]\n"
PK"v74,v52,v53\n"
PK"v75,v54,v55\n"
MF"v[8:11],a[76:79],a[28:31],v[8:11]\n"
PK"v76,v56,v57\n"
PK"v77,v58,v59\n"
MF"v[8:11],a[72:75],a[24:27],v[8:11]\n"
PK"v78,v60,v61\n"
PK"v79,v62,v63\n"
MF"v[16:19],a[72:75],a[40:43],v[16:19]\n"
MF"v[16:19],a[76:79],a[44:47],v[16:19]\n"
RD"a[96:99],v226 offset:8192\n"
MF"v[24:27],a[76:79],a[60:63],v[24:27]\n"
EX"v0,v0\n"
MF"v[24:27],a[72:75],a[56:59],v[24:27]\n"
EX"v1,v1\n"
MF"v[4:7],a[80:83],a[0:3],v[212:215]\n"
EX"v2,v2\n"
MF"v[4:7],a[84:87],a[4:7],v[4:7]\n"
RD"a[100:103],v226 offset:9216\n"
PK"v0,v0,v1\n"
MF"v[12:15],a[84:87],a[20:23],0\n"
EX"v3,v3\n"
MF"v[12:15],a[80:83],a[16:19],v[12:15]\n"
EX"v8,v8\n"
MF"v[20:23],a[80:83],a[32:35],0\n"
EX"v9,v9\n"
MF"v[20:23],a[84:87],a[36:39],v[20:23]\n"
RD"a[104:107],v226 offset:10240\n"
PK"v1,v2,v3\n"
MF"v[28:31],a[84:87],a[52:55],0\n"
EX"v10,v10\n"
MF"v[28:31],a[80:83],a[48:51],v[28:31]\n"
EX"v11,v11\n"
MF"v[4:7],a[88:91],a[8:11],v[4:7]\n"
WT"lgkmcnt(1)\n"
EX"v16,v16\n"
MF"v[4:7],a[92:95],a[12:15],v[4:7]\n"
RD"a[108:111],v226 offset:11264\n"
PK"v8,v8,v9\n"
MF"v[12:15],a[92:95],a[28:31],v[12:15]\n"
EX"v17,v17\n"
MF"v[12:15],a[88:91],a[24:27],v[12:15]\n"
EX"v18,v18\n"
MF"v[20:23],a[88:91],a[40:43],v[20:23]\n"
EX"v19,v19\n"
MF"v[20:23],a[92:95],a[44:47],v[20:23]\n"
RD"a[112:115],v226 offset:12288\n"
PK"v9,v10,v11\n"
MF"v[28:31],a[92:95],a[60:63],v[28:31]\n"
EX"v24,v24\n"
MF"v[28:31],a[88:91],a[56:59],v[28:31]\n"
EX"v25,v25\n"
MF"v[32:35],a[96:99],a[0:3],v[216:219]\n"
EX"v26,v26\n"
MF"v[32:35],a[100:103],a[4:7],v[32:35]\n"
RD"a[116:119],v226 offset:13312\n"
PK"v16,v16,v17\n"
MF"v[40:43],a[100:103],a[20:23],0\n"
EX"v27,v27\n"
MF"v[40:43],a[96:99],a[16:19],v[40:43]\n"
EX"v4,v4\n"
MF"v[48:51],a[96:99],a[32:35],0\n"
EX"v5,v5\n"
MF"v[48:51],a[100:103],a[36:39],v[48:51]\n"
RD"a[120:123],v226 offset:14336\n"
PK"v17,v18,v19\n"
MF"v[56:59],a[100:103],a[52:55],0\n"
EX"v6,v6\n"
MF"v[56:59],a[96:99],a[48:51],v[56:59]\n"
WT"lgkmcnt(2)\n"
EX"v7,v7\n"
MF"v[32:35],a[104:107],a[8:11],v[32:35]\n"
EX"v12,v12\n"
MF"v[32:35],a[108:111],a[12:15],v[32:35]\n"
RD"a[124:127],v226 offset:15360\n"
PK"v24,v24,v25\n"
MF"v[40:43],a[108:111],a[28:31],v[40:43]\n"
EX"v13,v13\n"
MF"v[40:43],a[104:107],a[24:27],v[40:43]\n"
EX"v14,v14\n"
MF"v[48:51],a[104:107],a[40:43],v[48:51]\n"
EX"v15,v15\n"
MF"v[48:51],a[108:111],a[44:47],v[48:51]\n"
RD"a[64:67],v227 offset:16384\n"
PK"v25,v26,v27\n"
MF"v[56:59],a[108:111],a[60:63],v[56:59]\n"
EX"v20,v20\n"
MF"v[56:59],a[104:107],a[56:59],v[56:59]\n"
EX"v21,v21\n"
MF"v[36:39],a[112:115],a[0:3],v[220:223]\n"
WT"lgkmcnt(1)\n"
EX"v22,v22\n"
MF"v[36:39],a[116:119],a[4:7],v[36:39]\n"
RD"a[68:71],v227 offset:24576\n"
PK"v2,v4,v5\n"
MF"v[44:47],a[116:119],a[20:23],0\n"
EX"v23,v23\n"
MF"v[44:47],a[112:115],a[16:19],v[44:47]\n"
EX"v28,v28\n"
MF"v[52:55],a[112:115],a[32:35],0\n"
EX"v29,v29\n"
MF"v[52:55],a[116:119],a[36:39],v[52:55]\n"
RD"a[72:75],v227 offset:17408\n"
PK"v3,v6,v7\n"
MF"v[60:63],a[116:119],a[52:55],0\n"
EX"v30,v30\n"
MF"v[60:63],a[112:115],a[48:51],v[60:63]\n"
EX"v31,v31\n"
MF"v[36:39],a[120:123],a[8:11],v[36:39]\n"
PK"v10,v12,v13\n"
PK"v11,v14,v15\n"
MF"v[36:39],a[124:127],a[12:15],v[36:39]\n"
RD"a[76:79],v227 offset:25600\n"
PK"v18,v20,v21\n"
MF"v[44:47],a[124:127],a[28:31],v[44:47]\n"
PK"v19,v22,v23\n"
PK"v26,v28,v29\n"
MF"v[44:47],a[120:123],a[24:27],v[44:47]\n"
PK"v27,v30,v31\n"
MF"v[52:55],a[120:123],a[40:43],v[52:55]\n"
MF"v[52:55],a[124:127],a[44:47],v[52:55]\n"
RD"a[80:83],v227 offset:18432\n"
MF"v[60:63],a[124:127],a[60:63],v[60:63]\n"
EX"v32,v32\n"
MF"v[60:63],a[120:123],a[56:59],v[60:63]\n"
WT"lgkmcnt(2)\n"
SA"s41,s41,32768\n"
"s_cmp_eq_u32 s41,163840\n"
MF"v[80:83],a[64:67],v[64:67],v[80:83]\n"
"s_cselect_b32 s41,0,s41\n"
EX"v33,v33\n"
MF"v[80:83],a[68:71],v[0:3],v[80:83]\n"
RD"a[84:87],v227 offset:26624\n"
"v_add_u32 v226,s41,v224\n"
MF"v[112:115],a[68:71],v[8:11],v[112:115]\n"
EX"v34,v34\n"
MF"v[112:115],a[64:67],v[68:71],v[112:115]\n"
MF"v[144:147],a[64:67],v[72:75],v[144:147]\n"
EX"v35,v35\n"
MF"v[144:147],a[68:71],v[16:19],v[144:147]\n"
RD"a[88:91],v227 offset:19456\n"
MF"v[176:179],a[68:71],v[24:27],v[176:179]\n"
EX"v40,v40\n"
MF"v[176:179],a[64:67],v[76:79],v[176:179]\n"
BL"v225,s[8:11],s43 offen offset:2048 sc1 lds\n"
MF"v[84:87],a[72:75],v[64:67],v[84:87]\n"
WT"lgkmcnt(1)\n"
EX"v41,v41\n"
MF"v[84:87],a[76:79],v[0:3],v[84:87]\n"
RD"a[92:95],v227 offset:27648\n"
MF"v[116:119],a[76:79],v[8:11],v[116:119]\n"
EX"v42,v42\n"
MF"v[116:119],a[72:75],v[68:71],v[116:119]\n"
MF"v[148:151],a[72:75],v[72:75],v[148:151]\n"
EX"v43,v43\n"
MF"v[148:151],a[76:79],v[16:19],v[148:151]\n"
RD"a[96:99],v227 offset:20480\n"
MF"v[180:183],a[76:79],v[24:27],v[180:183]\n"
EX"v48,v48\n"
MF"v[180:183],a[72:75],v[76:79],v[180:183]\n"
BL"v225,s[8:11],s43 offen offset:3072 sc1 lds\n"
MF"v[88:91],a[80:83],v[64:67],v[88:91]\n"
EX"v49,v49\n"
MF"v[88:91],a[84:87],v[0:3],v[88:91]\n"
RD"a[100:103],v227 offset:28672\n"
MF"v[120:123],a[84:87],v[8:11],v[120:123]\n"
EX"v50,v50\n"
MF"v[120:123],a[80:83],v[68:71],v[120:123]\n"
SA"m0,s40,16384\n"
MF"v[152:155],a[80:83],v[72:75],v[152:155]\n"
EX"v51,v51\n"
MF"v[152:155],a[84:87],v[16:19],v[152:155]\n"
RD"a[104:107],v227 offset:21504\n"
MF"v[184:187],a[84:87],v[24:27],v[184:187]\n"
EX"v56,v56\n"
MF"v[184:187],a[80:83],v[76:79],v[184:187]\n"
WT"lgkmcnt(2)\n"
BL"v225,s[8:11],s44 offen offset:0 sc1 lds\n"
MF"v[92:95],a[88:91],v[64:67],v[92:95]\n"
SA"s40,s40,32768\n"
EX"v57,v57\n"
MF"v[92:95],a[92:95],v[0:3],v[92:95]\n"
"s_cmp_eq_u32 s40,s46\n"
RD"a[108:111],v227 offset:29696\n"
"s_cselect_b32 s40,s47,s40\n"
MF"v[124:127],a[92:95],v[8:11],v[124:127]\n"
EX"v58,v58\n"
MF"v[124:127],a[88:91],v[68:71],v[124:127]\n"
MF"v[156:159],a[88:91],v[72:75],v[156:159]\n"
EX"v59,v59\n"
MF"v[156:159],a[92:95],v[16:19],v[156:159]\n"
RD"a[112:115],v227 offset:22528\n"
MF"v[188:191],a[92:95],v[24:27],v[188:191]\n"
EX"v36,v36\n"
MF"v[188:191],a[88:91],v[76:79],v[188:191]\n"
BL"v225,s[8:11],s44 offen offset:1024 sc1 lds\n"
MF"v[96:99],a[96:99],v[64:67],v[96:99]\n"
WT"lgkmcnt(1)\n"
EX"v37,v37\n"
MF"v[96:99],a[100:103],v[0:3],v[96:99]\n"
RD"a[116:119],v227 offset:30720\n"
MF"v[128:131],a[100:103],v[8:11],v[128:131]\n"
EX"v38,v38\n"
MF"v[128:131],a[96:99],v[68:71],v[128:131]\n"
MF"v[160:163],a[96:99],v[72:75],v[160:163]\n"
EX"v39,v39\n"
MF"v[160:163],a[100:103],v[16:19],v[160:163]\n"
RD"a[120:123],v227 offset:23552\n"
MF"v[192:195],a[100:103],v[24:27],v[192:195]\n"
EX"v44,v44\n"
MF"v[192:195],a[96:99],v[76:79],v[192:195]\n"
BL"v225,s[8:11],s44 offen offset:2048 sc1 lds\n"
MF"v[100:103],a[104:107],v[64:67],v[100:103]\n"
EX"v45,v45\n"
MF"v[100:103],a[108:111],v[0:3],v[100:103]\n"
RD"a[124:127],v227 offset:31744\n"
MF"v[132:135],a[108:111],v[8:11],v[132:135]\n"
EX"v46,v46\n"
MF"v[132:135],a[104:107],v[68:71],v[132:135]\n"
MF"v[164:167],a[104:107],v[72:75],v[164:167]\n"
EX"v47,v47\n"
MF"v[164:167],a[108:111],v[16:19],v[164:167]\n"
MF"v[196:199],a[108:111],v[24:27],v[196:199]\n"
EX"v52,v52\n"
MF"v[196:199],a[104:107],v[76:79],v[196:199]\n"
WT"lgkmcnt(1)\n"
BL"v225,s[8:11],s44 offen offset:3072 sc1 lds\n"
MF"v[104:107],a[112:115],v[64:67],v[104:107]\n"
EX"v53,v53\n"
MF"v[104:107],a[116:119],v[0:3],v[104:107]\n"
MF"v[136:139],a[116:119],v[8:11],v[136:139]\n"
EX"v54,v54\n"
MF"v[136:139],a[112:115],v[68:71],v[136:139]\n"
MF"v[168:171],a[112:115],v[72:75],v[168:171]\n"
WT"vmcnt(16) lgkmcnt(0)\n"
"s_barrier\n"
MF"v[168:171],a[116:119],v[16:19],v[168:171]\n"
RD"a[64:67],v226 offset:0\n"
MF"v[200:203],a[116:119],v[24:27],v[200:203]\n"
EX"v55,v55\n"
MF"v[200:203],a[112:115],v[76:79],v[200:203]\n"
MF"v[108:111],a[120:123],v[64:67],v[108:111]\n"
EX"v60,v60\n"
MF"v[108:111],a[124:127],v[0:3],v[108:111]\n"
RD"a[68:71],v226 offset:1024\n"
MF"v[140:143],a[124:127],v[8:11],v[140:143]\n"
EX"v61,v61\n"
MF"v[140:143],a[120:123],v[68:71],v[140:143]\n"
RD"a[72:75],v226 offset:2048\n"
MF"v[172:175],a[120:123],v[72:75],v[172:175]\n"
EX"v62,v62\n"
MF"v[172:175],a[124:127],v[16:19],v[172:175]\n"
RD"a[76:79],v226 offset:3072\n"
MF"v[204:207],a[124:127],v[24:27],v[204:207]\n"
EX"v63,v63\n"
MF"v[204:207],a[120:123],v[76:79],v[204:207]\n"
MF"a[132:135],a[128:131],v[64:67],a[132:135]\n"
MF"a[132:135],a[128:131],v[0:3],a[132:135]\n"
RD"a[80:83],v226 offset:4096\n"
MF"a[136:139],a[128:131],v[8:11],a[136:139]\n"
MF"a[136:139],a[128:131],v[68:71],a[136:139]\n"
RD"a[84:87],v226 offset:5120\n"
MF"a[140:143],a[128:131],v[72:75],a[140:143]\n"
MF"a[140:143],a[128:131],v[16:19],a[140:143]\n"
RD"a[88:91],v226 offset:6144\n"
MF"a[144:147],a[128:131],v[24:27],a[144:147]\n"
MF"a[144:147],a[128:131],v[76:79],a[144:147]\n"
"LDB_%=:\n"
WT"lgkmcnt(6)\n"
MF"v[8:11],a[64:67],a[16:19],v[208:211]\n"
WT"lgkmcnt(5)\n"
"v_readlane_b32 s43,v228,s42\n"
PK"v64,v32,v33\n"
MF"v[8:11],a[68:71],a[20:23],v[8:11]\n"
RD"a[92:95],v226 offset:7168\n"
PK"v65,v34,v35\n"
MF"v[16:19],a[68:71],a[36:39],0\n"
PK"v66,v36,v37\n"
PK"v67,v38,v39\n"
MF"v[16:19],a[64:67],a[32:35],v[16:19]\n"
"s_mov_b32 m0,s40\n"
PK"v68,v40,v41\n"
PK"v69,v42,v43\n"
MF"v[24:27],a[64:67],a[48:51],0\n"
PK"v70,v44,v45\n"
PK"v71,v46,v47\n"
MF"v[24:27],a[68:71],a[52:55],v[24:27]\n"
WT"lgkmcnt(1)\n"
RD"a[96:99],v226 offset:8192\n"
"v_readlane_b32 s44,v229,s42\n"
MF"v[8:11],a[72:75],a[24:27],v[8:11]\n"
PK"v72,v48,v49\n"
PK"v73,v50,v51\n"
MF"v[8:11],a[76:79],a[28:31],v[8:11]\n"
SA"s42,s42,1\n"
BL"v225,s[8:11],s43 offen offset:0 sc1 lds\n"
"v_mov_b32 v227,v226\n"
MF"v[16:19],a[76:79],a[44:47],v[16:19]\n"
PK"v74,v52,v53\n"
PK"v75,v54,v55\n"
MF"v[16:19],a[72:75],a[40:43],v[16:19]\n"
RD"a[100:103],v226 offset:9216\n"
PK"v76,v56,v57\n"
PK"v77,v58,v59\n"
MF"v[24:27],a[72:75],a[56:59],v[24:27]\n"
PK"v78,v60,v61\n"
PK"v79,v62,v63\n"
MF"v[24:27],a[76:79],a[60:63],v[24:27]\n"
RD"a[104:107],v226 offset:10240\n"
MF"v[12:15],a[80:83],a[16:19],v[212:215]\n"
EX"v8,v8\n"
MF"v[12:15],a[84:87],a[20:23],v[12:15]\n"
RD"a[108:111],v226 offset:11264\n"
"v_lshrrev_b32 v235,7,v224\n"
MF"v[20:23],a[84:87],a[36:39],0\n"
EX"v9,v9\n"
MF"v[20:23],a[80:83],a[32:35],v[20:23]\n"
BL"v225,s[8:11],s43 offen offset:1024 sc1 lds\n"
MF"v[28:31],a[80:83],a[48:51],0\n"
EX"v10,v10\n"
"v_lshrrev_b32 v230,1,v224\n"
MF"v[28:31],a[84:87],a[52:55],v[28:31]\n"
RD"a[112:115],v226 offset:12288\n"
"v_lshl_add_u32 v230,v235,27,v230\n"
MF"v[12:15],a[88:91],a[24:27],v[12:15]\n"
WT"lgkmcnt(2)\n"
EX"v11,v11\n"
PK"v8,v8,v9\n"
MF"v[12:15],a[92:95],a[28:31],v[12:15]\n"
RD"a[116:119],v226 offset:13312\n"
PK"v9,v10,v11\n"
MF"v[20:23],a[92:95],a[44:47],v[20:23]\n"
"s_lshl_b32 s48,s35,8\n"
EX"v16,v16\n"
MF"v[20:23],a[88:91],a[40:43],v[20:23]\n"
MF"v[28:31],a[88:91],a[56:59],v[28:31]\n"
SA"s48,s48,92160\n"
EX"v17,v17\n"
MF"v[28:31],a[92:95],a[60:63],v[28:31]\n"
BL"v225,s[8:11],s43 offen offset:2048 sc1 lds\n"
MF"v[40:43],a[96:99],a[16:19],v[216:219]\n"
"s_andn2_b32 s49,s61,255\n"
EX"v18,v18\n"
MF"v[40:43],a[100:103],a[20:23],v[40:43]\n"
RD"a[120:123],v226 offset:14336\n"
PK"v16,v16,v17\n"
MF"v[48:51],a[100:103],a[36:39],0\n"
EX"v19,v19\n"
MF"v[48:51],a[96:99],a[32:35],v[48:51]\n"
RD"a[124:127],v226 offset:15360\n"
"v_bfe_u32 v236,v224,4,2\n"
MF"v[56:59],a[96:99],a[48:51],0\n"
EX"v24,v24\n"
MF"v[56:59],a[100:103],a[52:55],v[56:59]\n"
PK"v17,v18,v19\n"
"v_lshl_add_u32 v236,v236,6,s49\n"
MF"v[40:43],a[104:107],a[24:27],v[40:43]\n"
WT"lgkmcnt(2)\n"
EX"v25,v25\n"
MF"v[40:43],a[108:111],a[28:31],v[40:43]\n"
BL"v225,s[8:11],s43 offen offset:3072 sc1 lds\n"
MF"v[48:51],a[108:111],a[44:47],v[48:51]\n"
EX"v26,v26\n"
MF"v[48:51],a[104:107],a[40:43],v[48:51]\n"
RD"a[64:67],v227 offset:16384\n"
"v_mov_b32 v237,s48\n"
MF"v[56:59],a[104:107],a[56:59],v[56:59]\n"
EX"v27,v27\n"
PK"v24,v24,v25\n"
MF"v[56:59],a[108:111],a[60:63],v[56:59]\n"
SA"m0,s40,16384\n"
"v_cmp_lt_u32 vcc,511,v224\n"
PK"v25,v26,v27\n"
MF"v[44:47],a[112:115],a[16:19],v[220:223]\n"
EX"v12,v12\n"
MF"v[44:47],a[116:119],a[20:23],v[44:47]\n"
RD"a[68:71],v227 offset:17408\n"
"v_cndmask_b32 v236,v236,v237,vcc\n"
MF"v[52:55],a[116:119],a[36:39],0\n"
EX"v13,v13\n"
MF"v[52:55],a[112:115],a[32:35],v[52:55]\n"
BL"v225,s[8:11],s44 offen offset:0 sc1 lds\n"
MF"v[60:63],a[112:115],a[48:51],0\n"
SA"s40,s40,32768\n"
EX"v14,v14\n"
MF"v[60:63],a[116:119],a[52:55],v[60:63]\n"
WT"lgkmcnt(1)\n"
RD"a[72:75],v227 offset:18432\n"
PK"v10,v12,v13\n"
MF"v[44:47],a[120:123],a[24:27],v[44:47]\n"
"s_cmp_eq_u32 s40,s46\n"
EX"v15,v15\n"
MF"v[44:47],a[124:127],a[28:31],v[44:47]\n"
"s_cselect_b32 s40,s47,s40\n"
PK"v11,v14,v15\n"
MF"v[52:55],a[124:127],a[44:47],v[52:55]\n"
EX"v20,v20\n"
MF"v[52:55],a[120:123],a[40:43],v[52:55]\n"
RD"a[76:79],v227 offset:19456\n"
MF"v[60:63],a[120:123],a[56:59],v[60:63]\n"
EX"v21,v21\n"
MF"v[60:63],a[124:127],a[60:63],v[60:63]\n"
SA"s41,s41,32768\n"
BL"v225,s[8:11],s44 offen offset:1024 sc1 lds\n"
MF"v[80:83],a[64:67],v[64:67],v[80:83]\n"
"s_cmp_eq_u32 s41,163840\n"
EX"v22,v22\n"
MF"v[112:115],a[64:67],v[68:71],v[112:115]\n"
"s_cselect_b32 s41,0,s41\n"
RD"a[80:83],v227 offset:20480\n"
"v_add_u32 v226,s41,v224\n"
MF"v[144:147],a[64:67],v[72:75],v[144:147]\n"
EX"v23,v23\n"
PK"v18,v20,v21\n"
MF"v[176:179],a[64:67],v[76:79],v[176:179]\n"
WT"lgkmcnt(2)\n"
PK"v19,v22,v23\n"
MF"v[84:87],a[68:71],v[64:67],v[84:87]\n"
EX"v28,v28\n"
MF"v[116:119],a[68:71],v[68:71],v[116:119]\n"
RD"a[84:87],v227 offset:21504\n"
MF"v[148:151],a[68:71],v[72:75],v[148:151]\n"
EX"v29,v29\n"
MF"v[180:183],a[68:71],v[76:79],v[180:183]\n"
BL"v225,s[8:11],s44 offen offset:2048 sc1 lds\n"
MF"v[88:91],a[72:75],v[64:67],v[88:91]\n"
EX"v30,v30\n"
MF"v[120:123],a[72:75],v[68:71],v[120:123]\n"
RD"a[88:91],v227 offset:22528\n"
PK"v26,v28,v29\n"
MF"v[152:155],a[72:75],v[72:75],v[152:155]\n"
EX"v31,v31\n"
MF"v[184:187],a[72:75],v[76:79],v[184:187]\n"
WT"lgkmcnt(2)\n"
PK"v27,v30,v31\n"
MF"v[92:95],a[76:79],v[64:67],v[92:95]\n"
EX"v40,v40\n"
MF"v[124:127],a[76:79],v[68:71],v[124:127]\n"
RD"a[92:95],v227 offset:23552\n"
MF"v[156:159],a[76:79],v[72:75],v[156:159]\n"
EX"v41,v41\n"
MF"v[188:191],a[76:79],v[76:79],v[188:191]\n"
BL"v225,s[8:11],s44 offen offset:3072 sc1 lds\n"
MF"v[96:99],a[80:83],v[64:67],v[96:99]\n"
EX"v42,v42\n"
MF"v[128:131],a[80:83],v[68:71],v[128:131]\n"
RD"a[96:99],v227 offset:24576\n"
MF"v[160:163],a[80:83],v[72:75],v[160:163]\n"
EX"v43,v43\n"
MF"v[192:195],a[80:83],v[76:79],v[192:195]\n"
WT"lgkmcnt(2)\n"
RD"a[100:103],v227 offset:25600\n"
MF"v[100:103],a[84:87],v[64:67],v[100:103]\n"
EX"v48,v48\n"
MF"v[132:135],a[84:87],v[68:71],v[132:135]\n"
MF"v[164:167],a[84:87],v[72:75],v[164:167]\n"
EX"v49,v49\n"
MF"v[196:199],a[84:87],v[76:79],v[196:199]\n"
MF"v[104:107],a[88:91],v[64:67],v[104:107]\n"
EX"v50,v50\n"
MF"v[136:139],a[88:91],v[68:71],v[136:139]\n"
RD"a[104:107],v227 offset:26624\n"
MF"v[168:171],a[88:91],v[72:75],v[168:171]\n"
EX"v51,v51\n"
MF"v[200:203],a[88:91],v[76:79],v[200:203]\n"
WT"lgkmcnt(1)\n"
RD"a[108:111],v227 offset:27648\n"
MF"v[108:111],a[92:95],v[64:67],v[108:111]\n"
EX"v44,v44\n"
MF"v[140:143],a[92:95],v[68:71],v[140:143]\n"
RD"a[112:115],v227 offset:28672\n"
MF"v[172:175],a[92:95],v[72:75],v[172:175]\n"
EX"v45,v45\n"
MF"v[204:207],a[92:95],v[76:79],v[204:207]\n"
MF"a[132:135],a[128:131],v[64:67],a[132:135]\n"
EX"v46,v46\n"
MF"a[136:139],a[128:131],v[68:71],a[136:139]\n"
RD"a[116:119],v227 offset:29696\n"
PK"v68,v40,v41\n"
PK"v69,v42,v43\n"
MF"a[140:143],a[128:131],v[72:75],a[140:143]\n"
EX"v47,v47\n"
PK"v72,v48,v49\n"
MF"a[144:147],a[128:131],v[76:79],a[144:147]\n"
RD"a[120:123],v227 offset:30720\n"
PK"v73,v50,v51\n"
PK"v70,v44,v45\n"
MF"v[112:115],a[96:99],v[8:11],v[112:115]\n"
EX"v52,v52\n"
MF"v[144:147],a[96:99],v[16:19],v[144:147]\n"
RD"a[124:127],v227 offset:31744\n"
PK"v71,v46,v47\n"
MF"v[176:179],a[96:99],v[24:27],v[176:179]\n"
EX"v53,v53\n"
MF"v[116:119],a[100:103],v[8:11],v[116:119]\n"
"v_accvgpr_read_b32 v251,a132\n"
"v_subrev_u32 v253,0x30800000,v251\n"
MF"v[148:151],a[100:103],v[16:19],v[148:151]\n"
"v_cmp_le_u32 vcc,0x1f000000,v253\n"
"s_cmp_lg_u64 vcc,0\n"
"s_cbranch_scc1 L1014_%=\n"
"L1013_%=:\n"
MF"v[180:183],a[100:103],v[24:27],v[180:183]\n"
WT"lgkmcnt(2)\n"
EX"v54,v54\n"
MF"v[120:123],a[104:107],v[8:11],v[120:123]\n"
"v_rcp_f32 v251,v251\n"
PK"v74,v52,v53\n"
MF"v[152:155],a[104:107],v[16:19],v[152:155]\n"
EX"v55,v55\n"
MF"v[184:187],a[104:107],v[24:27],v[184:187]\n"
WT"vmcnt(16) lgkmcnt(0)\n"
"s_barrier\n"
MF"v[124:127],a[108:111],v[8:11],v[124:127]\n"
RD"a[64:67],v226 offset:0\n"
MU"v80,v251,v80\n"
MU"v81,v251,v81\n"
MF"v[156:159],a[108:111],v[16:19],v[156:159]\n"
MU"v82,v251,v82\n"
MU"v83,v251,v83\n"
PK"v75,v54,v55\n"
MF"v[188:191],a[108:111],v[24:27],v[188:191]\n"
MU"v84,v251,v84\n"
MU"v85,v251,v85\n"
MU"v86,v251,v86\n"
MF"v[128:131],a[112:115],v[8:11],v[128:131]\n"
MU"v87,v251,v87\n"
PK"v80,v80,v81\n"
PK"v81,v82,v83\n"
MF"v[160:163],a[112:115],v[16:19],v[160:163]\n"
RD"a[68:71],v226 offset:1024\n"
PK"v82,v84,v85\n"
PK"v83,v86,v87\n"
MF"v[192:195],a[112:115],v[24:27],v[192:195]\n"
BS"v[80:83],v224,s[8:11],s59 offen offset:0 sc1\n"
MU"v88,v251,v88\n"
MF"v[132:135],a[116:119],v[8:11],v[132:135]\n"
RD"a[72:75],v226 offset:2048\n"
MU"v89,v251,v89\n"
MU"v90,v251,v90\n"
MF"v[164:167],a[116:119],v[16:19],v[164:167]\n"
MU"v91,v251,v91\n"
MU"v92,v251,v92\n"
MU"v93,v251,v93\n"
MF"v[196:199],a[116:119],v[24:27],v[196:199]\n"
RD"a[76:79],v226 offset:3072\n"
MU"v94,v251,v94\n"
MU"v95,v251,v95\n"
MF"v[136:139],a[120:123],v[8:11],v[136:139]\n"
PK"v88,v88,v89\n"
PK"v89,v90,v91\n"
MF"v[168:171],a[120:123],v[16:19],v[168:171]\n"
PK"v90,v92,v93\n"
PK"v91,v94,v95\n"
MF"v[200:203],a[120:123],v[24:27],v[200:203]\n"
BS"v[88:91],v224,s[8:11],s59 offen offset:1024 sc1\n"
MU"v96,v251,v96\n"
MF"v[140:143],a[124:127],v[8:11],v[140:143]\n"
RD"a[80:83],v226 offset:4096\n"
MU"v97,v251,v97\n"
MU"v98,v251,v98\n"
MF"v[172:175],a[124:127],v[16:19],v[172:175]\n"
MU"v99,v251,v99\n"
MU"v100,v251,v100\n"
MU"v101,v251,v101\n"
MF"v[204:207],a[124:127],v[24:27],v[204:207]\n"
MU"v104,v251,v104\n"
MU"v105,v251,v105\n"
MU"v102,v251,v102\n"
MF"a[136:139],a[128:131],v[8:11],a[136:139]\n"
PK"v96,v96,v97\n"
MU"v106,v251,v106\n"
MU"v103,v251,v103\n"
MF"a[140:143],a[128:131],v[16:19],a[140:143]\n"
MU"v107,v251,v107\n"
PK"v97,v98,v99\n"
PK"v98,v100,v101\n"
MF"a[144:147],a[128:131],v[24:27],a[144:147]\n"
MU"v108,v251,v108\n"
MU"v109,v251,v109\n"
PK"v99,v102,v103\n"
BS"v[96:99],v224,s[8:11],s59 offen offset:2048 sc1\n"
MU"v110,v251,v110\n"
MU"v111,v251,v111\n"
PK"v104,v104,v105\n"
PK"v105,v106,v107\n"
PK"v106,v108,v109\n"
PK"v107,v110,v111\n"
BS"v[104:107],v224,s[8:11],s59 offen offset:3072 sc1\n"
RD"a[84:87],v226 offset:5120\n"
RD"a[88:91],v226 offset:6144\n"
"s_cmp_eq_u32 s34,7\n"
"s_cbranch_scc1 LD2_%=\n"
WT"lgkmcnt(6)\n"
MF"v[0:3],a[64:67],a[32:35],0\n"
WT"lgkmcnt(5)\n"
EX"v56,v56\n"
MF"v[0:3],a[68:71],a[36:39],v[0:3]\n"
"s_mov_b32 m0,s40\n"
EX"v57,v57\n"
MF"v[8:11],a[68:71],a[52:55],0\n"
"s_lshl_b32 s50,s35,6\n"
RD"a[92:95],v226 offset:7168\n"
"v_readlane_b32 s43,v228,s42\n"
MF"v[8:11],a[64:67],a[48:51],v[8:11]\n"
WT"lgkmcnt(5)\n"
EX"v58,v58\n"
MF"v[0:3],a[72:75],a[40:43],v[0:3]\n"
WT"lgkmcnt(1)\n"
BL"v225,s[8:11],s43 offen offset:0 sc1 lds\n"
"v_readlane_b32 s44,v229,s42\n"
MF"v[0:3],a[76:79],a[44:47],v[0:3]\n"
SA"s50,s50,4352\n"
RD"a[96:99],v226 offset:8192\n"
RD"a[100:103],v226 offset:9216\n"
PK"v76,v56,v57\n"
MF"v[8:11],a[76:79],a[60:63],v[8:11]\n"
SA"s42,s42,1\n"
EX"v59,v59\n"
MF"v[8:11],a[72:75],a[56:59],v[8:11]\n"
SA"s41,s41,32768\n"
RD"a[104:107],v226 offset:10240\n"
"v_lshrrev_b32 v237,1,v224\n"
MF"v[4:7],a[80:83],a[32:35],0\n"
"s_cmp_eq_u32 s41,163840\n"
EX"v60,v60\n"
"v_and_b32 v237,56,v237\n"
MF"v[4:7],a[84:87],a[36:39],v[4:7]\n"
"s_cselect_b32 s41,0,s41\n"
EX"v61,v61\n"
"v_add_u32 v227,s41,v224\n"
MF"v[12:15],a[84:87],a[52:55],0\n"
RD"a[108:111],v226 offset:11264\n"
PK"v77,v58,v59\n"
MF"v[12:15],a[80:83],a[48:51],v[12:15]\n"
BL"v225,s[8:11],s43 offen offset:1024 sc1 lds\n"
MF"v[4:7],a[88:91],a[40:43],v[4:7]\n"
WT"lgkmcnt(2)\n"
EX"v62,v62\n"
MF"v[4:7],a[92:95],a[44:47],v[4:7]\n"
RD"a[112:115],v226 offset:12288\n"
RD"a[116:119],v226 offset:13312\n"
PK"v78,v60,v61\n"
MF"v[12:15],a[92:95],a[60:63],v[12:15]\n"
EX"v63,v63\n"
MF"v[12:15],a[88:91],a[56:59],v[12:15]\n"
RD"a[120:123],v226 offset:14336\n"
PK"v79,v62,v63\n"
MF"v[16:19],a[96:99],a[32:35],0\n"
EX"v0,v0\n"
MF"v[16:19],a[100:103],a[36:39],v[16:19]\n"
EX"v1,v1\n"
MF"v[24:27],a[100:103],a[52:55],0\n"
RD"a[124:127],v226 offset:15360\n"
PK"v0,v0,v1\n"
MF"v[24:27],a[96:99],a[48:51],v[24:27]\n"
WT"lgkmcnt(4)\n"
BL"v225,s[8:11],s43 offen offset:2048 sc1 lds\n"
MF"v[16:19],a[104:107],a[40:43],v[16:19]\n"
"s_lshl_b32 s56,s34,1\n"
EX"v2,v2\n"
MF"v[16:19],a[108:111],a[44:47],v[16:19]\n"
"s_sub_u32 s56,12,s56\n"
RD"a[64:67],v226 offset:16384\n"
RD"a[68:71],v226 offset:17408\n"
MF"v[24:27],a[108:111],a[60:63],v[24:27]\n"
"s_sub_u32 s22,s56,2\n"
EX"v3,v3\n"
MF"v[24:27],a[104:107],a[56:59],v[24:27]\n"
WT"lgkmcnt(3)\n"
RD"a[72:75],v226 offset:18432\n"
PK"v1,v2,v3\n"
MF"v[20:23],a[112:115],a[32:35],0\n"
"s_min_u32 s56,s56,1\n"
EX"v8,v8\n"
MF"v[20:23],a[116:119],a[36:39],v[20:23]\n"
"s_mov_b32 s57,s61\n"
EX"v9,v9\n"
MF"v[28:31],a[116:119],a[52:55],0\n"
BL"v225,s[8:11],s43 offen offset:3072 sc1 lds\n"
MF"v[28:31],a[112:115],a[48:51],v[28:31]\n"
RD"a[76:79],v226 offset:19456\n"
RD"a[80:83],v226 offset:20480\n"
MF"v[20:23],a[120:123],a[40:43],v[20:23]\n"
WT"lgkmcnt(3)\n"
EX"v10,v10\n"
PK"v8,v8,v9\n"
MF"v[20:23],a[124:127],a[44:47],v[20:23]\n"
SA"m0,s40,16384\n"
RD"a[84:87],v226 offset:21504\n"
RD"a[88:91],v226 offset:22528\n"
EX"v11,v11\n"
MF"v[28:31],a[124:127],a[60:63],v[28:31]\n"
EX"v4,v4\n"
MF"v[28:31],a[120:123],a[56:59],v[28:31]\n"
"buffer_load_dwordx2 v[238:239],v237,s[8:11],s50 offen sc1\n"
PK"v9,v10,v11\n"
MF"v[32:35],a[64:67],a[32:35],0\n"
EX"v5,v5\n"
MF"v[32:35],a[68:71],a[36:39],v[32:35]\n"
BL"v225,s[8:11],s44 offen offset:0 sc1 lds\n"
MF"v[40:43],a[68:71],a[52:55],0\n"
SA"s40,s40,32768\n"
RD"a[92:95],v226 offset:23552\n"
PK"v2,v4,v5\n"
MF"v[40:43],a[64:67],a[48:51],v[40:43]\n"
WT"lgkmcnt(3)\n"
EX"v6,v6\n"
MF"v[32:35],a[72:75],a[40:43],v[32:35]\n"
"s_cmp_eq_u32 s40,s46\n"
RD"a[96:99],v226 offset:24576\n"
RD"a[100:103],v226 offset:25600\n"
MF"v[32:35],a[76:79],a[44:47],v[32:35]\n"
"s_cselect_b32 s40,s47,s40\n"
EX"v7,v7\n"
MF"v[40:43],a[76:79],a[60:63],v[40:43]\n"
RD"a[104:107],v226 offset:26624\n"
PK"v3,v6,v7\n"
MF"v[40:43],a[72:75],a[56:59],v[40:43]\n"
EX"v12,v12\n"
MF"v[36:39],a[80:83],a[32:35],0\n"
WT"lgkmcnt(4)\n"
EX"v13,v13\n"
MF"v[36:39],a[84:87],a[36:39],v[36:39]\n"
BL"v225,s[8:11],s44 offen offset:1024 sc1 lds\n"
MF"v[44:47],a[84:87],a[52:55],0\n"
EX"v14,v14\n"
MF"v[44:47],a[80:83],a[48:51],v[44:47]\n"
RD"a[108:111],v226 offset:27648\n"
RD"a[112:115],v226 offset:28672\n"
PK"v10,v12,v13\n"
MF"v[36:39],a[88:91],a[40:43],v[36:39]\n"
WT"lgkmcnt(3)\n"
EX"v15,v15\n"
MF"v[36:39],a[92:95],a[44:47],v[36:39]\n"
RD"a[116:119],v226 offset:29696\n"
RD"a[120:123],v226 offset:30720\n"
MF"v[44:47],a[92:95],a[60:63],v[44:47]\n"
EX"v16,v16\n"
PK"v11,v14,v15\n"
MF"v[44:47],a[88:91],a[56:59],v[44:47]\n"
EX"v17,v17\n"
MF"v[48:51],a[96:99],a[32:35],0\n"
BL"v225,s[8:11],s44 offen offset:2048 sc1 lds\n"
MF"v[48:51],a[100:103],a[36:39],v[48:51]\n"
EX"v18,v18\n"
MF"v[56:59],a[100:103],a[52:55],0\n"
RD"a[124:127],v226 offset:31744\n"
PK"v16,v16,v17\n"
MF"v[56:59],a[96:99],a[48:51],v[56:59]\n"
WT"lgkmcnt(3)\n"
EX"v19,v19\n"
MF"v[48:51],a[104:107],a[40:43],v[48:51]\n"
WT"vmcnt(16)\n"
"s_barrier\n"
MF"v[48:51],a[108:111],a[44:47],v[48:51]\n"
RD"a[96:99],v227 offset:8192\n"
RD"a[100:103],v227 offset:9216\n"
MF"v[56:59],a[108:111],a[60:63],v[56:59]\n"
EX"v24,v24\n"
PK"v17,v18,v19\n"
MF"v[56:59],a[104:107],a[56:59],v[56:59]\n"
EX"v25,v25\n"
MF"v[52:55],a[112:115],a[32:35],0\n"
WT"lgkmcnt(3)\n"
BL"v225,s[8:11],s44 offen offset:3072 sc1 lds\n"
MF"v[52:55],a[116:119],a[36:39],v[52:55]\n"
EX"v26,v26\n"
PK"v24,v24,v25\n"
MF"v[60:63],a[116:119],a[52:55],0\n"
EX"v27,v27\n"
"v_readlane_b32 s43,v228,s42\n"
MF"v[60:63],a[112:115],a[48:51],v[60:63]\n"
"s_mov_b32 m0,s40\n"
RD"a[104:107],v227 offset:10240\n"
RD"a[108:111],v227 offset:11264\n"
MF"v[52:55],a[120:123],a[40:43],v[52:55]\n"
WT"lgkmcnt(2)\n"
EX"v20,v20\n"
MF"v[52:55],a[124:127],a[44:47],v[52:55]\n"
EX"v21,v21\n"
PK"v25,v26,v27\n"
MF"v[60:63],a[124:127],a[60:63],v[60:63]\n"
RD"a[112:115],v227 offset:12288\n"
"v_readlane_b32 s44,v229,s42\n"
MF"v[60:63],a[120:123],a[56:59],v[60:63]\n"
SA"s41,s41,32768\n"
EX"v22,v22\n"
MF"v[112:115],a[96:99],v[68:71],v[112:115]\n"
"s_cmp_eq_u32 s41,163840\n"
BL"v225,s[8:11],s43 offen offset:0 sc1 lds\n"
MF"v[144:147],a[96:99],v[72:75],v[144:147]\n"
"s_cselect_b32 s41,0,s41\n"
EX"v23,v23\n"
PK"v18,v20,v21\n"
MF"v[176:179],a[96:99],v[76:79],v[176:179]\n"
SA"s42,s42,1\n"
RD"a[116:119],v227 offset:13312\n"
"v_add_u32 v226,s41,v224\n"
MF"v[116:119],a[100:103],v[68:71],v[116:119]\n"
EX"v28,v28\n"
MF"v[148:151],a[100:103],v[72:75],v[148:151]\n"
RD"a[120:123],v227 offset:14336\n"
PK"v19,v22,v23\n"
MF"v[180:183],a[100:103],v[76:79],v[180:183]\n"
WT"lgkmcnt(2)\n"
EX"v29,v29\n"
MF"v[120:123],a[104:107],v[68:71],v[120:123]\n"
EX"v30,v30\n"
MF"v[152:155],a[104:107],v[72:75],v[152:155]\n"
BL"v225,s[8:11],s43 offen offset:1024 sc1 lds\n"
MF"v[184:187],a[104:107],v[76:79],v[184:187]\n"
EX"v31,v31\n"
PK"v26,v28,v29\n"
MF"v[124:127],a[108:111],v[68:71],v[124:127]\n"
RD"a[124:127],v227 offset:15360\n"
PK"v27,v30,v31\n"
MF"v[156:159],a[108:111],v[72:75],v[156:159]\n"
EX"v32,v32\n"
MF"v[188:191],a[108:111],v[76:79],v[188:191]\n"
MF"v[128:131],a[112:115],v[68:71],v[128:131]\n"
EX"v33,v33\n"
MF"v[160:163],a[112:115],v[72:75],v[160:163]\n"
RD"a[64:67],v227 offset:16384\n"
PK"v64,v32,v33\n"
MF"v[192:195],a[112:115],v[76:79],v[192:195]\n"
WT"lgkmcnt(2)\n"
BL"v225,s[8:11],s43 offen offset:2048 sc1 lds\n"
MF"v[132:135],a[116:119],v[68:71],v[132:135]\n"
EX"v34,v34\n"
MF"v[164:167],a[116:119],v[72:75],v[164:167]\n"
EX"v35,v35\n"
MF"v[196:199],a[116:119],v[76:79],v[196:199]\n"
EX"v40,v40\n"
MF"v[136:139],a[120:123],v[68:71],v[136:139]\n"
RD"a[68:71],v227 offset:24576\n"
PK"v65,v34,v35\n"
MF"v[168:171],a[120:123],v[72:75],v[168:171]\n"
EX"v41,v41\n"
MF"v[200:203],a[120:123],v[76:79],v[200:203]\n"
WT"lgkmcnt(1)\n"
RD"a[72:75],v227 offset:17408\n"
RD"a[76:79],v227 offset:25600\n"
MF"v[140:143],a[124:127],v[68:71],v[140:143]\n"
WT"vmcnt(7)\n"
"v_cmp_eq_u64 vcc,s[28:29],v[238:239]\n"
"s_cmp_eq_u64 vcc,exec\n"
"s_cbranch_scc0 L1019_%=\n"
"L1020_%=:\n"
MF"v[172:175],a[124:127],v[72:75],v[172:175]\n"
EX"v42,v42\n"
MF"v[204:207],a[124:127],v[76:79],v[204:207]\n"
RD"a[80:83],v227 offset:18432\n"
MF"a[136:139],a[128:131],v[68:71],a[136:139]\n"
BL"v225,s[8:11],s43 offen offset:3072 sc1 lds\n"
PK"v68,v40,v41\n"
MF"a[140:143],a[128:131],v[72:75],a[140:143]\n"
SA"m0,s40,16384\n"
EX"v43,v43\n"
MF"a[144:147],a[128:131],v[76:79],a[144:147]\n"
EX"v36,v36\n"
MF"v[144:147],a[64:67],v[0:3],v[144:147]\n"
WT"lgkmcnt(1)\n"
RD"a[84:87],v227 offset:26624\n"
RD"a[88:91],v227 offset:19456\n"
PK"v69,v42,v43\n"
MF"v[144:147],a[68:71],v[16:19],v[144:147]\n"
EX"v37,v37\n"
MF"v[176:179],a[68:71],v[24:27],v[176:179]\n"
SA"s51,s59,16384\n"
RD"a[92:95],v227 offset:27648\n"
RD"a[96:99],v227 offset:20480\n"
MF"v[176:179],a[64:67],v[8:11],v[176:179]\n"
SA"s40,s40,32768\n"
EX"v38,v38\n"
MF"v[148:151],a[72:75],v[0:3],v[148:151]\n"
"s_cmp_eq_u32 s40,s46\n"
BL"v225,s[8:11],s44 offen offset:0 sc1 lds\n"
PK"v66,v36,v37\n"
MF"v[148:151],a[76:79],v[16:19],v[148:151]\n"
"s_cselect_b32 s40,s47,s40\n"
EX"v39,v39\n"
"v_accvgpr_read_b32 v252,a136\n"
MF"v[180:183],a[76:79],v[24:27],v[180:183]\n"
EX"v44,v44\n"
"v_subrev_u32 v253,0x30800000,v252\n"
MF"v[180:183],a[72:75],v[8:11],v[180:183]\n"
WT"lgkmcnt(2)\n"
"v_cmp_le_u32 vcc,0x1f000000,v253\n"
"s_cmp_lg_u64 vcc,0\n"
"s_cbranch_scc1 L1023_%=\n"
"L1022_%=:\n"
MF"v[152:155],a[80:83],v[0:3],v[152:155]\n"
"v_rcp_f32 v252,v252\n"
PK"v67,v38,v39\n"
MF"v[152:155],a[84:87],v[16:19],v[152:155]\n"
RD"a[100:103],v227 offset:28672\n"
RD"a[104:107],v227 offset:21504\n"
EX"v45,v45\n"
MF"v[184:187],a[84:87],v[24:27],v[184:187]\n"
RD"a[108:111],v227 offset:29696\n"
RD"a[112:115],v227 offset:22528\n"
MU"v112,v252,v112\n"
MF"v[184:187],a[80:83],v[8:11],v[184:187]\n"
EX"v46,v46\n"
MU"v113,v252,v113\n"
MF"v[156:159],a[88:91],v[0:3],v[156:159]\n"
WT"lgkmcnt(4)\n"
BL"v225,s[8:11],s44 offen offset:1024 sc1 lds\n"
MU"v114,v252,v114\n"
MF"v[156:159],a[92:95],v[16:19],v[156:159]\n"
EX"v47,v47\n"
MU"v115,v252,v115\n"
MF"v[188:191],a[92:95],v[24:27],v[188:191]\n"
EX"v48,v48\n"
MU"v116,v252,v116\n"
MF"v[188:191],a[88:91],v[8:11],v[188:191]\n"
EX"v49,v49\n"
PK"v70,v44,v45\n"
MF"v[160:163],a[96:99],v[0:3],v[160:163]\n"
WT"lgkmcnt(2)\n"
RD"a[116:119],v227 offset:30720\n"
RD"a[120:123],v227 offset:23552\n"
MU"v117,v252,v117\n"
MF"v[160:163],a[100:103],v[16:19],v[160:163]\n"
EX"v50,v50\n"
MU"v118,v252,v118\n"
MF"v[192:195],a[100:103],v[24:27],v[192:195]\n"
RD"a[124:127],v227 offset:31744\n"
MU"v119,v252,v119\n"
PK"v112,v112,v113\n"
MF"v[192:195],a[96:99],v[8:11],v[192:195]\n"
BL"v225,s[8:11],s44 offen offset:2048 sc1 lds\n"
PK"v113,v114,v115\n"
MF"v[164:167],a[104:107],v[0:3],v[164:167]\n"
WT"lgkmcnt(3)\n"
EX"v51,v51\n"
PK"v114,v116,v117\n"
MF"v[164:167],a[108:111],v[16:19],v[164:167]\n"
EX"v52,v52\n"
PK"v115,v118,v119\n"
MF"v[196:199],a[108:111],v[24:27],v[196:199]\n"
BS"v[112:115],v224,s[8:11],s51 offen offset:0 sc1\n"
PK"v71,v46,v47\n"
MF"v[196:199],a[104:107],v[8:11],v[196:199]\n"
"s_barrier\n"
MF"v[168:171],a[112:115],v[0:3],v[168:171]\n"
WT"lgkmcnt(1)\n"
EX"v53,v53\n"
MU"v120,v252,v120\n"
MF"v[168:171],a[116:119],v[16:19],v[168:171]\n"
RD"a[64:67],v226 offset:0\n"
RD"a[68:71],v226 offset:1024\n"
MU"v121,v252,v121\n"
MF"v[200:203],a[116:119],v[24:27],v[200:203]\n"
EX"v54,v54\n"
MU"v122,v252,v122\n"
MF"v[200:203],a[112:115],v[8:11],v[200:203]\n"
BL"v225,s[8:11],s44 offen offset:3072 sc1 lds\n"
MU"v123,v252,v123\n"
MF"v[172:175],a[120:123],v[0:3],v[172:175]\n"
WT"lgkmcnt(2)\n"
EX"v55,v55\n"
MU"v124,v252,v124\n"
MF"v[172:175],a[124:127],v[16:19],v[172:175]\n"
RD"a[72:75],v226 offset:2048\n"
PK"v72,v48,v49\n"
MU"v125,v252,v125\n"
MF"v[204:207],a[124:127],v[24:27],v[204:207]\n"
RD"a[76:79],v226 offset:3072\n"
MU"v126,v252,v126\n"
MU"v127,v252,v127\n"
MF"v[204:207],a[120:123],v[8:11],v[204:207]\n"
PK"v120,v120,v121\n"
PK"v121,v122,v123\n"
PK"v122,v124,v125\n"
MF"a[140:143],a[128:131],v[0:3],a[140:143]\n"
MU"v128,v252,v128\n"
MU"v129,v252,v129\n"
MU"v136,v252,v136\n"
MF"a[140:143],a[128:131],v[16:19],a[140:143]\n"
MU"v137,v252,v137\n"
MU"v130,v252,v130\n"
MU"v131,v252,v131\n"
MF"a[144:147],a[128:131],v[24:27],a[144:147]\n"
PK"v128,v128,v129\n"
MU"v138,v252,v138\n"
MU"v139,v252,v139\n"
MF"a[144:147],a[128:131],v[8:11],a[144:147]\n"
PK"v136,v136,v137\n"
MU"v132,v252,v132\n"
MU"v133,v252,v133\n"
PK"v73,v50,v51\n"
PK"v74,v52,v53\n"
PK"v75,v54,v55\n"
PK"v123,v126,v127\n"
BS"v[120:123],v224,s[8:11],s51 offen offset:1024 sc1\n"
MU"v134,v252,v134\n"
MU"v135,v252,v135\n"
PK"v129,v130,v131\n"
PK"v130,v132,v133\n"
PK"v131,v134,v135\n"
BS"v[128:131],v224,s[8:11],s51 offen offset:2048 sc1\n"
MU"v140,v252,v140\n"
MU"v141,v252,v141\n"
MU"v142,v252,v142\n"
MU"v143,v252,v143\n"
PK"v137,v138,v139\n"
PK"v138,v140,v141\n"
PK"v139,v142,v143\n"
BS"v[136:139],v224,s[8:11],s51 offen offset:3072 sc1\n"
RD"a[80:83],v226 offset:4096\n"
RD"a[84:87],v226 offset:5120\n"
RD"a[88:91],v226 offset:6144\n"
".p2align 6\n"
"LP2_%=:\n"
WT"lgkmcnt(6)\n"
MF"v[0:3],a[64:67],a[32:35],0\n"
WT"lgkmcnt(5)\n"
EX"v56,v56\n"
MF"v[0:3],a[68:71],a[36:39],v[0:3]\n"
RD"a[92:95],v226 offset:7168\n"
"v_readlane_b32 s43,v228,s42\n"
MF"v[8:11],a[68:71],a[52:55],0\n"
EX"v57,v57\n"
MF"v[8:11],a[64:67],a[48:51],v[8:11]\n"
WT"lgkmcnt(5)\n"
RD"a[96:99],v226 offset:8192\n"
RD"a[100:103],v226 offset:9216\n"
MF"v[0:3],a[72:75],a[40:43],v[0:3]\n"
WT"lgkmcnt(3)\n"
EX"v58,v58\n"
MF"v[0:3],a[76:79],a[44:47],v[0:3]\n"
"s_mov_b32 m0,s40\n"
RD"a[104:107],v226 offset:10240\n"
"v_readlane_b32 s44,v229,s42\n"
MF"v[8:11],a[76:79],a[60:63],v[8:11]\n"
SA"s41,s41,32768\n"
EX"v59,v59\n"
MF"v[8:11],a[72:75],a[56:59],v[8:11]\n"
"s_cmp_eq_u32 s41,163840\n"
BL"v225,s[8:11],s43 offen offset:0 sc1 lds\n"
MF"v[4:7],a[80:83],a[32:35],0\n"
"s_cselect_b32 s41,0,s41\n"
EX"v60,v60\n"
MF"v[4:7],a[84:87],a[36:39],v[4:7]\n"
SA"s42,s42,1\n"
RD"a[108:111],v226 offset:11264\n"
"v_add_u32 v227,s41,v224\n"
MF"v[12:15],a[84:87],a[52:55],0\n"
EX"v61,v61\n"
MF"v[12:15],a[80:83],a[48:51],v[12:15]\n"
RD"a[112:115],v226 offset:12288\n"
RD"a[116:119],v226 offset:13312\n"
MF"v[4:7],a[88:91],a[40:43],v[4:7]\n"
WT"lgkmcnt(3)\n"
EX"v62,v62\n"
MF"v[4:7],a[92:95],a[44:47],v[4:7]\n"
RD"a[120:123],v226 offset:14336\n"
PK"v76,v56,v57\n"
MF"v[12:15],a[92:95],a[60:63],v[12:15]\n"
EX"v63,v63\n"
MF"v[12:15],a[88:91],a[56:59],v[12:15]\n"
BL"v225,s[8:11],s43 offen offset:1024 sc1 lds\n"
MF"v[16:19],a[96:99],a[32:35],0\n"
EX"v0,v0\n"
MF"v[16:19],a[100:103],a[36:39],v[16:19]\n"
RD"a[124:127],v226 offset:15360\n"
PK"v77,v58,v59\n"
MF"v[24:27],a[100:103],a[52:55],0\n"
EX"v1,v1\n"
MF"v[24:27],a[96:99],a[48:51],v[24:27]\n"
RD"a[64:67],v226 offset:16384\n"
RD"a[68:71],v226 offset:17408\n"
MF"v[16:19],a[104:107],a[40:43],v[16:19]\n"
WT"lgkmcnt(3)\n"
EX"v2,v2\n"
MF"v[16:19],a[108:111],a[44:47],v[16:19]\n"
RD"a[72:75],v226 offset:18432\n"
PK"v78,v60,v61\n"
MF"v[24:27],a[108:111],a[60:63],v[24:27]\n"
EX"v3,v3\n"
MF"v[24:27],a[104:107],a[56:59],v[24:27]\n"
BL"v225,s[8:11],s43 offen offset:2048 sc1 lds\n"
MF"v[20:23],a[112:115],a[32:35],0\n"
EX"v8,v8\n"
MF"v[20:23],a[116:119],a[36:39],v[20:23]\n"
RD"a[76:79],v226 offset:19456\n"
PK"v79,v62,v63\n"
MF"v[28:31],a[116:119],a[52:55],0\n"
EX"v9,v9\n"
MF"v[28:31],a[112:115],a[48:51],v[28:31]\n"
RD"a[80:83],v226 offset:20480\n"
RD"a[84:87],v226 offset:21504\n"
MF"v[20:23],a[120:123],a[40:43],v[20:23]\n"
WT"lgkmcnt(3)\n"
EX"v10,v10\n"
MF"v[20:23],a[124:127],a[44:47],v[20:23]\n"
RD"a[88:91],v226 offset:22528\n"
PK"v0,v0,v1\n"
MF"v[28:31],a[124:127],a[60:63],v[28:31]\n"
EX"v11,v11\n"
MF"v[28:31],a[120:123],a[56:59],v[28:31]\n"
BL"v225,s[8:11],s43 offen offset:3072 sc1 lds\n"
MF"v[32:35],a[64:67],a[32:35],0\n"
EX"v4,v4\n"
MF"v[32:35],a[68:71],a[36:39],v[32:35]\n"
RD"a[92:95],v226 offset:23552\n"
PK"v1,v2,v3\n"
MF"v[40:43],a[68:71],a[52:55],0\n"
EX"v5,v5\n"
MF"v[40:43],a[64:67],a[48:51],v[40:43]\n"
SA"m0,s40,16384\n"
RD"a[96:99],v226 offset:24576\n"
RD"a[100:103],v226 offset:25600\n"
MF"v[32:35],a[72:75],a[40:43],v[32:35]\n"
WT"lgkmcnt(3)\n"
EX"v6,v6\n"
MF"v[32:35],a[76:79],a[44:47],v[32:35]\n"
RD"a[104:107],v226 offset:26624\n"
PK"v8,v8,v9\n"
MF"v[40:43],a[76:79],a[60:63],v[40:43]\n"
EX"v7,v7\n"
MF"v[40:43],a[72:75],a[56:59],v[40:43]\n"
BL"v225,s[8:11],s44 offen offset:0 sc1 lds\n"
MF"v[36:39],a[80:83],a[32:35],0\n"
SA"s40,s40,32768\n"
EX"v12,v12\n"
MF"v[36:39],a[84:87],a[36:39],v[36:39]\n"
"s_cmp_eq_u32 s40,s46\n"
RD"a[108:111],v226 offset:27648\n"
PK"v9,v10,v11\n"
MF"v[44:47],a[84:87],a[52:55],0\n"
"s_cselect_b32 s40,s47,s40\n"
EX"v13,v13\n"
MF"v[44:47],a[80:83],a[48:51],v[44:47]\n"
RD"a[112:115],v226 offset:28672\n"
RD"a[116:119],v226 offset:29696\n"
MF"v[36:39],a[88:91],a[40:43],v[36:39]\n"
WT"lgkmcnt(3)\n"
EX"v14,v14\n"
MF"v[36:39],a[92:95],a[44:47],v[36:39]\n"
RD"a[120:123],v226 offset:30720\n"
PK"v2,v4,v5\n"
MF"v[44:47],a[92:95],a[60:63],v[44:47]\n"
EX"v15,v15\n"
MF"v[44:47],a[88:91],a[56:59],v[44:47]\n"
BL"v225,s[8:11],s44 offen offset:1024 sc1 lds\n"
MF"v[48:51],a[96:99],a[32:35],0\n"
EX"v16,v16\n"
MF"v[48:51],a[100:103],a[36:39],v[48:51]\n"
RD"a[124:127],v226 offset:31744\n"
PK"v3,v6,v7\n"
MF"v[56:59],a[100:103],a[52:55],0\n"
WT"vmcnt(14)\n"
"s_barrier\n"
MF"v[56:59],a[96:99],a[48:51],v[56:59]\n"
RD"a[64:67],v227 offset:0\n"
RD"a[68:71],v227 offset:8192\n"
MF"v[48:51],a[104:107],a[40:43],v[48:51]\n"
WT"lgkmcnt(3)\n"
EX"v17,v17\n"
MF"v[48:51],a[108:111],a[44:47],v[48:51]\n"
RD"a[72:75],v227 offset:1024\n"
PK"v10,v12,v13\n"
MF"v[56:59],a[108:111],a[60:63],v[56:59]\n"
EX"v18,v18\n"
MF"v[56:59],a[104:107],a[56:59],v[56:59]\n"
BL"v225,s[8:11],s44 offen offset:2048 sc1 lds\n"
MF"v[52:55],a[112:115],a[32:35],0\n"
EX"v19,v19\n"
MF"v[52:55],a[116:119],a[36:39],v[52:55]\n"
RD"a[76:79],v227 offset:9216\n"
PK"v11,v14,v15\n"
MF"v[60:63],a[116:119],a[52:55],0\n"
EX"v24,v24\n"
MF"v[60:63],a[112:115],a[48:51],v[60:63]\n"
RD"a[80:83],v227 offset:2048\n"
RD"a[84:87],v227 offset:10240\n"
MF"v[52:55],a[120:123],a[40:43],v[52:55]\n"
WT"lgkmcnt(3)\n"
EX"v25,v25\n"
MF"v[52:55],a[124:127],a[44:47],v[52:55]\n"
RD"a[88:91],v227 offset:3072\n"
PK"v16,v16,v17\n"
MF"v[60:63],a[124:127],a[60:63],v[60:63]\n"
EX"v26,v26\n"
MF"v[60:63],a[120:123],a[56:59],v[60:63]\n"
SA"s41,s41,32768\n"
BL"v225,s[8:11],s44 offen offset:3072 sc1 lds\n"
MF"v[144:147],a[64:67],v[64:67],v[144:147]\n"
"s_cmp_eq_u32 s41,163840\n"
EX"v27,v27\n"
MF"v[144:147],a[68:71],v[72:75],v[144:147]\n"
"s_cselect_b32 s41,0,s41\n"
RD"a[92:95],v227 offset:11264\n"
"v_add_u32 v226,s41,v224\n"
MF"v[176:179],a[68:71],v[76:79],v[176:179]\n"
EX"v20,v20\n"
MF"v[176:179],a[64:67],v[68:71],v[176:179]\n"
"s_mov_b32 m0,s40\n"
RD"a[96:99],v227 offset:4096\n"
RD"a[100:103],v227 offset:12288\n"
MF"v[148:151],a[72:75],v[64:67],v[148:151]\n"
WT"lgkmcnt(3)\n"
EX"v21,v21\n"
MF"v[148:151],a[76:79],v[72:75],v[148:151]\n"
RD"a[104:107],v227 offset:5120\n"
"v_readlane_b32 s43,v228,s42\n"
MF"v[180:183],a[76:79],v[76:79],v[180:183]\n"
EX"v22,v22\n"
MF"v[180:183],a[72:75],v[68:71],v[180:183]\n"
"v_readlane_b32 s44,v229,s42\n"
PK"v17,v18,v19\n"
MF"v[152:155],a[80:83],v[64:67],v[152:155]\n"
SA"s42,s42,1\n"
BL"v225,s[8:11],s43 offen offset:0 sc1 lds\n"
MF"v[152:155],a[84:87],v[72:75],v[152:155]\n"
EX"v23,v23\n"
MF"v[184:187],a[84:87],v[76:79],v[184:187]\n"
RD"a[108:111],v227 offset:13312\n"
PK"v24,v24,v25\n"
MF"v[184:187],a[80:83],v[68:71],v[184:187]\n"
EX"v28,v28\n"
MF"v[156:159],a[88:91],v[64:67],v[156:159]\n"
WT"lgkmcnt(1)\n"
RD"a[112:115],v227 offset:6144\n"
RD"a[116:119],v227 offset:14336\n"
MF"v[156:159],a[92:95],v[72:75],v[156:159]\n"
EX"v29,v29\n"
MF"v[188:191],a[92:95],v[76:79],v[188:191]\n"
RD"a[120:123],v227 offset:7168\n"
PK"v25,v26,v27\n"
MF"v[188:191],a[88:91],v[68:71],v[188:191]\n"
EX"v30,v30\n"
MF"v[160:163],a[96:99],v[64:67],v[160:163]\n"
BL"v225,s[8:11],s43 offen offset:1024 sc1 lds\n"
MF"v[160:163],a[100:103],v[72:75],v[160:163]\n"
EX"v31,v31\n"
MF"v[192:195],a[100:103],v[76:79],v[192:195]\n"
RD"a[124:127],v227 offset:15360\n"
PK"v18,v20,v21\n"
MF"v[192:195],a[96:99],v[68:71],v[192:195]\n"
EX"v32,v32\n"
MF"v[164:167],a[104:107],v[64:67],v[164:167]\n"
WT"lgkmcnt(2)\n"
EX"v33,v33\n"
MF"v[164:167],a[108:111],v[72:75],v[164:167]\n"
RD"a[64:67],v227 offset:16384\n"
PK"v19,v22,v23\n"
MF"v[196:199],a[108:111],v[76:79],v[196:199]\n"
EX"v34,v34\n"
MF"v[196:199],a[104:107],v[68:71],v[196:199]\n"
BL"v225,s[8:11],s43 offen offset:2048 sc1 lds\n"
MF"v[168:171],a[112:115],v[64:67],v[168:171]\n"
EX"v35,v35\n"
MF"v[168:171],a[116:119],v[72:75],v[168:171]\n"
RD"a[68:71],v227 offset:24576\n"
PK"v26,v28,v29\n"
MF"v[200:203],a[116:119],v[76:79],v[200:203]\n"
EX"v40,v40\n"
MF"v[200:203],a[112:115],v[68:71],v[200:203]\n"
WT"lgkmcnt(2)\n"
RD"a[72:75],v227 offset:17408\n"
RD"a[76:79],v227 offset:25600\n"
MF"v[172:175],a[120:123],v[64:67],v[172:175]\n"
EX"v41,v41\n"
MF"v[172:175],a[124:127],v[72:75],v[172:175]\n"
RD"a[80:83],v227 offset:18432\n"
PK"v27,v30,v31\n"
MF"v[204:207],a[124:127],v[76:79],v[204:207]\n"
EX"v42,v42\n"
MF"v[204:207],a[120:123],v[68:71],v[204:207]\n"
BL"v225,s[8:11],s43 offen offset:3072 sc1 lds\n"
MF"a[140:143],a[128:131],v[64:67],a[140:143]\n"
EX"v43,v43\n"
MF"a[140:143],a[128:131],v[72:75],a[140:143]\n"
RD"a[84:87],v227 offset:26624\n"
PK"v64,v32,v33\n"
MF"a[144:147],a[128:131],v[76:79],a[144:147]\n"
EX"v36,v36\n"
MF"a[144:147],a[128:131],v[68:71],a[144:147]\n"
WT"lgkmcnt(2)\n"
RD"a[88:91],v227 offset:19456\n"
RD"a[92:95],v227 offset:27648\n"
MF"v[144:147],a[64:67],v[0:3],v[144:147]\n"
SA"m0,s40,16384\n"
EX"v37,v37\n"
MF"v[144:147],a[68:71],v[16:19],v[144:147]\n"
"buffer_load_dwordx2 v[242:243],v236,s[8:11],0 offen sc1\n"
MF"v[176:179],a[68:71],v[24:27],v[176:179]\n"
RD"a[96:99],v227 offset:20480\n"
PK"v65,v34,v35\n"
MF"v[176:179],a[64:67],v[8:11],v[176:179]\n"
EX"v38,v38\n"
MF"v[148:151],a[72:75],v[0:3],v[148:151]\n"
SA"s40,s40,32768\n"
BL"v225,s[8:11],s44 offen offset:0 sc1 lds\n"
MF"v[148:151],a[76:79],v[16:19],v[148:151]\n"
"s_cmp_eq_u32 s40,s46\n"
EX"v39,v39\n"
MF"v[180:183],a[76:79],v[24:27],v[180:183]\n"
"s_cselect_b32 s40,s47,s40\n"
RD"a[100:103],v227 offset:28672\n"
PK"v68,v40,v41\n"
MF"v[180:183],a[72:75],v[8:11],v[180:183]\n"
WT"lgkmcnt(2)\n"
EX"v44,v44\n"
MF"v[152:155],a[80:83],v[0:3],v[152:155]\n"
RD"a[104:107],v227 offset:21504\n"
RD"a[108:111],v227 offset:29696\n"
MF"v[152:155],a[84:87],v[16:19],v[152:155]\n"
EX"v45,v45\n"
MF"v[184:187],a[84:87],v[24:27],v[184:187]\n"
RD"a[112:115],v227 offset:22528\n"
PK"v69,v42,v43\n"
MF"v[184:187],a[80:83],v[8:11],v[184:187]\n"
EX"v46,v46\n"
MF"v[156:159],a[88:91],v[0:3],v[156:159]\n"
BL"v225,s[8:11],s44 offen offset:1024 sc1 lds\n"
MF"v[156:159],a[92:95],v[16:19],v[156:159]\n"
EX"v47,v47\n"
MF"v[188:191],a[92:95],v[24:27],v[188:191]\n"
RD"a[116:119],v227 offset:30720\n"
PK"v66,v36,v37\n"
MF"v[188:191],a[88:91],v[8:11],v[188:191]\n"
WT"lgkmcnt(2)\n"
EX"v48,v48\n"
MF"v[160:163],a[96:99],v[0:3],v[160:163]\n"
RD"a[120:123],v227 offset:23552\n"
RD"a[124:127],v227 offset:31744\n"
MF"v[160:163],a[100:103],v[16:19],v[160:163]\n"
EX"v49,v49\n"
MF"v[192:195],a[100:103],v[24:27],v[192:195]\n"
EX"v50,v50\n"
MF"v[192:195],a[96:99],v[8:11],v[192:195]\n"
BL"v225,s[8:11],s44 offen offset:2048 sc1 lds\n"
MF"v[164:167],a[104:107],v[0:3],v[164:167]\n"
WT"vmcnt(16)\n"
"s_barrier\n"
MF"v[164:167],a[108:111],v[16:19],v[164:167]\n"
RD"a[64:67],v226 offset:0\n"
PK"v67,v38,v39\n"
MF"v[196:199],a[108:111],v[24:27],v[196:199]\n"
EX"v51,v51\n"
MF"v[196:199],a[104:107],v[8:11],v[196:199]\n"
WT"lgkmcnt(1)\n"
RD"a[68:71],v226 offset:1024\n"
PK"v70,v44,v45\n"
MF"v[168:171],a[112:115],v[0:3],v[168:171]\n"
EX"v52,v52\n"
MF"v[168:171],a[116:119],v[16:19],v[168:171]\n"
RD"a[72:75],v226 offset:2048\n"
PK"v71,v46,v47\n"
MF"v[200:203],a[116:119],v[24:27],v[200:203]\n"
EX"v53,v53\n"
MF"v[200:203],a[112:115],v[8:11],v[200:203]\n"
BL"v225,s[8:11],s44 offen offset:3072 sc1 lds\n"
MF"v[172:175],a[120:123],v[0:3],v[172:175]\n"
EX"v54,v54\n"
MF"v[172:175],a[124:127],v[16:19],v[172:175]\n"
"buffer_store_dwordx2 v[232:233],v230,s[8:11],s57 offen sc1\n"
MF"v[204:207],a[124:127],v[24:27],v[204:207]\n"
"s_mov_b32 s57,2130706432\n"
RD"a[76:79],v226 offset:3072\n"
PK"v72,v48,v49\n"
MF"v[204:207],a[120:123],v[8:11],v[204:207]\n"
EX"v55,v55\n"
MF"a[140:143],a[128:131],v[0:3],a[140:143]\n"
RD"a[80:83],v226 offset:4096\n"
RD"a[84:87],v226 offset:5120\n"
MF"a[140:143],a[128:131],v[16:19],a[140:143]\n"
RD"a[88:91],v226 offset:6144\n"
PK"v73,v50,v51\n"
MF"a[144:147],a[128:131],v[24:27],a[144:147]\n"
PK"v74,v52,v53\n"
PK"v75,v54,v55\n"
MF"a[144:147],a[128:131],v[8:11],a[144:147]\n"
"s_sub_u32 s56,s56,1\n"
"s_cbranch_scc0 LP2_%=\n"
"s_cmp_lt_i32 s22,0\n"
"s_cbranch_scc1 LE2_%=\n"
WT"lgkmcnt(6)\n"
MF"v[0:3],a[64:67],a[32:35],0\n"
WT"lgkmcnt(5)\n"
WT"vmcnt(5)\n"
"v_cmp_eq_u64 vcc,s[28:29],v[242:243]\n"
"s_or_b32 vcc_hi,vcc_hi,-1\n"
"s_cmp_eq_u64 vcc,exec\n"
"s_cbranch_scc0 L1028_%=\n"
"L1029_%=:\n"
MF"v[0:3],a[68:71],a[36:39],v[0:3]\n"
SA"s48,s59,32768\n"
EX"v56,v56\n"
MF"v[8:11],a[68:71],a[52:55],0\n"
"s_and_b32 s48,s48,0x18000\n"
RD"a[92:95],v226 offset:7168\n"
"v_readlane_b32 s43,v228,s42\n"
MF"v[8:11],a[64:67],a[48:51],v[8:11]\n"
WT"lgkmcnt(5)\n"
EX"v57,v57\n"
MF"v[0:3],a[72:75],a[40:43],v[0:3]\n"
WT"lgkmcnt(1)\n"
RD"a[96:99],v226 offset:8192\n"
RD"a[100:103],v226 offset:9216\n"
MF"v[0:3],a[76:79],a[44:47],v[0:3]\n"
"s_andn2_b32 s49,s59,0x18000\n"
EX"v58,v58\n"
MF"v[8:11],a[76:79],a[60:63],v[8:11]\n"
"s_or_b32 s52,s48,s49\n"
RD"a[104:107],v226 offset:10240\n"
"v_readlane_b32 s44,v229,s42\n"
MF"v[8:11],a[72:75],a[56:59],v[8:11]\n"
SA"s53,s52,16384\n"
BL"a[0:3],v224,s[8:11],s52 offen offset:0 sc1\n"
MF"v[4:7],a[80:83],a[32:35],0\n"
"s_mov_b32 m0,s40\n"
EX"v59,v59\n"
MF"v[4:7],a[84:87],a[36:39],v[4:7]\n"
SA"s41,s41,32768\n"
BL"v225,s[8:11],s43 offen offset:0 sc1 lds\n"
MF"v[12:15],a[84:87],a[52:55],0\n"
"s_cmp_eq_u32 s41,163840\n"
EX"v60,v60\n"
MF"v[12:15],a[80:83],a[48:51],v[12:15]\n"
"s_cselect_b32 s41,0,s41\n"
RD"a[108:111],v226 offset:11264\n"
RD"a[112:115],v226 offset:12288\n"
PK"v76,v56,v57\n"
MF"v[4:7],a[88:91],a[40:43],v[4:7]\n"
WT"lgkmcnt(3)\n"
BL"a[4:7],v224,s[8:11],s53 offen offset:0 sc1\n"
EX"v61,v61\n"
MF"v[4:7],a[92:95],a[44:47],v[4:7]\n"
SA"s42,s42,1\n"
RD"a[116:119],v226 offset:13312\n"
RD"a[120:123],v226 offset:14336\n"
"v_add_u32 v227,s41,v224\n"
MF"v[12:15],a[92:95],a[60:63],v[12:15]\n"
EX"v62,v62\n"
MF"v[12:15],a[88:91],a[56:59],v[12:15]\n"
EX"v63,v63\n"
PK"v77,v58,v59\n"
MF"v[16:19],a[96:99],a[32:35],0\n"
BL"a[8:11],v224,s[8:11],s52 offen offset:1024 sc1\n"
MF"v[16:19],a[100:103],a[36:39],v[16:19]\n"
BL"v225,s[8:11],s43 offen offset:1024 sc1 lds\n"
MF"v[24:27],a[100:103],a[52:55],0\n"
EX"v0,v0\n"
MF"v[24:27],a[96:99],a[48:51],v[24:27]\n"
WT"lgkmcnt(2)\n"
RD"a[124:127],v226 offset:15360\n"
RD"a[64:67],v226 offset:16384\n"
PK"v78,v60,v61\n"
MF"v[16:19],a[104:107],a[40:43],v[16:19]\n"
EX"v1,v1\n"
MF"v[16:19],a[108:111],a[44:47],v[16:19]\n"
RD"a[68:71],v226 offset:17408\n"
RD"a[72:75],v226 offset:18432\n"
MF"v[24:27],a[108:111],a[60:63],v[24:27]\n"
BL"a[12:15],v224,s[8:11],s53 offen offset:1024 sc1\n"
EX"v2,v2\n"
MF"v[24:27],a[104:107],a[56:59],v[24:27]\n"
EX"v3,v3\n"
PK"v79,v62,v63\n"
MF"v[20:23],a[112:115],a[32:35],0\n"
WT"lgkmcnt(4)\n"
BL"v225,s[8:11],s43 offen offset:2048 sc1 lds\n"
MF"v[20:23],a[116:119],a[36:39],v[20:23]\n"
EX"v8,v8\n"
MF"v[28:31],a[116:119],a[52:55],0\n"
RD"a[76:79],v226 offset:19456\n"
PK"v0,v0,v1\n"
MF"v[28:31],a[112:115],a[48:51],v[28:31]\n"
BL"a[16:19],v224,s[8:11],s52 offen offset:2048 sc1\n"
MF"v[20:23],a[120:123],a[40:43],v[20:23]\n"
WT"lgkmcnt(1)\n"
EX"v9,v9\n"
MF"v[20:23],a[124:127],a[44:47],v[20:23]\n"
RD"a[80:83],v226 offset:20480\n"
RD"a[84:87],v226 offset:21504\n"
MF"v[28:31],a[124:127],a[60:63],v[28:31]\n"
EX"v10,v10\n"
MF"v[28:31],a[120:123],a[56:59],v[28:31]\n"
RD"a[88:91],v226 offset:22528\n"
PK"v1,v2,v3\n"
MF"v[32:35],a[64:67],a[32:35],0\n"
EX"v11,v11\n"
MF"v[32:35],a[68:71],a[36:39],v[32:35]\n"
BL"a[20:23],v224,s[8:11],s53 offen offset:2048 sc1\n"
BL"v225,s[8:11],s43 offen offset:3072 sc1 lds\n"
MF"v[40:43],a[68:71],a[52:55],0\n"
EX"v4,v4\n"
MF"v[40:43],a[64:67],a[48:51],v[40:43]\n"
SA"m0,s40,16384\n"
RD"a[92:95],v226 offset:23552\n"
RD"a[96:99],v226 offset:24576\n"
PK"v8,v8,v9\n"
MF"v[32:35],a[72:75],a[40:43],v[32:35]\n"
WT"lgkmcnt(3)\n"
EX"v5,v5\n"
MF"v[32:35],a[76:79],a[44:47],v[32:35]\n"
RD"a[100:103],v226 offset:25600\n"
RD"a[104:107],v226 offset:26624\n"
MF"v[40:43],a[76:79],a[60:63],v[40:43]\n"
BL"a[24:27],v224,s[8:11],s52 offen offset:3072 sc1\n"
EX"v6,v6\n"
MF"v[40:43],a[72:75],a[56:59],v[40:43]\n"
EX"v7,v7\n"
PK"v9,v10,v11\n"
MF"v[36:39],a[80:83],a[32:35],0\n"
SA"s40,s40,32768\n"
BL"v225,s[8:11],s44 offen offset:0 sc1 lds\n"
MF"v[36:39],a[84:87],a[36:39],v[36:39]\n"
"s_cmp_eq_u32 s40,s46\n"
EX"v12,v12\n"
MF"v[44:47],a[84:87],a[52:55],0\n"
"s_cselect_b32 s40,s47,s40\n"
BL"a[28:31],v224,s[8:11],s53 offen offset:3072 sc1\n"
MF"v[44:47],a[80:83],a[48:51],v[44:47]\n"
WT"lgkmcnt(2)\n"
RD"a[108:111],v226 offset:27648\n"
RD"a[112:115],v226 offset:28672\n"
PK"v2,v4,v5\n"
MF"v[36:39],a[88:91],a[40:43],v[36:39]\n"
SA"s48,s59,65536\n"
EX"v13,v13\n"
MF"v[36:39],a[92:95],a[44:47],v[36:39]\n"
"s_and_b32 s48,s48,0x18000\n"
RD"a[116:119],v226 offset:29696\n"
RD"a[120:123],v226 offset:30720\n"
MF"v[44:47],a[92:95],a[60:63],v[44:47]\n"
"s_andn2_b32 s49,s59,0x18000\n"
EX"v14,v14\n"
MF"v[44:47],a[88:91],a[56:59],v[44:47]\n"
"s_or_b32 s52,s48,s49\n"
EX"v15,v15\n"
PK"v3,v6,v7\n"
MF"v[48:51],a[96:99],a[32:35],0\n"
WT"lgkmcnt(4)\n"
BL"a[180:183],v224,s[8:11],s52 offen offset:0 sc1\n"
MF"v[48:51],a[100:103],a[36:39],v[48:51]\n"
SA"s53,s52,16384\n"
BL"v225,s[8:11],s44 offen offset:1024 sc1 lds\n"
MF"v[56:59],a[100:103],a[52:55],0\n"
EX"v16,v16\n"
MF"v[56:59],a[96:99],a[48:51],v[56:59]\n"
RD"a[124:127],v226 offset:31744\n"
PK"v10,v12,v13\n"
MF"v[48:51],a[104:107],a[40:43],v[48:51]\n"
WT"lgkmcnt(1)\n"
"s_barrier\n"
MF"v[48:51],a[108:111],a[44:47],v[48:51]\n"
RD"a[64:67],v227 offset:0\n"
RD"a[68:71],v227 offset:8192\n"
MF"v[56:59],a[108:111],a[60:63],v[56:59]\n"
BL"a[184:187],v224,s[8:11],s53 offen offset:0 sc1\n"
EX"v17,v17\n"
MF"v[56:59],a[104:107],a[56:59],v[56:59]\n"
RD"a[72:75],v227 offset:1024\n"
PK"v11,v14,v15\n"
MF"v[52:55],a[112:115],a[32:35],0\n"
EX"v18,v18\n"
MF"v[52:55],a[116:119],a[36:39],v[52:55]\n"
BL"v225,s[8:11],s44 offen offset:2048 sc1 lds\n"
MF"v[60:63],a[116:119],a[52:55],0\n"
EX"v19,v19\n"
MF"v[60:63],a[112:115],a[48:51],v[60:63]\n"
BL"a[188:191],v224,s[8:11],s52 offen offset:1024 sc1\n"
RD"a[76:79],v227 offset:9216\n"
MF"v[52:55],a[120:123],a[40:43],v[52:55]\n"
WT"lgkmcnt(2)\n"
EX"v24,v24\n"
MF"v[52:55],a[124:127],a[44:47],v[52:55]\n"
RD"a[80:83],v227 offset:2048\n"
RD"a[84:87],v227 offset:10240\n"
PK"v16,v16,v17\n"
MF"v[60:63],a[124:127],a[60:63],v[60:63]\n"
EX"v25,v25\n"
MF"v[60:63],a[120:123],a[56:59],v[60:63]\n"
SA"s41,s41,32768\n"
RD"a[88:91],v227 offset:3072\n"
"v_readlane_b32 s43,v228,s42\n"
MF"v[144:147],a[64:67],v[64:67],v[144:147]\n"
"s_cmp_eq_u32 s41,163840\n"
BL"a[192:195],v224,s[8:11],s53 offen offset:1024 sc1\n"
EX"v26,v26\n"
MF"v[144:147],a[68:71],v[72:75],v[144:147]\n"
"s_cselect_b32 s41,0,s41\n"
BL"v225,s[8:11],s44 offen offset:3072 sc1 lds\n"
PK"v17,v18,v19\n"
MF"v[176:179],a[68:71],v[76:79],v[176:179]\n"
EX"v27,v27\n"
MF"v[176:179],a[64:67],v[68:71],v[176:179]\n"
WT"lgkmcnt(3)\n"
RD"a[92:95],v227 offset:11264\n"
RD"a[96:99],v227 offset:4096\n"
MF"v[148:151],a[72:75],v[64:67],v[148:151]\n"
"s_mov_b32 m0,s40\n"
EX"v20,v20\n"
MF"v[148:151],a[76:79],v[72:75],v[148:151]\n"
RD"a[100:103],v227 offset:12288\n"
RD"a[104:107],v227 offset:5120\n"
PK"v24,v24,v25\n"
MF"v[180:183],a[76:79],v[76:79],v[180:183]\n"
BL"a[196:199],v224,s[8:11],s52 offen offset:2048 sc1\n"
EX"v21,v21\n"
MF"v[180:183],a[72:75],v[68:71],v[180:183]\n"
WT"lgkmcnt(4)\n"
EX"v22,v22\n"
"v_add_u32 v226,s41,v224\n"
MF"v[152:155],a[80:83],v[64:67],v[152:155]\n"
BL"v225,s[8:11],s43 offen offset:0 sc1 lds\n"
MF"v[152:155],a[84:87],v[72:75],v[152:155]\n"
EX"v23,v23\n"
PK"v25,v26,v27\n"
MF"v[184:187],a[84:87],v[76:79],v[184:187]\n"
BL"a[200:203],v224,s[8:11],s53 offen offset:2048 sc1\n"
MF"v[184:187],a[80:83],v[68:71],v[184:187]\n"
RD"a[108:111],v227 offset:13312\n"
RD"a[112:115],v227 offset:6144\n"
MF"v[156:159],a[88:91],v[64:67],v[156:159]\n"
WT"lgkmcnt(2)\n"
EX"v28,v28\n"
MF"v[156:159],a[92:95],v[72:75],v[156:159]\n"
RD"a[116:119],v227 offset:14336\n"
RD"a[120:123],v227 offset:7168\n"
"v_readlane_b32 s44,v229,s42\n"
MF"v[188:191],a[92:95],v[76:79],v[188:191]\n"
SA"s42,s42,1\n"
EX"v29,v29\n"
PK"v18,v20,v21\n"
MF"v[188:191],a[88:91],v[68:71],v[188:191]\n"
BL"a[204:207],v224,s[8:11],s52 offen offset:3072 sc1\n"
MF"v[160:163],a[96:99],v[64:67],v[160:163]\n"
EX"v30,v30\n"
MF"v[160:163],a[100:103],v[72:75],v[160:163]\n"
BL"v225,s[8:11],s43 offen offset:1024 sc1 lds\n"
PK"v19,v22,v23\n"
MF"v[192:195],a[100:103],v[76:79],v[192:195]\n"
EX"v31,v31\n"
MF"v[192:195],a[96:99],v[68:71],v[192:195]\n"
RD"a[124:127],v227 offset:15360\n"
PK"v26,v28,v29\n"
MF"v[164:167],a[104:107],v[64:67],v[164:167]\n"
WT"lgkmcnt(1)\n"
EX"v32,v32\n"
MF"v[164:167],a[108:111],v[72:75],v[164:167]\n"
SA"s48,s59,98304\n"
BL"a[208:211],v224,s[8:11],s53 offen offset:3072 sc1\n"
MF"v[196:199],a[108:111],v[76:79],v[196:199]\n"
"s_and_b32 s48,s48,0x18000\n"
EX"v33,v33\n"
MF"v[196:199],a[104:107],v[68:71],v[196:199]\n"
"s_andn2_b32 s49,s59,0x18000\n"
RD"a[64:67],v227 offset:16384\n"
PK"v27,v30,v31\n"
MF"v[168:171],a[112:115],v[64:67],v[168:171]\n"
"s_or_b32 s52,s48,s49\n"
EX"v34,v34\n"
MF"v[168:171],a[116:119],v[72:75],v[168:171]\n"
SA"s53,s52,16384\n"
BL"v225,s[8:11],s43 offen offset:2048 sc1 lds\n"
MF"v[200:203],a[116:119],v[76:79],v[200:203]\n"
EX"v35,v35\n"
MF"v[200:203],a[112:115],v[68:71],v[200:203]\n"
BL"a[212:215],v224,s[8:11],s52 offen offset:0 sc1\n"
RD"a[68:71],v227 offset:24576\n"
MF"v[172:175],a[120:123],v[64:67],v[172:175]\n"
WT"lgkmcnt(2)\n"
EX"v40,v40\n"
MF"v[172:175],a[124:127],v[72:75],v[172:175]\n"
RD"a[72:75],v227 offset:17408\n"
RD"a[76:79],v227 offset:25600\n"
MF"v[204:207],a[124:127],v[76:79],v[204:207]\n"
EX"v41,v41\n"
MF"v[204:207],a[120:123],v[68:71],v[204:207]\n"
RD"a[80:83],v227 offset:18432\n"
MF"a[140:143],a[128:131],v[64:67],a[140:143]\n"
BL"a[216:219],v224,s[8:11],s53 offen offset:0 sc1\n"
EX"v42,v42\n"
MF"a[140:143],a[128:131],v[72:75],a[140:143]\n"
BL"v225,s[8:11],s43 offen offset:3072 sc1 lds\n"
PK"v64,v32,v33\n"
MF"a[144:147],a[128:131],v[76:79],a[144:147]\n"
EX"v43,v43\n"
PK"v65,v34,v35\n"
MF"a[144:147],a[128:131],v[68:71],a[144:147]\n"
WT"lgkmcnt(3)\n"
RD"a[84:87],v227 offset:26624\n"
RD"a[88:91],v227 offset:19456\n"
MF"v[144:147],a[64:67],v[0:3],v[144:147]\n"
SA"m0,s40,16384\n"
EX"v36,v36\n"
MF"v[144:147],a[68:71],v[16:19],v[144:147]\n"
BL"a[220:223],v224,s[8:11],s52 offen offset:1024 sc1\n"
RD"a[92:95],v227 offset:27648\n"
MF"v[176:179],a[68:71],v[24:27],v[176:179]\n"
"buffer_load_dwordx2 v[242:243],v236,s[8:11],0 offen sc1\n"
EX"v37,v37\n"
MF"v[176:179],a[64:67],v[8:11],v[176:179]\n"
WT"lgkmcnt(3)\n"
RD"a[96:99],v227 offset:20480\n"
PK"v68,v40,v41\n"
MF"v[148:151],a[72:75],v[0:3],v[148:151]\n"
SA"s40,s40,32768\n"
EX"v38,v38\n"
MF"v[148:151],a[76:79],v[16:19],v[148:151]\n"
"s_cmp_eq_u32 s40,s46\n"
BL"v225,s[8:11],s44 offen offset:0 sc1 lds\n"
PK"v69,v42,v43\n"
MF"v[180:183],a[76:79],v[24:27],v[180:183]\n"
"s_cselect_b32 s40,s47,s40\n"
BL"a[224:227],v224,s[8:11],s53 offen offset:1024 sc1\n"
EX"v39,v39\n"
MF"v[180:183],a[72:75],v[8:11],v[180:183]\n"
RD"a[100:103],v227 offset:28672\n"
RD"a[104:107],v227 offset:21504\n"
MF"v[152:155],a[80:83],v[0:3],v[152:155]\n"
WT"lgkmcnt(3)\n"
EX"v44,v44\n"
MF"v[152:155],a[84:87],v[16:19],v[152:155]\n"
RD"a[108:111],v227 offset:29696\n"
RD"a[112:115],v227 offset:22528\n"
PK"v66,v36,v37\n"
MF"v[184:187],a[84:87],v[24:27],v[184:187]\n"
EX"v45,v45\n"
MF"v[184:187],a[80:83],v[8:11],v[184:187]\n"
BL"a[228:231],v224,s[8:11],s52 offen offset:2048 sc1\n"
MF"v[156:159],a[88:91],v[0:3],v[156:159]\n"
EX"v46,v46\n"
MF"v[156:159],a[92:95],v[16:19],v[156:159]\n"
BL"v225,s[8:11],s44 offen offset:1024 sc1 lds\n"
PK"v67,v38,v39\n"
MF"v[188:191],a[92:95],v[24:27],v[188:191]\n"
EX"v47,v47\n"
MF"v[188:191],a[88:91],v[8:11],v[188:191]\n"
WT"lgkmcnt(2)\n"
RD"a[116:119],v227 offset:30720\n"
RD"a[120:123],v227 offset:23552\n"
MF"v[160:163],a[96:99],v[0:3],v[160:163]\n"
EX"v48,v48\n"
MF"v[160:163],a[100:103],v[16:19],v[160:163]\n"
BL"a[232:235],v224,s[8:11],s53 offen offset:2048 sc1\n"
RD"a[124:127],v227 offset:31744\n"
MF"v[192:195],a[100:103],v[24:27],v[192:195]\n"
EX"v49,v49\n"
PK"v70,v44,v45\n"
MF"v[192:195],a[96:99],v[8:11],v[192:195]\n"
EX"v50,v50\n"
MF"v[164:167],a[104:107],v[0:3],v[164:167]\n"
WT"lgkmcnt(3)\n"
BL"v225,s[8:11],s44 offen offset:2048 sc1 lds\n"
MF"v[164:167],a[108:111],v[16:19],v[164:167]\n"
BL"a[236:239],v224,s[8:11],s52 offen offset:3072 sc1\n"
PK"v71,v46,v47\n"
MF"v[196:199],a[108:111],v[24:27],v[196:199]\n"
WT"vmcnt(40)\n"
"s_barrier\n"
MF"v[196:199],a[104:107],v[8:11],v[196:199]\n"
RD"a[64:67],v226 offset:0\n"
RD"a[68:71],v226 offset:1024\n"
MF"v[168:171],a[112:115],v[0:3],v[168:171]\n"
WT"lgkmcnt(2)\n"
EX"v51,v51\n"
MF"v[168:171],a[116:119],v[16:19],v[168:171]\n"
EX"v52,v52\n"
PK"v72,v48,v49\n"
MF"v[200:203],a[116:119],v[24:27],v[200:203]\n"
BL"a[240:243],v224,s[8:11],s53 offen offset:3072 sc1\n"
MF"v[200:203],a[112:115],v[8:11],v[200:203]\n"
RD"a[72:75],v226 offset:2048\n"
PK"v73,v50,v51\n"
MF"v[172:175],a[120:123],v[0:3],v[172:175]\n"
EX"v53,v53\n"
MF"v[172:175],a[124:127],v[16:19],v[172:175]\n"
BL"v225,s[8:11],s44 offen offset:3072 sc1 lds\n"
MF"v[204:207],a[124:127],v[24:27],v[204:207]\n"
EX"v54,v54\n"
MF"v[204:207],a[120:123],v[8:11],v[204:207]\n"
RD"a[76:79],v226 offset:3072\n"
RD"a[80:83],v226 offset:4096\n"
MF"a[140:143],a[128:131],v[0:3],a[140:143]\n"
EX"v55,v55\n"
MF"a[140:143],a[128:131],v[16:19],a[140:143]\n"
RD"a[84:87],v226 offset:5120\n"
RD"a[88:91],v226 offset:6144\n"
MF"a[144:147],a[128:131],v[24:27],a[144:147]\n"
PK"v74,v52,v53\n"
PK"v75,v54,v55\n"
MF"a[144:147],a[128:131],v[8:11],a[144:147]\n"
"s_sub_u32 s56,s22,1\n"
"s_mov_b32 s22,-1\n"
"s_cbranch_scc0 LP2_%=\n"
"LE2_%=:\n"
WT"lgkmcnt(6)\n"
MF"v[0:3],a[64:67],a[32:35],v[208:211]\n"
WT"lgkmcnt(5)\n"
WT"vmcnt(5)\n"
"v_cmp_eq_u64 vcc,s[28:29],v[242:243]\n"
"s_or_b32 vcc_lo,vcc_lo,-1\n"
"s_cmp_eq_u64 vcc,exec\n"
"s_cbranch_scc0 L1031_%=\n"
"L1032_%=:\n"
MF"v[0:3],a[68:71],a[36:39],v[0:3]\n"
"s_mov_b32 m0,s40\n"
EX"v56,v56\n"
MF"v[8:11],a[68:71],a[52:55],0\n"
EX"v57,v57\n"
MF"v[8:11],a[64:67],a[48:51],v[8:11]\n"
WT"lgkmcnt(4)\n"
RD"a[92:95],v226 offset:7168\n"
RD"a[96:99],v226 offset:8192\n"
"v_readlane_b32 s43,v228,s42\n"
MF"v[0:3],a[72:75],a[40:43],v[0:3]\n"
WT"lgkmcnt(2)\n"
EX"v58,v58\n"
"v_readlane_b32 s44,v229,s42\n"
MF"v[0:3],a[76:79],a[44:47],v[0:3]\n"
SA"s42,s42,1\n"
BL"v225,s[8:11],s43 offen offset:0 sc1 lds\n"
RD"a[100:103],v226 offset:9216\n"
MF"v[8:11],a[76:79],a[60:63],v[8:11]\n"
SA"s41,s41,32768\n"
EX"v59,v59\n"
PK"v76,v56,v57\n"
MF"v[8:11],a[72:75],a[56:59],v[8:11]\n"
"s_cmp_eq_u32 s41,163840\n"
RD"a[104:107],v226 offset:10240\n"
PK"v77,v58,v59\n"
MF"v[4:7],a[80:83],a[32:35],v[212:215]\n"
"s_cselect_b32 s41,0,s41\n"
EX"v60,v60\n"
"v_add_u32 v227,s41,v224\n"
MF"v[4:7],a[84:87],a[36:39],v[4:7]\n"
BL"v225,s[8:11],s43 offen offset:1024 sc1 lds\n"
MF"v[12:15],a[84:87],a[52:55],0\n"
EX"v61,v61\n"
MF"v[12:15],a[80:83],a[48:51],v[12:15]\n"
RD"a[108:111],v226 offset:11264\n"
RD"a[112:115],v226 offset:12288\n"
MF"v[4:7],a[88:91],a[40:43],v[4:7]\n"
WT"lgkmcnt(3)\n"
EX"v62,v62\n"
MF"v[4:7],a[92:95],a[44:47],v[4:7]\n"
RD"a[116:119],v226 offset:13312\n"
RD"a[120:123],v226 offset:14336\n"
PK"v78,v60,v61\n"
MF"v[12:15],a[92:95],a[60:63],v[12:15]\n"
EX"v63,v63\n"
MF"v[12:15],a[88:91],a[56:59],v[12:15]\n"
BL"v225,s[8:11],s43 offen offset:2048 sc1 lds\n"
MF"v[16:19],a[96:99],a[32:35],v[216:219]\n"
EX"v0,v0\n"
MF"v[16:19],a[100:103],a[36:39],v[16:19]\n"
RD"a[124:127],v226 offset:15360\n"
RD"a[64:67],v226 offset:16384\n"
PK"v79,v62,v63\n"
MF"v[24:27],a[100:103],a[52:55],0\n"
EX"v1,v1\n"
MF"v[24:27],a[96:99],a[48:51],v[24:27]\n"
WT"lgkmcnt(4)\n"
RD"a[68:71],v226 offset:17408\n"
RD"a[72:75],v226 offset:18432\n"
MF"v[16:19],a[104:107],a[40:43],v[16:19]\n"
BL"v225,s[8:11],s43 offen offset:3072 sc1 lds\n"
MF"v[16:19],a[108:111],a[44:47],v[16:19]\n"
SA"m0,s40,16384\n"
EX"v2,v2\n"
PK"v0,v0,v1\n"
MF"v[24:27],a[108:111],a[60:63],v[24:27]\n"
RD"a[76:79],v226 offset:19456\n"
RD"a[80:83],v226 offset:20480\n"
MF"v[24:27],a[104:107],a[56:59],v[24:27]\n"
EX"v3,v3\n"
MF"v[20:23],a[112:115],a[32:35],v[220:223]\n"
WT"lgkmcnt(4)\n"
RD"a[84:87],v226 offset:21504\n"
RD"a[88:91],v226 offset:22528\n"
MF"v[20:23],a[116:119],a[36:39],v[20:23]\n"
EX"v8,v8\n"
PK"v1,v2,v3\n"
MF"v[28:31],a[116:119],a[52:55],0\n"
SA"s40,s40,32768\n"
BL"v225,s[8:11],s44 offen offset:0 sc1 lds\n"
MF"v[28:31],a[112:115],a[48:51],v[28:31]\n"
"s_cmp_eq_u32 s40,s46\n"
RD"a[92:95],v226 offset:23552\n"
RD"a[96:99],v226 offset:24576\n"
EX"v9,v9\n"
MF"v[20:23],a[120:123],a[40:43],v[20:23]\n"
"s_cselect_b32 s40,s47,s40\n"
EX"v10,v10\n"
MF"v[20:23],a[124:127],a[44:47],v[20:23]\n"
RD"a[100:103],v226 offset:25600\n"
RD"a[104:107],v226 offset:26624\n"
PK"v8,v8,v9\n"
MF"v[28:31],a[124:127],a[60:63],v[28:31]\n"
RD"a[108:111],v226 offset:27648\n"
RD"a[112:115],v226 offset:28672\n"
EX"v11,v11\n"
MF"v[28:31],a[120:123],a[56:59],v[28:31]\n"
BL"v225,s[8:11],s44 offen offset:1024 sc1 lds\n"
MF"v[40:43],a[64:67],a[48:51],v[208:211]\n"
WT"lgkmcnt(6)\n"
RD"a[116:119],v226 offset:29696\n"
RD"a[120:123],v226 offset:30720\n"
EX"v4,v4\n"
MF"v[40:43],a[68:71],a[52:55],v[40:43]\n"
EX"v5,v5\n"
PK"v9,v10,v11\n"
MF"v[40:43],a[72:75],a[56:59],v[40:43]\n"
RD"a[124:127],v226 offset:31744\n"
PK"v2,v4,v5\n"
MF"v[40:43],a[76:79],a[60:63],v[40:43]\n"
EX"v6,v6\n"
MF"v[44:47],a[80:83],a[48:51],v[212:215]\n"
"s_barrier\n"
MF"v[44:47],a[84:87],a[52:55],v[44:47]\n"
RD"a[64:67],v227 offset:0\n"
RD"a[68:71],v227 offset:8192\n"
MF"v[44:47],a[88:91],a[56:59],v[44:47]\n"
WT"lgkmcnt(5)\n"
BL"v225,s[8:11],s44 offen offset:2048 sc1 lds\n"
EX"v7,v7\n"
MF"v[44:47],a[92:95],a[60:63],v[44:47]\n"
RD"a[72:75],v227 offset:1024\n"
"v_readlane_b32 s43,v228,s42\n"
MF"v[56:59],a[96:99],a[48:51],v[216:219]\n"
EX"v12,v12\n"
PK"v3,v6,v7\n"
MF"v[56:59],a[100:103],a[52:55],v[56:59]\n"
EX"v13,v13\n"
MF"v[56:59],a[104:107],a[56:59],v[56:59]\n"
BL"v225,s[8:11],s44 offen offset:3072 sc1 lds\n"
MF"v[56:59],a[108:111],a[60:63],v[56:59]\n"
"s_mov_b32 m0,s40\n"
RD"a[76:79],v227 offset:9216\n"
RD"a[80:83],v227 offset:2048\n"
MF"v[60:63],a[112:115],a[48:51],v[220:223]\n"
WT"lgkmcnt(3)\n"
EX"v14,v14\n"
PK"v10,v12,v13\n"
MF"v[60:63],a[116:119],a[52:55],v[60:63]\n"
RD"a[84:87],v227 offset:10240\n"
RD"a[88:91],v227 offset:3072\n"
MF"v[60:63],a[120:123],a[56:59],v[60:63]\n"
EX"v15,v15\n"
MF"v[60:63],a[124:127],a[60:63],v[60:63]\n"
SA"s41,s41,32768\n"
EX"v16,v16\n"
MF"v[144:147],a[64:67],v[64:67],v[144:147]\n"
"s_cmp_eq_u32 s41,163840\n"
BL"v225,s[8:11],s43 offen offset:0 sc1 lds\n"
"v_readlane_b32 s44,v229,s42\n"
MF"v[144:147],a[68:71],v[72:75],v[144:147]\n"
"s_cselect_b32 s41,0,s41\n"
EX"v17,v17\n"
PK"v11,v14,v15\n"
MF"v[176:179],a[68:71],v[76:79],v[176:179]\n"
SA"s42,s42,1\n"
RD"a[92:95],v227 offset:11264\n"
"v_add_u32 v226,s41,v224\n"
MF"v[176:179],a[64:67],v[68:71],v[176:179]\n"
WT"lgkmcnt(3)\n"
EX"v18,v18\n"
MF"v[148:151],a[72:75],v[64:67],v[148:151]\n"
RD"a[96:99],v227 offset:4096\n"
RD"a[100:103],v227 offset:12288\n"
PK"v16,v16,v17\n"
MF"v[148:151],a[76:79],v[72:75],v[148:151]\n"
EX"v19,v19\n"
MF"v[180:183],a[76:79],v[76:79],v[180:183]\n"
BL"v225,s[8:11],s43 offen offset:1024 sc1 lds\n"
MF"v[180:183],a[72:75],v[68:71],v[180:183]\n"
RD"a[104:107],v227 offset:5120\n"
PK"v17,v18,v19\n"
MF"v[152:155],a[80:83],v[64:67],v[152:155]\n"
WT"lgkmcnt(4)\n"
EX"v24,v24\n"
MF"v[152:155],a[84:87],v[72:75],v[152:155]\n"
EX"v25,v25\n"
MF"v[184:187],a[84:87],v[76:79],v[184:187]\n"
EX"v26,v26\n"
MF"v[184:187],a[80:83],v[68:71],v[184:187]\n"
RD"a[108:111],v227 offset:13312\n"
RD"a[112:115],v227 offset:6144\n"
PK"v24,v24,v25\n"
MF"v[156:159],a[88:91],v[64:67],v[156:159]\n"
WT"lgkmcnt(3)\n"
BL"v225,s[8:11],s43 offen offset:2048 sc1 lds\n"
MF"v[156:159],a[92:95],v[72:75],v[156:159]\n"
EX"v27,v27\n"
MF"v[188:191],a[92:95],v[76:79],v[188:191]\n"
RD"a[116:119],v227 offset:14336\n"
RD"a[120:123],v227 offset:7168\n"
MF"v[188:191],a[88:91],v[68:71],v[188:191]\n"
EX"v20,v20\n"
PK"v25,v26,v27\n"
MF"v[160:163],a[96:99],v[64:67],v[160:163]\n"
EX"v21,v21\n"
MF"v[160:163],a[100:103],v[72:75],v[160:163]\n"
BL"v225,s[8:11],s43 offen offset:3072 sc1 lds\n"
MF"v[192:195],a[100:103],v[76:79],v[192:195]\n"
SA"m0,s40,16384\n"
EX"v22,v22\n"
MF"v[192:195],a[96:99],v[68:71],v[192:195]\n"
WT"lgkmcnt(2)\n"
RD"a[124:127],v227 offset:15360\n"
PK"v18,v20,v21\n"
MF"v[164:167],a[104:107],v[64:67],v[164:167]\n"
EX"v23,v23\n"
MF"v[164:167],a[108:111],v[72:75],v[164:167]\n"
EX"v28,v28\n"
MF"v[196:199],a[108:111],v[76:79],v[196:199]\n"
SA"s40,s40,32768\n"
BL"v225,s[8:11],s44 offen offset:0 sc1 lds\n"
MF"v[196:199],a[104:107],v[68:71],v[196:199]\n"
"s_cmp_eq_u32 s40,s46\n"
RD"a[64:67],v227 offset:16384\n"
PK"v19,v22,v23\n"
MF"v[168:171],a[112:115],v[64:67],v[168:171]\n"
WT"lgkmcnt(2)\n"
EX"v29,v29\n"
MF"v[168:171],a[116:119],v[72:75],v[168:171]\n"
"s_cselect_b32 s40,s47,s40\n"
EX"v30,v30\n"
MF"v[200:203],a[116:119],v[76:79],v[200:203]\n"
RD"a[68:71],v227 offset:24576\n"
PK"v26,v28,v29\n"
MF"v[200:203],a[112:115],v[68:71],v[200:203]\n"
BL"v225,s[8:11],s44 offen offset:1024 sc1 lds\n"
MF"v[172:175],a[120:123],v[64:67],v[172:175]\n"
WT"lgkmcnt(2)\n"
EX"v31,v31\n"
MF"v[172:175],a[124:127],v[72:75],v[172:175]\n"
RD"a[72:75],v227 offset:17408\n"
RD"a[76:79],v227 offset:25600\n"
MF"v[204:207],a[124:127],v[76:79],v[204:207]\n"
EX"v40,v40\n"
MF"v[204:207],a[120:123],v[68:71],v[204:207]\n"
RD"a[80:83],v227 offset:18432\n"
PK"v27,v30,v31\n"
MF"a[140:143],a[128:131],v[64:67],a[140:143]\n"
EX"v41,v41\n"
MF"a[140:143],a[128:131],v[72:75],a[140:143]\n"
BL"v225,s[8:11],s44 offen offset:2048 sc1 lds\n"
MF"a[144:147],a[128:131],v[76:79],a[144:147]\n"
EX"v42,v42\n"
MF"a[144:147],a[128:131],v[68:71],a[144:147]\n"
WT"lgkmcnt(3)\n"
RD"a[84:87],v227 offset:26624\n"
RD"a[88:91],v227 offset:19456\n"
PK"v68,v40,v41\n"
MF"v[144:147],a[64:67],v[0:3],v[144:147]\n"
EX"v43,v43\n"
MF"v[144:147],a[68:71],v[16:19],v[144:147]\n"
RD"a[92:95],v227 offset:27648\n"
RD"a[96:99],v227 offset:20480\n"
MF"v[176:179],a[68:71],v[24:27],v[176:179]\n"
EX"v44,v44\n"
"v_readlane_b32 s43,v228,s42\n"
MF"v[176:179],a[64:67],v[8:11],v[176:179]\n"
WT"lgkmcnt(4)\n"
BL"v225,s[8:11],s44 offen offset:3072 sc1 lds\n"
PK"v69,v42,v43\n"
MF"v[148:151],a[72:75],v[0:3],v[148:151]\n"
"s_mov_b32 m0,s40\n"
EX"v45,v45\n"
MF"v[148:151],a[76:79],v[16:19],v[148:151]\n"
EX"v46,v46\n"
MF"v[180:183],a[76:79],v[24:27],v[180:183]\n"
RD"a[100:103],v227 offset:28672\n"
"v_readlane_b32 s44,v229,s42\n"
PK"v70,v44,v45\n"
MF"v[180:183],a[72:75],v[8:11],v[180:183]\n"
SA"s42,s42,1\n"
EX"v47,v47\n"
MF"v[152:155],a[80:83],v[0:3],v[152:155]\n"
WT"lgkmcnt(1)\n"
RD"a[104:107],v227 offset:21504\n"
RD"a[108:111],v227 offset:29696\n"
MF"v[152:155],a[84:87],v[16:19],v[152:155]\n"
EX"v56,v56\n"
MF"v[184:187],a[84:87],v[24:27],v[184:187]\n"
RD"a[112:115],v227 offset:22528\n"
PK"v71,v46,v47\n"
MF"v[184:187],a[80:83],v[8:11],v[184:187]\n"
EX"v57,v57\n"
MF"v[156:159],a[88:91],v[0:3],v[156:159]\n"
EX"v58,v58\n"
MF"v[156:159],a[92:95],v[16:19],v[156:159]\n"
RD"a[116:119],v227 offset:30720\n"
PK"v76,v56,v57\n"
MF"v[188:191],a[92:95],v[24:27],v[188:191]\n"
EX"v59,v59\n"
MF"v[188:191],a[88:91],v[8:11],v[188:191]\n"
RD"a[120:123],v227 offset:23552\n"
RD"a[124:127],v227 offset:31744\n"
MF"v[160:163],a[96:99],v[0:3],v[160:163]\n"
WT"lgkmcnt(4)\n"
EX"v60,v60\n"
MF"v[160:163],a[100:103],v[16:19],v[160:163]\n"
PK"v77,v58,v59\n"
MF"v[192:195],a[100:103],v[24:27],v[192:195]\n"
EX"v61,v61\n"
MF"v[192:195],a[96:99],v[8:11],v[192:195]\n"
PK"v78,v60,v61\n"
MF"v[164:167],a[104:107],v[0:3],v[164:167]\n"
EX"v62,v62\n"
MF"v[164:167],a[108:111],v[16:19],v[164:167]\n"
SA"s41,s41,32768\n"
WT"vmcnt(16)\n"
"s_barrier\n"
MF"v[196:199],a[108:111],v[24:27],v[196:199]\n"
"s_cmp_eq_u32 s41,163840\n"
BL"v225,s[8:11],s43 offen offset:0 sc1 lds\n"
BL"v225,s[8:11],s43 offen offset:1024 sc1 lds\n"
MF"v[196:199],a[104:107],v[8:11],v[196:199]\n"
WT"lgkmcnt(0)\n"
BL"v225,s[8:11],s43 offen offset:2048 sc1 lds\n"
RD"a[64:67],v226 offset:0\n"
MF"v[168:171],a[112:115],v[0:3],v[168:171]\n"
"s_cselect_b32 s41,0,s41\n"
EX"v63,v63\n"
"v_add_u32 v227,s41,v224\n"
MF"v[168:171],a[116:119],v[16:19],v[168:171]\n"
RD"a[68:71],v226 offset:8192\n"
RD"a[72:75],v226 offset:1024\n"
BL"v225,s[8:11],s43 offen offset:3072 sc1 lds\n"
MF"v[200:203],a[116:119],v[24:27],v[200:203]\n"
SA"m0,s40,16384\n"
RD"a[76:79],v226 offset:9216\n"
RD"a[80:83],v226 offset:2048\n"
PK"v79,v62,v63\n"
MF"v[200:203],a[112:115],v[8:11],v[200:203]\n"
RD"a[84:87],v226 offset:10240\n"
MF"v[172:175],a[120:123],v[0:3],v[172:175]\n"
RD"a[88:91],v226 offset:3072\n"
MF"v[172:175],a[124:127],v[16:19],v[172:175]\n"
BL"v225,s[8:11],s44 offen offset:0 sc1 lds\n"
MF"v[204:207],a[124:127],v[24:27],v[204:207]\n"
SA"s40,s40,32768\n"
RD"a[92:95],v226 offset:11264\n"
RD"a[96:99],v226 offset:4096\n"
MF"v[204:207],a[120:123],v[8:11],v[204:207]\n"
"s_cmp_eq_u32 s40,s46\n"
RD"a[100:103],v226 offset:12288\n"
RD"a[104:107],v226 offset:5120\n"
MF"a[140:143],a[128:131],v[0:3],a[140:143]\n"
"s_cselect_b32 s40,s47,s40\n"
RD"a[108:111],v226 offset:13312\n"
MF"a[140:143],a[128:131],v[16:19],a[140:143]\n"
RD"a[112:115],v226 offset:6144\n"
MF"a[144:147],a[128:131],v[24:27],a[144:147]\n"
BL"v225,s[8:11],s44 offen offset:1024 sc1 lds\n"
MF"a[144:147],a[128:131],v[8:11],a[144:147]\n"
WT"lgkmcnt(6)\n"
RD"a[116:119],v226 offset:14336\n"
MF"v[176:179],a[64:67],v[68:71],v[176:179]\n"
RD"a[120:123],v226 offset:7168\n"
MF"v[176:179],a[68:71],v[76:79],v[176:179]\n"
RD"a[124:127],v226 offset:15360\n"
MF"v[180:183],a[72:75],v[68:71],v[180:183]\n"
WT"vmcnt(14)\n"
"s_barrier\n"
MF"v[180:183],a[76:79],v[76:79],v[180:183]\n"
RD"a[64:67],v227 offset:0\n"
MF"v[184:187],a[80:83],v[68:71],v[184:187]\n"
BL"v225,s[8:11],s44 offen offset:2048 sc1 lds\n"
MF"v[184:187],a[84:87],v[76:79],v[184:187]\n"
RD"a[68:71],v227 offset:1024\n"
MF"v[188:191],a[88:91],v[68:71],v[188:191]\n"
WT"lgkmcnt(4)\n"
MF"v[188:191],a[92:95],v[76:79],v[188:191]\n"
MF"v[192:195],a[96:99],v[68:71],v[192:195]\n"
MF"v[192:195],a[100:103],v[76:79],v[192:195]\n"
BL"v225,s[8:11],s44 offen offset:3072 sc1 lds\n"
MF"v[196:199],a[104:107],v[68:71],v[196:199]\n"
RD"a[72:75],v227 offset:2048\n"
MF"v[196:199],a[108:111],v[76:79],v[196:199]\n"
RD"a[76:79],v227 offset:3072\n"
RD"a[80:83],v227 offset:4096\n"
MF"v[200:203],a[112:115],v[68:71],v[200:203]\n"
MF"v[200:203],a[116:119],v[76:79],v[200:203]\n"
WT"lgkmcnt(3)\n"
RD"a[84:87],v227 offset:5120\n"
MF"v[204:207],a[120:123],v[68:71],v[204:207]\n"
MF"v[204:207],a[124:127],v[76:79],v[204:207]\n"
MF"a[144:147],a[128:131],v[68:71],a[144:147]\n"
MF"a[144:147],a[128:131],v[76:79],a[144:147]\n"
RD"a[88:91],v227 offset:6144\n"
"LTL_%=:\n"
"s_cmp_ge_u32 s34,6\n"
"s_cbranch_scc1 LTH_%=\n"
RD"a[92:95],v227 offset:7168\n"
MF"v[0:3],a[64:67],v[80:83],0\n"
"v_accvgpr_read_b32 v251,a140\n"
"v_subrev_u32 v253,0x30800000,v251\n"
MF"v[0:3],a[68:71],v[88:91],v[0:3]\n"
RD"a[96:99],v227 offset:8192\n"
MF"v[40:43],a[68:71],v[120:123],0\n"
"v_cmp_le_u32 vcc,0x1f000000,v253\n"
"s_cmp_lg_u64 vcc,0\n"
"s_cbranch_scc1 L1041_%=\n"
"L1040_%=:\n"
MF"v[40:43],a[64:67],v[112:115],v[40:43]\n"
WT"lgkmcnt(3)\n"
"v_rcp_f32 v251,v251\n"
MF"v[4:7],a[72:75],v[80:83],0\n"
SA"s41,s41,32768\n"
RD"a[100:103],v227 offset:9216\n"
MU"v144,v251,v144\n"
MF"v[4:7],a[76:79],v[88:91],v[4:7]\n"
"s_cmp_eq_u32 s41,163840\n"
RD"a[104:107],v227 offset:10240\n"
MU"v145,v251,v145\n"
MF"v[44:47],a[76:79],v[120:123],0\n"
"s_cselect_b32 s41,0,s41\n"
MU"v146,v251,v146\n"
"v_add_u32 v226,s41,v224\n"
MF"v[44:47],a[72:75],v[112:115],v[44:47]\n"
RD"a[108:111],v227 offset:11264\n"
MU"v147,v251,v147\n"
MF"v[8:11],a[80:83],v[80:83],0\n"
SA"s41,s41,32768\n"
MU"v148,v251,v148\n"
MU"v149,v251,v149\n"
MF"v[8:11],a[84:87],v[88:91],v[8:11]\n"
"s_cmp_eq_u32 s41,163840\n"
RD"a[112:115],v227 offset:12288\n"
MU"v150,v251,v150\n"
MU"v151,v251,v151\n"
MF"v[48:51],a[84:87],v[120:123],0\n"
"s_cselect_b32 s41,0,s41\n"
PK"v144,v144,v145\n"
"v_add_u32 v235,s41,v224\n"
PK"v145,v146,v147\n"
MF"v[48:51],a[80:83],v[112:115],v[48:51]\n"
WT"lgkmcnt(3)\n"
RD"a[116:119],v227 offset:13312\n"
PK"v146,v148,v149\n"
MF"v[12:15],a[88:91],v[80:83],0\n"
SA"s41,s41,32768\n"
PK"v147,v150,v151\n"
MU"v152,v251,v152\n"
MF"v[12:15],a[92:95],v[88:91],v[12:15]\n"
"s_cmp_eq_u32 s41,163840\n"
BS"v[144:147],v224,s[8:11],s60 offen offset:0 sc1\n"
MU"v153,v251,v153\n"
MF"v[52:55],a[92:95],v[120:123],0\n"
"s_cselect_b32 s41,0,s41\n"
RD"a[120:123],v227 offset:14336\n"
"v_add_u32 v236,s41,v224\n"
MU"v154,v251,v154\n"
MF"v[52:55],a[88:91],v[112:115],v[52:55]\n"
RD"a[124:127],v227 offset:15360\n"
MU"v155,v251,v155\n"
MU"v156,v251,v156\n"
MF"v[16:19],a[96:99],v[80:83],0\n"
SA"s41,s41,32768\n"
MU"v157,v251,v157\n"
MU"v158,v251,v158\n"
MU"v159,v251,v159\n"
MF"v[16:19],a[100:103],v[88:91],v[16:19]\n"
"s_cmp_eq_u32 s41,163840\n"
RD"a[64:67],v227 offset:16384\n"
PK"v152,v152,v153\n"
PK"v153,v154,v155\n"
MF"v[56:59],a[100:103],v[120:123],0\n"
"s_cselect_b32 s41,0,s41\n"
PK"v154,v156,v157\n"
"v_add_u32 v237,s41,v224\n"
PK"v155,v158,v159\n"
MF"v[56:59],a[96:99],v[112:115],v[56:59]\n"
WT"lgkmcnt(3)\n"
BS"v[152:155],v224,s[8:11],s60 offen offset:1024 sc1\n"
MU"v160,v251,v160\n"
MF"v[20:23],a[104:107],v[80:83],0\n"
RD"a[68:71],v227 offset:17408\n"
MU"v161,v251,v161\n"
MU"v162,v251,v162\n"
MF"v[20:23],a[108:111],v[88:91],v[20:23]\n"
RD"a[72:75],v227 offset:18432\n"
MU"v163,v251,v163\n"
MU"v164,v251,v164\n"
MF"v[60:63],a[108:111],v[120:123],0\n"
MU"v165,v251,v165\n"
MU"v166,v251,v166\n"
MF"v[60:63],a[104:107],v[112:115],v[60:63]\n"
RD"a[76:79],v227 offset:19456\n"
MU"v167,v251,v167\n"
PK"v160,v160,v161\n"
MF"v[24:27],a[112:115],v[80:83],0\n"
SA"s51,s60,16384\n"
"v_accvgpr_read_b32 v252,a144\n"
"v_subrev_u32 v253,0x30800000,v252\n"
PK"v161,v162,v163\n"
MF"v[24:27],a[116:119],v[88:91],v[24:27]\n"
"s_lshl_b32 s62,s34,14\n"
RD"a[80:83],v227 offset:20480\n"
PK"v162,v164,v165\n"
PK"v163,v166,v167\n"
MF"v[64:67],a[116:119],v[120:123],0\n"
"s_sub_u32 s62,s61,s62\n"
BS"v[160:163],v224,s[8:11],s60 offen offset:2048 sc1\n"
MU"v168,v251,v168\n"
MF"v[64:67],a[112:115],v[112:115],v[64:67]\n"
WT"lgkmcnt(3)\n"
"v_cmp_le_u32 vcc,0x1f000000,v253\n"
"s_cmp_lg_u64 vcc,0\n"
"s_cbranch_scc1 L1047_%=\n"
"L1046_%=:\n"
MF"v[28:31],a[120:123],v[80:83],0\n"
SA"s62,s62,122880\n"
"v_rcp_f32 v252,v252\n"
MU"v169,v251,v169\n"
MF"v[28:31],a[124:127],v[88:91],v[28:31]\n"
RD"a[84:87],v227 offset:21504\n"
RD"a[88:91],v227 offset:22528\n"
MU"v170,v251,v170\n"
MF"v[68:71],a[124:127],v[120:123],0\n"
MU"v171,v251,v171\n"
MU"v172,v251,v172\n"
MU"v173,v251,v173\n"
MF"v[68:71],a[120:123],v[112:115],v[68:71]\n"
RD"a[92:95],v227 offset:23552\n"
MU"v174,v251,v174\n"
MU"v175,v251,v175\n"
MF"v[32:35],a[64:67],v[80:83],0\n"
PK"v168,v168,v169\n"
PK"v169,v170,v171\n"
MU"v176,v252,v176\n"
MF"v[32:35],a[68:71],v[88:91],v[32:35]\n"
RD"a[96:99],v227 offset:24576\n"
MU"v177,v252,v177\n"
PK"v170,v172,v173\n"
MF"v[72:75],a[68:71],v[120:123],0\n"
PK"v171,v174,v175\n"
MU"v178,v252,v178\n"
MU"v179,v252,v179\n"
MF"v[72:75],a[64:67],v[112:115],v[72:75]\n"
WT"lgkmcnt(4)\n"
BS"v[168:171],v224,s[8:11],s60 offen offset:3072 sc1\n"
MU"v180,v252,v180\n"
MF"v[36:39],a[72:75],v[80:83],0\n"
RD"a[100:103],v227 offset:25600\n"
MU"v181,v252,v181\n"
MU"v182,v252,v182\n"
MF"v[36:39],a[76:79],v[88:91],v[36:39]\n"
RD"a[104:107],v227 offset:26624\n"
MU"v183,v252,v183\n"
PK"v176,v176,v177\n"
MF"v[76:79],a[76:79],v[120:123],0\n"
PK"v177,v178,v179\n"
PK"v178,v180,v181\n"
PK"v179,v182,v183\n"
MF"v[76:79],a[72:75],v[112:115],v[76:79]\n"
BS"v[176:179],v224,s[8:11],s51 offen offset:0 sc1\n"
MU"v184,v252,v184\n"
MF"v[0:3],a[80:83],v[96:99],v[0:3]\n"
WT"lgkmcnt(2)\n"
RD"a[108:111],v227 offset:27648\n"
MU"v185,v252,v185\n"
MU"v186,v252,v186\n"
MF"v[0:3],a[84:87],v[104:107],v[0:3]\n"
RD"a[112:115],v227 offset:28672\n"
MU"v187,v252,v187\n"
MU"v188,v252,v188\n"
MF"v[40:43],a[84:87],v[136:139],v[40:43]\n"
MU"v189,v252,v189\n"
MU"v190,v252,v190\n"
MU"v191,v252,v191\n"
MF"v[40:43],a[80:83],v[128:131],v[40:43]\n"
RD"a[116:119],v227 offset:29696\n"
PK"v184,v184,v185\n"
PK"v185,v186,v187\n"
MF"v[4:7],a[88:91],v[96:99],v[4:7]\n"
PK"v186,v188,v189\n"
PK"v187,v190,v191\n"
MU"v192,v252,v192\n"
MF"v[4:7],a[92:95],v[104:107],v[4:7]\n"
BS"v[184:187],v224,s[8:11],s51 offen offset:1024 sc1\n"
MU"v193,v252,v193\n"
MF"v[44:47],a[92:95],v[136:139],v[44:47]\n"
RD"a[120:123],v227 offset:30720\n"
MU"v194,v252,v194\n"
MU"v195,v252,v195\n"
MF"v[44:47],a[88:91],v[128:131],v[44:47]\n"
RD"a[124:127],v227 offset:31744\n"
MU"v196,v252,v196\n"
MU"v197,v252,v197\n"
MF"v[8:11],a[96:99],v[96:99],v[8:11]\n"
WT"lgkmcnt(3)\n"
WT"vmcnt(14)\n"
"s_barrier\n"
MF"v[8:11],a[100:103],v[104:107],v[8:11]\n"
RD"a[64:67],v226 offset:0\n"
MU"v198,v252,v198\n"
MU"v199,v252,v199\n"
MF"v[48:51],a[100:103],v[136:139],v[48:51]\n"
PK"v192,v192,v193\n"
PK"v193,v194,v195\n"
PK"v194,v196,v197\n"
MF"v[48:51],a[96:99],v[128:131],v[48:51]\n"
RD"a[68:71],v226 offset:1024\n"
PK"v195,v198,v199\n"
MU"v200,v252,v200\n"
MF"v[12:15],a[104:107],v[96:99],v[12:15]\n"
BS"v[192:195],v224,s[8:11],s51 offen offset:2048 sc1\n"
MU"v201,v252,v201\n"
MF"v[12:15],a[108:111],v[104:107],v[12:15]\n"
RD"a[72:75],v226 offset:2048\n"
MU"v202,v252,v202\n"
MU"v203,v252,v203\n"
MF"v[52:55],a[108:111],v[136:139],v[52:55]\n"
MU"v204,v252,v204\n"
MU"v205,v252,v205\n"
MU"v206,v252,v206\n"
MF"v[52:55],a[104:107],v[128:131],v[52:55]\n"
RD"a[76:79],v226 offset:3072\n"
MU"v207,v252,v207\n"
PK"v200,v200,v201\n"
MF"v[16:19],a[112:115],v[96:99],v[16:19]\n"
WT"lgkmcnt(3)\n"
PK"v201,v202,v203\n"
PK"v202,v204,v205\n"
PK"v203,v206,v207\n"
MF"v[16:19],a[116:119],v[104:107],v[16:19]\n"
BS"v[200:203],v224,s[8:11],s51 offen offset:3072 sc1\n"
MF"v[56:59],a[116:119],v[136:139],v[56:59]\n"
RD"a[80:83],v226 offset:4096\n"
"v_readlane_b32 s43,v228,s42\n"
MF"v[56:59],a[112:115],v[128:131],v[56:59]\n"
RD"a[84:87],v226 offset:5120\n"
MF"v[20:23],a[120:123],v[96:99],v[20:23]\n"
MF"v[20:23],a[124:127],v[104:107],v[20:23]\n"
RD"a[88:91],v226 offset:6144\n"
MF"v[60:63],a[124:127],v[136:139],v[60:63]\n"
"s_mov_b32 m0,s40\n"
MF"v[60:63],a[120:123],v[128:131],v[60:63]\n"
RD"a[92:95],v226 offset:7168\n"
MF"v[24:27],a[64:67],v[96:99],v[24:27]\n"
WT"lgkmcnt(4)\n"
"v_readlane_b32 s44,v229,s42\n"
MF"v[24:27],a[68:71],v[104:107],v[24:27]\n"
RD"a[96:99],v227 offset:0\n"
MF"v[64:67],a[68:71],v[136:139],v[64:67]\n"
SA"s42,s42,1\n"
BL"v225,s[8:11],s43 offen offset:0 sc1 lds\n"
MF"v[64:67],a[64:67],v[128:131],v[64:67]\n"
RD"a[100:103],v227 offset:1024\n"
MF"v[28:31],a[72:75],v[96:99],v[28:31]\n"
MF"v[28:31],a[76:79],v[104:107],v[28:31]\n"
RD"a[104:107],v227 offset:2048\n"
MF"v[68:71],a[76:79],v[136:139],v[68:71]\n"
MF"v[68:71],a[72:75],v[128:131],v[68:71]\n"
WT"lgkmcnt(3)\n"
RD"a[108:111],v227 offset:3072\n"
MF"v[32:35],a[80:83],v[96:99],v[32:35]\n"
MF"v[32:35],a[84:87],v[104:107],v[32:35]\n"
RD"a[112:115],v227 offset:4096\n"
MF"v[72:75],a[84:87],v[136:139],v[72:75]\n"
BL"v225,s[8:11],s43 offen offset:1024 sc1 lds\n"
MF"v[72:75],a[80:83],v[128:131],v[72:75]\n"
RD"a[116:119],v227 offset:5120\n"
MF"v[36:39],a[88:91],v[96:99],v[36:39]\n"
MF"v[36:39],a[92:95],v[104:107],v[36:39]\n"
RD"a[120:123],v227 offset:6144\n"
MF"v[76:79],a[92:95],v[136:139],v[76:79]\n"
MF"v[76:79],a[88:91],v[128:131],v[76:79]\n"
WT"lgkmcnt(3)\n"
RD"a[124:127],v227 offset:7168\n"
MF"v[84:87],a[96:99],v[144:147],0\n"
BL"v225,s[8:11],s43 offen offset:2048 sc1 lds\n"
MF"v[84:87],a[100:103],v[152:155],v[84:87]\n"
RD"a[64:67],v227 offset:8192\n"
MF"v[164:167],a[100:103],v[184:187],0\n"
MF"v[164:167],a[96:99],v[176:179],v[164:167]\n"
RD"a[68:71],v227 offset:9216\n"
MF"v[92:95],a[104:107],v[144:147],0\n"
MF"v[92:95],a[108:111],v[152:155],v[92:95]\n"
RD"a[72:75],v227 offset:10240\n"
MF"v[172:175],a[108:111],v[184:187],0\n"
MF"v[172:175],a[104:107],v[176:179],v[172:175]\n"
WT"lgkmcnt(3)\n"
RD"a[76:79],v227 offset:11264\n"
MF"v[100:103],a[112:115],v[144:147],0\n"
BL"v225,s[8:11],s43 offen offset:3072 sc1 lds\n"
MF"v[100:103],a[116:119],v[152:155],v[100:103]\n"
RD"a[80:83],v227 offset:12288\n"
MF"v[180:183],a[116:119],v[184:187],0\n"
SA"m0,s40,16384\n"
MF"v[180:183],a[112:115],v[176:179],v[180:183]\n"
RD"a[84:87],v227 offset:13312\n"
MF"v[108:111],a[120:123],v[144:147],0\n"
MF"v[108:111],a[124:127],v[152:155],v[108:111]\n"
RD"a[88:91],v227 offset:14336\n"
MF"v[188:191],a[124:127],v[184:187],0\n"
BL"v225,s[8:11],s44 offen offset:0 sc1 lds\n"
MF"v[188:191],a[120:123],v[176:179],v[188:191]\n"
WT"lgkmcnt(3)\n"
RD"a[92:95],v227 offset:15360\n"
SA"s40,s40,32768\n"
MF"v[116:119],a[64:67],v[144:147],0\n"
"s_cmp_eq_u32 s40,s46\n"
"s_cselect_b32 s40,s47,s40\n"
MF"v[116:119],a[68:71],v[152:155],v[116:119]\n"
RD"a[96:99],v227 offset:16384\n"
MF"v[196:199],a[68:71],v[184:187],0\n"
MF"v[196:199],a[64:67],v[176:179],v[196:199]\n"
RD"a[100:103],v227 offset:17408\n"
MF"v[124:127],a[72:75],v[144:147],0\n"
MF"v[124:127],a[76:79],v[152:155],v[124:127]\n"
RD"a[104:107],v227 offset:18432\n"
MF"v[204:207],a[76:79],v[184:187],0\n"
BL"v225,s[8:11],s44 offen offset:1024 sc1 lds\n"
MF"v[204:207],a[72:75],v[176:179],v[204:207]\n"
WT"lgkmcnt(3)\n"
RD"a[108:111],v227 offset:19456\n"
MF"v[132:135],a[80:83],v[144:147],0\n"
MF"v[132:135],a[84:87],v[152:155],v[132:135]\n"
RD"a[112:115],v227 offset:20480\n"
MF"v[208:211],a[84:87],v[184:187],0\n"
MF"v[208:211],a[80:83],v[176:179],v[208:211]\n"
RD"a[116:119],v227 offset:21504\n"
MF"v[140:143],a[88:91],v[144:147],0\n"
BL"v225,s[8:11],s44 offen offset:2048 sc1 lds\n"
MF"v[140:143],a[92:95],v[152:155],v[140:143]\n"
RD"a[120:123],v227 offset:22528\n"
MF"v[212:215],a[92:95],v[184:187],0\n"
WT"vmcnt(7)\n"
"buffer_store_dwordx2 v[232:233],v230,s[8:11],s62 offen sc1\n"
MF"v[212:215],a[88:91],v[176:179],v[212:215]\n"
WT"lgkmcnt(3)\n"
RD"a[124:127],v227 offset:23552\n"
MF"v[148:151],a[96:99],v[144:147],0\n"
MF"v[148:151],a[100:103],v[152:155],v[148:151]\n"
RD"a[64:67],v227 offset:24576\n"
MF"v[216:219],a[100:103],v[184:187],0\n"
"v_readlane_b32 s43,v228,s42\n"
MF"v[216:219],a[96:99],v[176:179],v[216:219]\n"
RD"a[68:71],v227 offset:25600\n"
MF"v[156:159],a[104:107],v[144:147],0\n"
BL"v225,s[8:11],s44 offen offset:3072 sc1 lds\n"
MF"v[156:159],a[108:111],v[152:155],v[156:159]\n"
RD"a[72:75],v227 offset:26624\n"
MF"v[220:223],a[108:111],v[184:187],0\n"
"s_mov_b32 m0,s40\n"
MF"v[220:223],a[104:107],v[176:179],v[220:223]\n"
WT"lgkmcnt(3)\n"
RD"a[76:79],v227 offset:27648\n"
MF"v[84:87],a[112:115],v[160:163],v[84:87]\n"
"s_lshr_b32 s89,s31,3\n"
"v_readlane_b32 s44,v229,s42\n"
MF"v[84:87],a[116:119],v[168:171],v[84:87]\n"
"s_lshl_b32 s89,s89,4\n"
RD"a[80:83],v227 offset:28672\n"
MF"v[164:167],a[116:119],v[200:203],v[164:167]\n"
SA"s42,s42,1\n"
BL"v225,s[8:11],s43 offen offset:0 sc1 lds\n"
MF"v[164:167],a[112:115],v[192:195],v[164:167]\n"
SA"s89,s89,s30\n"
RD"a[84:87],v227 offset:29696\n"
"s_lshl_b32 s89,s89,3\n"
MF"v[92:95],a[120:123],v[160:163],v[92:95]\n"
"s_xor_b32 s23,s32,s30\n"
MF"v[92:95],a[124:127],v[168:171],v[92:95]\n"
SA"s89,s89,s23\n"
RD"a[88:91],v227 offset:30720\n"
MF"v[172:175],a[124:127],v[200:203],v[172:175]\n"
"s_xor_b32 s90,s89,36\n"
MF"v[172:175],a[120:123],v[192:195],v[172:175]\n"
WT"lgkmcnt(3)\n"
RD"a[92:95],v227 offset:31744\n"
"s_mul_i32 s88,s89,5120\n"
MF"v[100:103],a[64:67],v[160:163],v[100:103]\n"
"s_mul_i32 s22,s90,5120\n"
MF"v[100:103],a[68:71],v[168:171],v[100:103]\n"
"s_sub_u32 s90,s90,s89\n"
RD"a[96:99],v226 offset:0\n"
MF"v[180:183],a[68:71],v[200:203],v[180:183]\n"
"s_lshl_b32 s90,s90,6\n"
BL"v225,s[8:11],s43 offen offset:1024 sc1 lds\n"
MF"v[180:183],a[64:67],v[192:195],v[180:183]\n"
SA"s88,s88,59506688\n"
RD"a[100:103],v226 offset:1024\n"
MF"v[108:111],a[72:75],v[160:163],v[108:111]\n"
SA"s22,s22,59506688\n"
MF"v[108:111],a[76:79],v[168:171],v[108:111]\n"
"s_cmp_eq_u32 s23,0\n"
RD"a[104:107],v226 offset:2048\n"
"s_cselect_b32 s88,2130706432,s88\n"
MF"v[188:191],a[76:79],v[200:203],v[188:191]\n"
"s_cmp_eq_u32 s23,4\n"
"s_cselect_b32 s22,2130706432,s22\n"
MF"v[188:191],a[72:75],v[192:195],v[188:191]\n"
WT"lgkmcnt(3)\n"
RD"a[108:111],v226 offset:3072\n"
"s_lshl_b32 s89,s89,6\n"
MF"v[116:119],a[80:83],v[160:163],v[116:119]\n"
SA"s89,s89,131072\n"
BL"v225,s[8:11],s43 offen offset:2048 sc1 lds\n"
MF"v[116:119],a[84:87],v[168:171],v[116:119]\n"
RD"a[112:115],v226 offset:4096\n"
"v_bfe_u32 v239,v224,1,6\n"
MF"v[196:199],a[84:87],v[200:203],v[196:199]\n"
"v_bfe_u32 v242,v224,7,1\n"
MF"v[196:199],a[80:83],v[192:195],v[196:199]\n"
RD"a[116:119],v226 offset:5120\n"
"v_mad_u32_u24 v239,v242,s90,v239\n"
MF"v[124:127],a[88:91],v[160:163],v[124:127]\n"
"v_lshrrev_b32 v242,8,v224\n"
MF"v[124:127],a[92:95],v[168:171],v[124:127]\n"
RD"a[120:123],v226 offset:6144\n"
"v_lshl_add_u32 v239,v242,27,v239\n"
MF"v[204:207],a[92:95],v[200:203],v[204:207]\n"
MF"v[204:207],a[88:91],v[192:195],v[204:207]\n"
WT"lgkmcnt(3)\n"
RD"a[124:127],v226 offset:7168\n"
MF"v[132:135],a[96:99],v[160:163],v[132:135]\n"
BL"v225,s[8:11],s43 offen offset:3072 sc1 lds\n"
MF"v[132:135],a[100:103],v[168:171],v[132:135]\n"
RD"a[64:67],v226 offset:8192\n"
MF"v[208:211],a[100:103],v[200:203],v[208:211]\n"
SA"m0,s40,16384\n"
MF"v[208:211],a[96:99],v[192:195],v[208:211]\n"
RD"a[68:71],v226 offset:9216\n"
MF"v[140:143],a[104:107],v[160:163],v[140:143]\n"
MF"v[140:143],a[108:111],v[168:171],v[140:143]\n"
RD"a[72:75],v226 offset:10240\n"
MF"v[212:215],a[108:111],v[200:203],v[212:215]\n"
BL"v225,s[8:11],s44 offen offset:0 sc1 lds\n"
MF"v[212:215],a[104:107],v[192:195],v[212:215]\n"
WT"lgkmcnt(3)\n"
RD"a[76:79],v226 offset:11264\n"
SA"s40,s40,32768\n"
MF"v[148:151],a[112:115],v[160:163],v[148:151]\n"
"s_cmp_eq_u32 s40,s46\n"
"s_cselect_b32 s40,s47,s40\n"
MF"v[148:151],a[116:119],v[168:171],v[148:151]\n"
RD"a[80:83],v226 offset:12288\n"
MF"v[216:219],a[116:119],v[200:203],v[216:219]\n"
MF"v[216:219],a[112:115],v[192:195],v[216:219]\n"
RD"a[84:87],v226 offset:13312\n"
MF"v[156:159],a[120:123],v[160:163],v[156:159]\n"
MF"v[156:159],a[124:127],v[168:171],v[156:159]\n"
RD"a[88:91],v226 offset:14336\n"
MF"v[220:223],a[124:127],v[200:203],v[220:223]\n"
BL"v225,s[8:11],s44 offen offset:1024 sc1 lds\n"
MF"v[220:223],a[120:123],v[192:195],v[220:223]\n"
WT"lgkmcnt(3)\n"
RD"a[92:95],v226 offset:15360\n"
MF"v[0:3],a[64:67],a[0:3],v[0:3]\n"
MF"v[0:3],a[68:71],a[8:11],v[0:3]\n"
RD"a[96:99],v226 offset:16384\n"
MF"v[40:43],a[68:71],a[12:15],v[40:43]\n"
MF"v[40:43],a[64:67],a[4:7],v[40:43]\n"
RD"a[100:103],v226 offset:17408\n"
MF"v[4:7],a[72:75],a[0:3],v[4:7]\n"
BL"v225,s[8:11],s44 offen offset:2048 sc1 lds\n"
MF"v[4:7],a[76:79],a[8:11],v[4:7]\n"
RD"a[104:107],v226 offset:18432\n"
MF"v[44:47],a[76:79],a[12:15],v[44:47]\n"
MF"v[44:47],a[72:75],a[4:7],v[44:47]\n"
WT"lgkmcnt(3)\n"
RD"a[108:111],v226 offset:19456\n"
MF"v[8:11],a[80:83],a[0:3],v[8:11]\n"
MF"v[8:11],a[84:87],a[8:11],v[8:11]\n"
RD"a[112:115],v226 offset:20480\n"
MF"v[48:51],a[84:87],a[12:15],v[48:51]\n"
MF"v[48:51],a[80:83],a[4:7],v[48:51]\n"
RD"a[116:119],v226 offset:21504\n"
MF"v[12:15],a[88:91],a[0:3],v[12:15]\n"
BL"v225,s[8:11],s44 offen offset:3072 sc1 lds\n"
MF"v[12:15],a[92:95],a[8:11],v[12:15]\n"
RD"a[120:123],v226 offset:22528\n"
MF"v[52:55],a[92:95],a[12:15],v[52:55]\n"
MF"v[52:55],a[88:91],a[4:7],v[52:55]\n"
WT"lgkmcnt(3)\n"
RD"a[124:127],v226 offset:23552\n"
MF"v[16:19],a[96:99],a[0:3],v[16:19]\n"
MF"v[16:19],a[100:103],a[8:11],v[16:19]\n"
RD"a[64:67],v226 offset:24576\n"
MF"v[56:59],a[100:103],a[12:15],v[56:59]\n"
MF"v[56:59],a[96:99],a[4:7],v[56:59]\n"
RD"a[68:71],v226 offset:25600\n"
MF"v[20:23],a[104:107],a[0:3],v[20:23]\n"
MF"v[20:23],a[108:111],a[8:11],v[20:23]\n"
RD"a[72:75],v226 offset:26624\n"
MF"v[60:63],a[108:111],a[12:15],v[60:63]\n"
MF"v[60:63],a[104:107],a[4:7],v[60:63]\n"
WT"lgkmcnt(3)\n"
RD"a[76:79],v226 offset:27648\n"
MF"v[24:27],a[112:115],a[0:3],v[24:27]\n"
MF"v[24:27],a[116:119],a[8:11],v[24:27]\n"
RD"a[80:83],v226 offset:28672\n"
MF"v[64:67],a[116:119],a[12:15],v[64:67]\n"
MF"v[64:67],a[112:115],a[4:7],v[64:67]\n"
RD"a[84:87],v226 offset:29696\n"
MF"v[28:31],a[120:123],a[0:3],v[28:31]\n"
MF"v[28:31],a[124:127],a[8:11],v[28:31]\n"
RD"a[88:91],v226 offset:30720\n"
MF"v[68:71],a[124:127],a[12:15],v[68:71]\n"
MF"v[68:71],a[120:123],a[4:7],v[68:71]\n"
WT"lgkmcnt(3)\n"
RD"a[92:95],v226 offset:31744\n"
MF"v[32:35],a[64:67],a[0:3],v[32:35]\n"
"s_barrier\n"
MF"v[32:35],a[68:71],a[8:11],v[32:35]\n"
RD"a[96:99],v235 offset:0\n"
MF"v[72:75],a[68:71],a[12:15],v[72:75]\n"
MF"v[72:75],a[64:67],a[4:7],v[72:75]\n"
RD"a[100:103],v235 offset:1024\n"
MF"v[36:39],a[72:75],a[0:3],v[36:39]\n"
MF"v[36:39],a[76:79],a[8:11],v[36:39]\n"
RD"a[104:107],v235 offset:2048\n"
MF"v[76:79],a[76:79],a[12:15],v[76:79]\n"
MF"v[76:79],a[72:75],a[4:7],v[76:79]\n"
WT"lgkmcnt(3)\n"
RD"a[108:111],v235 offset:3072\n"
MF"v[0:3],a[80:83],a[16:19],v[0:3]\n"
MF"v[0:3],a[84:87],a[24:27],v[0:3]\n"
RD"a[112:115],v235 offset:4096\n"
MF"v[40:43],a[84:87],a[28:31],v[40:43]\n"
MF"v[40:43],a[80:83],a[20:23],v[40:43]\n"
RD"a[116:119],v235 offset:5120\n"
MF"v[4:7],a[88:91],a[16:19],v[4:7]\n"
MF"v[4:7],a[92:95],a[24:27],v[4:7]\n"
RD"a[120:123],v235 offset:6144\n"
MF"v[44:47],a[92:95],a[28:31],v[44:47]\n"
MF"v[44:47],a[88:91],a[20:23],v[44:47]\n"
WT"lgkmcnt(3)\n"
RD"a[124:127],v235 offset:7168\n"
MF"v[8:11],a[96:99],a[16:19],v[8:11]\n"
MF"v[8:11],a[100:103],a[24:27],v[8:11]\n"
RD"a[64:67],v235 offset:8192\n"
MF"v[48:51],a[100:103],a[28:31],v[48:51]\n"
MF"v[48:51],a[96:99],a[20:23],v[48:51]\n"
RD"a[68:71],v235 offset:9216\n"
MF"v[12:15],a[104:107],a[16:19],v[12:15]\n"
MF"v[12:15],a[108:111],a[24:27],v[12:15]\n"
RD"a[72:75],v235 offset:10240\n"
"v_bfe_u32 v238,v224,4,2\n"
MF"v[52:55],a[108:111],a[28:31],v[52:55]\n"
"s_andn2_b32 s63,s62,255\n"
"v_lshlrev_b32 v238,6,v238\n"
MF"v[52:55],a[104:107],a[20:23],v[52:55]\n"
WT"lgkmcnt(3)\n"
RD"a[76:79],v235 offset:11264\n"
MF"v[16:19],a[112:115],a[16:19],v[16:19]\n"
"buffer_load_dwordx2 v[240:241],v238,s[8:11],s63 offen sc1\n"
MF"v[16:19],a[116:119],a[24:27],v[16:19]\n"
RD"a[80:83],v235 offset:12288\n"
MF"v[56:59],a[116:119],a[28:31],v[56:59]\n"
MF"v[56:59],a[112:115],a[20:23],v[56:59]\n"
RD"a[84:87],v235 offset:13312\n"
MF"v[20:23],a[120:123],a[16:19],v[20:23]\n"
MF"v[20:23],a[124:127],a[24:27],v[20:23]\n"
RD"a[88:91],v235 offset:14336\n"
MF"v[60:63],a[124:127],a[28:31],v[60:63]\n"
MF"v[60:63],a[120:123],a[20:23],v[60:63]\n"
RD"a[92:95],v235 offset:15360\n"
"LTJ_%=:\n"
WT"lgkmcnt(7)\n"
MF"v[24:27],a[64:67],a[16:19],v[24:27]\n"
WT"lgkmcnt(3)\n"
MF"v[24:27],a[68:71],a[24:27],v[24:27]\n"
RD"a[96:99],v235 offset:16384\n"
MF"v[64:67],a[68:71],a[28:31],v[64:67]\n"
MF"v[64:67],a[64:67],a[20:23],v[64:67]\n"
RD"a[100:103],v235 offset:17408\n"
MF"v[28:31],a[72:75],a[16:19],v[28:31]\n"
MF"v[28:31],a[76:79],a[24:27],v[28:31]\n"
RD"a[104:107],v235 offset:18432\n"
MF"v[68:71],a[76:79],a[28:31],v[68:71]\n"
MF"v[68:71],a[72:75],a[20:23],v[68:71]\n"
RD"a[108:111],v235 offset:19456\n"
MF"v[32:35],a[80:83],a[16:19],v[32:35]\n"
WT"lgkmcnt(3)\n"
WT"vmcnt(1)\n"
"s_barrier\n"
MF"v[32:35],a[84:87],a[24:27],v[32:35]\n"
RD"a[112:115],v236 offset:4096\n"
MF"v[72:75],a[84:87],a[28:31],v[72:75]\n"
MF"v[72:75],a[80:83],a[20:23],v[72:75]\n"
RD"a[116:119],v236 offset:5120\n"
MF"v[36:39],a[88:91],a[16:19],v[36:39]\n"
MF"v[36:39],a[92:95],a[24:27],v[36:39]\n"
RD"a[120:123],v236 offset:6144\n"
MF"v[76:79],a[92:95],a[28:31],v[76:79]\n"
MF"v[76:79],a[88:91],a[20:23],v[76:79]\n"
RD"a[124:127],v236 offset:7168\n"
MF"v[0:3],a[96:99],a[180:183],v[0:3]\n"
WT"lgkmcnt(3)\n"
MF"v[0:3],a[100:103],a[188:191],v[0:3]\n"
RD"a[64:67],v236 offset:24576\n"
MF"v[40:43],a[100:103],a[192:195],v[40:43]\n"
MF"v[40:43],a[96:99],a[184:187],v[40:43]\n"
RD"a[68:71],v236 offset:25600\n"
MF"v[4:7],a[104:107],a[180:183],v[4:7]\n"
MF"v[4:7],a[108:111],a[188:191],v[4:7]\n"
RD"a[72:75],v236 offset:26624\n"
MF"v[44:47],a[108:111],a[192:195],v[44:47]\n"
MF"v[44:47],a[104:107],a[184:187],v[44:47]\n"
RD"a[76:79],v236 offset:27648\n"
MF"v[0:3],a[112:115],a[196:199],v[0:3]\n"
WT"lgkmcnt(3)\n"
MF"v[0:3],a[116:119],a[204:207],v[0:3]\n"
RD"a[80:83],v237 offset:12288\n"
MF"v[40:43],a[116:119],a[208:211],v[40:43]\n"
MF"v[40:43],a[112:115],a[200:203],v[40:43]\n"
RD"a[84:87],v237 offset:13312\n"
MF"v[4:7],a[120:123],a[196:199],v[4:7]\n"
MF"v[4:7],a[124:127],a[204:207],v[4:7]\n"
SA"s48,s60,32768\n"
RD"a[88:91],v237 offset:14336\n"
MF"v[44:47],a[124:127],a[208:211],v[44:47]\n"
"s_and_b32 s48,s48,0x18000\n"
MF"v[44:47],a[120:123],a[200:203],v[44:47]\n"
"s_andn2_b32 s49,s60,0x18000\n"
RD"a[92:95],v237 offset:15360\n"
MF"v[0:3],a[64:67],a[212:215],v[0:3]\n"
WT"lgkmcnt(3)\n"
WT"vmcnt(0)\n"
"v_cmp_eq_u64 vcc,s[28:29],v[240:241]\n"
"s_cmp_eq_u64 vcc,exec\n"
"s_cbranch_scc0 L1083_%=\n"
"L1084_%=:\n"
MF"v[0:3],a[68:71],a[220:223],v[0:3]\n"
"s_or_b32 s52,s48,s49\n"
RD"a[96:99],v235 offset:20480\n"
SA"s53,s52,16384\n"
MF"v[40:43],a[68:71],a[224:227],v[40:43]\n"
BL"a[244:247],v224,s[8:11],s52 offen offset:0 sc1\n"
MF"v[40:43],a[64:67],a[216:219],v[40:43]\n"
RD"a[100:103],v235 offset:21504\n"
MF"v[4:7],a[72:75],a[212:215],v[4:7]\n"
MF"v[4:7],a[76:79],a[220:223],v[4:7]\n"
RD"a[104:107],v235 offset:22528\n"
MF"v[44:47],a[76:79],a[224:227],v[44:47]\n"
"v_bfe_u32 v243,v224,6,2\n"
MF"v[44:47],a[72:75],a[216:219],v[44:47]\n"
RD"a[108:111],v235 offset:23552\n"
"v_bfe_u32 v244,v224,4,2\n"
MF"v[0:3],a[80:83],a[228:231],v[0:3]\n"
WT"lgkmcnt(3)\n"
BL"a[248:251],v224,s[8:11],s53 offen offset:0 sc1\n"
MF"v[0:3],a[84:87],a[236:239],v[0:3]\n"
RD"a[112:115],v236 offset:8192\n"
"v_lshrrev_b32 v245,8,v224\n"
MF"v[40:43],a[84:87],a[240:243],v[40:43]\n"
"v_lshlrev_b32 v245,3,v245\n"
"v_sub_u32 v244,v244,v245\n"
MF"v[40:43],a[80:83],a[232:235],v[40:43]\n"
RD"a[116:119],v236 offset:9216\n"
"v_and_b32 v245,16,v224\n"
MF"v[4:7],a[88:91],a[228:231],v[4:7]\n"
"v_mov_b32 v246,0x3f80\n"
MF"v[4:7],a[92:95],a[236:239],v[4:7]\n"
RD"a[120:123],v236 offset:10240\n"
"v_lshlrev_b32 v245,v245,v246\n"
MF"v[44:47],a[92:95],a[240:243],v[44:47]\n"
BL"a[252:255],v224,s[8:11],s52 offen offset:1024 sc1\n"
MF"v[44:47],a[88:91],a[232:235],v[44:47]\n"
RD"a[124:127],v236 offset:11264\n"
"v_lshlrev_b32 v246,4,v243\n"
MF"v[8:11],a[96:99],a[180:183],v[8:11]\n"
WT"lgkmcnt(3)\n"
"v_and_b32 v246,16,v246\n"
"v_and_b32 v247,2,v243\n"
MF"v[8:11],a[100:103],a[188:191],v[8:11]\n"
SA"s90,s88,0\n"
RD"a[64:67],v236 offset:28672\n"
PK"v0,v0,v1\n"
MF"v[48:51],a[100:103],a[192:195],v[48:51]\n"
BL"a[128:131],v224,s[8:11],s53 offen offset:1024 sc1\n"
MF"v[48:51],a[96:99],a[184:187],v[48:51]\n"
RD"a[68:71],v236 offset:29696\n"
PK"v1,v2,v3\n"
MF"v[12:15],a[104:107],a[180:183],v[12:15]\n"
"v_lshl_add_u32 v246,v247,1,v246\n"
PK"v2,v4,v5\n"
MF"v[12:15],a[108:111],a[188:191],v[12:15]\n"
RD"a[72:75],v236 offset:30720\n"
PK"v3,v6,v7\n"
"v_add3_u32 v246,v246,v244,0\n"
MF"v[52:55],a[108:111],a[192:195],v[52:55]\n"
BS"v[0:3],v224,s[8:11],s90 offen sc1\n"
"v_ashrrev_i32 v246,1,v246\n"
MF"v[52:55],a[104:107],a[184:187],v[52:55]\n"
RD"a[76:79],v236 offset:31744\n"
"v_cmp_eq_u32_e64 vcc,0,v246\n"
MF"v[8:11],a[112:115],a[196:199],v[8:11]\n"
WT"lgkmcnt(3)\n"
BL"a[132:135],v224,s[8:11],s52 offen offset:2048 sc1\n"
"v_cndmask_b32_e64 v80,0,v245,vcc\n"
MF"v[8:11],a[116:119],a[204:207],v[8:11]\n"
SA"s91,s22,0\n"
RD"a[80:83],v237 offset:16384\n"
"v_cmp_eq_u32_e64 s[92:93],1,v246\n"
MF"v[48:51],a[116:119],a[208:211],v[48:51]\n"
"v_cmp_eq_u32_e64 s[94:95],2,v246\n"
"v_cndmask_b32_e64 v81,0,v245,s[92:93]\n"
MF"v[48:51],a[112:115],a[200:203],v[48:51]\n"
RD"a[84:87],v237 offset:17408\n"
PK"v40,v40,v41\n"
MF"v[12:15],a[120:123],a[196:199],v[12:15]\n"
PK"v41,v42,v43\n"
"v_cndmask_b32_e64 v82,0,v245,s[94:95]\n"
MF"v[12:15],a[124:127],a[204:207],v[12:15]\n"
RD"a[88:91],v237 offset:18432\n"
PK"v42,v44,v45\n"
PK"v43,v46,v47\n"
MF"v[52:55],a[124:127],a[208:211],v[52:55]\n"
BS"v[40:43],v224,s[8:11],s91 offen sc1\n"
"v_cmp_eq_u32_e64 vcc,3,v246\n"
MF"v[52:55],a[120:123],a[200:203],v[52:55]\n"
BL"a[136:139],v224,s[8:11],s53 offen offset:2048 sc1\n"
"v_cndmask_b32_e64 v83,0,v245,vcc\n"
MF"v[8:11],a[64:67],a[212:215],v[8:11]\n"
WT"lgkmcnt(2)\n"
RD"a[92:95],v237 offset:19456\n"
"v_lshlrev_b32 v246,4,v243\n"
"v_and_b32 v246,16,v246\n"
MF"v[8:11],a[68:71],a[220:223],v[8:11]\n"
RD"a[96:99],v235 offset:24576\n"
"v_and_b32 v247,2,v243\n"
MF"v[48:51],a[68:71],a[224:227],v[48:51]\n"
"v_lshl_add_u32 v246,v247,1,v246\n"
"v_add3_u32 v246,v246,v244,8\n"
MF"v[48:51],a[64:67],a[216:219],v[48:51]\n"
RD"a[100:103],v235 offset:25600\n"
"v_ashrrev_i32 v246,1,v246\n"
MF"v[12:15],a[72:75],a[212:215],v[12:15]\n"
BL"a[140:143],v224,s[8:11],s52 offen offset:3072 sc1\n"
MF"v[12:15],a[76:79],a[220:223],v[12:15]\n"
RD"a[104:107],v235 offset:26624\n"
"v_cmp_eq_u32_e64 vcc,0,v246\n"
MF"v[52:55],a[76:79],a[224:227],v[52:55]\n"
"v_cmp_eq_u32_e64 s[92:93],1,v246\n"
"v_cndmask_b32_e64 v88,0,v245,vcc\n"
MF"v[52:55],a[72:75],a[216:219],v[52:55]\n"
RD"a[108:111],v235 offset:27648\n"
"v_cndmask_b32_e64 v89,0,v245,s[92:93]\n"
MF"v[8:11],a[80:83],a[228:231],v[8:11]\n"
WT"lgkmcnt(3)\n"
BL"a[144:147],v224,s[8:11],s53 offen offset:3072 sc1\n"
MF"v[8:11],a[84:87],a[236:239],v[8:11]\n"
SA"s48,s60,65536\n"
RD"a[112:115],v236 offset:12288\n"
"v_cmp_eq_u32_e64 s[94:95],2,v246\n"
MF"v[48:51],a[84:87],a[240:243],v[48:51]\n"
"s_and_b32 s48,s48,0x18000\n"
"v_cndmask_b32_e64 v90,0,v245,s[94:95]\n"
"v_cmp_eq_u32_e64 vcc,3,v246\n"
MF"v[48:51],a[80:83],a[232:235],v[48:51]\n"
"s_andn2_b32 s49,s60,0x18000\n"
RD"a[116:119],v236 offset:13312\n"
"v_cndmask_b32_e64 v91,0,v245,vcc\n"
MF"v[12:15],a[88:91],a[228:231],v[12:15]\n"
"s_or_b32 s52,s48,s49\n"
"v_lshl_add_u32 v246,v243,3,v244\n"
"v_add_u32 v246,0,v246\n"
MF"v[12:15],a[92:95],a[236:239],v[12:15]\n"
SA"s53,s52,16384\n"
RD"a[120:123],v236 offset:14336\n"
"v_ashrrev_i32 v246,1,v246\n"
MF"v[52:55],a[92:95],a[240:243],v[52:55]\n"
BL"a[148:151],v224,s[8:11],s52 offen offset:0 sc1\n"
MF"v[52:55],a[88:91],a[232:235],v[52:55]\n"
RD"a[124:127],v236 offset:15360\n"
"v_cmp_eq_u32_e64 vcc,0,v246\n"
MF"v[16:19],a[96:99],a[180:183],v[16:19]\n"
WT"lgkmcnt(3)\n"
"v_cndmask_b32_e64 v96,0,v245,vcc\n"
"v_cmp_eq_u32_e64 s[92:93],1,v246\n"
MF"v[16:19],a[100:103],a[188:191],v[16:19]\n"
SA"s90,s88,1024\n"
RD"a[64:67],v237 offset:0\n"
"v_cndmask_b32_e64 v97,0,v245,s[92:93]\n"
MF"v[56:59],a[100:103],a[192:195],v[56:59]\n"
PK"v8,v8,v9\n"
PK"v9,v10,v11\n"
MF"v[56:59],a[96:99],a[184:187],v[56:59]\n"
RD"a[68:71],v237 offset:1024\n"
"v_cmp_eq_u32_e64 s[94:95],2,v246\n"
MF"v[20:23],a[104:107],a[180:183],v[20:23]\n"
BL"a[152:155],v224,s[8:11],s53 offen offset:0 sc1\n"
MF"v[20:23],a[108:111],a[188:191],v[20:23]\n"
RD"a[72:75],v237 offset:2048\n"
PK"v10,v12,v13\n"
PK"v11,v14,v15\n"
MF"v[60:63],a[108:111],a[192:195],v[60:63]\n"
BS"v[8:11],v224,s[8:11],s90 offen sc1\n"
"v_cndmask_b32_e64 v98,0,v245,s[94:95]\n"
MF"v[60:63],a[104:107],a[184:187],v[60:63]\n"
RD"a[76:79],v237 offset:3072\n"
"v_cmp_eq_u32_e64 vcc,3,v246\n"
MF"v[16:19],a[112:115],a[196:199],v[16:19]\n"
WT"lgkmcnt(3)\n"
"v_cndmask_b32_e64 v99,0,v245,vcc\n"
"v_lshl_add_u32 v246,v243,3,v244\n"
MF"v[16:19],a[116:119],a[204:207],v[16:19]\n"
SA"s91,s22,1024\n"
RD"a[80:83],v237 offset:20480\n"
"v_add_u32 v246,4,v246\n"
MF"v[56:59],a[116:119],a[208:211],v[56:59]\n"
BL"a[156:159],v224,s[8:11],s52 offen offset:1024 sc1\n"
"v_ashrrev_i32 v246,1,v246\n"
MF"v[56:59],a[112:115],a[200:203],v[56:59]\n"
RD"a[84:87],v237 offset:21504\n"
"v_cmp_eq_u32_e64 vcc,0,v246\n"
MF"v[20:23],a[120:123],a[196:199],v[20:23]\n"
PK"v48,v48,v49\n"
PK"v49,v50,v51\n"
"v_cndmask_b32_e64 v104,0,v245,vcc\n"
MF"v[20:23],a[124:127],a[204:207],v[20:23]\n"
RD"a[88:91],v237 offset:22528\n"
PK"v50,v52,v53\n"
PK"v51,v54,v55\n"
MF"v[60:63],a[124:127],a[208:211],v[60:63]\n"
BS"v[48:51],v224,s[8:11],s91 offen sc1\n"
"v_cmp_eq_u32_e64 s[92:93],1,v246\n"
MF"v[60:63],a[120:123],a[200:203],v[60:63]\n"
BL"a[160:163],v224,s[8:11],s53 offen offset:1024 sc1\n"
"v_cndmask_b32_e64 v105,0,v245,s[92:93]\n"
MF"v[16:19],a[64:67],a[212:215],v[16:19]\n"
WT"lgkmcnt(2)\n"
RD"a[92:95],v237 offset:23552\n"
"v_cmp_eq_u32_e64 s[94:95],2,v246\n"
MF"v[16:19],a[68:71],a[220:223],v[16:19]\n"
"s_mov_b32 s12,s64\n"
RD"a[96:99],v235 offset:28672\n"
"v_cndmask_b32_e64 v106,0,v245,s[94:95]\n"
"v_cmp_eq_u32_e64 vcc,3,v246\n"
MF"v[56:59],a[68:71],a[224:227],v[56:59]\n"
"s_and_b32 s13,s65,0xffff\n"
"v_cndmask_b32_e64 v107,0,v245,vcc\n"
"s_mov_b32 s14,2621440\n"
MF"v[56:59],a[64:67],a[216:219],v[56:59]\n"
"s_mov_b32 s15,0x20000\n"
RD"a[100:103],v235 offset:29696\n"
MF"v[20:23],a[72:75],a[212:215],v[20:23]\n"
"s_mov_b32 s16,s86\n"
BL"a[164:167],v224,s[8:11],s52 offen offset:2048 sc1\n"
MF"v[20:23],a[76:79],a[220:223],v[20:23]\n"
"s_and_b32 s17,s87,0xffff\n"
RD"a[104:107],v235 offset:30720\n"
MF"v[60:63],a[76:79],a[224:227],v[60:63]\n"
"s_mov_b32 s18,2621440\n"
MF"v[60:63],a[72:75],a[216:219],v[60:63]\n"
"s_mov_b32 s19,0x20000\n"
RD"a[108:111],v235 offset:31744\n"
MF"v[16:19],a[80:83],a[228:231],v[16:19]\n"
WT"lgkmcnt(3)\n"
"v_bfe_u32 v243,v224,4,4\n"
MF"v[16:19],a[84:87],a[236:239],v[16:19]\n"
"s_lshl_b32 s92,s34,3\n"
RD"a[112:115],v236 offset:16384\n"
MF"v[56:59],a[84:87],a[240:243],v[56:59]\n"
SA"s93,s92,s32\n"
BL"a[168:171],v224,s[8:11],s53 offen offset:2048 sc1\n"
MF"v[56:59],a[80:83],a[232:235],v[56:59]\n"
"s_sub_u32 s92,s32,s92\n"
RD"a[116:119],v236 offset:17408\n"
MF"v[20:23],a[88:91],a[228:231],v[20:23]\n"
SA"s92,s92,120\n"
MF"v[20:23],a[92:95],a[236:239],v[20:23]\n"
RD"a[120:123],v236 offset:18432\n"
"v_lshl_add_u32 v244,s93,4,v243\n"
MF"v[60:63],a[92:95],a[240:243],v[60:63]\n"
"v_lshl_add_u32 v245,s92,4,v243\n"
MF"v[60:63],a[88:91],a[232:235],v[60:63]\n"
"s_mul_i32 s93,s93,20480\n"
RD"a[124:127],v236 offset:19456\n"
MF"v[24:27],a[96:99],a[180:183],v[24:27]\n"
WT"lgkmcnt(3)\n"
BL"a[172:175],v224,s[8:11],s52 offen offset:3072 sc1\n"
MF"v[24:27],a[100:103],a[188:191],v[24:27]\n"
"s_mul_i32 s92,s92,20480\n"
RD"a[64:67],v237 offset:4096\n"
PK"v16,v16,v17\n"
MF"v[64:67],a[100:103],a[192:195],v[64:67]\n"
SA"s90,s88,2048\n"
PK"v17,v18,v19\n"
PK"v18,v20,v21\n"
MF"v[64:67],a[96:99],a[184:187],v[64:67]\n"
SA"s93,s93,452608\n"
RD"a[68:71],v237 offset:5120\n"
PK"v19,v22,v23\n"
MF"v[28:31],a[104:107],a[180:183],v[28:31]\n"
SA"s92,s92,452608\n"
BL"a[176:179],v224,s[8:11],s53 offen offset:3072 sc1\n"
MF"v[28:31],a[108:111],a[188:191],v[28:31]\n"
SA"s48,s60,98304\n"
BS"v[16:19],v224,s[8:11],s90 offen sc1\n"
MF"v[68:71],a[108:111],a[192:195],v[68:71]\n"
"s_and_b32 s48,s48,0x18000\n"
RD"a[72:75],v237 offset:6144\n"
"v_add_u32 v178,s93,v224\n"
MF"v[68:71],a[104:107],a[184:187],v[68:71]\n"
"s_andn2_b32 s49,s60,0x18000\n"
RD"a[76:79],v237 offset:7168\n"
"v_add_u32 v179,s92,v224\n"
MF"v[24:27],a[112:115],a[196:199],v[24:27]\n"
WT"lgkmcnt(3)\n"
"v_lshrrev_b32 v243,4,v224\n"
"v_and_b32 v243,48,v243\n"
MF"v[24:27],a[116:119],a[204:207],v[24:27]\n"
"s_or_b32 s52,s48,s49\n"
RD"a[80:83],v237 offset:24576\n"
"v_mul_u32_u24 v176,1280,v244\n"
MF"v[64:67],a[116:119],a[208:211],v[64:67]\n"
SA"s53,s52,16384\n"
BL"a[32:35],v224,s[8:11],s52 offen offset:0 sc1\n"
MF"v[64:67],a[112:115],a[200:203],v[64:67]\n"
SA"s91,s22,2048\n"
RD"a[84:87],v237 offset:25600\n"
"v_mul_u32_u24 v177,1280,v245\n"
MF"v[28:31],a[120:123],a[196:199],v[28:31]\n"
PK"v56,v56,v57\n"
PK"v57,v58,v59\n"
"v_add_u32 v176,v176,v243\n"
MF"v[28:31],a[124:127],a[204:207],v[28:31]\n"
RD"a[88:91],v237 offset:26624\n"
PK"v58,v60,v61\n"
PK"v59,v62,v63\n"
MF"v[68:71],a[124:127],a[208:211],v[68:71]\n"
BS"v[56:59],v224,s[8:11],s91 offen sc1\n"
"v_add_u32 v177,v177,v243\n"
MF"v[68:71],a[120:123],a[200:203],v[68:71]\n"
RD"a[92:95],v237 offset:27648\n"
"v_and_b32 v243,0x300,v224\n"
"v_sub_u32 v244,2047,v244\n"
MF"v[24:27],a[64:67],a[212:215],v[24:27]\n"
WT"lgkmcnt(3)\n"
BL"a[36:39],v224,s[8:11],s53 offen offset:0 sc1\n"
"v_sub_u32 v245,2047,v245\n"
MF"v[24:27],a[68:71],a[220:223],v[24:27]\n"
RD"a[96:99],v236 offset:0\n"
"v_readlane_b32 s92,v234,0\n"
MF"v[64:67],a[68:71],a[224:227],v[64:67]\n"
"s_and_b32 s92,s92,0xffff\n"
"v_cmp_eq_u32 vcc,s92,v244\n"
"v_cmp_eq_u32_e64 s[94:95],s92,v245\n"
MF"v[64:67],a[64:67],a[216:219],v[64:67]\n"
RD"a[100:103],v236 offset:1024\n"
"v_cndmask_b32 v178,v178,v243,vcc\n"
MF"v[28:31],a[72:75],a[212:215],v[28:31]\n"
"v_cndmask_b32_e64 v179,v179,v243,s[94:95]\n"
"v_add_u32 v243,16,v243\n"
MF"v[28:31],a[76:79],a[220:223],v[28:31]\n"
RD"a[104:107],v236 offset:2048\n"
"v_readlane_b32 s92,v234,1\n"
MF"v[68:71],a[76:79],a[224:227],v[68:71]\n"
"s_and_b32 s92,s92,0xffff\n"
BL"a[40:43],v224,s[8:11],s52 offen offset:1024 sc1\n"
MF"v[68:71],a[72:75],a[216:219],v[68:71]\n"
RD"a[108:111],v236 offset:3072\n"
"v_cmp_eq_u32 vcc,s92,v244\n"
MF"v[24:27],a[80:83],a[228:231],v[24:27]\n"
WT"lgkmcnt(3)\n"
"v_cmp_eq_u32_e64 s[94:95],s92,v245\n"
MF"v[24:27],a[84:87],a[236:239],v[24:27]\n"
RD"a[112:115],v236 offset:20480\n"
"v_cndmask_b32 v178,v178,v243,vcc\n"
MF"v[64:67],a[84:87],a[240:243],v[64:67]\n"
BL"a[44:47],v224,s[8:11],s53 offen offset:1024 sc1\n"
MF"v[64:67],a[80:83],a[232:235],v[64:67]\n"
RD"a[116:119],v236 offset:21504\n"
"v_cndmask_b32_e64 v179,v179,v243,s[94:95]\n"
MF"v[28:31],a[88:91],a[228:231],v[28:31]\n"
"v_add_u32 v243,16,v243\n"
"v_readlane_b32 s92,v234,2\n"
MF"v[28:31],a[92:95],a[236:239],v[28:31]\n"
"s_and_b32 s92,s92,0xffff\n"
RD"a[120:123],v236 offset:22528\n"
MF"v[68:71],a[92:95],a[240:243],v[68:71]\n"
"v_cmp_eq_u32 vcc,s92,v244\n"
MF"v[68:71],a[88:91],a[232:235],v[68:71]\n"
RD"a[124:127],v236 offset:23552\n"
"v_cmp_eq_u32_e64 s[94:95],s92,v245\n"
MF"v[32:35],a[96:99],a[180:183],v[32:35]\n"
WT"lgkmcnt(3)\n"
BL"a[48:51],v224,s[8:11],s52 offen offset:2048 sc1\n"
MF"v[32:35],a[100:103],a[188:191],v[32:35]\n"
SA"s90,s88,3072\n"
RD"a[64:67],v237 offset:8192\n"
"v_cndmask_b32 v178,v178,v243,vcc\n"
MF"v[72:75],a[100:103],a[192:195],v[72:75]\n"
PK"v24,v24,v25\n"
PK"v25,v26,v27\n"
MF"v[72:75],a[96:99],a[184:187],v[72:75]\n"
RD"a[68:71],v237 offset:9216\n"
"v_cndmask_b32_e64 v179,v179,v243,s[94:95]\n"
MF"v[36:39],a[104:107],a[180:183],v[36:39]\n"
PK"v26,v28,v29\n"
PK"v27,v30,v31\n"
MF"v[36:39],a[108:111],a[188:191],v[36:39]\n"
BS"v[24:27],v224,s[8:11],s90 offen sc1\n"
"v_add_u32 v243,16,v243\n"
MF"v[76:79],a[108:111],a[192:195],v[76:79]\n"
RD"a[72:75],v237 offset:10240\n"
"v_readlane_b32 s92,v234,3\n"
MF"v[76:79],a[104:107],a[184:187],v[76:79]\n"
"s_and_b32 s92,s92,0xffff\n"
BL"a[52:55],v224,s[8:11],s53 offen offset:2048 sc1\n"
MF"v[32:35],a[112:115],a[196:199],v[32:35]\n"
WT"lgkmcnt(2)\n"
RD"a[76:79],v237 offset:11264\n"
"v_cmp_eq_u32 vcc,s92,v244\n"
MF"v[32:35],a[116:119],a[204:207],v[32:35]\n"
SA"s91,s22,3072\n"
RD"a[80:83],v237 offset:28672\n"
"v_cmp_eq_u32_e64 s[94:95],s92,v245\n"
MF"v[72:75],a[116:119],a[208:211],v[72:75]\n"
"v_cndmask_b32 v178,v178,v243,vcc\n"
"v_cndmask_b32_e64 v179,v179,v243,s[94:95]\n"
MF"v[72:75],a[112:115],a[200:203],v[72:75]\n"
RD"a[84:87],v237 offset:29696\n"
PK"v64,v64,v65\n"
MF"v[36:39],a[120:123],a[196:199],v[36:39]\n"
BL"a[56:59],v224,s[8:11],s52 offen offset:3072 sc1\n"
PK"v65,v66,v67\n"
MF"v[36:39],a[124:127],a[204:207],v[36:39]\n"
RD"a[88:91],v237 offset:30720\n"
"v_add_u32 v243,16,v243\n"
PK"v66,v68,v69\n"
MF"v[76:79],a[124:127],a[208:211],v[76:79]\n"
PK"v67,v70,v71\n"
"v_readlane_b32 s92,v234,4\n"
MF"v[76:79],a[120:123],a[200:203],v[76:79]\n"
"s_and_b32 s92,s92,0xffff\n"
BS"v[64:67],v224,s[8:11],s91 offen sc1\n"
"v_cmp_eq_u32 vcc,s92,v244\n"
MF"v[32:35],a[64:67],a[212:215],v[32:35]\n"
WT"lgkmcnt(2)\n"
RD"a[92:95],v237 offset:31744\n"
"v_cmp_eq_u32_e64 s[94:95],s92,v245\n"
MF"v[32:35],a[68:71],a[220:223],v[32:35]\n"
BL"a[60:63],v224,s[8:11],s53 offen offset:3072 sc1\n"
"v_cndmask_b32 v178,v178,v243,vcc\n"
MF"v[72:75],a[68:71],a[224:227],v[72:75]\n"
RD"a[96:99],v226 offset:8192\n"
"v_cndmask_b32_e64 v179,v179,v243,s[94:95]\n"
MF"v[72:75],a[64:67],a[216:219],v[72:75]\n"
RD"a[100:103],v226 offset:9216\n"
"v_add_u32 v243,16,v243\n"
MF"v[36:39],a[72:75],a[212:215],v[36:39]\n"
"v_readlane_b32 s92,v234,5\n"
"s_and_b32 s92,s92,0xffff\n"
MF"v[36:39],a[76:79],a[220:223],v[36:39]\n"
RD"a[104:107],v226 offset:10240\n"
"v_cmp_eq_u32 vcc,s92,v244\n"
MF"v[76:79],a[76:79],a[224:227],v[76:79]\n"
"v_cmp_eq_u32_e64 s[94:95],s92,v245\n"
"v_cndmask_b32 v178,v178,v243,vcc\n"
MF"v[76:79],a[72:75],a[216:219],v[76:79]\n"
RD"a[108:111],v226 offset:11264\n"
"v_cndmask_b32_e64 v179,v179,v243,s[94:95]\n"
MF"v[32:35],a[80:83],a[228:231],v[32:35]\n"
WT"lgkmcnt(4)\n"
"v_add_u32 v243,16,v243\n"
"v_readlane_b32 s92,v234,6\n"
MF"v[32:35],a[84:87],a[236:239],v[32:35]\n"
"s_and_b32 s92,s92,0xffff\n"
RD"a[112:115],v226 offset:28672\n"
MF"v[72:75],a[84:87],a[240:243],v[72:75]\n"
"v_cmp_eq_u32 vcc,s92,v244\n"
MF"v[72:75],a[80:83],a[232:235],v[72:75]\n"
RD"a[116:119],v226 offset:29696\n"
"v_cmp_eq_u32_e64 s[94:95],s92,v245\n"
MF"v[36:39],a[88:91],a[228:231],v[36:39]\n"
"v_cndmask_b32 v178,v178,v243,vcc\n"
MF"v[36:39],a[92:95],a[236:239],v[36:39]\n"
RD"a[120:123],v226 offset:30720\n"
"v_cndmask_b32_e64 v179,v179,v243,s[94:95]\n"
MF"v[76:79],a[92:95],a[240:243],v[76:79]\n"
"v_add_u32 v243,16,v243\n"
MF"v[76:79],a[88:91],a[232:235],v[76:79]\n"
WT"vmcnt(31) lgkmcnt(3)\n"
RD"a[124:127],v226 offset:31744\n"
"v_readlane_b32 s92,v234,7\n"
MF"v[84:87],a[96:99],a[244:247],v[84:87]\n"
WT"vmcnt(29)\n"
"s_and_b32 s92,s92,0xffff\n"
MF"v[84:87],a[100:103],a[252:255],v[84:87]\n"
WT"vmcnt(28)\n"
RD"a[64:67],v235 offset:16384\n"
PK"v32,v32,v33\n"
MF"v[164:167],a[100:103],a[128:131],v[164:167]\n"
SA"s90,s88,4096\n"
PK"v33,v34,v35\n"
"v_cmp_eq_u32 vcc,s92,v244\n"
MF"v[164:167],a[96:99],a[248:251],v[164:167]\n"
RD"a[68:71],v235 offset:17408\n"
PK"v34,v36,v37\n"
MF"v[92:95],a[104:107],a[244:247],v[92:95]\n"
PK"v35,v38,v39\n"
"v_cmp_eq_u32_e64 s[94:95],s92,v245\n"
MF"v[92:95],a[108:111],a[252:255],v[92:95]\n"
"s_mul_i32 s92,s33,5\n"
BS"v[32:35],v224,s[8:11],s90 offen sc1\n"
MF"v[172:175],a[108:111],a[128:131],v[172:175]\n"
SA"s93,s92,s30\n"
RD"a[72:75],v235 offset:18432\n"
"v_cndmask_b32 v178,v178,v243,vcc\n"
MF"v[172:175],a[104:107],a[248:251],v[172:175]\n"
WT"vmcnt(27) lgkmcnt(3)\n"
RD"a[76:79],v235 offset:19456\n"
"v_cndmask_b32_e64 v179,v179,v243,s[94:95]\n"
MF"v[84:87],a[112:115],a[132:135],v[84:87]\n"
WT"vmcnt(24)\n"
SA"s92,s92,4\n"
"s_lshl_b32 s38,s93,6\n"
MF"v[84:87],a[116:119],a[140:143],v[84:87]\n"
WT"vmcnt(23)\n"
RD"a[80:83],v236 offset:4096\n"
PK"v72,v72,v73\n"
MF"v[164:167],a[116:119],a[144:147],v[164:167]\n"
SA"s91,s22,4096\n"
PK"v73,v74,v75\n"
PK"v74,v76,v77\n"
MF"v[164:167],a[112:115],a[136:139],v[164:167]\n"
"s_lshl_b32 s55,s92,6\n"
RD"a[84:87],v236 offset:5120\n"
PK"v75,v78,v79\n"
MF"v[92:95],a[120:123],a[132:135],v[92:95]\n"
SA"s55,s55,s45\n"
BS"v[72:75],v224,s[8:11],s91 offen sc1\n"
MF"v[92:95],a[124:127],a[140:143],v[92:95]\n"
"s_lshl_b32 s36,s93,10\n"
RD"a[88:91],v236 offset:6144\n"
"s_lshl_b32 s37,s92,10\n"
MF"v[172:175],a[124:127],a[144:147],v[172:175]\n"
SA"s36,s36,71680\n"
SA"s37,s37,71680\n"
MF"v[172:175],a[120:123],a[136:139],v[172:175]\n"
WT"vmcnt(23) lgkmcnt(3)\n"
RD"a[92:95],v236 offset:7168\n"
SA"s37,s37,s45\n"
MF"v[84:87],a[64:67],a[148:151],v[84:87]\n"
WT"vmcnt(20)\n"
"v_lshrrev_b32 v243,1,v224\n"
MF"v[84:87],a[68:71],a[156:159],v[84:87]\n"
WT"vmcnt(18)\n"
RD"a[96:99],v236 offset:24576\n"
"v_min_u32 v244,312,v243\n"
MF"v[164:167],a[68:71],a[160:163],v[164:167]\n"
"v_and_b32 v243,56,v243\n"
MF"v[164:167],a[64:67],a[152:155],v[164:167]\n"
RD"a[100:103],v236 offset:25600\n"
"v_lshlrev_b32 v243,3,v243\n"
MF"v[92:95],a[72:75],a[148:151],v[92:95]\n"
"s_mov_b32 s92,6400\n"
MF"v[92:95],a[76:79],a[156:159],v[92:95]\n"
RD"a[104:107],v236 offset:26624\n"
MF"v[172:175],a[76:79],a[160:163],v[172:175]\n"
MF"v[172:175],a[72:75],a[152:155],v[172:175]\n"
WT"vmcnt(17) lgkmcnt(3)\n"
RD"a[108:111],v236 offset:27648\n"
MF"v[84:87],a[80:83],a[164:167],v[84:87]\n"
WT"vmcnt(15)\n"
MF"v[84:87],a[84:87],a[172:175],v[84:87]\n"
WT"vmcnt(14)\n"
RD"a[112:115],v237 offset:12288\n"
MF"v[164:167],a[84:87],a[176:179],v[164:167]\n"
MF"v[164:167],a[80:83],a[168:171],v[164:167]\n"
RD"a[116:119],v237 offset:13312\n"
MF"v[92:95],a[88:91],a[164:167],v[92:95]\n"
MF"v[92:95],a[92:95],a[172:175],v[92:95]\n"
RD"a[120:123],v237 offset:14336\n"
MF"v[172:175],a[92:95],a[176:179],v[172:175]\n"
MF"v[172:175],a[88:91],a[168:171],v[172:175]\n"
WT"vmcnt(12) lgkmcnt(3)\n"
RD"a[124:127],v237 offset:15360\n"
MF"v[84:87],a[96:99],a[32:35],v[84:87]\n"
WT"vmcnt(9)\n"
MF"v[84:87],a[100:103],a[40:43],v[84:87]\n"
WT"vmcnt(8)\n"
RD"a[64:67],v226 offset:12288\n"
MF"v[164:167],a[100:103],a[44:47],v[164:167]\n"
MF"v[164:167],a[96:99],a[36:39],v[164:167]\n"
RD"a[68:71],v226 offset:13312\n"
MF"v[92:95],a[104:107],a[32:35],v[92:95]\n"
MF"v[92:95],a[108:111],a[40:43],v[92:95]\n"
RD"a[72:75],v226 offset:14336\n"
MF"v[172:175],a[108:111],a[44:47],v[172:175]\n"
MF"v[172:175],a[104:107],a[36:39],v[172:175]\n"
WT"vmcnt(7) lgkmcnt(3)\n"
RD"a[76:79],v226 offset:15360\n"
MF"v[84:87],a[112:115],a[48:51],v[84:87]\n"
WT"vmcnt(4)\n"
"buffer_load_dwordx2 v[240:241],v244,s[8:11],s92 offen sc1\n"
MF"v[84:87],a[116:119],a[56:59],v[84:87]\n"
WT"vmcnt(3)\n"
RD"a[80:83],v235 offset:0\n"
MF"v[164:167],a[116:119],a[60:63],v[164:167]\n"
MF"v[164:167],a[112:115],a[52:55],v[164:167]\n"
RD"a[84:87],v235 offset:1024\n"
MF"v[92:95],a[120:123],a[48:51],v[92:95]\n"
MF"v[92:95],a[124:127],a[56:59],v[92:95]\n"
RD"a[88:91],v235 offset:2048\n"
MF"v[172:175],a[124:127],a[60:63],v[172:175]\n"
MF"v[172:175],a[120:123],a[52:55],v[172:175]\n"
WT"lgkmcnt(3)\n"
RD"a[92:95],v235 offset:3072\n"
MF"v[100:103],a[64:67],a[244:247],v[100:103]\n"
MF"v[100:103],a[68:71],a[252:255],v[100:103]\n"
SA"s90,s88,327680\n"
RD"a[96:99],v235 offset:20480\n"
PK"v84,v84,v85\n"
MF"v[180:183],a[68:71],a[128:131],v[180:183]\n"
PK"v85,v86,v87\n"
PK"v86,v92,v93\n"
MF"v[180:183],a[64:67],a[248:251],v[180:183]\n"
RD"a[100:103],v235 offset:21504\n"
PK"v87,v94,v95\n"
MF"v[108:111],a[72:75],a[244:247],v[108:111]\n"
BS"v[84:87],v224,s[8:11],s90 offen sc1\n"
MF"v[108:111],a[76:79],a[252:255],v[108:111]\n"
RD"a[104:107],v235 offset:22528\n"
MF"v[188:191],a[76:79],a[128:131],v[188:191]\n"
MF"v[188:191],a[72:75],a[248:251],v[188:191]\n"
WT"lgkmcnt(3)\n"
RD"a[108:111],v235 offset:23552\n"
MF"v[100:103],a[80:83],a[132:135],v[100:103]\n"
MF"v[100:103],a[84:87],a[140:143],v[100:103]\n"
SA"s91,s22,327680\n"
RD"a[112:115],v236 offset:8192\n"
PK"v164,v164,v165\n"
MF"v[180:183],a[84:87],a[144:147],v[180:183]\n"
PK"v165,v166,v167\n"
PK"v166,v172,v173\n"
MF"v[180:183],a[80:83],a[136:139],v[180:183]\n"
RD"a[116:119],v236 offset:9216\n"
PK"v167,v174,v175\n"
MF"v[108:111],a[88:91],a[132:135],v[108:111]\n"
BS"v[164:167],v224,s[8:11],s91 offen sc1\n"
MF"v[108:111],a[92:95],a[140:143],v[108:111]\n"
RD"a[120:123],v236 offset:10240\n"
MF"v[188:191],a[92:95],a[144:147],v[188:191]\n"
MF"v[188:191],a[88:91],a[136:139],v[188:191]\n"
WT"lgkmcnt(3)\n"
RD"a[124:127],v236 offset:11264\n"
MF"v[100:103],a[96:99],a[148:151],v[100:103]\n"
MF"v[100:103],a[100:103],a[156:159],v[100:103]\n"
RD"a[64:67],v236 offset:28672\n"
MF"v[180:183],a[100:103],a[160:163],v[180:183]\n"
WT"vmcnt(3)\n"
"buffer_store_dwordx2 v[232:233],v239,s[8:11],s89 offen sc1\n"
MF"v[180:183],a[96:99],a[152:155],v[180:183]\n"
RD"a[68:71],v236 offset:29696\n"
MF"v[108:111],a[104:107],a[148:151],v[108:111]\n"
MF"v[108:111],a[108:111],a[156:159],v[108:111]\n"
RD"a[72:75],v236 offset:30720\n"
MF"v[188:191],a[108:111],a[160:163],v[188:191]\n"
MF"v[188:191],a[104:107],a[152:155],v[188:191]\n"
WT"lgkmcnt(3)\n"
RD"a[76:79],v236 offset:31744\n"
MF"v[100:103],a[112:115],a[164:167],v[100:103]\n"
MF"v[100:103],a[116:119],a[172:175],v[100:103]\n"
RD"a[80:83],v237 offset:16384\n"
MF"v[180:183],a[116:119],a[176:179],v[180:183]\n"
MF"v[180:183],a[112:115],a[168:171],v[180:183]\n"
RD"a[84:87],v237 offset:17408\n"
MF"v[108:111],a[120:123],a[164:167],v[108:111]\n"
MF"v[108:111],a[124:127],a[172:175],v[108:111]\n"
RD"a[88:91],v237 offset:18432\n"
MF"v[188:191],a[124:127],a[176:179],v[188:191]\n"
MF"v[188:191],a[120:123],a[168:171],v[188:191]\n"
WT"lgkmcnt(3)\n"
RD"a[92:95],v237 offset:19456\n"
MF"v[100:103],a[64:67],a[32:35],v[100:103]\n"
MF"v[100:103],a[68:71],a[40:43],v[100:103]\n"
RD"a[96:99],v226 offset:16384\n"
MF"v[180:183],a[68:71],a[44:47],v[180:183]\n"
MF"v[180:183],a[64:67],a[36:39],v[180:183]\n"
RD"a[100:103],v226 offset:17408\n"
MF"v[108:111],a[72:75],a[32:35],v[108:111]\n"
MF"v[108:111],a[76:79],a[40:43],v[108:111]\n"
RD"a[104:107],v226 offset:18432\n"
MF"v[188:191],a[76:79],a[44:47],v[188:191]\n"
MF"v[188:191],a[72:75],a[36:39],v[188:191]\n"
WT"lgkmcnt(3)\n"
RD"a[108:111],v226 offset:19456\n"
MF"v[100:103],a[80:83],a[48:51],v[100:103]\n"
MF"v[100:103],a[84:87],a[56:59],v[100:103]\n"
RD"a[112:115],v235 offset:4096\n"
MF"v[180:183],a[84:87],a[60:63],v[180:183]\n"
MF"v[180:183],a[80:83],a[52:55],v[180:183]\n"
RD"a[116:119],v235 offset:5120\n"
MF"v[108:111],a[88:91],a[48:51],v[108:111]\n"
MF"v[108:111],a[92:95],a[56:59],v[108:111]\n"
RD"a[120:123],v235 offset:6144\n"
MF"v[188:191],a[92:95],a[60:63],v[188:191]\n"
MF"v[188:191],a[88:91],a[52:55],v[188:191]\n"
WT"lgkmcnt(3)\n"
RD"a[124:127],v235 offset:7168\n"
MF"v[116:119],a[96:99],a[244:247],v[116:119]\n"
MF"v[116:119],a[100:103],a[252:255],v[116:119]\n"
SA"s90,s88,328704\n"
RD"a[64:67],v235 offset:24576\n"
PK"v100,v100,v101\n"
MF"v[196:199],a[100:103],a[128:131],v[196:199]\n"
PK"v101,v102,v103\n"
PK"v102,v108,v109\n"
MF"v[196:199],a[96:99],a[248:251],v[196:199]\n"
RD"a[68:71],v235 offset:25600\n"
PK"v103,v110,v111\n"
MF"v[124:127],a[104:107],a[244:247],v[124:127]\n"
BS"v[100:103],v224,s[8:11],s90 offen sc1\n"
MF"v[124:127],a[108:111],a[252:255],v[124:127]\n"
WT"vmcnt(4)\n"
"v_cmp_eq_u64 vcc,s[28:29],v[240:241]\n"
"s_cmp_eq_u64 vcc,exec\n"
"s_cbranch_scc0 L1086_%=\n"
"L1087_%=:\n"
MF"v[204:207],a[108:111],a[128:131],v[204:207]\n"
RD"a[72:75],v235 offset:26624\n"
MF"v[204:207],a[104:107],a[248:251],v[204:207]\n"
WT"lgkmcnt(3)\n"
RD"a[76:79],v235 offset:27648\n"
MF"v[116:119],a[112:115],a[132:135],v[116:119]\n"
BL"v[112:115],v176,s[12:15],s38 offen\n"
MF"v[116:119],a[116:119],a[140:143],v[116:119]\n"
SA"s91,s22,328704\n"
RD"a[80:83],v236 offset:12288\n"
PK"v180,v180,v181\n"
MF"v[196:199],a[116:119],a[144:147],v[196:199]\n"
BL"v[144:147],v178,s[8:11],s36 offen sc1\n"
MF"v[196:199],a[112:115],a[136:139],v[196:199]\n"
RD"a[84:87],v236 offset:13312\n"
PK"v181,v182,v183\n"
MF"v[124:127],a[120:123],a[132:135],v[124:127]\n"
PK"v182,v188,v189\n"
PK"v183,v190,v191\n"
MF"v[124:127],a[124:127],a[140:143],v[124:127]\n"
BS"v[180:183],v224,s[8:11],s91 offen sc1\n"
MF"v[204:207],a[124:127],a[144:147],v[204:207]\n"
RD"a[88:91],v236 offset:14336\n"
MF"v[204:207],a[120:123],a[136:139],v[204:207]\n"
WT"lgkmcnt(3)\n"
BL"v[120:123],v176,s[12:15],s55 offen\n"
MF"v[116:119],a[64:67],a[148:151],v[116:119]\n"
RD"a[92:95],v236 offset:15360\n"
MF"v[116:119],a[68:71],a[156:159],v[116:119]\n"
BL"v[152:155],v178,s[8:11],s37 offen sc1\n"
MF"v[196:199],a[68:71],a[160:163],v[196:199]\n"
RD"a[96:99],v237 offset:0\n"
MF"v[196:199],a[64:67],a[152:155],v[196:199]\n"
RD"a[100:103],v237 offset:1024\n"
MF"v[124:127],a[72:75],a[148:151],v[124:127]\n"
BL"v[128:131],v177,s[12:15],s38 offen\n"
MF"v[124:127],a[76:79],a[156:159],v[124:127]\n"
RD"a[104:107],v237 offset:2048\n"
MF"v[204:207],a[76:79],a[160:163],v[204:207]\n"
BL"v[160:163],v179,s[8:11],s36 offen sc1\n"
MF"v[204:207],a[72:75],a[152:155],v[204:207]\n"
WT"lgkmcnt(3)\n"
RD"a[108:111],v237 offset:3072\n"
MF"v[116:119],a[80:83],a[164:167],v[116:119]\n"
MF"v[116:119],a[84:87],a[172:175],v[116:119]\n"
RD"a[112:115],v237 offset:20480\n"
MF"v[196:199],a[84:87],a[176:179],v[196:199]\n"
BL"v[136:139],v177,s[12:15],s55 offen\n"
MF"v[196:199],a[80:83],a[168:171],v[196:199]\n"
RD"a[116:119],v237 offset:21504\n"
MF"v[124:127],a[88:91],a[164:167],v[124:127]\n"
BL"v[168:171],v179,s[8:11],s37 offen sc1\n"
MF"v[124:127],a[92:95],a[172:175],v[124:127]\n"
RD"a[120:123],v237 offset:22528\n"
MF"v[204:207],a[92:95],a[176:179],v[204:207]\n"
MF"v[204:207],a[88:91],a[168:171],v[204:207]\n"
WT"lgkmcnt(3)\n"
RD"a[124:127],v237 offset:23552\n"
MF"v[116:119],a[96:99],a[32:35],v[116:119]\n"
MF"v[116:119],a[100:103],a[40:43],v[116:119]\n"
RD"a[64:67],v226 offset:20480\n"
MF"v[196:199],a[100:103],a[44:47],v[196:199]\n"
"s_lshr_b32 s93,s31,3\n"
MF"v[196:199],a[96:99],a[36:39],v[196:199]\n"
"s_lshl_b32 s93,s93,4\n"
RD"a[68:71],v226 offset:21504\n"
MF"v[124:127],a[104:107],a[32:35],v[124:127]\n"
SA"s93,s93,s32\n"
MF"v[124:127],a[108:111],a[40:43],v[124:127]\n"
"s_mul_i32 s94,s93,40960\n"
RD"a[72:75],v226 offset:22528\n"
MF"v[204:207],a[108:111],a[44:47],v[204:207]\n"
"s_lshl_b32 s93,s93,9\n"
MF"v[204:207],a[104:107],a[36:39],v[204:207]\n"
WT"lgkmcnt(3)\n"
RD"a[76:79],v226 offset:23552\n"
SA"s93,s93,131072\n"
MF"v[116:119],a[112:115],a[48:51],v[116:119]\n"
SA"s94,s94,59506688\n"
MF"v[116:119],a[116:119],a[56:59],v[116:119]\n"
"s_lshl_b32 s92,s30,10\n"
RD"a[80:83],v235 offset:8192\n"
MF"v[196:199],a[116:119],a[60:63],v[196:199]\n"
"v_add_u32 v244,s92,v224\n"
MF"v[196:199],a[112:115],a[52:55],v[196:199]\n"
SA"s92,s45,4096\n"
RD"a[84:87],v235 offset:9216\n"
MF"v[124:127],a[120:123],a[48:51],v[124:127]\n"
"v_add_u32 v245,s92,v224\n"
MF"v[124:127],a[124:127],a[56:59],v[124:127]\n"
RD"a[88:91],v235 offset:10240\n"
MF"v[204:207],a[124:127],a[60:63],v[204:207]\n"
"v_and_b32 v246,128,v224\n"
MF"v[204:207],a[120:123],a[52:55],v[204:207]\n"
WT"lgkmcnt(3)\n"
RD"a[92:95],v235 offset:11264\n"
"v_lshl_add_u32 v243,v246,5,v243\n"
MF"v[132:135],a[64:67],a[244:247],v[132:135]\n"
"v_lshrrev_b32 v246,8,v224\n"
MF"v[132:135],a[68:71],a[252:255],v[132:135]\n"
SA"s90,s88,329728\n"
RD"a[96:99],v235 offset:28672\n"
PK"v116,v116,v117\n"
MF"v[208:211],a[68:71],a[128:131],v[208:211]\n"
"s_mov_b32 s95,0xffff0101\n"
PK"v117,v118,v119\n"
"v_lshl_add_u32 v243,v246,27,v243\n"
MF"v[208:211],a[64:67],a[248:251],v[208:211]\n"
"s_lshl_b32 s92,s30,10\n"
RD"a[100:103],v235 offset:29696\n"
PK"v118,v124,v125\n"
MF"v[140:143],a[72:75],a[244:247],v[140:143]\n"
PK"v119,v126,v127\n"
"v_add_u32 v247,s92,v227\n"
MF"v[140:143],a[76:79],a[252:255],v[140:143]\n"
BS"v[116:119],v224,s[8:11],s90 offen sc1\n"
MF"v[212:215],a[76:79],a[128:131],v[212:215]\n"
RD"a[104:107],v235 offset:30720\n"
MF"v[212:215],a[72:75],a[248:251],v[212:215]\n"
WT"lgkmcnt(3)\n"
"buffer_load_dwordx2 v[240:241],v243,s[8:11],s93 offen sc1\n"
MF"v[132:135],a[80:83],a[132:135],v[132:135]\n"
RD"a[108:111],v235 offset:31744\n"
MF"v[132:135],a[84:87],a[140:143],v[132:135]\n"
SA"s91,s22,329728\n"
RD"a[112:115],v236 offset:16384\n"
PK"v196,v196,v197\n"
MF"v[208:211],a[84:87],a[144:147],v[208:211]\n"
PK"v197,v198,v199\n"
PK"v198,v204,v205\n"
MF"v[208:211],a[80:83],a[136:139],v[208:211]\n"
RD"a[116:119],v236 offset:17408\n"
PK"v199,v206,v207\n"
MF"v[140:143],a[88:91],a[132:135],v[140:143]\n"
BS"v[196:199],v224,s[8:11],s91 offen sc1\n"
MF"v[140:143],a[92:95],a[140:143],v[140:143]\n"
RD"a[120:123],v236 offset:18432\n"
MF"v[212:215],a[92:95],a[144:147],v[212:215]\n"
MF"v[212:215],a[88:91],a[136:139],v[212:215]\n"
WT"lgkmcnt(3)\n"
RD"a[124:127],v236 offset:19456\n"
MF"v[132:135],a[96:99],a[148:151],v[132:135]\n"
MF"v[132:135],a[100:103],a[156:159],v[132:135]\n"
RD"a[64:67],v237 offset:4096\n"
MF"v[208:211],a[100:103],a[160:163],v[208:211]\n"
MF"v[208:211],a[96:99],a[152:155],v[208:211]\n"
RD"a[68:71],v237 offset:5120\n"
MF"v[140:143],a[104:107],a[148:151],v[140:143]\n"
MF"v[140:143],a[108:111],a[156:159],v[140:143]\n"
RD"a[72:75],v237 offset:6144\n"
MF"v[212:215],a[108:111],a[160:163],v[212:215]\n"
MF"v[212:215],a[104:107],a[152:155],v[212:215]\n"
WT"lgkmcnt(3)\n"
RD"a[76:79],v237 offset:7168\n"
MF"v[132:135],a[112:115],a[164:167],v[132:135]\n"
MF"v[132:135],a[116:119],a[172:175],v[132:135]\n"
RD"a[80:83],v237 offset:24576\n"
MF"v[208:211],a[116:119],a[176:179],v[208:211]\n"
MF"v[208:211],a[112:115],a[168:171],v[208:211]\n"
RD"a[84:87],v237 offset:25600\n"
MF"v[140:143],a[120:123],a[164:167],v[140:143]\n"
MF"v[140:143],a[124:127],a[172:175],v[140:143]\n"
RD"a[88:91],v237 offset:26624\n"
MF"v[212:215],a[124:127],a[176:179],v[212:215]\n"
MF"v[212:215],a[120:123],a[168:171],v[212:215]\n"
WT"lgkmcnt(3)\n"
RD"a[92:95],v237 offset:27648\n"
MF"v[132:135],a[64:67],a[32:35],v[132:135]\n"
MF"v[132:135],a[68:71],a[40:43],v[132:135]\n"
RD"a[96:99],v226 offset:24576\n"
MF"v[208:211],a[68:71],a[44:47],v[208:211]\n"
MF"v[208:211],a[64:67],a[36:39],v[208:211]\n"
RD"a[100:103],v226 offset:25600\n"
MF"v[140:143],a[72:75],a[32:35],v[140:143]\n"
MF"v[140:143],a[76:79],a[40:43],v[140:143]\n"
RD"a[104:107],v226 offset:26624\n"
MF"v[212:215],a[76:79],a[44:47],v[212:215]\n"
MF"v[212:215],a[72:75],a[36:39],v[212:215]\n"
WT"lgkmcnt(3)\n"
RD"a[108:111],v226 offset:27648\n"
MF"v[132:135],a[80:83],a[48:51],v[132:135]\n"
MF"v[132:135],a[84:87],a[56:59],v[132:135]\n"
RD"a[112:115],v235 offset:12288\n"
MF"v[208:211],a[84:87],a[60:63],v[208:211]\n"
WT"vmcnt(1)\n"
"v_cmp_eq_u64 vcc,s[28:29],v[240:241]\n"
MF"v[208:211],a[80:83],a[52:55],v[208:211]\n"
"s_and_b32 s50,vcc_lo,0xff\n"
RD"a[116:119],v235 offset:13312\n"
"s_or_b32 s95,s95,s50\n"
MF"v[140:143],a[88:91],a[48:51],v[140:143]\n"
"s_bitcmp1_b32 s95,1\n"
"s_cselect_b32 s51,s94,1065353216\n"
MF"v[140:143],a[92:95],a[56:59],v[140:143]\n"
SA"s92,s51,5120\n"
RD"a[120:123],v235 offset:14336\n"
MF"v[212:215],a[92:95],a[60:63],v[212:215]\n"
BL"a[184:187],v244,s[8:11],s92 offen sc1\n"
MF"v[212:215],a[88:91],a[52:55],v[212:215]\n"
WT"lgkmcnt(3)\n"
RD"a[124:127],v235 offset:15360\n"
MF"v[148:151],a[96:99],a[244:247],v[148:151]\n"
BL"a[216:219],v245,s[8:11],s92 offen sc1\n"
MF"v[148:151],a[100:103],a[252:255],v[148:151]\n"
SA"s90,s88,330752\n"
RD"a[64:67],v236 offset:0\n"
PK"v132,v132,v133\n"
MF"v[216:219],a[100:103],a[128:131],v[216:219]\n"
"s_bitcmp1_b32 s95,2\n"
PK"v133,v134,v135\n"
PK"v134,v140,v141\n"
MF"v[216:219],a[96:99],a[248:251],v[216:219]\n"
"s_cselect_b32 s51,s94,1065353216\n"
RD"a[68:71],v236 offset:1024\n"
PK"v135,v142,v143\n"
MF"v[156:159],a[104:107],a[244:247],v[156:159]\n"
SA"s92,s51,10240\n"
BS"v[132:135],v224,s[8:11],s90 offen sc1\n"
MF"v[156:159],a[108:111],a[252:255],v[156:159]\n"
BL"a[188:191],v244,s[8:11],s92 offen sc1\n"
MF"v[220:223],a[108:111],a[128:131],v[220:223]\n"
RD"a[72:75],v236 offset:2048\n"
MF"v[220:223],a[104:107],a[248:251],v[220:223]\n"
WT"lgkmcnt(3)\n"
BL"a[220:223],v245,s[8:11],s92 offen sc1\n"
MF"v[148:151],a[112:115],a[132:135],v[148:151]\n"
"s_bitcmp1_b32 s95,3\n"
RD"a[76:79],v236 offset:3072\n"
"s_cselect_b32 s51,s94,1065353216\n"
MF"v[148:151],a[116:119],a[140:143],v[148:151]\n"
SA"s91,s22,330752\n"
RD"a[80:83],v236 offset:20480\n"
PK"v208,v208,v209\n"
MF"v[216:219],a[116:119],a[144:147],v[216:219]\n"
SA"s92,s51,15360\n"
PK"v209,v210,v211\n"
PK"v210,v212,v213\n"
MF"v[216:219],a[112:115],a[136:139],v[216:219]\n"
BL"a[192:195],v244,s[8:11],s92 offen sc1\n"
MF"v[156:159],a[120:123],a[132:135],v[156:159]\n"
RD"a[84:87],v236 offset:21504\n"
PK"v211,v214,v215\n"
MF"v[156:159],a[124:127],a[140:143],v[156:159]\n"
BS"v[208:211],v224,s[8:11],s91 offen sc1\n"
MF"v[220:223],a[124:127],a[144:147],v[220:223]\n"
"s_bitcmp1_b32 s95,4\n"
BL"a[224:227],v245,s[8:11],s92 offen sc1\n"
MF"v[220:223],a[120:123],a[136:139],v[220:223]\n"
WT"lgkmcnt(2)\n"
RD"a[88:91],v236 offset:22528\n"
RD"a[92:95],v236 offset:23552\n"
MF"v[148:151],a[64:67],a[148:151],v[148:151]\n"
"s_cselect_b32 s51,s94,1065353216\n"
SA"s92,s51,20480\n"
MF"v[148:151],a[68:71],a[156:159],v[148:151]\n"
BL"a[196:199],v244,s[8:11],s92 offen sc1\n"
MF"v[216:219],a[68:71],a[160:163],v[216:219]\n"
RD"a[96:99],v237 offset:8192\n"
MF"v[216:219],a[64:67],a[152:155],v[216:219]\n"
BL"a[228:231],v245,s[8:11],s92 offen sc1\n"
MF"v[156:159],a[72:75],a[148:151],v[156:159]\n"
"s_bitcmp1_b32 s95,5\n"
RD"a[100:103],v237 offset:9216\n"
"s_cselect_b32 s51,s94,1065353216\n"
MF"v[156:159],a[76:79],a[156:159],v[156:159]\n"
SA"s92,s51,25600\n"
RD"a[104:107],v237 offset:10240\n"
MF"v[220:223],a[76:79],a[160:163],v[220:223]\n"
BL"a[200:203],v244,s[8:11],s92 offen sc1\n"
MF"v[220:223],a[72:75],a[152:155],v[220:223]\n"
WT"lgkmcnt(3)\n"
RD"a[108:111],v237 offset:11264\n"
MF"v[148:151],a[80:83],a[164:167],v[148:151]\n"
BL"a[232:235],v245,s[8:11],s92 offen sc1\n"
MF"v[148:151],a[84:87],a[172:175],v[148:151]\n"
RD"a[112:115],v237 offset:28672\n"
MF"v[216:219],a[84:87],a[176:179],v[216:219]\n"
"s_bitcmp1_b32 s95,6\n"
"s_cselect_b32 s51,s94,1065353216\n"
MF"v[216:219],a[80:83],a[168:171],v[216:219]\n"
SA"s92,s51,30720\n"
RD"a[116:119],v237 offset:29696\n"
MF"v[156:159],a[88:91],a[164:167],v[156:159]\n"
BL"a[204:207],v244,s[8:11],s92 offen sc1\n"
MF"v[156:159],a[92:95],a[172:175],v[156:159]\n"
RD"a[120:123],v237 offset:30720\n"
MF"v[220:223],a[92:95],a[176:179],v[220:223]\n"
BL"a[236:239],v245,s[8:11],s92 offen sc1\n"
MF"v[220:223],a[88:91],a[168:171],v[220:223]\n"
WT"lgkmcnt(3)\n"
RD"a[124:127],v237 offset:31744\n"
MF"v[148:151],a[96:99],a[32:35],v[148:151]\n"
"s_bitcmp1_b32 s95,7\n"
"s_cselect_b32 s51,s94,1065353216\n"
MF"v[148:151],a[100:103],a[40:43],v[148:151]\n"
SA"s92,s51,35840\n"
MF"v[216:219],a[100:103],a[44:47],v[216:219]\n"
BL"a[208:211],v244,s[8:11],s92 offen sc1\n"
MF"v[216:219],a[96:99],a[36:39],v[216:219]\n"
MF"v[156:159],a[104:107],a[32:35],v[156:159]\n"
BL"a[240:243],v245,s[8:11],s92 offen sc1\n"
MF"v[156:159],a[108:111],a[40:43],v[156:159]\n"
MF"v[220:223],a[108:111],a[44:47],v[220:223]\n"
MF"v[220:223],a[104:107],a[36:39],v[220:223]\n"
WT"lgkmcnt(0)\n"
MF"v[148:151],a[112:115],a[48:51],v[148:151]\n"
MF"v[148:151],a[116:119],a[56:59],v[148:151]\n"
MF"v[216:219],a[116:119],a[60:63],v[216:219]\n"
MF"v[216:219],a[112:115],a[52:55],v[216:219]\n"
MF"v[156:159],a[120:123],a[48:51],v[156:159]\n"
MF"v[156:159],a[124:127],a[56:59],v[156:159]\n"
MF"v[220:223],a[124:127],a[60:63],v[220:223]\n"
MF"v[220:223],a[120:123],a[52:55],v[220:223]\n"
"s_nop 5\n"
PK"v148,v148,v149\n"
PK"v149,v150,v151\n"
PK"v150,v156,v157\n"
PK"v151,v158,v159\n"
SA"s90,s88,331776\n"
BS"v[148:151],v224,s[8:11],s90 offen sc1\n"
PK"v216,v216,v217\n"
PK"v217,v218,v219\n"
PK"v218,v220,v221\n"
PK"v219,v222,v223\n"
SA"s91,s22,331776\n"
BS"v[216:219],v224,s[8:11],s91 offen sc1\n"
"s_cmp_eq_u32 s23,0\n"
"s_cbranch_scc0 L1089_%=\n"
"ds_write_b128 v227,v[0:3] offset:0\n"
"ds_write_b128 v227,v[8:11] offset:1024\n"
"ds_write_b128 v227,v[16:19] offset:2048\n"
"ds_write_b128 v227,v[24:27] offset:3072\n"
"ds_write_b128 v227,v[32:35] offset:4096\n"
"ds_write_b128 v227,v[84:87] offset:5120\n"
"ds_write_b128 v227,v[100:103] offset:6144\n"
"ds_write_b128 v227,v[116:119] offset:7168\n"
"ds_write_b128 v227,v[132:135] offset:8192\n"
"ds_write_b128 v227,v[148:151] offset:9216\n"
"L1089_%=:\n"
"s_cmp_eq_u32 s23,4\n"
"s_cbranch_scc0 L1090_%=\n"
"ds_write_b128 v227,v[40:43] offset:0\n"
"ds_write_b128 v227,v[48:51] offset:1024\n"
"ds_write_b128 v227,v[56:59] offset:2048\n"
"ds_write_b128 v227,v[64:67] offset:3072\n"
"ds_write_b128 v227,v[72:75] offset:4096\n"
"ds_write_b128 v227,v[164:167] offset:5120\n"
"ds_write_b128 v227,v[180:183] offset:6144\n"
"ds_write_b128 v227,v[196:199] offset:7168\n"
"ds_write_b128 v227,v[208:211] offset:8192\n"
"ds_write_b128 v227,v[216:219] offset:9216\n"
"L1090_%=:\n"
MF"v[4:7],v[96:99],v[112:115],0\n"
MF"v[12:15],v[104:107],v[112:115],0\n"
MF"v[20:23],v[96:99],v[120:123],0\n"
MF"v[28:31],v[104:107],v[120:123],0\n"
MF"v[4:7],v[96:99],v[144:147],v[4:7]\n"
MF"v[12:15],v[104:107],v[144:147],v[12:15]\n"
MF"v[20:23],v[96:99],v[152:155],v[20:23]\n"
MF"v[28:31],v[104:107],v[152:155],v[28:31]\n"
MF"v[36:39],v[96:99],v[128:131],0\n"
MF"v[44:47],v[104:107],v[128:131],0\n"
MF"v[52:55],v[96:99],v[136:139],0\n"
MF"v[60:63],v[104:107],v[136:139],0\n"
MF"v[36:39],v[96:99],v[160:163],v[36:39]\n"
MF"v[44:47],v[104:107],v[160:163],v[44:47]\n"
MF"v[52:55],v[96:99],v[168:171],v[52:55]\n"
MF"v[60:63],v[104:107],v[168:171],v[60:63]\n"
SA"s89,s89,4096\n"
"s_mov_b32 s54,1048576\n"
WT"vmcnt(0)\n"
"buffer_store_dwordx2 v[232:233],v239,s[8:11],s89 offen sc1\n"
"buffer_load_dwordx2 v[240:241],v243,s[8:11],s93 offen sc1\n"
WT"lgkmcnt(0)\n"
"s_barrier\n"
RD"a[180:183],v247 offset:0\n"
RD"a[212:215],v227 offset:4096\n"
RD"a[64:67],v247 offset:5120\n"
RD"a[96:99],v227 offset:9216\n"
"s_getpc_b64 s[52:53]\n"
"L1097_%=:\n"
SA"s52,s52,L1096_%=-L1097_%=-32\n"
"s_addc_u32 s53,s53,0\n"
"s_mov_b32 s89,0\n"
WT"lgkmcnt(0)\n"
MF"v[4:7],v[80:83],a[180:183],v[4:7]\n"
MF"v[12:15],v[88:91],a[180:183],v[12:15]\n"
MF"v[20:23],v[80:83],a[212:215],v[20:23]\n"
MF"v[28:31],v[88:91],a[212:215],v[28:31]\n"
MF"v[36:39],v[80:83],a[64:67],v[36:39]\n"
MF"v[44:47],v[88:91],a[64:67],v[44:47]\n"
MF"v[52:55],v[80:83],a[96:99],v[52:55]\n"
MF"v[60:63],v[88:91],a[96:99],v[60:63]\n"
"s_branch L1092_%=\n"
"L1094_%=:\n"
"s_sleep 3\n"
"buffer_load_dwordx2 v[240:241],v243,s[8:11],s93 offen sc1\n"
"L1091_%=:\n"
WT"vmcnt(0)\n"
"v_cmp_eq_u64 vcc,s[28:29],v[240:241]\n"
"s_sub_u32 s54,s54,1\n"
"s_cselect_b32 s90,-1,vcc_lo\n"
"s_andn2_b32 s90,s90,s95\n"
"s_cbranch_scc0 L1094_%=\n"
"s_or_b32 s95,s95,s90\n"
"L1098_%=:\n"
"s_ff1_i32_b32 s50,s90\n"
"s_bitset0_b32 s90,s50\n"
"s_lshl_b32 s50,s50,5\n"
SA"s50,s50,s52\n"
"s_addc_u32 s51,s53,0\n"
"s_setpc_b64 s[50:51]\n"
"L1096_%=:\n"
SA"s92,s94,5120\n"
BL"a[184:187],v244,s[8:11],s92 offen sc1\n"
BL"a[216:219],v245,s[8:11],s92 offen sc1\n"
"s_branch L1093_%=\n"
"s_nop 0\n"
SA"s92,s94,10240\n"
BL"a[188:191],v244,s[8:11],s92 offen sc1\n"
BL"a[220:223],v245,s[8:11],s92 offen sc1\n"
"s_branch L1093_%=\n"
"s_nop 0\n"
SA"s92,s94,15360\n"
BL"a[192:195],v244,s[8:11],s92 offen sc1\n"
BL"a[224:227],v245,s[8:11],s92 offen sc1\n"
"s_branch L1093_%=\n"
"s_nop 0\n"
SA"s92,s94,20480\n"
BL"a[196:199],v244,s[8:11],s92 offen sc1\n"
BL"a[228:231],v245,s[8:11],s92 offen sc1\n"
"s_branch L1093_%=\n"
"s_nop 0\n"
SA"s92,s94,25600\n"
BL"a[200:203],v244,s[8:11],s92 offen sc1\n"
BL"a[232:235],v245,s[8:11],s92 offen sc1\n"
"s_branch L1093_%=\n"
"s_nop 0\n"
SA"s92,s94,30720\n"
BL"a[204:207],v244,s[8:11],s92 offen sc1\n"
BL"a[236:239],v245,s[8:11],s92 offen sc1\n"
"s_branch L1093_%=\n"
"s_nop 0\n"
SA"s92,s94,35840\n"
BL"a[208:211],v244,s[8:11],s92 offen sc1\n"
BL"a[240:243],v245,s[8:11],s92 offen sc1\n"
"s_branch L1093_%=\n"
"s_nop 0\n"
"s_nop 0\n"
"s_nop 0\n"
"s_nop 0\n"
"s_nop 0\n"
"s_nop 0\n"
"s_nop 0\n"
"s_nop 0\n"
"s_nop 0\n"
SA"s92,s94,332800\n"
BL"a[68:71],v244,s[8:11],s92 offen sc1\n"
BL"a[100:103],v245,s[8:11],s92 offen sc1\n"
"s_branch L1093_%=\n"
"s_nop 0\n"
SA"s92,s94,337920\n"
BL"a[72:75],v244,s[8:11],s92 offen sc1\n"
BL"a[104:107],v245,s[8:11],s92 offen sc1\n"
"s_branch L1093_%=\n"
"s_nop 0\n"
SA"s92,s94,343040\n"
BL"a[76:79],v244,s[8:11],s92 offen sc1\n"
BL"a[108:111],v245,s[8:11],s92 offen sc1\n"
"s_branch L1093_%=\n"
"s_nop 0\n"
SA"s92,s94,348160\n"
BL"a[80:83],v244,s[8:11],s92 offen sc1\n"
BL"a[112:115],v245,s[8:11],s92 offen sc1\n"
"s_branch L1093_%=\n"
"s_nop 0\n"
SA"s92,s94,353280\n"
BL"a[84:87],v244,s[8:11],s92 offen sc1\n"
BL"a[116:119],v245,s[8:11],s92 offen sc1\n"
"s_branch L1093_%=\n"
"s_nop 0\n"
SA"s92,s94,358400\n"
BL"a[88:91],v244,s[8:11],s92 offen sc1\n"
BL"a[120:123],v245,s[8:11],s92 offen sc1\n"
"s_branch L1093_%=\n"
"s_nop 0\n"
SA"s92,s94,363520\n"
BL"a[92:95],v244,s[8:11],s92 offen sc1\n"
BL"a[124:127],v245,s[8:11],s92 offen sc1\n"
"s_branch L1093_%=\n"
"s_nop 0\n"
"L1093_%=:\n"
"s_cmp_lg_u32 s90,0\n"
"s_cbranch_scc1 L1098_%=\n"
"s_and_b32 s92,s95,s89\n"
"s_cmp_eq_u32 s92,-1\n"
"s_cbranch_scc1 L1095_%=\n"
"buffer_load_dwordx2 v[240:241],v243,s[8:11],s93 offen sc1\n"
WT"vmcnt(1)\n"
"L1092_%=:\n"
"s_or_b32 s92,s95,0xff00\n"
"s_andn2_b32 s92,s92,s89\n"
"s_cmp_eq_u32 s92,-1\n"
"s_cbranch_scc0 L1091_%=\n"
MF"v[4:7],v[80:83],a[184:187],v[4:7]\n"
MF"v[12:15],v[88:91],a[184:187],v[12:15]\n"
MF"v[4:7],v[80:83],a[188:191],v[4:7]\n"
MF"v[12:15],v[88:91],a[188:191],v[12:15]\n"
MF"v[4:7],v[80:83],a[192:195],v[4:7]\n"
MF"v[12:15],v[88:91],a[192:195],v[12:15]\n"
MF"v[4:7],v[80:83],a[196:199],v[4:7]\n"
MF"v[12:15],v[88:91],a[196:199],v[12:15]\n"
MF"v[4:7],v[80:83],a[200:203],v[4:7]\n"
MF"v[12:15],v[88:91],a[200:203],v[12:15]\n"
MF"v[4:7],v[80:83],a[204:207],v[4:7]\n"
MF"v[12:15],v[88:91],a[204:207],v[12:15]\n"
MF"v[4:7],v[80:83],a[208:211],v[4:7]\n"
MF"v[12:15],v[88:91],a[208:211],v[12:15]\n"
MF"v[20:23],v[80:83],a[216:219],v[20:23]\n"
MF"v[28:31],v[88:91],a[216:219],v[28:31]\n"
MF"v[20:23],v[80:83],a[220:223],v[20:23]\n"
MF"v[28:31],v[88:91],a[220:223],v[28:31]\n"
MF"v[20:23],v[80:83],a[224:227],v[20:23]\n"
MF"v[28:31],v[88:91],a[224:227],v[28:31]\n"
MF"v[20:23],v[80:83],a[228:231],v[20:23]\n"
MF"v[28:31],v[88:91],a[228:231],v[28:31]\n"
PK"v4,v4,v5\n"
PK"v5,v6,v7\n"
PK"v6,v12,v13\n"
PK"v7,v14,v15\n"
BS"v[4:7],v176,s[16:19],s38 offen\n"
MF"v[20:23],v[80:83],a[232:235],v[20:23]\n"
MF"v[28:31],v[88:91],a[232:235],v[28:31]\n"
MF"v[20:23],v[80:83],a[236:239],v[20:23]\n"
MF"v[28:31],v[88:91],a[236:239],v[28:31]\n"
MF"v[20:23],v[80:83],a[240:243],v[20:23]\n"
MF"v[28:31],v[88:91],a[240:243],v[28:31]\n"
"s_nop 7\n"
PK"v20,v20,v21\n"
PK"v21,v22,v23\n"
PK"v22,v28,v29\n"
PK"v23,v30,v31\n"
BS"v[20:23],v176,s[16:19],s55 offen\n"
"s_mov_b32 s89,-1\n"
"s_cmp_eq_u32 s95,-1\n"
"s_cbranch_scc0 L1091_%=\n"
"L1095_%=:\n"
WT"vmcnt(0)\n"
MF"v[36:39],v[80:83],a[68:71],v[36:39]\n"
MF"v[44:47],v[88:91],a[68:71],v[44:47]\n"
MF"v[36:39],v[80:83],a[72:75],v[36:39]\n"
MF"v[44:47],v[88:91],a[72:75],v[44:47]\n"
MF"v[36:39],v[80:83],a[76:79],v[36:39]\n"
MF"v[44:47],v[88:91],a[76:79],v[44:47]\n"
MF"v[36:39],v[80:83],a[80:83],v[36:39]\n"
MF"v[44:47],v[88:91],a[80:83],v[44:47]\n"
MF"v[36:39],v[80:83],a[84:87],v[36:39]\n"
MF"v[44:47],v[88:91],a[84:87],v[44:47]\n"
MF"v[36:39],v[80:83],a[88:91],v[36:39]\n"
MF"v[44:47],v[88:91],a[88:91],v[44:47]\n"
MF"v[36:39],v[80:83],a[92:95],v[36:39]\n"
MF"v[44:47],v[88:91],a[92:95],v[44:47]\n"
MF"v[52:55],v[80:83],a[100:103],v[52:55]\n"
MF"v[60:63],v[88:91],a[100:103],v[60:63]\n"
MF"v[52:55],v[80:83],a[104:107],v[52:55]\n"
MF"v[60:63],v[88:91],a[104:107],v[60:63]\n"
MF"v[52:55],v[80:83],a[108:111],v[52:55]\n"
MF"v[60:63],v[88:91],a[108:111],v[60:63]\n"
MF"v[52:55],v[80:83],a[112:115],v[52:55]\n"
MF"v[60:63],v[88:91],a[112:115],v[60:63]\n"
PK"v36,v36,v37\n"
PK"v37,v38,v39\n"
PK"v38,v44,v45\n"
PK"v39,v46,v47\n"
BS"v[36:39],v177,s[16:19],s38 offen\n"
MF"v[52:55],v[80:83],a[116:119],v[52:55]\n"
MF"v[60:63],v[88:91],a[116:119],v[60:63]\n"
MF"v[52:55],v[80:83],a[120:123],v[52:55]\n"
MF"v[60:63],v[88:91],a[120:123],v[60:63]\n"
MF"v[52:55],v[80:83],a[124:127],v[52:55]\n"
MF"v[60:63],v[88:91],a[124:127],v[60:63]\n"
"s_nop 7\n"
PK"v52,v52,v53\n"
PK"v53,v54,v55\n"
PK"v54,v60,v61\n"
PK"v55,v62,v63\n"
BS"v[52:55],v177,s[16:19],s55 offen\n"
WT"vmcnt(0) lgkmcnt(0)\n"
"s_endpgm\n"
"L1004_%=:\n"
"s_mov_b32 s54,0\n"
"L1006_%=:\n"
"s_sleep 2\n"
BL"v[160:163],v224,s[8:11],0 offen offset:256 sc1\n"
BL"v[164:167],v224,s[8:11],0 offen offset:1280 sc1\n"
WT"vmcnt(0)\n"
SA"s54,s54,1\n"
"v_cmp_eq_u64 vcc,s[28:29],v[160:161]\n"
"v_cmp_eq_u64_e64 s[62:63],s[28:29],v[162:163]\n"
"s_and_b64 vcc,vcc,s[62:63]\n"
"v_cmp_eq_u64_e64 s[62:63],s[28:29],v[164:165]\n"
"s_and_b64 vcc,vcc,s[62:63]\n"
"v_cmp_eq_u64_e64 s[62:63],s[28:29],v[166:167]\n"
"s_and_b64 vcc,vcc,s[62:63]\n"
"s_cmp_eq_u64 vcc,exec\n"
"s_cbranch_scc1 L1005_%=\n"
"s_cmp_lt_u32 s54,1048576\n"
"s_cbranch_scc1 L1006_%=\n"
"s_branch L1005_%=\n"
"L1007_%=:\n"
"s_mov_b32 s54,0\n"
"L1009_%=:\n"
"v_max3_u32 v226,v160,v161,v162\n"
"v_max3_u32 v226,v226,v163,v164\n"
"v_max3_u32 v226,v226,v165,v166\n"
"v_max3_u32 v226,v226,v167,v168\n"
"v_max3_u32 v226,v226,v169,v170\n"
"v_max3_u32 v226,v226,v171,v172\n"
"v_max3_u32 v226,v226,v173,v174\n"
"v_max3_u32 v226,v226,v175,v176\n"
"v_max3_u32 v226,v226,v177,v178\n"
"v_max3_u32 v226,v226,v179,v180\n"
"v_max3_u32 v226,v226,v181,v182\n"
"v_max3_u32 v226,v226,v183,v184\n"
"v_max3_u32 v226,v226,v185,v186\n"
"v_max3_u32 v226,v226,v187,v188\n"
"v_max3_u32 v226,v226,v189,v190\n"
"v_max_u32 v226,v226,v191\n"
"v_mov_b32 v229,v226\n"
"s_nop 1\n"
"v_max_u32_dpp v229,v229,v229 row_shr:1 row_mask:0xf bank_mask:0xf\n"
"s_nop 1\n"
"v_max_u32_dpp v229,v229,v229 row_shr:2 row_mask:0xf bank_mask:0xf\n"
"s_nop 1\n"
"v_max_u32_dpp v229,v229,v229 row_shr:4 row_mask:0xf bank_mask:0xf\n"
"s_nop 1\n"
"v_max_u32_dpp v229,v229,v229 row_shr:8 row_mask:0xf bank_mask:0xf\n"
"s_nop 1\n"
"v_max_u32_dpp v229,v229,v229 row_bcast:15 row_mask:0xa bank_mask:0xf\n"
"s_nop 1\n"
"v_max_u32_dpp v229,v229,v229 row_bcast:31 row_mask:0xc bank_mask:0xf\n"
"s_nop 1\n"
"v_readlane_b32 s62,v229,63\n"
"s_nop 1\n"
"v_cmp_eq_u32 vcc,s62,v160\n"
"s_nop 1\n"
"v_cndmask_b32_e64 v160,v160,0,vcc\n"
"v_cmp_eq_u32 vcc,s62,v161\n"
"s_nop 1\n"
"v_cndmask_b32_e64 v161,v161,0,vcc\n"
"v_cmp_eq_u32 vcc,s62,v162\n"
"s_nop 1\n"
"v_cndmask_b32_e64 v162,v162,0,vcc\n"
"v_cmp_eq_u32 vcc,s62,v163\n"
"s_nop 1\n"
"v_cndmask_b32_e64 v163,v163,0,vcc\n"
"v_cmp_eq_u32 vcc,s62,v164\n"
"s_nop 1\n"
"v_cndmask_b32_e64 v164,v164,0,vcc\n"
"v_cmp_eq_u32 vcc,s62,v165\n"
"s_nop 1\n"
"v_cndmask_b32_e64 v165,v165,0,vcc\n"
"v_cmp_eq_u32 vcc,s62,v166\n"
"s_nop 1\n"
"v_cndmask_b32_e64 v166,v166,0,vcc\n"
"v_cmp_eq_u32 vcc,s62,v167\n"
"s_nop 1\n"
"v_cndmask_b32_e64 v167,v167,0,vcc\n"
"v_cmp_eq_u32 vcc,s62,v168\n"
"s_nop 1\n"
"v_cndmask_b32_e64 v168,v168,0,vcc\n"
"v_cmp_eq_u32 vcc,s62,v169\n"
"s_nop 1\n"
"v_cndmask_b32_e64 v169,v169,0,vcc\n"
"v_cmp_eq_u32 vcc,s62,v170\n"
"s_nop 1\n"
"v_cndmask_b32_e64 v170,v170,0,vcc\n"
"v_cmp_eq_u32 vcc,s62,v171\n"
"s_nop 1\n"
"v_cndmask_b32_e64 v171,v171,0,vcc\n"
"v_cmp_eq_u32 vcc,s62,v172\n"
"s_nop 1\n"
"v_cndmask_b32_e64 v172,v172,0,vcc\n"
"v_cmp_eq_u32 vcc,s62,v173\n"
"s_nop 1\n"
"v_cndmask_b32_e64 v173,v173,0,vcc\n"
"v_cmp_eq_u32 vcc,s62,v174\n"
"s_nop 1\n"
"v_cndmask_b32_e64 v174,v174,0,vcc\n"
"v_cmp_eq_u32 vcc,s62,v175\n"
"s_nop 1\n"
"v_cndmask_b32_e64 v175,v175,0,vcc\n"
"v_cmp_eq_u32 vcc,s62,v176\n"
"s_nop 1\n"
"v_cndmask_b32_e64 v176,v176,0,vcc\n"
"v_cmp_eq_u32 vcc,s62,v177\n"
"s_nop 1\n"
"v_cndmask_b32_e64 v177,v177,0,vcc\n"
"v_cmp_eq_u32 vcc,s62,v178\n"
"s_nop 1\n"
"v_cndmask_b32_e64 v178,v178,0,vcc\n"
"v_cmp_eq_u32 vcc,s62,v179\n"
"s_nop 1\n"
"v_cndmask_b32_e64 v179,v179,0,vcc\n"
"v_cmp_eq_u32 vcc,s62,v180\n"
"s_nop 1\n"
"v_cndmask_b32_e64 v180,v180,0,vcc\n"
"v_cmp_eq_u32 vcc,s62,v181\n"
"s_nop 1\n"
"v_cndmask_b32_e64 v181,v181,0,vcc\n"
"v_cmp_eq_u32 vcc,s62,v182\n"
"s_nop 1\n"
"v_cndmask_b32_e64 v182,v182,0,vcc\n"
"v_cmp_eq_u32 vcc,s62,v183\n"
"s_nop 1\n"
"v_cndmask_b32_e64 v183,v183,0,vcc\n"
"v_cmp_eq_u32 vcc,s62,v184\n"
"s_nop 1\n"
"v_cndmask_b32_e64 v184,v184,0,vcc\n"
"v_cmp_eq_u32 vcc,s62,v185\n"
"s_nop 1\n"
"v_cndmask_b32_e64 v185,v185,0,vcc\n"
"v_cmp_eq_u32 vcc,s62,v186\n"
"s_nop 1\n"
"v_cndmask_b32_e64 v186,v186,0,vcc\n"
"v_cmp_eq_u32 vcc,s62,v187\n"
"s_nop 1\n"
"v_cndmask_b32_e64 v187,v187,0,vcc\n"
"v_cmp_eq_u32 vcc,s62,v188\n"
"s_nop 1\n"
"v_cndmask_b32_e64 v188,v188,0,vcc\n"
"v_cmp_eq_u32 vcc,s62,v189\n"
"s_nop 1\n"
"v_cndmask_b32_e64 v189,v189,0,vcc\n"
"v_cmp_eq_u32 vcc,s62,v190\n"
"s_nop 1\n"
"v_cndmask_b32_e64 v190,v190,0,vcc\n"
"v_cmp_eq_u32 vcc,s62,v191\n"
"s_nop 1\n"
"v_cndmask_b32_e64 v191,v191,0,vcc\n"
"v_mov_b32 v229,s62\n"
"s_lshl_b64 exec,1,s54\n"
"v_mov_b32 v225,v229\n"
"s_mov_b64 exec,-1\n"
SA"s54,s54,1\n"
"s_cmp_lt_u32 s54,8\n"
"s_cbranch_scc1 L1009_%=\n"
"s_nop 4\n"
"s_branch L1008_%=\n"
"L1010_%=:\n"
"s_mov_b32 s54,0\n"
"L1012_%=:\n"
"s_sleep 2\n"
"buffer_load_dwordx2 v[208:209],v252,s[8:11],s50 offen sc1\n"
WT"vmcnt(0)\n"
SA"s54,s54,1\n"
"v_cmp_eq_u64 vcc,s[28:29],v[208:209]\n"
"s_cmp_eq_u64 vcc,exec\n"
"s_cbranch_scc1 L1011_%=\n"
"s_cmp_lt_u32 s54,1048576\n"
"s_cbranch_scc1 L1012_%=\n"
"s_branch L1011_%=\n"
"L1014_%=:\n"
"s_nop 15\n"
WT"vmcnt(0)\n"
"s_lshl_b32 s89,s34,3\n"
SA"s89,s89,s30\n"
SA"s89,s89,0\n"
"s_lshl_b32 s91,s89,4\n"
"s_lshr_b32 s89,s89,1\n"
"v_bfe_u32 v249,v224,4,4\n"
"v_lshrrev_b32 v254,8,v224\n"
"v_lshlrev_b32 v254,2,v254\n"
"v_sub_u32 v249,v249,v254\n"
"v_add_u32 v249,s91,v249\n"
"v_mov_b32 v255,0xff800000\n"
"v_mov_b32 v248,v255\n"
"s_mov_b32 s90,0\n"
"s_mov_b32 s88,0\n"
"v_mov_b32 v80,0\n"
"v_mov_b32 v81,0\n"
"v_mov_b32 v82,0\n"
"v_mov_b32 v83,0\n"
"v_mov_b32 v84,0\n"
"v_mov_b32 v85,0\n"
"v_mov_b32 v86,0\n"
"v_mov_b32 v87,0\n"
"v_mov_b32 v88,0\n"
"v_mov_b32 v89,0\n"
"v_mov_b32 v90,0\n"
"v_mov_b32 v91,0\n"
"v_mov_b32 v92,0\n"
"v_mov_b32 v93,0\n"
"v_mov_b32 v94,0\n"
"v_mov_b32 v95,0\n"
"v_mov_b32 v96,0\n"
"v_mov_b32 v97,0\n"
"v_mov_b32 v98,0\n"
"v_mov_b32 v99,0\n"
"v_mov_b32 v100,0\n"
"v_mov_b32 v101,0\n"
"v_mov_b32 v102,0\n"
"v_mov_b32 v103,0\n"
"v_mov_b32 v104,0\n"
"v_mov_b32 v105,0\n"
"v_mov_b32 v106,0\n"
"v_mov_b32 v107,0\n"
"v_mov_b32 v108,0\n"
"v_mov_b32 v109,0\n"
"v_mov_b32 v110,0\n"
"v_mov_b32 v111,0\n"
"v_accvgpr_write_b32 a132,0\n"
"v_accvgpr_write_b32 a133,0\n"
"v_accvgpr_write_b32 a134,0\n"
"v_accvgpr_write_b32 a135,0\n"
"L1015_%=:\n"
"s_lshl_b32 s91,s88,13\n"
SA"s91,s91,s39\n"
SA"s91,s91,0\n"
BL"a[148:151],v224,s[8:11],s91 offen offset:0 sc1\n"
BL"a[152:155],v224,s[8:11],s91 offen offset:1024 sc1\n"
BL"a[156:159],v224,s[8:11],s91 offen offset:2048 sc1\n"
BL"a[160:163],v224,s[8:11],s91 offen offset:3072 sc1\n"
SA"s91,s91,4096\n"
BL"a[164:167],v224,s[8:11],s91 offen offset:0 sc1\n"
BL"a[168:171],v224,s[8:11],s91 offen offset:1024 sc1\n"
BL"a[172:175],v224,s[8:11],s91 offen offset:2048 sc1\n"
BL"a[176:179],v224,s[8:11],s91 offen offset:3072 sc1\n"
WT"vmcnt(0)\n"
MF"v[240:243],a[148:151],a[0:3],0\n"
MF"v[240:243],a[152:155],a[4:7],v[240:243]\n"
MF"v[240:243],a[156:159],a[8:11],v[240:243]\n"
MF"v[240:243],a[160:163],a[12:15],v[240:243]\n"
MF"v[244:247],a[164:167],a[0:3],0\n"
MF"v[244:247],a[168:171],a[4:7],v[244:247]\n"
MF"v[244:247],a[172:175],a[8:11],v[244:247]\n"
MF"v[244:247],a[176:179],a[12:15],v[244:247]\n"
"s_lshl_b32 s91,s88,5\n"
"v_subrev_u32 v250,s91,v249\n"
"s_nop 7\n"
"v_cmp_gt_i32 vcc,0,v250\n"
"s_nop 1\n"
"v_cndmask_b32 v240,v240,v255,vcc\n"
"v_cmp_gt_i32 vcc,1,v250\n"
"s_nop 1\n"
"v_cndmask_b32 v241,v241,v255,vcc\n"
"v_cmp_gt_i32 vcc,2,v250\n"
"s_nop 1\n"
"v_cndmask_b32 v242,v242,v255,vcc\n"
"v_cmp_gt_i32 vcc,3,v250\n"
"s_nop 1\n"
"v_cndmask_b32 v243,v243,v255,vcc\n"
"v_cmp_gt_i32 vcc,16,v250\n"
"s_nop 1\n"
"v_cndmask_b32 v244,v244,v255,vcc\n"
"v_cmp_gt_i32 vcc,17,v250\n"
"s_nop 1\n"
"v_cndmask_b32 v245,v245,v255,vcc\n"
"v_cmp_gt_i32 vcc,18,v250\n"
"s_nop 1\n"
"v_cndmask_b32 v246,v246,v255,vcc\n"
"v_cmp_gt_i32 vcc,19,v250\n"
"s_nop 1\n"
"v_cndmask_b32 v247,v247,v255,vcc\n"
"s_cmp_lg_u32 s90,0\n"
"s_cbranch_scc1 L1016_%=\n"
"v_max3_f32 v248,v248,v240,v241\n"
"v_max3_f32 v248,v248,v242,v243\n"
"v_max3_f32 v248,v248,v244,v245\n"
"v_max3_f32 v248,v248,v246,v247\n"
"s_branch L1017_%=\n"
"L1016_%=:\n"
"s_lshl_b32 s91,s88,13\n"
SA"s91,s91,s39\n"
SA"s91,s91,524288\n"
BL"a[148:151],v224,s[8:11],s91 offen offset:0 sc1\n"
BL"a[152:155],v224,s[8:11],s91 offen offset:1024 sc1\n"
BL"a[156:159],v224,s[8:11],s91 offen offset:2048 sc1\n"
BL"a[160:163],v224,s[8:11],s91 offen offset:3072 sc1\n"
SA"s91,s91,4096\n"
BL"a[164:167],v224,s[8:11],s91 offen offset:0 sc1\n"
BL"a[168:171],v224,s[8:11],s91 offen offset:1024 sc1\n"
BL"a[172:175],v224,s[8:11],s91 offen offset:2048 sc1\n"
BL"a[176:179],v224,s[8:11],s91 offen offset:3072 sc1\n"
WT"vmcnt(0)\n"
"v_sub_f32 v240,v240,v248\n"
"v_sub_f32 v241,v241,v248\n"
"v_sub_f32 v242,v242,v248\n"
"v_sub_f32 v243,v243,v248\n"
"v_sub_f32 v244,v244,v248\n"
"v_sub_f32 v245,v245,v248\n"
"v_sub_f32 v246,v246,v248\n"
"v_sub_f32 v247,v247,v248\n"
EX"v240,v240\n"
EX"v241,v241\n"
EX"v242,v242\n"
EX"v243,v243\n"
EX"v244,v244\n"
EX"v245,v245\n"
EX"v246,v246\n"
EX"v247,v247\n"
"s_nop 0\n"
PK"v240,v240,v241\n"
PK"v241,v242,v243\n"
PK"v242,v244,v245\n"
PK"v243,v246,v247\n"
"s_nop 1\n"
MF"v[80:83],a[148:151],v[240:243],v[80:83]\n"
MF"v[84:87],a[152:155],v[240:243],v[84:87]\n"
MF"v[88:91],a[156:159],v[240:243],v[88:91]\n"
MF"v[92:95],a[160:163],v[240:243],v[92:95]\n"
MF"v[96:99],a[164:167],v[240:243],v[96:99]\n"
MF"v[100:103],a[168:171],v[240:243],v[100:103]\n"
MF"v[104:107],a[172:175],v[240:243],v[104:107]\n"
MF"v[108:111],a[176:179],v[240:243],v[108:111]\n"
MF"a[132:135],a[128:131],v[240:243],a[132:135]\n"
"s_nop 7\n"
"L1017_%=:\n"
SA"s88,s88,1\n"
"s_cmp_le_u32 s88,s89\n"
"s_cbranch_scc1 L1015_%=\n"
"s_cmp_lg_u32 s90,0\n"
"s_cbranch_scc1 L1018_%=\n"
"v_mov_b32 v254,v248\n"
"s_nop 1\n"
"v_permlane16_swap_b32 v254,v248\n"
"s_nop 1\n"
"v_max_f32 v248,v248,v254\n"
"v_mov_b32 v254,v248\n"
"s_nop 1\n"
"v_permlane32_swap_b32 v254,v248\n"
"s_nop 1\n"
"v_max_f32 v248,v248,v254\n"
"s_mov_b32 s90,1\n"
"s_mov_b32 s88,0\n"
"s_branch L1015_%=\n"
"L1018_%=:\n"
"s_nop 7\n"
"v_accvgpr_read_b32 v251,a132\n"
"s_nop 1\n"
"s_branch L1013_%=\n"
"L1019_%=:\n"
"s_mov_b32 s54,0\n"
"L1021_%=:\n"
"s_sleep 2\n"
"buffer_load_dwordx2 v[238:239],v237,s[8:11],s50 offen sc1\n"
WT"vmcnt(0)\n"
SA"s54,s54,1\n"
"v_cmp_eq_u64 vcc,s[28:29],v[238:239]\n"
"s_cmp_eq_u64 vcc,exec\n"
"s_cbranch_scc1 L1020_%=\n"
"s_cmp_lt_u32 s54,1048576\n"
"s_cbranch_scc1 L1021_%=\n"
"s_branch L1020_%=\n"
"L1023_%=:\n"
"s_nop 15\n"
WT"vmcnt(0)\n"
"s_lshl_b32 s89,s34,3\n"
SA"s89,s89,s30\n"
SA"s89,s89,4\n"
"s_lshl_b32 s91,s89,4\n"
"s_lshr_b32 s89,s89,1\n"
"v_bfe_u32 v249,v224,4,4\n"
"v_lshrrev_b32 v254,8,v224\n"
"v_lshlrev_b32 v254,2,v254\n"
"v_sub_u32 v249,v249,v254\n"
"v_add_u32 v249,s91,v249\n"
"v_mov_b32 v255,0xff800000\n"
"v_mov_b32 v248,v255\n"
"s_mov_b32 s90,0\n"
"s_mov_b32 s88,0\n"
"v_mov_b32 v112,0\n"
"v_mov_b32 v113,0\n"
"v_mov_b32 v114,0\n"
"v_mov_b32 v115,0\n"
"v_mov_b32 v116,0\n"
"v_mov_b32 v117,0\n"
"v_mov_b32 v118,0\n"
"v_mov_b32 v119,0\n"
"v_mov_b32 v120,0\n"
"v_mov_b32 v121,0\n"
"v_mov_b32 v122,0\n"
"v_mov_b32 v123,0\n"
"v_mov_b32 v124,0\n"
"v_mov_b32 v125,0\n"
"v_mov_b32 v126,0\n"
"v_mov_b32 v127,0\n"
"v_mov_b32 v128,0\n"
"v_mov_b32 v129,0\n"
"v_mov_b32 v130,0\n"
"v_mov_b32 v131,0\n"
"v_mov_b32 v132,0\n"
"v_mov_b32 v133,0\n"
"v_mov_b32 v134,0\n"
"v_mov_b32 v135,0\n"
"v_mov_b32 v136,0\n"
"v_mov_b32 v137,0\n"
"v_mov_b32 v138,0\n"
"v_mov_b32 v139,0\n"
"v_mov_b32 v140,0\n"
"v_mov_b32 v141,0\n"
"v_mov_b32 v142,0\n"
"v_mov_b32 v143,0\n"
"v_accvgpr_write_b32 a136,0\n"
"v_accvgpr_write_b32 a137,0\n"
"v_accvgpr_write_b32 a138,0\n"
"v_accvgpr_write_b32 a139,0\n"
"L1024_%=:\n"
"s_lshl_b32 s91,s88,13\n"
SA"s91,s91,s39\n"
SA"s91,s91,0\n"
BL"a[148:151],v224,s[8:11],s91 offen offset:0 sc1\n"
BL"a[152:155],v224,s[8:11],s91 offen offset:1024 sc1\n"
BL"a[156:159],v224,s[8:11],s91 offen offset:2048 sc1\n"
BL"a[160:163],v224,s[8:11],s91 offen offset:3072 sc1\n"
SA"s91,s91,4096\n"
BL"a[164:167],v224,s[8:11],s91 offen offset:0 sc1\n"
BL"a[168:171],v224,s[8:11],s91 offen offset:1024 sc1\n"
BL"a[172:175],v224,s[8:11],s91 offen offset:2048 sc1\n"
BL"a[176:179],v224,s[8:11],s91 offen offset:3072 sc1\n"
WT"vmcnt(0)\n"
MF"v[240:243],a[148:151],a[16:19],0\n"
MF"v[240:243],a[152:155],a[20:23],v[240:243]\n"
MF"v[240:243],a[156:159],a[24:27],v[240:243]\n"
MF"v[240:243],a[160:163],a[28:31],v[240:243]\n"
MF"v[244:247],a[164:167],a[16:19],0\n"
MF"v[244:247],a[168:171],a[20:23],v[244:247]\n"
MF"v[244:247],a[172:175],a[24:27],v[244:247]\n"
MF"v[244:247],a[176:179],a[28:31],v[244:247]\n"
"s_lshl_b32 s91,s88,5\n"
"v_subrev_u32 v250,s91,v249\n"
"s_nop 7\n"
"v_cmp_gt_i32 vcc,0,v250\n"
"s_nop 1\n"
"v_cndmask_b32 v240,v240,v255,vcc\n"
"v_cmp_gt_i32 vcc,1,v250\n"
"s_nop 1\n"
"v_cndmask_b32 v241,v241,v255,vcc\n"
"v_cmp_gt_i32 vcc,2,v250\n"
"s_nop 1\n"
"v_cndmask_b32 v242,v242,v255,vcc\n"
"v_cmp_gt_i32 vcc,3,v250\n"
"s_nop 1\n"
"v_cndmask_b32 v243,v243,v255,vcc\n"
"v_cmp_gt_i32 vcc,16,v250\n"
"s_nop 1\n"
"v_cndmask_b32 v244,v244,v255,vcc\n"
"v_cmp_gt_i32 vcc,17,v250\n"
"s_nop 1\n"
"v_cndmask_b32 v245,v245,v255,vcc\n"
"v_cmp_gt_i32 vcc,18,v250\n"
"s_nop 1\n"
"v_cndmask_b32 v246,v246,v255,vcc\n"
"v_cmp_gt_i32 vcc,19,v250\n"
"s_nop 1\n"
"v_cndmask_b32 v247,v247,v255,vcc\n"
"s_cmp_lg_u32 s90,0\n"
"s_cbranch_scc1 L1025_%=\n"
"v_max3_f32 v248,v248,v240,v241\n"
"v_max3_f32 v248,v248,v242,v243\n"
"v_max3_f32 v248,v248,v244,v245\n"
"v_max3_f32 v248,v248,v246,v247\n"
"s_branch L1026_%=\n"
"L1025_%=:\n"
"s_lshl_b32 s91,s88,13\n"
SA"s91,s91,s39\n"
SA"s91,s91,524288\n"
BL"a[148:151],v224,s[8:11],s91 offen offset:0 sc1\n"
BL"a[152:155],v224,s[8:11],s91 offen offset:1024 sc1\n"
BL"a[156:159],v224,s[8:11],s91 offen offset:2048 sc1\n"
BL"a[160:163],v224,s[8:11],s91 offen offset:3072 sc1\n"
SA"s91,s91,4096\n"
BL"a[164:167],v224,s[8:11],s91 offen offset:0 sc1\n"
BL"a[168:171],v224,s[8:11],s91 offen offset:1024 sc1\n"
BL"a[172:175],v224,s[8:11],s91 offen offset:2048 sc1\n"
BL"a[176:179],v224,s[8:11],s91 offen offset:3072 sc1\n"
WT"vmcnt(0)\n"
"v_sub_f32 v240,v240,v248\n"
"v_sub_f32 v241,v241,v248\n"
"v_sub_f32 v242,v242,v248\n"
"v_sub_f32 v243,v243,v248\n"
"v_sub_f32 v244,v244,v248\n"
"v_sub_f32 v245,v245,v248\n"
"v_sub_f32 v246,v246,v248\n"
"v_sub_f32 v247,v247,v248\n"
EX"v240,v240\n"
EX"v241,v241\n"
EX"v242,v242\n"
EX"v243,v243\n"
EX"v244,v244\n"
EX"v245,v245\n"
EX"v246,v246\n"
EX"v247,v247\n"
"s_nop 0\n"
PK"v240,v240,v241\n"
PK"v241,v242,v243\n"
PK"v242,v244,v245\n"
PK"v243,v246,v247\n"
"s_nop 1\n"
MF"v[112:115],a[148:151],v[240:243],v[112:115]\n"
MF"v[116:119],a[152:155],v[240:243],v[116:119]\n"
MF"v[120:123],a[156:159],v[240:243],v[120:123]\n"
MF"v[124:127],a[160:163],v[240:243],v[124:127]\n"
MF"v[128:131],a[164:167],v[240:243],v[128:131]\n"
MF"v[132:135],a[168:171],v[240:243],v[132:135]\n"
MF"v[136:139],a[172:175],v[240:243],v[136:139]\n"
MF"v[140:143],a[176:179],v[240:243],v[140:143]\n"
MF"a[136:139],a[128:131],v[240:243],a[136:139]\n"
"s_nop 7\n"
"L1026_%=:\n"
SA"s88,s88,1\n"
"s_cmp_le_u32 s88,s89\n"
"s_cbranch_scc1 L1024_%=\n"
"s_cmp_lg_u32 s90,0\n"
"s_cbranch_scc1 L1027_%=\n"
"v_mov_b32 v254,v248\n"
"s_nop 1\n"
"v_permlane16_swap_b32 v254,v248\n"
"s_nop 1\n"
"v_max_f32 v248,v248,v254\n"
"v_mov_b32 v254,v248\n"
"s_nop 1\n"
"v_permlane32_swap_b32 v254,v248\n"
"s_nop 1\n"
"v_max_f32 v248,v248,v254\n"
"s_mov_b32 s90,1\n"
"s_mov_b32 s88,0\n"
"s_branch L1024_%=\n"
"L1027_%=:\n"
"s_nop 7\n"
"v_accvgpr_read_b32 v252,a136\n"
"s_nop 1\n"
"s_branch L1022_%=\n"
"L1028_%=:\n"
"s_mov_b32 s54,0\n"
"L1030_%=:\n"
"s_sleep 2\n"
"buffer_load_dwordx2 v[242:243],v236,s[8:11],0 offen sc1\n"
WT"vmcnt(0)\n"
SA"s54,s54,1\n"
"v_cmp_eq_u64 vcc,s[28:29],v[242:243]\n"
"s_or_b32 vcc_hi,vcc_hi,-1\n"
"s_cmp_eq_u64 vcc,exec\n"
"s_cbranch_scc1 L1029_%=\n"
"s_cmp_lt_u32 s54,1048576\n"
"s_cbranch_scc1 L1030_%=\n"
"s_branch L1029_%=\n"
"L1031_%=:\n"
"s_mov_b32 s54,0\n"
"L1033_%=:\n"
"s_sleep 2\n"
"buffer_load_dwordx2 v[242:243],v236,s[8:11],0 offen sc1\n"
WT"vmcnt(0)\n"
SA"s54,s54,1\n"
"v_cmp_eq_u64 vcc,s[28:29],v[242:243]\n"
"s_or_b32 vcc_lo,vcc_lo,-1\n"
"s_cmp_eq_u64 vcc,exec\n"
"s_cbranch_scc1 L1032_%=\n"
"s_cmp_lt_u32 s54,1048576\n"
"s_cbranch_scc1 L1033_%=\n"
"s_branch L1032_%=\n"
"L1035_%=:\n"
"s_nop 15\n"
WT"vmcnt(0)\n"
"s_lshl_b32 s89,s34,3\n"
SA"s89,s89,s30\n"
SA"s89,s89,4\n"
"s_lshl_b32 s91,s89,4\n"
"s_lshr_b32 s89,s89,1\n"
"v_bfe_u32 v249,v224,4,4\n"
"v_lshrrev_b32 v254,8,v224\n"
"v_lshlrev_b32 v254,2,v254\n"
"v_sub_u32 v249,v249,v254\n"
"v_add_u32 v249,s91,v249\n"
"v_mov_b32 v255,0xff800000\n"
"v_mov_b32 v248,v255\n"
"s_mov_b32 s90,0\n"
"s_mov_b32 s88,0\n"
"v_mov_b32 v112,0\n"
"v_mov_b32 v113,0\n"
"v_mov_b32 v114,0\n"
"v_mov_b32 v115,0\n"
"v_mov_b32 v116,0\n"
"v_mov_b32 v117,0\n"
"v_mov_b32 v118,0\n"
"v_mov_b32 v119,0\n"
"v_mov_b32 v120,0\n"
"v_mov_b32 v121,0\n"
"v_mov_b32 v122,0\n"
"v_mov_b32 v123,0\n"
"v_mov_b32 v124,0\n"
"v_mov_b32 v125,0\n"
"v_mov_b32 v126,0\n"
"v_mov_b32 v127,0\n"
"v_mov_b32 v128,0\n"
"v_mov_b32 v129,0\n"
"v_mov_b32 v130,0\n"
"v_mov_b32 v131,0\n"
"v_mov_b32 v132,0\n"
"v_mov_b32 v133,0\n"
"v_mov_b32 v134,0\n"
"v_mov_b32 v135,0\n"
"v_mov_b32 v136,0\n"
"v_mov_b32 v137,0\n"
"v_mov_b32 v138,0\n"
"v_mov_b32 v139,0\n"
"v_mov_b32 v140,0\n"
"v_mov_b32 v141,0\n"
"v_mov_b32 v142,0\n"
"v_mov_b32 v143,0\n"
"v_accvgpr_write_b32 a136,0\n"
"v_accvgpr_write_b32 a137,0\n"
"v_accvgpr_write_b32 a138,0\n"
"v_accvgpr_write_b32 a139,0\n"
"L1036_%=:\n"
"s_lshl_b32 s91,s88,13\n"
SA"s91,s91,s39\n"
SA"s91,s91,0\n"
BL"a[148:151],v224,s[8:11],s91 offen offset:0 sc1\n"
BL"a[152:155],v224,s[8:11],s91 offen offset:1024 sc1\n"
BL"a[156:159],v224,s[8:11],s91 offen offset:2048 sc1\n"
BL"a[160:163],v224,s[8:11],s91 offen offset:3072 sc1\n"
SA"s91,s91,4096\n"
BL"a[164:167],v224,s[8:11],s91 offen offset:0 sc1\n"
BL"a[168:171],v224,s[8:11],s91 offen offset:1024 sc1\n"
BL"a[172:175],v224,s[8:11],s91 offen offset:2048 sc1\n"
BL"a[176:179],v224,s[8:11],s91 offen offset:3072 sc1\n"
WT"vmcnt(0)\n"
MF"v[240:243],a[148:151],a[16:19],0\n"
MF"v[240:243],a[152:155],a[20:23],v[240:243]\n"
MF"v[240:243],a[156:159],a[24:27],v[240:243]\n"
MF"v[240:243],a[160:163],a[28:31],v[240:243]\n"
MF"v[244:247],a[164:167],a[16:19],0\n"
MF"v[244:247],a[168:171],a[20:23],v[244:247]\n"
MF"v[244:247],a[172:175],a[24:27],v[244:247]\n"
MF"v[244:247],a[176:179],a[28:31],v[244:247]\n"
"s_lshl_b32 s91,s88,5\n"
"v_subrev_u32 v250,s91,v249\n"
"s_nop 7\n"
"v_cmp_gt_i32 vcc,0,v250\n"
"s_nop 1\n"
"v_cndmask_b32 v240,v240,v255,vcc\n"
"v_cmp_gt_i32 vcc,1,v250\n"
"s_nop 1\n"
"v_cndmask_b32 v241,v241,v255,vcc\n"
"v_cmp_gt_i32 vcc,2,v250\n"
"s_nop 1\n"
"v_cndmask_b32 v242,v242,v255,vcc\n"
"v_cmp_gt_i32 vcc,3,v250\n"
"s_nop 1\n"
"v_cndmask_b32 v243,v243,v255,vcc\n"
"v_cmp_gt_i32 vcc,16,v250\n"
"s_nop 1\n"
"v_cndmask_b32 v244,v244,v255,vcc\n"
"v_cmp_gt_i32 vcc,17,v250\n"
"s_nop 1\n"
"v_cndmask_b32 v245,v245,v255,vcc\n"
"v_cmp_gt_i32 vcc,18,v250\n"
"s_nop 1\n"
"v_cndmask_b32 v246,v246,v255,vcc\n"
"v_cmp_gt_i32 vcc,19,v250\n"
"s_nop 1\n"
"v_cndmask_b32 v247,v247,v255,vcc\n"
"s_cmp_lg_u32 s90,0\n"
"s_cbranch_scc1 L1037_%=\n"
"v_max3_f32 v248,v248,v240,v241\n"
"v_max3_f32 v248,v248,v242,v243\n"
"v_max3_f32 v248,v248,v244,v245\n"
"v_max3_f32 v248,v248,v246,v247\n"
"s_branch L1038_%=\n"
"L1037_%=:\n"
"s_lshl_b32 s91,s88,13\n"
SA"s91,s91,s39\n"
SA"s91,s91,524288\n"
BL"a[148:151],v224,s[8:11],s91 offen offset:0 sc1\n"
BL"a[152:155],v224,s[8:11],s91 offen offset:1024 sc1\n"
BL"a[156:159],v224,s[8:11],s91 offen offset:2048 sc1\n"
BL"a[160:163],v224,s[8:11],s91 offen offset:3072 sc1\n"
SA"s91,s91,4096\n"
BL"a[164:167],v224,s[8:11],s91 offen offset:0 sc1\n"
BL"a[168:171],v224,s[8:11],s91 offen offset:1024 sc1\n"
BL"a[172:175],v224,s[8:11],s91 offen offset:2048 sc1\n"
BL"a[176:179],v224,s[8:11],s91 offen offset:3072 sc1\n"
WT"vmcnt(0)\n"
"v_sub_f32 v240,v240,v248\n"
"v_sub_f32 v241,v241,v248\n"
"v_sub_f32 v242,v242,v248\n"
"v_sub_f32 v243,v243,v248\n"
"v_sub_f32 v244,v244,v248\n"
"v_sub_f32 v245,v245,v248\n"
"v_sub_f32 v246,v246,v248\n"
"v_sub_f32 v247,v247,v248\n"
EX"v240,v240\n"
EX"v241,v241\n"
EX"v242,v242\n"
EX"v243,v243\n"
EX"v244,v244\n"
EX"v245,v245\n"
EX"v246,v246\n"
EX"v247,v247\n"
"s_nop 0\n"
PK"v240,v240,v241\n"
PK"v241,v242,v243\n"
PK"v242,v244,v245\n"
PK"v243,v246,v247\n"
"s_nop 1\n"
MF"v[112:115],a[148:151],v[240:243],v[112:115]\n"
MF"v[116:119],a[152:155],v[240:243],v[116:119]\n"
MF"v[120:123],a[156:159],v[240:243],v[120:123]\n"
MF"v[124:127],a[160:163],v[240:243],v[124:127]\n"
MF"v[128:131],a[164:167],v[240:243],v[128:131]\n"
MF"v[132:135],a[168:171],v[240:243],v[132:135]\n"
MF"v[136:139],a[172:175],v[240:243],v[136:139]\n"
MF"v[140:143],a[176:179],v[240:243],v[140:143]\n"
MF"a[136:139],a[128:131],v[240:243],a[136:139]\n"
"s_nop 7\n"
"L1038_%=:\n"
SA"s88,s88,1\n"
"s_cmp_le_u32 s88,s89\n"
"s_cbranch_scc1 L1036_%=\n"
"s_cmp_lg_u32 s90,0\n"
"s_cbranch_scc1 L1039_%=\n"
"v_mov_b32 v254,v248\n"
"s_nop 1\n"
"v_permlane16_swap_b32 v254,v248\n"
"s_nop 1\n"
"v_max_f32 v248,v248,v254\n"
"v_mov_b32 v254,v248\n"
"s_nop 1\n"
"v_permlane32_swap_b32 v254,v248\n"
"s_nop 1\n"
"v_max_f32 v248,v248,v254\n"
"s_mov_b32 s90,1\n"
"s_mov_b32 s88,0\n"
"s_branch L1036_%=\n"
"L1039_%=:\n"
"s_nop 7\n"
"v_accvgpr_read_b32 v252,a136\n"
"s_nop 1\n"
"s_branch L1034_%=\n"
"L1041_%=:\n"
"s_nop 15\n"
WT"vmcnt(0)\n"
"s_lshl_b32 s89,s34,3\n"
"s_sub_u32 s89,120,s89\n"
SA"s89,s89,s30\n"
SA"s89,s89,0\n"
"s_lshl_b32 s91,s89,4\n"
"s_lshr_b32 s89,s89,1\n"
"v_bfe_u32 v249,v224,4,4\n"
"v_lshrrev_b32 v254,8,v224\n"
"v_lshlrev_b32 v254,2,v254\n"
"v_sub_u32 v249,v249,v254\n"
"v_add_u32 v249,s91,v249\n"
"v_mov_b32 v255,0xff800000\n"
"v_mov_b32 v248,v255\n"
"s_mov_b32 s90,0\n"
"s_mov_b32 s88,0\n"
"v_mov_b32 v144,0\n"
"v_mov_b32 v145,0\n"
"v_mov_b32 v146,0\n"
"v_mov_b32 v147,0\n"
"v_mov_b32 v148,0\n"
"v_mov_b32 v149,0\n"
"v_mov_b32 v150,0\n"
"v_mov_b32 v151,0\n"
"v_mov_b32 v152,0\n"
"v_mov_b32 v153,0\n"
"v_mov_b32 v154,0\n"
"v_mov_b32 v155,0\n"
"v_mov_b32 v156,0\n"
"v_mov_b32 v157,0\n"
"v_mov_b32 v158,0\n"
"v_mov_b32 v159,0\n"
"v_mov_b32 v160,0\n"
"v_mov_b32 v161,0\n"
"v_mov_b32 v162,0\n"
"v_mov_b32 v163,0\n"
"v_mov_b32 v164,0\n"
"v_mov_b32 v165,0\n"
"v_mov_b32 v166,0\n"
"v_mov_b32 v167,0\n"
"v_mov_b32 v168,0\n"
"v_mov_b32 v169,0\n"
"v_mov_b32 v170,0\n"
"v_mov_b32 v171,0\n"
"v_mov_b32 v172,0\n"
"v_mov_b32 v173,0\n"
"v_mov_b32 v174,0\n"
"v_mov_b32 v175,0\n"
"v_accvgpr_write_b32 a140,0\n"
"v_accvgpr_write_b32 a141,0\n"
"v_accvgpr_write_b32 a142,0\n"
"v_accvgpr_write_b32 a143,0\n"
"L1042_%=:\n"
"s_lshl_b32 s91,s88,13\n"
SA"s91,s91,s39\n"
SA"s91,s91,0\n"
BL"a[148:151],v224,s[8:11],s91 offen offset:0 sc1\n"
BL"a[152:155],v224,s[8:11],s91 offen offset:1024 sc1\n"
BL"a[156:159],v224,s[8:11],s91 offen offset:2048 sc1\n"
BL"a[160:163],v224,s[8:11],s91 offen offset:3072 sc1\n"
SA"s91,s91,4096\n"
BL"a[164:167],v224,s[8:11],s91 offen offset:0 sc1\n"
BL"a[168:171],v224,s[8:11],s91 offen offset:1024 sc1\n"
BL"a[172:175],v224,s[8:11],s91 offen offset:2048 sc1\n"
BL"a[176:179],v224,s[8:11],s91 offen offset:3072 sc1\n"
WT"vmcnt(0)\n"
MF"v[240:243],a[148:151],a[32:35],0\n"
MF"v[240:243],a[152:155],a[36:39],v[240:243]\n"
MF"v[240:243],a[156:159],a[40:43],v[240:243]\n"
MF"v[240:243],a[160:163],a[44:47],v[240:243]\n"
MF"v[244:247],a[164:167],a[32:35],0\n"
MF"v[244:247],a[168:171],a[36:39],v[244:247]\n"
MF"v[244:247],a[172:175],a[40:43],v[244:247]\n"
MF"v[244:247],a[176:179],a[44:47],v[244:247]\n"
"s_lshl_b32 s91,s88,5\n"
"v_subrev_u32 v250,s91,v249\n"
"s_nop 7\n"
"v_cmp_gt_i32 vcc,0,v250\n"
"s_nop 1\n"
"v_cndmask_b32 v240,v240,v255,vcc\n"
"v_cmp_gt_i32 vcc,1,v250\n"
"s_nop 1\n"
"v_cndmask_b32 v241,v241,v255,vcc\n"
"v_cmp_gt_i32 vcc,2,v250\n"
"s_nop 1\n"
"v_cndmask_b32 v242,v242,v255,vcc\n"
"v_cmp_gt_i32 vcc,3,v250\n"
"s_nop 1\n"
"v_cndmask_b32 v243,v243,v255,vcc\n"
"v_cmp_gt_i32 vcc,16,v250\n"
"s_nop 1\n"
"v_cndmask_b32 v244,v244,v255,vcc\n"
"v_cmp_gt_i32 vcc,17,v250\n"
"s_nop 1\n"
"v_cndmask_b32 v245,v245,v255,vcc\n"
"v_cmp_gt_i32 vcc,18,v250\n"
"s_nop 1\n"
"v_cndmask_b32 v246,v246,v255,vcc\n"
"v_cmp_gt_i32 vcc,19,v250\n"
"s_nop 1\n"
"v_cndmask_b32 v247,v247,v255,vcc\n"
"s_cmp_lg_u32 s90,0\n"
"s_cbranch_scc1 L1043_%=\n"
"v_max3_f32 v248,v248,v240,v241\n"
"v_max3_f32 v248,v248,v242,v243\n"
"v_max3_f32 v248,v248,v244,v245\n"
"v_max3_f32 v248,v248,v246,v247\n"
"s_branch L1044_%=\n"
"L1043_%=:\n"
"s_lshl_b32 s91,s88,13\n"
SA"s91,s91,s39\n"
SA"s91,s91,524288\n"
BL"a[148:151],v224,s[8:11],s91 offen offset:0 sc1\n"
BL"a[152:155],v224,s[8:11],s91 offen offset:1024 sc1\n"
BL"a[156:159],v224,s[8:11],s91 offen offset:2048 sc1\n"
BL"a[160:163],v224,s[8:11],s91 offen offset:3072 sc1\n"
SA"s91,s91,4096\n"
BL"a[164:167],v224,s[8:11],s91 offen offset:0 sc1\n"
BL"a[168:171],v224,s[8:11],s91 offen offset:1024 sc1\n"
BL"a[172:175],v224,s[8:11],s91 offen offset:2048 sc1\n"
BL"a[176:179],v224,s[8:11],s91 offen offset:3072 sc1\n"
WT"vmcnt(0)\n"
"v_sub_f32 v240,v240,v248\n"
"v_sub_f32 v241,v241,v248\n"
"v_sub_f32 v242,v242,v248\n"
"v_sub_f32 v243,v243,v248\n"
"v_sub_f32 v244,v244,v248\n"
"v_sub_f32 v245,v245,v248\n"
"v_sub_f32 v246,v246,v248\n"
"v_sub_f32 v247,v247,v248\n"
EX"v240,v240\n"
EX"v241,v241\n"
EX"v242,v242\n"
EX"v243,v243\n"
EX"v244,v244\n"
EX"v245,v245\n"
EX"v246,v246\n"
EX"v247,v247\n"
"s_nop 0\n"
PK"v240,v240,v241\n"
PK"v241,v242,v243\n"
PK"v242,v244,v245\n"
PK"v243,v246,v247\n"
"s_nop 1\n"
MF"v[144:147],a[148:151],v[240:243],v[144:147]\n"
MF"v[148:151],a[152:155],v[240:243],v[148:151]\n"
MF"v[152:155],a[156:159],v[240:243],v[152:155]\n"
MF"v[156:159],a[160:163],v[240:243],v[156:159]\n"
MF"v[160:163],a[164:167],v[240:243],v[160:163]\n"
MF"v[164:167],a[168:171],v[240:243],v[164:167]\n"
MF"v[168:171],a[172:175],v[240:243],v[168:171]\n"
MF"v[172:175],a[176:179],v[240:243],v[172:175]\n"
MF"a[140:143],a[128:131],v[240:243],a[140:143]\n"
"s_nop 7\n"
"L1044_%=:\n"
SA"s88,s88,1\n"
"s_cmp_le_u32 s88,s89\n"
"s_cbranch_scc1 L1042_%=\n"
"s_cmp_lg_u32 s90,0\n"
"s_cbranch_scc1 L1045_%=\n"
"v_mov_b32 v254,v248\n"
"s_nop 1\n"
"v_permlane16_swap_b32 v254,v248\n"
"s_nop 1\n"
"v_max_f32 v248,v248,v254\n"
"v_mov_b32 v254,v248\n"
"s_nop 1\n"
"v_permlane32_swap_b32 v254,v248\n"
"s_nop 1\n"
"v_max_f32 v248,v248,v254\n"
"s_mov_b32 s90,1\n"
"s_mov_b32 s88,0\n"
"s_branch L1042_%=\n"
"L1045_%=:\n"
"s_nop 7\n"
"v_accvgpr_read_b32 v251,a140\n"
"s_nop 1\n"
"s_branch L1040_%=\n"
"L1047_%=:\n"
"s_nop 15\n"
WT"vmcnt(0)\n"
"s_lshl_b32 s89,s34,3\n"
"s_sub_u32 s89,120,s89\n"
SA"s89,s89,s30\n"
SA"s89,s89,4\n"
"s_lshl_b32 s91,s89,4\n"
"s_lshr_b32 s89,s89,1\n"
"v_bfe_u32 v249,v224,4,4\n"
"v_lshrrev_b32 v254,8,v224\n"
"v_lshlrev_b32 v254,2,v254\n"
"v_sub_u32 v249,v249,v254\n"
"v_add_u32 v249,s91,v249\n"
"v_mov_b32 v255,0xff800000\n"
"v_mov_b32 v248,v255\n"
"s_mov_b32 s90,0\n"
"s_mov_b32 s88,0\n"
"v_mov_b32 v176,0\n"
"v_mov_b32 v177,0\n"
"v_mov_b32 v178,0\n"
"v_mov_b32 v179,0\n"
"v_mov_b32 v180,0\n"
"v_mov_b32 v181,0\n"
"v_mov_b32 v182,0\n"
"v_mov_b32 v183,0\n"
"v_mov_b32 v184,0\n"
"v_mov_b32 v185,0\n"
"v_mov_b32 v186,0\n"
"v_mov_b32 v187,0\n"
"v_mov_b32 v188,0\n"
"v_mov_b32 v189,0\n"
"v_mov_b32 v190,0\n"
"v_mov_b32 v191,0\n"
"v_mov_b32 v192,0\n"
"v_mov_b32 v193,0\n"
"v_mov_b32 v194,0\n"
"v_mov_b32 v195,0\n"
"v_mov_b32 v196,0\n"
"v_mov_b32 v197,0\n"
"v_mov_b32 v198,0\n"
"v_mov_b32 v199,0\n"
"v_mov_b32 v200,0\n"
"v_mov_b32 v201,0\n"
"v_mov_b32 v202,0\n"
"v_mov_b32 v203,0\n"
"v_mov_b32 v204,0\n"
"v_mov_b32 v205,0\n"
"v_mov_b32 v206,0\n"
"v_mov_b32 v207,0\n"
"v_accvgpr_write_b32 a144,0\n"
"v_accvgpr_write_b32 a145,0\n"
"v_accvgpr_write_b32 a146,0\n"
"v_accvgpr_write_b32 a147,0\n"
"L1048_%=:\n"
"s_lshl_b32 s91,s88,13\n"
SA"s91,s91,s39\n"
SA"s91,s91,0\n"
BL"a[148:151],v224,s[8:11],s91 offen offset:0 sc1\n"
BL"a[152:155],v224,s[8:11],s91 offen offset:1024 sc1\n"
BL"a[156:159],v224,s[8:11],s91 offen offset:2048 sc1\n"
BL"a[160:163],v224,s[8:11],s91 offen offset:3072 sc1\n"
SA"s91,s91,4096\n"
BL"a[164:167],v224,s[8:11],s91 offen offset:0 sc1\n"
BL"a[168:171],v224,s[8:11],s91 offen offset:1024 sc1\n"
BL"a[172:175],v224,s[8:11],s91 offen offset:2048 sc1\n"
BL"a[176:179],v224,s[8:11],s91 offen offset:3072 sc1\n"
WT"vmcnt(0)\n"
MF"v[240:243],a[148:151],a[48:51],0\n"
MF"v[240:243],a[152:155],a[52:55],v[240:243]\n"
MF"v[240:243],a[156:159],a[56:59],v[240:243]\n"
MF"v[240:243],a[160:163],a[60:63],v[240:243]\n"
MF"v[244:247],a[164:167],a[48:51],0\n"
MF"v[244:247],a[168:171],a[52:55],v[244:247]\n"
MF"v[244:247],a[172:175],a[56:59],v[244:247]\n"
MF"v[244:247],a[176:179],a[60:63],v[244:247]\n"
"s_lshl_b32 s91,s88,5\n"
"v_subrev_u32 v250,s91,v249\n"
"s_nop 7\n"
"v_cmp_gt_i32 vcc,0,v250\n"
"s_nop 1\n"
"v_cndmask_b32 v240,v240,v255,vcc\n"
"v_cmp_gt_i32 vcc,1,v250\n"
"s_nop 1\n"
"v_cndmask_b32 v241,v241,v255,vcc\n"
"v_cmp_gt_i32 vcc,2,v250\n"
"s_nop 1\n"
"v_cndmask_b32 v242,v242,v255,vcc\n"
"v_cmp_gt_i32 vcc,3,v250\n"
"s_nop 1\n"
"v_cndmask_b32 v243,v243,v255,vcc\n"
"v_cmp_gt_i32 vcc,16,v250\n"
"s_nop 1\n"
"v_cndmask_b32 v244,v244,v255,vcc\n"
"v_cmp_gt_i32 vcc,17,v250\n"
"s_nop 1\n"
"v_cndmask_b32 v245,v245,v255,vcc\n"
"v_cmp_gt_i32 vcc,18,v250\n"
"s_nop 1\n"
"v_cndmask_b32 v246,v246,v255,vcc\n"
"v_cmp_gt_i32 vcc,19,v250\n"
"s_nop 1\n"
"v_cndmask_b32 v247,v247,v255,vcc\n"
"s_cmp_lg_u32 s90,0\n"
"s_cbranch_scc1 L1049_%=\n"
"v_max3_f32 v248,v248,v240,v241\n"
"v_max3_f32 v248,v248,v242,v243\n"
"v_max3_f32 v248,v248,v244,v245\n"
"v_max3_f32 v248,v248,v246,v247\n"
"s_branch L1050_%=\n"
"L1049_%=:\n"
"s_lshl_b32 s91,s88,13\n"
SA"s91,s91,s39\n"
SA"s91,s91,524288\n"
BL"a[148:151],v224,s[8:11],s91 offen offset:0 sc1\n"
BL"a[152:155],v224,s[8:11],s91 offen offset:1024 sc1\n"
BL"a[156:159],v224,s[8:11],s91 offen offset:2048 sc1\n"
BL"a[160:163],v224,s[8:11],s91 offen offset:3072 sc1\n"
SA"s91,s91,4096\n"
BL"a[164:167],v224,s[8:11],s91 offen offset:0 sc1\n"
BL"a[168:171],v224,s[8:11],s91 offen offset:1024 sc1\n"
BL"a[172:175],v224,s[8:11],s91 offen offset:2048 sc1\n"
BL"a[176:179],v224,s[8:11],s91 offen offset:3072 sc1\n"
WT"vmcnt(0)\n"
"v_sub_f32 v240,v240,v248\n"
"v_sub_f32 v241,v241,v248\n"
"v_sub_f32 v242,v242,v248\n"
"v_sub_f32 v243,v243,v248\n"
"v_sub_f32 v244,v244,v248\n"
"v_sub_f32 v245,v245,v248\n"
"v_sub_f32 v246,v246,v248\n"
"v_sub_f32 v247,v247,v248\n"
EX"v240,v240\n"
EX"v241,v241\n"
EX"v242,v242\n"
EX"v243,v243\n"
EX"v244,v244\n"
EX"v245,v245\n"
EX"v246,v246\n"
EX"v247,v247\n"
"s_nop 0\n"
PK"v240,v240,v241\n"
PK"v241,v242,v243\n"
PK"v242,v244,v245\n"
PK"v243,v246,v247\n"
"s_nop 1\n"
MF"v[176:179],a[148:151],v[240:243],v[176:179]\n"
MF"v[180:183],a[152:155],v[240:243],v[180:183]\n"
MF"v[184:187],a[156:159],v[240:243],v[184:187]\n"
MF"v[188:191],a[160:163],v[240:243],v[188:191]\n"
MF"v[192:195],a[164:167],v[240:243],v[192:195]\n"
MF"v[196:199],a[168:171],v[240:243],v[196:199]\n"
MF"v[200:203],a[172:175],v[240:243],v[200:203]\n"
MF"v[204:207],a[176:179],v[240:243],v[204:207]\n"
MF"a[144:147],a[128:131],v[240:243],a[144:147]\n"
"s_nop 7\n"
"L1050_%=:\n"
SA"s88,s88,1\n"
"s_cmp_le_u32 s88,s89\n"
"s_cbranch_scc1 L1048_%=\n"
"s_cmp_lg_u32 s90,0\n"
"s_cbranch_scc1 L1051_%=\n"
"v_mov_b32 v254,v248\n"
"s_nop 1\n"
"v_permlane16_swap_b32 v254,v248\n"
"s_nop 1\n"
"v_max_f32 v248,v248,v254\n"
"v_mov_b32 v254,v248\n"
"s_nop 1\n"
"v_permlane32_swap_b32 v254,v248\n"
"s_nop 1\n"
"v_max_f32 v248,v248,v254\n"
"s_mov_b32 s90,1\n"
"s_mov_b32 s88,0\n"
"s_branch L1048_%=\n"
"L1051_%=:\n"
"s_nop 7\n"
"v_accvgpr_read_b32 v252,a144\n"
"s_nop 1\n"
"s_branch L1046_%=\n"
"L1069_%=:\n"
"s_nop 15\n"
WT"vmcnt(0)\n"
"s_lshl_b32 s89,s34,3\n"
"s_sub_u32 s89,120,s89\n"
SA"s89,s89,s30\n"
SA"s89,s89,0\n"
"s_lshl_b32 s91,s89,4\n"
"s_lshr_b32 s89,s89,1\n"
"v_bfe_u32 v249,v224,4,4\n"
"v_lshrrev_b32 v254,8,v224\n"
"v_lshlrev_b32 v254,2,v254\n"
"v_sub_u32 v249,v249,v254\n"
"v_add_u32 v249,s91,v249\n"
"v_mov_b32 v255,0xff800000\n"
"v_mov_b32 v248,v255\n"
"s_mov_b32 s90,0\n"
"s_mov_b32 s88,0\n"
"v_mov_b32 v144,0\n"
"v_mov_b32 v145,0\n"
"v_mov_b32 v146,0\n"
"v_mov_b32 v147,0\n"
"v_mov_b32 v148,0\n"
"v_mov_b32 v149,0\n"
"v_mov_b32 v150,0\n"
"v_mov_b32 v151,0\n"
"v_mov_b32 v152,0\n"
"v_mov_b32 v153,0\n"
"v_mov_b32 v154,0\n"
"v_mov_b32 v155,0\n"
"v_mov_b32 v156,0\n"
"v_mov_b32 v157,0\n"
"v_mov_b32 v158,0\n"
"v_mov_b32 v159,0\n"
"v_mov_b32 v160,0\n"
"v_mov_b32 v161,0\n"
"v_mov_b32 v162,0\n"
"v_mov_b32 v163,0\n"
"v_mov_b32 v164,0\n"
"v_mov_b32 v165,0\n"
"v_mov_b32 v166,0\n"
"v_mov_b32 v167,0\n"
"v_mov_b32 v168,0\n"
"v_mov_b32 v169,0\n"
"v_mov_b32 v170,0\n"
"v_mov_b32 v171,0\n"
"v_mov_b32 v172,0\n"
"v_mov_b32 v173,0\n"
"v_mov_b32 v174,0\n"
"v_mov_b32 v175,0\n"
"v_accvgpr_write_b32 a140,0\n"
"v_accvgpr_write_b32 a141,0\n"
"v_accvgpr_write_b32 a142,0\n"
"v_accvgpr_write_b32 a143,0\n"
"L1070_%=:\n"
"s_lshl_b32 s91,s88,13\n"
SA"s91,s91,s39\n"
SA"s91,s91,0\n"
BL"a[148:151],v224,s[8:11],s91 offen offset:0 sc1\n"
BL"a[152:155],v224,s[8:11],s91 offen offset:1024 sc1\n"
BL"a[156:159],v224,s[8:11],s91 offen offset:2048 sc1\n"
BL"a[160:163],v224,s[8:11],s91 offen offset:3072 sc1\n"
SA"s91,s91,4096\n"
BL"a[164:167],v224,s[8:11],s91 offen offset:0 sc1\n"
BL"a[168:171],v224,s[8:11],s91 offen offset:1024 sc1\n"
BL"a[172:175],v224,s[8:11],s91 offen offset:2048 sc1\n"
BL"a[176:179],v224,s[8:11],s91 offen offset:3072 sc1\n"
WT"vmcnt(0)\n"
MF"v[240:243],a[148:151],a[32:35],0\n"
MF"v[240:243],a[152:155],a[36:39],v[240:243]\n"
MF"v[240:243],a[156:159],a[40:43],v[240:243]\n"
MF"v[240:243],a[160:163],a[44:47],v[240:243]\n"
MF"v[244:247],a[164:167],a[32:35],0\n"
MF"v[244:247],a[168:171],a[36:39],v[244:247]\n"
MF"v[244:247],a[172:175],a[40:43],v[244:247]\n"
MF"v[244:247],a[176:179],a[44:47],v[244:247]\n"
"s_lshl_b32 s91,s88,5\n"
"v_subrev_u32 v250,s91,v249\n"
"s_nop 7\n"
"v_cmp_gt_i32 vcc,0,v250\n"
"s_nop 1\n"
"v_cndmask_b32 v240,v240,v255,vcc\n"
"v_cmp_gt_i32 vcc,1,v250\n"
"s_nop 1\n"
"v_cndmask_b32 v241,v241,v255,vcc\n"
"v_cmp_gt_i32 vcc,2,v250\n"
"s_nop 1\n"
"v_cndmask_b32 v242,v242,v255,vcc\n"
"v_cmp_gt_i32 vcc,3,v250\n"
"s_nop 1\n"
"v_cndmask_b32 v243,v243,v255,vcc\n"
"v_cmp_gt_i32 vcc,16,v250\n"
"s_nop 1\n"
"v_cndmask_b32 v244,v244,v255,vcc\n"
"v_cmp_gt_i32 vcc,17,v250\n"
"s_nop 1\n"
"v_cndmask_b32 v245,v245,v255,vcc\n"
"v_cmp_gt_i32 vcc,18,v250\n"
"s_nop 1\n"
"v_cndmask_b32 v246,v246,v255,vcc\n"
"v_cmp_gt_i32 vcc,19,v250\n"
"s_nop 1\n"
"v_cndmask_b32 v247,v247,v255,vcc\n"
"s_cmp_lg_u32 s90,0\n"
"s_cbranch_scc1 L1071_%=\n"
"v_max3_f32 v248,v248,v240,v241\n"
"v_max3_f32 v248,v248,v242,v243\n"
"v_max3_f32 v248,v248,v244,v245\n"
"v_max3_f32 v248,v248,v246,v247\n"
"s_branch L1072_%=\n"
"L1071_%=:\n"
"s_lshl_b32 s91,s88,13\n"
SA"s91,s91,s39\n"
SA"s91,s91,524288\n"
BL"a[148:151],v224,s[8:11],s91 offen offset:0 sc1\n"
BL"a[152:155],v224,s[8:11],s91 offen offset:1024 sc1\n"
BL"a[156:159],v224,s[8:11],s91 offen offset:2048 sc1\n"
BL"a[160:163],v224,s[8:11],s91 offen offset:3072 sc1\n"
SA"s91,s91,4096\n"
BL"a[164:167],v224,s[8:11],s91 offen offset:0 sc1\n"
BL"a[168:171],v224,s[8:11],s91 offen offset:1024 sc1\n"
BL"a[172:175],v224,s[8:11],s91 offen offset:2048 sc1\n"
BL"a[176:179],v224,s[8:11],s91 offen offset:3072 sc1\n"
WT"vmcnt(0)\n"
"v_sub_f32 v240,v240,v248\n"
"v_sub_f32 v241,v241,v248\n"
"v_sub_f32 v242,v242,v248\n"
"v_sub_f32 v243,v243,v248\n"
"v_sub_f32 v244,v244,v248\n"
"v_sub_f32 v245,v245,v248\n"
"v_sub_f32 v246,v246,v248\n"
"v_sub_f32 v247,v247,v248\n"
EX"v240,v240\n"
EX"v241,v241\n"
EX"v242,v242\n"
EX"v243,v243\n"
EX"v244,v244\n"
EX"v245,v245\n"
EX"v246,v246\n"
EX"v247,v247\n"
"s_nop 0\n"
PK"v240,v240,v241\n"
PK"v241,v242,v243\n"
PK"v242,v244,v245\n"
PK"v243,v246,v247\n"
"s_nop 1\n"
MF"v[144:147],a[148:151],v[240:243],v[144:147]\n"
MF"v[148:151],a[152:155],v[240:243],v[148:151]\n"
MF"v[152:155],a[156:159],v[240:243],v[152:155]\n"
MF"v[156:159],a[160:163],v[240:243],v[156:159]\n"
MF"v[160:163],a[164:167],v[240:243],v[160:163]\n"
MF"v[164:167],a[168:171],v[240:243],v[164:167]\n"
MF"v[168:171],a[172:175],v[240:243],v[168:171]\n"
MF"v[172:175],a[176:179],v[240:243],v[172:175]\n"
MF"a[140:143],a[128:131],v[240:243],a[140:143]\n"
"s_nop 7\n"
"L1072_%=:\n"
SA"s88,s88,1\n"
"s_cmp_le_u32 s88,s89\n"
"s_cbranch_scc1 L1070_%=\n"
"s_cmp_lg_u32 s90,0\n"
"s_cbranch_scc1 L1073_%=\n"
"v_mov_b32 v254,v248\n"
"s_nop 1\n"
"v_permlane16_swap_b32 v254,v248\n"
"s_nop 1\n"
"v_max_f32 v248,v248,v254\n"
"v_mov_b32 v254,v248\n"
"s_nop 1\n"
"v_permlane32_swap_b32 v254,v248\n"
"s_nop 1\n"
"v_max_f32 v248,v248,v254\n"
"s_mov_b32 s90,1\n"
"s_mov_b32 s88,0\n"
"s_branch L1070_%=\n"
"L1073_%=:\n"
"s_nop 7\n"
"v_accvgpr_read_b32 v251,a140\n"
"s_nop 1\n"
"s_branch L1068_%=\n"
"L1075_%=:\n"
"s_nop 15\n"
WT"vmcnt(0)\n"
"s_lshl_b32 s89,s34,3\n"
"s_sub_u32 s89,120,s89\n"
SA"s89,s89,s30\n"
SA"s89,s89,4\n"
"s_lshl_b32 s91,s89,4\n"
"s_lshr_b32 s89,s89,1\n"
"v_bfe_u32 v249,v224,4,4\n"
"v_lshrrev_b32 v254,8,v224\n"
"v_lshlrev_b32 v254,2,v254\n"
"v_sub_u32 v249,v249,v254\n"
"v_add_u32 v249,s91,v249\n"
"v_mov_b32 v255,0xff800000\n"
"v_mov_b32 v248,v255\n"
"s_mov_b32 s90,0\n"
"s_mov_b32 s88,0\n"
"v_mov_b32 v176,0\n"
"v_mov_b32 v177,0\n"
"v_mov_b32 v178,0\n"
"v_mov_b32 v179,0\n"
"v_mov_b32 v180,0\n"
"v_mov_b32 v181,0\n"
"v_mov_b32 v182,0\n"
"v_mov_b32 v183,0\n"
"v_mov_b32 v184,0\n"
"v_mov_b32 v185,0\n"
"v_mov_b32 v186,0\n"
"v_mov_b32 v187,0\n"
"v_mov_b32 v188,0\n"
"v_mov_b32 v189,0\n"
"v_mov_b32 v190,0\n"
"v_mov_b32 v191,0\n"
"v_mov_b32 v192,0\n"
"v_mov_b32 v193,0\n"
"v_mov_b32 v194,0\n"
"v_mov_b32 v195,0\n"
"v_mov_b32 v196,0\n"
"v_mov_b32 v197,0\n"
"v_mov_b32 v198,0\n"
"v_mov_b32 v199,0\n"
"v_mov_b32 v200,0\n"
"v_mov_b32 v201,0\n"
"v_mov_b32 v202,0\n"
"v_mov_b32 v203,0\n"
"v_mov_b32 v204,0\n"
"v_mov_b32 v205,0\n"
"v_mov_b32 v206,0\n"
"v_mov_b32 v207,0\n"
"v_accvgpr_write_b32 a144,0\n"
"v_accvgpr_write_b32 a145,0\n"
"v_accvgpr_write_b32 a146,0\n"
"v_accvgpr_write_b32 a147,0\n"
"L1076_%=:\n"
"s_lshl_b32 s91,s88,13\n"
SA"s91,s91,s39\n"
SA"s91,s91,0\n"
BL"a[148:151],v224,s[8:11],s91 offen offset:0 sc1\n"
BL"a[152:155],v224,s[8:11],s91 offen offset:1024 sc1\n"
BL"a[156:159],v224,s[8:11],s91 offen offset:2048 sc1\n"
BL"a[160:163],v224,s[8:11],s91 offen offset:3072 sc1\n"
SA"s91,s91,4096\n"
BL"a[164:167],v224,s[8:11],s91 offen offset:0 sc1\n"
BL"a[168:171],v224,s[8:11],s91 offen offset:1024 sc1\n"
BL"a[172:175],v224,s[8:11],s91 offen offset:2048 sc1\n"
BL"a[176:179],v224,s[8:11],s91 offen offset:3072 sc1\n"
WT"vmcnt(0)\n"
MF"v[240:243],a[148:151],a[48:51],0\n"
MF"v[240:243],a[152:155],a[52:55],v[240:243]\n"
MF"v[240:243],a[156:159],a[56:59],v[240:243]\n"
MF"v[240:243],a[160:163],a[60:63],v[240:243]\n"
MF"v[244:247],a[164:167],a[48:51],0\n"
MF"v[244:247],a[168:171],a[52:55],v[244:247]\n"
MF"v[244:247],a[172:175],a[56:59],v[244:247]\n"
MF"v[244:247],a[176:179],a[60:63],v[244:247]\n"
"s_lshl_b32 s91,s88,5\n"
"v_subrev_u32 v250,s91,v249\n"
"s_nop 7\n"
"v_cmp_gt_i32 vcc,0,v250\n"
"s_nop 1\n"
"v_cndmask_b32 v240,v240,v255,vcc\n"
"v_cmp_gt_i32 vcc,1,v250\n"
"s_nop 1\n"
"v_cndmask_b32 v241,v241,v255,vcc\n"
"v_cmp_gt_i32 vcc,2,v250\n"
"s_nop 1\n"
"v_cndmask_b32 v242,v242,v255,vcc\n"
"v_cmp_gt_i32 vcc,3,v250\n"
"s_nop 1\n"
"v_cndmask_b32 v243,v243,v255,vcc\n"
"v_cmp_gt_i32 vcc,16,v250\n"
"s_nop 1\n"
"v_cndmask_b32 v244,v244,v255,vcc\n"
"v_cmp_gt_i32 vcc,17,v250\n"
"s_nop 1\n"
"v_cndmask_b32 v245,v245,v255,vcc\n"
"v_cmp_gt_i32 vcc,18,v250\n"
"s_nop 1\n"
"v_cndmask_b32 v246,v246,v255,vcc\n"
"v_cmp_gt_i32 vcc,19,v250\n"
"s_nop 1\n"
"v_cndmask_b32 v247,v247,v255,vcc\n"
"s_cmp_lg_u32 s90,0\n"
"s_cbranch_scc1 L1077_%=\n"
"v_max3_f32 v248,v248,v240,v241\n"
"v_max3_f32 v248,v248,v242,v243\n"
"v_max3_f32 v248,v248,v244,v245\n"
"v_max3_f32 v248,v248,v246,v247\n"
"s_branch L1078_%=\n"
"L1077_%=:\n"
"s_lshl_b32 s91,s88,13\n"
SA"s91,s91,s39\n"
SA"s91,s91,524288\n"
BL"a[148:151],v224,s[8:11],s91 offen offset:0 sc1\n"
BL"a[152:155],v224,s[8:11],s91 offen offset:1024 sc1\n"
BL"a[156:159],v224,s[8:11],s91 offen offset:2048 sc1\n"
BL"a[160:163],v224,s[8:11],s91 offen offset:3072 sc1\n"
SA"s91,s91,4096\n"
BL"a[164:167],v224,s[8:11],s91 offen offset:0 sc1\n"
BL"a[168:171],v224,s[8:11],s91 offen offset:1024 sc1\n"
BL"a[172:175],v224,s[8:11],s91 offen offset:2048 sc1\n"
BL"a[176:179],v224,s[8:11],s91 offen offset:3072 sc1\n"
WT"vmcnt(0)\n"
"v_sub_f32 v240,v240,v248\n"
"v_sub_f32 v241,v241,v248\n"
"v_sub_f32 v242,v242,v248\n"
"v_sub_f32 v243,v243,v248\n"
"v_sub_f32 v244,v244,v248\n"
"v_sub_f32 v245,v245,v248\n"
"v_sub_f32 v246,v246,v248\n"
"v_sub_f32 v247,v247,v248\n"
EX"v240,v240\n"
EX"v241,v241\n"
EX"v242,v242\n"
EX"v243,v243\n"
EX"v244,v244\n"
EX"v245,v245\n"
EX"v246,v246\n"
EX"v247,v247\n"
"s_nop 0\n"
PK"v240,v240,v241\n"
PK"v241,v242,v243\n"
PK"v242,v244,v245\n"
PK"v243,v246,v247\n"
"s_nop 1\n"
MF"v[176:179],a[148:151],v[240:243],v[176:179]\n"
MF"v[180:183],a[152:155],v[240:243],v[180:183]\n"
MF"v[184:187],a[156:159],v[240:243],v[184:187]\n"
MF"v[188:191],a[160:163],v[240:243],v[188:191]\n"
MF"v[192:195],a[164:167],v[240:243],v[192:195]\n"
MF"v[196:199],a[168:171],v[240:243],v[196:199]\n"
MF"v[200:203],a[172:175],v[240:243],v[200:203]\n"
MF"v[204:207],a[176:179],v[240:243],v[204:207]\n"
MF"a[144:147],a[128:131],v[240:243],a[144:147]\n"
"s_nop 7\n"
"L1078_%=:\n"
SA"s88,s88,1\n"
"s_cmp_le_u32 s88,s89\n"
"s_cbranch_scc1 L1076_%=\n"
"s_cmp_lg_u32 s90,0\n"
"s_cbranch_scc1 L1079_%=\n"
"v_mov_b32 v254,v248\n"
"s_nop 1\n"
"v_permlane16_swap_b32 v254,v248\n"
"s_nop 1\n"
"v_max_f32 v248,v248,v254\n"
"v_mov_b32 v254,v248\n"
"s_nop 1\n"
"v_permlane32_swap_b32 v254,v248\n"
"s_nop 1\n"
"v_max_f32 v248,v248,v254\n"
"s_mov_b32 s90,1\n"
"s_mov_b32 s88,0\n"
"s_branch L1076_%=\n"
"L1079_%=:\n"
"s_nop 7\n"
"v_accvgpr_read_b32 v252,a144\n"
"s_nop 1\n"
"s_branch L1074_%=\n"
"L1080_%=:\n"
"s_mov_b32 s54,0\n"
"L1082_%=:\n"
"s_sleep 2\n"
"buffer_load_dwordx2 v[240:241],v238,s[8:11],s63 offen sc1\n"
WT"vmcnt(0)\n"
SA"s54,s54,1\n"
"v_cmp_eq_u64 vcc,s[28:29],v[240:241]\n"
"s_cmp_eq_u64 vcc,exec\n"
"s_cbranch_scc1 L1081_%=\n"
"s_cmp_lt_u32 s54,1048576\n"
"s_cbranch_scc1 L1082_%=\n"
"s_branch L1081_%=\n"
"L1083_%=:\n"
"s_mov_b32 s54,0\n"
"L1085_%=:\n"
"s_sleep 2\n"
"buffer_load_dwordx2 v[240:241],v238,s[8:11],s63 offen sc1\n"
WT"vmcnt(0)\n"
SA"s54,s54,1\n"
"v_cmp_eq_u64 vcc,s[28:29],v[240:241]\n"
"s_cmp_eq_u64 vcc,exec\n"
"s_cbranch_scc1 L1084_%=\n"
"s_cmp_lt_u32 s54,1048576\n"
"s_cbranch_scc1 L1085_%=\n"
"s_branch L1084_%=\n"
"L1086_%=:\n"
"s_mov_b32 s54,0\n"
"L1088_%=:\n"
"s_sleep 2\n"
"buffer_load_dwordx2 v[240:241],v244,s[8:11],s92 offen sc1\n"
WT"vmcnt(0)\n"
SA"s54,s54,1\n"
"v_cmp_eq_u64 vcc,s[28:29],v[240:241]\n"
"s_cmp_eq_u64 vcc,exec\n"
"s_cbranch_scc1 L1087_%=\n"
"s_cmp_lt_u32 s54,1048576\n"
"s_cbranch_scc1 L1088_%=\n"
"s_branch L1087_%=\n"
"LTH_%=:\n"
RD"a[92:95],v227 offset:7168\n"
MF"v[0:3],a[64:67],v[80:83],0\n"
WT"lgkmcnt(4)\n"
"v_accvgpr_read_b32 v251,a140\n"
"v_subrev_u32 v253,0x30800000,v251\n"
MF"v[0:3],a[68:71],v[88:91],v[0:3]\n"
RD"a[96:99],v227 offset:8192\n"
MF"v[40:43],a[68:71],v[120:123],0\n"
"v_cmp_le_u32 vcc,0x1f000000,v253\n"
"s_cmp_lg_u64 vcc,0\n"
"s_cbranch_scc1 L1069_%=\n"
"L1068_%=:\n"
MF"v[40:43],a[64:67],v[112:115],v[40:43]\n"
"v_rcp_f32 v251,v251\n"
MF"v[4:7],a[72:75],v[80:83],0\n"
SA"s41,s41,32768\n"
RD"a[100:103],v227 offset:9216\n"
MU"v144,v251,v144\n"
MF"v[4:7],a[76:79],v[88:91],v[4:7]\n"
"s_cmp_eq_u32 s41,163840\n"
RD"a[104:107],v227 offset:10240\n"
MU"v145,v251,v145\n"
MF"v[44:47],a[76:79],v[120:123],0\n"
"s_cselect_b32 s41,0,s41\n"
MU"v146,v251,v146\n"
"v_add_u32 v226,s41,v224\n"
MF"v[44:47],a[72:75],v[112:115],v[44:47]\n"
WT"lgkmcnt(3)\n"
RD"a[108:111],v227 offset:11264\n"
MU"v147,v251,v147\n"
MF"v[8:11],a[80:83],v[80:83],0\n"
SA"s41,s41,32768\n"
MU"v148,v251,v148\n"
MU"v149,v251,v149\n"
MF"v[8:11],a[84:87],v[88:91],v[8:11]\n"
"s_cmp_eq_u32 s41,163840\n"
RD"a[112:115],v227 offset:12288\n"
MU"v150,v251,v150\n"
MU"v151,v251,v151\n"
MF"v[48:51],a[84:87],v[120:123],0\n"
"s_cselect_b32 s41,0,s41\n"
PK"v144,v144,v145\n"
"v_add_u32 v235,s41,v224\n"
PK"v145,v146,v147\n"
MF"v[48:51],a[80:83],v[112:115],v[48:51]\n"
RD"a[116:119],v227 offset:13312\n"
PK"v146,v148,v149\n"
MF"v[12:15],a[88:91],v[80:83],0\n"
SA"s41,s41,32768\n"
PK"v147,v150,v151\n"
MU"v152,v251,v152\n"
MF"v[12:15],a[92:95],v[88:91],v[12:15]\n"
"s_cmp_eq_u32 s41,163840\n"
BS"v[144:147],v224,s[8:11],s60 offen offset:0 sc1\n"
MU"v153,v251,v153\n"
MF"v[52:55],a[92:95],v[120:123],0\n"
"s_cselect_b32 s41,0,s41\n"
RD"a[120:123],v227 offset:14336\n"
"v_add_u32 v236,s41,v224\n"
MU"v154,v251,v154\n"
MF"v[52:55],a[88:91],v[112:115],v[52:55]\n"
WT"lgkmcnt(3)\n"
RD"a[124:127],v227 offset:15360\n"
MU"v155,v251,v155\n"
MU"v156,v251,v156\n"
MF"v[16:19],a[96:99],v[80:83],0\n"
SA"s41,s41,32768\n"
MU"v157,v251,v157\n"
MU"v158,v251,v158\n"
MU"v159,v251,v159\n"
MF"v[16:19],a[100:103],v[88:91],v[16:19]\n"
"s_cmp_eq_u32 s41,163840\n"
RD"a[64:67],v227 offset:16384\n"
PK"v152,v152,v153\n"
PK"v153,v154,v155\n"
MF"v[56:59],a[100:103],v[120:123],0\n"
"s_cselect_b32 s41,0,s41\n"
PK"v154,v156,v157\n"
"v_add_u32 v237,s41,v224\n"
PK"v155,v158,v159\n"
MF"v[56:59],a[96:99],v[112:115],v[56:59]\n"
BS"v[152:155],v224,s[8:11],s60 offen offset:1024 sc1\n"
MU"v160,v251,v160\n"
MF"v[20:23],a[104:107],v[80:83],0\n"
RD"a[68:71],v227 offset:17408\n"
MU"v161,v251,v161\n"
MU"v162,v251,v162\n"
MF"v[20:23],a[108:111],v[88:91],v[20:23]\n"
WT"vmcnt(2)\n"
"buffer_store_dwordx2 v[232:233],v230,s[8:11],s61 offen sc1\n"
MU"v163,v251,v163\n"
MF"v[60:63],a[108:111],v[120:123],0\n"
RD"a[72:75],v227 offset:18432\n"
MU"v164,v251,v164\n"
MU"v165,v251,v165\n"
MF"v[60:63],a[104:107],v[112:115],v[60:63]\n"
WT"lgkmcnt(3)\n"
RD"a[76:79],v227 offset:19456\n"
MU"v166,v251,v166\n"
MU"v167,v251,v167\n"
MF"v[24:27],a[112:115],v[80:83],0\n"
SA"s51,s60,16384\n"
"v_accvgpr_read_b32 v252,a144\n"
"v_subrev_u32 v253,0x30800000,v252\n"
PK"v160,v160,v161\n"
MF"v[24:27],a[116:119],v[88:91],v[24:27]\n"
"s_lshl_b32 s62,s34,14\n"
RD"a[80:83],v227 offset:20480\n"
PK"v161,v162,v163\n"
PK"v162,v164,v165\n"
MF"v[64:67],a[116:119],v[120:123],0\n"
"s_sub_u32 s62,s61,s62\n"
"v_cmp_le_u32 vcc,0x1f000000,v253\n"
"s_cmp_lg_u64 vcc,0\n"
"s_cbranch_scc1 L1075_%=\n"
"L1074_%=:\n"
MF"v[64:67],a[112:115],v[112:115],v[64:67]\n"
SA"s62,s62,122880\n"
"v_rcp_f32 v252,v252\n"
PK"v163,v166,v167\n"
MF"v[28:31],a[120:123],v[80:83],0\n"
BS"v[160:163],v224,s[8:11],s60 offen offset:2048 sc1\n"
MU"v168,v251,v168\n"
MF"v[28:31],a[124:127],v[88:91],v[28:31]\n"
RD"a[84:87],v227 offset:21504\n"
RD"a[88:91],v227 offset:22528\n"
MU"v169,v251,v169\n"
MF"v[68:71],a[124:127],v[120:123],0\n"
MU"v170,v251,v170\n"
MU"v171,v251,v171\n"
MU"v172,v251,v172\n"
MF"v[68:71],a[120:123],v[112:115],v[68:71]\n"
WT"lgkmcnt(3)\n"
RD"a[92:95],v227 offset:23552\n"
MU"v173,v251,v173\n"
MU"v174,v251,v174\n"
MF"v[32:35],a[64:67],v[80:83],0\n"
MU"v175,v251,v175\n"
PK"v168,v168,v169\n"
PK"v169,v170,v171\n"
MF"v[32:35],a[68:71],v[88:91],v[32:35]\n"
RD"a[96:99],v227 offset:24576\n"
MU"v176,v252,v176\n"
MU"v177,v252,v177\n"
MF"v[72:75],a[68:71],v[120:123],0\n"
PK"v170,v172,v173\n"
PK"v171,v174,v175\n"
MU"v178,v252,v178\n"
MF"v[72:75],a[64:67],v[112:115],v[72:75]\n"
BS"v[168:171],v224,s[8:11],s60 offen offset:3072 sc1\n"
MU"v179,v252,v179\n"
MF"v[36:39],a[72:75],v[80:83],0\n"
RD"a[100:103],v227 offset:25600\n"
MU"v180,v252,v180\n"
MU"v181,v252,v181\n"
MF"v[36:39],a[76:79],v[88:91],v[36:39]\n"
RD"a[104:107],v227 offset:26624\n"
MU"v182,v252,v182\n"
MU"v183,v252,v183\n"
MF"v[76:79],a[76:79],v[120:123],0\n"
PK"v176,v176,v177\n"
PK"v177,v178,v179\n"
PK"v178,v180,v181\n"
MF"v[76:79],a[72:75],v[112:115],v[76:79]\n"
WT"lgkmcnt(3)\n"
RD"a[108:111],v227 offset:27648\n"
PK"v179,v182,v183\n"
MU"v184,v252,v184\n"
MF"v[0:3],a[80:83],v[96:99],v[0:3]\n"
BS"v[176:179],v224,s[8:11],s51 offen offset:0 sc1\n"
MU"v185,v252,v185\n"
MF"v[0:3],a[84:87],v[104:107],v[0:3]\n"
RD"a[112:115],v227 offset:28672\n"
MU"v186,v252,v186\n"
MU"v187,v252,v187\n"
MF"v[40:43],a[84:87],v[136:139],v[40:43]\n"
MU"v188,v252,v188\n"
MU"v189,v252,v189\n"
MU"v190,v252,v190\n"
MF"v[40:43],a[80:83],v[128:131],v[40:43]\n"
RD"a[116:119],v227 offset:29696\n"
MU"v191,v252,v191\n"
PK"v184,v184,v185\n"
MF"v[4:7],a[88:91],v[96:99],v[4:7]\n"
PK"v185,v186,v187\n"
PK"v186,v188,v189\n"
PK"v187,v190,v191\n"
MF"v[4:7],a[92:95],v[104:107],v[4:7]\n"
BS"v[184:187],v224,s[8:11],s51 offen offset:1024 sc1\n"
MU"v192,v252,v192\n"
MF"v[44:47],a[92:95],v[136:139],v[44:47]\n"
RD"a[120:123],v227 offset:30720\n"
MU"v193,v252,v193\n"
MU"v194,v252,v194\n"
MF"v[44:47],a[88:91],v[128:131],v[44:47]\n"
WT"lgkmcnt(3)\n"
RD"a[124:127],v227 offset:31744\n"
"v_bfe_u32 v238,v224,4,2\n"
MU"v195,v252,v195\n"
MF"v[8:11],a[96:99],v[96:99],v[8:11]\n"
"s_andn2_b32 s63,s61,255\n"
"s_barrier\n"
MF"v[8:11],a[100:103],v[104:107],v[8:11]\n"
RD"a[64:67],v226 offset:0\n"
"v_lshlrev_b32 v238,6,v238\n"
MU"v196,v252,v196\n"
MF"v[48:51],a[100:103],v[136:139],v[48:51]\n"
"buffer_load_dwordx2 v[240:241],v238,s[8:11],s63 offen sc1\n"
MU"v197,v252,v197\n"
MF"v[48:51],a[96:99],v[128:131],v[48:51]\n"
RD"a[68:71],v226 offset:1024\n"
MU"v198,v252,v198\n"
MU"v199,v252,v199\n"
MF"v[12:15],a[104:107],v[96:99],v[12:15]\n"
PK"v192,v192,v193\n"
PK"v193,v194,v195\n"
PK"v194,v196,v197\n"
MF"v[12:15],a[108:111],v[104:107],v[12:15]\n"
RD"a[72:75],v226 offset:2048\n"
PK"v195,v198,v199\n"
MU"v200,v252,v200\n"
MF"v[52:55],a[108:111],v[136:139],v[52:55]\n"
BS"v[192:195],v224,s[8:11],s51 offen offset:2048 sc1\n"
MU"v201,v252,v201\n"
MF"v[52:55],a[104:107],v[128:131],v[52:55]\n"
WT"lgkmcnt(3)\n"
RD"a[76:79],v226 offset:3072\n"
MU"v202,v252,v202\n"
MU"v203,v252,v203\n"
MF"v[16:19],a[112:115],v[96:99],v[16:19]\n"
MU"v204,v252,v204\n"
MU"v205,v252,v205\n"
MU"v206,v252,v206\n"
MF"v[16:19],a[116:119],v[104:107],v[16:19]\n"
RD"a[80:83],v226 offset:4096\n"
MU"v207,v252,v207\n"
PK"v200,v200,v201\n"
MF"v[56:59],a[116:119],v[136:139],v[56:59]\n"
"v_readlane_b32 s43,v228,s42\n"
PK"v201,v202,v203\n"
PK"v202,v204,v205\n"
MF"v[56:59],a[112:115],v[128:131],v[56:59]\n"
RD"a[84:87],v226 offset:5120\n"
PK"v203,v206,v207\n"
MF"v[20:23],a[120:123],v[96:99],v[20:23]\n"
BS"v[200:203],v224,s[8:11],s51 offen offset:3072 sc1\n"
MF"v[20:23],a[124:127],v[104:107],v[20:23]\n"
RD"a[88:91],v226 offset:6144\n"
MF"v[60:63],a[124:127],v[136:139],v[60:63]\n"
"s_mov_b32 m0,s40\n"
MF"v[60:63],a[120:123],v[128:131],v[60:63]\n"
WT"lgkmcnt(3)\n"
RD"a[92:95],v226 offset:7168\n"
MF"v[24:27],a[64:67],v[96:99],v[24:27]\n"
"v_readlane_b32 s44,v229,s42\n"
MF"v[24:27],a[68:71],v[104:107],v[24:27]\n"
RD"a[96:99],v227 offset:0\n"
MF"v[64:67],a[68:71],v[136:139],v[64:67]\n"
SA"s42,s42,1\n"
BL"v225,s[8:11],s43 offen offset:0 sc1 lds\n"
MF"v[64:67],a[64:67],v[128:131],v[64:67]\n"
RD"a[100:103],v227 offset:1024\n"
MF"v[28:31],a[72:75],v[96:99],v[28:31]\n"
MF"v[28:31],a[76:79],v[104:107],v[28:31]\n"
RD"a[104:107],v227 offset:2048\n"
MF"v[68:71],a[76:79],v[136:139],v[68:71]\n"
MF"v[68:71],a[72:75],v[128:131],v[68:71]\n"
WT"lgkmcnt(3)\n"
RD"a[108:111],v227 offset:3072\n"
MF"v[32:35],a[80:83],v[96:99],v[32:35]\n"
BL"v225,s[8:11],s43 offen offset:1024 sc1 lds\n"
MF"v[32:35],a[84:87],v[104:107],v[32:35]\n"
RD"a[112:115],v227 offset:4096\n"
MF"v[72:75],a[84:87],v[136:139],v[72:75]\n"
MF"v[72:75],a[80:83],v[128:131],v[72:75]\n"
RD"a[116:119],v227 offset:5120\n"
MF"v[36:39],a[88:91],v[96:99],v[36:39]\n"
MF"v[36:39],a[92:95],v[104:107],v[36:39]\n"
RD"a[120:123],v227 offset:6144\n"
MF"v[76:79],a[92:95],v[136:139],v[76:79]\n"
BL"v225,s[8:11],s43 offen offset:2048 sc1 lds\n"
MF"v[76:79],a[88:91],v[128:131],v[76:79]\n"
WT"lgkmcnt(3)\n"
RD"a[124:127],v227 offset:7168\n"
MF"v[84:87],a[96:99],v[144:147],0\n"
MF"v[84:87],a[100:103],v[152:155],v[84:87]\n"
RD"a[64:67],v227 offset:8192\n"
MF"v[164:167],a[100:103],v[184:187],0\n"
MF"v[164:167],a[96:99],v[176:179],v[164:167]\n"
RD"a[68:71],v227 offset:9216\n"
MF"v[92:95],a[104:107],v[144:147],0\n"
BL"v225,s[8:11],s43 offen offset:3072 sc1 lds\n"
MF"v[92:95],a[108:111],v[152:155],v[92:95]\n"
RD"a[72:75],v227 offset:10240\n"
MF"v[172:175],a[108:111],v[184:187],0\n"
SA"m0,s40,16384\n"
MF"v[172:175],a[104:107],v[176:179],v[172:175]\n"
WT"lgkmcnt(3)\n"
RD"a[76:79],v227 offset:11264\n"
MF"v[100:103],a[112:115],v[144:147],0\n"
MF"v[100:103],a[116:119],v[152:155],v[100:103]\n"
SA"s48,s59,32768\n"
RD"a[80:83],v227 offset:12288\n"
MF"v[180:183],a[116:119],v[184:187],0\n"
"s_and_b32 s48,s48,0x18000\n"
BL"v225,s[8:11],s44 offen offset:0 sc1 lds\n"
MF"v[180:183],a[112:115],v[176:179],v[180:183]\n"
SA"s40,s40,32768\n"
RD"a[84:87],v227 offset:13312\n"
"s_andn2_b32 s49,s59,0x18000\n"
MF"v[108:111],a[120:123],v[144:147],0\n"
"s_cmp_eq_u32 s40,s46\n"
"s_cselect_b32 s40,s47,s40\n"
WT"vmcnt(7)\n"
"v_cmp_eq_u64 vcc,s[28:29],v[240:241]\n"
"s_cmp_eq_u64 vcc,exec\n"
"s_cbranch_scc0 L1080_%=\n"
"L1081_%=:\n"
MF"v[108:111],a[124:127],v[152:155],v[108:111]\n"
"s_or_b32 s52,s48,s49\n"
RD"a[88:91],v227 offset:14336\n"
SA"s53,s52,16384\n"
MF"v[188:191],a[124:127],v[184:187],0\n"
BL"a[0:3],v224,s[8:11],s52 offen offset:0 sc1\n"
MF"v[188:191],a[120:123],v[176:179],v[188:191]\n"
WT"lgkmcnt(3)\n"
RD"a[92:95],v227 offset:15360\n"
MF"v[116:119],a[64:67],v[144:147],0\n"
BL"v225,s[8:11],s44 offen offset:1024 sc1 lds\n"
MF"v[116:119],a[68:71],v[152:155],v[116:119]\n"
RD"a[96:99],v227 offset:16384\n"
MF"v[196:199],a[68:71],v[184:187],0\n"
BL"a[4:7],v224,s[8:11],s53 offen offset:0 sc1\n"
MF"v[196:199],a[64:67],v[176:179],v[196:199]\n"
RD"a[100:103],v227 offset:17408\n"
MF"v[124:127],a[72:75],v[144:147],0\n"
MF"v[124:127],a[76:79],v[152:155],v[124:127]\n"
RD"a[104:107],v227 offset:18432\n"
MF"v[204:207],a[76:79],v[184:187],0\n"
BL"v225,s[8:11],s44 offen offset:2048 sc1 lds\n"
MF"v[204:207],a[72:75],v[176:179],v[204:207]\n"
WT"lgkmcnt(3)\n"
RD"a[108:111],v227 offset:19456\n"
MF"v[132:135],a[80:83],v[144:147],0\n"
BL"a[8:11],v224,s[8:11],s52 offen offset:1024 sc1\n"
MF"v[132:135],a[84:87],v[152:155],v[132:135]\n"
RD"a[112:115],v227 offset:20480\n"
MF"v[208:211],a[84:87],v[184:187],0\n"
"v_readlane_b32 s43,v228,s42\n"
MF"v[208:211],a[80:83],v[176:179],v[208:211]\n"
RD"a[116:119],v227 offset:21504\n"
MF"v[140:143],a[88:91],v[144:147],0\n"
BL"a[12:15],v224,s[8:11],s53 offen offset:1024 sc1\n"
MF"v[140:143],a[92:95],v[152:155],v[140:143]\n"
BL"v225,s[8:11],s44 offen offset:3072 sc1 lds\n"
MF"v[212:215],a[92:95],v[184:187],0\n"
"s_mov_b32 m0,s40\n"
RD"a[120:123],v227 offset:22528\n"
MF"v[212:215],a[88:91],v[176:179],v[212:215]\n"
WT"lgkmcnt(3)\n"
RD"a[124:127],v227 offset:23552\n"
MF"v[148:151],a[96:99],v[144:147],0\n"
WT"vmcnt(12)\n"
"buffer_store_dwordx2 v[232:233],v230,s[8:11],s62 offen sc1\n"
MF"v[148:151],a[100:103],v[152:155],v[148:151]\n"
BL"a[16:19],v224,s[8:11],s52 offen offset:2048 sc1\n"
MF"v[216:219],a[100:103],v[184:187],0\n"
RD"a[64:67],v227 offset:24576\n"
"v_readlane_b32 s44,v229,s42\n"
MF"v[216:219],a[96:99],v[176:179],v[216:219]\n"
SA"s42,s42,1\n"
BL"v225,s[8:11],s43 offen offset:0 sc1 lds\n"
MF"v[156:159],a[104:107],v[144:147],0\n"
RD"a[68:71],v227 offset:25600\n"
MF"v[156:159],a[108:111],v[152:155],v[156:159]\n"
BL"a[20:23],v224,s[8:11],s53 offen offset:2048 sc1\n"
MF"v[220:223],a[108:111],v[184:187],0\n"
RD"a[72:75],v227 offset:26624\n"
MF"v[220:223],a[104:107],v[176:179],v[220:223]\n"
WT"lgkmcnt(3)\n"
RD"a[76:79],v227 offset:27648\n"
MF"v[84:87],a[112:115],v[160:163],v[84:87]\n"
"s_lshr_b32 s89,s31,3\n"
BL"v225,s[8:11],s43 offen offset:1024 sc1 lds\n"
MF"v[84:87],a[116:119],v[168:171],v[84:87]\n"
"s_lshl_b32 s89,s89,4\n"
RD"a[80:83],v227 offset:28672\n"
MF"v[164:167],a[116:119],v[200:203],v[164:167]\n"
SA"s89,s89,s30\n"
BL"a[24:27],v224,s[8:11],s52 offen offset:3072 sc1\n"
MF"v[164:167],a[112:115],v[192:195],v[164:167]\n"
"s_lshl_b32 s89,s89,3\n"
RD"a[84:87],v227 offset:29696\n"
MF"v[92:95],a[120:123],v[160:163],v[92:95]\n"
"s_xor_b32 s23,s32,s30\n"
MF"v[92:95],a[124:127],v[168:171],v[92:95]\n"
SA"s48,s59,65536\n"
RD"a[88:91],v227 offset:30720\n"
SA"s89,s89,s23\n"
MF"v[172:175],a[124:127],v[200:203],v[172:175]\n"
"s_and_b32 s48,s48,0x18000\n"
BL"a[28:31],v224,s[8:11],s53 offen offset:3072 sc1\n"
MF"v[172:175],a[120:123],v[192:195],v[172:175]\n"
WT"lgkmcnt(3)\n"
BL"v225,s[8:11],s43 offen offset:2048 sc1 lds\n"
MF"v[100:103],a[64:67],v[160:163],v[100:103]\n"
"s_xor_b32 s90,s89,36\n"
RD"a[92:95],v227 offset:31744\n"
"s_andn2_b32 s49,s59,0x18000\n"
MF"v[100:103],a[68:71],v[168:171],v[100:103]\n"
"s_mul_i32 s88,s89,5120\n"
RD"a[96:99],v226 offset:0\n"
"s_or_b32 s52,s48,s49\n"
MF"v[180:183],a[68:71],v[200:203],v[180:183]\n"
SA"s53,s52,16384\n"
BL"a[180:183],v224,s[8:11],s52 offen offset:0 sc1\n"
MF"v[180:183],a[64:67],v[192:195],v[180:183]\n"
"s_mul_i32 s22,s90,5120\n"
RD"a[100:103],v226 offset:1024\n"
"s_sub_u32 s90,s90,s89\n"
MF"v[108:111],a[72:75],v[160:163],v[108:111]\n"
"s_lshl_b32 s90,s90,6\n"
BL"v225,s[8:11],s43 offen offset:3072 sc1 lds\n"
MF"v[108:111],a[76:79],v[168:171],v[108:111]\n"
SA"s88,s88,59506688\n"
RD"a[104:107],v226 offset:2048\n"
SA"s22,s22,59506688\n"
MF"v[188:191],a[76:79],v[200:203],v[188:191]\n"
"s_cmp_eq_u32 s23,0\n"
BL"a[184:187],v224,s[8:11],s53 offen offset:0 sc1\n"
MF"v[188:191],a[72:75],v[192:195],v[188:191]\n"
WT"lgkmcnt(3)\n"
RD"a[108:111],v226 offset:3072\n"
"s_cselect_b32 s88,2130706432,s88\n"
MF"v[116:119],a[80:83],v[160:163],v[116:119]\n"
SA"m0,s40,16384\n"
"s_cmp_eq_u32 s23,4\n"
"s_cselect_b32 s22,2130706432,s22\n"
MF"v[116:119],a[84:87],v[168:171],v[116:119]\n"
"s_lshl_b32 s89,s89,6\n"
RD"a[112:115],v226 offset:4096\n"
"v_bfe_u32 v239,v224,1,6\n"
MF"v[196:199],a[84:87],v[200:203],v[196:199]\n"
SA"s89,s89,131072\n"
BL"v225,s[8:11],s44 offen offset:0 sc1 lds\n"
MF"v[196:199],a[80:83],v[192:195],v[196:199]\n"
SA"s40,s40,32768\n"
RD"a[116:119],v226 offset:5120\n"
"v_bfe_u32 v242,v224,7,1\n"
MF"v[124:127],a[88:91],v[160:163],v[124:127]\n"
"s_cmp_eq_u32 s40,s46\n"
BL"a[188:191],v224,s[8:11],s52 offen offset:1024 sc1\n"
MF"v[124:127],a[92:95],v[168:171],v[124:127]\n"
"s_cselect_b32 s40,s47,s40\n"
RD"a[120:123],v226 offset:6144\n"
"v_mad_u32_u24 v239,v242,s90,v239\n"
MF"v[204:207],a[92:95],v[200:203],v[204:207]\n"
"v_lshrrev_b32 v242,8,v224\n"
"v_lshl_add_u32 v239,v242,27,v239\n"
MF"v[204:207],a[88:91],v[192:195],v[204:207]\n"
WT"lgkmcnt(3)\n"
RD"a[124:127],v226 offset:7168\n"
MF"v[132:135],a[96:99],v[160:163],v[132:135]\n"
BL"a[192:195],v224,s[8:11],s53 offen offset:1024 sc1\n"
MF"v[132:135],a[100:103],v[168:171],v[132:135]\n"
BL"v225,s[8:11],s44 offen offset:1024 sc1 lds\n"
MF"v[208:211],a[100:103],v[200:203],v[208:211]\n"
RD"a[64:67],v226 offset:8192\n"
MF"v[208:211],a[96:99],v[192:195],v[208:211]\n"
RD"a[68:71],v226 offset:9216\n"
MF"v[140:143],a[104:107],v[160:163],v[140:143]\n"
BL"a[196:199],v224,s[8:11],s52 offen offset:2048 sc1\n"
MF"v[140:143],a[108:111],v[168:171],v[140:143]\n"
RD"a[72:75],v226 offset:10240\n"
MF"v[212:215],a[108:111],v[200:203],v[212:215]\n"
BL"v225,s[8:11],s44 offen offset:2048 sc1 lds\n"
MF"v[212:215],a[104:107],v[192:195],v[212:215]\n"
WT"lgkmcnt(3)\n"
RD"a[76:79],v226 offset:11264\n"
MF"v[148:151],a[112:115],v[160:163],v[148:151]\n"
BL"a[200:203],v224,s[8:11],s53 offen offset:2048 sc1\n"
MF"v[148:151],a[116:119],v[168:171],v[148:151]\n"
RD"a[80:83],v226 offset:12288\n"
MF"v[216:219],a[116:119],v[200:203],v[216:219]\n"
MF"v[216:219],a[112:115],v[192:195],v[216:219]\n"
RD"a[84:87],v226 offset:13312\n"
MF"v[156:159],a[120:123],v[160:163],v[156:159]\n"
BL"v225,s[8:11],s44 offen offset:3072 sc1 lds\n"
MF"v[156:159],a[124:127],v[168:171],v[156:159]\n"
RD"a[88:91],v226 offset:14336\n"
MF"v[220:223],a[124:127],v[200:203],v[220:223]\n"
BL"a[204:207],v224,s[8:11],s52 offen offset:3072 sc1\n"
MF"v[220:223],a[120:123],v[192:195],v[220:223]\n"
WT"vmcnt(26) lgkmcnt(3)\n"
RD"a[92:95],v226 offset:15360\n"
MF"v[0:3],a[64:67],a[0:3],v[0:3]\n"
WT"vmcnt(22)\n"
MF"v[0:3],a[68:71],a[8:11],v[0:3]\n"
WT"vmcnt(21)\n"
RD"a[96:99],v226 offset:16384\n"
SA"s48,s59,98304\n"
MF"v[40:43],a[68:71],a[12:15],v[40:43]\n"
"s_and_b32 s48,s48,0x18000\n"
BL"a[208:211],v224,s[8:11],s53 offen offset:3072 sc1\n"
MF"v[40:43],a[64:67],a[4:7],v[40:43]\n"
"s_andn2_b32 s49,s59,0x18000\n"
RD"a[100:103],v226 offset:17408\n"
MF"v[4:7],a[72:75],a[0:3],v[4:7]\n"
"s_or_b32 s52,s48,s49\n"
SA"s53,s52,16384\n"
MF"v[4:7],a[76:79],a[8:11],v[4:7]\n"
RD"a[104:107],v226 offset:18432\n"
MF"v[44:47],a[76:79],a[12:15],v[44:47]\n"
BL"a[212:215],v224,s[8:11],s52 offen offset:0 sc1\n"
MF"v[44:47],a[72:75],a[4:7],v[44:47]\n"
WT"lgkmcnt(3)\n"
RD"a[108:111],v226 offset:19456\n"
MF"v[8:11],a[80:83],a[0:3],v[8:11]\n"
MF"v[8:11],a[84:87],a[8:11],v[8:11]\n"
RD"a[112:115],v226 offset:20480\n"
MF"v[48:51],a[84:87],a[12:15],v[48:51]\n"
BL"a[216:219],v224,s[8:11],s53 offen offset:0 sc1\n"
MF"v[48:51],a[80:83],a[4:7],v[48:51]\n"
RD"a[116:119],v226 offset:21504\n"
MF"v[12:15],a[88:91],a[0:3],v[12:15]\n"
MF"v[12:15],a[92:95],a[8:11],v[12:15]\n"
RD"a[120:123],v226 offset:22528\n"
MF"v[52:55],a[92:95],a[12:15],v[52:55]\n"
MF"v[52:55],a[88:91],a[4:7],v[52:55]\n"
WT"lgkmcnt(3)\n"
RD"a[124:127],v226 offset:23552\n"
MF"v[16:19],a[96:99],a[0:3],v[16:19]\n"
BL"a[220:223],v224,s[8:11],s52 offen offset:1024 sc1\n"
MF"v[16:19],a[100:103],a[8:11],v[16:19]\n"
RD"a[64:67],v226 offset:24576\n"
MF"v[56:59],a[100:103],a[12:15],v[56:59]\n"
MF"v[56:59],a[96:99],a[4:7],v[56:59]\n"
RD"a[68:71],v226 offset:25600\n"
MF"v[20:23],a[104:107],a[0:3],v[20:23]\n"
BL"a[224:227],v224,s[8:11],s53 offen offset:1024 sc1\n"
MF"v[20:23],a[108:111],a[8:11],v[20:23]\n"
RD"a[72:75],v226 offset:26624\n"
MF"v[60:63],a[108:111],a[12:15],v[60:63]\n"
MF"v[60:63],a[104:107],a[4:7],v[60:63]\n"
WT"lgkmcnt(3)\n"
RD"a[76:79],v226 offset:27648\n"
MF"v[24:27],a[112:115],a[0:3],v[24:27]\n"
BL"a[228:231],v224,s[8:11],s52 offen offset:2048 sc1\n"
MF"v[24:27],a[116:119],a[8:11],v[24:27]\n"
RD"a[80:83],v226 offset:28672\n"
MF"v[64:67],a[116:119],a[12:15],v[64:67]\n"
MF"v[64:67],a[112:115],a[4:7],v[64:67]\n"
RD"a[84:87],v226 offset:29696\n"
MF"v[28:31],a[120:123],a[0:3],v[28:31]\n"
BL"a[232:235],v224,s[8:11],s53 offen offset:2048 sc1\n"
MF"v[28:31],a[124:127],a[8:11],v[28:31]\n"
RD"a[88:91],v226 offset:30720\n"
MF"v[68:71],a[124:127],a[12:15],v[68:71]\n"
MF"v[68:71],a[120:123],a[4:7],v[68:71]\n"
WT"lgkmcnt(3)\n"
RD"a[92:95],v226 offset:31744\n"
MF"v[32:35],a[64:67],a[0:3],v[32:35]\n"
"s_barrier\n"
MF"v[32:35],a[68:71],a[8:11],v[32:35]\n"
RD"a[96:99],v235 offset:0\n"
MF"v[72:75],a[68:71],a[12:15],v[72:75]\n"
BL"a[236:239],v224,s[8:11],s52 offen offset:3072 sc1\n"
MF"v[72:75],a[64:67],a[4:7],v[72:75]\n"
RD"a[100:103],v235 offset:1024\n"
MF"v[36:39],a[72:75],a[0:3],v[36:39]\n"
MF"v[36:39],a[76:79],a[8:11],v[36:39]\n"
RD"a[104:107],v235 offset:2048\n"
MF"v[76:79],a[76:79],a[12:15],v[76:79]\n"
BL"a[240:243],v224,s[8:11],s53 offen offset:3072 sc1\n"
MF"v[76:79],a[72:75],a[4:7],v[76:79]\n"
WT"vmcnt(27) lgkmcnt(3)\n"
RD"a[108:111],v235 offset:3072\n"
MF"v[0:3],a[80:83],a[16:19],v[0:3]\n"
WT"vmcnt(23)\n"
MF"v[0:3],a[84:87],a[24:27],v[0:3]\n"
WT"vmcnt(22)\n"
RD"a[112:115],v235 offset:4096\n"
MF"v[40:43],a[84:87],a[28:31],v[40:43]\n"
MF"v[40:43],a[80:83],a[20:23],v[40:43]\n"
RD"a[116:119],v235 offset:5120\n"
MF"v[4:7],a[88:91],a[16:19],v[4:7]\n"
MF"v[4:7],a[92:95],a[24:27],v[4:7]\n"
RD"a[120:123],v235 offset:6144\n"
MF"v[44:47],a[92:95],a[28:31],v[44:47]\n"
MF"v[44:47],a[88:91],a[20:23],v[44:47]\n"
WT"lgkmcnt(3)\n"
RD"a[124:127],v235 offset:7168\n"
MF"v[8:11],a[96:99],a[16:19],v[8:11]\n"
MF"v[8:11],a[100:103],a[24:27],v[8:11]\n"
RD"a[64:67],v235 offset:8192\n"
MF"v[48:51],a[100:103],a[28:31],v[48:51]\n"
MF"v[48:51],a[96:99],a[20:23],v[48:51]\n"
RD"a[68:71],v235 offset:9216\n"
MF"v[12:15],a[104:107],a[16:19],v[12:15]\n"
MF"v[12:15],a[108:111],a[24:27],v[12:15]\n"
RD"a[72:75],v235 offset:10240\n"
"v_bfe_u32 v238,v224,4,2\n"
MF"v[52:55],a[108:111],a[28:31],v[52:55]\n"
"s_andn2_b32 s63,s62,255\n"
"v_lshlrev_b32 v238,6,v238\n"
MF"v[52:55],a[104:107],a[20:23],v[52:55]\n"
WT"lgkmcnt(3)\n"
RD"a[76:79],v235 offset:11264\n"
MF"v[16:19],a[112:115],a[16:19],v[16:19]\n"
"buffer_load_dwordx2 v[240:241],v238,s[8:11],s63 offen sc1\n"
MF"v[16:19],a[116:119],a[24:27],v[16:19]\n"
RD"a[80:83],v235 offset:12288\n"
MF"v[56:59],a[116:119],a[28:31],v[56:59]\n"
MF"v[56:59],a[112:115],a[20:23],v[56:59]\n"
RD"a[84:87],v235 offset:13312\n"
MF"v[20:23],a[120:123],a[16:19],v[20:23]\n"
MF"v[20:23],a[124:127],a[24:27],v[20:23]\n"
RD"a[88:91],v235 offset:14336\n"
MF"v[60:63],a[124:127],a[28:31],v[60:63]\n"
MF"v[60:63],a[120:123],a[20:23],v[60:63]\n"
RD"a[92:95],v235 offset:15360\n"
"s_branch LTJ_%=\n"
"L1099_%=:\n"
"s_mov_b32 s54,0\n"
"L1101_%=:\n"
"s_sleep 2\n"
BL"v[240:243],v238,s[8:11],0 offen sc1\n"
WT"vmcnt(0)\n"
SA"s54,s54,1\n"
"v_cmp_eq_u64 vcc,s[28:29],v[240:241]\n"
"v_cmp_eq_u64_e64 s[50:51],s[28:29],v[242:243]\n"
"s_and_b64 vcc,vcc,s[50:51]\n"
"s_cmp_eq_u64 vcc,exec\n"
"s_cbranch_scc1 L1100_%=\n"
"s_cmp_lt_u32 s54,1048576\n"
"s_cbranch_scc1 L1101_%=\n"
"s_branch L1100_%=\n"
"L1102_%=:\n"
"s_mov_b32 s54,0\n"
"L1104_%=:\n"
"s_sleep 2\n"
"buffer_load_dwordx2 v[240:241],v238,s[8:11],s50 offen sc1\n"
WT"vmcnt(0)\n"
SA"s54,s54,1\n"
"v_cmp_eq_u64 vcc,s[28:29],v[240:241]\n"
"s_cmp_eq_u64 vcc,exec\n"
"s_cbranch_scc1 L1103_%=\n"
"s_cmp_lt_u32 s54,1048576\n"
"s_cbranch_scc1 L1104_%=\n"
"s_branch L1103_%=\n"
"L1105_%=:\n"
"s_mov_b32 s54,0\n"
"L1107_%=:\n"
"s_sleep 2\n"
"buffer_load_dwordx2 v[244:245],v242,s[8:11],s23 offen sc1\n"
WT"vmcnt(0)\n"
SA"s54,s54,1\n"
"v_cmp_eq_u64 vcc,s[28:29],v[244:245]\n"
"s_cmp_eq_u64 vcc,exec\n"
"s_cbranch_scc1 L1106_%=\n"
"s_cmp_lt_u32 s54,1048576\n"
"s_cbranch_scc1 L1107_%=\n"
"s_branch L1106_%=\n"
"LFA_%=:\n"
WT"lgkmcnt(6)\n"
MF"v[0:3],a[64:67],a[0:3],v[208:211]\n"
WT"lgkmcnt(3)\n"
PK"v192,v192,v193\n"
PK"v193,v194,v195\n"
MF"v[0:3],a[68:71],a[4:7],v[0:3]\n"
"s_mov_b32 m0,s40\n"
"v_readlane_b32 s43,v228,s42\n"
PK"v194,v196,v197\n"
MF"v[8:11],a[68:71],a[20:23],0\n"
PK"v195,v198,v199\n"
"v_readlane_b32 s44,v229,s42\n"
MF"v[8:11],a[64:67],a[16:19],v[8:11]\n"
SA"s42,s42,1\n"
BL"v225,s[8:11],s43 offen offset:0 sc1 lds\n"
MF"v[16:19],a[64:67],a[32:35],0\n"
"v_accvgpr_write_b32 a44,v192\n"
"v_accvgpr_write_b32 a45,v193\n"
MF"v[16:19],a[68:71],a[36:39],v[16:19]\n"
RD"a[92:95],v226 offset:7168\n"
"v_accvgpr_write_b32 a46,v194\n"
MF"v[24:27],a[68:71],a[52:55],0\n"
"v_accvgpr_write_b32 a47,v195\n"
PK"v200,v200,v201\n"
MF"v[24:27],a[64:67],a[48:51],v[24:27]\n"
PK"v201,v202,v203\n"
PK"v202,v204,v205\n"
"v_mov_b32 v227,v226\n"
MF"v[0:3],a[72:75],a[8:11],v[0:3]\n"
BL"v225,s[8:11],s43 offen offset:1024 sc1 lds\n"
PK"v203,v206,v207\n"
MF"v[0:3],a[76:79],a[12:15],v[0:3]\n"
"v_accvgpr_write_b32 a60,v200\n"
"v_accvgpr_write_b32 a61,v201\n"
MF"v[8:11],a[76:79],a[28:31],v[8:11]\n"
"s_mul_i32 s48,s32,655360\n"
"v_accvgpr_write_b32 a62,v202\n"
"v_accvgpr_write_b32 a63,v203\n"
MF"v[8:11],a[72:75],a[24:27],v[8:11]\n"
SA"s12,s74,s48\n"
"s_addc_u32 s13,s75,0\n"
MF"v[16:19],a[72:75],a[40:43],v[16:19]\n"
"s_and_b32 s13,s13,0xffff\n"
MF"v[16:19],a[76:79],a[44:47],v[16:19]\n"
"s_mov_b32 s14,655360\n"
RD"a[96:99],v226 offset:8192\n"
MF"v[24:27],a[76:79],a[60:63],v[24:27]\n"
"s_lshl_b32 s48,s33,3\n"
BL"v225,s[8:11],s43 offen offset:2048 sc1 lds\n"
MF"v[24:27],a[72:75],a[56:59],v[24:27]\n"
WT"lgkmcnt(1)\n"
EX"v0,v0\n"
MF"v[4:7],a[80:83],a[0:3],v[212:215]\n"
"s_and_b32 s49,s34,2\n"
EX"v1,v1\n"
MF"v[4:7],a[84:87],a[4:7],v[4:7]\n"
"s_lshl_b32 s49,s49,1\n"
EX"v2,v2\n"
MF"v[12:15],a[84:87],a[20:23],0\n"
SA"s48,s48,s49\n"
RD"a[100:103],v226 offset:9216\n"
PK"v0,v0,v1\n"
MF"v[12:15],a[80:83],a[16:19],v[12:15]\n"
SA"s48,s48,s30\n"
EX"v3,v3\n"
MF"v[20:23],a[80:83],a[32:35],0\n"
"s_mul_i32 s48,s48,5120\n"
EX"v8,v8\n"
MF"v[20:23],a[84:87],a[36:39],v[20:23]\n"
"s_or_b32 s49,s34,2\n"
BL"v225,s[8:11],s43 offen offset:3072 sc1 lds\n"
MF"v[28:31],a[84:87],a[52:55],0\n"
"s_cmp_eq_u32 s49,7\n"
EX"v9,v9\n"
PK"v1,v2,v3\n"
MF"v[28:31],a[80:83],a[48:51],v[28:31]\n"
"s_cselect_b32 s55,s48,2130706432\n"
RD"a[104:107],v226 offset:10240\n"
PK"v8,v8,v9\n"
MF"v[4:7],a[88:91],a[8:11],v[4:7]\n"
SA"m0,s40,16384\n"
EX"v10,v10\n"
MF"v[4:7],a[92:95],a[12:15],v[4:7]\n"
EX"v11,v11\n"
MF"v[12:15],a[92:95],a[28:31],v[12:15]\n"
EX"v16,v16\n"
MF"v[12:15],a[88:91],a[24:27],v[12:15]\n"
SA"s40,s40,32768\n"
RD"a[108:111],v226 offset:11264\n"
PK"v9,v10,v11\n"
MF"v[20:23],a[88:91],a[40:43],v[20:23]\n"
"s_cmp_eq_u32 s40,s46\n"
BL"v225,s[8:11],s44 offen offset:0 sc1 lds\n"
EX"v17,v17\n"
MF"v[20:23],a[92:95],a[44:47],v[20:23]\n"
"s_cselect_b32 s40,s47,s40\n"
EX"v18,v18\n"
MF"v[28:31],a[92:95],a[60:63],v[28:31]\n"
EX"v19,v19\n"
PK"v16,v16,v17\n"
MF"v[28:31],a[88:91],a[56:59],v[28:31]\n"
WT"lgkmcnt(1)\n"
RD"a[112:115],v226 offset:12288\n"
PK"v17,v18,v19\n"
MF"v[32:35],a[96:99],a[0:3],v[216:219]\n"
EX"v24,v24\n"
MF"v[32:35],a[100:103],a[4:7],v[32:35]\n"
EX"v25,v25\n"
MF"v[40:43],a[100:103],a[20:23],0\n"
BL"v225,s[8:11],s44 offen offset:1024 sc1 lds\n"
EX"v26,v26\n"
MF"v[40:43],a[96:99],a[16:19],v[40:43]\n"
RD"a[116:119],v226 offset:13312\n"
PK"v24,v24,v25\n"
MF"v[48:51],a[96:99],a[32:35],0\n"
EX"v27,v27\n"
MF"v[48:51],a[100:103],a[36:39],v[48:51]\n"
EX"v4,v4\n"
MF"v[56:59],a[100:103],a[52:55],0\n"
EX"v5,v5\n"
PK"v25,v26,v27\n"
MF"v[56:59],a[96:99],a[48:51],v[56:59]\n"
RD"a[120:123],v226 offset:14336\n"
PK"v2,v4,v5\n"
MF"v[32:35],a[104:107],a[8:11],v[32:35]\n"
WT"lgkmcnt(2)\n"
BL"v225,s[8:11],s44 offen offset:2048 sc1 lds\n"
EX"v6,v6\n"
MF"v[32:35],a[108:111],a[12:15],v[32:35]\n"
EX"v7,v7\n"
MF"v[40:43],a[108:111],a[28:31],v[40:43]\n"
EX"v12,v12\n"
MF"v[40:43],a[104:107],a[24:27],v[40:43]\n"
RD"a[124:127],v226 offset:15360\n"
"v_readlane_b32 s43,v228,s42\n"
PK"v3,v6,v7\n"
MF"v[48:51],a[104:107],a[40:43],v[48:51]\n"
EX"v13,v13\n"
MF"v[48:51],a[108:111],a[44:47],v[48:51]\n"
EX"v14,v14\n"
MF"v[56:59],a[108:111],a[60:63],v[56:59]\n"
BL"v225,s[8:11],s44 offen offset:3072 sc1 lds\n"
EX"v15,v15\n"
MF"v[56:59],a[104:107],a[56:59],v[56:59]\n"
"s_mov_b32 m0,s40\n"
RD"a[96:99],v227 offset:24576\n"
PK"v10,v12,v13\n"
MF"v[36:39],a[112:115],a[0:3],v[220:223]\n"
WT"lgkmcnt(2)\n"
EX"v20,v20\n"
PK"v11,v14,v15\n"
MF"v[36:39],a[116:119],a[4:7],v[36:39]\n"
EX"v21,v21\n"
MF"v[44:47],a[116:119],a[20:23],0\n"
EX"v22,v22\n"
MF"v[44:47],a[112:115],a[16:19],v[44:47]\n"
RD"a[100:103],v227 offset:25600\n"
"v_readlane_b32 s44,v229,s42\n"
PK"v18,v20,v21\n"
MF"v[52:55],a[112:115],a[32:35],0\n"
SA"s42,s42,1\n"
BL"v225,s[8:11],s43 offen offset:0 sc1 lds\n"
MF"v[52:55],a[116:119],a[36:39],v[52:55]\n"
EX"v23,v23\n"
MF"v[60:63],a[116:119],a[52:55],0\n"
EX"v28,v28\n"
MF"v[60:63],a[112:115],a[48:51],v[60:63]\n"
RD"a[104:107],v227 offset:26624\n"
PK"v19,v22,v23\n"
MF"v[36:39],a[120:123],a[8:11],v[36:39]\n"
WT"lgkmcnt(2)\n"
EX"v29,v29\n"
MF"v[36:39],a[124:127],a[12:15],v[36:39]\n"
BL"v225,s[8:11],s43 offen offset:1024 sc1 lds\n"
MF"v[44:47],a[124:127],a[28:31],v[44:47]\n"
EX"v30,v30\n"
PK"v26,v28,v29\n"
MF"v[44:47],a[120:123],a[24:27],v[44:47]\n"
RD"a[108:111],v227 offset:27648\n"
MF"v[52:55],a[120:123],a[40:43],v[52:55]\n"
EX"v31,v31\n"
MF"v[52:55],a[124:127],a[44:47],v[52:55]\n"
EX"v32,v32\n"
MF"v[60:63],a[124:127],a[60:63],v[60:63]\n"
EX"v33,v33\n"
PK"v27,v30,v31\n"
MF"v[60:63],a[120:123],a[56:59],v[60:63]\n"
SA"s41,s41,32768\n"
BL"v225,s[8:11],s43 offen offset:2048 sc1 lds\n"
RD"a[112:115],v227 offset:28672\n"
MF"v[80:83],a[96:99],v[0:3],0\n"
"s_cmp_eq_u32 s41,163840\n"
EX"v34,v34\n"
MF"v[112:115],a[96:99],v[8:11],0\n"
"s_cselect_b32 s41,0,s41\n"
EX"v35,v35\n"
MF"v[144:147],a[96:99],v[16:19],0\n"
EX"v40,v40\n"
"v_add_u32 v226,s41,v224\n"
MF"v[176:179],a[96:99],v[24:27],0\n"
WT"lgkmcnt(1)\n"
RD"a[116:119],v227 offset:29696\n"
MF"v[84:87],a[100:103],v[0:3],0\n"
EX"v41,v41\n"
MF"v[116:119],a[100:103],v[8:11],0\n"
BL"v225,s[8:11],s43 offen offset:3072 sc1 lds\n"
MF"v[148:151],a[100:103],v[16:19],0\n"
SA"m0,s40,16384\n"
EX"v42,v42\n"
MF"v[180:183],a[100:103],v[24:27],0\n"
RD"a[120:123],v227 offset:30720\n"
MF"v[88:91],a[104:107],v[0:3],0\n"
EX"v43,v43\n"
MF"v[120:123],a[104:107],v[8:11],0\n"
EX"v48,v48\n"
MF"v[152:155],a[104:107],v[16:19],0\n"
SA"s40,s40,32768\n"
EX"v49,v49\n"
MF"v[184:187],a[104:107],v[24:27],0\n"
"s_cmp_eq_u32 s40,s46\n"
BL"v225,s[8:11],s44 offen offset:0 sc1 lds\n"
RD"a[124:127],v227 offset:31744\n"
MF"v[92:95],a[108:111],v[0:3],0\n"
"s_cselect_b32 s40,s47,s40\n"
EX"v50,v50\n"
MF"v[124:127],a[108:111],v[8:11],0\n"
EX"v51,v51\n"
MF"v[156:159],a[108:111],v[16:19],0\n"
EX"v56,v56\n"
MF"v[188:191],a[108:111],v[24:27],0\n"
WT"lgkmcnt(1)\n"
EX"v57,v57\n"
MF"v[96:99],a[112:115],v[0:3],0\n"
EX"v58,v58\n"
MF"v[128:131],a[112:115],v[8:11],0\n"
BL"v225,s[8:11],s44 offen offset:1024 sc1 lds\n"
MF"v[160:163],a[112:115],v[16:19],0\n"
EX"v59,v59\n"
MF"v[192:195],a[112:115],v[24:27],0\n"
EX"v36,v36\n"
MF"v[100:103],a[116:119],v[0:3],0\n"
WT"vmcnt(16) lgkmcnt(0)\n"
"s_barrier\n"
MF"v[132:135],a[116:119],v[8:11],0\n"
RD"a[64:67],v226 offset:0\n"
RD"a[68:71],v226 offset:1024\n"
"buffer_store_dwordx2 v[232:233],v230,s[8:11],s98 offen sc1\n"
MF"v[164:167],a[116:119],v[16:19],0\n"
EX"v37,v37\n"
MF"v[196:199],a[116:119],v[24:27],0\n"
BL"v225,s[8:11],s44 offen offset:2048 sc1 lds\n"
MF"v[104:107],a[120:123],v[0:3],0\n"
EX"v38,v38\n"
MF"v[136:139],a[120:123],v[8:11],0\n"
EX"v39,v39\n"
MF"v[168:171],a[120:123],v[16:19],0\n"
EX"v44,v44\n"
MF"v[200:203],a[120:123],v[24:27],0\n"
RD"a[72:75],v226 offset:2048\n"
RD"a[76:79],v226 offset:3072\n"
MF"v[108:111],a[124:127],v[0:3],0\n"
EX"v45,v45\n"
MF"v[140:143],a[124:127],v[8:11],0\n"
BL"v225,s[8:11],s44 offen offset:3072 sc1 lds\n"
MF"v[172:175],a[124:127],v[16:19],0\n"
EX"v46,v46\n"
MF"v[204:207],a[124:127],v[24:27],0\n"
RD"a[80:83],v226 offset:4096\n"
MF"a[132:135],a[128:131],v[0:3],0\n"
EX"v47,v47\n"
MF"a[136:139],a[128:131],v[8:11],0\n"
"buffer_load_dword v255,v253,s[12:15],s55 offen\n"
EX"v52,v52\n"
MF"a[140:143],a[128:131],v[16:19],0\n"
SA"s55,s55,163840\n"
EX"v53,v53\n"
MF"a[144:147],a[128:131],v[24:27],0\n"
RD"a[84:87],v226 offset:5120\n"
RD"a[88:91],v226 offset:6144\n"
EX"v54,v54\n"
EX"v55,v55\n"
EX"v60,v60\n"
EX"v61,v61\n"
EX"v62,v62\n"
EX"v63,v63\n"
"s_branch LDB_%=\n"
"LD2_%=:\n"
WT"lgkmcnt(6)\n"
MF"v[0:3],a[64:67],a[32:35],v[208:211]\n"
WT"lgkmcnt(3)\n"
EX"v56,v56\n"
MF"v[0:3],a[68:71],a[36:39],v[0:3]\n"
"s_mov_b32 m0,s40\n"
EX"v57,v57\n"
MF"v[8:11],a[68:71],a[52:55],0\n"
RD"a[92:95],v226 offset:7168\n"
"v_readlane_b32 s43,v228,s42\n"
MF"v[8:11],a[64:67],a[48:51],v[8:11]\n"
EX"v58,v58\n"
MF"v[0:3],a[72:75],a[40:43],v[0:3]\n"
SA"s41,s41,32768\n"
RD"a[96:99],v226 offset:8192\n"
RD"a[100:103],v226 offset:9216\n"
"v_readlane_b32 s44,v229,s42\n"
MF"v[0:3],a[76:79],a[44:47],v[0:3]\n"
SA"s42,s42,1\n"
BL"v225,s[8:11],s43 offen offset:0 sc1 lds\n"
PK"v76,v56,v57\n"
MF"v[8:11],a[76:79],a[60:63],v[8:11]\n"
"s_cmp_eq_u32 s41,163840\n"
EX"v59,v59\n"
MF"v[8:11],a[72:75],a[56:59],v[8:11]\n"
WT"lgkmcnt(3)\n"
RD"a[104:107],v226 offset:10240\n"
PK"v77,v58,v59\n"
MF"v[4:7],a[80:83],a[32:35],v[212:215]\n"
"s_cselect_b32 s41,0,s41\n"
EX"v60,v60\n"
"v_add_u32 v227,s41,v224\n"
MF"v[4:7],a[84:87],a[36:39],v[4:7]\n"
EX"v61,v61\n"
MF"v[12:15],a[84:87],a[52:55],0\n"
BL"v225,s[8:11],s43 offen offset:1024 sc1 lds\n"
MF"v[12:15],a[80:83],a[48:51],v[12:15]\n"
RD"a[108:111],v226 offset:11264\n"
RD"a[112:115],v226 offset:12288\n"
MF"v[4:7],a[88:91],a[40:43],v[4:7]\n"
WT"lgkmcnt(3)\n"
EX"v62,v62\n"
PK"v78,v60,v61\n"
MF"v[4:7],a[92:95],a[44:47],v[4:7]\n"
RD"a[116:119],v226 offset:13312\n"
RD"a[120:123],v226 offset:14336\n"
MF"v[12:15],a[92:95],a[60:63],v[12:15]\n"
EX"v63,v63\n"
MF"v[12:15],a[88:91],a[56:59],v[12:15]\n"
BL"v225,s[8:11],s43 offen offset:2048 sc1 lds\n"
MF"v[16:19],a[96:99],a[32:35],v[216:219]\n"
EX"v0,v0\n"
PK"v79,v62,v63\n"
MF"v[16:19],a[100:103],a[36:39],v[16:19]\n"
EX"v1,v1\n"
MF"v[24:27],a[100:103],a[52:55],0\n"
RD"a[124:127],v226 offset:15360\n"
RD"a[64:67],v226 offset:16384\n"
MF"v[24:27],a[96:99],a[48:51],v[24:27]\n"
WT"lgkmcnt(4)\n"
EX"v2,v2\n"
PK"v0,v0,v1\n"
MF"v[16:19],a[104:107],a[40:43],v[16:19]\n"
BL"v225,s[8:11],s43 offen offset:3072 sc1 lds\n"
RD"a[68:71],v226 offset:17408\n"
MF"v[16:19],a[108:111],a[44:47],v[16:19]\n"
SA"m0,s40,16384\n"
RD"a[72:75],v226 offset:18432\n"
RD"a[76:79],v226 offset:19456\n"
EX"v3,v3\n"
MF"v[24:27],a[108:111],a[60:63],v[24:27]\n"
RD"a[80:83],v226 offset:20480\n"
RD"a[84:87],v226 offset:21504\n"
MF"v[24:27],a[104:107],a[56:59],v[24:27]\n"
EX"v8,v8\n"
PK"v1,v2,v3\n"
MF"v[20:23],a[112:115],a[32:35],v[220:223]\n"
WT"lgkmcnt(7)\n"
EX"v9,v9\n"
MF"v[20:23],a[116:119],a[36:39],v[20:23]\n"
SA"s40,s40,32768\n"
BL"v225,s[8:11],s44 offen offset:0 sc1 lds\n"
MF"v[28:31],a[116:119],a[52:55],0\n"
"s_cmp_eq_u32 s40,s46\n"
RD"a[88:91],v226 offset:22528\n"
RD"a[92:95],v226 offset:23552\n"
EX"v10,v10\n"
MF"v[28:31],a[112:115],a[48:51],v[28:31]\n"
"s_cselect_b32 s40,s47,s40\n"
RD"a[96:99],v226 offset:24576\n"
RD"a[100:103],v226 offset:25600\n"
PK"v8,v8,v9\n"
MF"v[20:23],a[120:123],a[40:43],v[20:23]\n"
WT"lgkmcnt(6)\n"
EX"v11,v11\n"
MF"v[20:23],a[124:127],a[44:47],v[20:23]\n"
RD"a[104:107],v226 offset:26624\n"
RD"a[108:111],v226 offset:27648\n"
MF"v[28:31],a[124:127],a[60:63],v[28:31]\n"
BL"v225,s[8:11],s44 offen offset:1024 sc1 lds\n"
EX"v4,v4\n"
MF"v[28:31],a[120:123],a[56:59],v[28:31]\n"
RD"a[112:115],v226 offset:28672\n"
RD"a[116:119],v226 offset:29696\n"
PK"v9,v10,v11\n"
MF"v[40:43],a[64:67],a[48:51],v[208:211]\n"
EX"v5,v5\n"
MF"v[40:43],a[68:71],a[52:55],v[40:43]\n"
EX"v6,v6\n"
MF"v[40:43],a[72:75],a[56:59],v[40:43]\n"
RD"a[120:123],v226 offset:30720\n"
RD"a[124:127],v226 offset:31744\n"
PK"v2,v4,v5\n"
MF"v[40:43],a[76:79],a[60:63],v[40:43]\n"
WT"lgkmcnt(6)\n"
BL"v225,s[8:11],s44 offen offset:2048 sc1 lds\n"
EX"v7,v7\n"
MF"v[44:47],a[80:83],a[48:51],v[212:215]\n"
WT"vmcnt(19)\n"
"s_barrier\n"
MF"v[44:47],a[84:87],a[52:55],v[44:47]\n"
EX"v12,v12\n"
PK"v3,v6,v7\n"
MF"v[44:47],a[88:91],a[56:59],v[44:47]\n"
EX"v13,v13\n"
MF"v[44:47],a[92:95],a[60:63],v[44:47]\n"
BL"v225,s[8:11],s44 offen offset:3072 sc1 lds\n"
"v_readlane_b32 s43,v228,s42\n"
MF"v[56:59],a[96:99],a[48:51],v[216:219]\n"
"s_mov_b32 m0,s40\n"
RD"a[96:99],v227 offset:8192\n"
PK"v10,v12,v13\n"
EX"v14,v14\n"
MF"v[56:59],a[100:103],a[52:55],v[56:59]\n"
WT"lgkmcnt(1)\n"
RD"a[100:103],v227 offset:9216\n"
MF"v[56:59],a[104:107],a[56:59],v[56:59]\n"
EX"v15,v15\n"
MF"v[56:59],a[108:111],a[60:63],v[56:59]\n"
RD"a[104:107],v227 offset:10240\n"
RD"a[108:111],v227 offset:11264\n"
MF"v[60:63],a[112:115],a[48:51],v[220:223]\n"
EX"v16,v16\n"
PK"v11,v14,v15\n"
MF"v[60:63],a[116:119],a[52:55],v[60:63]\n"
EX"v17,v17\n"
"v_readlane_b32 s44,v229,s42\n"
MF"v[60:63],a[120:123],a[56:59],v[60:63]\n"
SA"s42,s42,1\n"
BL"v225,s[8:11],s43 offen offset:0 sc1 lds\n"
MF"v[60:63],a[124:127],a[60:63],v[60:63]\n"
WT"lgkmcnt(3)\n"
EX"v18,v18\n"
MF"v[112:115],a[96:99],v[68:71],v[112:115]\n"
SA"s41,s41,32768\n"
RD"a[112:115],v227 offset:12288\n"
PK"v16,v16,v17\n"
MF"v[144:147],a[96:99],v[72:75],v[144:147]\n"
"s_cmp_eq_u32 s41,163840\n"
EX"v19,v19\n"
MF"v[176:179],a[96:99],v[76:79],v[176:179]\n"
WT"lgkmcnt(1)\n"
EX"v24,v24\n"
MF"v[116:119],a[100:103],v[68:71],v[116:119]\n"
"s_cselect_b32 s41,0,s41\n"
BL"v225,s[8:11],s43 offen offset:1024 sc1 lds\n"
RD"a[116:119],v227 offset:13312\n"
MF"v[148:151],a[100:103],v[72:75],v[148:151]\n"
EX"v25,v25\n"
"v_add_u32 v226,s41,v224\n"
MF"v[180:183],a[100:103],v[76:79],v[180:183]\n"
RD"a[120:123],v227 offset:14336\n"
PK"v17,v18,v19\n"
MF"v[120:123],a[104:107],v[68:71],v[120:123]\n"
EX"v26,v26\n"
MF"v[152:155],a[104:107],v[72:75],v[152:155]\n"
EX"v27,v27\n"
PK"v24,v24,v25\n"
MF"v[184:187],a[104:107],v[76:79],v[184:187]\n"
BL"v225,s[8:11],s43 offen offset:2048 sc1 lds\n"
MF"v[124:127],a[108:111],v[68:71],v[124:127]\n"
EX"v20,v20\n"
MF"v[156:159],a[108:111],v[72:75],v[156:159]\n"
RD"a[124:127],v227 offset:15360\n"
PK"v25,v26,v27\n"
MF"v[188:191],a[108:111],v[76:79],v[188:191]\n"
WT"lgkmcnt(2)\n"
EX"v21,v21\n"
MF"v[128:131],a[112:115],v[68:71],v[128:131]\n"
BL"v225,s[8:11],s43 offen offset:3072 sc1 lds\n"
MF"v[160:163],a[112:115],v[72:75],v[160:163]\n"
SA"m0,s40,16384\n"
EX"v22,v22\n"
MF"v[192:195],a[112:115],v[76:79],v[192:195]\n"
EX"v23,v23\n"
PK"v18,v20,v21\n"
MF"v[132:135],a[116:119],v[68:71],v[132:135]\n"
RD"a[64:67],v227 offset:16384\n"
PK"v19,v22,v23\n"
MF"v[164:167],a[116:119],v[72:75],v[164:167]\n"
EX"v28,v28\n"
MF"v[196:199],a[116:119],v[76:79],v[196:199]\n"
WT"lgkmcnt(1)\n"
BL"v225,s[8:11],s44 offen offset:0 sc1 lds\n"
MF"v[136:139],a[120:123],v[68:71],v[136:139]\n"
SA"s40,s40,32768\n"
EX"v29,v29\n"
MF"v[168:171],a[120:123],v[72:75],v[168:171]\n"
"s_cmp_eq_u32 s40,s46\n"
RD"a[68:71],v227 offset:24576\n"
RD"a[72:75],v227 offset:17408\n"
MF"v[200:203],a[120:123],v[76:79],v[200:203]\n"
"s_cselect_b32 s40,s47,s40\n"
EX"v30,v30\n"
MF"v[140:143],a[124:127],v[68:71],v[140:143]\n"
RD"a[76:79],v227 offset:25600\n"
RD"a[80:83],v227 offset:18432\n"
PK"v26,v28,v29\n"
MF"v[172:175],a[124:127],v[72:75],v[172:175]\n"
EX"v31,v31\n"
MF"v[204:207],a[124:127],v[76:79],v[204:207]\n"
BL"v225,s[8:11],s44 offen offset:1024 sc1 lds\n"
MF"a[136:139],a[128:131],v[68:71],a[136:139]\n"
EX"v40,v40\n"
PK"v27,v30,v31\n"
MF"a[140:143],a[128:131],v[72:75],a[140:143]\n"
EX"v41,v41\n"
MF"a[144:147],a[128:131],v[76:79],a[144:147]\n"
WT"lgkmcnt(2)\n"
RD"a[84:87],v227 offset:26624\n"
PK"v68,v40,v41\n"
MF"v[144:147],a[64:67],v[0:3],v[144:147]\n"
BL"v225,s[8:11],s44 offen offset:2048 sc1 lds\n"
MF"v[144:147],a[68:71],v[16:19],v[144:147]\n"
EX"v42,v42\n"
MF"v[176:179],a[68:71],v[24:27],v[176:179]\n"
SA"s51,s59,16384\n"
RD"a[88:91],v227 offset:19456\n"
RD"a[92:95],v227 offset:27648\n"
MF"v[176:179],a[64:67],v[8:11],v[176:179]\n"
EX"v43,v43\n"
MF"v[148:151],a[72:75],v[0:3],v[148:151]\n"
WT"lgkmcnt(3)\n"
EX"v44,v44\n"
MF"v[148:151],a[76:79],v[16:19],v[148:151]\n"
RD"a[96:99],v227 offset:20480\n"
"v_accvgpr_read_b32 v252,a136\n"
"v_subrev_u32 v253,0x30800000,v252\n"
MF"v[180:183],a[76:79],v[24:27],v[180:183]\n"
"v_cmp_le_u32 vcc,0x1f000000,v253\n"
"s_cmp_lg_u64 vcc,0\n"
"s_cbranch_scc1 L1035_%=\n"
"L1034_%=:\n"
MF"v[180:183],a[72:75],v[8:11],v[180:183]\n"
BL"v225,s[8:11],s44 offen offset:3072 sc1 lds\n"
EX"v45,v45\n"
MF"v[152:155],a[80:83],v[0:3],v[152:155]\n"
WT"lgkmcnt(3)\n"
"v_rcp_f32 v252,v252\n"
EX"v46,v46\n"
MF"v[152:155],a[84:87],v[16:19],v[152:155]\n"
"s_mov_b32 m0,s40\n"
RD"a[100:103],v227 offset:28672\n"
RD"a[104:107],v227 offset:21504\n"
"v_readlane_b32 s43,v228,s42\n"
MF"v[184:187],a[84:87],v[24:27],v[184:187]\n"
EX"v47,v47\n"
PK"v69,v42,v43\n"
MF"v[184:187],a[80:83],v[8:11],v[184:187]\n"
WT"lgkmcnt(3)\n"
RD"a[108:111],v227 offset:29696\n"
RD"a[112:115],v227 offset:22528\n"
EX"v56,v56\n"
MF"v[156:159],a[88:91],v[0:3],v[156:159]\n"
EX"v57,v57\n"
"v_readlane_b32 s44,v229,s42\n"
MF"v[156:159],a[92:95],v[16:19],v[156:159]\n"
SA"s42,s42,1\n"
EX"v58,v58\n"
PK"v70,v44,v45\n"
MF"v[188:191],a[92:95],v[24:27],v[188:191]\n"
RD"a[116:119],v227 offset:30720\n"
MU"v112,v252,v112\n"
MU"v113,v252,v113\n"
MF"v[188:191],a[88:91],v[8:11],v[188:191]\n"
WT"lgkmcnt(5)\n"
EX"v59,v59\n"
MU"v114,v252,v114\n"
MF"v[160:163],a[96:99],v[0:3],v[160:163]\n"
WT"lgkmcnt(3)\n"
EX"v60,v60\n"
MU"v115,v252,v115\n"
MF"v[160:163],a[100:103],v[16:19],v[160:163]\n"
RD"a[120:123],v227 offset:23552\n"
RD"a[124:127],v227 offset:31744\n"
MU"v116,v252,v116\n"
MF"v[192:195],a[100:103],v[24:27],v[192:195]\n"
EX"v61,v61\n"
MU"v117,v252,v117\n"
MF"v[192:195],a[96:99],v[8:11],v[192:195]\n"
MU"v118,v252,v118\n"
MU"v119,v252,v119\n"
PK"v71,v46,v47\n"
MF"v[164:167],a[104:107],v[0:3],v[164:167]\n"
WT"lgkmcnt(3)\n"
EX"v62,v62\n"
PK"v112,v112,v113\n"
MF"v[164:167],a[108:111],v[16:19],v[164:167]\n"
SA"s41,s41,32768\n"
WT"vmcnt(20)\n"
"s_barrier\n"
MF"v[196:199],a[108:111],v[24:27],v[196:199]\n"
"s_cmp_eq_u32 s41,163840\n"
BL"v225,s[8:11],s43 offen offset:0 sc1 lds\n"
BL"v225,s[8:11],s43 offen offset:1024 sc1 lds\n"
MF"v[196:199],a[104:107],v[8:11],v[196:199]\n"
"s_cselect_b32 s41,0,s41\n"
BL"v225,s[8:11],s43 offen offset:2048 sc1 lds\n"
RD"a[64:67],v226 offset:0\n"
MF"v[168:171],a[112:115],v[0:3],v[168:171]\n"
WT"lgkmcnt(1)\n"
EX"v63,v63\n"
"v_add_u32 v227,s41,v224\n"
MF"v[168:171],a[116:119],v[16:19],v[168:171]\n"
RD"a[68:71],v226 offset:8192\n"
RD"a[72:75],v226 offset:1024\n"
PK"v113,v114,v115\n"
MF"v[200:203],a[116:119],v[24:27],v[200:203]\n"
RD"a[76:79],v226 offset:9216\n"
RD"a[80:83],v226 offset:2048\n"
PK"v114,v116,v117\n"
MF"v[200:203],a[112:115],v[8:11],v[200:203]\n"
BL"v225,s[8:11],s43 offen offset:3072 sc1 lds\n"
PK"v115,v118,v119\n"
MF"v[172:175],a[120:123],v[0:3],v[172:175]\n"
SA"m0,s40,16384\n"
BS"v[112:115],v224,s[8:11],s51 offen offset:0 sc1\n"
RD"a[84:87],v226 offset:10240\n"
MF"v[172:175],a[124:127],v[16:19],v[172:175]\n"
RD"a[88:91],v226 offset:3072\n"
RD"a[92:95],v226 offset:11264\n"
MU"v120,v252,v120\n"
MF"v[204:207],a[124:127],v[24:27],v[204:207]\n"
BL"v225,s[8:11],s44 offen offset:0 sc1 lds\n"
MU"v121,v252,v121\n"
MF"v[204:207],a[120:123],v[8:11],v[204:207]\n"
SA"s40,s40,32768\n"
RD"a[96:99],v226 offset:4096\n"
RD"a[100:103],v226 offset:12288\n"
PK"v76,v56,v57\n"
MF"a[140:143],a[128:131],v[0:3],a[140:143]\n"
"s_cmp_eq_u32 s40,s46\n"
RD"a[104:107],v226 offset:5120\n"
RD"a[108:111],v226 offset:13312\n"
MU"v122,v252,v122\n"
MF"a[140:143],a[128:131],v[16:19],a[140:143]\n"
"s_cselect_b32 s40,s47,s40\n"
RD"a[112:115],v226 offset:6144\n"
MU"v123,v252,v123\n"
MU"v124,v252,v124\n"
MF"a[144:147],a[128:131],v[24:27],a[144:147]\n"
PK"v77,v58,v59\n"
PK"v78,v60,v61\n"
PK"v79,v62,v63\n"
MF"a[144:147],a[128:131],v[8:11],a[144:147]\n"
WT"lgkmcnt(7)\n"
RD"a[116:119],v226 offset:14336\n"
RD"a[120:123],v226 offset:7168\n"
MU"v125,v252,v125\n"
MF"v[176:179],a[64:67],v[68:71],v[176:179]\n"
BL"v225,s[8:11],s44 offen offset:1024 sc1 lds\n"
MU"v126,v252,v126\n"
MF"v[176:179],a[68:71],v[76:79],v[176:179]\n"
RD"a[124:127],v226 offset:15360\n"
MU"v127,v252,v127\n"
PK"v120,v120,v121\n"
MF"v[180:183],a[72:75],v[68:71],v[180:183]\n"
WT"vmcnt(15)\n"
"s_barrier\n"
MF"v[180:183],a[76:79],v[76:79],v[180:183]\n"
RD"a[64:67],v227 offset:0\n"
PK"v121,v122,v123\n"
PK"v122,v124,v125\n"
MF"v[184:187],a[80:83],v[68:71],v[184:187]\n"
BL"v225,s[8:11],s44 offen offset:2048 sc1 lds\n"
PK"v123,v126,v127\n"
MF"v[184:187],a[84:87],v[76:79],v[184:187]\n"
WT"lgkmcnt(4)\n"
BS"v[120:123],v224,s[8:11],s51 offen offset:1024 sc1\n"
MU"v128,v252,v128\n"
MF"v[188:191],a[88:91],v[68:71],v[188:191]\n"
RD"a[68:71],v227 offset:1024\n"
RD"a[72:75],v227 offset:2048\n"
MU"v129,v252,v129\n"
MF"v[188:191],a[92:95],v[76:79],v[188:191]\n"
MU"v130,v252,v130\n"
MU"v131,v252,v131\n"
MU"v132,v252,v132\n"
MF"v[192:195],a[96:99],v[68:71],v[192:195]\n"
MU"v133,v252,v133\n"
MU"v134,v252,v134\n"
MU"v135,v252,v135\n"
MF"v[192:195],a[100:103],v[76:79],v[192:195]\n"
BL"v225,s[8:11],s44 offen offset:3072 sc1 lds\n"
PK"v128,v128,v129\n"
MF"v[196:199],a[104:107],v[68:71],v[196:199]\n"
RD"a[76:79],v227 offset:3072\n"
PK"v129,v130,v131\n"
PK"v130,v132,v133\n"
MF"v[196:199],a[108:111],v[76:79],v[196:199]\n"
PK"v131,v134,v135\n"
MU"v136,v252,v136\n"
MU"v137,v252,v137\n"
MF"v[200:203],a[112:115],v[68:71],v[200:203]\n"
WT"lgkmcnt(3)\n"
BS"v[128:131],v224,s[8:11],s51 offen offset:2048 sc1\n"
MU"v138,v252,v138\n"
MF"v[200:203],a[116:119],v[76:79],v[200:203]\n"
RD"a[80:83],v227 offset:4096\n"
RD"a[84:87],v227 offset:5120\n"
MU"v139,v252,v139\n"
MF"v[204:207],a[120:123],v[68:71],v[204:207]\n"
RD"a[88:91],v227 offset:6144\n"
MU"v140,v252,v140\n"
MU"v141,v252,v141\n"
MF"v[204:207],a[124:127],v[76:79],v[204:207]\n"
PK"v136,v136,v137\n"
MU"v142,v252,v142\n"
MU"v143,v252,v143\n"
MF"a[144:147],a[128:131],v[68:71],a[144:147]\n"
PK"v137,v138,v139\n"
PK"v138,v140,v141\n"
PK"v139,v142,v143\n"
MF"a[144:147],a[128:131],v[76:79],a[144:147]\n"
BS"v[136:139],v224,s[8:11],s51 offen offset:3072 sc1\n"
"s_branch LTL_%=\n"
"LEV_%=:\n"
"s_mov_b32 s14,0\n"
WT"lgkmcnt(6)\n"
MF"v[0:3],a[64:67],a[0:3],0\n"
WT"lgkmcnt(5)\n"
PK"v64,v32,v33\n"
PK"v65,v34,v35\n"
MF"v[0:3],a[68:71],a[4:7],v[0:3]\n"
SA"s95,s40,32768\n"
PK"v66,v36,v37\n"
PK"v67,v38,v39\n"
MF"v[8:11],a[68:71],a[20:23],0\n"
"s_cmp_eq_u32 s95,s46\n"
PK"v68,v40,v41\n"
PK"v69,v42,v43\n"
MF"v[8:11],a[64:67],a[16:19],v[8:11]\n"
"s_cselect_b32 s95,s47,s95\n"
PK"v70,v44,v45\n"
PK"v71,v46,v47\n"
MF"v[16:19],a[64:67],a[32:35],0\n"
SA"m0,s95,0\n"
PK"v72,v48,v49\n"
PK"v73,v50,v51\n"
MF"v[16:19],a[68:71],a[36:39],v[16:19]\n"
RD"a[92:95],v226 offset:7168\n"
"v_mov_b32 v227,v226\n"
MF"v[24:27],a[68:71],a[52:55],0\n"
PK"v74,v52,v53\n"
PK"v75,v54,v55\n"
MF"v[24:27],a[64:67],a[48:51],v[24:27]\n"
WT"lgkmcnt(1)\n"
BL"v235,s[24:27],s52 offen lds\n"
MF"v[0:3],a[72:75],a[8:11],v[0:3]\n"
SA"s52,s52,s62\n"
PK"v76,v56,v57\n"
PK"v77,v58,v59\n"
MF"v[0:3],a[76:79],a[12:15],v[0:3]\n"
PK"v78,v60,v61\n"
PK"v79,v62,v63\n"
MF"v[8:11],a[76:79],a[28:31],v[8:11]\n"
MF"v[8:11],a[72:75],a[24:27],v[8:11]\n"
MF"v[16:19],a[72:75],a[40:43],v[16:19]\n"
SA"m0,s95,1024\n"
MF"v[16:19],a[76:79],a[44:47],v[16:19]\n"
RD"a[96:99],v226 offset:8192\n"
MF"v[24:27],a[76:79],a[60:63],v[24:27]\n"
EX"v0,v0\n"
MF"v[24:27],a[72:75],a[56:59],v[24:27]\n"
BL"v235,s[24:27],s52 offen lds\n"
MF"v[4:7],a[80:83],a[0:3],0\n"
SA"s52,s52,s62\n"
EX"v1,v1\n"
MF"v[4:7],a[84:87],a[4:7],v[4:7]\n"
RD"a[100:103],v226 offset:9216\n"
PK"v0,v0,v1\n"
MF"v[12:15],a[84:87],a[20:23],0\n"
EX"v2,v2\n"
MF"v[12:15],a[80:83],a[16:19],v[12:15]\n"
EX"v3,v3\n"
MF"v[20:23],a[80:83],a[32:35],0\n"
SA"m0,s95,2048\n"
EX"v8,v8\n"
MF"v[20:23],a[84:87],a[36:39],v[20:23]\n"
RD"a[104:107],v226 offset:10240\n"
PK"v1,v2,v3\n"
MF"v[28:31],a[84:87],a[52:55],0\n"
EX"v9,v9\n"
MF"v[28:31],a[80:83],a[48:51],v[28:31]\n"
BL"v235,s[24:27],s52 offen lds\n"
MF"v[4:7],a[88:91],a[8:11],v[4:7]\n"
WT"lgkmcnt(1)\n"
EX"v10,v10\n"
MF"v[4:7],a[92:95],a[12:15],v[4:7]\n"
SA"s52,s52,s62\n"
RD"a[108:111],v226 offset:11264\n"
PK"v8,v8,v9\n"
MF"v[12:15],a[92:95],a[28:31],v[12:15]\n"
EX"v11,v11\n"
MF"v[12:15],a[88:91],a[24:27],v[12:15]\n"
EX"v16,v16\n"
MF"v[20:23],a[88:91],a[40:43],v[20:23]\n"
SA"m0,s95,3072\n"
EX"v17,v17\n"
MF"v[20:23],a[92:95],a[44:47],v[20:23]\n"
RD"a[112:115],v226 offset:12288\n"
PK"v9,v10,v11\n"
MF"v[28:31],a[92:95],a[60:63],v[28:31]\n"
EX"v18,v18\n"
MF"v[28:31],a[88:91],a[56:59],v[28:31]\n"
BL"v235,s[24:27],s52 offen lds\n"
MF"v[32:35],a[96:99],a[0:3],0\n"
SA"s52,s52,s62\n"
EX"v19,v19\n"
MF"v[32:35],a[100:103],a[4:7],v[32:35]\n"
RD"a[116:119],v226 offset:13312\n"
PK"v16,v16,v17\n"
MF"v[40:43],a[100:103],a[20:23],0\n"
EX"v24,v24\n"
MF"v[40:43],a[96:99],a[16:19],v[40:43]\n"
EX"v25,v25\n"
MF"v[48:51],a[96:99],a[32:35],0\n"
SA"m0,s95,16384\n"
EX"v26,v26\n"
MF"v[48:51],a[100:103],a[36:39],v[48:51]\n"
RD"a[120:123],v226 offset:14336\n"
PK"v17,v18,v19\n"
MF"v[56:59],a[100:103],a[52:55],0\n"
EX"v27,v27\n"
MF"v[56:59],a[96:99],a[48:51],v[56:59]\n"
WT"lgkmcnt(2)\n"
BL"v235,s[24:27],s52 offen lds\n"
MF"v[32:35],a[104:107],a[8:11],v[32:35]\n"
SA"s52,s52,s62\n"
EX"v4,v4\n"
MF"v[32:35],a[108:111],a[12:15],v[32:35]\n"
RD"a[124:127],v226 offset:15360\n"
PK"v24,v24,v25\n"
MF"v[40:43],a[108:111],a[28:31],v[40:43]\n"
EX"v5,v5\n"
MF"v[40:43],a[104:107],a[24:27],v[40:43]\n"
EX"v6,v6\n"
MF"v[48:51],a[104:107],a[40:43],v[48:51]\n"
SA"m0,s95,17408\n"
EX"v7,v7\n"
MF"v[48:51],a[108:111],a[44:47],v[48:51]\n"
PK"v25,v26,v27\n"
PK"v2,v4,v5\n"
MF"v[56:59],a[108:111],a[60:63],v[56:59]\n"
EX"v12,v12\n"
MF"v[56:59],a[104:107],a[56:59],v[56:59]\n"
BL"v235,s[24:27],s52 offen lds\n"
MF"v[36:39],a[112:115],a[0:3],0\n"
WT"lgkmcnt(0)\n"
EX"v13,v13\n"
MF"v[36:39],a[116:119],a[4:7],v[36:39]\n"
SA"s52,s52,s62\n"
RD"a[64:67],v227 offset:16384\n"
PK"v3,v6,v7\n"
MF"v[44:47],a[116:119],a[20:23],0\n"
EX"v14,v14\n"
MF"v[44:47],a[112:115],a[16:19],v[44:47]\n"
PK"v10,v12,v13\n"
MF"v[52:55],a[112:115],a[32:35],0\n"
SA"m0,s95,18432\n"
EX"v15,v15\n"
MF"v[52:55],a[116:119],a[36:39],v[52:55]\n"
RD"a[68:71],v227 offset:17408\n"
PK"v11,v14,v15\n"
MF"v[60:63],a[116:119],a[52:55],0\n"
EX"v20,v20\n"
MF"v[60:63],a[112:115],a[48:51],v[60:63]\n"
BL"v235,s[24:27],s52 offen lds\n"
MF"v[36:39],a[120:123],a[8:11],v[36:39]\n"
SA"s52,s52,s62\n"
EX"v21,v21\n"
MF"v[36:39],a[124:127],a[12:15],v[36:39]\n"
RD"a[72:75],v227 offset:18432\n"
"v_readlane_b32 s43,v228,s42\n"
MF"v[44:47],a[124:127],a[28:31],v[44:47]\n"
EX"v22,v22\n"
MF"v[44:47],a[120:123],a[24:27],v[44:47]\n"
"s_mov_b32 m0,s40\n"
PK"v18,v20,v21\n"
MF"v[52:55],a[120:123],a[40:43],v[52:55]\n"
EX"v23,v23\n"
MF"v[52:55],a[124:127],a[44:47],v[52:55]\n"
RD"a[76:79],v227 offset:19456\n"
"v_readlane_b32 s44,v229,s42\n"
MF"v[60:63],a[124:127],a[60:63],v[60:63]\n"
EX"v28,v28\n"
MF"v[60:63],a[120:123],a[56:59],v[60:63]\n"
WT"lgkmcnt(2)\n"
BL"v225,s[8:11],s43 offen offset:0 sc1 lds\n"
MF"v[80:83],a[64:67],v[64:67],v[80:83]\n"
SA"s42,s42,1\n"
EX"v29,v29\n"
MF"v[112:115],a[64:67],v[68:71],v[112:115]\n"
SA"s41,s41,32768\n"
RD"a[80:83],v227 offset:20480\n"
PK"v19,v22,v23\n"
MF"v[144:147],a[64:67],v[72:75],v[144:147]\n"
"s_cmp_eq_u32 s41,163840\n"
EX"v30,v30\n"
MF"v[176:179],a[64:67],v[76:79],v[176:179]\n"
"s_cselect_b32 s41,0,s41\n"
PK"v26,v28,v29\n"
"v_add_u32 v226,s41,v224\n"
MF"v[84:87],a[68:71],v[64:67],v[84:87]\n"
EX"v31,v31\n"
MF"v[116:119],a[68:71],v[68:71],v[116:119]\n"
RD"a[84:87],v227 offset:21504\n"
PK"v27,v30,v31\n"
MF"v[148:151],a[68:71],v[72:75],v[148:151]\n"
EX"v32,v32\n"
MF"v[180:183],a[68:71],v[76:79],v[180:183]\n"
WT"lgkmcnt(2)\n"
BL"v225,s[8:11],s43 offen offset:1024 sc1 lds\n"
MF"v[88:91],a[72:75],v[64:67],v[88:91]\n"
EX"v33,v33\n"
MF"v[120:123],a[72:75],v[68:71],v[120:123]\n"
RD"a[88:91],v227 offset:22528\n"
MF"v[152:155],a[72:75],v[72:75],v[152:155]\n"
EX"v34,v34\n"
MF"v[184:187],a[72:75],v[76:79],v[184:187]\n"
MF"v[92:95],a[76:79],v[64:67],v[92:95]\n"
EX"v35,v35\n"
MF"v[124:127],a[76:79],v[68:71],v[124:127]\n"
RD"a[92:95],v227 offset:23552\n"
MF"v[156:159],a[76:79],v[72:75],v[156:159]\n"
EX"v40,v40\n"
MF"v[188:191],a[76:79],v[76:79],v[188:191]\n"
WT"lgkmcnt(2)\n"
BL"v225,s[8:11],s43 offen offset:2048 sc1 lds\n"
MF"v[96:99],a[80:83],v[64:67],v[96:99]\n"
EX"v41,v41\n"
MF"v[128:131],a[80:83],v[68:71],v[128:131]\n"
RD"a[96:99],v227 offset:24576\n"
MF"v[160:163],a[80:83],v[72:75],v[160:163]\n"
EX"v42,v42\n"
MF"v[192:195],a[80:83],v[76:79],v[192:195]\n"
MF"v[100:103],a[84:87],v[64:67],v[100:103]\n"
EX"v43,v43\n"
MF"v[132:135],a[84:87],v[68:71],v[132:135]\n"
RD"a[100:103],v227 offset:25600\n"
MF"v[164:167],a[84:87],v[72:75],v[164:167]\n"
EX"v48,v48\n"
MF"v[196:199],a[84:87],v[76:79],v[196:199]\n"
WT"lgkmcnt(2)\n"
BL"v225,s[8:11],s43 offen offset:3072 sc1 lds\n"
MF"v[104:107],a[88:91],v[64:67],v[104:107]\n"
EX"v49,v49\n"
MF"v[136:139],a[88:91],v[68:71],v[136:139]\n"
RD"a[104:107],v227 offset:26624\n"
MF"v[168:171],a[88:91],v[72:75],v[168:171]\n"
EX"v50,v50\n"
MF"v[200:203],a[88:91],v[76:79],v[200:203]\n"
SA"m0,s40,16384\n"
MF"v[108:111],a[92:95],v[64:67],v[108:111]\n"
EX"v51,v51\n"
MF"v[140:143],a[92:95],v[68:71],v[140:143]\n"
RD"a[108:111],v227 offset:27648\n"
MF"v[172:175],a[92:95],v[72:75],v[172:175]\n"
EX"v56,v56\n"
MF"v[204:207],a[92:95],v[76:79],v[204:207]\n"
BL"v225,s[8:11],s44 offen offset:0 sc1 lds\n"
MF"a[132:135],a[128:131],v[64:67],a[132:135]\n"
SA"s40,s40,32768\n"
EX"v57,v57\n"
MF"a[136:139],a[128:131],v[68:71],a[136:139]\n"
"s_cmp_eq_u32 s40,s46\n"
RD"a[112:115],v227 offset:28672\n"
"s_cselect_b32 s40,s47,s40\n"
MF"a[140:143],a[128:131],v[72:75],a[140:143]\n"
EX"v58,v58\n"
MF"a[144:147],a[128:131],v[76:79],a[144:147]\n"
WT"lgkmcnt(2)\n"
BL"v[240:243],v238,s[8:11],0 offen sc1\n"
MF"v[80:83],a[96:99],v[0:3],v[80:83]\n"
EX"v59,v59\n"
MF"v[112:115],a[96:99],v[8:11],v[112:115]\n"
RD"a[116:119],v227 offset:29696\n"
MF"v[144:147],a[96:99],v[16:19],v[144:147]\n"
EX"v36,v36\n"
MF"v[176:179],a[96:99],v[24:27],v[176:179]\n"
BL"v225,s[8:11],s44 offen offset:1024 sc1 lds\n"
MF"v[84:87],a[100:103],v[0:3],v[84:87]\n"
EX"v37,v37\n"
MF"v[116:119],a[100:103],v[8:11],v[116:119]\n"
RD"a[120:123],v227 offset:30720\n"
MF"v[148:151],a[100:103],v[16:19],v[148:151]\n"
EX"v38,v38\n"
MF"v[180:183],a[100:103],v[24:27],v[180:183]\n"
MF"v[88:91],a[104:107],v[0:3],v[88:91]\n"
EX"v39,v39\n"
MF"v[120:123],a[104:107],v[8:11],v[120:123]\n"
RD"a[124:127],v227 offset:31744\n"
MF"v[152:155],a[104:107],v[16:19],v[152:155]\n"
EX"v44,v44\n"
MF"v[184:187],a[104:107],v[24:27],v[184:187]\n"
WT"lgkmcnt(2)\n"
BL"v225,s[8:11],s44 offen offset:2048 sc1 lds\n"
MF"v[92:95],a[108:111],v[0:3],v[92:95]\n"
EX"v45,v45\n"
MF"v[124:127],a[108:111],v[8:11],v[124:127]\n"
MF"v[156:159],a[108:111],v[16:19],v[156:159]\n"
EX"v46,v46\n"
MF"v[188:191],a[108:111],v[24:27],v[188:191]\n"
MF"v[96:99],a[112:115],v[0:3],v[96:99]\n"
WT"vmcnt(23) lgkmcnt(0)\n"
"s_barrier\n"
MF"v[128:131],a[112:115],v[8:11],v[128:131]\n"
RD"a[64:67],v226 offset:0\n"
MF"v[160:163],a[112:115],v[16:19],v[160:163]\n"
EX"v47,v47\n"
MF"v[192:195],a[112:115],v[24:27],v[192:195]\n"
BL"v225,s[8:11],s44 offen offset:3072 sc1 lds\n"
MF"v[100:103],a[116:119],v[0:3],v[100:103]\n"
EX"v52,v52\n"
MF"v[132:135],a[116:119],v[8:11],v[132:135]\n"
RD"a[68:71],v226 offset:1024\n"
MF"v[164:167],a[116:119],v[16:19],v[164:167]\n"
EX"v53,v53\n"
MF"v[196:199],a[116:119],v[24:27],v[196:199]\n"
RD"a[72:75],v226 offset:2048\n"
MF"v[104:107],a[120:123],v[0:3],v[104:107]\n"
EX"v54,v54\n"
MF"v[136:139],a[120:123],v[8:11],v[136:139]\n"
RD"a[76:79],v226 offset:3072\n"
MF"v[168:171],a[120:123],v[16:19],v[168:171]\n"
EX"v55,v55\n"
MF"v[200:203],a[120:123],v[24:27],v[200:203]\n"
MF"v[108:111],a[124:127],v[0:3],v[108:111]\n"
EX"v60,v60\n"
MF"v[140:143],a[124:127],v[8:11],v[140:143]\n"
RD"a[80:83],v226 offset:4096\n"
MF"v[172:175],a[124:127],v[16:19],v[172:175]\n"
EX"v61,v61\n"
MF"v[204:207],a[124:127],v[24:27],v[204:207]\n"
RD"a[84:87],v226 offset:5120\n"
MF"a[132:135],a[128:131],v[0:3],a[132:135]\n"
EX"v62,v62\n"
MF"a[136:139],a[128:131],v[8:11],a[136:139]\n"
RD"a[88:91],v226 offset:6144\n"
MF"a[140:143],a[128:131],v[16:19],a[140:143]\n"
EX"v63,v63\n"
MF"a[144:147],a[128:131],v[24:27],a[144:147]\n"
WT"lgkmcnt(6)\n"
MF"v[0:3],a[64:67],a[0:3],0\n"
WT"lgkmcnt(2)\n"
"v_lshrrev_b32 v237,1,v224\n"
PK"v64,v32,v33\n"
MF"v[0:3],a[68:71],a[4:7],v[0:3]\n"
SA"s95,s40,32768\n"
"v_and_b32 v237,0x78,v237\n"
PK"v65,v34,v35\n"
MF"v[8:11],a[68:71],a[20:23],0\n"
"s_cmp_eq_u32 s95,s46\n"
"v_and_b32 v239,0x300,v224\n"
PK"v66,v36,v37\n"
MF"v[8:11],a[64:67],a[16:19],v[8:11]\n"
"s_cselect_b32 s95,s47,s95\n"
"v_add3_u32 v237,v237,v239,s40\n"
PK"v67,v38,v39\n"
MF"v[16:19],a[64:67],a[32:35],0\n"
SA"m0,s95,0\n"
PK"v68,v40,v41\n"
PK"v69,v42,v43\n"
MF"v[16:19],a[68:71],a[36:39],v[16:19]\n"
RD"a[92:95],v226 offset:7168\n"
PK"v70,v44,v45\n"
MF"v[24:27],a[68:71],a[52:55],0\n"
PK"v71,v46,v47\n"
PK"v72,v48,v49\n"
MF"v[24:27],a[64:67],a[48:51],v[24:27]\n"
BL"v235,s[24:27],s52 offen lds\n"
MF"v[0:3],a[72:75],a[8:11],v[0:3]\n"
SA"s52,s52,s62\n"
PK"v73,v50,v51\n"
"v_mov_b32 v227,v226\n"
MF"v[0:3],a[76:79],a[12:15],v[0:3]\n"
PK"v74,v52,v53\n"
PK"v75,v54,v55\n"
MF"v[8:11],a[76:79],a[28:31],v[8:11]\n"
PK"v76,v56,v57\n"
PK"v77,v58,v59\n"
MF"v[8:11],a[72:75],a[24:27],v[8:11]\n"
WT"vmcnt(4)\n"
"v_cmp_eq_u64 vcc,s[28:29],v[240:241]\n"
"v_cmp_eq_u64_e64 s[50:51],s[28:29],v[242:243]\n"
"s_and_b64 vcc,vcc,s[50:51]\n"
"s_cmp_eq_u64 vcc,exec\n"
"s_cbranch_scc0 L1099_%=\n"
"L1100_%=:\n"
MF"v[16:19],a[72:75],a[40:43],v[16:19]\n"
SA"m0,s95,1024\n"
PK"v78,v60,v61\n"
PK"v79,v62,v63\n"
MF"v[16:19],a[76:79],a[44:47],v[16:19]\n"
RD"a[96:99],v226 offset:8192\n"
MF"v[24:27],a[76:79],a[60:63],v[24:27]\n"
EX"v0,v0\n"
MF"v[24:27],a[72:75],a[56:59],v[24:27]\n"
BL"v235,s[24:27],s52 offen lds\n"
MF"v[4:7],a[80:83],a[0:3],0\n"
WT"lgkmcnt(1)\n"
EX"v1,v1\n"
MF"v[4:7],a[84:87],a[4:7],v[4:7]\n"
SA"s52,s52,s62\n"
RD"a[100:103],v226 offset:9216\n"
PK"v0,v0,v1\n"
MF"v[12:15],a[84:87],a[20:23],0\n"
EX"v2,v2\n"
MF"v[12:15],a[80:83],a[16:19],v[12:15]\n"
EX"v3,v3\n"
MF"v[20:23],a[80:83],a[32:35],0\n"
SA"m0,s95,2048\n"
EX"v8,v8\n"
MF"v[20:23],a[84:87],a[36:39],v[20:23]\n"
RD"a[104:107],v226 offset:10240\n"
PK"v1,v2,v3\n"
MF"v[28:31],a[84:87],a[52:55],0\n"
EX"v9,v9\n"
MF"v[28:31],a[80:83],a[48:51],v[28:31]\n"
BL"v235,s[24:27],s52 offen lds\n"
MF"v[4:7],a[88:91],a[8:11],v[4:7]\n"
SA"s52,s52,s62\n"
EX"v10,v10\n"
MF"v[4:7],a[92:95],a[12:15],v[4:7]\n"
RD"a[108:111],v226 offset:11264\n"
PK"v8,v8,v9\n"
MF"v[12:15],a[92:95],a[28:31],v[12:15]\n"
EX"v11,v11\n"
MF"v[12:15],a[88:91],a[24:27],v[12:15]\n"
RT"a[148:149],v237 offset:0\n"
RT"a[150:151],v237 offset:128\n"
MF"v[20:23],a[88:91],a[40:43],v[20:23]\n"
SA"m0,s95,3072\n"
EX"v16,v16\n"
MF"v[20:23],a[92:95],a[44:47],v[20:23]\n"
EX"v17,v17\n"
MF"v[28:31],a[92:95],a[60:63],v[28:31]\n"
RD"a[112:115],v226 offset:12288\n"
PK"v9,v10,v11\n"
MF"v[28:31],a[88:91],a[56:59],v[28:31]\n"
WT"lgkmcnt(4)\n"
EX"v18,v18\n"
MF"v[32:35],a[96:99],a[0:3],0\n"
RT"a[152:153],v237 offset:1024\n"
RT"a[154:155],v237 offset:1152\n"
MF"v[32:35],a[100:103],a[4:7],v[32:35]\n"
BL"v235,s[24:27],s52 offen lds\n"
MF"v[40:43],a[100:103],a[20:23],0\n"
SA"s52,s52,s62\n"
EX"v19,v19\n"
MF"v[40:43],a[96:99],a[16:19],v[40:43]\n"
RD"a[116:119],v226 offset:13312\n"
RT"a[156:157],v237 offset:2048\n"
MF"v[48:51],a[96:99],a[32:35],0\n"
SA"m0,s95,16384\n"
EX"v24,v24\n"
MF"v[48:51],a[100:103],a[36:39],v[48:51]\n"
RT"a[158:159],v237 offset:2176\n"
RD"a[120:123],v226 offset:14336\n"
EX"v25,v25\n"
MF"v[56:59],a[100:103],a[52:55],0\n"
EX"v26,v26\n"
PK"v16,v16,v17\n"
MF"v[56:59],a[96:99],a[48:51],v[56:59]\n"
EX"v27,v27\n"
MF"v[32:35],a[104:107],a[8:11],v[32:35]\n"
WT"lgkmcnt(4)\n"
RT"a[160:161],v237 offset:3072\n"
RT"a[162:163],v237 offset:3200\n"
MF"v[32:35],a[108:111],a[12:15],v[32:35]\n"
BL"v235,s[24:27],s52 offen lds\n"
PK"v17,v18,v19\n"
MF"v[40:43],a[108:111],a[28:31],v[40:43]\n"
SA"s52,s52,s62\n"
EX"v4,v4\n"
MF"v[40:43],a[104:107],a[24:27],v[40:43]\n"
RD"a[124:127],v226 offset:15360\n"
RT"a[164:165],v237 offset:16384\n"
MF"v[48:51],a[104:107],a[40:43],v[48:51]\n"
SA"m0,s95,17408\n"
EX"v5,v5\n"
PK"v24,v24,v25\n"
MF"v[48:51],a[108:111],a[44:47],v[48:51]\n"
RT"a[166:167],v237 offset:16512\n"
PK"v25,v26,v27\n"
EX"v6,v6\n"
MF"v[56:59],a[108:111],a[60:63],v[56:59]\n"
EX"v7,v7\n"
MF"v[56:59],a[104:107],a[56:59],v[56:59]\n"
EX"v12,v12\n"
MF"v[36:39],a[112:115],a[0:3],0\n"
WT"lgkmcnt(3)\n"
RT"a[168:169],v237 offset:17408\n"
RT"a[170:171],v237 offset:17536\n"
MF"v[36:39],a[116:119],a[4:7],v[36:39]\n"
BL"v235,s[24:27],s52 offen lds\n"
MF"v[44:47],a[116:119],a[20:23],0\n"
SA"s52,s52,s62\n"
EX"v13,v13\n"
MF"v[44:47],a[112:115],a[16:19],v[44:47]\n"
RD"a[64:67],v227 offset:16384\n"
RT"a[172:173],v237 offset:18432\n"
PK"v2,v4,v5\n"
MF"v[52:55],a[112:115],a[32:35],0\n"
SA"m0,s95,18432\n"
EX"v14,v14\n"
MF"v[52:55],a[116:119],a[36:39],v[52:55]\n"
RT"a[174:175],v237 offset:18560\n"
RD"a[68:71],v227 offset:17408\n"
PK"v3,v6,v7\n"
MF"v[60:63],a[116:119],a[52:55],0\n"
EX"v15,v15\n"
MF"v[60:63],a[112:115],a[48:51],v[60:63]\n"
EX"v20,v20\n"
MF"v[36:39],a[120:123],a[8:11],v[36:39]\n"
WT"lgkmcnt(4)\n"
BL"v235,s[24:27],s52 offen lds\n"
MF"v[36:39],a[124:127],a[12:15],v[36:39]\n"
SA"s52,s52,s62\n"
EX"v21,v21\n"
PK"v10,v12,v13\n"
MF"v[44:47],a[124:127],a[28:31],v[44:47]\n"
"s_cmp_gt_u32 s92,0\n"
RD"a[72:75],v227 offset:18432\n"
"v_readlane_b32 s43,v228,s42\n"
MF"v[44:47],a[120:123],a[24:27],v[44:47]\n"
"s_cselect_b32 s63,s63,1065353216\n"
EX"v22,v22\n"
MF"v[52:55],a[120:123],a[40:43],v[52:55]\n"
"s_mov_b32 m0,s40\n"
BL"a[204:207],v236,s[8:11],s63 offen offset:0 sc1\n"
MF"v[52:55],a[124:127],a[44:47],v[52:55]\n"
EX"v23,v23\n"
PK"v11,v14,v15\n"
MF"v[60:63],a[124:127],a[60:63],v[60:63]\n"
RD"a[76:79],v227 offset:19456\n"
"v_readlane_b32 s44,v229,s42\n"
MF"v[60:63],a[120:123],a[56:59],v[60:63]\n"
WT"lgkmcnt(2)\n"
EX"v28,v28\n"
MF"v[80:83],a[64:67],v[64:67],v[80:83]\n"
SA"s42,s42,1\n"
BL"v225,s[8:11],s43 offen offset:0 sc1 lds\n"
MF"v[112:115],a[64:67],v[68:71],v[112:115]\n"
SA"s41,s41,32768\n"
EX"v29,v29\n"
PK"v18,v20,v21\n"
MF"v[144:147],a[64:67],v[72:75],v[144:147]\n"
"s_cmp_eq_u32 s41,163840\n"
RD"a[80:83],v227 offset:20480\n"
PK"v19,v22,v23\n"
MF"v[176:179],a[64:67],v[76:79],v[176:179]\n"
"s_cselect_b32 s41,0,s41\n"
EX"v30,v30\n"
MF"v[84:87],a[68:71],v[64:67],v[84:87]\n"
BL"a[208:211],v236,s[8:11],s63 offen offset:1024 sc1\n"
MF"v[116:119],a[68:71],v[68:71],v[116:119]\n"
EX"v31,v31\n"
MF"v[148:151],a[68:71],v[72:75],v[148:151]\n"
RD"a[84:87],v227 offset:21504\n"
"v_add_u32 v226,s41,v224\n"
MF"v[180:183],a[68:71],v[76:79],v[180:183]\n"
WT"lgkmcnt(2)\n"
EX"v32,v32\n"
MF"v[88:91],a[72:75],v[64:67],v[88:91]\n"
BL"v225,s[8:11],s43 offen offset:1024 sc1 lds\n"
MF"v[120:123],a[72:75],v[68:71],v[120:123]\n"
EX"v33,v33\n"
PK"v26,v28,v29\n"
MF"v[152:155],a[72:75],v[72:75],v[152:155]\n"
RD"a[88:91],v227 offset:22528\n"
PK"v27,v30,v31\n"
MF"v[184:187],a[72:75],v[76:79],v[184:187]\n"
EX"v34,v34\n"
MF"v[92:95],a[76:79],v[64:67],v[92:95]\n"
BL"a[212:215],v236,s[8:11],s63 offen offset:2048 sc1\n"
MF"v[124:127],a[76:79],v[68:71],v[124:127]\n"
EX"v35,v35\n"
MF"v[156:159],a[76:79],v[72:75],v[156:159]\n"
RD"a[92:95],v227 offset:23552\n"
MF"v[188:191],a[76:79],v[76:79],v[188:191]\n"
WT"lgkmcnt(2)\n"
EX"v40,v40\n"
MF"v[96:99],a[80:83],v[64:67],v[96:99]\n"
BL"v225,s[8:11],s43 offen offset:2048 sc1 lds\n"
MF"v[128:131],a[80:83],v[68:71],v[128:131]\n"
EX"v41,v41\n"
MF"v[160:163],a[80:83],v[72:75],v[160:163]\n"
RD"a[96:99],v227 offset:24576\n"
MF"v[192:195],a[80:83],v[76:79],v[192:195]\n"
EX"v42,v42\n"
MF"v[100:103],a[84:87],v[64:67],v[100:103]\n"
BL"a[216:219],v236,s[8:11],s63 offen offset:3072 sc1\n"
MF"v[132:135],a[84:87],v[68:71],v[132:135]\n"
EX"v43,v43\n"
MF"v[164:167],a[84:87],v[72:75],v[164:167]\n"
RD"a[100:103],v227 offset:25600\n"
MF"v[196:199],a[84:87],v[76:79],v[196:199]\n"
WT"lgkmcnt(2)\n"
EX"v48,v48\n"
MF"v[104:107],a[88:91],v[64:67],v[104:107]\n"
SA"s63,s63,4096\n"
BL"v225,s[8:11],s43 offen offset:3072 sc1 lds\n"
MF"v[136:139],a[88:91],v[68:71],v[136:139]\n"
EX"v49,v49\n"
MF"v[168:171],a[88:91],v[72:75],v[168:171]\n"
RD"a[104:107],v227 offset:26624\n"
MF"v[200:203],a[88:91],v[76:79],v[200:203]\n"
SA"m0,s40,16384\n"
EX"v50,v50\n"
MF"v[108:111],a[92:95],v[64:67],v[108:111]\n"
BL"a[220:223],v236,s[8:11],s63 offen offset:0 sc1\n"
MF"v[140:143],a[92:95],v[68:71],v[140:143]\n"
EX"v51,v51\n"
MF"v[172:175],a[92:95],v[72:75],v[172:175]\n"
RD"a[108:111],v227 offset:27648\n"
MF"v[204:207],a[92:95],v[76:79],v[204:207]\n"
EX"v56,v56\n"
MF"a[132:135],a[128:131],v[64:67],a[132:135]\n"
SA"s40,s40,32768\n"
BL"v225,s[8:11],s44 offen offset:0 sc1 lds\n"
MF"a[136:139],a[128:131],v[68:71],a[136:139]\n"
"s_cmp_eq_u32 s40,s46\n"
EX"v57,v57\n"
MF"a[140:143],a[128:131],v[72:75],a[140:143]\n"
"s_cselect_b32 s40,s47,s40\n"
RD"a[112:115],v227 offset:28672\n"
MF"a[144:147],a[128:131],v[76:79],a[144:147]\n"
WT"lgkmcnt(2)\n"
EX"v58,v58\n"
MF"v[80:83],a[96:99],v[0:3],v[80:83]\n"
BL"a[224:227],v236,s[8:11],s63 offen offset:1024 sc1\n"
MF"v[112:115],a[96:99],v[8:11],v[112:115]\n"
EX"v59,v59\n"
MF"v[144:147],a[96:99],v[16:19],v[144:147]\n"
RD"a[116:119],v227 offset:29696\n"
MF"v[176:179],a[96:99],v[24:27],v[176:179]\n"
EX"v36,v36\n"
MF"v[84:87],a[100:103],v[0:3],v[84:87]\n"
BL"v225,s[8:11],s44 offen offset:1024 sc1 lds\n"
MF"v[116:119],a[100:103],v[8:11],v[116:119]\n"
EX"v37,v37\n"
MF"v[148:151],a[100:103],v[16:19],v[148:151]\n"
RD"a[120:123],v227 offset:30720\n"
MF"v[180:183],a[100:103],v[24:27],v[180:183]\n"
EX"v38,v38\n"
MF"v[88:91],a[104:107],v[0:3],v[88:91]\n"
BL"a[228:231],v236,s[8:11],s63 offen offset:2048 sc1\n"
MF"v[120:123],a[104:107],v[8:11],v[120:123]\n"
EX"v39,v39\n"
MF"v[152:155],a[104:107],v[16:19],v[152:155]\n"
RD"a[124:127],v227 offset:31744\n"
MF"v[184:187],a[104:107],v[24:27],v[184:187]\n"
WT"lgkmcnt(2)\n"
EX"v44,v44\n"
MF"v[92:95],a[108:111],v[0:3],v[92:95]\n"
BL"v225,s[8:11],s44 offen offset:2048 sc1 lds\n"
MF"v[124:127],a[108:111],v[8:11],v[124:127]\n"
EX"v45,v45\n"
MF"v[156:159],a[108:111],v[16:19],v[156:159]\n"
EX"v46,v46\n"
MF"v[188:191],a[108:111],v[24:27],v[188:191]\n"
BL"a[232:235],v236,s[8:11],s63 offen offset:3072 sc1\n"
MF"v[96:99],a[112:115],v[0:3],v[96:99]\n"
WT"lgkmcnt(0)\n"
"s_barrier\n"
MF"v[128:131],a[112:115],v[8:11],v[128:131]\n"
RD"a[64:67],v226 offset:0\n"
MF"v[160:163],a[112:115],v[16:19],v[160:163]\n"
EX"v47,v47\n"
MF"v[192:195],a[112:115],v[24:27],v[192:195]\n"
SA"s63,s63,4096\n"
BL"v225,s[8:11],s44 offen offset:3072 sc1 lds\n"
MF"v[100:103],a[116:119],v[0:3],v[100:103]\n"
EX"v52,v52\n"
MF"v[132:135],a[116:119],v[8:11],v[132:135]\n"
RD"a[68:71],v226 offset:1024\n"
MF"v[164:167],a[116:119],v[16:19],v[164:167]\n"
EX"v53,v53\n"
MF"v[196:199],a[116:119],v[24:27],v[196:199]\n"
BL"a[236:239],v236,s[8:11],s63 offen offset:0 sc1\n"
MF"v[104:107],a[120:123],v[0:3],v[104:107]\n"
RD"a[72:75],v226 offset:2048\n"
MF"v[136:139],a[120:123],v[8:11],v[136:139]\n"
EX"v54,v54\n"
MF"v[168:171],a[120:123],v[16:19],v[168:171]\n"
RD"a[76:79],v226 offset:3072\n"
MF"v[200:203],a[120:123],v[24:27],v[200:203]\n"
EX"v55,v55\n"
MF"v[108:111],a[124:127],v[0:3],v[108:111]\n"
EX"v60,v60\n"
MF"v[140:143],a[124:127],v[8:11],v[140:143]\n"
RD"a[80:83],v226 offset:4096\n"
MF"v[172:175],a[124:127],v[16:19],v[172:175]\n"
EX"v61,v61\n"
MF"v[204:207],a[124:127],v[24:27],v[204:207]\n"
RD"a[84:87],v226 offset:5120\n"
MF"a[132:135],a[128:131],v[0:3],a[132:135]\n"
EX"v62,v62\n"
MF"a[136:139],a[128:131],v[8:11],a[136:139]\n"
RD"a[88:91],v226 offset:6144\n"
MF"a[140:143],a[128:131],v[16:19],a[140:143]\n"
EX"v63,v63\n"
MF"a[144:147],a[128:131],v[24:27],a[144:147]\n"
WT"lgkmcnt(6)\n"
MF"v[0:3],a[64:67],a[0:3],0\n"
WT"lgkmcnt(2)\n"
"v_lshrrev_b32 v237,1,v224\n"
PK"v64,v32,v33\n"
MF"v[0:3],a[68:71],a[4:7],v[0:3]\n"
SA"s95,s40,32768\n"
"v_and_b32 v237,0x78,v237\n"
PK"v65,v34,v35\n"
MF"v[8:11],a[68:71],a[20:23],0\n"
"s_cmp_eq_u32 s95,s46\n"
"v_and_b32 v239,0x300,v224\n"
PK"v66,v36,v37\n"
MF"v[8:11],a[64:67],a[16:19],v[8:11]\n"
"s_cselect_b32 s95,s47,s95\n"
BL"a[240:243],v236,s[8:11],s63 offen offset:1024 sc1\n"
MF"v[16:19],a[64:67],a[32:35],0\n"
"s_sub_u32 s95,s95,s47\n"
"v_add3_u32 v237,v237,v239,s40\n"
PK"v67,v38,v39\n"
MF"v[16:19],a[68:71],a[36:39],v[16:19]\n"
"s_lshr_b32 s53,s47,2\n"
RD"a[92:95],v226 offset:7168\n"
PK"v68,v40,v41\n"
MF"v[24:27],a[68:71],a[52:55],0\n"
"s_cmp_gt_u32 s92,10\n"
PK"v69,v42,v43\n"
PK"v70,v44,v45\n"
MF"v[24:27],a[64:67],a[48:51],v[24:27]\n"
"s_cselect_b32 s63,s63,1065353216\n"
PK"v71,v46,v47\n"
"v_add_u32 v235,s95,v224\n"
MF"v[0:3],a[72:75],a[8:11],v[0:3]\n"
"s_lshl_b32 s50,s35,6\n"
BL"a[244:247],v236,s[8:11],s63 offen offset:2048 sc1\n"
MF"v[0:3],a[76:79],a[12:15],v[0:3]\n"
SA"s50,s50,4352\n"
PK"v72,v48,v49\n"
PK"v73,v50,v51\n"
MF"v[8:11],a[76:79],a[28:31],v[8:11]\n"
"v_mov_b32 v227,v226\n"
PK"v74,v52,v53\n"
MF"v[8:11],a[72:75],a[24:27],v[8:11]\n"
BL"a[248:251],v236,s[8:11],s63 offen offset:3072 sc1\n"
MF"v[16:19],a[72:75],a[40:43],v[16:19]\n"
SA"s63,s63,4096\n"
PK"v75,v54,v55\n"
PK"v76,v56,v57\n"
MF"v[16:19],a[76:79],a[44:47],v[16:19]\n"
RD"a[96:99],v226 offset:8192\n"
PK"v77,v58,v59\n"
MF"v[24:27],a[76:79],a[60:63],v[24:27]\n"
EX"v0,v0\n"
MF"v[24:27],a[72:75],a[56:59],v[24:27]\n"
BL"a[252:255],v236,s[8:11],s63 offen offset:0 sc1\n"
PK"v78,v60,v61\n"
MF"v[4:7],a[80:83],a[0:3],0\n"
WT"lgkmcnt(1)\n"
EX"v1,v1\n"
PK"v79,v62,v63\n"
MF"v[4:7],a[84:87],a[4:7],v[4:7]\n"
"s_cmp_gt_u32 s92,13\n"
RD"a[100:103],v226 offset:9216\n"
"v_lshrrev_b32 v238,1,v224\n"
"v_and_b32 v238,56,v238\n"
MF"v[12:15],a[84:87],a[20:23],0\n"
"s_cselect_b32 s63,s63,1065353216\n"
EX"v2,v2\n"
MF"v[12:15],a[80:83],a[16:19],v[12:15]\n"
BL"v[244:247],v236,s[8:11],s63 offen offset:1024 sc1\n"
"v_add_u32 v236,s53,v235\n"
MF"v[20:23],a[80:83],a[32:35],0\n"
EX"v3,v3\n"
MF"v[20:23],a[84:87],a[36:39],v[20:23]\n"
EX"v8,v8\n"
MF"v[28:31],a[84:87],a[52:55],0\n"
RD"a[104:107],v226 offset:10240\n"
PK"v0,v0,v1\n"
MF"v[28:31],a[80:83],a[48:51],v[28:31]\n"
EX"v9,v9\n"
MF"v[4:7],a[88:91],a[8:11],v[4:7]\n"
"buffer_load_dwordx2 v[240:241],v238,s[8:11],s50 offen sc1\n"
MF"v[4:7],a[92:95],a[12:15],v[4:7]\n"
EX"v10,v10\n"
MF"v[12:15],a[92:95],a[28:31],v[12:15]\n"
RD"a[108:111],v226 offset:11264\n"
PK"v1,v2,v3\n"
MF"v[12:15],a[88:91],a[24:27],v[12:15]\n"
EX"v11,v11\n"
MF"v[20:23],a[88:91],a[40:43],v[20:23]\n"
WT"vmcnt(23)\n"
RT"a[176:177],v237 offset:0\n"
RT"a[178:179],v237 offset:128\n"
MF"v[20:23],a[92:95],a[44:47],v[20:23]\n"
EX"v16,v16\n"
MF"v[28:31],a[92:95],a[60:63],v[28:31]\n"
EX"v17,v17\n"
MF"v[28:31],a[88:91],a[56:59],v[28:31]\n"
WT"lgkmcnt(3)\n"
RD"a[112:115],v226 offset:12288\n"
RT"a[180:181],v237 offset:1024\n"
PK"v8,v8,v9\n"
MF"v[32:35],a[96:99],a[0:3],0\n"
EX"v18,v18\n"
MF"v[32:35],a[100:103],a[4:7],v[32:35]\n"
RT"a[182:183],v237 offset:1152\n"
RD"a[116:119],v226 offset:13312\n"
MF"v[40:43],a[100:103],a[20:23],0\n"
EX"v19,v19\n"
MF"v[40:43],a[96:99],a[16:19],v[40:43]\n"
EX"v24,v24\n"
PK"v9,v10,v11\n"
MF"v[48:51],a[96:99],a[32:35],0\n"
RT"a[184:185],v237 offset:2048\n"
RT"a[186:187],v237 offset:2176\n"
MF"v[48:51],a[100:103],a[36:39],v[48:51]\n"
EX"v25,v25\n"
PK"v16,v16,v17\n"
MF"v[56:59],a[100:103],a[52:55],0\n"
EX"v26,v26\n"
MF"v[56:59],a[96:99],a[48:51],v[56:59]\n"
RD"a[120:123],v226 offset:14336\n"
RT"a[188:189],v237 offset:3072\n"
MF"v[32:35],a[104:107],a[8:11],v[32:35]\n"
WT"lgkmcnt(4)\n"
EX"v27,v27\n"
MF"v[32:35],a[108:111],a[12:15],v[32:35]\n"
RT"a[190:191],v237 offset:3200\n"
RD"a[124:127],v226 offset:15360\n"
PK"v17,v18,v19\n"
MF"v[40:43],a[108:111],a[28:31],v[40:43]\n"
EX"v4,v4\n"
MF"v[40:43],a[104:107],a[24:27],v[40:43]\n"
EX"v5,v5\n"
MF"v[48:51],a[104:107],a[40:43],v[48:51]\n"
RT"a[192:193],v237 offset:16384\n"
RT"a[194:195],v237 offset:16512\n"
PK"v24,v24,v25\n"
MF"v[48:51],a[108:111],a[44:47],v[48:51]\n"
EX"v6,v6\n"
MF"v[56:59],a[108:111],a[60:63],v[56:59]\n"
EX"v7,v7\n"
MF"v[56:59],a[104:107],a[56:59],v[56:59]\n"
EX"v12,v12\n"
PK"v25,v26,v27\n"
MF"v[36:39],a[112:115],a[0:3],0\n"
RT"a[196:197],v237 offset:17408\n"
RT"a[198:199],v237 offset:17536\n"
MF"v[36:39],a[116:119],a[4:7],v[36:39]\n"
EX"v13,v13\n"
MF"v[44:47],a[116:119],a[20:23],0\n"
RD"a[64:67],v227 offset:16384\n"
PK"v2,v4,v5\n"
MF"v[44:47],a[112:115],a[16:19],v[44:47]\n"
EX"v14,v14\n"
MF"v[52:55],a[112:115],a[32:35],0\n"
RT"a[200:201],v237 offset:18432\n"
RT"a[202:203],v237 offset:18560\n"
MF"v[52:55],a[116:119],a[36:39],v[52:55]\n"
EX"v15,v15\n"
PK"v3,v6,v7\n"
MF"v[60:63],a[116:119],a[52:55],0\n"
RD"a[68:71],v227 offset:17408\n"
PK"v10,v12,v13\n"
MF"v[60:63],a[112:115],a[48:51],v[60:63]\n"
WT"lgkmcnt(4)\n"
EX"v20,v20\n"
MF"v[36:39],a[120:123],a[8:11],v[36:39]\n"
EX"v21,v21\n"
MF"v[36:39],a[124:127],a[12:15],v[36:39]\n"
RD"a[72:75],v227 offset:18432\n"
"v_readlane_b32 s43,v228,s42\n"
MF"v[44:47],a[124:127],a[28:31],v[44:47]\n"
EX"v22,v22\n"
MF"v[44:47],a[120:123],a[24:27],v[44:47]\n"
"s_mov_b32 m0,s40\n"
PK"v11,v14,v15\n"
PK"v18,v20,v21\n"
MF"v[52:55],a[120:123],a[40:43],v[52:55]\n"
EX"v23,v23\n"
MF"v[52:55],a[124:127],a[44:47],v[52:55]\n"
RD"a[76:79],v227 offset:19456\n"
"v_readlane_b32 s44,v229,s42\n"
MF"v[60:63],a[124:127],a[60:63],v[60:63]\n"
EX"v28,v28\n"
MF"v[60:63],a[120:123],a[56:59],v[60:63]\n"
WT"lgkmcnt(2)\n"
BL"v225,s[8:11],s43 offen offset:0 sc1 lds\n"
MF"v[80:83],a[64:67],v[64:67],v[80:83]\n"
SA"s42,s42,1\n"
EX"v29,v29\n"
MF"v[112:115],a[64:67],v[68:71],v[112:115]\n"
SA"s41,s41,32768\n"
RD"a[80:83],v227 offset:20480\n"
PK"v19,v22,v23\n"
MF"v[144:147],a[64:67],v[72:75],v[144:147]\n"
"s_cmp_eq_u32 s41,163840\n"
EX"v30,v30\n"
MF"v[176:179],a[64:67],v[76:79],v[176:179]\n"
"s_cselect_b32 s41,0,s41\n"
PK"v26,v28,v29\n"
"v_add_u32 v226,s41,v224\n"
MF"v[84:87],a[68:71],v[64:67],v[84:87]\n"
EX"v31,v31\n"
MF"v[116:119],a[68:71],v[68:71],v[116:119]\n"
RD"a[84:87],v227 offset:21504\n"
PK"v27,v30,v31\n"
MF"v[148:151],a[68:71],v[72:75],v[148:151]\n"
WT"vmcnt(1)\n"
"v_cmp_eq_u64 vcc,s[28:29],v[240:241]\n"
"s_cmp_eq_u64 vcc,exec\n"
"s_cbranch_scc0 L1102_%=\n"
"L1103_%=:\n"
MF"v[180:183],a[68:71],v[76:79],v[180:183]\n"
WT"lgkmcnt(2)\n"
EX"v32,v32\n"
MF"v[88:91],a[72:75],v[64:67],v[88:91]\n"
BL"v225,s[8:11],s43 offen offset:1024 sc1 lds\n"
MF"v[120:123],a[72:75],v[68:71],v[120:123]\n"
EX"v33,v33\n"
MF"v[152:155],a[72:75],v[72:75],v[152:155]\n"
RD"a[88:91],v227 offset:22528\n"
MF"v[184:187],a[72:75],v[76:79],v[184:187]\n"
EX"v34,v34\n"
MF"v[92:95],a[76:79],v[64:67],v[92:95]\n"
EX"v35,v35\n"
MF"v[124:127],a[76:79],v[68:71],v[124:127]\n"
RD"a[92:95],v227 offset:23552\n"
MF"v[156:159],a[76:79],v[72:75],v[156:159]\n"
EX"v40,v40\n"
MF"v[188:191],a[76:79],v[76:79],v[188:191]\n"
WT"lgkmcnt(2)\n"
BL"v225,s[8:11],s43 offen offset:2048 sc1 lds\n"
MF"v[96:99],a[80:83],v[64:67],v[96:99]\n"
EX"v41,v41\n"
MF"v[128:131],a[80:83],v[68:71],v[128:131]\n"
RD"a[96:99],v227 offset:24576\n"
MF"v[160:163],a[80:83],v[72:75],v[160:163]\n"
EX"v42,v42\n"
MF"v[192:195],a[80:83],v[76:79],v[192:195]\n"
MF"v[100:103],a[84:87],v[64:67],v[100:103]\n"
EX"v43,v43\n"
MF"v[132:135],a[84:87],v[68:71],v[132:135]\n"
RD"a[100:103],v227 offset:25600\n"
MF"v[164:167],a[84:87],v[72:75],v[164:167]\n"
EX"v48,v48\n"
MF"v[196:199],a[84:87],v[76:79],v[196:199]\n"
WT"lgkmcnt(2)\n"
BL"v225,s[8:11],s43 offen offset:3072 sc1 lds\n"
MF"v[104:107],a[88:91],v[64:67],v[104:107]\n"
EX"v49,v49\n"
MF"v[136:139],a[88:91],v[68:71],v[136:139]\n"
RD"a[104:107],v227 offset:26624\n"
MF"v[168:171],a[88:91],v[72:75],v[168:171]\n"
EX"v50,v50\n"
MF"v[200:203],a[88:91],v[76:79],v[200:203]\n"
SA"m0,s40,16384\n"
MF"v[108:111],a[92:95],v[64:67],v[108:111]\n"
EX"v51,v51\n"
MF"v[140:143],a[92:95],v[68:71],v[140:143]\n"
RD"a[108:111],v227 offset:27648\n"
MF"v[172:175],a[92:95],v[72:75],v[172:175]\n"
EX"v56,v56\n"
MF"v[204:207],a[92:95],v[76:79],v[204:207]\n"
BL"v225,s[8:11],s44 offen offset:0 sc1 lds\n"
MF"a[132:135],a[128:131],v[64:67],a[132:135]\n"
SA"s40,s40,32768\n"
EX"v57,v57\n"
MF"a[136:139],a[128:131],v[68:71],a[136:139]\n"
"s_cmp_eq_u32 s40,s46\n"
RD"a[112:115],v227 offset:28672\n"
"s_cselect_b32 s40,s47,s40\n"
MF"a[140:143],a[128:131],v[72:75],a[140:143]\n"
EX"v58,v58\n"
MF"a[144:147],a[128:131],v[76:79],a[144:147]\n"
MF"v[240:243],a[148:151],a[204:207],0\n"
EX"v59,v59\n"
MF"v[240:243],a[152:155],a[208:211],v[240:243]\n"
RD"a[116:119],v227 offset:29696\n"
MF"v[240:243],a[156:159],a[212:215],v[240:243]\n"
EX"v36,v36\n"
MF"v[240:243],a[160:163],a[216:219],v[240:243]\n"
BL"v225,s[8:11],s44 offen offset:1024 sc1 lds\n"
MF"v[240:243],a[164:167],a[220:223],v[240:243]\n"
EX"v37,v37\n"
MF"v[240:243],a[168:171],a[224:227],v[240:243]\n"
RD"a[120:123],v227 offset:30720\n"
MF"v[240:243],a[172:175],a[228:231],v[240:243]\n"
EX"v38,v38\n"
MF"v[240:243],a[176:179],a[232:235],v[240:243]\n"
MF"v[240:243],a[180:183],a[236:239],v[240:243]\n"
EX"v39,v39\n"
MF"v[240:243],a[184:187],a[240:243],v[240:243]\n"
RD"a[124:127],v227 offset:31744\n"
MF"v[240:243],a[188:191],a[244:247],v[240:243]\n"
EX"v44,v44\n"
MF"v[240:243],a[192:195],a[248:251],v[240:243]\n"
BL"v225,s[8:11],s44 offen offset:2048 sc1 lds\n"
MF"v[240:243],a[196:199],a[252:255],v[240:243]\n"
EX"v45,v45\n"
MF"v[240:243],a[200:203],v[244:247],v[240:243]\n"
WT"lgkmcnt(1)\n"
MF"v[80:83],a[96:99],v[0:3],v[80:83]\n"
EX"v46,v46\n"
MF"v[112:115],a[96:99],v[8:11],v[112:115]\n"
MF"v[144:147],a[96:99],v[16:19],v[144:147]\n"
MF"v[176:179],a[96:99],v[24:27],v[176:179]\n"
MF"v[84:87],a[100:103],v[0:3],v[84:87]\n"
EX"v47,v47\n"
MF"v[116:119],a[100:103],v[8:11],v[116:119]\n"
"ds_write_b128 v236,v[240:243]\n"
MF"v[148:151],a[100:103],v[16:19],v[148:151]\n"
BL"v225,s[8:11],s44 offen offset:3072 sc1 lds\n"
MF"v[180:183],a[100:103],v[24:27],v[180:183]\n"
EX"v52,v52\n"
MF"v[88:91],a[104:107],v[0:3],v[88:91]\n"
EX"v53,v53\n"
MF"v[120:123],a[104:107],v[8:11],v[120:123]\n"
MF"v[152:155],a[104:107],v[16:19],v[152:155]\n"
EX"v54,v54\n"
MF"v[184:187],a[104:107],v[24:27],v[184:187]\n"
MF"v[92:95],a[108:111],v[0:3],v[92:95]\n"
EX"v55,v55\n"
MF"v[124:127],a[108:111],v[8:11],v[124:127]\n"
MF"v[156:159],a[108:111],v[16:19],v[156:159]\n"
EX"v60,v60\n"
MF"v[188:191],a[108:111],v[24:27],v[188:191]\n"
MF"v[96:99],a[112:115],v[0:3],v[96:99]\n"
EX"v61,v61\n"
MF"v[128:131],a[112:115],v[8:11],v[128:131]\n"
WT"lgkmcnt(0)\n"
"s_barrier\n"
MF"v[160:163],a[112:115],v[16:19],v[160:163]\n"
RD"a[64:67],v226 offset:0\n"
RD"v[240:243],v235 offset:0\n"
MF"v[192:195],a[112:115],v[24:27],v[192:195]\n"
EX"v62,v62\n"
MF"v[100:103],a[116:119],v[0:3],v[100:103]\n"
EX"v63,v63\n"
MF"v[132:135],a[116:119],v[8:11],v[132:135]\n"
RD"v[244:247],v235 offset:1024\n"
RD"a[68:71],v226 offset:1024\n"
MF"v[164:167],a[116:119],v[16:19],v[164:167]\n"
RD"v[248:251],v235 offset:2048\n"
MF"v[196:199],a[116:119],v[24:27],v[196:199]\n"
RD"a[72:75],v226 offset:2048\n"
MF"v[104:107],a[120:123],v[0:3],v[104:107]\n"
RD"v[252:255],v235 offset:3072\n"
MF"v[136:139],a[120:123],v[8:11],v[136:139]\n"
RD"a[76:79],v226 offset:3072\n"
MF"v[168:171],a[120:123],v[16:19],v[168:171]\n"
MF"v[200:203],a[120:123],v[24:27],v[200:203]\n"
MF"v[108:111],a[124:127],v[0:3],v[108:111]\n"
MF"v[140:143],a[124:127],v[8:11],v[140:143]\n"
RD"a[80:83],v226 offset:4096\n"
MF"v[172:175],a[124:127],v[16:19],v[172:175]\n"
MF"v[204:207],a[124:127],v[24:27],v[204:207]\n"
RD"a[84:87],v226 offset:5120\n"
MF"a[132:135],a[128:131],v[0:3],a[132:135]\n"
MF"a[136:139],a[128:131],v[8:11],a[136:139]\n"
RD"a[88:91],v226 offset:6144\n"
MF"a[140:143],a[128:131],v[16:19],a[140:143]\n"
MF"a[144:147],a[128:131],v[24:27],a[144:147]\n"
"s_cmp_eq_u32 s57,2\n"
"s_cbranch_scc1 LMD_%=\n"
"s_cmp_eq_u32 s34,2\n"
"s_cbranch_scc1 LMA_%=\n"
WT"lgkmcnt(10)\n"
MF"v[0:3],a[64:67],a[0:3],0\n"
WT"lgkmcnt(2)\n"
"v_add_f32 v240,v240,v244\n"
PK"v64,v32,v33\n"
MF"v[0:3],a[68:71],a[4:7],v[0:3]\n"
PK"v65,v34,v35\n"
"v_add_f32 v248,v248,v252\n"
MF"v[8:11],a[68:71],a[20:23],0\n"
PK"v66,v36,v37\n"
PK"v67,v38,v39\n"
MF"v[8:11],a[64:67],a[16:19],v[8:11]\n"
"v_add_f32 v241,v241,v245\n"
PK"v68,v40,v41\n"
MF"v[16:19],a[64:67],a[32:35],0\n"
PK"v69,v42,v43\n"
"v_add_f32 v249,v249,v253\n"
MF"v[16:19],a[68:71],a[36:39],v[16:19]\n"
RD"a[92:95],v226 offset:7168\n"
PK"v70,v44,v45\n"
MF"v[24:27],a[68:71],a[52:55],0\n"
PK"v71,v46,v47\n"
"v_add_f32 v242,v242,v246\n"
MF"v[24:27],a[64:67],a[48:51],v[24:27]\n"
PK"v72,v48,v49\n"
PK"v73,v50,v51\n"
MF"v[0:3],a[72:75],a[8:11],v[0:3]\n"
"v_mov_b32 v227,v226\n"
"v_add_f32 v250,v250,v254\n"
MF"v[0:3],a[76:79],a[12:15],v[0:3]\n"
PK"v74,v52,v53\n"
PK"v75,v54,v55\n"
MF"v[8:11],a[76:79],a[28:31],v[8:11]\n"
"v_add_f32 v243,v243,v247\n"
PK"v76,v56,v57\n"
MF"v[8:11],a[72:75],a[24:27],v[8:11]\n"
PK"v77,v58,v59\n"
"v_add_f32 v251,v251,v255\n"
MF"v[16:19],a[72:75],a[40:43],v[16:19]\n"
PK"v78,v60,v61\n"
PK"v79,v62,v63\n"
MF"v[16:19],a[76:79],a[44:47],v[16:19]\n"
RD"a[96:99],v226 offset:8192\n"
PK"v240,v240,v240\n"
MF"v[24:27],a[76:79],a[60:63],v[24:27]\n"
EX"v0,v0\n"
MF"v[24:27],a[72:75],a[56:59],v[24:27]\n"
"v_lshlrev_b32 v240,16,v240\n"
PK"v241,v241,v241\n"
MF"v[4:7],a[80:83],a[0:3],0\n"
WT"lgkmcnt(1)\n"
EX"v1,v1\n"
MF"v[4:7],a[84:87],a[4:7],v[4:7]\n"
RD"a[100:103],v226 offset:9216\n"
"v_lshlrev_b32 v241,16,v241\n"
MF"v[12:15],a[84:87],a[20:23],0\n"
EX"v2,v2\n"
MF"v[12:15],a[80:83],a[16:19],v[12:15]\n"
EX"v3,v3\n"
MF"v[20:23],a[80:83],a[32:35],0\n"
EX"v8,v8\n"
PK"v242,v242,v242\n"
MF"v[20:23],a[84:87],a[36:39],v[20:23]\n"
RD"a[104:107],v226 offset:10240\n"
"v_lshlrev_b32 v242,16,v242\n"
MF"v[28:31],a[84:87],a[52:55],0\n"
EX"v9,v9\n"
PK"v243,v243,v243\n"
MF"v[28:31],a[80:83],a[48:51],v[28:31]\n"
"v_lshlrev_b32 v243,16,v243\n"
MU"v244,0xbfb8aa3b,v240\n"
EX"v244,v244\n"
MF"v[4:7],a[88:91],a[8:11],v[4:7]\n"
EX"v10,v10\n"
MF"v[4:7],a[92:95],a[12:15],v[4:7]\n"
RD"a[108:111],v226 offset:11264\n"
MU"v245,0xbfb8aa3b,v241\n"
EX"v245,v245\n"
MF"v[12:15],a[92:95],a[28:31],v[12:15]\n"
EX"v11,v11\n"
PK"v0,v0,v1\n"
MF"v[12:15],a[88:91],a[24:27],v[12:15]\n"
EX"v16,v16\n"
MU"v246,0xbfb8aa3b,v242\n"
MF"v[20:23],a[88:91],a[40:43],v[20:23]\n"
EX"v246,v246\n"
MU"v247,0xbfb8aa3b,v243\n"
MF"v[20:23],a[92:95],a[44:47],v[20:23]\n"
EX"v17,v17\n"
PK"v1,v2,v3\n"
MF"v[28:31],a[92:95],a[60:63],v[28:31]\n"
EX"v247,v247\n"
PK"v248,v248,v248\n"
MF"v[28:31],a[88:91],a[56:59],v[28:31]\n"
WT"lgkmcnt(1)\n"
RD"a[112:115],v226 offset:12288\n"
"v_lshlrev_b32 v248,16,v248\n"
PK"v8,v8,v9\n"
MF"v[32:35],a[96:99],a[0:3],0\n"
EX"v18,v18\n"
PK"v249,v249,v249\n"
MF"v[32:35],a[100:103],a[4:7],v[32:35]\n"
EX"v19,v19\n"
"v_lshlrev_b32 v249,16,v249\n"
MF"v[40:43],a[100:103],a[20:23],0\n"
RD"a[116:119],v226 offset:13312\n"
PK"v250,v250,v250\n"
MF"v[40:43],a[96:99],a[16:19],v[40:43]\n"
EX"v24,v24\n"
PK"v9,v10,v11\n"
MF"v[48:51],a[96:99],a[32:35],0\n"
EX"v25,v25\n"
"v_lshlrev_b32 v250,16,v250\n"
MF"v[48:51],a[100:103],a[36:39],v[48:51]\n"
EX"v26,v26\n"
PK"v16,v16,v17\n"
MF"v[56:59],a[100:103],a[52:55],0\n"
RD"a[120:123],v226 offset:14336\n"
PK"v251,v251,v251\n"
MF"v[56:59],a[96:99],a[48:51],v[56:59]\n"
EX"v27,v27\n"
"v_lshlrev_b32 v251,16,v251\n"
MF"v[32:35],a[104:107],a[8:11],v[32:35]\n"
WT"lgkmcnt(2)\n"
EX"v4,v4\n"
"v_add_f32 v244,1.0,v244\n"
MF"v[32:35],a[108:111],a[12:15],v[32:35]\n"
"v_rcp_f32 v244,v244\n"
"v_add_f32 v245,1.0,v245\n"
MF"v[40:43],a[108:111],a[28:31],v[40:43]\n"
"v_rcp_f32 v245,v245\n"
PK"v17,v18,v19\n"
MF"v[40:43],a[104:107],a[24:27],v[40:43]\n"
RD"a[124:127],v226 offset:15360\n"
"v_add_f32 v246,1.0,v246\n"
"v_rcp_f32 v246,v246\n"
MF"v[48:51],a[104:107],a[40:43],v[48:51]\n"
EX"v5,v5\n"
"v_add_f32 v247,1.0,v247\n"
MF"v[48:51],a[108:111],a[44:47],v[48:51]\n"
"v_rcp_f32 v247,v247\n"
EX"v6,v6\n"
MF"v[56:59],a[108:111],a[60:63],v[56:59]\n"
EX"v7,v7\n"
PK"v24,v24,v25\n"
MF"v[56:59],a[104:107],a[56:59],v[56:59]\n"
EX"v12,v12\n"
PK"v25,v26,v27\n"
MF"v[36:39],a[112:115],a[0:3],0\n"
WT"lgkmcnt(1)\n"
EX"v13,v13\n"
MU"v240,v240,v244\n"
MF"v[36:39],a[116:119],a[4:7],v[36:39]\n"
RD"a[64:67],v227 offset:16384\n"
MU"v241,v241,v245\n"
MF"v[44:47],a[116:119],a[20:23],0\n"
EX"v14,v14\n"
MU"v242,v242,v246\n"
MF"v[44:47],a[112:115],a[16:19],v[44:47]\n"
MU"v243,v243,v247\n"
PK"v2,v4,v5\n"
MF"v[52:55],a[112:115],a[32:35],0\n"
EX"v15,v15\n"
PK"v240,v240,v240\n"
MF"v[52:55],a[116:119],a[36:39],v[52:55]\n"
RD"a[68:71],v227 offset:17408\n"
"v_lshlrev_b32 v240,16,v240\n"
PK"v3,v6,v7\n"
MF"v[60:63],a[116:119],a[52:55],0\n"
EX"v20,v20\n"
PK"v241,v241,v241\n"
MF"v[60:63],a[112:115],a[48:51],v[60:63]\n"
"v_readlane_b32 s43,v228,s42\n"
"v_lshlrev_b32 v241,16,v241\n"
MF"v[36:39],a[120:123],a[8:11],v[36:39]\n"
WT"lgkmcnt(1)\n"
EX"v21,v21\n"
PK"v242,v242,v242\n"
MF"v[36:39],a[124:127],a[12:15],v[36:39]\n"
RD"a[72:75],v227 offset:18432\n"
"v_lshlrev_b32 v242,16,v242\n"
PK"v10,v12,v13\n"
MF"v[44:47],a[124:127],a[28:31],v[44:47]\n"
EX"v22,v22\n"
PK"v243,v243,v243\n"
MF"v[44:47],a[120:123],a[24:27],v[44:47]\n"
"s_mov_b32 m0,s40\n"
"v_lshlrev_b32 v243,16,v243\n"
MU"v240,v240,v248\n"
MF"v[52:55],a[120:123],a[40:43],v[52:55]\n"
EX"v23,v23\n"
MF"v[52:55],a[124:127],a[44:47],v[52:55]\n"
RD"a[76:79],v227 offset:19456\n"
"v_readlane_b32 s44,v229,s42\n"
MU"v241,v241,v249\n"
MF"v[60:63],a[124:127],a[60:63],v[60:63]\n"
EX"v28,v28\n"
PK"v11,v14,v15\n"
MF"v[60:63],a[120:123],a[56:59],v[60:63]\n"
SA"s42,s42,1\n"
BL"v225,s[8:11],s43 offen offset:0 sc1 lds\n"
MU"v242,v242,v250\n"
MF"v[80:83],a[64:67],v[64:67],v[80:83]\n"
SA"s41,s41,32768\n"
EX"v29,v29\n"
MU"v243,v243,v251\n"
MF"v[112:115],a[64:67],v[68:71],v[112:115]\n"
"s_cmp_eq_u32 s41,163840\n"
RD"a[80:83],v227 offset:20480\n"
PK"v240,v240,v241\n"
PK"v241,v242,v243\n"
MF"v[144:147],a[64:67],v[72:75],v[144:147]\n"
"s_cselect_b32 s41,0,s41\n"
EX"v30,v30\n"
PK"v18,v20,v21\n"
MF"v[176:179],a[64:67],v[76:79],v[176:179]\n"
WT"lgkmcnt(2)\n"
"v_add_u32 v226,s41,v224\n"
"v_lshrrev_b32 v239,9,v224\n"
"v_lshlrev_b32 v239,8,v239\n"
MF"v[84:87],a[68:71],v[64:67],v[84:87]\n"
EX"v31,v31\n"
"v_bfe_u32 v237,v224,8,1\n"
MF"v[116:119],a[68:71],v[68:71],v[116:119]\n"
RD"a[84:87],v227 offset:21504\n"
"v_lshl_add_u32 v239,v237,3,v239\n"
PK"v19,v22,v23\n"
MF"v[148:151],a[68:71],v[72:75],v[148:151]\n"
EX"v32,v32\n"
"v_and_b32 v237,0x80,v224\n"
MF"v[180:183],a[68:71],v[76:79],v[180:183]\n"
BL"v225,s[8:11],s43 offen offset:1024 sc1 lds\n"
"v_lshl_add_u32 v239,v237,23,v239\n"
MF"v[88:91],a[72:75],v[64:67],v[88:91]\n"
EX"v33,v33\n"
"v_and_b32 v237,0x70,v224\n"
MF"v[120:123],a[72:75],v[68:71],v[120:123]\n"
RD"a[88:91],v227 offset:22528\n"
"v_add_u32 v239,v239,v237\n"
PK"v26,v28,v29\n"
MF"v[152:155],a[72:75],v[72:75],v[152:155]\n"
"buffer_store_dwordx2 v[240:241],v239,s[8:11],s93 offen sc1\n"
MF"v[184:187],a[72:75],v[76:79],v[184:187]\n"
WT"lgkmcnt(2)\n"
EX"v34,v34\n"
MF"v[92:95],a[76:79],v[64:67],v[92:95]\n"
EX"v35,v35\n"
MF"v[124:127],a[76:79],v[68:71],v[124:127]\n"
RD"a[92:95],v227 offset:23552\n"
PK"v27,v30,v31\n"
MF"v[156:159],a[76:79],v[72:75],v[156:159]\n"
EX"v40,v40\n"
MF"v[188:191],a[76:79],v[76:79],v[188:191]\n"
BL"v225,s[8:11],s43 offen offset:2048 sc1 lds\n"
MF"v[96:99],a[80:83],v[64:67],v[96:99]\n"
EX"v41,v41\n"
MF"v[128:131],a[80:83],v[68:71],v[128:131]\n"
RD"a[96:99],v227 offset:24576\n"
MF"v[160:163],a[80:83],v[72:75],v[160:163]\n"
EX"v42,v42\n"
MF"v[192:195],a[80:83],v[76:79],v[192:195]\n"
WT"lgkmcnt(2)\n"
MF"v[100:103],a[84:87],v[64:67],v[100:103]\n"
EX"v43,v43\n"
MF"v[132:135],a[84:87],v[68:71],v[132:135]\n"
RD"a[100:103],v227 offset:25600\n"
MF"v[164:167],a[84:87],v[72:75],v[164:167]\n"
EX"v48,v48\n"
MF"v[196:199],a[84:87],v[76:79],v[196:199]\n"
BL"v225,s[8:11],s43 offen offset:3072 sc1 lds\n"
MF"v[104:107],a[88:91],v[64:67],v[104:107]\n"
EX"v49,v49\n"
MF"v[136:139],a[88:91],v[68:71],v[136:139]\n"
RD"a[104:107],v227 offset:26624\n"
MF"v[168:171],a[88:91],v[72:75],v[168:171]\n"
EX"v50,v50\n"
MF"v[200:203],a[88:91],v[76:79],v[200:203]\n"
WT"lgkmcnt(2)\n"
SA"m0,s40,16384\n"
MF"v[108:111],a[92:95],v[64:67],v[108:111]\n"
EX"v51,v51\n"
MF"v[140:143],a[92:95],v[68:71],v[140:143]\n"
RD"a[108:111],v227 offset:27648\n"
MF"v[172:175],a[92:95],v[72:75],v[172:175]\n"
EX"v56,v56\n"
MF"v[204:207],a[92:95],v[76:79],v[204:207]\n"
BL"v225,s[8:11],s44 offen offset:0 sc1 lds\n"
MF"a[132:135],a[128:131],v[64:67],a[132:135]\n"
SA"s40,s40,32768\n"
EX"v57,v57\n"
MF"a[136:139],a[128:131],v[68:71],a[136:139]\n"
"s_cmp_eq_u32 s40,s46\n"
RD"a[112:115],v227 offset:28672\n"
"s_cselect_b32 s40,s47,s40\n"
MF"a[140:143],a[128:131],v[72:75],a[140:143]\n"
EX"v58,v58\n"
MF"a[144:147],a[128:131],v[76:79],a[144:147]\n"
MF"v[80:83],a[96:99],v[0:3],v[80:83]\n"
EX"v59,v59\n"
MF"v[112:115],a[96:99],v[8:11],v[112:115]\n"
RD"a[116:119],v227 offset:29696\n"
MF"v[144:147],a[96:99],v[16:19],v[144:147]\n"
EX"v36,v36\n"
MF"v[176:179],a[96:99],v[24:27],v[176:179]\n"
WT"lgkmcnt(2)\n"
BL"v225,s[8:11],s44 offen offset:1024 sc1 lds\n"
MF"v[84:87],a[100:103],v[0:3],v[84:87]\n"
EX"v37,v37\n"
MF"v[116:119],a[100:103],v[8:11],v[116:119]\n"
RD"a[120:123],v227 offset:30720\n"
MF"v[148:151],a[100:103],v[16:19],v[148:151]\n"
EX"v38,v38\n"
MF"v[180:183],a[100:103],v[24:27],v[180:183]\n"
MF"v[88:91],a[104:107],v[0:3],v[88:91]\n"
EX"v39,v39\n"
MF"v[120:123],a[104:107],v[8:11],v[120:123]\n"
RD"a[124:127],v227 offset:31744\n"
MF"v[152:155],a[104:107],v[16:19],v[152:155]\n"
EX"v44,v44\n"
MF"v[184:187],a[104:107],v[24:27],v[184:187]\n"
BL"v225,s[8:11],s44 offen offset:2048 sc1 lds\n"
MF"v[92:95],a[108:111],v[0:3],v[92:95]\n"
EX"v45,v45\n"
MF"v[124:127],a[108:111],v[8:11],v[124:127]\n"
MF"v[156:159],a[108:111],v[16:19],v[156:159]\n"
EX"v46,v46\n"
MF"v[188:191],a[108:111],v[24:27],v[188:191]\n"
WT"lgkmcnt(1)\n"
MF"v[96:99],a[112:115],v[0:3],v[96:99]\n"
WT"lgkmcnt(0)\n"
"s_barrier\n"
MF"v[128:131],a[112:115],v[8:11],v[128:131]\n"
RD"a[64:67],v226 offset:0\n"
MF"v[160:163],a[112:115],v[16:19],v[160:163]\n"
EX"v47,v47\n"
MF"v[192:195],a[112:115],v[24:27],v[192:195]\n"
BL"v225,s[8:11],s44 offen offset:3072 sc1 lds\n"
MF"v[100:103],a[116:119],v[0:3],v[100:103]\n"
EX"v52,v52\n"
MF"v[132:135],a[116:119],v[8:11],v[132:135]\n"
RD"a[68:71],v226 offset:1024\n"
MF"v[164:167],a[116:119],v[16:19],v[164:167]\n"
EX"v53,v53\n"
MF"v[196:199],a[116:119],v[24:27],v[196:199]\n"
RD"a[72:75],v226 offset:2048\n"
MF"v[104:107],a[120:123],v[0:3],v[104:107]\n"
EX"v54,v54\n"
MF"v[136:139],a[120:123],v[8:11],v[136:139]\n"
RD"a[76:79],v226 offset:3072\n"
MF"v[168:171],a[120:123],v[16:19],v[168:171]\n"
EX"v55,v55\n"
MF"v[200:203],a[120:123],v[24:27],v[200:203]\n"
MF"v[108:111],a[124:127],v[0:3],v[108:111]\n"
WT"vmcnt(6)\n"
"buffer_store_dwordx2 v[232:233],v230,s[8:11],s94 offen sc1\n"
MF"v[140:143],a[124:127],v[8:11],v[140:143]\n"
EX"v60,v60\n"
MF"v[172:175],a[124:127],v[16:19],v[172:175]\n"
RD"a[80:83],v226 offset:4096\n"
MF"v[204:207],a[124:127],v[24:27],v[204:207]\n"
EX"v61,v61\n"
MF"a[132:135],a[128:131],v[0:3],a[132:135]\n"
RD"a[84:87],v226 offset:5120\n"
MF"a[136:139],a[128:131],v[8:11],a[136:139]\n"
EX"v62,v62\n"
MF"a[140:143],a[128:131],v[16:19],a[140:143]\n"
RD"a[88:91],v226 offset:6144\n"
MF"a[144:147],a[128:131],v[24:27],a[144:147]\n"
EX"v63,v63\n"
"s_mov_b32 s57,0\n"
"s_lshl_b32 s56,s34,1\n"
"s_sub_u32 s56,s56,6\n"
"s_branch LP1_%=\n"
"LMA_%=:\n"
WT"lgkmcnt(10)\n"
MF"v[0:3],a[64:67],a[0:3],v[208:211]\n"
WT"lgkmcnt(2)\n"
"v_add_f32 v240,v240,v244\n"
PK"v64,v32,v33\n"
MF"v[0:3],a[68:71],a[4:7],v[0:3]\n"
PK"v65,v34,v35\n"
"v_add_f32 v248,v248,v252\n"
MF"v[8:11],a[68:71],a[20:23],0\n"
PK"v66,v36,v37\n"
PK"v67,v38,v39\n"
MF"v[8:11],a[64:67],a[16:19],v[8:11]\n"
"v_add_f32 v241,v241,v245\n"
PK"v68,v40,v41\n"
MF"v[16:19],a[64:67],a[32:35],0\n"
PK"v69,v42,v43\n"
"v_add_f32 v249,v249,v253\n"
MF"v[16:19],a[68:71],a[36:39],v[16:19]\n"
RD"a[92:95],v226 offset:7168\n"
PK"v70,v44,v45\n"
MF"v[24:27],a[68:71],a[52:55],0\n"
PK"v71,v46,v47\n"
"v_add_f32 v242,v242,v246\n"
MF"v[24:27],a[64:67],a[48:51],v[24:27]\n"
PK"v72,v48,v49\n"
PK"v73,v50,v51\n"
MF"v[0:3],a[72:75],a[8:11],v[0:3]\n"
"v_mov_b32 v227,v226\n"
"v_add_f32 v250,v250,v254\n"
MF"v[0:3],a[76:79],a[12:15],v[0:3]\n"
PK"v74,v52,v53\n"
PK"v75,v54,v55\n"
MF"v[8:11],a[76:79],a[28:31],v[8:11]\n"
"v_add_f32 v243,v243,v247\n"
PK"v76,v56,v57\n"
MF"v[8:11],a[72:75],a[24:27],v[8:11]\n"
PK"v77,v58,v59\n"
"v_add_f32 v251,v251,v255\n"
MF"v[16:19],a[72:75],a[40:43],v[16:19]\n"
PK"v78,v60,v61\n"
PK"v79,v62,v63\n"
MF"v[16:19],a[76:79],a[44:47],v[16:19]\n"
RD"a[96:99],v226 offset:8192\n"
PK"v240,v240,v240\n"
MF"v[24:27],a[76:79],a[60:63],v[24:27]\n"
EX"v0,v0\n"
MF"v[24:27],a[72:75],a[56:59],v[24:27]\n"
"v_lshlrev_b32 v240,16,v240\n"
PK"v241,v241,v241\n"
MF"v[4:7],a[80:83],a[0:3],v[212:215]\n"
WT"lgkmcnt(1)\n"
EX"v1,v1\n"
MF"v[4:7],a[84:87],a[4:7],v[4:7]\n"
RD"a[100:103],v226 offset:9216\n"
"v_lshlrev_b32 v241,16,v241\n"
MF"v[12:15],a[84:87],a[20:23],0\n"
EX"v2,v2\n"
MF"v[12:15],a[80:83],a[16:19],v[12:15]\n"
EX"v3,v3\n"
MF"v[20:23],a[80:83],a[32:35],0\n"
EX"v8,v8\n"
PK"v242,v242,v242\n"
MF"v[20:23],a[84:87],a[36:39],v[20:23]\n"
RD"a[104:107],v226 offset:10240\n"
"v_lshlrev_b32 v242,16,v242\n"
MF"v[28:31],a[84:87],a[52:55],0\n"
EX"v9,v9\n"
PK"v243,v243,v243\n"
MF"v[28:31],a[80:83],a[48:51],v[28:31]\n"
"v_lshlrev_b32 v243,16,v243\n"
MU"v244,0xbfb8aa3b,v240\n"
EX"v244,v244\n"
MF"v[4:7],a[88:91],a[8:11],v[4:7]\n"
EX"v10,v10\n"
MF"v[4:7],a[92:95],a[12:15],v[4:7]\n"
RD"a[108:111],v226 offset:11264\n"
MU"v245,0xbfb8aa3b,v241\n"
EX"v245,v245\n"
MF"v[12:15],a[92:95],a[28:31],v[12:15]\n"
EX"v11,v11\n"
PK"v0,v0,v1\n"
MF"v[12:15],a[88:91],a[24:27],v[12:15]\n"
EX"v16,v16\n"
MU"v246,0xbfb8aa3b,v242\n"
MF"v[20:23],a[88:91],a[40:43],v[20:23]\n"
EX"v246,v246\n"
MU"v247,0xbfb8aa3b,v243\n"
MF"v[20:23],a[92:95],a[44:47],v[20:23]\n"
EX"v17,v17\n"
PK"v1,v2,v3\n"
MF"v[28:31],a[92:95],a[60:63],v[28:31]\n"
EX"v247,v247\n"
PK"v248,v248,v248\n"
MF"v[28:31],a[88:91],a[56:59],v[28:31]\n"
WT"lgkmcnt(1)\n"
RD"a[112:115],v226 offset:12288\n"
"v_lshlrev_b32 v248,16,v248\n"
PK"v8,v8,v9\n"
MF"v[32:35],a[96:99],a[0:3],v[216:219]\n"
EX"v18,v18\n"
PK"v249,v249,v249\n"
MF"v[32:35],a[100:103],a[4:7],v[32:35]\n"
EX"v19,v19\n"
"v_lshlrev_b32 v249,16,v249\n"
MF"v[40:43],a[100:103],a[20:23],0\n"
RD"a[116:119],v226 offset:13312\n"
PK"v250,v250,v250\n"
MF"v[40:43],a[96:99],a[16:19],v[40:43]\n"
EX"v24,v24\n"
PK"v9,v10,v11\n"
MF"v[48:51],a[96:99],a[32:35],0\n"
EX"v25,v25\n"
"v_lshlrev_b32 v250,16,v250\n"
MF"v[48:51],a[100:103],a[36:39],v[48:51]\n"
EX"v26,v26\n"
PK"v16,v16,v17\n"
MF"v[56:59],a[100:103],a[52:55],0\n"
RD"a[120:123],v226 offset:14336\n"
PK"v251,v251,v251\n"
MF"v[56:59],a[96:99],a[48:51],v[56:59]\n"
EX"v27,v27\n"
"v_lshlrev_b32 v251,16,v251\n"
MF"v[32:35],a[104:107],a[8:11],v[32:35]\n"
WT"lgkmcnt(2)\n"
EX"v4,v4\n"
"v_add_f32 v244,1.0,v244\n"
MF"v[32:35],a[108:111],a[12:15],v[32:35]\n"
"v_rcp_f32 v244,v244\n"
"v_add_f32 v245,1.0,v245\n"
MF"v[40:43],a[108:111],a[28:31],v[40:43]\n"
"v_rcp_f32 v245,v245\n"
PK"v17,v18,v19\n"
MF"v[40:43],a[104:107],a[24:27],v[40:43]\n"
RD"a[124:127],v226 offset:15360\n"
"v_add_f32 v246,1.0,v246\n"
"v_rcp_f32 v246,v246\n"
MF"v[48:51],a[104:107],a[40:43],v[48:51]\n"
EX"v5,v5\n"
"v_add_f32 v247,1.0,v247\n"
MF"v[48:51],a[108:111],a[44:47],v[48:51]\n"
"v_rcp_f32 v247,v247\n"
EX"v6,v6\n"
MF"v[56:59],a[108:111],a[60:63],v[56:59]\n"
EX"v7,v7\n"
PK"v24,v24,v25\n"
MF"v[56:59],a[104:107],a[56:59],v[56:59]\n"
EX"v12,v12\n"
PK"v25,v26,v27\n"
MF"v[36:39],a[112:115],a[0:3],v[220:223]\n"
WT"lgkmcnt(1)\n"
EX"v13,v13\n"
MU"v240,v240,v244\n"
MF"v[36:39],a[116:119],a[4:7],v[36:39]\n"
RD"a[64:67],v227 offset:16384\n"
MU"v241,v241,v245\n"
MF"v[44:47],a[116:119],a[20:23],0\n"
EX"v14,v14\n"
MU"v242,v242,v246\n"
MF"v[44:47],a[112:115],a[16:19],v[44:47]\n"
MU"v243,v243,v247\n"
PK"v2,v4,v5\n"
MF"v[52:55],a[112:115],a[32:35],0\n"
EX"v15,v15\n"
PK"v240,v240,v240\n"
MF"v[52:55],a[116:119],a[36:39],v[52:55]\n"
RD"a[68:71],v227 offset:17408\n"
"v_lshlrev_b32 v240,16,v240\n"
PK"v3,v6,v7\n"
MF"v[60:63],a[116:119],a[52:55],0\n"
EX"v20,v20\n"
PK"v241,v241,v241\n"
MF"v[60:63],a[112:115],a[48:51],v[60:63]\n"
"v_readlane_b32 s43,v228,s42\n"
"v_lshlrev_b32 v241,16,v241\n"
MF"v[36:39],a[120:123],a[8:11],v[36:39]\n"
WT"lgkmcnt(1)\n"
EX"v21,v21\n"
PK"v242,v242,v242\n"
MF"v[36:39],a[124:127],a[12:15],v[36:39]\n"
RD"a[72:75],v227 offset:18432\n"
"v_lshlrev_b32 v242,16,v242\n"
PK"v10,v12,v13\n"
MF"v[44:47],a[124:127],a[28:31],v[44:47]\n"
EX"v22,v22\n"
PK"v243,v243,v243\n"
MF"v[44:47],a[120:123],a[24:27],v[44:47]\n"
"s_mov_b32 m0,s40\n"
"v_lshlrev_b32 v243,16,v243\n"
MU"v240,v240,v248\n"
MF"v[52:55],a[120:123],a[40:43],v[52:55]\n"
EX"v23,v23\n"
MF"v[52:55],a[124:127],a[44:47],v[52:55]\n"
RD"a[76:79],v227 offset:19456\n"
"v_readlane_b32 s44,v229,s42\n"
MU"v241,v241,v249\n"
MF"v[60:63],a[124:127],a[60:63],v[60:63]\n"
EX"v28,v28\n"
PK"v11,v14,v15\n"
MF"v[60:63],a[120:123],a[56:59],v[60:63]\n"
SA"s42,s42,1\n"
BL"v225,s[8:11],s43 offen offset:0 sc1 lds\n"
MU"v242,v242,v250\n"
MF"v[80:83],a[64:67],v[64:67],v[80:83]\n"
SA"s41,s41,32768\n"
EX"v29,v29\n"
MU"v243,v243,v251\n"
MF"v[112:115],a[64:67],v[68:71],v[112:115]\n"
"s_cmp_eq_u32 s41,163840\n"
RD"a[80:83],v227 offset:20480\n"
PK"v240,v240,v241\n"
PK"v241,v242,v243\n"
MF"v[144:147],a[64:67],v[72:75],v[144:147]\n"
"s_cselect_b32 s41,0,s41\n"
EX"v30,v30\n"
PK"v18,v20,v21\n"
MF"v[176:179],a[64:67],v[76:79],v[176:179]\n"
WT"lgkmcnt(2)\n"
"v_add_u32 v226,s41,v224\n"
"v_lshrrev_b32 v239,9,v224\n"
"v_lshlrev_b32 v239,8,v239\n"
MF"v[84:87],a[68:71],v[64:67],v[84:87]\n"
EX"v31,v31\n"
"v_bfe_u32 v237,v224,8,1\n"
MF"v[116:119],a[68:71],v[68:71],v[116:119]\n"
RD"a[84:87],v227 offset:21504\n"
"v_lshl_add_u32 v239,v237,3,v239\n"
PK"v19,v22,v23\n"
MF"v[148:151],a[68:71],v[72:75],v[148:151]\n"
EX"v32,v32\n"
"v_and_b32 v237,0x80,v224\n"
MF"v[180:183],a[68:71],v[76:79],v[180:183]\n"
BL"v225,s[8:11],s43 offen offset:1024 sc1 lds\n"
"v_lshl_add_u32 v239,v237,23,v239\n"
MF"v[88:91],a[72:75],v[64:67],v[88:91]\n"
EX"v33,v33\n"
"v_and_b32 v237,0x70,v224\n"
MF"v[120:123],a[72:75],v[68:71],v[120:123]\n"
RD"a[88:91],v227 offset:22528\n"
"v_add_u32 v239,v239,v237\n"
PK"v26,v28,v29\n"
MF"v[152:155],a[72:75],v[72:75],v[152:155]\n"
"buffer_store_dwordx2 v[240:241],v239,s[8:11],s93 offen sc1\n"
MF"v[184:187],a[72:75],v[76:79],v[184:187]\n"
WT"lgkmcnt(2)\n"
EX"v34,v34\n"
MF"v[92:95],a[76:79],v[64:67],v[92:95]\n"
EX"v35,v35\n"
MF"v[124:127],a[76:79],v[68:71],v[124:127]\n"
RD"a[92:95],v227 offset:23552\n"
PK"v27,v30,v31\n"
MF"v[156:159],a[76:79],v[72:75],v[156:159]\n"
EX"v40,v40\n"
MF"v[188:191],a[76:79],v[76:79],v[188:191]\n"
BL"v225,s[8:11],s43 offen offset:2048 sc1 lds\n"
MF"v[96:99],a[80:83],v[64:67],v[96:99]\n"
EX"v41,v41\n"
MF"v[128:131],a[80:83],v[68:71],v[128:131]\n"
RD"a[96:99],v227 offset:24576\n"
MF"v[160:163],a[80:83],v[72:75],v[160:163]\n"
EX"v42,v42\n"
MF"v[192:195],a[80:83],v[76:79],v[192:195]\n"
WT"lgkmcnt(2)\n"
MF"v[100:103],a[84:87],v[64:67],v[100:103]\n"
EX"v43,v43\n"
MF"v[132:135],a[84:87],v[68:71],v[132:135]\n"
RD"a[100:103],v227 offset:25600\n"
MF"v[164:167],a[84:87],v[72:75],v[164:167]\n"
EX"v48,v48\n"
MF"v[196:199],a[84:87],v[76:79],v[196:199]\n"
BL"v225,s[8:11],s43 offen offset:3072 sc1 lds\n"
MF"v[104:107],a[88:91],v[64:67],v[104:107]\n"
EX"v49,v49\n"
MF"v[136:139],a[88:91],v[68:71],v[136:139]\n"
RD"a[104:107],v227 offset:26624\n"
MF"v[168:171],a[88:91],v[72:75],v[168:171]\n"
EX"v50,v50\n"
MF"v[200:203],a[88:91],v[76:79],v[200:203]\n"
WT"lgkmcnt(2)\n"
SA"m0,s40,16384\n"
MF"v[108:111],a[92:95],v[64:67],v[108:111]\n"
EX"v51,v51\n"
MF"v[140:143],a[92:95],v[68:71],v[140:143]\n"
RD"a[108:111],v227 offset:27648\n"
MF"v[172:175],a[92:95],v[72:75],v[172:175]\n"
EX"v56,v56\n"
MF"v[204:207],a[92:95],v[76:79],v[204:207]\n"
BL"v225,s[8:11],s44 offen offset:0 sc1 lds\n"
MF"a[132:135],a[128:131],v[64:67],a[132:135]\n"
SA"s40,s40,32768\n"
EX"v57,v57\n"
MF"a[136:139],a[128:131],v[68:71],a[136:139]\n"
"s_cmp_eq_u32 s40,s46\n"
RD"a[112:115],v227 offset:28672\n"
"s_cselect_b32 s40,s47,s40\n"
MF"a[140:143],a[128:131],v[72:75],a[140:143]\n"
EX"v58,v58\n"
MF"a[144:147],a[128:131],v[76:79],a[144:147]\n"
MF"v[80:83],a[96:99],v[0:3],v[80:83]\n"
EX"v59,v59\n"
MF"v[112:115],a[96:99],v[8:11],v[112:115]\n"
RD"a[116:119],v227 offset:29696\n"
MF"v[144:147],a[96:99],v[16:19],v[144:147]\n"
EX"v36,v36\n"
MF"v[176:179],a[96:99],v[24:27],v[176:179]\n"
WT"lgkmcnt(2)\n"
BL"v225,s[8:11],s44 offen offset:1024 sc1 lds\n"
MF"v[84:87],a[100:103],v[0:3],v[84:87]\n"
EX"v37,v37\n"
MF"v[116:119],a[100:103],v[8:11],v[116:119]\n"
RD"a[120:123],v227 offset:30720\n"
MF"v[148:151],a[100:103],v[16:19],v[148:151]\n"
EX"v38,v38\n"
MF"v[180:183],a[100:103],v[24:27],v[180:183]\n"
MF"v[88:91],a[104:107],v[0:3],v[88:91]\n"
EX"v39,v39\n"
MF"v[120:123],a[104:107],v[8:11],v[120:123]\n"
RD"a[124:127],v227 offset:31744\n"
MF"v[152:155],a[104:107],v[16:19],v[152:155]\n"
EX"v44,v44\n"
MF"v[184:187],a[104:107],v[24:27],v[184:187]\n"
BL"v225,s[8:11],s44 offen offset:2048 sc1 lds\n"
MF"v[92:95],a[108:111],v[0:3],v[92:95]\n"
EX"v45,v45\n"
MF"v[124:127],a[108:111],v[8:11],v[124:127]\n"
MF"v[156:159],a[108:111],v[16:19],v[156:159]\n"
EX"v46,v46\n"
MF"v[188:191],a[108:111],v[24:27],v[188:191]\n"
WT"lgkmcnt(1)\n"
MF"v[96:99],a[112:115],v[0:3],v[96:99]\n"
WT"lgkmcnt(0)\n"
"s_barrier\n"
MF"v[128:131],a[112:115],v[8:11],v[128:131]\n"
RD"a[64:67],v226 offset:0\n"
MF"v[160:163],a[112:115],v[16:19],v[160:163]\n"
EX"v47,v47\n"
MF"v[192:195],a[112:115],v[24:27],v[192:195]\n"
BL"v225,s[8:11],s44 offen offset:3072 sc1 lds\n"
MF"v[100:103],a[116:119],v[0:3],v[100:103]\n"
EX"v52,v52\n"
MF"v[132:135],a[116:119],v[8:11],v[132:135]\n"
RD"a[68:71],v226 offset:1024\n"
MF"v[164:167],a[116:119],v[16:19],v[164:167]\n"
EX"v53,v53\n"
MF"v[196:199],a[116:119],v[24:27],v[196:199]\n"
RD"a[72:75],v226 offset:2048\n"
MF"v[104:107],a[120:123],v[0:3],v[104:107]\n"
EX"v54,v54\n"
MF"v[136:139],a[120:123],v[8:11],v[136:139]\n"
RD"a[76:79],v226 offset:3072\n"
MF"v[168:171],a[120:123],v[16:19],v[168:171]\n"
EX"v55,v55\n"
MF"v[200:203],a[120:123],v[24:27],v[200:203]\n"
MF"v[108:111],a[124:127],v[0:3],v[108:111]\n"
WT"vmcnt(6)\n"
"buffer_store_dwordx2 v[232:233],v230,s[8:11],s94 offen sc1\n"
MF"v[140:143],a[124:127],v[8:11],v[140:143]\n"
EX"v60,v60\n"
MF"v[172:175],a[124:127],v[16:19],v[172:175]\n"
RD"a[80:83],v226 offset:4096\n"
MF"v[204:207],a[124:127],v[24:27],v[204:207]\n"
EX"v61,v61\n"
MF"a[132:135],a[128:131],v[0:3],a[132:135]\n"
RD"a[84:87],v226 offset:5120\n"
MF"a[136:139],a[128:131],v[8:11],a[136:139]\n"
EX"v62,v62\n"
MF"a[140:143],a[128:131],v[16:19],a[140:143]\n"
RD"a[88:91],v226 offset:6144\n"
MF"a[144:147],a[128:131],v[24:27],a[144:147]\n"
EX"v63,v63\n"
"s_branch LDB_%=\n"
"LMD_%=:\n"
"s_cmp_eq_u32 s34,6\n"
"s_cbranch_scc1 LME_%=\n"
WT"lgkmcnt(10)\n"
MF"v[0:3],a[64:67],a[0:3],v[208:211]\n"
WT"lgkmcnt(2)\n"
"v_lshrrev_b32 v239,9,v224\n"
"v_lshlrev_b32 v239,8,v239\n"
MF"v[0:3],a[68:71],a[4:7],v[0:3]\n"
PK"v64,v32,v33\n"
PK"v65,v34,v35\n"
MF"v[8:11],a[68:71],a[20:23],0\n"
"v_bfe_u32 v237,v224,8,1\n"
PK"v66,v36,v37\n"
MF"v[8:11],a[64:67],a[16:19],v[8:11]\n"
PK"v67,v38,v39\n"
"v_lshl_add_u32 v239,v237,3,v239\n"
MF"v[16:19],a[64:67],a[32:35],0\n"
PK"v68,v40,v41\n"
PK"v69,v42,v43\n"
MF"v[16:19],a[68:71],a[36:39],v[16:19]\n"
RD"a[92:95],v226 offset:7168\n"
"v_and_b32 v237,0x80,v224\n"
MF"v[24:27],a[68:71],a[52:55],0\n"
PK"v70,v44,v45\n"
PK"v71,v46,v47\n"
MF"v[24:27],a[64:67],a[48:51],v[24:27]\n"
"v_lshl_add_u32 v239,v237,23,v239\n"
"v_not_b32 v236,v234\n"
MF"v[0:3],a[72:75],a[8:11],v[0:3]\n"
PK"v72,v48,v49\n"
PK"v73,v50,v51\n"
MF"v[0:3],a[76:79],a[12:15],v[0:3]\n"
SA"s53,s93,452608\n"
"v_mov_b32 v227,v226\n"
"v_and_b32 v237,15,v236\n"
MF"v[8:11],a[76:79],a[28:31],v[8:11]\n"
PK"v74,v52,v53\n"
PK"v75,v54,v55\n"
MF"v[8:11],a[72:75],a[24:27],v[8:11]\n"
"v_bfe_u32 v236,v236,4,7\n"
PK"v76,v56,v57\n"
MF"v[16:19],a[72:75],a[40:43],v[16:19]\n"
PK"v77,v58,v59\n"
"v_mul_u32_u24 v236,20480,v236\n"
MF"v[16:19],a[76:79],a[44:47],v[16:19]\n"
RD"a[96:99],v226 offset:8192\n"
PK"v78,v60,v61\n"
MF"v[24:27],a[76:79],a[60:63],v[24:27]\n"
EX"v0,v0\n"
MF"v[24:27],a[72:75],a[56:59],v[24:27]\n"
PK"v79,v62,v63\n"
"v_lshl_add_u32 v236,v237,4,v236\n"
MF"v[4:7],a[80:83],a[0:3],v[212:215]\n"
WT"lgkmcnt(1)\n"
EX"v1,v1\n"
"v_add_u32 v236,v236,v239\n"
MF"v[4:7],a[84:87],a[4:7],v[4:7]\n"
RD"a[100:103],v226 offset:9216\n"
"v_and_b32 v237,0x70,v224\n"
MF"v[12:15],a[84:87],a[20:23],0\n"
EX"v2,v2\n"
MF"v[12:15],a[80:83],a[16:19],v[12:15]\n"
EX"v3,v3\n"
"v_add_u32 v239,v239,v237\n"
MF"v[20:23],a[80:83],a[32:35],0\n"
EX"v8,v8\n"
"v_add_f32 v240,v240,v244\n"
MF"v[20:23],a[84:87],a[36:39],v[20:23]\n"
RD"a[104:107],v226 offset:10240\n"
"v_add_f32 v248,v248,v252\n"
"v_add_f32 v241,v241,v245\n"
MF"v[28:31],a[84:87],a[52:55],0\n"
EX"v9,v9\n"
"v_add_f32 v249,v249,v253\n"
MF"v[28:31],a[80:83],a[48:51],v[28:31]\n"
"v_add_f32 v242,v242,v246\n"
"v_add_f32 v250,v250,v254\n"
MF"v[4:7],a[88:91],a[8:11],v[4:7]\n"
EX"v10,v10\n"
"v_add_f32 v243,v243,v247\n"
MF"v[4:7],a[92:95],a[12:15],v[4:7]\n"
RD"a[108:111],v226 offset:11264\n"
"v_add_f32 v251,v251,v255\n"
PK"v0,v0,v1\n"
MF"v[12:15],a[92:95],a[28:31],v[12:15]\n"
"buffer_load_dwordx2 v[254:255],v236,s[8:11],s53 offen sc1\n"
"v_ashrrev_i32 v235,31,v234\n"
MF"v[12:15],a[88:91],a[24:27],v[12:15]\n"
EX"v11,v11\n"
"v_not_b32 v235,v235\n"
MF"v[20:23],a[88:91],a[40:43],v[20:23]\n"
EX"v16,v16\n"
"v_or_b32 v235,0x80000000,v235\n"
MF"v[20:23],a[92:95],a[44:47],v[20:23]\n"
EX"v17,v17\n"
PK"v1,v2,v3\n"
MF"v[28:31],a[92:95],a[60:63],v[28:31]\n"
RD"a[112:115],v226 offset:12288\n"
"v_xor_b32 v235,v235,v234\n"
"v_and_b32 v235,0xffff0000,v235\n"
MF"v[28:31],a[88:91],a[56:59],v[28:31]\n"
WT"lgkmcnt(2)\n"
EX"v18,v18\n"
MU"v235,0xbfb8aa3b,v235\n"
MF"v[32:35],a[96:99],a[0:3],v[216:219]\n"
EX"v235,v235\n"
PK"v8,v8,v9\n"
MF"v[32:35],a[100:103],a[4:7],v[32:35]\n"
EX"v19,v19\n"
"v_add_f32 v235,1.0,v235\n"
MF"v[40:43],a[100:103],a[20:23],0\n"
"v_rcp_f32 v235,v235\n"
MF"v[40:43],a[96:99],a[16:19],v[40:43]\n"
RD"a[116:119],v226 offset:13312\n"
PK"v235,v235,v235\n"
PK"v9,v10,v11\n"
MF"v[48:51],a[96:99],a[32:35],0\n"
EX"v24,v24\n"
"v_lshlrev_b32 v235,16,v235\n"
MF"v[48:51],a[100:103],a[36:39],v[48:51]\n"
EX"v25,v25\n"
"v_add_f32 v240,v240,v248\n"
MF"v[56:59],a[100:103],a[52:55],0\n"
EX"v26,v26\n"
PK"v16,v16,v17\n"
MF"v[56:59],a[96:99],a[48:51],v[56:59]\n"
RD"a[120:123],v226 offset:14336\n"
"v_add_f32 v241,v241,v249\n"
"v_add_f32 v242,v242,v250\n"
MF"v[32:35],a[104:107],a[8:11],v[32:35]\n"
WT"lgkmcnt(2)\n"
EX"v27,v27\n"
"v_add_f32 v243,v243,v251\n"
MF"v[32:35],a[108:111],a[12:15],v[32:35]\n"
EX"v4,v4\n"
PK"v240,v240,v240\n"
MF"v[40:43],a[108:111],a[28:31],v[40:43]\n"
RD"a[124:127],v226 offset:15360\n"
PK"v17,v18,v19\n"
"v_lshlrev_b32 v240,16,v240\n"
MF"v[40:43],a[104:107],a[24:27],v[40:43]\n"
EX"v5,v5\n"
MU"v240,v240,v235\n"
MF"v[48:51],a[104:107],a[40:43],v[48:51]\n"
EX"v6,v6\n"
PK"v240,v240,v240\n"
MF"v[48:51],a[108:111],a[44:47],v[48:51]\n"
EX"v7,v7\n"
PK"v24,v24,v25\n"
MF"v[56:59],a[108:111],a[60:63],v[56:59]\n"
EX"v12,v12\n"
"v_lshlrev_b32 v240,16,v240\n"
MF"v[56:59],a[104:107],a[56:59],v[56:59]\n"
PK"v241,v241,v241\n"
"v_lshlrev_b32 v241,16,v241\n"
PK"v25,v26,v27\n"
MF"v[36:39],a[112:115],a[0:3],v[220:223]\n"
WT"lgkmcnt(1)\n"
EX"v13,v13\n"
MU"v241,v241,v235\n"
MF"v[36:39],a[116:119],a[4:7],v[36:39]\n"
RD"a[64:67],v227 offset:16384\n"
PK"v241,v241,v241\n"
MF"v[44:47],a[116:119],a[20:23],0\n"
EX"v14,v14\n"
"v_lshlrev_b32 v241,16,v241\n"
MF"v[44:47],a[112:115],a[16:19],v[44:47]\n"
PK"v242,v242,v242\n"
PK"v2,v4,v5\n"
MF"v[52:55],a[112:115],a[32:35],0\n"
EX"v15,v15\n"
"v_lshlrev_b32 v242,16,v242\n"
MF"v[52:55],a[116:119],a[36:39],v[52:55]\n"
RD"a[68:71],v227 offset:17408\n"
MU"v242,v242,v235\n"
PK"v3,v6,v7\n"
MF"v[60:63],a[116:119],a[52:55],0\n"
EX"v20,v20\n"
PK"v242,v242,v242\n"
MF"v[60:63],a[112:115],a[48:51],v[60:63]\n"
"v_readlane_b32 s43,v228,s42\n"
"v_lshlrev_b32 v242,16,v242\n"
MF"v[36:39],a[120:123],a[8:11],v[36:39]\n"
WT"lgkmcnt(1)\n"
EX"v21,v21\n"
PK"v243,v243,v243\n"
MF"v[36:39],a[124:127],a[12:15],v[36:39]\n"
RD"a[72:75],v227 offset:18432\n"
"v_lshlrev_b32 v243,16,v243\n"
PK"v10,v12,v13\n"
MF"v[44:47],a[124:127],a[28:31],v[44:47]\n"
EX"v22,v22\n"
MU"v243,v243,v235\n"
MF"v[44:47],a[120:123],a[24:27],v[44:47]\n"
"s_mov_b32 m0,s40\n"
PK"v243,v243,v243\n"
"v_lshlrev_b32 v243,16,v243\n"
MF"v[52:55],a[120:123],a[40:43],v[52:55]\n"
EX"v23,v23\n"
MF"v[52:55],a[124:127],a[44:47],v[52:55]\n"
RD"a[76:79],v227 offset:19456\n"
"v_readlane_b32 s44,v229,s42\n"
PK"v11,v14,v15\n"
MF"v[60:63],a[124:127],a[60:63],v[60:63]\n"
EX"v28,v28\n"
MF"v[60:63],a[120:123],a[56:59],v[60:63]\n"
SA"s42,s42,1\n"
BL"v225,s[8:11],s43 offen offset:0 sc1 lds\n"
MF"v[80:83],a[64:67],v[64:67],v[80:83]\n"
SA"s41,s41,32768\n"
EX"v29,v29\n"
MF"v[112:115],a[64:67],v[68:71],v[112:115]\n"
"s_cmp_eq_u32 s41,163840\n"
RD"a[80:83],v227 offset:20480\n"
PK"v18,v20,v21\n"
MF"v[144:147],a[64:67],v[72:75],v[144:147]\n"
"s_cselect_b32 s41,0,s41\n"
EX"v30,v30\n"
MF"v[176:179],a[64:67],v[76:79],v[176:179]\n"
WT"lgkmcnt(2)\n"
"v_add_u32 v226,s41,v224\n"
PK"v19,v22,v23\n"
MF"v[84:87],a[68:71],v[64:67],v[84:87]\n"
EX"v31,v31\n"
MF"v[116:119],a[68:71],v[68:71],v[116:119]\n"
RD"a[84:87],v227 offset:21504\n"
PK"v26,v28,v29\n"
MF"v[148:151],a[68:71],v[72:75],v[148:151]\n"
EX"v32,v32\n"
MF"v[180:183],a[68:71],v[76:79],v[180:183]\n"
BL"v225,s[8:11],s43 offen offset:1024 sc1 lds\n"
MF"v[88:91],a[72:75],v[64:67],v[88:91]\n"
EX"v33,v33\n"
MF"v[120:123],a[72:75],v[68:71],v[120:123]\n"
RD"a[88:91],v227 offset:22528\n"
PK"v27,v30,v31\n"
MF"v[152:155],a[72:75],v[72:75],v[152:155]\n"
EX"v34,v34\n"
MF"v[184:187],a[72:75],v[76:79],v[184:187]\n"
WT"lgkmcnt(2)\n"
WT"vmcnt(2)\n"
"v_lshlrev_b32 v244,16,v254\n"
"v_and_b32 v245,0xffff0000,v254\n"
MF"v[92:95],a[76:79],v[64:67],v[92:95]\n"
EX"v35,v35\n"
"v_lshlrev_b32 v246,16,v255\n"
MF"v[124:127],a[76:79],v[68:71],v[124:127]\n"
RD"a[92:95],v227 offset:23552\n"
"v_and_b32 v247,0xffff0000,v255\n"
"v_add_f32 v240,v240,v244\n"
MF"v[156:159],a[76:79],v[72:75],v[156:159]\n"
EX"v40,v40\n"
"v_add_f32 v241,v241,v245\n"
MF"v[188:191],a[76:79],v[76:79],v[188:191]\n"
BL"v225,s[8:11],s43 offen offset:2048 sc1 lds\n"
"v_add_f32 v242,v242,v246\n"
MF"v[96:99],a[80:83],v[64:67],v[96:99]\n"
EX"v41,v41\n"
"v_add_f32 v243,v243,v247\n"
MF"v[128:131],a[80:83],v[68:71],v[128:131]\n"
RD"a[96:99],v227 offset:24576\n"
PK"v240,v240,v241\n"
PK"v241,v242,v243\n"
MF"v[160:163],a[80:83],v[72:75],v[160:163]\n"
"buffer_store_dwordx2 v[240:241],v239,s[8:11],s93 offen sc1\n"
MF"v[192:195],a[80:83],v[76:79],v[192:195]\n"
WT"lgkmcnt(2)\n"
EX"v42,v42\n"
MF"v[100:103],a[84:87],v[64:67],v[100:103]\n"
EX"v43,v43\n"
MF"v[132:135],a[84:87],v[68:71],v[132:135]\n"
RD"a[100:103],v227 offset:25600\n"
MF"v[164:167],a[84:87],v[72:75],v[164:167]\n"
EX"v48,v48\n"
MF"v[196:199],a[84:87],v[76:79],v[196:199]\n"
BL"v225,s[8:11],s43 offen offset:3072 sc1 lds\n"
MF"v[104:107],a[88:91],v[64:67],v[104:107]\n"
EX"v49,v49\n"
MF"v[136:139],a[88:91],v[68:71],v[136:139]\n"
RD"a[104:107],v227 offset:26624\n"
MF"v[168:171],a[88:91],v[72:75],v[168:171]\n"
EX"v50,v50\n"
MF"v[200:203],a[88:91],v[76:79],v[200:203]\n"
WT"lgkmcnt(2)\n"
SA"m0,s40,16384\n"
MF"v[108:111],a[92:95],v[64:67],v[108:111]\n"
EX"v51,v51\n"
MF"v[140:143],a[92:95],v[68:71],v[140:143]\n"
RD"a[108:111],v227 offset:27648\n"
MF"v[172:175],a[92:95],v[72:75],v[172:175]\n"
EX"v56,v56\n"
MF"v[204:207],a[92:95],v[76:79],v[204:207]\n"
BL"v225,s[8:11],s44 offen offset:0 sc1 lds\n"
MF"a[132:135],a[128:131],v[64:67],a[132:135]\n"
SA"s40,s40,32768\n"
EX"v57,v57\n"
MF"a[136:139],a[128:131],v[68:71],a[136:139]\n"
"s_cmp_eq_u32 s40,s46\n"
RD"a[112:115],v227 offset:28672\n"
"s_cselect_b32 s40,s47,s40\n"
MF"a[140:143],a[128:131],v[72:75],a[140:143]\n"
EX"v58,v58\n"
MF"a[144:147],a[128:131],v[76:79],a[144:147]\n"
MF"v[80:83],a[96:99],v[0:3],v[80:83]\n"
EX"v59,v59\n"
MF"v[112:115],a[96:99],v[8:11],v[112:115]\n"
RD"a[116:119],v227 offset:29696\n"
MF"v[144:147],a[96:99],v[16:19],v[144:147]\n"
EX"v36,v36\n"
MF"v[176:179],a[96:99],v[24:27],v[176:179]\n"
WT"lgkmcnt(2)\n"
BL"v225,s[8:11],s44 offen offset:1024 sc1 lds\n"
MF"v[84:87],a[100:103],v[0:3],v[84:87]\n"
EX"v37,v37\n"
MF"v[116:119],a[100:103],v[8:11],v[116:119]\n"
RD"a[120:123],v227 offset:30720\n"
MF"v[148:151],a[100:103],v[16:19],v[148:151]\n"
EX"v38,v38\n"
MF"v[180:183],a[100:103],v[24:27],v[180:183]\n"
MF"v[88:91],a[104:107],v[0:3],v[88:91]\n"
EX"v39,v39\n"
MF"v[120:123],a[104:107],v[8:11],v[120:123]\n"
RD"a[124:127],v227 offset:31744\n"
MF"v[152:155],a[104:107],v[16:19],v[152:155]\n"
EX"v44,v44\n"
MF"v[184:187],a[104:107],v[24:27],v[184:187]\n"
BL"v225,s[8:11],s44 offen offset:2048 sc1 lds\n"
MF"v[92:95],a[108:111],v[0:3],v[92:95]\n"
EX"v45,v45\n"
MF"v[124:127],a[108:111],v[8:11],v[124:127]\n"
MF"v[156:159],a[108:111],v[16:19],v[156:159]\n"
EX"v46,v46\n"
MF"v[188:191],a[108:111],v[24:27],v[188:191]\n"
WT"lgkmcnt(1)\n"
MF"v[96:99],a[112:115],v[0:3],v[96:99]\n"
WT"lgkmcnt(0)\n"
"s_barrier\n"
MF"v[128:131],a[112:115],v[8:11],v[128:131]\n"
RD"a[64:67],v226 offset:0\n"
MF"v[160:163],a[112:115],v[16:19],v[160:163]\n"
EX"v47,v47\n"
MF"v[192:195],a[112:115],v[24:27],v[192:195]\n"
BL"v225,s[8:11],s44 offen offset:3072 sc1 lds\n"
MF"v[100:103],a[116:119],v[0:3],v[100:103]\n"
EX"v52,v52\n"
MF"v[132:135],a[116:119],v[8:11],v[132:135]\n"
RD"a[68:71],v226 offset:1024\n"
MF"v[164:167],a[116:119],v[16:19],v[164:167]\n"
EX"v53,v53\n"
MF"v[196:199],a[116:119],v[24:27],v[196:199]\n"
RD"a[72:75],v226 offset:2048\n"
MF"v[104:107],a[120:123],v[0:3],v[104:107]\n"
EX"v54,v54\n"
MF"v[136:139],a[120:123],v[8:11],v[136:139]\n"
RD"a[76:79],v226 offset:3072\n"
MF"v[168:171],a[120:123],v[16:19],v[168:171]\n"
EX"v55,v55\n"
MF"v[200:203],a[120:123],v[24:27],v[200:203]\n"
MF"v[108:111],a[124:127],v[0:3],v[108:111]\n"
WT"vmcnt(5)\n"
"buffer_store_dwordx2 v[232:233],v230,s[8:11],s94 offen sc1\n"
MF"v[140:143],a[124:127],v[8:11],v[140:143]\n"
EX"v60,v60\n"
MF"v[172:175],a[124:127],v[16:19],v[172:175]\n"
RD"a[80:83],v226 offset:4096\n"
MF"v[204:207],a[124:127],v[24:27],v[204:207]\n"
EX"v61,v61\n"
MF"a[132:135],a[128:131],v[0:3],a[132:135]\n"
RD"a[84:87],v226 offset:5120\n"
MF"a[136:139],a[128:131],v[8:11],a[136:139]\n"
EX"v62,v62\n"
MF"a[140:143],a[128:131],v[16:19],a[140:143]\n"
RD"a[88:91],v226 offset:6144\n"
MF"a[144:147],a[128:131],v[24:27],a[144:147]\n"
EX"v63,v63\n"
"s_branch LDB_%=\n"
"LME_%=:\n"
WT"lgkmcnt(10)\n"
MF"v[0:3],a[64:67],a[0:3],0\n"
WT"lgkmcnt(2)\n"
"v_lshrrev_b32 v239,9,v224\n"
"v_lshlrev_b32 v239,8,v239\n"
MF"v[0:3],a[68:71],a[4:7],v[0:3]\n"
PK"v64,v32,v33\n"
PK"v65,v34,v35\n"
MF"v[8:11],a[68:71],a[20:23],0\n"
"v_bfe_u32 v237,v224,8,1\n"
PK"v66,v36,v37\n"
MF"v[8:11],a[64:67],a[16:19],v[8:11]\n"
PK"v67,v38,v39\n"
"v_lshl_add_u32 v239,v237,3,v239\n"
MF"v[16:19],a[64:67],a[32:35],0\n"
PK"v68,v40,v41\n"
PK"v69,v42,v43\n"
MF"v[16:19],a[68:71],a[36:39],v[16:19]\n"
RD"a[92:95],v226 offset:7168\n"
"v_and_b32 v237,0x80,v224\n"
MF"v[24:27],a[68:71],a[52:55],0\n"
PK"v70,v44,v45\n"
PK"v71,v46,v47\n"
MF"v[24:27],a[64:67],a[48:51],v[24:27]\n"
"v_lshl_add_u32 v239,v237,23,v239\n"
"v_not_b32 v236,v234\n"
MF"v[0:3],a[72:75],a[8:11],v[0:3]\n"
PK"v72,v48,v49\n"
PK"v73,v50,v51\n"
MF"v[0:3],a[76:79],a[12:15],v[0:3]\n"
SA"s53,s93,452608\n"
"v_mov_b32 v227,v226\n"
"v_and_b32 v237,15,v236\n"
MF"v[8:11],a[76:79],a[28:31],v[8:11]\n"
PK"v74,v52,v53\n"
PK"v75,v54,v55\n"
MF"v[8:11],a[72:75],a[24:27],v[8:11]\n"
"v_bfe_u32 v236,v236,4,7\n"
PK"v76,v56,v57\n"
MF"v[16:19],a[72:75],a[40:43],v[16:19]\n"
PK"v77,v58,v59\n"
"v_mul_u32_u24 v236,20480,v236\n"
MF"v[16:19],a[76:79],a[44:47],v[16:19]\n"
RD"a[96:99],v226 offset:8192\n"
PK"v78,v60,v61\n"
MF"v[24:27],a[76:79],a[60:63],v[24:27]\n"
EX"v0,v0\n"
MF"v[24:27],a[72:75],a[56:59],v[24:27]\n"
PK"v79,v62,v63\n"
"v_lshl_add_u32 v236,v237,4,v236\n"
MF"v[4:7],a[80:83],a[0:3],0\n"
WT"lgkmcnt(1)\n"
EX"v1,v1\n"
"v_add_u32 v236,v236,v239\n"
MF"v[4:7],a[84:87],a[4:7],v[4:7]\n"
RD"a[100:103],v226 offset:9216\n"
"v_and_b32 v237,0x70,v224\n"
MF"v[12:15],a[84:87],a[20:23],0\n"
EX"v2,v2\n"
MF"v[12:15],a[80:83],a[16:19],v[12:15]\n"
EX"v3,v3\n"
"v_add_u32 v239,v239,v237\n"
MF"v[20:23],a[80:83],a[32:35],0\n"
EX"v8,v8\n"
"v_add_f32 v240,v240,v244\n"
MF"v[20:23],a[84:87],a[36:39],v[20:23]\n"
RD"a[104:107],v226 offset:10240\n"
"v_add_f32 v248,v248,v252\n"
"v_add_f32 v241,v241,v245\n"
MF"v[28:31],a[84:87],a[52:55],0\n"
EX"v9,v9\n"
"v_add_f32 v249,v249,v253\n"
MF"v[28:31],a[80:83],a[48:51],v[28:31]\n"
"v_add_f32 v242,v242,v246\n"
"v_add_f32 v250,v250,v254\n"
MF"v[4:7],a[88:91],a[8:11],v[4:7]\n"
EX"v10,v10\n"
"v_add_f32 v243,v243,v247\n"
MF"v[4:7],a[92:95],a[12:15],v[4:7]\n"
RD"a[108:111],v226 offset:11264\n"
"v_add_f32 v251,v251,v255\n"
PK"v0,v0,v1\n"
MF"v[12:15],a[92:95],a[28:31],v[12:15]\n"
"buffer_load_dwordx2 v[254:255],v236,s[8:11],s53 offen sc1\n"
"v_ashrrev_i32 v235,31,v234\n"
MF"v[12:15],a[88:91],a[24:27],v[12:15]\n"
EX"v11,v11\n"
"v_not_b32 v235,v235\n"
MF"v[20:23],a[88:91],a[40:43],v[20:23]\n"
EX"v16,v16\n"
"v_or_b32 v235,0x80000000,v235\n"
MF"v[20:23],a[92:95],a[44:47],v[20:23]\n"
EX"v17,v17\n"
PK"v1,v2,v3\n"
MF"v[28:31],a[92:95],a[60:63],v[28:31]\n"
RD"a[112:115],v226 offset:12288\n"
"v_xor_b32 v235,v235,v234\n"
"v_and_b32 v235,0xffff0000,v235\n"
MF"v[28:31],a[88:91],a[56:59],v[28:31]\n"
WT"lgkmcnt(2)\n"
EX"v18,v18\n"
MU"v235,0xbfb8aa3b,v235\n"
MF"v[32:35],a[96:99],a[0:3],0\n"
EX"v235,v235\n"
PK"v8,v8,v9\n"
MF"v[32:35],a[100:103],a[4:7],v[32:35]\n"
EX"v19,v19\n"
"v_add_f32 v235,1.0,v235\n"
MF"v[40:43],a[100:103],a[20:23],0\n"
"v_rcp_f32 v235,v235\n"
MF"v[40:43],a[96:99],a[16:19],v[40:43]\n"
RD"a[116:119],v226 offset:13312\n"
PK"v235,v235,v235\n"
PK"v9,v10,v11\n"
MF"v[48:51],a[96:99],a[32:35],0\n"
EX"v24,v24\n"
"v_lshlrev_b32 v235,16,v235\n"
MF"v[48:51],a[100:103],a[36:39],v[48:51]\n"
EX"v25,v25\n"
"v_add_f32 v240,v240,v248\n"
MF"v[56:59],a[100:103],a[52:55],0\n"
EX"v26,v26\n"
PK"v16,v16,v17\n"
MF"v[56:59],a[96:99],a[48:51],v[56:59]\n"
RD"a[120:123],v226 offset:14336\n"
"v_add_f32 v241,v241,v249\n"
"v_add_f32 v242,v242,v250\n"
MF"v[32:35],a[104:107],a[8:11],v[32:35]\n"
WT"lgkmcnt(2)\n"
EX"v27,v27\n"
"v_add_f32 v243,v243,v251\n"
MF"v[32:35],a[108:111],a[12:15],v[32:35]\n"
EX"v4,v4\n"
PK"v240,v240,v240\n"
MF"v[40:43],a[108:111],a[28:31],v[40:43]\n"
RD"a[124:127],v226 offset:15360\n"
PK"v17,v18,v19\n"
"v_lshlrev_b32 v240,16,v240\n"
MF"v[40:43],a[104:107],a[24:27],v[40:43]\n"
EX"v5,v5\n"
MU"v240,v240,v235\n"
MF"v[48:51],a[104:107],a[40:43],v[48:51]\n"
EX"v6,v6\n"
PK"v240,v240,v240\n"
MF"v[48:51],a[108:111],a[44:47],v[48:51]\n"
EX"v7,v7\n"
PK"v24,v24,v25\n"
MF"v[56:59],a[108:111],a[60:63],v[56:59]\n"
EX"v12,v12\n"
"v_lshlrev_b32 v240,16,v240\n"
MF"v[56:59],a[104:107],a[56:59],v[56:59]\n"
PK"v241,v241,v241\n"
"v_lshlrev_b32 v241,16,v241\n"
PK"v25,v26,v27\n"
MF"v[36:39],a[112:115],a[0:3],0\n"
WT"lgkmcnt(1)\n"
EX"v13,v13\n"
MU"v241,v241,v235\n"
MF"v[36:39],a[116:119],a[4:7],v[36:39]\n"
RD"a[64:67],v227 offset:16384\n"
PK"v241,v241,v241\n"
MF"v[44:47],a[116:119],a[20:23],0\n"
EX"v14,v14\n"
"v_lshlrev_b32 v241,16,v241\n"
MF"v[44:47],a[112:115],a[16:19],v[44:47]\n"
PK"v242,v242,v242\n"
PK"v2,v4,v5\n"
MF"v[52:55],a[112:115],a[32:35],0\n"
EX"v15,v15\n"
"v_lshlrev_b32 v242,16,v242\n"
MF"v[52:55],a[116:119],a[36:39],v[52:55]\n"
RD"a[68:71],v227 offset:17408\n"
MU"v242,v242,v235\n"
PK"v3,v6,v7\n"
MF"v[60:63],a[116:119],a[52:55],0\n"
EX"v20,v20\n"
PK"v242,v242,v242\n"
MF"v[60:63],a[112:115],a[48:51],v[60:63]\n"
"v_readlane_b32 s43,v228,s42\n"
"v_lshlrev_b32 v242,16,v242\n"
MF"v[36:39],a[120:123],a[8:11],v[36:39]\n"
WT"lgkmcnt(1)\n"
EX"v21,v21\n"
PK"v243,v243,v243\n"
MF"v[36:39],a[124:127],a[12:15],v[36:39]\n"
RD"a[72:75],v227 offset:18432\n"
"v_lshlrev_b32 v243,16,v243\n"
PK"v10,v12,v13\n"
MF"v[44:47],a[124:127],a[28:31],v[44:47]\n"
EX"v22,v22\n"
MU"v243,v243,v235\n"
MF"v[44:47],a[120:123],a[24:27],v[44:47]\n"
"s_mov_b32 m0,s40\n"
PK"v243,v243,v243\n"
"v_lshlrev_b32 v243,16,v243\n"
MF"v[52:55],a[120:123],a[40:43],v[52:55]\n"
EX"v23,v23\n"
MF"v[52:55],a[124:127],a[44:47],v[52:55]\n"
RD"a[76:79],v227 offset:19456\n"
"v_readlane_b32 s44,v229,s42\n"
PK"v11,v14,v15\n"
MF"v[60:63],a[124:127],a[60:63],v[60:63]\n"
EX"v28,v28\n"
MF"v[60:63],a[120:123],a[56:59],v[60:63]\n"
SA"s42,s42,1\n"
BL"v225,s[8:11],s43 offen offset:0 sc1 lds\n"
MF"v[80:83],a[64:67],v[64:67],v[80:83]\n"
SA"s41,s41,32768\n"
EX"v29,v29\n"
MF"v[112:115],a[64:67],v[68:71],v[112:115]\n"
"s_cmp_eq_u32 s41,163840\n"
RD"a[80:83],v227 offset:20480\n"
PK"v18,v20,v21\n"
MF"v[144:147],a[64:67],v[72:75],v[144:147]\n"
"s_cselect_b32 s41,0,s41\n"
EX"v30,v30\n"
MF"v[176:179],a[64:67],v[76:79],v[176:179]\n"
WT"lgkmcnt(2)\n"
"v_add_u32 v226,s41,v224\n"
PK"v19,v22,v23\n"
MF"v[84:87],a[68:71],v[64:67],v[84:87]\n"
EX"v31,v31\n"
MF"v[116:119],a[68:71],v[68:71],v[116:119]\n"
RD"a[84:87],v227 offset:21504\n"
PK"v26,v28,v29\n"
MF"v[148:151],a[68:71],v[72:75],v[148:151]\n"
EX"v32,v32\n"
MF"v[180:183],a[68:71],v[76:79],v[180:183]\n"
BL"v225,s[8:11],s43 offen offset:1024 sc1 lds\n"
MF"v[88:91],a[72:75],v[64:67],v[88:91]\n"
EX"v33,v33\n"
MF"v[120:123],a[72:75],v[68:71],v[120:123]\n"
RD"a[88:91],v227 offset:22528\n"
PK"v27,v30,v31\n"
MF"v[152:155],a[72:75],v[72:75],v[152:155]\n"
EX"v34,v34\n"
MF"v[184:187],a[72:75],v[76:79],v[184:187]\n"
WT"lgkmcnt(2)\n"
WT"vmcnt(2)\n"
"v_lshlrev_b32 v244,16,v254\n"
"v_and_b32 v245,0xffff0000,v254\n"
MF"v[92:95],a[76:79],v[64:67],v[92:95]\n"
EX"v35,v35\n"
"v_lshlrev_b32 v246,16,v255\n"
MF"v[124:127],a[76:79],v[68:71],v[124:127]\n"
RD"a[92:95],v227 offset:23552\n"
"v_and_b32 v247,0xffff0000,v255\n"
"v_add_f32 v240,v240,v244\n"
MF"v[156:159],a[76:79],v[72:75],v[156:159]\n"
EX"v40,v40\n"
"v_add_f32 v241,v241,v245\n"
MF"v[188:191],a[76:79],v[76:79],v[188:191]\n"
BL"v225,s[8:11],s43 offen offset:2048 sc1 lds\n"
"v_add_f32 v242,v242,v246\n"
MF"v[96:99],a[80:83],v[64:67],v[96:99]\n"
EX"v41,v41\n"
"v_add_f32 v243,v243,v247\n"
MF"v[128:131],a[80:83],v[68:71],v[128:131]\n"
RD"a[96:99],v227 offset:24576\n"
PK"v240,v240,v241\n"
PK"v241,v242,v243\n"
MF"v[160:163],a[80:83],v[72:75],v[160:163]\n"
"buffer_store_dwordx2 v[240:241],v239,s[8:11],s93 offen sc1\n"
MF"v[192:195],a[80:83],v[76:79],v[192:195]\n"
WT"lgkmcnt(2)\n"
EX"v42,v42\n"
MF"v[100:103],a[84:87],v[64:67],v[100:103]\n"
EX"v43,v43\n"
MF"v[132:135],a[84:87],v[68:71],v[132:135]\n"
RD"a[100:103],v227 offset:25600\n"
MF"v[164:167],a[84:87],v[72:75],v[164:167]\n"
EX"v48,v48\n"
MF"v[196:199],a[84:87],v[76:79],v[196:199]\n"
BL"v225,s[8:11],s43 offen offset:3072 sc1 lds\n"
MF"v[104:107],a[88:91],v[64:67],v[104:107]\n"
EX"v49,v49\n"
MF"v[136:139],a[88:91],v[68:71],v[136:139]\n"
RD"a[104:107],v227 offset:26624\n"
MF"v[168:171],a[88:91],v[72:75],v[168:171]\n"
EX"v50,v50\n"
MF"v[200:203],a[88:91],v[76:79],v[200:203]\n"
WT"lgkmcnt(2)\n"
SA"m0,s40,16384\n"
MF"v[108:111],a[92:95],v[64:67],v[108:111]\n"
EX"v51,v51\n"
MF"v[140:143],a[92:95],v[68:71],v[140:143]\n"
RD"a[108:111],v227 offset:27648\n"
MF"v[172:175],a[92:95],v[72:75],v[172:175]\n"
EX"v56,v56\n"
MF"v[204:207],a[92:95],v[76:79],v[204:207]\n"
BL"v225,s[8:11],s44 offen offset:0 sc1 lds\n"
MF"a[132:135],a[128:131],v[64:67],a[132:135]\n"
SA"s40,s40,32768\n"
EX"v57,v57\n"
MF"a[136:139],a[128:131],v[68:71],a[136:139]\n"
"s_cmp_eq_u32 s40,s46\n"
RD"a[112:115],v227 offset:28672\n"
"s_cselect_b32 s40,s47,s40\n"
MF"a[140:143],a[128:131],v[72:75],a[140:143]\n"
EX"v58,v58\n"
MF"a[144:147],a[128:131],v[76:79],a[144:147]\n"
MF"v[80:83],a[96:99],v[0:3],v[80:83]\n"
EX"v59,v59\n"
MF"v[112:115],a[96:99],v[8:11],v[112:115]\n"
RD"a[116:119],v227 offset:29696\n"
MF"v[144:147],a[96:99],v[16:19],v[144:147]\n"
EX"v36,v36\n"
MF"v[176:179],a[96:99],v[24:27],v[176:179]\n"
WT"lgkmcnt(2)\n"
BL"v225,s[8:11],s44 offen offset:1024 sc1 lds\n"
MF"v[84:87],a[100:103],v[0:3],v[84:87]\n"
EX"v37,v37\n"
MF"v[116:119],a[100:103],v[8:11],v[116:119]\n"
RD"a[120:123],v227 offset:30720\n"
MF"v[148:151],a[100:103],v[16:19],v[148:151]\n"
EX"v38,v38\n"
MF"v[180:183],a[100:103],v[24:27],v[180:183]\n"
MF"v[88:91],a[104:107],v[0:3],v[88:91]\n"
EX"v39,v39\n"
MF"v[120:123],a[104:107],v[8:11],v[120:123]\n"
RD"a[124:127],v227 offset:31744\n"
MF"v[152:155],a[104:107],v[16:19],v[152:155]\n"
EX"v44,v44\n"
MF"v[184:187],a[104:107],v[24:27],v[184:187]\n"
BL"v225,s[8:11],s44 offen offset:2048 sc1 lds\n"
MF"v[92:95],a[108:111],v[0:3],v[92:95]\n"
EX"v45,v45\n"
MF"v[124:127],a[108:111],v[8:11],v[124:127]\n"
MF"v[156:159],a[108:111],v[16:19],v[156:159]\n"
EX"v46,v46\n"
MF"v[188:191],a[108:111],v[24:27],v[188:191]\n"
WT"lgkmcnt(1)\n"
MF"v[96:99],a[112:115],v[0:3],v[96:99]\n"
WT"lgkmcnt(0)\n"
"s_barrier\n"
MF"v[128:131],a[112:115],v[8:11],v[128:131]\n"
RD"a[64:67],v226 offset:0\n"
MF"v[160:163],a[112:115],v[16:19],v[160:163]\n"
EX"v47,v47\n"
MF"v[192:195],a[112:115],v[24:27],v[192:195]\n"
BL"v225,s[8:11],s44 offen offset:3072 sc1 lds\n"
MF"v[100:103],a[116:119],v[0:3],v[100:103]\n"
EX"v52,v52\n"
MF"v[132:135],a[116:119],v[8:11],v[132:135]\n"
RD"a[68:71],v226 offset:1024\n"
MF"v[164:167],a[116:119],v[16:19],v[164:167]\n"
EX"v53,v53\n"
MF"v[196:199],a[116:119],v[24:27],v[196:199]\n"
RD"a[72:75],v226 offset:2048\n"
MF"v[104:107],a[120:123],v[0:3],v[104:107]\n"
EX"v54,v54\n"
MF"v[136:139],a[120:123],v[8:11],v[136:139]\n"
RD"a[76:79],v226 offset:3072\n"
MF"v[168:171],a[120:123],v[16:19],v[168:171]\n"
EX"v55,v55\n"
MF"v[200:203],a[120:123],v[24:27],v[200:203]\n"
MF"v[108:111],a[124:127],v[0:3],v[108:111]\n"
WT"vmcnt(5)\n"
"buffer_store_dwordx2 v[232:233],v230,s[8:11],s94 offen sc1\n"
MF"v[140:143],a[124:127],v[8:11],v[140:143]\n"
EX"v60,v60\n"
MF"v[172:175],a[124:127],v[16:19],v[172:175]\n"
RD"a[80:83],v226 offset:4096\n"
MF"v[204:207],a[124:127],v[24:27],v[204:207]\n"
EX"v61,v61\n"
MF"a[132:135],a[128:131],v[0:3],a[132:135]\n"
RD"a[84:87],v226 offset:5120\n"
MF"a[136:139],a[128:131],v[8:11],a[136:139]\n"
EX"v62,v62\n"
MF"a[140:143],a[128:131],v[16:19],a[140:143]\n"
RD"a[88:91],v226 offset:6144\n"
MF"a[144:147],a[128:131],v[24:27],a[144:147]\n"
EX"v63,v63\n"
"s_mov_b32 s57,0\n"
"s_lshl_b32 s56,s34,1\n"
"s_sub_u32 s56,s56,12\n"
"s_branch LP1_%=\n"
"LRT_%=:\n"
WT"lgkmcnt(6)\n"
MF"v[0:3],a[64:67],a[0:3],0\n"
WT"lgkmcnt(5)\n"
"v_lshrrev_b32 v242,1,v224\n"
PK"v64,v32,v33\n"
MF"v[0:3],a[68:71],a[4:7],v[0:3]\n"
"s_mov_b32 s12,s74\n"
PK"v65,v34,v35\n"
"v_and_b32 v242,56,v242\n"
MF"v[8:11],a[68:71],a[20:23],0\n"
"s_and_b32 s13,s75,0xffff\n"
PK"v66,v36,v37\n"
PK"v67,v38,v39\n"
MF"v[8:11],a[64:67],a[16:19],v[8:11]\n"
"s_mov_b32 s14,5242880\n"
PK"v68,v40,v41\n"
PK"v69,v42,v43\n"
MF"v[16:19],a[64:67],a[32:35],0\n"
"s_lshl_b32 s23,s35,6\n"
PK"v70,v44,v45\n"
PK"v71,v46,v47\n"
MF"v[16:19],a[68:71],a[36:39],v[16:19]\n"
"s_mov_b32 s15,0x20000\n"
RD"a[92:95],v226 offset:7168\n"
"v_add_u32 v243,s40,v252\n"
MF"v[24:27],a[68:71],a[52:55],0\n"
SA"s23,s23,4352\n"
PK"v72,v48,v49\n"
PK"v73,v50,v51\n"
MF"v[24:27],a[64:67],a[48:51],v[24:27]\n"
WT"lgkmcnt(1)\n"
"buffer_load_dwordx2 v[244:245],v242,s[8:11],s23 offen sc1\n"
MF"v[0:3],a[72:75],a[8:11],v[0:3]\n"
SA"s19,s40,32768\n"
"v_mov_b32 v227,v226\n"
PK"v74,v52,v53\n"
MF"v[0:3],a[76:79],a[12:15],v[0:3]\n"
"s_cmp_eq_u32 s19,s46\n"
PK"v75,v54,v55\n"
PK"v76,v56,v57\n"
MF"v[8:11],a[76:79],a[28:31],v[8:11]\n"
"s_cselect_b32 s19,s47,s19\n"
PK"v77,v58,v59\n"
PK"v78,v60,v61\n"
MF"v[8:11],a[72:75],a[24:27],v[8:11]\n"
"s_cmp_eq_u32 s20,0\n"
PK"v79,v62,v63\n"
"s_cselect_b32 s17,2130706432,s17\n"
MF"v[16:19],a[72:75],a[40:43],v[16:19]\n"
SA"m0,s19,0\n"
MF"v[16:19],a[76:79],a[44:47],v[16:19]\n"
BL"v240,s[12:15],s17 offen offset:0 lds\n"
MF"v[24:27],a[76:79],a[60:63],v[24:27]\n"
RD"a[96:99],v226 offset:8192\n"
MF"v[24:27],a[72:75],a[56:59],v[24:27]\n"
EX"v0,v0\n"
MF"v[4:7],a[80:83],a[0:3],0\n"
BL"v241,s[12:15],s17 offen offset:1024 lds\n"
MF"v[4:7],a[84:87],a[4:7],v[4:7]\n"
EX"v1,v1\n"
MF"v[12:15],a[84:87],a[20:23],0\n"
RD"a[100:103],v226 offset:9216\n"
PK"v0,v0,v1\n"
MF"v[12:15],a[80:83],a[16:19],v[12:15]\n"
EX"v2,v2\n"
MF"v[20:23],a[80:83],a[32:35],0\n"
BL"v251,s[12:15],s17 offen offset:2048 lds\n"
MF"v[20:23],a[84:87],a[36:39],v[20:23]\n"
SA"m0,s19,16384\n"
EX"v3,v3\n"
MF"v[28:31],a[84:87],a[52:55],0\n"
SA"s17,s17,20480\n"
EX"v8,v8\n"
MF"v[28:31],a[80:83],a[48:51],v[28:31]\n"
RD"a[104:107],v226 offset:10240\n"
PK"v1,v2,v3\n"
MF"v[4:7],a[88:91],a[8:11],v[4:7]\n"
WT"lgkmcnt(2)\n"
EX"v9,v9\n"
MF"v[4:7],a[92:95],a[12:15],v[4:7]\n"
BL"v240,s[12:15],s17 offen offset:0 lds\n"
MF"v[12:15],a[92:95],a[28:31],v[12:15]\n"
EX"v10,v10\n"
MF"v[12:15],a[88:91],a[24:27],v[12:15]\n"
RD"a[108:111],v226 offset:11264\n"
PK"v8,v8,v9\n"
MF"v[20:23],a[88:91],a[40:43],v[20:23]\n"
EX"v11,v11\n"
MF"v[20:23],a[92:95],a[44:47],v[20:23]\n"
WT"vmcnt(13)\n"
RT"a[148:149],v243 offset:0\n"
RT"a[150:151],v243 offset:16384\n"
EX"v16,v16\n"
MF"v[28:31],a[92:95],a[60:63],v[28:31]\n"
EX"v17,v17\n"
MF"v[28:31],a[88:91],a[56:59],v[28:31]\n"
RD"a[112:115],v226 offset:12288\n"
PK"v9,v10,v11\n"
MF"v[32:35],a[96:99],a[0:3],0\n"
WT"lgkmcnt(4)\n"
EX"v18,v18\n"
MF"v[32:35],a[100:103],a[4:7],v[32:35]\n"
BL"v241,s[12:15],s17 offen offset:1024 lds\n"
MF"v[40:43],a[100:103],a[20:23],0\n"
EX"v19,v19\n"
MF"v[40:43],a[96:99],a[16:19],v[40:43]\n"
RD"a[116:119],v226 offset:13312\n"
RT"a[152:153],v243 offset:32\n"
MF"v[48:51],a[96:99],a[32:35],0\n"
EX"v24,v24\n"
MF"v[48:51],a[100:103],a[36:39],v[48:51]\n"
RT"a[154:155],v243 offset:16416\n"
RD"a[120:123],v226 offset:14336\n"
EX"v25,v25\n"
MF"v[56:59],a[100:103],a[52:55],0\n"
EX"v26,v26\n"
PK"v16,v16,v17\n"
MF"v[56:59],a[96:99],a[48:51],v[56:59]\n"
EX"v27,v27\n"
MF"v[32:35],a[104:107],a[8:11],v[32:35]\n"
WT"lgkmcnt(4)\n"
BL"v251,s[12:15],s17 offen offset:2048 lds\n"
MF"v[32:35],a[108:111],a[12:15],v[32:35]\n"
EX"v4,v4\n"
PK"v17,v18,v19\n"
MF"v[40:43],a[108:111],a[28:31],v[40:43]\n"
RD"a[124:127],v226 offset:15360\n"
PK"v24,v24,v25\n"
MF"v[40:43],a[104:107],a[24:27],v[40:43]\n"
EX"v5,v5\n"
MF"v[48:51],a[104:107],a[40:43],v[48:51]\n"
RT"a[156:157],v243 offset:64\n"
RT"a[158:159],v243 offset:16448\n"
MF"v[48:51],a[108:111],a[44:47],v[48:51]\n"
EX"v6,v6\n"
MF"v[56:59],a[108:111],a[60:63],v[56:59]\n"
EX"v7,v7\n"
MF"v[56:59],a[104:107],a[56:59],v[56:59]\n"
EX"v12,v12\n"
PK"v25,v26,v27\n"
MF"v[36:39],a[112:115],a[0:3],0\n"
WT"lgkmcnt(3)\n"
EX"v13,v13\n"
MF"v[36:39],a[116:119],a[4:7],v[36:39]\n"
RD"a[64:67],v227 offset:16384\n"
PK"v2,v4,v5\n"
MF"v[44:47],a[116:119],a[20:23],0\n"
EX"v14,v14\n"
MF"v[44:47],a[112:115],a[16:19],v[44:47]\n"
RT"a[160:161],v243 offset:96\n"
RT"a[162:163],v243 offset:16480\n"
MF"v[52:55],a[112:115],a[32:35],0\n"
EX"v15,v15\n"
MF"v[52:55],a[116:119],a[36:39],v[52:55]\n"
RD"a[68:71],v227 offset:17408\n"
PK"v3,v6,v7\n"
MF"v[60:63],a[116:119],a[52:55],0\n"
EX"v20,v20\n"
MF"v[60:63],a[112:115],a[48:51],v[60:63]\n"
"v_readlane_b32 s43,v228,s42\n"
PK"v10,v12,v13\n"
MF"v[36:39],a[120:123],a[8:11],v[36:39]\n"
WT"lgkmcnt(3)\n"
EX"v21,v21\n"
MF"v[36:39],a[124:127],a[12:15],v[36:39]\n"
RD"a[72:75],v227 offset:18432\n"
PK"v11,v14,v15\n"
MF"v[44:47],a[124:127],a[28:31],v[44:47]\n"
EX"v22,v22\n"
MF"v[44:47],a[120:123],a[24:27],v[44:47]\n"
"s_mov_b32 m0,s40\n"
RT"a[164:165],v243 offset:128\n"
RT"a[166:167],v243 offset:16512\n"
MF"v[52:55],a[120:123],a[40:43],v[52:55]\n"
SA"s17,s17,61440\n"
EX"v23,v23\n"
MF"v[52:55],a[124:127],a[44:47],v[52:55]\n"
RD"a[76:79],v227 offset:19456\n"
"v_readlane_b32 s44,v229,s42\n"
MF"v[60:63],a[124:127],a[60:63],v[60:63]\n"
EX"v28,v28\n"
MF"v[60:63],a[120:123],a[56:59],v[60:63]\n"
WT"lgkmcnt(1)\n"
BL"v225,s[8:11],s43 offen offset:0 sc1 lds\n"
MF"v[80:83],a[64:67],v[64:67],v[80:83]\n"
SA"s42,s42,1\n"
EX"v29,v29\n"
MF"v[112:115],a[64:67],v[68:71],v[112:115]\n"
SA"s41,s41,32768\n"
RD"a[80:83],v227 offset:20480\n"
PK"v18,v20,v21\n"
MF"v[144:147],a[64:67],v[72:75],v[144:147]\n"
"s_cmp_eq_u32 s41,163840\n"
EX"v30,v30\n"
MF"v[176:179],a[64:67],v[76:79],v[176:179]\n"
"s_cselect_b32 s41,0,s41\n"
BS"a[148:151],v224,s[8:11],s18 offen offset:0 sc1\n"
MF"v[84:87],a[68:71],v[64:67],v[84:87]\n"
EX"v31,v31\n"
MF"v[116:119],a[68:71],v[68:71],v[116:119]\n"
RD"a[84:87],v227 offset:21504\n"
"v_add_u32 v226,s41,v224\n"
PK"v19,v22,v23\n"
MF"v[148:151],a[68:71],v[72:75],v[148:151]\n"
EX"v32,v32\n"
MF"v[180:183],a[68:71],v[76:79],v[180:183]\n"
BL"v225,s[8:11],s43 offen offset:1024 sc1 lds\n"
MF"v[88:91],a[72:75],v[64:67],v[88:91]\n"
EX"v33,v33\n"
MF"v[120:123],a[72:75],v[68:71],v[120:123]\n"
RD"a[88:91],v227 offset:22528\n"
PK"v26,v28,v29\n"
MF"v[152:155],a[72:75],v[72:75],v[152:155]\n"
EX"v34,v34\n"
MF"v[184:187],a[72:75],v[76:79],v[184:187]\n"
WT"lgkmcnt(2)\n"
BS"a[152:155],v224,s[8:11],s18 offen offset:2048 sc1\n"
MF"v[92:95],a[76:79],v[64:67],v[92:95]\n"
EX"v35,v35\n"
MF"v[124:127],a[76:79],v[68:71],v[124:127]\n"
RD"a[92:95],v227 offset:23552\n"
PK"v27,v30,v31\n"
MF"v[156:159],a[76:79],v[72:75],v[156:159]\n"
SA"s18,s18,4096\n"
EX"v40,v40\n"
MF"v[188:191],a[76:79],v[76:79],v[188:191]\n"
BL"v225,s[8:11],s43 offen offset:2048 sc1 lds\n"
MF"v[96:99],a[80:83],v[64:67],v[96:99]\n"
EX"v41,v41\n"
MF"v[128:131],a[80:83],v[68:71],v[128:131]\n"
RD"a[96:99],v227 offset:24576\n"
MF"v[160:163],a[80:83],v[72:75],v[160:163]\n"
EX"v42,v42\n"
MF"v[192:195],a[80:83],v[76:79],v[192:195]\n"
WT"lgkmcnt(2)\n"
BS"a[156:159],v224,s[8:11],s18 offen offset:0 sc1\n"
MF"v[100:103],a[84:87],v[64:67],v[100:103]\n"
EX"v43,v43\n"
MF"v[132:135],a[84:87],v[68:71],v[132:135]\n"
RD"a[100:103],v227 offset:25600\n"
MF"v[164:167],a[84:87],v[72:75],v[164:167]\n"
EX"v48,v48\n"
MF"v[196:199],a[84:87],v[76:79],v[196:199]\n"
BL"v225,s[8:11],s43 offen offset:3072 sc1 lds\n"
MF"v[104:107],a[88:91],v[64:67],v[104:107]\n"
EX"v49,v49\n"
MF"v[136:139],a[88:91],v[68:71],v[136:139]\n"
RD"a[104:107],v227 offset:26624\n"
MF"v[168:171],a[88:91],v[72:75],v[168:171]\n"
EX"v50,v50\n"
MF"v[200:203],a[88:91],v[76:79],v[200:203]\n"
WT"lgkmcnt(2)\n"
BS"a[160:163],v224,s[8:11],s18 offen offset:2048 sc1\n"
MF"v[108:111],a[92:95],v[64:67],v[108:111]\n"
SA"m0,s40,16384\n"
EX"v51,v51\n"
MF"v[140:143],a[92:95],v[68:71],v[140:143]\n"
RD"a[108:111],v227 offset:27648\n"
MF"v[172:175],a[92:95],v[72:75],v[172:175]\n"
SA"s18,s18,4096\n"
EX"v56,v56\n"
MF"v[204:207],a[92:95],v[76:79],v[204:207]\n"
BL"v225,s[8:11],s44 offen offset:0 sc1 lds\n"
MF"a[132:135],a[128:131],v[64:67],a[132:135]\n"
SA"s40,s40,32768\n"
EX"v57,v57\n"
MF"a[136:139],a[128:131],v[68:71],a[136:139]\n"
"s_cmp_eq_u32 s40,s46\n"
RD"a[112:115],v227 offset:28672\n"
"s_cselect_b32 s40,s47,s40\n"
MF"a[140:143],a[128:131],v[72:75],a[140:143]\n"
EX"v58,v58\n"
MF"a[144:147],a[128:131],v[76:79],a[144:147]\n"
BS"a[164:167],v224,s[8:11],s18 offen offset:0 sc1\n"
MF"v[80:83],a[96:99],v[0:3],v[80:83]\n"
"s_lshr_b32 s21,s16,1\n"
EX"v59,v59\n"
MF"v[112:115],a[96:99],v[8:11],v[112:115]\n"
"s_sub_u32 s21,s21,s33\n"
RD"a[116:119],v227 offset:29696\n"
MF"v[144:147],a[96:99],v[16:19],v[144:147]\n"
"s_and_b32 s21,s21,3\n"
EX"v36,v36\n"
MF"v[176:179],a[96:99],v[24:27],v[176:179]\n"
WT"lgkmcnt(2)\n"
BL"v225,s[8:11],s44 offen offset:1024 sc1 lds\n"
MF"v[84:87],a[100:103],v[0:3],v[84:87]\n"
"s_lshl_b32 s21,s21,1\n"
EX"v37,v37\n"
MF"v[116:119],a[100:103],v[8:11],v[116:119]\n"
"s_and_b32 s22,s16,1\n"
RD"a[120:123],v227 offset:30720\n"
SA"s21,s21,s22\n"
MF"v[148:151],a[100:103],v[16:19],v[148:151]\n"
"s_lshl_b32 s21,s21,1\n"
EX"v38,v38\n"
MF"v[180:183],a[100:103],v[24:27],v[180:183]\n"
"s_and_b32 s22,s30,1\n"
MF"v[88:91],a[104:107],v[0:3],v[88:91]\n"
SA"s21,s21,s22\n"
EX"v39,v39\n"
MF"v[120:123],a[104:107],v[8:11],v[120:123]\n"
"s_mul_i32 s21,s21,10240\n"
RD"a[124:127],v227 offset:31744\n"
MF"v[152:155],a[104:107],v[16:19],v[152:155]\n"
"s_lshr_b32 s22,s30,1\n"
EX"v44,v44\n"
MF"v[184:187],a[104:107],v[24:27],v[184:187]\n"
"s_lshl_b32 s22,s22,10\n"
BL"v225,s[8:11],s44 offen offset:2048 sc1 lds\n"
MF"v[92:95],a[108:111],v[0:3],v[92:95]\n"
SA"s21,s21,s22\n"
EX"v45,v45\n"
MF"v[124:127],a[108:111],v[8:11],v[124:127]\n"
"s_mul_i32 s22,s35,163840\n"
MF"v[156:159],a[108:111],v[16:19],v[156:159]\n"
SA"s21,s21,s22\n"
EX"v46,v46\n"
MF"v[188:191],a[108:111],v[24:27],v[188:191]\n"
WT"lgkmcnt(1)\n"
SA"s18,s21,3801088\n"
MF"v[96:99],a[112:115],v[0:3],v[96:99]\n"
SA"s16,s16,1\n"
WT"lgkmcnt(0)\n"
"s_barrier\n"
MF"v[128:131],a[112:115],v[8:11],v[128:131]\n"
RD"a[64:67],v226 offset:0\n"
MF"v[160:163],a[112:115],v[16:19],v[160:163]\n"
EX"v47,v47\n"
MF"v[192:195],a[112:115],v[24:27],v[192:195]\n"
BL"v225,s[8:11],s44 offen offset:3072 sc1 lds\n"
MF"v[100:103],a[116:119],v[0:3],v[100:103]\n"
EX"v52,v52\n"
MF"v[132:135],a[116:119],v[8:11],v[132:135]\n"
RD"a[68:71],v226 offset:1024\n"
MF"v[164:167],a[116:119],v[16:19],v[164:167]\n"
EX"v53,v53\n"
MF"v[196:199],a[116:119],v[24:27],v[196:199]\n"
RD"a[72:75],v226 offset:2048\n"
MF"v[104:107],a[120:123],v[0:3],v[104:107]\n"
EX"v54,v54\n"
MF"v[136:139],a[120:123],v[8:11],v[136:139]\n"
RD"a[76:79],v226 offset:3072\n"
MF"v[168:171],a[120:123],v[16:19],v[168:171]\n"
EX"v55,v55\n"
MF"v[200:203],a[120:123],v[24:27],v[200:203]\n"
MF"v[108:111],a[124:127],v[0:3],v[108:111]\n"
EX"v60,v60\n"
MF"v[140:143],a[124:127],v[8:11],v[140:143]\n"
RD"a[80:83],v226 offset:4096\n"
MF"v[172:175],a[124:127],v[16:19],v[172:175]\n"
EX"v61,v61\n"
MF"v[204:207],a[124:127],v[24:27],v[204:207]\n"
RD"a[84:87],v226 offset:5120\n"
MF"a[132:135],a[128:131],v[0:3],a[132:135]\n"
EX"v62,v62\n"
MF"a[136:139],a[128:131],v[8:11],a[136:139]\n"
RD"a[88:91],v226 offset:6144\n"
MF"a[140:143],a[128:131],v[16:19],a[140:143]\n"
EX"v63,v63\n"
MF"a[144:147],a[128:131],v[24:27],a[144:147]\n"
"s_sub_u32 s20,s20,1\n"
"s_cbranch_scc0 LRT_%=\n"
"s_lshl_b32 s21,s35,8\n"
SA"s21,s21,92160\n"
SA"s21,s21,s45\n"
WT"vmcnt(3)\n"
"s_barrier\n"
"buffer_store_dwordx2 v[232:233],v230,s[8:11],s21 offen sc1\n"
"v_cmp_eq_u64 vcc,s[28:29],v[244:245]\n"
"s_cmp_eq_u64 vcc,exec\n"
"s_cbranch_scc0 L1105_%=\n"
"L1106_%=:\n"
"s_mov_b32 s57,0\n"
"s_mov_b32 s56,0\n"
"s_mov_b32 s14,0\n"
"s_branch LP1_%=\n"
"LEND_%=:\n"
      : : [ka] "s"(__builtin_amdgcn_kernarg_segment_ptr()), [b] "s"(blockIdx.x), [t] "v"(threadIdx.x)
      : "v1", "v2", "v3", "v4", "v5", "v6", "v7", "v8", "v9", "v10", "v11", "v12", "v13", "v14", "v15", "v16", "v17", "v18", "v19", "v20", "v21", "v22", "v23", "v24", "v25", "v26", "v27", "v28", "v29", "v30", "v31", "v32", "v33", "v34", "v35", "v36", "v37", "v38", "v39", "v40", "v41", "v42", "v43", "v44", "v45", "v46", "v47", "v48", "v49", "v50", "v51", "v52", "v53", "v54", "v55", "v56", "v57", "v58", "v59", "v60", "v61", "v62", "v63", "v64", "v65", "v66", "v67", "v68", "v69", "v70", "v71", "v72", "v73", "v74", "v75", "v76", "v77", "v78", "v79", "v80", "v81", "v82", "v83", "v84", "v85", "v86", "v87", "v88", "v89", "v90", "v91", "v92", "v93", "v94", "v95", "v96", "v97", "v98", "v99", "v100", "v101", "v102", "v103", "v104", "v105", "v106", "v107", "v108", "v109", "v110", "v111", "v112", "v113", "v114", "v115", "v116", "v117", "v118", "v119", "v120", "v121", "v122", "v123", "v124", "v125", "v126", "v127", "v128", "v129", "v130", "v131", "v132", "v133", "v134", "v135", "v136", "v137", "v138", "v139", "v140", "v141", "v142", "v143", "v144", "v145", "v146", "v147", "v148", "v149", "v150", "v151", "v152", "v153", "v154", "v155", "v156", "v157", "v158", "v159", "v160", "v161", "v162", "v163", "v164", "v165", "v166", "v167", "v168", "v169", "v170", "v171", "v172", "v173", "v174", "v175", "v176", "v177", "v178", "v179", "v180", "v181", "v182", "v183", "v184", "v185", "v186", "v187", "v188", "v189", "v190", "v191", "v192", "v193", "v194", "v195", "v196", "v197", "v198", "v199", "v200", "v201", "v202", "v203", "v204", "v205", "v206", "v207", "v208", "v209", "v210", "v211", "v212", "v213", "v214", "v215", "v216", "v217", "v218", "v219", "v220", "v221", "v222", "v223", "v224", "v225", "v226", "v227", "v228", "v229", "v230", "v231", "v232", "v233", "v234", "v235", "v236", "v237", "v238", "v239", "v240", "v241", "v242", "v243", "v244", "v245", "v246", "v247", "v248", "v249", "v250", "v251", "v252", "v253", "v254", "v255", "a0", "a1", "a2", "a3", "a4", "a5", "a6", "a7", "a8", "a9", "a10", "a11", "a12", "a13", "a14", "a15", "a16", "a17", "a18", "a19", "a20", "a21", "a22", "a23", "a24", "a25", "a26", "a27", "a28", "a29", "a30", "a31", "a32", "a33", "a34", "a35", "a36", "a37", "a38", "a39", "a40", "a41", "a42", "a43", "a44", "a45", "a46", "a47", "a48", "a49", "a50", "a51", "a52", "a53", "a54", "a55", "a56", "a57", "a58", "a59", "a60", "a61", "a62", "a63", "a64", "a65", "a66", "a67", "a68", "a69", "a70", "a71", "a72", "a73", "a74", "a75", "a76", "a77", "a78", "a79", "a80", "a81", "a82", "a83", "a84", "a85", "a86", "a87", "a88", "a89", "a90", "a91", "a92", "a93", "a94", "a95", "a96", "a97", "a98", "a99", "a100", "a101", "a102", "a103", "a104", "a105", "a106", "a107", "a108", "a109", "a110", "a111", "a112", "a113", "a114", "a115", "a116", "a117", "a118", "a119", "a120", "a121", "a122", "a123", "a124", "a125", "a126", "a127", "a128", "a129", "a130", "a131", "a132", "a133", "a134", "a135", "a136", "a137", "a138", "a139", "a140", "a141", "a142", "a143", "a144", "a145", "a146", "a147", "a148", "a149", "a150", "a151", "a152", "a153", "a154", "a155", "a156", "a157", "a158", "a159", "a160", "a161", "a162", "a163", "a164", "a165", "a166", "a167", "a168", "a169", "a170", "a171", "a172", "a173", "a174", "a175", "a176", "a177", "a178", "a179", "a180", "a181", "a182", "a183", "a184", "a185", "a186", "a187", "a188", "a189", "a190", "a191", "a192", "a193", "a194", "a195", "a196", "a197", "a198", "a199", "a200", "a201", "a202", "a203", "a204", "a205", "a206", "a207", "a208", "a209", "a210", "a211", "a212", "a213", "a214", "a215", "a216", "a217", "a218", "a219", "a220", "a221", "a222", "a223", "a224", "a225", "a226", "a227", "a228", "a229", "a230", "a231", "a232", "a233", "a234", "a235", "a236", "a237", "a238", "a239", "a240", "a241", "a242", "a243", "a244", "a245", "a246", "a247", "a248", "a249", "a250", "a251", "a252", "a253", "a254", "a255", "s8", "s9", "s10", "s11", "s12", "s13", "s14", "s15", "s16", "s17", "s18", "s19", "s20", "s21", "s22", "s23", "s24", "s25", "s26", "s27", "s28", "s29", "s30", "s31", "s32", "s33", "s34", "s35", "s36", "s37", "s38", "s39", "s40", "s41", "s42", "s43", "s44", "s45", "s46", "s47", "s48", "s49", "s50", "s51", "s52", "s53", "s54", "s55", "s56", "s57", "s58", "s59", "s60", "s61", "s62", "s63", "s64", "s65", "s66", "s67", "s68", "s69", "s70", "s71", "s72", "s73", "s74", "s75", "s76", "s77", "s78", "s79", "s80", "s81", "s82", "s83", "s84", "s85", "s86", "s87", "s88", "s89", "s90", "s91", "s92", "s93", "s94", "s95", "s96", "s97", "s98", "s99", "scc", "vcc", "memory");
}

extern "C" void kernel_launch(void* const* d_in, const int* in_sizes, int n_in, void* d_out, int out_size, void* d_ws, size_t ws_size, hipStream_t stream) {
  (void)hipFuncSetAttribute((const void*)mega_kernel, hipFuncAttributeMaxDynamicSharedMemorySize, 163840);
  mega_kernel<<<256, 256, 163840, stream>>>((const bf16*)d_in[0], (const bf16*)d_in[1], (const bf16*)d_in[2], (const bf16*)d_in[3], (const bf16*)d_in[4], (const bf16*)d_in[5],
      (const bf16*)d_in[7], (const bf16*)d_in[8], (const bf16*)d_in[9], (const bf16*)d_in[10], (char*)d_ws + (size_t)0 * 1048576, (bf16*)d_out);
}
```

```cpp
#include <hip/hip_runtime.h>
#include <hip/hip_bf16.h>
typedef __hip_bfloat16 bf16;
#define MF "v_mfma_f32_16x16x32_bf16 "
#define RD "ds_read_b128 "
#define RT "ds_read_b64_tr_b16 "
#define BL "buffer_load_dwordx4 "
#define BS "buffer_store_dwordx4 "
#define PK "v_cvt_pk_bf16_f32 "
#define EX "v_exp_f32 "
#define WT "s_waitcnt "
#define MU "v_mul_f32 "
#define SA "s_add_u32 "

__global__ __launch_bounds__(256, 1) void mega_kernel(const bf16* x, const bf16* ckv, const bf16* wq, const bf16* wuk, const bf16* wuv, const bf16* wo, const bf16* w_r,
                                                      const bf16* wg, const bf16* wu, const bf16* wd, char* ws, bf16* out) {
  asm volatile(
"s_load_dwordx16 s[64:79],%[ka],0\n"
"s_load_dwordx8 s[80:87],%[ka],64\n"
"s_mov_b32 s31,%[b]\n"
"v_lshrrev_b32 v1,6,%[t]\n"
"v_and_b32 v2,63,%[t]\n"
"v_readfirstlane_b32 s30,v1\n"
"v_lshlrev_b32 v224,4,v2\n"
"s_getpc_b64 s[24:25]\n"
"LPC_%=:\n"
"s_and_b32 s25,s25,0xffff\n"
"s_mov_b32 s26,LEND_%=-LPC_%=\n"
"s_mov_b32 s27,0x20000\n"
"s_lshr_b32 s49,s31,3\n"
"s_lshl_b32 s49,s49,2\n"
SA"s49,s49,s30\n"
"s_lshl_b32 s49,s49,7\n"
"v_lshlrev_b32 v10,14,v2\n"
"buffer_load_dword v255,v10,s[24:27],s49 offen\n"
"s_and_b32 s32,s31,7\n"
"s_lshr_b32 s33,s31,6\n"
"s_bfe_u32 s34,s31,0x30003\n"
"s_lshl_b32 s35,s32,2\n"
SA"s35,s35,s33\n"
"s_mov_b32 s46,0x65327410\n"
"s_mov_b32 s47,0x54217630\n"
"s_bitcmp1_b32 s34,0\n"
"s_cselect_b32 s46,0x64317520,s46\n"
"s_cselect_b32 s47,0x64327510,s47\n"
"s_bitcmp1_b32 s34,1\n"
"s_cselect_b32 s46,s47,s46\n"
"s_mul_i32 s47,s32,0x11111111\n"
SA"s46,s46,s47\n"
"s_lshl_b32 s36,s30,12\n"
"s_mul_i32 s37,s30,24576\n"
SA"s37,s37,65536\n"
"v_lshrrev_b32 v3,3,v2\n"
"v_lshrrev_b32 v4,4,v2\n"
"v_and_b32 v5,7,v2\n"
"v_xor_b32 v6,v5,v4\n"
"v_lshlrev_b32 v6,4,v6\n"
"v_lshl_add_u32 v6,v3,10,v6\n"
"s_lshl_b32 s50,s30,15\n"
"v_add_u32 v234,s50,v6\n"
"v_xor_b32 v235,64,v234\n"
"v_add_u32 v235,7168,v235\n"
"v_add_u32 v236,14336,v234\n"
"v_add_u32 v237,14336,v235\n"
"v_and_b32 v7,1,v4\n"
"v_lshlrev_b32 v7,1,v7\n"
"v_xor_b32 v7,v7,v5\n"
"v_lshlrev_b32 v7,4,v7\n"
"v_lshl_add_u32 v238,v3,13,v7\n"
"v_xor_b32 v239,64,v238\n"
"v_add_u32 v239,64512,v239\n"
"v_add_u32 v240,129024,v238\n"
"v_add_u32 v241,129024,v239\n"
"v_and_b32 v8,31,v2\n"
"v_lshlrev_b32 v8,3,v8\n"
"v_lshrrev_b32 v9,5,v2\n"
"s_lshl_b32 s50,s31,2\n"
SA"s50,s50,s30\n"
"s_mul_i32 s50,s50,2560\n"
"v_mul_u32_u24 v9,1280,v9\n"
"v_add3_u32 v9,v9,v8,s50\n"
"s_lshl_b32 s50,s34,17\n"
"s_lshl_b32 s51,s35,8\n"
"s_and_b32 s52,s30,1\n"
"s_lshl_b32 s52,s52,7\n"
SA"s52,s52,s51\n"
"s_mov_b32 s14,0x120000\n"
"s_mov_b32 s15,0x20000\n"
"s_sub_u32 s18,4194304,s52\n"
"s_mov_b32 s19,0x20000\n"
WT"lgkmcnt(0)\n"
"s_load_dwordx2 s[28:29],s[84:85],0\n"
SA"s12,s66,s50\n"
"s_addc_u32 s13,s67,0\n"
"s_and_b32 s13,s13,0xffff\n"
"s_cmp_lt_u32 s30,2\n"
"s_cselect_b64 s[16:17],s[70:71],s[72:73]\n"
SA"s16,s16,s52\n"
"s_addc_u32 s17,s17,0\n"
"s_and_b32 s17,s17,0xffff\n"
"global_load_dwordx2 v[128:129],v9,s[64:65] offset:0\n"
"global_load_dwordx2 v[130:131],v9,s[64:65] offset:256\n"
"global_load_dwordx2 v[132:133],v9,s[64:65] offset:512\n"
"global_load_dwordx2 v[134:135],v9,s[64:65] offset:768\n"
"global_load_dwordx2 v[136:137],v9,s[64:65] offset:1024\n"
"global_load_dwordx2 v[138:139],v8,s[76:77] offset:0\n"
"global_load_dwordx2 v[140:141],v8,s[76:77] offset:256\n"
"global_load_dwordx2 v[142:143],v8,s[76:77] offset:512\n"
"global_load_dwordx2 v[144:145],v8,s[76:77] offset:768\n"
"global_load_dwordx2 v[146:147],v8,s[76:77] offset:1024\n"
SA"m0,s37,0\n"
"s_bfe_u32 s48,s46,0x30000\n"
"s_lshl_b32 s48,s48,19\n"
BL"v238,s[16:19],s48 offen offset:0 lds\n"
BL"v239,s[16:19],s48 offen offset:1024 lds\n"
BL"v240,s[16:19],s48 offen offset:2048 lds\n"
BL"v241,s[16:19],s48 offen offset:3072 lds\n"
SA"m0,s37,4096\n"
"s_bfe_u32 s48,s46,0x30000\n"
"s_lshl_b32 s48,s48,19\n"
"s_bitset1_b32 s48,18\n"
BL"v238,s[16:19],s48 offen offset:0 lds\n"
BL"v239,s[16:19],s48 offen offset:1024 lds\n"
BL"v240,s[16:19],s48 offen offset:2048 lds\n"
BL"v241,s[16:19],s48 offen offset:3072 lds\n"
"v_lshlrev_b32 v253,7,%[t]\n"
"s_mov_b32 s8,s84\n"
"s_and_b32 s9,s85,0xffff\n"
"s_mov_b32 s10,82575360\n"
"s_mov_b32 s11,0x20000\n"
SA"s20,s68,s51\n"
"s_addc_u32 s21,s69,0\n"
"s_and_b32 s21,s21,0xffff\n"
"s_sub_u32 s22,5242880,s51\n"
"s_mov_b32 s23,0x20000\n"
"v_and_b32 v11,15,v2\n"
"v_lshrrev_b32 v12,1,v11\n"
"v_xor_b32 v12,v12,v4\n"
"v_lshlrev_b32 v12,4,v12\n"
"v_lshl_add_u32 v242,v11,7,v12\n"
"v_xor_b32 v243,64,v242\n"
"v_lshrrev_b32 v12,2,v11\n"
"v_lshl_add_u32 v12,v4,3,v12\n"
"v_lshlrev_b32 v12,7,v12\n"
"v_and_b32 v13,3,v2\n"
"v_lshl_add_u32 v12,v13,3,v12\n"
"v_and_b32 v13,3,v3\n"
"v_lshl_add_u32 v12,v13,5,v12\n"
"v_add_u32 v246,s37,v12\n"
"v_xor_b32 v247,32,v246\n"
"v_xor_b32 v248,64,v246\n"
"v_xor_b32 v249,96,v246\n"
"v_mov_b32 v13,2130706432\n"
"v_cmp_eq_u32 vcc,0,v2\n"
"s_cmp_eq_u32 s30,0\n"
"s_cselect_b32 s45,0,2130706432\n"
"v_cndmask_b32_e64 v230,v13,0,vcc\n"
"s_lshl_b32 s39,s30,3\n"
"s_lshr_b32 s98,s46,s39\n"
"s_and_b32 s39,s98,7\n"
"s_lshl_b32 s39,s39,7\n"
"s_and_b32 s98,s98,0x70\n"
"s_lshl_b32 s98,s98,3\n"
"s_lshl_b32 s50,s30,15\n"
"s_sub_u32 s50,1048576,s50\n"
SA"s39,s39,s50\n"
SA"s98,s98,s50\n"
"s_mov_b32 s55,0x3e0293ee\n"
"s_cmp_lt_u32 s30,2\n"
"s_cbranch_scc0 LV_%=\n"
WT"vmcnt(17)\n"
"v_lshlrev_b32 v148,16,v128\n"
"v_and_b32 v149,0xffff0000,v128\n"
"v_lshlrev_b32 v150,16,v129\n"
"v_and_b32 v151,0xffff0000,v129\n"
WT"vmcnt(16)\n"
"v_lshlrev_b32 v152,16,v130\n"
"v_and_b32 v153,0xffff0000,v130\n"
"v_lshlrev_b32 v154,16,v131\n"
"v_and_b32 v155,0xffff0000,v131\n"
WT"vmcnt(15)\n"
"v_lshlrev_b32 v156,16,v132\n"
"v_and_b32 v157,0xffff0000,v132\n"
"v_lshlrev_b32 v158,16,v133\n"
"v_and_b32 v159,0xffff0000,v133\n"
WT"vmcnt(14)\n"
"v_lshlrev_b32 v160,16,v134\n"
"v_and_b32 v161,0xffff0000,v134\n"
"v_lshlrev_b32 v162,16,v135\n"
"v_and_b32 v163,0xffff0000,v135\n"
WT"vmcnt(13)\n"
"v_lshlrev_b32 v164,16,v136\n"
"v_and_b32 v165,0xffff0000,v136\n"
"v_lshlrev_b32 v166,16,v137\n"
"v_and_b32 v167,0xffff0000,v137\n"
"v_add_f32 v188,v148,v149\n"
"v_add_f32 v189,v150,v151\n"
"v_add_f32 v188,v188,v152\n"
"v_add_f32 v189,v189,v153\n"
"v_add_f32 v188,v188,v154\n"
"v_add_f32 v189,v189,v155\n"
"v_add_f32 v188,v188,v156\n"
"v_add_f32 v189,v189,v157\n"
"v_add_f32 v188,v188,v158\n"
"v_add_f32 v189,v189,v159\n"
"v_add_f32 v188,v188,v160\n"
"v_add_f32 v189,v189,v161\n"
"v_add_f32 v188,v188,v162\n"
"v_add_f32 v189,v189,v163\n"
"v_add_f32 v188,v188,v164\n"
"v_add_f32 v189,v189,v165\n"
"v_add_f32 v188,v188,v166\n"
"v_add_f32 v189,v189,v167\n"
"v_add_f32 v188,v188,v189\n"
"s_nop 1\n"
"v_add_f32_dpp v188,v188,v188 quad_perm:[1,0,3,2] row_mask:0xf bank_mask:0xf\n"
WT"vmcnt(12)\n"
"v_lshlrev_b32 v168,16,v138\n"
"v_and_b32 v169,0xffff0000,v138\n"
"v_add_f32_dpp v188,v188,v188 quad_perm:[2,3,0,1] row_mask:0xf bank_mask:0xf\n"
"v_lshlrev_b32 v170,16,v139\n"
"v_and_b32 v171,0xffff0000,v139\n"
"v_add_f32_dpp v188,v188,v188 row_half_mirror row_mask:0xf bank_mask:0xf\n"
WT"vmcnt(11)\n"
"v_lshlrev_b32 v172,16,v140\n"
"v_and_b32 v173,0xffff0000,v140\n"
"v_add_f32_dpp v188,v188,v188 row_mirror row_mask:0xf bank_mask:0xf\n"
"v_lshlrev_b32 v174,16,v141\n"
"v_and_b32 v175,0xffff0000,v141\n"
"v_mov_b32 v189,v188\n"
WT"vmcnt(10)\n"
"v_lshlrev_b32 v176,16,v142\n"
"v_and_b32 v177,0xffff0000,v142\n"
"v_permlane16_swap_b32 v189,v188\n"
"v_lshlrev_b32 v178,16,v143\n"
"v_and_b32 v179,0xffff0000,v143\n"
"v_add_f32 v188,v188,v189\n"
WT"vmcnt(9)\n"
"v_lshlrev_b32 v180,16,v144\n"
SA"m0,s36,0\n"
"s_bfe_u32 s48,s46,0x30000\n"
"s_lshl_b32 s48,s48,7\n"
BL"v234,s[12:15],s48 offen offset:0 lds\n"
BL"v235,s[12:15],s48 offen offset:1024 lds\n"
BL"v236,s[12:15],s48 offen offset:2048 lds\n"
BL"v237,s[12:15],s48 offen offset:3072 lds\n"
"v_and_b32 v181,0xffff0000,v144\n"
MU"v191,0x3acccccd,v188\n"
"v_lshlrev_b32 v182,16,v145\n"
"v_and_b32 v183,0xffff0000,v145\n"
PK"v128,v191,v191\n"
WT"vmcnt(12)\n"
"v_lshlrev_b32 v184,16,v146\n"
"v_and_b32 v185,0xffff0000,v146\n"
"v_lshlrev_b32 v128,16,v128\n"
"v_lshlrev_b32 v186,16,v147\n"
"v_and_b32 v187,0xffff0000,v147\n"
"v_sub_f32 v148,v148,v128\n"
"v_sub_f32 v149,v149,v128\n"
"v_sub_f32 v150,v150,v128\n"
"v_sub_f32 v151,v151,v128\n"
"v_sub_f32 v152,v152,v128\n"
"v_sub_f32 v153,v153,v128\n"
"v_sub_f32 v154,v154,v128\n"
"v_sub_f32 v155,v155,v128\n"
"v_sub_f32 v156,v156,v128\n"
"v_sub_f32 v157,v157,v128\n"
"v_sub_f32 v158,v158,v128\n"
"v_sub_f32 v159,v159,v128\n"
"v_sub_f32 v160,v160,v128\n"
"v_sub_f32 v161,v161,v128\n"
"v_sub_f32 v162,v162,v128\n"
"v_sub_f32 v163,v163,v128\n"
"v_sub_f32 v164,v164,v128\n"
"v_sub_f32 v165,v165,v128\n"
"v_sub_f32 v166,v166,v128\n"
"v_sub_f32 v167,v167,v128\n"
MU"v188,v148,v148\n"
MU"v189,v149,v149\n"
"v_fmac_f32 v188,v150,v150\n"
"v_fmac_f32 v189,v151,v151\n"
"v_fmac_f32 v188,v152,v152\n"
"v_fmac_f32 v189,v153,v153\n"
"v_fmac_f32 v188,v154,v154\n"
"v_fmac_f32 v189,v155,v155\n"
"v_fmac_f32 v188,v156,v156\n"
"v_fmac_f32 v189,v157,v157\n"
"v_fmac_f32 v188,v158,v158\n"
"v_fmac_f32 v189,v159,v159\n"
"v_fmac_f32 v188,v160,v160\n"
"v_fmac_f32 v189,v161,v161\n"
"v_fmac_f32 v188,v162,v162\n"
"v_fmac_f32 v189,v163,v163\n"
"v_fmac_f32 v188,v164,v164\n"
"v_fmac_f32 v189,v165,v165\n"
"v_fmac_f32 v188,v166,v166\n"
"v_fmac_f32 v189,v167,v167\n"
"v_add_f32 v188,v188,v189\n"
PK"v149,v148,v149\n"
"v_lshlrev_b32 v148,16,v149\n"
"v_add_f32_dpp v188,v188,v188 quad_perm:[1,0,3,2] row_mask:0xf bank_mask:0xf\n"
"v_and_b32 v149,0xffff0000,v149\n"
PK"v151,v150,v151\n"
"v_add_f32_dpp v188,v188,v188 quad_perm:[2,3,0,1] row_mask:0xf bank_mask:0xf\n"
"v_lshlrev_b32 v150,16,v151\n"
"v_and_b32 v151,0xffff0000,v151\n"
SA"m0,s37,8192\n"
"s_bfe_u32 s48,s46,0x30004\n"
"s_lshl_b32 s48,s48,19\n"
BL"v238,s[16:19],s48 offen offset:0 lds\n"
BL"v239,s[16:19],s48 offen offset:1024 lds\n"
BL"v240,s[16:19],s48 offen offset:2048 lds\n"
BL"v241,s[16:19],s48 offen offset:3072 lds\n"
"v_add_f32_dpp v188,v188,v188 row_half_mirror row_mask:0xf bank_mask:0xf\n"
PK"v153,v152,v153\n"
"v_lshlrev_b32 v152,16,v153\n"
"v_add_f32_dpp v188,v188,v188 row_mirror row_mask:0xf bank_mask:0xf\n"
"v_and_b32 v153,0xffff0000,v153\n"
PK"v155,v154,v155\n"
"v_mov_b32 v189,v188\n"
"v_lshlrev_b32 v154,16,v155\n"
"v_and_b32 v155,0xffff0000,v155\n"
"v_permlane16_swap_b32 v189,v188\n"
PK"v157,v156,v157\n"
"v_lshlrev_b32 v156,16,v157\n"
"v_add_f32 v188,v188,v189\n"
"v_and_b32 v157,0xffff0000,v157\n"
PK"v159,v158,v159\n"
"v_sub_f32 v191,v191,v128\n"
"v_lshlrev_b32 v158,16,v159\n"
"v_and_b32 v159,0xffff0000,v159\n"
MU"v188,0x3acccccd,v188\n"
PK"v161,v160,v161\n"
"v_lshlrev_b32 v160,16,v161\n"
"v_fma_f32 v188,-v191,v191,v188\n"
"v_and_b32 v161,0xffff0000,v161\n"
PK"v163,v162,v163\n"
"v_max_f32 v188,0,v188\n"
"v_lshlrev_b32 v162,16,v163\n"
"v_and_b32 v163,0xffff0000,v163\n"
PK"v188,v188,v188\n"
PK"v165,v164,v165\n"
"v_lshlrev_b32 v164,16,v165\n"
"v_lshlrev_b32 v188,16,v188\n"
"v_and_b32 v165,0xffff0000,v165\n"
PK"v167,v166,v167\n"
"v_add_f32 v188,0x3727c5ac,v188\n"
"v_lshlrev_b32 v166,16,v167\n"
"v_and_b32 v167,0xffff0000,v167\n"
PK"v188,v188,v188\n"
"v_bfe_u32 v130,v224,5,4\n"
"v_mul_u32_u24 v130,144,v130\n"
"v_lshlrev_b32 v188,16,v188\n"
"v_bfe_u32 v131,v224,4,1\n"
"v_lshl_add_u32 v130,v131,3,v130\n"
"v_sqrt_f32 v189,v188\n"
"v_lshrrev_b32 v131,9,v224\n"
"v_lshl_add_u32 v130,v131,4,v130\n"
"v_rcp_f32 v190,v189\n"
"s_lshl_b32 s52,s30,5\n"
"v_add_u32 v130,s52,v130\n"
"v_fma_f32 v188,-v189,v189,v188\n"
MU"v190,0.5,v190\n"
"v_fma_f32 v189,v188,v190,v189\n"
PK"v189,v189,v189\n"
"v_lshlrev_b32 v189,16,v189\n"
"v_rcp_f32 v129,v189\n"
"s_nop 0\n"
MU"v148,v148,v129\n"
MU"v149,v149,v129\n"
PK"v138,v148,v149\n"
MU"v150,v150,v129\n"
MU"v151,v151,v129\n"
SA"m0,s37,12288\n"
"s_bfe_u32 s48,s46,0x30004\n"
"s_lshl_b32 s48,s48,19\n"
"s_bitset1_b32 s48,18\n"
BL"v238,s[16:19],s48 offen offset:0 lds\n"
BL"v239,s[16:19],s48 offen offset:1024 lds\n"
BL"v240,s[16:19],s48 offen offset:2048 lds\n"
BL"v241,s[16:19],s48 offen offset:3072 lds\n"
PK"v139,v150,v151\n"
"ds_write_b64 v130,v[138:139] offset:49152\n"
MU"v152,v152,v129\n"
MU"v153,v153,v129\n"
PK"v140,v152,v153\n"
MU"v154,v154,v129\n"
MU"v155,v155,v129\n"
PK"v141,v154,v155\n"
"ds_write_b64 v130,v[140:141] offset:51456\n"
MU"v156,v156,v129\n"
MU"v157,v157,v129\n"
PK"v142,v156,v157\n"
MU"v158,v158,v129\n"
MU"v159,v159,v129\n"
PK"v143,v158,v159\n"
"ds_write_b64 v130,v[142:143] offset:53760\n"
MU"v160,v160,v129\n"
MU"v161,v161,v129\n"
PK"v144,v160,v161\n"
MU"v162,v162,v129\n"
MU"v163,v163,v129\n"
PK"v145,v162,v163\n"
"ds_write_b64 v130,v[144:145] offset:56064\n"
MU"v164,v164,v129\n"
MU"v165,v165,v129\n"
PK"v146,v164,v165\n"
MU"v166,v166,v129\n"
MU"v167,v167,v129\n"
PK"v147,v166,v167\n"
"ds_write_b64 v130,v[146:147] offset:58368\n"
"v_lshlrev_b32 v148,16,v138\n"
"v_and_b32 v149,0xffff0000,v138\n"
MU"v188,v148,v168\n"
MU"v189,v149,v169\n"
"v_lshlrev_b32 v150,16,v139\n"
"v_and_b32 v151,0xffff0000,v139\n"
"v_fmac_f32 v188,v150,v170\n"
"v_fmac_f32 v189,v151,v171\n"
"v_lshlrev_b32 v152,16,v140\n"
"v_and_b32 v153,0xffff0000,v140\n"
"v_fmac_f32 v188,v152,v172\n"
"v_fmac_f32 v189,v153,v173\n"
"v_lshlrev_b32 v154,16,v141\n"
"v_and_b32 v155,0xffff0000,v141\n"
"v_fmac_f32 v188,v154,v174\n"
"v_fmac_f32 v189,v155,v175\n"
"v_lshlrev_b32 v156,16,v142\n"
"v_and_b32 v157,0xffff0000,v142\n"
"v_fmac_f32 v188,v156,v176\n"
"v_fmac_f32 v189,v157,v177\n"
"v_lshlrev_b32 v158,16,v143\n"
"v_and_b32 v159,0xffff0000,v143\n"
"v_fmac_f32 v188,v158,v178\n"
"v_fmac_f32 v189,v159,v179\n"
"v_lshlrev_b32 v160,16,v144\n"
"v_and_b32 v161,0xffff0000,v144\n"
"v_fmac_f32 v188,v160,v180\n"
"v_fmac_f32 v189,v161,v181\n"
"v_lshlrev_b32 v162,16,v145\n"
"v_and_b32 v163,0xffff0000,v145\n"
WT"lgkmcnt(0)\n"
SA"s28,s28,0x7f4a7c15\n"
"s_addc_u32 s29,s29,0x9e3779b9\n"
"v_mov_b32 v232,s28\n"
"v_mov_b32 v233,s29\n"
WT"vmcnt(16)\n"
RT"a[0:1],v246 offset:0\n"
RT"a[2:3],v246 offset:512\n"
RT"a[4:5],v247 offset:0\n"
RT"a[6:7],v247 offset:512\n"
RT"a[8:9],v248 offset:0\n"
RT"a[10:11],v248 offset:512\n"
RT"a[12:13],v249 offset:0\n"
RT"a[14:15],v249 offset:512\n"
WT"vmcnt(12)\n"
RT"a[16:17],v246 offset:4096\n"
RT"a[18:19],v246 offset:4608\n"
RT"a[20:21],v247 offset:4096\n"
RT"a[22:23],v247 offset:4608\n"
RT"a[24:25],v248 offset:4096\n"
RT"a[26:27],v248 offset:4608\n"
RT"a[28:29],v249 offset:4096\n"
RT"a[30:31],v249 offset:4608\n"
WT"vmcnt(8) lgkmcnt(15)\n"
"s_barrier\n"
RD"v[192:195],v242 offset:0\n"
RD"v[196:199],v243 offset:0\n"
RD"v[200:203],v242 offset:2048\n"
RD"v[204:207],v243 offset:2048\n"
RD"v[208:211],v242 offset:4096\n"
"v_lshrrev_b32 v131,7,v224\n"
WT"lgkmcnt(4)\n"
MF"v[0:3],a[0:3],v[192:195],0\n"
WT"lgkmcnt(3)\n"
RD"v[212:215],v243 offset:4096\n"
"v_lshl_add_u32 v131,v131,4,v224\n"
MF"v[0:3],a[16:19],v[196:199],v[0:3]\n"
SA"m0,s36,16384\n"
"s_bfe_u32 s48,s46,0x30004\n"
"s_lshl_b32 s48,s48,7\n"
MF"v[4:7],a[20:23],v[196:199],0\n"
"s_mul_i32 s52,s30,1152\n"
BL"v234,s[12:15],s48 offen offset:0 lds\n"
MF"v[4:7],a[4:7],v[192:195],v[4:7]\n"
"v_add_u32 v128,s52,v131\n"
"s_and_b32 s52,s30,1\n"
MF"v[8:11],a[8:11],v[192:195],0\n"
"s_mul_i32 s51,s52,1152\n"
RD"v[216:219],v242 offset:6144\n"
"v_add_u32 v131,s51,v131\n"
MF"v[8:11],a[24:27],v[196:199],v[8:11]\n"
"s_lshl_b32 s52,s52,10\n"
"v_fmac_f32 v188,v162,v182\n"
"v_fmac_f32 v189,v163,v183\n"
MF"v[12:15],a[28:31],v[196:199],0\n"
BL"v235,s[12:15],s48 offen offset:1024 lds\n"
MF"v[12:15],a[12:15],v[192:195],v[12:15]\n"
WT"lgkmcnt(1)\n"
RD"v[220:223],v243 offset:6144\n"
"v_lshlrev_b32 v164,16,v146\n"
MF"v[16:19],a[0:3],v[200:203],0\n"
RD"v[132:135],v128 offset:49152\n"
"v_and_b32 v129,0x70,v224\n"
MF"v[16:19],a[16:19],v[204:207],v[16:19]\n"
"s_lshr_b32 s50,s31,1\n"
WT"vmcnt(6)\n"
RT"a[32:33],v246 offset:8192\n"
MF"v[20:23],a[20:23],v[204:207],0\n"
"s_mul_i32 s50,s50,20480\n"
RD"a[240:243],v128 offset:53760\n"
"v_and_b32 v165,0xffff0000,v146\n"
MF"v[20:23],a[4:7],v[200:203],v[20:23]\n"
"s_and_b32 s51,s31,1\n"
RD"a[244:247],v131 offset:58368\n"
"v_and_b32 v130,0x380,v224\n"
MF"v[24:27],a[8:11],v[200:203],0\n"
"s_lshl_b32 s51,s51,7\n"
RD"v[192:195],v242 offset:8192\n"
"v_fmac_f32 v188,v164,v184\n"
MF"v[24:27],a[24:27],v[204:207],v[24:27]\n"
SA"s50,s50,s51\n"
RT"a[34:35],v246 offset:8704\n"
"v_lshl_add_u32 v129,v130,1,v129\n"
MF"v[28:31],a[28:31],v[204:207],0\n"
SA"s50,s50,524288\n"
BL"v236,s[12:15],s48 offen offset:2048 lds\n"
MF"v[28:31],a[12:15],v[200:203],v[28:31]\n"
"s_lshl_b32 s52,s52,1\n"
RT"a[36:37],v247 offset:8192\n"
"v_fmac_f32 v189,v165,v185\n"
MF"v[32:35],a[0:3],v[208:211],0\n"
SA"s52,s52,s50\n"
RD"v[196:199],v243 offset:8192\n"
"v_lshlrev_b32 v166,16,v147\n"
MF"v[32:35],a[16:19],v[212:215],v[32:35]\n"
SA"s52,s52,16384\n"
BL"v237,s[12:15],s48 offen offset:3072 lds\n"
MF"v[36:39],a[20:23],v[212:215],0\n"
SA"m0,s37,16384\n"
RT"a[38:39],v247 offset:8704\n"
"v_and_b32 v167,0xffff0000,v147\n"
MF"v[36:39],a[4:7],v[208:211],v[36:39]\n"
"s_bfe_u32 s48,s46,0x30008\n"
RD"v[200:203],v242 offset:10240\n"
"v_fmac_f32 v188,v166,v186\n"
MF"v[40:43],a[8:11],v[208:211],0\n"
"s_lshl_b32 s48,s48,19\n"
RT"a[40:41],v248 offset:8192\n"
"v_fmac_f32 v189,v167,v187\n"
MF"v[40:43],a[24:27],v[212:215],v[40:43]\n"
"s_lshl_b32 s51,s30,11\n"
BL"v238,s[16:19],s48 offen offset:0 lds\n"
MF"v[44:47],a[28:31],v[212:215],0\n"
SA"s50,s50,s51\n"
RD"v[204:207],v243 offset:10240\n"
"v_add_f32 v188,v188,v189\n"
MF"v[44:47],a[12:15],v[208:211],v[44:47]\n"
WT"lgkmcnt(4)\n"
BS"v[132:135],v129,s[8:11],s50 offen sc1\n"
MF"v[48:51],a[0:3],v[216:219],0\n"
SA"s51,s50,8192\n"
RT"a[42:43],v248 offset:8704\n"
"v_add_f32_dpp v188,v188,v188 quad_perm:[1,0,3,2] row_mask:0xf bank_mask:0xf\n"
MF"v[48:51],a[16:19],v[220:223],v[48:51]\n"
"s_lshl_b32 s90,s31,2\n"
BL"v239,s[16:19],s48 offen offset:1024 lds\n"
MF"v[52:55],a[20:23],v[220:223],0\n"
SA"s90,s90,s30\n"
BS"a[240:243],v129,s[8:11],s51 offen sc1\n"
MF"v[52:55],a[4:7],v[216:219],v[52:55]\n"
"s_lshl_b32 s90,s90,1\n"
RD"v[208:211],v242 offset:12288\n"
"v_add_f32_dpp v188,v188,v188 quad_perm:[2,3,0,1] row_mask:0xf bank_mask:0xf\n"
MF"v[56:59],a[8:11],v[216:219],0\n"
"s_sub_u32 s91,2047,s90\n"
RT"a[44:45],v249 offset:8192\n"
"v_add_f32_dpp v188,v188,v188 row_half_mirror row_mask:0xf bank_mask:0xf\n"
MF"v[56:59],a[24:27],v[220:223],v[56:59]\n"
BS"a[244:247],v129,s[8:11],s52 offen sc1\n"
MF"v[60:63],a[28:31],v[220:223],0\n"
RD"v[212:215],v243 offset:12288\n"
"v_add_f32_dpp v188,v188,v188 row_mirror row_mask:0xf bank_mask:0xf\n"
MF"v[60:63],a[12:15],v[216:219],v[60:63]\n"
RT"a[46:47],v249 offset:8704\n"
"v_mov_b32 v189,v188\n"
MF"v[64:67],a[0:3],v[192:195],0\n"
BL"v240,s[16:19],s48 offen offset:2048 lds\n"
MF"v[64:67],a[16:19],v[196:199],v[64:67]\n"
RD"v[216:219],v242 offset:14336\n"
"v_permlane16_swap_b32 v189,v188\n"
MF"v[68:71],a[20:23],v[196:199],0\n"
"s_lshl_b32 s90,s90,2\n"
WT"vmcnt(10)\n"
RT"a[48:49],v246 offset:12288\n"
MF"v[68:71],a[4:7],v[192:195],v[68:71]\n"
SA"s90,s90,8192\n"
BL"v241,s[16:19],s48 offen offset:3072 lds\n"
MF"v[72:75],a[8:11],v[192:195],0\n"
SA"m0,s37,20480\n"
RT"a[50:51],v246 offset:12800\n"
"v_add_f32 v188,v188,v189\n"
MF"v[72:75],a[24:27],v[196:199],v[72:75]\n"
"s_bfe_u32 s48,s46,0x30008\n"
RD"v[220:223],v243 offset:14336\n"
PK"v188,v188,v188\n"
MF"v[76:79],a[28:31],v[196:199],0\n"
"s_lshl_b32 s48,s48,19\n"
RT"a[52:53],v247 offset:12288\n"
"v_lshlrev_b32 v188,16,v188\n"
MF"v[76:79],a[12:15],v[192:195],v[76:79]\n"
WT"lgkmcnt(5)\n"
"v_ashrrev_i32 v189,31,v188\n"
"v_or_b32 v189,0x80000000,v189\n"
MF"v[80:83],a[0:3],v[200:203],0\n"
"s_bitset1_b32 s48,18\n"
"v_xor_b32 v188,v188,v189\n"
"v_and_b32 v188,0xffff0000,v188\n"
MF"v[80:83],a[16:19],v[204:207],v[80:83]\n"
BL"v238,s[16:19],s48 offen offset:0 lds\n"
MF"v[84:87],a[20:23],v[204:207],0\n"
RT"a[54:55],v247 offset:12800\n"
"v_lshrrev_b32 v189,9,v224\n"
MF"v[84:87],a[4:7],v[200:203],v[84:87]\n"
BL"v239,s[16:19],s48 offen offset:1024 lds\n"
MF"v[88:91],a[8:11],v[200:203],0\n"
WT"vmcnt(9) lgkmcnt(2)\n"
RT"a[56:57],v248 offset:12288\n"
"v_sub_u32 v190,s91,v189\n"
MF"v[88:91],a[24:27],v[204:207],v[88:91]\n"
"s_barrier\n"
MF"v[92:95],a[28:31],v[204:207],0\n"
RD"v[192:195],v242 offset:16384\n"
"v_or_b32 v188,v188,v190\n"
MF"v[92:95],a[12:15],v[200:203],v[92:95]\n"
RT"a[58:59],v248 offset:12800\n"
"v_and_b32 v190,0x1f0,v224\n"
MF"v[96:99],a[0:3],v[208:211],0\n"
BL"v240,s[16:19],s48 offen offset:2048 lds\n"
MF"v[96:99],a[16:19],v[212:215],v[96:99]\n"
"v_lshlrev_b32 v190,23,v190\n"
"v_lshl_add_u32 v190,v189,2,v190\n"
MF"v[100:103],a[20:23],v[212:215],0\n"
"buffer_store_dword v188,v190,s[8:11],s90 offen sc1\n"
MF"v[100:103],a[4:7],v[208:211],v[100:103]\n"
RD"v[196:199],v243 offset:16384\n"
MF"v[104:107],a[8:11],v[208:211],0\n"
RT"a[60:61],v249 offset:12288\n"
MF"v[104:107],a[24:27],v[212:215],v[104:107]\n"
BL"v241,s[16:19],s48 offen offset:3072 lds\n"
MF"v[108:111],a[28:31],v[212:215],0\n"
SA"m0,s36,32768\n"
RT"a[62:63],v249 offset:12800\n"
"s_bfe_u32 s48,s46,0x30008\n"
MF"v[108:111],a[12:15],v[208:211],v[108:111]\n"
"s_lshl_b32 s48,s48,7\n"
RD"v[200:203],v242 offset:18432\n"
MF"v[112:115],a[0:3],v[216:219],0\n"
BL"v234,s[12:15],s48 offen offset:0 lds\n"
MF"v[112:115],a[16:19],v[220:223],v[112:115]\n"
RD"v[204:207],v243 offset:18432\n"
MF"v[116:119],a[20:23],v[220:223],0\n"
MF"v[116:119],a[4:7],v[216:219],v[116:119]\n"
MF"v[120:123],a[8:11],v[216:219],0\n"
BL"v235,s[12:15],s48 offen offset:1024 lds\n"
MF"v[120:123],a[24:27],v[220:223],v[120:123]\n"
RD"v[208:211],v242 offset:20480\n"
MF"v[124:127],a[28:31],v[220:223],0\n"
MF"v[124:127],a[12:15],v[216:219],v[124:127]\n"
WT"lgkmcnt(2)\n"
MF"v[0:3],a[32:35],v[192:195],v[0:3]\n"
RD"v[212:215],v243 offset:20480\n"
MF"v[0:3],a[48:51],v[196:199],v[0:3]\n"
BL"v236,s[12:15],s48 offen offset:2048 lds\n"
MF"v[4:7],a[52:55],v[196:199],v[4:7]\n"
MF"v[4:7],a[36:39],v[192:195],v[4:7]\n"
MF"v[8:11],a[40:43],v[192:195],v[8:11]\n"
RD"v[216:219],v242 offset:22528\n"
MF"v[8:11],a[56:59],v[196:199],v[8:11]\n"
BL"v237,s[12:15],s48 offen offset:3072 lds\n"
MF"v[12:15],a[60:63],v[196:199],v[12:15]\n"
MF"v[12:15],a[44:47],v[192:195],v[12:15]\n"
RD"v[220:223],v243 offset:22528\n"
MF"v[16:19],a[32:35],v[200:203],v[16:19]\n"
WT"lgkmcnt(2)\n"
WT"vmcnt(9)\n"
RT"a[64:65],v246 offset:16384\n"
MF"v[16:19],a[48:51],v[204:207],v[16:19]\n"
SA"m0,s37,0\n"
"s_bfe_u32 s48,s46,0x3000c\n"
"s_lshl_b32 s48,s48,19\n"
MF"v[20:23],a[52:55],v[204:207],v[20:23]\n"
BL"v238,s[16:19],s48 offen offset:0 lds\n"
MF"v[20:23],a[36:39],v[200:203],v[20:23]\n"
RD"v[192:195],v242 offset:24576\n"
MF"v[24:27],a[40:43],v[200:203],v[24:27]\n"
RT"a[66:67],v246 offset:16896\n"
MF"v[24:27],a[56:59],v[204:207],v[24:27]\n"
MF"v[28:31],a[60:63],v[204:207],v[28:31]\n"
RT"a[68:69],v247 offset:16384\n"
MF"v[28:31],a[44:47],v[200:203],v[28:31]\n"
BL"v239,s[16:19],s48 offen offset:1024 lds\n"
MF"v[32:35],a[32:35],v[208:211],v[32:35]\n"
RD"v[196:199],v243 offset:24576\n"
MF"v[32:35],a[48:51],v[212:215],v[32:35]\n"
RT"a[70:71],v247 offset:16896\n"
MF"v[36:39],a[52:55],v[212:215],v[36:39]\n"
RD"v[200:203],v242 offset:26624\n"
MF"v[36:39],a[36:39],v[208:211],v[36:39]\n"
BL"v240,s[16:19],s48 offen offset:2048 lds\n"
MF"v[40:43],a[40:43],v[208:211],v[40:43]\n"
RT"a[72:73],v248 offset:16384\n"
MF"v[40:43],a[56:59],v[212:215],v[40:43]\n"
MF"v[44:47],a[60:63],v[212:215],v[44:47]\n"
RD"v[204:207],v243 offset:26624\n"
MF"v[44:47],a[44:47],v[208:211],v[44:47]\n"
WT"lgkmcnt(4)\n"
RT"a[74:75],v248 offset:16896\n"
MF"v[48:51],a[32:35],v[216:219],v[48:51]\n"
BL"v241,s[16:19],s48 offen offset:3072 lds\n"
MF"v[48:51],a[48:51],v[220:223],v[48:51]\n"
RD"v[208:211],v242 offset:28672\n"
MF"v[52:55],a[52:55],v[220:223],v[52:55]\n"
SA"m0,s37,4096\n"
RT"a[76:77],v249 offset:16384\n"
"s_bfe_u32 s48,s46,0x3000c\n"
MF"v[52:55],a[36:39],v[216:219],v[52:55]\n"
"s_lshl_b32 s48,s48,19\n"
"s_bitset1_b32 s48,18\n"
MF"v[56:59],a[40:43],v[216:219],v[56:59]\n"
BL"v238,s[16:19],s48 offen offset:0 lds\n"
MF"v[56:59],a[56:59],v[220:223],v[56:59]\n"
RD"v[212:215],v243 offset:28672\n"
MF"v[60:63],a[60:63],v[220:223],v[60:63]\n"
RT"a[78:79],v249 offset:16896\n"
MF"v[60:63],a[44:47],v[216:219],v[60:63]\n"
MF"v[64:67],a[32:35],v[192:195],v[64:67]\n"
RD"v[216:219],v242 offset:30720\n"
MF"v[64:67],a[48:51],v[196:199],v[64:67]\n"
WT"vmcnt(9)\n"
RT"a[80:81],v246 offset:20480\n"
MF"v[68:71],a[52:55],v[196:199],v[68:71]\n"
BL"v239,s[16:19],s48 offen offset:1024 lds\n"
MF"v[68:71],a[36:39],v[192:195],v[68:71]\n"
RT"a[82:83],v246 offset:20992\n"
MF"v[72:75],a[40:43],v[192:195],v[72:75]\n"
RD"v[220:223],v243 offset:30720\n"
MF"v[72:75],a[56:59],v[196:199],v[72:75]\n"
BL"v240,s[16:19],s48 offen offset:2048 lds\n"
MF"v[76:79],a[60:63],v[196:199],v[76:79]\n"
RT"a[84:85],v247 offset:20480\n"
MF"v[76:79],a[44:47],v[192:195],v[76:79]\n"
WT"lgkmcnt(4)\n"
MF"v[80:83],a[32:35],v[200:203],v[80:83]\n"
"s_lshl_b32 s51,s32,8\n"
"s_andn2_b32 s50,s31,7\n"
SA"s50,s50,s51\n"
MF"v[80:83],a[48:51],v[204:207],v[80:83]\n"
SA"s50,s50,256\n"
RT"a[86:87],v247 offset:20992\n"
SA"s50,s50,s45\n"
MF"v[84:87],a[52:55],v[204:207],v[84:87]\n"
BL"v241,s[16:19],s48 offen offset:3072 lds\n"
MF"v[84:87],a[36:39],v[200:203],v[84:87]\n"
MF"v[88:91],a[40:43],v[200:203],v[88:91]\n"
WT"vmcnt(8) lgkmcnt(2)\n"
RT"a[88:89],v248 offset:20480\n"
SA"m0,s36,0\n"
MF"v[88:91],a[56:59],v[204:207],v[88:91]\n"
"s_bfe_u32 s48,s46,0x3000c\n"
"s_barrier\n"
MF"v[92:95],a[60:63],v[204:207],v[92:95]\n"
"s_lshl_b32 s48,s48,7\n"
RD"v[192:195],v242 offset:32768\n"
MF"v[92:95],a[44:47],v[200:203],v[92:95]\n"
BL"v234,s[12:15],s48 offen offset:0 lds\n"
MF"v[96:99],a[32:35],v[208:211],v[96:99]\n"
"buffer_store_dwordx2 v[232:233],v230,s[8:11],s50 offen sc1\n"
MF"v[96:99],a[48:51],v[212:215],v[96:99]\n"
RT"a[90:91],v248 offset:20992\n"
MF"v[100:103],a[52:55],v[212:215],v[100:103]\n"
RD"v[196:199],v243 offset:32768\n"
MF"v[100:103],a[36:39],v[208:211],v[100:103]\n"
RT"a[92:93],v249 offset:20480\n"
MF"v[104:107],a[40:43],v[208:211],v[104:107]\n"
BL"v235,s[12:15],s48 offen offset:1024 lds\n"
MF"v[104:107],a[56:59],v[212:215],v[104:107]\n"
RT"a[94:95],v249 offset:20992\n"
MF"v[108:111],a[60:63],v[212:215],v[108:111]\n"
RD"v[200:203],v242 offset:34816\n"
MF"v[108:111],a[44:47],v[208:211],v[108:111]\n"
BL"v236,s[12:15],s48 offen offset:2048 lds\n"
MF"v[112:115],a[32:35],v[216:219],v[112:115]\n"
MF"v[112:115],a[48:51],v[220:223],v[112:115]\n"
RD"v[204:207],v243 offset:34816\n"
MF"v[116:119],a[52:55],v[220:223],v[116:119]\n"
MF"v[116:119],a[36:39],v[216:219],v[116:119]\n"
BL"v237,s[12:15],s48 offen offset:3072 lds\n"
MF"v[120:123],a[40:43],v[216:219],v[120:123]\n"
MF"v[120:123],a[56:59],v[220:223],v[120:123]\n"
RD"v[208:211],v242 offset:36864\n"
MF"v[124:127],a[60:63],v[220:223],v[124:127]\n"
SA"m0,s37,8192\n"
"s_bfe_u32 s48,s46,0x30010\n"
"s_lshl_b32 s48,s48,19\n"
MF"v[124:127],a[44:47],v[216:219],v[124:127]\n"
WT"lgkmcnt(2)\n"
MF"v[0:3],a[64:67],v[192:195],v[0:3]\n"
RD"v[212:215],v243 offset:36864\n"
MF"v[0:3],a[80:83],v[196:199],v[0:3]\n"
BL"v238,s[16:19],s48 offen offset:0 lds\n"
MF"v[4:7],a[84:87],v[196:199],v[4:7]\n"
MF"v[4:7],a[68:71],v[192:195],v[4:7]\n"
MF"v[8:11],a[72:75],v[192:195],v[8:11]\n"
RD"v[216:219],v242 offset:38912\n"
MF"v[8:11],a[88:91],v[196:199],v[8:11]\n"
BL"v239,s[16:19],s48 offen offset:1024 lds\n"
MF"v[12:15],a[92:95],v[196:199],v[12:15]\n"
MF"v[12:15],a[76:79],v[192:195],v[12:15]\n"
RD"v[220:223],v243 offset:38912\n"
MF"v[16:19],a[64:67],v[200:203],v[16:19]\n"
WT"lgkmcnt(2)\n"
WT"vmcnt(11)\n"
RT"a[96:97],v246 offset:0\n"
MF"v[16:19],a[80:83],v[204:207],v[16:19]\n"
BL"v240,s[16:19],s48 offen offset:2048 lds\n"
MF"v[20:23],a[84:87],v[204:207],v[20:23]\n"
MF"v[20:23],a[68:71],v[200:203],v[20:23]\n"
RD"v[192:195],v242 offset:40960\n"
MF"v[24:27],a[72:75],v[200:203],v[24:27]\n"
RT"a[98:99],v246 offset:512\n"
MF"v[24:27],a[88:91],v[204:207],v[24:27]\n"
BL"v241,s[16:19],s48 offen offset:3072 lds\n"
MF"v[28:31],a[92:95],v[204:207],v[28:31]\n"
RT"a[100:101],v247 offset:0\n"
MF"v[28:31],a[76:79],v[200:203],v[28:31]\n"
RD"v[196:199],v243 offset:40960\n"
MF"v[32:35],a[64:67],v[208:211],v[32:35]\n"
SA"m0,s37,12288\n"
"s_bfe_u32 s48,s46,0x30010\n"
"s_lshl_b32 s48,s48,19\n"
MF"v[32:35],a[80:83],v[212:215],v[32:35]\n"
"s_bitset1_b32 s48,18\n"
RT"a[102:103],v247 offset:512\n"
MF"v[36:39],a[84:87],v[212:215],v[36:39]\n"
RD"v[200:203],v242 offset:43008\n"
MF"v[36:39],a[68:71],v[208:211],v[36:39]\n"
BL"v238,s[16:19],s48 offen offset:0 lds\n"
MF"v[40:43],a[72:75],v[208:211],v[40:43]\n"
RT"a[104:105],v248 offset:0\n"
MF"v[40:43],a[88:91],v[212:215],v[40:43]\n"
MF"v[44:47],a[92:95],v[212:215],v[44:47]\n"
RD"v[204:207],v243 offset:43008\n"
MF"v[44:47],a[76:79],v[208:211],v[44:47]\n"
WT"lgkmcnt(4)\n"
BL"v239,s[16:19],s48 offen offset:1024 lds\n"
MF"v[48:51],a[64:67],v[216:219],v[48:51]\n"
RT"a[106:107],v248 offset:512\n"
MF"v[48:51],a[80:83],v[220:223],v[48:51]\n"
RD"v[208:211],v242 offset:45056\n"
MF"v[52:55],a[84:87],v[220:223],v[52:55]\n"
RT"a[108:109],v249 offset:0\n"
MF"v[52:55],a[68:71],v[216:219],v[52:55]\n"
BL"v240,s[16:19],s48 offen offset:2048 lds\n"
MF"v[56:59],a[72:75],v[216:219],v[56:59]\n"
MF"v[56:59],a[88:91],v[220:223],v[56:59]\n"
RD"v[212:215],v243 offset:45056\n"
MF"v[60:63],a[92:95],v[220:223],v[60:63]\n"
RT"a[110:111],v249 offset:512\n"
MF"v[60:63],a[76:79],v[216:219],v[60:63]\n"
BL"v241,s[16:19],s48 offen offset:3072 lds\n"
MF"v[64:67],a[64:67],v[192:195],v[64:67]\n"
RD"v[216:219],v242 offset:47104\n"
MF"v[64:67],a[80:83],v[196:199],v[64:67]\n"
WT"vmcnt(13)\n"
RT"a[112:113],v246 offset:4096\n"
MF"v[68:71],a[84:87],v[196:199],v[68:71]\n"
SA"m0,s36,16384\n"
"s_bfe_u32 s48,s46,0x30010\n"
"s_lshl_b32 s48,s48,7\n"
MF"v[68:71],a[68:71],v[192:195],v[68:71]\n"
RT"a[114:115],v246 offset:4608\n"
MF"v[72:75],a[72:75],v[192:195],v[72:75]\n"
RD"v[220:223],v243 offset:47104\n"
MF"v[72:75],a[88:91],v[196:199],v[72:75]\n"
BL"v234,s[12:15],s48 offen offset:0 lds\n"
MF"v[76:79],a[92:95],v[196:199],v[76:79]\n"
RT"a[116:117],v247 offset:4096\n"
MF"v[76:79],a[76:79],v[192:195],v[76:79]\n"
WT"lgkmcnt(4)\n"
MF"v[80:83],a[64:67],v[200:203],v[80:83]\n"
BL"v235,s[12:15],s48 offen offset:1024 lds\n"
MF"v[80:83],a[80:83],v[204:207],v[80:83]\n"
RT"a[118:119],v247 offset:4608\n"
MF"v[84:87],a[84:87],v[204:207],v[84:87]\n"
MF"v[84:87],a[68:71],v[200:203],v[84:87]\n"
MF"v[88:91],a[72:75],v[200:203],v[88:91]\n"
WT"vmcnt(10) lgkmcnt(2)\n"
RT"a[120:121],v248 offset:4096\n"
MF"v[88:91],a[88:91],v[204:207],v[88:91]\n"
"s_barrier\n"
MF"v[92:95],a[92:95],v[204:207],v[92:95]\n"
BL"v236,s[12:15],s48 offen offset:2048 lds\n"
MF"v[92:95],a[76:79],v[200:203],v[92:95]\n"
RD"v[192:195],v242 offset:0\n"
MF"v[96:99],a[64:67],v[208:211],v[96:99]\n"
RT"a[122:123],v248 offset:4608\n"
MF"v[96:99],a[80:83],v[212:215],v[96:99]\n"
BL"v237,s[12:15],s48 offen offset:3072 lds\n"
MF"v[100:103],a[84:87],v[212:215],v[100:103]\n"
RD"v[196:199],v243 offset:0\n"
MF"v[100:103],a[68:71],v[208:211],v[100:103]\n"
RT"a[124:125],v249 offset:4096\n"
MF"v[104:107],a[72:75],v[208:211],v[104:107]\n"
SA"m0,s37,16384\n"
"s_bfe_u32 s48,s46,0x30014\n"
"s_lshl_b32 s48,s48,19\n"
MF"v[104:107],a[88:91],v[212:215],v[104:107]\n"
RT"a[126:127],v249 offset:4608\n"
MF"v[108:111],a[92:95],v[212:215],v[108:111]\n"
RD"v[200:203],v242 offset:2048\n"
MF"v[108:111],a[76:79],v[208:211],v[108:111]\n"
BL"v238,s[16:19],s48 offen offset:0 lds\n"
MF"v[112:115],a[64:67],v[216:219],v[112:115]\n"
MF"v[112:115],a[80:83],v[220:223],v[112:115]\n"
RD"v[204:207],v243 offset:2048\n"
MF"v[116:119],a[84:87],v[220:223],v[116:119]\n"
BL"v239,s[16:19],s48 offen offset:1024 lds\n"
MF"v[116:119],a[68:71],v[216:219],v[116:119]\n"
MF"v[120:123],a[72:75],v[216:219],v[120:123]\n"
MF"v[120:123],a[88:91],v[220:223],v[120:123]\n"
RD"v[208:211],v242 offset:4096\n"
MF"v[124:127],a[92:95],v[220:223],v[124:127]\n"
MF"v[124:127],a[76:79],v[216:219],v[124:127]\n"
WT"lgkmcnt(2)\n"
BL"v240,s[16:19],s48 offen offset:2048 lds\n"
MF"v[0:3],a[96:99],v[192:195],v[0:3]\n"
RD"v[212:215],v243 offset:4096\n"
MF"v[0:3],a[112:115],v[196:199],v[0:3]\n"
MF"v[4:7],a[116:119],v[196:199],v[4:7]\n"
MF"v[4:7],a[100:103],v[192:195],v[4:7]\n"
BL"v241,s[16:19],s48 offen offset:3072 lds\n"
MF"v[8:11],a[104:107],v[192:195],v[8:11]\n"
RD"v[216:219],v242 offset:6144\n"
MF"v[8:11],a[120:123],v[196:199],v[8:11]\n"
MF"v[12:15],a[124:127],v[196:199],v[12:15]\n"
SA"m0,s37,20480\n"
"s_bfe_u32 s48,s46,0x30014\n"
"s_lshl_b32 s48,s48,19\n"
MF"v[12:15],a[108:111],v[192:195],v[12:15]\n"
"s_bitset1_b32 s48,18\n"
RD"v[220:223],v243 offset:6144\n"
MF"v[16:19],a[96:99],v[200:203],v[16:19]\n"
WT"lgkmcnt(2)\n"
WT"vmcnt(12)\n"
RT"a[128:129],v246 offset:8192\n"
MF"v[16:19],a[112:115],v[204:207],v[16:19]\n"
BL"v238,s[16:19],s48 offen offset:0 lds\n"
MF"v[20:23],a[116:119],v[204:207],v[20:23]\n"
MF"v[20:23],a[100:103],v[200:203],v[20:23]\n"
RD"v[192:195],v242 offset:8192\n"
MF"v[24:27],a[104:107],v[200:203],v[24:27]\n"
RT"a[130:131],v246 offset:8704\n"
MF"v[24:27],a[120:123],v[204:207],v[24:27]\n"
BL"v239,s[16:19],s48 offen offset:1024 lds\n"
MF"v[28:31],a[124:127],v[204:207],v[28:31]\n"
RT"a[132:133],v247 offset:8192\n"
MF"v[28:31],a[108:111],v[200:203],v[28:31]\n"
RD"v[196:199],v243 offset:8192\n"
MF"v[32:35],a[96:99],v[208:211],v[32:35]\n"
MF"v[32:35],a[112:115],v[212:215],v[32:35]\n"
RT"a[134:135],v247 offset:8704\n"
MF"v[36:39],a[116:119],v[212:215],v[36:39]\n"
BL"v240,s[16:19],s48 offen offset:2048 lds\n"
MF"v[36:39],a[100:103],v[208:211],v[36:39]\n"
RD"v[200:203],v242 offset:10240\n"
MF"v[40:43],a[104:107],v[208:211],v[40:43]\n"
RT"a[136:137],v248 offset:8192\n"
MF"v[40:43],a[120:123],v[212:215],v[40:43]\n"
BL"v241,s[16:19],s48 offen offset:3072 lds\n"
MF"v[44:47],a[124:127],v[212:215],v[44:47]\n"
RD"v[204:207],v243 offset:10240\n"
MF"v[44:47],a[108:111],v[208:211],v[44:47]\n"
WT"lgkmcnt(4)\n"
RT"a[138:139],v248 offset:8704\n"
MF"v[48:51],a[96:99],v[216:219],v[48:51]\n"
SA"m0,s36,32768\n"
"s_bfe_u32 s48,s46,0x30014\n"
"s_lshl_b32 s48,s48,7\n"
MF"v[48:51],a[112:115],v[220:223],v[48:51]\n"
RD"v[208:211],v242 offset:12288\n"
MF"v[52:55],a[116:119],v[220:223],v[52:55]\n"
RT"a[140:141],v249 offset:8192\n"
MF"v[52:55],a[100:103],v[216:219],v[52:55]\n"
BL"v234,s[12:15],s48 offen offset:0 lds\n"
MF"v[56:59],a[104:107],v[216:219],v[56:59]\n"
MF"v[56:59],a[120:123],v[220:223],v[56:59]\n"
RD"v[212:215],v243 offset:12288\n"
MF"v[60:63],a[124:127],v[220:223],v[60:63]\n"
RT"a[142:143],v249 offset:8704\n"
MF"v[60:63],a[108:111],v[216:219],v[60:63]\n"
BL"v235,s[12:15],s48 offen offset:1024 lds\n"
MF"v[64:67],a[96:99],v[192:195],v[64:67]\n"
RD"v[216:219],v242 offset:14336\n"
MF"v[64:67],a[112:115],v[196:199],v[64:67]\n"
WT"vmcnt(14)\n"
RT"a[144:145],v246 offset:12288\n"
MF"v[68:71],a[116:119],v[196:199],v[68:71]\n"
MF"v[68:71],a[100:103],v[192:195],v[68:71]\n"
RT"a[146:147],v246 offset:12800\n"
MF"v[72:75],a[104:107],v[192:195],v[72:75]\n"
BL"v236,s[12:15],s48 offen offset:2048 lds\n"
MF"v[72:75],a[120:123],v[196:199],v[72:75]\n"
RD"v[220:223],v243 offset:14336\n"
MF"v[76:79],a[124:127],v[196:199],v[76:79]\n"
RT"a[148:149],v247 offset:12288\n"
MF"v[76:79],a[108:111],v[192:195],v[76:79]\n"
WT"lgkmcnt(4)\n"
BL"v237,s[12:15],s48 offen offset:3072 lds\n"
MF"v[80:83],a[96:99],v[200:203],v[80:83]\n"
MF"v[80:83],a[112:115],v[204:207],v[80:83]\n"
RT"a[150:151],v247 offset:12800\n"
MF"v[84:87],a[116:119],v[204:207],v[84:87]\n"
SA"m0,s37,0\n"
"s_bfe_u32 s48,s46,0x30018\n"
"s_lshl_b32 s48,s48,19\n"
MF"v[84:87],a[100:103],v[200:203],v[84:87]\n"
MF"v[88:91],a[104:107],v[200:203],v[88:91]\n"
WT"vmcnt(12) lgkmcnt(2)\n"
RT"a[152:153],v248 offset:12288\n"
MF"v[88:91],a[120:123],v[204:207],v[88:91]\n"
BL"v238,s[16:19],s48 offen offset:0 lds\n"
MF"v[92:95],a[124:127],v[204:207],v[92:95]\n"
"s_barrier\n"
MF"v[92:95],a[108:111],v[200:203],v[92:95]\n"
RD"v[192:195],v242 offset:16384\n"
MF"v[96:99],a[96:99],v[208:211],v[96:99]\n"
RT"a[154:155],v248 offset:12800\n"
MF"v[96:99],a[112:115],v[212:215],v[96:99]\n"
BL"v239,s[16:19],s48 offen offset:1024 lds\n"
MF"v[100:103],a[116:119],v[212:215],v[100:103]\n"
RD"v[196:199],v243 offset:16384\n"
MF"v[100:103],a[100:103],v[208:211],v[100:103]\n"
RT"a[156:157],v249 offset:12288\n"
MF"v[104:107],a[104:107],v[208:211],v[104:107]\n"
MF"v[104:107],a[120:123],v[212:215],v[104:107]\n"
RT"a[158:159],v249 offset:12800\n"
MF"v[108:111],a[124:127],v[212:215],v[108:111]\n"
BL"v240,s[16:19],s48 offen offset:2048 lds\n"
MF"v[108:111],a[108:111],v[208:211],v[108:111]\n"
RD"v[200:203],v242 offset:18432\n"
MF"v[112:115],a[96:99],v[216:219],v[112:115]\n"
MF"v[112:115],a[112:115],v[220:223],v[112:115]\n"
RD"v[204:207],v243 offset:18432\n"
MF"v[116:119],a[116:119],v[220:223],v[116:119]\n"
BL"v241,s[16:19],s48 offen offset:3072 lds\n"
MF"v[116:119],a[100:103],v[216:219],v[116:119]\n"
MF"v[120:123],a[104:107],v[216:219],v[120:123]\n"
SA"m0,s37,4096\n"
"s_bfe_u32 s48,s46,0x30018\n"
"s_lshl_b32 s48,s48,19\n"
MF"v[120:123],a[120:123],v[220:223],v[120:123]\n"
"s_bitset1_b32 s48,18\n"
RD"v[208:211],v242 offset:20480\n"
MF"v[124:127],a[124:127],v[220:223],v[124:127]\n"
BL"v238,s[16:19],s48 offen offset:0 lds\n"
MF"v[124:127],a[108:111],v[216:219],v[124:127]\n"
WT"lgkmcnt(2)\n"
MF"v[0:3],a[128:131],v[192:195],v[0:3]\n"
RD"v[212:215],v243 offset:20480\n"
MF"v[0:3],a[144:147],v[196:199],v[0:3]\n"
MF"v[4:7],a[148:151],v[196:199],v[4:7]\n"
BL"v239,s[16:19],s48 offen offset:1024 lds\n"
MF"v[4:7],a[132:135],v[192:195],v[4:7]\n"
MF"v[8:11],a[136:139],v[192:195],v[8:11]\n"
RD"v[216:219],v242 offset:22528\n"
MF"v[8:11],a[152:155],v[196:199],v[8:11]\n"
MF"v[12:15],a[156:159],v[196:199],v[12:15]\n"
MF"v[12:15],a[140:143],v[192:195],v[12:15]\n"
RD"v[220:223],v243 offset:22528\n"
MF"v[16:19],a[128:131],v[200:203],v[16:19]\n"
WT"lgkmcnt(2)\n"
BL"v240,s[16:19],s48 offen offset:2048 lds\n"
MF"v[16:19],a[144:147],v[204:207],v[16:19]\n"
WT"vmcnt(15)\n"
RT"a[160:161],v246 offset:16384\n"
MF"v[20:23],a[148:151],v[204:207],v[20:23]\n"
MF"v[20:23],a[132:135],v[200:203],v[20:23]\n"
RD"v[192:195],v242 offset:24576\n"
MF"v[24:27],a[136:139],v[200:203],v[24:27]\n"
RT"a[162:163],v246 offset:16896\n"
MF"v[24:27],a[152:155],v[204:207],v[24:27]\n"
BL"v241,s[16:19],s48 offen offset:3072 lds\n"
MF"v[28:31],a[156:159],v[204:207],v[28:31]\n"
SA"m0,s36,0\n"
RT"a[164:165],v247 offset:16384\n"
"s_bfe_u32 s48,s46,0x30018\n"
MF"v[28:31],a[140:143],v[200:203],v[28:31]\n"
"s_lshl_b32 s48,s48,7\n"
RD"v[196:199],v243 offset:24576\n"
MF"v[32:35],a[128:131],v[208:211],v[32:35]\n"
BL"v234,s[12:15],s48 offen offset:0 lds\n"
MF"v[32:35],a[144:147],v[212:215],v[32:35]\n"
RT"a[166:167],v247 offset:16896\n"
MF"v[36:39],a[148:151],v[212:215],v[36:39]\n"
RD"v[200:203],v242 offset:26624\n"
MF"v[36:39],a[132:135],v[208:211],v[36:39]\n"
MF"v[40:43],a[136:139],v[208:211],v[40:43]\n"
RT"a[168:169],v248 offset:16384\n"
MF"v[40:43],a[152:155],v[212:215],v[40:43]\n"
BL"v235,s[12:15],s48 offen offset:1024 lds\n"
MF"v[44:47],a[156:159],v[212:215],v[44:47]\n"
RD"v[204:207],v243 offset:26624\n"
MF"v[44:47],a[140:143],v[208:211],v[44:47]\n"
WT"lgkmcnt(4)\n"
RT"a[170:171],v248 offset:16896\n"
MF"v[48:51],a[128:131],v[216:219],v[48:51]\n"
MF"v[48:51],a[144:147],v[220:223],v[48:51]\n"
RD"v[208:211],v242 offset:28672\n"
MF"v[52:55],a[148:151],v[220:223],v[52:55]\n"
BL"v236,s[12:15],s48 offen offset:2048 lds\n"
MF"v[52:55],a[132:135],v[216:219],v[52:55]\n"
RT"a[172:173],v249 offset:16384\n"
MF"v[56:59],a[136:139],v[216:219],v[56:59]\n"
MF"v[56:59],a[152:155],v[220:223],v[56:59]\n"
RD"v[212:215],v243 offset:28672\n"
MF"v[60:63],a[156:159],v[220:223],v[60:63]\n"
RT"a[174:175],v249 offset:16896\n"
MF"v[60:63],a[140:143],v[216:219],v[60:63]\n"
BL"v237,s[12:15],s48 offen offset:3072 lds\n"
MF"v[64:67],a[128:131],v[192:195],v[64:67]\n"
SA"m0,s37,8192\n"
RD"v[216:219],v242 offset:30720\n"
"s_bfe_u32 s48,s46,0x3001c\n"
MF"v[64:67],a[144:147],v[196:199],v[64:67]\n"
"s_lshl_b32 s48,s48,19\n"
WT"vmcnt(16)\n"
RT"a[176:177],v246 offset:20480\n"
MF"v[68:71],a[148:151],v[196:199],v[68:71]\n"
BL"v238,s[16:19],s48 offen offset:0 lds\n"
MF"v[68:71],a[132:135],v[192:195],v[68:71]\n"
RT"a[178:179],v246 offset:20992\n"
MF"v[72:75],a[136:139],v[192:195],v[72:75]\n"
RD"v[220:223],v243 offset:30720\n"
MF"v[72:75],a[152:155],v[196:199],v[72:75]\n"
MF"v[76:79],a[156:159],v[196:199],v[76:79]\n"
RT"a[180:181],v247 offset:20480\n"
MF"v[76:79],a[140:143],v[192:195],v[76:79]\n"
WT"lgkmcnt(4)\n"
BL"v239,s[16:19],s48 offen offset:1024 lds\n"
MF"v[80:83],a[128:131],v[200:203],v[80:83]\n"
MF"v[80:83],a[144:147],v[204:207],v[80:83]\n"
RT"a[182:183],v247 offset:20992\n"
MF"v[84:87],a[148:151],v[204:207],v[84:87]\n"
MF"v[84:87],a[132:135],v[200:203],v[84:87]\n"
BL"v240,s[16:19],s48 offen offset:2048 lds\n"
MF"v[88:91],a[136:139],v[200:203],v[88:91]\n"
WT"vmcnt(15) lgkmcnt(2)\n"
RT"a[184:185],v248 offset:20480\n"
MF"v[88:91],a[152:155],v[204:207],v[88:91]\n"
"s_barrier\n"
MF"v[92:95],a[156:159],v[204:207],v[92:95]\n"
RD"v[192:195],v242 offset:32768\n"
MF"v[92:95],a[140:143],v[200:203],v[92:95]\n"
RT"a[186:187],v248 offset:20992\n"
MF"v[96:99],a[128:131],v[208:211],v[96:99]\n"
BL"v241,s[16:19],s48 offen offset:3072 lds\n"
MF"v[96:99],a[144:147],v[212:215],v[96:99]\n"
MF"v[100:103],a[148:151],v[212:215],v[100:103]\n"
SA"m0,s37,12288\n"
RD"v[196:199],v243 offset:32768\n"
"s_bfe_u32 s48,s46,0x3001c\n"
MF"v[100:103],a[132:135],v[208:211],v[100:103]\n"
"s_lshl_b32 s48,s48,19\n"
RT"a[188:189],v249 offset:20480\n"
"s_bitset1_b32 s48,18\n"
MF"v[104:107],a[136:139],v[208:211],v[104:107]\n"
BL"v238,s[16:19],s48 offen offset:0 lds\n"
MF"v[104:107],a[152:155],v[212:215],v[104:107]\n"
RT"a[190:191],v249 offset:20992\n"
MF"v[108:111],a[156:159],v[212:215],v[108:111]\n"
RD"v[200:203],v242 offset:34816\n"
MF"v[108:111],a[140:143],v[208:211],v[108:111]\n"
MF"v[112:115],a[128:131],v[216:219],v[112:115]\n"
BL"v239,s[16:19],s48 offen offset:1024 lds\n"
MF"v[112:115],a[144:147],v[220:223],v[112:115]\n"
RD"v[204:207],v243 offset:34816\n"
MF"v[116:119],a[148:151],v[220:223],v[116:119]\n"
MF"v[116:119],a[132:135],v[216:219],v[116:119]\n"
MF"v[120:123],a[136:139],v[216:219],v[120:123]\n"
MF"v[120:123],a[152:155],v[220:223],v[120:123]\n"
RD"v[208:211],v242 offset:36864\n"
MF"v[124:127],a[156:159],v[220:223],v[124:127]\n"
BL"v240,s[16:19],s48 offen offset:2048 lds\n"
MF"v[124:127],a[140:143],v[216:219],v[124:127]\n"
WT"lgkmcnt(2)\n"
MF"v[0:3],a[160:163],v[192:195],v[0:3]\n"
RD"v[212:215],v243 offset:36864\n"
MF"v[0:3],a[176:179],v[196:199],v[0:3]\n"
BL"v241,s[16:19],s48 offen offset:3072 lds\n"
MF"v[4:7],a[180:183],v[196:199],v[4:7]\n"
MF"v[4:7],a[164:167],v[192:195],v[4:7]\n"
MF"v[8:11],a[168:171],v[192:195],v[8:11]\n"
SA"m0,s36,16384\n"
RD"v[216:219],v242 offset:38912\n"
"s_bfe_u32 s48,s46,0x3001c\n"
MF"v[8:11],a[184:187],v[196:199],v[8:11]\n"
"s_lshl_b32 s48,s48,7\n"
MF"v[12:15],a[188:191],v[196:199],v[12:15]\n"
BL"v234,s[12:15],s48 offen offset:0 lds\n"
MF"v[12:15],a[172:175],v[192:195],v[12:15]\n"
RD"v[220:223],v243 offset:38912\n"
MF"v[16:19],a[160:163],v[200:203],v[16:19]\n"
WT"lgkmcnt(2)\n"
WT"vmcnt(17)\n"
RT"a[192:193],v246 offset:0\n"
MF"v[16:19],a[176:179],v[204:207],v[16:19]\n"
MF"v[20:23],a[180:183],v[204:207],v[20:23]\n"
BL"v235,s[12:15],s48 offen offset:1024 lds\n"
MF"v[20:23],a[164:167],v[200:203],v[20:23]\n"
RD"v[192:195],v242 offset:40960\n"
MF"v[24:27],a[168:171],v[200:203],v[24:27]\n"
RT"a[194:195],v246 offset:512\n"
MF"v[24:27],a[184:187],v[204:207],v[24:27]\n"
MF"v[28:31],a[188:191],v[204:207],v[28:31]\n"
RT"a[196:197],v247 offset:0\n"
MF"v[28:31],a[172:175],v[200:203],v[28:31]\n"
RD"v[196:199],v243 offset:40960\n"
MF"v[32:35],a[160:163],v[208:211],v[32:35]\n"
BL"v236,s[12:15],s48 offen offset:2048 lds\n"
MF"v[32:35],a[176:179],v[212:215],v[32:35]\n"
RT"a[198:199],v247 offset:512\n"
MF"v[36:39],a[180:183],v[212:215],v[36:39]\n"
RD"v[200:203],v242 offset:43008\n"
MF"v[36:39],a[164:167],v[208:211],v[36:39]\n"
BL"v237,s[12:15],s48 offen offset:3072 lds\n"
MF"v[40:43],a[168:171],v[208:211],v[40:43]\n"
RT"a[200:201],v248 offset:0\n"
MF"v[40:43],a[184:187],v[212:215],v[40:43]\n"
MF"v[44:47],a[188:191],v[212:215],v[44:47]\n"
SA"m0,s37,16384\n"
RD"v[204:207],v243 offset:43008\n"
SA"s48,s39,0\n"
MF"v[44:47],a[172:175],v[208:211],v[44:47]\n"
WT"lgkmcnt(4)\n"
RT"a[202:203],v248 offset:512\n"
"s_lshr_b32 s50,s30,1\n"
MF"v[48:51],a[160:163],v[216:219],v[48:51]\n"
"s_and_b32 s51,s30,1\n"
BL"v234,s[12:15],s48 offen offset:0 lds\n"
MF"v[48:51],a[176:179],v[220:223],v[48:51]\n"
"s_mul_i32 s38,s50,24576\n"
RD"v[208:211],v242 offset:45056\n"
MF"v[52:55],a[180:183],v[220:223],v[52:55]\n"
"s_lshl_b32 s60,s50,18\n"
RT"a[204:205],v249 offset:0\n"
MF"v[52:55],a[164:167],v[216:219],v[52:55]\n"
"s_lshl_b32 s50,s51,12\n"
MF"v[56:59],a[168:171],v[216:219],v[56:59]\n"
SA"s38,s38,s50\n"
BL"v235,s[12:15],s48 offen offset:1024 lds\n"
MF"v[56:59],a[184:187],v[220:223],v[56:59]\n"
"s_lshl_b32 s50,s51,7\n"
RD"v[212:215],v243 offset:45056\n"
MF"v[60:63],a[188:191],v[220:223],v[60:63]\n"
SA"s60,s60,s50\n"
RT"a[206:207],v249 offset:512\n"
MF"v[60:63],a[172:175],v[216:219],v[60:63]\n"
MF"v[64:67],a[160:163],v[192:195],v[64:67]\n"
RD"v[216:219],v242 offset:47104\n"
MF"v[64:67],a[176:179],v[196:199],v[64:67]\n"
"s_sub_u32 s99,s34,5\n"
WT"vmcnt(18)\n"
RT"a[208:209],v246 offset:4096\n"
MF"v[68:71],a[180:183],v[196:199],v[68:71]\n"
"s_cmp_lt_u32 s34,5\n"
BL"v236,s[12:15],s48 offen offset:2048 lds\n"
MF"v[68:71],a[164:167],v[192:195],v[68:71]\n"
"s_cselect_b32 s99,s34,s99\n"
RT"a[210:211],v246 offset:4608\n"
"s_mul_i32 s99,s99,1\n"
MF"v[72:75],a[168:171],v[192:195],v[72:75]\n"
"s_lshl_b32 s61,s99,20\n"
RD"v[220:223],v243 offset:47104\n"
MF"v[72:75],a[184:187],v[196:199],v[72:75]\n"
BL"v237,s[12:15],s48 offen offset:3072 lds\n"
MF"v[76:79],a[188:191],v[196:199],v[76:79]\n"
RT"a[212:213],v247 offset:4096\n"
MF"v[76:79],a[172:175],v[192:195],v[76:79]\n"
WT"lgkmcnt(4)\n"
MF"v[80:83],a[160:163],v[200:203],v[80:83]\n"
SA"m0,s37,20480\n"
SA"s48,s98,0\n"
MF"v[80:83],a[176:179],v[204:207],v[80:83]\n"
RT"a[214:215],v247 offset:4608\n"
MF"v[84:87],a[180:183],v[204:207],v[84:87]\n"
BL"v234,s[12:15],s48 offen offset:0 lds\n"
MF"v[84:87],a[164:167],v[200:203],v[84:87]\n"
MF"v[88:91],a[168:171],v[200:203],v[88:91]\n"
WT"vmcnt(17) lgkmcnt(2)\n"
RT"a[216:217],v248 offset:4096\n"
MF"v[88:91],a[184:187],v[204:207],v[88:91]\n"
"s_barrier\n"
MF"v[92:95],a[188:191],v[204:207],v[92:95]\n"
RD"v[192:195],v242 offset:0\n"
MF"v[92:95],a[172:175],v[200:203],v[92:95]\n"
BL"v235,s[12:15],s48 offen offset:1024 lds\n"
MF"v[96:99],a[160:163],v[208:211],v[96:99]\n"
RT"a[218:219],v248 offset:4608\n"
MF"v[96:99],a[176:179],v[212:215],v[96:99]\n"
MF"v[100:103],a[180:183],v[212:215],v[100:103]\n"
RD"v[196:199],v243 offset:0\n"
MF"v[100:103],a[164:167],v[208:211],v[100:103]\n"
RT"a[220:221],v249 offset:4096\n"
MF"v[104:107],a[168:171],v[208:211],v[104:107]\n"
BL"v236,s[12:15],s48 offen offset:2048 lds\n"
MF"v[104:107],a[184:187],v[212:215],v[104:107]\n"
RT"a[222:223],v249 offset:4608\n"
MF"v[108:111],a[188:191],v[212:215],v[108:111]\n"
RD"v[200:203],v242 offset:2048\n"
MF"v[108:111],a[172:175],v[208:211],v[108:111]\n"
BL"v237,s[12:15],s48 offen offset:3072 lds\n"
MF"v[112:115],a[160:163],v[216:219],v[112:115]\n"
MF"v[112:115],a[176:179],v[220:223],v[112:115]\n"
RD"v[204:207],v243 offset:2048\n"
MF"v[116:119],a[180:183],v[220:223],v[116:119]\n"
SA"m0,s37,0\n"
SA"s48,s39,32768\n"
MF"v[116:119],a[164:167],v[216:219],v[116:119]\n"
MF"v[120:123],a[168:171],v[216:219],v[120:123]\n"
BL"v234,s[12:15],s48 offen offset:0 lds\n"
MF"v[120:123],a[184:187],v[220:223],v[120:123]\n"
RD"v[208:211],v242 offset:4096\n"
MF"v[124:127],a[188:191],v[220:223],v[124:127]\n"
MF"v[124:127],a[172:175],v[216:219],v[124:127]\n"
WT"lgkmcnt(2)\n"
MF"v[0:3],a[192:195],v[192:195],v[0:3]\n"
RD"v[212:215],v243 offset:4096\n"
MF"v[0:3],a[208:211],v[196:199],v[0:3]\n"
BL"v235,s[12:15],s48 offen offset:1024 lds\n"
MF"v[4:7],a[212:215],v[196:199],v[4:7]\n"
MF"v[4:7],a[196:199],v[192:195],v[4:7]\n"
MF"v[8:11],a[200:203],v[192:195],v[8:11]\n"
RD"v[216:219],v242 offset:6144\n"
MF"v[8:11],a[216:219],v[196:199],v[8:11]\n"
BL"v236,s[12:15],s48 offen offset:2048 lds\n"
MF"v[12:15],a[220:223],v[196:199],v[12:15]\n"
MF"v[12:15],a[204:207],v[192:195],v[12:15]\n"
RD"v[220:223],v243 offset:6144\n"
MF"v[16:19],a[192:195],v[200:203],v[16:19]\n"
WT"lgkmcnt(2)\n"
WT"vmcnt(19)\n"
RT"a[224:225],v246 offset:8192\n"
MF"v[16:19],a[208:211],v[204:207],v[16:19]\n"
BL"v237,s[12:15],s48 offen offset:3072 lds\n"
MF"v[20:23],a[212:215],v[204:207],v[20:23]\n"
MF"v[20:23],a[196:199],v[200:203],v[20:23]\n"
RD"v[192:195],v242 offset:8192\n"
MF"v[24:27],a[200:203],v[200:203],v[24:27]\n"
SA"m0,s37,4096\n"
RT"a[226:227],v246 offset:8704\n"
SA"s48,s98,32768\n"
MF"v[24:27],a[216:219],v[204:207],v[24:27]\n"
MF"v[28:31],a[220:223],v[204:207],v[28:31]\n"
RT"a[228:229],v247 offset:8192\n"
MF"v[28:31],a[204:207],v[200:203],v[28:31]\n"
BL"v234,s[12:15],s48 offen offset:0 lds\n"
MF"v[32:35],a[192:195],v[208:211],v[32:35]\n"
RD"v[196:199],v243 offset:8192\n"
MF"v[32:35],a[208:211],v[212:215],v[32:35]\n"
RT"a[230:231],v247 offset:8704\n"
MF"v[36:39],a[212:215],v[212:215],v[36:39]\n"
RD"v[200:203],v242 offset:10240\n"
MF"v[36:39],a[196:199],v[208:211],v[36:39]\n"
BL"v235,s[12:15],s48 offen offset:1024 lds\n"
MF"v[40:43],a[200:203],v[208:211],v[40:43]\n"
RT"a[232:233],v248 offset:8192\n"
MF"v[40:43],a[216:219],v[212:215],v[40:43]\n"
MF"v[44:47],a[220:223],v[212:215],v[44:47]\n"
RD"v[204:207],v243 offset:10240\n"
MF"v[44:47],a[204:207],v[208:211],v[44:47]\n"
WT"lgkmcnt(4)\n"
RT"a[234:235],v248 offset:8704\n"
MF"v[48:51],a[192:195],v[216:219],v[48:51]\n"
BL"v236,s[12:15],s48 offen offset:2048 lds\n"
MF"v[48:51],a[208:211],v[220:223],v[48:51]\n"
RD"v[208:211],v242 offset:12288\n"
MF"v[52:55],a[212:215],v[220:223],v[52:55]\n"
RT"a[236:237],v249 offset:8192\n"
MF"v[52:55],a[196:199],v[216:219],v[52:55]\n"
BL"v237,s[12:15],s48 offen offset:3072 lds\n"
MF"v[56:59],a[200:203],v[216:219],v[56:59]\n"
MF"v[56:59],a[216:219],v[220:223],v[56:59]\n"
RD"v[212:215],v243 offset:12288\n"
MF"v[60:63],a[220:223],v[220:223],v[60:63]\n"
RT"a[238:239],v249 offset:8704\n"
MF"v[60:63],a[204:207],v[216:219],v[60:63]\n"
MF"v[64:67],a[192:195],v[192:195],v[64:67]\n"
RD"v[216:219],v242 offset:14336\n"
MF"v[64:67],a[208:211],v[196:199],v[64:67]\n"
SA"m0,s37,8192\n"
WT"vmcnt(20)\n"
RT"a[240:241],v246 offset:12288\n"
MF"v[68:71],a[212:215],v[196:199],v[68:71]\n"
SA"s48,s39,65536\n"
MF"v[68:71],a[196:199],v[192:195],v[68:71]\n"
RT"a[242:243],v246 offset:12800\n"
MF"v[72:75],a[200:203],v[192:195],v[72:75]\n"
WT"lgkmcnt(3)\n"
BL"v234,s[12:15],s48 offen offset:0 lds\n"
MF"v[72:75],a[216:219],v[196:199],v[72:75]\n"
RD"v[220:223],v243 offset:14336\n"
MF"v[76:79],a[220:223],v[196:199],v[76:79]\n"
RT"a[244:245],v247 offset:12288\n"
MF"v[76:79],a[204:207],v[192:195],v[76:79]\n"
BL"v235,s[12:15],s48 offen offset:1024 lds\n"
MF"v[80:83],a[192:195],v[200:203],v[80:83]\n"
MF"v[80:83],a[208:211],v[204:207],v[80:83]\n"
RT"a[246:247],v247 offset:12800\n"
MF"v[84:87],a[212:215],v[204:207],v[84:87]\n"
MF"v[84:87],a[196:199],v[200:203],v[84:87]\n"
MF"v[88:91],a[200:203],v[200:203],v[88:91]\n"
WT"vmcnt(18) lgkmcnt(2)\n"
RT"a[248:249],v248 offset:12288\n"
MF"v[88:91],a[216:219],v[204:207],v[88:91]\n"
BL"v236,s[12:15],s48 offen offset:2048 lds\n"
MF"v[92:95],a[220:223],v[204:207],v[92:95]\n"
"s_barrier\n"
MF"v[92:95],a[204:207],v[200:203],v[92:95]\n"
RD"v[192:195],v242 offset:16384\n"
MF"v[96:99],a[192:195],v[208:211],v[96:99]\n"
RT"a[250:251],v248 offset:12800\n"
MF"v[96:99],a[208:211],v[212:215],v[96:99]\n"
BL"v237,s[12:15],s48 offen offset:3072 lds\n"
MF"v[100:103],a[212:215],v[212:215],v[100:103]\n"
RD"v[196:199],v243 offset:16384\n"
MF"v[100:103],a[196:199],v[208:211],v[100:103]\n"
RT"a[252:253],v249 offset:12288\n"
MF"v[104:107],a[200:203],v[208:211],v[104:107]\n"
MF"v[104:107],a[216:219],v[212:215],v[104:107]\n"
RT"a[254:255],v249 offset:12800\n"
MF"v[108:111],a[220:223],v[212:215],v[108:111]\n"
RD"v[200:203],v242 offset:18432\n"
MF"v[108:111],a[204:207],v[208:211],v[108:111]\n"
MF"v[112:115],a[192:195],v[216:219],v[112:115]\n"
"s_mul_i32 s50,s35,1048576\n"
MF"v[112:115],a[208:211],v[220:223],v[112:115]\n"
"s_lshl_b32 s51,s34,15\n"
RD"v[204:207],v243 offset:18432\n"
SA"m0,s37,12288\n"
MF"v[116:119],a[212:215],v[220:223],v[116:119]\n"
SA"s48,s98,65536\n"
SA"s50,s50,s51\n"
MF"v[116:119],a[196:199],v[216:219],v[116:119]\n"
WT"lgkmcnt(2)\n"
BL"v234,s[12:15],s48 offen offset:0 lds\n"
MF"v[120:123],a[200:203],v[216:219],v[120:123]\n"
"s_and_b32 s51,s30,1\n"
"s_lshl_b32 s51,s51,11\n"
MF"v[120:123],a[216:219],v[220:223],v[120:123]\n"
SA"s50,s50,s51\n"
RD"v[208:211],v242 offset:20480\n"
MF"v[124:127],a[220:223],v[220:223],v[124:127]\n"
SA"s50,s50,9175040\n"
MF"v[124:127],a[204:207],v[216:219],v[124:127]\n"
BL"v235,s[12:15],s48 offen offset:1024 lds\n"
MF"v[0:3],a[224:227],v[192:195],v[0:3]\n"
RD"v[212:215],v243 offset:20480\n"
"v_add_u32 v250,s50,v224\n"
MF"v[0:3],a[240:243],v[196:199],v[0:3]\n"
MF"v[4:7],a[244:247],v[196:199],v[4:7]\n"
MF"v[4:7],a[228:231],v[192:195],v[4:7]\n"
MF"v[8:11],a[232:235],v[192:195],v[8:11]\n"
RD"v[216:219],v242 offset:22528\n"
MF"v[8:11],a[248:251],v[196:199],v[8:11]\n"
BL"v236,s[12:15],s48 offen offset:2048 lds\n"
MF"v[12:15],a[252:255],v[196:199],v[12:15]\n"
MF"v[12:15],a[236:239],v[192:195],v[12:15]\n"
WT"lgkmcnt(1)\n"
RD"v[220:223],v243 offset:22528\n"
MF"v[16:19],a[224:227],v[200:203],v[16:19]\n"
BL"v237,s[12:15],s48 offen offset:3072 lds\n"
MF"v[16:19],a[240:243],v[204:207],v[16:19]\n"
MF"v[20:23],a[244:247],v[204:207],v[20:23]\n"
MF"v[20:23],a[228:231],v[200:203],v[20:23]\n"
RD"v[192:195],v242 offset:24576\n"
MF"v[24:27],a[232:235],v[200:203],v[24:27]\n"
MF"v[24:27],a[248:251],v[204:207],v[24:27]\n"
MF"v[28:31],a[252:255],v[204:207],v[28:31]\n"
MF"v[28:31],a[236:239],v[200:203],v[28:31]\n"
RD"v[196:199],v243 offset:24576\n"
MF"v[32:35],a[224:227],v[208:211],v[32:35]\n"
MF"v[32:35],a[240:243],v[212:215],v[32:35]\n"
MF"v[36:39],a[244:247],v[212:215],v[36:39]\n"
RD"v[200:203],v242 offset:26624\n"
MF"v[36:39],a[228:231],v[208:211],v[36:39]\n"
MF"v[40:43],a[232:235],v[208:211],v[40:43]\n"
MF"v[40:43],a[248:251],v[212:215],v[40:43]\n"
MF"v[44:47],a[252:255],v[212:215],v[44:47]\n"
RD"v[204:207],v243 offset:26624\n"
MF"v[44:47],a[236:239],v[208:211],v[44:47]\n"
WT"lgkmcnt(2)\n"
MF"v[48:51],a[224:227],v[216:219],v[48:51]\n"
MF"v[48:51],a[240:243],v[220:223],v[48:51]\n"
RD"v[208:211],v242 offset:28672\n"
MF"v[52:55],a[244:247],v[220:223],v[52:55]\n"
MF"v[52:55],a[228:231],v[216:219],v[52:55]\n"
MF"v[56:59],a[232:235],v[216:219],v[56:59]\n"
MF"v[56:59],a[248:251],v[220:223],v[56:59]\n"
RD"v[212:215],v243 offset:28672\n"
MF"v[60:63],a[252:255],v[220:223],v[60:63]\n"
MF"v[60:63],a[236:239],v[216:219],v[60:63]\n"
MF"v[64:67],a[224:227],v[192:195],v[64:67]\n"
RD"v[216:219],v242 offset:30720\n"
MF"v[64:67],a[240:243],v[196:199],v[64:67]\n"
MF"v[68:71],a[244:247],v[196:199],v[68:71]\n"
MF"v[68:71],a[228:231],v[192:195],v[68:71]\n"
MF"v[72:75],a[232:235],v[192:195],v[72:75]\n"
RD"v[220:223],v243 offset:30720\n"
MF"v[72:75],a[248:251],v[196:199],v[72:75]\n"
MF"v[76:79],a[252:255],v[196:199],v[76:79]\n"
MF"v[76:79],a[236:239],v[192:195],v[76:79]\n"
WT"lgkmcnt(1)\n"
MF"v[80:83],a[224:227],v[200:203],v[80:83]\n"
MF"v[80:83],a[240:243],v[204:207],v[80:83]\n"
MF"v[84:87],a[244:247],v[204:207],v[84:87]\n"
"v_add_u32 v244,114688,v242\n"
"v_add_u32 v245,114688,v243\n"
MF"v[84:87],a[228:231],v[200:203],v[84:87]\n"
"v_add_u32 v242,65536,v242\n"
"v_add_u32 v243,65536,v243\n"
MF"v[88:91],a[232:235],v[200:203],v[88:91]\n"
WT"vmcnt(16) lgkmcnt(0)\n"
MF"v[88:91],a[248:251],v[204:207],v[88:91]\n"
"s_barrier\n"
MF"v[92:95],a[252:255],v[204:207],v[92:95]\n"
RD"v[192:195],v242 offset:16384\n"
MF"v[92:95],a[236:239],v[200:203],v[92:95]\n"
MF"v[96:99],a[224:227],v[208:211],v[96:99]\n"
MF"v[96:99],a[240:243],v[212:215],v[96:99]\n"
MF"v[100:103],a[244:247],v[212:215],v[100:103]\n"
SA"m0,s36,49152\n"
RD"v[196:199],v243 offset:16384\n"
SA"s48,s60,s61\n"
MF"v[100:103],a[228:231],v[208:211],v[100:103]\n"
MF"v[104:107],a[232:235],v[208:211],v[104:107]\n"
BL"v238,s[20:23],s48 offen offset:0 lds\n"
MF"v[104:107],a[248:251],v[212:215],v[104:107]\n"
MF"v[108:111],a[252:255],v[212:215],v[108:111]\n"
RD"v[200:203],v242 offset:18432\n"
MF"v[108:111],a[236:239],v[208:211],v[108:111]\n"
MF"v[112:115],a[224:227],v[216:219],v[112:115]\n"
BL"v239,s[20:23],s48 offen offset:1024 lds\n"
MF"v[112:115],a[240:243],v[220:223],v[112:115]\n"
MF"v[116:119],a[244:247],v[220:223],v[116:119]\n"
RD"v[204:207],v243 offset:18432\n"
MF"v[116:119],a[228:231],v[216:219],v[116:119]\n"
MF"v[120:123],a[232:235],v[216:219],v[120:123]\n"
MF"v[120:123],a[248:251],v[220:223],v[120:123]\n"
BL"v240,s[20:23],s48 offen offset:2048 lds\n"
MF"v[124:127],a[252:255],v[220:223],v[124:127]\n"
SA"s61,s61,524288\n"
RD"v[208:211],v242 offset:20480\n"
MF"v[124:127],a[236:239],v[216:219],v[124:127]\n"
WT"lgkmcnt(2)\n"
MF"v[128:131],a[0:3],v[192:195],0\n"
MF"v[128:131],a[16:19],v[196:199],v[128:131]\n"
RD"v[212:215],v243 offset:20480\n"
MF"v[132:135],a[20:23],v[196:199],0\n"
BL"v241,s[20:23],s48 offen offset:3072 lds\n"
MF"v[132:135],a[4:7],v[192:195],v[132:135]\n"
MU"v0,s55,v0\n"
MU"v1,s55,v1\n"
MF"v[136:139],a[8:11],v[192:195],0\n"
MU"v2,s55,v2\n"
MU"v3,s55,v3\n"
MF"v[136:139],a[24:27],v[196:199],v[136:139]\n"
RD"v[216:219],v242 offset:22528\n"
MU"v4,s55,v4\n"
MF"v[140:143],a[28:31],v[196:199],0\n"
SA"m0,s36,32768\n"
MU"v5,s55,v5\n"
MU"v6,s55,v6\n"
MF"v[140:143],a[12:15],v[192:195],v[140:143]\n"
SA"s48,s60,s61\n"
MU"v7,s55,v7\n"
PK"v0,v0,v1\n"
MF"v[144:147],a[0:3],v[200:203],0\n"
WT"lgkmcnt(1)\n"
BL"v238,s[20:23],s48 offen offset:0 lds\n"
MF"v[144:147],a[16:19],v[204:207],v[144:147]\n"
RD"v[220:223],v243 offset:22528\n"
PK"v1,v2,v3\n"
MF"v[148:151],a[20:23],v[204:207],0\n"
PK"v2,v4,v5\n"
PK"v3,v6,v7\n"
MF"v[148:151],a[4:7],v[200:203],v[148:151]\n"
MF"v[152:155],a[8:11],v[200:203],0\n"
BL"v239,s[20:23],s48 offen offset:1024 lds\n"
MF"v[152:155],a[24:27],v[204:207],v[152:155]\n"
RD"v[192:195],v242 offset:40960\n"
MU"v8,s55,v8\n"
MF"v[156:159],a[28:31],v[204:207],0\n"
MU"v9,s55,v9\n"
MU"v10,s55,v10\n"
MF"v[156:159],a[12:15],v[200:203],v[156:159]\n"
MU"v11,s55,v11\n"
MU"v12,s55,v12\n"
MF"v[128:131],a[32:35],v[208:211],v[128:131]\n"
MU"v13,s55,v13\n"
MU"v14,s55,v14\n"
MF"v[128:131],a[48:51],v[212:215],v[128:131]\n"
RD"v[196:199],v243 offset:40960\n"
MU"v15,s55,v15\n"
MF"v[132:135],a[52:55],v[212:215],v[132:135]\n"
SA"s61,s61,524288\n"
BL"v240,s[20:23],s48 offen offset:2048 lds\n"
MF"v[132:135],a[36:39],v[208:211],v[132:135]\n"
"s_cmp_eq_u32 s61,5242880\n"
PK"v8,v8,v9\n"
PK"v9,v10,v11\n"
MF"v[136:139],a[40:43],v[208:211],v[136:139]\n"
"s_cselect_b32 s61,0,s61\n"
RD"v[200:203],v242 offset:43008\n"
PK"v10,v12,v13\n"
MF"v[136:139],a[56:59],v[212:215],v[136:139]\n"
BL"v241,s[20:23],s48 offen offset:3072 lds\n"
MF"v[140:143],a[60:63],v[212:215],v[140:143]\n"
PK"v11,v14,v15\n"
MU"v16,s55,v16\n"
MF"v[140:143],a[44:47],v[208:211],v[140:143]\n"
WT"lgkmcnt(2)\n"
MU"v17,s55,v17\n"
MU"v18,s55,v18\n"
MF"v[144:147],a[32:35],v[216:219],v[144:147]\n"
RD"v[204:207],v243 offset:43008\n"
MU"v19,s55,v19\n"
MF"v[144:147],a[48:51],v[220:223],v[144:147]\n"
MU"v20,s55,v20\n"
MU"v21,s55,v21\n"
MF"v[148:151],a[52:55],v[220:223],v[148:151]\n"
MU"v22,s55,v22\n"
MU"v23,s55,v23\n"
MF"v[148:151],a[36:39],v[216:219],v[148:151]\n"
PK"v16,v16,v17\n"
PK"v17,v18,v19\n"
MF"v[152:155],a[40:43],v[216:219],v[152:155]\n"
RD"v[208:211],v242 offset:45056\n"
PK"v18,v20,v21\n"
MF"v[152:155],a[56:59],v[220:223],v[152:155]\n"
"s_lshl_b32 s90,s31,2\n"
PK"v19,v22,v23\n"
"v_min_u32 v254,48,v224\n"
MF"v[156:159],a[60:63],v[220:223],v[156:159]\n"
SA"s90,s90,s30\n"
"v_lshlrev_b32 v254,3,v254\n"
MF"v[156:159],a[44:47],v[216:219],v[156:159]\n"
"s_mov_b32 s49,0\n"
"s_lshl_b32 s90,s90,9\n"
MF"v[128:131],a[64:67],v[192:195],v[128:131]\n"
WT"lgkmcnt(1)\n"
RD"v[212:215],v243 offset:45056\n"
MU"v24,s55,v24\n"
MF"v[128:131],a[80:83],v[196:199],v[128:131]\n"
BS"v[0:3],v250,s[8:11],s49 offen offset:0 sc1\n"
MF"v[132:135],a[84:87],v[196:199],v[132:135]\n"
"buffer_load_dword v255,v254,s[8:11],s90 offen sc1\n"
MF"v[132:135],a[68:71],v[192:195],v[132:135]\n"
RD"v[216:219],v242 offset:47104\n"
MU"v25,s55,v25\n"
MF"v[136:139],a[72:75],v[192:195],v[136:139]\n"
MU"v26,s55,v26\n"
MU"v27,s55,v27\n"
MF"v[136:139],a[88:91],v[196:199],v[136:139]\n"
MU"v28,s55,v28\n"
MU"v29,s55,v29\n"
MF"v[140:143],a[92:95],v[196:199],v[140:143]\n"
MU"v30,s55,v30\n"
MU"v31,s55,v31\n"
MF"v[140:143],a[76:79],v[192:195],v[140:143]\n"
RD"v[220:223],v243 offset:47104\n"
PK"v24,v24,v25\n"
MF"v[144:147],a[64:67],v[200:203],v[144:147]\n"
PK"v25,v26,v27\n"
PK"v26,v28,v29\n"
MF"v[144:147],a[80:83],v[204:207],v[144:147]\n"
PK"v27,v30,v31\n"
MF"v[148:151],a[84:87],v[204:207],v[148:151]\n"
MU"v32,s55,v32\n"
MU"v33,s55,v33\n"
MF"v[148:151],a[68:71],v[200:203],v[148:151]\n"
RD"v[192:195],v244 offset:16384\n"
MU"v34,s55,v34\n"
MF"v[152:155],a[72:75],v[200:203],v[152:155]\n"
MU"v35,s55,v35\n"
MU"v36,s55,v36\n"
MF"v[152:155],a[88:91],v[204:207],v[152:155]\n"
MU"v37,s55,v37\n"
MU"v38,s55,v38\n"
MF"v[156:159],a[92:95],v[204:207],v[156:159]\n"
MU"v39,s55,v39\n"
PK"v32,v32,v33\n"
MF"v[156:159],a[76:79],v[200:203],v[156:159]\n"
WT"lgkmcnt(1)\n"
RD"v[196:199],v245 offset:16384\n"
PK"v33,v34,v35\n"
MF"v[128:131],a[96:99],v[208:211],v[128:131]\n"
PK"v34,v36,v37\n"
PK"v35,v38,v39\n"
MF"v[128:131],a[112:115],v[212:215],v[128:131]\n"
MF"v[132:135],a[116:119],v[212:215],v[132:135]\n"
MF"v[132:135],a[100:103],v[208:211],v[132:135]\n"
RD"v[200:203],v244 offset:18432\n"
MF"v[136:139],a[104:107],v[208:211],v[136:139]\n"
MU"v40,s55,v40\n"
MU"v41,s55,v41\n"
MF"v[136:139],a[120:123],v[212:215],v[136:139]\n"
MU"v42,s55,v42\n"
MU"v43,s55,v43\n"
MF"v[140:143],a[124:127],v[212:215],v[140:143]\n"
RD"v[204:207],v245 offset:18432\n"
MU"v44,s55,v44\n"
MF"v[140:143],a[108:111],v[208:211],v[140:143]\n"
MU"v45,s55,v45\n"
MU"v46,s55,v46\n"
MF"v[144:147],a[96:99],v[216:219],v[144:147]\n"
MU"v47,s55,v47\n"
PK"v40,v40,v41\n"
MF"v[144:147],a[112:115],v[220:223],v[144:147]\n"
PK"v41,v42,v43\n"
PK"v42,v44,v45\n"
MF"v[148:151],a[116:119],v[220:223],v[148:151]\n"
RD"v[208:211],v244 offset:20480\n"
PK"v43,v46,v47\n"
MF"v[148:151],a[100:103],v[216:219],v[148:151]\n"
MF"v[152:155],a[104:107],v[216:219],v[152:155]\n"
MF"v[152:155],a[120:123],v[220:223],v[152:155]\n"
MF"v[156:159],a[124:127],v[220:223],v[156:159]\n"
RD"v[212:215],v245 offset:20480\n"
MU"v48,s55,v48\n"
MF"v[156:159],a[108:111],v[216:219],v[156:159]\n"
WT"lgkmcnt(2)\n"
MU"v49,s55,v49\n"
MU"v50,s55,v50\n"
MF"v[128:131],a[128:131],v[192:195],v[128:131]\n"
BS"v[8:11],v250,s[8:11],s49 offen offset:1024 sc1\n"
MF"v[128:131],a[144:147],v[196:199],v[128:131]\n"
MU"v51,s55,v51\n"
MU"v52,s55,v52\n"
MF"v[132:135],a[148:151],v[196:199],v[132:135]\n"
RD"v[216:219],v244 offset:22528\n"
MU"v53,s55,v53\n"
MF"v[132:135],a[132:135],v[192:195],v[132:135]\n"
MU"v54,s55,v54\n"
MU"v55,s55,v55\n"
MF"v[136:139],a[136:139],v[192:195],v[136:139]\n"
PK"v48,v48,v49\n"
PK"v49,v50,v51\n"
MF"v[136:139],a[152:155],v[196:199],v[136:139]\n"
RD"v[220:223],v245 offset:22528\n"
PK"v50,v52,v53\n"
MF"v[140:143],a[156:159],v[196:199],v[140:143]\n"
PK"v51,v54,v55\n"
MF"v[140:143],a[140:143],v[192:195],v[140:143]\n"
MF"v[144:147],a[128:131],v[200:203],v[144:147]\n"
MU"v56,s55,v56\n"
MU"v57,s55,v57\n"
MF"v[144:147],a[144:147],v[204:207],v[144:147]\n"
RD"v[192:195],v244 offset:40960\n"
MU"v58,s55,v58\n"
MF"v[148:151],a[148:151],v[204:207],v[148:151]\n"
MU"v59,s55,v59\n"
MU"v60,s55,v60\n"
MF"v[148:151],a[132:135],v[200:203],v[148:151]\n"
MU"v61,s55,v61\n"
MU"v62,s55,v62\n"
MF"v[152:155],a[136:139],v[200:203],v[152:155]\n"
MU"v63,s55,v63\n"
PK"v56,v56,v57\n"
MF"v[152:155],a[152:155],v[204:207],v[152:155]\n"
RD"v[196:199],v245 offset:40960\n"
PK"v57,v58,v59\n"
MF"v[156:159],a[156:159],v[204:207],v[156:159]\n"
PK"v58,v60,v61\n"
PK"v59,v62,v63\n"
MF"v[156:159],a[140:143],v[200:203],v[156:159]\n"
WT"lgkmcnt(2)\n"
MF"v[128:131],a[160:163],v[208:211],v[128:131]\n"
MF"v[128:131],a[176:179],v[212:215],v[128:131]\n"
RD"v[200:203],v244 offset:43008\n"
MF"v[132:135],a[180:183],v[212:215],v[132:135]\n"
MU"v64,s55,v64\n"
MU"v65,s55,v65\n"
MF"v[132:135],a[164:167],v[208:211],v[132:135]\n"
MU"v66,s55,v66\n"
MU"v67,s55,v67\n"
MF"v[136:139],a[168:171],v[208:211],v[136:139]\n"
MU"v68,s55,v68\n"
MU"v69,s55,v69\n"
MF"v[136:139],a[184:187],v[212:215],v[136:139]\n"
RD"v[204:207],v245 offset:43008\n"
MU"v70,s55,v70\n"
MF"v[140:143],a[188:191],v[212:215],v[140:143]\n"
MU"v71,s55,v71\n"
PK"v64,v64,v65\n"
MF"v[140:143],a[172:175],v[208:211],v[140:143]\n"
PK"v65,v66,v67\n"
PK"v66,v68,v69\n"
MF"v[144:147],a[160:163],v[216:219],v[144:147]\n"
RD"v[208:211],v244 offset:45056\n"
PK"v67,v70,v71\n"
MF"v[144:147],a[176:179],v[220:223],v[144:147]\n"
MF"v[148:151],a[180:183],v[220:223],v[148:151]\n"
MF"v[148:151],a[164:167],v[216:219],v[148:151]\n"
MF"v[152:155],a[168:171],v[216:219],v[152:155]\n"
RD"v[212:215],v245 offset:45056\n"
MU"v72,s55,v72\n"
MF"v[152:155],a[184:187],v[220:223],v[152:155]\n"
"s_mov_b32 s49,4096\n"
MU"v73,s55,v73\n"
MU"v74,s55,v74\n"
MF"v[156:159],a[188:191],v[220:223],v[156:159]\n"
MU"v75,s55,v75\n"
MU"v76,s55,v76\n"
MF"v[156:159],a[172:175],v[216:219],v[156:159]\n"
WT"lgkmcnt(1)\n"
BS"v[16:19],v250,s[8:11],s49 offen offset:0 sc1\n"
MF"v[128:131],a[192:195],v[192:195],v[128:131]\n"
RD"v[216:219],v244 offset:47104\n"
MU"v77,s55,v77\n"
MF"v[128:131],a[208:211],v[196:199],v[128:131]\n"
MU"v78,s55,v78\n"
MU"v79,s55,v79\n"
MF"v[132:135],a[212:215],v[196:199],v[132:135]\n"
PK"v72,v72,v73\n"
PK"v73,v74,v75\n"
MF"v[132:135],a[196:199],v[192:195],v[132:135]\n"
PK"v74,v76,v77\n"
PK"v75,v78,v79\n"
MF"v[136:139],a[200:203],v[192:195],v[136:139]\n"
RD"v[220:223],v245 offset:47104\n"
MF"v[136:139],a[216:219],v[196:199],v[136:139]\n"
MF"v[140:143],a[220:223],v[196:199],v[140:143]\n"
MU"v80,s55,v80\n"
MU"v81,s55,v81\n"
MF"v[140:143],a[204:207],v[192:195],v[140:143]\n"
MU"v82,s55,v82\n"
MU"v83,s55,v83\n"
MF"v[144:147],a[192:195],v[200:203],v[144:147]\n"
MU"v84,s55,v84\n"
MU"v85,s55,v85\n"
MF"v[144:147],a[208:211],v[204:207],v[144:147]\n"
MU"v86,s55,v86\n"
MU"v87,s55,v87\n"
MF"v[148:151],a[212:215],v[204:207],v[148:151]\n"
PK"v80,v80,v81\n"
PK"v81,v82,v83\n"
MF"v[148:151],a[196:199],v[200:203],v[148:151]\n"
PK"v82,v84,v85\n"
PK"v83,v86,v87\n"
MF"v[152:155],a[200:203],v[200:203],v[152:155]\n"
WT"vmcnt(20) lgkmcnt(0)\n"
MF"v[152:155],a[216:219],v[204:207],v[152:155]\n"
"s_barrier\n"
MF"v[156:159],a[220:223],v[204:207],v[156:159]\n"
RD"v[192:195],v242 offset:0\n"
MF"v[156:159],a[204:207],v[200:203],v[156:159]\n"
MF"v[128:131],a[224:227],v[208:211],v[128:131]\n"
MU"v88,s55,v88\n"
MU"v89,s55,v89\n"
MF"v[128:131],a[240:243],v[212:215],v[128:131]\n"
MU"v90,s55,v90\n"
MU"v91,s55,v91\n"
MF"v[132:135],a[244:247],v[212:215],v[132:135]\n"
RD"v[196:199],v243 offset:0\n"
MU"v92,s55,v92\n"
MF"v[132:135],a[228:231],v[208:211],v[132:135]\n"
MU"v93,s55,v93\n"
MU"v94,s55,v94\n"
MF"v[136:139],a[232:235],v[208:211],v[136:139]\n"
MU"v95,s55,v95\n"
PK"v88,v88,v89\n"
MF"v[136:139],a[248:251],v[212:215],v[136:139]\n"
PK"v89,v90,v91\n"
PK"v90,v92,v93\n"
MF"v[140:143],a[252:255],v[212:215],v[140:143]\n"
RD"v[200:203],v242 offset:2048\n"
PK"v91,v94,v95\n"
MF"v[140:143],a[236:239],v[208:211],v[140:143]\n"
MF"v[144:147],a[224:227],v[216:219],v[144:147]\n"
MF"v[144:147],a[240:243],v[220:223],v[144:147]\n"
MF"v[148:151],a[244:247],v[220:223],v[148:151]\n"
RD"v[204:207],v243 offset:2048\n"
MF"v[148:151],a[228:231],v[216:219],v[148:151]\n"
MF"v[152:155],a[232:235],v[216:219],v[152:155]\n"
MF"v[152:155],a[248:251],v[220:223],v[152:155]\n"
MF"v[156:159],a[252:255],v[220:223],v[156:159]\n"
RD"v[208:211],v242 offset:4096\n"
MF"v[156:159],a[236:239],v[216:219],v[156:159]\n"
WT"lgkmcnt(2)\n"
BS"v[24:27],v250,s[8:11],s49 offen offset:1024 sc1\n"
MF"v[36:39],a[0:3],v[192:195],0\n"
MF"v[36:39],a[16:19],v[196:199],v[36:39]\n"
RD"v[212:215],v243 offset:4096\n"
MF"v[68:71],a[20:23],v[196:199],0\n"
MF"v[68:71],a[4:7],v[192:195],v[68:71]\n"
MU"v128,s55,v128\n"
MU"v129,s55,v129\n"
MF"v[44:47],a[8:11],v[192:195],0\n"
MU"v130,s55,v130\n"
MU"v131,s55,v131\n"
MF"v[44:47],a[24:27],v[196:199],v[44:47]\n"
RD"v[216:219],v242 offset:6144\n"
MU"v132,s55,v132\n"
MF"v[76:79],a[28:31],v[196:199],0\n"
MU"v133,s55,v133\n"
MU"v134,s55,v134\n"
MF"v[76:79],a[12:15],v[192:195],v[76:79]\n"
MU"v135,s55,v135\n"
PK"v4,v128,v129\n"
MF"v[52:55],a[0:3],v[200:203],0\n"
WT"lgkmcnt(1)\n"
PK"v5,v130,v131\n"
PK"v6,v132,v133\n"
MF"v[52:55],a[16:19],v[204:207],v[52:55]\n"
RD"v[220:223],v243 offset:6144\n"
PK"v7,v134,v135\n"
MF"v[84:87],a[20:23],v[204:207],0\n"
MF"v[84:87],a[4:7],v[200:203],v[84:87]\n"
MF"v[60:63],a[8:11],v[200:203],0\n"
MF"v[60:63],a[24:27],v[204:207],v[60:63]\n"
RD"v[192:195],v242 offset:24576\n"
MU"v136,s55,v136\n"
MF"v[92:95],a[28:31],v[204:207],0\n"
MU"v137,s55,v137\n"
MU"v138,s55,v138\n"
MF"v[92:95],a[12:15],v[200:203],v[92:95]\n"
MU"v139,s55,v139\n"
MU"v140,s55,v140\n"
MF"v[36:39],a[32:35],v[208:211],v[36:39]\n"
MU"v141,s55,v141\n"
MU"v142,s55,v142\n"
MF"v[36:39],a[48:51],v[212:215],v[36:39]\n"
RD"v[196:199],v243 offset:24576\n"
MU"v143,s55,v143\n"
MF"v[68:71],a[52:55],v[212:215],v[68:71]\n"
PK"v12,v136,v137\n"
PK"v13,v138,v139\n"
MF"v[68:71],a[36:39],v[208:211],v[68:71]\n"
PK"v14,v140,v141\n"
PK"v15,v142,v143\n"
MF"v[44:47],a[40:43],v[208:211],v[44:47]\n"
RD"v[200:203],v242 offset:26624\n"
MF"v[44:47],a[56:59],v[212:215],v[44:47]\n"
MF"v[76:79],a[60:63],v[212:215],v[76:79]\n"
MF"v[76:79],a[44:47],v[208:211],v[76:79]\n"
WT"lgkmcnt(2)\n"
MU"v144,s55,v144\n"
MU"v145,s55,v145\n"
MF"v[52:55],a[32:35],v[216:219],v[52:55]\n"
RD"v[204:207],v243 offset:26624\n"
MU"v146,s55,v146\n"
MF"v[52:55],a[48:51],v[220:223],v[52:55]\n"
MU"v147,s55,v147\n"
MU"v148,s55,v148\n"
MF"v[84:87],a[52:55],v[220:223],v[84:87]\n"
MU"v149,s55,v149\n"
MU"v150,s55,v150\n"
MF"v[84:87],a[36:39],v[216:219],v[84:87]\n"
"s_mov_b32 s49,8192\n"
MU"v151,s55,v151\n"
PK"v20,v144,v145\n"
MF"v[60:63],a[40:43],v[216:219],v[60:63]\n"
RD"v[208:211],v242 offset:28672\n"
PK"v21,v146,v147\n"
MF"v[60:63],a[56:59],v[220:223],v[60:63]\n"
BS"v[32:35],v250,s[8:11],s49 offen offset:0 sc1\n"
MF"v[92:95],a[60:63],v[220:223],v[92:95]\n"
PK"v22,v148,v149\n"
PK"v23,v150,v151\n"
MF"v[92:95],a[44:47],v[216:219],v[92:95]\n"
MF"v[36:39],a[64:67],v[192:195],v[36:39]\n"
WT"lgkmcnt(1)\n"
RD"v[212:215],v243 offset:28672\n"
MF"v[36:39],a[80:83],v[196:199],v[36:39]\n"
MU"v152,s55,v152\n"
MU"v153,s55,v153\n"
MF"v[68:71],a[84:87],v[196:199],v[68:71]\n"
MU"v154,s55,v154\n"
MU"v155,s55,v155\n"
MF"v[68:71],a[68:71],v[192:195],v[68:71]\n"
RD"v[216:219],v242 offset:30720\n"
MU"v156,s55,v156\n"
MF"v[44:47],a[72:75],v[192:195],v[44:47]\n"
MU"v157,s55,v157\n"
MU"v158,s55,v158\n"
MF"v[44:47],a[88:91],v[196:199],v[44:47]\n"
MU"v159,s55,v159\n"
PK"v28,v152,v153\n"
MF"v[76:79],a[92:95],v[196:199],v[76:79]\n"
PK"v29,v154,v155\n"
PK"v30,v156,v157\n"
MF"v[76:79],a[76:79],v[192:195],v[76:79]\n"
RD"v[220:223],v243 offset:30720\n"
PK"v31,v158,v159\n"
MF"v[52:55],a[64:67],v[200:203],v[52:55]\n"
MF"v[52:55],a[80:83],v[204:207],v[52:55]\n"
MF"v[84:87],a[84:87],v[204:207],v[84:87]\n"
MF"v[84:87],a[68:71],v[200:203],v[84:87]\n"
RD"v[192:195],v244 offset:0\n"
MF"v[60:63],a[72:75],v[200:203],v[60:63]\n"
MF"v[60:63],a[88:91],v[204:207],v[60:63]\n"
MF"v[92:95],a[92:95],v[204:207],v[92:95]\n"
MF"v[92:95],a[76:79],v[200:203],v[92:95]\n"
WT"lgkmcnt(1)\n"
RD"v[196:199],v245 offset:0\n"
MF"v[36:39],a[96:99],v[208:211],v[36:39]\n"
MF"v[36:39],a[112:115],v[212:215],v[36:39]\n"
MF"v[68:71],a[116:119],v[212:215],v[68:71]\n"
MF"v[68:71],a[100:103],v[208:211],v[68:71]\n"
RD"v[200:203],v244 offset:2048\n"
MF"v[44:47],a[104:107],v[208:211],v[44:47]\n"
MF"v[44:47],a[120:123],v[212:215],v[44:47]\n"
MF"v[76:79],a[124:127],v[212:215],v[76:79]\n"
RD"v[204:207],v245 offset:2048\n"
MF"v[76:79],a[108:111],v[208:211],v[76:79]\n"
MF"v[52:55],a[96:99],v[216:219],v[52:55]\n"
MF"v[52:55],a[112:115],v[220:223],v[52:55]\n"
MF"v[84:87],a[116:119],v[220:223],v[84:87]\n"
SA"m0,s37,16384\n"
RD"v[208:211],v244 offset:4096\n"
SA"s48,s39,98304\n"
MF"v[84:87],a[100:103],v[216:219],v[84:87]\n"
MF"v[60:63],a[104:107],v[216:219],v[60:63]\n"
BS"v[40:43],v250,s[8:11],s49 offen offset:1024 sc1\n"
MF"v[60:63],a[120:123],v[220:223],v[60:63]\n"
BL"v234,s[12:15],s48 offen offset:0 lds\n"
MF"v[92:95],a[124:127],v[220:223],v[92:95]\n"
RD"v[212:215],v245 offset:4096\n"
MU"v96,s55,v96\n"
MF"v[92:95],a[108:111],v[216:219],v[92:95]\n"
WT"lgkmcnt(2)\n"
MU"v97,s55,v97\n"
MU"v98,s55,v98\n"
MF"v[36:39],a[128:131],v[192:195],v[36:39]\n"
MU"v99,s55,v99\n"
MU"v100,s55,v100\n"
MF"v[36:39],a[144:147],v[196:199],v[36:39]\n"
MU"v101,s55,v101\n"
MU"v102,s55,v102\n"
MF"v[68:71],a[148:151],v[196:199],v[68:71]\n"
RD"v[216:219],v244 offset:6144\n"
MU"v103,s55,v103\n"
MF"v[68:71],a[132:135],v[192:195],v[68:71]\n"
BL"v235,s[12:15],s48 offen offset:1024 lds\n"
MF"v[44:47],a[136:139],v[192:195],v[44:47]\n"
PK"v96,v96,v97\n"
PK"v97,v98,v99\n"
MF"v[44:47],a[152:155],v[196:199],v[44:47]\n"
RD"v[220:223],v245 offset:6144\n"
PK"v98,v100,v101\n"
MF"v[76:79],a[156:159],v[196:199],v[76:79]\n"
PK"v99,v102,v103\n"
MU"v104,s55,v104\n"
MF"v[76:79],a[140:143],v[192:195],v[76:79]\n"
MU"v105,s55,v105\n"
MU"v106,s55,v106\n"
MF"v[52:55],a[128:131],v[200:203],v[52:55]\n"
BL"v236,s[12:15],s48 offen offset:2048 lds\n"
MF"v[52:55],a[144:147],v[204:207],v[52:55]\n"
RD"v[192:195],v244 offset:24576\n"
MU"v107,s55,v107\n"
MF"v[84:87],a[148:151],v[204:207],v[84:87]\n"
MU"v108,s55,v108\n"
MU"v109,s55,v109\n"
MF"v[84:87],a[132:135],v[200:203],v[84:87]\n"
MU"v110,s55,v110\n"
MU"v111,s55,v111\n"
MF"v[60:63],a[136:139],v[200:203],v[60:63]\n"
PK"v104,v104,v105\n"
PK"v105,v106,v107\n"
MF"v[60:63],a[152:155],v[204:207],v[60:63]\n"
RD"v[196:199],v245 offset:24576\n"
PK"v106,v108,v109\n"
MF"v[92:95],a[156:159],v[204:207],v[92:95]\n"
BL"v237,s[12:15],s48 offen offset:3072 lds\n"
MF"v[92:95],a[140:143],v[200:203],v[92:95]\n"
WT"lgkmcnt(2)\n"
PK"v107,v110,v111\n"
MF"v[36:39],a[160:163],v[208:211],v[36:39]\n"
MU"v112,s55,v112\n"
MU"v113,s55,v113\n"
MF"v[36:39],a[176:179],v[212:215],v[36:39]\n"
RD"v[200:203],v244 offset:26624\n"
MU"v114,s55,v114\n"
MF"v[68:71],a[180:183],v[212:215],v[68:71]\n"
SA"m0,s37,20480\n"
MU"v115,s55,v115\n"
MU"v116,s55,v116\n"
MF"v[68:71],a[164:167],v[208:211],v[68:71]\n"
SA"s48,s98,98304\n"
MU"v117,s55,v117\n"
MU"v118,s55,v118\n"
MF"v[44:47],a[168:171],v[208:211],v[44:47]\n"
BL"v234,s[12:15],s48 offen offset:0 lds\n"
MF"v[44:47],a[184:187],v[212:215],v[44:47]\n"
RD"v[204:207],v245 offset:26624\n"
MU"v119,s55,v119\n"
MF"v[76:79],a[188:191],v[212:215],v[76:79]\n"
PK"v112,v112,v113\n"
PK"v113,v114,v115\n"
MF"v[76:79],a[172:175],v[208:211],v[76:79]\n"
PK"v114,v116,v117\n"
PK"v115,v118,v119\n"
MF"v[52:55],a[160:163],v[216:219],v[52:55]\n"
RD"v[208:211],v244 offset:28672\n"
MF"v[52:55],a[176:179],v[220:223],v[52:55]\n"
"s_mov_b32 s49,12288\n"
MF"v[84:87],a[180:183],v[220:223],v[84:87]\n"
BL"v235,s[12:15],s48 offen offset:1024 lds\n"
MF"v[84:87],a[164:167],v[216:219],v[84:87]\n"
BS"v[48:51],v250,s[8:11],s49 offen offset:0 sc1\n"
MF"v[60:63],a[168:171],v[216:219],v[60:63]\n"
RD"v[212:215],v245 offset:28672\n"
MU"v120,s55,v120\n"
MF"v[60:63],a[184:187],v[220:223],v[60:63]\n"
MU"v121,s55,v121\n"
MU"v122,s55,v122\n"
MF"v[92:95],a[188:191],v[220:223],v[92:95]\n"
MU"v123,s55,v123\n"
MU"v124,s55,v124\n"
MF"v[92:95],a[172:175],v[216:219],v[92:95]\n"
WT"lgkmcnt(1)\n"
MU"v125,s55,v125\n"
MU"v126,s55,v126\n"
MF"v[36:39],a[192:195],v[192:195],v[36:39]\n"
RD"v[216:219],v244 offset:30720\n"
MU"v127,s55,v127\n"
MF"v[36:39],a[208:211],v[196:199],v[36:39]\n"
BL"v236,s[12:15],s48 offen offset:2048 lds\n"
MF"v[68:71],a[212:215],v[196:199],v[68:71]\n"
PK"v120,v120,v121\n"
PK"v121,v122,v123\n"
MF"v[68:71],a[196:199],v[192:195],v[68:71]\n"
PK"v122,v124,v125\n"
PK"v123,v126,v127\n"
MF"v[44:47],a[200:203],v[192:195],v[44:47]\n"
RD"v[220:223],v245 offset:30720\n"
MF"v[44:47],a[216:219],v[196:199],v[44:47]\n"
MF"v[76:79],a[220:223],v[196:199],v[76:79]\n"
BL"v237,s[12:15],s48 offen offset:3072 lds\n"
MF"v[76:79],a[204:207],v[192:195],v[76:79]\n"
MF"v[52:55],a[192:195],v[200:203],v[52:55]\n"
MF"v[52:55],a[208:211],v[204:207],v[52:55]\n"
MF"v[84:87],a[212:215],v[204:207],v[84:87]\n"
MF"v[84:87],a[196:199],v[200:203],v[84:87]\n"
"v_lshrrev_b32 v251,2,v224\n"
MF"v[60:63],a[200:203],v[200:203],v[60:63]\n"
WT"vmcnt(24) lgkmcnt(0)\n"
"v_and_b32 v251,28,v251\n"
MF"v[60:63],a[216:219],v[204:207],v[60:63]\n"
"s_barrier\n"
MF"v[92:95],a[220:223],v[204:207],v[92:95]\n"
RD"v[192:195],v242 offset:8192\n"
MF"v[92:95],a[204:207],v[200:203],v[92:95]\n"
"ds_read_b32 v234,v251 offset:16384\n"
MF"v[36:39],a[224:227],v[208:211],v[36:39]\n"
MF"v[36:39],a[240:243],v[212:215],v[36:39]\n"
MF"v[68:71],a[244:247],v[212:215],v[68:71]\n"
RD"v[196:199],v243 offset:8192\n"
MF"v[68:71],a[228:231],v[208:211],v[68:71]\n"
MF"v[44:47],a[232:235],v[208:211],v[44:47]\n"
MF"v[44:47],a[248:251],v[212:215],v[44:47]\n"
MF"v[76:79],a[252:255],v[212:215],v[76:79]\n"
RD"v[200:203],v242 offset:10240\n"
MF"v[76:79],a[236:239],v[208:211],v[76:79]\n"
WT"lgkmcnt(2)\n"
"v_not_b32 v251,v234\n"
"v_bfe_u32 v252,v251,4,7\n"
MF"v[52:55],a[224:227],v[216:219],v[52:55]\n"
"v_mul_u32_u24 v252,20480,v252\n"
MF"v[52:55],a[240:243],v[220:223],v[52:55]\n"
"v_and_b32 v251,15,v251\n"
MF"v[84:87],a[244:247],v[220:223],v[84:87]\n"
RD"v[204:207],v243 offset:10240\n"
"v_lshl_add_u32 v251,v251,4,v252\n"
MF"v[84:87],a[228:231],v[216:219],v[84:87]\n"
BS"v[56:59],v250,s[8:11],s49 offen offset:1024 sc1\n"
MF"v[60:63],a[232:235],v[216:219],v[60:63]\n"
"v_and_b32 v252,0x380,v224\n"
"v_lshl_add_u32 v251,v252,1,v251\n"
MF"v[60:63],a[248:251],v[220:223],v[60:63]\n"
"v_add_u32 v252,v252,v224\n"
MF"v[92:95],a[252:255],v[220:223],v[92:95]\n"
"s_xor_b32 s92,s34,0\n"
RD"v[208:211],v242 offset:12288\n"
MF"v[92:95],a[236:239],v[216:219],v[92:95]\n"
"s_lshl_b32 s92,s92,2\n"
MF"v[128:131],a[0:3],v[192:195],0\n"
WT"lgkmcnt(2)\n"
SA"s92,s92,s30\n"
MF"v[128:131],a[16:19],v[196:199],v[128:131]\n"
"s_lshl_b32 s93,s92,11\n"
RD"v[212:215],v243 offset:12288\n"
MF"v[132:135],a[20:23],v[196:199],0\n"
"s_cmp_lt_u32 s92,10\n"
"s_cselect_b32 s93,s93,2130706432\n"
MF"v[132:135],a[4:7],v[192:195],v[132:135]\n"
SA"s92,s93,524288\n"
MU"v36,s55,v36\n"
MU"v37,s55,v37\n"
MF"v[136:139],a[8:11],v[192:195],0\n"
BL"v[100:103],v251,s[8:11],s92 offen sc1\n"
MF"v[136:139],a[24:27],v[196:199],v[136:139]\n"
"s_mul_i32 s92,s35,20480\n"
RD"v[216:219],v242 offset:14336\n"
MU"v38,s55,v38\n"
MF"v[140:143],a[28:31],v[196:199],0\n"
SA"s92,s92,s93\n"
MU"v39,s55,v39\n"
MU"v68,s55,v68\n"
MF"v[140:143],a[12:15],v[192:195],v[140:143]\n"
SA"s92,s92,3145728\n"
MU"v69,s55,v69\n"
MU"v70,s55,v70\n"
MF"v[144:147],a[0:3],v[200:203],0\n"
WT"lgkmcnt(1)\n"
MU"v71,s55,v71\n"
PK"v36,v36,v37\n"
MF"v[144:147],a[16:19],v[204:207],v[144:147]\n"
RD"v[220:223],v243 offset:14336\n"
PK"v37,v38,v39\n"
MF"v[148:151],a[20:23],v[204:207],0\n"
PK"v38,v68,v69\n"
PK"v39,v70,v71\n"
MF"v[148:151],a[4:7],v[200:203],v[148:151]\n"
MF"v[152:155],a[8:11],v[200:203],0\n"
MF"v[152:155],a[24:27],v[204:207],v[152:155]\n"
RD"v[192:195],v242 offset:32768\n"
MU"v44,s55,v44\n"
MF"v[156:159],a[28:31],v[204:207],0\n"
MU"v45,s55,v45\n"
MU"v46,s55,v46\n"
MF"v[156:159],a[12:15],v[200:203],v[156:159]\n"
MU"v47,s55,v47\n"
MU"v76,s55,v76\n"
MF"v[128:131],a[32:35],v[208:211],v[128:131]\n"
MU"v77,s55,v77\n"
MU"v78,s55,v78\n"
MF"v[128:131],a[48:51],v[212:215],v[128:131]\n"
RD"v[196:199],v243 offset:32768\n"
MU"v79,s55,v79\n"
MF"v[132:135],a[52:55],v[212:215],v[132:135]\n"
PK"v44,v44,v45\n"
PK"v45,v46,v47\n"
MF"v[132:135],a[36:39],v[208:211],v[132:135]\n"
PK"v46,v76,v77\n"
PK"v47,v78,v79\n"
MF"v[136:139],a[40:43],v[208:211],v[136:139]\n"
SA"m0,s36,0\n"
RD"v[200:203],v242 offset:34816\n"
SA"s48,s60,s61\n"
MF"v[136:139],a[56:59],v[212:215],v[136:139]\n"
MF"v[140:143],a[60:63],v[212:215],v[140:143]\n"
BL"v238,s[20:23],s48 offen offset:0 lds\n"
MF"v[140:143],a[44:47],v[208:211],v[140:143]\n"
WT"lgkmcnt(2)\n"
MU"v52,s55,v52\n"
MU"v53,s55,v53\n"
MF"v[144:147],a[32:35],v[216:219],v[144:147]\n"
"s_mov_b32 s49,16384\n"
RD"v[204:207],v243 offset:34816\n"
MU"v54,s55,v54\n"
MF"v[144:147],a[48:51],v[220:223],v[144:147]\n"
BS"v[64:67],v250,s[8:11],s49 offen offset:0 sc1\n"
MF"v[148:151],a[52:55],v[220:223],v[148:151]\n"
MU"v55,s55,v55\n"
MU"v84,s55,v84\n"
MF"v[148:151],a[36:39],v[216:219],v[148:151]\n"
MU"v85,s55,v85\n"
MU"v86,s55,v86\n"
MF"v[152:155],a[40:43],v[216:219],v[152:155]\n"
RD"v[208:211],v242 offset:36864\n"
MU"v87,s55,v87\n"
MF"v[152:155],a[56:59],v[220:223],v[152:155]\n"
PK"v52,v52,v53\n"
PK"v53,v54,v55\n"
MF"v[156:159],a[60:63],v[220:223],v[156:159]\n"
BL"v239,s[20:23],s48 offen offset:1024 lds\n"
MF"v[156:159],a[44:47],v[216:219],v[156:159]\n"
PK"v54,v84,v85\n"
PK"v55,v86,v87\n"
MF"v[128:131],a[64:67],v[192:195],v[128:131]\n"
WT"lgkmcnt(1)\n"
RD"v[212:215],v243 offset:36864\n"
MF"v[128:131],a[80:83],v[196:199],v[128:131]\n"
MU"v60,s55,v60\n"
MU"v61,s55,v61\n"
MF"v[132:135],a[84:87],v[196:199],v[132:135]\n"
MU"v62,s55,v62\n"
MU"v63,s55,v63\n"
MF"v[132:135],a[68:71],v[192:195],v[132:135]\n"
RD"v[216:219],v242 offset:38912\n"
MU"v92,s55,v92\n"
MF"v[136:139],a[72:75],v[192:195],v[136:139]\n"
MU"v93,s55,v93\n"
MU"v94,s55,v94\n"
MF"v[136:139],a[88:91],v[196:199],v[136:139]\n"
MU"v95,s55,v95\n"
PK"v60,v60,v61\n"
MF"v[140:143],a[92:95],v[196:199],v[140:143]\n"
BL"v240,s[20:23],s48 offen offset:2048 lds\n"
MF"v[140:143],a[76:79],v[192:195],v[140:143]\n"
RD"v[220:223],v243 offset:38912\n"
PK"v61,v62,v63\n"
MF"v[144:147],a[64:67],v[200:203],v[144:147]\n"
PK"v62,v92,v93\n"
PK"v63,v94,v95\n"
MF"v[144:147],a[80:83],v[204:207],v[144:147]\n"
MF"v[148:151],a[84:87],v[204:207],v[148:151]\n"
MF"v[148:151],a[68:71],v[200:203],v[148:151]\n"
RD"v[192:195],v244 offset:8192\n"
MF"v[152:155],a[72:75],v[200:203],v[152:155]\n"
MF"v[152:155],a[88:91],v[204:207],v[152:155]\n"
MF"v[156:159],a[92:95],v[204:207],v[156:159]\n"
SA"s61,s61,524288\n"
BL"v241,s[20:23],s48 offen offset:3072 lds\n"
MF"v[156:159],a[76:79],v[200:203],v[156:159]\n"
WT"lgkmcnt(1)\n"
RD"v[196:199],v245 offset:8192\n"
MF"v[128:131],a[96:99],v[208:211],v[128:131]\n"
MF"v[128:131],a[112:115],v[212:215],v[128:131]\n"
MF"v[132:135],a[116:119],v[212:215],v[132:135]\n"
MF"v[132:135],a[100:103],v[208:211],v[132:135]\n"
RD"v[200:203],v244 offset:10240\n"
MF"v[136:139],a[104:107],v[208:211],v[136:139]\n"
MF"v[136:139],a[120:123],v[212:215],v[136:139]\n"
MF"v[140:143],a[124:127],v[212:215],v[140:143]\n"
RD"v[204:207],v245 offset:10240\n"
MF"v[140:143],a[108:111],v[208:211],v[140:143]\n"
MF"v[144:147],a[96:99],v[216:219],v[144:147]\n"
BS"v[72:75],v250,s[8:11],s49 offen offset:1024 sc1\n"
MF"v[144:147],a[112:115],v[220:223],v[144:147]\n"
MF"v[148:151],a[116:119],v[220:223],v[148:151]\n"
RD"v[208:211],v244 offset:12288\n"
MF"v[148:151],a[100:103],v[216:219],v[148:151]\n"
MF"v[152:155],a[104:107],v[216:219],v[152:155]\n"
SA"m0,s38,65536\n"
SA"s48,s60,s61\n"
MF"v[152:155],a[120:123],v[220:223],v[152:155]\n"
MF"v[156:159],a[124:127],v[220:223],v[156:159]\n"
RD"v[212:215],v245 offset:12288\n"
MF"v[156:159],a[108:111],v[216:219],v[156:159]\n"
WT"lgkmcnt(2)\n"
BL"v238,s[20:23],s48 offen offset:0 lds\n"
MF"v[128:131],a[128:131],v[192:195],v[128:131]\n"
MF"v[128:131],a[144:147],v[196:199],v[128:131]\n"
MF"v[132:135],a[148:151],v[196:199],v[132:135]\n"
RD"v[216:219],v244 offset:14336\n"
MF"v[132:135],a[132:135],v[192:195],v[132:135]\n"
MF"v[136:139],a[136:139],v[192:195],v[136:139]\n"
MF"v[136:139],a[152:155],v[196:199],v[136:139]\n"
RD"v[220:223],v245 offset:14336\n"
MF"v[140:143],a[156:159],v[196:199],v[140:143]\n"
BL"v239,s[20:23],s48 offen offset:1024 lds\n"
MF"v[140:143],a[140:143],v[192:195],v[140:143]\n"
MF"v[144:147],a[128:131],v[200:203],v[144:147]\n"
MF"v[144:147],a[144:147],v[204:207],v[144:147]\n"
RD"v[192:195],v244 offset:32768\n"
MF"v[148:151],a[148:151],v[204:207],v[148:151]\n"
MF"v[148:151],a[132:135],v[200:203],v[148:151]\n"
MF"v[152:155],a[136:139],v[200:203],v[152:155]\n"
MF"v[152:155],a[152:155],v[204:207],v[152:155]\n"
RD"v[196:199],v245 offset:32768\n"
MF"v[156:159],a[156:159],v[204:207],v[156:159]\n"
BL"v240,s[20:23],s48 offen offset:2048 lds\n"
MF"v[156:159],a[140:143],v[200:203],v[156:159]\n"
WT"lgkmcnt(2)\n"
MF"v[128:131],a[160:163],v[208:211],v[128:131]\n"
MF"v[128:131],a[176:179],v[212:215],v[128:131]\n"
RD"v[200:203],v244 offset:34816\n"
MF"v[132:135],a[180:183],v[212:215],v[132:135]\n"
MF"v[132:135],a[164:167],v[208:211],v[132:135]\n"
MF"v[136:139],a[168:171],v[208:211],v[136:139]\n"
MF"v[136:139],a[184:187],v[212:215],v[136:139]\n"
"s_mov_b32 s49,20480\n"
RD"v[204:207],v245 offset:34816\n"
MF"v[140:143],a[188:191],v[212:215],v[140:143]\n"
SA"s61,s61,524288\n"
BL"v241,s[20:23],s48 offen offset:3072 lds\n"
MF"v[140:143],a[172:175],v[208:211],v[140:143]\n"
"s_cmp_eq_u32 s61,5242880\n"
BS"v[80:83],v250,s[8:11],s49 offen offset:0 sc1\n"
MF"v[144:147],a[160:163],v[216:219],v[144:147]\n"
"s_cselect_b32 s61,0,s61\n"
RD"v[208:211],v244 offset:36864\n"
MF"v[144:147],a[176:179],v[220:223],v[144:147]\n"
MF"v[148:151],a[180:183],v[220:223],v[148:151]\n"
MF"v[148:151],a[164:167],v[216:219],v[148:151]\n"
MF"v[152:155],a[168:171],v[216:219],v[152:155]\n"
SA"m0,s38,114688\n"
RD"v[212:215],v245 offset:36864\n"
SA"s48,s60,s61\n"
MF"v[152:155],a[184:187],v[220:223],v[152:155]\n"
MF"v[156:159],a[188:191],v[220:223],v[156:159]\n"
BL"v238,s[20:23],s48 offen offset:0 lds\n"
MF"v[156:159],a[172:175],v[216:219],v[156:159]\n"
WT"lgkmcnt(1)\n"
MF"v[128:131],a[192:195],v[192:195],v[128:131]\n"
RD"v[216:219],v244 offset:38912\n"
MF"v[128:131],a[208:211],v[196:199],v[128:131]\n"
MF"v[132:135],a[212:215],v[196:199],v[132:135]\n"
MF"v[132:135],a[196:199],v[192:195],v[132:135]\n"
MF"v[136:139],a[200:203],v[192:195],v[136:139]\n"
RD"v[220:223],v245 offset:38912\n"
MF"v[136:139],a[216:219],v[196:199],v[136:139]\n"
MF"v[140:143],a[220:223],v[196:199],v[140:143]\n"
BL"v239,s[20:23],s48 offen offset:1024 lds\n"
MF"v[140:143],a[204:207],v[192:195],v[140:143]\n"
MF"v[144:147],a[192:195],v[200:203],v[144:147]\n"
WT"vmcnt(13)\n"
BS"v[100:103],v252,s[8:11],s92 offen sc1\n"
MF"v[144:147],a[208:211],v[204:207],v[144:147]\n"
MF"v[148:151],a[212:215],v[204:207],v[148:151]\n"
MF"v[148:151],a[196:199],v[200:203],v[148:151]\n"
MF"v[152:155],a[200:203],v[200:203],v[152:155]\n"
WT"lgkmcnt(0)\n"
MF"v[152:155],a[216:219],v[204:207],v[152:155]\n"
"s_barrier\n"
MF"v[156:159],a[220:223],v[204:207],v[156:159]\n"
RD"v[192:195],v242 offset:16384\n"
MF"v[156:159],a[204:207],v[200:203],v[156:159]\n"
BL"v240,s[20:23],s48 offen offset:2048 lds\n"
MF"v[128:131],a[224:227],v[208:211],v[128:131]\n"
MF"v[128:131],a[240:243],v[212:215],v[128:131]\n"
MF"v[132:135],a[244:247],v[212:215],v[132:135]\n"
RD"v[196:199],v243 offset:16384\n"
MF"v[132:135],a[228:231],v[208:211],v[132:135]\n"
MF"v[136:139],a[232:235],v[208:211],v[136:139]\n"
MF"v[136:139],a[248:251],v[212:215],v[136:139]\n"
MF"v[140:143],a[252:255],v[212:215],v[140:143]\n"
SA"s61,s61,524288\n"
RD"v[200:203],v242 offset:18432\n"
MF"v[140:143],a[236:239],v[208:211],v[140:143]\n"
BS"v[88:91],v250,s[8:11],s49 offen offset:1024 sc1\n"
MF"v[144:147],a[224:227],v[216:219],v[144:147]\n"
"s_lshl_b32 s50,s34,3\n"
BL"v241,s[20:23],s48 offen offset:3072 lds\n"
MF"v[144:147],a[240:243],v[220:223],v[144:147]\n"
SA"s51,s50,s30\n"
MF"v[148:151],a[244:247],v[220:223],v[148:151]\n"
"s_mul_i32 s51,s51,20480\n"
RD"v[204:207],v243 offset:18432\n"
MF"v[148:151],a[228:231],v[216:219],v[148:151]\n"
SA"s56,s51,524288\n"
MF"v[152:155],a[232:235],v[216:219],v[152:155]\n"
SA"m0,s36,16384\n"
SA"s48,s60,s61\n"
SA"s57,s56,81920\n"
MF"v[152:155],a[248:251],v[220:223],v[152:155]\n"
"s_sub_u32 s51,s30,s50\n"
MF"v[156:159],a[252:255],v[220:223],v[156:159]\n"
SA"s51,s51,120\n"
RD"v[208:211],v242 offset:20480\n"
MF"v[156:159],a[236:239],v[216:219],v[156:159]\n"
WT"lgkmcnt(2)\n"
BL"v238,s[20:23],s48 offen offset:0 lds\n"
MF"v[0:3],a[0:3],v[192:195],0\n"
"s_mul_i32 s51,s51,20480\n"
SA"s58,s51,524288\n"
"s_lshl_b32 s99,s99,12\n"
MF"v[0:3],a[16:19],v[196:199],v[0:3]\n"
SA"s59,s58,81920\n"
RD"v[212:215],v243 offset:20480\n"
MF"v[8:11],a[20:23],v[196:199],0\n"
"v_add_u32 v235,s99,v224\n"
MF"v[8:11],a[4:7],v[192:195],v[8:11]\n"
MU"v128,s55,v128\n"
MU"v129,s55,v129\n"
MF"v[16:19],a[8:11],v[192:195],0\n"
MU"v130,s55,v130\n"
MU"v131,s55,v131\n"
MF"v[16:19],a[24:27],v[196:199],v[16:19]\n"
RD"v[216:219],v242 offset:22528\n"
MU"v132,s55,v132\n"
MF"v[24:27],a[28:31],v[196:199],0\n"
BL"v239,s[20:23],s48 offen offset:1024 lds\n"
MF"v[24:27],a[12:15],v[192:195],v[24:27]\n"
MU"v133,s55,v133\n"
MU"v134,s55,v134\n"
MF"v[32:35],a[0:3],v[200:203],0\n"
WT"lgkmcnt(1)\n"
MU"v135,s55,v135\n"
PK"v64,v128,v129\n"
MF"v[32:35],a[16:19],v[204:207],v[32:35]\n"
RD"v[220:223],v243 offset:22528\n"
PK"v65,v130,v131\n"
MF"v[40:43],a[20:23],v[204:207],0\n"
PK"v66,v132,v133\n"
PK"v67,v134,v135\n"
MF"v[40:43],a[4:7],v[200:203],v[40:43]\n"
MF"v[48:51],a[8:11],v[200:203],0\n"
MF"v[48:51],a[24:27],v[204:207],v[48:51]\n"
RD"v[192:195],v242 offset:40960\n"
MU"v136,s55,v136\n"
MF"v[56:59],a[28:31],v[204:207],0\n"
BL"v240,s[20:23],s48 offen offset:2048 lds\n"
MF"v[56:59],a[12:15],v[200:203],v[56:59]\n"
MU"v137,s55,v137\n"
MU"v138,s55,v138\n"
MF"v[0:3],a[32:35],v[208:211],v[0:3]\n"
BL"a[0:3],v235,s[8:11],s56 offen offset:0 sc1\n"
MF"v[0:3],a[48:51],v[212:215],v[0:3]\n"
RD"v[196:199],v243 offset:40960\n"
MU"v139,s55,v139\n"
MF"v[8:11],a[52:55],v[212:215],v[8:11]\n"
MU"v140,s55,v140\n"
MU"v141,s55,v141\n"
MF"v[8:11],a[36:39],v[208:211],v[8:11]\n"
"s_mov_b32 s49,24576\n"
MU"v142,s55,v142\n"
MU"v143,s55,v143\n"
MF"v[16:19],a[40:43],v[208:211],v[16:19]\n"
RD"v[200:203],v242 offset:43008\n"
PK"v68,v136,v137\n"
MF"v[16:19],a[56:59],v[212:215],v[16:19]\n"
BS"v[96:99],v250,s[8:11],s49 offen offset:0 sc1\n"
MF"v[24:27],a[60:63],v[212:215],v[24:27]\n"
SA"s61,s61,524288\n"
BL"v241,s[20:23],s48 offen offset:3072 lds\n"
MF"v[24:27],a[44:47],v[208:211],v[24:27]\n"
WT"lgkmcnt(2)\n"
BL"a[4:7],v235,s[8:11],s57 offen offset:0 sc1\n"
MF"v[32:35],a[32:35],v[216:219],v[32:35]\n"
"s_cmp_eq_u32 s61,5242880\n"
RD"v[204:207],v243 offset:43008\n"
PK"v69,v138,v139\n"
MF"v[32:35],a[48:51],v[220:223],v[32:35]\n"
"s_cselect_b32 s61,0,s61\n"
PK"v70,v140,v141\n"
PK"v71,v142,v143\n"
MF"v[40:43],a[52:55],v[220:223],v[40:43]\n"
MU"v144,s55,v144\n"
MU"v145,s55,v145\n"
MF"v[40:43],a[36:39],v[216:219],v[40:43]\n"
MU"v146,s55,v146\n"
MU"v147,s55,v147\n"
MF"v[48:51],a[40:43],v[216:219],v[48:51]\n"
RD"v[208:211],v242 offset:45056\n"
MU"v148,s55,v148\n"
MF"v[48:51],a[56:59],v[220:223],v[48:51]\n"
BL"a[8:11],v235,s[8:11],s58 offen offset:0 sc1\n"
MF"v[56:59],a[60:63],v[220:223],v[56:59]\n"
MU"v149,s55,v149\n"
MU"v150,s55,v150\n"
MF"v[56:59],a[44:47],v[216:219],v[56:59]\n"
MU"v151,s55,v151\n"
PK"v72,v144,v145\n"
MF"v[0:3],a[64:67],v[192:195],v[0:3]\n"
WT"lgkmcnt(1)\n"
RD"v[212:215],v243 offset:45056\n"
PK"v73,v146,v147\n"
MF"v[0:3],a[80:83],v[196:199],v[0:3]\n"
PK"v74,v148,v149\n"
PK"v75,v150,v151\n"
MF"v[8:11],a[84:87],v[196:199],v[8:11]\n"
BL"a[12:15],v235,s[8:11],s59 offen offset:0 sc1\n"
MF"v[8:11],a[68:71],v[192:195],v[8:11]\n"
RD"v[216:219],v242 offset:47104\n"
MU"v152,s55,v152\n"
MF"v[16:19],a[72:75],v[192:195],v[16:19]\n"
MU"v153,s55,v153\n"
MU"v154,s55,v154\n"
MF"v[16:19],a[88:91],v[196:199],v[16:19]\n"
MU"v155,s55,v155\n"
MU"v156,s55,v156\n"
MF"v[24:27],a[92:95],v[196:199],v[24:27]\n"
MU"v157,s55,v157\n"
MU"v158,s55,v158\n"
MF"v[24:27],a[76:79],v[192:195],v[24:27]\n"
RD"v[220:223],v243 offset:47104\n"
MU"v159,s55,v159\n"
MF"v[32:35],a[64:67],v[200:203],v[32:35]\n"
BL"a[16:19],v235,s[8:11],s56 offen offset:1024 sc1\n"
MF"v[32:35],a[80:83],v[204:207],v[32:35]\n"
PK"v76,v152,v153\n"
PK"v77,v154,v155\n"
MF"v[40:43],a[84:87],v[204:207],v[40:43]\n"
PK"v78,v156,v157\n"
PK"v79,v158,v159\n"
MF"v[40:43],a[68:71],v[200:203],v[40:43]\n"
RD"v[192:195],v244 offset:16384\n"
MF"v[48:51],a[72:75],v[200:203],v[48:51]\n"
MF"v[48:51],a[88:91],v[204:207],v[48:51]\n"
MF"v[56:59],a[92:95],v[204:207],v[56:59]\n"
BL"a[20:23],v235,s[8:11],s57 offen offset:1024 sc1\n"
MF"v[56:59],a[76:79],v[200:203],v[56:59]\n"
WT"lgkmcnt(1)\n"
RD"v[196:199],v245 offset:16384\n"
MF"v[0:3],a[96:99],v[208:211],v[0:3]\n"
MF"v[0:3],a[112:115],v[212:215],v[0:3]\n"
MF"v[8:11],a[116:119],v[212:215],v[8:11]\n"
MF"v[8:11],a[100:103],v[208:211],v[8:11]\n"
RD"v[200:203],v244 offset:18432\n"
MF"v[16:19],a[104:107],v[208:211],v[16:19]\n"
BS"v[104:107],v250,s[8:11],s49 offen offset:1024 sc1\n"
MF"v[16:19],a[120:123],v[212:215],v[16:19]\n"
BL"a[24:27],v235,s[8:11],s58 offen offset:1024 sc1\n"
MF"v[24:27],a[124:127],v[212:215],v[24:27]\n"
RD"v[204:207],v245 offset:18432\n"
MF"v[24:27],a[108:111],v[208:211],v[24:27]\n"
MF"v[32:35],a[96:99],v[216:219],v[32:35]\n"
MF"v[32:35],a[112:115],v[220:223],v[32:35]\n"
MF"v[40:43],a[116:119],v[220:223],v[40:43]\n"
RD"v[208:211],v244 offset:20480\n"
MF"v[40:43],a[100:103],v[216:219],v[40:43]\n"
BL"a[28:31],v235,s[8:11],s59 offen offset:1024 sc1\n"
MF"v[48:51],a[104:107],v[216:219],v[48:51]\n"
MF"v[48:51],a[120:123],v[220:223],v[48:51]\n"
MF"v[56:59],a[124:127],v[220:223],v[56:59]\n"
RD"v[212:215],v245 offset:20480\n"
MF"v[56:59],a[108:111],v[216:219],v[56:59]\n"
WT"lgkmcnt(2)\n"
MF"v[0:3],a[128:131],v[192:195],v[0:3]\n"
BL"a[32:35],v235,s[8:11],s56 offen offset:2048 sc1\n"
MF"v[0:3],a[144:147],v[196:199],v[0:3]\n"
MF"v[8:11],a[148:151],v[196:199],v[8:11]\n"
RD"v[216:219],v244 offset:22528\n"
MF"v[8:11],a[132:135],v[192:195],v[8:11]\n"
MF"v[16:19],a[136:139],v[192:195],v[16:19]\n"
MF"v[16:19],a[152:155],v[196:199],v[16:19]\n"
RD"v[220:223],v245 offset:22528\n"
MF"v[24:27],a[156:159],v[196:199],v[24:27]\n"
BL"a[36:39],v235,s[8:11],s57 offen offset:2048 sc1\n"
MF"v[24:27],a[140:143],v[192:195],v[24:27]\n"
MF"v[32:35],a[128:131],v[200:203],v[32:35]\n"
MF"v[32:35],a[144:147],v[204:207],v[32:35]\n"
RD"v[192:195],v244 offset:40960\n"
MF"v[40:43],a[148:151],v[204:207],v[40:43]\n"
MF"v[40:43],a[132:135],v[200:203],v[40:43]\n"
MF"v[48:51],a[136:139],v[200:203],v[48:51]\n"
BL"a[40:43],v235,s[8:11],s58 offen offset:2048 sc1\n"
MF"v[48:51],a[152:155],v[204:207],v[48:51]\n"
RD"v[196:199],v245 offset:40960\n"
MF"v[56:59],a[156:159],v[204:207],v[56:59]\n"
MF"v[56:59],a[140:143],v[200:203],v[56:59]\n"
WT"lgkmcnt(2)\n"
MF"v[0:3],a[160:163],v[208:211],v[0:3]\n"
"s_sub_u32 s53,s37,65536\n"
MF"v[0:3],a[176:179],v[212:215],v[0:3]\n"
"s_mov_b32 s49,28672\n"
RD"v[200:203],v244 offset:43008\n"
MF"v[8:11],a[180:183],v[212:215],v[8:11]\n"
BL"a[44:47],v235,s[8:11],s59 offen offset:2048 sc1\n"
MF"v[8:11],a[164:167],v[208:211],v[8:11]\n"
BS"v[112:115],v250,s[8:11],s49 offen offset:0 sc1\n"
MF"v[16:19],a[168:171],v[208:211],v[16:19]\n"
"v_subrev_u32 v246,s53,v246\n"
"v_subrev_u32 v247,s53,v247\n"
MF"v[16:19],a[184:187],v[212:215],v[16:19]\n"
RD"v[204:207],v245 offset:43008\n"
"v_subrev_u32 v248,s53,v248\n"
MF"v[24:27],a[188:191],v[212:215],v[24:27]\n"
"v_subrev_u32 v249,s53,v249\n"
"v_subrev_u32 v236,65536,v246\n"
MF"v[24:27],a[172:175],v[208:211],v[24:27]\n"
"v_subrev_u32 v237,65536,v247\n"
"v_subrev_u32 v228,65536,v248\n"
MF"v[32:35],a[160:163],v[216:219],v[32:35]\n"
RD"v[208:211],v244 offset:45056\n"
"v_subrev_u32 v229,65536,v249\n"
MF"v[32:35],a[176:179],v[220:223],v[32:35]\n"
BL"a[48:51],v235,s[8:11],s56 offen offset:3072 sc1\n"
MF"v[40:43],a[180:183],v[220:223],v[40:43]\n"
MF"v[40:43],a[164:167],v[216:219],v[40:43]\n"
MF"v[48:51],a[168:171],v[216:219],v[48:51]\n"
RD"v[212:215],v245 offset:45056\n"
MF"v[48:51],a[184:187],v[220:223],v[48:51]\n"
MF"v[56:59],a[188:191],v[220:223],v[56:59]\n"
BL"a[52:55],v235,s[8:11],s57 offen offset:3072 sc1\n"
MF"v[56:59],a[172:175],v[216:219],v[56:59]\n"
WT"lgkmcnt(1)\n"
MF"v[0:3],a[192:195],v[192:195],v[0:3]\n"
RD"v[216:219],v244 offset:47104\n"
MF"v[0:3],a[208:211],v[196:199],v[0:3]\n"
MF"v[8:11],a[212:215],v[196:199],v[8:11]\n"
MF"v[8:11],a[196:199],v[192:195],v[8:11]\n"
MF"v[16:19],a[200:203],v[192:195],v[16:19]\n"
RD"v[220:223],v245 offset:47104\n"
MF"v[16:19],a[216:219],v[196:199],v[16:19]\n"
BL"a[56:59],v235,s[8:11],s58 offen offset:3072 sc1\n"
MF"v[24:27],a[220:223],v[196:199],v[24:27]\n"
MF"v[24:27],a[204:207],v[192:195],v[24:27]\n"
MF"v[32:35],a[192:195],v[200:203],v[32:35]\n"
SA"s99,s99,4096\n"
MF"v[32:35],a[208:211],v[204:207],v[32:35]\n"
"s_cmp_eq_u32 s99,20480\n"
"s_cselect_b32 s99,0,s99\n"
MF"v[40:43],a[212:215],v[204:207],v[40:43]\n"
BL"a[60:63],v235,s[8:11],s59 offen offset:3072 sc1\n"
MF"v[40:43],a[196:199],v[200:203],v[40:43]\n"
MF"v[48:51],a[200:203],v[200:203],v[48:51]\n"
WT"lgkmcnt(0)\n"
MF"v[48:51],a[216:219],v[204:207],v[48:51]\n"
"s_barrier\n"
MF"v[56:59],a[220:223],v[204:207],v[56:59]\n"
RT"a[128:129],v236 offset:49152\n"
MF"v[56:59],a[204:207],v[200:203],v[56:59]\n"
"v_add_u32 v235,s99,v224\n"
MF"v[0:3],a[224:227],v[208:211],v[0:3]\n"
SA"m0,s38,73728\n"
RT"a[130:131],v236 offset:49664\n"
SA"s48,s60,s61\n"
MF"v[0:3],a[240:243],v[212:215],v[0:3]\n"
BL"a[64:67],v235,s[8:11],s56 offen offset:0 sc1\n"
MF"v[8:11],a[244:247],v[212:215],v[8:11]\n"
RT"a[132:133],v236 offset:57344\n"
"v_add_u32 v242,49152,v246\n"
MF"v[8:11],a[228:231],v[208:211],v[8:11]\n"
BS"v[120:123],v250,s[8:11],s49 offen offset:1024 sc1\n"
MF"v[16:19],a[232:235],v[208:211],v[16:19]\n"
BL"v238,s[20:23],s48 offen offset:0 lds\n"
MF"v[16:19],a[248:251],v[212:215],v[16:19]\n"
RT"a[134:135],v236 offset:57856\n"
"v_add_u32 v243,49152,v247\n"
MF"v[24:27],a[252:255],v[212:215],v[24:27]\n"
RT"a[136:137],v237 offset:49152\n"
"v_add_u32 v244,49152,v248\n"
MF"v[24:27],a[236:239],v[208:211],v[24:27]\n"
BL"a[68:71],v235,s[8:11],s57 offen offset:0 sc1\n"
MF"v[32:35],a[224:227],v[216:219],v[32:35]\n"
RT"a[138:139],v237 offset:49664\n"
"v_add_u32 v245,49152,v249\n"
MF"v[32:35],a[240:243],v[220:223],v[32:35]\n"
MF"v[40:43],a[244:247],v[220:223],v[40:43]\n"
RT"a[140:141],v237 offset:57344\n"
MF"v[40:43],a[228:231],v[216:219],v[40:43]\n"
BL"v239,s[20:23],s48 offen offset:1024 lds\n"
MF"v[48:51],a[232:235],v[216:219],v[48:51]\n"
RT"a[142:143],v237 offset:57856\n"
MF"v[48:51],a[248:251],v[220:223],v[48:51]\n"
BL"a[72:75],v235,s[8:11],s58 offen offset:0 sc1\n"
MF"v[56:59],a[252:255],v[220:223],v[56:59]\n"
RT"a[144:145],v228 offset:49152\n"
MF"v[56:59],a[236:239],v[216:219],v[56:59]\n"
WT"vmcnt(25) lgkmcnt(3)\n"
MF"v[80:83],a[128:131],a[0:3],0\n"
WT"vmcnt(19)\n"
RT"a[146:147],v228 offset:49664\n"
MF"v[80:83],a[132:135],a[16:19],v[80:83]\n"
WT"vmcnt(18)\n"
MF"v[88:91],a[132:135],a[20:23],0\n"
RT"a[148:149],v228 offset:57344\n"
MF"v[88:91],a[128:131],a[4:7],v[88:91]\n"
BL"v240,s[20:23],s48 offen offset:2048 lds\n"
MF"v[96:99],a[128:131],a[8:11],0\n"
WT"vmcnt(17)\n"
BL"a[76:79],v235,s[8:11],s59 offen offset:0 sc1\n"
MF"v[96:99],a[132:135],a[24:27],v[96:99]\n"
WT"vmcnt(17)\n"
RT"a[150:151],v228 offset:57856\n"
MU"v0,s55,v0\n"
MF"v[104:107],a[132:135],a[28:31],0\n"
RT"a[152:153],v229 offset:49152\n"
MU"v1,s55,v1\n"
MF"v[104:107],a[128:131],a[12:15],v[104:107]\n"
RT"a[154:155],v229 offset:49664\n"
MU"v2,s55,v2\n"
MF"v[84:87],a[136:139],a[0:3],0\n"
WT"lgkmcnt(4)\n"
MU"v3,s55,v3\n"
MU"v8,s55,v8\n"
MF"v[84:87],a[140:143],a[16:19],v[84:87]\n"
RT"a[156:157],v229 offset:57344\n"
MU"v9,s55,v9\n"
MF"v[92:95],a[140:143],a[20:23],0\n"
SA"s61,s61,524288\n"
BL"a[80:83],v235,s[8:11],s56 offen offset:1024 sc1\n"
MF"v[92:95],a[136:139],a[4:7],v[92:95]\n"
BL"v241,s[20:23],s48 offen offset:3072 lds\n"
MF"v[100:103],a[136:139],a[8:11],0\n"
RT"a[158:159],v229 offset:57856\n"
MU"v10,s55,v10\n"
MF"v[100:103],a[140:143],a[24:27],v[100:103]\n"
RT"a[160:161],v236 offset:53248\n"
MU"v11,s55,v11\n"
MF"v[108:111],a[140:143],a[28:31],0\n"
PK"v0,v0,v1\n"
PK"v1,v2,v3\n"
MF"v[108:111],a[136:139],a[12:15],v[108:111]\n"
RT"a[162:163],v236 offset:53760\n"
PK"v2,v8,v9\n"
MF"v[112:115],a[144:147],a[0:3],0\n"
WT"lgkmcnt(3)\n"
BL"a[84:87],v235,s[8:11],s57 offen offset:1024 sc1\n"
MF"v[112:115],a[148:151],a[16:19],v[112:115]\n"
RT"a[164:165],v236 offset:61440\n"
PK"v3,v10,v11\n"
MF"v[120:123],a[148:151],a[20:23],0\n"
MU"v16,s55,v16\n"
MU"v17,s55,v17\n"
MF"v[120:123],a[144:147],a[4:7],v[120:123]\n"
RT"a[166:167],v236 offset:61952\n"
MU"v18,s55,v18\n"
MF"v[128:131],a[144:147],a[8:11],0\n"
MU"v19,s55,v19\n"
MU"v24,s55,v24\n"
MF"v[128:131],a[148:151],a[24:27],v[128:131]\n"
RT"a[168:169],v237 offset:53248\n"
MU"v25,s55,v25\n"
MF"v[136:139],a[148:151],a[28:31],0\n"
MU"v26,s55,v26\n"
MU"v27,s55,v27\n"
MF"v[136:139],a[144:147],a[12:15],v[136:139]\n"
RT"a[170:171],v237 offset:53760\n"
PK"v16,v16,v17\n"
MF"v[116:119],a[152:155],a[0:3],0\n"
WT"lgkmcnt(3)\n"
BL"a[88:91],v235,s[8:11],s58 offen offset:1024 sc1\n"
MF"v[116:119],a[156:159],a[16:19],v[116:119]\n"
"s_mov_b32 s49,262144\n"
RT"a[172:173],v237 offset:61440\n"
PK"v17,v18,v19\n"
MF"v[124:127],a[156:159],a[20:23],0\n"
RT"a[174:175],v237 offset:61952\n"
PK"v18,v24,v25\n"
MF"v[124:127],a[152:155],a[4:7],v[124:127]\n"
BS"v[4:7],v250,s[8:11],s49 offen offset:0 sc1\n"
MF"v[132:135],a[152:155],a[8:11],0\n"
RT"a[176:177],v228 offset:53248\n"
PK"v19,v26,v27\n"
MF"v[132:135],a[156:159],a[24:27],v[132:135]\n"
MU"v32,s55,v32\n"
MU"v33,s55,v33\n"
MF"v[140:143],a[156:159],a[28:31],0\n"
RT"a[178:179],v228 offset:53760\n"
MU"v34,s55,v34\n"
MF"v[140:143],a[152:155],a[12:15],v[140:143]\n"
BL"a[92:95],v235,s[8:11],s59 offen offset:1024 sc1\n"
MF"v[144:147],a[160:163],a[0:3],0\n"
WT"lgkmcnt(3)\n"
RT"a[180:181],v228 offset:61440\n"
MU"v35,s55,v35\n"
MF"v[144:147],a[164:167],a[16:19],v[144:147]\n"
MU"v40,s55,v40\n"
MU"v41,s55,v41\n"
MF"v[152:155],a[164:167],a[20:23],0\n"
SA"m0,s38,122880\n"
RT"a[182:183],v228 offset:61952\n"
MU"v42,s55,v42\n"
MF"v[152:155],a[160:163],a[4:7],v[152:155]\n"
SA"s48,s60,s61\n"
MU"v43,s55,v43\n"
PK"v32,v32,v33\n"
MF"v[160:163],a[160:163],a[8:11],0\n"
"s_mov_b32 s88,4096\n"
RT"a[184:185],v229 offset:53248\n"
PK"v33,v34,v35\n"
MF"v[160:163],a[164:167],a[24:27],v[160:163]\n"
BL"v238,s[20:23],s48 offen offset:0 lds\n"
MF"v[168:171],a[164:167],a[28:31],0\n"
BL"a[96:99],v235,s[8:11],s56 offen offset:2048 sc1\n"
MF"v[168:171],a[160:163],a[12:15],v[168:171]\n"
RT"a[186:187],v229 offset:53760\n"
PK"v34,v40,v41\n"
MF"v[148:151],a[168:171],a[0:3],0\n"
WT"lgkmcnt(3)\n"
RT"a[188:189],v229 offset:61440\n"
PK"v35,v42,v43\n"
MF"v[148:151],a[172:175],a[16:19],v[148:151]\n"
MU"v48,s55,v48\n"
MU"v49,s55,v49\n"
MF"v[156:159],a[172:175],a[20:23],0\n"
RT"a[190:191],v229 offset:61952\n"
MU"v50,s55,v50\n"
MF"v[156:159],a[168:171],a[4:7],v[156:159]\n"
RT"a[128:129],v236 offset:32768\n"
MU"v51,s55,v51\n"
MF"v[164:167],a[168:171],a[8:11],0\n"
BL"v239,s[20:23],s48 offen offset:1024 lds\n"
MF"v[164:167],a[172:175],a[24:27],v[164:167]\n"
BL"a[100:103],v235,s[8:11],s57 offen offset:2048 sc1\n"
MF"v[172:175],a[172:175],a[28:31],0\n"
RT"a[130:131],v236 offset:33280\n"
MU"v56,s55,v56\n"
MF"v[172:175],a[168:171],a[12:15],v[172:175]\n"
RT"a[132:133],v236 offset:40960\n"
MU"v57,s55,v57\n"
MF"v[176:179],a[176:179],a[0:3],0\n"
WT"lgkmcnt(4)\n"
MU"v58,s55,v58\n"
MU"v59,s55,v59\n"
MF"v[176:179],a[180:183],a[16:19],v[176:179]\n"
RT"a[134:135],v236 offset:41472\n"
PK"v48,v48,v49\n"
MF"v[184:187],a[180:183],a[20:23],0\n"
PK"v49,v50,v51\n"
PK"v50,v56,v57\n"
MF"v[184:187],a[176:179],a[4:7],v[184:187]\n"
RT"a[136:137],v237 offset:32768\n"
PK"v51,v58,v59\n"
MF"v[192:195],a[176:179],a[8:11],0\n"
BL"a[104:107],v235,s[8:11],s58 offen offset:2048 sc1\n"
MF"v[192:195],a[180:183],a[24:27],v[192:195]\n"
BL"v240,s[20:23],s48 offen offset:2048 lds\n"
MF"v[200:203],a[180:183],a[28:31],0\n"
RT"a[138:139],v237 offset:33280\n"
MF"v[200:203],a[176:179],a[12:15],v[200:203]\n"
RT"a[140:141],v237 offset:40960\n"
MF"v[180:183],a[184:187],a[0:3],0\n"
WT"lgkmcnt(3)\n"
MF"v[180:183],a[188:191],a[16:19],v[180:183]\n"
RT"a[142:143],v237 offset:41472\n"
MF"v[188:191],a[188:191],a[20:23],0\n"
BL"a[108:111],v235,s[8:11],s59 offen offset:2048 sc1\n"
MF"v[188:191],a[184:187],a[4:7],v[188:191]\n"
RT"a[144:145],v228 offset:32768\n"
MF"v[196:199],a[184:187],a[8:11],0\n"
SA"s61,s61,524288\n"
BS"v[12:15],v250,s[8:11],s49 offen offset:1024 sc1\n"
MF"v[196:199],a[188:191],a[24:27],v[196:199]\n"
"s_cmp_eq_u32 s61,5242880\n"
BL"v241,s[20:23],s48 offen offset:3072 lds\n"
MF"v[204:207],a[188:191],a[28:31],0\n"
"s_cselect_b32 s61,0,s61\n"
RT"a[146:147],v228 offset:33280\n"
MF"v[204:207],a[184:187],a[12:15],v[204:207]\n"
WT"vmcnt(31)\n"
RT"a[148:149],v228 offset:40960\n"
MF"v[80:83],a[128:131],a[32:35],v[80:83]\n"
WT"vmcnt(26)\n"
MF"v[80:83],a[132:135],a[48:51],v[80:83]\n"
WT"vmcnt(25)\n"
RT"a[150:151],v228 offset:41472\n"
MF"v[88:91],a[132:135],a[52:55],v[88:91]\n"
BL"a[112:115],v235,s[8:11],s56 offen offset:3072 sc1\n"
MF"v[88:91],a[128:131],a[36:39],v[88:91]\n"
RT"a[152:153],v229 offset:32768\n"
MF"v[96:99],a[128:131],a[40:43],v[96:99]\n"
WT"vmcnt(25)\n"
RT"a[154:155],v229 offset:33280\n"
MF"v[96:99],a[132:135],a[56:59],v[96:99]\n"
WT"vmcnt(24)\n"
MF"v[104:107],a[132:135],a[60:63],v[104:107]\n"
RT"a[156:157],v229 offset:40960\n"
MF"v[104:107],a[128:131],a[44:47],v[104:107]\n"
WT"lgkmcnt(4)\n"
MF"v[84:87],a[136:139],a[32:35],v[84:87]\n"
RT"a[158:159],v229 offset:41472\n"
MF"v[84:87],a[140:143],a[48:51],v[84:87]\n"
BL"a[116:119],v235,s[8:11],s57 offen offset:3072 sc1\n"
MF"v[92:95],a[140:143],a[52:55],v[92:95]\n"
RT"a[160:161],v236 offset:36864\n"
MF"v[92:95],a[136:139],a[36:39],v[92:95]\n"
MF"v[100:103],a[136:139],a[40:43],v[100:103]\n"
RT"a[162:163],v236 offset:37376\n"
MF"v[100:103],a[140:143],a[56:59],v[100:103]\n"
MF"v[108:111],a[140:143],a[60:63],v[108:111]\n"
RT"a[164:165],v236 offset:45056\n"
MF"v[108:111],a[136:139],a[44:47],v[108:111]\n"
BL"a[120:123],v235,s[8:11],s58 offen offset:3072 sc1\n"
MF"v[112:115],a[144:147],a[32:35],v[112:115]\n"
WT"lgkmcnt(3)\n"
RT"a[166:167],v236 offset:45568\n"
MF"v[112:115],a[148:151],a[48:51],v[112:115]\n"
MF"v[120:123],a[148:151],a[52:55],v[120:123]\n"
RT"a[168:169],v237 offset:36864\n"
MF"v[120:123],a[144:147],a[36:39],v[120:123]\n"
MF"v[128:131],a[144:147],a[40:43],v[128:131]\n"
RT"a[170:171],v237 offset:37376\n"
MF"v[128:131],a[148:151],a[56:59],v[128:131]\n"
SA"s99,s99,4096\n"
RT"a[172:173],v237 offset:45056\n"
MF"v[136:139],a[148:151],a[60:63],v[136:139]\n"
"s_cmp_eq_u32 s99,20480\n"
BL"a[124:127],v235,s[8:11],s59 offen offset:3072 sc1\n"
MF"v[136:139],a[144:147],a[44:47],v[136:139]\n"
"s_cselect_b32 s99,0,s99\n"
RT"a[174:175],v237 offset:45568\n"
MF"v[116:119],a[152:155],a[32:35],v[116:119]\n"
MF"v[116:119],a[156:159],a[48:51],v[116:119]\n"
RT"a[176:177],v228 offset:36864\n"
MF"v[124:127],a[156:159],a[52:55],v[124:127]\n"
MF"v[124:127],a[152:155],a[36:39],v[124:127]\n"
RT"a[178:179],v228 offset:37376\n"
MF"v[132:135],a[152:155],a[40:43],v[132:135]\n"
"s_mov_b32 s49,266240\n"
"v_add_u32 v235,s99,v224\n"
MF"v[132:135],a[156:159],a[56:59],v[132:135]\n"
RT"a[180:181],v228 offset:45056\n"
MF"v[140:143],a[156:159],a[60:63],v[140:143]\n"
BL"a[0:3],v235,s[8:11],s56 offen offset:0 sc1\n"
MF"v[140:143],a[152:155],a[44:47],v[140:143]\n"
WT"lgkmcnt(3)\n"
BS"v[20:23],v250,s[8:11],s49 offen offset:0 sc1\n"
MF"v[144:147],a[160:163],a[32:35],v[144:147]\n"
RT"a[182:183],v228 offset:45568\n"
MF"v[144:147],a[164:167],a[48:51],v[144:147]\n"
RT"a[184:185],v229 offset:36864\n"
MF"v[152:155],a[164:167],a[52:55],v[152:155]\n"
MF"v[152:155],a[160:163],a[36:39],v[152:155]\n"
RT"a[186:187],v229 offset:37376\n"
MF"v[160:163],a[160:163],a[40:43],v[160:163]\n"
BL"a[4:7],v235,s[8:11],s57 offen offset:0 sc1\n"
MF"v[160:163],a[164:167],a[56:59],v[160:163]\n"
RT"a[188:189],v229 offset:45056\n"
MF"v[168:171],a[164:167],a[60:63],v[168:171]\n"
MF"v[168:171],a[160:163],a[44:47],v[168:171]\n"
RT"a[190:191],v229 offset:45568\n"
MF"v[148:151],a[168:171],a[32:35],v[148:151]\n"
"s_lshl_b32 s50,s35,3\n"
SA"s50,s50,s34\n"
"s_lshl_b32 s50,s50,3\n"
MF"v[148:151],a[172:175],a[48:51],v[148:151]\n"
SA"s50,s50,2304\n"
SA"s51,s50,s45\n"
MF"v[156:159],a[172:175],a[52:55],v[156:159]\n"
MF"v[156:159],a[168:171],a[36:39],v[156:159]\n"
BL"a[8:11],v235,s[8:11],s58 offen offset:0 sc1\n"
MF"v[164:167],a[168:171],a[40:43],v[164:167]\n"
WT"vmcnt(29) lgkmcnt(0)\n"
MF"v[164:167],a[172:175],a[56:59],v[164:167]\n"
"s_barrier\n"
MF"v[172:175],a[172:175],a[60:63],v[172:175]\n"
RT"a[128:129],v236 offset:0\n"
MF"v[172:175],a[168:171],a[44:47],v[172:175]\n"
"buffer_store_dwordx2 v[232:233],v230,s[8:11],s51 offen sc1\n"
MF"v[176:179],a[176:179],a[32:35],v[176:179]\n"
RT"a[130:131],v236 offset:512\n"
MF"v[176:179],a[180:183],a[48:51],v[176:179]\n"
MF"v[184:187],a[180:183],a[52:55],v[184:187]\n"
SA"m0,s38,81920\n"
RT"a[132:133],v236 offset:8192\n"
SA"s48,s60,s61\n"
MF"v[184:187],a[176:179],a[36:39],v[184:187]\n"
BL"a[12:15],v235,s[8:11],s59 offen offset:0 sc1\n"
MF"v[192:195],a[176:179],a[40:43],v[192:195]\n"
RT"a[134:135],v236 offset:8704\n"
MF"v[192:195],a[180:183],a[56:59],v[192:195]\n"
BL"v238,s[20:23],s48 offen offset:0 lds\n"
MF"v[200:203],a[180:183],a[60:63],v[200:203]\n"
RT"a[136:137],v237 offset:0\n"
MF"v[200:203],a[176:179],a[44:47],v[200:203]\n"
MF"v[180:183],a[184:187],a[32:35],v[180:183]\n"
RT"a[138:139],v237 offset:512\n"
MF"v[180:183],a[188:191],a[48:51],v[180:183]\n"
BL"a[16:19],v235,s[8:11],s56 offen offset:1024 sc1\n"
MF"v[188:191],a[188:191],a[52:55],v[188:191]\n"
RT"a[140:141],v237 offset:8192\n"
MF"v[188:191],a[184:187],a[36:39],v[188:191]\n"
MF"v[196:199],a[184:187],a[40:43],v[196:199]\n"
RT"a[142:143],v237 offset:8704\n"
MF"v[196:199],a[188:191],a[56:59],v[196:199]\n"
BL"v239,s[20:23],s48 offen offset:1024 lds\n"
MF"v[204:207],a[188:191],a[60:63],v[204:207]\n"
SA"s49,s88,262144\n"
RT"a[144:145],v228 offset:0\n"
MF"v[204:207],a[184:187],a[44:47],v[204:207]\n"
"s_branch LQ_%=\n"
".p2align 6\n"
"LV_%=:\n"
WT"vmcnt(17)\n"
"v_lshlrev_b32 v148,16,v128\n"
"v_and_b32 v149,0xffff0000,v128\n"
"v_lshlrev_b32 v150,16,v129\n"
"v_and_b32 v151,0xffff0000,v129\n"
WT"vmcnt(16)\n"
"v_lshlrev_b32 v152,16,v130\n"
"v_and_b32 v153,0xffff0000,v130\n"
"v_lshlrev_b32 v154,16,v131\n"
"v_and_b32 v155,0xffff0000,v131\n"
WT"vmcnt(15)\n"
"v_lshlrev_b32 v156,16,v132\n"
"v_and_b32 v157,0xffff0000,v132\n"
"v_lshlrev_b32 v158,16,v133\n"
"v_and_b32 v159,0xffff0000,v133\n"
WT"vmcnt(14)\n"
"v_lshlrev_b32 v160,16,v134\n"
"v_and_b32 v161,0xffff0000,v134\n"
"v_lshlrev_b32 v162,16,v135\n"
"v_and_b32 v163,0xffff0000,v135\n"
WT"vmcnt(13)\n"
"v_lshlrev_b32 v164,16,v136\n"
"v_and_b32 v165,0xffff0000,v136\n"
"v_lshlrev_b32 v166,16,v137\n"
"v_and_b32 v167,0xffff0000,v137\n"
"v_add_f32 v188,v148,v149\n"
"v_add_f32 v189,v150,v151\n"
"v_add_f32 v188,v188,v152\n"
"v_add_f32 v189,v189,v153\n"
"v_add_f32 v188,v188,v154\n"
"v_add_f32 v189,v189,v155\n"
"v_add_f32 v188,v188,v156\n"
"v_add_f32 v189,v189,v157\n"
"v_add_f32 v188,v188,v158\n"
"v_add_f32 v189,v189,v159\n"
"v_add_f32 v188,v188,v160\n"
"v_add_f32 v189,v189,v161\n"
"v_add_f32 v188,v188,v162\n"
"v_add_f32 v189,v189,v163\n"
"v_add_f32 v188,v188,v164\n"
"v_add_f32 v189,v189,v165\n"
"v_add_f32 v188,v188,v166\n"
"v_add_f32 v189,v189,v167\n"
"v_add_f32 v188,v188,v189\n"
"s_nop 1\n"
"v_add_f32_dpp v188,v188,v188 quad_perm:[1,0,3,2] row_mask:0xf bank_mask:0xf\n"
WT"vmcnt(12)\n"
"v_lshlrev_b32 v168,16,v138\n"
"v_and_b32 v169,0xffff0000,v138\n"
"v_add_f32_dpp v188,v188,v188 quad_perm:[2,3,0,1] row_mask:0xf bank_mask:0xf\n"
"v_lshlrev_b32 v170,16,v139\n"
"v_and_b32 v171,0xffff0000,v139\n"
"v_add_f32_dpp v188,v188,v188 row_half_mirror row_mask:0xf bank_mask:0xf\n"
WT"vmcnt(11)\n"
"v_lshlrev_b32 v172,16,v140\n"
"v_and_b32 v173,0xffff0000,v140\n"
"v_add_f32_dpp v188,v188,v188 row_mirror row_mask:0xf bank_mask:0xf\n"
"v_lshlrev_b32 v174,16,v141\n"
"v_and_b32 v175,0xffff0000,v141\n"
"v_mov_b32 v189,v188\n"
WT"vmcnt(10)\n"
"v_lshlrev_b32 v176,16,v142\n"
"v_and_b32 v177,0xffff0000,v142\n"
"v_permlane16_swap_b32 v189,v188\n"
"v_lshlrev_b32 v178,16,v143\n"
"v_and_b32 v179,0xffff0000,v143\n"
"v_add_f32 v188,v188,v189\n"
WT"vmcnt(9)\n"
"v_lshlrev_b32 v180,16,v144\n"
SA"m0,s36,0\n"
"s_bfe_u32 s48,s46,0x30000\n"
"s_lshl_b32 s48,s48,7\n"
BL"v234,s[12:15],s48 offen offset:0 lds\n"
BL"v235,s[12:15],s48 offen offset:1024 lds\n"
BL"v236,s[12:15],s48 offen offset:2048 lds\n"
BL"v237,s[12:15],s48 offen offset:3072 lds\n"
"v_and_b32 v181,0xffff0000,v144\n"
MU"v191,0x3acccccd,v188\n"
"v_lshlrev_b32 v182,16,v145\n"
"v_and_b32 v183,0xffff0000,v145\n"
PK"v128,v191,v191\n"
WT"vmcnt(12)\n"
"v_lshlrev_b32 v184,16,v146\n"
"v_and_b32 v185,0xffff0000,v146\n"
"v_lshlrev_b32 v128,16,v128\n"
"v_lshlrev_b32 v186,16,v147\n"
"v_and_b32 v187,0xffff0000,v147\n"
"v_sub_f32 v148,v148,v128\n"
"v_sub_f32 v149,v149,v128\n"
"v_sub_f32 v150,v150,v128\n"
"v_sub_f32 v151,v151,v128\n"
"v_sub_f32 v152,v152,v128\n"
"v_sub_f32 v153,v153,v128\n"
"v_sub_f32 v154,v154,v128\n"
"v_sub_f32 v155,v155,v128\n"
"v_sub_f32 v156,v156,v128\n"
"v_sub_f32 v157,v157,v128\n"
"v_sub_f32 v158,v158,v128\n"
"v_sub_f32 v159,v159,v128\n"
"v_sub_f32 v160,v160,v128\n"
"v_sub_f32 v161,v161,v128\n"
"v_sub_f32 v162,v162,v128\n"
"v_sub_f32 v163,v163,v128\n"
"v_sub_f32 v164,v164,v128\n"
"v_sub_f32 v165,v165,v128\n"
"v_sub_f32 v166,v166,v128\n"
"v_sub_f32 v167,v167,v128\n"
MU"v188,v148,v148\n"
MU"v189,v149,v149\n"
"v_fmac_f32 v188,v150,v150\n"
"v_fmac_f32 v189,v151,v151\n"
"v_fmac_f32 v188,v152,v152\n"
"v_fmac_f32 v189,v153,v153\n"
"v_fmac_f32 v188,v154,v154\n"
"v_fmac_f32 v189,v155,v155\n"
"v_fmac_f32 v188,v156,v156\n"
"v_fmac_f32 v189,v157,v157\n"
"v_fmac_f32 v188,v158,v158\n"
"v_fmac_f32 v189,v159,v159\n"
"v_fmac_f32 v188,v160,v160\n"
"v_fmac_f32 v189,v161,v161\n"
"v_fmac_f32 v188,v162,v162\n"
"v_fmac_f32 v189,v163,v163\n"
"v_fmac_f32 v188,v164,v164\n"
"v_fmac_f32 v189,v165,v165\n"
"v_fmac_f32 v188,v166,v166\n"
"v_fmac_f32 v189,v167,v167\n"
"v_add_f32 v188,v188,v189\n"
PK"v149,v148,v149\n"
"v_lshlrev_b32 v148,16,v149\n"
"v_add_f32_dpp v188,v188,v188 quad_perm:[1,0,3,2] row_mask:0xf bank_mask:0xf\n"
"v_and_b32 v149,0xffff0000,v149\n"
PK"v151,v150,v151\n"
"v_add_f32_dpp v188,v188,v188 quad_perm:[2,3,0,1] row_mask:0xf bank_mask:0xf\n"
"v_lshlrev_b32 v150,16,v151\n"
"v_and_b32 v151,0xffff0000,v151\n"
SA"m0,s37,8192\n"
"s_bfe_u32 s48,s46,0x30004\n"
"s_lshl_b32 s48,s48,19\n"
BL"v238,s[16:19],s48 offen offset:0 lds\n"
BL"v239,s[16:19],s48 offen offset:1024 lds\n"
BL"v240,s[16:19],s48 offen offset:2048 lds\n"
BL"v241,s[16:19],s48 offen offset:3072 lds\n"
"v_add_f32_dpp v188,v188,v188 row_half_mirror row_mask:0xf bank_mask:0xf\n"
PK"v153,v152,v153\n"
"v_lshlrev_b32 v152,16,v153\n"
"v_add_f32_dpp v188,v188,v188 row_mirror row_mask:0xf bank_mask:0xf\n"
"v_and_b32 v153,0xffff0000,v153\n"
PK"v155,v154,v155\n"
"v_mov_b32 v189,v188\n"
"v_lshlrev_b32 v154,16,v155\n"
"v_and_b32 v155,0xffff0000,v155\n"
"v_permlane16_swap_b32 v189,v188\n"
PK"v157,v156,v157\n"
"v_lshlrev_b32 v156,16,v157\n"
"v_add_f32 v188,v188,v189\n"
"v_and_b32 v157,0xffff0000,v157\n"
PK"v159,v158,v159\n"
"v_sub_f32 v191,v191,v128\n"
"v_lshlrev_b32 v158,16,v159\n"
"v_and_b32 v159,0xffff0000,v159\n"
MU"v188,0x3acccccd,v188\n"
PK"v161,v160,v161\n"
"v_lshlrev_b32 v160,16,v161\n"
"v_fma_f32 v188,-v191,v191,v188\n"
"v_and_b32 v161,0xffff0000,v161\n"
PK"v163,v162,v163\n"
"v_max_f32 v188,0,v188\n"
"v_lshlrev_b32 v162,16,v163\n"
"v_and_b32 v163,0xffff0000,v163\n"
PK"v188,v188,v188\n"
PK"v165,v164,v165\n"
"v_lshlrev_b32 v164,16,v165\n"
"v_lshlrev_b32 v188,16,v188\n"
"v_and_b32 v165,0xffff0000,v165\n"
PK"v167,v166,v167\n"
"v_add_f32 v188,0x3727c5ac,v188\n"
"v_lshlrev_b32 v166,16,v167\n"
"v_and_b32 v167,0xffff0000,v167\n"
PK"v188,v188,v188\n"
"v_bfe_u32 v130,v224,5,4\n"
"v_mul_u32_u24 v130,144,v130\n"
"v_lshlrev_b32 v188,16,v188\n"
"v_bfe_u32 v131,v224,4,1\n"
"v_lshl_add_u32 v130,v131,3,v130\n"
"v_sqrt_f32 v189,v188\n"
"v_lshrrev_b32 v131,9,v224\n"
"v_lshl_add_u32 v130,v131,4,v130\n"
"v_rcp_f32 v190,v189\n"
"s_lshl_b32 s52,s30,5\n"
"v_add_u32 v130,s52,v130\n"
"v_fma_f32 v188,-v189,v189,v188\n"
MU"v190,0.5,v190\n"
"v_fma_f32 v189,v188,v190,v189\n"
PK"v189,v189,v189\n"
"v_lshlrev_b32 v189,16,v189\n"
"v_rcp_f32 v129,v189\n"
"s_nop 0\n"
MU"v148,v148,v129\n"
MU"v149,v149,v129\n"
PK"v138,v148,v149\n"
MU"v150,v150,v129\n"
MU"v151,v151,v129\n"
SA"m0,s37,12288\n"
"s_bfe_u32 s48,s46,0x30004\n"
"s_lshl_b32 s48,s48,19\n"
"s_bitset1_b32 s48,18\n"
BL"v238,s[16:19],s48 offen offset:0 lds\n"
BL"v239,s[16:19],s48 offen offset:1024 lds\n"
BL"v240,s[16:19],s48 offen offset:2048 lds\n"
BL"v241,s[16:19],s48 offen offset:3072 lds\n"
PK"v139,v150,v151\n"
"ds_write_b64 v130,v[138:139] offset:49152\n"
MU"v152,v152,v129\n"
MU"v153,v153,v129\n"
PK"v140,v152,v153\n"
MU"v154,v154,v129\n"
MU"v155,v155,v129\n"
PK"v141,v154,v155\n"
"ds_write_b64 v130,v[140:141] offset:51456\n"
MU"v156,v156,v129\n"
MU"v157,v157,v129\n"
PK"v142,v156,v157\n"
MU"v158,v158,v129\n"
MU"v159,v159,v129\n"
PK"v143,v158,v159\n"
"ds_write_b64 v130,v[142:143] offset:53760\n"
MU"v160,v160,v129\n"
MU"v161,v161,v129\n"
PK"v144,v160,v161\n"
MU"v162,v162,v129\n"
MU"v163,v163,v129\n"
PK"v145,v162,v163\n"
"ds_write_b64 v130,v[144:145] offset:56064\n"
MU"v164,v164,v129\n"
MU"v165,v165,v129\n"
PK"v146,v164,v165\n"
MU"v166,v166,v129\n"
MU"v167,v167,v129\n"
PK"v147,v166,v167\n"
"ds_write_b64 v130,v[146:147] offset:58368\n"
"v_lshlrev_b32 v148,16,v138\n"
"v_and_b32 v149,0xffff0000,v138\n"
MU"v188,v148,v168\n"
MU"v189,v149,v169\n"
"v_lshlrev_b32 v150,16,v139\n"
"v_and_b32 v151,0xffff0000,v139\n"
"v_fmac_f32 v188,v150,v170\n"
"v_fmac_f32 v189,v151,v171\n"
"v_lshlrev_b32 v152,16,v140\n"
"v_and_b32 v153,0xffff0000,v140\n"
"v_fmac_f32 v188,v152,v172\n"
"v_fmac_f32 v189,v153,v173\n"
"v_lshlrev_b32 v154,16,v141\n"
"v_and_b32 v155,0xffff0000,v141\n"
"v_fmac_f32 v188,v154,v174\n"
"v_fmac_f32 v189,v155,v175\n"
"v_lshlrev_b32 v156,16,v142\n"
"v_and_b32 v157,0xffff0000,v142\n"
"v_fmac_f32 v188,v156,v176\n"
"v_fmac_f32 v189,v157,v177\n"
"v_lshlrev_b32 v158,16,v143\n"
"v_and_b32 v159,0xffff0000,v143\n"
"v_fmac_f32 v188,v158,v178\n"
"v_fmac_f32 v189,v159,v179\n"
"v_lshlrev_b32 v160,16,v144\n"
"v_and_b32 v161,0xffff0000,v144\n"
"v_fmac_f32 v188,v160,v180\n"
"v_fmac_f32 v189,v161,v181\n"
"v_lshlrev_b32 v162,16,v145\n"
"v_and_b32 v163,0xffff0000,v145\n"
WT"lgkmcnt(0)\n"
SA"s28,s28,0x7f4a7c15\n"
"s_addc_u32 s29,s29,0x9e3779b9\n"
"v_mov_b32 v232,s28\n"
"v_mov_b32 v233,s29\n"
WT"vmcnt(16)\n"
RT"a[0:1],v246 offset:0\n"
RT"a[2:3],v246 offset:512\n"
RT"a[4:5],v247 offset:0\n"
RT"a[6:7],v247 offset:512\n"
RT"a[8:9],v248 offset:0\n"
RT"a[10:11],v248 offset:512\n"
RT"a[12:13],v249 offset:0\n"
RT"a[14:15],v249 offset:512\n"
WT"vmcnt(12)\n"
RT"a[16:17],v246 offset:4096\n"
RT"a[18:19],v246 offset:4608\n"
RT"a[20:21],v247 offset:4096\n"
RT"a[22:23],v247 offset:4608\n"
RT"a[24:25],v248 offset:4096\n"
RT"a[26:27],v248 offset:4608\n"
RT"a[28:29],v249 offset:4096\n"
RT"a[30:31],v249 offset:4608\n"
WT"vmcnt(8) lgkmcnt(15)\n"
"s_barrier\n"
RD"v[192:195],v242 offset:0\n"
RD"v[196:199],v243 offset:0\n"
RD"v[200:203],v242 offset:2048\n"
RD"v[204:207],v243 offset:2048\n"
RD"v[208:211],v242 offset:4096\n"
"v_lshrrev_b32 v131,7,v224\n"
WT"lgkmcnt(4)\n"
MF"v[0:3],v[192:195],a[0:3],0\n"
WT"lgkmcnt(3)\n"
RD"v[212:215],v243 offset:4096\n"
"v_lshl_add_u32 v131,v131,4,v224\n"
MF"v[0:3],v[196:199],a[16:19],v[0:3]\n"
SA"m0,s36,16384\n"
"s_bfe_u32 s48,s46,0x30004\n"
"s_lshl_b32 s48,s48,7\n"
MF"v[8:11],v[196:199],a[20:23],0\n"
"s_mul_i32 s52,s30,1152\n"
BL"v234,s[12:15],s48 offen offset:0 lds\n"
MF"v[8:11],v[192:195],a[4:7],v[8:11]\n"
"v_add_u32 v128,s52,v131\n"
"s_and_b32 s52,s30,1\n"
MF"v[16:19],v[192:195],a[8:11],0\n"
"s_mul_i32 s51,s52,1152\n"
RD"v[216:219],v242 offset:6144\n"
"v_add_u32 v131,s51,v131\n"
MF"v[16:19],v[196:199],a[24:27],v[16:19]\n"
"s_lshl_b32 s52,s52,10\n"
"v_fmac_f32 v188,v162,v182\n"
"v_fmac_f32 v189,v163,v183\n"
MF"v[24:27],v[196:199],a[28:31],0\n"
BL"v235,s[12:15],s48 offen offset:1024 lds\n"
MF"v[24:27],v[192:195],a[12:15],v[24:27]\n"
WT"lgkmcnt(1)\n"
RD"v[220:223],v243 offset:6144\n"
"v_lshlrev_b32 v164,16,v146\n"
MF"v[4:7],v[200:203],a[0:3],0\n"
RD"v[132:135],v128 offset:49152\n"
"v_and_b32 v129,0x70,v224\n"
MF"v[4:7],v[204:207],a[16:19],v[4:7]\n"
"s_lshr_b32 s50,s31,1\n"
WT"vmcnt(6)\n"
RT"a[32:33],v246 offset:8192\n"
MF"v[12:15],v[204:207],a[20:23],0\n"
"s_mul_i32 s50,s50,20480\n"
RD"a[240:243],v128 offset:53760\n"
"v_and_b32 v165,0xffff0000,v146\n"
MF"v[12:15],v[200:203],a[4:7],v[12:15]\n"
"s_and_b32 s51,s31,1\n"
RD"a[244:247],v131 offset:58368\n"
"v_and_b32 v130,0x380,v224\n"
MF"v[20:23],v[200:203],a[8:11],0\n"
"s_lshl_b32 s51,s51,7\n"
RD"v[192:195],v242 offset:8192\n"
"v_fmac_f32 v188,v164,v184\n"
MF"v[20:23],v[204:207],a[24:27],v[20:23]\n"
SA"s50,s50,s51\n"
RT"a[34:35],v246 offset:8704\n"
"v_lshl_add_u32 v129,v130,1,v129\n"
MF"v[28:31],v[204:207],a[28:31],0\n"
SA"s50,s50,524288\n"
BL"v236,s[12:15],s48 offen offset:2048 lds\n"
MF"v[28:31],v[200:203],a[12:15],v[28:31]\n"
"s_lshl_b32 s52,s52,1\n"
RT"a[36:37],v247 offset:8192\n"
"v_fmac_f32 v189,v165,v185\n"
MF"v[32:35],v[208:211],a[0:3],0\n"
SA"s52,s52,s50\n"
RD"v[196:199],v243 offset:8192\n"
"v_lshlrev_b32 v166,16,v147\n"
MF"v[32:35],v[212:215],a[16:19],v[32:35]\n"
SA"s52,s52,16384\n"
BL"v237,s[12:15],s48 offen offset:3072 lds\n"
MF"v[40:43],v[212:215],a[20:23],0\n"
SA"m0,s37,16384\n"
RT"a[38:39],v247 offset:8704\n"
"v_and_b32 v167,0xffff0000,v147\n"
MF"v[40:43],v[208:211],a[4:7],v[40:43]\n"
"s_bfe_u32 s48,s46,0x30008\n"
RD"v[200:203],v242 offset:10240\n"
"v_fmac_f32 v188,v166,v186\n"
MF"v[48:51],v[208:211],a[8:11],0\n"
"s_lshl_b32 s48,s48,19\n"
RT"a[40:41],v248 offset:8192\n"
"v_fmac_f32 v189,v167,v187\n"
MF"v[48:51],v[212:215],a[24:27],v[48:51]\n"
"s_lshl_b32 s51,s30,11\n"
BL"v238,s[16:19],s48 offen offset:0 lds\n"
MF"v[56:59],v[212:215],a[28:31],0\n"
SA"s50,s50,s51\n"
RD"v[204:207],v243 offset:10240\n"
"v_add_f32 v188,v188,v189\n"
MF"v[56:59],v[208:211],a[12:15],v[56:59]\n"
WT"lgkmcnt(4)\n"
BS"v[132:135],v129,s[8:11],s50 offen sc1\n"
MF"v[36:39],v[216:219],a[0:3],0\n"
SA"s51,s50,8192\n"
RT"a[42:43],v248 offset:8704\n"
"v_add_f32_dpp v188,v188,v188 quad_perm:[1,0,3,2] row_mask:0xf bank_mask:0xf\n"
MF"v[36:39],v[220:223],a[16:19],v[36:39]\n"
"s_lshl_b32 s90,s31,2\n"
BL"v239,s[16:19],s48 offen offset:1024 lds\n"
MF"v[44:47],v[220:223],a[20:23],0\n"
SA"s90,s90,s30\n"
BS"a[240:243],v129,s[8:11],s51 offen sc1\n"
MF"v[44:47],v[216:219],a[4:7],v[44:47]\n"
"s_lshl_b32 s90,s90,1\n"
RD"v[208:211],v242 offset:12288\n"
"v_add_f32_dpp v188,v188,v188 quad_perm:[2,3,0,1] row_mask:0xf bank_mask:0xf\n"
MF"v[52:55],v[216:219],a[8:11],0\n"
"s_sub_u32 s91,2047,s90\n"
RT"a[44:45],v249 offset:8192\n"
"v_add_f32_dpp v188,v188,v188 row_half_mirror row_mask:0xf bank_mask:0xf\n"
MF"v[52:55],v[220:223],a[24:27],v[52:55]\n"
BS"a[244:247],v129,s[8:11],s52 offen sc1\n"
MF"v[60:63],v[220:223],a[28:31],0\n"
RD"v[212:215],v243 offset:12288\n"
"v_add_f32_dpp v188,v188,v188 row_mirror row_mask:0xf bank_mask:0xf\n"
MF"v[60:63],v[216:219],a[12:15],v[60:63]\n"
RT"a[46:47],v249 offset:8704\n"
"v_mov_b32 v189,v188\n"
MF"v[64:67],v[192:195],a[0:3],0\n"
BL"v240,s[16:19],s48 offen offset:2048 lds\n"
MF"v[64:67],v[196:199],a[16:19],v[64:67]\n"
RD"v[216:219],v242 offset:14336\n"
"v_permlane16_swap_b32 v189,v188\n"
MF"v[72:75],v[196:199],a[20:23],0\n"
"s_lshl_b32 s90,s90,2\n"
WT"vmcnt(10)\n"
RT"a[48:49],v246 offset:12288\n"
MF"v[72:75],v[192:195],a[4:7],v[72:75]\n"
SA"s90,s90,8192\n"
BL"v241,s[16:19],s48 offen offset:3072 lds\n"
MF"v[80:83],v[192:195],a[8:11],0\n"
SA"m0,s37,20480\n"
RT"a[50:51],v246 offset:12800\n"
"v_add_f32 v188,v188,v189\n"
MF"v[80:83],v[196:199],a[24:27],v[80:83]\n"
"s_bfe_u32 s48,s46,0x30008\n"
RD"v[220:223],v243 offset:14336\n"
PK"v188,v188,v188\n"
MF"v[88:91],v[196:199],a[28:31],0\n"
"s_lshl_b32 s48,s48,19\n"
RT"a[52:53],v247 offset:12288\n"
"v_lshlrev_b32 v188,16,v188\n"
MF"v[88:91],v[192:195],a[12:15],v[88:91]\n"
WT"lgkmcnt(5)\n"
"v_ashrrev_i32 v189,31,v188\n"
"v_or_b32 v189,0x80000000,v189\n"
MF"v[68:71],v[200:203],a[0:3],0\n"
"s_bitset1_b32 s48,18\n"
"v_xor_b32 v188,v188,v189\n"
"v_and_b32 v188,0xffff0000,v188\n"
MF"v[68:71],v[204:207],a[16:19],v[68:71]\n"
BL"v238,s[16:19],s48 offen offset:0 lds\n"
MF"v[76:79],v[204:207],a[20:23],0\n"
RT"a[54:55],v247 offset:12800\n"
"v_lshrrev_b32 v189,9,v224\n"
MF"v[76:79],v[200:203],a[4:7],v[76:79]\n"
BL"v239,s[16:19],s48 offen offset:1024 lds\n"
MF"v[84:87],v[200:203],a[8:11],0\n"
WT"vmcnt(9) lgkmcnt(2)\n"
RT"a[56:57],v248 offset:12288\n"
"v_sub_u32 v190,s91,v189\n"
MF"v[84:87],v[204:207],a[24:27],v[84:87]\n"
"s_barrier\n"
MF"v[92:95],v[204:207],a[28:31],0\n"
RD"v[192:195],v242 offset:16384\n"
"v_or_b32 v188,v188,v190\n"
MF"v[92:95],v[200:203],a[12:15],v[92:95]\n"
RT"a[58:59],v248 offset:12800\n"
"v_and_b32 v190,0x1f0,v224\n"
MF"v[96:99],v[208:211],a[0:3],0\n"
BL"v240,s[16:19],s48 offen offset:2048 lds\n"
MF"v[96:99],v[212:215],a[16:19],v[96:99]\n"
"v_lshlrev_b32 v190,23,v190\n"
"v_lshl_add_u32 v190,v189,2,v190\n"
MF"v[104:107],v[212:215],a[20:23],0\n"
"buffer_store_dword v188,v190,s[8:11],s90 offen sc1\n"
MF"v[104:107],v[208:211],a[4:7],v[104:107]\n"
RD"v[196:199],v243 offset:16384\n"
MF"v[112:115],v[208:211],a[8:11],0\n"
RT"a[60:61],v249 offset:12288\n"
MF"v[112:115],v[212:215],a[24:27],v[112:115]\n"
BL"v241,s[16:19],s48 offen offset:3072 lds\n"
MF"v[120:123],v[212:215],a[28:31],0\n"
SA"m0,s36,32768\n"
RT"a[62:63],v249 offset:12800\n"
"s_bfe_u32 s48,s46,0x30008\n"
MF"v[120:123],v[208:211],a[12:15],v[120:123]\n"
"s_lshl_b32 s48,s48,7\n"
RD"v[200:203],v242 offset:18432\n"
MF"v[100:103],v[216:219],a[0:3],0\n"
BL"v234,s[12:15],s48 offen offset:0 lds\n"
MF"v[100:103],v[220:223],a[16:19],v[100:103]\n"
RD"v[204:207],v243 offset:18432\n"
MF"v[108:111],v[220:223],a[20:23],0\n"
MF"v[108:111],v[216:219],a[4:7],v[108:111]\n"
MF"v[116:119],v[216:219],a[8:11],0\n"
BL"v235,s[12:15],s48 offen offset:1024 lds\n"
MF"v[116:119],v[220:223],a[24:27],v[116:119]\n"
RD"v[208:211],v242 offset:20480\n"
MF"v[124:127],v[220:223],a[28:31],0\n"
MF"v[124:127],v[216:219],a[12:15],v[124:127]\n"
WT"lgkmcnt(2)\n"
MF"v[0:3],v[192:195],a[32:35],v[0:3]\n"
RD"v[212:215],v243 offset:20480\n"
MF"v[0:3],v[196:199],a[48:51],v[0:3]\n"
BL"v236,s[12:15],s48 offen offset:2048 lds\n"
MF"v[8:11],v[196:199],a[52:55],v[8:11]\n"
MF"v[8:11],v[192:195],a[36:39],v[8:11]\n"
MF"v[16:19],v[192:195],a[40:43],v[16:19]\n"
RD"v[216:219],v242 offset:22528\n"
MF"v[16:19],v[196:199],a[56:59],v[16:19]\n"
BL"v237,s[12:15],s48 offen offset:3072 lds\n"
MF"v[24:27],v[196:199],a[60:63],v[24:27]\n"
MF"v[24:27],v[192:195],a[44:47],v[24:27]\n"
RD"v[220:223],v243 offset:22528\n"
MF"v[4:7],v[200:203],a[32:35],v[4:7]\n"
WT"lgkmcnt(2)\n"
WT"vmcnt(9)\n"
RT"a[64:65],v246 offset:16384\n"
MF"v[4:7],v[204:207],a[48:51],v[4:7]\n"
SA"m0,s37,0\n"
"s_bfe_u32 s48,s46,0x3000c\n"
"s_lshl_b32 s48,s48,19\n"
MF"v[12:15],v[204:207],a[52:55],v[12:15]\n"
BL"v238,s[16:19],s48 offen offset:0 lds\n"
MF"v[12:15],v[200:203],a[36:39],v[12:15]\n"
RD"v[192:195],v242 offset:24576\n"
MF"v[20:23],v[200:203],a[40:43],v[20:23]\n"
RT"a[66:67],v246 offset:16896\n"
MF"v[20:23],v[204:207],a[56:59],v[20:23]\n"
MF"v[28:31],v[204:207],a[60:63],v[28:31]\n"
RT"a[68:69],v247 offset:16384\n"
MF"v[28:31],v[200:203],a[44:47],v[28:31]\n"
BL"v239,s[16:19],s48 offen offset:1024 lds\n"
MF"v[32:35],v[208:211],a[32:35],v[32:35]\n"
RD"v[196:199],v243 offset:24576\n"
MF"v[32:35],v[212:215],a[48:51],v[32:35]\n"
RT"a[70:71],v247 offset:16896\n"
MF"v[40:43],v[212:215],a[52:55],v[40:43]\n"
RD"v[200:203],v242 offset:26624\n"
MF"v[40:43],v[208:211],a[36:39],v[40:43]\n"
BL"v240,s[16:19],s48 offen offset:2048 lds\n"
MF"v[48:51],v[208:211],a[40:43],v[48:51]\n"
RT"a[72:73],v248 offset:16384\n"
MF"v[48:51],v[212:215],a[56:59],v[48:51]\n"
MF"v[56:59],v[212:215],a[60:63],v[56:59]\n"
RD"v[204:207],v243 offset:26624\n"
MF"v[56:59],v[208:211],a[44:47],v[56:59]\n"
WT"lgkmcnt(4)\n"
RT"a[74:75],v248 offset:16896\n"
MF"v[36:39],v[216:219],a[32:35],v[36:39]\n"
BL"v241,s[16:19],s48 offen offset:3072 lds\n"
MF"v[36:39],v[220:223],a[48:51],v[36:39]\n"
RD"v[208:211],v242 offset:28672\n"
MF"v[44:47],v[220:223],a[52:55],v[44:47]\n"
SA"m0,s37,4096\n"
RT"a[76:77],v249 offset:16384\n"
"s_bfe_u32 s48,s46,0x3000c\n"
MF"v[44:47],v[216:219],a[36:39],v[44:47]\n"
"s_lshl_b32 s48,s48,19\n"
"s_bitset1_b32 s48,18\n"
MF"v[52:55],v[216:219],a[40:43],v[52:55]\n"
BL"v238,s[16:19],s48 offen offset:0 lds\n"
MF"v[52:55],v[220:223],a[56:59],v[52:55]\n"
RD"v[212:215],v243 offset:28672\n"
MF"v[60:63],v[220:223],a[60:63],v[60:63]\n"
RT"a[78:79],v249 offset:16896\n"
MF"v[60:63],v[216:219],a[44:47],v[60:63]\n"
MF"v[64:67],v[192:195],a[32:35],v[64:67]\n"
RD"v[216:219],v242 offset:30720\n"
MF"v[64:67],v[196:199],a[48:51],v[64:67]\n"
WT"vmcnt(9)\n"
RT"a[80:81],v246 offset:20480\n"
MF"v[72:75],v[196:199],a[52:55],v[72:75]\n"
BL"v239,s[16:19],s48 offen offset:1024 lds\n"
MF"v[72:75],v[192:195],a[36:39],v[72:75]\n"
RT"a[82:83],v246 offset:20992\n"
MF"v[80:83],v[192:195],a[40:43],v[80:83]\n"
RD"v[220:223],v243 offset:30720\n"
MF"v[80:83],v[196:199],a[56:59],v[80:83]\n"
BL"v240,s[16:19],s48 offen offset:2048 lds\n"
MF"v[88:91],v[196:199],a[60:63],v[88:91]\n"
RT"a[84:85],v247 offset:20480\n"
MF"v[88:91],v[192:195],a[44:47],v[88:91]\n"
WT"lgkmcnt(4)\n"
MF"v[68:71],v[200:203],a[32:35],v[68:71]\n"
"s_lshl_b32 s51,s32,8\n"
"s_andn2_b32 s50,s31,7\n"
SA"s50,s50,s51\n"
MF"v[68:71],v[204:207],a[48:51],v[68:71]\n"
SA"s50,s50,256\n"
RT"a[86:87],v247 offset:20992\n"
SA"s50,s50,s45\n"
MF"v[76:79],v[204:207],a[52:55],v[76:79]\n"
BL"v241,s[16:19],s48 offen offset:3072 lds\n"
MF"v[76:79],v[200:203],a[36:39],v[76:79]\n"
MF"v[84:87],v[200:203],a[40:43],v[84:87]\n"
WT"vmcnt(8) lgkmcnt(2)\n"
RT"a[88:89],v248 offset:20480\n"
SA"m0,s36,0\n"
MF"v[84:87],v[204:207],a[56:59],v[84:87]\n"
"s_bfe_u32 s48,s46,0x3000c\n"
"s_barrier\n"
MF"v[92:95],v[204:207],a[60:63],v[92:95]\n"
"s_lshl_b32 s48,s48,7\n"
RD"v[192:195],v242 offset:32768\n"
MF"v[92:95],v[200:203],a[44:47],v[92:95]\n"
BL"v234,s[12:15],s48 offen offset:0 lds\n"
MF"v[96:99],v[208:211],a[32:35],v[96:99]\n"
"buffer_store_dwordx2 v[232:233],v230,s[8:11],s50 offen sc1\n"
MF"v[96:99],v[212:215],a[48:51],v[96:99]\n"
RT"a[90:91],v248 offset:20992\n"
MF"v[104:107],v[212:215],a[52:55],v[104:107]\n"
RD"v[196:199],v243 offset:32768\n"
MF"v[104:107],v[208:211],a[36:39],v[104:107]\n"
RT"a[92:93],v249 offset:20480\n"
MF"v[112:115],v[208:211],a[40:43],v[112:115]\n"
BL"v235,s[12:15],s48 offen offset:1024 lds\n"
MF"v[112:115],v[212:215],a[56:59],v[112:115]\n"
RT"a[94:95],v249 offset:20992\n"
MF"v[120:123],v[212:215],a[60:63],v[120:123]\n"
RD"v[200:203],v242 offset:34816\n"
MF"v[120:123],v[208:211],a[44:47],v[120:123]\n"
BL"v236,s[12:15],s48 offen offset:2048 lds\n"
MF"v[100:103],v[216:219],a[32:35],v[100:103]\n"
MF"v[100:103],v[220:223],a[48:51],v[100:103]\n"
RD"v[204:207],v243 offset:34816\n"
MF"v[108:111],v[220:223],a[52:55],v[108:111]\n"
MF"v[108:111],v[216:219],a[36:39],v[108:111]\n"
BL"v237,s[12:15],s48 offen offset:3072 lds\n"
MF"v[116:119],v[216:219],a[40:43],v[116:119]\n"
MF"v[116:119],v[220:223],a[56:59],v[116:119]\n"
RD"v[208:211],v242 offset:36864\n"
MF"v[124:127],v[220:223],a[60:63],v[124:127]\n"
SA"m0,s37,8192\n"
"s_bfe_u32 s48,s46,0x30010\n"
"s_lshl_b32 s48,s48,19\n"
MF"v[124:127],v[216:219],a[44:47],v[124:127]\n"
WT"lgkmcnt(2)\n"
MF"v[0:3],v[192:195],a[64:67],v[0:3]\n"
RD"v[212:215],v243 offset:36864\n"
MF"v[0:3],v[196:199],a[80:83],v[0:3]\n"
BL"v238,s[16:19],s48 offen offset:0 lds\n"
MF"v[8:11],v[196:199],a[84:87],v[8:11]\n"
MF"v[8:11],v[192:195],a[68:71],v[8:11]\n"
MF"v[16:19],v[192:195],a[72:75],v[16:19]\n"
RD"v[216:219],v242 offset:38912\n"
MF"v[16:19],v[196:199],a[88:91],v[16:19]\n"
BL"v239,s[16:19],s48 offen offset:1024 lds\n"
MF"v[24:27],v[196:199],a[92:95],v[24:27]\n"
MF"v[24:27],v[192:195],a[76:79],v[24:27]\n"
RD"v[220:223],v243 offset:38912\n"
MF"v[4:7],v[200:203],a[64:67],v[4:7]\n"
WT"lgkmcnt(2)\n"
WT"vmcnt(11)\n"
RT"a[96:97],v246 offset:0\n"
MF"v[4:7],v[204:207],a[80:83],v[4:7]\n"
BL"v240,s[16:19],s48 offen offset:2048 lds\n"
MF"v[12:15],v[204:207],a[84:87],v[12:15]\n"
MF"v[12:15],v[200:203],a[68:71],v[12:15]\n"
RD"v[192:195],v242 offset:40960\n"
MF"v[20:23],v[200:203],a[72:75],v[20:23]\n"
RT"a[98:99],v246 offset:512\n"
MF"v[20:23],v[204:207],a[88:91],v[20:23]\n"
BL"v241,s[16:19],s48 offen offset:3072 lds\n"
MF"v[28:31],v[204:207],a[92:95],v[28:31]\n"
RT"a[100:101],v247 offset:0\n"
MF"v[28:31],v[200:203],a[76:79],v[28:31]\n"
RD"v[196:199],v243 offset:40960\n"
MF"v[32:35],v[208:211],a[64:67],v[32:35]\n"
SA"m0,s37,12288\n"
"s_bfe_u32 s48,s46,0x30010\n"
"s_lshl_b32 s48,s48,19\n"
MF"v[32:35],v[212:215],a[80:83],v[32:35]\n"
"s_bitset1_b32 s48,18\n"
RT"a[102:103],v247 offset:512\n"
MF"v[40:43],v[212:215],a[84:87],v[40:43]\n"
RD"v[200:203],v242 offset:43008\n"
MF"v[40:43],v[208:211],a[68:71],v[40:43]\n"
BL"v238,s[16:19],s48 offen offset:0 lds\n"
MF"v[48:51],v[208:211],a[72:75],v[48:51]\n"
RT"a[104:105],v248 offset:0\n"
MF"v[48:51],v[212:215],a[88:91],v[48:51]\n"
MF"v[56:59],v[212:215],a[92:95],v[56:59]\n"
RD"v[204:207],v243 offset:43008\n"
MF"v[56:59],v[208:211],a[76:79],v[56:59]\n"
WT"lgkmcnt(4)\n"
BL"v239,s[16:19],s48 offen offset:1024 lds\n"
MF"v[36:39],v[216:219],a[64:67],v[36:39]\n"
RT"a[106:107],v248 offset:512\n"
MF"v[36:39],v[220:223],a[80:83],v[36:39]\n"
RD"v[208:211],v242 offset:45056\n"
MF"v[44:47],v[220:223],a[84:87],v[44:47]\n"
RT"a[108:109],v249 offset:0\n"
MF"v[44:47],v[216:219],a[68:71],v[44:47]\n"
BL"v240,s[16:19],s48 offen offset:2048 lds\n"
MF"v[52:55],v[216:219],a[72:75],v[52:55]\n"
MF"v[52:55],v[220:223],a[88:91],v[52:55]\n"
RD"v[212:215],v243 offset:45056\n"
MF"v[60:63],v[220:223],a[92:95],v[60:63]\n"
RT"a[110:111],v249 offset:512\n"
MF"v[60:63],v[216:219],a[76:79],v[60:63]\n"
BL"v241,s[16:19],s48 offen offset:3072 lds\n"
MF"v[64:67],v[192:195],a[64:67],v[64:67]\n"
RD"v[216:219],v242 offset:47104\n"
MF"v[64:67],v[196:199],a[80:83],v[64:67]\n"
WT"vmcnt(13)\n"
RT"a[112:113],v246 offset:4096\n"
MF"v[72:75],v[196:199],a[84:87],v[72:75]\n"
SA"m0,s36,16384\n"
"s_bfe_u32 s48,s46,0x30010\n"
"s_lshl_b32 s48,s48,7\n"
MF"v[72:75],v[192:195],a[68:71],v[72:75]\n"
RT"a[114:115],v246 offset:4608\n"
MF"v[80:83],v[192:195],a[72:75],v[80:83]\n"
RD"v[220:223],v243 offset:47104\n"
MF"v[80:83],v[196:199],a[88:91],v[80:83]\n"
BL"v234,s[12:15],s48 offen offset:0 lds\n"
MF"v[88:91],v[196:199],a[92:95],v[88:91]\n"
RT"a[116:117],v247 offset:4096\n"
MF"v[88:91],v[192:195],a[76:79],v[88:91]\n"
WT"lgkmcnt(4)\n"
MF"v[68:71],v[200:203],a[64:67],v[68:71]\n"
BL"v235,s[12:15],s48 offen offset:1024 lds\n"
MF"v[68:71],v[204:207],a[80:83],v[68:71]\n"
RT"a[118:119],v247 offset:4608\n"
MF"v[76:79],v[204:207],a[84:87],v[76:79]\n"
MF"v[76:79],v[200:203],a[68:71],v[76:79]\n"
MF"v[84:87],v[200:203],a[72:75],v[84:87]\n"
WT"vmcnt(10) lgkmcnt(2)\n"
RT"a[120:121],v248 offset:4096\n"
MF"v[84:87],v[204:207],a[88:91],v[84:87]\n"
"s_barrier\n"
MF"v[92:95],v[204:207],a[92:95],v[92:95]\n"
BL"v236,s[12:15],s48 offen offset:2048 lds\n"
MF"v[92:95],v[200:203],a[76:79],v[92:95]\n"
RD"v[192:195],v242 offset:0\n"
MF"v[96:99],v[208:211],a[64:67],v[96:99]\n"
RT"a[122:123],v248 offset:4608\n"
MF"v[96:99],v[212:215],a[80:83],v[96:99]\n"
BL"v237,s[12:15],s48 offen offset:3072 lds\n"
MF"v[104:107],v[212:215],a[84:87],v[104:107]\n"
RD"v[196:199],v243 offset:0\n"
MF"v[104:107],v[208:211],a[68:71],v[104:107]\n"
RT"a[124:125],v249 offset:4096\n"
MF"v[112:115],v[208:211],a[72:75],v[112:115]\n"
SA"m0,s37,16384\n"
"s_bfe_u32 s48,s46,0x30014\n"
"s_lshl_b32 s48,s48,19\n"
MF"v[112:115],v[212:215],a[88:91],v[112:115]\n"
RT"a[126:127],v249 offset:4608\n"
MF"v[120:123],v[212:215],a[92:95],v[120:123]\n"
RD"v[200:203],v242 offset:2048\n"
MF"v[120:123],v[208:211],a[76:79],v[120:123]\n"
BL"v238,s[16:19],s48 offen offset:0 lds\n"
MF"v[100:103],v[216:219],a[64:67],v[100:103]\n"
MF"v[100:103],v[220:223],a[80:83],v[100:103]\n"
RD"v[204:207],v243 offset:2048\n"
MF"v[108:111],v[220:223],a[84:87],v[108:111]\n"
BL"v239,s[16:19],s48 offen offset:1024 lds\n"
MF"v[108:111],v[216:219],a[68:71],v[108:111]\n"
MF"v[116:119],v[216:219],a[72:75],v[116:119]\n"
MF"v[116:119],v[220:223],a[88:91],v[116:119]\n"
RD"v[208:211],v242 offset:4096\n"
MF"v[124:127],v[220:223],a[92:95],v[124:127]\n"
MF"v[124:127],v[216:219],a[76:79],v[124:127]\n"
WT"lgkmcnt(2)\n"
BL"v240,s[16:19],s48 offen offset:2048 lds\n"
MF"v[0:3],v[192:195],a[96:99],v[0:3]\n"
RD"v[212:215],v243 offset:4096\n"
MF"v[0:3],v[196:199],a[112:115],v[0:3]\n"
MF"v[8:11],v[196:199],a[116:119],v[8:11]\n"
MF"v[8:11],v[192:195],a[100:103],v[8:11]\n"
BL"v241,s[16:19],s48 offen offset:3072 lds\n"
MF"v[16:19],v[192:195],a[104:107],v[16:19]\n"
RD"v[216:219],v242 offset:6144\n"
MF"v[16:19],v[196:199],a[120:123],v[16:19]\n"
MF"v[24:27],v[196:199],a[124:127],v[24:27]\n"
SA"m0,s37,20480\n"
"s_bfe_u32 s48,s46,0x30014\n"
"s_lshl_b32 s48,s48,19\n"
MF"v[24:27],v[192:195],a[108:111],v[24:27]\n"
"s_bitset1_b32 s48,18\n"
RD"v[220:223],v243 offset:6144\n"
MF"v[4:7],v[200:203],a[96:99],v[4:7]\n"
WT"lgkmcnt(2)\n"
WT"vmcnt(12)\n"
RT"a[128:129],v246 offset:8192\n"
MF"v[4:7],v[204:207],a[112:115],v[4:7]\n"
BL"v238,s[16:19],s48 offen offset:0 lds\n"
MF"v[12:15],v[204:207],a[116:119],v[12:15]\n"
MF"v[12:15],v[200:203],a[100:103],v[12:15]\n"
RD"v[192:195],v242 offset:8192\n"
MF"v[20:23],v[200:203],a[104:107],v[20:23]\n"
RT"a[130:131],v246 offset:8704\n"
MF"v[20:23],v[204:207],a[120:123],v[20:23]\n"
BL"v239,s[16:19],s48 offen offset:1024 lds\n"
MF"v[28:31],v[204:207],a[124:127],v[28:31]\n"
RT"a[132:133],v247 offset:8192\n"
MF"v[28:31],v[200:203],a[108:111],v[28:31]\n"
RD"v[196:199],v243 offset:8192\n"
MF"v[32:35],v[208:211],a[96:99],v[32:35]\n"
MF"v[32:35],v[212:215],a[112:115],v[32:35]\n"
RT"a[134:135],v247 offset:8704\n"
MF"v[40:43],v[212:215],a[116:119],v[40:43]\n"
BL"v240,s[16:19],s48 offen offset:2048 lds\n"
MF"v[40:43],v[208:211],a[100:103],v[40:43]\n"
RD"v[200:203],v242 offset:10240\n"
MF"v[48:51],v[208:211],a[104:107],v[48:51]\n"
RT"a[136:137],v248 offset:8192\n"
MF"v[48:51],v[212:215],a[120:123],v[48:51]\n"
BL"v241,s[16:19],s48 offen offset:3072 lds\n"
MF"v[56:59],v[212:215],a[124:127],v[56:59]\n"
RD"v[204:207],v243 offset:10240\n"
MF"v[56:59],v[208:211],a[108:111],v[56:59]\n"
WT"lgkmcnt(4)\n"
RT"a[138:139],v248 offset:8704\n"
MF"v[36:39],v[216:219],a[96:99],v[36:39]\n"
SA"m0,s36,32768\n"
"s_bfe_u32 s48,s46,0x30014\n"
"s_lshl_b32 s48,s48,7\n"
MF"v[36:39],v[220:223],a[112:115],v[36:39]\n"
RD"v[208:211],v242 offset:12288\n"
MF"v[44:47],v[220:223],a[116:119],v[44:47]\n"
RT"a[140:141],v249 offset:8192\n"
MF"v[44:47],v[216:219],a[100:103],v[44:47]\n"
BL"v234,s[12:15],s48 offen offset:0 lds\n"
MF"v[52:55],v[216:219],a[104:107],v[52:55]\n"
MF"v[52:55],v[220:223],a[120:123],v[52:55]\n"
RD"v[212:215],v243 offset:12288\n"
MF"v[60:63],v[220:223],a[124:127],v[60:63]\n"
RT"a[142:143],v249 offset:8704\n"
MF"v[60:63],v[216:219],a[108:111],v[60:63]\n"
BL"v235,s[12:15],s48 offen offset:1024 lds\n"
MF"v[64:67],v[192:195],a[96:99],v[64:67]\n"
RD"v[216:219],v242 offset:14336\n"
MF"v[64:67],v[196:199],a[112:115],v[64:67]\n"
WT"vmcnt(14)\n"
RT"a[144:145],v246 offset:12288\n"
MF"v[72:75],v[196:199],a[116:119],v[72:75]\n"
MF"v[72:75],v[192:195],a[100:103],v[72:75]\n"
RT"a[146:147],v246 offset:12800\n"
MF"v[80:83],v[192:195],a[104:107],v[80:83]\n"
BL"v236,s[12:15],s48 offen offset:2048 lds\n"
MF"v[80:83],v[196:199],a[120:123],v[80:83]\n"
RD"v[220:223],v243 offset:14336\n"
MF"v[88:91],v[196:199],a[124:127],v[88:91]\n"
RT"a[148:149],v247 offset:12288\n"
MF"v[88:91],v[192:195],a[108:111],v[88:91]\n"
WT"lgkmcnt(4)\n"
BL"v237,s[12:15],s48 offen offset:3072 lds\n"
MF"v[68:71],v[200:203],a[96:99],v[68:71]\n"
MF"v[68:71],v[204:207],a[112:115],v[68:71]\n"
RT"a[150:151],v247 offset:12800\n"
MF"v[76:79],v[204:207],a[116:119],v[76:79]\n"
SA"m0,s37,0\n"
"s_bfe_u32 s48,s46,0x30018\n"
"s_lshl_b32 s48,s48,19\n"
MF"v[76:79],v[200:203],a[100:103],v[76:79]\n"
MF"v[84:87],v[200:203],a[104:107],v[84:87]\n"
WT"vmcnt(12) lgkmcnt(2)\n"
RT"a[152:153],v248 offset:12288\n"
MF"v[84:87],v[204:207],a[120:123],v[84:87]\n"
BL"v238,s[16:19],s48 offen offset:0 lds\n"
MF"v[92:95],v[204:207],a[124:127],v[92:95]\n"
"s_barrier\n"
MF"v[92:95],v[200:203],a[108:111],v[92:95]\n"
RD"v[192:195],v242 offset:16384\n"
MF"v[96:99],v[208:211],a[96:99],v[96:99]\n"
RT"a[154:155],v248 offset:12800\n"
MF"v[96:99],v[212:215],a[112:115],v[96:99]\n"
BL"v239,s[16:19],s48 offen offset:1024 lds\n"
MF"v[104:107],v[212:215],a[116:119],v[104:107]\n"
RD"v[196:199],v243 offset:16384\n"
MF"v[104:107],v[208:211],a[100:103],v[104:107]\n"
RT"a[156:157],v249 offset:12288\n"
MF"v[112:115],v[208:211],a[104:107],v[112:115]\n"
MF"v[112:115],v[212:215],a[120:123],v[112:115]\n"
RT"a[158:159],v249 offset:12800\n"
MF"v[120:123],v[212:215],a[124:127],v[120:123]\n"
BL"v240,s[16:19],s48 offen offset:2048 lds\n"
MF"v[120:123],v[208:211],a[108:111],v[120:123]\n"
RD"v[200:203],v242 offset:18432\n"
MF"v[100:103],v[216:219],a[96:99],v[100:103]\n"
MF"v[100:103],v[220:223],a[112:115],v[100:103]\n"
RD"v[204:207],v243 offset:18432\n"
MF"v[108:111],v[220:223],a[116:119],v[108:111]\n"
BL"v241,s[16:19],s48 offen offset:3072 lds\n"
MF"v[108:111],v[216:219],a[100:103],v[108:111]\n"
MF"v[116:119],v[216:219],a[104:107],v[116:119]\n"
SA"m0,s37,4096\n"
"s_bfe_u32 s48,s46,0x30018\n"
"s_lshl_b32 s48,s48,19\n"
MF"v[116:119],v[220:223],a[120:123],v[116:119]\n"
"s_bitset1_b32 s48,18\n"
RD"v[208:211],v242 offset:20480\n"
MF"v[124:127],v[220:223],a[124:127],v[124:127]\n"
BL"v238,s[16:19],s48 offen offset:0 lds\n"
MF"v[124:127],v[216:219],a[108:111],v[124:127]\n"
WT"lgkmcnt(2)\n"
MF"v[0:3],v[192:195],a[128:131],v[0:3]\n"
RD"v[212:215],v243 offset:20480\n"
MF"v[0:3],v[196:199],a[144:147],v[0:3]\n"
MF"v[8:11],v[196:199],a[148:151],v[8:11]\n"
BL"v239,s[16:19],s48 offen offset:1024 lds\n"
MF"v[8:11],v[192:195],a[132:135],v[8:11]\n"
MF"v[16:19],v[192:195],a[136:139],v[16:19]\n"
RD"v[216:219],v242 offset:22528\n"
MF"v[16:19],v[196:199],a[152:155],v[16:19]\n"
MF"v[24:27],v[196:199],a[156:159],v[24:27]\n"
MF"v[24:27],v[192:195],a[140:143],v[24:27]\n"
RD"v[220:223],v243 offset:22528\n"
MF"v[4:7],v[200:203],a[128:131],v[4:7]\n"
WT"lgkmcnt(2)\n"
BL"v240,s[16:19],s48 offen offset:2048 lds\n"
MF"v[4:7],v[204:207],a[144:147],v[4:7]\n"
WT"vmcnt(15)\n"
RT"a[160:161],v246 offset:16384\n"
MF"v[12:15],v[204:207],a[148:151],v[12:15]\n"
MF"v[12:15],v[200:203],a[132:135],v[12:15]\n"
RD"v[192:195],v242 offset:24576\n"
MF"v[20:23],v[200:203],a[136:139],v[20:23]\n"
RT"a[162:163],v246 offset:16896\n"
MF"v[20:23],v[204:207],a[152:155],v[20:23]\n"
BL"v241,s[16:19],s48 offen offset:3072 lds\n"
MF"v[28:31],v[204:207],a[156:159],v[28:31]\n"
SA"m0,s36,0\n"
RT"a[164:165],v247 offset:16384\n"
"s_bfe_u32 s48,s46,0x30018\n"
MF"v[28:31],v[200:203],a[140:143],v[28:31]\n"
"s_lshl_b32 s48,s48,7\n"
RD"v[196:199],v243 offset:24576\n"
MF"v[32:35],v[208:211],a[128:131],v[32:35]\n"
BL"v234,s[12:15],s48 offen offset:0 lds\n"
MF"v[32:35],v[212:215],a[144:147],v[32:35]\n"
RT"a[166:167],v247 offset:16896\n"
MF"v[40:43],v[212:215],a[148:151],v[40:43]\n"
RD"v[200:203],v242 offset:26624\n"
MF"v[40:43],v[208:211],a[132:135],v[40:43]\n"
MF"v[48:51],v[208:211],a[136:139],v[48:51]\n"
RT"a[168:169],v248 offset:16384\n"
MF"v[48:51],v[212:215],a[152:155],v[48:51]\n"
BL"v235,s[12:15],s48 offen offset:1024 lds\n"
MF"v[56:59],v[212:215],a[156:159],v[56:59]\n"
RD"v[204:207],v243 offset:26624\n"
MF"v[56:59],v[208:211],a[140:143],v[56:59]\n"
WT"lgkmcnt(4)\n"
RT"a[170:171],v248 offset:16896\n"
MF"v[36:39],v[216:219],a[128:131],v[36:39]\n"
MF"v[36:39],v[220:223],a[144:147],v[36:39]\n"
RD"v[208:211],v242 offset:28672\n"
MF"v[44:47],v[220:223],a[148:151],v[44:47]\n"
BL"v236,s[12:15],s48 offen offset:2048 lds\n"
MF"v[44:47],v[216:219],a[132:135],v[44:47]\n"
RT"a[172:173],v249 offset:16384\n"
MF"v[52:55],v[216:219],a[136:139],v[52:55]\n"
MF"v[52:55],v[220:223],a[152:155],v[52:55]\n"
RD"v[212:215],v243 offset:28672\n"
MF"v[60:63],v[220:223],a[156:159],v[60:63]\n"
RT"a[174:175],v249 offset:16896\n"
MF"v[60:63],v[216:219],a[140:143],v[60:63]\n"
BL"v237,s[12:15],s48 offen offset:3072 lds\n"
MF"v[64:67],v[192:195],a[128:131],v[64:67]\n"
SA"m0,s37,8192\n"
RD"v[216:219],v242 offset:30720\n"
"s_bfe_u32 s48,s46,0x3001c\n"
MF"v[64:67],v[196:199],a[144:147],v[64:67]\n"
"s_lshl_b32 s48,s48,19\n"
WT"vmcnt(16)\n"
RT"a[176:177],v246 offset:20480\n"
MF"v[72:75],v[196:199],a[148:151],v[72:75]\n"
BL"v238,s[16:19],s48 offen offset:0 lds\n"
MF"v[72:75],v[192:195],a[132:135],v[72:75]\n"
RT"a[178:179],v246 offset:20992\n"
MF"v[80:83],v[192:195],a[136:139],v[80:83]\n"
RD"v[220:223],v243 offset:30720\n"
MF"v[80:83],v[196:199],a[152:155],v[80:83]\n"
MF"v[88:91],v[196:199],a[156:159],v[88:91]\n"
RT"a[180:181],v247 offset:20480\n"
MF"v[88:91],v[192:195],a[140:143],v[88:91]\n"
WT"lgkmcnt(4)\n"
BL"v239,s[16:19],s48 offen offset:1024 lds\n"
MF"v[68:71],v[200:203],a[128:131],v[68:71]\n"
MF"v[68:71],v[204:207],a[144:147],v[68:71]\n"
RT"a[182:183],v247 offset:20992\n"
MF"v[76:79],v[204:207],a[148:151],v[76:79]\n"
MF"v[76:79],v[200:203],a[132:135],v[76:79]\n"
BL"v240,s[16:19],s48 offen offset:2048 lds\n"
MF"v[84:87],v[200:203],a[136:139],v[84:87]\n"
WT"vmcnt(15) lgkmcnt(2)\n"
RT"a[184:185],v248 offset:20480\n"
MF"v[84:87],v[204:207],a[152:155],v[84:87]\n"
"s_barrier\n"
MF"v[92:95],v[204:207],a[156:159],v[92:95]\n"
RD"v[192:195],v242 offset:32768\n"
MF"v[92:95],v[200:203],a[140:143],v[92:95]\n"
RT"a[186:187],v248 offset:20992\n"
MF"v[96:99],v[208:211],a[128:131],v[96:99]\n"
BL"v241,s[16:19],s48 offen offset:3072 lds\n"
MF"v[96:99],v[212:215],a[144:147],v[96:99]\n"
MF"v[104:107],v[212:215],a[148:151],v[104:107]\n"
SA"m0,s37,12288\n"
RD"v[196:199],v243 offset:32768\n"
"s_bfe_u32 s48,s46,0x3001c\n"
MF"v[104:107],v[208:211],a[132:135],v[104:107]\n"
"s_lshl_b32 s48,s48,19\n"
RT"a[188:189],v249 offset:20480\n"
"s_bitset1_b32 s48,18\n"
MF"v[112:115],v[208:211],a[136:139],v[112:115]\n"
BL"v238,s[16:19],s48 offen offset:0 lds\n"
MF"v[112:115],v[212:215],a[152:155],v[112:115]\n"
RT"a[190:191],v249 offset:20992\n"
MF"v[120:123],v[212:215],a[156:159],v[120:123]\n"
RD"v[200:203],v242 offset:34816\n"
MF"v[120:123],v[208:211],a[140:143],v[120:123]\n"
MF"v[100:103],v[216:219],a[128:131],v[100:103]\n"
BL"v239,s[16:19],s48 offen offset:1024 lds\n"
MF"v[100:103],v[220:223],a[144:147],v[100:103]\n"
RD"v[204:207],v243 offset:34816\n"
MF"v[108:111],v[220:223],a[148:151],v[108:111]\n"
MF"v[108:111],v[216:219],a[132:135],v[108:111]\n"
MF"v[116:119],v[216:219],a[136:139],v[116:119]\n"
MF"v[116:119],v[220:223],a[152:155],v[116:119]\n"
RD"v[208:211],v242 offset:36864\n"
MF"v[124:127],v[220:223],a[156:159],v[124:127]\n"
BL"v240,s[16:19],s48 offen offset:2048 lds\n"
MF"v[124:127],v[216:219],a[140:143],v[124:127]\n"
WT"lgkmcnt(2)\n"
MF"v[0:3],v[192:195],a[160:163],v[0:3]\n"
RD"v[212:215],v243 offset:36864\n"
MF"v[0:3],v[196:199],a[176:179],v[0:3]\n"
BL"v241,s[16:19],s48 offen offset:3072 lds\n"
MF"v[8:11],v[196:199],a[180:183],v[8:11]\n"
MF"v[8:11],v[192:195],a[164:167],v[8:11]\n"
MF"v[16:19],v[192:195],a[168:171],v[16:19]\n"
SA"m0,s36,16384\n"
RD"v[216:219],v242 offset:38912\n"
"s_bfe_u32 s48,s46,0x3001c\n"
MF"v[16:19],v[196:199],a[184:187],v[16:19]\n"
"s_lshl_b32 s48,s48,7\n"
MF"v[24:27],v[196:199],a[188:191],v[24:27]\n"
BL"v234,s[12:15],s48 offen offset:0 lds\n"
MF"v[24:27],v[192:195],a[172:175],v[24:27]\n"
RD"v[220:223],v243 offset:38912\n"
MF"v[4:7],v[200:203],a[160:163],v[4:7]\n"
WT"lgkmcnt(2)\n"
WT"vmcnt(17)\n"
RT"a[192:193],v246 offset:0\n"
MF"v[4:7],v[204:207],a[176:179],v[4:7]\n"
MF"v[12:15],v[204:207],a[180:183],v[12:15]\n"
BL"v235,s[12:15],s48 offen offset:1024 lds\n"
MF"v[12:15],v[200:203],a[164:167],v[12:15]\n"
RD"v[192:195],v242 offset:40960\n"
MF"v[20:23],v[200:203],a[168:171],v[20:23]\n"
RT"a[194:195],v246 offset:512\n"
MF"v[20:23],v[204:207],a[184:187],v[20:23]\n"
MF"v[28:31],v[204:207],a[188:191],v[28:31]\n"
RT"a[196:197],v247 offset:0\n"
MF"v[28:31],v[200:203],a[172:175],v[28:31]\n"
RD"v[196:199],v243 offset:40960\n"
MF"v[32:35],v[208:211],a[160:163],v[32:35]\n"
BL"v236,s[12:15],s48 offen offset:2048 lds\n"
MF"v[32:35],v[212:215],a[176:179],v[32:35]\n"
RT"a[198:199],v247 offset:512\n"
MF"v[40:43],v[212:215],a[180:183],v[40:43]\n"
RD"v[200:203],v242 offset:43008\n"
MF"v[40:43],v[208:211],a[164:167],v[40:43]\n"
BL"v237,s[12:15],s48 offen offset:3072 lds\n"
MF"v[48:51],v[208:211],a[168:171],v[48:51]\n"
RT"a[200:201],v248 offset:0\n"
MF"v[48:51],v[212:215],a[184:187],v[48:51]\n"
MF"v[56:59],v[212:215],a[188:191],v[56:59]\n"
SA"m0,s37,16384\n"
RD"v[204:207],v243 offset:43008\n"
SA"s48,s39,0\n"
MF"v[56:59],v[208:211],a[172:175],v[56:59]\n"
WT"lgkmcnt(4)\n"
RT"a[202:203],v248 offset:512\n"
"s_lshr_b32 s50,s30,1\n"
MF"v[36:39],v[216:219],a[160:163],v[36:39]\n"
"s_and_b32 s51,s30,1\n"
BL"v234,s[12:15],s48 offen offset:0 lds\n"
MF"v[36:39],v[220:223],a[176:179],v[36:39]\n"
"s_mul_i32 s38,s50,24576\n"
RD"v[208:211],v242 offset:45056\n"
MF"v[44:47],v[220:223],a[180:183],v[44:47]\n"
"s_lshl_b32 s60,s50,18\n"
RT"a[204:205],v249 offset:0\n"
MF"v[44:47],v[216:219],a[164:167],v[44:47]\n"
"s_lshl_b32 s50,s51,12\n"
MF"v[52:55],v[216:219],a[168:171],v[52:55]\n"
SA"s38,s38,s50\n"
BL"v235,s[12:15],s48 offen offset:1024 lds\n"
MF"v[52:55],v[220:223],a[184:187],v[52:55]\n"
"s_lshl_b32 s50,s51,7\n"
RD"v[212:215],v243 offset:45056\n"
MF"v[60:63],v[220:223],a[188:191],v[60:63]\n"
SA"s60,s60,s50\n"
RT"a[206:207],v249 offset:512\n"
MF"v[60:63],v[216:219],a[172:175],v[60:63]\n"
MF"v[64:67],v[192:195],a[160:163],v[64:67]\n"
RD"v[216:219],v242 offset:47104\n"
MF"v[64:67],v[196:199],a[176:179],v[64:67]\n"
"s_sub_u32 s99,s34,5\n"
WT"vmcnt(18)\n"
RT"a[208:209],v246 offset:4096\n"
MF"v[72:75],v[196:199],a[180:183],v[72:75]\n"
"s_cmp_lt_u32 s34,5\n"
BL"v236,s[12:15],s48 offen offset:2048 lds\n"
MF"v[72:75],v[192:195],a[164:167],v[72:75]\n"
"s_cselect_b32 s99,s34,s99\n"
RT"a[210:211],v246 offset:4608\n"
"s_mul_i32 s99,s99,1\n"
MF"v[80:83],v[192:195],a[168:171],v[80:83]\n"
"s_lshl_b32 s61,s99,20\n"
RD"v[220:223],v243 offset:47104\n"
MF"v[80:83],v[196:199],a[184:187],v[80:83]\n"
BL"v237,s[12:15],s48 offen offset:3072 lds\n"
MF"v[88:91],v[196:199],a[188:191],v[88:91]\n"
RT"a[212:213],v247 offset:4096\n"
MF"v[88:91],v[192:195],a[172:175],v[88:91]\n"
WT"lgkmcnt(4)\n"
MF"v[68:71],v[200:203],a[160:163],v[68:71]\n"
SA"m0,s37,20480\n"
SA"s48,s98,0\n"
MF"v[68:71],v[204:207],a[176:179],v[68:71]\n"
RT"a[214:215],v247 offset:4608\n"
MF"v[76:79],v[204:207],a[180:183],v[76:79]\n"
BL"v234,s[12:15],s48 offen offset:0 lds\n"
MF"v[76:79],v[200:203],a[164:167],v[76:79]\n"
MF"v[84:87],v[200:203],a[168:171],v[84:87]\n"
WT"vmcnt(17) lgkmcnt(2)\n"
RT"a[216:217],v248 offset:4096\n"
MF"v[84:87],v[204:207],a[184:187],v[84:87]\n"
"s_barrier\n"
MF"v[92:95],v[204:207],a[188:191],v[92:95]\n"
RD"v[192:195],v242 offset:0\n"
MF"v[92:95],v[200:203],a[172:175],v[92:95]\n"
BL"v235,s[12:15],s48 offen offset:1024 lds\n"
MF"v[96:99],v[208:211],a[160:163],v[96:99]\n"
RT"a[218:219],v248 offset:4608\n"
MF"v[96:99],v[212:215],a[176:179],v[96:99]\n"
MF"v[104:107],v[212:215],a[180:183],v[104:107]\n"
RD"v[196:199],v243 offset:0\n"
MF"v[104:107],v[208:211],a[164:167],v[104:107]\n"
RT"a[220:221],v249 offset:4096\n"
MF"v[112:115],v[208:211],a[168:171],v[112:115]\n"
BL"v236,s[12:15],s48 offen offset:2048 lds\n"
MF"v[112:115],v[212:215],a[184:187],v[112:115]\n"
RT"a[222:223],v249 offset:4608\n"
MF"v[120:123],v[212:215],a[188:191],v[120:123]\n"
RD"v[200:203],v242 offset:2048\n"
MF"v[120:123],v[208:211],a[172:175],v[120:123]\n"
BL"v237,s[12:15],s48 offen offset:3072 lds\n"
MF"v[100:103],v[216:219],a[160:163],v[100:103]\n"
MF"v[100:103],v[220:223],a[176:179],v[100:103]\n"
RD"v[204:207],v243 offset:2048\n"
MF"v[108:111],v[220:223],a[180:183],v[108:111]\n"
SA"m0,s37,0\n"
SA"s48,s39,32768\n"
MF"v[108:111],v[216:219],a[164:167],v[108:111]\n"
MF"v[116:119],v[216:219],a[168:171],v[116:119]\n"
BL"v234,s[12:15],s48 offen offset:0 lds\n"
MF"v[116:119],v[220:223],a[184:187],v[116:119]\n"
RD"v[208:211],v242 offset:4096\n"
MF"v[124:127],v[220:223],a[188:191],v[124:127]\n"
MF"v[124:127],v[216:219],a[172:175],v[124:127]\n"
WT"lgkmcnt(2)\n"
MF"v[0:3],v[192:195],a[192:195],v[0:3]\n"
RD"v[212:215],v243 offset:4096\n"
MF"v[0:3],v[196:199],a[208:211],v[0:3]\n"
BL"v235,s[12:15],s48 offen offset:1024 lds\n"
MF"v[8:11],v[196:199],a[212:215],v[8:11]\n"
MF"v[8:11],v[192:195],a[196:199],v[8:11]\n"
MF"v[16:19],v[192:195],a[200:203],v[16:19]\n"
RD"v[216:219],v242 offset:6144\n"
MF"v[16:19],v[196:199],a[216:219],v[16:19]\n"
BL"v236,s[12:15],s48 offen offset:2048 lds\n"
MF"v[24:27],v[196:199],a[220:223],v[24:27]\n"
BL"v[160:163],v224,s[8:11],0 offen offset:256 sc1\n"
MF"v[24:27],v[192:195],a[204:207],v[24:27]\n"
RD"v[220:223],v243 offset:6144\n"
MF"v[4:7],v[200:203],a[192:195],v[4:7]\n"
WT"lgkmcnt(2)\n"
WT"vmcnt(20)\n"
RT"a[224:225],v246 offset:8192\n"
MF"v[4:7],v[204:207],a[208:211],v[4:7]\n"
BL"v[164:167],v224,s[8:11],0 offen offset:1280 sc1\n"
MF"v[12:15],v[204:207],a[212:215],v[12:15]\n"
BL"v237,s[12:15],s48 offen offset:3072 lds\n"
MF"v[12:15],v[200:203],a[196:199],v[12:15]\n"
RD"v[192:195],v242 offset:8192\n"
MF"v[20:23],v[200:203],a[200:203],v[20:23]\n"
SA"m0,s37,4096\n"
RT"a[226:227],v246 offset:8704\n"
SA"s48,s98,32768\n"
MF"v[20:23],v[204:207],a[216:219],v[20:23]\n"
MF"v[28:31],v[204:207],a[220:223],v[28:31]\n"
RT"a[228:229],v247 offset:8192\n"
MF"v[28:31],v[200:203],a[204:207],v[28:31]\n"
BL"v234,s[12:15],s48 offen offset:0 lds\n"
MF"v[32:35],v[208:211],a[192:195],v[32:35]\n"
RD"v[196:199],v243 offset:8192\n"
MF"v[32:35],v[212:215],a[208:211],v[32:35]\n"
RT"a[230:231],v247 offset:8704\n"
MF"v[40:43],v[212:215],a[212:215],v[40:43]\n"
RD"v[200:203],v242 offset:10240\n"
MF"v[40:43],v[208:211],a[196:199],v[40:43]\n"
BL"v235,s[12:15],s48 offen offset:1024 lds\n"
MF"v[48:51],v[208:211],a[200:203],v[48:51]\n"
RT"a[232:233],v248 offset:8192\n"
MF"v[48:51],v[212:215],a[216:219],v[48:51]\n"
MF"v[56:59],v[212:215],a[220:223],v[56:59]\n"
RD"v[204:207],v243 offset:10240\n"
MF"v[56:59],v[208:211],a[204:207],v[56:59]\n"
WT"lgkmcnt(4)\n"
RT"a[234:235],v248 offset:8704\n"
MF"v[36:39],v[216:219],a[192:195],v[36:39]\n"
BL"v236,s[12:15],s48 offen offset:2048 lds\n"
MF"v[36:39],v[220:223],a[208:211],v[36:39]\n"
RD"v[208:211],v242 offset:12288\n"
MF"v[44:47],v[220:223],a[212:215],v[44:47]\n"
RT"a[236:237],v249 offset:8192\n"
MF"v[44:47],v[216:219],a[196:199],v[44:47]\n"
BL"v237,s[12:15],s48 offen offset:3072 lds\n"
MF"v[52:55],v[216:219],a[200:203],v[52:55]\n"
MF"v[52:55],v[220:223],a[216:219],v[52:55]\n"
RD"v[212:215],v243 offset:12288\n"
MF"v[60:63],v[220:223],a[220:223],v[60:63]\n"
RT"a[238:239],v249 offset:8704\n"
MF"v[60:63],v[216:219],a[204:207],v[60:63]\n"
MF"v[64:67],v[192:195],a[192:195],v[64:67]\n"
RD"v[216:219],v242 offset:14336\n"
MF"v[64:67],v[196:199],a[208:211],v[64:67]\n"
SA"m0,s37,8192\n"
WT"vmcnt(22)\n"
RT"a[240:241],v246 offset:12288\n"
MF"v[72:75],v[196:199],a[212:215],v[72:75]\n"
SA"s48,s39,65536\n"
MF"v[72:75],v[192:195],a[196:199],v[72:75]\n"
RT"a[242:243],v246 offset:12800\n"
MF"v[80:83],v[192:195],a[200:203],v[80:83]\n"
WT"lgkmcnt(3)\n"
BL"v234,s[12:15],s48 offen offset:0 lds\n"
MF"v[80:83],v[196:199],a[216:219],v[80:83]\n"
RD"v[220:223],v243 offset:14336\n"
MF"v[88:91],v[196:199],a[220:223],v[88:91]\n"
RT"a[244:245],v247 offset:12288\n"
MF"v[88:91],v[192:195],a[204:207],v[88:91]\n"
BL"v235,s[12:15],s48 offen offset:1024 lds\n"
MF"v[68:71],v[200:203],a[192:195],v[68:71]\n"
MF"v[68:71],v[204:207],a[208:211],v[68:71]\n"
RT"a[246:247],v247 offset:12800\n"
MF"v[76:79],v[204:207],a[212:215],v[76:79]\n"
MF"v[76:79],v[200:203],a[196:199],v[76:79]\n"
MF"v[84:87],v[200:203],a[200:203],v[84:87]\n"
WT"vmcnt(20) lgkmcnt(2)\n"
RT"a[248:249],v248 offset:12288\n"
MF"v[84:87],v[204:207],a[216:219],v[84:87]\n"
BL"v236,s[12:15],s48 offen offset:2048 lds\n"
MF"v[92:95],v[204:207],a[220:223],v[92:95]\n"
"s_barrier\n"
MF"v[92:95],v[200:203],a[204:207],v[92:95]\n"
RD"v[192:195],v242 offset:16384\n"
MF"v[96:99],v[208:211],a[192:195],v[96:99]\n"
RT"a[250:251],v248 offset:12800\n"
MF"v[96:99],v[212:215],a[208:211],v[96:99]\n"
BL"v237,s[12:15],s48 offen offset:3072 lds\n"
MF"v[104:107],v[212:215],a[212:215],v[104:107]\n"
RD"v[196:199],v243 offset:16384\n"
MF"v[104:107],v[208:211],a[196:199],v[104:107]\n"
RT"a[252:253],v249 offset:12288\n"
MF"v[112:115],v[208:211],a[200:203],v[112:115]\n"
MF"v[112:115],v[212:215],a[216:219],v[112:115]\n"
RT"a[254:255],v249 offset:12800\n"
MF"v[120:123],v[212:215],a[220:223],v[120:123]\n"
RD"v[200:203],v242 offset:18432\n"
MF"v[120:123],v[208:211],a[204:207],v[120:123]\n"
MF"v[100:103],v[216:219],a[192:195],v[100:103]\n"
"s_mul_i32 s50,s35,1048576\n"
MF"v[100:103],v[220:223],a[208:211],v[100:103]\n"
"s_lshl_b32 s51,s34,15\n"
RD"v[204:207],v243 offset:18432\n"
SA"m0,s37,12288\n"
MF"v[108:111],v[220:223],a[212:215],v[108:111]\n"
SA"s48,s98,65536\n"
SA"s50,s50,s51\n"
MF"v[108:111],v[216:219],a[196:199],v[108:111]\n"
WT"lgkmcnt(2)\n"
BL"v234,s[12:15],s48 offen offset:0 lds\n"
MF"v[116:119],v[216:219],a[200:203],v[116:119]\n"
"s_and_b32 s51,s30,1\n"
"s_lshl_b32 s51,s51,12\n"
MF"v[116:119],v[220:223],a[216:219],v[116:119]\n"
SA"s50,s50,s51\n"
RD"v[208:211],v242 offset:20480\n"
MF"v[124:127],v[220:223],a[220:223],v[124:127]\n"
SA"s50,s50,9699328\n"
WT"vmcnt(10)\n"
"v_cmp_eq_u64 vcc,s[28:29],v[160:161]\n"
"v_cmp_eq_u64_e64 s[62:63],s[28:29],v[162:163]\n"
"s_and_b64 vcc,vcc,s[62:63]\n"
"v_cmp_eq_u64_e64 s[62:63],s[28:29],v[164:165]\n"
"s_and_b64 vcc,vcc,s[62:63]\n"
"v_cmp_eq_u64_e64 s[62:63],s[28:29],v[166:167]\n"
"s_and_b64 vcc,vcc,s[62:63]\n"
"s_cmp_eq_u64 vcc,exec\n"
"s_cbranch_scc0 L1004_%=\n"
"L1005_%=:\n"
MF"v[124:127],v[216:219],a[204:207],v[124:127]\n"
"s_mov_b32 s94,8192\n"
BL"v235,s[12:15],s48 offen offset:1024 lds\n"
MF"v[0:3],v[192:195],a[224:227],v[0:3]\n"
"s_mov_b32 s95,12288\n"
RD"v[212:215],v243 offset:20480\n"
"v_add_u32 v250,s50,v224\n"
MF"v[0:3],v[196:199],a[240:243],v[0:3]\n"
BL"v[160:163],v224,s[8:11],s94 offen offset:0 sc1\n"
MF"v[8:11],v[196:199],a[244:247],v[8:11]\n"
BL"v[164:167],v224,s[8:11],s94 offen offset:1024 sc1\n"
MF"v[8:11],v[192:195],a[228:231],v[8:11]\n"
MF"v[16:19],v[192:195],a[232:235],v[16:19]\n"
RD"v[216:219],v242 offset:22528\n"
MF"v[16:19],v[196:199],a[248:251],v[16:19]\n"
BL"v236,s[12:15],s48 offen offset:2048 lds\n"
MF"v[24:27],v[196:199],a[252:255],v[24:27]\n"
BL"v[168:171],v224,s[8:11],s94 offen offset:2048 sc1\n"
MF"v[24:27],v[192:195],a[236:239],v[24:27]\n"
WT"lgkmcnt(1)\n"
BL"v[172:175],v224,s[8:11],s94 offen offset:3072 sc1\n"
MF"v[4:7],v[200:203],a[224:227],v[4:7]\n"
RD"v[220:223],v243 offset:22528\n"
MF"v[4:7],v[204:207],a[240:243],v[4:7]\n"
BL"v237,s[12:15],s48 offen offset:3072 lds\n"
MF"v[12:15],v[204:207],a[244:247],v[12:15]\n"
BL"v[176:179],v224,s[8:11],s95 offen offset:0 sc1\n"
MF"v[12:15],v[200:203],a[228:231],v[12:15]\n"
BL"v[180:183],v224,s[8:11],s95 offen offset:1024 sc1\n"
MF"v[20:23],v[200:203],a[232:235],v[20:23]\n"
RD"v[192:195],v242 offset:24576\n"
MF"v[20:23],v[204:207],a[248:251],v[20:23]\n"
BL"v[184:187],v224,s[8:11],s95 offen offset:2048 sc1\n"
MF"v[28:31],v[204:207],a[252:255],v[28:31]\n"
BL"v[188:191],v224,s[8:11],s95 offen offset:3072 sc1\n"
MF"v[28:31],v[200:203],a[236:239],v[28:31]\n"
RD"v[196:199],v243 offset:24576\n"
MF"v[32:35],v[208:211],a[224:227],v[32:35]\n"
MF"v[32:35],v[212:215],a[240:243],v[32:35]\n"
MF"v[40:43],v[212:215],a[244:247],v[40:43]\n"
RD"v[200:203],v242 offset:26624\n"
MF"v[40:43],v[208:211],a[228:231],v[40:43]\n"
MF"v[48:51],v[208:211],a[232:235],v[48:51]\n"
MF"v[48:51],v[212:215],a[248:251],v[48:51]\n"
MF"v[56:59],v[212:215],a[252:255],v[56:59]\n"
RD"v[204:207],v243 offset:26624\n"
MF"v[56:59],v[208:211],a[236:239],v[56:59]\n"
WT"lgkmcnt(2)\n"
MF"v[36:39],v[216:219],a[224:227],v[36:39]\n"
MF"v[36:39],v[220:223],a[240:243],v[36:39]\n"
RD"v[208:211],v242 offset:28672\n"
MF"v[44:47],v[220:223],a[244:247],v[44:47]\n"
MF"v[44:47],v[216:219],a[228:231],v[44:47]\n"
MF"v[52:55],v[216:219],a[232:235],v[52:55]\n"
MF"v[52:55],v[220:223],a[248:251],v[52:55]\n"
RD"v[212:215],v243 offset:28672\n"
MF"v[60:63],v[220:223],a[252:255],v[60:63]\n"
MF"v[60:63],v[216:219],a[236:239],v[60:63]\n"
MF"v[64:67],v[192:195],a[224:227],v[64:67]\n"
RD"v[216:219],v242 offset:30720\n"
MF"v[64:67],v[196:199],a[240:243],v[64:67]\n"
MF"v[72:75],v[196:199],a[244:247],v[72:75]\n"
MF"v[72:75],v[192:195],a[228:231],v[72:75]\n"
MF"v[80:83],v[192:195],a[232:235],v[80:83]\n"
RD"v[220:223],v243 offset:30720\n"
MF"v[80:83],v[196:199],a[248:251],v[80:83]\n"
MF"v[88:91],v[196:199],a[252:255],v[88:91]\n"
MF"v[88:91],v[192:195],a[236:239],v[88:91]\n"
WT"lgkmcnt(1)\n"
MF"v[68:71],v[200:203],a[224:227],v[68:71]\n"
MF"v[68:71],v[204:207],a[240:243],v[68:71]\n"
MF"v[76:79],v[204:207],a[244:247],v[76:79]\n"
"v_add_u32 v244,114688,v242\n"
"v_add_u32 v245,114688,v243\n"
MF"v[76:79],v[200:203],a[228:231],v[76:79]\n"
"v_add_u32 v242,65536,v242\n"
"v_add_u32 v243,65536,v243\n"
MF"v[84:87],v[200:203],a[232:235],v[84:87]\n"
WT"lgkmcnt(0)\n"
MF"v[84:87],v[204:207],a[248:251],v[84:87]\n"
"s_barrier\n"
MF"v[92:95],v[204:207],a[252:255],v[92:95]\n"
RD"v[192:195],v242 offset:16384\n"
MF"v[92:95],v[200:203],a[236:239],v[92:95]\n"
MF"v[96:99],v[208:211],a[224:227],v[96:99]\n"
MF"v[96:99],v[212:215],a[240:243],v[96:99]\n"
MF"v[104:107],v[212:215],a[244:247],v[104:107]\n"
SA"m0,s36,49152\n"
RD"v[196:199],v243 offset:16384\n"
SA"s48,s60,s61\n"
MF"v[104:107],v[208:211],a[228:231],v[104:107]\n"
MF"v[112:115],v[208:211],a[232:235],v[112:115]\n"
BL"v238,s[20:23],s48 offen offset:0 lds\n"
MF"v[112:115],v[212:215],a[248:251],v[112:115]\n"
MF"v[120:123],v[212:215],a[252:255],v[120:123]\n"
RD"v[200:203],v242 offset:18432\n"
MF"v[120:123],v[208:211],a[236:239],v[120:123]\n"
MF"v[100:103],v[216:219],a[224:227],v[100:103]\n"
BL"v239,s[20:23],s48 offen offset:1024 lds\n"
MF"v[100:103],v[220:223],a[240:243],v[100:103]\n"
"s_cmp_eq_u32 s31,1\n"
"s_cselect_b32 s94,0,2130706432\n"
MF"v[108:111],v[220:223],a[244:247],v[108:111]\n"
"buffer_store_dwordx2 v[232:233],v230,s[8:11],s94 offen sc1\n"
MF"v[108:111],v[216:219],a[228:231],v[108:111]\n"
RD"v[204:207],v243 offset:18432\n"
"v_mov_b32 v227,0\n"
MF"v[116:119],v[216:219],a[232:235],v[116:119]\n"
WT"vmcnt(12)\n"
"v_mov_b32 v226,v160\n"
"v_mov_b32 v228,0\n"
MF"v[116:119],v[220:223],a[248:251],v[116:119]\n"
BL"v240,s[20:23],s48 offen offset:2048 lds\n"
MF"v[124:127],v[220:223],a[252:255],v[124:127]\n"
SA"s61,s61,524288\n"
RD"v[208:211],v242 offset:20480\n"
"v_med3_u32 v228,v227,v228,v161\n"
MF"v[124:127],v[216:219],a[236:239],v[124:127]\n"
WT"lgkmcnt(2)\n"
"v_med3_u32 v227,v226,v227,v161\n"
"v_max_u32 v226,v226,v161\n"
MF"v[128:131],v[192:195],a[0:3],0\n"
"v_med3_u32 v228,v227,v228,v162\n"
"v_med3_u32 v227,v226,v227,v162\n"
MF"v[128:131],v[196:199],a[16:19],v[128:131]\n"
RD"v[212:215],v243 offset:20480\n"
"v_max_u32 v226,v226,v162\n"
MF"v[136:139],v[196:199],a[20:23],0\n"
BL"v241,s[20:23],s48 offen offset:3072 lds\n"
MF"v[136:139],v[192:195],a[4:7],v[136:139]\n"
"v_med3_u32 v228,v227,v228,v163\n"
"v_med3_u32 v227,v226,v227,v163\n"
MF"v[144:147],v[192:195],a[8:11],0\n"
"v_max_u32 v226,v226,v163\n"
PK"v0,v0,v1\n"
MF"v[144:147],v[196:199],a[24:27],v[144:147]\n"
RD"v[216:219],v242 offset:22528\n"
PK"v1,v2,v3\n"
MF"v[152:155],v[196:199],a[28:31],0\n"
SA"m0,s36,32768\n"
WT"vmcnt(13)\n"
"v_med3_u32 v228,v227,v228,v164\n"
MF"v[152:155],v[192:195],a[12:15],v[152:155]\n"
SA"s48,s60,s61\n"
PK"v2,v4,v5\n"
PK"v3,v6,v7\n"
MF"v[132:135],v[200:203],a[0:3],0\n"
WT"lgkmcnt(1)\n"
BL"v238,s[20:23],s48 offen offset:0 lds\n"
MF"v[132:135],v[204:207],a[16:19],v[132:135]\n"
RD"v[220:223],v243 offset:22528\n"
"v_med3_u32 v227,v226,v227,v164\n"
MF"v[140:143],v[204:207],a[20:23],0\n"
"v_max_u32 v226,v226,v164\n"
"v_med3_u32 v228,v227,v228,v165\n"
MF"v[140:143],v[200:203],a[4:7],v[140:143]\n"
"v_med3_u32 v227,v226,v227,v165\n"
"v_max_u32 v226,v226,v165\n"
MF"v[148:151],v[200:203],a[8:11],0\n"
BL"v239,s[20:23],s48 offen offset:1024 lds\n"
MF"v[148:151],v[204:207],a[24:27],v[148:151]\n"
RD"v[192:195],v242 offset:40960\n"
"v_med3_u32 v228,v227,v228,v166\n"
MF"v[156:159],v[204:207],a[28:31],0\n"
PK"v8,v8,v9\n"
PK"v9,v10,v11\n"
MF"v[156:159],v[200:203],a[12:15],v[156:159]\n"
"v_med3_u32 v227,v226,v227,v166\n"
PK"v10,v12,v13\n"
MF"v[128:131],v[208:211],a[32:35],v[128:131]\n"
"s_mov_b32 s49,0\n"
PK"v11,v14,v15\n"
"v_max_u32 v226,v226,v166\n"
MF"v[128:131],v[212:215],a[48:51],v[128:131]\n"
RD"v[196:199],v243 offset:40960\n"
"v_med3_u32 v228,v227,v228,v167\n"
MF"v[136:139],v[212:215],a[52:55],v[136:139]\n"
SA"s61,s61,524288\n"
BL"v240,s[20:23],s48 offen offset:2048 lds\n"
MF"v[136:139],v[208:211],a[36:39],v[136:139]\n"
"s_cmp_eq_u32 s61,5242880\n"
BS"v[0:3],v250,s[8:11],s49 offen offset:0 sc1\n"
MF"v[144:147],v[208:211],a[40:43],v[144:147]\n"
"s_cselect_b32 s61,0,s61\n"
RD"v[200:203],v242 offset:43008\n"
"v_med3_u32 v227,v226,v227,v167\n"
MF"v[144:147],v[212:215],a[56:59],v[144:147]\n"
BL"v241,s[20:23],s48 offen offset:3072 lds\n"
MF"v[152:155],v[212:215],a[60:63],v[152:155]\n"
"v_max_u32 v226,v226,v167\n"
PK"v16,v16,v17\n"
MF"v[152:155],v[208:211],a[44:47],v[152:155]\n"
WT"lgkmcnt(2)\n"
WT"vmcnt(16)\n"
"v_med3_u32 v228,v227,v228,v168\n"
MF"v[132:135],v[216:219],a[32:35],v[132:135]\n"
RD"v[204:207],v243 offset:43008\n"
"v_med3_u32 v227,v226,v227,v168\n"
MF"v[132:135],v[220:223],a[48:51],v[132:135]\n"
PK"v17,v18,v19\n"
"v_max_u32 v226,v226,v168\n"
MF"v[140:143],v[220:223],a[52:55],v[140:143]\n"
"v_med3_u32 v228,v227,v228,v169\n"
PK"v18,v20,v21\n"
MF"v[140:143],v[216:219],a[36:39],v[140:143]\n"
PK"v19,v22,v23\n"
"v_med3_u32 v227,v226,v227,v169\n"
MF"v[148:151],v[216:219],a[40:43],v[148:151]\n"
RD"v[208:211],v242 offset:45056\n"
"v_max_u32 v226,v226,v169\n"
MF"v[148:151],v[220:223],a[56:59],v[148:151]\n"
"s_lshl_b32 s90,s31,2\n"
"v_med3_u32 v228,v227,v228,v170\n"
"v_med3_u32 v227,v226,v227,v170\n"
MF"v[156:159],v[220:223],a[60:63],v[156:159]\n"
SA"s90,s90,s30\n"
"v_max_u32 v226,v226,v170\n"
PK"v24,v24,v25\n"
MF"v[156:159],v[216:219],a[44:47],v[156:159]\n"
"s_lshl_b32 s90,s90,9\n"
PK"v25,v26,v27\n"
"v_med3_u32 v228,v227,v228,v171\n"
MF"v[128:131],v[192:195],a[64:67],v[128:131]\n"
WT"lgkmcnt(1)\n"
RD"v[212:215],v243 offset:45056\n"
"v_med3_u32 v227,v226,v227,v171\n"
MF"v[128:131],v[196:199],a[80:83],v[128:131]\n"
PK"v26,v28,v29\n"
PK"v27,v30,v31\n"
MF"v[136:139],v[196:199],a[84:87],v[136:139]\n"
"v_max_u32 v226,v226,v171\n"
PK"v32,v32,v33\n"
MF"v[136:139],v[192:195],a[68:71],v[136:139]\n"
RD"v[216:219],v242 offset:47104\n"
PK"v33,v34,v35\n"
MF"v[144:147],v[192:195],a[72:75],v[144:147]\n"
WT"vmcnt(15)\n"
"v_med3_u32 v228,v227,v228,v172\n"
"v_med3_u32 v227,v226,v227,v172\n"
MF"v[144:147],v[196:199],a[88:91],v[144:147]\n"
"v_max_u32 v226,v226,v172\n"
"v_med3_u32 v228,v227,v228,v173\n"
MF"v[152:155],v[196:199],a[92:95],v[152:155]\n"
"v_med3_u32 v227,v226,v227,v173\n"
"v_max_u32 v226,v226,v173\n"
MF"v[152:155],v[192:195],a[76:79],v[152:155]\n"
RD"v[220:223],v243 offset:47104\n"
PK"v34,v36,v37\n"
MF"v[132:135],v[200:203],a[64:67],v[132:135]\n"
PK"v35,v38,v39\n"
"v_med3_u32 v228,v227,v228,v174\n"
MF"v[132:135],v[204:207],a[80:83],v[132:135]\n"
"v_med3_u32 v227,v226,v227,v174\n"
"v_max_u32 v226,v226,v174\n"
MF"v[140:143],v[204:207],a[84:87],v[140:143]\n"
"v_med3_u32 v228,v227,v228,v175\n"
"v_med3_u32 v227,v226,v227,v175\n"
MF"v[140:143],v[200:203],a[68:71],v[140:143]\n"
RD"v[192:195],v244 offset:16384\n"
"v_max_u32 v226,v226,v175\n"
MF"v[148:151],v[200:203],a[72:75],v[148:151]\n"
PK"v40,v40,v41\n"
PK"v41,v42,v43\n"
MF"v[148:151],v[204:207],a[88:91],v[148:151]\n"
"v_min_u32 v254,48,v224\n"
PK"v42,v44,v45\n"
MF"v[156:159],v[204:207],a[92:95],v[156:159]\n"
WT"vmcnt(13)\n"
"v_med3_u32 v228,v227,v228,v176\n"
PK"v43,v46,v47\n"
MF"v[156:159],v[200:203],a[76:79],v[156:159]\n"
WT"lgkmcnt(1)\n"
RD"v[196:199],v245 offset:16384\n"
"v_lshlrev_b32 v254,3,v254\n"
MF"v[128:131],v[208:211],a[96:99],v[128:131]\n"
"buffer_load_dword v255,v254,s[8:11],s90 offen sc1\n"
MF"v[128:131],v[212:215],a[112:115],v[128:131]\n"
BS"v[8:11],v250,s[8:11],s49 offen offset:1024 sc1\n"
MF"v[136:139],v[212:215],a[116:119],v[136:139]\n"
"v_med3_u32 v227,v226,v227,v176\n"
"v_max_u32 v226,v226,v176\n"
MF"v[136:139],v[208:211],a[100:103],v[136:139]\n"
RD"v[200:203],v244 offset:18432\n"
"v_med3_u32 v228,v227,v228,v177\n"
MF"v[144:147],v[208:211],a[104:107],v[144:147]\n"
"v_med3_u32 v227,v226,v227,v177\n"
"v_max_u32 v226,v226,v177\n"
MF"v[144:147],v[212:215],a[120:123],v[144:147]\n"
"v_med3_u32 v228,v227,v228,v178\n"
PK"v48,v48,v49\n"
MF"v[152:155],v[212:215],a[124:127],v[152:155]\n"
RD"v[204:207],v245 offset:18432\n"
PK"v49,v50,v51\n"
MF"v[152:155],v[208:211],a[108:111],v[152:155]\n"
"v_med3_u32 v227,v226,v227,v178\n"
PK"v50,v52,v53\n"
MF"v[132:135],v[216:219],a[96:99],v[132:135]\n"
PK"v51,v54,v55\n"
"v_max_u32 v226,v226,v178\n"
MF"v[132:135],v[220:223],a[112:115],v[132:135]\n"
"v_med3_u32 v228,v227,v228,v179\n"
"v_med3_u32 v227,v226,v227,v179\n"
MF"v[140:143],v[220:223],a[116:119],v[140:143]\n"
RD"v[208:211],v244 offset:20480\n"
"v_max_u32 v226,v226,v179\n"
MF"v[140:143],v[216:219],a[100:103],v[140:143]\n"
WT"vmcnt(14)\n"
"v_med3_u32 v228,v227,v228,v180\n"
"v_med3_u32 v227,v226,v227,v180\n"
MF"v[148:151],v[216:219],a[104:107],v[148:151]\n"
PK"v56,v56,v57\n"
PK"v57,v58,v59\n"
MF"v[148:151],v[220:223],a[120:123],v[148:151]\n"
"v_max_u32 v226,v226,v180\n"
PK"v58,v60,v61\n"
MF"v[156:159],v[220:223],a[124:127],v[156:159]\n"
RD"v[212:215],v245 offset:20480\n"
PK"v59,v62,v63\n"
MF"v[156:159],v[216:219],a[108:111],v[156:159]\n"
WT"lgkmcnt(2)\n"
"v_med3_u32 v228,v227,v228,v181\n"
"v_med3_u32 v227,v226,v227,v181\n"
MF"v[128:131],v[192:195],a[128:131],v[128:131]\n"
"v_max_u32 v226,v226,v181\n"
"v_med3_u32 v228,v227,v228,v182\n"
MF"v[128:131],v[196:199],a[144:147],v[128:131]\n"
"v_med3_u32 v227,v226,v227,v182\n"
"v_max_u32 v226,v226,v182\n"
MF"v[136:139],v[196:199],a[148:151],v[136:139]\n"
RD"v[216:219],v244 offset:22528\n"
PK"v64,v64,v65\n"
MF"v[136:139],v[192:195],a[132:135],v[136:139]\n"
PK"v65,v66,v67\n"
"v_med3_u32 v228,v227,v228,v183\n"
MF"v[144:147],v[192:195],a[136:139],v[144:147]\n"
PK"v66,v68,v69\n"
PK"v67,v70,v71\n"
MF"v[144:147],v[196:199],a[152:155],v[144:147]\n"
RD"v[220:223],v245 offset:22528\n"
"v_med3_u32 v227,v226,v227,v183\n"
MF"v[152:155],v[196:199],a[156:159],v[152:155]\n"
"v_max_u32 v226,v226,v183\n"
PK"v72,v72,v73\n"
MF"v[152:155],v[192:195],a[140:143],v[152:155]\n"
WT"vmcnt(13)\n"
"v_med3_u32 v228,v227,v228,v184\n"
"v_med3_u32 v227,v226,v227,v184\n"
MF"v[132:135],v[200:203],a[128:131],v[132:135]\n"
"v_max_u32 v226,v226,v184\n"
"v_med3_u32 v228,v227,v228,v185\n"
MF"v[132:135],v[204:207],a[144:147],v[132:135]\n"
RD"v[192:195],v244 offset:40960\n"
PK"v73,v74,v75\n"
MF"v[140:143],v[204:207],a[148:151],v[140:143]\n"
"v_med3_u32 v227,v226,v227,v185\n"
"v_max_u32 v226,v226,v185\n"
MF"v[140:143],v[200:203],a[132:135],v[140:143]\n"
PK"v74,v76,v77\n"
PK"v75,v78,v79\n"
MF"v[148:151],v[200:203],a[136:139],v[148:151]\n"
"v_med3_u32 v228,v227,v228,v186\n"
"v_med3_u32 v227,v226,v227,v186\n"
MF"v[148:151],v[204:207],a[152:155],v[148:151]\n"
RD"v[196:199],v245 offset:40960\n"
"v_max_u32 v226,v226,v186\n"
MF"v[156:159],v[204:207],a[156:159],v[156:159]\n"
"v_med3_u32 v228,v227,v228,v187\n"
"v_med3_u32 v227,v226,v227,v187\n"
MF"v[156:159],v[200:203],a[140:143],v[156:159]\n"
WT"lgkmcnt(2)\n"
PK"v80,v80,v81\n"
PK"v81,v82,v83\n"
MF"v[128:131],v[208:211],a[160:163],v[128:131]\n"
BS"v[16:19],v250,s[8:11],s49 offen offset:2048 sc1\n"
MF"v[128:131],v[212:215],a[176:179],v[128:131]\n"
RD"v[200:203],v244 offset:43008\n"
"v_max_u32 v226,v226,v187\n"
MF"v[136:139],v[212:215],a[180:183],v[136:139]\n"
WT"vmcnt(13)\n"
"v_med3_u32 v228,v227,v228,v188\n"
PK"v82,v84,v85\n"
MF"v[136:139],v[208:211],a[164:167],v[136:139]\n"
PK"v83,v86,v87\n"
"v_med3_u32 v227,v226,v227,v188\n"
MF"v[144:147],v[208:211],a[168:171],v[144:147]\n"
"v_max_u32 v226,v226,v188\n"
"v_med3_u32 v228,v227,v228,v189\n"
MF"v[144:147],v[212:215],a[184:187],v[144:147]\n"
RD"v[204:207],v245 offset:43008\n"
"v_med3_u32 v227,v226,v227,v189\n"
MF"v[152:155],v[212:215],a[188:191],v[152:155]\n"
"v_max_u32 v226,v226,v189\n"
PK"v88,v88,v89\n"
MF"v[152:155],v[208:211],a[172:175],v[152:155]\n"
PK"v89,v90,v91\n"
"v_med3_u32 v228,v227,v228,v190\n"
MF"v[132:135],v[216:219],a[160:163],v[132:135]\n"
RD"v[208:211],v244 offset:45056\n"
"v_med3_u32 v227,v226,v227,v190\n"
MF"v[132:135],v[220:223],a[176:179],v[132:135]\n"
PK"v90,v92,v93\n"
PK"v91,v94,v95\n"
MF"v[140:143],v[220:223],a[180:183],v[140:143]\n"
"v_max_u32 v226,v226,v190\n"
"v_med3_u32 v228,v227,v228,v191\n"
MF"v[140:143],v[216:219],a[164:167],v[140:143]\n"
"v_med3_u32 v227,v226,v227,v191\n"
"v_max_u32 v226,v226,v191\n"
MF"v[148:151],v[216:219],a[168:171],v[148:151]\n"
RD"v[212:215],v245 offset:45056\n"
"v_mov_b32 v229,v226\n"
MF"v[148:151],v[220:223],a[184:187],v[148:151]\n"
MF"v[156:159],v[220:223],a[188:191],v[156:159]\n"
"v_max_u32_dpp v229,v229,v229 row_shr:1 row_mask:0xf bank_mask:0xf\n"
MF"v[156:159],v[216:219],a[172:175],v[156:159]\n"
WT"lgkmcnt(1)\n"
"v_max_u32_dpp v229,v229,v229 row_shr:2 row_mask:0xf bank_mask:0xf\n"
MF"v[128:131],v[192:195],a[192:195],v[128:131]\n"
RD"v[216:219],v244 offset:47104\n"
"v_max_u32_dpp v229,v229,v229 row_shr:4 row_mask:0xf bank_mask:0xf\n"
MF"v[128:131],v[196:199],a[208:211],v[128:131]\n"
MF"v[136:139],v[196:199],a[212:215],v[136:139]\n"
"v_max_u32_dpp v229,v229,v229 row_shr:8 row_mask:0xf bank_mask:0xf\n"
MF"v[136:139],v[192:195],a[196:199],v[136:139]\n"
MF"v[144:147],v[192:195],a[200:203],v[144:147]\n"
RD"v[220:223],v245 offset:47104\n"
"v_max_u32_dpp v229,v229,v229 row_bcast:15 row_mask:0xa bank_mask:0xf\n"
MF"v[144:147],v[196:199],a[216:219],v[144:147]\n"
MF"v[152:155],v[196:199],a[220:223],v[152:155]\n"
"v_max_u32_dpp v229,v229,v229 row_bcast:31 row_mask:0xc bank_mask:0xf\n"
MF"v[152:155],v[192:195],a[204:207],v[152:155]\n"
"v_readlane_b32 s62,v229,63\n"
MF"v[132:135],v[200:203],a[192:195],v[132:135]\n"
MF"v[132:135],v[204:207],a[208:211],v[132:135]\n"
"v_cmp_eq_u32 vcc,s62,v226\n"
"v_writelane_b32 v225,s62,0\n"
MF"v[140:143],v[204:207],a[212:215],v[140:143]\n"
"v_cndmask_b32 v226,v226,v227,vcc\n"
"v_cndmask_b32_e64 v227,v227,0,vcc\n"
MF"v[140:143],v[200:203],a[196:199],v[140:143]\n"
"v_mov_b32 v229,v226\n"
MF"v[148:151],v[200:203],a[200:203],v[148:151]\n"
WT"lgkmcnt(0)\n"
"v_max_u32_dpp v229,v229,v229 row_shr:1 row_mask:0xf bank_mask:0xf\n"
MF"v[148:151],v[204:207],a[216:219],v[148:151]\n"
"s_barrier\n"
MF"v[156:159],v[204:207],a[220:223],v[156:159]\n"
RD"v[192:195],v242 offset:0\n"
"v_max_u32_dpp v229,v229,v229 row_shr:2 row_mask:0xf bank_mask:0xf\n"
MF"v[156:159],v[200:203],a[204:207],v[156:159]\n"
BS"v[24:27],v250,s[8:11],s49 offen offset:3072 sc1\n"
MF"v[128:131],v[208:211],a[224:227],v[128:131]\n"
"v_max_u32_dpp v229,v229,v229 row_shr:4 row_mask:0xf bank_mask:0xf\n"
MF"v[128:131],v[212:215],a[240:243],v[128:131]\n"
MF"v[136:139],v[212:215],a[244:247],v[136:139]\n"
RD"v[196:199],v243 offset:0\n"
"v_max_u32_dpp v229,v229,v229 row_shr:8 row_mask:0xf bank_mask:0xf\n"
MF"v[136:139],v[208:211],a[228:231],v[136:139]\n"
MF"v[144:147],v[208:211],a[232:235],v[144:147]\n"
"v_max_u32_dpp v229,v229,v229 row_bcast:15 row_mask:0xa bank_mask:0xf\n"
MF"v[144:147],v[212:215],a[248:251],v[144:147]\n"
MF"v[152:155],v[212:215],a[252:255],v[152:155]\n"
RD"v[200:203],v242 offset:2048\n"
"v_max_u32_dpp v229,v229,v229 row_bcast:31 row_mask:0xc bank_mask:0xf\n"
MF"v[152:155],v[208:211],a[236:239],v[152:155]\n"
"v_readlane_b32 s62,v229,63\n"
MF"v[132:135],v[216:219],a[224:227],v[132:135]\n"
MF"v[132:135],v[220:223],a[240:243],v[132:135]\n"
"v_cmp_eq_u32 vcc,s62,v226\n"
"v_writelane_b32 v225,s62,1\n"
MF"v[140:143],v[220:223],a[244:247],v[140:143]\n"
RD"v[204:207],v243 offset:2048\n"
"v_cndmask_b32 v226,v226,v227,vcc\n"
MF"v[140:143],v[216:219],a[228:231],v[140:143]\n"
"v_cndmask_b32_e64 v227,v227,0,vcc\n"
"v_mov_b32 v229,v226\n"
MF"v[148:151],v[216:219],a[232:235],v[148:151]\n"
MF"v[148:151],v[220:223],a[248:251],v[148:151]\n"
"v_max_u32_dpp v229,v229,v229 row_shr:1 row_mask:0xf bank_mask:0xf\n"
MF"v[156:159],v[220:223],a[252:255],v[156:159]\n"
RD"v[208:211],v242 offset:4096\n"
"v_max_u32_dpp v229,v229,v229 row_shr:2 row_mask:0xf bank_mask:0xf\n"
MF"v[156:159],v[216:219],a[236:239],v[156:159]\n"
WT"lgkmcnt(2)\n"
"v_max_u32_dpp v229,v229,v229 row_shr:4 row_mask:0xf bank_mask:0xf\n"
MF"v[36:39],v[192:195],a[0:3],0\n"
MF"v[36:39],v[196:199],a[16:19],v[36:39]\n"
RD"v[212:215],v243 offset:4096\n"
"v_max_u32_dpp v229,v229,v229 row_shr:8 row_mask:0xf bank_mask:0xf\n"
MF"v[44:47],v[196:199],a[20:23],0\n"
MF"v[44:47],v[192:195],a[4:7],v[44:47]\n"
"v_max_u32_dpp v229,v229,v229 row_bcast:15 row_mask:0xa bank_mask:0xf\n"
PK"v4,v128,v129\n"
MF"v[52:55],v[192:195],a[8:11],0\n"
"v_max_u32_dpp v229,v229,v229 row_bcast:31 row_mask:0xc bank_mask:0xf\n"
PK"v5,v130,v131\n"
MF"v[52:55],v[196:199],a[24:27],v[52:55]\n"
RD"v[216:219],v242 offset:6144\n"
"v_readlane_b32 s62,v229,63\n"
MF"v[60:63],v[196:199],a[28:31],0\n"
PK"v6,v132,v133\n"
"v_cmp_eq_u32 vcc,s62,v226\n"
MF"v[60:63],v[192:195],a[12:15],v[60:63]\n"
"v_writelane_b32 v225,s62,2\n"
"v_cndmask_b32 v226,v226,v227,vcc\n"
MF"v[68:71],v[200:203],a[0:3],0\n"
WT"lgkmcnt(1)\n"
"v_cndmask_b32_e64 v227,v227,0,vcc\n"
"v_mov_b32 v229,v226\n"
MF"v[68:71],v[204:207],a[16:19],v[68:71]\n"
RD"v[220:223],v243 offset:6144\n"
"v_max_u32_dpp v229,v229,v229 row_shr:1 row_mask:0xf bank_mask:0xf\n"
MF"v[76:79],v[204:207],a[20:23],0\n"
PK"v7,v134,v135\n"
"v_max_u32_dpp v229,v229,v229 row_shr:2 row_mask:0xf bank_mask:0xf\n"
MF"v[76:79],v[200:203],a[4:7],v[76:79]\n"
PK"v12,v136,v137\n"
"v_max_u32_dpp v229,v229,v229 row_shr:4 row_mask:0xf bank_mask:0xf\n"
MF"v[84:87],v[200:203],a[8:11],0\n"
"s_mov_b32 s49,8192\n"
"v_max_u32_dpp v229,v229,v229 row_shr:8 row_mask:0xf bank_mask:0xf\n"
PK"v13,v138,v139\n"
MF"v[84:87],v[204:207],a[24:27],v[84:87]\n"
RD"v[192:195],v242 offset:24576\n"
"v_max_u32_dpp v229,v229,v229 row_bcast:15 row_mask:0xa bank_mask:0xf\n"
MF"v[92:95],v[204:207],a[28:31],0\n"
BS"v[32:35],v250,s[8:11],s49 offen offset:0 sc1\n"
MF"v[92:95],v[200:203],a[12:15],v[92:95]\n"
"v_max_u32_dpp v229,v229,v229 row_bcast:31 row_mask:0xc bank_mask:0xf\n"
PK"v14,v140,v141\n"
MF"v[36:39],v[208:211],a[32:35],v[36:39]\n"
"v_readlane_b32 s62,v229,63\n"
PK"v15,v142,v143\n"
MF"v[36:39],v[212:215],a[48:51],v[36:39]\n"
RD"v[196:199],v243 offset:24576\n"
"v_cmp_eq_u32 vcc,s62,v226\n"
MF"v[44:47],v[212:215],a[52:55],v[44:47]\n"
"v_writelane_b32 v225,s62,3\n"
"v_cndmask_b32 v226,v226,v227,vcc\n"
MF"v[44:47],v[208:211],a[36:39],v[44:47]\n"
"v_cndmask_b32_e64 v227,v227,0,vcc\n"
"v_mov_b32 v229,v226\n"
MF"v[52:55],v[208:211],a[40:43],v[52:55]\n"
RD"v[200:203],v242 offset:26624\n"
"v_max_u32_dpp v229,v229,v229 row_shr:1 row_mask:0xf bank_mask:0xf\n"
MF"v[52:55],v[212:215],a[56:59],v[52:55]\n"
PK"v20,v144,v145\n"
"v_max_u32_dpp v229,v229,v229 row_shr:2 row_mask:0xf bank_mask:0xf\n"
MF"v[60:63],v[212:215],a[60:63],v[60:63]\n"
PK"v21,v146,v147\n"
"v_max_u32_dpp v229,v229,v229 row_shr:4 row_mask:0xf bank_mask:0xf\n"
MF"v[60:63],v[208:211],a[44:47],v[60:63]\n"
WT"lgkmcnt(2)\n"
"v_max_u32_dpp v229,v229,v229 row_shr:8 row_mask:0xf bank_mask:0xf\n"
PK"v22,v148,v149\n"
MF"v[68:71],v[216:219],a[32:35],v[68:71]\n"
RD"v[204:207],v243 offset:26624\n"
"v_max_u32_dpp v229,v229,v229 row_bcast:15 row_mask:0xa bank_mask:0xf\n"
MF"v[68:71],v[220:223],a[48:51],v[68:71]\n"
PK"v23,v150,v151\n"
"v_max_u32_dpp v229,v229,v229 row_bcast:31 row_mask:0xc bank_mask:0xf\n"
MF"v[76:79],v[220:223],a[52:55],v[76:79]\n"
"v_readlane_b32 s62,v229,63\n"
PK"v28,v152,v153\n"
MF"v[76:79],v[216:219],a[36:39],v[76:79]\n"
"v_cmp_eq_u32 vcc,s62,v226\n"
"v_writelane_b32 v225,s62,4\n"
MF"v[84:87],v[216:219],a[40:43],v[84:87]\n"
RD"v[208:211],v242 offset:28672\n"
"v_cndmask_b32 v226,v226,v227,vcc\n"
MF"v[84:87],v[220:223],a[56:59],v[84:87]\n"
"v_cndmask_b32_e64 v227,v227,0,vcc\n"
"v_mov_b32 v229,v226\n"
MF"v[92:95],v[220:223],a[60:63],v[92:95]\n"
PK"v29,v154,v155\n"
"v_max_u32_dpp v229,v229,v229 row_shr:1 row_mask:0xf bank_mask:0xf\n"
MF"v[92:95],v[216:219],a[44:47],v[92:95]\n"
PK"v30,v156,v157\n"
"v_max_u32_dpp v229,v229,v229 row_shr:2 row_mask:0xf bank_mask:0xf\n"
MF"v[36:39],v[192:195],a[64:67],v[36:39]\n"
WT"lgkmcnt(1)\n"
RD"v[212:215],v243 offset:28672\n"
"v_max_u32_dpp v229,v229,v229 row_shr:4 row_mask:0xf bank_mask:0xf\n"
MF"v[36:39],v[196:199],a[80:83],v[36:39]\n"
PK"v31,v158,v159\n"
"v_max_u32_dpp v229,v229,v229 row_shr:8 row_mask:0xf bank_mask:0xf\n"
MF"v[44:47],v[196:199],a[84:87],v[44:47]\n"
MF"v[44:47],v[192:195],a[68:71],v[44:47]\n"
RD"v[216:219],v242 offset:30720\n"
"v_max_u32_dpp v229,v229,v229 row_bcast:15 row_mask:0xa bank_mask:0xf\n"
MF"v[52:55],v[192:195],a[72:75],v[52:55]\n"
MF"v[52:55],v[196:199],a[88:91],v[52:55]\n"
"v_max_u32_dpp v229,v229,v229 row_bcast:31 row_mask:0xc bank_mask:0xf\n"
MF"v[60:63],v[196:199],a[92:95],v[60:63]\n"
"v_readlane_b32 s62,v229,63\n"
MF"v[60:63],v[192:195],a[76:79],v[60:63]\n"
RD"v[220:223],v243 offset:30720\n"
"v_cmp_eq_u32 vcc,s62,v226\n"
MF"v[68:71],v[200:203],a[64:67],v[68:71]\n"
"v_writelane_b32 v225,s62,5\n"
"v_cndmask_b32 v226,v226,v227,vcc\n"
MF"v[68:71],v[204:207],a[80:83],v[68:71]\n"
"v_cndmask_b32_e64 v227,v227,0,vcc\n"
"v_mov_b32 v229,v226\n"
MF"v[76:79],v[204:207],a[84:87],v[76:79]\n"
MF"v[76:79],v[200:203],a[68:71],v[76:79]\n"
RD"v[192:195],v244 offset:0\n"
"v_max_u32_dpp v229,v229,v229 row_shr:1 row_mask:0xf bank_mask:0xf\n"
MF"v[84:87],v[200:203],a[72:75],v[84:87]\n"
MF"v[84:87],v[204:207],a[88:91],v[84:87]\n"
BS"v[40:43],v250,s[8:11],s49 offen offset:1024 sc1\n"
MF"v[92:95],v[204:207],a[92:95],v[92:95]\n"
"v_max_u32_dpp v229,v229,v229 row_shr:2 row_mask:0xf bank_mask:0xf\n"
MF"v[92:95],v[200:203],a[76:79],v[92:95]\n"
WT"lgkmcnt(1)\n"
RD"v[196:199],v245 offset:0\n"
"v_max_u32_dpp v229,v229,v229 row_shr:4 row_mask:0xf bank_mask:0xf\n"
MF"v[36:39],v[208:211],a[96:99],v[36:39]\n"
MF"v[36:39],v[212:215],a[112:115],v[36:39]\n"
"v_max_u32_dpp v229,v229,v229 row_shr:8 row_mask:0xf bank_mask:0xf\n"
MF"v[44:47],v[212:215],a[116:119],v[44:47]\n"
MF"v[44:47],v[208:211],a[100:103],v[44:47]\n"
RD"v[200:203],v244 offset:2048\n"
"v_max_u32_dpp v229,v229,v229 row_bcast:15 row_mask:0xa bank_mask:0xf\n"
MF"v[52:55],v[208:211],a[104:107],v[52:55]\n"
MF"v[52:55],v[212:215],a[120:123],v[52:55]\n"
"v_max_u32_dpp v229,v229,v229 row_bcast:31 row_mask:0xc bank_mask:0xf\n"
MF"v[60:63],v[212:215],a[124:127],v[60:63]\n"
RD"v[204:207],v245 offset:2048\n"
"v_readlane_b32 s62,v229,63\n"
MF"v[60:63],v[208:211],a[108:111],v[60:63]\n"
MF"v[68:71],v[216:219],a[96:99],v[68:71]\n"
"v_cmp_eq_u32 vcc,s62,v226\n"
"v_writelane_b32 v225,s62,6\n"
MF"v[68:71],v[220:223],a[112:115],v[68:71]\n"
"v_cndmask_b32 v226,v226,v227,vcc\n"
"v_cndmask_b32_e64 v227,v227,0,vcc\n"
MF"v[76:79],v[220:223],a[116:119],v[76:79]\n"
SA"m0,s37,16384\n"
RD"v[208:211],v244 offset:4096\n"
"v_mov_b32 v229,v226\n"
MF"v[76:79],v[216:219],a[100:103],v[76:79]\n"
SA"s48,s39,98304\n"
"v_max_u32_dpp v229,v229,v229 row_shr:1 row_mask:0xf bank_mask:0xf\n"
MF"v[84:87],v[216:219],a[104:107],v[84:87]\n"
BL"v234,s[12:15],s48 offen offset:0 lds\n"
MF"v[84:87],v[220:223],a[120:123],v[84:87]\n"
"v_max_u32_dpp v229,v229,v229 row_shr:2 row_mask:0xf bank_mask:0xf\n"
PK"v96,v96,v97\n"
MF"v[92:95],v[220:223],a[124:127],v[92:95]\n"
RD"v[212:215],v245 offset:4096\n"
"v_max_u32_dpp v229,v229,v229 row_shr:4 row_mask:0xf bank_mask:0xf\n"
MF"v[92:95],v[216:219],a[108:111],v[92:95]\n"
WT"lgkmcnt(2)\n"
"v_max_u32_dpp v229,v229,v229 row_shr:8 row_mask:0xf bank_mask:0xf\n"
PK"v97,v98,v99\n"
MF"v[36:39],v[192:195],a[128:131],v[36:39]\n"
"v_max_u32_dpp v229,v229,v229 row_bcast:15 row_mask:0xa bank_mask:0xf\n"
PK"v98,v100,v101\n"
MF"v[36:39],v[196:199],a[144:147],v[36:39]\n"
"v_max_u32_dpp v229,v229,v229 row_bcast:31 row_mask:0xc bank_mask:0xf\n"
PK"v99,v102,v103\n"
MF"v[44:47],v[196:199],a[148:151],v[44:47]\n"
RD"v[216:219],v244 offset:6144\n"
"v_readlane_b32 s62,v229,63\n"
MF"v[44:47],v[192:195],a[132:135],v[44:47]\n"
BL"v235,s[12:15],s48 offen offset:1024 lds\n"
MF"v[52:55],v[192:195],a[136:139],v[52:55]\n"
"v_cmp_eq_u32 vcc,s62,v226\n"
"v_writelane_b32 v225,s62,7\n"
MF"v[52:55],v[196:199],a[152:155],v[52:55]\n"
RD"v[220:223],v245 offset:6144\n"
"v_cndmask_b32 v226,v226,v227,vcc\n"
MF"v[60:63],v[196:199],a[156:159],v[60:63]\n"
"v_cndmask_b32_e64 v227,v227,0,vcc\n"
"v_mov_b32 v229,v228\n"
MF"v[60:63],v[192:195],a[140:143],v[60:63]\n"
PK"v104,v104,v105\n"
"v_max_u32_dpp v229,v229,v229 row_shr:1 row_mask:0xf bank_mask:0xf\n"
MF"v[68:71],v[200:203],a[128:131],v[68:71]\n"
BL"v236,s[12:15],s48 offen offset:2048 lds\n"
MF"v[68:71],v[204:207],a[144:147],v[68:71]\n"
RD"v[192:195],v244 offset:24576\n"
"v_max_u32_dpp v229,v229,v229 row_shr:2 row_mask:0xf bank_mask:0xf\n"
MF"v[76:79],v[204:207],a[148:151],v[76:79]\n"
PK"v105,v106,v107\n"
"v_max_u32_dpp v229,v229,v229 row_shr:4 row_mask:0xf bank_mask:0xf\n"
MF"v[76:79],v[200:203],a[132:135],v[76:79]\n"
PK"v106,v108,v109\n"
"v_max_u32_dpp v229,v229,v229 row_shr:8 row_mask:0xf bank_mask:0xf\n"
MF"v[84:87],v[200:203],a[136:139],v[84:87]\n"
BS"v[48:51],v250,s[8:11],s49 offen offset:2048 sc1\n"
MF"v[84:87],v[204:207],a[152:155],v[84:87]\n"
RD"v[196:199],v245 offset:24576\n"
"v_max_u32_dpp v229,v229,v229 row_bcast:15 row_mask:0xa bank_mask:0xf\n"
MF"v[92:95],v[204:207],a[156:159],v[92:95]\n"
BL"v237,s[12:15],s48 offen offset:3072 lds\n"
MF"v[92:95],v[200:203],a[140:143],v[92:95]\n"
WT"lgkmcnt(2)\n"
"v_max_u32_dpp v229,v229,v229 row_bcast:31 row_mask:0xc bank_mask:0xf\n"
PK"v107,v110,v111\n"
MF"v[36:39],v[208:211],a[160:163],v[36:39]\n"
"v_readlane_b32 s63,v229,63\n"
PK"v112,v112,v113\n"
MF"v[36:39],v[212:215],a[176:179],v[36:39]\n"
"s_cmp_gt_u32 s63,s62\n"
"s_cbranch_scc1 L1007_%=\n"
"L1008_%=:\n"
MF"v[44:47],v[212:215],a[180:183],v[44:47]\n"
SA"m0,s37,20480\n"
RD"v[200:203],v244 offset:26624\n"
"v_lshrrev_b32 v229,2,v224\n"
MF"v[44:47],v[208:211],a[164:167],v[44:47]\n"
SA"s48,s98,98304\n"
"ds_write_b32 v229,v225 offset:16384\n"
MF"v[52:55],v[208:211],a[168:171],v[52:55]\n"
BL"v234,s[12:15],s48 offen offset:0 lds\n"
MF"v[52:55],v[212:215],a[184:187],v[52:55]\n"
RD"v[204:207],v245 offset:26624\n"
PK"v113,v114,v115\n"
MF"v[60:63],v[212:215],a[188:191],v[60:63]\n"
PK"v114,v116,v117\n"
PK"v115,v118,v119\n"
MF"v[60:63],v[208:211],a[172:175],v[60:63]\n"
PK"v120,v120,v121\n"
PK"v121,v122,v123\n"
MF"v[68:71],v[216:219],a[160:163],v[68:71]\n"
RD"v[208:211],v244 offset:28672\n"
PK"v122,v124,v125\n"
MF"v[68:71],v[220:223],a[176:179],v[68:71]\n"
PK"v123,v126,v127\n"
MF"v[76:79],v[220:223],a[180:183],v[76:79]\n"
BL"v235,s[12:15],s48 offen offset:1024 lds\n"
MF"v[76:79],v[216:219],a[164:167],v[76:79]\n"
MF"v[84:87],v[216:219],a[168:171],v[84:87]\n"
RD"v[212:215],v245 offset:28672\n"
MF"v[84:87],v[220:223],a[184:187],v[84:87]\n"
MF"v[92:95],v[220:223],a[188:191],v[92:95]\n"
MF"v[92:95],v[216:219],a[172:175],v[92:95]\n"
WT"lgkmcnt(1)\n"
MF"v[36:39],v[192:195],a[192:195],v[36:39]\n"
RD"v[216:219],v244 offset:30720\n"
MF"v[36:39],v[196:199],a[208:211],v[36:39]\n"
BL"v236,s[12:15],s48 offen offset:2048 lds\n"
MF"v[44:47],v[196:199],a[212:215],v[44:47]\n"
MF"v[44:47],v[192:195],a[196:199],v[44:47]\n"
MF"v[52:55],v[192:195],a[200:203],v[52:55]\n"
RD"v[220:223],v245 offset:30720\n"
MF"v[52:55],v[196:199],a[216:219],v[52:55]\n"
MF"v[60:63],v[196:199],a[220:223],v[60:63]\n"
BL"v237,s[12:15],s48 offen offset:3072 lds\n"
MF"v[60:63],v[192:195],a[204:207],v[60:63]\n"
MF"v[68:71],v[200:203],a[192:195],v[68:71]\n"
MF"v[68:71],v[204:207],a[208:211],v[68:71]\n"
MF"v[76:79],v[204:207],a[212:215],v[76:79]\n"
MF"v[76:79],v[200:203],a[196:199],v[76:79]\n"
BS"v[56:59],v250,s[8:11],s49 offen offset:3072 sc1\n"
MF"v[84:87],v[200:203],a[200:203],v[84:87]\n"
WT"lgkmcnt(0)\n"
"v_lshrrev_b32 v251,2,v224\n"
"v_and_b32 v251,28,v251\n"
MF"v[84:87],v[204:207],a[216:219],v[84:87]\n"
"s_barrier\n"
MF"v[92:95],v[204:207],a[220:223],v[92:95]\n"
RD"v[192:195],v242 offset:8192\n"
MF"v[92:95],v[200:203],a[204:207],v[92:95]\n"
"ds_read_b32 v234,v251 offset:16384\n"
MF"v[36:39],v[208:211],a[224:227],v[36:39]\n"
MF"v[36:39],v[212:215],a[240:243],v[36:39]\n"
MF"v[44:47],v[212:215],a[244:247],v[44:47]\n"
RD"v[196:199],v243 offset:8192\n"
MF"v[44:47],v[208:211],a[228:231],v[44:47]\n"
MF"v[52:55],v[208:211],a[232:235],v[52:55]\n"
MF"v[52:55],v[212:215],a[248:251],v[52:55]\n"
MF"v[60:63],v[212:215],a[252:255],v[60:63]\n"
RD"v[200:203],v242 offset:10240\n"
MF"v[60:63],v[208:211],a[236:239],v[60:63]\n"
WT"lgkmcnt(2)\n"
"v_not_b32 v251,v234\n"
"v_bfe_u32 v252,v251,4,7\n"
MF"v[68:71],v[216:219],a[224:227],v[68:71]\n"
"v_mul_u32_u24 v252,20480,v252\n"
MF"v[68:71],v[220:223],a[240:243],v[68:71]\n"
"v_and_b32 v251,15,v251\n"
MF"v[76:79],v[220:223],a[244:247],v[76:79]\n"
RD"v[204:207],v243 offset:10240\n"
"v_lshl_add_u32 v251,v251,4,v252\n"
MF"v[76:79],v[216:219],a[228:231],v[76:79]\n"
"v_and_b32 v252,0x380,v224\n"
MF"v[84:87],v[216:219],a[232:235],v[84:87]\n"
"v_lshl_add_u32 v251,v252,1,v251\n"
MF"v[84:87],v[220:223],a[248:251],v[84:87]\n"
"v_add_u32 v252,v252,v224\n"
MF"v[92:95],v[220:223],a[252:255],v[92:95]\n"
"s_xor_b32 s92,s34,0\n"
RD"v[208:211],v242 offset:12288\n"
MF"v[92:95],v[216:219],a[236:239],v[92:95]\n"
"s_lshl_b32 s92,s92,2\n"
MF"v[128:131],v[192:195],a[0:3],0\n"
WT"lgkmcnt(2)\n"
SA"s92,s92,s30\n"
MF"v[128:131],v[196:199],a[16:19],v[128:131]\n"
"s_lshl_b32 s93,s92,11\n"
RD"v[212:215],v243 offset:12288\n"
MF"v[136:139],v[196:199],a[20:23],0\n"
"s_cmp_lt_u32 s92,10\n"
"s_cselect_b32 s93,s93,2130706432\n"
MF"v[136:139],v[192:195],a[4:7],v[136:139]\n"
SA"s92,s93,524288\n"
PK"v36,v36,v37\n"
PK"v37,v38,v39\n"
MF"v[144:147],v[192:195],a[8:11],0\n"
BL"v[100:103],v251,s[8:11],s92 offen sc1\n"
MF"v[144:147],v[196:199],a[24:27],v[144:147]\n"
"s_mul_i32 s92,s35,20480\n"
RD"v[216:219],v242 offset:14336\n"
PK"v38,v68,v69\n"
MF"v[152:155],v[196:199],a[28:31],0\n"
SA"s92,s92,s93\n"
PK"v39,v70,v71\n"
MF"v[152:155],v[192:195],a[12:15],v[152:155]\n"
SA"s92,s92,3145728\n"
MF"v[132:135],v[200:203],a[0:3],0\n"
WT"lgkmcnt(1)\n"
PK"v44,v44,v45\n"
PK"v45,v46,v47\n"
MF"v[132:135],v[204:207],a[16:19],v[132:135]\n"
"s_mov_b32 s49,16384\n"
RD"v[220:223],v243 offset:14336\n"
PK"v46,v76,v77\n"
MF"v[140:143],v[204:207],a[20:23],0\n"
BS"v[64:67],v250,s[8:11],s49 offen offset:0 sc1\n"
MF"v[140:143],v[200:203],a[4:7],v[140:143]\n"
PK"v47,v78,v79\n"
MF"v[148:151],v[200:203],a[8:11],0\n"
MF"v[148:151],v[204:207],a[24:27],v[148:151]\n"
RD"v[192:195],v242 offset:32768\n"
PK"v52,v52,v53\n"
MF"v[156:159],v[204:207],a[28:31],0\n"
PK"v53,v54,v55\n"
PK"v54,v84,v85\n"
MF"v[156:159],v[200:203],a[12:15],v[156:159]\n"
PK"v55,v86,v87\n"
MF"v[128:131],v[208:211],a[32:35],v[128:131]\n"
MF"v[128:131],v[212:215],a[48:51],v[128:131]\n"
RD"v[196:199],v243 offset:32768\n"
MF"v[136:139],v[212:215],a[52:55],v[136:139]\n"
PK"v60,v60,v61\n"
PK"v61,v62,v63\n"
MF"v[136:139],v[208:211],a[36:39],v[136:139]\n"
PK"v62,v92,v93\n"
PK"v63,v94,v95\n"
MF"v[144:147],v[208:211],a[40:43],v[144:147]\n"
SA"m0,s36,0\n"
RD"v[200:203],v242 offset:34816\n"
SA"s48,s60,s61\n"
MF"v[144:147],v[212:215],a[56:59],v[144:147]\n"
MF"v[152:155],v[212:215],a[60:63],v[152:155]\n"
BL"v238,s[20:23],s48 offen offset:0 lds\n"
MF"v[152:155],v[208:211],a[44:47],v[152:155]\n"
WT"lgkmcnt(2)\n"
MF"v[132:135],v[216:219],a[32:35],v[132:135]\n"
RD"v[204:207],v243 offset:34816\n"
MF"v[132:135],v[220:223],a[48:51],v[132:135]\n"
MF"v[140:143],v[220:223],a[52:55],v[140:143]\n"
MF"v[140:143],v[216:219],a[36:39],v[140:143]\n"
MF"v[148:151],v[216:219],a[40:43],v[148:151]\n"
RD"v[208:211],v242 offset:36864\n"
MF"v[148:151],v[220:223],a[56:59],v[148:151]\n"
MF"v[156:159],v[220:223],a[60:63],v[156:159]\n"
BL"v239,s[20:23],s48 offen offset:1024 lds\n"
MF"v[156:159],v[216:219],a[44:47],v[156:159]\n"
MF"v[128:131],v[192:195],a[64:67],v[128:131]\n"
WT"lgkmcnt(1)\n"
RD"v[212:215],v243 offset:36864\n"
MF"v[128:131],v[196:199],a[80:83],v[128:131]\n"
MF"v[136:139],v[196:199],a[84:87],v[136:139]\n"
MF"v[136:139],v[192:195],a[68:71],v[136:139]\n"
RD"v[216:219],v242 offset:38912\n"
MF"v[144:147],v[192:195],a[72:75],v[144:147]\n"
MF"v[144:147],v[196:199],a[88:91],v[144:147]\n"
MF"v[152:155],v[196:199],a[92:95],v[152:155]\n"
BL"v240,s[20:23],s48 offen offset:2048 lds\n"
MF"v[152:155],v[192:195],a[76:79],v[152:155]\n"
RD"v[220:223],v243 offset:38912\n"
MF"v[132:135],v[200:203],a[64:67],v[132:135]\n"
MF"v[132:135],v[204:207],a[80:83],v[132:135]\n"
BS"v[72:75],v250,s[8:11],s49 offen offset:1024 sc1\n"
MF"v[140:143],v[204:207],a[84:87],v[140:143]\n"
MF"v[140:143],v[200:203],a[68:71],v[140:143]\n"
RD"v[192:195],v244 offset:8192\n"
MF"v[148:151],v[200:203],a[72:75],v[148:151]\n"
MF"v[148:151],v[204:207],a[88:91],v[148:151]\n"
MF"v[156:159],v[204:207],a[92:95],v[156:159]\n"
SA"s61,s61,524288\n"
BL"v241,s[20:23],s48 offen offset:3072 lds\n"
MF"v[156:159],v[200:203],a[76:79],v[156:159]\n"
WT"lgkmcnt(1)\n"
RD"v[196:199],v245 offset:8192\n"
MF"v[128:131],v[208:211],a[96:99],v[128:131]\n"
MF"v[128:131],v[212:215],a[112:115],v[128:131]\n"
MF"v[136:139],v[212:215],a[116:119],v[136:139]\n"
MF"v[136:139],v[208:211],a[100:103],v[136:139]\n"
RD"v[200:203],v244 offset:10240\n"
MF"v[144:147],v[208:211],a[104:107],v[144:147]\n"
MF"v[144:147],v[212:215],a[120:123],v[144:147]\n"
MF"v[152:155],v[212:215],a[124:127],v[152:155]\n"
RD"v[204:207],v245 offset:10240\n"
MF"v[152:155],v[208:211],a[108:111],v[152:155]\n"
MF"v[132:135],v[216:219],a[96:99],v[132:135]\n"
MF"v[132:135],v[220:223],a[112:115],v[132:135]\n"
MF"v[140:143],v[220:223],a[116:119],v[140:143]\n"
RD"v[208:211],v244 offset:12288\n"
MF"v[140:143],v[216:219],a[100:103],v[140:143]\n"
MF"v[148:151],v[216:219],a[104:107],v[148:151]\n"
SA"m0,s38,65536\n"
SA"s48,s60,s61\n"
MF"v[148:151],v[220:223],a[120:123],v[148:151]\n"
MF"v[156:159],v[220:223],a[124:127],v[156:159]\n"
RD"v[212:215],v245 offset:12288\n"
MF"v[156:159],v[216:219],a[108:111],v[156:159]\n"
WT"lgkmcnt(2)\n"
BL"v238,s[20:23],s48 offen offset:0 lds\n"
MF"v[128:131],v[192:195],a[128:131],v[128:131]\n"
MF"v[128:131],v[196:199],a[144:147],v[128:131]\n"
MF"v[136:139],v[196:199],a[148:151],v[136:139]\n"
RD"v[216:219],v244 offset:14336\n"
MF"v[136:139],v[192:195],a[132:135],v[136:139]\n"
MF"v[144:147],v[192:195],a[136:139],v[144:147]\n"
MF"v[144:147],v[196:199],a[152:155],v[144:147]\n"
RD"v[220:223],v245 offset:14336\n"
MF"v[152:155],v[196:199],a[156:159],v[152:155]\n"
BL"v239,s[20:23],s48 offen offset:1024 lds\n"
MF"v[152:155],v[192:195],a[140:143],v[152:155]\n"
MF"v[132:135],v[200:203],a[128:131],v[132:135]\n"
BS"v[80:83],v250,s[8:11],s49 offen offset:2048 sc1\n"
MF"v[132:135],v[204:207],a[144:147],v[132:135]\n"
RD"v[192:195],v244 offset:32768\n"
MF"v[140:143],v[204:207],a[148:151],v[140:143]\n"
MF"v[140:143],v[200:203],a[132:135],v[140:143]\n"
MF"v[148:151],v[200:203],a[136:139],v[148:151]\n"
MF"v[148:151],v[204:207],a[152:155],v[148:151]\n"
RD"v[196:199],v245 offset:32768\n"
MF"v[156:159],v[204:207],a[156:159],v[156:159]\n"
BL"v240,s[20:23],s48 offen offset:2048 lds\n"
MF"v[156:159],v[200:203],a[140:143],v[156:159]\n"
WT"lgkmcnt(2)\n"
MF"v[128:131],v[208:211],a[160:163],v[128:131]\n"
MF"v[128:131],v[212:215],a[176:179],v[128:131]\n"
RD"v[200:203],v244 offset:34816\n"
MF"v[136:139],v[212:215],a[180:183],v[136:139]\n"
MF"v[136:139],v[208:211],a[164:167],v[136:139]\n"
MF"v[144:147],v[208:211],a[168:171],v[144:147]\n"
MF"v[144:147],v[212:215],a[184:187],v[144:147]\n"
RD"v[204:207],v245 offset:34816\n"
MF"v[152:155],v[212:215],a[188:191],v[152:155]\n"
SA"s61,s61,524288\n"
BL"v241,s[20:23],s48 offen offset:3072 lds\n"
MF"v[152:155],v[208:211],a[172:175],v[152:155]\n"
"s_cmp_eq_u32 s61,5242880\n"
"s_cselect_b32 s61,0,s61\n"
MF"v[132:135],v[216:219],a[160:163],v[132:135]\n"
RD"v[208:211],v244 offset:36864\n"
MF"v[132:135],v[220:223],a[176:179],v[132:135]\n"
MF"v[140:143],v[220:223],a[180:183],v[140:143]\n"
MF"v[140:143],v[216:219],a[164:167],v[140:143]\n"
MF"v[148:151],v[216:219],a[168:171],v[148:151]\n"
SA"m0,s38,114688\n"
RD"v[212:215],v245 offset:36864\n"
SA"s48,s60,s61\n"
MF"v[148:151],v[220:223],a[184:187],v[148:151]\n"
MF"v[156:159],v[220:223],a[188:191],v[156:159]\n"
BL"v238,s[20:23],s48 offen offset:0 lds\n"
MF"v[156:159],v[216:219],a[172:175],v[156:159]\n"
WT"lgkmcnt(1)\n"
MF"v[128:131],v[192:195],a[192:195],v[128:131]\n"
RD"v[216:219],v244 offset:38912\n"
MF"v[128:131],v[196:199],a[208:211],v[128:131]\n"
MF"v[136:139],v[196:199],a[212:215],v[136:139]\n"
MF"v[136:139],v[192:195],a[196:199],v[136:139]\n"
MF"v[144:147],v[192:195],a[200:203],v[144:147]\n"
RD"v[220:223],v245 offset:38912\n"
MF"v[144:147],v[196:199],a[216:219],v[144:147]\n"
MF"v[152:155],v[196:199],a[220:223],v[152:155]\n"
BL"v239,s[20:23],s48 offen offset:1024 lds\n"
MF"v[152:155],v[192:195],a[204:207],v[152:155]\n"
BS"v[88:91],v250,s[8:11],s49 offen offset:3072 sc1\n"
MF"v[132:135],v[200:203],a[192:195],v[132:135]\n"
WT"vmcnt(14)\n"
BS"v[100:103],v252,s[8:11],s92 offen sc1\n"
MF"v[132:135],v[204:207],a[208:211],v[132:135]\n"
MF"v[140:143],v[204:207],a[212:215],v[140:143]\n"
MF"v[140:143],v[200:203],a[196:199],v[140:143]\n"
MF"v[148:151],v[200:203],a[200:203],v[148:151]\n"
WT"lgkmcnt(0)\n"
MF"v[148:151],v[204:207],a[216:219],v[148:151]\n"
"s_barrier\n"
MF"v[156:159],v[204:207],a[220:223],v[156:159]\n"
RD"v[192:195],v242 offset:16384\n"
MF"v[156:159],v[200:203],a[204:207],v[156:159]\n"
BL"v240,s[20:23],s48 offen offset:2048 lds\n"
MF"v[128:131],v[208:211],a[224:227],v[128:131]\n"
MF"v[128:131],v[212:215],a[240:243],v[128:131]\n"
MF"v[136:139],v[212:215],a[244:247],v[136:139]\n"
RD"v[196:199],v243 offset:16384\n"
MF"v[136:139],v[208:211],a[228:231],v[136:139]\n"
MF"v[144:147],v[208:211],a[232:235],v[144:147]\n"
MF"v[144:147],v[212:215],a[248:251],v[144:147]\n"
MF"v[152:155],v[212:215],a[252:255],v[152:155]\n"
SA"s61,s61,524288\n"
RD"v[200:203],v242 offset:18432\n"
MF"v[152:155],v[208:211],a[236:239],v[152:155]\n"
BL"v241,s[20:23],s48 offen offset:3072 lds\n"
MF"v[132:135],v[216:219],a[224:227],v[132:135]\n"
"s_lshl_b32 s50,s34,3\n"
MF"v[132:135],v[220:223],a[240:243],v[132:135]\n"
SA"s51,s50,s30\n"
MF"v[140:143],v[220:223],a[244:247],v[140:143]\n"
"s_mul_i32 s51,s51,20480\n"
RD"v[204:207],v243 offset:18432\n"
MF"v[140:143],v[216:219],a[228:231],v[140:143]\n"
SA"s56,s51,524288\n"
MF"v[148:151],v[216:219],a[232:235],v[148:151]\n"
SA"m0,s36,16384\n"
SA"s48,s60,s61\n"
SA"s57,s56,81920\n"
MF"v[148:151],v[220:223],a[248:251],v[148:151]\n"
"s_sub_u32 s51,s30,s50\n"
MF"v[156:159],v[220:223],a[252:255],v[156:159]\n"
SA"s51,s51,120\n"
RD"v[208:211],v242 offset:20480\n"
MF"v[156:159],v[216:219],a[236:239],v[156:159]\n"
WT"lgkmcnt(2)\n"
BL"v238,s[20:23],s48 offen offset:0 lds\n"
MF"v[0:3],v[192:195],a[0:3],0\n"
"s_mul_i32 s51,s51,20480\n"
SA"s58,s51,524288\n"
"s_lshl_b32 s99,s99,12\n"
MF"v[0:3],v[196:199],a[16:19],v[0:3]\n"
SA"s59,s58,81920\n"
RD"v[212:215],v243 offset:20480\n"
MF"v[16:19],v[196:199],a[20:23],0\n"
"v_add_u32 v235,s99,v224\n"
MF"v[16:19],v[192:195],a[4:7],v[16:19]\n"
PK"v64,v128,v129\n"
PK"v65,v130,v131\n"
MF"v[32:35],v[192:195],a[8:11],0\n"
"s_mov_b32 s49,24576\n"
PK"v66,v132,v133\n"
PK"v67,v134,v135\n"
MF"v[32:35],v[196:199],a[24:27],v[32:35]\n"
RD"v[216:219],v242 offset:22528\n"
MF"v[48:51],v[196:199],a[28:31],0\n"
BS"v[96:99],v250,s[8:11],s49 offen offset:0 sc1\n"
MF"v[48:51],v[192:195],a[12:15],v[48:51]\n"
BL"v239,s[20:23],s48 offen offset:1024 lds\n"
MF"v[8:11],v[200:203],a[0:3],0\n"
WT"lgkmcnt(1)\n"
PK"v68,v136,v137\n"
PK"v69,v138,v139\n"
MF"v[8:11],v[204:207],a[16:19],v[8:11]\n"
RD"v[220:223],v243 offset:22528\n"
PK"v70,v140,v141\n"
MF"v[24:27],v[204:207],a[20:23],0\n"
PK"v71,v142,v143\n"
MF"v[24:27],v[200:203],a[4:7],v[24:27]\n"
MF"v[40:43],v[200:203],a[8:11],0\n"
MF"v[40:43],v[204:207],a[24:27],v[40:43]\n"
RD"v[192:195],v242 offset:40960\n"
PK"v72,v144,v145\n"
MF"v[56:59],v[204:207],a[28:31],0\n"
BL"v240,s[20:23],s48 offen offset:2048 lds\n"
MF"v[56:59],v[200:203],a[12:15],v[56:59]\n"
PK"v73,v146,v147\n"
PK"v74,v148,v149\n"
MF"v[0:3],v[208:211],a[32:35],v[0:3]\n"
BL"a[0:3],v235,s[8:11],s56 offen offset:0 sc1\n"
MF"v[0:3],v[212:215],a[48:51],v[0:3]\n"
RD"v[196:199],v243 offset:40960\n"
PK"v75,v150,v151\n"
MF"v[16:19],v[212:215],a[52:55],v[16:19]\n"
PK"v76,v152,v153\n"
PK"v77,v154,v155\n"
MF"v[16:19],v[208:211],a[36:39],v[16:19]\n"
PK"v78,v156,v157\n"
PK"v79,v158,v159\n"
MF"v[32:35],v[208:211],a[40:43],v[32:35]\n"
RD"v[200:203],v242 offset:43008\n"
MF"v[32:35],v[212:215],a[56:59],v[32:35]\n"
MF"v[48:51],v[212:215],a[60:63],v[48:51]\n"
SA"s61,s61,524288\n"
BL"v241,s[20:23],s48 offen offset:3072 lds\n"
MF"v[48:51],v[208:211],a[44:47],v[48:51]\n"
WT"lgkmcnt(2)\n"
BL"a[4:7],v235,s[8:11],s57 offen offset:0 sc1\n"
MF"v[8:11],v[216:219],a[32:35],v[8:11]\n"
"s_cmp_eq_u32 s61,5242880\n"
RD"v[204:207],v243 offset:43008\n"
"s_cselect_b32 s61,0,s61\n"
MF"v[8:11],v[220:223],a[48:51],v[8:11]\n"
MF"v[24:27],v[220:223],a[52:55],v[24:27]\n"
MF"v[24:27],v[216:219],a[36:39],v[24:27]\n"
MF"v[40:43],v[216:219],a[40:43],v[40:43]\n"
RD"v[208:211],v242 offset:45056\n"
MF"v[40:43],v[220:223],a[56:59],v[40:43]\n"
BL"a[8:11],v235,s[8:11],s58 offen offset:0 sc1\n"
MF"v[56:59],v[220:223],a[60:63],v[56:59]\n"
MF"v[56:59],v[216:219],a[44:47],v[56:59]\n"
MF"v[0:3],v[192:195],a[64:67],v[0:3]\n"
WT"lgkmcnt(1)\n"
RD"v[212:215],v243 offset:45056\n"
MF"v[0:3],v[196:199],a[80:83],v[0:3]\n"
MF"v[16:19],v[196:199],a[84:87],v[16:19]\n"
BL"a[12:15],v235,s[8:11],s59 offen offset:0 sc1\n"
MF"v[16:19],v[192:195],a[68:71],v[16:19]\n"
RD"v[216:219],v242 offset:47104\n"
MF"v[32:35],v[192:195],a[72:75],v[32:35]\n"
MF"v[32:35],v[196:199],a[88:91],v[32:35]\n"
BS"v[104:107],v250,s[8:11],s49 offen offset:1024 sc1\n"
MF"v[48:51],v[196:199],a[92:95],v[48:51]\n"
MF"v[48:51],v[192:195],a[76:79],v[48:51]\n"
RD"v[220:223],v243 offset:47104\n"
MF"v[8:11],v[200:203],a[64:67],v[8:11]\n"
BL"a[16:19],v235,s[8:11],s56 offen offset:1024 sc1\n"
MF"v[8:11],v[204:207],a[80:83],v[8:11]\n"
MF"v[24:27],v[204:207],a[84:87],v[24:27]\n"
MF"v[24:27],v[200:203],a[68:71],v[24:27]\n"
RD"v[192:195],v244 offset:16384\n"
MF"v[40:43],v[200:203],a[72:75],v[40:43]\n"
MF"v[40:43],v[204:207],a[88:91],v[40:43]\n"
MF"v[56:59],v[204:207],a[92:95],v[56:59]\n"
BL"a[20:23],v235,s[8:11],s57 offen offset:1024 sc1\n"
MF"v[56:59],v[200:203],a[76:79],v[56:59]\n"
WT"lgkmcnt(1)\n"
RD"v[196:199],v245 offset:16384\n"
MF"v[0:3],v[208:211],a[96:99],v[0:3]\n"
MF"v[0:3],v[212:215],a[112:115],v[0:3]\n"
MF"v[16:19],v[212:215],a[116:119],v[16:19]\n"
MF"v[16:19],v[208:211],a[100:103],v[16:19]\n"
RD"v[200:203],v244 offset:18432\n"
MF"v[32:35],v[208:211],a[104:107],v[32:35]\n"
BL"a[24:27],v235,s[8:11],s58 offen offset:1024 sc1\n"
MF"v[32:35],v[212:215],a[120:123],v[32:35]\n"
MF"v[48:51],v[212:215],a[124:127],v[48:51]\n"
RD"v[204:207],v245 offset:18432\n"
MF"v[48:51],v[208:211],a[108:111],v[48:51]\n"
MF"v[8:11],v[216:219],a[96:99],v[8:11]\n"
MF"v[8:11],v[220:223],a[112:115],v[8:11]\n"
MF"v[24:27],v[220:223],a[116:119],v[24:27]\n"
RD"v[208:211],v244 offset:20480\n"
MF"v[24:27],v[216:219],a[100:103],v[24:27]\n"
BL"a[28:31],v235,s[8:11],s59 offen offset:1024 sc1\n"
MF"v[40:43],v[216:219],a[104:107],v[40:43]\n"
MF"v[40:43],v[220:223],a[120:123],v[40:43]\n"
MF"v[56:59],v[220:223],a[124:127],v[56:59]\n"
RD"v[212:215],v245 offset:20480\n"
MF"v[56:59],v[216:219],a[108:111],v[56:59]\n"
WT"lgkmcnt(2)\n"
MF"v[0:3],v[192:195],a[128:131],v[0:3]\n"
BL"a[32:35],v235,s[8:11],s56 offen offset:2048 sc1\n"
MF"v[0:3],v[196:199],a[144:147],v[0:3]\n"
MF"v[16:19],v[196:199],a[148:151],v[16:19]\n"
RD"v[216:219],v244 offset:22528\n"
MF"v[16:19],v[192:195],a[132:135],v[16:19]\n"
MF"v[32:35],v[192:195],a[136:139],v[32:35]\n"
BS"v[112:115],v250,s[8:11],s49 offen offset:2048 sc1\n"
MF"v[32:35],v[196:199],a[152:155],v[32:35]\n"
RD"v[220:223],v245 offset:22528\n"
MF"v[48:51],v[196:199],a[156:159],v[48:51]\n"
BL"a[36:39],v235,s[8:11],s57 offen offset:2048 sc1\n"
MF"v[48:51],v[192:195],a[140:143],v[48:51]\n"
MF"v[8:11],v[200:203],a[128:131],v[8:11]\n"
MF"v[8:11],v[204:207],a[144:147],v[8:11]\n"
RD"v[192:195],v244 offset:40960\n"
MF"v[24:27],v[204:207],a[148:151],v[24:27]\n"
MF"v[24:27],v[200:203],a[132:135],v[24:27]\n"
MF"v[40:43],v[200:203],a[136:139],v[40:43]\n"
BL"a[40:43],v235,s[8:11],s58 offen offset:2048 sc1\n"
MF"v[40:43],v[204:207],a[152:155],v[40:43]\n"
RD"v[196:199],v245 offset:40960\n"
MF"v[56:59],v[204:207],a[156:159],v[56:59]\n"
MF"v[56:59],v[200:203],a[140:143],v[56:59]\n"
WT"lgkmcnt(2)\n"
MF"v[0:3],v[208:211],a[160:163],v[0:3]\n"
"s_sub_u32 s53,s37,65536\n"
MF"v[0:3],v[212:215],a[176:179],v[0:3]\n"
RD"v[200:203],v244 offset:43008\n"
MF"v[16:19],v[212:215],a[180:183],v[16:19]\n"
BL"a[44:47],v235,s[8:11],s59 offen offset:2048 sc1\n"
MF"v[16:19],v[208:211],a[164:167],v[16:19]\n"
"v_subrev_u32 v246,s53,v246\n"
"v_subrev_u32 v247,s53,v247\n"
MF"v[32:35],v[208:211],a[168:171],v[32:35]\n"
"v_subrev_u32 v248,s53,v248\n"
"v_subrev_u32 v249,s53,v249\n"
MF"v[32:35],v[212:215],a[184:187],v[32:35]\n"
RD"v[204:207],v245 offset:43008\n"
"v_subrev_u32 v236,65536,v246\n"
MF"v[48:51],v[212:215],a[188:191],v[48:51]\n"
"v_subrev_u32 v237,65536,v247\n"
"v_subrev_u32 v228,65536,v248\n"
MF"v[48:51],v[208:211],a[172:175],v[48:51]\n"
"v_subrev_u32 v229,65536,v249\n"
MF"v[8:11],v[216:219],a[160:163],v[8:11]\n"
RD"v[208:211],v244 offset:45056\n"
MF"v[8:11],v[220:223],a[176:179],v[8:11]\n"
BL"a[48:51],v235,s[8:11],s56 offen offset:3072 sc1\n"
MF"v[24:27],v[220:223],a[180:183],v[24:27]\n"
MF"v[24:27],v[216:219],a[164:167],v[24:27]\n"
MF"v[40:43],v[216:219],a[168:171],v[40:43]\n"
RD"v[212:215],v245 offset:45056\n"
MF"v[40:43],v[220:223],a[184:187],v[40:43]\n"
MF"v[56:59],v[220:223],a[188:191],v[56:59]\n"
BL"a[52:55],v235,s[8:11],s57 offen offset:3072 sc1\n"
MF"v[56:59],v[216:219],a[172:175],v[56:59]\n"
WT"lgkmcnt(1)\n"
MF"v[0:3],v[192:195],a[192:195],v[0:3]\n"
RD"v[216:219],v244 offset:47104\n"
MF"v[0:3],v[196:199],a[208:211],v[0:3]\n"
MF"v[16:19],v[196:199],a[212:215],v[16:19]\n"
MF"v[16:19],v[192:195],a[196:199],v[16:19]\n"
BS"v[120:123],v250,s[8:11],s49 offen offset:3072 sc1\n"
MF"v[32:35],v[192:195],a[200:203],v[32:35]\n"
RD"v[220:223],v245 offset:47104\n"
MF"v[32:35],v[196:199],a[216:219],v[32:35]\n"
BL"a[56:59],v235,s[8:11],s58 offen offset:3072 sc1\n"
MF"v[48:51],v[196:199],a[220:223],v[48:51]\n"
MF"v[48:51],v[192:195],a[204:207],v[48:51]\n"
MF"v[8:11],v[200:203],a[192:195],v[8:11]\n"
SA"s99,s99,4096\n"
MF"v[8:11],v[204:207],a[208:211],v[8:11]\n"
"s_cmp_eq_u32 s99,20480\n"
"s_cselect_b32 s99,0,s99\n"
MF"v[24:27],v[204:207],a[212:215],v[24:27]\n"
BL"a[60:63],v235,s[8:11],s59 offen offset:3072 sc1\n"
MF"v[24:27],v[200:203],a[196:199],v[24:27]\n"
MF"v[40:43],v[200:203],a[200:203],v[40:43]\n"
WT"lgkmcnt(0)\n"
MF"v[40:43],v[204:207],a[216:219],v[40:43]\n"
"s_barrier\n"
MF"v[56:59],v[204:207],a[220:223],v[56:59]\n"
RT"a[128:129],v236 offset:49152\n"
MF"v[56:59],v[200:203],a[204:207],v[56:59]\n"
"v_add_u32 v235,s99,v224\n"
MF"v[0:3],v[208:211],a[224:227],v[0:3]\n"
SA"m0,s38,73728\n"
RT"a[130:131],v236 offset:49664\n"
SA"s48,s60,s61\n"
MF"v[0:3],v[212:215],a[240:243],v[0:3]\n"
BL"a[64:67],v235,s[8:11],s56 offen offset:0 sc1\n"
MF"v[16:19],v[212:215],a[244:247],v[16:19]\n"
RT"a[132:133],v236 offset:57344\n"
"v_add_u32 v242,49152,v246\n"
MF"v[16:19],v[208:211],a[228:231],v[16:19]\n"
BL"v238,s[20:23],s48 offen offset:0 lds\n"
MF"v[32:35],v[208:211],a[232:235],v[32:35]\n"
RT"a[134:135],v236 offset:57856\n"
"v_add_u32 v243,49152,v247\n"
MF"v[32:35],v[212:215],a[248:251],v[32:35]\n"
"v_add_u32 v244,49152,v248\n"
"v_add_u32 v245,49152,v249\n"
MF"v[48:51],v[212:215],a[252:255],v[48:51]\n"
RT"a[136:137],v237 offset:49152\n"
MF"v[48:51],v[208:211],a[236:239],v[48:51]\n"
BL"a[68:71],v235,s[8:11],s57 offen offset:0 sc1\n"
MF"v[8:11],v[216:219],a[224:227],v[8:11]\n"
RT"a[138:139],v237 offset:49664\n"
MF"v[8:11],v[220:223],a[240:243],v[8:11]\n"
MF"v[24:27],v[220:223],a[244:247],v[24:27]\n"
RT"a[140:141],v237 offset:57344\n"
MF"v[24:27],v[216:219],a[228:231],v[24:27]\n"
BL"v239,s[20:23],s48 offen offset:1024 lds\n"
MF"v[40:43],v[216:219],a[232:235],v[40:43]\n"
RT"a[142:143],v237 offset:57856\n"
MF"v[40:43],v[220:223],a[248:251],v[40:43]\n"
BL"a[72:75],v235,s[8:11],s58 offen offset:0 sc1\n"
MF"v[56:59],v[220:223],a[252:255],v[56:59]\n"
RT"a[144:145],v228 offset:49152\n"
MF"v[56:59],v[216:219],a[236:239],v[56:59]\n"
WT"vmcnt(24) lgkmcnt(3)\n"
MF"v[80:83],a[128:131],a[0:3],0\n"
WT"vmcnt(18)\n"
RT"a[146:147],v228 offset:49664\n"
MF"v[80:83],a[132:135],a[16:19],v[80:83]\n"
WT"vmcnt(17)\n"
MF"v[88:91],a[132:135],a[20:23],0\n"
RT"a[148:149],v228 offset:57344\n"
MF"v[88:91],a[128:131],a[4:7],v[88:91]\n"
BL"v240,s[20:23],s48 offen offset:2048 lds\n"
MF"v[96:99],a[128:131],a[8:11],0\n"
WT"vmcnt(17)\n"
BL"a[76:79],v235,s[8:11],s59 offen offset:0 sc1\n"
MF"v[96:99],a[132:135],a[24:27],v[96:99]\n"
WT"vmcnt(17)\n"
RT"a[150:151],v228 offset:57856\n"
PK"v0,v0,v1\n"
MF"v[104:107],a[132:135],a[28:31],0\n"
RT"a[152:153],v229 offset:49152\n"
PK"v1,v2,v3\n"
MF"v[104:107],a[128:131],a[12:15],v[104:107]\n"
RT"a[154:155],v229 offset:49664\n"
PK"v2,v8,v9\n"
MF"v[84:87],a[136:139],a[0:3],0\n"
WT"lgkmcnt(4)\n"
PK"v3,v10,v11\n"
PK"v16,v16,v17\n"
MF"v[84:87],a[140:143],a[16:19],v[84:87]\n"
RT"a[156:157],v229 offset:57344\n"
PK"v17,v18,v19\n"
MF"v[92:95],a[140:143],a[20:23],0\n"
SA"s61,s61,524288\n"
BL"a[80:83],v235,s[8:11],s56 offen offset:1024 sc1\n"
MF"v[92:95],a[136:139],a[4:7],v[92:95]\n"
BL"v241,s[20:23],s48 offen offset:3072 lds\n"
MF"v[100:103],a[136:139],a[8:11],0\n"
RT"a[158:159],v229 offset:57856\n"
PK"v18,v24,v25\n"
MF"v[100:103],a[140:143],a[24:27],v[100:103]\n"
RT"a[160:161],v236 offset:53248\n"
PK"v19,v26,v27\n"
MF"v[108:111],a[140:143],a[28:31],0\n"
PK"v32,v32,v33\n"
PK"v33,v34,v35\n"
MF"v[108:111],a[136:139],a[12:15],v[108:111]\n"
RT"a[162:163],v236 offset:53760\n"
PK"v34,v40,v41\n"
MF"v[112:115],a[144:147],a[0:3],0\n"
WT"lgkmcnt(3)\n"
BL"a[84:87],v235,s[8:11],s57 offen offset:1024 sc1\n"
MF"v[112:115],a[148:151],a[16:19],v[112:115]\n"
RT"a[164:165],v236 offset:61440\n"
PK"v35,v42,v43\n"
MF"v[120:123],a[148:151],a[20:23],0\n"
PK"v48,v48,v49\n"
PK"v49,v50,v51\n"
MF"v[120:123],a[144:147],a[4:7],v[120:123]\n"
RT"a[166:167],v236 offset:61952\n"
PK"v50,v56,v57\n"
MF"v[128:131],a[144:147],a[8:11],0\n"
PK"v51,v58,v59\n"
MF"v[128:131],a[148:151],a[24:27],v[128:131]\n"
RT"a[168:169],v237 offset:53248\n"
MF"v[136:139],a[148:151],a[28:31],0\n"
MF"v[136:139],a[144:147],a[12:15],v[136:139]\n"
RT"a[170:171],v237 offset:53760\n"
MF"v[116:119],a[152:155],a[0:3],0\n"
WT"lgkmcnt(3)\n"
BL"a[88:91],v235,s[8:11],s58 offen offset:1024 sc1\n"
MF"v[116:119],a[156:159],a[16:19],v[116:119]\n"
"s_mov_b32 s49,262144\n"
RT"a[172:173],v237 offset:61440\n"
MF"v[124:127],a[156:159],a[20:23],0\n"
RT"a[174:175],v237 offset:61952\n"
MF"v[124:127],a[152:155],a[4:7],v[124:127]\n"
BS"v[4:7],v250,s[8:11],s49 offen offset:0 sc1\n"
MF"v[132:135],a[152:155],a[8:11],0\n"
RT"a[176:177],v228 offset:53248\n"
MF"v[132:135],a[156:159],a[24:27],v[132:135]\n"
MF"v[140:143],a[156:159],a[28:31],0\n"
RT"a[178:179],v228 offset:53760\n"
MF"v[140:143],a[152:155],a[12:15],v[140:143]\n"
BL"a[92:95],v235,s[8:11],s59 offen offset:1024 sc1\n"
MF"v[144:147],a[160:163],a[0:3],0\n"
WT"lgkmcnt(3)\n"
RT"a[180:181],v228 offset:61440\n"
MF"v[144:147],a[164:167],a[16:19],v[144:147]\n"
MF"v[152:155],a[164:167],a[20:23],0\n"
SA"m0,s38,122880\n"
RT"a[182:183],v228 offset:61952\n"
SA"s48,s60,s61\n"
MF"v[152:155],a[160:163],a[4:7],v[152:155]\n"
MF"v[160:163],a[160:163],a[8:11],0\n"
"s_mov_b32 s88,2048\n"
RT"a[184:185],v229 offset:53248\n"
MF"v[160:163],a[164:167],a[24:27],v[160:163]\n"
BL"v238,s[20:23],s48 offen offset:0 lds\n"
MF"v[168:171],a[164:167],a[28:31],0\n"
BL"a[96:99],v235,s[8:11],s56 offen offset:2048 sc1\n"
MF"v[168:171],a[160:163],a[12:15],v[168:171]\n"
RT"a[186:187],v229 offset:53760\n"
MF"v[148:151],a[168:171],a[0:3],0\n"
WT"lgkmcnt(3)\n"
RT"a[188:189],v229 offset:61440\n"
MF"v[148:151],a[172:175],a[16:19],v[148:151]\n"
MF"v[156:159],a[172:175],a[20:23],0\n"
RT"a[190:191],v229 offset:61952\n"
MF"v[156:159],a[168:171],a[4:7],v[156:159]\n"
RT"a[128:129],v236 offset:32768\n"
MF"v[164:167],a[168:171],a[8:11],0\n"
BL"v239,s[20:23],s48 offen offset:1024 lds\n"
MF"v[164:167],a[172:175],a[24:27],v[164:167]\n"
BL"a[100:103],v235,s[8:11],s57 offen offset:2048 sc1\n"
MF"v[172:175],a[172:175],a[28:31],0\n"
RT"a[130:131],v236 offset:33280\n"
MF"v[172:175],a[168:171],a[12:15],v[172:175]\n"
RT"a[132:133],v236 offset:40960\n"
MF"v[176:179],a[176:179],a[0:3],0\n"
WT"lgkmcnt(4)\n"
MF"v[176:179],a[180:183],a[16:19],v[176:179]\n"
RT"a[134:135],v236 offset:41472\n"
MF"v[184:187],a[180:183],a[20:23],0\n"
MF"v[184:187],a[176:179],a[4:7],v[184:187]\n"
RT"a[136:137],v237 offset:32768\n"
MF"v[192:195],a[176:179],a[8:11],0\n"
BL"a[104:107],v235,s[8:11],s58 offen offset:2048 sc1\n"
MF"v[192:195],a[180:183],a[24:27],v[192:195]\n"
BL"v240,s[20:23],s48 offen offset:2048 lds\n"
MF"v[200:203],a[180:183],a[28:31],0\n"
RT"a[138:139],v237 offset:33280\n"
MF"v[200:203],a[176:179],a[12:15],v[200:203]\n"
RT"a[140:141],v237 offset:40960\n"
MF"v[180:183],a[184:187],a[0:3],0\n"
WT"lgkmcnt(3)\n"
MF"v[180:183],a[188:191],a[16:19],v[180:183]\n"
RT"a[142:143],v237 offset:41472\n"
MF"v[188:191],a[188:191],a[20:23],0\n"
BL"a[108:111],v235,s[8:11],s59 offen offset:2048 sc1\n"
MF"v[188:191],a[184:187],a[4:7],v[188:191]\n"
RT"a[144:145],v228 offset:32768\n"
MF"v[196:199],a[184:187],a[8:11],0\n"
SA"s61,s61,524288\n"
BS"v[12:15],v250,s[8:11],s49 offen offset:1024 sc1\n"
MF"v[196:199],a[188:191],a[24:27],v[196:199]\n"
"s_cmp_eq_u32 s61,5242880\n"
BL"v241,s[20:23],s48 offen offset:3072 lds\n"
MF"v[204:207],a[188:191],a[28:31],0\n"
"s_cselect_b32 s61,0,s61\n"
RT"a[146:147],v228 offset:33280\n"
MF"v[204:207],a[184:187],a[12:15],v[204:207]\n"
WT"vmcnt(31)\n"
RT"a[148:149],v228 offset:40960\n"
MF"v[80:83],a[128:131],a[32:35],v[80:83]\n"
WT"vmcnt(26)\n"
MF"v[80:83],a[132:135],a[48:51],v[80:83]\n"
WT"vmcnt(25)\n"
RT"a[150:151],v228 offset:41472\n"
MF"v[88:91],a[132:135],a[52:55],v[88:91]\n"
BL"a[112:115],v235,s[8:11],s56 offen offset:3072 sc1\n"
MF"v[88:91],a[128:131],a[36:39],v[88:91]\n"
RT"a[152:153],v229 offset:32768\n"
MF"v[96:99],a[128:131],a[40:43],v[96:99]\n"
WT"vmcnt(24)\n"
RT"a[154:155],v229 offset:33280\n"
MF"v[96:99],a[132:135],a[56:59],v[96:99]\n"
WT"vmcnt(23)\n"
MF"v[104:107],a[132:135],a[60:63],v[104:107]\n"
RT"a[156:157],v229 offset:40960\n"
MF"v[104:107],a[128:131],a[44:47],v[104:107]\n"
WT"lgkmcnt(4)\n"
MF"v[84:87],a[136:139],a[32:35],v[84:87]\n"
RT"a[158:159],v229 offset:41472\n"
MF"v[84:87],a[140:143],a[48:51],v[84:87]\n"
BL"a[116:119],v235,s[8:11],s57 offen offset:3072 sc1\n"
MF"v[92:95],a[140:143],a[52:55],v[92:95]\n"
RT"a[160:161],v236 offset:36864\n"
MF"v[92:95],a[136:139],a[36:39],v[92:95]\n"
MF"v[100:103],a[136:139],a[40:43],v[100:103]\n"
RT"a[162:163],v236 offset:37376\n"
MF"v[100:103],a[140:143],a[56:59],v[100:103]\n"
MF"v[108:111],a[140:143],a[60:63],v[108:111]\n"
RT"a[164:165],v236 offset:45056\n"
MF"v[108:111],a[136:139],a[44:47],v[108:111]\n"
BL"a[120:123],v235,s[8:11],s58 offen offset:3072 sc1\n"
MF"v[112:115],a[144:147],a[32:35],v[112:115]\n"
WT"lgkmcnt(3)\n"
RT"a[166:167],v236 offset:45568\n"
MF"v[112:115],a[148:151],a[48:51],v[112:115]\n"
MF"v[120:123],a[148:151],a[52:55],v[120:123]\n"
RT"a[168:169],v237 offset:36864\n"
MF"v[120:123],a[144:147],a[36:39],v[120:123]\n"
MF"v[128:131],a[144:147],a[40:43],v[128:131]\n"
RT"a[170:171],v237 offset:37376\n"
MF"v[128:131],a[148:151],a[56:59],v[128:131]\n"
SA"s99,s99,4096\n"
RT"a[172:173],v237 offset:45056\n"
MF"v[136:139],a[148:151],a[60:63],v[136:139]\n"
"s_cmp_eq_u32 s99,20480\n"
BL"a[124:127],v235,s[8:11],s59 offen offset:3072 sc1\n"
MF"v[136:139],a[144:147],a[44:47],v[136:139]\n"
"s_cselect_b32 s99,0,s99\n"
RT"a[174:175],v237 offset:45568\n"
MF"v[116:119],a[152:155],a[32:35],v[116:119]\n"
MF"v[116:119],a[156:159],a[48:51],v[116:119]\n"
RT"a[176:177],v228 offset:36864\n"
MF"v[124:127],a[156:159],a[52:55],v[124:127]\n"
MF"v[124:127],a[152:155],a[36:39],v[124:127]\n"
RT"a[178:179],v228 offset:37376\n"
MF"v[132:135],a[152:155],a[40:43],v[132:135]\n"
"v_add_u32 v235,s99,v224\n"
MF"v[132:135],a[156:159],a[56:59],v[132:135]\n"
RT"a[180:181],v228 offset:45056\n"
MF"v[140:143],a[156:159],a[60:63],v[140:143]\n"
BL"a[0:3],v235,s[8:11],s56 offen offset:0 sc1\n"
MF"v[140:143],a[152:155],a[44:47],v[140:143]\n"
WT"lgkmcnt(3)\n"
BS"v[20:23],v250,s[8:11],s49 offen offset:2048 sc1\n"
MF"v[144:147],a[160:163],a[32:35],v[144:147]\n"
RT"a[182:183],v228 offset:45568\n"
MF"v[144:147],a[164:167],a[48:51],v[144:147]\n"
RT"a[184:185],v229 offset:36864\n"
MF"v[152:155],a[164:167],a[52:55],v[152:155]\n"
MF"v[152:155],a[160:163],a[36:39],v[152:155]\n"
RT"a[186:187],v229 offset:37376\n"
MF"v[160:163],a[160:163],a[40:43],v[160:163]\n"
BL"a[4:7],v235,s[8:11],s57 offen offset:0 sc1\n"
MF"v[160:163],a[164:167],a[56:59],v[160:163]\n"
RT"a[188:189],v229 offset:45056\n"
MF"v[168:171],a[164:167],a[60:63],v[168:171]\n"
MF"v[168:171],a[160:163],a[44:47],v[168:171]\n"
RT"a[190:191],v229 offset:45568\n"
MF"v[148:151],a[168:171],a[32:35],v[148:151]\n"
"s_lshl_b32 s50,s35,3\n"
SA"s50,s50,s34\n"
"s_lshl_b32 s50,s50,3\n"
MF"v[148:151],a[172:175],a[48:51],v[148:151]\n"
SA"s50,s50,2304\n"
SA"s51,s50,s45\n"
MF"v[156:159],a[172:175],a[52:55],v[156:159]\n"
MF"v[156:159],a[168:171],a[36:39],v[156:159]\n"
BL"a[8:11],v235,s[8:11],s58 offen offset:0 sc1\n"
MF"v[164:167],a[168:171],a[40:43],v[164:167]\n"
WT"lgkmcnt(0)\n"
MF"v[164:167],a[172:175],a[56:59],v[164:167]\n"
"s_barrier\n"
MF"v[172:175],a[172:175],a[60:63],v[172:175]\n"
RT"a[128:129],v236 offset:0\n"
MF"v[172:175],a[168:171],a[44:47],v[172:175]\n"
"buffer_store_dwordx2 v[232:233],v230,s[8:11],s51 offen sc1\n"
MF"v[176:179],a[176:179],a[32:35],v[176:179]\n"
RT"a[130:131],v236 offset:512\n"
MF"v[176:179],a[180:183],a[48:51],v[176:179]\n"
MF"v[184:187],a[180:183],a[52:55],v[184:187]\n"
SA"m0,s38,81920\n"
RT"a[132:133],v236 offset:8192\n"
SA"s48,s60,s61\n"
MF"v[184:187],a[176:179],a[36:39],v[184:187]\n"
BL"a[12:15],v235,s[8:11],s59 offen offset:0 sc1\n"
MF"v[192:195],a[176:179],a[40:43],v[192:195]\n"
RT"a[134:135],v236 offset:8704\n"
MF"v[192:195],a[180:183],a[56:59],v[192:195]\n"
BL"v238,s[20:23],s48 offen offset:0 lds\n"
MF"v[200:203],a[180:183],a[60:63],v[200:203]\n"
RT"a[136:137],v237 offset:0\n"
MF"v[200:203],a[176:179],a[44:47],v[200:203]\n"
MF"v[180:183],a[184:187],a[32:35],v[180:183]\n"
RT"a[138:139],v237 offset:512\n"
MF"v[180:183],a[188:191],a[48:51],v[180:183]\n"
BL"a[16:19],v235,s[8:11],s56 offen offset:1024 sc1\n"
MF"v[188:191],a[188:191],a[52:55],v[188:191]\n"
RT"a[140:141],v237 offset:8192\n"
MF"v[188:191],a[184:187],a[36:39],v[188:191]\n"
MF"v[196:199],a[184:187],a[40:43],v[196:199]\n"
RT"a[142:143],v237 offset:8704\n"
MF"v[196:199],a[188:191],a[56:59],v[196:199]\n"
BL"v239,s[20:23],s48 offen offset:1024 lds\n"
MF"v[204:207],a[188:191],a[60:63],v[204:207]\n"
SA"s49,s88,262144\n"
RT"a[144:145],v228 offset:0\n"
MF"v[204:207],a[184:187],a[44:47],v[204:207]\n"
"LQ_%=:\n"
WT"vmcnt(34) lgkmcnt(7)\n"
MF"v[80:83],a[128:131],a[64:67],v[80:83]\n"
WT"vmcnt(27) lgkmcnt(3)\n"
RT"a[146:147],v228 offset:512\n"
MF"v[80:83],a[132:135],a[80:83],v[80:83]\n"
WT"vmcnt(25)\n"
BS"v[28:31],v250,s[8:11],s49 offen offset:1024 sc1\n"
MF"v[88:91],a[132:135],a[84:87],v[88:91]\n"
BL"a[20:23],v235,s[8:11],s57 offen offset:1024 sc1\n"
MF"v[88:91],a[128:131],a[68:71],v[88:91]\n"
RT"a[148:149],v228 offset:8192\n"
MF"v[96:99],a[128:131],a[72:75],v[96:99]\n"
WT"vmcnt(26)\n"
RT"a[150:151],v228 offset:8704\n"
MF"v[96:99],a[132:135],a[88:91],v[96:99]\n"
WT"vmcnt(24)\n"
BL"v240,s[20:23],s48 offen offset:2048 lds\n"
MF"v[104:107],a[132:135],a[92:95],v[104:107]\n"
RT"a[152:153],v229 offset:0\n"
MF"v[104:107],a[128:131],a[76:79],v[104:107]\n"
RT"a[154:155],v229 offset:512\n"
MF"v[84:87],a[136:139],a[64:67],v[84:87]\n"
WT"lgkmcnt(4)\n"
BL"a[24:27],v235,s[8:11],s58 offen offset:1024 sc1\n"
MF"v[84:87],a[140:143],a[80:83],v[84:87]\n"
RT"a[156:157],v229 offset:8192\n"
MF"v[92:95],a[140:143],a[84:87],v[92:95]\n"
MF"v[92:95],a[136:139],a[68:71],v[92:95]\n"
RT"a[158:159],v229 offset:8704\n"
MF"v[100:103],a[136:139],a[72:75],v[100:103]\n"
SA"s61,s61,524288\n"
BL"v241,s[20:23],s48 offen offset:3072 lds\n"
MF"v[100:103],a[140:143],a[88:91],v[100:103]\n"
RT"a[160:161],v236 offset:4096\n"
MF"v[108:111],a[140:143],a[92:95],v[108:111]\n"
BL"a[28:31],v235,s[8:11],s59 offen offset:1024 sc1\n"
MF"v[108:111],a[136:139],a[76:79],v[108:111]\n"
RT"a[162:163],v236 offset:4608\n"
MF"v[112:115],a[144:147],a[64:67],v[112:115]\n"
WT"lgkmcnt(3)\n"
MF"v[112:115],a[148:151],a[80:83],v[112:115]\n"
RT"a[164:165],v236 offset:12288\n"
MF"v[120:123],a[148:151],a[84:87],v[120:123]\n"
MF"v[120:123],a[144:147],a[68:71],v[120:123]\n"
RT"a[166:167],v236 offset:12800\n"
MF"v[128:131],a[144:147],a[72:75],v[128:131]\n"
MF"v[128:131],a[148:151],a[88:91],v[128:131]\n"
RT"a[168:169],v237 offset:4096\n"
MF"v[136:139],a[148:151],a[92:95],v[136:139]\n"
BL"a[32:35],v235,s[8:11],s56 offen offset:2048 sc1\n"
MF"v[136:139],a[144:147],a[76:79],v[136:139]\n"
RT"a[170:171],v237 offset:4608\n"
MF"v[116:119],a[152:155],a[64:67],v[116:119]\n"
WT"lgkmcnt(3)\n"
RT"a[172:173],v237 offset:12288\n"
MF"v[116:119],a[156:159],a[80:83],v[116:119]\n"
MF"v[124:127],a[156:159],a[84:87],v[124:127]\n"
RT"a[174:175],v237 offset:12800\n"
MF"v[124:127],a[152:155],a[68:71],v[124:127]\n"
MF"v[132:135],a[152:155],a[72:75],v[132:135]\n"
RT"a[176:177],v228 offset:4096\n"
MF"v[132:135],a[156:159],a[88:91],v[132:135]\n"
BL"a[36:39],v235,s[8:11],s57 offen offset:2048 sc1\n"
MF"v[140:143],a[156:159],a[92:95],v[140:143]\n"
RT"a[178:179],v228 offset:4608\n"
MF"v[140:143],a[152:155],a[76:79],v[140:143]\n"
MF"v[144:147],a[160:163],a[64:67],v[144:147]\n"
WT"lgkmcnt(3)\n"
RT"a[180:181],v228 offset:12288\n"
"s_mov_b32 s49,286720\n"
MF"v[144:147],a[164:167],a[80:83],v[144:147]\n"
MF"v[152:155],a[164:167],a[84:87],v[152:155]\n"
RT"a[182:183],v228 offset:12800\n"
MF"v[152:155],a[160:163],a[68:71],v[152:155]\n"
BS"v[0:3],v250,s[8:11],s49 offen offset:0 sc1\n"
MF"v[160:163],a[160:163],a[72:75],v[160:163]\n"
BL"a[40:43],v235,s[8:11],s58 offen offset:2048 sc1\n"
MF"v[160:163],a[164:167],a[88:91],v[160:163]\n"
RT"a[184:185],v229 offset:4096\n"
MF"v[168:171],a[164:167],a[92:95],v[168:171]\n"
RT"a[186:187],v229 offset:4608\n"
MF"v[168:171],a[160:163],a[76:79],v[168:171]\n"
MF"v[148:151],a[168:171],a[64:67],v[148:151]\n"
WT"lgkmcnt(3)\n"
RT"a[188:189],v229 offset:12288\n"
MF"v[148:151],a[172:175],a[80:83],v[148:151]\n"
MF"v[156:159],a[172:175],a[84:87],v[156:159]\n"
RT"a[190:191],v229 offset:12800\n"
MF"v[156:159],a[168:171],a[68:71],v[156:159]\n"
BL"a[44:47],v235,s[8:11],s59 offen offset:2048 sc1\n"
MF"v[164:167],a[168:171],a[72:75],v[164:167]\n"
RT"a[128:129],v246 offset:0\n"
MF"v[164:167],a[172:175],a[88:91],v[164:167]\n"
RT"a[130:131],v246 offset:512\n"
MF"v[172:175],a[172:175],a[92:95],v[172:175]\n"
SA"m0,s38,131072\n"
SA"s48,s60,s61\n"
MF"v[172:175],a[168:171],a[76:79],v[172:175]\n"
RT"a[132:133],v246 offset:24576\n"
MF"v[176:179],a[176:179],a[64:67],v[176:179]\n"
WT"lgkmcnt(4)\n"
BL"v238,s[20:23],s48 offen offset:0 lds\n"
MF"v[176:179],a[180:183],a[80:83],v[176:179]\n"
RT"a[134:135],v246 offset:25088\n"
MF"v[184:187],a[180:183],a[84:87],v[184:187]\n"
BL"a[48:51],v235,s[8:11],s56 offen offset:3072 sc1\n"
MF"v[184:187],a[176:179],a[68:71],v[184:187]\n"
RT"a[136:137],v247 offset:0\n"
MF"v[192:195],a[176:179],a[72:75],v[192:195]\n"
MF"v[192:195],a[180:183],a[88:91],v[192:195]\n"
RT"a[138:139],v247 offset:512\n"
MF"v[200:203],a[180:183],a[92:95],v[200:203]\n"
MF"v[200:203],a[176:179],a[76:79],v[200:203]\n"
RT"a[140:141],v247 offset:24576\n"
MF"v[180:183],a[184:187],a[64:67],v[180:183]\n"
WT"lgkmcnt(3)\n"
BL"v239,s[20:23],s48 offen offset:1024 lds\n"
MF"v[180:183],a[188:191],a[80:83],v[180:183]\n"
BL"a[52:55],v235,s[8:11],s57 offen offset:3072 sc1\n"
MF"v[188:191],a[188:191],a[84:87],v[188:191]\n"
RT"a[142:143],v247 offset:25088\n"
MF"v[188:191],a[184:187],a[68:71],v[188:191]\n"
RT"a[144:145],v248 offset:0\n"
MF"v[196:199],a[184:187],a[72:75],v[196:199]\n"
MF"v[196:199],a[188:191],a[88:91],v[196:199]\n"
RT"a[146:147],v248 offset:512\n"
MF"v[204:207],a[188:191],a[92:95],v[204:207]\n"
MF"v[204:207],a[184:187],a[76:79],v[204:207]\n"
WT"vmcnt(35)\n"
RT"a[148:149],v248 offset:24576\n"
MF"v[80:83],a[128:131],a[96:99],v[80:83]\n"
WT"vmcnt(27)\n"
BL"a[56:59],v235,s[8:11],s58 offen offset:3072 sc1\n"
MF"v[80:83],a[132:135],a[112:115],v[80:83]\n"
WT"vmcnt(27)\n"
BL"v240,s[20:23],s48 offen offset:2048 lds\n"
MF"v[88:91],a[132:135],a[116:119],v[88:91]\n"
RT"a[150:151],v248 offset:25088\n"
MF"v[88:91],a[128:131],a[100:103],v[88:91]\n"
RT"a[152:153],v249 offset:0\n"
MF"v[96:99],a[128:131],a[104:107],v[96:99]\n"
WT"vmcnt(27)\n"
RT"a[154:155],v249 offset:512\n"
MF"v[96:99],a[132:135],a[120:123],v[96:99]\n"
WT"vmcnt(26)\n"
BS"v[16:19],v250,s[8:11],s49 offen offset:1024 sc1\n"
MF"v[104:107],a[132:135],a[124:127],v[104:107]\n"
SA"s99,s99,4096\n"
RT"a[156:157],v249 offset:24576\n"
"s_cmp_eq_u32 s99,20480\n"
MF"v[104:107],a[128:131],a[108:111],v[104:107]\n"
WT"lgkmcnt(4)\n"
BL"a[60:63],v235,s[8:11],s59 offen offset:3072 sc1\n"
MF"v[84:87],a[136:139],a[96:99],v[84:87]\n"
"s_cselect_b32 s99,0,s99\n"
RT"a[158:159],v249 offset:25088\n"
SA"s61,s61,524288\n"
MF"v[84:87],a[140:143],a[112:115],v[84:87]\n"
"s_cmp_eq_u32 s61,5242880\n"
BL"v241,s[20:23],s48 offen offset:3072 lds\n"
MF"v[92:95],a[140:143],a[116:119],v[92:95]\n"
"s_cselect_b32 s61,0,s61\n"
RT"a[160:161],v246 offset:4096\n"
MF"v[92:95],a[136:139],a[100:103],v[92:95]\n"
MF"v[100:103],a[136:139],a[104:107],v[100:103]\n"
RT"a[162:163],v246 offset:4608\n"
"v_add_u32 v235,s99,v224\n"
MF"v[100:103],a[140:143],a[120:123],v[100:103]\n"
BL"a[64:67],v235,s[8:11],s56 offen offset:0 sc1\n"
MF"v[108:111],a[140:143],a[124:127],v[108:111]\n"
RT"a[164:165],v246 offset:28672\n"
MF"v[108:111],a[136:139],a[108:111],v[108:111]\n"
MF"v[112:115],a[144:147],a[96:99],v[112:115]\n"
WT"lgkmcnt(3)\n"
RT"a[166:167],v246 offset:29184\n"
"v_add_u32 v225,s36,v224\n"
MF"v[112:115],a[148:151],a[112:115],v[112:115]\n"
"s_mul_i32 s39,s35,1048576\n"
SA"s39,s39,9175040\n"
MF"v[120:123],a[148:151],a[116:119],v[120:123]\n"
RT"a[168:169],v247 offset:4096\n"
MF"v[120:123],a[144:147],a[100:103],v[120:123]\n"
MF"v[128:131],a[144:147],a[104:107],v[128:131]\n"
RT"a[170:171],v247 offset:4608\n"
MF"v[128:131],a[148:151],a[120:123],v[128:131]\n"
BL"a[68:71],v235,s[8:11],s57 offen offset:0 sc1\n"
MF"v[136:139],a[148:151],a[124:127],v[136:139]\n"
RT"a[172:173],v247 offset:28672\n"
MF"v[136:139],a[144:147],a[108:111],v[136:139]\n"
RT"a[174:175],v247 offset:29184\n"
MF"v[116:119],a[152:155],a[96:99],v[116:119]\n"
MF"v[116:119],a[156:159],a[112:115],v[116:119]\n"
RT"a[176:177],v248 offset:4096\n"
MF"v[124:127],a[156:159],a[116:119],v[124:127]\n"
MF"v[124:127],a[152:155],a[100:103],v[124:127]\n"
RT"a[178:179],v248 offset:4608\n"
MF"v[132:135],a[152:155],a[104:107],v[132:135]\n"
BL"a[72:75],v235,s[8:11],s58 offen offset:0 sc1\n"
MF"v[132:135],a[156:159],a[120:123],v[132:135]\n"
RT"a[180:181],v248 offset:28672\n"
MF"v[140:143],a[156:159],a[124:127],v[140:143]\n"
MF"v[140:143],a[152:155],a[108:111],v[140:143]\n"
WT"lgkmcnt(3)\n"
RT"a[182:183],v248 offset:29184\n"
MF"v[144:147],a[160:163],a[96:99],v[144:147]\n"
MF"v[144:147],a[164:167],a[112:115],v[144:147]\n"
RT"a[184:185],v249 offset:4096\n"
MF"v[152:155],a[164:167],a[116:119],v[152:155]\n"
BL"a[76:79],v235,s[8:11],s59 offen offset:0 sc1\n"
MF"v[152:155],a[160:163],a[100:103],v[152:155]\n"
RT"a[186:187],v249 offset:4608\n"
MF"v[160:163],a[160:163],a[104:107],v[160:163]\n"
SA"s49,s88,286720\n"
MF"v[160:163],a[164:167],a[120:123],v[160:163]\n"
RT"a[188:189],v249 offset:28672\n"
MF"v[168:171],a[164:167],a[124:127],v[168:171]\n"
BS"v[32:35],v250,s[8:11],s49 offen offset:0 sc1\n"
MF"v[168:171],a[160:163],a[108:111],v[168:171]\n"
RT"a[190:191],v249 offset:29184\n"
MF"v[148:151],a[168:171],a[96:99],v[148:151]\n"
MF"v[148:151],a[172:175],a[112:115],v[148:151]\n"
BL"a[80:83],v235,s[8:11],s56 offen offset:1024 sc1\n"
MF"v[156:159],a[172:175],a[116:119],v[156:159]\n"
MF"v[156:159],a[168:171],a[100:103],v[156:159]\n"
MF"v[164:167],a[168:171],a[104:107],v[164:167]\n"
MF"v[164:167],a[172:175],a[120:123],v[164:167]\n"
"s_barrier\n"
MF"v[172:175],a[172:175],a[124:127],v[172:175]\n"
RT"a[128:129],v242 offset:0\n"
MF"v[172:175],a[168:171],a[108:111],v[172:175]\n"
WT"lgkmcnt(1)\n"
MF"v[176:179],a[176:179],a[96:99],v[176:179]\n"
RT"a[130:131],v242 offset:512\n"
MF"v[176:179],a[180:183],a[112:115],v[176:179]\n"
BL"a[84:87],v235,s[8:11],s57 offen offset:1024 sc1\n"
MF"v[184:187],a[180:183],a[116:119],v[184:187]\n"
RT"a[132:133],v242 offset:24576\n"
MF"v[184:187],a[176:179],a[100:103],v[184:187]\n"
MF"v[192:195],a[176:179],a[104:107],v[192:195]\n"
RT"a[134:135],v242 offset:25088\n"
MF"v[192:195],a[180:183],a[120:123],v[192:195]\n"
MF"v[200:203],a[180:183],a[124:127],v[200:203]\n"
RT"a[136:137],v243 offset:0\n"
MF"v[200:203],a[176:179],a[108:111],v[200:203]\n"
"s_mov_b32 s14,LEND_%=-LP2C_%=\n"
"s_getpc_b64 s[12:13]\n"
"LP2C_%=:\n"
"s_and_b32 s13,s13,0xffff\n"
MF"v[180:183],a[184:187],a[96:99],v[180:183]\n"
"s_lshr_b32 s90,s31,3\n"
BL"a[88:91],v235,s[8:11],s58 offen offset:1024 sc1\n"
MF"v[180:183],a[188:191],a[112:115],v[180:183]\n"
"s_lshl_b32 s90,s90,2\n"
RT"a[138:139],v243 offset:512\n"
MF"v[188:191],a[188:191],a[116:119],v[188:191]\n"
SA"s90,s90,s30\n"
RT"a[140:141],v243 offset:24576\n"
MF"v[188:191],a[184:187],a[100:103],v[188:191]\n"
"s_lshl_b32 s90,s90,7\n"
MF"v[196:199],a[184:187],a[104:107],v[196:199]\n"
RT"a[142:143],v243 offset:25088\n"
"v_lshlrev_b32 v253,10,v224\n"
MF"v[196:199],a[188:191],a[120:123],v[196:199]\n"
MF"v[204:207],a[188:191],a[124:127],v[204:207]\n"
RT"a[144:145],v244 offset:0\n"
MF"v[204:207],a[184:187],a[108:111],v[204:207]\n"
WT"vmcnt(36) lgkmcnt(4)\n"
"buffer_load_dword v253,v253,s[12:15],s90 offen\n"
MF"v[80:83],a[128:131],a[0:3],v[80:83]\n"
WT"vmcnt(30)\n"
BL"a[92:95],v235,s[8:11],s59 offen offset:1024 sc1\n"
MF"v[80:83],a[132:135],a[16:19],v[80:83]\n"
WT"vmcnt(28)\n"
RT"a[146:147],v244 offset:512\n"
MF"v[88:91],a[132:135],a[20:23],v[88:91]\n"
RT"a[148:149],v244 offset:24576\n"
MF"v[88:91],a[128:131],a[4:7],v[88:91]\n"
MF"v[96:99],a[128:131],a[8:11],v[96:99]\n"
WT"vmcnt(26)\n"
RT"a[150:151],v244 offset:25088\n"
MF"v[96:99],a[132:135],a[24:27],v[96:99]\n"
WT"vmcnt(24)\n"
RT"a[152:153],v245 offset:0\n"
MF"v[104:107],a[132:135],a[28:31],v[104:107]\n"
BL"a[96:99],v235,s[8:11],s56 offen offset:2048 sc1\n"
MF"v[104:107],a[128:131],a[12:15],v[104:107]\n"
WT"lgkmcnt(4)\n"
RT"a[154:155],v245 offset:512\n"
MF"v[84:87],a[136:139],a[0:3],v[84:87]\n"
BS"v[48:51],v250,s[8:11],s49 offen offset:1024 sc1\n"
MF"v[84:87],a[140:143],a[16:19],v[84:87]\n"
RT"a[156:157],v245 offset:24576\n"
MF"v[92:95],a[140:143],a[20:23],v[92:95]\n"
MF"v[92:95],a[136:139],a[4:7],v[92:95]\n"
RT"a[158:159],v245 offset:25088\n"
MF"v[100:103],a[136:139],a[8:11],v[100:103]\n"
BL"a[100:103],v235,s[8:11],s57 offen offset:2048 sc1\n"
MF"v[100:103],a[140:143],a[24:27],v[100:103]\n"
RT"a[160:161],v242 offset:4096\n"
MF"v[108:111],a[140:143],a[28:31],v[108:111]\n"
MF"v[108:111],a[136:139],a[12:15],v[108:111]\n"
WT"lgkmcnt(3)\n"
RT"a[162:163],v242 offset:4608\n"
MF"v[112:115],a[144:147],a[0:3],v[112:115]\n"
MF"v[112:115],a[148:151],a[16:19],v[112:115]\n"
RT"a[164:165],v242 offset:28672\n"
MF"v[120:123],a[148:151],a[20:23],v[120:123]\n"
MF"v[120:123],a[144:147],a[4:7],v[120:123]\n"
RT"a[166:167],v242 offset:29184\n"
MF"v[128:131],a[144:147],a[8:11],v[128:131]\n"
BL"a[104:107],v235,s[8:11],s58 offen offset:2048 sc1\n"
MF"v[128:131],a[148:151],a[24:27],v[128:131]\n"
RT"a[168:169],v243 offset:4096\n"
MF"v[136:139],a[148:151],a[28:31],v[136:139]\n"
MF"v[136:139],a[144:147],a[12:15],v[136:139]\n"
RT"a[170:171],v243 offset:4608\n"
MF"v[116:119],a[152:155],a[0:3],v[116:119]\n"
WT"lgkmcnt(3)\n"
RT"a[172:173],v243 offset:28672\n"
MF"v[116:119],a[156:159],a[16:19],v[116:119]\n"
MF"v[124:127],a[156:159],a[20:23],v[124:127]\n"
RT"a[174:175],v243 offset:29184\n"
MF"v[124:127],a[152:155],a[4:7],v[124:127]\n"
BL"a[108:111],v235,s[8:11],s59 offen offset:2048 sc1\n"
MF"v[132:135],a[152:155],a[8:11],v[132:135]\n"
RT"a[176:177],v244 offset:4096\n"
MF"v[132:135],a[156:159],a[24:27],v[132:135]\n"
MF"v[140:143],a[156:159],a[28:31],v[140:143]\n"
RT"a[178:179],v244 offset:4608\n"
MF"v[140:143],a[152:155],a[12:15],v[140:143]\n"
MF"v[144:147],a[160:163],a[0:3],v[144:147]\n"
WT"lgkmcnt(3)\n"
RT"a[180:181],v244 offset:28672\n"
MF"v[144:147],a[164:167],a[16:19],v[144:147]\n"
BL"a[112:115],v235,s[8:11],s56 offen offset:3072 sc1\n"
MF"v[152:155],a[164:167],a[20:23],v[152:155]\n"
RT"a[182:183],v244 offset:29184\n"
MF"v[152:155],a[160:163],a[4:7],v[152:155]\n"
MF"v[160:163],a[160:163],a[8:11],v[160:163]\n"
RT"a[184:185],v245 offset:4096\n"
MF"v[160:163],a[164:167],a[24:27],v[160:163]\n"
MF"v[168:171],a[164:167],a[28:31],v[168:171]\n"
RT"a[186:187],v245 offset:4608\n"
MF"v[168:171],a[160:163],a[12:15],v[168:171]\n"
MF"v[148:151],a[168:171],a[0:3],v[148:151]\n"
WT"lgkmcnt(3)\n"
RT"a[188:189],v245 offset:28672\n"
"s_mov_b32 s49,270336\n"
MF"v[148:151],a[172:175],a[16:19],v[148:151]\n"
BL"a[116:119],v235,s[8:11],s57 offen offset:3072 sc1\n"
MF"v[156:159],a[172:175],a[20:23],v[156:159]\n"
RT"a[190:191],v245 offset:29184\n"
MF"v[156:159],a[168:171],a[4:7],v[156:159]\n"
BS"v[36:39],v250,s[8:11],s49 offen offset:0 sc1\n"
MF"v[164:167],a[168:171],a[8:11],v[164:167]\n"
RT"a[128:129],v236 offset:16384\n"
MF"v[164:167],a[172:175],a[24:27],v[164:167]\n"
RT"a[130:131],v236 offset:16896\n"
MF"v[172:175],a[172:175],a[28:31],v[172:175]\n"
MF"v[172:175],a[168:171],a[12:15],v[172:175]\n"
RT"a[132:133],v236 offset:24576\n"
MF"v[176:179],a[176:179],a[0:3],v[176:179]\n"
WT"lgkmcnt(4)\n"
BL"a[120:123],v235,s[8:11],s58 offen offset:3072 sc1\n"
MF"v[176:179],a[180:183],a[16:19],v[176:179]\n"
RT"a[134:135],v236 offset:25088\n"
MF"v[184:187],a[180:183],a[20:23],v[184:187]\n"
MF"v[184:187],a[176:179],a[4:7],v[184:187]\n"
RT"a[136:137],v237 offset:16384\n"
MF"v[192:195],a[176:179],a[8:11],v[192:195]\n"
MF"v[192:195],a[180:183],a[24:27],v[192:195]\n"
SA"s99,s99,4096\n"
RT"a[138:139],v237 offset:16896\n"
MF"v[200:203],a[180:183],a[28:31],v[200:203]\n"
"s_cmp_eq_u32 s99,20480\n"
BL"a[124:127],v235,s[8:11],s59 offen offset:3072 sc1\n"
MF"v[200:203],a[176:179],a[12:15],v[200:203]\n"
"s_cselect_b32 s99,0,s99\n"
RT"a[140:141],v237 offset:24576\n"
MF"v[180:183],a[184:187],a[0:3],v[180:183]\n"
WT"lgkmcnt(3)\n"
"v_lshrrev_b32 v252,1,v224\n"
MF"v[180:183],a[188:191],a[16:19],v[180:183]\n"
RT"a[142:143],v237 offset:25088\n"
"v_and_b32 v252,56,v252\n"
MF"v[188:191],a[188:191],a[20:23],v[188:191]\n"
"s_lshl_b32 s50,s35,6\n"
MF"v[188:191],a[184:187],a[4:7],v[188:191]\n"
SA"s50,s50,2304\n"
RT"a[144:145],v228 offset:16384\n"
MF"v[196:199],a[184:187],a[8:11],v[196:199]\n"
"buffer_load_dwordx2 v[208:209],v252,s[8:11],s50 offen sc1\n"
MF"v[196:199],a[188:191],a[24:27],v[196:199]\n"
RT"a[146:147],v228 offset:16896\n"
"v_add_u32 v235,s99,v224\n"
MF"v[204:207],a[188:191],a[28:31],v[204:207]\n"
BL"a[192:195],v235,s[8:11],s56 offen offset:0 sc1\n"
MF"v[204:207],a[184:187],a[12:15],v[204:207]\n"
WT"vmcnt(35)\n"
RT"a[148:149],v228 offset:24576\n"
MF"v[80:83],a[128:131],a[32:35],v[80:83]\n"
WT"vmcnt(29)\n"
MF"v[80:83],a[132:135],a[48:51],v[80:83]\n"
WT"vmcnt(27)\n"
RT"a[150:151],v228 offset:25088\n"
MF"v[88:91],a[132:135],a[52:55],v[88:91]\n"
RT"a[152:153],v229 offset:16384\n"
MF"v[88:91],a[128:131],a[36:39],v[88:91]\n"
MF"v[96:99],a[128:131],a[40:43],v[96:99]\n"
WT"vmcnt(26)\n"
RT"a[154:155],v229 offset:16896\n"
MF"v[96:99],a[132:135],a[56:59],v[96:99]\n"
WT"vmcnt(23)\n"
BL"a[196:199],v235,s[8:11],s57 offen offset:0 sc1\n"
MF"v[104:107],a[132:135],a[60:63],v[104:107]\n"
RT"a[156:157],v229 offset:24576\n"
MF"v[104:107],a[128:131],a[44:47],v[104:107]\n"
WT"lgkmcnt(4)\n"
MF"v[84:87],a[136:139],a[32:35],v[84:87]\n"
RT"a[158:159],v229 offset:25088\n"
MF"v[84:87],a[140:143],a[48:51],v[84:87]\n"
MF"v[92:95],a[140:143],a[52:55],v[92:95]\n"
RT"a[160:161],v236 offset:20480\n"
MF"v[92:95],a[136:139],a[36:39],v[92:95]\n"
BL"a[200:203],v235,s[8:11],s58 offen offset:0 sc1\n"
MF"v[100:103],a[136:139],a[40:43],v[100:103]\n"
RT"a[162:163],v236 offset:20992\n"
MF"v[100:103],a[140:143],a[56:59],v[100:103]\n"
BS"v[44:47],v250,s[8:11],s49 offen offset:1024 sc1\n"
MF"v[108:111],a[140:143],a[60:63],v[108:111]\n"
RT"a[164:165],v236 offset:28672\n"
MF"v[108:111],a[136:139],a[44:47],v[108:111]\n"
MF"v[112:115],a[144:147],a[32:35],v[112:115]\n"
WT"lgkmcnt(3)\n"
RT"a[166:167],v236 offset:29184\n"
MF"v[112:115],a[148:151],a[48:51],v[112:115]\n"
MF"v[120:123],a[148:151],a[52:55],v[120:123]\n"
RT"a[168:169],v237 offset:20480\n"
MF"v[120:123],a[144:147],a[36:39],v[120:123]\n"
BL"a[204:207],v235,s[8:11],s59 offen offset:0 sc1\n"
MF"v[128:131],a[144:147],a[40:43],v[128:131]\n"
RT"a[170:171],v237 offset:20992\n"
MF"v[128:131],a[148:151],a[56:59],v[128:131]\n"
RT"a[172:173],v237 offset:28672\n"
MF"v[136:139],a[148:151],a[60:63],v[136:139]\n"
MF"v[136:139],a[144:147],a[44:47],v[136:139]\n"
RT"a[174:175],v237 offset:29184\n"
MF"v[116:119],a[152:155],a[32:35],v[116:119]\n"
MF"v[116:119],a[156:159],a[48:51],v[116:119]\n"
RT"a[176:177],v228 offset:20480\n"
MF"v[124:127],a[156:159],a[52:55],v[124:127]\n"
BL"a[208:211],v235,s[8:11],s56 offen offset:1024 sc1\n"
MF"v[124:127],a[152:155],a[36:39],v[124:127]\n"
RT"a[178:179],v228 offset:20992\n"
MF"v[132:135],a[152:155],a[40:43],v[132:135]\n"
MF"v[132:135],a[156:159],a[56:59],v[132:135]\n"
RT"a[180:181],v228 offset:28672\n"
MF"v[140:143],a[156:159],a[60:63],v[140:143]\n"
MF"v[140:143],a[152:155],a[44:47],v[140:143]\n"
WT"lgkmcnt(3)\n"
RT"a[182:183],v228 offset:29184\n"
MF"v[144:147],a[160:163],a[32:35],v[144:147]\n"
BL"a[212:215],v235,s[8:11],s57 offen offset:1024 sc1\n"
MF"v[144:147],a[164:167],a[48:51],v[144:147]\n"
RT"a[184:185],v229 offset:20480\n"
MF"v[152:155],a[164:167],a[52:55],v[152:155]\n"
MF"v[152:155],a[160:163],a[36:39],v[152:155]\n"
RT"a[186:187],v229 offset:20992\n"
MF"v[160:163],a[160:163],a[40:43],v[160:163]\n"
MF"v[160:163],a[164:167],a[56:59],v[160:163]\n"
RT"a[188:189],v229 offset:28672\n"
MF"v[168:171],a[164:167],a[60:63],v[168:171]\n"
MF"v[168:171],a[160:163],a[44:47],v[168:171]\n"
RT"a[190:191],v229 offset:29184\n"
MF"v[148:151],a[168:171],a[32:35],v[148:151]\n"
BL"a[216:219],v235,s[8:11],s58 offen offset:1024 sc1\n"
MF"v[148:151],a[172:175],a[48:51],v[148:151]\n"
WT"vmcnt(8)\n"
"v_cmp_eq_u64 vcc,s[28:29],v[208:209]\n"
"s_cmp_eq_u64 vcc,exec\n"
"s_cbranch_scc0 L1010_%=\n"
"L1011_%=:\n"
MF"v[156:159],a[172:175],a[52:55],v[156:159]\n"
MF"v[156:159],a[168:171],a[36:39],v[156:159]\n"
MF"v[164:167],a[168:171],a[40:43],v[164:167]\n"
WT"lgkmcnt(0)\n"
SA"s49,s88,270336\n"
MF"v[164:167],a[172:175],a[56:59],v[164:167]\n"
"s_barrier\n"
MF"v[172:175],a[172:175],a[60:63],v[172:175]\n"
SA"m0,s36,0\n"
RT"a[128:129],v246 offset:8192\n"
SA"s48,s39,0\n"
MF"v[172:175],a[168:171],a[44:47],v[172:175]\n"
BS"v[52:55],v250,s[8:11],s49 offen offset:0 sc1\n"
MF"v[176:179],a[176:179],a[32:35],v[176:179]\n"
BL"a[220:223],v235,s[8:11],s59 offen offset:1024 sc1\n"
MF"v[176:179],a[180:183],a[48:51],v[176:179]\n"
RT"a[130:131],v246 offset:8704\n"
MF"v[184:187],a[180:183],a[52:55],v[184:187]\n"
BL"v225,s[8:11],s48 offen offset:0 sc1 lds\n"
MF"v[184:187],a[176:179],a[36:39],v[184:187]\n"
RT"a[132:133],v246 offset:32768\n"
"v_lshlrev_b32 v254,10,v224\n"
MF"v[192:195],a[176:179],a[40:43],v[192:195]\n"
"s_lshl_b32 s94,s34,1\n"
RT"a[134:135],v246 offset:33280\n"
"v_lshrrev_b32 v255,4,v224\n"
MF"v[192:195],a[180:183],a[56:59],v[192:195]\n"
SA"s95,s94,2\n"
BL"a[224:227],v235,s[8:11],s56 offen offset:2048 sc1\n"
MF"v[200:203],a[180:183],a[60:63],v[200:203]\n"
"s_sub_u32 s94,32,s94\n"
RT"a[136:137],v247 offset:8192\n"
MF"v[200:203],a[176:179],a[44:47],v[200:203]\n"
"v_add_u32 v228,s39,v254\n"
MF"v[180:183],a[184:187],a[32:35],v[180:183]\n"
RT"a[138:139],v247 offset:8704\n"
"v_and_b32 v254,1,v255\n"
MF"v[180:183],a[188:191],a[48:51],v[180:183]\n"
BL"v225,s[8:11],s48 offen offset:1024 sc1 lds\n"
MF"v[188:191],a[188:191],a[52:55],v[188:191]\n"
RT"a[140:141],v247 offset:32768\n"
"v_mul_u32_u24 v254,491520,v254\n"
MF"v[188:191],a[184:187],a[36:39],v[188:191]\n"
"v_cmp_gt_u32 vcc,s95,v255\n"
"v_mov_b32 v229,16384\n"
MF"v[196:199],a[184:187],a[40:43],v[196:199]\n"
RT"a[142:143],v247 offset:33280\n"
"v_mov_b32 v253,516096\n"
MF"v[196:199],a[188:191],a[56:59],v[196:199]\n"
BL"a[228:231],v235,s[8:11],s57 offen offset:2048 sc1\n"
MF"v[204:207],a[188:191],a[60:63],v[204:207]\n"
RT"a[144:145],v248 offset:8192\n"
"v_cndmask_b32_e64 v254,v254,0,vcc\n"
MF"v[204:207],a[184:187],a[44:47],v[204:207]\n"
WT"lgkmcnt(3)\n"
"v_cndmask_b32 v229,v229,v253,vcc\n"
"v_cmp_eq_u32 vcc,s94,v255\n"
MF"v[80:83],a[128:131],a[64:67],v[80:83]\n"
RT"a[146:147],v248 offset:8704\n"
"v_mov_b32 v253,507904\n"
MF"v[80:83],a[132:135],a[80:83],v[80:83]\n"
BL"v225,s[8:11],s48 offen offset:2048 sc1 lds\n"
MF"v[88:91],a[132:135],a[84:87],v[88:91]\n"
RT"a[148:149],v248 offset:32768\n"
"v_cndmask_b32 v254,v254,v253,vcc\n"
MF"v[88:91],a[128:131],a[68:71],v[88:91]\n"
BL"a[232:235],v235,s[8:11],s58 offen offset:2048 sc1\n"
MF"v[96:99],a[128:131],a[72:75],v[96:99]\n"
RT"a[150:151],v248 offset:33280\n"
"v_mov_b32 v253,1862270976\n"
MF"v[96:99],a[132:135],a[88:91],v[96:99]\n"
"s_mul_i32 s95,s35,163840\n"
RT"a[152:153],v249 offset:8192\n"
"v_cndmask_b32 v229,v229,v253,vcc\n"
MF"v[104:107],a[132:135],a[92:95],v[104:107]\n"
SA"s95,s95,3768320\n"
"v_add_u32 v228,v228,v254\n"
MF"v[104:107],a[128:131],a[76:79],v[104:107]\n"
RT"a[154:155],v249 offset:8704\n"
"v_subrev_u32 v254,s94,v255\n"
MF"v[84:87],a[136:139],a[64:67],v[84:87]\n"
WT"lgkmcnt(4)\n"
BL"v225,s[8:11],s48 offen offset:3072 sc1 lds\n"
MF"v[84:87],a[140:143],a[80:83],v[84:87]\n"
RT"a[156:157],v249 offset:32768\n"
"v_lshlrev_b32 v254,15,v254\n"
MF"v[92:95],a[140:143],a[84:87],v[92:95]\n"
BL"a[236:239],v235,s[8:11],s59 offen offset:2048 sc1\n"
MF"v[92:95],a[136:139],a[68:71],v[92:95]\n"
RT"a[158:159],v249 offset:33280\n"
"v_add_u32 v254,s95,v254\n"
MF"v[100:103],a[136:139],a[72:75],v[100:103]\n"
"v_cmp_lt_u32 vcc,s94,v255\n"
MF"v[100:103],a[140:143],a[88:91],v[100:103]\n"
"s_mov_b32 s47,s36\n"
RT"a[160:161],v246 offset:12288\n"
"v_cndmask_b32 v228,v228,v254,vcc\n"
MF"v[108:111],a[140:143],a[92:95],v[108:111]\n"
SA"m0,s36,16384\n"
"v_add_u32 v229,v229,v228\n"
SA"s48,s39,516096\n"
MF"v[108:111],a[136:139],a[76:79],v[108:111]\n"
"s_cmp_lt_u32 s30,2\n"
RT"a[162:163],v246 offset:12800\n"
"s_cselect_b32 s48,2130706432,s48\n"
MF"v[112:115],a[144:147],a[64:67],v[112:115]\n"
WT"lgkmcnt(3)\n"
BS"v[60:63],v250,s[8:11],s49 offen offset:1024 sc1\n"
MF"v[112:115],a[148:151],a[80:83],v[112:115]\n"
SA"s46,s36,163840\n"
BL"v225,s[8:11],s48 offen offset:0 sc1 lds\n"
MF"v[120:123],a[148:151],a[84:87],v[120:123]\n"
SA"s40,s36,65536\n"
RT"a[164:165],v246 offset:36864\n"
"v_mov_b32 v226,v224\n"
MF"v[120:123],a[144:147],a[68:71],v[120:123]\n"
"s_mov_b32 s41,0\n"
BL"a[240:243],v235,s[8:11],s56 offen offset:3072 sc1\n"
MF"v[128:131],a[144:147],a[72:75],v[128:131]\n"
"s_mov_b32 s42,2\n"
RT"a[166:167],v246 offset:37376\n"
"v_bfe_u32 v254,v224,4,4\n"
MF"v[128:131],a[148:151],a[88:91],v[128:131]\n"
RT"a[168:169],v247 offset:12288\n"
"v_lshrrev_b32 v255,8,v224\n"
MF"v[136:139],a[148:151],a[92:95],v[136:139]\n"
"s_lshl_b32 s94,s30,4\n"
"v_lshlrev_b32 v255,2,v255\n"
"v_sub_u32 v254,v254,v255\n"
MF"v[136:139],a[144:147],a[76:79],v[136:139]\n"
RT"a[170:171],v247 offset:12800\n"
"v_add_u32 v254,s94,v254\n"
MF"v[116:119],a[152:155],a[64:67],v[116:119]\n"
WT"lgkmcnt(4)\n"
RT"a[172:173],v247 offset:36864\n"
"v_mov_b32 v255,0xff800000\n"
MF"v[116:119],a[156:159],a[80:83],v[116:119]\n"
BL"a[244:247],v235,s[8:11],s57 offen offset:3072 sc1\n"
MF"v[124:127],a[156:159],a[84:87],v[124:127]\n"
BL"v225,s[8:11],s48 offen offset:1024 sc1 lds\n"
MF"v[124:127],a[152:155],a[68:71],v[124:127]\n"
RT"a[174:175],v247 offset:37376\n"
"v_cmp_gt_i32_e64 vcc,0,v254\n"
MF"v[132:135],a[152:155],a[72:75],v[132:135]\n"
RT"a[176:177],v248 offset:12288\n"
"v_cndmask_b32_e64 v208,0,v255,vcc\n"
MF"v[132:135],a[156:159],a[88:91],v[132:135]\n"
"v_cmp_gt_i32_e64 s[52:53],1,v254\n"
"v_cmp_gt_i32_e64 s[62:63],2,v254\n"
MF"v[140:143],a[156:159],a[92:95],v[140:143]\n"
RT"a[178:179],v248 offset:12800\n"
"v_cndmask_b32_e64 v209,0,v255,s[52:53]\n"
MF"v[140:143],a[152:155],a[76:79],v[140:143]\n"
BL"a[248:251],v235,s[8:11],s58 offen offset:3072 sc1\n"
MF"v[144:147],a[160:163],a[64:67],v[144:147]\n"
WT"lgkmcnt(3)\n"
RT"a[180:181],v248 offset:36864\n"
"v_cndmask_b32_e64 v210,0,v255,s[62:63]\n"
MF"v[144:147],a[164:167],a[80:83],v[144:147]\n"
BL"v225,s[8:11],s48 offen offset:2048 sc1 lds\n"
MF"v[152:155],a[164:167],a[84:87],v[152:155]\n"
RT"a[182:183],v248 offset:37376\n"
"v_cmp_gt_i32_e64 vcc,3,v254\n"
MF"v[152:155],a[160:163],a[68:71],v[152:155]\n"
"v_cmp_gt_i32_e64 s[52:53],16,v254\n"
"v_cndmask_b32_e64 v211,0,v255,vcc\n"
MF"v[160:163],a[160:163],a[72:75],v[160:163]\n"
RT"a[184:185],v249 offset:12288\n"
"v_cndmask_b32_e64 v212,0,v255,s[52:53]\n"
MF"v[160:163],a[164:167],a[88:91],v[160:163]\n"
"v_cmp_gt_i32_e64 s[62:63],17,v254\n"
"v_cmp_gt_i32_e64 vcc,18,v254\n"
MF"v[168:171],a[164:167],a[92:95],v[168:171]\n"
RT"a[186:187],v249 offset:12800\n"
"v_cndmask_b32_e64 v213,0,v255,s[62:63]\n"
MF"v[168:171],a[160:163],a[76:79],v[168:171]\n"
BL"a[252:255],v235,s[8:11],s59 offen offset:3072 sc1\n"
MF"v[148:151],a[168:171],a[64:67],v[148:151]\n"
WT"lgkmcnt(3)\n"
RT"a[188:189],v249 offset:36864\n"
"v_cndmask_b32_e64 v214,0,v255,vcc\n"
MF"v[148:151],a[172:175],a[80:83],v[148:151]\n"
BL"v225,s[8:11],s48 offen offset:3072 sc1 lds\n"
MF"v[156:159],a[172:175],a[84:87],v[156:159]\n"
RT"a[190:191],v249 offset:37376\n"
"v_cmp_gt_i32_e64 s[52:53],19,v254\n"
MF"v[156:159],a[168:171],a[68:71],v[156:159]\n"
RT"a[128:129],v242 offset:8192\n"
"v_cndmask_b32_e64 v215,0,v255,s[52:53]\n"
MF"v[164:167],a[168:171],a[72:75],v[164:167]\n"
"v_cmp_gt_i32_e64 s[62:63],32,v254\n"
"v_cmp_gt_i32_e64 vcc,33,v254\n"
MF"v[164:167],a[172:175],a[88:91],v[164:167]\n"
RT"a[130:131],v242 offset:8704\n"
"v_cndmask_b32_e64 v216,0,v255,s[62:63]\n"
MF"v[172:175],a[172:175],a[92:95],v[172:175]\n"
SA"m0,s36,32768\n"
"v_cndmask_b32_e64 v217,0,v255,vcc\n"
"v_cmp_gt_i32_e64 s[52:53],34,v254\n"
MF"v[172:175],a[168:171],a[76:79],v[172:175]\n"
SA"s48,s39,16384\n"
RT"a[132:133],v242 offset:32768\n"
"v_cndmask_b32_e64 v218,0,v255,s[52:53]\n"
MF"v[176:179],a[176:179],a[64:67],v[176:179]\n"
WT"lgkmcnt(4)\n"
BL"v225,s[8:11],s48 offen offset:0 sc1 lds\n"
MF"v[176:179],a[180:183],a[80:83],v[176:179]\n"
"s_mov_b32 s49,278528\n"
RT"a[134:135],v242 offset:33280\n"
"v_cmp_gt_i32_e64 s[62:63],35,v254\n"
MF"v[184:187],a[180:183],a[84:87],v[184:187]\n"
BS"v[64:67],v250,s[8:11],s49 offen offset:0 sc1\n"
MF"v[184:187],a[176:179],a[68:71],v[184:187]\n"
RT"a[136:137],v243 offset:8192\n"
"v_cndmask_b32_e64 v219,0,v255,s[62:63]\n"
MF"v[192:195],a[176:179],a[72:75],v[192:195]\n"
"s_lshl_b32 s59,s34,5\n"
"v_cmp_gt_i32_e64 vcc,48,v254\n"
"v_cmp_gt_i32_e64 s[52:53],49,v254\n"
MF"v[192:195],a[180:183],a[88:91],v[192:195]\n"
"s_sub_u32 s60,480,s59\n"
RT"a[138:139],v243 offset:8704\n"
"v_cndmask_b32_e64 v220,0,v255,vcc\n"
MF"v[200:203],a[180:183],a[92:95],v[200:203]\n"
SA"s59,s59,s35\n"
"v_cndmask_b32_e64 v221,0,v255,s[52:53]\n"
"v_cmp_gt_i32_e64 s[62:63],50,v254\n"
MF"v[200:203],a[176:179],a[76:79],v[200:203]\n"
SA"s60,s60,s35\n"
RT"a[140:141],v243 offset:32768\n"
"v_cndmask_b32_e64 v222,0,v255,s[62:63]\n"
MF"v[180:183],a[184:187],a[64:67],v[180:183]\n"
WT"lgkmcnt(3)\n"
BL"v225,s[8:11],s48 offen offset:1024 sc1 lds\n"
MF"v[180:183],a[188:191],a[80:83],v[180:183]\n"
"s_lshl_b32 s59,s59,15\n"
RT"a[142:143],v243 offset:33280\n"
"v_cmp_gt_i32_e64 vcc,51,v254\n"
MF"v[188:191],a[188:191],a[84:87],v[188:191]\n"
"s_lshl_b32 s60,s60,15\n"
"v_cndmask_b32_e64 v223,0,v255,vcc\n"
SA"s50,s36,44826624\n"
MF"v[188:191],a[184:187],a[68:71],v[188:191]\n"
SA"s59,s59,s50\n"
RT"a[144:145],v244 offset:8192\n"
SA"s60,s60,s50\n"
MF"v[196:199],a[184:187],a[72:75],v[196:199]\n"
"s_lshl_b32 s61,s34,3\n"
SA"s61,s61,s32\n"
"s_lshl_b32 s61,s61,2\n"
MF"v[196:199],a[188:191],a[88:91],v[196:199]\n"
SA"s61,s61,s30\n"
RT"a[146:147],v244 offset:8704\n"
"s_lshl_b32 s61,s61,2\n"
MF"v[204:207],a[188:191],a[92:95],v[204:207]\n"
SA"s61,s61,s33\n"
"s_lshl_b32 s61,s61,6\n"
MF"v[204:207],a[184:187],a[76:79],v[204:207]\n"
SA"s61,s61,393216\n"
RT"a[148:149],v244 offset:32768\n"
"s_lshl_b32 s98,s35,3\n"
MF"v[80:83],a[128:131],a[96:99],v[80:83]\n"
SA"s98,s98,s34\n"
BL"v225,s[8:11],s48 offen offset:2048 sc1 lds\n"
MF"v[80:83],a[132:135],a[112:115],v[80:83]\n"
"s_lshl_b32 s98,s98,3\n"
RT"a[150:151],v244 offset:33280\n"
MF"v[88:91],a[132:135],a[116:119],v[88:91]\n"
SA"s98,s98,4352\n"
RT"a[152:153],v245 offset:8192\n"
SA"s98,s98,s45\n"
MF"v[88:91],a[128:131],a[100:103],v[88:91]\n"
"s_lshl_b32 s50,s34,2\n"
MF"v[96:99],a[128:131],a[104:107],v[96:99]\n"
"s_mul_i32 s51,s33,0x1011100\n"
RT"a[154:155],v245 offset:8704\n"
SA"s51,s51,0xf48c00\n"
MF"v[96:99],a[132:135],a[120:123],v[96:99]\n"
"s_lshr_b32 s51,s51,s50\n"
MF"v[104:107],a[132:135],a[124:127],v[104:107]\n"
"s_and_b32 s51,s51,15\n"
RT"a[156:157],v245 offset:32768\n"
"s_lshr_b32 s50,s31,3\n"
MF"v[104:107],a[128:131],a[108:111],v[104:107]\n"
WT"lgkmcnt(4)\n"
"s_cmp_eq_u32 s50,6\n"
"s_cselect_b32 s51,15,s51\n"
MF"v[84:87],a[136:139],a[96:99],v[84:87]\n"
"s_cmp_eq_u32 s50,26\n"
RT"a[158:159],v245 offset:33280\n"
"s_cselect_b32 s51,0,s51\n"
MF"v[84:87],a[140:143],a[112:115],v[84:87]\n"
"s_mul_i32 s58,s32,14\n"
BL"v225,s[8:11],s48 offen offset:3072 sc1 lds\n"
MF"v[92:95],a[140:143],a[116:119],v[92:95]\n"
SA"s58,s58,s51\n"
RT"a[160:161],v242 offset:12288\n"
"s_sub_u32 s58,s58,1\n"
MF"v[92:95],a[136:139],a[100:103],v[92:95]\n"
"s_cmp_lt_u32 s58,108\n"
"s_cselect_b32 s90,s51,0\n"
"s_cmp_lg_u32 s90,0\n"
MF"v[100:103],a[136:139],a[104:107],v[100:103]\n"
"s_cselect_b32 s57,1,0\n"
RT"a[162:163],v242 offset:12800\n"
"s_cmp_eq_u32 s51,15\n"
MF"v[100:103],a[140:143],a[120:123],v[100:103]\n"
"s_cselect_b32 s57,2,s57\n"
"s_cmp_eq_u32 s50,6\n"
"s_cselect_b32 s90,4,s33\n"
MF"v[108:111],a[140:143],a[124:127],v[108:111]\n"
"s_mul_i32 s50,s32,5\n"
RT"a[164:165],v242 offset:36864\n"
SA"m0,s36,49152\n"
MF"v[108:111],a[136:139],a[108:111],v[108:111]\n"
SA"s48,s39,532480\n"
SA"s90,s90,s50\n"
"s_cmp_eq_u32 s57,2\n"
MF"v[112:115],a[144:147],a[96:99],v[112:115]\n"
WT"lgkmcnt(3)\n"
RT"a[166:167],v242 offset:37376\n"
"s_cselect_b32 s58,s90,s58\n"
MF"v[112:115],a[148:151],a[112:115],v[112:115]\n"
"s_lshl_b32 s56,s34,1\n"
BL"v225,s[8:11],s48 offen offset:0 sc1 lds\n"
MF"v[120:123],a[148:151],a[116:119],v[120:123]\n"
"s_sub_u32 s56,s56,2\n"
RT"a[168:169],v243 offset:12288\n"
"s_cmp_eq_u32 s34,7\n"
MF"v[120:123],a[144:147],a[100:103],v[120:123]\n"
"s_cselect_b32 s56,2,s56\n"
"s_cmp_eq_u32 s57,2\n"
"s_cselect_b32 s56,5,s56\n"
MF"v[128:131],a[144:147],a[104:107],v[128:131]\n"
"s_cmp_lt_u32 s30,2\n"
RT"a[170:171],v243 offset:12800\n"
"s_cselect_b64 s[24:25],s[78:79],s[80:81]\n"
MF"v[128:131],a[148:151],a[120:123],v[128:131]\n"
"s_cmp_eq_u32 s57,2\n"
BS"v[68:71],v250,s[8:11],s49 offen offset:1024 sc1\n"
MF"v[136:139],a[148:151],a[124:127],v[136:139]\n"
"s_cselect_b64 s[24:25],s[82:83],s[24:25]\n"
RT"a[172:173],v243 offset:36864\n"
"s_and_b32 s50,s30,1\n"
MF"v[136:139],a[144:147],a[108:111],v[136:139]\n"
"s_mul_i32 s50,s50,10\n"
RT"a[174:175],v243 offset:37376\n"
"s_mul_i32 s51,s30,14\n"
MF"v[116:119],a[152:155],a[96:99],v[116:119]\n"
"s_min_u32 s51,s51,41\n"
BL"v225,s[8:11],s48 offen offset:1024 sc1 lds\n"
MF"v[116:119],a[156:159],a[112:115],v[116:119]\n"
"s_cmp_eq_u32 s57,2\n"
RT"a[176:177],v244 offset:12288\n"
"s_cselect_b32 s50,s51,s50\n"
MF"v[124:127],a[156:159],a[116:119],v[124:127]\n"
"s_cmp_lt_u32 s30,2\n"
"s_cselect_b32 s51,14,13\n"
"s_cmp_lt_u32 s58,108\n"
MF"v[124:127],a[152:155],a[100:103],v[124:127]\n"
"s_cselect_b32 s92,10,0\n"
RT"a[178:179],v244 offset:12800\n"
"s_cmp_eq_u32 s57,2\n"
MF"v[132:135],a[152:155],a[104:107],v[132:135]\n"
"s_cselect_b32 s92,s51,s92\n"
"s_mov_b32 s51,3456\n"
"s_cmp_eq_u32 s57,2\n"
MF"v[132:135],a[156:159],a[120:123],v[132:135]\n"
"s_cselect_b32 s51,1280,s51\n"
RT"a[180:181],v244 offset:36864\n"
"s_lshl_b32 s62,s51,5\n"
MF"v[140:143],a[156:159],a[124:127],v[140:143]\n"
"s_mul_i32 s26,s62,s92\n"
"s_mul_i32 s90,s62,s50\n"
"s_lshl_b32 s91,s58,5\n"
MF"v[140:143],a[152:155],a[108:111],v[140:143]\n"
WT"lgkmcnt(3)\n"
RT"a[182:183],v244 offset:37376\n"
SA"s90,s90,s91\n"
MF"v[144:147],a[160:163],a[96:99],v[144:147]\n"
SA"s24,s24,s90\n"
BL"v225,s[8:11],s48 offen offset:2048 sc1 lds\n"
MF"v[144:147],a[164:167],a[112:115],v[144:147]\n"
"s_addc_u32 s25,s25,0\n"
RT"a[184:185],v245 offset:12288\n"
"s_and_b32 s25,s25,0xffff\n"
MF"v[152:155],a[164:167],a[116:119],v[152:155]\n"
"s_sub_u32 s90,s51,32\n"
"v_lshrrev_b32 v235,5,v224\n"
"v_mul_u32_u24 v235,s51,v235\n"
MF"v[152:155],a[160:163],a[100:103],v[152:155]\n"
"s_sub_u32 s26,s26,s90\n"
RT"a[186:187],v245 offset:12800\n"
"v_and_b32 v239,16,v224\n"
MF"v[160:163],a[160:163],a[104:107],v[160:163]\n"
"s_cselect_b32 s26,0,s26\n"
"v_add_u32 v235,v235,v239\n"
"s_mov_b32 s27,0x20000\n"
MF"v[160:163],a[164:167],a[120:123],v[160:163]\n"
"s_mul_i32 s63,s35,20480\n"
RT"a[188:189],v245 offset:36864\n"
SA"s63,s63,3145728\n"
MF"v[168:171],a[164:167],a[124:127],v[168:171]\n"
"s_cmp_eq_u32 s57,2\n"
"s_cselect_b32 s63,16384,s63\n"
MF"v[168:171],a[160:163],a[108:111],v[168:171]\n"
"s_lshl_b32 s50,s50,10\n"
RT"a[190:191],v245 offset:37376\n"
SA"s63,s63,s50\n"
MF"v[148:151],a[168:171],a[96:99],v[148:151]\n"
"s_mov_b32 s50,1065353216\n"
BL"v225,s[8:11],s48 offen offset:3072 sc1 lds\n"
MF"v[148:151],a[172:175],a[112:115],v[148:151]\n"
"s_cmp_lg_u32 s92,0\n"
"s_cselect_b32 s50,s45,s50\n"
"s_mov_b32 s93,16384\n"
MF"v[156:159],a[172:175],a[116:119],v[156:159]\n"
"s_cmp_eq_u32 s57,2\n"
"s_cselect_b32 s93,71680,s93\n"
MF"v[156:159],a[168:171],a[100:103],v[156:159]\n"
"s_lshl_b32 s51,s58,9\n"
MF"v[164:167],a[168:171],a[104:107],v[164:167]\n"
SA"s93,s93,s51\n"
SA"s93,s93,s50\n"
MF"v[164:167],a[172:175],a[120:123],v[164:167]\n"
"s_mov_b32 s94,6912\n"
"s_barrier\n"
MF"v[172:175],a[172:175],a[124:127],v[172:175]\n"
"s_cmp_eq_u32 s57,2\n"
RT"a[128:129],v246 offset:16384\n"
"s_cselect_b32 s94,6400,s94\n"
MF"v[172:175],a[168:171],a[108:111],v[172:175]\n"
WT"lgkmcnt(1)\n"
"s_lshl_b32 s51,s58,3\n"
SA"s94,s94,s51\n"
MF"v[176:179],a[176:179],a[96:99],v[176:179]\n"
SA"s94,s94,s50\n"
RT"a[130:131],v246 offset:16896\n"
"s_mov_b32 s52,0\n"
MF"v[176:179],a[180:183],a[112:115],v[176:179]\n"
"s_lshl_b32 s50,s35,6\n"
RT"a[132:133],v246 offset:40960\n"
MF"v[184:187],a[180:183],a[116:119],v[184:187]\n"
SA"s50,s50,2304\n"
MF"v[184:187],a[176:179],a[100:103],v[184:187]\n"
"s_cmp_eq_u32 s57,2\n"
RT"a[134:135],v246 offset:41472\n"
"s_cselect_b32 s50,6912,s50\n"
MF"v[192:195],a[176:179],a[104:107],v[192:195]\n"
"s_mov_b32 s51,48\n"
RT"a[136:137],v242 offset:16384\n"
SA"s49,s88,278528\n"
MF"v[192:195],a[180:183],a[120:123],v[192:195]\n"
"s_cmp_eq_u32 s57,2\n"
"s_cselect_b32 s51,848,s51\n"
"v_min_u32 v238,s51,v224\n"
MF"v[200:203],a[180:183],a[124:127],v[200:203]\n"
RT"a[138:139],v242 offset:16896\n"
"v_add_u32 v238,s50,v238\n"
MF"v[200:203],a[176:179],a[108:111],v[200:203]\n"
"s_mov_b32 s16,0\n"
BS"v[72:75],v250,s[8:11],s49 offen offset:0 sc1\n"
MF"v[180:183],a[184:187],a[96:99],v[180:183]\n"
"s_mov_b32 s20,8\n"
RT"a[140:141],v242 offset:40960\n"
"v_and_b32 v239,0x80,v224\n"
MF"v[180:183],a[188:191],a[112:115],v[180:183]\n"
"s_mov_b32 s18,2130706432\n"
RT"a[142:143],v242 offset:41472\n"
"v_lshl_add_u32 v236,v239,23,v224\n"
MF"v[188:191],a[188:191],a[116:119],v[188:191]\n"
"s_lshl_b32 s21,s32,4\n"
"s_lshr_b32 s22,s30,1\n"
MF"v[188:191],a[184:187],a[100:103],v[188:191]\n"
SA"s21,s21,s22\n"
RT"a[144:145],v247 offset:16384\n"
MF"v[196:199],a[184:187],a[104:107],v[196:199]\n"
"s_mul_i32 s21,s21,40960\n"
"s_lshl_b32 s22,s33,1\n"
MF"v[196:199],a[188:191],a[120:123],v[196:199]\n"
"s_and_b32 s23,s30,1\n"
RT"a[146:147],v247 offset:16896\n"
MF"v[204:207],a[188:191],a[124:127],v[204:207]\n"
SA"s22,s22,s23\n"
RT"a[148:149],v247 offset:40960\n"
"s_mul_i32 s22,s22,160\n"
MF"v[204:207],a[184:187],a[108:111],v[204:207]\n"
WT"vmcnt(37) lgkmcnt(4)\n"
SA"s17,s21,s22\n"
MF"v[80:83],a[128:131],a[192:195],v[80:83]\n"
WT"vmcnt(32)\n"
RT"a[150:151],v247 offset:41472\n"
"v_lshrrev_b32 v240,4,v224\n"
MF"v[80:83],a[132:135],a[208:211],v[80:83]\n"
WT"vmcnt(31)\n"
RT"a[152:153],v243 offset:16384\n"
"v_add_u32 v240,0,v240\n"
MF"v[88:91],a[132:135],a[212:215],v[88:91]\n"
"v_mul_u32_u24 v240,205,v240\n"
"v_lshrrev_b32 v240,11,v240\n"
MF"v[88:91],a[128:131],a[196:199],v[88:91]\n"
RT"a[154:155],v243 offset:16896\n"
"v_mul_u32_u24 v240,1120,v240\n"
MF"v[96:99],a[128:131],a[200:203],v[96:99]\n"
WT"vmcnt(30)\n"
RT"a[156:157],v243 offset:40960\n"
"v_add_u32 v240,v240,v224\n"
MF"v[96:99],a[132:135],a[216:219],v[96:99]\n"
WT"vmcnt(28)\n"
"v_lshrrev_b32 v241,4,v224\n"
"v_add_u32 v241,64,v241\n"
MF"v[104:107],a[132:135],a[220:223],v[104:107]\n"
RT"a[158:159],v243 offset:41472\n"
"v_mul_u32_u24 v241,205,v241\n"
MF"v[104:107],a[128:131],a[204:207],v[104:107]\n"
WT"vmcnt(26)\n"
"v_lshrrev_b32 v241,11,v241\n"
"v_mul_u32_u24 v241,1120,v241\n"
MF"v[80:83],a[136:139],a[224:227],v[80:83]\n"
WT"vmcnt(17) lgkmcnt(3)\n"
RT"a[160:161],v248 offset:16384\n"
"v_add_u32 v241,v241,v224\n"
MF"v[80:83],a[140:143],a[240:243],v[80:83]\n"
WT"vmcnt(16)\n"
"v_lshrrev_b32 v251,4,v224\n"
"v_add_u32 v251,128,v251\n"
MF"v[88:91],a[140:143],a[244:247],v[88:91]\n"
RT"a[162:163],v248 offset:16896\n"
"v_mul_u32_u24 v251,205,v251\n"
MF"v[88:91],a[136:139],a[228:231],v[88:91]\n"
RT"a[164:165],v248 offset:40960\n"
"v_lshrrev_b32 v251,11,v251\n"
MF"v[96:99],a[136:139],a[232:235],v[96:99]\n"
WT"vmcnt(14)\n"
"v_mul_u32_u24 v251,1120,v251\n"
"v_add_u32 v251,v251,v224\n"
MF"v[96:99],a[140:143],a[248:251],v[96:99]\n"
WT"vmcnt(12)\n"
RT"a[166:167],v248 offset:41472\n"
"v_and_b32 v237,512,v224\n"
MF"v[104:107],a[140:143],a[252:255],v[104:107]\n"
RT"a[168:169],v244 offset:16384\n"
"v_lshl_add_u32 v251,v237,20,v251\n"
MF"v[104:107],a[136:139],a[236:239],v[104:107]\n"
"v_lshrrev_b32 v252,6,v224\n"
"v_mul_u32_u24 v252,160,v252\n"
MF"v[84:87],a[144:147],a[192:195],v[84:87]\n"
RT"a[170:171],v244 offset:16896\n"
"v_bfe_u32 v237,v224,4,2\n"
MF"v[84:87],a[148:151],a[208:211],v[84:87]\n"
"s_cmp_eq_u32 s34,7\n"
RT"a[172:173],v244 offset:40960\n"
"v_lshl_add_u32 v252,v237,3,v252\n"
MF"v[92:95],a[148:151],a[212:215],v[92:95]\n"
"s_cselect_b32 s57,3,s57\n"
"v_lshrrev_b32 v254,4,v224\n"
"v_min_u32 v254,49,v254\n"
MF"v[92:95],a[144:147],a[196:199],v[92:95]\n"
RT"a[174:175],v244 offset:41472\n"
"v_mul_u32_u24 v253,52,v254\n"
MF"v[100:103],a[144:147],a[200:203],v[100:103]\n"
"v_lshrrev_b32 v253,8,v253\n"
MF"v[100:103],a[148:151],a[216:219],v[100:103]\n"
RT"a[176:177],v249 offset:16384\n"
"v_mul_u32_u24 v253,2901,v253\n"
MF"v[108:111],a[148:151],a[220:223],v[108:111]\n"
"v_mul_u32_u24 v254,111,v254\n"
"v_add_u32 v254,v254,v253\n"
MF"v[108:111],a[144:147],a[204:207],v[108:111]\n"
WT"lgkmcnt(3)\n"
RT"a[178:179],v249 offset:16896\n"
"v_and_b32 v254,-4,v254\n"
MF"v[84:87],a[152:155],a[224:227],v[84:87]\n"
RT"a[180:181],v249 offset:40960\n"
"v_lshrrev_b32 v253,5,v224\n"
MF"v[84:87],a[156:159],a[240:243],v[84:87]\n"
BS"v[76:79],v250,s[8:11],s49 offen offset:1024 sc1\n"
MF"v[92:95],a[156:159],a[244:247],v[92:95]\n"
RT"a[182:183],v249 offset:41472\n"
"v_min_u32 v253,13,v253\n"
MF"v[92:95],a[152:155],a[228:231],v[92:95]\n"
RT"a[184:185],v245 offset:16384\n"
"v_mul_u32_u24 v253,1280,v253\n"
MF"v[100:103],a[152:155],a[232:235],v[100:103]\n"
"v_bfe_u32 v237,v224,4,1\n"
"v_mul_u32_u24 v237,156,v237\n"
MF"v[100:103],a[156:159],a[248:251],v[100:103]\n"
"s_cmp_lt_u32 s30,2\n"
RT"a[186:187],v245 offset:16896\n"
"v_add_u32 v253,v253,v237\n"
MF"v[108:111],a[156:159],a[252:255],v[108:111]\n"
"s_cselect_b64 s[12:13],s[78:79],s[80:81]\n"
RT"a[188:189],v245 offset:40960\n"
"s_and_b32 s13,s13,0xffff\n"
MF"v[108:111],a[152:155],a[236:239],v[108:111]\n"
"s_mov_b32 s14,2211840\n"
"s_mov_b32 s15,0x20000\n"
MF"v[112:115],a[160:163],a[192:195],v[112:115]\n"
"s_lshr_b32 s90,s31,3\n"
RT"a[190:191],v245 offset:41472\n"
MF"v[112:115],a[164:167],a[208:211],v[112:115]\n"
"s_lshl_b32 s91,s90,1\n"
MF"v[120:123],a[164:167],a[212:215],v[120:123]\n"
"s_and_b32 s50,s30,1\n"
RT"a[128:129],v246 offset:20480\n"
PK"v80,v80,v81\n"
MF"v[120:123],a[160:163],a[196:199],v[120:123]\n"
SA"s91,s91,s50\n"
PK"v81,v82,v83\n"
PK"v82,v84,v85\n"
MF"v[128:131],a[160:163],a[200:203],v[128:131]\n"
"s_mul_i32 s91,s91,34560\n"
RT"a[130:131],v246 offset:20992\n"
PK"v83,v86,v87\n"
MF"v[128:131],a[164:167],a[216:219],v[128:131]\n"
"s_mul_i32 s50,s32,448\n"
RT"a[132:133],v246 offset:45056\n"
"v_accvgpr_write_b32 a0,v80\n"
MF"v[136:139],a[164:167],a[220:223],v[136:139]\n"
SA"s91,s91,s50\n"
"v_accvgpr_write_b32 a1,v81\n"
"v_accvgpr_write_b32 a2,v82\n"
MF"v[136:139],a[160:163],a[204:207],v[136:139]\n"
"buffer_load_dword v255,v254,s[12:15],s91 offen\n"
MF"v[112:115],a[168:171],a[224:227],v[112:115]\n"
WT"lgkmcnt(3)\n"
RT"a[134:135],v246 offset:45568\n"
"v_accvgpr_write_b32 a3,v83\n"
MF"v[112:115],a[172:175],a[240:243],v[112:115]\n"
"s_mov_b32 s12,s82\n"
RT"a[136:137],v242 offset:20480\n"
PK"v88,v88,v89\n"
MF"v[120:123],a[172:175],a[244:247],v[120:123]\n"
"s_and_b32 s13,s83,0xffff\n"
RT"a[138:139],v242 offset:20992\n"
PK"v89,v90,v91\n"
MF"v[120:123],a[168:171],a[228:231],v[120:123]\n"
"s_lshl_b32 s90,s90,2\n"
RT"a[140:141],v242 offset:45056\n"
PK"v90,v92,v93\n"
MF"v[128:131],a[168:171],a[232:235],v[128:131]\n"
SA"s90,s90,s30\n"
PK"v91,v94,v95\n"
"v_accvgpr_write_b32 a16,v88\n"
MF"v[128:131],a[172:175],a[248:251],v[128:131]\n"
"s_mul_i32 s90,s90,17920\n"
RT"a[142:143],v242 offset:45568\n"
"v_accvgpr_write_b32 a17,v89\n"
MF"v[136:139],a[172:175],a[252:255],v[136:139]\n"
"s_mul_i32 s50,s32,160\n"
"v_accvgpr_write_b32 a18,v90\n"
"v_accvgpr_write_b32 a19,v91\n"
MF"v[136:139],a[168:171],a[236:239],v[136:139]\n"
SA"s90,s90,s50\n"
RT"a[144:145],v247 offset:20480\n"
PK"v96,v96,v97\n"
MF"v[116:119],a[176:179],a[192:195],v[116:119]\n"
"buffer_load_dword v255,v253,s[12:15],s90 offen\n"
MF"v[116:119],a[180:183],a[208:211],v[116:119]\n"
RT"a[146:147],v247 offset:20992\n"
"v_min_u32 v253,624,v224\n"
MF"v[124:127],a[180:183],a[212:215],v[124:127]\n"
RT"a[148:149],v247 offset:45056\n"
"v_lshlrev_b32 v253,3,v253\n"
MF"v[124:127],a[176:179],a[196:199],v[124:127]\n"
PK"v97,v98,v99\n"
PK"v98,v100,v101\n"
MF"v[132:135],a[176:179],a[200:203],v[132:135]\n"
RT"a[150:151],v247 offset:45568\n"
PK"v99,v102,v103\n"
MF"v[132:135],a[180:183],a[216:219],v[132:135]\n"
RT"a[152:153],v243 offset:20480\n"
"v_accvgpr_write_b32 a32,v96\n"
MF"v[140:143],a[180:183],a[220:223],v[140:143]\n"
"v_accvgpr_write_b32 a33,v97\n"
"v_accvgpr_write_b32 a34,v98\n"
MF"v[140:143],a[176:179],a[204:207],v[140:143]\n"
RT"a[154:155],v243 offset:20992\n"
"v_accvgpr_write_b32 a35,v99\n"
MF"v[116:119],a[184:187],a[224:227],v[116:119]\n"
RT"a[156:157],v243 offset:45056\n"
PK"v104,v104,v105\n"
MF"v[116:119],a[188:191],a[240:243],v[116:119]\n"
PK"v105,v106,v107\n"
PK"v106,v108,v109\n"
MF"v[124:127],a[188:191],a[244:247],v[124:127]\n"
RT"a[158:159],v243 offset:45568\n"
PK"v107,v110,v111\n"
MF"v[124:127],a[184:187],a[228:231],v[124:127]\n"
"v_accvgpr_write_b32 a48,v104\n"
"v_accvgpr_write_b32 a49,v105\n"
MF"v[132:135],a[184:187],a[232:235],v[132:135]\n"
RT"a[160:161],v248 offset:20480\n"
"v_accvgpr_write_b32 a50,v106\n"
MF"v[132:135],a[188:191],a[248:251],v[132:135]\n"
"v_accvgpr_write_b32 a51,v107\n"
MF"v[140:143],a[188:191],a[252:255],v[140:143]\n"
RT"a[162:163],v248 offset:20992\n"
MF"v[140:143],a[184:187],a[236:239],v[140:143]\n"
WT"lgkmcnt(3)\n"
RT"a[164:165],v248 offset:45056\n"
MF"v[144:147],a[128:131],a[192:195],v[144:147]\n"
MF"v[144:147],a[132:135],a[208:211],v[144:147]\n"
RT"a[166:167],v248 offset:45568\n"
MF"v[152:155],a[132:135],a[212:215],v[152:155]\n"
RT"a[168:169],v244 offset:20480\n"
PK"v112,v112,v113\n"
MF"v[152:155],a[128:131],a[196:199],v[152:155]\n"
PK"v113,v114,v115\n"
PK"v114,v116,v117\n"
MF"v[160:163],a[128:131],a[200:203],v[160:163]\n"
RT"a[170:171],v244 offset:20992\n"
PK"v115,v118,v119\n"
MF"v[160:163],a[132:135],a[216:219],v[160:163]\n"
RT"a[172:173],v244 offset:45056\n"
"v_accvgpr_write_b32 a4,v112\n"
MF"v[168:171],a[132:135],a[220:223],v[168:171]\n"
"v_accvgpr_write_b32 a5,v113\n"
"v_accvgpr_write_b32 a6,v114\n"
MF"v[168:171],a[128:131],a[204:207],v[168:171]\n"
RT"a[174:175],v244 offset:45568\n"
"v_accvgpr_write_b32 a7,v115\n"
MF"v[144:147],a[136:139],a[224:227],v[144:147]\n"
PK"v120,v120,v121\n"
PK"v121,v122,v123\n"
MF"v[144:147],a[140:143],a[240:243],v[144:147]\n"
RT"a[176:177],v249 offset:20480\n"
PK"v122,v124,v125\n"
MF"v[152:155],a[140:143],a[244:247],v[152:155]\n"
PK"v123,v126,v127\n"
"v_accvgpr_write_b32 a20,v120\n"
MF"v[152:155],a[136:139],a[228:231],v[152:155]\n"
RT"a[178:179],v249 offset:20992\n"
"v_accvgpr_write_b32 a21,v121\n"
MF"v[160:163],a[136:139],a[232:235],v[160:163]\n"
RT"a[180:181],v249 offset:45056\n"
"v_accvgpr_write_b32 a22,v122\n"
MF"v[160:163],a[140:143],a[248:251],v[160:163]\n"
"v_accvgpr_write_b32 a23,v123\n"
MF"v[168:171],a[140:143],a[252:255],v[168:171]\n"
RT"a[182:183],v249 offset:45568\n"
PK"v128,v128,v129\n"
MF"v[168:171],a[136:139],a[236:239],v[168:171]\n"
RT"a[184:185],v245 offset:20480\n"
PK"v129,v130,v131\n"
MF"v[148:151],a[144:147],a[192:195],v[148:151]\n"
PK"v130,v132,v133\n"
PK"v131,v134,v135\n"
MF"v[148:151],a[148:151],a[208:211],v[148:151]\n"
RT"a[186:187],v245 offset:20992\n"
"v_accvgpr_write_b32 a36,v128\n"
MF"v[156:159],a[148:151],a[212:215],v[156:159]\n"
RT"a[188:189],v245 offset:45056\n"
"v_accvgpr_write_b32 a37,v129\n"
MF"v[156:159],a[144:147],a[196:199],v[156:159]\n"
"v_accvgpr_write_b32 a38,v130\n"
"v_accvgpr_write_b32 a39,v131\n"
MF"v[164:167],a[144:147],a[200:203],v[164:167]\n"
RT"a[190:191],v245 offset:45568\n"
PK"v136,v136,v137\n"
MF"v[164:167],a[148:151],a[216:219],v[164:167]\n"
PK"v137,v138,v139\n"
PK"v138,v140,v141\n"
MF"v[172:175],a[148:151],a[220:223],v[172:175]\n"
PK"v139,v142,v143\n"
"v_accvgpr_write_b32 a52,v136\n"
MF"v[172:175],a[144:147],a[204:207],v[172:175]\n"
"v_accvgpr_write_b32 a53,v137\n"
"v_accvgpr_write_b32 a54,v138\n"
MF"v[148:151],a[152:155],a[224:227],v[148:151]\n"
WT"lgkmcnt(2)\n"
"v_accvgpr_write_b32 a55,v139\n"
MF"v[148:151],a[156:159],a[240:243],v[148:151]\n"
MF"v[156:159],a[156:159],a[244:247],v[156:159]\n"
MF"v[156:159],a[152:155],a[228:231],v[156:159]\n"
MF"v[164:167],a[152:155],a[232:235],v[164:167]\n"
MF"v[164:167],a[156:159],a[248:251],v[164:167]\n"
MF"v[172:175],a[156:159],a[252:255],v[172:175]\n"
MF"v[172:175],a[152:155],a[236:239],v[172:175]\n"
MF"v[176:179],a[160:163],a[192:195],v[176:179]\n"
MF"v[176:179],a[164:167],a[208:211],v[176:179]\n"
MF"v[176:179],a[168:171],a[224:227],v[176:179]\n"
PK"v144,v144,v145\n"
PK"v145,v146,v147\n"
MF"v[176:179],a[172:175],a[240:243],v[176:179]\n"
PK"v146,v148,v149\n"
PK"v147,v150,v151\n"
MF"v[180:183],a[176:179],a[192:195],v[180:183]\n"
"v_mov_b32 v254,0x3f803f80\n"
"v_accvgpr_write_b32 a8,v144\n"
MF"v[180:183],a[180:183],a[208:211],v[180:183]\n"
"v_accvgpr_write_b32 a9,v145\n"
"v_accvgpr_write_b32 a128,v254\n"
MF"v[180:183],a[184:187],a[224:227],v[180:183]\n"
WT"lgkmcnt(0)\n"
"v_accvgpr_write_b32 a129,v254\n"
"v_accvgpr_write_b32 a130,v254\n"
MF"v[180:183],a[188:191],a[240:243],v[180:183]\n"
"v_accvgpr_write_b32 a131,v254\n"
"v_accvgpr_write_b32 a10,v146\n"
MF"v[184:187],a[160:163],a[196:199],v[184:187]\n"
"v_accvgpr_write_b32 a11,v147\n"
PK"v152,v152,v153\n"
MF"v[184:187],a[164:167],a[212:215],v[184:187]\n"
PK"v153,v154,v155\n"
PK"v154,v156,v157\n"
MF"v[184:187],a[168:171],a[228:231],v[184:187]\n"
PK"v155,v158,v159\n"
"v_accvgpr_write_b32 a24,v152\n"
MF"v[184:187],a[172:175],a[244:247],v[184:187]\n"
"v_accvgpr_write_b32 a25,v153\n"
PK"v176,v176,v177\n"
MF"v[188:191],a[176:179],a[196:199],v[188:191]\n"
PK"v177,v178,v179\n"
"v_accvgpr_write_b32 a26,v154\n"
MF"v[188:191],a[180:183],a[212:215],v[188:191]\n"
"v_accvgpr_write_b32 a27,v155\n"
PK"v178,v180,v181\n"
MF"v[188:191],a[184:187],a[228:231],v[188:191]\n"
PK"v179,v182,v183\n"
"v_accvgpr_write_b32 a12,v176\n"
MF"v[188:191],a[188:191],a[244:247],v[188:191]\n"
"v_accvgpr_write_b32 a13,v177\n"
"v_accvgpr_write_b32 a14,v178\n"
MF"v[192:195],a[160:163],a[200:203],v[192:195]\n"
WT"vmcnt(14)\n"
"v_accvgpr_write_b32 a15,v179\n"
PK"v160,v160,v161\n"
MF"v[192:195],a[164:167],a[216:219],v[192:195]\n"
"s_barrier\n"
MF"v[192:195],a[168:171],a[232:235],v[192:195]\n"
RD"a[64:67],v226 offset:0\n"
PK"v161,v162,v163\n"
MF"v[192:195],a[172:175],a[248:251],v[192:195]\n"
PK"v162,v164,v165\n"
PK"v163,v166,v167\n"
MF"v[196:199],a[176:179],a[200:203],v[196:199]\n"
RD"a[68:71],v226 offset:1024\n"
"v_accvgpr_write_b32 a40,v160\n"
MF"v[196:199],a[180:183],a[216:219],v[196:199]\n"
"v_accvgpr_write_b32 a41,v161\n"
"v_accvgpr_write_b32 a42,v162\n"
MF"v[196:199],a[184:187],a[232:235],v[196:199]\n"
RD"a[72:75],v226 offset:2048\n"
"v_accvgpr_write_b32 a43,v163\n"
MF"v[196:199],a[188:191],a[248:251],v[196:199]\n"
PK"v184,v184,v185\n"
PK"v185,v186,v187\n"
MF"v[200:203],a[160:163],a[204:207],v[200:203]\n"
RD"a[76:79],v226 offset:3072\n"
PK"v186,v188,v189\n"
MF"v[200:203],a[164:167],a[220:223],v[200:203]\n"
PK"v187,v190,v191\n"
PK"v168,v168,v169\n"
MF"v[200:203],a[168:171],a[236:239],v[200:203]\n"
RD"a[80:83],v226 offset:4096\n"
PK"v169,v170,v171\n"
MF"v[200:203],a[172:175],a[252:255],v[200:203]\n"
"v_accvgpr_write_b32 a28,v184\n"
"v_accvgpr_write_b32 a29,v185\n"
MF"v[204:207],a[176:179],a[204:207],v[204:207]\n"
RD"a[84:87],v226 offset:5120\n"
PK"v170,v172,v173\n"
MF"v[204:207],a[180:183],a[220:223],v[204:207]\n"
PK"v171,v174,v175\n"
"v_accvgpr_write_b32 a30,v186\n"
MF"v[204:207],a[184:187],a[236:239],v[204:207]\n"
"v_accvgpr_write_b32 a31,v187\n"
"v_accvgpr_write_b32 a56,v168\n"
MF"v[204:207],a[188:191],a[252:255],v[204:207]\n"
"v_accvgpr_write_b32 a57,v169\n"
"v_accvgpr_write_b32 a58,v170\n"
"v_accvgpr_write_b32 a59,v171\n"
RD"a[88:91],v226 offset:6144\n"
"s_cmp_eq_u32 s34,0\n"
"s_cbranch_scc1 LFA_%=\n"
WT"lgkmcnt(6)\n"
MF"v[0:3],a[64:67],a[0:3],0\n"
WT"lgkmcnt(3)\n"
PK"v192,v192,v193\n"
PK"v193,v194,v195\n"
MF"v[0:3],a[68:71],a[4:7],v[0:3]\n"
"s_mov_b32 m0,s40\n"
"v_readlane_b32 s43,v228,s42\n"
PK"v194,v196,v197\n"
MF"v[8:11],a[68:71],a[20:23],0\n"
PK"v195,v198,v199\n"
"v_readlane_b32 s44,v229,s42\n"
MF"v[8:11],a[64:67],a[16:19],v[8:11]\n"
SA"s42,s42,1\n"
BL"v225,s[8:11],s43 offen offset:0 sc1 lds\n"
MF"v[16:19],a[64:67],a[32:35],0\n"
"v_accvgpr_write_b32 a44,v192\n"
"v_accvgpr_write_b32 a45,v193\n"
MF"v[16:19],a[68:71],a[36:39],v[16:19]\n"
RD"a[92:95],v226 offset:7168\n"
"v_accvgpr_write_b32 a46,v194\n"
MF"v[24:27],a[68:71],a[52:55],0\n"
"v_accvgpr_write_b32 a47,v195\n"
PK"v200,v200,v201\n"
MF"v[24:27],a[64:67],a[48:51],v[24:27]\n"
PK"v201,v202,v203\n"
PK"v202,v204,v205\n"
MF"v[0:3],a[72:75],a[8:11],v[0:3]\n"
BL"v225,s[8:11],s43 offen offset:1024 sc1 lds\n"
PK"v203,v206,v207\n"
MF"v[0:3],a[76:79],a[12:15],v[0:3]\n"
"v_accvgpr_write_b32 a60,v200\n"
"v_accvgpr_write_b32 a61,v201\n"
MF"v[8:11],a[76:79],a[28:31],v[8:11]\n"
"s_mul_i32 s48,s32,655360\n"
"v_accvgpr_write_b32 a62,v202\n"
"v_accvgpr_write_b32 a63,v203\n"
MF"v[8:11],a[72:75],a[24:27],v[8:11]\n"
SA"s12,s74,s48\n"
"s_addc_u32 s13,s75,0\n"
MF"v[16:19],a[72:75],a[40:43],v[16:19]\n"
"s_and_b32 s13,s13,0xffff\n"
MF"v[16:19],a[76:79],a[44:47],v[16:19]\n"
"s_mov_b32 s14,655360\n"
RD"a[96:99],v226 offset:8192\n"
MF"v[24:27],a[76:79],a[60:63],v[24:27]\n"
"s_lshl_b32 s48,s33,3\n"
BL"v225,s[8:11],s43 offen offset:2048 sc1 lds\n"
MF"v[24:27],a[72:75],a[56:59],v[24:27]\n"
WT"lgkmcnt(1)\n"
EX"v0,v0\n"
MF"v[4:7],a[80:83],a[0:3],0\n"
"s_and_b32 s49,s34,2\n"
EX"v1,v1\n"
MF"v[4:7],a[84:87],a[4:7],v[4:7]\n"
"s_lshl_b32 s49,s49,1\n"
EX"v2,v2\n"
MF"v[12:15],a[84:87],a[20:23],0\n"
SA"s48,s48,s49\n"
RD"a[100:103],v226 offset:9216\n"
PK"v0,v0,v1\n"
MF"v[12:15],a[80:83],a[16:19],v[12:15]\n"
SA"s48,s48,s30\n"
EX"v3,v3\n"
MF"v[20:23],a[80:83],a[32:35],0\n"
"s_mul_i32 s48,s48,5120\n"
EX"v8,v8\n"
MF"v[20:23],a[84:87],a[36:39],v[20:23]\n"
"s_or_b32 s49,s34,2\n"
BL"v225,s[8:11],s43 offen offset:3072 sc1 lds\n"
MF"v[28:31],a[84:87],a[52:55],0\n"
"s_cmp_eq_u32 s49,7\n"
EX"v9,v9\n"
PK"v1,v2,v3\n"
MF"v[28:31],a[80:83],a[48:51],v[28:31]\n"
"s_cselect_b32 s55,s48,2130706432\n"
RD"a[104:107],v226 offset:10240\n"
PK"v8,v8,v9\n"
MF"v[4:7],a[88:91],a[8:11],v[4:7]\n"
SA"m0,s40,16384\n"
EX"v10,v10\n"
MF"v[4:7],a[92:95],a[12:15],v[4:7]\n"
EX"v11,v11\n"
MF"v[12:15],a[92:95],a[28:31],v[12:15]\n"
EX"v16,v16\n"
MF"v[12:15],a[88:91],a[24:27],v[12:15]\n"
SA"s40,s40,32768\n"
RD"a[108:111],v226 offset:11264\n"
PK"v9,v10,v11\n"
MF"v[20:23],a[88:91],a[40:43],v[20:23]\n"
"s_cmp_eq_u32 s40,s46\n"
BL"v225,s[8:11],s44 offen offset:0 sc1 lds\n"
EX"v17,v17\n"
MF"v[20:23],a[92:95],a[44:47],v[20:23]\n"
"s_cselect_b32 s40,s47,s40\n"
EX"v18,v18\n"
MF"v[28:31],a[92:95],a[60:63],v[28:31]\n"
EX"v19,v19\n"
PK"v16,v16,v17\n"
MF"v[28:31],a[88:91],a[56:59],v[28:31]\n"
WT"lgkmcnt(1)\n"
RD"a[112:115],v226 offset:12288\n"
PK"v17,v18,v19\n"
MF"v[32:35],a[96:99],a[0:3],0\n"
EX"v24,v24\n"
MF"v[32:35],a[100:103],a[4:7],v[32:35]\n"
EX"v25,v25\n"
"v_mov_b32 v227,v226\n"
MF"v[40:43],a[100:103],a[20:23],0\n"
BL"v225,s[8:11],s44 offen offset:1024 sc1 lds\n"
EX"v26,v26\n"
MF"v[40:43],a[96:99],a[16:19],v[40:43]\n"
RD"a[116:119],v226 offset:13312\n"
PK"v24,v24,v25\n"
MF"v[48:51],a[96:99],a[32:35],0\n"
EX"v27,v27\n"
MF"v[48:51],a[100:103],a[36:39],v[48:51]\n"
EX"v4,v4\n"
MF"v[56:59],a[100:103],a[52:55],0\n"
EX"v5,v5\n"
PK"v25,v26,v27\n"
MF"v[56:59],a[96:99],a[48:51],v[56:59]\n"
RD"a[120:123],v226 offset:14336\n"
PK"v2,v4,v5\n"
MF"v[32:35],a[104:107],a[8:11],v[32:35]\n"
WT"lgkmcnt(2)\n"
BL"v225,s[8:11],s44 offen offset:2048 sc1 lds\n"
EX"v6,v6\n"
MF"v[32:35],a[108:111],a[12:15],v[32:35]\n"
EX"v7,v7\n"
MF"v[40:43],a[108:111],a[28:31],v[40:43]\n"
EX"v12,v12\n"
MF"v[40:43],a[104:107],a[24:27],v[40:43]\n"
RD"a[124:127],v226 offset:15360\n"
"v_readlane_b32 s43,v228,s42\n"
PK"v3,v6,v7\n"
MF"v[48:51],a[104:107],a[40:43],v[48:51]\n"
EX"v13,v13\n"
MF"v[48:51],a[108:111],a[44:47],v[48:51]\n"
EX"v14,v14\n"
MF"v[56:59],a[108:111],a[60:63],v[56:59]\n"
BL"v225,s[8:11],s44 offen offset:3072 sc1 lds\n"
EX"v15,v15\n"
MF"v[56:59],a[104:107],a[56:59],v[56:59]\n"
"s_mov_b32 m0,s40\n"
RD"a[96:99],v227 offset:24576\n"
PK"v10,v12,v13\n"
MF"v[36:39],a[112:115],a[0:3],0\n"
WT"lgkmcnt(2)\n"
EX"v20,v20\n"
PK"v11,v14,v15\n"
MF"v[36:39],a[116:119],a[4:7],v[36:39]\n"
EX"v21,v21\n"
MF"v[44:47],a[116:119],a[20:23],0\n"
EX"v22,v22\n"
MF"v[44:47],a[112:115],a[16:19],v[44:47]\n"
RD"a[100:103],v227 offset:25600\n"
"v_readlane_b32 s44,v229,s42\n"
PK"v18,v20,v21\n"
MF"v[52:55],a[112:115],a[32:35],0\n"
SA"s42,s42,1\n"
BL"v225,s[8:11],s43 offen offset:0 sc1 lds\n"
MF"v[52:55],a[116:119],a[36:39],v[52:55]\n"
EX"v23,v23\n"
MF"v[60:63],a[116:119],a[52:55],0\n"
EX"v28,v28\n"
MF"v[60:63],a[112:115],a[48:51],v[60:63]\n"
RD"a[104:107],v227 offset:26624\n"
PK"v19,v22,v23\n"
MF"v[36:39],a[120:123],a[8:11],v[36:39]\n"
WT"lgkmcnt(2)\n"
EX"v29,v29\n"
MF"v[36:39],a[124:127],a[12:15],v[36:39]\n"
BL"v225,s[8:11],s43 offen offset:1024 sc1 lds\n"
MF"v[44:47],a[124:127],a[28:31],v[44:47]\n"
EX"v30,v30\n"
PK"v26,v28,v29\n"
MF"v[44:47],a[120:123],a[24:27],v[44:47]\n"
RD"a[108:111],v227 offset:27648\n"
MF"v[52:55],a[120:123],a[40:43],v[52:55]\n"
EX"v31,v31\n"
MF"v[52:55],a[124:127],a[44:47],v[52:55]\n"
EX"v32,v32\n"
MF"v[60:63],a[124:127],a[60:63],v[60:63]\n"
EX"v33,v33\n"
PK"v27,v30,v31\n"
MF"v[60:63],a[120:123],a[56:59],v[60:63]\n"
SA"s41,s41,32768\n"
BL"v225,s[8:11],s43 offen offset:2048 sc1 lds\n"
RD"a[112:115],v227 offset:28672\n"
MF"v[80:83],a[96:99],v[0:3],0\n"
"s_cmp_eq_u32 s41,163840\n"
EX"v34,v34\n"
MF"v[112:115],a[96:99],v[8:11],0\n"
"s_cselect_b32 s41,0,s41\n"
EX"v35,v35\n"
MF"v[144:147],a[96:99],v[16:19],0\n"
EX"v40,v40\n"
"v_add_u32 v226,s41,v224\n"
MF"v[176:179],a[96:99],v[24:27],0\n"
WT"lgkmcnt(1)\n"
RD"a[116:119],v227 offset:29696\n"
MF"v[84:87],a[100:103],v[0:3],0\n"
EX"v41,v41\n"
MF"v[116:119],a[100:103],v[8:11],0\n"
BL"v225,s[8:11],s43 offen offset:3072 sc1 lds\n"
MF"v[148:151],a[100:103],v[16:19],0\n"
SA"m0,s40,16384\n"
EX"v42,v42\n"
MF"v[180:183],a[100:103],v[24:27],0\n"
RD"a[120:123],v227 offset:30720\n"
MF"v[88:91],a[104:107],v[0:3],0\n"
EX"v43,v43\n"
MF"v[120:123],a[104:107],v[8:11],0\n"
EX"v48,v48\n"
MF"v[152:155],a[104:107],v[16:19],0\n"
SA"s40,s40,32768\n"
EX"v49,v49\n"
MF"v[184:187],a[104:107],v[24:27],0\n"
"s_cmp_eq_u32 s40,s46\n"
BL"v225,s[8:11],s44 offen offset:0 sc1 lds\n"
RD"a[124:127],v227 offset:31744\n"
MF"v[92:95],a[108:111],v[0:3],0\n"
"s_cselect_b32 s40,s47,s40\n"
EX"v50,v50\n"
MF"v[124:127],a[108:111],v[8:11],0\n"
EX"v51,v51\n"
MF"v[156:159],a[108:111],v[16:19],0\n"
EX"v56,v56\n"
MF"v[188:191],a[108:111],v[24:27],0\n"
WT"lgkmcnt(1)\n"
EX"v57,v57\n"
MF"v[96:99],a[112:115],v[0:3],0\n"
EX"v58,v58\n"
MF"v[128:131],a[112:115],v[8:11],0\n"
WT"vmcnt(15) lgkmcnt(0)\n"
BL"v225,s[8:11],s44 offen offset:1024 sc1 lds\n"
MF"v[160:163],a[112:115],v[16:19],0\n"
EX"v59,v59\n"
MF"v[192:195],a[112:115],v[24:27],0\n"
EX"v36,v36\n"
MF"v[100:103],a[116:119],v[0:3],0\n"
"s_barrier\n"
MF"v[132:135],a[116:119],v[8:11],0\n"
RD"a[64:67],v226 offset:0\n"
"buffer_store_dwordx2 v[232:233],v230,s[8:11],s98 offen sc1\n"
MF"v[164:167],a[116:119],v[16:19],0\n"
EX"v37,v37\n"
MF"v[196:199],a[116:119],v[24:27],0\n"
BL"v225,s[8:11],s44 offen offset:2048 sc1 lds\n"
RD"a[68:71],v226 offset:1024\n"
MF"v[104:107],a[120:123],v[0:3],0\n"
EX"v38,v38\n"
MF"v[136:139],a[120:123],v[8:11],0\n"
EX"v39,v39\n"
MF"v[168:171],a[120:123],v[16:19],0\n"
EX"v44,v44\n"
MF"v[200:203],a[120:123],v[24:27],0\n"
RD"a[72:75],v226 offset:2048\n"
RD"a[76:79],v226 offset:3072\n"
MF"v[108:111],a[124:127],v[0:3],0\n"
EX"v45,v45\n"
MF"v[140:143],a[124:127],v[8:11],0\n"
BL"v225,s[8:11],s44 offen offset:3072 sc1 lds\n"
MF"v[172:175],a[124:127],v[16:19],0\n"
EX"v46,v46\n"
MF"v[204:207],a[124:127],v[24:27],0\n"
RD"a[80:83],v226 offset:4096\n"
MF"a[132:135],a[128:131],v[0:3],0\n"
EX"v47,v47\n"
MF"a[136:139],a[128:131],v[8:11],0\n"
"buffer_load_dword v255,v253,s[12:15],s55 offen\n"
EX"v52,v52\n"
MF"a[140:143],a[128:131],v[16:19],0\n"
WT"lgkmcnt(3)\n"
EX"v53,v53\n"
MF"a[144:147],a[128:131],v[24:27],0\n"
SA"s55,s55,163840\n"
RD"a[84:87],v226 offset:5120\n"
RD"a[88:91],v226 offset:6144\n"
EX"v54,v54\n"
EX"v55,v55\n"
EX"v60,v60\n"
EX"v61,v61\n"
EX"v62,v62\n"
EX"v63,v63\n"
"s_cmp_eq_u32 s57,1\n"
"s_cbranch_scc1 LEV_%=\n"
".p2align 6\n"
"LP1_%=:\n"
MF"v[0:3],a[64:67],a[0:3],0\n"
MF"v[0:3],a[68:71],a[4:7],v[0:3]\n"
MF"v[8:11],a[68:71],a[20:23],0\n"
MF"v[8:11],a[64:67],a[16:19],v[8:11]\n"
MF"v[16:19],a[64:67],a[32:35],0\n"
MF"v[16:19],a[68:71],a[36:39],v[16:19]\n"
RD"a[92:95],v226 offset:7168\n"
PK"v64,v32,v33\n"
MF"v[24:27],a[68:71],a[52:55],0\n"
PK"v65,v34,v35\n"
PK"v66,v36,v37\n"
MF"v[24:27],a[64:67],a[48:51],v[24:27]\n"
WT"lgkmcnt(1)\n"
PK"v67,v38,v39\n"
PK"v68,v40,v41\n"
MF"v[0:3],a[72:75],a[8:11],v[0:3]\n"
PK"v69,v42,v43\n"
PK"v70,v44,v45\n"
MF"v[0:3],a[76:79],a[12:15],v[0:3]\n"
PK"v71,v46,v47\n"
PK"v72,v48,v49\n"
MF"v[8:11],a[76:79],a[28:31],v[8:11]\n"
PK"v73,v50,v51\n"
PK"v74,v52,v53\n"
MF"v[8:11],a[72:75],a[24:27],v[8:11]\n"
PK"v75,v54,v55\n"
PK"v76,v56,v57\n"
MF"v[16:19],a[72:75],a[40:43],v[16:19]\n"
PK"v77,v58,v59\n"
PK"v78,v60,v61\n"
MF"v[16:19],a[76:79],a[44:47],v[16:19]\n"
RD"a[96:99],v226 offset:8192\n"
PK"v79,v62,v63\n"
MF"v[24:27],a[76:79],a[60:63],v[24:27]\n"
EX"v0,v0\n"
MF"v[24:27],a[72:75],a[56:59],v[24:27]\n"
EX"v1,v1\n"
MF"v[4:7],a[80:83],a[0:3],0\n"
EX"v2,v2\n"
MF"v[4:7],a[84:87],a[4:7],v[4:7]\n"
RD"a[100:103],v226 offset:9216\n"
PK"v0,v0,v1\n"
MF"v[12:15],a[84:87],a[20:23],0\n"
EX"v3,v3\n"
MF"v[12:15],a[80:83],a[16:19],v[12:15]\n"
EX"v8,v8\n"
MF"v[20:23],a[80:83],a[32:35],0\n"
EX"v9,v9\n"
MF"v[20:23],a[84:87],a[36:39],v[20:23]\n"
RD"a[104:107],v226 offset:10240\n"
PK"v1,v2,v3\n"
MF"v[28:31],a[84:87],a[52:55],0\n"
EX"v10,v10\n"
MF"v[28:31],a[80:83],a[48:51],v[28:31]\n"
EX"v11,v11\n"
MF"v[4:7],a[88:91],a[8:11],v[4:7]\n"
WT"lgkmcnt(1)\n"
EX"v16,v16\n"
MF"v[4:7],a[92:95],a[12:15],v[4:7]\n"
RD"a[108:111],v226 offset:11264\n"
PK"v8,v8,v9\n"
MF"v[12:15],a[92:95],a[28:31],v[12:15]\n"
EX"v17,v17\n"
MF"v[12:15],a[88:91],a[24:27],v[12:15]\n"
EX"v18,v18\n"
MF"v[20:23],a[88:91],a[40:43],v[20:23]\n"
EX"v19,v19\n"
MF"v[20:23],a[92:95],a[44:47],v[20:23]\n"
RD"a[112:115],v226 offset:12288\n"
PK"v9,v10,v11\n"
MF"v[28:31],a[92:95],a[60:63],v[28:31]\n"
EX"v24,v24\n"
MF"v[28:31],a[88:91],a[56:59],v[28:31]\n"
EX"v25,v25\n"
MF"v[32:35],a[96:99],a[0:3],0\n"
EX"v26,v26\n"
MF"v[32:35],a[100:103],a[4:7],v[32:35]\n"
RD"a[116:119],v226 offset:13312\n"
PK"v16,v16,v17\n"
MF"v[40:43],a[100:103],a[20:23],0\n"
EX"v27,v27\n"
MF"v[40:43],a[96:99],a[16:19],v[40:43]\n"
EX"v4,v4\n"
MF"v[48:51],a[96:99],a[32:35],0\n"
EX"v5,v5\n"
MF"v[48:51],a[100:103],a[36:39],v[48:51]\n"
RD"a[120:123],v226 offset:14336\n"
PK"v17,v18,v19\n"
MF"v[56:59],a[100:103],a[52:55],0\n"
EX"v6,v6\n"
MF"v[56:59],a[96:99],a[48:51],v[56:59]\n"
WT"lgkmcnt(2)\n"
EX"v7,v7\n"
MF"v[32:35],a[104:107],a[8:11],v[32:35]\n"
EX"v12,v12\n"
MF"v[32:35],a[108:111],a[12:15],v[32:35]\n"
RD"a[124:127],v226 offset:15360\n"
"v_mov_b32 v227,v226\n"
MF"v[40:43],a[108:111],a[28:31],v[40:43]\n"
EX"v13,v13\n"
MF"v[40:43],a[104:107],a[24:27],v[40:43]\n"
EX"v14,v14\n"
MF"v[48:51],a[104:107],a[40:43],v[48:51]\n"
EX"v15,v15\n"
MF"v[48:51],a[108:111],a[44:47],v[48:51]\n"
RD"a[64:67],v227 offset:16384\n"
PK"v24,v24,v25\n"
MF"v[56:59],a[108:111],a[60:63],v[56:59]\n"
EX"v20,v20\n"
MF"v[56:59],a[104:107],a[56:59],v[56:59]\n"
EX"v21,v21\n"
MF"v[36:39],a[112:115],a[0:3],0\n"
WT"lgkmcnt(1)\n"
EX"v22,v22\n"
MF"v[36:39],a[116:119],a[4:7],v[36:39]\n"
RD"a[68:71],v227 offset:24576\n"
PK"v25,v26,v27\n"
MF"v[44:47],a[116:119],a[20:23],0\n"
EX"v23,v23\n"
MF"v[44:47],a[112:115],a[16:19],v[44:47]\n"
EX"v28,v28\n"
MF"v[52:55],a[112:115],a[32:35],0\n"
EX"v29,v29\n"
MF"v[52:55],a[116:119],a[36:39],v[52:55]\n"
RD"a[72:75],v227 offset:17408\n"
"v_readlane_b32 s43,v228,s42\n"
MF"v[60:63],a[116:119],a[52:55],0\n"
EX"v30,v30\n"
MF"v[60:63],a[112:115],a[48:51],v[60:63]\n"
"s_mov_b32 m0,s40\n"
EX"v31,v31\n"
MF"v[36:39],a[120:123],a[8:11],v[36:39]\n"
PK"v2,v4,v5\n"
PK"v3,v6,v7\n"
MF"v[36:39],a[124:127],a[12:15],v[36:39]\n"
RD"a[76:79],v227 offset:25600\n"
"v_readlane_b32 s44,v229,s42\n"
MF"v[44:47],a[124:127],a[28:31],v[44:47]\n"
PK"v10,v12,v13\n"
PK"v11,v14,v15\n"
MF"v[44:47],a[120:123],a[24:27],v[44:47]\n"
SA"s42,s42,1\n"
BL"v225,s[8:11],s43 offen offset:0 sc1 lds\n"
MF"v[52:55],a[120:123],a[40:43],v[52:55]\n"
PK"v18,v20,v21\n"
PK"v19,v22,v23\n"
MF"v[52:55],a[124:127],a[44:47],v[52:55]\n"
RD"a[80:83],v227 offset:18432\n"
PK"v26,v28,v29\n"
MF"v[60:63],a[124:127],a[60:63],v[60:63]\n"
EX"v32,v32\n"
MF"v[60:63],a[120:123],a[56:59],v[60:63]\n"
WT"lgkmcnt(2)\n"
PK"v27,v30,v31\n"
SA"s41,s41,32768\n"
MF"v[80:83],a[64:67],v[64:67],v[80:83]\n"
"s_cmp_eq_u32 s41,163840\n"
EX"v33,v33\n"
MF"v[80:83],a[68:71],v[0:3],v[80:83]\n"
"s_cselect_b32 s41,0,s41\n"
RD"a[84:87],v227 offset:26624\n"
"v_add_u32 v226,s41,v224\n"
MF"v[112:115],a[68:71],v[8:11],v[112:115]\n"
EX"v34,v34\n"
MF"v[112:115],a[64:67],v[68:71],v[112:115]\n"
BL"v225,s[8:11],s43 offen offset:1024 sc1 lds\n"
MF"v[144:147],a[64:67],v[72:75],v[144:147]\n"
EX"v35,v35\n"
MF"v[144:147],a[68:71],v[16:19],v[144:147]\n"
RD"a[88:91],v227 offset:19456\n"
MF"v[176:179],a[68:71],v[24:27],v[176:179]\n"
EX"v40,v40\n"
MF"v[176:179],a[64:67],v[76:79],v[176:179]\n"
MF"v[84:87],a[72:75],v[64:67],v[84:87]\n"
WT"lgkmcnt(1)\n"
EX"v41,v41\n"
MF"v[84:87],a[76:79],v[0:3],v[84:87]\n"
RD"a[92:95],v227 offset:27648\n"
MF"v[116:119],a[76:79],v[8:11],v[116:119]\n"
EX"v42,v42\n"
MF"v[116:119],a[72:75],v[68:71],v[116:119]\n"
BL"v225,s[8:11],s43 offen offset:2048 sc1 lds\n"
MF"v[148:151],a[72:75],v[72:75],v[148:151]\n"
EX"v43,v43\n"
MF"v[148:151],a[76:79],v[16:19],v[148:151]\n"
RD"a[96:99],v227 offset:20480\n"
MF"v[180:183],a[76:79],v[24:27],v[180:183]\n"
EX"v48,v48\n"
MF"v[180:183],a[72:75],v[76:79],v[180:183]\n"
MF"v[88:91],a[80:83],v[64:67],v[88:91]\n"
EX"v49,v49\n"
MF"v[88:91],a[84:87],v[0:3],v[88:91]\n"
RD"a[100:103],v227 offset:28672\n"
MF"v[120:123],a[84:87],v[8:11],v[120:123]\n"
EX"v50,v50\n"
MF"v[120:123],a[80:83],v[68:71],v[120:123]\n"
BL"v225,s[8:11],s43 offen offset:3072 sc1 lds\n"
MF"v[152:155],a[80:83],v[72:75],v[152:155]\n"
EX"v51,v51\n"
MF"v[152:155],a[84:87],v[16:19],v[152:155]\n"
RD"a[104:107],v227 offset:21504\n"
MF"v[184:187],a[84:87],v[24:27],v[184:187]\n"
EX"v56,v56\n"
MF"v[184:187],a[80:83],v[76:79],v[184:187]\n"
WT"lgkmcnt(2)\n"
SA"m0,s40,16384\n"
MF"v[92:95],a[88:91],v[64:67],v[92:95]\n"
EX"v57,v57\n"
MF"v[92:95],a[92:95],v[0:3],v[92:95]\n"
RD"a[108:111],v227 offset:29696\n"
MF"v[124:127],a[92:95],v[8:11],v[124:127]\n"
EX"v58,v58\n"
MF"v[124:127],a[88:91],v[68:71],v[124:127]\n"
BL"v225,s[8:11],s44 offen offset:0 sc1 lds\n"
MF"v[156:159],a[88:91],v[72:75],v[156:159]\n"
SA"s40,s40,32768\n"
EX"v59,v59\n"
MF"v[156:159],a[92:95],v[16:19],v[156:159]\n"
"s_cmp_eq_u32 s40,s46\n"
RD"a[112:115],v227 offset:22528\n"
"s_cselect_b32 s40,s47,s40\n"
MF"v[188:191],a[92:95],v[24:27],v[188:191]\n"
EX"v36,v36\n"
MF"v[188:191],a[88:91],v[76:79],v[188:191]\n"
MF"v[96:99],a[96:99],v[64:67],v[96:99]\n"
WT"lgkmcnt(1)\n"
EX"v37,v37\n"
MF"v[96:99],a[100:103],v[0:3],v[96:99]\n"
RD"a[116:119],v227 offset:30720\n"
MF"v[128:131],a[100:103],v[8:11],v[128:131]\n"
EX"v38,v38\n"
MF"v[128:131],a[96:99],v[68:71],v[128:131]\n"
BL"v225,s[8:11],s44 offen offset:1024 sc1 lds\n"
MF"v[160:163],a[96:99],v[72:75],v[160:163]\n"
EX"v39,v39\n"
MF"v[160:163],a[100:103],v[16:19],v[160:163]\n"
RD"a[120:123],v227 offset:23552\n"
MF"v[192:195],a[100:103],v[24:27],v[192:195]\n"
EX"v44,v44\n"
MF"v[192:195],a[96:99],v[76:79],v[192:195]\n"
MF"v[100:103],a[104:107],v[64:67],v[100:103]\n"
EX"v45,v45\n"
MF"v[100:103],a[108:111],v[0:3],v[100:103]\n"
RD"a[124:127],v227 offset:31744\n"
MF"v[132:135],a[108:111],v[8:11],v[132:135]\n"
EX"v46,v46\n"
MF"v[132:135],a[104:107],v[68:71],v[132:135]\n"
BL"v225,s[8:11],s44 offen offset:2048 sc1 lds\n"
MF"v[164:167],a[104:107],v[72:75],v[164:167]\n"
EX"v47,v47\n"
MF"v[164:167],a[108:111],v[16:19],v[164:167]\n"
MF"v[196:199],a[108:111],v[24:27],v[196:199]\n"
EX"v52,v52\n"
MF"v[196:199],a[104:107],v[76:79],v[196:199]\n"
WT"lgkmcnt(1)\n"
MF"v[104:107],a[112:115],v[64:67],v[104:107]\n"
EX"v53,v53\n"
MF"v[104:107],a[116:119],v[0:3],v[104:107]\n"
MF"v[136:139],a[116:119],v[8:11],v[136:139]\n"
EX"v54,v54\n"
MF"v[136:139],a[112:115],v[68:71],v[136:139]\n"
WT"vmcnt(15) lgkmcnt(0)\n"
BL"v225,s[8:11],s44 offen offset:3072 sc1 lds\n"
MF"v[168:171],a[112:115],v[72:75],v[168:171]\n"
"s_barrier\n"
MF"v[168:171],a[116:119],v[16:19],v[168:171]\n"
RD"a[64:67],v226 offset:0\n"
MF"v[200:203],a[116:119],v[24:27],v[200:203]\n"
EX"v55,v55\n"
MF"v[200:203],a[112:115],v[76:79],v[200:203]\n"
"buffer_load_dword v255,v253,s[12:15],s55 offen\n"
MF"v[108:111],a[120:123],v[64:67],v[108:111]\n"
EX"v60,v60\n"
MF"v[108:111],a[124:127],v[0:3],v[108:111]\n"
SA"s55,s55,163840\n"
RD"a[68:71],v226 offset:1024\n"
MF"v[140:143],a[124:127],v[8:11],v[140:143]\n"
EX"v61,v61\n"
MF"v[140:143],a[120:123],v[68:71],v[140:143]\n"
RD"a[72:75],v226 offset:2048\n"
MF"v[172:175],a[120:123],v[72:75],v[172:175]\n"
EX"v62,v62\n"
MF"v[172:175],a[124:127],v[16:19],v[172:175]\n"
RD"a[76:79],v226 offset:3072\n"
MF"v[204:207],a[124:127],v[24:27],v[204:207]\n"
EX"v63,v63\n"
MF"v[204:207],a[120:123],v[76:79],v[204:207]\n"
MF"a[132:135],a[128:131],v[64:67],a[132:135]\n"
MF"a[132:135],a[128:131],v[0:3],a[132:135]\n"
RD"a[80:83],v226 offset:4096\n"
MF"a[136:139],a[128:131],v[8:11],a[136:139]\n"
MF"a[136:139],a[128:131],v[68:71],a[136:139]\n"
RD"a[84:87],v226 offset:5120\n"
MF"a[140:143],a[128:131],v[72:75],a[140:143]\n"
MF"a[140:143],a[128:131],v[16:19],a[140:143]\n"
RD"a[88:91],v226 offset:6144\n"
MF"a[144:147],a[128:131],v[24:27],a[144:147]\n"
WT"lgkmcnt(5)\n"
"s_sub_u32 s56,s56,1\n"
MF"a[144:147],a[128:131],v[76:79],a[144:147]\n"
"s_cbranch_scc0 LP1_%=\n"
"s_cmp_eq_u32 s57,2\n"
"s_cbranch_scc1 LEV_%=\n"
"s_cmp_eq_u32 s57,3\n"
"s_cbranch_scc1 LRT_%=\n"
"LDA_%=:\n"
MF"v[0:3],a[64:67],a[0:3],v[208:211]\n"
MF"v[0:3],a[68:71],a[4:7],v[0:3]\n"
MF"v[8:11],a[68:71],a[20:23],0\n"
MF"v[8:11],a[64:67],a[16:19],v[8:11]\n"
MF"v[16:19],a[64:67],a[32:35],0\n"
MF"v[16:19],a[68:71],a[36:39],v[16:19]\n"
RD"a[92:95],v226 offset:7168\n"
PK"v64,v32,v33\n"
MF"v[24:27],a[68:71],a[52:55],0\n"
PK"v65,v34,v35\n"
PK"v66,v36,v37\n"
MF"v[24:27],a[64:67],a[48:51],v[24:27]\n"
WT"lgkmcnt(1)\n"
PK"v67,v38,v39\n"
PK"v68,v40,v41\n"
MF"v[0:3],a[72:75],a[8:11],v[0:3]\n"
PK"v69,v42,v43\n"
PK"v70,v44,v45\n"
MF"v[0:3],a[76:79],a[12:15],v[0:3]\n"
PK"v71,v46,v47\n"
PK"v72,v48,v49\n"
MF"v[8:11],a[76:79],a[28:31],v[8:11]\n"
PK"v73,v50,v51\n"
PK"v74,v52,v53\n"
MF"v[8:11],a[72:75],a[24:27],v[8:11]\n"
PK"v75,v54,v55\n"
PK"v76,v56,v57\n"
MF"v[16:19],a[72:75],a[40:43],v[16:19]\n"
PK"v77,v58,v59\n"
PK"v78,v60,v61\n"
MF"v[16:19],a[76:79],a[44:47],v[16:19]\n"
RD"a[96:99],v226 offset:8192\n"
PK"v79,v62,v63\n"
MF"v[24:27],a[76:79],a[60:63],v[24:27]\n"
EX"v0,v0\n"
MF"v[24:27],a[72:75],a[56:59],v[24:27]\n"
EX"v1,v1\n"
MF"v[4:7],a[80:83],a[0:3],v[212:215]\n"
EX"v2,v2\n"
MF"v[4:7],a[84:87],a[4:7],v[4:7]\n"
RD"a[100:103],v226 offset:9216\n"
PK"v0,v0,v1\n"
MF"v[12:15],a[84:87],a[20:23],0\n"
EX"v3,v3\n"
MF"v[12:15],a[80:83],a[16:19],v[12:15]\n"
EX"v8,v8\n"
MF"v[20:23],a[80:83],a[32:35],0\n"
EX"v9,v9\n"
MF"v[20:23],a[84:87],a[36:39],v[20:23]\n"
RD"a[104:107],v226 offset:10240\n"
PK"v1,v2,v3\n"
MF"v[28:31],a[84:87],a[52:55],0\n"
EX"v10,v10\n"
MF"v[28:31],a[80:83],a[48:51],v[28:31]\n"
EX"v11,v11\n"
MF"v[4:7],a[88:91],a[8:11],v[4:7]\n"
WT"lgkmcnt(1)\n"
EX"v16,v16\n"
MF"v[4:7],a[92:95],a[12:15],v[4:7]\n"
RD"a[108:111],v226 offset:11264\n"
PK"v8,v8,v9\n"
MF"v[12:15],a[92:95],a[28:31],v[12:15]\n"
EX"v17,v17\n"
MF"v[12:15],a[88:91],a[24:27],v[12:15]\n"
EX"v18,v18\n"
MF"v[20:23],a[88:91],a[40:43],v[20:23]\n"
EX"v19,v19\n"
MF"v[20:23],a[92:95],a[44:47],v[20:23]\n"
RD"a[112:115],v226 offset:12288\n"
PK"v9,v10,v11\n"
MF"v[28:31],a[92:95],a[60:63],v[28:31]\n"
EX"v24,v24\n"
MF"v[28:31],a[88:91],a[56:59],v[28:31]\n"
EX"v25,v25\n"
MF"v[32:35],a[96:99],a[0:3],v[216:219]\n"
EX"v26,v26\n"
MF"v[32:35],a[100:103],a[4:7],v[32:35]\n"
RD"a[116:119],v226 offset:13312\n"
PK"v16,v16,v17\n"
MF"v[40:43],a[100:103],a[20:23],0\n"
EX"v27,v27\n"
MF"v[40:43],a[96:99],a[16:19],v[40:43]\n"
EX"v4,v4\n"
MF"v[48:51],a[96:99],a[32:35],0\n"
EX"v5,v5\n"
MF"v[48:51],a[100:103],a[36:39],v[48:51]\n"
RD"a[120:123],v226 offset:14336\n"
PK"v17,v18,v19\n"
MF"v[56:59],a[100:103],a[52:55],0\n"
EX"v6,v6\n"
MF"v[56:59],a[96:99],a[48:51],v[56:59]\n"
WT"lgkmcnt(2)\n"
EX"v7,v7\n"
MF"v[32:35],a[104:107],a[8:11],v[32:35]\n"
EX"v12,v12\n"
MF"v[32:35],a[108:111],a[12:15],v[32:35]\n"
RD"a[124:127],v226 offset:15360\n"
"v_mov_b32 v227,v226\n"
MF"v[40:43],a[108:111],a[28:31],v[40:43]\n"
EX"v13,v13\n"
MF"v[40:43],a[104:107],a[24:27],v[40:43]\n"
EX"v14,v14\n"
MF"v[48:51],a[104:107],a[40:43],v[48:51]\n"
EX"v15,v15\n"
MF"v[48:51],a[108:111],a[44:47],v[48:51]\n"
RD"a[64:67],v227 offset:16384\n"
PK"v24,v24,v25\n"
MF"v[56:59],a[108:111],a[60:63],v[56:59]\n"
EX"v20,v20\n"
MF"v[56:59],a[104:107],a[56:59],v[56:59]\n"
EX"v21,v21\n"
MF"v[36:39],a[112:115],a[0:3],v[220:223]\n"
WT"lgkmcnt(1)\n"
EX"v22,v22\n"
MF"v[36:39],a[116:119],a[4:7],v[36:39]\n"
RD"a[68:71],v227 offset:24576\n"
PK"v25,v26,v27\n"
MF"v[44:47],a[116:119],a[20:23],0\n"
EX"v23,v23\n"
MF"v[44:47],a[112:115],a[16:19],v[44:47]\n"
EX"v28,v28\n"
MF"v[52:55],a[112:115],a[32:35],0\n"
EX"v29,v29\n"
MF"v[52:55],a[116:119],a[36:39],v[52:55]\n"
RD"a[72:75],v227 offset:17408\n"
"v_readlane_b32 s43,v228,s42\n"
MF"v[60:63],a[116:119],a[52:55],0\n"
EX"v30,v30\n"
MF"v[60:63],a[112:115],a[48:51],v[60:63]\n"
"s_mov_b32 m0,s40\n"
EX"v31,v31\n"
MF"v[36:39],a[120:123],a[8:11],v[36:39]\n"
PK"v2,v4,v5\n"
PK"v3,v6,v7\n"
MF"v[36:39],a[124:127],a[12:15],v[36:39]\n"
RD"a[76:79],v227 offset:25600\n"
"v_readlane_b32 s44,v229,s42\n"
MF"v[44:47],a[124:127],a[28:31],v[44:47]\n"
PK"v10,v12,v13\n"
PK"v11,v14,v15\n"
MF"v[44:47],a[120:123],a[24:27],v[44:47]\n"
SA"s42,s42,1\n"
BL"v225,s[8:11],s43 offen offset:0 sc1 lds\n"
MF"v[52:55],a[120:123],a[40:43],v[52:55]\n"
PK"v18,v20,v21\n"
PK"v19,v22,v23\n"
MF"v[52:55],a[124:127],a[44:47],v[52:55]\n"
RD"a[80:83],v227 offset:18432\n"
PK"v26,v28,v29\n"
MF"v[60:63],a[124:127],a[60:63],v[60:63]\n"
EX"v32,v32\n"
MF"v[60:63],a[120:123],a[56:59],v[60:63]\n"
WT"lgkmcnt(2)\n"
PK"v27,v30,v31\n"
SA"s41,s41,32768\n"
MF"v[80:83],a[64:67],v[64:67],v[80:83]\n"
"s_cmp_eq_u32 s41,163840\n"
EX"v33,v33\n"
MF"v[80:83],a[68:71],v[0:3],v[80:83]\n"
"s_cselect_b32 s41,0,s41\n"
RD"a[84:87],v227 offset:26624\n"
"v_add_u32 v226,s41,v224\n"
MF"v[112:115],a[68:71],v[8:11],v[112:115]\n"
EX"v34,v34\n"
MF"v[112:115],a[64:67],v[68:71],v[112:115]\n"
BL"v225,s[8:11],s43 offen offset:1024 sc1 lds\n"
MF"v[144:147],a[64:67],v[72:75],v[144:147]\n"
EX"v35,v35\n"
MF"v[144:147],a[68:71],v[16:19],v[144:147]\n"
RD"a[88:91],v227 offset:19456\n"
MF"v[176:179],a[68:71],v[24:27],v[176:179]\n"
EX"v40,v40\n"
MF"v[176:179],a[64:67],v[76:79],v[176:179]\n"
MF"v[84:87],a[72:75],v[64:67],v[84:87]\n"
WT"lgkmcnt(1)\n"
EX"v41,v41\n"
MF"v[84:87],a[76:79],v[0:3],v[84:87]\n"
RD"a[92:95],v227 offset:27648\n"
MF"v[116:119],a[76:79],v[8:11],v[116:119]\n"
EX"v42,v42\n"
MF"v[116:119],a[72:75],v[68:71],v[116:119]\n"
BL"v225,s[8:11],s43 offen offset:2048 sc1 lds\n"
MF"v[148:151],a[72:75],v[72:75],v[148:151]\n"
EX"v43,v43\n"
MF"v[148:151],a[76:79],v[16:19],v[148:151]\n"
RD"a[96:99],v227 offset:20480\n"
MF"v[180:183],a[76:79],v[24:27],v[180:183]\n"
EX"v48,v48\n"
MF"v[180:183],a[72:75],v[76:79],v[180:183]\n"
MF"v[88:91],a[80:83],v[64:67],v[88:91]\n"
EX"v49,v49\n"
MF"v[88:91],a[84:87],v[0:3],v[88:91]\n"
RD"a[100:103],v227 offset:28672\n"
MF"v[120:123],a[84:87],v[8:11],v[120:123]\n"
EX"v50,v50\n"
MF"v[120:123],a[80:83],v[68:71],v[120:123]\n"
BL"v225,s[8:11],s43 offen offset:3072 sc1 lds\n"
MF"v[152:155],a[80:83],v[72:75],v[152:155]\n"
EX"v51,v51\n"
MF"v[152:155],a[84:87],v[16:19],v[152:155]\n"
RD"a[104:107],v227 offset:21504\n"
MF"v[184:187],a[84:87],v[24:27],v[184:187]\n"
EX"v56,v56\n"
MF"v[184:187],a[80:83],v[76:79],v[184:187]\n"
WT"lgkmcnt(2)\n"
SA"m0,s40,16384\n"
MF"v[92:95],a[88:91],v[64:67],v[92:95]\n"
EX"v57,v57\n"
MF"v[92:95],a[92:95],v[0:3],v[92:95]\n"
RD"a[108:111],v227 offset:29696\n"
MF"v[124:127],a[92:95],v[8:11],v[124:127]\n"
EX"v58,v58\n"
MF"v[124:127],a[88:91],v[68:71],v[124:127]\n"
BL"v225,s[8:11],s44 offen offset:0 sc1 lds\n"
MF"v[156:159],a[88:91],v[72:75],v[156:159]\n"
SA"s40,s40,32768\n"
EX"v59,v59\n"
MF"v[156:159],a[92:95],v[16:19],v[156:159]\n"
"s_cmp_eq_u32 s40,s46\n"
RD"a[112:115],v227 offset:22528\n"
"s_cselect_b32 s40,s47,s40\n"
MF"v[188:191],a[92:95],v[24:27],v[188:191]\n"
EX"v36,v36\n"
MF"v[188:191],a[88:91],v[76:79],v[188:191]\n"
MF"v[96:99],a[96:99],v[64:67],v[96:99]\n"
WT"lgkmcnt(1)\n"
EX"v37,v37\n"
MF"v[96:99],a[100:103],v[0:3],v[96:99]\n"
RD"a[116:119],v227 offset:30720\n"
MF"v[128:131],a[100:103],v[8:11],v[128:131]\n"
EX"v38,v38\n"
MF"v[128:131],a[96:99],v[68:71],v[128:131]\n"
BL"v225,s[8:11],s44 offen offset:1024 sc1 lds\n"
MF"v[160:163],a[96:99],v[72:75],v[160:163]\n"
EX"v39,v39\n"
MF"v[160:163],a[100:103],v[16:19],v[160:163]\n"
RD"a[120:123],v227 offset:23552\n"
MF"v[192:195],a[100:103],v[24:27],v[192:195]\n"
EX"v44,v44\n"
MF"v[192:195],a[96:99],v[76:79],v[192:195]\n"
MF"v[100:103],a[104:107],v[64:67],v[100:103]\n"
EX"v45,v45\n"
MF"v[100:103],a[108:111],v[0:3],v[100:103]\n"
RD"a[124:127],v227 offset:31744\n"
MF"v[132:135],a[108:111],v[8:11],v[132:135]\n"
EX"v46,v46\n"
MF"v[132:135],a[104:107],v[68:71],v[132:135]\n"
BL"v225,s[8:11],s44 offen offset:2048 sc1 lds\n"
MF"v[164:167],a[104:107],v[72:75],v[164:167]\n"
EX"v47,v47\n"
MF"v[164:167],a[108:111],v[16:19],v[164:167]\n"
MF"v[196:199],a[108:111],v[24:27],v[196:199]\n"
EX"v52,v52\n"
MF"v[196:199],a[104:107],v[76:79],v[196:199]\n"
WT"lgkmcnt(1)\n"
MF"v[104:107],a[112:115],v[64:67],v[104:107]\n"
EX"v53,v53\n"
MF"v[104:107],a[116:119],v[0:3],v[104:107]\n"
MF"v[136:139],a[116:119],v[8:11],v[136:139]\n"
EX"v54,v54\n"
MF"v[136:139],a[112:115],v[68:71],v[136:139]\n"
WT"vmcnt(15) lgkmcnt(0)\n"
BL"v225,s[8:11],s44 offen offset:3072 sc1 lds\n"
MF"v[168:171],a[112:115],v[72:75],v[168:171]\n"
"s_barrier\n"
MF"v[168:171],a[116:119],v[16:19],v[168:171]\n"
RD"a[64:67],v226 offset:0\n"
MF"v[200:203],a[116:119],v[24:27],v[200:203]\n"
EX"v55,v55\n"
MF"v[200:203],a[112:115],v[76:79],v[200:203]\n"
MF"v[108:111],a[120:123],v[64:67],v[108:111]\n"
EX"v60,v60\n"
MF"v[108:111],a[124:127],v[0:3],v[108:111]\n"
RD"a[68:71],v226 offset:1024\n"
MF"v[140:143],a[124:127],v[8:11],v[140:143]\n"
EX"v61,v61\n"
MF"v[140:143],a[120:123],v[68:71],v[140:143]\n"
RD"a[72:75],v226 offset:2048\n"
MF"v[172:175],a[120:123],v[72:75],v[172:175]\n"
EX"v62,v62\n"
MF"v[172:175],a[124:127],v[16:19],v[172:175]\n"
RD"a[76:79],v226 offset:3072\n"
MF"v[204:207],a[124:127],v[24:27],v[204:207]\n"
EX"v63,v63\n"
MF"v[204:207],a[120:123],v[76:79],v[204:207]\n"
MF"a[132:135],a[128:131],v[64:67],a[132:135]\n"
MF"a[132:135],a[128:131],v[0:3],a[132:135]\n"
RD"a[80:83],v226 offset:4096\n"
MF"a[136:139],a[128:131],v[8:11],a[136:139]\n"
MF"a[136:139],a[128:131],v[68:71],a[136:139]\n"
RD"a[84:87],v226 offset:5120\n"
MF"a[140:143],a[128:131],v[72:75],a[140:143]\n"
MF"a[140:143],a[128:131],v[16:19],a[140:143]\n"
RD"a[88:91],v226 offset:6144\n"
MF"a[144:147],a[128:131],v[24:27],a[144:147]\n"
WT"lgkmcnt(5)\n"
MF"a[144:147],a[128:131],v[76:79],a[144:147]\n"
"LDB_%=:\n"
MF"v[8:11],a[64:67],a[16:19],v[208:211]\n"
"v_readlane_b32 s43,v228,s42\n"
PK"v64,v32,v33\n"
MF"v[8:11],a[68:71],a[20:23],v[8:11]\n"
RD"a[92:95],v226 offset:7168\n"
PK"v65,v34,v35\n"
MF"v[16:19],a[68:71],a[36:39],0\n"
PK"v66,v36,v37\n"
PK"v67,v38,v39\n"
MF"v[16:19],a[64:67],a[32:35],v[16:19]\n"
"s_mov_b32 m0,s40\n"
PK"v68,v40,v41\n"
PK"v69,v42,v43\n"
MF"v[24:27],a[64:67],a[48:51],0\n"
PK"v70,v44,v45\n"
PK"v71,v46,v47\n"
MF"v[24:27],a[68:71],a[52:55],v[24:27]\n"
WT"lgkmcnt(1)\n"
RD"a[96:99],v226 offset:8192\n"
"v_readlane_b32 s44,v229,s42\n"
MF"v[8:11],a[72:75],a[24:27],v[8:11]\n"
PK"v72,v48,v49\n"
PK"v73,v50,v51\n"
MF"v[8:11],a[76:79],a[28:31],v[8:11]\n"
SA"s42,s42,1\n"
BL"v225,s[8:11],s43 offen offset:0 sc1 lds\n"
MF"v[16:19],a[76:79],a[44:47],v[16:19]\n"
PK"v74,v52,v53\n"
PK"v75,v54,v55\n"
MF"v[16:19],a[72:75],a[40:43],v[16:19]\n"
RD"a[100:103],v226 offset:9216\n"
PK"v76,v56,v57\n"
PK"v77,v58,v59\n"
MF"v[24:27],a[72:75],a[56:59],v[24:27]\n"
PK"v78,v60,v61\n"
PK"v79,v62,v63\n"
MF"v[24:27],a[76:79],a[60:63],v[24:27]\n"
RD"a[104:107],v226 offset:10240\n"
MF"v[12:15],a[80:83],a[16:19],v[212:215]\n"
EX"v8,v8\n"
MF"v[12:15],a[84:87],a[20:23],v[12:15]\n"
RD"a[108:111],v226 offset:11264\n"
"v_lshrrev_b32 v235,7,v224\n"
MF"v[20:23],a[84:87],a[36:39],0\n"
EX"v9,v9\n"
MF"v[20:23],a[80:83],a[32:35],v[20:23]\n"
BL"v225,s[8:11],s43 offen offset:1024 sc1 lds\n"
MF"v[28:31],a[80:83],a[48:51],0\n"
EX"v10,v10\n"
"v_lshrrev_b32 v230,1,v224\n"
MF"v[28:31],a[84:87],a[52:55],v[28:31]\n"
RD"a[112:115],v226 offset:12288\n"
"v_lshl_add_u32 v230,v235,27,v230\n"
MF"v[12:15],a[88:91],a[24:27],v[12:15]\n"
WT"lgkmcnt(2)\n"
EX"v11,v11\n"
PK"v8,v8,v9\n"
MF"v[12:15],a[92:95],a[28:31],v[12:15]\n"
RD"a[116:119],v226 offset:13312\n"
PK"v9,v10,v11\n"
MF"v[20:23],a[92:95],a[44:47],v[20:23]\n"
"s_lshl_b32 s48,s35,8\n"
EX"v16,v16\n"
MF"v[20:23],a[88:91],a[40:43],v[20:23]\n"
MF"v[28:31],a[88:91],a[56:59],v[28:31]\n"
SA"s48,s48,92160\n"
EX"v17,v17\n"
MF"v[28:31],a[92:95],a[60:63],v[28:31]\n"
BL"v225,s[8:11],s43 offen offset:2048 sc1 lds\n"
MF"v[40:43],a[96:99],a[16:19],v[216:219]\n"
"s_andn2_b32 s49,s61,255\n"
EX"v18,v18\n"
MF"v[40:43],a[100:103],a[20:23],v[40:43]\n"
RD"a[120:123],v226 offset:14336\n"
PK"v16,v16,v17\n"
MF"v[48:51],a[100:103],a[36:39],0\n"
EX"v19,v19\n"
MF"v[48:51],a[96:99],a[32:35],v[48:51]\n"
RD"a[124:127],v226 offset:15360\n"
"v_bfe_u32 v236,v224,4,2\n"
MF"v[56:59],a[96:99],a[48:51],0\n"
EX"v24,v24\n"
MF"v[56:59],a[100:103],a[52:55],v[56:59]\n"
PK"v17,v18,v19\n"
"v_lshl_add_u32 v236,v236,6,s49\n"
MF"v[40:43],a[104:107],a[24:27],v[40:43]\n"
WT"lgkmcnt(2)\n"
EX"v25,v25\n"
MF"v[40:43],a[108:111],a[28:31],v[40:43]\n"
BL"v225,s[8:11],s43 offen offset:3072 sc1 lds\n"
MF"v[48:51],a[108:111],a[44:47],v[48:51]\n"
EX"v26,v26\n"
MF"v[48:51],a[104:107],a[40:43],v[48:51]\n"
"v_mov_b32 v237,s48\n"
"v_mov_b32 v227,v226\n"
MF"v[56:59],a[104:107],a[56:59],v[56:59]\n"
RD"a[64:67],v227 offset:16384\n"
PK"v24,v24,v25\n"
MF"v[56:59],a[108:111],a[60:63],v[56:59]\n"
SA"m0,s40,16384\n"
EX"v27,v27\n"
"v_cmp_lt_u32 vcc,511,v224\n"
MF"v[44:47],a[112:115],a[16:19],v[220:223]\n"
EX"v12,v12\n"
MF"v[44:47],a[116:119],a[20:23],v[44:47]\n"
RD"a[68:71],v227 offset:17408\n"
"v_cndmask_b32 v236,v236,v237,vcc\n"
MF"v[52:55],a[116:119],a[36:39],0\n"
EX"v13,v13\n"
PK"v25,v26,v27\n"
MF"v[52:55],a[112:115],a[32:35],v[52:55]\n"
BL"v225,s[8:11],s44 offen offset:0 sc1 lds\n"
MF"v[60:63],a[112:115],a[48:51],0\n"
SA"s40,s40,32768\n"
EX"v14,v14\n"
MF"v[60:63],a[116:119],a[52:55],v[60:63]\n"
WT"lgkmcnt(1)\n"
RD"a[72:75],v227 offset:18432\n"
PK"v10,v12,v13\n"
MF"v[44:47],a[120:123],a[24:27],v[44:47]\n"
"s_cmp_eq_u32 s40,s46\n"
EX"v15,v15\n"
MF"v[44:47],a[124:127],a[28:31],v[44:47]\n"
"s_cselect_b32 s40,s47,s40\n"
PK"v11,v14,v15\n"
MF"v[52:55],a[124:127],a[44:47],v[52:55]\n"
EX"v20,v20\n"
MF"v[52:55],a[120:123],a[40:43],v[52:55]\n"
RD"a[76:79],v227 offset:19456\n"
MF"v[60:63],a[120:123],a[56:59],v[60:63]\n"
EX"v21,v21\n"
MF"v[60:63],a[124:127],a[60:63],v[60:63]\n"
SA"s41,s41,32768\n"
BL"v225,s[8:11],s44 offen offset:1024 sc1 lds\n"
MF"v[80:83],a[64:67],v[64:67],v[80:83]\n"
"s_cmp_eq_u32 s41,163840\n"
EX"v22,v22\n"
MF"v[112:115],a[64:67],v[68:71],v[112:115]\n"
"s_cselect_b32 s41,0,s41\n"
RD"a[80:83],v227 offset:20480\n"
"v_add_u32 v226,s41,v224\n"
MF"v[144:147],a[64:67],v[72:75],v[144:147]\n"
EX"v23,v23\n"
PK"v18,v20,v21\n"
MF"v[176:179],a[64:67],v[76:79],v[176:179]\n"
WT"lgkmcnt(2)\n"
PK"v19,v22,v23\n"
MF"v[84:87],a[68:71],v[64:67],v[84:87]\n"
EX"v28,v28\n"
MF"v[116:119],a[68:71],v[68:71],v[116:119]\n"
RD"a[84:87],v227 offset:21504\n"
MF"v[148:151],a[68:71],v[72:75],v[148:151]\n"
EX"v29,v29\n"
MF"v[180:183],a[68:71],v[76:79],v[180:183]\n"
BL"v225,s[8:11],s44 offen offset:2048 sc1 lds\n"
MF"v[88:91],a[72:75],v[64:67],v[88:91]\n"
EX"v30,v30\n"
MF"v[120:123],a[72:75],v[68:71],v[120:123]\n"
RD"a[88:91],v227 offset:22528\n"
PK"v26,v28,v29\n"
MF"v[152:155],a[72:75],v[72:75],v[152:155]\n"
EX"v31,v31\n"
MF"v[184:187],a[72:75],v[76:79],v[184:187]\n"
WT"lgkmcnt(2)\n"
PK"v27,v30,v31\n"
MF"v[92:95],a[76:79],v[64:67],v[92:95]\n"
EX"v40,v40\n"
MF"v[124:127],a[76:79],v[68:71],v[124:127]\n"
RD"a[92:95],v227 offset:23552\n"
MF"v[156:159],a[76:79],v[72:75],v[156:159]\n"
EX"v41,v41\n"
MF"v[188:191],a[76:79],v[76:79],v[188:191]\n"
BL"v225,s[8:11],s44 offen offset:3072 sc1 lds\n"
MF"v[96:99],a[80:83],v[64:67],v[96:99]\n"
EX"v42,v42\n"
MF"v[128:131],a[80:83],v[68:71],v[128:131]\n"
RD"a[96:99],v227 offset:24576\n"
MF"v[160:163],a[80:83],v[72:75],v[160:163]\n"
EX"v43,v43\n"
MF"v[192:195],a[80:83],v[76:79],v[192:195]\n"
WT"lgkmcnt(2)\n"
RD"a[100:103],v227 offset:25600\n"
MF"v[100:103],a[84:87],v[64:67],v[100:103]\n"
EX"v48,v48\n"
MF"v[132:135],a[84:87],v[68:71],v[132:135]\n"
MF"v[164:167],a[84:87],v[72:75],v[164:167]\n"
EX"v49,v49\n"
MF"v[196:199],a[84:87],v[76:79],v[196:199]\n"
MF"v[104:107],a[88:91],v[64:67],v[104:107]\n"
EX"v50,v50\n"
MF"v[136:139],a[88:91],v[68:71],v[136:139]\n"
RD"a[104:107],v227 offset:26624\n"
MF"v[168:171],a[88:91],v[72:75],v[168:171]\n"
EX"v51,v51\n"
MF"v[200:203],a[88:91],v[76:79],v[200:203]\n"
WT"lgkmcnt(1)\n"
RD"a[108:111],v227 offset:27648\n"
MF"v[108:111],a[92:95],v[64:67],v[108:111]\n"
EX"v44,v44\n"
MF"v[140:143],a[92:95],v[68:71],v[140:143]\n"
RD"a[112:115],v227 offset:28672\n"
MF"v[172:175],a[92:95],v[72:75],v[172:175]\n"
EX"v45,v45\n"
MF"v[204:207],a[92:95],v[76:79],v[204:207]\n"
MF"a[132:135],a[128:131],v[64:67],a[132:135]\n"
EX"v46,v46\n"
MF"a[136:139],a[128:131],v[68:71],a[136:139]\n"
RD"a[116:119],v227 offset:29696\n"
PK"v68,v40,v41\n"
PK"v69,v42,v43\n"
MF"a[140:143],a[128:131],v[72:75],a[140:143]\n"
EX"v47,v47\n"
PK"v72,v48,v49\n"
MF"a[144:147],a[128:131],v[76:79],a[144:147]\n"
RD"a[120:123],v227 offset:30720\n"
PK"v73,v50,v51\n"
PK"v70,v44,v45\n"
MF"v[112:115],a[96:99],v[8:11],v[112:115]\n"
EX"v52,v52\n"
MF"v[144:147],a[96:99],v[16:19],v[144:147]\n"
RD"a[124:127],v227 offset:31744\n"
PK"v71,v46,v47\n"
MF"v[176:179],a[96:99],v[24:27],v[176:179]\n"
EX"v53,v53\n"
MF"v[116:119],a[100:103],v[8:11],v[116:119]\n"
"v_accvgpr_read_b32 v251,a132\n"
"v_subrev_u32 v253,0x30800000,v251\n"
MF"v[148:151],a[100:103],v[16:19],v[148:151]\n"
"v_cmp_le_u32 vcc,0x1f000000,v253\n"
"s_cmp_lg_u64 vcc,0\n"
"s_cbranch_scc1 L1014_%=\n"
"L1013_%=:\n"
MF"v[180:183],a[100:103],v[24:27],v[180:183]\n"
WT"lgkmcnt(2)\n"
EX"v54,v54\n"
MF"v[120:123],a[104:107],v[8:11],v[120:123]\n"
"v_rcp_f32 v251,v251\n"
PK"v74,v52,v53\n"
MF"v[152:155],a[104:107],v[16:19],v[152:155]\n"
WT"vmcnt(16) lgkmcnt(0)\n"
EX"v55,v55\n"
MF"v[184:187],a[104:107],v[24:27],v[184:187]\n"
"s_barrier\n"
MF"v[124:127],a[108:111],v[8:11],v[124:127]\n"
RD"a[64:67],v226 offset:0\n"
MU"v80,v251,v80\n"
MU"v81,v251,v81\n"
MF"v[156:159],a[108:111],v[16:19],v[156:159]\n"
MU"v82,v251,v82\n"
MU"v83,v251,v83\n"
PK"v75,v54,v55\n"
MF"v[188:191],a[108:111],v[24:27],v[188:191]\n"
MU"v84,v251,v84\n"
MU"v85,v251,v85\n"
MU"v86,v251,v86\n"
MF"v[128:131],a[112:115],v[8:11],v[128:131]\n"
MU"v87,v251,v87\n"
PK"v80,v80,v81\n"
PK"v81,v82,v83\n"
MF"v[160:163],a[112:115],v[16:19],v[160:163]\n"
RD"a[68:71],v226 offset:1024\n"
PK"v82,v84,v85\n"
PK"v83,v86,v87\n"
MF"v[192:195],a[112:115],v[24:27],v[192:195]\n"
BS"v[80:83],v224,s[8:11],s59 offen offset:0 sc1\n"
MU"v88,v251,v88\n"
MF"v[132:135],a[116:119],v[8:11],v[132:135]\n"
RD"a[72:75],v226 offset:2048\n"
MU"v89,v251,v89\n"
MU"v90,v251,v90\n"
MF"v[164:167],a[116:119],v[16:19],v[164:167]\n"
MU"v91,v251,v91\n"
MU"v92,v251,v92\n"
MU"v93,v251,v93\n"
MF"v[196:199],a[116:119],v[24:27],v[196:199]\n"
RD"a[76:79],v226 offset:3072\n"
MU"v94,v251,v94\n"
MU"v95,v251,v95\n"
MF"v[136:139],a[120:123],v[8:11],v[136:139]\n"
PK"v88,v88,v89\n"
PK"v89,v90,v91\n"
MF"v[168:171],a[120:123],v[16:19],v[168:171]\n"
PK"v90,v92,v93\n"
PK"v91,v94,v95\n"
MF"v[200:203],a[120:123],v[24:27],v[200:203]\n"
BS"v[88:91],v224,s[8:11],s59 offen offset:1024 sc1\n"
MU"v96,v251,v96\n"
MF"v[140:143],a[124:127],v[8:11],v[140:143]\n"
RD"a[80:83],v226 offset:4096\n"
MU"v97,v251,v97\n"
MU"v98,v251,v98\n"
MF"v[172:175],a[124:127],v[16:19],v[172:175]\n"
MU"v99,v251,v99\n"
MU"v100,v251,v100\n"
MU"v101,v251,v101\n"
MF"v[204:207],a[124:127],v[24:27],v[204:207]\n"
MU"v104,v251,v104\n"
MU"v105,v251,v105\n"
MU"v102,v251,v102\n"
MF"a[136:139],a[128:131],v[8:11],a[136:139]\n"
PK"v96,v96,v97\n"
MU"v106,v251,v106\n"
MU"v103,v251,v103\n"
MF"a[140:143],a[128:131],v[16:19],a[140:143]\n"
WT"lgkmcnt(3)\n"
MU"v107,v251,v107\n"
PK"v97,v98,v99\n"
PK"v98,v100,v101\n"
MF"a[144:147],a[128:131],v[24:27],a[144:147]\n"
MU"v108,v251,v108\n"
MU"v109,v251,v109\n"
PK"v99,v102,v103\n"
BS"v[96:99],v224,s[8:11],s59 offen offset:2048 sc1\n"
MU"v110,v251,v110\n"
MU"v111,v251,v111\n"
PK"v104,v104,v105\n"
PK"v105,v106,v107\n"
PK"v106,v108,v109\n"
PK"v107,v110,v111\n"
BS"v[104:107],v224,s[8:11],s59 offen offset:3072 sc1\n"
RD"a[84:87],v226 offset:5120\n"
RD"a[88:91],v226 offset:6144\n"
"s_cmp_eq_u32 s34,7\n"
"s_cbranch_scc1 LD2_%=\n"
MF"v[0:3],a[64:67],a[32:35],0\n"
"s_mov_b32 m0,s40\n"
EX"v56,v56\n"
MF"v[0:3],a[68:71],a[36:39],v[0:3]\n"
EX"v57,v57\n"
MF"v[8:11],a[68:71],a[52:55],0\n"
"s_lshl_b32 s50,s35,6\n"
RD"a[92:95],v226 offset:7168\n"
"v_readlane_b32 s43,v228,s42\n"
MF"v[8:11],a[64:67],a[48:51],v[8:11]\n"
WT"lgkmcnt(5)\n"
EX"v58,v58\n"
MF"v[0:3],a[72:75],a[40:43],v[0:3]\n"
WT"lgkmcnt(1)\n"
BL"v225,s[8:11],s43 offen offset:0 sc1 lds\n"
"v_readlane_b32 s44,v229,s42\n"
MF"v[0:3],a[76:79],a[44:47],v[0:3]\n"
SA"s50,s50,4352\n"
RD"a[96:99],v226 offset:8192\n"
PK"v76,v56,v57\n"
MF"v[8:11],a[76:79],a[60:63],v[8:11]\n"
SA"s42,s42,1\n"
RD"a[100:103],v226 offset:9216\n"
"v_lshrrev_b32 v237,1,v224\n"
MF"v[8:11],a[72:75],a[56:59],v[8:11]\n"
EX"v59,v59\n"
MF"v[4:7],a[80:83],a[32:35],0\n"
RD"a[104:107],v226 offset:10240\n"
"v_and_b32 v237,56,v237\n"
MF"v[4:7],a[84:87],a[36:39],v[4:7]\n"
EX"v60,v60\n"
PK"v77,v58,v59\n"
MF"v[12:15],a[84:87],a[52:55],0\n"
EX"v61,v61\n"
MF"v[12:15],a[80:83],a[48:51],v[12:15]\n"
RD"a[108:111],v226 offset:11264\n"
RD"a[112:115],v226 offset:12288\n"
MF"v[4:7],a[88:91],a[40:43],v[4:7]\n"
WT"lgkmcnt(4)\n"
BL"v225,s[8:11],s43 offen offset:1024 sc1 lds\n"
MF"v[4:7],a[92:95],a[44:47],v[4:7]\n"
EX"v62,v62\n"
PK"v78,v60,v61\n"
MF"v[12:15],a[92:95],a[60:63],v[12:15]\n"
RD"a[116:119],v226 offset:13312\n"
RD"a[120:123],v226 offset:14336\n"
MF"v[12:15],a[88:91],a[56:59],v[12:15]\n"
EX"v63,v63\n"
MF"v[16:19],a[96:99],a[32:35],0\n"
WT"lgkmcnt(4)\n"
EX"v0,v0\n"
MF"v[16:19],a[100:103],a[36:39],v[16:19]\n"
EX"v1,v1\n"
PK"v79,v62,v63\n"
MF"v[24:27],a[100:103],a[52:55],0\n"
RD"a[124:127],v226 offset:15360\n"
PK"v0,v0,v1\n"
MF"v[24:27],a[96:99],a[48:51],v[24:27]\n"
BL"v225,s[8:11],s43 offen offset:2048 sc1 lds\n"
MF"v[16:19],a[104:107],a[40:43],v[16:19]\n"
WT"lgkmcnt(3)\n"
EX"v2,v2\n"
MF"v[16:19],a[108:111],a[44:47],v[16:19]\n"
"s_lshl_b32 s56,s34,1\n"
RD"a[64:67],v226 offset:16384\n"
RD"a[68:71],v226 offset:17408\n"
MF"v[24:27],a[108:111],a[60:63],v[24:27]\n"
SA"s41,s41,32768\n"
EX"v3,v3\n"
MF"v[24:27],a[104:107],a[56:59],v[24:27]\n"
"s_cmp_eq_u32 s41,163840\n"
RD"a[72:75],v226 offset:18432\n"
PK"v1,v2,v3\n"
MF"v[20:23],a[112:115],a[32:35],0\n"
WT"lgkmcnt(4)\n"
EX"v8,v8\n"
MF"v[20:23],a[116:119],a[36:39],v[20:23]\n"
"s_cselect_b32 s41,0,s41\n"
EX"v9,v9\n"
"v_add_u32 v227,s41,v224\n"
MF"v[28:31],a[116:119],a[52:55],0\n"
"s_sub_u32 s56,12,s56\n"
BL"v225,s[8:11],s43 offen offset:3072 sc1 lds\n"
MF"v[28:31],a[112:115],a[48:51],v[28:31]\n"
"s_sub_u32 s22,s56,2\n"
RD"a[76:79],v226 offset:19456\n"
PK"v8,v8,v9\n"
MF"v[20:23],a[120:123],a[40:43],v[20:23]\n"
WT"lgkmcnt(2)\n"
EX"v10,v10\n"
MF"v[20:23],a[124:127],a[44:47],v[20:23]\n"
"s_min_u32 s56,s56,1\n"
RD"a[80:83],v226 offset:20480\n"
RD"a[84:87],v226 offset:21504\n"
EX"v11,v11\n"
MF"v[28:31],a[124:127],a[60:63],v[28:31]\n"
"s_mov_b32 s57,s61\n"
RD"a[88:91],v226 offset:22528\n"
PK"v9,v10,v11\n"
MF"v[28:31],a[120:123],a[56:59],v[28:31]\n"
SA"m0,s40,16384\n"
EX"v4,v4\n"
MF"v[32:35],a[64:67],a[32:35],0\n"
"buffer_load_dwordx2 v[238:239],v237,s[8:11],s50 offen sc1\n"
MF"v[32:35],a[68:71],a[36:39],v[32:35]\n"
EX"v5,v5\n"
MF"v[40:43],a[68:71],a[52:55],0\n"
SA"s40,s40,32768\n"
BL"v225,s[8:11],s44 offen offset:0 sc1 lds\n"
MF"v[40:43],a[64:67],a[48:51],v[40:43]\n"
WT"lgkmcnt(3)\n"
RD"a[92:95],v226 offset:23552\n"
PK"v2,v4,v5\n"
EX"v6,v6\n"
MF"v[32:35],a[72:75],a[40:43],v[32:35]\n"
"s_cmp_eq_u32 s40,s46\n"
RD"a[96:99],v226 offset:24576\n"
RD"a[100:103],v226 offset:25600\n"
MF"v[32:35],a[76:79],a[44:47],v[32:35]\n"
"s_cselect_b32 s40,s47,s40\n"
EX"v7,v7\n"
MF"v[40:43],a[76:79],a[60:63],v[40:43]\n"
RD"a[104:107],v226 offset:26624\n"
PK"v3,v6,v7\n"
MF"v[40:43],a[72:75],a[56:59],v[40:43]\n"
WT"lgkmcnt(4)\n"
EX"v12,v12\n"
MF"v[36:39],a[80:83],a[32:35],0\n"
EX"v13,v13\n"
MF"v[36:39],a[84:87],a[36:39],v[36:39]\n"
BL"v225,s[8:11],s44 offen offset:1024 sc1 lds\n"
MF"v[44:47],a[84:87],a[52:55],0\n"
EX"v14,v14\n"
MF"v[44:47],a[80:83],a[48:51],v[44:47]\n"
RD"a[108:111],v226 offset:27648\n"
RD"a[112:115],v226 offset:28672\n"
PK"v10,v12,v13\n"
MF"v[36:39],a[88:91],a[40:43],v[36:39]\n"
WT"lgkmcnt(3)\n"
EX"v15,v15\n"
MF"v[36:39],a[92:95],a[44:47],v[36:39]\n"
RD"a[116:119],v226 offset:29696\n"
RD"a[120:123],v226 offset:30720\n"
MF"v[44:47],a[92:95],a[60:63],v[44:47]\n"
EX"v16,v16\n"
PK"v11,v14,v15\n"
MF"v[44:47],a[88:91],a[56:59],v[44:47]\n"
EX"v17,v17\n"
MF"v[48:51],a[96:99],a[32:35],0\n"
BL"v225,s[8:11],s44 offen offset:2048 sc1 lds\n"
MF"v[48:51],a[100:103],a[36:39],v[48:51]\n"
WT"vmcnt(16)\n"
EX"v18,v18\n"
MF"v[56:59],a[100:103],a[52:55],0\n"
RD"a[124:127],v226 offset:31744\n"
PK"v16,v16,v17\n"
MF"v[56:59],a[96:99],a[48:51],v[56:59]\n"
WT"lgkmcnt(3)\n"
EX"v19,v19\n"
MF"v[48:51],a[104:107],a[40:43],v[48:51]\n"
"s_barrier\n"
MF"v[48:51],a[108:111],a[44:47],v[48:51]\n"
RD"a[96:99],v227 offset:8192\n"
PK"v17,v18,v19\n"
MF"v[56:59],a[108:111],a[60:63],v[56:59]\n"
EX"v24,v24\n"
MF"v[56:59],a[104:107],a[56:59],v[56:59]\n"
EX"v25,v25\n"
MF"v[52:55],a[112:115],a[32:35],0\n"
WT"lgkmcnt(2)\n"
RD"a[100:103],v227 offset:9216\n"
"v_readlane_b32 s43,v228,s42\n"
MF"v[52:55],a[116:119],a[36:39],v[52:55]\n"
BL"v225,s[8:11],s44 offen offset:3072 sc1 lds\n"
EX"v26,v26\n"
MF"v[60:63],a[116:119],a[52:55],0\n"
EX"v27,v27\n"
PK"v24,v24,v25\n"
MF"v[60:63],a[112:115],a[48:51],v[60:63]\n"
"s_mov_b32 m0,s40\n"
RD"a[104:107],v227 offset:10240\n"
RD"a[108:111],v227 offset:11264\n"
MF"v[52:55],a[120:123],a[40:43],v[52:55]\n"
WT"lgkmcnt(3)\n"
EX"v20,v20\n"
MF"v[52:55],a[124:127],a[44:47],v[52:55]\n"
EX"v21,v21\n"
PK"v25,v26,v27\n"
MF"v[60:63],a[124:127],a[60:63],v[60:63]\n"
RD"a[112:115],v227 offset:12288\n"
"v_readlane_b32 s44,v229,s42\n"
MF"v[60:63],a[120:123],a[56:59],v[60:63]\n"
SA"s41,s41,32768\n"
EX"v22,v22\n"
MF"v[112:115],a[96:99],v[68:71],v[112:115]\n"
"s_cmp_eq_u32 s41,163840\n"
BL"v225,s[8:11],s43 offen offset:0 sc1 lds\n"
MF"v[144:147],a[96:99],v[72:75],v[144:147]\n"
"s_cselect_b32 s41,0,s41\n"
EX"v23,v23\n"
PK"v18,v20,v21\n"
MF"v[176:179],a[96:99],v[76:79],v[176:179]\n"
WT"lgkmcnt(1)\n"
RD"a[116:119],v227 offset:13312\n"
"v_add_u32 v226,s41,v224\n"
MF"v[116:119],a[100:103],v[68:71],v[116:119]\n"
SA"s42,s42,1\n"
EX"v28,v28\n"
MF"v[148:151],a[100:103],v[72:75],v[148:151]\n"
RD"a[120:123],v227 offset:14336\n"
PK"v19,v22,v23\n"
MF"v[180:183],a[100:103],v[76:79],v[180:183]\n"
EX"v29,v29\n"
MF"v[120:123],a[104:107],v[68:71],v[120:123]\n"
EX"v30,v30\n"
MF"v[152:155],a[104:107],v[72:75],v[152:155]\n"
BL"v225,s[8:11],s43 offen offset:1024 sc1 lds\n"
MF"v[184:187],a[104:107],v[76:79],v[184:187]\n"
EX"v31,v31\n"
PK"v26,v28,v29\n"
MF"v[124:127],a[108:111],v[68:71],v[124:127]\n"
RD"a[124:127],v227 offset:15360\n"
PK"v27,v30,v31\n"
MF"v[156:159],a[108:111],v[72:75],v[156:159]\n"
EX"v32,v32\n"
MF"v[188:191],a[108:111],v[76:79],v[188:191]\n"
WT"lgkmcnt(1)\n"
MF"v[128:131],a[112:115],v[68:71],v[128:131]\n"
EX"v33,v33\n"
MF"v[160:163],a[112:115],v[72:75],v[160:163]\n"
RD"a[64:67],v227 offset:16384\n"
PK"v64,v32,v33\n"
MF"v[192:195],a[112:115],v[76:79],v[192:195]\n"
BL"v225,s[8:11],s43 offen offset:2048 sc1 lds\n"
MF"v[132:135],a[116:119],v[68:71],v[132:135]\n"
EX"v34,v34\n"
MF"v[164:167],a[116:119],v[72:75],v[164:167]\n"
EX"v35,v35\n"
MF"v[196:199],a[116:119],v[76:79],v[196:199]\n"
EX"v40,v40\n"
MF"v[136:139],a[120:123],v[68:71],v[136:139]\n"
RD"a[68:71],v227 offset:24576\n"
PK"v65,v34,v35\n"
MF"v[168:171],a[120:123],v[72:75],v[168:171]\n"
EX"v41,v41\n"
MF"v[200:203],a[120:123],v[76:79],v[200:203]\n"
WT"lgkmcnt(1)\n"
RD"a[72:75],v227 offset:17408\n"
RD"a[76:79],v227 offset:25600\n"
MF"v[140:143],a[124:127],v[68:71],v[140:143]\n"
WT"vmcnt(7)\n"
"v_cmp_eq_u64 vcc,s[28:29],v[238:239]\n"
"s_cmp_eq_u64 vcc,exec\n"
"s_cbranch_scc0 L1019_%=\n"
"L1020_%=:\n"
MF"v[172:175],a[124:127],v[72:75],v[172:175]\n"
EX"v42,v42\n"
MF"v[204:207],a[124:127],v[76:79],v[204:207]\n"
RD"a[80:83],v227 offset:18432\n"
MF"a[136:139],a[128:131],v[68:71],a[136:139]\n"
BL"v225,s[8:11],s43 offen offset:3072 sc1 lds\n"
PK"v68,v40,v41\n"
MF"a[140:143],a[128:131],v[72:75],a[140:143]\n"
SA"m0,s40,16384\n"
EX"v43,v43\n"
MF"a[144:147],a[128:131],v[76:79],a[144:147]\n"
EX"v36,v36\n"
MF"v[144:147],a[64:67],v[0:3],v[144:147]\n"
WT"lgkmcnt(1)\n"
RD"a[84:87],v227 offset:26624\n"
RD"a[88:91],v227 offset:19456\n"
PK"v69,v42,v43\n"
MF"v[144:147],a[68:71],v[16:19],v[144:147]\n"
EX"v37,v37\n"
MF"v[176:179],a[68:71],v[24:27],v[176:179]\n"
SA"s51,s59,16384\n"
RD"a[92:95],v227 offset:27648\n"
RD"a[96:99],v227 offset:20480\n"
MF"v[176:179],a[64:67],v[8:11],v[176:179]\n"
SA"s40,s40,32768\n"
EX"v38,v38\n"
MF"v[148:151],a[72:75],v[0:3],v[148:151]\n"
"s_cmp_eq_u32 s40,s46\n"
BL"v225,s[8:11],s44 offen offset:0 sc1 lds\n"
PK"v66,v36,v37\n"
MF"v[148:151],a[76:79],v[16:19],v[148:151]\n"
"s_cselect_b32 s40,s47,s40\n"
EX"v39,v39\n"
"v_accvgpr_read_b32 v252,a136\n"
MF"v[180:183],a[76:79],v[24:27],v[180:183]\n"
EX"v44,v44\n"
"v_subrev_u32 v253,0x30800000,v252\n"
MF"v[180:183],a[72:75],v[8:11],v[180:183]\n"
WT"lgkmcnt(2)\n"
"v_cmp_le_u32 vcc,0x1f000000,v253\n"
"s_cmp_lg_u64 vcc,0\n"
"s_cbranch_scc1 L1023_%=\n"
"L1022_%=:\n"
MF"v[152:155],a[80:83],v[0:3],v[152:155]\n"
"v_rcp_f32 v252,v252\n"
PK"v67,v38,v39\n"
MF"v[152:155],a[84:87],v[16:19],v[152:155]\n"
RD"a[100:103],v227 offset:28672\n"
RD"a[104:107],v227 offset:21504\n"
EX"v45,v45\n"
MF"v[184:187],a[84:87],v[24:27],v[184:187]\n"
RD"a[108:111],v227 offset:29696\n"
RD"a[112:115],v227 offset:22528\n"
MU"v112,v252,v112\n"
MF"v[184:187],a[80:83],v[8:11],v[184:187]\n"
EX"v46,v46\n"
MU"v113,v252,v113\n"
MF"v[156:159],a[88:91],v[0:3],v[156:159]\n"
WT"lgkmcnt(4)\n"
BL"v225,s[8:11],s44 offen offset:1024 sc1 lds\n"
MU"v114,v252,v114\n"
MF"v[156:159],a[92:95],v[16:19],v[156:159]\n"
EX"v47,v47\n"
MU"v115,v252,v115\n"
MF"v[188:191],a[92:95],v[24:27],v[188:191]\n"
EX"v48,v48\n"
MU"v116,v252,v116\n"
MF"v[188:191],a[88:91],v[8:11],v[188:191]\n"
EX"v49,v49\n"
PK"v70,v44,v45\n"
MF"v[160:163],a[96:99],v[0:3],v[160:163]\n"
WT"lgkmcnt(2)\n"
RD"a[116:119],v227 offset:30720\n"
RD"a[120:123],v227 offset:23552\n"
MU"v117,v252,v117\n"
MF"v[160:163],a[100:103],v[16:19],v[160:163]\n"
EX"v50,v50\n"
MU"v118,v252,v118\n"
MF"v[192:195],a[100:103],v[24:27],v[192:195]\n"
RD"a[124:127],v227 offset:31744\n"
MU"v119,v252,v119\n"
PK"v112,v112,v113\n"
MF"v[192:195],a[96:99],v[8:11],v[192:195]\n"
BL"v225,s[8:11],s44 offen offset:2048 sc1 lds\n"
PK"v113,v114,v115\n"
MF"v[164:167],a[104:107],v[0:3],v[164:167]\n"
WT"lgkmcnt(3)\n"
EX"v51,v51\n"
PK"v114,v116,v117\n"
MF"v[164:167],a[108:111],v[16:19],v[164:167]\n"
EX"v52,v52\n"
PK"v115,v118,v119\n"
MF"v[196:199],a[108:111],v[24:27],v[196:199]\n"
BS"v[112:115],v224,s[8:11],s51 offen offset:0 sc1\n"
PK"v71,v46,v47\n"
MF"v[196:199],a[104:107],v[8:11],v[196:199]\n"
"s_barrier\n"
MF"v[168:171],a[112:115],v[0:3],v[168:171]\n"
WT"lgkmcnt(1)\n"
EX"v53,v53\n"
MU"v120,v252,v120\n"
MF"v[168:171],a[116:119],v[16:19],v[168:171]\n"
RD"a[64:67],v226 offset:0\n"
RD"a[68:71],v226 offset:1024\n"
MU"v121,v252,v121\n"
MF"v[200:203],a[116:119],v[24:27],v[200:203]\n"
EX"v54,v54\n"
MU"v122,v252,v122\n"
MF"v[200:203],a[112:115],v[8:11],v[200:203]\n"
BL"v225,s[8:11],s44 offen offset:3072 sc1 lds\n"
MU"v123,v252,v123\n"
MF"v[172:175],a[120:123],v[0:3],v[172:175]\n"
WT"lgkmcnt(2)\n"
EX"v55,v55\n"
MU"v124,v252,v124\n"
MF"v[172:175],a[124:127],v[16:19],v[172:175]\n"
RD"a[72:75],v226 offset:2048\n"
PK"v72,v48,v49\n"
MU"v125,v252,v125\n"
MF"v[204:207],a[124:127],v[24:27],v[204:207]\n"
RD"a[76:79],v226 offset:3072\n"
MU"v126,v252,v126\n"
MU"v127,v252,v127\n"
MF"v[204:207],a[120:123],v[8:11],v[204:207]\n"
PK"v120,v120,v121\n"
PK"v121,v122,v123\n"
PK"v122,v124,v125\n"
MF"a[140:143],a[128:131],v[0:3],a[140:143]\n"
MU"v128,v252,v128\n"
MU"v129,v252,v129\n"
MU"v136,v252,v136\n"
MF"a[140:143],a[128:131],v[16:19],a[140:143]\n"
MU"v137,v252,v137\n"
MU"v130,v252,v130\n"
MU"v131,v252,v131\n"
MF"a[144:147],a[128:131],v[24:27],a[144:147]\n"
WT"lgkmcnt(2)\n"
PK"v128,v128,v129\n"
MU"v138,v252,v138\n"
MU"v139,v252,v139\n"
MF"a[144:147],a[128:131],v[8:11],a[144:147]\n"
PK"v136,v136,v137\n"
MU"v132,v252,v132\n"
MU"v133,v252,v133\n"
PK"v73,v50,v51\n"
PK"v74,v52,v53\n"
PK"v75,v54,v55\n"
PK"v123,v126,v127\n"
BS"v[120:123],v224,s[8:11],s51 offen offset:1024 sc1\n"
MU"v134,v252,v134\n"
MU"v135,v252,v135\n"
PK"v129,v130,v131\n"
PK"v130,v132,v133\n"
PK"v131,v134,v135\n"
BS"v[128:131],v224,s[8:11],s51 offen offset:2048 sc1\n"
MU"v140,v252,v140\n"
MU"v141,v252,v141\n"
MU"v142,v252,v142\n"
MU"v143,v252,v143\n"
PK"v137,v138,v139\n"
PK"v138,v140,v141\n"
PK"v139,v142,v143\n"
BS"v[136:139],v224,s[8:11],s51 offen offset:3072 sc1\n"
RD"a[80:83],v226 offset:4096\n"
RD"a[84:87],v226 offset:5120\n"
RD"a[88:91],v226 offset:6144\n"
".p2align 6\n"
"LP2_%=:\n"
MF"v[0:3],a[64:67],a[32:35],0\n"
EX"v56,v56\n"
MF"v[0:3],a[68:71],a[36:39],v[0:3]\n"
RD"a[92:95],v226 offset:7168\n"
"v_readlane_b32 s43,v228,s42\n"
MF"v[8:11],a[68:71],a[52:55],0\n"
EX"v57,v57\n"
MF"v[8:11],a[64:67],a[48:51],v[8:11]\n"
WT"lgkmcnt(5)\n"
RD"a[96:99],v226 offset:8192\n"
RD"a[100:103],v226 offset:9216\n"
MF"v[0:3],a[72:75],a[40:43],v[0:3]\n"
WT"lgkmcnt(3)\n"
EX"v58,v58\n"
MF"v[0:3],a[76:79],a[44:47],v[0:3]\n"
"s_mov_b32 m0,s40\n"
RD"a[104:107],v226 offset:10240\n"
"v_readlane_b32 s44,v229,s42\n"
MF"v[8:11],a[76:79],a[60:63],v[8:11]\n"
EX"v59,v59\n"
MF"v[8:11],a[72:75],a[56:59],v[8:11]\n"
SA"s42,s42,1\n"
BL"v225,s[8:11],s43 offen offset:0 sc1 lds\n"
MF"v[4:7],a[80:83],a[32:35],0\n"
EX"v60,v60\n"
MF"v[4:7],a[84:87],a[36:39],v[4:7]\n"
RD"a[108:111],v226 offset:11264\n"
PK"v76,v56,v57\n"
MF"v[12:15],a[84:87],a[52:55],0\n"
EX"v61,v61\n"
MF"v[12:15],a[80:83],a[48:51],v[12:15]\n"
RD"a[112:115],v226 offset:12288\n"
RD"a[116:119],v226 offset:13312\n"
MF"v[4:7],a[88:91],a[40:43],v[4:7]\n"
WT"lgkmcnt(3)\n"
EX"v62,v62\n"
MF"v[4:7],a[92:95],a[44:47],v[4:7]\n"
RD"a[120:123],v226 offset:14336\n"
PK"v77,v58,v59\n"
MF"v[12:15],a[92:95],a[60:63],v[12:15]\n"
EX"v63,v63\n"
MF"v[12:15],a[88:91],a[56:59],v[12:15]\n"
BL"v225,s[8:11],s43 offen offset:1024 sc1 lds\n"
MF"v[16:19],a[96:99],a[32:35],0\n"
EX"v0,v0\n"
MF"v[16:19],a[100:103],a[36:39],v[16:19]\n"
RD"a[124:127],v226 offset:15360\n"
PK"v78,v60,v61\n"
MF"v[24:27],a[100:103],a[52:55],0\n"
EX"v1,v1\n"
MF"v[24:27],a[96:99],a[48:51],v[24:27]\n"
RD"a[64:67],v226 offset:16384\n"
RD"a[68:71],v226 offset:17408\n"
MF"v[16:19],a[104:107],a[40:43],v[16:19]\n"
WT"lgkmcnt(3)\n"
EX"v2,v2\n"
MF"v[16:19],a[108:111],a[44:47],v[16:19]\n"
SA"s41,s41,32768\n"
RD"a[72:75],v226 offset:18432\n"
PK"v79,v62,v63\n"
MF"v[24:27],a[108:111],a[60:63],v[24:27]\n"
"s_cmp_eq_u32 s41,163840\n"
EX"v3,v3\n"
MF"v[24:27],a[104:107],a[56:59],v[24:27]\n"
"s_cselect_b32 s41,0,s41\n"
BL"v225,s[8:11],s43 offen offset:2048 sc1 lds\n"
MF"v[20:23],a[112:115],a[32:35],0\n"
EX"v8,v8\n"
MF"v[20:23],a[116:119],a[36:39],v[20:23]\n"
RD"a[76:79],v226 offset:19456\n"
"v_add_u32 v227,s41,v224\n"
MF"v[28:31],a[116:119],a[52:55],0\n"
EX"v9,v9\n"
MF"v[28:31],a[112:115],a[48:51],v[28:31]\n"
RD"a[80:83],v226 offset:20480\n"
RD"a[84:87],v226 offset:21504\n"
MF"v[20:23],a[120:123],a[40:43],v[20:23]\n"
WT"lgkmcnt(3)\n"
EX"v10,v10\n"
MF"v[20:23],a[124:127],a[44:47],v[20:23]\n"
RD"a[88:91],v226 offset:22528\n"
PK"v0,v0,v1\n"
MF"v[28:31],a[124:127],a[60:63],v[28:31]\n"
EX"v11,v11\n"
MF"v[28:31],a[120:123],a[56:59],v[28:31]\n"
BL"v225,s[8:11],s43 offen offset:3072 sc1 lds\n"
MF"v[32:35],a[64:67],a[32:35],0\n"
EX"v4,v4\n"
MF"v[32:35],a[68:71],a[36:39],v[32:35]\n"
RD"a[92:95],v226 offset:23552\n"
PK"v1,v2,v3\n"
MF"v[40:43],a[68:71],a[52:55],0\n"
EX"v5,v5\n"
MF"v[40:43],a[64:67],a[48:51],v[40:43]\n"
SA"m0,s40,16384\n"
RD"a[96:99],v226 offset:24576\n"
RD"a[100:103],v226 offset:25600\n"
MF"v[32:35],a[72:75],a[40:43],v[32:35]\n"
WT"lgkmcnt(3)\n"
EX"v6,v6\n"
MF"v[32:35],a[76:79],a[44:47],v[32:35]\n"
RD"a[104:107],v226 offset:26624\n"
PK"v8,v8,v9\n"
MF"v[40:43],a[76:79],a[60:63],v[40:43]\n"
EX"v7,v7\n"
MF"v[40:43],a[72:75],a[56:59],v[40:43]\n"
BL"v225,s[8:11],s44 offen offset:0 sc1 lds\n"
MF"v[36:39],a[80:83],a[32:35],0\n"
SA"s40,s40,32768\n"
EX"v12,v12\n"
MF"v[36:39],a[84:87],a[36:39],v[36:39]\n"
"s_cmp_eq_u32 s40,s46\n"
RD"a[108:111],v226 offset:27648\n"
PK"v9,v10,v11\n"
MF"v[44:47],a[84:87],a[52:55],0\n"
"s_cselect_b32 s40,s47,s40\n"
EX"v13,v13\n"
MF"v[44:47],a[80:83],a[48:51],v[44:47]\n"
RD"a[112:115],v226 offset:28672\n"
RD"a[116:119],v226 offset:29696\n"
MF"v[36:39],a[88:91],a[40:43],v[36:39]\n"
WT"lgkmcnt(3)\n"
EX"v14,v14\n"
MF"v[36:39],a[92:95],a[44:47],v[36:39]\n"
RD"a[120:123],v226 offset:30720\n"
PK"v2,v4,v5\n"
MF"v[44:47],a[92:95],a[60:63],v[44:47]\n"
EX"v15,v15\n"
MF"v[44:47],a[88:91],a[56:59],v[44:47]\n"
BL"v225,s[8:11],s44 offen offset:1024 sc1 lds\n"
MF"v[48:51],a[96:99],a[32:35],0\n"
EX"v16,v16\n"
MF"v[48:51],a[100:103],a[36:39],v[48:51]\n"
WT"vmcnt(14)\n"
RD"a[124:127],v226 offset:31744\n"
PK"v3,v6,v7\n"
MF"v[56:59],a[100:103],a[52:55],0\n"
"s_barrier\n"
MF"v[56:59],a[96:99],a[48:51],v[56:59]\n"
RD"a[64:67],v227 offset:0\n"
PK"v10,v12,v13\n"
MF"v[48:51],a[104:107],a[40:43],v[48:51]\n"
WT"lgkmcnt(2)\n"
RD"a[68:71],v227 offset:8192\n"
PK"v11,v14,v15\n"
MF"v[48:51],a[108:111],a[44:47],v[48:51]\n"
EX"v17,v17\n"
MF"v[56:59],a[108:111],a[60:63],v[56:59]\n"
RD"a[72:75],v227 offset:1024\n"
PK"v16,v16,v17\n"
MF"v[56:59],a[104:107],a[56:59],v[56:59]\n"
EX"v18,v18\n"
MF"v[52:55],a[112:115],a[32:35],0\n"
BL"v225,s[8:11],s44 offen offset:2048 sc1 lds\n"
MF"v[52:55],a[116:119],a[36:39],v[52:55]\n"
EX"v19,v19\n"
MF"v[60:63],a[116:119],a[52:55],0\n"
RD"a[76:79],v227 offset:9216\n"
PK"v17,v18,v19\n"
MF"v[60:63],a[112:115],a[48:51],v[60:63]\n"
EX"v24,v24\n"
MF"v[52:55],a[120:123],a[40:43],v[52:55]\n"
WT"lgkmcnt(2)\n"
RD"a[80:83],v227 offset:2048\n"
RD"a[84:87],v227 offset:10240\n"
MF"v[52:55],a[124:127],a[44:47],v[52:55]\n"
EX"v25,v25\n"
MF"v[60:63],a[124:127],a[60:63],v[60:63]\n"
RD"a[88:91],v227 offset:3072\n"
PK"v24,v24,v25\n"
MF"v[60:63],a[120:123],a[56:59],v[60:63]\n"
SA"s41,s41,32768\n"
EX"v26,v26\n"
MF"v[144:147],a[64:67],v[64:67],v[144:147]\n"
"s_cmp_eq_u32 s41,163840\n"
BL"v225,s[8:11],s44 offen offset:3072 sc1 lds\n"
MF"v[144:147],a[68:71],v[72:75],v[144:147]\n"
"s_cselect_b32 s41,0,s41\n"
EX"v27,v27\n"
MF"v[176:179],a[68:71],v[76:79],v[176:179]\n"
RD"a[92:95],v227 offset:11264\n"
"v_add_u32 v226,s41,v224\n"
MF"v[176:179],a[64:67],v[68:71],v[176:179]\n"
WT"lgkmcnt(2)\n"
EX"v20,v20\n"
MF"v[148:151],a[72:75],v[64:67],v[148:151]\n"
"s_mov_b32 m0,s40\n"
RD"a[96:99],v227 offset:4096\n"
RD"a[100:103],v227 offset:12288\n"
MF"v[148:151],a[76:79],v[72:75],v[148:151]\n"
EX"v21,v21\n"
MF"v[180:183],a[76:79],v[76:79],v[180:183]\n"
RD"a[104:107],v227 offset:5120\n"
"v_readlane_b32 s43,v228,s42\n"
MF"v[180:183],a[72:75],v[68:71],v[180:183]\n"
EX"v22,v22\n"
MF"v[152:155],a[80:83],v[64:67],v[152:155]\n"
EX"v23,v23\n"
MF"v[152:155],a[84:87],v[72:75],v[152:155]\n"
BL"v225,s[8:11],s43 offen offset:0 sc1 lds\n"
MF"v[184:187],a[84:87],v[76:79],v[184:187]\n"
RD"a[108:111],v227 offset:13312\n"
"v_readlane_b32 s44,v229,s42\n"
MF"v[184:187],a[80:83],v[68:71],v[184:187]\n"
WT"lgkmcnt(2)\n"
EX"v28,v28\n"
MF"v[156:159],a[88:91],v[64:67],v[156:159]\n"
SA"s42,s42,1\n"
RD"a[112:115],v227 offset:6144\n"
RD"a[116:119],v227 offset:14336\n"
MF"v[156:159],a[92:95],v[72:75],v[156:159]\n"
EX"v29,v29\n"
MF"v[188:191],a[92:95],v[76:79],v[188:191]\n"
RD"a[120:123],v227 offset:7168\n"
PK"v25,v26,v27\n"
MF"v[188:191],a[88:91],v[68:71],v[188:191]\n"
EX"v30,v30\n"
MF"v[160:163],a[96:99],v[64:67],v[160:163]\n"
BL"v225,s[8:11],s43 offen offset:1024 sc1 lds\n"
MF"v[160:163],a[100:103],v[72:75],v[160:163]\n"
EX"v31,v31\n"
MF"v[192:195],a[100:103],v[76:79],v[192:195]\n"
RD"a[124:127],v227 offset:15360\n"
PK"v18,v20,v21\n"
MF"v[192:195],a[96:99],v[68:71],v[192:195]\n"
WT"lgkmcnt(2)\n"
EX"v32,v32\n"
MF"v[164:167],a[104:107],v[64:67],v[164:167]\n"
EX"v33,v33\n"
MF"v[164:167],a[108:111],v[72:75],v[164:167]\n"
RD"a[64:67],v227 offset:16384\n"
PK"v19,v22,v23\n"
MF"v[196:199],a[108:111],v[76:79],v[196:199]\n"
EX"v34,v34\n"
MF"v[196:199],a[104:107],v[68:71],v[196:199]\n"
BL"v225,s[8:11],s43 offen offset:2048 sc1 lds\n"
MF"v[168:171],a[112:115],v[64:67],v[168:171]\n"
EX"v35,v35\n"
MF"v[168:171],a[116:119],v[72:75],v[168:171]\n"
RD"a[68:71],v227 offset:24576\n"
PK"v26,v28,v29\n"
MF"v[200:203],a[116:119],v[76:79],v[200:203]\n"
EX"v40,v40\n"
MF"v[200:203],a[112:115],v[68:71],v[200:203]\n"
WT"lgkmcnt(2)\n"
RD"a[72:75],v227 offset:17408\n"
RD"a[76:79],v227 offset:25600\n"
MF"v[172:175],a[120:123],v[64:67],v[172:175]\n"
EX"v41,v41\n"
MF"v[172:175],a[124:127],v[72:75],v[172:175]\n"
RD"a[80:83],v227 offset:18432\n"
PK"v27,v30,v31\n"
MF"v[204:207],a[124:127],v[76:79],v[204:207]\n"
EX"v42,v42\n"
MF"v[204:207],a[120:123],v[68:71],v[204:207]\n"
BL"v225,s[8:11],s43 offen offset:3072 sc1 lds\n"
MF"a[140:143],a[128:131],v[64:67],a[140:143]\n"
EX"v43,v43\n"
MF"a[140:143],a[128:131],v[72:75],a[140:143]\n"
RD"a[84:87],v227 offset:26624\n"
PK"v64,v32,v33\n"
MF"a[144:147],a[128:131],v[76:79],a[144:147]\n"
EX"v36,v36\n"
MF"a[144:147],a[128:131],v[68:71],a[144:147]\n"
WT"lgkmcnt(2)\n"
RD"a[88:91],v227 offset:19456\n"
RD"a[92:95],v227 offset:27648\n"
MF"v[144:147],a[64:67],v[0:3],v[144:147]\n"
SA"m0,s40,16384\n"
EX"v37,v37\n"
MF"v[144:147],a[68:71],v[16:19],v[144:147]\n"
RD"a[96:99],v227 offset:20480\n"
PK"v65,v34,v35\n"
MF"v[176:179],a[68:71],v[24:27],v[176:179]\n"
EX"v38,v38\n"
MF"v[176:179],a[64:67],v[8:11],v[176:179]\n"
BL"v225,s[8:11],s44 offen offset:0 sc1 lds\n"
MF"v[148:151],a[72:75],v[0:3],v[148:151]\n"
SA"s40,s40,32768\n"
EX"v39,v39\n"
MF"v[148:151],a[76:79],v[16:19],v[148:151]\n"
"s_cmp_eq_u32 s40,s46\n"
RD"a[100:103],v227 offset:28672\n"
PK"v68,v40,v41\n"
MF"v[180:183],a[76:79],v[24:27],v[180:183]\n"
"s_cselect_b32 s40,s47,s40\n"
EX"v44,v44\n"
MF"v[180:183],a[72:75],v[8:11],v[180:183]\n"
WT"lgkmcnt(2)\n"
RD"a[104:107],v227 offset:21504\n"
RD"a[108:111],v227 offset:29696\n"
MF"v[152:155],a[80:83],v[0:3],v[152:155]\n"
EX"v45,v45\n"
MF"v[152:155],a[84:87],v[16:19],v[152:155]\n"
RD"a[112:115],v227 offset:22528\n"
PK"v69,v42,v43\n"
MF"v[184:187],a[84:87],v[24:27],v[184:187]\n"
EX"v46,v46\n"
MF"v[184:187],a[80:83],v[8:11],v[184:187]\n"
BL"v225,s[8:11],s44 offen offset:1024 sc1 lds\n"
MF"v[156:159],a[88:91],v[0:3],v[156:159]\n"
EX"v47,v47\n"
MF"v[156:159],a[92:95],v[16:19],v[156:159]\n"
RD"a[116:119],v227 offset:30720\n"
PK"v66,v36,v37\n"
MF"v[188:191],a[92:95],v[24:27],v[188:191]\n"
EX"v48,v48\n"
MF"v[188:191],a[88:91],v[8:11],v[188:191]\n"
WT"lgkmcnt(2)\n"
RD"a[120:123],v227 offset:23552\n"
RD"a[124:127],v227 offset:31744\n"
MF"v[160:163],a[96:99],v[0:3],v[160:163]\n"
EX"v49,v49\n"
MF"v[160:163],a[100:103],v[16:19],v[160:163]\n"
"buffer_load_dwordx2 v[242:243],v236,s[8:11],0 offen sc1\n"
MF"v[192:195],a[100:103],v[24:27],v[192:195]\n"
EX"v50,v50\n"
MF"v[192:195],a[96:99],v[8:11],v[192:195]\n"
WT"vmcnt(15)\n"
BL"v225,s[8:11],s44 offen offset:2048 sc1 lds\n"
MF"v[164:167],a[104:107],v[0:3],v[164:167]\n"
"s_barrier\n"
MF"v[164:167],a[108:111],v[16:19],v[164:167]\n"
RD"a[64:67],v226 offset:0\n"
PK"v67,v38,v39\n"
MF"v[196:199],a[108:111],v[24:27],v[196:199]\n"
EX"v51,v51\n"
MF"v[196:199],a[104:107],v[8:11],v[196:199]\n"
WT"lgkmcnt(1)\n"
RD"a[68:71],v226 offset:1024\n"
PK"v70,v44,v45\n"
MF"v[168:171],a[112:115],v[0:3],v[168:171]\n"
EX"v52,v52\n"
MF"v[168:171],a[116:119],v[16:19],v[168:171]\n"
RD"a[72:75],v226 offset:2048\n"
PK"v71,v46,v47\n"
MF"v[200:203],a[116:119],v[24:27],v[200:203]\n"
EX"v53,v53\n"
MF"v[200:203],a[112:115],v[8:11],v[200:203]\n"
BL"v225,s[8:11],s44 offen offset:3072 sc1 lds\n"
MF"v[172:175],a[120:123],v[0:3],v[172:175]\n"
EX"v54,v54\n"
MF"v[172:175],a[124:127],v[16:19],v[172:175]\n"
"buffer_store_dwordx2 v[232:233],v230,s[8:11],s57 offen sc1\n"
MF"v[204:207],a[124:127],v[24:27],v[204:207]\n"
"s_mov_b32 s57,2130706432\n"
RD"a[76:79],v226 offset:3072\n"
PK"v72,v48,v49\n"
MF"v[204:207],a[120:123],v[8:11],v[204:207]\n"
EX"v55,v55\n"
MF"a[140:143],a[128:131],v[0:3],a[140:143]\n"
RD"a[80:83],v226 offset:4096\n"
RD"a[84:87],v226 offset:5120\n"
MF"a[140:143],a[128:131],v[16:19],a[140:143]\n"
RD"a[88:91],v226 offset:6144\n"
PK"v73,v50,v51\n"
MF"a[144:147],a[128:131],v[24:27],a[144:147]\n"
WT"lgkmcnt(5)\n"
"s_sub_u32 s56,s56,1\n"
PK"v74,v52,v53\n"
PK"v75,v54,v55\n"
MF"a[144:147],a[128:131],v[8:11],a[144:147]\n"
"s_cbranch_scc0 LP2_%=\n"
"s_cmp_lt_i32 s22,0\n"
"s_cbranch_scc1 LE2_%=\n"
MF"v[0:3],a[64:67],a[32:35],0\n"
SA"s48,s59,32768\n"
WT"vmcnt(3)\n"
"v_cmp_eq_u64 vcc,s[28:29],v[242:243]\n"
"s_or_b32 vcc_hi,vcc_hi,-1\n"
"s_cmp_eq_u64 vcc,exec\n"
"s_cbranch_scc0 L1028_%=\n"
"L1029_%=:\n"
MF"v[0:3],a[68:71],a[36:39],v[0:3]\n"
"s_and_b32 s48,s48,0x18000\n"
EX"v56,v56\n"
MF"v[8:11],a[68:71],a[52:55],0\n"
"s_andn2_b32 s49,s59,0x18000\n"
RD"a[92:95],v226 offset:7168\n"
"v_readlane_b32 s43,v228,s42\n"
MF"v[8:11],a[64:67],a[48:51],v[8:11]\n"
WT"lgkmcnt(5)\n"
EX"v57,v57\n"
MF"v[0:3],a[72:75],a[40:43],v[0:3]\n"
WT"lgkmcnt(1)\n"
RD"a[96:99],v226 offset:8192\n"
RD"a[100:103],v226 offset:9216\n"
MF"v[0:3],a[76:79],a[44:47],v[0:3]\n"
"s_or_b32 s52,s48,s49\n"
EX"v58,v58\n"
MF"v[8:11],a[76:79],a[60:63],v[8:11]\n"
SA"s53,s52,16384\n"
BL"a[0:3],v224,s[8:11],s52 offen offset:0 sc1\n"
MF"v[8:11],a[72:75],a[56:59],v[8:11]\n"
"s_mov_b32 m0,s40\n"
RD"a[104:107],v226 offset:10240\n"
"v_readlane_b32 s44,v229,s42\n"
MF"v[4:7],a[80:83],a[32:35],0\n"
SA"s42,s42,1\n"
EX"v59,v59\n"
MF"v[4:7],a[84:87],a[36:39],v[4:7]\n"
BL"v225,s[8:11],s43 offen offset:0 sc1 lds\n"
MF"v[12:15],a[84:87],a[52:55],0\n"
EX"v60,v60\n"
MF"v[12:15],a[80:83],a[48:51],v[12:15]\n"
RD"a[108:111],v226 offset:11264\n"
RD"a[112:115],v226 offset:12288\n"
PK"v76,v56,v57\n"
MF"v[4:7],a[88:91],a[40:43],v[4:7]\n"
WT"lgkmcnt(3)\n"
BL"a[4:7],v224,s[8:11],s53 offen offset:0 sc1\n"
EX"v61,v61\n"
MF"v[4:7],a[92:95],a[44:47],v[4:7]\n"
RD"a[116:119],v226 offset:13312\n"
RD"a[120:123],v226 offset:14336\n"
MF"v[12:15],a[92:95],a[60:63],v[12:15]\n"
EX"v62,v62\n"
MF"v[12:15],a[88:91],a[56:59],v[12:15]\n"
EX"v63,v63\n"
PK"v77,v58,v59\n"
MF"v[16:19],a[96:99],a[32:35],0\n"
BL"a[8:11],v224,s[8:11],s52 offen offset:1024 sc1\n"
MF"v[16:19],a[100:103],a[36:39],v[16:19]\n"
BL"v225,s[8:11],s43 offen offset:1024 sc1 lds\n"
MF"v[24:27],a[100:103],a[52:55],0\n"
EX"v0,v0\n"
MF"v[24:27],a[96:99],a[48:51],v[24:27]\n"
WT"lgkmcnt(2)\n"
RD"a[124:127],v226 offset:15360\n"
RD"a[64:67],v226 offset:16384\n"
PK"v78,v60,v61\n"
MF"v[16:19],a[104:107],a[40:43],v[16:19]\n"
SA"s41,s41,32768\n"
EX"v1,v1\n"
MF"v[16:19],a[108:111],a[44:47],v[16:19]\n"
"s_cmp_eq_u32 s41,163840\n"
RD"a[68:71],v226 offset:17408\n"
RD"a[72:75],v226 offset:18432\n"
MF"v[24:27],a[108:111],a[60:63],v[24:27]\n"
"s_cselect_b32 s41,0,s41\n"
BL"a[12:15],v224,s[8:11],s53 offen offset:1024 sc1\n"
EX"v2,v2\n"
MF"v[24:27],a[104:107],a[56:59],v[24:27]\n"
EX"v3,v3\n"
PK"v79,v62,v63\n"
MF"v[20:23],a[112:115],a[32:35],0\n"
WT"lgkmcnt(4)\n"
BL"v225,s[8:11],s43 offen offset:2048 sc1 lds\n"
MF"v[20:23],a[116:119],a[36:39],v[20:23]\n"
EX"v8,v8\n"
MF"v[28:31],a[116:119],a[52:55],0\n"
RD"a[76:79],v226 offset:19456\n"
"v_add_u32 v227,s41,v224\n"
MF"v[28:31],a[112:115],a[48:51],v[28:31]\n"
BL"a[16:19],v224,s[8:11],s52 offen offset:2048 sc1\n"
PK"v0,v0,v1\n"
MF"v[20:23],a[120:123],a[40:43],v[20:23]\n"
WT"lgkmcnt(1)\n"
EX"v9,v9\n"
MF"v[20:23],a[124:127],a[44:47],v[20:23]\n"
RD"a[80:83],v226 offset:20480\n"
RD"a[84:87],v226 offset:21504\n"
MF"v[28:31],a[124:127],a[60:63],v[28:31]\n"
EX"v10,v10\n"
MF"v[28:31],a[120:123],a[56:59],v[28:31]\n"
RD"a[88:91],v226 offset:22528\n"
PK"v1,v2,v3\n"
MF"v[32:35],a[64:67],a[32:35],0\n"
EX"v11,v11\n"
MF"v[32:35],a[68:71],a[36:39],v[32:35]\n"
BL"a[20:23],v224,s[8:11],s53 offen offset:2048 sc1\n"
BL"v225,s[8:11],s43 offen offset:3072 sc1 lds\n"
MF"v[40:43],a[68:71],a[52:55],0\n"
EX"v4,v4\n"
MF"v[40:43],a[64:67],a[48:51],v[40:43]\n"
SA"m0,s40,16384\n"
RD"a[92:95],v226 offset:23552\n"
RD"a[96:99],v226 offset:24576\n"
PK"v8,v8,v9\n"
MF"v[32:35],a[72:75],a[40:43],v[32:35]\n"
WT"lgkmcnt(3)\n"
EX"v5,v5\n"
MF"v[32:35],a[76:79],a[44:47],v[32:35]\n"
RD"a[100:103],v226 offset:25600\n"
RD"a[104:107],v226 offset:26624\n"
MF"v[40:43],a[76:79],a[60:63],v[40:43]\n"
BL"a[24:27],v224,s[8:11],s52 offen offset:3072 sc1\n"
EX"v6,v6\n"
MF"v[40:43],a[72:75],a[56:59],v[40:43]\n"
EX"v7,v7\n"
PK"v9,v10,v11\n"
MF"v[36:39],a[80:83],a[32:35],0\n"
SA"s40,s40,32768\n"
BL"v225,s[8:11],s44 offen offset:0 sc1 lds\n"
MF"v[36:39],a[84:87],a[36:39],v[36:39]\n"
"s_cmp_eq_u32 s40,s46\n"
EX"v12,v12\n"
MF"v[44:47],a[84:87],a[52:55],0\n"
"s_cselect_b32 s40,s47,s40\n"
BL"a[28:31],v224,s[8:11],s53 offen offset:3072 sc1\n"
MF"v[44:47],a[80:83],a[48:51],v[44:47]\n"
WT"lgkmcnt(2)\n"
RD"a[108:111],v226 offset:27648\n"
PK"v2,v4,v5\n"
MF"v[36:39],a[88:91],a[40:43],v[36:39]\n"
SA"s48,s59,65536\n"
EX"v13,v13\n"
MF"v[36:39],a[92:95],a[44:47],v[36:39]\n"
"s_and_b32 s48,s48,0x18000\n"
RD"a[112:115],v226 offset:28672\n"
RD"a[116:119],v226 offset:29696\n"
MF"v[44:47],a[92:95],a[60:63],v[44:47]\n"
"s_andn2_b32 s49,s59,0x18000\n"
EX"v14,v14\n"
MF"v[44:47],a[88:91],a[56:59],v[44:47]\n"
"s_or_b32 s52,s48,s49\n"
RD"a[120:123],v226 offset:30720\n"
PK"v3,v6,v7\n"
MF"v[48:51],a[96:99],a[32:35],0\n"
WT"lgkmcnt(4)\n"
EX"v15,v15\n"
MF"v[48:51],a[100:103],a[36:39],v[48:51]\n"
BL"a[180:183],v224,s[8:11],s52 offen offset:0 sc1\n"
BL"v225,s[8:11],s44 offen offset:1024 sc1 lds\n"
MF"v[56:59],a[100:103],a[52:55],0\n"
SA"s53,s52,16384\n"
EX"v16,v16\n"
MF"v[56:59],a[96:99],a[48:51],v[56:59]\n"
RD"a[124:127],v226 offset:31744\n"
PK"v10,v12,v13\n"
MF"v[48:51],a[104:107],a[40:43],v[48:51]\n"
WT"lgkmcnt(2)\n"
"s_barrier\n"
MF"v[48:51],a[108:111],a[44:47],v[48:51]\n"
RD"a[64:67],v227 offset:0\n"
PK"v11,v14,v15\n"
MF"v[56:59],a[108:111],a[60:63],v[56:59]\n"
RD"a[68:71],v227 offset:8192\n"
BL"a[184:187],v224,s[8:11],s53 offen offset:0 sc1\n"
MF"v[56:59],a[104:107],a[56:59],v[56:59]\n"
EX"v17,v17\n"
MF"v[52:55],a[112:115],a[32:35],0\n"
RD"a[72:75],v227 offset:1024\n"
PK"v16,v16,v17\n"
EX"v18,v18\n"
MF"v[52:55],a[116:119],a[36:39],v[52:55]\n"
BL"v225,s[8:11],s44 offen offset:2048 sc1 lds\n"
MF"v[60:63],a[116:119],a[52:55],0\n"
EX"v19,v19\n"
MF"v[60:63],a[112:115],a[48:51],v[60:63]\n"
WT"lgkmcnt(3)\n"
BL"a[188:191],v224,s[8:11],s52 offen offset:1024 sc1\n"
RD"a[76:79],v227 offset:9216\n"
MF"v[52:55],a[120:123],a[40:43],v[52:55]\n"
EX"v24,v24\n"
MF"v[52:55],a[124:127],a[44:47],v[52:55]\n"
RD"a[80:83],v227 offset:2048\n"
RD"a[84:87],v227 offset:10240\n"
MF"v[60:63],a[124:127],a[60:63],v[60:63]\n"
EX"v25,v25\n"
MF"v[60:63],a[120:123],a[56:59],v[60:63]\n"
WT"lgkmcnt(3)\n"
RD"a[88:91],v227 offset:3072\n"
"v_readlane_b32 s43,v228,s42\n"
MF"v[144:147],a[64:67],v[64:67],v[144:147]\n"
SA"s41,s41,32768\n"
BL"a[192:195],v224,s[8:11],s53 offen offset:1024 sc1\n"
EX"v26,v26\n"
MF"v[144:147],a[68:71],v[72:75],v[144:147]\n"
"s_cmp_eq_u32 s41,163840\n"
BL"v225,s[8:11],s44 offen offset:3072 sc1 lds\n"
PK"v17,v18,v19\n"
MF"v[176:179],a[68:71],v[76:79],v[176:179]\n"
"s_cselect_b32 s41,0,s41\n"
EX"v27,v27\n"
MF"v[176:179],a[64:67],v[68:71],v[176:179]\n"
"s_mov_b32 m0,s40\n"
RD"a[92:95],v227 offset:11264\n"
RD"a[96:99],v227 offset:4096\n"
MF"v[148:151],a[72:75],v[64:67],v[148:151]\n"
WT"lgkmcnt(3)\n"
EX"v20,v20\n"
MF"v[148:151],a[76:79],v[72:75],v[148:151]\n"
RD"a[100:103],v227 offset:12288\n"
RD"a[104:107],v227 offset:5120\n"
PK"v24,v24,v25\n"
MF"v[180:183],a[76:79],v[76:79],v[180:183]\n"
BL"a[196:199],v224,s[8:11],s52 offen offset:2048 sc1\n"
EX"v21,v21\n"
MF"v[180:183],a[72:75],v[68:71],v[180:183]\n"
EX"v22,v22\n"
"v_add_u32 v226,s41,v224\n"
MF"v[152:155],a[80:83],v[64:67],v[152:155]\n"
BL"v225,s[8:11],s43 offen offset:0 sc1 lds\n"
MF"v[152:155],a[84:87],v[72:75],v[152:155]\n"
EX"v23,v23\n"
PK"v25,v26,v27\n"
MF"v[184:187],a[84:87],v[76:79],v[184:187]\n"
BL"a[200:203],v224,s[8:11],s53 offen offset:2048 sc1\n"
MF"v[184:187],a[80:83],v[68:71],v[184:187]\n"
WT"lgkmcnt(2)\n"
RD"a[108:111],v227 offset:13312\n"
"v_readlane_b32 s44,v229,s42\n"
MF"v[156:159],a[88:91],v[64:67],v[156:159]\n"
SA"s42,s42,1\n"
EX"v28,v28\n"
MF"v[156:159],a[92:95],v[72:75],v[156:159]\n"
RD"a[112:115],v227 offset:6144\n"
RD"a[116:119],v227 offset:14336\n"
PK"v18,v20,v21\n"
MF"v[188:191],a[92:95],v[76:79],v[188:191]\n"
EX"v29,v29\n"
MF"v[188:191],a[88:91],v[68:71],v[188:191]\n"
RD"a[120:123],v227 offset:7168\n"
PK"v19,v22,v23\n"
MF"v[160:163],a[96:99],v[64:67],v[160:163]\n"
WT"lgkmcnt(4)\n"
BL"a[204:207],v224,s[8:11],s52 offen offset:3072 sc1\n"
EX"v30,v30\n"
MF"v[160:163],a[100:103],v[72:75],v[160:163]\n"
BL"v225,s[8:11],s43 offen offset:1024 sc1 lds\n"
MF"v[192:195],a[100:103],v[76:79],v[192:195]\n"
EX"v31,v31\n"
MF"v[192:195],a[96:99],v[68:71],v[192:195]\n"
RD"a[124:127],v227 offset:15360\n"
PK"v26,v28,v29\n"
MF"v[164:167],a[104:107],v[64:67],v[164:167]\n"
WT"lgkmcnt(2)\n"
EX"v32,v32\n"
MF"v[164:167],a[108:111],v[72:75],v[164:167]\n"
SA"s48,s59,98304\n"
BL"a[208:211],v224,s[8:11],s53 offen offset:3072 sc1\n"
MF"v[196:199],a[108:111],v[76:79],v[196:199]\n"
"s_and_b32 s48,s48,0x18000\n"
EX"v33,v33\n"
MF"v[196:199],a[104:107],v[68:71],v[196:199]\n"
"s_andn2_b32 s49,s59,0x18000\n"
RD"a[64:67],v227 offset:16384\n"
PK"v27,v30,v31\n"
MF"v[168:171],a[112:115],v[64:67],v[168:171]\n"
"s_or_b32 s52,s48,s49\n"
EX"v34,v34\n"
MF"v[168:171],a[116:119],v[72:75],v[168:171]\n"
SA"s53,s52,16384\n"
BL"v225,s[8:11],s43 offen offset:2048 sc1 lds\n"
MF"v[200:203],a[116:119],v[76:79],v[200:203]\n"
EX"v35,v35\n"
MF"v[200:203],a[112:115],v[68:71],v[200:203]\n"
WT"lgkmcnt(1)\n"
BL"a[212:215],v224,s[8:11],s52 offen offset:0 sc1\n"
RD"a[68:71],v227 offset:24576\n"
MF"v[172:175],a[120:123],v[64:67],v[172:175]\n"
EX"v40,v40\n"
MF"v[172:175],a[124:127],v[72:75],v[172:175]\n"
RD"a[72:75],v227 offset:17408\n"
RD"a[76:79],v227 offset:25600\n"
MF"v[204:207],a[124:127],v[76:79],v[204:207]\n"
EX"v41,v41\n"
MF"v[204:207],a[120:123],v[68:71],v[204:207]\n"
RD"a[80:83],v227 offset:18432\n"
MF"a[140:143],a[128:131],v[64:67],a[140:143]\n"
BL"a[216:219],v224,s[8:11],s53 offen offset:0 sc1\n"
EX"v42,v42\n"
MF"a[140:143],a[128:131],v[72:75],a[140:143]\n"
BL"v225,s[8:11],s43 offen offset:3072 sc1 lds\n"
PK"v64,v32,v33\n"
MF"a[144:147],a[128:131],v[76:79],a[144:147]\n"
EX"v43,v43\n"
PK"v65,v34,v35\n"
MF"a[144:147],a[128:131],v[68:71],a[144:147]\n"
WT"lgkmcnt(3)\n"
RD"a[84:87],v227 offset:26624\n"
RD"a[88:91],v227 offset:19456\n"
MF"v[144:147],a[64:67],v[0:3],v[144:147]\n"
SA"m0,s40,16384\n"
EX"v36,v36\n"
MF"v[144:147],a[68:71],v[16:19],v[144:147]\n"
BL"a[220:223],v224,s[8:11],s52 offen offset:1024 sc1\n"
RD"a[92:95],v227 offset:27648\n"
MF"v[176:179],a[68:71],v[24:27],v[176:179]\n"
EX"v37,v37\n"
PK"v68,v40,v41\n"
MF"v[176:179],a[64:67],v[8:11],v[176:179]\n"
WT"lgkmcnt(3)\n"
RD"a[96:99],v227 offset:20480\n"
PK"v69,v42,v43\n"
MF"v[148:151],a[72:75],v[0:3],v[148:151]\n"
SA"s40,s40,32768\n"
EX"v38,v38\n"
MF"v[148:151],a[76:79],v[16:19],v[148:151]\n"
"s_cmp_eq_u32 s40,s46\n"
BL"v225,s[8:11],s44 offen offset:0 sc1 lds\n"
MF"v[180:183],a[76:79],v[24:27],v[180:183]\n"
"s_cselect_b32 s40,s47,s40\n"
BL"a[224:227],v224,s[8:11],s53 offen offset:1024 sc1\n"
EX"v39,v39\n"
MF"v[180:183],a[72:75],v[8:11],v[180:183]\n"
RD"a[100:103],v227 offset:28672\n"
RD"a[104:107],v227 offset:21504\n"
MF"v[152:155],a[80:83],v[0:3],v[152:155]\n"
WT"lgkmcnt(3)\n"
EX"v44,v44\n"
MF"v[152:155],a[84:87],v[16:19],v[152:155]\n"
RD"a[108:111],v227 offset:29696\n"
RD"a[112:115],v227 offset:22528\n"
PK"v66,v36,v37\n"
MF"v[184:187],a[84:87],v[24:27],v[184:187]\n"
EX"v45,v45\n"
MF"v[184:187],a[80:83],v[8:11],v[184:187]\n"
BL"a[228:231],v224,s[8:11],s52 offen offset:2048 sc1\n"
MF"v[156:159],a[88:91],v[0:3],v[156:159]\n"
EX"v46,v46\n"
MF"v[156:159],a[92:95],v[16:19],v[156:159]\n"
BL"v225,s[8:11],s44 offen offset:1024 sc1 lds\n"
PK"v67,v38,v39\n"
MF"v[188:191],a[92:95],v[24:27],v[188:191]\n"
EX"v47,v47\n"
MF"v[188:191],a[88:91],v[8:11],v[188:191]\n"
WT"lgkmcnt(2)\n"
RD"a[116:119],v227 offset:30720\n"
RD"a[120:123],v227 offset:23552\n"
MF"v[160:163],a[96:99],v[0:3],v[160:163]\n"
EX"v48,v48\n"
MF"v[160:163],a[100:103],v[16:19],v[160:163]\n"
BL"a[232:235],v224,s[8:11],s53 offen offset:2048 sc1\n"
RD"a[124:127],v227 offset:31744\n"
MF"v[192:195],a[100:103],v[24:27],v[192:195]\n"
"buffer_load_dwordx2 v[242:243],v236,s[8:11],0 offen sc1\n"
EX"v49,v49\n"
MF"v[192:195],a[96:99],v[8:11],v[192:195]\n"
WT"vmcnt(38)\n"
EX"v50,v50\n"
PK"v70,v44,v45\n"
MF"v[164:167],a[104:107],v[0:3],v[164:167]\n"
WT"lgkmcnt(3)\n"
BL"v225,s[8:11],s44 offen offset:2048 sc1 lds\n"
MF"v[164:167],a[108:111],v[16:19],v[164:167]\n"
BL"a[236:239],v224,s[8:11],s52 offen offset:3072 sc1\n"
PK"v71,v46,v47\n"
MF"v[196:199],a[108:111],v[24:27],v[196:199]\n"
"s_barrier\n"
MF"v[196:199],a[104:107],v[8:11],v[196:199]\n"
RD"a[64:67],v226 offset:0\n"
PK"v72,v48,v49\n"
MF"v[168:171],a[112:115],v[0:3],v[168:171]\n"
WT"lgkmcnt(1)\n"
EX"v51,v51\n"
MF"v[168:171],a[116:119],v[16:19],v[168:171]\n"
RD"a[68:71],v226 offset:1024\n"
RD"a[72:75],v226 offset:2048\n"
MF"v[200:203],a[116:119],v[24:27],v[200:203]\n"
EX"v52,v52\n"
MF"v[200:203],a[112:115],v[8:11],v[200:203]\n"
BL"a[240:243],v224,s[8:11],s53 offen offset:3072 sc1\n"
MF"v[172:175],a[120:123],v[0:3],v[172:175]\n"
EX"v53,v53\n"
MF"v[172:175],a[124:127],v[16:19],v[172:175]\n"
BL"v225,s[8:11],s44 offen offset:3072 sc1 lds\n"
MF"v[204:207],a[124:127],v[24:27],v[204:207]\n"
EX"v54,v54\n"
MF"v[204:207],a[120:123],v[8:11],v[204:207]\n"
RD"a[76:79],v226 offset:3072\n"
RD"a[80:83],v226 offset:4096\n"
PK"v73,v50,v51\n"
MF"a[140:143],a[128:131],v[0:3],a[140:143]\n"
EX"v55,v55\n"
MF"a[140:143],a[128:131],v[16:19],a[140:143]\n"
RD"a[84:87],v226 offset:5120\n"
RD"a[88:91],v226 offset:6144\n"
MF"a[144:147],a[128:131],v[24:27],a[144:147]\n"
WT"lgkmcnt(5)\n"
PK"v74,v52,v53\n"
PK"v75,v54,v55\n"
MF"a[144:147],a[128:131],v[8:11],a[144:147]\n"
"s_sub_u32 s56,s22,1\n"
"s_mov_b32 s22,-1\n"
"s_cbranch_scc0 LP2_%=\n"
"LE2_%=:\n"
MF"v[0:3],a[64:67],a[32:35],v[208:211]\n"
WT"vmcnt(3)\n"
"v_cmp_eq_u64 vcc,s[28:29],v[242:243]\n"
"s_or_b32 vcc_lo,vcc_lo,-1\n"
"s_cmp_eq_u64 vcc,exec\n"
"s_cbranch_scc0 L1031_%=\n"
"L1032_%=:\n"
MF"v[0:3],a[68:71],a[36:39],v[0:3]\n"
"s_mov_b32 m0,s40\n"
EX"v56,v56\n"
MF"v[8:11],a[68:71],a[52:55],0\n"
EX"v57,v57\n"
MF"v[8:11],a[64:67],a[48:51],v[8:11]\n"
WT"lgkmcnt(4)\n"
RD"a[92:95],v226 offset:7168\n"
RD"a[96:99],v226 offset:8192\n"
"v_readlane_b32 s43,v228,s42\n"
MF"v[0:3],a[72:75],a[40:43],v[0:3]\n"
WT"lgkmcnt(2)\n"
EX"v58,v58\n"
"v_readlane_b32 s44,v229,s42\n"
MF"v[0:3],a[76:79],a[44:47],v[0:3]\n"
SA"s42,s42,1\n"
BL"v225,s[8:11],s43 offen offset:0 sc1 lds\n"
RD"a[100:103],v226 offset:9216\n"
MF"v[8:11],a[76:79],a[60:63],v[8:11]\n"
EX"v59,v59\n"
PK"v76,v56,v57\n"
MF"v[8:11],a[72:75],a[56:59],v[8:11]\n"
RD"a[104:107],v226 offset:10240\n"
PK"v77,v58,v59\n"
MF"v[4:7],a[80:83],a[32:35],v[212:215]\n"
EX"v60,v60\n"
MF"v[4:7],a[84:87],a[36:39],v[4:7]\n"
BL"v225,s[8:11],s43 offen offset:1024 sc1 lds\n"
MF"v[12:15],a[84:87],a[52:55],0\n"
EX"v61,v61\n"
MF"v[12:15],a[80:83],a[48:51],v[12:15]\n"
RD"a[108:111],v226 offset:11264\n"
RD"a[112:115],v226 offset:12288\n"
MF"v[4:7],a[88:91],a[40:43],v[4:7]\n"
WT"lgkmcnt(3)\n"
EX"v62,v62\n"
MF"v[4:7],a[92:95],a[44:47],v[4:7]\n"
RD"a[116:119],v226 offset:13312\n"
RD"a[120:123],v226 offset:14336\n"
PK"v78,v60,v61\n"
MF"v[12:15],a[92:95],a[60:63],v[12:15]\n"
EX"v63,v63\n"
MF"v[12:15],a[88:91],a[56:59],v[12:15]\n"
BL"v225,s[8:11],s43 offen offset:2048 sc1 lds\n"
MF"v[16:19],a[96:99],a[32:35],v[216:219]\n"
EX"v0,v0\n"
MF"v[16:19],a[100:103],a[36:39],v[16:19]\n"
RD"a[124:127],v226 offset:15360\n"
RD"a[64:67],v226 offset:16384\n"
PK"v79,v62,v63\n"
MF"v[24:27],a[100:103],a[52:55],0\n"
EX"v1,v1\n"
MF"v[24:27],a[96:99],a[48:51],v[24:27]\n"
WT"lgkmcnt(4)\n"
RD"a[68:71],v226 offset:17408\n"
RD"a[72:75],v226 offset:18432\n"
MF"v[16:19],a[104:107],a[40:43],v[16:19]\n"
SA"s41,s41,32768\n"
BL"v225,s[8:11],s43 offen offset:3072 sc1 lds\n"
MF"v[16:19],a[108:111],a[44:47],v[16:19]\n"
"s_cmp_eq_u32 s41,163840\n"
EX"v2,v2\n"
PK"v0,v0,v1\n"
MF"v[24:27],a[108:111],a[60:63],v[24:27]\n"
"s_cselect_b32 s41,0,s41\n"
RD"a[76:79],v226 offset:19456\n"
RD"a[80:83],v226 offset:20480\n"
MF"v[24:27],a[104:107],a[56:59],v[24:27]\n"
SA"m0,s40,16384\n"
EX"v3,v3\n"
"v_add_u32 v227,s41,v224\n"
MF"v[20:23],a[112:115],a[32:35],v[220:223]\n"
WT"lgkmcnt(4)\n"
RD"a[84:87],v226 offset:21504\n"
RD"a[88:91],v226 offset:22528\n"
MF"v[20:23],a[116:119],a[36:39],v[20:23]\n"
EX"v8,v8\n"
PK"v1,v2,v3\n"
MF"v[28:31],a[116:119],a[52:55],0\n"
SA"s40,s40,32768\n"
BL"v225,s[8:11],s44 offen offset:0 sc1 lds\n"
MF"v[28:31],a[112:115],a[48:51],v[28:31]\n"
"s_cmp_eq_u32 s40,s46\n"
RD"a[92:95],v226 offset:23552\n"
EX"v9,v9\n"
MF"v[20:23],a[120:123],a[40:43],v[20:23]\n"
"s_cselect_b32 s40,s47,s40\n"
RD"a[96:99],v226 offset:24576\n"
RD"a[100:103],v226 offset:25600\n"
EX"v10,v10\n"
MF"v[20:23],a[124:127],a[44:47],v[20:23]\n"
RD"a[104:107],v226 offset:26624\n"
RD"a[108:111],v226 offset:27648\n"
PK"v8,v8,v9\n"
MF"v[28:31],a[124:127],a[60:63],v[28:31]\n"
EX"v11,v11\n"
MF"v[28:31],a[120:123],a[56:59],v[28:31]\n"
RD"a[112:115],v226 offset:28672\n"
RD"a[116:119],v226 offset:29696\n"
MF"v[40:43],a[64:67],a[48:51],v[208:211]\n"
WT"lgkmcnt(7)\n"
BL"v225,s[8:11],s44 offen offset:1024 sc1 lds\n"
EX"v4,v4\n"
MF"v[40:43],a[68:71],a[52:55],v[40:43]\n"
RD"a[120:123],v226 offset:30720\n"
RD"a[124:127],v226 offset:31744\n"
PK"v9,v10,v11\n"
MF"v[40:43],a[72:75],a[56:59],v[40:43]\n"
EX"v5,v5\n"
MF"v[40:43],a[76:79],a[60:63],v[40:43]\n"
EX"v6,v6\n"
MF"v[44:47],a[80:83],a[48:51],v[212:215]\n"
"s_barrier\n"
MF"v[44:47],a[84:87],a[52:55],v[44:47]\n"
RD"a[64:67],v227 offset:0\n"
PK"v2,v4,v5\n"
MF"v[44:47],a[88:91],a[56:59],v[44:47]\n"
WT"lgkmcnt(3)\n"
RD"a[68:71],v227 offset:8192\n"
BL"v225,s[8:11],s44 offen offset:2048 sc1 lds\n"
MF"v[44:47],a[92:95],a[60:63],v[44:47]\n"
EX"v7,v7\n"
MF"v[56:59],a[96:99],a[48:51],v[216:219]\n"
RD"a[72:75],v227 offset:1024\n"
"v_readlane_b32 s43,v228,s42\n"
EX"v12,v12\n"
MF"v[56:59],a[100:103],a[52:55],v[56:59]\n"
EX"v13,v13\n"
PK"v3,v6,v7\n"
MF"v[56:59],a[104:107],a[56:59],v[56:59]\n"
BL"v225,s[8:11],s44 offen offset:3072 sc1 lds\n"
MF"v[56:59],a[108:111],a[60:63],v[56:59]\n"
"s_mov_b32 m0,s40\n"
RD"a[76:79],v227 offset:9216\n"
PK"v10,v12,v13\n"
MF"v[60:63],a[112:115],a[48:51],v[220:223]\n"
EX"v14,v14\n"
MF"v[60:63],a[116:119],a[52:55],v[60:63]\n"
WT"lgkmcnt(2)\n"
RD"a[80:83],v227 offset:2048\n"
RD"a[84:87],v227 offset:10240\n"
MF"v[60:63],a[120:123],a[56:59],v[60:63]\n"
EX"v15,v15\n"
MF"v[60:63],a[124:127],a[60:63],v[60:63]\n"
SA"s41,s41,32768\n"
RD"a[88:91],v227 offset:3072\n"
"v_readlane_b32 s44,v229,s42\n"
MF"v[144:147],a[64:67],v[64:67],v[144:147]\n"
"s_cmp_eq_u32 s41,163840\n"
EX"v16,v16\n"
PK"v11,v14,v15\n"
MF"v[144:147],a[68:71],v[72:75],v[144:147]\n"
"s_cselect_b32 s41,0,s41\n"
BL"v225,s[8:11],s43 offen offset:0 sc1 lds\n"
MF"v[176:179],a[68:71],v[76:79],v[176:179]\n"
SA"s42,s42,1\n"
EX"v17,v17\n"
"v_add_u32 v226,s41,v224\n"
MF"v[176:179],a[64:67],v[68:71],v[176:179]\n"
WT"lgkmcnt(3)\n"
RD"a[92:95],v227 offset:11264\n"
RD"a[96:99],v227 offset:4096\n"
MF"v[148:151],a[72:75],v[64:67],v[148:151]\n"
EX"v18,v18\n"
PK"v16,v16,v17\n"
MF"v[148:151],a[76:79],v[72:75],v[148:151]\n"
RD"a[100:103],v227 offset:12288\n"
RD"a[104:107],v227 offset:5120\n"
MF"v[180:183],a[76:79],v[76:79],v[180:183]\n"
EX"v19,v19\n"
MF"v[180:183],a[72:75],v[68:71],v[180:183]\n"
WT"lgkmcnt(4)\n"
BL"v225,s[8:11],s43 offen offset:1024 sc1 lds\n"
MF"v[152:155],a[80:83],v[64:67],v[152:155]\n"
EX"v24,v24\n"
PK"v17,v18,v19\n"
MF"v[152:155],a[84:87],v[72:75],v[152:155]\n"
EX"v25,v25\n"
MF"v[184:187],a[84:87],v[76:79],v[184:187]\n"
EX"v26,v26\n"
MF"v[184:187],a[80:83],v[68:71],v[184:187]\n"
RD"a[108:111],v227 offset:13312\n"
RD"a[112:115],v227 offset:6144\n"
PK"v24,v24,v25\n"
MF"v[156:159],a[88:91],v[64:67],v[156:159]\n"
WT"lgkmcnt(2)\n"
BL"v225,s[8:11],s43 offen offset:2048 sc1 lds\n"
MF"v[156:159],a[92:95],v[72:75],v[156:159]\n"
EX"v27,v27\n"
MF"v[188:191],a[92:95],v[76:79],v[188:191]\n"
RD"a[116:119],v227 offset:14336\n"
RD"a[120:123],v227 offset:7168\n"
MF"v[188:191],a[88:91],v[68:71],v[188:191]\n"
EX"v20,v20\n"
PK"v25,v26,v27\n"
MF"v[160:163],a[96:99],v[64:67],v[160:163]\n"
EX"v21,v21\n"
MF"v[160:163],a[100:103],v[72:75],v[160:163]\n"
BL"v225,s[8:11],s43 offen offset:3072 sc1 lds\n"
MF"v[192:195],a[100:103],v[76:79],v[192:195]\n"
SA"m0,s40,16384\n"
EX"v22,v22\n"
MF"v[192:195],a[96:99],v[68:71],v[192:195]\n"
RD"a[124:127],v227 offset:15360\n"
PK"v18,v20,v21\n"
MF"v[164:167],a[104:107],v[64:67],v[164:167]\n"
WT"lgkmcnt(3)\n"
EX"v23,v23\n"
MF"v[164:167],a[108:111],v[72:75],v[164:167]\n"
EX"v28,v28\n"
MF"v[196:199],a[108:111],v[76:79],v[196:199]\n"
SA"s40,s40,32768\n"
BL"v225,s[8:11],s44 offen offset:0 sc1 lds\n"
MF"v[196:199],a[104:107],v[68:71],v[196:199]\n"
"s_cmp_eq_u32 s40,s46\n"
RD"a[64:67],v227 offset:16384\n"
PK"v19,v22,v23\n"
MF"v[168:171],a[112:115],v[64:67],v[168:171]\n"
WT"lgkmcnt(2)\n"
EX"v29,v29\n"
MF"v[168:171],a[116:119],v[72:75],v[168:171]\n"
"s_cselect_b32 s40,s47,s40\n"
EX"v30,v30\n"
MF"v[200:203],a[116:119],v[76:79],v[200:203]\n"
RD"a[68:71],v227 offset:24576\n"
PK"v26,v28,v29\n"
MF"v[200:203],a[112:115],v[68:71],v[200:203]\n"
BL"v225,s[8:11],s44 offen offset:1024 sc1 lds\n"
MF"v[172:175],a[120:123],v[64:67],v[172:175]\n"
WT"lgkmcnt(2)\n"
EX"v31,v31\n"
MF"v[172:175],a[124:127],v[72:75],v[172:175]\n"
RD"a[72:75],v227 offset:17408\n"
RD"a[76:79],v227 offset:25600\n"
MF"v[204:207],a[124:127],v[76:79],v[204:207]\n"
EX"v40,v40\n"
MF"v[204:207],a[120:123],v[68:71],v[204:207]\n"
RD"a[80:83],v227 offset:18432\n"
PK"v27,v30,v31\n"
MF"a[140:143],a[128:131],v[64:67],a[140:143]\n"
EX"v41,v41\n"
MF"a[140:143],a[128:131],v[72:75],a[140:143]\n"
BL"v225,s[8:11],s44 offen offset:2048 sc1 lds\n"
MF"a[144:147],a[128:131],v[76:79],a[144:147]\n"
EX"v42,v42\n"
MF"a[144:147],a[128:131],v[68:71],a[144:147]\n"
WT"lgkmcnt(3)\n"
RD"a[84:87],v227 offset:26624\n"
RD"a[88:91],v227 offset:19456\n"
PK"v68,v40,v41\n"
MF"v[144:147],a[64:67],v[0:3],v[144:147]\n"
EX"v43,v43\n"
MF"v[144:147],a[68:71],v[16:19],v[144:147]\n"
RD"a[92:95],v227 offset:27648\n"
RD"a[96:99],v227 offset:20480\n"
MF"v[176:179],a[68:71],v[24:27],v[176:179]\n"
EX"v44,v44\n"
"v_readlane_b32 s43,v228,s42\n"
MF"v[176:179],a[64:67],v[8:11],v[176:179]\n"
WT"lgkmcnt(4)\n"
BL"v225,s[8:11],s44 offen offset:3072 sc1 lds\n"
PK"v69,v42,v43\n"
MF"v[148:151],a[72:75],v[0:3],v[148:151]\n"
"s_mov_b32 m0,s40\n"
EX"v45,v45\n"
MF"v[148:151],a[76:79],v[16:19],v[148:151]\n"
EX"v46,v46\n"
MF"v[180:183],a[76:79],v[24:27],v[180:183]\n"
RD"a[100:103],v227 offset:28672\n"
"v_readlane_b32 s44,v229,s42\n"
PK"v70,v44,v45\n"
MF"v[180:183],a[72:75],v[8:11],v[180:183]\n"
SA"s42,s42,1\n"
EX"v47,v47\n"
MF"v[152:155],a[80:83],v[0:3],v[152:155]\n"
WT"lgkmcnt(1)\n"
RD"a[104:107],v227 offset:21504\n"
RD"a[108:111],v227 offset:29696\n"
MF"v[152:155],a[84:87],v[16:19],v[152:155]\n"
EX"v56,v56\n"
MF"v[184:187],a[84:87],v[24:27],v[184:187]\n"
RD"a[112:115],v227 offset:22528\n"
PK"v71,v46,v47\n"
MF"v[184:187],a[80:83],v[8:11],v[184:187]\n"
EX"v57,v57\n"
MF"v[156:159],a[88:91],v[0:3],v[156:159]\n"
EX"v58,v58\n"
MF"v[156:159],a[92:95],v[16:19],v[156:159]\n"
RD"a[116:119],v227 offset:30720\n"
PK"v76,v56,v57\n"
MF"v[188:191],a[92:95],v[24:27],v[188:191]\n"
EX"v59,v59\n"
MF"v[188:191],a[88:91],v[8:11],v[188:191]\n"
RD"a[120:123],v227 offset:23552\n"
RD"a[124:127],v227 offset:31744\n"
MF"v[160:163],a[96:99],v[0:3],v[160:163]\n"
WT"lgkmcnt(4)\n"
EX"v60,v60\n"
MF"v[160:163],a[100:103],v[16:19],v[160:163]\n"
PK"v77,v58,v59\n"
MF"v[192:195],a[100:103],v[24:27],v[192:195]\n"
EX"v61,v61\n"
MF"v[192:195],a[96:99],v[8:11],v[192:195]\n"
WT"vmcnt(16)\n"
PK"v78,v60,v61\n"
MF"v[164:167],a[104:107],v[0:3],v[164:167]\n"
EX"v62,v62\n"
MF"v[164:167],a[108:111],v[16:19],v[164:167]\n"
SA"s41,s41,32768\n"
"s_barrier\n"
MF"v[196:199],a[108:111],v[24:27],v[196:199]\n"
"s_cmp_eq_u32 s41,163840\n"
BL"v225,s[8:11],s43 offen offset:0 sc1 lds\n"
BL"v225,s[8:11],s43 offen offset:1024 sc1 lds\n"
MF"v[196:199],a[104:107],v[8:11],v[196:199]\n"
WT"lgkmcnt(0)\n"
BL"v225,s[8:11],s43 offen offset:2048 sc1 lds\n"
RD"a[64:67],v226 offset:0\n"
MF"v[168:171],a[112:115],v[0:3],v[168:171]\n"
"s_cselect_b32 s41,0,s41\n"
EX"v63,v63\n"
"v_add_u32 v227,s41,v224\n"
MF"v[168:171],a[116:119],v[16:19],v[168:171]\n"
RD"a[68:71],v226 offset:8192\n"
RD"a[72:75],v226 offset:1024\n"
BL"v225,s[8:11],s43 offen offset:3072 sc1 lds\n"
MF"v[200:203],a[116:119],v[24:27],v[200:203]\n"
SA"m0,s40,16384\n"
RD"a[76:79],v226 offset:9216\n"
PK"v79,v62,v63\n"
MF"v[200:203],a[112:115],v[8:11],v[200:203]\n"
RD"a[80:83],v226 offset:2048\n"
MF"v[172:175],a[120:123],v[0:3],v[172:175]\n"
RD"a[84:87],v226 offset:10240\n"
MF"v[172:175],a[124:127],v[16:19],v[172:175]\n"
RD"a[88:91],v226 offset:3072\n"
MF"v[204:207],a[124:127],v[24:27],v[204:207]\n"
SA"s40,s40,32768\n"
BL"v225,s[8:11],s44 offen offset:0 sc1 lds\n"
MF"v[204:207],a[120:123],v[8:11],v[204:207]\n"
"s_cmp_eq_u32 s40,s46\n"
RD"a[92:95],v226 offset:11264\n"
"s_cselect_b32 s40,s47,s40\n"
MF"a[140:143],a[128:131],v[0:3],a[140:143]\n"
RD"a[96:99],v226 offset:4096\n"
MF"a[140:143],a[128:131],v[16:19],a[140:143]\n"
RD"a[100:103],v226 offset:12288\n"
RD"a[104:107],v226 offset:5120\n"
MF"a[144:147],a[128:131],v[24:27],a[144:147]\n"
RD"a[108:111],v226 offset:13312\n"
MF"a[144:147],a[128:131],v[8:11],a[144:147]\n"
WT"lgkmcnt(6)\n"
RD"a[112:115],v226 offset:6144\n"
MF"v[176:179],a[64:67],v[68:71],v[176:179]\n"
BL"v225,s[8:11],s44 offen offset:1024 sc1 lds\n"
RD"a[116:119],v226 offset:14336\n"
MF"v[176:179],a[68:71],v[76:79],v[176:179]\n"
WT"vmcnt(14)\n"
RD"a[120:123],v226 offset:7168\n"
MF"v[180:183],a[72:75],v[68:71],v[180:183]\n"
RD"a[124:127],v226 offset:15360\n"
MF"v[180:183],a[76:79],v[76:79],v[180:183]\n"
"s_barrier\n"
MF"v[184:187],a[80:83],v[68:71],v[184:187]\n"
RD"a[64:67],v227 offset:0\n"
MF"v[184:187],a[84:87],v[76:79],v[184:187]\n"
WT"lgkmcnt(5)\n"
BL"v225,s[8:11],s44 offen offset:2048 sc1 lds\n"
MF"v[188:191],a[88:91],v[68:71],v[188:191]\n"
RD"a[68:71],v227 offset:1024\n"
MF"v[188:191],a[92:95],v[76:79],v[188:191]\n"
MF"v[192:195],a[96:99],v[68:71],v[192:195]\n"
MF"v[192:195],a[100:103],v[76:79],v[192:195]\n"
BL"v225,s[8:11],s44 offen offset:3072 sc1 lds\n"
MF"v[196:199],a[104:107],v[68:71],v[196:199]\n"
RD"a[72:75],v227 offset:2048\n"
MF"v[196:199],a[108:111],v[76:79],v[196:199]\n"
WT"lgkmcnt(2)\n"
RD"a[76:79],v227 offset:3072\n"
MF"v[200:203],a[112:115],v[68:71],v[200:203]\n"
RD"a[80:83],v227 offset:4096\n"
MF"v[200:203],a[116:119],v[76:79],v[200:203]\n"
RD"a[84:87],v227 offset:5120\n"
MF"v[204:207],a[120:123],v[68:71],v[204:207]\n"
MF"v[204:207],a[124:127],v[76:79],v[204:207]\n"
MF"a[144:147],a[128:131],v[68:71],a[144:147]\n"
WT"lgkmcnt(4)\n"
MF"a[144:147],a[128:131],v[76:79],a[144:147]\n"
RD"a[88:91],v227 offset:6144\n"
"LTL_%=:\n"
"s_cmp_ge_u32 s34,6\n"
"s_cbranch_scc1 LTH_%=\n"
RD"a[92:95],v227 offset:7168\n"
MF"v[0:3],a[64:67],v[80:83],0\n"
"v_accvgpr_read_b32 v251,a140\n"
"v_subrev_u32 v253,0x30800000,v251\n"
MF"v[0:3],a[68:71],v[88:91],v[0:3]\n"
RD"a[96:99],v227 offset:8192\n"
MF"v[40:43],a[68:71],v[120:123],0\n"
"v_cmp_le_u32 vcc,0x1f000000,v253\n"
"s_cmp_lg_u64 vcc,0\n"
"s_cbranch_scc1 L1041_%=\n"
"L1040_%=:\n"
MF"v[40:43],a[64:67],v[112:115],v[40:43]\n"
WT"lgkmcnt(3)\n"
"v_rcp_f32 v251,v251\n"
MF"v[4:7],a[72:75],v[80:83],0\n"
SA"s41,s41,32768\n"
RD"a[100:103],v227 offset:9216\n"
MU"v144,v251,v144\n"
MF"v[4:7],a[76:79],v[88:91],v[4:7]\n"
"s_cmp_eq_u32 s41,163840\n"
RD"a[104:107],v227 offset:10240\n"
MU"v145,v251,v145\n"
MF"v[44:47],a[76:79],v[120:123],0\n"
"s_cselect_b32 s41,0,s41\n"
MU"v146,v251,v146\n"
"v_add_u32 v226,s41,v224\n"
MF"v[44:47],a[72:75],v[112:115],v[44:47]\n"
RD"a[108:111],v227 offset:11264\n"
MU"v147,v251,v147\n"
MF"v[8:11],a[80:83],v[80:83],0\n"
SA"s41,s41,32768\n"
MU"v148,v251,v148\n"
MU"v149,v251,v149\n"
MF"v[8:11],a[84:87],v[88:91],v[8:11]\n"
"s_cmp_eq_u32 s41,163840\n"
RD"a[112:115],v227 offset:12288\n"
MU"v150,v251,v150\n"
MU"v151,v251,v151\n"
MF"v[48:51],a[84:87],v[120:123],0\n"
"s_cselect_b32 s41,0,s41\n"
PK"v144,v144,v145\n"
"v_add_u32 v235,s41,v224\n"
PK"v145,v146,v147\n"
MF"v[48:51],a[80:83],v[112:115],v[48:51]\n"
WT"lgkmcnt(3)\n"
RD"a[116:119],v227 offset:13312\n"
PK"v146,v148,v149\n"
MF"v[12:15],a[88:91],v[80:83],0\n"
SA"s41,s41,32768\n"
PK"v147,v150,v151\n"
MU"v152,v251,v152\n"
MF"v[12:15],a[92:95],v[88:91],v[12:15]\n"
"s_cmp_eq_u32 s41,163840\n"
BS"v[144:147],v224,s[8:11],s60 offen offset:0 sc1\n"
MU"v153,v251,v153\n"
MF"v[52:55],a[92:95],v[120:123],0\n"
"s_cselect_b32 s41,0,s41\n"
RD"a[120:123],v227 offset:14336\n"
"v_add_u32 v236,s41,v224\n"
MU"v154,v251,v154\n"
MF"v[52:55],a[88:91],v[112:115],v[52:55]\n"
RD"a[124:127],v227 offset:15360\n"
MU"v155,v251,v155\n"
MU"v156,v251,v156\n"
MF"v[16:19],a[96:99],v[80:83],0\n"
SA"s41,s41,32768\n"
MU"v157,v251,v157\n"
MU"v158,v251,v158\n"
MU"v159,v251,v159\n"
MF"v[16:19],a[100:103],v[88:91],v[16:19]\n"
"s_cmp_eq_u32 s41,163840\n"
RD"a[64:67],v227 offset:16384\n"
PK"v152,v152,v153\n"
PK"v153,v154,v155\n"
MF"v[56:59],a[100:103],v[120:123],0\n"
"s_cselect_b32 s41,0,s41\n"
PK"v154,v156,v157\n"
"v_add_u32 v237,s41,v224\n"
PK"v155,v158,v159\n"
MF"v[56:59],a[96:99],v[112:115],v[56:59]\n"
WT"lgkmcnt(3)\n"
BS"v[152:155],v224,s[8:11],s60 offen offset:1024 sc1\n"
MU"v160,v251,v160\n"
MF"v[20:23],a[104:107],v[80:83],0\n"
RD"a[68:71],v227 offset:17408\n"
MU"v161,v251,v161\n"
MU"v162,v251,v162\n"
MF"v[20:23],a[108:111],v[88:91],v[20:23]\n"
RD"a[72:75],v227 offset:18432\n"
MU"v163,v251,v163\n"
MU"v164,v251,v164\n"
MF"v[60:63],a[108:111],v[120:123],0\n"
MU"v165,v251,v165\n"
MU"v166,v251,v166\n"
MF"v[60:63],a[104:107],v[112:115],v[60:63]\n"
RD"a[76:79],v227 offset:19456\n"
MU"v167,v251,v167\n"
PK"v160,v160,v161\n"
MF"v[24:27],a[112:115],v[80:83],0\n"
SA"s51,s60,16384\n"
"v_accvgpr_read_b32 v252,a144\n"
"v_subrev_u32 v253,0x30800000,v252\n"
PK"v161,v162,v163\n"
MF"v[24:27],a[116:119],v[88:91],v[24:27]\n"
"s_lshl_b32 s62,s34,14\n"
RD"a[80:83],v227 offset:20480\n"
PK"v162,v164,v165\n"
PK"v163,v166,v167\n"
MF"v[64:67],a[116:119],v[120:123],0\n"
"s_sub_u32 s62,s61,s62\n"
BS"v[160:163],v224,s[8:11],s60 offen offset:2048 sc1\n"
MU"v168,v251,v168\n"
MF"v[64:67],a[112:115],v[112:115],v[64:67]\n"
WT"lgkmcnt(3)\n"
"v_cmp_le_u32 vcc,0x1f000000,v253\n"
"s_cmp_lg_u64 vcc,0\n"
"s_cbranch_scc1 L1047_%=\n"
"L1046_%=:\n"
MF"v[28:31],a[120:123],v[80:83],0\n"
SA"s62,s62,122880\n"
"v_rcp_f32 v252,v252\n"
MU"v169,v251,v169\n"
MF"v[28:31],a[124:127],v[88:91],v[28:31]\n"
RD"a[84:87],v227 offset:21504\n"
RD"a[88:91],v227 offset:22528\n"
MU"v170,v251,v170\n"
MF"v[68:71],a[124:127],v[120:123],0\n"
MU"v171,v251,v171\n"
MU"v172,v251,v172\n"
MU"v173,v251,v173\n"
MF"v[68:71],a[120:123],v[112:115],v[68:71]\n"
RD"a[92:95],v227 offset:23552\n"
MU"v174,v251,v174\n"
MU"v175,v251,v175\n"
MF"v[32:35],a[64:67],v[80:83],0\n"
PK"v168,v168,v169\n"
PK"v169,v170,v171\n"
MU"v176,v252,v176\n"
MF"v[32:35],a[68:71],v[88:91],v[32:35]\n"
RD"a[96:99],v227 offset:24576\n"
MU"v177,v252,v177\n"
PK"v170,v172,v173\n"
MF"v[72:75],a[68:71],v[120:123],0\n"
PK"v171,v174,v175\n"
MU"v178,v252,v178\n"
MU"v179,v252,v179\n"
MF"v[72:75],a[64:67],v[112:115],v[72:75]\n"
WT"lgkmcnt(4)\n"
BS"v[168:171],v224,s[8:11],s60 offen offset:3072 sc1\n"
MU"v180,v252,v180\n"
MF"v[36:39],a[72:75],v[80:83],0\n"
RD"a[100:103],v227 offset:25600\n"
MU"v181,v252,v181\n"
MU"v182,v252,v182\n"
MF"v[36:39],a[76:79],v[88:91],v[36:39]\n"
RD"a[104:107],v227 offset:26624\n"
MU"v183,v252,v183\n"
PK"v176,v176,v177\n"
MF"v[76:79],a[76:79],v[120:123],0\n"
PK"v177,v178,v179\n"
PK"v178,v180,v181\n"
PK"v179,v182,v183\n"
MF"v[76:79],a[72:75],v[112:115],v[76:79]\n"
BS"v[176:179],v224,s[8:11],s51 offen offset:0 sc1\n"
MU"v184,v252,v184\n"
MF"v[0:3],a[80:83],v[96:99],v[0:3]\n"
WT"lgkmcnt(2)\n"
RD"a[108:111],v227 offset:27648\n"
MU"v185,v252,v185\n"
MU"v186,v252,v186\n"
MF"v[0:3],a[84:87],v[104:107],v[0:3]\n"
RD"a[112:115],v227 offset:28672\n"
MU"v187,v252,v187\n"
MU"v188,v252,v188\n"
MF"v[40:43],a[84:87],v[136:139],v[40:43]\n"
MU"v189,v252,v189\n"
MU"v190,v252,v190\n"
MU"v191,v252,v191\n"
MF"v[40:43],a[80:83],v[128:131],v[40:43]\n"
RD"a[116:119],v227 offset:29696\n"
PK"v184,v184,v185\n"
PK"v185,v186,v187\n"
MF"v[4:7],a[88:91],v[96:99],v[4:7]\n"
PK"v186,v188,v189\n"
PK"v187,v190,v191\n"
MU"v192,v252,v192\n"
MF"v[4:7],a[92:95],v[104:107],v[4:7]\n"
BS"v[184:187],v224,s[8:11],s51 offen offset:1024 sc1\n"
MU"v193,v252,v193\n"
MF"v[44:47],a[92:95],v[136:139],v[44:47]\n"
WT"vmcnt(14)\n"
RD"a[120:123],v227 offset:30720\n"
MU"v194,v252,v194\n"
MU"v195,v252,v195\n"
MF"v[44:47],a[88:91],v[128:131],v[44:47]\n"
RD"a[124:127],v227 offset:31744\n"
MU"v196,v252,v196\n"
MU"v197,v252,v197\n"
MF"v[8:11],a[96:99],v[96:99],v[8:11]\n"
WT"lgkmcnt(3)\n"
"s_barrier\n"
MF"v[8:11],a[100:103],v[104:107],v[8:11]\n"
RD"a[64:67],v226 offset:0\n"
MU"v198,v252,v198\n"
MU"v199,v252,v199\n"
MF"v[48:51],a[100:103],v[136:139],v[48:51]\n"
PK"v192,v192,v193\n"
PK"v193,v194,v195\n"
PK"v194,v196,v197\n"
MF"v[48:51],a[96:99],v[128:131],v[48:51]\n"
RD"a[68:71],v226 offset:1024\n"
PK"v195,v198,v199\n"
MU"v200,v252,v200\n"
MF"v[12:15],a[104:107],v[96:99],v[12:15]\n"
BS"v[192:195],v224,s[8:11],s51 offen offset:2048 sc1\n"
MU"v201,v252,v201\n"
MF"v[12:15],a[108:111],v[104:107],v[12:15]\n"
RD"a[72:75],v226 offset:2048\n"
MU"v202,v252,v202\n"
MU"v203,v252,v203\n"
MF"v[52:55],a[108:111],v[136:139],v[52:55]\n"
MU"v204,v252,v204\n"
MU"v205,v252,v205\n"
MU"v206,v252,v206\n"
MF"v[52:55],a[104:107],v[128:131],v[52:55]\n"
RD"a[76:79],v226 offset:3072\n"
MU"v207,v252,v207\n"
PK"v200,v200,v201\n"
MF"v[16:19],a[112:115],v[96:99],v[16:19]\n"
WT"lgkmcnt(3)\n"
PK"v201,v202,v203\n"
PK"v202,v204,v205\n"
PK"v203,v206,v207\n"
MF"v[16:19],a[116:119],v[104:107],v[16:19]\n"
BS"v[200:203],v224,s[8:11],s51 offen offset:3072 sc1\n"
MF"v[56:59],a[116:119],v[136:139],v[56:59]\n"
RD"a[80:83],v226 offset:4096\n"
"v_readlane_b32 s43,v228,s42\n"
MF"v[56:59],a[112:115],v[128:131],v[56:59]\n"
RD"a[84:87],v226 offset:5120\n"
MF"v[20:23],a[120:123],v[96:99],v[20:23]\n"
MF"v[20:23],a[124:127],v[104:107],v[20:23]\n"
RD"a[88:91],v226 offset:6144\n"
MF"v[60:63],a[124:127],v[136:139],v[60:63]\n"
"s_mov_b32 m0,s40\n"
MF"v[60:63],a[120:123],v[128:131],v[60:63]\n"
RD"a[92:95],v226 offset:7168\n"
MF"v[24:27],a[64:67],v[96:99],v[24:27]\n"
WT"lgkmcnt(4)\n"
"v_readlane_b32 s44,v229,s42\n"
MF"v[24:27],a[68:71],v[104:107],v[24:27]\n"
RD"a[96:99],v227 offset:0\n"
MF"v[64:67],a[68:71],v[136:139],v[64:67]\n"
SA"s42,s42,1\n"
BL"v225,s[8:11],s43 offen offset:0 sc1 lds\n"
MF"v[64:67],a[64:67],v[128:131],v[64:67]\n"
RD"a[100:103],v227 offset:1024\n"
MF"v[28:31],a[72:75],v[96:99],v[28:31]\n"
MF"v[28:31],a[76:79],v[104:107],v[28:31]\n"
RD"a[104:107],v227 offset:2048\n"
MF"v[68:71],a[76:79],v[136:139],v[68:71]\n"
MF"v[68:71],a[72:75],v[128:131],v[68:71]\n"
WT"lgkmcnt(3)\n"
RD"a[108:111],v227 offset:3072\n"
MF"v[32:35],a[80:83],v[96:99],v[32:35]\n"
MF"v[32:35],a[84:87],v[104:107],v[32:35]\n"
RD"a[112:115],v227 offset:4096\n"
MF"v[72:75],a[84:87],v[136:139],v[72:75]\n"
BL"v225,s[8:11],s43 offen offset:1024 sc1 lds\n"
MF"v[72:75],a[80:83],v[128:131],v[72:75]\n"
RD"a[116:119],v227 offset:5120\n"
MF"v[36:39],a[88:91],v[96:99],v[36:39]\n"
MF"v[36:39],a[92:95],v[104:107],v[36:39]\n"
RD"a[120:123],v227 offset:6144\n"
MF"v[76:79],a[92:95],v[136:139],v[76:79]\n"
MF"v[76:79],a[88:91],v[128:131],v[76:79]\n"
WT"lgkmcnt(3)\n"
RD"a[124:127],v227 offset:7168\n"
MF"v[84:87],a[96:99],v[144:147],0\n"
BL"v225,s[8:11],s43 offen offset:2048 sc1 lds\n"
MF"v[84:87],a[100:103],v[152:155],v[84:87]\n"
RD"a[64:67],v227 offset:8192\n"
MF"v[164:167],a[100:103],v[184:187],0\n"
MF"v[164:167],a[96:99],v[176:179],v[164:167]\n"
RD"a[68:71],v227 offset:9216\n"
MF"v[92:95],a[104:107],v[144:147],0\n"
MF"v[92:95],a[108:111],v[152:155],v[92:95]\n"
RD"a[72:75],v227 offset:10240\n"
MF"v[172:175],a[108:111],v[184:187],0\n"
MF"v[172:175],a[104:107],v[176:179],v[172:175]\n"
WT"lgkmcnt(3)\n"
RD"a[76:79],v227 offset:11264\n"
MF"v[100:103],a[112:115],v[144:147],0\n"
BL"v225,s[8:11],s43 offen offset:3072 sc1 lds\n"
MF"v[100:103],a[116:119],v[152:155],v[100:103]\n"
RD"a[80:83],v227 offset:12288\n"
MF"v[180:183],a[116:119],v[184:187],0\n"
SA"m0,s40,16384\n"
MF"v[180:183],a[112:115],v[176:179],v[180:183]\n"
RD"a[84:87],v227 offset:13312\n"
MF"v[108:111],a[120:123],v[144:147],0\n"
MF"v[108:111],a[124:127],v[152:155],v[108:111]\n"
RD"a[88:91],v227 offset:14336\n"
MF"v[188:191],a[124:127],v[184:187],0\n"
BL"v225,s[8:11],s44 offen offset:0 sc1 lds\n"
MF"v[188:191],a[120:123],v[176:179],v[188:191]\n"
WT"lgkmcnt(3)\n"
RD"a[92:95],v227 offset:15360\n"
SA"s40,s40,32768\n"
MF"v[116:119],a[64:67],v[144:147],0\n"
"s_cmp_eq_u32 s40,s46\n"
"s_cselect_b32 s40,s47,s40\n"
MF"v[116:119],a[68:71],v[152:155],v[116:119]\n"
RD"a[96:99],v227 offset:16384\n"
MF"v[196:199],a[68:71],v[184:187],0\n"
MF"v[196:199],a[64:67],v[176:179],v[196:199]\n"
RD"a[100:103],v227 offset:17408\n"
MF"v[124:127],a[72:75],v[144:147],0\n"
MF"v[124:127],a[76:79],v[152:155],v[124:127]\n"
RD"a[104:107],v227 offset:18432\n"
MF"v[204:207],a[76:79],v[184:187],0\n"
BL"v225,s[8:11],s44 offen offset:1024 sc1 lds\n"
MF"v[204:207],a[72:75],v[176:179],v[204:207]\n"
WT"lgkmcnt(3)\n"
RD"a[108:111],v227 offset:19456\n"
MF"v[132:135],a[80:83],v[144:147],0\n"
MF"v[132:135],a[84:87],v[152:155],v[132:135]\n"
RD"a[112:115],v227 offset:20480\n"
MF"v[208:211],a[84:87],v[184:187],0\n"
MF"v[208:211],a[80:83],v[176:179],v[208:211]\n"
RD"a[116:119],v227 offset:21504\n"
MF"v[140:143],a[88:91],v[144:147],0\n"
BL"v225,s[8:11],s44 offen offset:2048 sc1 lds\n"
MF"v[140:143],a[92:95],v[152:155],v[140:143]\n"
RD"a[120:123],v227 offset:22528\n"
MF"v[212:215],a[92:95],v[184:187],0\n"
WT"vmcnt(7)\n"
"buffer_store_dwordx2 v[232:233],v230,s[8:11],s62 offen sc1\n"
MF"v[212:215],a[88:91],v[176:179],v[212:215]\n"
WT"lgkmcnt(3)\n"
RD"a[124:127],v227 offset:23552\n"
MF"v[148:151],a[96:99],v[144:147],0\n"
MF"v[148:151],a[100:103],v[152:155],v[148:151]\n"
RD"a[64:67],v227 offset:24576\n"
MF"v[216:219],a[100:103],v[184:187],0\n"
"v_readlane_b32 s43,v228,s42\n"
MF"v[216:219],a[96:99],v[176:179],v[216:219]\n"
RD"a[68:71],v227 offset:25600\n"
MF"v[156:159],a[104:107],v[144:147],0\n"
BL"v225,s[8:11],s44 offen offset:3072 sc1 lds\n"
MF"v[156:159],a[108:111],v[152:155],v[156:159]\n"
RD"a[72:75],v227 offset:26624\n"
MF"v[220:223],a[108:111],v[184:187],0\n"
"s_mov_b32 m0,s40\n"
MF"v[220:223],a[104:107],v[176:179],v[220:223]\n"
WT"lgkmcnt(3)\n"
RD"a[76:79],v227 offset:27648\n"
MF"v[84:87],a[112:115],v[160:163],v[84:87]\n"
"s_lshr_b32 s89,s31,3\n"
"v_readlane_b32 s44,v229,s42\n"
MF"v[84:87],a[116:119],v[168:171],v[84:87]\n"
"s_lshl_b32 s89,s89,4\n"
RD"a[80:83],v227 offset:28672\n"
MF"v[164:167],a[116:119],v[200:203],v[164:167]\n"
SA"s42,s42,1\n"
BL"v225,s[8:11],s43 offen offset:0 sc1 lds\n"
MF"v[164:167],a[112:115],v[192:195],v[164:167]\n"
SA"s89,s89,s30\n"
RD"a[84:87],v227 offset:29696\n"
"s_lshl_b32 s89,s89,3\n"
MF"v[92:95],a[120:123],v[160:163],v[92:95]\n"
"s_xor_b32 s23,s32,s30\n"
MF"v[92:95],a[124:127],v[168:171],v[92:95]\n"
SA"s89,s89,s23\n"
RD"a[88:91],v227 offset:30720\n"
MF"v[172:175],a[124:127],v[200:203],v[172:175]\n"
"s_xor_b32 s90,s89,36\n"
MF"v[172:175],a[120:123],v[192:195],v[172:175]\n"
WT"lgkmcnt(3)\n"
RD"a[92:95],v227 offset:31744\n"
"s_mul_i32 s88,s89,5120\n"
MF"v[100:103],a[64:67],v[160:163],v[100:103]\n"
"s_mul_i32 s22,s90,5120\n"
MF"v[100:103],a[68:71],v[168:171],v[100:103]\n"
"s_sub_u32 s90,s90,s89\n"
RD"a[96:99],v226 offset:0\n"
MF"v[180:183],a[68:71],v[200:203],v[180:183]\n"
"s_lshl_b32 s90,s90,6\n"
BL"v225,s[8:11],s43 offen offset:1024 sc1 lds\n"
MF"v[180:183],a[64:67],v[192:195],v[180:183]\n"
SA"s88,s88,61603840\n"
RD"a[100:103],v226 offset:1024\n"
MF"v[108:111],a[72:75],v[160:163],v[108:111]\n"
SA"s22,s22,61603840\n"
MF"v[108:111],a[76:79],v[168:171],v[108:111]\n"
"s_cmp_eq_u32 s23,0\n"
RD"a[104:107],v226 offset:2048\n"
"s_cselect_b32 s88,2130706432,s88\n"
MF"v[188:191],a[76:79],v[200:203],v[188:191]\n"
"s_cmp_eq_u32 s23,4\n"
"s_cselect_b32 s22,2130706432,s22\n"
MF"v[188:191],a[72:75],v[192:195],v[188:191]\n"
WT"lgkmcnt(3)\n"
RD"a[108:111],v226 offset:3072\n"
"s_lshl_b32 s89,s89,6\n"
MF"v[116:119],a[80:83],v[160:163],v[116:119]\n"
SA"s89,s89,131072\n"
BL"v225,s[8:11],s43 offen offset:2048 sc1 lds\n"
MF"v[116:119],a[84:87],v[168:171],v[116:119]\n"
RD"a[112:115],v226 offset:4096\n"
"v_bfe_u32 v239,v224,1,6\n"
MF"v[196:199],a[84:87],v[200:203],v[196:199]\n"
"v_bfe_u32 v242,v224,7,1\n"
MF"v[196:199],a[80:83],v[192:195],v[196:199]\n"
RD"a[116:119],v226 offset:5120\n"
"v_mad_u32_u24 v239,v242,s90,v239\n"
MF"v[124:127],a[88:91],v[160:163],v[124:127]\n"
"v_lshrrev_b32 v242,8,v224\n"
MF"v[124:127],a[92:95],v[168:171],v[124:127]\n"
RD"a[120:123],v226 offset:6144\n"
"v_lshl_add_u32 v239,v242,27,v239\n"
MF"v[204:207],a[92:95],v[200:203],v[204:207]\n"
MF"v[204:207],a[88:91],v[192:195],v[204:207]\n"
WT"lgkmcnt(3)\n"
RD"a[124:127],v226 offset:7168\n"
MF"v[132:135],a[96:99],v[160:163],v[132:135]\n"
BL"v225,s[8:11],s43 offen offset:3072 sc1 lds\n"
MF"v[132:135],a[100:103],v[168:171],v[132:135]\n"
RD"a[64:67],v226 offset:8192\n"
MF"v[208:211],a[100:103],v[200:203],v[208:211]\n"
SA"m0,s40,16384\n"
MF"v[208:211],a[96:99],v[192:195],v[208:211]\n"
RD"a[68:71],v226 offset:9216\n"
MF"v[140:143],a[104:107],v[160:163],v[140:143]\n"
MF"v[140:143],a[108:111],v[168:171],v[140:143]\n"
RD"a[72:75],v226 offset:10240\n"
MF"v[212:215],a[108:111],v[200:203],v[212:215]\n"
BL"v225,s[8:11],s44 offen offset:0 sc1 lds\n"
MF"v[212:215],a[104:107],v[192:195],v[212:215]\n"
WT"lgkmcnt(3)\n"
RD"a[76:79],v226 offset:11264\n"
SA"s40,s40,32768\n"
MF"v[148:151],a[112:115],v[160:163],v[148:151]\n"
"s_cmp_eq_u32 s40,s46\n"
"s_cselect_b32 s40,s47,s40\n"
MF"v[148:151],a[116:119],v[168:171],v[148:151]\n"
RD"a[80:83],v226 offset:12288\n"
MF"v[216:219],a[116:119],v[200:203],v[216:219]\n"
MF"v[216:219],a[112:115],v[192:195],v[216:219]\n"
RD"a[84:87],v226 offset:13312\n"
MF"v[156:159],a[120:123],v[160:163],v[156:159]\n"
MF"v[156:159],a[124:127],v[168:171],v[156:159]\n"
RD"a[88:91],v226 offset:14336\n"
MF"v[220:223],a[124:127],v[200:203],v[220:223]\n"
BL"v225,s[8:11],s44 offen offset:1024 sc1 lds\n"
MF"v[220:223],a[120:123],v[192:195],v[220:223]\n"
WT"lgkmcnt(3)\n"
RD"a[92:95],v226 offset:15360\n"
MF"v[0:3],a[64:67],a[0:3],v[0:3]\n"
MF"v[0:3],a[68:71],a[8:11],v[0:3]\n"
RD"a[96:99],v226 offset:16384\n"
MF"v[40:43],a[68:71],a[12:15],v[40:43]\n"
MF"v[40:43],a[64:67],a[4:7],v[40:43]\n"
RD"a[100:103],v226 offset:17408\n"
MF"v[4:7],a[72:75],a[0:3],v[4:7]\n"
BL"v225,s[8:11],s44 offen offset:2048 sc1 lds\n"
MF"v[4:7],a[76:79],a[8:11],v[4:7]\n"
RD"a[104:107],v226 offset:18432\n"
MF"v[44:47],a[76:79],a[12:15],v[44:47]\n"
MF"v[44:47],a[72:75],a[4:7],v[44:47]\n"
WT"lgkmcnt(3)\n"
RD"a[108:111],v226 offset:19456\n"
MF"v[8:11],a[80:83],a[0:3],v[8:11]\n"
MF"v[8:11],a[84:87],a[8:11],v[8:11]\n"
RD"a[112:115],v226 offset:20480\n"
MF"v[48:51],a[84:87],a[12:15],v[48:51]\n"
MF"v[48:51],a[80:83],a[4:7],v[48:51]\n"
RD"a[116:119],v226 offset:21504\n"
MF"v[12:15],a[88:91],a[0:3],v[12:15]\n"
BL"v225,s[8:11],s44 offen offset:3072 sc1 lds\n"
MF"v[12:15],a[92:95],a[8:11],v[12:15]\n"
RD"a[120:123],v226 offset:22528\n"
MF"v[52:55],a[92:95],a[12:15],v[52:55]\n"
MF"v[52:55],a[88:91],a[4:7],v[52:55]\n"
WT"lgkmcnt(3)\n"
RD"a[124:127],v226 offset:23552\n"
MF"v[16:19],a[96:99],a[0:3],v[16:19]\n"
MF"v[16:19],a[100:103],a[8:11],v[16:19]\n"
RD"a[64:67],v226 offset:24576\n"
MF"v[56:59],a[100:103],a[12:15],v[56:59]\n"
MF"v[56:59],a[96:99],a[4:7],v[56:59]\n"
RD"a[68:71],v226 offset:25600\n"
MF"v[20:23],a[104:107],a[0:3],v[20:23]\n"
MF"v[20:23],a[108:111],a[8:11],v[20:23]\n"
RD"a[72:75],v226 offset:26624\n"
MF"v[60:63],a[108:111],a[12:15],v[60:63]\n"
MF"v[60:63],a[104:107],a[4:7],v[60:63]\n"
WT"lgkmcnt(3)\n"
RD"a[76:79],v226 offset:27648\n"
MF"v[24:27],a[112:115],a[0:3],v[24:27]\n"
MF"v[24:27],a[116:119],a[8:11],v[24:27]\n"
RD"a[80:83],v226 offset:28672\n"
MF"v[64:67],a[116:119],a[12:15],v[64:67]\n"
MF"v[64:67],a[112:115],a[4:7],v[64:67]\n"
RD"a[84:87],v226 offset:29696\n"
MF"v[28:31],a[120:123],a[0:3],v[28:31]\n"
MF"v[28:31],a[124:127],a[8:11],v[28:31]\n"
RD"a[88:91],v226 offset:30720\n"
MF"v[68:71],a[124:127],a[12:15],v[68:71]\n"
MF"v[68:71],a[120:123],a[4:7],v[68:71]\n"
WT"lgkmcnt(3)\n"
RD"a[92:95],v226 offset:31744\n"
MF"v[32:35],a[64:67],a[0:3],v[32:35]\n"
"s_barrier\n"
MF"v[32:35],a[68:71],a[8:11],v[32:35]\n"
RD"a[96:99],v235 offset:0\n"
MF"v[72:75],a[68:71],a[12:15],v[72:75]\n"
MF"v[72:75],a[64:67],a[4:7],v[72:75]\n"
RD"a[100:103],v235 offset:1024\n"
MF"v[36:39],a[72:75],a[0:3],v[36:39]\n"
MF"v[36:39],a[76:79],a[8:11],v[36:39]\n"
RD"a[104:107],v235 offset:2048\n"
MF"v[76:79],a[76:79],a[12:15],v[76:79]\n"
MF"v[76:79],a[72:75],a[4:7],v[76:79]\n"
WT"lgkmcnt(3)\n"
RD"a[108:111],v235 offset:3072\n"
MF"v[0:3],a[80:83],a[16:19],v[0:3]\n"
MF"v[0:3],a[84:87],a[24:27],v[0:3]\n"
RD"a[112:115],v235 offset:4096\n"
MF"v[40:43],a[84:87],a[28:31],v[40:43]\n"
MF"v[40:43],a[80:83],a[20:23],v[40:43]\n"
RD"a[116:119],v235 offset:5120\n"
MF"v[4:7],a[88:91],a[16:19],v[4:7]\n"
MF"v[4:7],a[92:95],a[24:27],v[4:7]\n"
RD"a[120:123],v235 offset:6144\n"
MF"v[44:47],a[92:95],a[28:31],v[44:47]\n"
MF"v[44:47],a[88:91],a[20:23],v[44:47]\n"
WT"lgkmcnt(3)\n"
RD"a[124:127],v235 offset:7168\n"
MF"v[8:11],a[96:99],a[16:19],v[8:11]\n"
MF"v[8:11],a[100:103],a[24:27],v[8:11]\n"
RD"a[64:67],v235 offset:8192\n"
MF"v[48:51],a[100:103],a[28:31],v[48:51]\n"
MF"v[48:51],a[96:99],a[20:23],v[48:51]\n"
RD"a[68:71],v235 offset:9216\n"
MF"v[12:15],a[104:107],a[16:19],v[12:15]\n"
MF"v[12:15],a[108:111],a[24:27],v[12:15]\n"
RD"a[72:75],v235 offset:10240\n"
"v_bfe_u32 v238,v224,4,2\n"
MF"v[52:55],a[108:111],a[28:31],v[52:55]\n"
"s_andn2_b32 s63,s62,255\n"
"v_lshlrev_b32 v238,6,v238\n"
MF"v[52:55],a[104:107],a[20:23],v[52:55]\n"
WT"lgkmcnt(3)\n"
RD"a[76:79],v235 offset:11264\n"
MF"v[16:19],a[112:115],a[16:19],v[16:19]\n"
"buffer_load_dwordx2 v[240:241],v238,s[8:11],s63 offen sc1\n"
MF"v[16:19],a[116:119],a[24:27],v[16:19]\n"
RD"a[80:83],v235 offset:12288\n"
MF"v[56:59],a[116:119],a[28:31],v[56:59]\n"
MF"v[56:59],a[112:115],a[20:23],v[56:59]\n"
RD"a[84:87],v235 offset:13312\n"
MF"v[20:23],a[120:123],a[16:19],v[20:23]\n"
MF"v[20:23],a[124:127],a[24:27],v[20:23]\n"
RD"a[88:91],v235 offset:14336\n"
MF"v[60:63],a[124:127],a[28:31],v[60:63]\n"
MF"v[60:63],a[120:123],a[20:23],v[60:63]\n"
RD"a[92:95],v235 offset:15360\n"
"LTJ_%=:\n"
WT"lgkmcnt(7)\n"
MF"v[24:27],a[64:67],a[16:19],v[24:27]\n"
WT"lgkmcnt(3)\n"
MF"v[24:27],a[68:71],a[24:27],v[24:27]\n"
RD"a[96:99],v235 offset:16384\n"
MF"v[64:67],a[68:71],a[28:31],v[64:67]\n"
MF"v[64:67],a[64:67],a[20:23],v[64:67]\n"
RD"a[100:103],v235 offset:17408\n"
MF"v[28:31],a[72:75],a[16:19],v[28:31]\n"
MF"v[28:31],a[76:79],a[24:27],v[28:31]\n"
RD"a[104:107],v235 offset:18432\n"
MF"v[68:71],a[76:79],a[28:31],v[68:71]\n"
WT"vmcnt(1)\n"
MF"v[68:71],a[72:75],a[20:23],v[68:71]\n"
RD"a[108:111],v235 offset:19456\n"
MF"v[32:35],a[80:83],a[16:19],v[32:35]\n"
WT"lgkmcnt(3)\n"
"s_barrier\n"
MF"v[32:35],a[84:87],a[24:27],v[32:35]\n"
RD"a[112:115],v236 offset:4096\n"
MF"v[72:75],a[84:87],a[28:31],v[72:75]\n"
MF"v[72:75],a[80:83],a[20:23],v[72:75]\n"
RD"a[116:119],v236 offset:5120\n"
MF"v[36:39],a[88:91],a[16:19],v[36:39]\n"
MF"v[36:39],a[92:95],a[24:27],v[36:39]\n"
RD"a[120:123],v236 offset:6144\n"
MF"v[76:79],a[92:95],a[28:31],v[76:79]\n"
MF"v[76:79],a[88:91],a[20:23],v[76:79]\n"
RD"a[124:127],v236 offset:7168\n"
MF"v[0:3],a[96:99],a[180:183],v[0:3]\n"
WT"lgkmcnt(3)\n"
MF"v[0:3],a[100:103],a[188:191],v[0:3]\n"
RD"a[64:67],v236 offset:24576\n"
MF"v[40:43],a[100:103],a[192:195],v[40:43]\n"
MF"v[40:43],a[96:99],a[184:187],v[40:43]\n"
RD"a[68:71],v236 offset:25600\n"
MF"v[4:7],a[104:107],a[180:183],v[4:7]\n"
MF"v[4:7],a[108:111],a[188:191],v[4:7]\n"
RD"a[72:75],v236 offset:26624\n"
MF"v[44:47],a[108:111],a[192:195],v[44:47]\n"
MF"v[44:47],a[104:107],a[184:187],v[44:47]\n"
RD"a[76:79],v236 offset:27648\n"
MF"v[0:3],a[112:115],a[196:199],v[0:3]\n"
WT"lgkmcnt(3)\n"
MF"v[0:3],a[116:119],a[204:207],v[0:3]\n"
RD"a[80:83],v237 offset:12288\n"
MF"v[40:43],a[116:119],a[208:211],v[40:43]\n"
MF"v[40:43],a[112:115],a[200:203],v[40:43]\n"
RD"a[84:87],v237 offset:13312\n"
MF"v[4:7],a[120:123],a[196:199],v[4:7]\n"
MF"v[4:7],a[124:127],a[204:207],v[4:7]\n"
SA"s48,s60,32768\n"
RD"a[88:91],v237 offset:14336\n"
MF"v[44:47],a[124:127],a[208:211],v[44:47]\n"
"s_and_b32 s48,s48,0x18000\n"
MF"v[44:47],a[120:123],a[200:203],v[44:47]\n"
"s_andn2_b32 s49,s60,0x18000\n"
RD"a[92:95],v237 offset:15360\n"
MF"v[0:3],a[64:67],a[212:215],v[0:3]\n"
WT"lgkmcnt(3)\n"
WT"vmcnt(0)\n"
"v_cmp_eq_u64 vcc,s[28:29],v[240:241]\n"
"s_cmp_eq_u64 vcc,exec\n"
"s_cbranch_scc0 L1083_%=\n"
"L1084_%=:\n"
MF"v[0:3],a[68:71],a[220:223],v[0:3]\n"
"s_or_b32 s52,s48,s49\n"
RD"a[96:99],v235 offset:20480\n"
SA"s53,s52,16384\n"
MF"v[40:43],a[68:71],a[224:227],v[40:43]\n"
BL"a[244:247],v224,s[8:11],s52 offen offset:0 sc1\n"
MF"v[40:43],a[64:67],a[216:219],v[40:43]\n"
RD"a[100:103],v235 offset:21504\n"
MF"v[4:7],a[72:75],a[212:215],v[4:7]\n"
MF"v[4:7],a[76:79],a[220:223],v[4:7]\n"
RD"a[104:107],v235 offset:22528\n"
MF"v[44:47],a[76:79],a[224:227],v[44:47]\n"
"v_bfe_u32 v243,v224,6,2\n"
MF"v[44:47],a[72:75],a[216:219],v[44:47]\n"
RD"a[108:111],v235 offset:23552\n"
"v_bfe_u32 v244,v224,4,2\n"
MF"v[0:3],a[80:83],a[228:231],v[0:3]\n"
WT"lgkmcnt(3)\n"
BL"a[248:251],v224,s[8:11],s53 offen offset:0 sc1\n"
MF"v[0:3],a[84:87],a[236:239],v[0:3]\n"
RD"a[112:115],v236 offset:8192\n"
"v_lshrrev_b32 v245,8,v224\n"
MF"v[40:43],a[84:87],a[240:243],v[40:43]\n"
"v_lshlrev_b32 v245,3,v245\n"
"v_sub_u32 v244,v244,v245\n"
MF"v[40:43],a[80:83],a[232:235],v[40:43]\n"
RD"a[116:119],v236 offset:9216\n"
"v_and_b32 v245,16,v224\n"
MF"v[4:7],a[88:91],a[228:231],v[4:7]\n"
"v_mov_b32 v246,0x3f80\n"
MF"v[4:7],a[92:95],a[236:239],v[4:7]\n"
RD"a[120:123],v236 offset:10240\n"
"v_lshlrev_b32 v245,v245,v246\n"
MF"v[44:47],a[92:95],a[240:243],v[44:47]\n"
BL"a[252:255],v224,s[8:11],s52 offen offset:1024 sc1\n"
MF"v[44:47],a[88:91],a[232:235],v[44:47]\n"
RD"a[124:127],v236 offset:11264\n"
"v_lshlrev_b32 v246,4,v243\n"
MF"v[8:11],a[96:99],a[180:183],v[8:11]\n"
WT"lgkmcnt(3)\n"
"v_and_b32 v246,16,v246\n"
"v_and_b32 v247,2,v243\n"
MF"v[8:11],a[100:103],a[188:191],v[8:11]\n"
SA"s90,s88,0\n"
RD"a[64:67],v236 offset:28672\n"
PK"v0,v0,v1\n"
MF"v[48:51],a[100:103],a[192:195],v[48:51]\n"
BL"a[128:131],v224,s[8:11],s53 offen offset:1024 sc1\n"
MF"v[48:51],a[96:99],a[184:187],v[48:51]\n"
RD"a[68:71],v236 offset:29696\n"
PK"v1,v2,v3\n"
MF"v[12:15],a[104:107],a[180:183],v[12:15]\n"
"v_lshl_add_u32 v246,v247,1,v246\n"
PK"v2,v4,v5\n"
MF"v[12:15],a[108:111],a[188:191],v[12:15]\n"
RD"a[72:75],v236 offset:30720\n"
PK"v3,v6,v7\n"
"v_add3_u32 v246,v246,v244,0\n"
MF"v[52:55],a[108:111],a[192:195],v[52:55]\n"
BS"v[0:3],v224,s[8:11],s90 offen sc1\n"
"v_ashrrev_i32 v246,1,v246\n"
MF"v[52:55],a[104:107],a[184:187],v[52:55]\n"
RD"a[76:79],v236 offset:31744\n"
"v_cmp_eq_u32_e64 vcc,0,v246\n"
MF"v[8:11],a[112:115],a[196:199],v[8:11]\n"
WT"lgkmcnt(3)\n"
BL"a[132:135],v224,s[8:11],s52 offen offset:2048 sc1\n"
"v_cndmask_b32_e64 v80,0,v245,vcc\n"
MF"v[8:11],a[116:119],a[204:207],v[8:11]\n"
SA"s91,s22,0\n"
RD"a[80:83],v237 offset:16384\n"
"v_cmp_eq_u32_e64 s[92:93],1,v246\n"
MF"v[48:51],a[116:119],a[208:211],v[48:51]\n"
"v_cmp_eq_u32_e64 s[94:95],2,v246\n"
"v_cndmask_b32_e64 v81,0,v245,s[92:93]\n"
MF"v[48:51],a[112:115],a[200:203],v[48:51]\n"
RD"a[84:87],v237 offset:17408\n"
PK"v40,v40,v41\n"
MF"v[12:15],a[120:123],a[196:199],v[12:15]\n"
PK"v41,v42,v43\n"
"v_cndmask_b32_e64 v82,0,v245,s[94:95]\n"
MF"v[12:15],a[124:127],a[204:207],v[12:15]\n"
RD"a[88:91],v237 offset:18432\n"
PK"v42,v44,v45\n"
PK"v43,v46,v47\n"
MF"v[52:55],a[124:127],a[208:211],v[52:55]\n"
BS"v[40:43],v224,s[8:11],s91 offen sc1\n"
"v_cmp_eq_u32_e64 vcc,3,v246\n"
MF"v[52:55],a[120:123],a[200:203],v[52:55]\n"
BL"a[136:139],v224,s[8:11],s53 offen offset:2048 sc1\n"
"v_cndmask_b32_e64 v83,0,v245,vcc\n"
MF"v[8:11],a[64:67],a[212:215],v[8:11]\n"
WT"lgkmcnt(2)\n"
RD"a[92:95],v237 offset:19456\n"
"v_lshlrev_b32 v246,4,v243\n"
"v_and_b32 v246,16,v246\n"
MF"v[8:11],a[68:71],a[220:223],v[8:11]\n"
RD"a[96:99],v235 offset:24576\n"
"v_and_b32 v247,2,v243\n"
MF"v[48:51],a[68:71],a[224:227],v[48:51]\n"
"v_lshl_add_u32 v246,v247,1,v246\n"
"v_add3_u32 v246,v246,v244,8\n"
MF"v[48:51],a[64:67],a[216:219],v[48:51]\n"
RD"a[100:103],v235 offset:25600\n"
"v_ashrrev_i32 v246,1,v246\n"
MF"v[12:15],a[72:75],a[212:215],v[12:15]\n"
BL"a[140:143],v224,s[8:11],s52 offen offset:3072 sc1\n"
MF"v[12:15],a[76:79],a[220:223],v[12:15]\n"
RD"a[104:107],v235 offset:26624\n"
"v_cmp_eq_u32_e64 vcc,0,v246\n"
MF"v[52:55],a[76:79],a[224:227],v[52:55]\n"
"v_cmp_eq_u32_e64 s[92:93],1,v246\n"
"v_cndmask_b32_e64 v88,0,v245,vcc\n"
MF"v[52:55],a[72:75],a[216:219],v[52:55]\n"
RD"a[108:111],v235 offset:27648\n"
"v_cndmask_b32_e64 v89,0,v245,s[92:93]\n"
MF"v[8:11],a[80:83],a[228:231],v[8:11]\n"
WT"lgkmcnt(3)\n"
BL"a[144:147],v224,s[8:11],s53 offen offset:3072 sc1\n"
MF"v[8:11],a[84:87],a[236:239],v[8:11]\n"
SA"s48,s60,65536\n"
RD"a[112:115],v236 offset:12288\n"
"v_cmp_eq_u32_e64 s[94:95],2,v246\n"
MF"v[48:51],a[84:87],a[240:243],v[48:51]\n"
"s_and_b32 s48,s48,0x18000\n"
"v_cndmask_b32_e64 v90,0,v245,s[94:95]\n"
"v_cmp_eq_u32_e64 vcc,3,v246\n"
MF"v[48:51],a[80:83],a[232:235],v[48:51]\n"
"s_andn2_b32 s49,s60,0x18000\n"
RD"a[116:119],v236 offset:13312\n"
"v_cndmask_b32_e64 v91,0,v245,vcc\n"
MF"v[12:15],a[88:91],a[228:231],v[12:15]\n"
"s_or_b32 s52,s48,s49\n"
"v_lshl_add_u32 v246,v243,3,v244\n"
"v_add_u32 v246,0,v246\n"
MF"v[12:15],a[92:95],a[236:239],v[12:15]\n"
SA"s53,s52,16384\n"
RD"a[120:123],v236 offset:14336\n"
"v_ashrrev_i32 v246,1,v246\n"
MF"v[52:55],a[92:95],a[240:243],v[52:55]\n"
BL"a[148:151],v224,s[8:11],s52 offen offset:0 sc1\n"
MF"v[52:55],a[88:91],a[232:235],v[52:55]\n"
RD"a[124:127],v236 offset:15360\n"
"v_cmp_eq_u32_e64 vcc,0,v246\n"
MF"v[16:19],a[96:99],a[180:183],v[16:19]\n"
WT"lgkmcnt(3)\n"
"v_cndmask_b32_e64 v96,0,v245,vcc\n"
"v_cmp_eq_u32_e64 s[92:93],1,v246\n"
MF"v[16:19],a[100:103],a[188:191],v[16:19]\n"
SA"s90,s88,1024\n"
RD"a[64:67],v237 offset:0\n"
"v_cndmask_b32_e64 v97,0,v245,s[92:93]\n"
MF"v[56:59],a[100:103],a[192:195],v[56:59]\n"
PK"v8,v8,v9\n"
PK"v9,v10,v11\n"
MF"v[56:59],a[96:99],a[184:187],v[56:59]\n"
RD"a[68:71],v237 offset:1024\n"
"v_cmp_eq_u32_e64 s[94:95],2,v246\n"
MF"v[20:23],a[104:107],a[180:183],v[20:23]\n"
BL"a[152:155],v224,s[8:11],s53 offen offset:0 sc1\n"
MF"v[20:23],a[108:111],a[188:191],v[20:23]\n"
RD"a[72:75],v237 offset:2048\n"
PK"v10,v12,v13\n"
PK"v11,v14,v15\n"
MF"v[60:63],a[108:111],a[192:195],v[60:63]\n"
BS"v[8:11],v224,s[8:11],s90 offen sc1\n"
"v_cndmask_b32_e64 v98,0,v245,s[94:95]\n"
MF"v[60:63],a[104:107],a[184:187],v[60:63]\n"
RD"a[76:79],v237 offset:3072\n"
"v_cmp_eq_u32_e64 vcc,3,v246\n"
MF"v[16:19],a[112:115],a[196:199],v[16:19]\n"
WT"lgkmcnt(3)\n"
"v_cndmask_b32_e64 v99,0,v245,vcc\n"
"v_lshl_add_u32 v246,v243,3,v244\n"
MF"v[16:19],a[116:119],a[204:207],v[16:19]\n"
SA"s91,s22,1024\n"
RD"a[80:83],v237 offset:20480\n"
"v_add_u32 v246,4,v246\n"
MF"v[56:59],a[116:119],a[208:211],v[56:59]\n"
BL"a[156:159],v224,s[8:11],s52 offen offset:1024 sc1\n"
"v_ashrrev_i32 v246,1,v246\n"
MF"v[56:59],a[112:115],a[200:203],v[56:59]\n"
RD"a[84:87],v237 offset:21504\n"
"v_cmp_eq_u32_e64 vcc,0,v246\n"
MF"v[20:23],a[120:123],a[196:199],v[20:23]\n"
PK"v48,v48,v49\n"
PK"v49,v50,v51\n"
"v_cndmask_b32_e64 v104,0,v245,vcc\n"
MF"v[20:23],a[124:127],a[204:207],v[20:23]\n"
RD"a[88:91],v237 offset:22528\n"
PK"v50,v52,v53\n"
PK"v51,v54,v55\n"
MF"v[60:63],a[124:127],a[208:211],v[60:63]\n"
BS"v[48:51],v224,s[8:11],s91 offen sc1\n"
"v_cmp_eq_u32_e64 s[92:93],1,v246\n"
MF"v[60:63],a[120:123],a[200:203],v[60:63]\n"
BL"a[160:163],v224,s[8:11],s53 offen offset:1024 sc1\n"
"v_cndmask_b32_e64 v105,0,v245,s[92:93]\n"
MF"v[16:19],a[64:67],a[212:215],v[16:19]\n"
WT"lgkmcnt(2)\n"
RD"a[92:95],v237 offset:23552\n"
"v_cmp_eq_u32_e64 s[94:95],2,v246\n"
MF"v[16:19],a[68:71],a[220:223],v[16:19]\n"
"s_mov_b32 s12,s64\n"
RD"a[96:99],v235 offset:28672\n"
"v_cndmask_b32_e64 v106,0,v245,s[94:95]\n"
"v_cmp_eq_u32_e64 vcc,3,v246\n"
MF"v[56:59],a[68:71],a[224:227],v[56:59]\n"
"s_and_b32 s13,s65,0xffff\n"
"v_cndmask_b32_e64 v107,0,v245,vcc\n"
"s_mov_b32 s14,2621440\n"
MF"v[56:59],a[64:67],a[216:219],v[56:59]\n"
"s_mov_b32 s15,0x20000\n"
RD"a[100:103],v235 offset:29696\n"
MF"v[20:23],a[72:75],a[212:215],v[20:23]\n"
"s_mov_b32 s16,s86\n"
BL"a[164:167],v224,s[8:11],s52 offen offset:2048 sc1\n"
MF"v[20:23],a[76:79],a[220:223],v[20:23]\n"
"s_and_b32 s17,s87,0xffff\n"
RD"a[104:107],v235 offset:30720\n"
MF"v[60:63],a[76:79],a[224:227],v[60:63]\n"
"s_mov_b32 s18,2621440\n"
MF"v[60:63],a[72:75],a[216:219],v[60:63]\n"
"s_mov_b32 s19,0x20000\n"
RD"a[108:111],v235 offset:31744\n"
MF"v[16:19],a[80:83],a[228:231],v[16:19]\n"
WT"lgkmcnt(3)\n"
"v_bfe_u32 v243,v224,4,4\n"
MF"v[16:19],a[84:87],a[236:239],v[16:19]\n"
"s_lshl_b32 s92,s34,3\n"
RD"a[112:115],v236 offset:16384\n"
MF"v[56:59],a[84:87],a[240:243],v[56:59]\n"
SA"s93,s92,s32\n"
BL"a[168:171],v224,s[8:11],s53 offen offset:2048 sc1\n"
MF"v[56:59],a[80:83],a[232:235],v[56:59]\n"
"s_sub_u32 s92,s32,s92\n"
RD"a[116:119],v236 offset:17408\n"
MF"v[20:23],a[88:91],a[228:231],v[20:23]\n"
SA"s92,s92,120\n"
MF"v[20:23],a[92:95],a[236:239],v[20:23]\n"
RD"a[120:123],v236 offset:18432\n"
"v_lshl_add_u32 v244,s93,4,v243\n"
MF"v[60:63],a[92:95],a[240:243],v[60:63]\n"
"v_lshl_add_u32 v245,s92,4,v243\n"
MF"v[60:63],a[88:91],a[232:235],v[60:63]\n"
"s_mul_i32 s93,s93,20480\n"
RD"a[124:127],v236 offset:19456\n"
MF"v[24:27],a[96:99],a[180:183],v[24:27]\n"
WT"lgkmcnt(3)\n"
BL"a[172:175],v224,s[8:11],s52 offen offset:3072 sc1\n"
MF"v[24:27],a[100:103],a[188:191],v[24:27]\n"
"s_mul_i32 s92,s92,20480\n"
RD"a[64:67],v237 offset:4096\n"
PK"v16,v16,v17\n"
MF"v[64:67],a[100:103],a[192:195],v[64:67]\n"
SA"s90,s88,2048\n"
PK"v17,v18,v19\n"
PK"v18,v20,v21\n"
MF"v[64:67],a[96:99],a[184:187],v[64:67]\n"
SA"s93,s93,452608\n"
RD"a[68:71],v237 offset:5120\n"
PK"v19,v22,v23\n"
MF"v[28:31],a[104:107],a[180:183],v[28:31]\n"
SA"s92,s92,452608\n"
BL"a[176:179],v224,s[8:11],s53 offen offset:3072 sc1\n"
MF"v[28:31],a[108:111],a[188:191],v[28:31]\n"
SA"s48,s60,98304\n"
BS"v[16:19],v224,s[8:11],s90 offen sc1\n"
MF"v[68:71],a[108:111],a[192:195],v[68:71]\n"
"s_and_b32 s48,s48,0x18000\n"
RD"a[72:75],v237 offset:6144\n"
"v_add_u32 v178,s93,v224\n"
MF"v[68:71],a[104:107],a[184:187],v[68:71]\n"
"s_andn2_b32 s49,s60,0x18000\n"
RD"a[76:79],v237 offset:7168\n"
"v_add_u32 v179,s92,v224\n"
MF"v[24:27],a[112:115],a[196:199],v[24:27]\n"
WT"lgkmcnt(3)\n"
"v_lshrrev_b32 v243,4,v224\n"
"v_and_b32 v243,48,v243\n"
MF"v[24:27],a[116:119],a[204:207],v[24:27]\n"
"s_or_b32 s52,s48,s49\n"
RD"a[80:83],v237 offset:24576\n"
"v_mul_u32_u24 v176,1280,v244\n"
MF"v[64:67],a[116:119],a[208:211],v[64:67]\n"
SA"s53,s52,16384\n"
BL"a[32:35],v224,s[8:11],s52 offen offset:0 sc1\n"
MF"v[64:67],a[112:115],a[200:203],v[64:67]\n"
SA"s91,s22,2048\n"
RD"a[84:87],v237 offset:25600\n"
"v_mul_u32_u24 v177,1280,v245\n"
MF"v[28:31],a[120:123],a[196:199],v[28:31]\n"
PK"v56,v56,v57\n"
PK"v57,v58,v59\n"
"v_add_u32 v176,v176,v243\n"
MF"v[28:31],a[124:127],a[204:207],v[28:31]\n"
RD"a[88:91],v237 offset:26624\n"
PK"v58,v60,v61\n"
PK"v59,v62,v63\n"
MF"v[68:71],a[124:127],a[208:211],v[68:71]\n"
BS"v[56:59],v224,s[8:11],s91 offen sc1\n"
"v_add_u32 v177,v177,v243\n"
MF"v[68:71],a[120:123],a[200:203],v[68:71]\n"
RD"a[92:95],v237 offset:27648\n"
"v_and_b32 v243,0x300,v224\n"
"v_sub_u32 v244,2047,v244\n"
MF"v[24:27],a[64:67],a[212:215],v[24:27]\n"
WT"lgkmcnt(3)\n"
BL"a[36:39],v224,s[8:11],s53 offen offset:0 sc1\n"
"v_sub_u32 v245,2047,v245\n"
MF"v[24:27],a[68:71],a[220:223],v[24:27]\n"
RD"a[96:99],v236 offset:0\n"
"v_readlane_b32 s92,v234,0\n"
MF"v[64:67],a[68:71],a[224:227],v[64:67]\n"
"s_and_b32 s92,s92,0xffff\n"
"v_cmp_eq_u32 vcc,s92,v244\n"
"v_cmp_eq_u32_e64 s[94:95],s92,v245\n"
MF"v[64:67],a[64:67],a[216:219],v[64:67]\n"
RD"a[100:103],v236 offset:1024\n"
"v_cndmask_b32 v178,v178,v243,vcc\n"
MF"v[28:31],a[72:75],a[212:215],v[28:31]\n"
"v_cndmask_b32_e64 v179,v179,v243,s[94:95]\n"
"v_add_u32 v243,16,v243\n"
MF"v[28:31],a[76:79],a[220:223],v[28:31]\n"
RD"a[104:107],v236 offset:2048\n"
"v_readlane_b32 s92,v234,1\n"
MF"v[68:71],a[76:79],a[224:227],v[68:71]\n"
"s_and_b32 s92,s92,0xffff\n"
BL"a[40:43],v224,s[8:11],s52 offen offset:1024 sc1\n"
MF"v[68:71],a[72:75],a[216:219],v[68:71]\n"
RD"a[108:111],v236 offset:3072\n"
"v_cmp_eq_u32 vcc,s92,v244\n"
MF"v[24:27],a[80:83],a[228:231],v[24:27]\n"
WT"lgkmcnt(3)\n"
"v_cmp_eq_u32_e64 s[94:95],s92,v245\n"
MF"v[24:27],a[84:87],a[236:239],v[24:27]\n"
RD"a[112:115],v236 offset:20480\n"
"v_cndmask_b32 v178,v178,v243,vcc\n"
MF"v[64:67],a[84:87],a[240:243],v[64:67]\n"
BL"a[44:47],v224,s[8:11],s53 offen offset:1024 sc1\n"
MF"v[64:67],a[80:83],a[232:235],v[64:67]\n"
RD"a[116:119],v236 offset:21504\n"
"v_cndmask_b32_e64 v179,v179,v243,s[94:95]\n"
MF"v[28:31],a[88:91],a[228:231],v[28:31]\n"
"v_add_u32 v243,16,v243\n"
"v_readlane_b32 s92,v234,2\n"
MF"v[28:31],a[92:95],a[236:239],v[28:31]\n"
"s_and_b32 s92,s92,0xffff\n"
RD"a[120:123],v236 offset:22528\n"
MF"v[68:71],a[92:95],a[240:243],v[68:71]\n"
"v_cmp_eq_u32 vcc,s92,v244\n"
MF"v[68:71],a[88:91],a[232:235],v[68:71]\n"
RD"a[124:127],v236 offset:23552\n"
"v_cmp_eq_u32_e64 s[94:95],s92,v245\n"
MF"v[32:35],a[96:99],a[180:183],v[32:35]\n"
WT"lgkmcnt(3)\n"
BL"a[48:51],v224,s[8:11],s52 offen offset:2048 sc1\n"
MF"v[32:35],a[100:103],a[188:191],v[32:35]\n"
SA"s90,s88,3072\n"
RD"a[64:67],v237 offset:8192\n"
"v_cndmask_b32 v178,v178,v243,vcc\n"
MF"v[72:75],a[100:103],a[192:195],v[72:75]\n"
PK"v24,v24,v25\n"
PK"v25,v26,v27\n"
MF"v[72:75],a[96:99],a[184:187],v[72:75]\n"
RD"a[68:71],v237 offset:9216\n"
"v_cndmask_b32_e64 v179,v179,v243,s[94:95]\n"
MF"v[36:39],a[104:107],a[180:183],v[36:39]\n"
PK"v26,v28,v29\n"
PK"v27,v30,v31\n"
MF"v[36:39],a[108:111],a[188:191],v[36:39]\n"
BS"v[24:27],v224,s[8:11],s90 offen sc1\n"
"v_add_u32 v243,16,v243\n"
MF"v[76:79],a[108:111],a[192:195],v[76:79]\n"
RD"a[72:75],v237 offset:10240\n"
"v_readlane_b32 s92,v234,3\n"
MF"v[76:79],a[104:107],a[184:187],v[76:79]\n"
"s_and_b32 s92,s92,0xffff\n"
BL"a[52:55],v224,s[8:11],s53 offen offset:2048 sc1\n"
MF"v[32:35],a[112:115],a[196:199],v[32:35]\n"
WT"lgkmcnt(2)\n"
RD"a[76:79],v237 offset:11264\n"
"v_cmp_eq_u32 vcc,s92,v244\n"
MF"v[32:35],a[116:119],a[204:207],v[32:35]\n"
SA"s91,s22,3072\n"
RD"a[80:83],v237 offset:28672\n"
"v_cmp_eq_u32_e64 s[94:95],s92,v245\n"
MF"v[72:75],a[116:119],a[208:211],v[72:75]\n"
"v_cndmask_b32 v178,v178,v243,vcc\n"
"v_cndmask_b32_e64 v179,v179,v243,s[94:95]\n"
MF"v[72:75],a[112:115],a[200:203],v[72:75]\n"
RD"a[84:87],v237 offset:29696\n"
PK"v64,v64,v65\n"
MF"v[36:39],a[120:123],a[196:199],v[36:39]\n"
BL"a[56:59],v224,s[8:11],s52 offen offset:3072 sc1\n"
PK"v65,v66,v67\n"
MF"v[36:39],a[124:127],a[204:207],v[36:39]\n"
RD"a[88:91],v237 offset:30720\n"
"v_add_u32 v243,16,v243\n"
PK"v66,v68,v69\n"
MF"v[76:79],a[124:127],a[208:211],v[76:79]\n"
PK"v67,v70,v71\n"
"v_readlane_b32 s92,v234,4\n"
MF"v[76:79],a[120:123],a[200:203],v[76:79]\n"
"s_and_b32 s92,s92,0xffff\n"
BS"v[64:67],v224,s[8:11],s91 offen sc1\n"
"v_cmp_eq_u32 vcc,s92,v244\n"
MF"v[32:35],a[64:67],a[212:215],v[32:35]\n"
WT"lgkmcnt(2)\n"
RD"a[92:95],v237 offset:31744\n"
"v_cmp_eq_u32_e64 s[94:95],s92,v245\n"
MF"v[32:35],a[68:71],a[220:223],v[32:35]\n"
BL"a[60:63],v224,s[8:11],s53 offen offset:3072 sc1\n"
"v_cndmask_b32 v178,v178,v243,vcc\n"
MF"v[72:75],a[68:71],a[224:227],v[72:75]\n"
RD"a[96:99],v226 offset:8192\n"
"v_cndmask_b32_e64 v179,v179,v243,s[94:95]\n"
MF"v[72:75],a[64:67],a[216:219],v[72:75]\n"
RD"a[100:103],v226 offset:9216\n"
"v_add_u32 v243,16,v243\n"
MF"v[36:39],a[72:75],a[212:215],v[36:39]\n"
"v_readlane_b32 s92,v234,5\n"
"s_and_b32 s92,s92,0xffff\n"
MF"v[36:39],a[76:79],a[220:223],v[36:39]\n"
RD"a[104:107],v226 offset:10240\n"
"v_cmp_eq_u32 vcc,s92,v244\n"
MF"v[76:79],a[76:79],a[224:227],v[76:79]\n"
"v_cmp_eq_u32_e64 s[94:95],s92,v245\n"
"v_cndmask_b32 v178,v178,v243,vcc\n"
MF"v[76:79],a[72:75],a[216:219],v[76:79]\n"
RD"a[108:111],v226 offset:11264\n"
"v_cndmask_b32_e64 v179,v179,v243,s[94:95]\n"
MF"v[32:35],a[80:83],a[228:231],v[32:35]\n"
WT"lgkmcnt(4)\n"
"v_add_u32 v243,16,v243\n"
"v_readlane_b32 s92,v234,6\n"
MF"v[32:35],a[84:87],a[236:239],v[32:35]\n"
"s_and_b32 s92,s92,0xffff\n"
RD"a[112:115],v226 offset:28672\n"
MF"v[72:75],a[84:87],a[240:243],v[72:75]\n"
"v_cmp_eq_u32 vcc,s92,v244\n"
MF"v[72:75],a[80:83],a[232:235],v[72:75]\n"
RD"a[116:119],v226 offset:29696\n"
"v_cmp_eq_u32_e64 s[94:95],s92,v245\n"
MF"v[36:39],a[88:91],a[228:231],v[36:39]\n"
"v_cndmask_b32 v178,v178,v243,vcc\n"
MF"v[36:39],a[92:95],a[236:239],v[36:39]\n"
RD"a[120:123],v226 offset:30720\n"
"v_cndmask_b32_e64 v179,v179,v243,s[94:95]\n"
MF"v[76:79],a[92:95],a[240:243],v[76:79]\n"
"v_add_u32 v243,16,v243\n"
MF"v[76:79],a[88:91],a[232:235],v[76:79]\n"
WT"vmcnt(31) lgkmcnt(3)\n"
RD"a[124:127],v226 offset:31744\n"
"v_readlane_b32 s92,v234,7\n"
MF"v[84:87],a[96:99],a[244:247],v[84:87]\n"
WT"vmcnt(29)\n"
"s_and_b32 s92,s92,0xffff\n"
MF"v[84:87],a[100:103],a[252:255],v[84:87]\n"
WT"vmcnt(28)\n"
RD"a[64:67],v235 offset:16384\n"
PK"v32,v32,v33\n"
MF"v[164:167],a[100:103],a[128:131],v[164:167]\n"
SA"s90,s88,4096\n"
PK"v33,v34,v35\n"
"v_cmp_eq_u32 vcc,s92,v244\n"
MF"v[164:167],a[96:99],a[248:251],v[164:167]\n"
RD"a[68:71],v235 offset:17408\n"
PK"v34,v36,v37\n"
MF"v[92:95],a[104:107],a[244:247],v[92:95]\n"
PK"v35,v38,v39\n"
"v_cmp_eq_u32_e64 s[94:95],s92,v245\n"
MF"v[92:95],a[108:111],a[252:255],v[92:95]\n"
"s_mul_i32 s92,s33,5\n"
BS"v[32:35],v224,s[8:11],s90 offen sc1\n"
MF"v[172:175],a[108:111],a[128:131],v[172:175]\n"
SA"s93,s92,s30\n"
RD"a[72:75],v235 offset:18432\n"
"v_cndmask_b32 v178,v178,v243,vcc\n"
MF"v[172:175],a[104:107],a[248:251],v[172:175]\n"
WT"vmcnt(27) lgkmcnt(3)\n"
RD"a[76:79],v235 offset:19456\n"
"v_cndmask_b32_e64 v179,v179,v243,s[94:95]\n"
MF"v[84:87],a[112:115],a[132:135],v[84:87]\n"
WT"vmcnt(24)\n"
SA"s92,s92,4\n"
"s_lshl_b32 s38,s93,6\n"
MF"v[84:87],a[116:119],a[140:143],v[84:87]\n"
WT"vmcnt(23)\n"
RD"a[80:83],v236 offset:4096\n"
PK"v72,v72,v73\n"
MF"v[164:167],a[116:119],a[144:147],v[164:167]\n"
SA"s91,s22,4096\n"
PK"v73,v74,v75\n"
PK"v74,v76,v77\n"
MF"v[164:167],a[112:115],a[136:139],v[164:167]\n"
"s_lshl_b32 s55,s92,6\n"
RD"a[84:87],v236 offset:5120\n"
PK"v75,v78,v79\n"
MF"v[92:95],a[120:123],a[132:135],v[92:95]\n"
SA"s55,s55,s45\n"
BS"v[72:75],v224,s[8:11],s91 offen sc1\n"
MF"v[92:95],a[124:127],a[140:143],v[92:95]\n"
"s_lshl_b32 s36,s93,10\n"
RD"a[88:91],v236 offset:6144\n"
"s_lshl_b32 s37,s92,10\n"
MF"v[172:175],a[124:127],a[144:147],v[172:175]\n"
SA"s36,s36,71680\n"
SA"s37,s37,71680\n"
MF"v[172:175],a[120:123],a[136:139],v[172:175]\n"
WT"vmcnt(23) lgkmcnt(3)\n"
RD"a[92:95],v236 offset:7168\n"
SA"s37,s37,s45\n"
MF"v[84:87],a[64:67],a[148:151],v[84:87]\n"
WT"vmcnt(20)\n"
"v_lshrrev_b32 v243,1,v224\n"
MF"v[84:87],a[68:71],a[156:159],v[84:87]\n"
WT"vmcnt(18)\n"
RD"a[96:99],v236 offset:24576\n"
"v_min_u32 v244,312,v243\n"
MF"v[164:167],a[68:71],a[160:163],v[164:167]\n"
"v_and_b32 v243,56,v243\n"
MF"v[164:167],a[64:67],a[152:155],v[164:167]\n"
RD"a[100:103],v236 offset:25600\n"
"v_lshlrev_b32 v243,3,v243\n"
MF"v[92:95],a[72:75],a[148:151],v[92:95]\n"
"s_mov_b32 s92,6400\n"
MF"v[92:95],a[76:79],a[156:159],v[92:95]\n"
RD"a[104:107],v236 offset:26624\n"
MF"v[172:175],a[76:79],a[160:163],v[172:175]\n"
MF"v[172:175],a[72:75],a[152:155],v[172:175]\n"
WT"vmcnt(17) lgkmcnt(3)\n"
RD"a[108:111],v236 offset:27648\n"
MF"v[84:87],a[80:83],a[164:167],v[84:87]\n"
WT"vmcnt(15)\n"
MF"v[84:87],a[84:87],a[172:175],v[84:87]\n"
WT"vmcnt(14)\n"
RD"a[112:115],v237 offset:12288\n"
MF"v[164:167],a[84:87],a[176:179],v[164:167]\n"
MF"v[164:167],a[80:83],a[168:171],v[164:167]\n"
RD"a[116:119],v237 offset:13312\n"
MF"v[92:95],a[88:91],a[164:167],v[92:95]\n"
MF"v[92:95],a[92:95],a[172:175],v[92:95]\n"
RD"a[120:123],v237 offset:14336\n"
MF"v[172:175],a[92:95],a[176:179],v[172:175]\n"
MF"v[172:175],a[88:91],a[168:171],v[172:175]\n"
WT"vmcnt(12) lgkmcnt(3)\n"
RD"a[124:127],v237 offset:15360\n"
MF"v[84:87],a[96:99],a[32:35],v[84:87]\n"
WT"vmcnt(9)\n"
MF"v[84:87],a[100:103],a[40:43],v[84:87]\n"
WT"vmcnt(8)\n"
RD"a[64:67],v226 offset:12288\n"
MF"v[164:167],a[100:103],a[44:47],v[164:167]\n"
MF"v[164:167],a[96:99],a[36:39],v[164:167]\n"
RD"a[68:71],v226 offset:13312\n"
MF"v[92:95],a[104:107],a[32:35],v[92:95]\n"
MF"v[92:95],a[108:111],a[40:43],v[92:95]\n"
RD"a[72:75],v226 offset:14336\n"
MF"v[172:175],a[108:111],a[44:47],v[172:175]\n"
MF"v[172:175],a[104:107],a[36:39],v[172:175]\n"
WT"vmcnt(7) lgkmcnt(3)\n"
RD"a[76:79],v226 offset:15360\n"
MF"v[84:87],a[112:115],a[48:51],v[84:87]\n"
WT"vmcnt(4)\n"
"buffer_load_dwordx2 v[240:241],v244,s[8:11],s92 offen sc1\n"
MF"v[84:87],a[116:119],a[56:59],v[84:87]\n"
WT"vmcnt(3)\n"
RD"a[80:83],v235 offset:0\n"
MF"v[164:167],a[116:119],a[60:63],v[164:167]\n"
MF"v[164:167],a[112:115],a[52:55],v[164:167]\n"
RD"a[84:87],v235 offset:1024\n"
MF"v[92:95],a[120:123],a[48:51],v[92:95]\n"
MF"v[92:95],a[124:127],a[56:59],v[92:95]\n"
RD"a[88:91],v235 offset:2048\n"
MF"v[172:175],a[124:127],a[60:63],v[172:175]\n"
MF"v[172:175],a[120:123],a[52:55],v[172:175]\n"
WT"lgkmcnt(3)\n"
RD"a[92:95],v235 offset:3072\n"
MF"v[100:103],a[64:67],a[244:247],v[100:103]\n"
MF"v[100:103],a[68:71],a[252:255],v[100:103]\n"
SA"s90,s88,327680\n"
RD"a[96:99],v235 offset:20480\n"
PK"v84,v84,v85\n"
MF"v[180:183],a[68:71],a[128:131],v[180:183]\n"
PK"v85,v86,v87\n"
PK"v86,v92,v93\n"
MF"v[180:183],a[64:67],a[248:251],v[180:183]\n"
RD"a[100:103],v235 offset:21504\n"
PK"v87,v94,v95\n"
MF"v[108:111],a[72:75],a[244:247],v[108:111]\n"
BS"v[84:87],v224,s[8:11],s90 offen sc1\n"
MF"v[108:111],a[76:79],a[252:255],v[108:111]\n"
RD"a[104:107],v235 offset:22528\n"
MF"v[188:191],a[76:79],a[128:131],v[188:191]\n"
MF"v[188:191],a[72:75],a[248:251],v[188:191]\n"
WT"lgkmcnt(3)\n"
RD"a[108:111],v235 offset:23552\n"
MF"v[100:103],a[80:83],a[132:135],v[100:103]\n"
MF"v[100:103],a[84:87],a[140:143],v[100:103]\n"
SA"s91,s22,327680\n"
RD"a[112:115],v236 offset:8192\n"
PK"v164,v164,v165\n"
MF"v[180:183],a[84:87],a[144:147],v[180:183]\n"
PK"v165,v166,v167\n"
PK"v166,v172,v173\n"
MF"v[180:183],a[80:83],a[136:139],v[180:183]\n"
RD"a[116:119],v236 offset:9216\n"
PK"v167,v174,v175\n"
MF"v[108:111],a[88:91],a[132:135],v[108:111]\n"
BS"v[164:167],v224,s[8:11],s91 offen sc1\n"
MF"v[108:111],a[92:95],a[140:143],v[108:111]\n"
RD"a[120:123],v236 offset:10240\n"
MF"v[188:191],a[92:95],a[144:147],v[188:191]\n"
MF"v[188:191],a[88:91],a[136:139],v[188:191]\n"
WT"lgkmcnt(3)\n"
RD"a[124:127],v236 offset:11264\n"
MF"v[100:103],a[96:99],a[148:151],v[100:103]\n"
MF"v[100:103],a[100:103],a[156:159],v[100:103]\n"
RD"a[64:67],v236 offset:28672\n"
MF"v[180:183],a[100:103],a[160:163],v[180:183]\n"
WT"vmcnt(3)\n"
"buffer_store_dwordx2 v[232:233],v239,s[8:11],s89 offen sc1\n"
MF"v[180:183],a[96:99],a[152:155],v[180:183]\n"
RD"a[68:71],v236 offset:29696\n"
MF"v[108:111],a[104:107],a[148:151],v[108:111]\n"
MF"v[108:111],a[108:111],a[156:159],v[108:111]\n"
RD"a[72:75],v236 offset:30720\n"
MF"v[188:191],a[108:111],a[160:163],v[188:191]\n"
MF"v[188:191],a[104:107],a[152:155],v[188:191]\n"
WT"lgkmcnt(3)\n"
RD"a[76:79],v236 offset:31744\n"
MF"v[100:103],a[112:115],a[164:167],v[100:103]\n"
MF"v[100:103],a[116:119],a[172:175],v[100:103]\n"
RD"a[80:83],v237 offset:16384\n"
MF"v[180:183],a[116:119],a[176:179],v[180:183]\n"
MF"v[180:183],a[112:115],a[168:171],v[180:183]\n"
RD"a[84:87],v237 offset:17408\n"
MF"v[108:111],a[120:123],a[164:167],v[108:111]\n"
MF"v[108:111],a[124:127],a[172:175],v[108:111]\n"
RD"a[88:91],v237 offset:18432\n"
MF"v[188:191],a[124:127],a[176:179],v[188:191]\n"
MF"v[188:191],a[120:123],a[168:171],v[188:191]\n"
WT"lgkmcnt(3)\n"
RD"a[92:95],v237 offset:19456\n"
MF"v[100:103],a[64:67],a[32:35],v[100:103]\n"
MF"v[100:103],a[68:71],a[40:43],v[100:103]\n"
RD"a[96:99],v226 offset:16384\n"
MF"v[180:183],a[68:71],a[44:47],v[180:183]\n"
MF"v[180:183],a[64:67],a[36:39],v[180:183]\n"
RD"a[100:103],v226 offset:17408\n"
MF"v[108:111],a[72:75],a[32:35],v[108:111]\n"
MF"v[108:111],a[76:79],a[40:43],v[108:111]\n"
RD"a[104:107],v226 offset:18432\n"
MF"v[188:191],a[76:79],a[44:47],v[188:191]\n"
MF"v[188:191],a[72:75],a[36:39],v[188:191]\n"
WT"lgkmcnt(3)\n"
RD"a[108:111],v226 offset:19456\n"
MF"v[100:103],a[80:83],a[48:51],v[100:103]\n"
MF"v[100:103],a[84:87],a[56:59],v[100:103]\n"
RD"a[112:115],v235 offset:4096\n"
MF"v[180:183],a[84:87],a[60:63],v[180:183]\n"
MF"v[180:183],a[80:83],a[52:55],v[180:183]\n"
RD"a[116:119],v235 offset:5120\n"
MF"v[108:111],a[88:91],a[48:51],v[108:111]\n"
MF"v[108:111],a[92:95],a[56:59],v[108:111]\n"
RD"a[120:123],v235 offset:6144\n"
MF"v[188:191],a[92:95],a[60:63],v[188:191]\n"
MF"v[188:191],a[88:91],a[52:55],v[188:191]\n"
WT"lgkmcnt(3)\n"
RD"a[124:127],v235 offset:7168\n"
MF"v[116:119],a[96:99],a[244:247],v[116:119]\n"
MF"v[116:119],a[100:103],a[252:255],v[116:119]\n"
SA"s90,s88,328704\n"
RD"a[64:67],v235 offset:24576\n"
PK"v100,v100,v101\n"
MF"v[196:199],a[100:103],a[128:131],v[196:199]\n"
PK"v101,v102,v103\n"
PK"v102,v108,v109\n"
MF"v[196:199],a[96:99],a[248:251],v[196:199]\n"
RD"a[68:71],v235 offset:25600\n"
PK"v103,v110,v111\n"
MF"v[124:127],a[104:107],a[244:247],v[124:127]\n"
BS"v[100:103],v224,s[8:11],s90 offen sc1\n"
MF"v[124:127],a[108:111],a[252:255],v[124:127]\n"
WT"vmcnt(4)\n"
"v_cmp_eq_u64 vcc,s[28:29],v[240:241]\n"
"s_cmp_eq_u64 vcc,exec\n"
"s_cbranch_scc0 L1086_%=\n"
"L1087_%=:\n"
MF"v[204:207],a[108:111],a[128:131],v[204:207]\n"
RD"a[72:75],v235 offset:26624\n"
MF"v[204:207],a[104:107],a[248:251],v[204:207]\n"
WT"lgkmcnt(3)\n"
RD"a[76:79],v235 offset:27648\n"
MF"v[116:119],a[112:115],a[132:135],v[116:119]\n"
BL"v[112:115],v176,s[12:15],s38 offen\n"
MF"v[116:119],a[116:119],a[140:143],v[116:119]\n"
SA"s91,s22,328704\n"
RD"a[80:83],v236 offset:12288\n"
PK"v180,v180,v181\n"
MF"v[196:199],a[116:119],a[144:147],v[196:199]\n"
BL"v[144:147],v178,s[8:11],s36 offen sc1\n"
MF"v[196:199],a[112:115],a[136:139],v[196:199]\n"
RD"a[84:87],v236 offset:13312\n"
PK"v181,v182,v183\n"
MF"v[124:127],a[120:123],a[132:135],v[124:127]\n"
PK"v182,v188,v189\n"
PK"v183,v190,v191\n"
MF"v[124:127],a[124:127],a[140:143],v[124:127]\n"
BS"v[180:183],v224,s[8:11],s91 offen sc1\n"
MF"v[204:207],a[124:127],a[144:147],v[204:207]\n"
RD"a[88:91],v236 offset:14336\n"
MF"v[204:207],a[120:123],a[136:139],v[204:207]\n"
WT"lgkmcnt(3)\n"
BL"v[120:123],v176,s[12:15],s55 offen\n"
MF"v[116:119],a[64:67],a[148:151],v[116:119]\n"
RD"a[92:95],v236 offset:15360\n"
MF"v[116:119],a[68:71],a[156:159],v[116:119]\n"
BL"v[152:155],v178,s[8:11],s37 offen sc1\n"
MF"v[196:199],a[68:71],a[160:163],v[196:199]\n"
RD"a[96:99],v237 offset:0\n"
MF"v[196:199],a[64:67],a[152:155],v[196:199]\n"
RD"a[100:103],v237 offset:1024\n"
MF"v[124:127],a[72:75],a[148:151],v[124:127]\n"
BL"v[128:131],v177,s[12:15],s38 offen\n"
MF"v[124:127],a[76:79],a[156:159],v[124:127]\n"
RD"a[104:107],v237 offset:2048\n"
MF"v[204:207],a[76:79],a[160:163],v[204:207]\n"
BL"v[160:163],v179,s[8:11],s36 offen sc1\n"
MF"v[204:207],a[72:75],a[152:155],v[204:207]\n"
WT"lgkmcnt(3)\n"
RD"a[108:111],v237 offset:3072\n"
MF"v[116:119],a[80:83],a[164:167],v[116:119]\n"
MF"v[116:119],a[84:87],a[172:175],v[116:119]\n"
RD"a[112:115],v237 offset:20480\n"
MF"v[196:199],a[84:87],a[176:179],v[196:199]\n"
BL"v[136:139],v177,s[12:15],s55 offen\n"
MF"v[196:199],a[80:83],a[168:171],v[196:199]\n"
RD"a[116:119],v237 offset:21504\n"
MF"v[124:127],a[88:91],a[164:167],v[124:127]\n"
BL"v[168:171],v179,s[8:11],s37 offen sc1\n"
MF"v[124:127],a[92:95],a[172:175],v[124:127]\n"
RD"a[120:123],v237 offset:22528\n"
MF"v[204:207],a[92:95],a[176:179],v[204:207]\n"
MF"v[204:207],a[88:91],a[168:171],v[204:207]\n"
WT"lgkmcnt(3)\n"
RD"a[124:127],v237 offset:23552\n"
MF"v[116:119],a[96:99],a[32:35],v[116:119]\n"
MF"v[116:119],a[100:103],a[40:43],v[116:119]\n"
RD"a[64:67],v226 offset:20480\n"
MF"v[196:199],a[100:103],a[44:47],v[196:199]\n"
"s_lshr_b32 s93,s31,3\n"
MF"v[196:199],a[96:99],a[36:39],v[196:199]\n"
"s_lshl_b32 s93,s93,4\n"
RD"a[68:71],v226 offset:21504\n"
MF"v[124:127],a[104:107],a[32:35],v[124:127]\n"
SA"s93,s93,s32\n"
MF"v[124:127],a[108:111],a[40:43],v[124:127]\n"
"s_mul_i32 s94,s93,40960\n"
RD"a[72:75],v226 offset:22528\n"
MF"v[204:207],a[108:111],a[44:47],v[204:207]\n"
"s_lshl_b32 s93,s93,9\n"
MF"v[204:207],a[104:107],a[36:39],v[204:207]\n"
WT"lgkmcnt(3)\n"
RD"a[76:79],v226 offset:23552\n"
SA"s93,s93,131072\n"
MF"v[116:119],a[112:115],a[48:51],v[116:119]\n"
SA"s94,s94,61603840\n"
MF"v[116:119],a[116:119],a[56:59],v[116:119]\n"
"s_lshl_b32 s92,s30,10\n"
RD"a[80:83],v235 offset:8192\n"
MF"v[196:199],a[116:119],a[60:63],v[196:199]\n"
"v_add_u32 v244,s92,v224\n"
MF"v[196:199],a[112:115],a[52:55],v[196:199]\n"
SA"s92,s45,4096\n"
RD"a[84:87],v235 offset:9216\n"
MF"v[124:127],a[120:123],a[48:51],v[124:127]\n"
"v_add_u32 v245,s92,v224\n"
MF"v[124:127],a[124:127],a[56:59],v[124:127]\n"
RD"a[88:91],v235 offset:10240\n"
MF"v[204:207],a[124:127],a[60:63],v[204:207]\n"
"v_and_b32 v246,128,v224\n"
MF"v[204:207],a[120:123],a[52:55],v[204:207]\n"
WT"lgkmcnt(3)\n"
RD"a[92:95],v235 offset:11264\n"
"v_lshl_add_u32 v243,v246,5,v243\n"
MF"v[132:135],a[64:67],a[244:247],v[132:135]\n"
"v_lshrrev_b32 v246,8,v224\n"
MF"v[132:135],a[68:71],a[252:255],v[132:135]\n"
SA"s90,s88,329728\n"
RD"a[96:99],v235 offset:28672\n"
PK"v116,v116,v117\n"
MF"v[208:211],a[68:71],a[128:131],v[208:211]\n"
"s_mov_b32 s95,0xffff0101\n"
PK"v117,v118,v119\n"
"v_lshl_add_u32 v243,v246,27,v243\n"
MF"v[208:211],a[64:67],a[248:251],v[208:211]\n"
"s_lshl_b32 s92,s30,10\n"
RD"a[100:103],v235 offset:29696\n"
PK"v118,v124,v125\n"
MF"v[140:143],a[72:75],a[244:247],v[140:143]\n"
PK"v119,v126,v127\n"
"v_add_u32 v247,s92,v227\n"
MF"v[140:143],a[76:79],a[252:255],v[140:143]\n"
BS"v[116:119],v224,s[8:11],s90 offen sc1\n"
MF"v[212:215],a[76:79],a[128:131],v[212:215]\n"
RD"a[104:107],v235 offset:30720\n"
MF"v[212:215],a[72:75],a[248:251],v[212:215]\n"
WT"lgkmcnt(3)\n"
"buffer_load_dwordx2 v[240:241],v243,s[8:11],s93 offen sc1\n"
MF"v[132:135],a[80:83],a[132:135],v[132:135]\n"
RD"a[108:111],v235 offset:31744\n"
MF"v[132:135],a[84:87],a[140:143],v[132:135]\n"
SA"s91,s22,329728\n"
RD"a[112:115],v236 offset:16384\n"
PK"v196,v196,v197\n"
MF"v[208:211],a[84:87],a[144:147],v[208:211]\n"
PK"v197,v198,v199\n"
PK"v198,v204,v205\n"
MF"v[208:211],a[80:83],a[136:139],v[208:211]\n"
RD"a[116:119],v236 offset:17408\n"
PK"v199,v206,v207\n"
MF"v[140:143],a[88:91],a[132:135],v[140:143]\n"
BS"v[196:199],v224,s[8:11],s91 offen sc1\n"
MF"v[140:143],a[92:95],a[140:143],v[140:143]\n"
RD"a[120:123],v236 offset:18432\n"
MF"v[212:215],a[92:95],a[144:147],v[212:215]\n"
MF"v[212:215],a[88:91],a[136:139],v[212:215]\n"
WT"lgkmcnt(3)\n"
RD"a[124:127],v236 offset:19456\n"
MF"v[132:135],a[96:99],a[148:151],v[132:135]\n"
MF"v[132:135],a[100:103],a[156:159],v[132:135]\n"
RD"a[64:67],v237 offset:4096\n"
MF"v[208:211],a[100:103],a[160:163],v[208:211]\n"
MF"v[208:211],a[96:99],a[152:155],v[208:211]\n"
RD"a[68:71],v237 offset:5120\n"
MF"v[140:143],a[104:107],a[148:151],v[140:143]\n"
MF"v[140:143],a[108:111],a[156:159],v[140:143]\n"
RD"a[72:75],v237 offset:6144\n"
MF"v[212:215],a[108:111],a[160:163],v[212:215]\n"
MF"v[212:215],a[104:107],a[152:155],v[212:215]\n"
WT"lgkmcnt(3)\n"
RD"a[76:79],v237 offset:7168\n"
MF"v[132:135],a[112:115],a[164:167],v[132:135]\n"
MF"v[132:135],a[116:119],a[172:175],v[132:135]\n"
RD"a[80:83],v237 offset:24576\n"
MF"v[208:211],a[116:119],a[176:179],v[208:211]\n"
MF"v[208:211],a[112:115],a[168:171],v[208:211]\n"
RD"a[84:87],v237 offset:25600\n"
MF"v[140:143],a[120:123],a[164:167],v[140:143]\n"
MF"v[140:143],a[124:127],a[172:175],v[140:143]\n"
RD"a[88:91],v237 offset:26624\n"
MF"v[212:215],a[124:127],a[176:179],v[212:215]\n"
MF"v[212:215],a[120:123],a[168:171],v[212:215]\n"
WT"lgkmcnt(3)\n"
RD"a[92:95],v237 offset:27648\n"
MF"v[132:135],a[64:67],a[32:35],v[132:135]\n"
MF"v[132:135],a[68:71],a[40:43],v[132:135]\n"
RD"a[96:99],v226 offset:24576\n"
MF"v[208:211],a[68:71],a[44:47],v[208:211]\n"
MF"v[208:211],a[64:67],a[36:39],v[208:211]\n"
RD"a[100:103],v226 offset:25600\n"
MF"v[140:143],a[72:75],a[32:35],v[140:143]\n"
MF"v[140:143],a[76:79],a[40:43],v[140:143]\n"
RD"a[104:107],v226 offset:26624\n"
MF"v[212:215],a[76:79],a[44:47],v[212:215]\n"
MF"v[212:215],a[72:75],a[36:39],v[212:215]\n"
WT"lgkmcnt(3)\n"
RD"a[108:111],v226 offset:27648\n"
MF"v[132:135],a[80:83],a[48:51],v[132:135]\n"
MF"v[132:135],a[84:87],a[56:59],v[132:135]\n"
RD"a[112:115],v235 offset:12288\n"
MF"v[208:211],a[84:87],a[60:63],v[208:211]\n"
WT"vmcnt(1)\n"
"v_cmp_eq_u64 vcc,s[28:29],v[240:241]\n"
MF"v[208:211],a[80:83],a[52:55],v[208:211]\n"
"s_and_b32 s50,vcc_lo,0xff\n"
RD"a[116:119],v235 offset:13312\n"
"s_or_b32 s95,s95,s50\n"
MF"v[140:143],a[88:91],a[48:51],v[140:143]\n"
"s_bitcmp1_b32 s95,1\n"
"s_cselect_b32 s51,s94,1065353216\n"
MF"v[140:143],a[92:95],a[56:59],v[140:143]\n"
SA"s92,s51,5120\n"
RD"a[120:123],v235 offset:14336\n"
MF"v[212:215],a[92:95],a[60:63],v[212:215]\n"
BL"a[184:187],v244,s[8:11],s92 offen sc1\n"
MF"v[212:215],a[88:91],a[52:55],v[212:215]\n"
WT"lgkmcnt(3)\n"
RD"a[124:127],v235 offset:15360\n"
MF"v[148:151],a[96:99],a[244:247],v[148:151]\n"
BL"a[216:219],v245,s[8:11],s92 offen sc1\n"
MF"v[148:151],a[100:103],a[252:255],v[148:151]\n"
SA"s90,s88,330752\n"
RD"a[64:67],v236 offset:0\n"
PK"v132,v132,v133\n"
MF"v[216:219],a[100:103],a[128:131],v[216:219]\n"
"s_bitcmp1_b32 s95,2\n"
PK"v133,v134,v135\n"
PK"v134,v140,v141\n"
MF"v[216:219],a[96:99],a[248:251],v[216:219]\n"
"s_cselect_b32 s51,s94,1065353216\n"
RD"a[68:71],v236 offset:1024\n"
PK"v135,v142,v143\n"
MF"v[156:159],a[104:107],a[244:247],v[156:159]\n"
SA"s92,s51,10240\n"
BS"v[132:135],v224,s[8:11],s90 offen sc1\n"
MF"v[156:159],a[108:111],a[252:255],v[156:159]\n"
BL"a[188:191],v244,s[8:11],s92 offen sc1\n"
MF"v[220:223],a[108:111],a[128:131],v[220:223]\n"
RD"a[72:75],v236 offset:2048\n"
MF"v[220:223],a[104:107],a[248:251],v[220:223]\n"
WT"lgkmcnt(3)\n"
BL"a[220:223],v245,s[8:11],s92 offen sc1\n"
MF"v[148:151],a[112:115],a[132:135],v[148:151]\n"
"s_bitcmp1_b32 s95,3\n"
RD"a[76:79],v236 offset:3072\n"
"s_cselect_b32 s51,s94,1065353216\n"
MF"v[148:151],a[116:119],a[140:143],v[148:151]\n"
SA"s91,s22,330752\n"
RD"a[80:83],v236 offset:20480\n"
PK"v208,v208,v209\n"
MF"v[216:219],a[116:119],a[144:147],v[216:219]\n"
SA"s92,s51,15360\n"
PK"v209,v210,v211\n"
PK"v210,v212,v213\n"
MF"v[216:219],a[112:115],a[136:139],v[216:219]\n"
BL"a[192:195],v244,s[8:11],s92 offen sc1\n"
MF"v[156:159],a[120:123],a[132:135],v[156:159]\n"
RD"a[84:87],v236 offset:21504\n"
PK"v211,v214,v215\n"
MF"v[156:159],a[124:127],a[140:143],v[156:159]\n"
BS"v[208:211],v224,s[8:11],s91 offen sc1\n"
MF"v[220:223],a[124:127],a[144:147],v[220:223]\n"
"s_bitcmp1_b32 s95,4\n"
BL"a[224:227],v245,s[8:11],s92 offen sc1\n"
MF"v[220:223],a[120:123],a[136:139],v[220:223]\n"
WT"lgkmcnt(2)\n"
RD"a[88:91],v236 offset:22528\n"
"s_cselect_b32 s51,s94,1065353216\n"
MF"v[148:151],a[64:67],a[148:151],v[148:151]\n"
SA"s92,s51,20480\n"
RD"a[92:95],v236 offset:23552\n"
MF"v[148:151],a[68:71],a[156:159],v[148:151]\n"
BL"a[196:199],v244,s[8:11],s92 offen sc1\n"
MF"v[216:219],a[68:71],a[160:163],v[216:219]\n"
RD"a[96:99],v237 offset:8192\n"
MF"v[216:219],a[64:67],a[152:155],v[216:219]\n"
BL"a[228:231],v245,s[8:11],s92 offen sc1\n"
MF"v[156:159],a[72:75],a[148:151],v[156:159]\n"
"s_bitcmp1_b32 s95,5\n"
RD"a[100:103],v237 offset:9216\n"
"s_cselect_b32 s51,s94,1065353216\n"
MF"v[156:159],a[76:79],a[156:159],v[156:159]\n"
SA"s92,s51,25600\n"
RD"a[104:107],v237 offset:10240\n"
MF"v[220:223],a[76:79],a[160:163],v[220:223]\n"
BL"a[200:203],v244,s[8:11],s92 offen sc1\n"
MF"v[220:223],a[72:75],a[152:155],v[220:223]\n"
WT"lgkmcnt(3)\n"
RD"a[108:111],v237 offset:11264\n"
MF"v[148:151],a[80:83],a[164:167],v[148:151]\n"
BL"a[232:235],v245,s[8:11],s92 offen sc1\n"
MF"v[148:151],a[84:87],a[172:175],v[148:151]\n"
RD"a[112:115],v237 offset:28672\n"
MF"v[216:219],a[84:87],a[176:179],v[216:219]\n"
"s_bitcmp1_b32 s95,6\n"
"s_cselect_b32 s51,s94,1065353216\n"
MF"v[216:219],a[80:83],a[168:171],v[216:219]\n"
SA"s92,s51,30720\n"
RD"a[116:119],v237 offset:29696\n"
MF"v[156:159],a[88:91],a[164:167],v[156:159]\n"
BL"a[204:207],v244,s[8:11],s92 offen sc1\n"
MF"v[156:159],a[92:95],a[172:175],v[156:159]\n"
RD"a[120:123],v237 offset:30720\n"
MF"v[220:223],a[92:95],a[176:179],v[220:223]\n"
BL"a[236:239],v245,s[8:11],s92 offen sc1\n"
MF"v[220:223],a[88:91],a[168:171],v[220:223]\n"
WT"lgkmcnt(3)\n"
RD"a[124:127],v237 offset:31744\n"
MF"v[148:151],a[96:99],a[32:35],v[148:151]\n"
"s_bitcmp1_b32 s95,7\n"
"s_cselect_b32 s51,s94,1065353216\n"
MF"v[148:151],a[100:103],a[40:43],v[148:151]\n"
SA"s92,s51,35840\n"
MF"v[216:219],a[100:103],a[44:47],v[216:219]\n"
BL"a[208:211],v244,s[8:11],s92 offen sc1\n"
MF"v[216:219],a[96:99],a[36:39],v[216:219]\n"
MF"v[156:159],a[104:107],a[32:35],v[156:159]\n"
BL"a[240:243],v245,s[8:11],s92 offen sc1\n"
MF"v[156:159],a[108:111],a[40:43],v[156:159]\n"
MF"v[220:223],a[108:111],a[44:47],v[220:223]\n"
MF"v[220:223],a[104:107],a[36:39],v[220:223]\n"
WT"lgkmcnt(0)\n"
MF"v[148:151],a[112:115],a[48:51],v[148:151]\n"
MF"v[148:151],a[116:119],a[56:59],v[148:151]\n"
MF"v[216:219],a[116:119],a[60:63],v[216:219]\n"
MF"v[216:219],a[112:115],a[52:55],v[216:219]\n"
MF"v[156:159],a[120:123],a[48:51],v[156:159]\n"
MF"v[156:159],a[124:127],a[56:59],v[156:159]\n"
MF"v[220:223],a[124:127],a[60:63],v[220:223]\n"
MF"v[220:223],a[120:123],a[52:55],v[220:223]\n"
"s_nop 5\n"
PK"v148,v148,v149\n"
PK"v149,v150,v151\n"
PK"v150,v156,v157\n"
PK"v151,v158,v159\n"
SA"s90,s88,331776\n"
BS"v[148:151],v224,s[8:11],s90 offen sc1\n"
PK"v216,v216,v217\n"
PK"v217,v218,v219\n"
PK"v218,v220,v221\n"
PK"v219,v222,v223\n"
SA"s91,s22,331776\n"
BS"v[216:219],v224,s[8:11],s91 offen sc1\n"
"s_cmp_eq_u32 s23,0\n"
"s_cbranch_scc0 L1089_%=\n"
"ds_write_b128 v227,v[0:3] offset:0\n"
"ds_write_b128 v227,v[8:11] offset:1024\n"
"ds_write_b128 v227,v[16:19] offset:2048\n"
"ds_write_b128 v227,v[24:27] offset:3072\n"
"ds_write_b128 v227,v[32:35] offset:4096\n"
"ds_write_b128 v227,v[84:87] offset:5120\n"
"ds_write_b128 v227,v[100:103] offset:6144\n"
"ds_write_b128 v227,v[116:119] offset:7168\n"
"ds_write_b128 v227,v[132:135] offset:8192\n"
"ds_write_b128 v227,v[148:151] offset:9216\n"
"L1089_%=:\n"
"s_cmp_eq_u32 s23,4\n"
"s_cbranch_scc0 L1090_%=\n"
"ds_write_b128 v227,v[40:43] offset:0\n"
"ds_write_b128 v227,v[48:51] offset:1024\n"
"ds_write_b128 v227,v[56:59] offset:2048\n"
"ds_write_b128 v227,v[64:67] offset:3072\n"
"ds_write_b128 v227,v[72:75] offset:4096\n"
"ds_write_b128 v227,v[164:167] offset:5120\n"
"ds_write_b128 v227,v[180:183] offset:6144\n"
"ds_write_b128 v227,v[196:199] offset:7168\n"
"ds_write_b128 v227,v[208:211] offset:8192\n"
"ds_write_b128 v227,v[216:219] offset:9216\n"
"L1090_%=:\n"
MF"v[4:7],v[96:99],v[112:115],0\n"
MF"v[12:15],v[104:107],v[112:115],0\n"
MF"v[20:23],v[96:99],v[120:123],0\n"
MF"v[28:31],v[104:107],v[120:123],0\n"
MF"v[4:7],v[96:99],v[144:147],v[4:7]\n"
MF"v[12:15],v[104:107],v[144:147],v[12:15]\n"
MF"v[20:23],v[96:99],v[152:155],v[20:23]\n"
MF"v[28:31],v[104:107],v[152:155],v[28:31]\n"
MF"v[36:39],v[96:99],v[128:131],0\n"
MF"v[44:47],v[104:107],v[128:131],0\n"
MF"v[52:55],v[96:99],v[136:139],0\n"
MF"v[60:63],v[104:107],v[136:139],0\n"
MF"v[36:39],v[96:99],v[160:163],v[36:39]\n"
MF"v[44:47],v[104:107],v[160:163],v[44:47]\n"
MF"v[52:55],v[96:99],v[168:171],v[52:55]\n"
MF"v[60:63],v[104:107],v[168:171],v[60:63]\n"
SA"s89,s89,4096\n"
"s_mov_b32 s54,1048576\n"
WT"vmcnt(0)\n"
"buffer_store_dwordx2 v[232:233],v239,s[8:11],s89 offen sc1\n"
"buffer_load_dwordx2 v[240:241],v243,s[8:11],s93 offen sc1\n"
WT"lgkmcnt(0)\n"
"s_barrier\n"
RD"a[180:183],v247 offset:0\n"
RD"a[212:215],v227 offset:4096\n"
RD"a[64:67],v247 offset:5120\n"
RD"a[96:99],v227 offset:9216\n"
"s_getpc_b64 s[52:53]\n"
"L1097_%=:\n"
SA"s52,s52,L1096_%=-L1097_%=-32\n"
"s_addc_u32 s53,s53,0\n"
"s_mov_b32 s89,0\n"
WT"lgkmcnt(0)\n"
MF"v[4:7],v[80:83],a[180:183],v[4:7]\n"
MF"v[12:15],v[88:91],a[180:183],v[12:15]\n"
MF"v[20:23],v[80:83],a[212:215],v[20:23]\n"
MF"v[28:31],v[88:91],a[212:215],v[28:31]\n"
MF"v[36:39],v[80:83],a[64:67],v[36:39]\n"
MF"v[44:47],v[88:91],a[64:67],v[44:47]\n"
MF"v[52:55],v[80:83],a[96:99],v[52:55]\n"
MF"v[60:63],v[88:91],a[96:99],v[60:63]\n"
"s_branch L1092_%=\n"
"L1094_%=:\n"
"s_sleep 3\n"
"buffer_load_dwordx2 v[240:241],v243,s[8:11],s93 offen sc1\n"
"L1091_%=:\n"
WT"vmcnt(0)\n"
"v_cmp_eq_u64 vcc,s[28:29],v[240:241]\n"
"s_sub_u32 s54,s54,1\n"
"s_cselect_b32 s90,-1,vcc_lo\n"
"s_andn2_b32 s90,s90,s95\n"
"s_cbranch_scc0 L1094_%=\n"
"s_or_b32 s95,s95,s90\n"
"L1098_%=:\n"
"s_ff1_i32_b32 s50,s90\n"
"s_bitset0_b32 s90,s50\n"
"s_lshl_b32 s50,s50,5\n"
SA"s50,s50,s52\n"
"s_addc_u32 s51,s53,0\n"
"s_setpc_b64 s[50:51]\n"
"L1096_%=:\n"
SA"s92,s94,5120\n"
BL"a[184:187],v244,s[8:11],s92 offen sc1\n"
BL"a[216:219],v245,s[8:11],s92 offen sc1\n"
"s_branch L1093_%=\n"
"s_nop 0\n"
SA"s92,s94,10240\n"
BL"a[188:191],v244,s[8:11],s92 offen sc1\n"
BL"a[220:223],v245,s[8:11],s92 offen sc1\n"
"s_branch L1093_%=\n"
"s_nop 0\n"
SA"s92,s94,15360\n"
BL"a[192:195],v244,s[8:11],s92 offen sc1\n"
BL"a[224:227],v245,s[8:11],s92 offen sc1\n"
"s_branch L1093_%=\n"
"s_nop 0\n"
SA"s92,s94,20480\n"
BL"a[196:199],v244,s[8:11],s92 offen sc1\n"
BL"a[228:231],v245,s[8:11],s92 offen sc1\n"
"s_branch L1093_%=\n"
"s_nop 0\n"
SA"s92,s94,25600\n"
BL"a[200:203],v244,s[8:11],s92 offen sc1\n"
BL"a[232:235],v245,s[8:11],s92 offen sc1\n"
"s_branch L1093_%=\n"
"s_nop 0\n"
SA"s92,s94,30720\n"
BL"a[204:207],v244,s[8:11],s92 offen sc1\n"
BL"a[236:239],v245,s[8:11],s92 offen sc1\n"
"s_branch L1093_%=\n"
"s_nop 0\n"
SA"s92,s94,35840\n"
BL"a[208:211],v244,s[8:11],s92 offen sc1\n"
BL"a[240:243],v245,s[8:11],s92 offen sc1\n"
"s_branch L1093_%=\n"
"s_nop 0\n"
"s_nop 0\n"
"s_nop 0\n"
"s_nop 0\n"
"s_nop 0\n"
"s_nop 0\n"
"s_nop 0\n"
"s_nop 0\n"
"s_nop 0\n"
SA"s92,s94,332800\n"
BL"a[68:71],v244,s[8:11],s92 offen sc1\n"
BL"a[100:103],v245,s[8:11],s92 offen sc1\n"
"s_branch L1093_%=\n"
"s_nop 0\n"
SA"s92,s94,337920\n"
BL"a[72:75],v244,s[8:11],s92 offen sc1\n"
BL"a[104:107],v245,s[8:11],s92 offen sc1\n"
"s_branch L1093_%=\n"
"s_nop 0\n"
SA"s92,s94,343040\n"
BL"a[76:79],v244,s[8:11],s92 offen sc1\n"
BL"a[108:111],v245,s[8:11],s92 offen sc1\n"
"s_branch L1093_%=\n"
"s_nop 0\n"
SA"s92,s94,348160\n"
BL"a[80:83],v244,s[8:11],s92 offen sc1\n"
BL"a[112:115],v245,s[8:11],s92 offen sc1\n"
"s_branch L1093_%=\n"
"s_nop 0\n"
SA"s92,s94,353280\n"
BL"a[84:87],v244,s[8:11],s92 offen sc1\n"
BL"a[116:119],v245,s[8:11],s92 offen sc1\n"
"s_branch L1093_%=\n"
"s_nop 0\n"
SA"s92,s94,358400\n"
BL"a[88:91],v244,s[8:11],s92 offen sc1\n"
BL"a[120:123],v245,s[8:11],s92 offen sc1\n"
"s_branch L1093_%=\n"
"s_nop 0\n"
SA"s92,s94,363520\n"
BL"a[92:95],v244,s[8:11],s92 offen sc1\n"
BL"a[124:127],v245,s[8:11],s92 offen sc1\n"
"s_branch L1093_%=\n"
"s_nop 0\n"
"L1093_%=:\n"
"s_cmp_lg_u32 s90,0\n"
"s_cbranch_scc1 L1098_%=\n"
"s_and_b32 s92,s95,s89\n"
"s_cmp_eq_u32 s92,-1\n"
"s_cbranch_scc1 L1095_%=\n"
"buffer_load_dwordx2 v[240:241],v243,s[8:11],s93 offen sc1\n"
WT"vmcnt(1)\n"
"L1092_%=:\n"
"s_or_b32 s92,s95,0xff00\n"
"s_andn2_b32 s92,s92,s89\n"
"s_cmp_eq_u32 s92,-1\n"
"s_cbranch_scc0 L1091_%=\n"
MF"v[4:7],v[80:83],a[184:187],v[4:7]\n"
MF"v[12:15],v[88:91],a[184:187],v[12:15]\n"
MF"v[4:7],v[80:83],a[188:191],v[4:7]\n"
MF"v[12:15],v[88:91],a[188:191],v[12:15]\n"
MF"v[4:7],v[80:83],a[192:195],v[4:7]\n"
MF"v[12:15],v[88:91],a[192:195],v[12:15]\n"
MF"v[4:7],v[80:83],a[196:199],v[4:7]\n"
MF"v[12:15],v[88:91],a[196:199],v[12:15]\n"
MF"v[4:7],v[80:83],a[200:203],v[4:7]\n"
MF"v[12:15],v[88:91],a[200:203],v[12:15]\n"
MF"v[4:7],v[80:83],a[204:207],v[4:7]\n"
MF"v[12:15],v[88:91],a[204:207],v[12:15]\n"
MF"v[4:7],v[80:83],a[208:211],v[4:7]\n"
MF"v[12:15],v[88:91],a[208:211],v[12:15]\n"
MF"v[20:23],v[80:83],a[216:219],v[20:23]\n"
MF"v[28:31],v[88:91],a[216:219],v[28:31]\n"
MF"v[20:23],v[80:83],a[220:223],v[20:23]\n"
MF"v[28:31],v[88:91],a[220:223],v[28:31]\n"
MF"v[20:23],v[80:83],a[224:227],v[20:23]\n"
MF"v[28:31],v[88:91],a[224:227],v[28:31]\n"
MF"v[20:23],v[80:83],a[228:231],v[20:23]\n"
MF"v[28:31],v[88:91],a[228:231],v[28:31]\n"
PK"v4,v4,v5\n"
PK"v5,v6,v7\n"
PK"v6,v12,v13\n"
PK"v7,v14,v15\n"
BS"v[4:7],v176,s[16:19],s38 offen\n"
MF"v[20:23],v[80:83],a[232:235],v[20:23]\n"
MF"v[28:31],v[88:91],a[232:235],v[28:31]\n"
MF"v[20:23],v[80:83],a[236:239],v[20:23]\n"
MF"v[28:31],v[88:91],a[236:239],v[28:31]\n"
MF"v[20:23],v[80:83],a[240:243],v[20:23]\n"
MF"v[28:31],v[88:91],a[240:243],v[28:31]\n"
"s_nop 7\n"
PK"v20,v20,v21\n"
PK"v21,v22,v23\n"
PK"v22,v28,v29\n"
PK"v23,v30,v31\n"
BS"v[20:23],v176,s[16:19],s55 offen\n"
"s_mov_b32 s89,-1\n"
"s_cmp_eq_u32 s95,-1\n"
"s_cbranch_scc0 L1091_%=\n"
"L1095_%=:\n"
WT"vmcnt(0)\n"
MF"v[36:39],v[80:83],a[68:71],v[36:39]\n"
MF"v[44:47],v[88:91],a[68:71],v[44:47]\n"
MF"v[36:39],v[80:83],a[72:75],v[36:39]\n"
MF"v[44:47],v[88:91],a[72:75],v[44:47]\n"
MF"v[36:39],v[80:83],a[76:79],v[36:39]\n"
MF"v[44:47],v[88:91],a[76:79],v[44:47]\n"
MF"v[36:39],v[80:83],a[80:83],v[36:39]\n"
MF"v[44:47],v[88:91],a[80:83],v[44:47]\n"
MF"v[36:39],v[80:83],a[84:87],v[36:39]\n"
MF"v[44:47],v[88:91],a[84:87],v[44:47]\n"
MF"v[36:39],v[80:83],a[88:91],v[36:39]\n"
MF"v[44:47],v[88:91],a[88:91],v[44:47]\n"
MF"v[36:39],v[80:83],a[92:95],v[36:39]\n"
MF"v[44:47],v[88:91],a[92:95],v[44:47]\n"
MF"v[52:55],v[80:83],a[100:103],v[52:55]\n"
MF"v[60:63],v[88:91],a[100:103],v[60:63]\n"
MF"v[52:55],v[80:83],a[104:107],v[52:55]\n"
MF"v[60:63],v[88:91],a[104:107],v[60:63]\n"
MF"v[52:55],v[80:83],a[108:111],v[52:55]\n"
MF"v[60:63],v[88:91],a[108:111],v[60:63]\n"
MF"v[52:55],v[80:83],a[112:115],v[52:55]\n"
MF"v[60:63],v[88:91],a[112:115],v[60:63]\n"
PK"v36,v36,v37\n"
PK"v37,v38,v39\n"
PK"v38,v44,v45\n"
PK"v39,v46,v47\n"
BS"v[36:39],v177,s[16:19],s38 offen\n"
MF"v[52:55],v[80:83],a[116:119],v[52:55]\n"
MF"v[60:63],v[88:91],a[116:119],v[60:63]\n"
MF"v[52:55],v[80:83],a[120:123],v[52:55]\n"
MF"v[60:63],v[88:91],a[120:123],v[60:63]\n"
MF"v[52:55],v[80:83],a[124:127],v[52:55]\n"
MF"v[60:63],v[88:91],a[124:127],v[60:63]\n"
"s_nop 7\n"
PK"v52,v52,v53\n"
PK"v53,v54,v55\n"
PK"v54,v60,v61\n"
PK"v55,v62,v63\n"
BS"v[52:55],v177,s[16:19],s55 offen\n"
WT"vmcnt(0) lgkmcnt(0)\n"
"s_endpgm\n"
".p2align 6\n"
"LD2_%=:\n"
MF"v[0:3],a[64:67],a[32:35],v[208:211]\n"
"s_mov_b32 m0,s40\n"
EX"v56,v56\n"
MF"v[0:3],a[68:71],a[36:39],v[0:3]\n"
EX"v57,v57\n"
MF"v[8:11],a[68:71],a[52:55],0\n"
RD"a[92:95],v226 offset:7168\n"
"v_readlane_b32 s43,v228,s42\n"
MF"v[8:11],a[64:67],a[48:51],v[8:11]\n"
WT"lgkmcnt(3)\n"
EX"v58,v58\n"
MF"v[0:3],a[72:75],a[40:43],v[0:3]\n"
RD"a[96:99],v226 offset:8192\n"
RD"a[100:103],v226 offset:9216\n"
"v_readlane_b32 s44,v229,s42\n"
MF"v[0:3],a[76:79],a[44:47],v[0:3]\n"
SA"s42,s42,1\n"
BL"v225,s[8:11],s43 offen offset:0 sc1 lds\n"
PK"v76,v56,v57\n"
MF"v[8:11],a[76:79],a[60:63],v[8:11]\n"
EX"v59,v59\n"
MF"v[8:11],a[72:75],a[56:59],v[8:11]\n"
RD"a[104:107],v226 offset:10240\n"
PK"v77,v58,v59\n"
MF"v[4:7],a[80:83],a[32:35],v[212:215]\n"
WT"lgkmcnt(4)\n"
EX"v60,v60\n"
MF"v[4:7],a[84:87],a[36:39],v[4:7]\n"
EX"v61,v61\n"
MF"v[12:15],a[84:87],a[52:55],0\n"
BL"v225,s[8:11],s43 offen offset:1024 sc1 lds\n"
MF"v[12:15],a[80:83],a[48:51],v[12:15]\n"
RD"a[108:111],v226 offset:11264\n"
PK"v78,v60,v61\n"
MF"v[4:7],a[88:91],a[40:43],v[4:7]\n"
WT"lgkmcnt(2)\n"
EX"v62,v62\n"
MF"v[4:7],a[92:95],a[44:47],v[4:7]\n"
RD"a[112:115],v226 offset:12288\n"
RD"a[116:119],v226 offset:13312\n"
MF"v[12:15],a[92:95],a[60:63],v[12:15]\n"
EX"v63,v63\n"
MF"v[12:15],a[88:91],a[56:59],v[12:15]\n"
BL"v225,s[8:11],s43 offen offset:2048 sc1 lds\n"
RD"a[120:123],v226 offset:14336\n"
MF"v[16:19],a[96:99],a[32:35],v[216:219]\n"
EX"v0,v0\n"
PK"v79,v62,v63\n"
MF"v[16:19],a[100:103],a[36:39],v[16:19]\n"
EX"v1,v1\n"
MF"v[24:27],a[100:103],a[52:55],0\n"
RD"a[124:127],v226 offset:15360\n"
RD"a[64:67],v226 offset:16384\n"
MF"v[24:27],a[96:99],a[48:51],v[24:27]\n"
WT"lgkmcnt(5)\n"
EX"v2,v2\n"
PK"v0,v0,v1\n"
MF"v[16:19],a[104:107],a[40:43],v[16:19]\n"
SA"s41,s41,32768\n"
BL"v225,s[8:11],s43 offen offset:3072 sc1 lds\n"
RD"a[68:71],v226 offset:17408\n"
MF"v[16:19],a[108:111],a[44:47],v[16:19]\n"
SA"m0,s40,16384\n"
RD"a[72:75],v226 offset:18432\n"
EX"v3,v3\n"
MF"v[24:27],a[108:111],a[60:63],v[24:27]\n"
"s_cmp_eq_u32 s41,163840\n"
RD"a[76:79],v226 offset:19456\n"
RD"a[80:83],v226 offset:20480\n"
MF"v[24:27],a[104:107],a[56:59],v[24:27]\n"
WT"lgkmcnt(6)\n"
EX"v8,v8\n"
PK"v1,v2,v3\n"
MF"v[20:23],a[112:115],a[32:35],v[220:223]\n"
"s_cselect_b32 s41,0,s41\n"
RD"a[84:87],v226 offset:21504\n"
RD"a[88:91],v226 offset:22528\n"
EX"v9,v9\n"
MF"v[20:23],a[116:119],a[36:39],v[20:23]\n"
SA"s40,s40,32768\n"
BL"v225,s[8:11],s44 offen offset:0 sc1 lds\n"
"v_add_u32 v227,s41,v224\n"
MF"v[28:31],a[116:119],a[52:55],0\n"
"s_cmp_eq_u32 s40,s46\n"
EX"v10,v10\n"
PK"v8,v8,v9\n"
MF"v[28:31],a[112:115],a[48:51],v[28:31]\n"
"s_cselect_b32 s40,s47,s40\n"
RD"a[92:95],v226 offset:23552\n"
RD"a[96:99],v226 offset:24576\n"
MF"v[20:23],a[120:123],a[40:43],v[20:23]\n"
WT"lgkmcnt(6)\n"
EX"v11,v11\n"
MF"v[20:23],a[124:127],a[44:47],v[20:23]\n"
RD"a[100:103],v226 offset:25600\n"
RD"a[104:107],v226 offset:26624\n"
MF"v[28:31],a[124:127],a[60:63],v[28:31]\n"
BL"v225,s[8:11],s44 offen offset:1024 sc1 lds\n"
RD"a[108:111],v226 offset:27648\n"
MF"v[28:31],a[120:123],a[56:59],v[28:31]\n"
EX"v4,v4\n"
PK"v9,v10,v11\n"
MF"v[40:43],a[64:67],a[48:51],v[208:211]\n"
RD"a[112:115],v226 offset:28672\n"
RD"a[116:119],v226 offset:29696\n"
EX"v5,v5\n"
MF"v[40:43],a[68:71],a[52:55],v[40:43]\n"
WT"vmcnt(18)\n"
EX"v6,v6\n"
MF"v[40:43],a[72:75],a[56:59],v[40:43]\n"
WT"lgkmcnt(5)\n"
RD"a[120:123],v226 offset:30720\n"
RD"a[124:127],v226 offset:31744\n"
PK"v2,v4,v5\n"
MF"v[40:43],a[76:79],a[60:63],v[40:43]\n"
BL"v225,s[8:11],s44 offen offset:2048 sc1 lds\n"
EX"v7,v7\n"
MF"v[44:47],a[80:83],a[48:51],v[212:215]\n"
"s_barrier\n"
MF"v[44:47],a[84:87],a[52:55],v[44:47]\n"
EX"v12,v12\n"
PK"v3,v6,v7\n"
MF"v[44:47],a[88:91],a[56:59],v[44:47]\n"
EX"v13,v13\n"
MF"v[44:47],a[92:95],a[60:63],v[44:47]\n"
BL"v225,s[8:11],s44 offen offset:3072 sc1 lds\n"
"v_readlane_b32 s43,v228,s42\n"
MF"v[56:59],a[96:99],a[48:51],v[216:219]\n"
WT"lgkmcnt(2)\n"
RD"a[96:99],v227 offset:8192\n"
PK"v10,v12,v13\n"
EX"v14,v14\n"
MF"v[56:59],a[100:103],a[52:55],v[56:59]\n"
"s_mov_b32 m0,s40\n"
RD"a[100:103],v227 offset:9216\n"
MF"v[56:59],a[104:107],a[56:59],v[56:59]\n"
EX"v15,v15\n"
MF"v[56:59],a[108:111],a[60:63],v[56:59]\n"
RD"a[104:107],v227 offset:10240\n"
RD"a[108:111],v227 offset:11264\n"
MF"v[60:63],a[112:115],a[48:51],v[220:223]\n"
EX"v16,v16\n"
PK"v11,v14,v15\n"
MF"v[60:63],a[116:119],a[52:55],v[60:63]\n"
WT"lgkmcnt(4)\n"
EX"v17,v17\n"
"v_readlane_b32 s44,v229,s42\n"
MF"v[60:63],a[120:123],a[56:59],v[60:63]\n"
SA"s42,s42,1\n"
BL"v225,s[8:11],s43 offen offset:0 sc1 lds\n"
MF"v[60:63],a[124:127],a[60:63],v[60:63]\n"
WT"lgkmcnt(3)\n"
EX"v18,v18\n"
MF"v[112:115],a[96:99],v[68:71],v[112:115]\n"
SA"s41,s41,32768\n"
RD"a[112:115],v227 offset:12288\n"
PK"v16,v16,v17\n"
MF"v[144:147],a[96:99],v[72:75],v[144:147]\n"
"s_cmp_eq_u32 s41,163840\n"
EX"v19,v19\n"
MF"v[176:179],a[96:99],v[76:79],v[176:179]\n"
WT"lgkmcnt(1)\n"
EX"v24,v24\n"
MF"v[116:119],a[100:103],v[68:71],v[116:119]\n"
"s_cselect_b32 s41,0,s41\n"
BL"v225,s[8:11],s43 offen offset:1024 sc1 lds\n"
RD"a[116:119],v227 offset:13312\n"
MF"v[148:151],a[100:103],v[72:75],v[148:151]\n"
EX"v25,v25\n"
"v_add_u32 v226,s41,v224\n"
MF"v[180:183],a[100:103],v[76:79],v[180:183]\n"
RD"a[120:123],v227 offset:14336\n"
PK"v17,v18,v19\n"
MF"v[120:123],a[104:107],v[68:71],v[120:123]\n"
EX"v26,v26\n"
MF"v[152:155],a[104:107],v[72:75],v[152:155]\n"
EX"v27,v27\n"
PK"v24,v24,v25\n"
MF"v[184:187],a[104:107],v[76:79],v[184:187]\n"
BL"v225,s[8:11],s43 offen offset:2048 sc1 lds\n"
MF"v[124:127],a[108:111],v[68:71],v[124:127]\n"
EX"v20,v20\n"
MF"v[156:159],a[108:111],v[72:75],v[156:159]\n"
RD"a[124:127],v227 offset:15360\n"
PK"v25,v26,v27\n"
MF"v[188:191],a[108:111],v[76:79],v[188:191]\n"
WT"lgkmcnt(2)\n"
EX"v21,v21\n"
MF"v[128:131],a[112:115],v[68:71],v[128:131]\n"
BL"v225,s[8:11],s43 offen offset:3072 sc1 lds\n"
MF"v[160:163],a[112:115],v[72:75],v[160:163]\n"
SA"m0,s40,16384\n"
EX"v22,v22\n"
MF"v[192:195],a[112:115],v[76:79],v[192:195]\n"
EX"v23,v23\n"
PK"v18,v20,v21\n"
MF"v[132:135],a[116:119],v[68:71],v[132:135]\n"
RD"a[64:67],v227 offset:16384\n"
PK"v19,v22,v23\n"
MF"v[164:167],a[116:119],v[72:75],v[164:167]\n"
EX"v28,v28\n"
MF"v[196:199],a[116:119],v[76:79],v[196:199]\n"
WT"lgkmcnt(1)\n"
BL"v225,s[8:11],s44 offen offset:0 sc1 lds\n"
MF"v[136:139],a[120:123],v[68:71],v[136:139]\n"
SA"s40,s40,32768\n"
EX"v29,v29\n"
MF"v[168:171],a[120:123],v[72:75],v[168:171]\n"
"s_cmp_eq_u32 s40,s46\n"
RD"a[68:71],v227 offset:24576\n"
RD"a[72:75],v227 offset:17408\n"
MF"v[200:203],a[120:123],v[76:79],v[200:203]\n"
"s_cselect_b32 s40,s47,s40\n"
EX"v30,v30\n"
MF"v[140:143],a[124:127],v[68:71],v[140:143]\n"
RD"a[76:79],v227 offset:25600\n"
RD"a[80:83],v227 offset:18432\n"
PK"v26,v28,v29\n"
MF"v[172:175],a[124:127],v[72:75],v[172:175]\n"
EX"v31,v31\n"
MF"v[204:207],a[124:127],v[76:79],v[204:207]\n"
BL"v225,s[8:11],s44 offen offset:1024 sc1 lds\n"
MF"a[136:139],a[128:131],v[68:71],a[136:139]\n"
EX"v40,v40\n"
PK"v27,v30,v31\n"
MF"a[140:143],a[128:131],v[72:75],a[140:143]\n"
EX"v41,v41\n"
MF"a[144:147],a[128:131],v[76:79],a[144:147]\n"
WT"lgkmcnt(2)\n"
RD"a[84:87],v227 offset:26624\n"
PK"v68,v40,v41\n"
MF"v[144:147],a[64:67],v[0:3],v[144:147]\n"
BL"v225,s[8:11],s44 offen offset:2048 sc1 lds\n"
MF"v[144:147],a[68:71],v[16:19],v[144:147]\n"
EX"v42,v42\n"
MF"v[176:179],a[68:71],v[24:27],v[176:179]\n"
SA"s51,s59,16384\n"
RD"a[88:91],v227 offset:19456\n"
RD"a[92:95],v227 offset:27648\n"
MF"v[176:179],a[64:67],v[8:11],v[176:179]\n"
EX"v43,v43\n"
MF"v[148:151],a[72:75],v[0:3],v[148:151]\n"
WT"lgkmcnt(3)\n"
EX"v44,v44\n"
MF"v[148:151],a[76:79],v[16:19],v[148:151]\n"
RD"a[96:99],v227 offset:20480\n"
"v_accvgpr_read_b32 v252,a136\n"
"v_subrev_u32 v253,0x30800000,v252\n"
MF"v[180:183],a[76:79],v[24:27],v[180:183]\n"
"v_cmp_le_u32 vcc,0x1f000000,v253\n"
"s_cmp_lg_u64 vcc,0\n"
"s_cbranch_scc1 L1035_%=\n"
"L1034_%=:\n"
MF"v[180:183],a[72:75],v[8:11],v[180:183]\n"
BL"v225,s[8:11],s44 offen offset:3072 sc1 lds\n"
EX"v45,v45\n"
MF"v[152:155],a[80:83],v[0:3],v[152:155]\n"
WT"lgkmcnt(3)\n"
"v_rcp_f32 v252,v252\n"
EX"v46,v46\n"
MF"v[152:155],a[84:87],v[16:19],v[152:155]\n"
"s_mov_b32 m0,s40\n"
RD"a[100:103],v227 offset:28672\n"
RD"a[104:107],v227 offset:21504\n"
"v_readlane_b32 s43,v228,s42\n"
MF"v[184:187],a[84:87],v[24:27],v[184:187]\n"
EX"v47,v47\n"
PK"v69,v42,v43\n"
MF"v[184:187],a[80:83],v[8:11],v[184:187]\n"
WT"lgkmcnt(3)\n"
RD"a[108:111],v227 offset:29696\n"
RD"a[112:115],v227 offset:22528\n"
EX"v56,v56\n"
MF"v[156:159],a[88:91],v[0:3],v[156:159]\n"
EX"v57,v57\n"
"v_readlane_b32 s44,v229,s42\n"
MF"v[156:159],a[92:95],v[16:19],v[156:159]\n"
SA"s42,s42,1\n"
EX"v58,v58\n"
PK"v70,v44,v45\n"
MF"v[188:191],a[92:95],v[24:27],v[188:191]\n"
RD"a[116:119],v227 offset:30720\n"
MU"v112,v252,v112\n"
MU"v113,v252,v113\n"
MF"v[188:191],a[88:91],v[8:11],v[188:191]\n"
WT"lgkmcnt(5)\n"
EX"v59,v59\n"
MU"v114,v252,v114\n"
MF"v[160:163],a[96:99],v[0:3],v[160:163]\n"
WT"lgkmcnt(3)\n"
EX"v60,v60\n"
MU"v115,v252,v115\n"
MF"v[160:163],a[100:103],v[16:19],v[160:163]\n"
RD"a[120:123],v227 offset:23552\n"
RD"a[124:127],v227 offset:31744\n"
MU"v116,v252,v116\n"
MF"v[192:195],a[100:103],v[24:27],v[192:195]\n"
EX"v61,v61\n"
MU"v117,v252,v117\n"
MF"v[192:195],a[96:99],v[8:11],v[192:195]\n"
WT"vmcnt(20)\n"
MU"v118,v252,v118\n"
MU"v119,v252,v119\n"
PK"v71,v46,v47\n"
MF"v[164:167],a[104:107],v[0:3],v[164:167]\n"
WT"lgkmcnt(3)\n"
EX"v62,v62\n"
PK"v112,v112,v113\n"
MF"v[164:167],a[108:111],v[16:19],v[164:167]\n"
SA"s41,s41,32768\n"
"s_barrier\n"
MF"v[196:199],a[108:111],v[24:27],v[196:199]\n"
"s_cmp_eq_u32 s41,163840\n"
BL"v225,s[8:11],s43 offen offset:0 sc1 lds\n"
BL"v225,s[8:11],s43 offen offset:1024 sc1 lds\n"
MF"v[196:199],a[104:107],v[8:11],v[196:199]\n"
"s_cselect_b32 s41,0,s41\n"
BL"v225,s[8:11],s43 offen offset:2048 sc1 lds\n"
RD"a[64:67],v226 offset:0\n"
MF"v[168:171],a[112:115],v[0:3],v[168:171]\n"
WT"lgkmcnt(1)\n"
EX"v63,v63\n"
"v_add_u32 v227,s41,v224\n"
MF"v[168:171],a[116:119],v[16:19],v[168:171]\n"
RD"a[68:71],v226 offset:8192\n"
RD"a[72:75],v226 offset:1024\n"
PK"v113,v114,v115\n"
MF"v[200:203],a[116:119],v[24:27],v[200:203]\n"
RD"a[76:79],v226 offset:9216\n"
PK"v114,v116,v117\n"
PK"v115,v118,v119\n"
MF"v[200:203],a[112:115],v[8:11],v[200:203]\n"
BS"v[112:115],v224,s[8:11],s51 offen offset:0 sc1\n"
BL"v225,s[8:11],s43 offen offset:3072 sc1 lds\n"
MF"v[172:175],a[120:123],v[0:3],v[172:175]\n"
SA"m0,s40,16384\n"
RD"a[80:83],v226 offset:2048\n"
MU"v120,v252,v120\n"
MU"v121,v252,v121\n"
MF"v[172:175],a[124:127],v[16:19],v[172:175]\n"
RD"a[84:87],v226 offset:10240\n"
PK"v76,v56,v57\n"
MU"v122,v252,v122\n"
MF"v[204:207],a[124:127],v[24:27],v[204:207]\n"
RD"a[88:91],v226 offset:3072\n"
MU"v123,v252,v123\n"
MU"v124,v252,v124\n"
MF"v[204:207],a[120:123],v[8:11],v[204:207]\n"
SA"s40,s40,32768\n"
RD"a[92:95],v226 offset:11264\n"
MU"v125,v252,v125\n"
MU"v126,v252,v126\n"
MF"a[140:143],a[128:131],v[0:3],a[140:143]\n"
"s_cmp_eq_u32 s40,s46\n"
BL"v225,s[8:11],s44 offen offset:0 sc1 lds\n"
RD"a[96:99],v226 offset:4096\n"
MF"a[140:143],a[128:131],v[16:19],a[140:143]\n"
"s_cselect_b32 s40,s47,s40\n"
RD"a[100:103],v226 offset:12288\n"
MU"v127,v252,v127\n"
PK"v120,v120,v121\n"
MF"a[144:147],a[128:131],v[24:27],a[144:147]\n"
PK"v77,v58,v59\n"
PK"v78,v60,v61\n"
PK"v79,v62,v63\n"
MF"a[144:147],a[128:131],v[8:11],a[144:147]\n"
WT"lgkmcnt(5)\n"
RD"a[104:107],v226 offset:5120\n"
PK"v121,v122,v123\n"
PK"v122,v124,v125\n"
MF"v[176:179],a[64:67],v[68:71],v[176:179]\n"
RD"a[108:111],v226 offset:13312\n"
PK"v123,v126,v127\n"
MU"v128,v252,v128\n"
MF"v[176:179],a[68:71],v[76:79],v[176:179]\n"
WT"vmcnt(14)\n"
BS"v[120:123],v224,s[8:11],s51 offen offset:1024 sc1\n"
RD"a[112:115],v226 offset:6144\n"
MF"v[180:183],a[72:75],v[68:71],v[180:183]\n"
RD"a[116:119],v226 offset:14336\n"
MU"v129,v252,v129\n"
MU"v130,v252,v130\n"
MF"v[180:183],a[76:79],v[76:79],v[180:183]\n"
BL"v225,s[8:11],s44 offen offset:1024 sc1 lds\n"
RD"a[120:123],v226 offset:7168\n"
MF"v[184:187],a[80:83],v[68:71],v[184:187]\n"
WT"lgkmcnt(5)\n"
RD"a[124:127],v226 offset:15360\n"
MU"v131,v252,v131\n"
MU"v132,v252,v132\n"
MF"v[184:187],a[84:87],v[76:79],v[184:187]\n"
"s_barrier\n"
MF"v[188:191],a[88:91],v[68:71],v[188:191]\n"
RD"a[64:67],v227 offset:0\n"
MU"v133,v252,v133\n"
MU"v134,v252,v134\n"
MF"v[188:191],a[92:95],v[76:79],v[188:191]\n"
BL"v225,s[8:11],s44 offen offset:2048 sc1 lds\n"
RD"a[68:71],v227 offset:1024\n"
MF"v[192:195],a[96:99],v[68:71],v[192:195]\n"
RD"a[72:75],v227 offset:2048\n"
MU"v135,v252,v135\n"
PK"v128,v128,v129\n"
MF"v[192:195],a[100:103],v[76:79],v[192:195]\n"
WT"lgkmcnt(5)\n"
BL"v225,s[8:11],s44 offen offset:3072 sc1 lds\n"
PK"v129,v130,v131\n"
MF"v[196:199],a[104:107],v[68:71],v[196:199]\n"
RD"a[76:79],v227 offset:3072\n"
PK"v130,v132,v133\n"
PK"v131,v134,v135\n"
MF"v[196:199],a[108:111],v[76:79],v[196:199]\n"
BS"v[128:131],v224,s[8:11],s51 offen offset:2048 sc1\n"
MU"v136,v252,v136\n"
MF"v[200:203],a[112:115],v[68:71],v[200:203]\n"
RD"a[80:83],v227 offset:4096\n"
MU"v137,v252,v137\n"
MU"v138,v252,v138\n"
MF"v[200:203],a[116:119],v[76:79],v[200:203]\n"
WT"lgkmcnt(4)\n"
RD"a[84:87],v227 offset:5120\n"
MU"v139,v252,v139\n"
MU"v140,v252,v140\n"
MF"v[204:207],a[120:123],v[68:71],v[204:207]\n"
RD"a[88:91],v227 offset:6144\n"
MU"v141,v252,v141\n"
MU"v142,v252,v142\n"
MF"v[204:207],a[124:127],v[76:79],v[204:207]\n"
PK"v136,v136,v137\n"
MU"v143,v252,v143\n"
PK"v137,v138,v139\n"
MF"a[144:147],a[128:131],v[68:71],a[144:147]\n"
WT"lgkmcnt(5)\n"
PK"v138,v140,v141\n"
PK"v139,v142,v143\n"
MF"a[144:147],a[128:131],v[76:79],a[144:147]\n"
BS"v[136:139],v224,s[8:11],s51 offen offset:3072 sc1\n"
"s_branch LTL_%=\n"
".p2align 6\n"
"LTH_%=:\n"
RD"a[92:95],v227 offset:7168\n"
MF"v[0:3],a[64:67],v[80:83],0\n"
"v_accvgpr_read_b32 v251,a140\n"
"v_subrev_u32 v253,0x30800000,v251\n"
MF"v[0:3],a[68:71],v[88:91],v[0:3]\n"
RD"a[96:99],v227 offset:8192\n"
MF"v[40:43],a[68:71],v[120:123],0\n"
"v_cmp_le_u32 vcc,0x1f000000,v253\n"
"s_cmp_lg_u64 vcc,0\n"
"s_cbranch_scc1 L1069_%=\n"
"L1068_%=:\n"
MF"v[40:43],a[64:67],v[112:115],v[40:43]\n"
WT"lgkmcnt(3)\n"
"v_rcp_f32 v251,v251\n"
MF"v[4:7],a[72:75],v[80:83],0\n"
SA"s41,s41,32768\n"
RD"a[100:103],v227 offset:9216\n"
MU"v144,v251,v144\n"
MF"v[4:7],a[76:79],v[88:91],v[4:7]\n"
"s_cmp_eq_u32 s41,163840\n"
RD"a[104:107],v227 offset:10240\n"
MU"v145,v251,v145\n"
MF"v[44:47],a[76:79],v[120:123],0\n"
"s_cselect_b32 s41,0,s41\n"
MU"v146,v251,v146\n"
"v_add_u32 v226,s41,v224\n"
MF"v[44:47],a[72:75],v[112:115],v[44:47]\n"
RD"a[108:111],v227 offset:11264\n"
MU"v147,v251,v147\n"
MF"v[8:11],a[80:83],v[80:83],0\n"
SA"s41,s41,32768\n"
MU"v148,v251,v148\n"
MU"v149,v251,v149\n"
MF"v[8:11],a[84:87],v[88:91],v[8:11]\n"
"s_cmp_eq_u32 s41,163840\n"
RD"a[112:115],v227 offset:12288\n"
MU"v150,v251,v150\n"
MU"v151,v251,v151\n"
MF"v[48:51],a[84:87],v[120:123],0\n"
"s_cselect_b32 s41,0,s41\n"
PK"v144,v144,v145\n"
"v_add_u32 v235,s41,v224\n"
PK"v145,v146,v147\n"
MF"v[48:51],a[80:83],v[112:115],v[48:51]\n"
WT"lgkmcnt(3)\n"
RD"a[116:119],v227 offset:13312\n"
PK"v146,v148,v149\n"
MF"v[12:15],a[88:91],v[80:83],0\n"
SA"s41,s41,32768\n"
PK"v147,v150,v151\n"
MU"v152,v251,v152\n"
MF"v[12:15],a[92:95],v[88:91],v[12:15]\n"
"s_cmp_eq_u32 s41,163840\n"
BS"v[144:147],v224,s[8:11],s60 offen offset:0 sc1\n"
MU"v153,v251,v153\n"
MF"v[52:55],a[92:95],v[120:123],0\n"
"s_cselect_b32 s41,0,s41\n"
RD"a[120:123],v227 offset:14336\n"
"v_add_u32 v236,s41,v224\n"
MU"v154,v251,v154\n"
MF"v[52:55],a[88:91],v[112:115],v[52:55]\n"
RD"a[124:127],v227 offset:15360\n"
MU"v155,v251,v155\n"
MU"v156,v251,v156\n"
MF"v[16:19],a[96:99],v[80:83],0\n"
SA"s41,s41,32768\n"
MU"v157,v251,v157\n"
MU"v158,v251,v158\n"
MU"v159,v251,v159\n"
MF"v[16:19],a[100:103],v[88:91],v[16:19]\n"
"s_cmp_eq_u32 s41,163840\n"
RD"a[64:67],v227 offset:16384\n"
PK"v152,v152,v153\n"
PK"v153,v154,v155\n"
MF"v[56:59],a[100:103],v[120:123],0\n"
"s_cselect_b32 s41,0,s41\n"
PK"v154,v156,v157\n"
"v_add_u32 v237,s41,v224\n"
PK"v155,v158,v159\n"
MF"v[56:59],a[96:99],v[112:115],v[56:59]\n"
WT"lgkmcnt(3)\n"
BS"v[152:155],v224,s[8:11],s60 offen offset:1024 sc1\n"
MU"v160,v251,v160\n"
MF"v[20:23],a[104:107],v[80:83],0\n"
RD"a[68:71],v227 offset:17408\n"
MU"v161,v251,v161\n"
MU"v162,v251,v162\n"
MF"v[20:23],a[108:111],v[88:91],v[20:23]\n"
WT"vmcnt(2)\n"
"buffer_store_dwordx2 v[232:233],v230,s[8:11],s61 offen sc1\n"
MU"v163,v251,v163\n"
MF"v[60:63],a[108:111],v[120:123],0\n"
RD"a[72:75],v227 offset:18432\n"
MU"v164,v251,v164\n"
MU"v165,v251,v165\n"
MF"v[60:63],a[104:107],v[112:115],v[60:63]\n"
RD"a[76:79],v227 offset:19456\n"
MU"v166,v251,v166\n"
MU"v167,v251,v167\n"
MF"v[24:27],a[112:115],v[80:83],0\n"
SA"s51,s60,16384\n"
"v_accvgpr_read_b32 v252,a144\n"
"v_subrev_u32 v253,0x30800000,v252\n"
PK"v160,v160,v161\n"
MF"v[24:27],a[116:119],v[88:91],v[24:27]\n"
"s_lshl_b32 s62,s34,14\n"
RD"a[80:83],v227 offset:20480\n"
PK"v161,v162,v163\n"
PK"v162,v164,v165\n"
MF"v[64:67],a[116:119],v[120:123],0\n"
"s_sub_u32 s62,s61,s62\n"
"v_cmp_le_u32 vcc,0x1f000000,v253\n"
"s_cmp_lg_u64 vcc,0\n"
"s_cbranch_scc1 L1075_%=\n"
"L1074_%=:\n"
MF"v[64:67],a[112:115],v[112:115],v[64:67]\n"
WT"lgkmcnt(3)\n"
"v_rcp_f32 v252,v252\n"
PK"v163,v166,v167\n"
MF"v[28:31],a[120:123],v[80:83],0\n"
SA"s62,s62,122880\n"
BS"v[160:163],v224,s[8:11],s60 offen offset:2048 sc1\n"
MU"v168,v251,v168\n"
MF"v[28:31],a[124:127],v[88:91],v[28:31]\n"
RD"a[84:87],v227 offset:21504\n"
MU"v169,v251,v169\n"
MU"v170,v251,v170\n"
MF"v[68:71],a[124:127],v[120:123],0\n"
RD"a[88:91],v227 offset:22528\n"
MU"v171,v251,v171\n"
MU"v172,v251,v172\n"
MF"v[68:71],a[120:123],v[112:115],v[68:71]\n"
RD"a[92:95],v227 offset:23552\n"
MU"v173,v251,v173\n"
MU"v174,v251,v174\n"
MF"v[32:35],a[64:67],v[80:83],0\n"
MU"v175,v251,v175\n"
PK"v168,v168,v169\n"
PK"v169,v170,v171\n"
MF"v[32:35],a[68:71],v[88:91],v[32:35]\n"
RD"a[96:99],v227 offset:24576\n"
MU"v176,v252,v176\n"
MU"v177,v252,v177\n"
MF"v[72:75],a[68:71],v[120:123],0\n"
PK"v170,v172,v173\n"
PK"v171,v174,v175\n"
MU"v178,v252,v178\n"
MF"v[72:75],a[64:67],v[112:115],v[72:75]\n"
WT"lgkmcnt(4)\n"
BS"v[168:171],v224,s[8:11],s60 offen offset:3072 sc1\n"
MU"v179,v252,v179\n"
MF"v[36:39],a[72:75],v[80:83],0\n"
RD"a[100:103],v227 offset:25600\n"
MU"v180,v252,v180\n"
MU"v181,v252,v181\n"
MF"v[36:39],a[76:79],v[88:91],v[36:39]\n"
RD"a[104:107],v227 offset:26624\n"
MU"v182,v252,v182\n"
MU"v183,v252,v183\n"
MF"v[76:79],a[76:79],v[120:123],0\n"
PK"v176,v176,v177\n"
PK"v177,v178,v179\n"
PK"v178,v180,v181\n"
MF"v[76:79],a[72:75],v[112:115],v[76:79]\n"
RD"a[108:111],v227 offset:27648\n"
PK"v179,v182,v183\n"
MU"v184,v252,v184\n"
MF"v[0:3],a[80:83],v[96:99],v[0:3]\n"
WT"lgkmcnt(3)\n"
BS"v[176:179],v224,s[8:11],s51 offen offset:0 sc1\n"
MU"v185,v252,v185\n"
MF"v[0:3],a[84:87],v[104:107],v[0:3]\n"
RD"a[112:115],v227 offset:28672\n"
MU"v186,v252,v186\n"
MU"v187,v252,v187\n"
MF"v[40:43],a[84:87],v[136:139],v[40:43]\n"
MU"v188,v252,v188\n"
MU"v189,v252,v189\n"
MU"v190,v252,v190\n"
MF"v[40:43],a[80:83],v[128:131],v[40:43]\n"
RD"a[116:119],v227 offset:29696\n"
MU"v191,v252,v191\n"
PK"v184,v184,v185\n"
MF"v[4:7],a[88:91],v[96:99],v[4:7]\n"
PK"v185,v186,v187\n"
PK"v186,v188,v189\n"
PK"v187,v190,v191\n"
MF"v[4:7],a[92:95],v[104:107],v[4:7]\n"
BS"v[184:187],v224,s[8:11],s51 offen offset:1024 sc1\n"
MU"v192,v252,v192\n"
MF"v[44:47],a[92:95],v[136:139],v[44:47]\n"
RD"a[120:123],v227 offset:30720\n"
MU"v193,v252,v193\n"
MU"v194,v252,v194\n"
MF"v[44:47],a[88:91],v[128:131],v[44:47]\n"
RD"a[124:127],v227 offset:31744\n"
"v_bfe_u32 v238,v224,4,2\n"
MU"v195,v252,v195\n"
MF"v[8:11],a[96:99],v[96:99],v[8:11]\n"
WT"lgkmcnt(3)\n"
"s_barrier\n"
MF"v[8:11],a[100:103],v[104:107],v[8:11]\n"
"s_andn2_b32 s63,s61,255\n"
RD"a[64:67],v226 offset:0\n"
"v_lshlrev_b32 v238,6,v238\n"
MU"v196,v252,v196\n"
MF"v[48:51],a[100:103],v[136:139],v[48:51]\n"
"buffer_load_dwordx2 v[240:241],v238,s[8:11],s63 offen sc1\n"
MU"v197,v252,v197\n"
MF"v[48:51],a[96:99],v[128:131],v[48:51]\n"
RD"a[68:71],v226 offset:1024\n"
MU"v198,v252,v198\n"
MU"v199,v252,v199\n"
MF"v[12:15],a[104:107],v[96:99],v[12:15]\n"
PK"v192,v192,v193\n"
PK"v193,v194,v195\n"
PK"v194,v196,v197\n"
MF"v[12:15],a[108:111],v[104:107],v[12:15]\n"
RD"a[72:75],v226 offset:2048\n"
PK"v195,v198,v199\n"
MU"v200,v252,v200\n"
MF"v[52:55],a[108:111],v[136:139],v[52:55]\n"
BS"v[192:195],v224,s[8:11],s51 offen offset:2048 sc1\n"
MU"v201,v252,v201\n"
MF"v[52:55],a[104:107],v[128:131],v[52:55]\n"
RD"a[76:79],v226 offset:3072\n"
MU"v202,v252,v202\n"
MU"v203,v252,v203\n"
MF"v[16:19],a[112:115],v[96:99],v[16:19]\n"
WT"lgkmcnt(3)\n"
MU"v204,v252,v204\n"
MU"v205,v252,v205\n"
MU"v206,v252,v206\n"
MF"v[16:19],a[116:119],v[104:107],v[16:19]\n"
RD"a[80:83],v226 offset:4096\n"
MU"v207,v252,v207\n"
PK"v200,v200,v201\n"
MF"v[56:59],a[116:119],v[136:139],v[56:59]\n"
"v_readlane_b32 s43,v228,s42\n"
PK"v201,v202,v203\n"
PK"v202,v204,v205\n"
MF"v[56:59],a[112:115],v[128:131],v[56:59]\n"
RD"a[84:87],v226 offset:5120\n"
PK"v203,v206,v207\n"
MF"v[20:23],a[120:123],v[96:99],v[20:23]\n"
BS"v[200:203],v224,s[8:11],s51 offen offset:3072 sc1\n"
MF"v[20:23],a[124:127],v[104:107],v[20:23]\n"
RD"a[88:91],v226 offset:6144\n"
MF"v[60:63],a[124:127],v[136:139],v[60:63]\n"
"s_mov_b32 m0,s40\n"
MF"v[60:63],a[120:123],v[128:131],v[60:63]\n"
RD"a[92:95],v226 offset:7168\n"
MF"v[24:27],a[64:67],v[96:99],v[24:27]\n"
WT"lgkmcnt(3)\n"
"v_readlane_b32 s44,v229,s42\n"
MF"v[24:27],a[68:71],v[104:107],v[24:27]\n"
RD"a[96:99],v227 offset:0\n"
MF"v[64:67],a[68:71],v[136:139],v[64:67]\n"
SA"s42,s42,1\n"
BL"v225,s[8:11],s43 offen offset:0 sc1 lds\n"
MF"v[64:67],a[64:67],v[128:131],v[64:67]\n"
RD"a[100:103],v227 offset:1024\n"
MF"v[28:31],a[72:75],v[96:99],v[28:31]\n"
MF"v[28:31],a[76:79],v[104:107],v[28:31]\n"
RD"a[104:107],v227 offset:2048\n"
MF"v[68:71],a[76:79],v[136:139],v[68:71]\n"
MF"v[68:71],a[72:75],v[128:131],v[68:71]\n"
RD"a[108:111],v227 offset:3072\n"
MF"v[32:35],a[80:83],v[96:99],v[32:35]\n"
WT"lgkmcnt(3)\n"
BL"v225,s[8:11],s43 offen offset:1024 sc1 lds\n"
MF"v[32:35],a[84:87],v[104:107],v[32:35]\n"
RD"a[112:115],v227 offset:4096\n"
MF"v[72:75],a[84:87],v[136:139],v[72:75]\n"
MF"v[72:75],a[80:83],v[128:131],v[72:75]\n"
RD"a[116:119],v227 offset:5120\n"
MF"v[36:39],a[88:91],v[96:99],v[36:39]\n"
MF"v[36:39],a[92:95],v[104:107],v[36:39]\n"
RD"a[120:123],v227 offset:6144\n"
MF"v[76:79],a[92:95],v[136:139],v[76:79]\n"
BL"v225,s[8:11],s43 offen offset:2048 sc1 lds\n"
MF"v[76:79],a[88:91],v[128:131],v[76:79]\n"
RD"a[124:127],v227 offset:7168\n"
MF"v[84:87],a[96:99],v[144:147],0\n"
WT"lgkmcnt(3)\n"
MF"v[84:87],a[100:103],v[152:155],v[84:87]\n"
RD"a[64:67],v227 offset:8192\n"
MF"v[164:167],a[100:103],v[184:187],0\n"
MF"v[164:167],a[96:99],v[176:179],v[164:167]\n"
RD"a[68:71],v227 offset:9216\n"
MF"v[92:95],a[104:107],v[144:147],0\n"
BL"v225,s[8:11],s43 offen offset:3072 sc1 lds\n"
MF"v[92:95],a[108:111],v[152:155],v[92:95]\n"
RD"a[72:75],v227 offset:10240\n"
MF"v[172:175],a[108:111],v[184:187],0\n"
SA"m0,s40,16384\n"
MF"v[172:175],a[104:107],v[176:179],v[172:175]\n"
RD"a[76:79],v227 offset:11264\n"
MF"v[100:103],a[112:115],v[144:147],0\n"
WT"lgkmcnt(3)\n"
MF"v[100:103],a[116:119],v[152:155],v[100:103]\n"
SA"s48,s59,32768\n"
RD"a[80:83],v227 offset:12288\n"
MF"v[180:183],a[116:119],v[184:187],0\n"
"s_and_b32 s48,s48,0x18000\n"
BL"v225,s[8:11],s44 offen offset:0 sc1 lds\n"
MF"v[180:183],a[112:115],v[176:179],v[180:183]\n"
SA"s40,s40,32768\n"
RD"a[84:87],v227 offset:13312\n"
"s_andn2_b32 s49,s59,0x18000\n"
MF"v[108:111],a[120:123],v[144:147],0\n"
"s_cmp_eq_u32 s40,s46\n"
"s_cselect_b32 s40,s47,s40\n"
WT"vmcnt(7)\n"
"v_cmp_eq_u64 vcc,s[28:29],v[240:241]\n"
"s_cmp_eq_u64 vcc,exec\n"
"s_cbranch_scc0 L1080_%=\n"
"L1081_%=:\n"
MF"v[108:111],a[124:127],v[152:155],v[108:111]\n"
"s_or_b32 s52,s48,s49\n"
RD"a[88:91],v227 offset:14336\n"
SA"s53,s52,16384\n"
MF"v[188:191],a[124:127],v[184:187],0\n"
BL"a[0:3],v224,s[8:11],s52 offen offset:0 sc1\n"
MF"v[188:191],a[120:123],v[176:179],v[188:191]\n"
RD"a[92:95],v227 offset:15360\n"
MF"v[116:119],a[64:67],v[144:147],0\n"
WT"lgkmcnt(3)\n"
BL"v225,s[8:11],s44 offen offset:1024 sc1 lds\n"
MF"v[116:119],a[68:71],v[152:155],v[116:119]\n"
RD"a[96:99],v227 offset:16384\n"
MF"v[196:199],a[68:71],v[184:187],0\n"
BL"a[4:7],v224,s[8:11],s53 offen offset:0 sc1\n"
MF"v[196:199],a[64:67],v[176:179],v[196:199]\n"
RD"a[100:103],v227 offset:17408\n"
MF"v[124:127],a[72:75],v[144:147],0\n"
MF"v[124:127],a[76:79],v[152:155],v[124:127]\n"
RD"a[104:107],v227 offset:18432\n"
MF"v[204:207],a[76:79],v[184:187],0\n"
BL"v225,s[8:11],s44 offen offset:2048 sc1 lds\n"
MF"v[204:207],a[72:75],v[176:179],v[204:207]\n"
RD"a[108:111],v227 offset:19456\n"
MF"v[132:135],a[80:83],v[144:147],0\n"
WT"lgkmcnt(3)\n"
BL"a[8:11],v224,s[8:11],s52 offen offset:1024 sc1\n"
MF"v[132:135],a[84:87],v[152:155],v[132:135]\n"
RD"a[112:115],v227 offset:20480\n"
MF"v[208:211],a[84:87],v[184:187],0\n"
"v_readlane_b32 s43,v228,s42\n"
MF"v[208:211],a[80:83],v[176:179],v[208:211]\n"
RD"a[116:119],v227 offset:21504\n"
MF"v[140:143],a[88:91],v[144:147],0\n"
BL"a[12:15],v224,s[8:11],s53 offen offset:1024 sc1\n"
MF"v[140:143],a[92:95],v[152:155],v[140:143]\n"
BL"v225,s[8:11],s44 offen offset:3072 sc1 lds\n"
MF"v[212:215],a[92:95],v[184:187],0\n"
"s_mov_b32 m0,s40\n"
RD"a[120:123],v227 offset:22528\n"
MF"v[212:215],a[88:91],v[176:179],v[212:215]\n"
WT"vmcnt(12)\n"
"buffer_store_dwordx2 v[232:233],v230,s[8:11],s62 offen sc1\n"
MF"v[148:151],a[96:99],v[144:147],0\n"
WT"lgkmcnt(2)\n"
RD"a[124:127],v227 offset:23552\n"
"v_readlane_b32 s44,v229,s42\n"
MF"v[148:151],a[100:103],v[152:155],v[148:151]\n"
BL"a[16:19],v224,s[8:11],s52 offen offset:2048 sc1\n"
MF"v[216:219],a[100:103],v[184:187],0\n"
SA"s42,s42,1\n"
RD"a[64:67],v227 offset:24576\n"
MF"v[216:219],a[96:99],v[176:179],v[216:219]\n"
BL"v225,s[8:11],s43 offen offset:0 sc1 lds\n"
MF"v[156:159],a[104:107],v[144:147],0\n"
RD"a[68:71],v227 offset:25600\n"
MF"v[156:159],a[108:111],v[152:155],v[156:159]\n"
BL"a[20:23],v224,s[8:11],s53 offen offset:2048 sc1\n"
MF"v[220:223],a[108:111],v[184:187],0\n"
RD"a[72:75],v227 offset:26624\n"
MF"v[220:223],a[104:107],v[176:179],v[220:223]\n"
RD"a[76:79],v227 offset:27648\n"
MF"v[84:87],a[112:115],v[160:163],v[84:87]\n"
WT"lgkmcnt(4)\n"
BL"v225,s[8:11],s43 offen offset:1024 sc1 lds\n"
MF"v[84:87],a[116:119],v[168:171],v[84:87]\n"
"s_lshr_b32 s89,s31,3\n"
RD"a[80:83],v227 offset:28672\n"
"s_lshl_b32 s89,s89,4\n"
MF"v[164:167],a[116:119],v[200:203],v[164:167]\n"
SA"s89,s89,s30\n"
BL"a[24:27],v224,s[8:11],s52 offen offset:3072 sc1\n"
MF"v[164:167],a[112:115],v[192:195],v[164:167]\n"
"s_lshl_b32 s89,s89,3\n"
RD"a[84:87],v227 offset:29696\n"
MF"v[92:95],a[120:123],v[160:163],v[92:95]\n"
"s_xor_b32 s23,s32,s30\n"
MF"v[92:95],a[124:127],v[168:171],v[92:95]\n"
SA"s48,s59,65536\n"
RD"a[88:91],v227 offset:30720\n"
SA"s89,s89,s23\n"
MF"v[172:175],a[124:127],v[200:203],v[172:175]\n"
"s_and_b32 s48,s48,0x18000\n"
BL"a[28:31],v224,s[8:11],s53 offen offset:3072 sc1\n"
MF"v[172:175],a[120:123],v[192:195],v[172:175]\n"
WT"lgkmcnt(3)\n"
BL"v225,s[8:11],s43 offen offset:2048 sc1 lds\n"
MF"v[100:103],a[64:67],v[160:163],v[100:103]\n"
"s_xor_b32 s90,s89,36\n"
RD"a[92:95],v227 offset:31744\n"
"s_andn2_b32 s49,s59,0x18000\n"
MF"v[100:103],a[68:71],v[168:171],v[100:103]\n"
"s_mul_i32 s88,s89,5120\n"
RD"a[96:99],v226 offset:0\n"
"s_or_b32 s52,s48,s49\n"
MF"v[180:183],a[68:71],v[200:203],v[180:183]\n"
SA"s53,s52,16384\n"
BL"a[180:183],v224,s[8:11],s52 offen offset:0 sc1\n"
MF"v[180:183],a[64:67],v[192:195],v[180:183]\n"
"s_mul_i32 s22,s90,5120\n"
RD"a[100:103],v226 offset:1024\n"
"s_sub_u32 s90,s90,s89\n"
MF"v[108:111],a[72:75],v[160:163],v[108:111]\n"
"s_lshl_b32 s90,s90,6\n"
BL"v225,s[8:11],s43 offen offset:3072 sc1 lds\n"
MF"v[108:111],a[76:79],v[168:171],v[108:111]\n"
SA"s88,s88,61603840\n"
RD"a[104:107],v226 offset:2048\n"
SA"s22,s22,61603840\n"
MF"v[188:191],a[76:79],v[200:203],v[188:191]\n"
"s_cmp_eq_u32 s23,0\n"
BL"a[184:187],v224,s[8:11],s53 offen offset:0 sc1\n"
MF"v[188:191],a[72:75],v[192:195],v[188:191]\n"
WT"lgkmcnt(3)\n"
RD"a[108:111],v226 offset:3072\n"
"s_cselect_b32 s88,2130706432,s88\n"
MF"v[116:119],a[80:83],v[160:163],v[116:119]\n"
SA"m0,s40,16384\n"
"s_cmp_eq_u32 s23,4\n"
"s_cselect_b32 s22,2130706432,s22\n"
MF"v[116:119],a[84:87],v[168:171],v[116:119]\n"
"s_lshl_b32 s89,s89,6\n"
RD"a[112:115],v226 offset:4096\n"
"v_bfe_u32 v239,v224,1,6\n"
MF"v[196:199],a[84:87],v[200:203],v[196:199]\n"
SA"s89,s89,131072\n"
BL"v225,s[8:11],s44 offen offset:0 sc1 lds\n"
MF"v[196:199],a[80:83],v[192:195],v[196:199]\n"
SA"s40,s40,32768\n"
RD"a[116:119],v226 offset:5120\n"
"v_bfe_u32 v242,v224,7,1\n"
MF"v[124:127],a[88:91],v[160:163],v[124:127]\n"
"s_cmp_eq_u32 s40,s46\n"
BL"a[188:191],v224,s[8:11],s52 offen offset:1024 sc1\n"
MF"v[124:127],a[92:95],v[168:171],v[124:127]\n"
"s_cselect_b32 s40,s47,s40\n"
RD"a[120:123],v226 offset:6144\n"
"v_mad_u32_u24 v239,v242,s90,v239\n"
MF"v[204:207],a[92:95],v[200:203],v[204:207]\n"
"v_lshrrev_b32 v242,8,v224\n"
"v_lshl_add_u32 v239,v242,27,v239\n"
MF"v[204:207],a[88:91],v[192:195],v[204:207]\n"
WT"lgkmcnt(3)\n"
RD"a[124:127],v226 offset:7168\n"
MF"v[132:135],a[96:99],v[160:163],v[132:135]\n"
BL"a[192:195],v224,s[8:11],s53 offen offset:1024 sc1\n"
MF"v[132:135],a[100:103],v[168:171],v[132:135]\n"
BL"v225,s[8:11],s44 offen offset:1024 sc1 lds\n"
MF"v[208:211],a[100:103],v[200:203],v[208:211]\n"
RD"a[64:67],v226 offset:8192\n"
MF"v[208:211],a[96:99],v[192:195],v[208:211]\n"
RD"a[68:71],v226 offset:9216\n"
MF"v[140:143],a[104:107],v[160:163],v[140:143]\n"
BL"a[196:199],v224,s[8:11],s52 offen offset:2048 sc1\n"
MF"v[140:143],a[108:111],v[168:171],v[140:143]\n"
RD"a[72:75],v226 offset:10240\n"
MF"v[212:215],a[108:111],v[200:203],v[212:215]\n"
BL"v225,s[8:11],s44 offen offset:2048 sc1 lds\n"
MF"v[212:215],a[104:107],v[192:195],v[212:215]\n"
WT"lgkmcnt(3)\n"
RD"a[76:79],v226 offset:11264\n"
MF"v[148:151],a[112:115],v[160:163],v[148:151]\n"
BL"a[200:203],v224,s[8:11],s53 offen offset:2048 sc1\n"
MF"v[148:151],a[116:119],v[168:171],v[148:151]\n"
RD"a[80:83],v226 offset:12288\n"
MF"v[216:219],a[116:119],v[200:203],v[216:219]\n"
MF"v[216:219],a[112:115],v[192:195],v[216:219]\n"
RD"a[84:87],v226 offset:13312\n"
MF"v[156:159],a[120:123],v[160:163],v[156:159]\n"
BL"v225,s[8:11],s44 offen offset:3072 sc1 lds\n"
MF"v[156:159],a[124:127],v[168:171],v[156:159]\n"
RD"a[88:91],v226 offset:14336\n"
MF"v[220:223],a[124:127],v[200:203],v[220:223]\n"
BL"a[204:207],v224,s[8:11],s52 offen offset:3072 sc1\n"
MF"v[220:223],a[120:123],v[192:195],v[220:223]\n"
WT"vmcnt(26) lgkmcnt(3)\n"
RD"a[92:95],v226 offset:15360\n"
MF"v[0:3],a[64:67],a[0:3],v[0:3]\n"
WT"vmcnt(22)\n"
MF"v[0:3],a[68:71],a[8:11],v[0:3]\n"
WT"vmcnt(21)\n"
RD"a[96:99],v226 offset:16384\n"
SA"s48,s59,98304\n"
MF"v[40:43],a[68:71],a[12:15],v[40:43]\n"
"s_and_b32 s48,s48,0x18000\n"
BL"a[208:211],v224,s[8:11],s53 offen offset:3072 sc1\n"
MF"v[40:43],a[64:67],a[4:7],v[40:43]\n"
"s_andn2_b32 s49,s59,0x18000\n"
RD"a[100:103],v226 offset:17408\n"
MF"v[4:7],a[72:75],a[0:3],v[4:7]\n"
"s_or_b32 s52,s48,s49\n"
SA"s53,s52,16384\n"
MF"v[4:7],a[76:79],a[8:11],v[4:7]\n"
RD"a[104:107],v226 offset:18432\n"
MF"v[44:47],a[76:79],a[12:15],v[44:47]\n"
BL"a[212:215],v224,s[8:11],s52 offen offset:0 sc1\n"
MF"v[44:47],a[72:75],a[4:7],v[44:47]\n"
WT"lgkmcnt(3)\n"
RD"a[108:111],v226 offset:19456\n"
MF"v[8:11],a[80:83],a[0:3],v[8:11]\n"
MF"v[8:11],a[84:87],a[8:11],v[8:11]\n"
RD"a[112:115],v226 offset:20480\n"
MF"v[48:51],a[84:87],a[12:15],v[48:51]\n"
BL"a[216:219],v224,s[8:11],s53 offen offset:0 sc1\n"
MF"v[48:51],a[80:83],a[4:7],v[48:51]\n"
RD"a[116:119],v226 offset:21504\n"
MF"v[12:15],a[88:91],a[0:3],v[12:15]\n"
MF"v[12:15],a[92:95],a[8:11],v[12:15]\n"
RD"a[120:123],v226 offset:22528\n"
MF"v[52:55],a[92:95],a[12:15],v[52:55]\n"
MF"v[52:55],a[88:91],a[4:7],v[52:55]\n"
WT"lgkmcnt(3)\n"
RD"a[124:127],v226 offset:23552\n"
MF"v[16:19],a[96:99],a[0:3],v[16:19]\n"
BL"a[220:223],v224,s[8:11],s52 offen offset:1024 sc1\n"
MF"v[16:19],a[100:103],a[8:11],v[16:19]\n"
RD"a[64:67],v226 offset:24576\n"
MF"v[56:59],a[100:103],a[12:15],v[56:59]\n"
MF"v[56:59],a[96:99],a[4:7],v[56:59]\n"
RD"a[68:71],v226 offset:25600\n"
MF"v[20:23],a[104:107],a[0:3],v[20:23]\n"
BL"a[224:227],v224,s[8:11],s53 offen offset:1024 sc1\n"
MF"v[20:23],a[108:111],a[8:11],v[20:23]\n"
RD"a[72:75],v226 offset:26624\n"
MF"v[60:63],a[108:111],a[12:15],v[60:63]\n"
MF"v[60:63],a[104:107],a[4:7],v[60:63]\n"
WT"lgkmcnt(3)\n"
RD"a[76:79],v226 offset:27648\n"
MF"v[24:27],a[112:115],a[0:3],v[24:27]\n"
BL"a[228:231],v224,s[8:11],s52 offen offset:2048 sc1\n"
MF"v[24:27],a[116:119],a[8:11],v[24:27]\n"
RD"a[80:83],v226 offset:28672\n"
MF"v[64:67],a[116:119],a[12:15],v[64:67]\n"
MF"v[64:67],a[112:115],a[4:7],v[64:67]\n"
RD"a[84:87],v226 offset:29696\n"
MF"v[28:31],a[120:123],a[0:3],v[28:31]\n"
BL"a[232:235],v224,s[8:11],s53 offen offset:2048 sc1\n"
MF"v[28:31],a[124:127],a[8:11],v[28:31]\n"
RD"a[88:91],v226 offset:30720\n"
MF"v[68:71],a[124:127],a[12:15],v[68:71]\n"
MF"v[68:71],a[120:123],a[4:7],v[68:71]\n"
WT"lgkmcnt(3)\n"
RD"a[92:95],v226 offset:31744\n"
MF"v[32:35],a[64:67],a[0:3],v[32:35]\n"
"s_barrier\n"
MF"v[32:35],a[68:71],a[8:11],v[32:35]\n"
RD"a[96:99],v235 offset:0\n"
MF"v[72:75],a[68:71],a[12:15],v[72:75]\n"
BL"a[236:239],v224,s[8:11],s52 offen offset:3072 sc1\n"
MF"v[72:75],a[64:67],a[4:7],v[72:75]\n"
RD"a[100:103],v235 offset:1024\n"
MF"v[36:39],a[72:75],a[0:3],v[36:39]\n"
MF"v[36:39],a[76:79],a[8:11],v[36:39]\n"
RD"a[104:107],v235 offset:2048\n"
MF"v[76:79],a[76:79],a[12:15],v[76:79]\n"
BL"a[240:243],v224,s[8:11],s53 offen offset:3072 sc1\n"
MF"v[76:79],a[72:75],a[4:7],v[76:79]\n"
WT"vmcnt(27) lgkmcnt(3)\n"
RD"a[108:111],v235 offset:3072\n"
MF"v[0:3],a[80:83],a[16:19],v[0:3]\n"
WT"vmcnt(23)\n"
MF"v[0:3],a[84:87],a[24:27],v[0:3]\n"
WT"vmcnt(22)\n"
RD"a[112:115],v235 offset:4096\n"
MF"v[40:43],a[84:87],a[28:31],v[40:43]\n"
MF"v[40:43],a[80:83],a[20:23],v[40:43]\n"
RD"a[116:119],v235 offset:5120\n"
MF"v[4:7],a[88:91],a[16:19],v[4:7]\n"
MF"v[4:7],a[92:95],a[24:27],v[4:7]\n"
RD"a[120:123],v235 offset:6144\n"
MF"v[44:47],a[92:95],a[28:31],v[44:47]\n"
MF"v[44:47],a[88:91],a[20:23],v[44:47]\n"
WT"lgkmcnt(3)\n"
RD"a[124:127],v235 offset:7168\n"
MF"v[8:11],a[96:99],a[16:19],v[8:11]\n"
MF"v[8:11],a[100:103],a[24:27],v[8:11]\n"
RD"a[64:67],v235 offset:8192\n"
MF"v[48:51],a[100:103],a[28:31],v[48:51]\n"
MF"v[48:51],a[96:99],a[20:23],v[48:51]\n"
RD"a[68:71],v235 offset:9216\n"
MF"v[12:15],a[104:107],a[16:19],v[12:15]\n"
MF"v[12:15],a[108:111],a[24:27],v[12:15]\n"
RD"a[72:75],v235 offset:10240\n"
"v_bfe_u32 v238,v224,4,2\n"
MF"v[52:55],a[108:111],a[28:31],v[52:55]\n"
"s_andn2_b32 s63,s62,255\n"
"v_lshlrev_b32 v238,6,v238\n"
MF"v[52:55],a[104:107],a[20:23],v[52:55]\n"
WT"lgkmcnt(3)\n"
RD"a[76:79],v235 offset:11264\n"
MF"v[16:19],a[112:115],a[16:19],v[16:19]\n"
"buffer_load_dwordx2 v[240:241],v238,s[8:11],s63 offen sc1\n"
MF"v[16:19],a[116:119],a[24:27],v[16:19]\n"
RD"a[80:83],v235 offset:12288\n"
MF"v[56:59],a[116:119],a[28:31],v[56:59]\n"
MF"v[56:59],a[112:115],a[20:23],v[56:59]\n"
RD"a[84:87],v235 offset:13312\n"
MF"v[20:23],a[120:123],a[16:19],v[20:23]\n"
MF"v[20:23],a[124:127],a[24:27],v[20:23]\n"
RD"a[88:91],v235 offset:14336\n"
MF"v[60:63],a[124:127],a[28:31],v[60:63]\n"
MF"v[60:63],a[120:123],a[20:23],v[60:63]\n"
RD"a[92:95],v235 offset:15360\n"
"s_branch LTJ_%=\n"
"L1004_%=:\n"
"s_mov_b32 s54,0\n"
"L1006_%=:\n"
"s_sleep 2\n"
BL"v[160:163],v224,s[8:11],0 offen offset:256 sc1\n"
BL"v[164:167],v224,s[8:11],0 offen offset:1280 sc1\n"
WT"vmcnt(0)\n"
SA"s54,s54,1\n"
"v_cmp_eq_u64 vcc,s[28:29],v[160:161]\n"
"v_cmp_eq_u64_e64 s[62:63],s[28:29],v[162:163]\n"
"s_and_b64 vcc,vcc,s[62:63]\n"
"v_cmp_eq_u64_e64 s[62:63],s[28:29],v[164:165]\n"
"s_and_b64 vcc,vcc,s[62:63]\n"
"v_cmp_eq_u64_e64 s[62:63],s[28:29],v[166:167]\n"
"s_and_b64 vcc,vcc,s[62:63]\n"
"s_cmp_eq_u64 vcc,exec\n"
"s_cbranch_scc1 L1005_%=\n"
"s_cmp_lt_u32 s54,1048576\n"
"s_cbranch_scc1 L1006_%=\n"
"s_branch L1005_%=\n"
"L1007_%=:\n"
"s_mov_b32 s54,0\n"
"L1009_%=:\n"
"v_max3_u32 v226,v160,v161,v162\n"
"v_max3_u32 v226,v226,v163,v164\n"
"v_max3_u32 v226,v226,v165,v166\n"
"v_max3_u32 v226,v226,v167,v168\n"
"v_max3_u32 v226,v226,v169,v170\n"
"v_max3_u32 v226,v226,v171,v172\n"
"v_max3_u32 v226,v226,v173,v174\n"
"v_max3_u32 v226,v226,v175,v176\n"
"v_max3_u32 v226,v226,v177,v178\n"
"v_max3_u32 v226,v226,v179,v180\n"
"v_max3_u32 v226,v226,v181,v182\n"
"v_max3_u32 v226,v226,v183,v184\n"
"v_max3_u32 v226,v226,v185,v186\n"
"v_max3_u32 v226,v226,v187,v188\n"
"v_max3_u32 v226,v226,v189,v190\n"
"v_max_u32 v226,v226,v191\n"
"v_mov_b32 v229,v226\n"
"s_nop 1\n"
"v_max_u32_dpp v229,v229,v229 row_shr:1 row_mask:0xf bank_mask:0xf\n"
"s_nop 1\n"
"v_max_u32_dpp v229,v229,v229 row_shr:2 row_mask:0xf bank_mask:0xf\n"
"s_nop 1\n"
"v_max_u32_dpp v229,v229,v229 row_shr:4 row_mask:0xf bank_mask:0xf\n"
"s_nop 1\n"
"v_max_u32_dpp v229,v229,v229 row_shr:8 row_mask:0xf bank_mask:0xf\n"
"s_nop 1\n"
"v_max_u32_dpp v229,v229,v229 row_bcast:15 row_mask:0xa bank_mask:0xf\n"
"s_nop 1\n"
"v_max_u32_dpp v229,v229,v229 row_bcast:31 row_mask:0xc bank_mask:0xf\n"
"s_nop 1\n"
"v_readlane_b32 s62,v229,63\n"
"s_nop 1\n"
"v_cmp_eq_u32 vcc,s62,v160\n"
"s_nop 1\n"
"v_cndmask_b32_e64 v160,v160,0,vcc\n"
"v_cmp_eq_u32 vcc,s62,v161\n"
"s_nop 1\n"
"v_cndmask_b32_e64 v161,v161,0,vcc\n"
"v_cmp_eq_u32 vcc,s62,v162\n"
"s_nop 1\n"
"v_cndmask_b32_e64 v162,v162,0,vcc\n"
"v_cmp_eq_u32 vcc,s62,v163\n"
"s_nop 1\n"
"v_cndmask_b32_e64 v163,v163,0,vcc\n"
"v_cmp_eq_u32 vcc,s62,v164\n"
"s_nop 1\n"
"v_cndmask_b32_e64 v164,v164,0,vcc\n"
"v_cmp_eq_u32 vcc,s62,v165\n"
"s_nop 1\n"
"v_cndmask_b32_e64 v165,v165,0,vcc\n"
"v_cmp_eq_u32 vcc,s62,v166\n"
"s_nop 1\n"
"v_cndmask_b32_e64 v166,v166,0,vcc\n"
"v_cmp_eq_u32 vcc,s62,v167\n"
"s_nop 1\n"
"v_cndmask_b32_e64 v167,v167,0,vcc\n"
"v_cmp_eq_u32 vcc,s62,v168\n"
"s_nop 1\n"
"v_cndmask_b32_e64 v168,v168,0,vcc\n"
"v_cmp_eq_u32 vcc,s62,v169\n"
"s_nop 1\n"
"v_cndmask_b32_e64 v169,v169,0,vcc\n"
"v_cmp_eq_u32 vcc,s62,v170\n"
"s_nop 1\n"
"v_cndmask_b32_e64 v170,v170,0,vcc\n"
"v_cmp_eq_u32 vcc,s62,v171\n"
"s_nop 1\n"
"v_cndmask_b32_e64 v171,v171,0,vcc\n"
"v_cmp_eq_u32 vcc,s62,v172\n"
"s_nop 1\n"
"v_cndmask_b32_e64 v172,v172,0,vcc\n"
"v_cmp_eq_u32 vcc,s62,v173\n"
"s_nop 1\n"
"v_cndmask_b32_e64 v173,v173,0,vcc\n"
"v_cmp_eq_u32 vcc,s62,v174\n"
"s_nop 1\n"
"v_cndmask_b32_e64 v174,v174,0,vcc\n"
"v_cmp_eq_u32 vcc,s62,v175\n"
"s_nop 1\n"
"v_cndmask_b32_e64 v175,v175,0,vcc\n"
"v_cmp_eq_u32 vcc,s62,v176\n"
"s_nop 1\n"
"v_cndmask_b32_e64 v176,v176,0,vcc\n"
"v_cmp_eq_u32 vcc,s62,v177\n"
"s_nop 1\n"
"v_cndmask_b32_e64 v177,v177,0,vcc\n"
"v_cmp_eq_u32 vcc,s62,v178\n"
"s_nop 1\n"
"v_cndmask_b32_e64 v178,v178,0,vcc\n"
"v_cmp_eq_u32 vcc,s62,v179\n"
"s_nop 1\n"
"v_cndmask_b32_e64 v179,v179,0,vcc\n"
"v_cmp_eq_u32 vcc,s62,v180\n"
"s_nop 1\n"
"v_cndmask_b32_e64 v180,v180,0,vcc\n"
"v_cmp_eq_u32 vcc,s62,v181\n"
"s_nop 1\n"
"v_cndmask_b32_e64 v181,v181,0,vcc\n"
"v_cmp_eq_u32 vcc,s62,v182\n"
"s_nop 1\n"
"v_cndmask_b32_e64 v182,v182,0,vcc\n"
"v_cmp_eq_u32 vcc,s62,v183\n"
"s_nop 1\n"
"v_cndmask_b32_e64 v183,v183,0,vcc\n"
"v_cmp_eq_u32 vcc,s62,v184\n"
"s_nop 1\n"
"v_cndmask_b32_e64 v184,v184,0,vcc\n"
"v_cmp_eq_u32 vcc,s62,v185\n"
"s_nop 1\n"
"v_cndmask_b32_e64 v185,v185,0,vcc\n"
"v_cmp_eq_u32 vcc,s62,v186\n"
"s_nop 1\n"
"v_cndmask_b32_e64 v186,v186,0,vcc\n"
"v_cmp_eq_u32 vcc,s62,v187\n"
"s_nop 1\n"
"v_cndmask_b32_e64 v187,v187,0,vcc\n"
"v_cmp_eq_u32 vcc,s62,v188\n"
"s_nop 1\n"
"v_cndmask_b32_e64 v188,v188,0,vcc\n"
"v_cmp_eq_u32 vcc,s62,v189\n"
"s_nop 1\n"
"v_cndmask_b32_e64 v189,v189,0,vcc\n"
"v_cmp_eq_u32 vcc,s62,v190\n"
"s_nop 1\n"
"v_cndmask_b32_e64 v190,v190,0,vcc\n"
"v_cmp_eq_u32 vcc,s62,v191\n"
"s_nop 1\n"
"v_cndmask_b32_e64 v191,v191,0,vcc\n"
"v_mov_b32 v229,s62\n"
"s_lshl_b64 exec,1,s54\n"
"v_mov_b32 v225,v229\n"
"s_mov_b64 exec,-1\n"
SA"s54,s54,1\n"
"s_cmp_lt_u32 s54,8\n"
"s_cbranch_scc1 L1009_%=\n"
"s_nop 4\n"
"s_branch L1008_%=\n"
"L1010_%=:\n"
"s_mov_b32 s54,0\n"
"L1012_%=:\n"
"s_sleep 2\n"
"buffer_load_dwordx2 v[208:209],v252,s[8:11],s50 offen sc1\n"
WT"vmcnt(0)\n"
SA"s54,s54,1\n"
"v_cmp_eq_u64 vcc,s[28:29],v[208:209]\n"
"s_cmp_eq_u64 vcc,exec\n"
"s_cbranch_scc1 L1011_%=\n"
"s_cmp_lt_u32 s54,1048576\n"
"s_cbranch_scc1 L1012_%=\n"
"s_branch L1011_%=\n"
"L1014_%=:\n"
"s_nop 15\n"
WT"vmcnt(0)\n"
"s_lshl_b32 s89,s34,3\n"
SA"s89,s89,s30\n"
SA"s89,s89,0\n"
"s_lshl_b32 s91,s89,4\n"
"s_lshr_b32 s89,s89,1\n"
"v_bfe_u32 v249,v224,4,4\n"
"v_lshrrev_b32 v254,8,v224\n"
"v_lshlrev_b32 v254,2,v254\n"
"v_sub_u32 v249,v249,v254\n"
"v_add_u32 v249,s91,v249\n"
"v_mov_b32 v255,0xff800000\n"
"v_mov_b32 v248,v255\n"
"s_mov_b32 s90,0\n"
"s_mov_b32 s88,0\n"
"v_mov_b32 v80,0\n"
"v_mov_b32 v81,0\n"
"v_mov_b32 v82,0\n"
"v_mov_b32 v83,0\n"
"v_mov_b32 v84,0\n"
"v_mov_b32 v85,0\n"
"v_mov_b32 v86,0\n"
"v_mov_b32 v87,0\n"
"v_mov_b32 v88,0\n"
"v_mov_b32 v89,0\n"
"v_mov_b32 v90,0\n"
"v_mov_b32 v91,0\n"
"v_mov_b32 v92,0\n"
"v_mov_b32 v93,0\n"
"v_mov_b32 v94,0\n"
"v_mov_b32 v95,0\n"
"v_mov_b32 v96,0\n"
"v_mov_b32 v97,0\n"
"v_mov_b32 v98,0\n"
"v_mov_b32 v99,0\n"
"v_mov_b32 v100,0\n"
"v_mov_b32 v101,0\n"
"v_mov_b32 v102,0\n"
"v_mov_b32 v103,0\n"
"v_mov_b32 v104,0\n"
"v_mov_b32 v105,0\n"
"v_mov_b32 v106,0\n"
"v_mov_b32 v107,0\n"
"v_mov_b32 v108,0\n"
"v_mov_b32 v109,0\n"
"v_mov_b32 v110,0\n"
"v_mov_b32 v111,0\n"
"v_accvgpr_write_b32 a132,0\n"
"v_accvgpr_write_b32 a133,0\n"
"v_accvgpr_write_b32 a134,0\n"
"v_accvgpr_write_b32 a135,0\n"
"L1015_%=:\n"
"s_lshl_b32 s91,s88,13\n"
SA"s91,s91,s39\n"
SA"s91,s91,0\n"
BL"a[148:151],v224,s[8:11],s91 offen offset:0 sc1\n"
BL"a[152:155],v224,s[8:11],s91 offen offset:1024 sc1\n"
BL"a[156:159],v224,s[8:11],s91 offen offset:2048 sc1\n"
BL"a[160:163],v224,s[8:11],s91 offen offset:3072 sc1\n"
SA"s91,s91,4096\n"
BL"a[164:167],v224,s[8:11],s91 offen offset:0 sc1\n"
BL"a[168:171],v224,s[8:11],s91 offen offset:1024 sc1\n"
BL"a[172:175],v224,s[8:11],s91 offen offset:2048 sc1\n"
BL"a[176:179],v224,s[8:11],s91 offen offset:3072 sc1\n"
WT"vmcnt(0)\n"
MF"v[240:243],a[148:151],a[0:3],0\n"
MF"v[240:243],a[152:155],a[4:7],v[240:243]\n"
MF"v[240:243],a[156:159],a[8:11],v[240:243]\n"
MF"v[240:243],a[160:163],a[12:15],v[240:243]\n"
MF"v[244:247],a[164:167],a[0:3],0\n"
MF"v[244:247],a[168:171],a[4:7],v[244:247]\n"
MF"v[244:247],a[172:175],a[8:11],v[244:247]\n"
MF"v[244:247],a[176:179],a[12:15],v[244:247]\n"
"s_lshl_b32 s91,s88,5\n"
"v_subrev_u32 v250,s91,v249\n"
"s_nop 7\n"
"v_cmp_gt_i32 vcc,0,v250\n"
"s_nop 1\n"
"v_cndmask_b32 v240,v240,v255,vcc\n"
"v_cmp_gt_i32 vcc,1,v250\n"
"s_nop 1\n"
"v_cndmask_b32 v241,v241,v255,vcc\n"
"v_cmp_gt_i32 vcc,2,v250\n"
"s_nop 1\n"
"v_cndmask_b32 v242,v242,v255,vcc\n"
"v_cmp_gt_i32 vcc,3,v250\n"
"s_nop 1\n"
"v_cndmask_b32 v243,v243,v255,vcc\n"
"v_cmp_gt_i32 vcc,16,v250\n"
"s_nop 1\n"
"v_cndmask_b32 v244,v244,v255,vcc\n"
"v_cmp_gt_i32 vcc,17,v250\n"
"s_nop 1\n"
"v_cndmask_b32 v245,v245,v255,vcc\n"
"v_cmp_gt_i32 vcc,18,v250\n"
"s_nop 1\n"
"v_cndmask_b32 v246,v246,v255,vcc\n"
"v_cmp_gt_i32 vcc,19,v250\n"
"s_nop 1\n"
"v_cndmask_b32 v247,v247,v255,vcc\n"
"s_cmp_lg_u32 s90,0\n"
"s_cbranch_scc1 L1016_%=\n"
"v_max3_f32 v248,v248,v240,v241\n"
"v_max3_f32 v248,v248,v242,v243\n"
"v_max3_f32 v248,v248,v244,v245\n"
"v_max3_f32 v248,v248,v246,v247\n"
"s_branch L1017_%=\n"
"L1016_%=:\n"
"s_lshl_b32 s91,s88,13\n"
SA"s91,s91,s39\n"
SA"s91,s91,524288\n"
BL"a[148:151],v224,s[8:11],s91 offen offset:0 sc1\n"
BL"a[152:155],v224,s[8:11],s91 offen offset:1024 sc1\n"
BL"a[156:159],v224,s[8:11],s91 offen offset:2048 sc1\n"
BL"a[160:163],v224,s[8:11],s91 offen offset:3072 sc1\n"
SA"s91,s91,4096\n"
BL"a[164:167],v224,s[8:11],s91 offen offset:0 sc1\n"
BL"a[168:171],v224,s[8:11],s91 offen offset:1024 sc1\n"
BL"a[172:175],v224,s[8:11],s91 offen offset:2048 sc1\n"
BL"a[176:179],v224,s[8:11],s91 offen offset:3072 sc1\n"
WT"vmcnt(0)\n"
"v_sub_f32 v240,v240,v248\n"
"v_sub_f32 v241,v241,v248\n"
"v_sub_f32 v242,v242,v248\n"
"v_sub_f32 v243,v243,v248\n"
"v_sub_f32 v244,v244,v248\n"
"v_sub_f32 v245,v245,v248\n"
"v_sub_f32 v246,v246,v248\n"
"v_sub_f32 v247,v247,v248\n"
EX"v240,v240\n"
EX"v241,v241\n"
EX"v242,v242\n"
EX"v243,v243\n"
EX"v244,v244\n"
EX"v245,v245\n"
EX"v246,v246\n"
EX"v247,v247\n"
"s_nop 0\n"
PK"v240,v240,v241\n"
PK"v241,v242,v243\n"
PK"v242,v244,v245\n"
PK"v243,v246,v247\n"
"s_nop 1\n"
MF"v[80:83],a[148:151],v[240:243],v[80:83]\n"
MF"v[84:87],a[152:155],v[240:243],v[84:87]\n"
MF"v[88:91],a[156:159],v[240:243],v[88:91]\n"
MF"v[92:95],a[160:163],v[240:243],v[92:95]\n"
MF"v[96:99],a[164:167],v[240:243],v[96:99]\n"
MF"v[100:103],a[168:171],v[240:243],v[100:103]\n"
MF"v[104:107],a[172:175],v[240:243],v[104:107]\n"
MF"v[108:111],a[176:179],v[240:243],v[108:111]\n"
MF"a[132:135],a[128:131],v[240:243],a[132:135]\n"
"s_nop 7\n"
"L1017_%=:\n"
SA"s88,s88,1\n"
"s_cmp_le_u32 s88,s89\n"
"s_cbranch_scc1 L1015_%=\n"
"s_cmp_lg_u32 s90,0\n"
"s_cbranch_scc1 L1018_%=\n"
"v_mov_b32 v254,v248\n"
"s_nop 1\n"
"v_permlane16_swap_b32 v254,v248\n"
"s_nop 1\n"
"v_max_f32 v248,v248,v254\n"
"v_mov_b32 v254,v248\n"
"s_nop 1\n"
"v_permlane32_swap_b32 v254,v248\n"
"s_nop 1\n"
"v_max_f32 v248,v248,v254\n"
"s_mov_b32 s90,1\n"
"s_mov_b32 s88,0\n"
"s_branch L1015_%=\n"
"L1018_%=:\n"
"s_nop 7\n"
"v_accvgpr_read_b32 v251,a132\n"
"s_nop 1\n"
"s_branch L1013_%=\n"
"L1019_%=:\n"
"s_mov_b32 s54,0\n"
"L1021_%=:\n"
"s_sleep 2\n"
"buffer_load_dwordx2 v[238:239],v237,s[8:11],s50 offen sc1\n"
WT"vmcnt(0)\n"
SA"s54,s54,1\n"
"v_cmp_eq_u64 vcc,s[28:29],v[238:239]\n"
"s_cmp_eq_u64 vcc,exec\n"
"s_cbranch_scc1 L1020_%=\n"
"s_cmp_lt_u32 s54,1048576\n"
"s_cbranch_scc1 L1021_%=\n"
"s_branch L1020_%=\n"
"L1023_%=:\n"
"s_nop 15\n"
WT"vmcnt(0)\n"
"s_lshl_b32 s89,s34,3\n"
SA"s89,s89,s30\n"
SA"s89,s89,4\n"
"s_lshl_b32 s91,s89,4\n"
"s_lshr_b32 s89,s89,1\n"
"v_bfe_u32 v249,v224,4,4\n"
"v_lshrrev_b32 v254,8,v224\n"
"v_lshlrev_b32 v254,2,v254\n"
"v_sub_u32 v249,v249,v254\n"
"v_add_u32 v249,s91,v249\n"
"v_mov_b32 v255,0xff800000\n"
"v_mov_b32 v248,v255\n"
"s_mov_b32 s90,0\n"
"s_mov_b32 s88,0\n"
"v_mov_b32 v112,0\n"
"v_mov_b32 v113,0\n"
"v_mov_b32 v114,0\n"
"v_mov_b32 v115,0\n"
"v_mov_b32 v116,0\n"
"v_mov_b32 v117,0\n"
"v_mov_b32 v118,0\n"
"v_mov_b32 v119,0\n"
"v_mov_b32 v120,0\n"
"v_mov_b32 v121,0\n"
"v_mov_b32 v122,0\n"
"v_mov_b32 v123,0\n"
"v_mov_b32 v124,0\n"
"v_mov_b32 v125,0\n"
"v_mov_b32 v126,0\n"
"v_mov_b32 v127,0\n"
"v_mov_b32 v128,0\n"
"v_mov_b32 v129,0\n"
"v_mov_b32 v130,0\n"
"v_mov_b32 v131,0\n"
"v_mov_b32 v132,0\n"
"v_mov_b32 v133,0\n"
"v_mov_b32 v134,0\n"
"v_mov_b32 v135,0\n"
"v_mov_b32 v136,0\n"
"v_mov_b32 v137,0\n"
"v_mov_b32 v138,0\n"
"v_mov_b32 v139,0\n"
"v_mov_b32 v140,0\n"
"v_mov_b32 v141,0\n"
"v_mov_b32 v142,0\n"
"v_mov_b32 v143,0\n"
"v_accvgpr_write_b32 a136,0\n"
"v_accvgpr_write_b32 a137,0\n"
"v_accvgpr_write_b32 a138,0\n"
"v_accvgpr_write_b32 a139,0\n"
"L1024_%=:\n"
"s_lshl_b32 s91,s88,13\n"
SA"s91,s91,s39\n"
SA"s91,s91,0\n"
BL"a[148:151],v224,s[8:11],s91 offen offset:0 sc1\n"
BL"a[152:155],v224,s[8:11],s91 offen offset:1024 sc1\n"
BL"a[156:159],v224,s[8:11],s91 offen offset:2048 sc1\n"
BL"a[160:163],v224,s[8:11],s91 offen offset:3072 sc1\n"
SA"s91,s91,4096\n"
BL"a[164:167],v224,s[8:11],s91 offen offset:0 sc1\n"
BL"a[168:171],v224,s[8:11],s91 offen offset:1024 sc1\n"
BL"a[172:175],v224,s[8:11],s91 offen offset:2048 sc1\n"
BL"a[176:179],v224,s[8:11],s91 offen offset:3072 sc1\n"
WT"vmcnt(0)\n"
MF"v[240:243],a[148:151],a[16:19],0\n"
MF"v[240:243],a[152:155],a[20:23],v[240:243]\n"
MF"v[240:243],a[156:159],a[24:27],v[240:243]\n"
MF"v[240:243],a[160:163],a[28:31],v[240:243]\n"
MF"v[244:247],a[164:167],a[16:19],0\n"
MF"v[244:247],a[168:171],a[20:23],v[244:247]\n"
MF"v[244:247],a[172:175],a[24:27],v[244:247]\n"
MF"v[244:247],a[176:179],a[28:31],v[244:247]\n"
"s_lshl_b32 s91,s88,5\n"
"v_subrev_u32 v250,s91,v249\n"
"s_nop 7\n"
"v_cmp_gt_i32 vcc,0,v250\n"
"s_nop 1\n"
"v_cndmask_b32 v240,v240,v255,vcc\n"
"v_cmp_gt_i32 vcc,1,v250\n"
"s_nop 1\n"
"v_cndmask_b32 v241,v241,v255,vcc\n"
"v_cmp_gt_i32 vcc,2,v250\n"
"s_nop 1\n"
"v_cndmask_b32 v242,v242,v255,vcc\n"
"v_cmp_gt_i32 vcc,3,v250\n"
"s_nop 1\n"
"v_cndmask_b32 v243,v243,v255,vcc\n"
"v_cmp_gt_i32 vcc,16,v250\n"
"s_nop 1\n"
"v_cndmask_b32 v244,v244,v255,vcc\n"
"v_cmp_gt_i32 vcc,17,v250\n"
"s_nop 1\n"
"v_cndmask_b32 v245,v245,v255,vcc\n"
"v_cmp_gt_i32 vcc,18,v250\n"
"s_nop 1\n"
"v_cndmask_b32 v246,v246,v255,vcc\n"
"v_cmp_gt_i32 vcc,19,v250\n"
"s_nop 1\n"
"v_cndmask_b32 v247,v247,v255,vcc\n"
"s_cmp_lg_u32 s90,0\n"
"s_cbranch_scc1 L1025_%=\n"
"v_max3_f32 v248,v248,v240,v241\n"
"v_max3_f32 v248,v248,v242,v243\n"
"v_max3_f32 v248,v248,v244,v245\n"
"v_max3_f32 v248,v248,v246,v247\n"
"s_branch L1026_%=\n"
"L1025_%=:\n"
"s_lshl_b32 s91,s88,13\n"
SA"s91,s91,s39\n"
SA"s91,s91,524288\n"
BL"a[148:151],v224,s[8:11],s91 offen offset:0 sc1\n"
BL"a[152:155],v224,s[8:11],s91 offen offset:1024 sc1\n"
BL"a[156:159],v224,s[8:11],s91 offen offset:2048 sc1\n"
BL"a[160:163],v224,s[8:11],s91 offen offset:3072 sc1\n"
SA"s91,s91,4096\n"
BL"a[164:167],v224,s[8:11],s91 offen offset:0 sc1\n"
BL"a[168:171],v224,s[8:11],s91 offen offset:1024 sc1\n"
BL"a[172:175],v224,s[8:11],s91 offen offset:2048 sc1\n"
BL"a[176:179],v224,s[8:11],s91 offen offset:3072 sc1\n"
WT"vmcnt(0)\n"
"v_sub_f32 v240,v240,v248\n"
"v_sub_f32 v241,v241,v248\n"
"v_sub_f32 v242,v242,v248\n"
"v_sub_f32 v243,v243,v248\n"
"v_sub_f32 v244,v244,v248\n"
"v_sub_f32 v245,v245,v248\n"
"v_sub_f32 v246,v246,v248\n"
"v_sub_f32 v247,v247,v248\n"
EX"v240,v240\n"
EX"v241,v241\n"
EX"v242,v242\n"
EX"v243,v243\n"
EX"v244,v244\n"
EX"v245,v245\n"
EX"v246,v246\n"
EX"v247,v247\n"
"s_nop 0\n"
PK"v240,v240,v241\n"
PK"v241,v242,v243\n"
PK"v242,v244,v245\n"
PK"v243,v246,v247\n"
"s_nop 1\n"
MF"v[112:115],a[148:151],v[240:243],v[112:115]\n"
MF"v[116:119],a[152:155],v[240:243],v[116:119]\n"
MF"v[120:123],a[156:159],v[240:243],v[120:123]\n"
MF"v[124:127],a[160:163],v[240:243],v[124:127]\n"
MF"v[128:131],a[164:167],v[240:243],v[128:131]\n"
MF"v[132:135],a[168:171],v[240:243],v[132:135]\n"
MF"v[136:139],a[172:175],v[240:243],v[136:139]\n"
MF"v[140:143],a[176:179],v[240:243],v[140:143]\n"
MF"a[136:139],a[128:131],v[240:243],a[136:139]\n"
"s_nop 7\n"
"L1026_%=:\n"
SA"s88,s88,1\n"
"s_cmp_le_u32 s88,s89\n"
"s_cbranch_scc1 L1024_%=\n"
"s_cmp_lg_u32 s90,0\n"
"s_cbranch_scc1 L1027_%=\n"
"v_mov_b32 v254,v248\n"
"s_nop 1\n"
"v_permlane16_swap_b32 v254,v248\n"
"s_nop 1\n"
"v_max_f32 v248,v248,v254\n"
"v_mov_b32 v254,v248\n"
"s_nop 1\n"
"v_permlane32_swap_b32 v254,v248\n"
"s_nop 1\n"
"v_max_f32 v248,v248,v254\n"
"s_mov_b32 s90,1\n"
"s_mov_b32 s88,0\n"
"s_branch L1024_%=\n"
"L1027_%=:\n"
"s_nop 7\n"
"v_accvgpr_read_b32 v252,a136\n"
"s_nop 1\n"
"s_branch L1022_%=\n"
"L1028_%=:\n"
"s_mov_b32 s54,0\n"
"L1030_%=:\n"
"s_sleep 2\n"
"buffer_load_dwordx2 v[242:243],v236,s[8:11],0 offen sc1\n"
WT"vmcnt(0)\n"
SA"s54,s54,1\n"
"v_cmp_eq_u64 vcc,s[28:29],v[242:243]\n"
"s_or_b32 vcc_hi,vcc_hi,-1\n"
"s_cmp_eq_u64 vcc,exec\n"
"s_cbranch_scc1 L1029_%=\n"
"s_cmp_lt_u32 s54,1048576\n"
"s_cbranch_scc1 L1030_%=\n"
"s_branch L1029_%=\n"
"L1031_%=:\n"
"s_mov_b32 s54,0\n"
"L1033_%=:\n"
"s_sleep 2\n"
"buffer_load_dwordx2 v[242:243],v236,s[8:11],0 offen sc1\n"
WT"vmcnt(0)\n"
SA"s54,s54,1\n"
"v_cmp_eq_u64 vcc,s[28:29],v[242:243]\n"
"s_or_b32 vcc_lo,vcc_lo,-1\n"
"s_cmp_eq_u64 vcc,exec\n"
"s_cbranch_scc1 L1032_%=\n"
"s_cmp_lt_u32 s54,1048576\n"
"s_cbranch_scc1 L1033_%=\n"
"s_branch L1032_%=\n"
"L1035_%=:\n"
"s_nop 15\n"
WT"vmcnt(0)\n"
"s_lshl_b32 s89,s34,3\n"
SA"s89,s89,s30\n"
SA"s89,s89,4\n"
"s_lshl_b32 s91,s89,4\n"
"s_lshr_b32 s89,s89,1\n"
"v_bfe_u32 v249,v224,4,4\n"
"v_lshrrev_b32 v254,8,v224\n"
"v_lshlrev_b32 v254,2,v254\n"
"v_sub_u32 v249,v249,v254\n"
"v_add_u32 v249,s91,v249\n"
"v_mov_b32 v255,0xff800000\n"
"v_mov_b32 v248,v255\n"
"s_mov_b32 s90,0\n"
"s_mov_b32 s88,0\n"
"v_mov_b32 v112,0\n"
"v_mov_b32 v113,0\n"
"v_mov_b32 v114,0\n"
"v_mov_b32 v115,0\n"
"v_mov_b32 v116,0\n"
"v_mov_b32 v117,0\n"
"v_mov_b32 v118,0\n"
"v_mov_b32 v119,0\n"
"v_mov_b32 v120,0\n"
"v_mov_b32 v121,0\n"
"v_mov_b32 v122,0\n"
"v_mov_b32 v123,0\n"
"v_mov_b32 v124,0\n"
"v_mov_b32 v125,0\n"
"v_mov_b32 v126,0\n"
"v_mov_b32 v127,0\n"
"v_mov_b32 v128,0\n"
"v_mov_b32 v129,0\n"
"v_mov_b32 v130,0\n"
"v_mov_b32 v131,0\n"
"v_mov_b32 v132,0\n"
"v_mov_b32 v133,0\n"
"v_mov_b32 v134,0\n"
"v_mov_b32 v135,0\n"
"v_mov_b32 v136,0\n"
"v_mov_b32 v137,0\n"
"v_mov_b32 v138,0\n"
"v_mov_b32 v139,0\n"
"v_mov_b32 v140,0\n"
"v_mov_b32 v141,0\n"
"v_mov_b32 v142,0\n"
"v_mov_b32 v143,0\n"
"v_accvgpr_write_b32 a136,0\n"
"v_accvgpr_write_b32 a137,0\n"
"v_accvgpr_write_b32 a138,0\n"
"v_accvgpr_write_b32 a139,0\n"
"L1036_%=:\n"
"s_lshl_b32 s91,s88,13\n"
SA"s91,s91,s39\n"
SA"s91,s91,0\n"
BL"a[148:151],v224,s[8:11],s91 offen offset:0 sc1\n"
BL"a[152:155],v224,s[8:11],s91 offen offset:1024 sc1\n"
BL"a[156:159],v224,s[8:11],s91 offen offset:2048 sc1\n"
BL"a[160:163],v224,s[8:11],s91 offen offset:3072 sc1\n"
SA"s91,s91,4096\n"
BL"a[164:167],v224,s[8:11],s91 offen offset:0 sc1\n"
BL"a[168:171],v224,s[8:11],s91 offen offset:1024 sc1\n"
BL"a[172:175],v224,s[8:11],s91 offen offset:2048 sc1\n"
BL"a[176:179],v224,s[8:11],s91 offen offset:3072 sc1\n"
WT"vmcnt(0)\n"
MF"v[240:243],a[148:151],a[16:19],0\n"
MF"v[240:243],a[152:155],a[20:23],v[240:243]\n"
MF"v[240:243],a[156:159],a[24:27],v[240:243]\n"
MF"v[240:243],a[160:163],a[28:31],v[240:243]\n"
MF"v[244:247],a[164:167],a[16:19],0\n"
MF"v[244:247],a[168:171],a[20:23],v[244:247]\n"
MF"v[244:247],a[172:175],a[24:27],v[244:247]\n"
MF"v[244:247],a[176:179],a[28:31],v[244:247]\n"
"s_lshl_b32 s91,s88,5\n"
"v_subrev_u32 v250,s91,v249\n"
"s_nop 7\n"
"v_cmp_gt_i32 vcc,0,v250\n"
"s_nop 1\n"
"v_cndmask_b32 v240,v240,v255,vcc\n"
"v_cmp_gt_i32 vcc,1,v250\n"
"s_nop 1\n"
"v_cndmask_b32 v241,v241,v255,vcc\n"
"v_cmp_gt_i32 vcc,2,v250\n"
"s_nop 1\n"
"v_cndmask_b32 v242,v242,v255,vcc\n"
"v_cmp_gt_i32 vcc,3,v250\n"
"s_nop 1\n"
"v_cndmask_b32 v243,v243,v255,vcc\n"
"v_cmp_gt_i32 vcc,16,v250\n"
"s_nop 1\n"
"v_cndmask_b32 v244,v244,v255,vcc\n"
"v_cmp_gt_i32 vcc,17,v250\n"
"s_nop 1\n"
"v_cndmask_b32 v245,v245,v255,vcc\n"
"v_cmp_gt_i32 vcc,18,v250\n"
"s_nop 1\n"
"v_cndmask_b32 v246,v246,v255,vcc\n"
"v_cmp_gt_i32 vcc,19,v250\n"
"s_nop 1\n"
"v_cndmask_b32 v247,v247,v255,vcc\n"
"s_cmp_lg_u32 s90,0\n"
"s_cbranch_scc1 L1037_%=\n"
"v_max3_f32 v248,v248,v240,v241\n"
"v_max3_f32 v248,v248,v242,v243\n"
"v_max3_f32 v248,v248,v244,v245\n"
"v_max3_f32 v248,v248,v246,v247\n"
"s_branch L1038_%=\n"
"L1037_%=:\n"
"s_lshl_b32 s91,s88,13\n"
SA"s91,s91,s39\n"
SA"s91,s91,524288\n"
BL"a[148:151],v224,s[8:11],s91 offen offset:0 sc1\n"
BL"a[152:155],v224,s[8:11],s91 offen offset:1024 sc1\n"
BL"a[156:159],v224,s[8:11],s91 offen offset:2048 sc1\n"
BL"a[160:163],v224,s[8:11],s91 offen offset:3072 sc1\n"
SA"s91,s91,4096\n"
BL"a[164:167],v224,s[8:11],s91 offen offset:0 sc1\n"
BL"a[168:171],v224,s[8:11],s91 offen offset:1024 sc1\n"
BL"a[172:175],v224,s[8:11],s91 offen offset:2048 sc1\n"
BL"a[176:179],v224,s[8:11],s91 offen offset:3072 sc1\n"
WT"vmcnt(0)\n"
"v_sub_f32 v240,v240,v248\n"
"v_sub_f32 v241,v241,v248\n"
"v_sub_f32 v242,v242,v248\n"
"v_sub_f32 v243,v243,v248\n"
"v_sub_f32 v244,v244,v248\n"
"v_sub_f32 v245,v245,v248\n"
"v_sub_f32 v246,v246,v248\n"
"v_sub_f32 v247,v247,v248\n"
EX"v240,v240\n"
EX"v241,v241\n"
EX"v242,v242\n"
EX"v243,v243\n"
EX"v244,v244\n"
EX"v245,v245\n"
EX"v246,v246\n"
EX"v247,v247\n"
"s_nop 0\n"
PK"v240,v240,v241\n"
PK"v241,v242,v243\n"
PK"v242,v244,v245\n"
PK"v243,v246,v247\n"
"s_nop 1\n"
MF"v[112:115],a[148:151],v[240:243],v[112:115]\n"
MF"v[116:119],a[152:155],v[240:243],v[116:119]\n"
MF"v[120:123],a[156:159],v[240:243],v[120:123]\n"
MF"v[124:127],a[160:163],v[240:243],v[124:127]\n"
MF"v[128:131],a[164:167],v[240:243],v[128:131]\n"
MF"v[132:135],a[168:171],v[240:243],v[132:135]\n"
MF"v[136:139],a[172:175],v[240:243],v[136:139]\n"
MF"v[140:143],a[176:179],v[240:243],v[140:143]\n"
MF"a[136:139],a[128:131],v[240:243],a[136:139]\n"
"s_nop 7\n"
"L1038_%=:\n"
SA"s88,s88,1\n"
"s_cmp_le_u32 s88,s89\n"
"s_cbranch_scc1 L1036_%=\n"
"s_cmp_lg_u32 s90,0\n"
"s_cbranch_scc1 L1039_%=\n"
"v_mov_b32 v254,v248\n"
"s_nop 1\n"
"v_permlane16_swap_b32 v254,v248\n"
"s_nop 1\n"
"v_max_f32 v248,v248,v254\n"
"v_mov_b32 v254,v248\n"
"s_nop 1\n"
"v_permlane32_swap_b32 v254,v248\n"
"s_nop 1\n"
"v_max_f32 v248,v248,v254\n"
"s_mov_b32 s90,1\n"
"s_mov_b32 s88,0\n"
"s_branch L1036_%=\n"
"L1039_%=:\n"
"s_nop 7\n"
"v_accvgpr_read_b32 v252,a136\n"
"s_nop 1\n"
"s_branch L1034_%=\n"
"L1041_%=:\n"
"s_nop 15\n"
WT"vmcnt(0)\n"
"s_lshl_b32 s89,s34,3\n"
"s_sub_u32 s89,120,s89\n"
SA"s89,s89,s30\n"
SA"s89,s89,0\n"
"s_lshl_b32 s91,s89,4\n"
"s_lshr_b32 s89,s89,1\n"
"v_bfe_u32 v249,v224,4,4\n"
"v_lshrrev_b32 v254,8,v224\n"
"v_lshlrev_b32 v254,2,v254\n"
"v_sub_u32 v249,v249,v254\n"
"v_add_u32 v249,s91,v249\n"
"v_mov_b32 v255,0xff800000\n"
"v_mov_b32 v248,v255\n"
"s_mov_b32 s90,0\n"
"s_mov_b32 s88,0\n"
"v_mov_b32 v144,0\n"
"v_mov_b32 v145,0\n"
"v_mov_b32 v146,0\n"
"v_mov_b32 v147,0\n"
"v_mov_b32 v148,0\n"
"v_mov_b32 v149,0\n"
"v_mov_b32 v150,0\n"
"v_mov_b32 v151,0\n"
"v_mov_b32 v152,0\n"
"v_mov_b32 v153,0\n"
"v_mov_b32 v154,0\n"
"v_mov_b32 v155,0\n"
"v_mov_b32 v156,0\n"
"v_mov_b32 v157,0\n"
"v_mov_b32 v158,0\n"
"v_mov_b32 v159,0\n"
"v_mov_b32 v160,0\n"
"v_mov_b32 v161,0\n"
"v_mov_b32 v162,0\n"
"v_mov_b32 v163,0\n"
"v_mov_b32 v164,0\n"
"v_mov_b32 v165,0\n"
"v_mov_b32 v166,0\n"
"v_mov_b32 v167,0\n"
"v_mov_b32 v168,0\n"
"v_mov_b32 v169,0\n"
"v_mov_b32 v170,0\n"
"v_mov_b32 v171,0\n"
"v_mov_b32 v172,0\n"
"v_mov_b32 v173,0\n"
"v_mov_b32 v174,0\n"
"v_mov_b32 v175,0\n"
"v_accvgpr_write_b32 a140,0\n"
"v_accvgpr_write_b32 a141,0\n"
"v_accvgpr_write_b32 a142,0\n"
"v_accvgpr_write_b32 a143,0\n"
"L1042_%=:\n"
"s_lshl_b32 s91,s88,13\n"
SA"s91,s91,s39\n"
SA"s91,s91,0\n"
BL"a[148:151],v224,s[8:11],s91 offen offset:0 sc1\n"
BL"a[152:155],v224,s[8:11],s91 offen offset:1024 sc1\n"
BL"a[156:159],v224,s[8:11],s91 offen offset:2048 sc1\n"
BL"a[160:163],v224,s[8:11],s91 offen offset:3072 sc1\n"
SA"s91,s91,4096\n"
BL"a[164:167],v224,s[8:11],s91 offen offset:0 sc1\n"
BL"a[168:171],v224,s[8:11],s91 offen offset:1024 sc1\n"
BL"a[172:175],v224,s[8:11],s91 offen offset:2048 sc1\n"
BL"a[176:179],v224,s[8:11],s91 offen offset:3072 sc1\n"
WT"vmcnt(0)\n"
MF"v[240:243],a[148:151],a[32:35],0\n"
MF"v[240:243],a[152:155],a[36:39],v[240:243]\n"
MF"v[240:243],a[156:159],a[40:43],v[240:243]\n"
MF"v[240:243],a[160:163],a[44:47],v[240:243]\n"
MF"v[244:247],a[164:167],a[32:35],0\n"
MF"v[244:247],a[168:171],a[36:39],v[244:247]\n"
MF"v[244:247],a[172:175],a[40:43],v[244:247]\n"
MF"v[244:247],a[176:179],a[44:47],v[244:247]\n"
"s_lshl_b32 s91,s88,5\n"
"v_subrev_u32 v250,s91,v249\n"
"s_nop 7\n"
"v_cmp_gt_i32 vcc,0,v250\n"
"s_nop 1\n"
"v_cndmask_b32 v240,v240,v255,vcc\n"
"v_cmp_gt_i32 vcc,1,v250\n"
"s_nop 1\n"
"v_cndmask_b32 v241,v241,v255,vcc\n"
"v_cmp_gt_i32 vcc,2,v250\n"
"s_nop 1\n"
"v_cndmask_b32 v242,v242,v255,vcc\n"
"v_cmp_gt_i32 vcc,3,v250\n"
"s_nop 1\n"
"v_cndmask_b32 v243,v243,v255,vcc\n"
"v_cmp_gt_i32 vcc,16,v250\n"
"s_nop 1\n"
"v_cndmask_b32 v244,v244,v255,vcc\n"
"v_cmp_gt_i32 vcc,17,v250\n"
"s_nop 1\n"
"v_cndmask_b32 v245,v245,v255,vcc\n"
"v_cmp_gt_i32 vcc,18,v250\n"
"s_nop 1\n"
"v_cndmask_b32 v246,v246,v255,vcc\n"
"v_cmp_gt_i32 vcc,19,v250\n"
"s_nop 1\n"
"v_cndmask_b32 v247,v247,v255,vcc\n"
"s_cmp_lg_u32 s90,0\n"
"s_cbranch_scc1 L1043_%=\n"
"v_max3_f32 v248,v248,v240,v241\n"
"v_max3_f32 v248,v248,v242,v243\n"
"v_max3_f32 v248,v248,v244,v245\n"
"v_max3_f32 v248,v248,v246,v247\n"
"s_branch L1044_%=\n"
"L1043_%=:\n"
"s_lshl_b32 s91,s88,13\n"
SA"s91,s91,s39\n"
SA"s91,s91,524288\n"
BL"a[148:151],v224,s[8:11],s91 offen offset:0 sc1\n"
BL"a[152:155],v224,s[8:11],s91 offen offset:1024 sc1\n"
BL"a[156:159],v224,s[8:11],s91 offen offset:2048 sc1\n"
BL"a[160:163],v224,s[8:11],s91 offen offset:3072 sc1\n"
SA"s91,s91,4096\n"
BL"a[164:167],v224,s[8:11],s91 offen offset:0 sc1\n"
BL"a[168:171],v224,s[8:11],s91 offen offset:1024 sc1\n"
BL"a[172:175],v224,s[8:11],s91 offen offset:2048 sc1\n"
BL"a[176:179],v224,s[8:11],s91 offen offset:3072 sc1\n"
WT"vmcnt(0)\n"
"v_sub_f32 v240,v240,v248\n"
"v_sub_f32 v241,v241,v248\n"
"v_sub_f32 v242,v242,v248\n"
"v_sub_f32 v243,v243,v248\n"
"v_sub_f32 v244,v244,v248\n"
"v_sub_f32 v245,v245,v248\n"
"v_sub_f32 v246,v246,v248\n"
"v_sub_f32 v247,v247,v248\n"
EX"v240,v240\n"
EX"v241,v241\n"
EX"v242,v242\n"
EX"v243,v243\n"
EX"v244,v244\n"
EX"v245,v245\n"
EX"v246,v246\n"
EX"v247,v247\n"
"s_nop 0\n"
PK"v240,v240,v241\n"
PK"v241,v242,v243\n"
PK"v242,v244,v245\n"
PK"v243,v246,v247\n"
"s_nop 1\n"
MF"v[144:147],a[148:151],v[240:243],v[144:147]\n"
MF"v[148:151],a[152:155],v[240:243],v[148:151]\n"
MF"v[152:155],a[156:159],v[240:243],v[152:155]\n"
MF"v[156:159],a[160:163],v[240:243],v[156:159]\n"
MF"v[160:163],a[164:167],v[240:243],v[160:163]\n"
MF"v[164:167],a[168:171],v[240:243],v[164:167]\n"
MF"v[168:171],a[172:175],v[240:243],v[168:171]\n"
MF"v[172:175],a[176:179],v[240:243],v[172:175]\n"
MF"a[140:143],a[128:131],v[240:243],a[140:143]\n"
"s_nop 7\n"
"L1044_%=:\n"
SA"s88,s88,1\n"
"s_cmp_le_u32 s88,s89\n"
"s_cbranch_scc1 L1042_%=\n"
"s_cmp_lg_u32 s90,0\n"
"s_cbranch_scc1 L1045_%=\n"
"v_mov_b32 v254,v248\n"
"s_nop 1\n"
"v_permlane16_swap_b32 v254,v248\n"
"s_nop 1\n"
"v_max_f32 v248,v248,v254\n"
"v_mov_b32 v254,v248\n"
"s_nop 1\n"
"v_permlane32_swap_b32 v254,v248\n"
"s_nop 1\n"
"v_max_f32 v248,v248,v254\n"
"s_mov_b32 s90,1\n"
"s_mov_b32 s88,0\n"
"s_branch L1042_%=\n"
"L1045_%=:\n"
"s_nop 7\n"
"v_accvgpr_read_b32 v251,a140\n"
"s_nop 1\n"
"s_branch L1040_%=\n"
"L1047_%=:\n"
"s_nop 15\n"
WT"vmcnt(0)\n"
"s_lshl_b32 s89,s34,3\n"
"s_sub_u32 s89,120,s89\n"
SA"s89,s89,s30\n"
SA"s89,s89,4\n"
"s_lshl_b32 s91,s89,4\n"
"s_lshr_b32 s89,s89,1\n"
"v_bfe_u32 v249,v224,4,4\n"
"v_lshrrev_b32 v254,8,v224\n"
"v_lshlrev_b32 v254,2,v254\n"
"v_sub_u32 v249,v249,v254\n"
"v_add_u32 v249,s91,v249\n"
"v_mov_b32 v255,0xff800000\n"
"v_mov_b32 v248,v255\n"
"s_mov_b32 s90,0\n"
"s_mov_b32 s88,0\n"
"v_mov_b32 v176,0\n"
"v_mov_b32 v177,0\n"
"v_mov_b32 v178,0\n"
"v_mov_b32 v179,0\n"
"v_mov_b32 v180,0\n"
"v_mov_b32 v181,0\n"
"v_mov_b32 v182,0\n"
"v_mov_b32 v183,0\n"
"v_mov_b32 v184,0\n"
"v_mov_b32 v185,0\n"
"v_mov_b32 v186,0\n"
"v_mov_b32 v187,0\n"
"v_mov_b32 v188,0\n"
"v_mov_b32 v189,0\n"
"v_mov_b32 v190,0\n"
"v_mov_b32 v191,0\n"
"v_mov_b32 v192,0\n"
"v_mov_b32 v193,0\n"
"v_mov_b32 v194,0\n"
"v_mov_b32 v195,0\n"
"v_mov_b32 v196,0\n"
"v_mov_b32 v197,0\n"
"v_mov_b32 v198,0\n"
"v_mov_b32 v199,0\n"
"v_mov_b32 v200,0\n"
"v_mov_b32 v201,0\n"
"v_mov_b32 v202,0\n"
"v_mov_b32 v203,0\n"
"v_mov_b32 v204,0\n"
"v_mov_b32 v205,0\n"
"v_mov_b32 v206,0\n"
"v_mov_b32 v207,0\n"
"v_accvgpr_write_b32 a144,0\n"
"v_accvgpr_write_b32 a145,0\n"
"v_accvgpr_write_b32 a146,0\n"
"v_accvgpr_write_b32 a147,0\n"
"L1048_%=:\n"
"s_lshl_b32 s91,s88,13\n"
SA"s91,s91,s39\n"
SA"s91,s91,0\n"
BL"a[148:151],v224,s[8:11],s91 offen offset:0 sc1\n"
BL"a[152:155],v224,s[8:11],s91 offen offset:1024 sc1\n"
BL"a[156:159],v224,s[8:11],s91 offen offset:2048 sc1\n"
BL"a[160:163],v224,s[8:11],s91 offen offset:3072 sc1\n"
SA"s91,s91,4096\n"
BL"a[164:167],v224,s[8:11],s91 offen offset:0 sc1\n"
BL"a[168:171],v224,s[8:11],s91 offen offset:1024 sc1\n"
BL"a[172:175],v224,s[8:11],s91 offen offset:2048 sc1\n"
BL"a[176:179],v224,s[8:11],s91 offen offset:3072 sc1\n"
WT"vmcnt(0)\n"
MF"v[240:243],a[148:151],a[48:51],0\n"
MF"v[240:243],a[152:155],a[52:55],v[240:243]\n"
MF"v[240:243],a[156:159],a[56:59],v[240:243]\n"
MF"v[240:243],a[160:163],a[60:63],v[240:243]\n"
MF"v[244:247],a[164:167],a[48:51],0\n"
MF"v[244:247],a[168:171],a[52:55],v[244:247]\n"
MF"v[244:247],a[172:175],a[56:59],v[244:247]\n"
MF"v[244:247],a[176:179],a[60:63],v[244:247]\n"
"s_lshl_b32 s91,s88,5\n"
"v_subrev_u32 v250,s91,v249\n"
"s_nop 7\n"
"v_cmp_gt_i32 vcc,0,v250\n"
"s_nop 1\n"
"v_cndmask_b32 v240,v240,v255,vcc\n"
"v_cmp_gt_i32 vcc,1,v250\n"
"s_nop 1\n"
"v_cndmask_b32 v241,v241,v255,vcc\n"
"v_cmp_gt_i32 vcc,2,v250\n"
"s_nop 1\n"
"v_cndmask_b32 v242,v242,v255,vcc\n"
"v_cmp_gt_i32 vcc,3,v250\n"
"s_nop 1\n"
"v_cndmask_b32 v243,v243,v255,vcc\n"
"v_cmp_gt_i32 vcc,16,v250\n"
"s_nop 1\n"
"v_cndmask_b32 v244,v244,v255,vcc\n"
"v_cmp_gt_i32 vcc,17,v250\n"
"s_nop 1\n"
"v_cndmask_b32 v245,v245,v255,vcc\n"
"v_cmp_gt_i32 vcc,18,v250\n"
"s_nop 1\n"
"v_cndmask_b32 v246,v246,v255,vcc\n"
"v_cmp_gt_i32 vcc,19,v250\n"
"s_nop 1\n"
"v_cndmask_b32 v247,v247,v255,vcc\n"
"s_cmp_lg_u32 s90,0\n"
"s_cbranch_scc1 L1049_%=\n"
"v_max3_f32 v248,v248,v240,v241\n"
"v_max3_f32 v248,v248,v242,v243\n"
"v_max3_f32 v248,v248,v244,v245\n"
"v_max3_f32 v248,v248,v246,v247\n"
"s_branch L1050_%=\n"
"L1049_%=:\n"
"s_lshl_b32 s91,s88,13\n"
SA"s91,s91,s39\n"
SA"s91,s91,524288\n"
BL"a[148:151],v224,s[8:11],s91 offen offset:0 sc1\n"
BL"a[152:155],v224,s[8:11],s91 offen offset:1024 sc1\n"
BL"a[156:159],v224,s[8:11],s91 offen offset:2048 sc1\n"
BL"a[160:163],v224,s[8:11],s91 offen offset:3072 sc1\n"
SA"s91,s91,4096\n"
BL"a[164:167],v224,s[8:11],s91 offen offset:0 sc1\n"
BL"a[168:171],v224,s[8:11],s91 offen offset:1024 sc1\n"
BL"a[172:175],v224,s[8:11],s91 offen offset:2048 sc1\n"
BL"a[176:179],v224,s[8:11],s91 offen offset:3072 sc1\n"
WT"vmcnt(0)\n"
"v_sub_f32 v240,v240,v248\n"
"v_sub_f32 v241,v241,v248\n"
"v_sub_f32 v242,v242,v248\n"
"v_sub_f32 v243,v243,v248\n"
"v_sub_f32 v244,v244,v248\n"
"v_sub_f32 v245,v245,v248\n"
"v_sub_f32 v246,v246,v248\n"
"v_sub_f32 v247,v247,v248\n"
EX"v240,v240\n"
EX"v241,v241\n"
EX"v242,v242\n"
EX"v243,v243\n"
EX"v244,v244\n"
EX"v245,v245\n"
EX"v246,v246\n"
EX"v247,v247\n"
"s_nop 0\n"
PK"v240,v240,v241\n"
PK"v241,v242,v243\n"
PK"v242,v244,v245\n"
PK"v243,v246,v247\n"
"s_nop 1\n"
MF"v[176:179],a[148:151],v[240:243],v[176:179]\n"
MF"v[180:183],a[152:155],v[240:243],v[180:183]\n"
MF"v[184:187],a[156:159],v[240:243],v[184:187]\n"
MF"v[188:191],a[160:163],v[240:243],v[188:191]\n"
MF"v[192:195],a[164:167],v[240:243],v[192:195]\n"
MF"v[196:199],a[168:171],v[240:243],v[196:199]\n"
MF"v[200:203],a[172:175],v[240:243],v[200:203]\n"
MF"v[204:207],a[176:179],v[240:243],v[204:207]\n"
MF"a[144:147],a[128:131],v[240:243],a[144:147]\n"
"s_nop 7\n"
"L1050_%=:\n"
SA"s88,s88,1\n"
"s_cmp_le_u32 s88,s89\n"
"s_cbranch_scc1 L1048_%=\n"
"s_cmp_lg_u32 s90,0\n"
"s_cbranch_scc1 L1051_%=\n"
"v_mov_b32 v254,v248\n"
"s_nop 1\n"
"v_permlane16_swap_b32 v254,v248\n"
"s_nop 1\n"
"v_max_f32 v248,v248,v254\n"
"v_mov_b32 v254,v248\n"
"s_nop 1\n"
"v_permlane32_swap_b32 v254,v248\n"
"s_nop 1\n"
"v_max_f32 v248,v248,v254\n"
"s_mov_b32 s90,1\n"
"s_mov_b32 s88,0\n"
"s_branch L1048_%=\n"
"L1051_%=:\n"
"s_nop 7\n"
"v_accvgpr_read_b32 v252,a144\n"
"s_nop 1\n"
"s_branch L1046_%=\n"
"L1069_%=:\n"
"s_nop 15\n"
WT"vmcnt(0)\n"
"s_lshl_b32 s89,s34,3\n"
"s_sub_u32 s89,120,s89\n"
SA"s89,s89,s30\n"
SA"s89,s89,0\n"
"s_lshl_b32 s91,s89,4\n"
"s_lshr_b32 s89,s89,1\n"
"v_bfe_u32 v249,v224,4,4\n"
"v_lshrrev_b32 v254,8,v224\n"
"v_lshlrev_b32 v254,2,v254\n"
"v_sub_u32 v249,v249,v254\n"
"v_add_u32 v249,s91,v249\n"
"v_mov_b32 v255,0xff800000\n"
"v_mov_b32 v248,v255\n"
"s_mov_b32 s90,0\n"
"s_mov_b32 s88,0\n"
"v_mov_b32 v144,0\n"
"v_mov_b32 v145,0\n"
"v_mov_b32 v146,0\n"
"v_mov_b32 v147,0\n"
"v_mov_b32 v148,0\n"
"v_mov_b32 v149,0\n"
"v_mov_b32 v150,0\n"
"v_mov_b32 v151,0\n"
"v_mov_b32 v152,0\n"
"v_mov_b32 v153,0\n"
"v_mov_b32 v154,0\n"
"v_mov_b32 v155,0\n"
"v_mov_b32 v156,0\n"
"v_mov_b32 v157,0\n"
"v_mov_b32 v158,0\n"
"v_mov_b32 v159,0\n"
"v_mov_b32 v160,0\n"
"v_mov_b32 v161,0\n"
"v_mov_b32 v162,0\n"
"v_mov_b32 v163,0\n"
"v_mov_b32 v164,0\n"
"v_mov_b32 v165,0\n"
"v_mov_b32 v166,0\n"
"v_mov_b32 v167,0\n"
"v_mov_b32 v168,0\n"
"v_mov_b32 v169,0\n"
"v_mov_b32 v170,0\n"
"v_mov_b32 v171,0\n"
"v_mov_b32 v172,0\n"
"v_mov_b32 v173,0\n"
"v_mov_b32 v174,0\n"
"v_mov_b32 v175,0\n"
"v_accvgpr_write_b32 a140,0\n"
"v_accvgpr_write_b32 a141,0\n"
"v_accvgpr_write_b32 a142,0\n"
"v_accvgpr_write_b32 a143,0\n"
"L1070_%=:\n"
"s_lshl_b32 s91,s88,13\n"
SA"s91,s91,s39\n"
SA"s91,s91,0\n"
BL"a[148:151],v224,s[8:11],s91 offen offset:0 sc1\n"
BL"a[152:155],v224,s[8:11],s91 offen offset:1024 sc1\n"
BL"a[156:159],v224,s[8:11],s91 offen offset:2048 sc1\n"
BL"a[160:163],v224,s[8:11],s91 offen offset:3072 sc1\n"
SA"s91,s91,4096\n"
BL"a[164:167],v224,s[8:11],s91 offen offset:0 sc1\n"
BL"a[168:171],v224,s[8:11],s91 offen offset:1024 sc1\n"
BL"a[172:175],v224,s[8:11],s91 offen offset:2048 sc1\n"
BL"a[176:179],v224,s[8:11],s91 offen offset:3072 sc1\n"
WT"vmcnt(0)\n"
MF"v[240:243],a[148:151],a[32:35],0\n"
MF"v[240:243],a[152:155],a[36:39],v[240:243]\n"
MF"v[240:243],a[156:159],a[40:43],v[240:243]\n"
MF"v[240:243],a[160:163],a[44:47],v[240:243]\n"
MF"v[244:247],a[164:167],a[32:35],0\n"
MF"v[244:247],a[168:171],a[36:39],v[244:247]\n"
MF"v[244:247],a[172:175],a[40:43],v[244:247]\n"
MF"v[244:247],a[176:179],a[44:47],v[244:247]\n"
"s_lshl_b32 s91,s88,5\n"
"v_subrev_u32 v250,s91,v249\n"
"s_nop 7\n"
"v_cmp_gt_i32 vcc,0,v250\n"
"s_nop 1\n"
"v_cndmask_b32 v240,v240,v255,vcc\n"
"v_cmp_gt_i32 vcc,1,v250\n"
"s_nop 1\n"
"v_cndmask_b32 v241,v241,v255,vcc\n"
"v_cmp_gt_i32 vcc,2,v250\n"
"s_nop 1\n"
"v_cndmask_b32 v242,v242,v255,vcc\n"
"v_cmp_gt_i32 vcc,3,v250\n"
"s_nop 1\n"
"v_cndmask_b32 v243,v243,v255,vcc\n"
"v_cmp_gt_i32 vcc,16,v250\n"
"s_nop 1\n"
"v_cndmask_b32 v244,v244,v255,vcc\n"
"v_cmp_gt_i32 vcc,17,v250\n"
"s_nop 1\n"
"v_cndmask_b32 v245,v245,v255,vcc\n"
"v_cmp_gt_i32 vcc,18,v250\n"
"s_nop 1\n"
"v_cndmask_b32 v246,v246,v255,vcc\n"
"v_cmp_gt_i32 vcc,19,v250\n"
"s_nop 1\n"
"v_cndmask_b32 v247,v247,v255,vcc\n"
"s_cmp_lg_u32 s90,0\n"
"s_cbranch_scc1 L1071_%=\n"
"v_max3_f32 v248,v248,v240,v241\n"
"v_max3_f32 v248,v248,v242,v243\n"
"v_max3_f32 v248,v248,v244,v245\n"
"v_max3_f32 v248,v248,v246,v247\n"
"s_branch L1072_%=\n"
"L1071_%=:\n"
"s_lshl_b32 s91,s88,13\n"
SA"s91,s91,s39\n"
SA"s91,s91,524288\n"
BL"a[148:151],v224,s[8:11],s91 offen offset:0 sc1\n"
BL"a[152:155],v224,s[8:11],s91 offen offset:1024 sc1\n"
BL"a[156:159],v224,s[8:11],s91 offen offset:2048 sc1\n"
BL"a[160:163],v224,s[8:11],s91 offen offset:3072 sc1\n"
SA"s91,s91,4096\n"
BL"a[164:167],v224,s[8:11],s91 offen offset:0 sc1\n"
BL"a[168:171],v224,s[8:11],s91 offen offset:1024 sc1\n"
BL"a[172:175],v224,s[8:11],s91 offen offset:2048 sc1\n"
BL"a[176:179],v224,s[8:11],s91 offen offset:3072 sc1\n"
WT"vmcnt(0)\n"
"v_sub_f32 v240,v240,v248\n"
"v_sub_f32 v241,v241,v248\n"
"v_sub_f32 v242,v242,v248\n"
"v_sub_f32 v243,v243,v248\n"
"v_sub_f32 v244,v244,v248\n"
"v_sub_f32 v245,v245,v248\n"
"v_sub_f32 v246,v246,v248\n"
"v_sub_f32 v247,v247,v248\n"
EX"v240,v240\n"
EX"v241,v241\n"
EX"v242,v242\n"
EX"v243,v243\n"
EX"v244,v244\n"
EX"v245,v245\n"
EX"v246,v246\n"
EX"v247,v247\n"
"s_nop 0\n"
PK"v240,v240,v241\n"
PK"v241,v242,v243\n"
PK"v242,v244,v245\n"
PK"v243,v246,v247\n"
"s_nop 1\n"
MF"v[144:147],a[148:151],v[240:243],v[144:147]\n"
MF"v[148:151],a[152:155],v[240:243],v[148:151]\n"
MF"v[152:155],a[156:159],v[240:243],v[152:155]\n"
MF"v[156:159],a[160:163],v[240:243],v[156:159]\n"
MF"v[160:163],a[164:167],v[240:243],v[160:163]\n"
MF"v[164:167],a[168:171],v[240:243],v[164:167]\n"
MF"v[168:171],a[172:175],v[240:243],v[168:171]\n"
MF"v[172:175],a[176:179],v[240:243],v[172:175]\n"
MF"a[140:143],a[128:131],v[240:243],a[140:143]\n"
"s_nop 7\n"
"L1072_%=:\n"
SA"s88,s88,1\n"
"s_cmp_le_u32 s88,s89\n"
"s_cbranch_scc1 L1070_%=\n"
"s_cmp_lg_u32 s90,0\n"
"s_cbranch_scc1 L1073_%=\n"
"v_mov_b32 v254,v248\n"
"s_nop 1\n"
"v_permlane16_swap_b32 v254,v248\n"
"s_nop 1\n"
"v_max_f32 v248,v248,v254\n"
"v_mov_b32 v254,v248\n"
"s_nop 1\n"
"v_permlane32_swap_b32 v254,v248\n"
"s_nop 1\n"
"v_max_f32 v248,v248,v254\n"
"s_mov_b32 s90,1\n"
"s_mov_b32 s88,0\n"
"s_branch L1070_%=\n"
"L1073_%=:\n"
"s_nop 7\n"
"v_accvgpr_read_b32 v251,a140\n"
"s_nop 1\n"
"s_branch L1068_%=\n"
"L1075_%=:\n"
"s_nop 15\n"
WT"vmcnt(0)\n"
"s_lshl_b32 s89,s34,3\n"
"s_sub_u32 s89,120,s89\n"
SA"s89,s89,s30\n"
SA"s89,s89,4\n"
"s_lshl_b32 s91,s89,4\n"
"s_lshr_b32 s89,s89,1\n"
"v_bfe_u32 v249,v224,4,4\n"
"v_lshrrev_b32 v254,8,v224\n"
"v_lshlrev_b32 v254,2,v254\n"
"v_sub_u32 v249,v249,v254\n"
"v_add_u32 v249,s91,v249\n"
"v_mov_b32 v255,0xff800000\n"
"v_mov_b32 v248,v255\n"
"s_mov_b32 s90,0\n"
"s_mov_b32 s88,0\n"
"v_mov_b32 v176,0\n"
"v_mov_b32 v177,0\n"
"v_mov_b32 v178,0\n"
"v_mov_b32 v179,0\n"
"v_mov_b32 v180,0\n"
"v_mov_b32 v181,0\n"
"v_mov_b32 v182,0\n"
"v_mov_b32 v183,0\n"
"v_mov_b32 v184,0\n"
"v_mov_b32 v185,0\n"
"v_mov_b32 v186,0\n"
"v_mov_b32 v187,0\n"
"v_mov_b32 v188,0\n"
"v_mov_b32 v189,0\n"
"v_mov_b32 v190,0\n"
"v_mov_b32 v191,0\n"
"v_mov_b32 v192,0\n"
"v_mov_b32 v193,0\n"
"v_mov_b32 v194,0\n"
"v_mov_b32 v195,0\n"
"v_mov_b32 v196,0\n"
"v_mov_b32 v197,0\n"
"v_mov_b32 v198,0\n"
"v_mov_b32 v199,0\n"
"v_mov_b32 v200,0\n"
"v_mov_b32 v201,0\n"
"v_mov_b32 v202,0\n"
"v_mov_b32 v203,0\n"
"v_mov_b32 v204,0\n"
"v_mov_b32 v205,0\n"
"v_mov_b32 v206,0\n"
"v_mov_b32 v207,0\n"
"v_accvgpr_write_b32 a144,0\n"
"v_accvgpr_write_b32 a145,0\n"
"v_accvgpr_write_b32 a146,0\n"
"v_accvgpr_write_b32 a147,0\n"
"L1076_%=:\n"
"s_lshl_b32 s91,s88,13\n"
SA"s91,s91,s39\n"
SA"s91,s91,0\n"
BL"a[148:151],v224,s[8:11],s91 offen offset:0 sc1\n"
BL"a[152:155],v224,s[8:11],s91 offen offset:1024 sc1\n"
BL"a[156:159],v224,s[8:11],s91 offen offset:2048 sc1\n"
BL"a[160:163],v224,s[8:11],s91 offen offset:3072 sc1\n"
SA"s91,s91,4096\n"
BL"a[164:167],v224,s[8:11],s91 offen offset:0 sc1\n"
BL"a[168:171],v224,s[8:11],s91 offen offset:1024 sc1\n"
BL"a[172:175],v224,s[8:11],s91 offen offset:2048 sc1\n"
BL"a[176:179],v224,s[8:11],s91 offen offset:3072 sc1\n"
WT"vmcnt(0)\n"
MF"v[240:243],a[148:151],a[48:51],0\n"
MF"v[240:243],a[152:155],a[52:55],v[240:243]\n"
MF"v[240:243],a[156:159],a[56:59],v[240:243]\n"
MF"v[240:243],a[160:163],a[60:63],v[240:243]\n"
MF"v[244:247],a[164:167],a[48:51],0\n"
MF"v[244:247],a[168:171],a[52:55],v[244:247]\n"
MF"v[244:247],a[172:175],a[56:59],v[244:247]\n"
MF"v[244:247],a[176:179],a[60:63],v[244:247]\n"
"s_lshl_b32 s91,s88,5\n"
"v_subrev_u32 v250,s91,v249\n"
"s_nop 7\n"
"v_cmp_gt_i32 vcc,0,v250\n"
"s_nop 1\n"
"v_cndmask_b32 v240,v240,v255,vcc\n"
"v_cmp_gt_i32 vcc,1,v250\n"
"s_nop 1\n"
"v_cndmask_b32 v241,v241,v255,vcc\n"
"v_cmp_gt_i32 vcc,2,v250\n"
"s_nop 1\n"
"v_cndmask_b32 v242,v242,v255,vcc\n"
"v_cmp_gt_i32 vcc,3,v250\n"
"s_nop 1\n"
"v_cndmask_b32 v243,v243,v255,vcc\n"
"v_cmp_gt_i32 vcc,16,v250\n"
"s_nop 1\n"
"v_cndmask_b32 v244,v244,v255,vcc\n"
"v_cmp_gt_i32 vcc,17,v250\n"
"s_nop 1\n"
"v_cndmask_b32 v245,v245,v255,vcc\n"
"v_cmp_gt_i32 vcc,18,v250\n"
"s_nop 1\n"
"v_cndmask_b32 v246,v246,v255,vcc\n"
"v_cmp_gt_i32 vcc,19,v250\n"
"s_nop 1\n"
"v_cndmask_b32 v247,v247,v255,vcc\n"
"s_cmp_lg_u32 s90,0\n"
"s_cbranch_scc1 L1077_%=\n"
"v_max3_f32 v248,v248,v240,v241\n"
"v_max3_f32 v248,v248,v242,v243\n"
"v_max3_f32 v248,v248,v244,v245\n"
"v_max3_f32 v248,v248,v246,v247\n"
"s_branch L1078_%=\n"
"L1077_%=:\n"
"s_lshl_b32 s91,s88,13\n"
SA"s91,s91,s39\n"
SA"s91,s91,524288\n"
BL"a[148:151],v224,s[8:11],s91 offen offset:0 sc1\n"
BL"a[152:155],v224,s[8:11],s91 offen offset:1024 sc1\n"
BL"a[156:159],v224,s[8:11],s91 offen offset:2048 sc1\n"
BL"a[160:163],v224,s[8:11],s91 offen offset:3072 sc1\n"
SA"s91,s91,4096\n"
BL"a[164:167],v224,s[8:11],s91 offen offset:0 sc1\n"
BL"a[168:171],v224,s[8:11],s91 offen offset:1024 sc1\n"
BL"a[172:175],v224,s[8:11],s91 offen offset:2048 sc1\n"
BL"a[176:179],v224,s[8:11],s91 offen offset:3072 sc1\n"
WT"vmcnt(0)\n"
"v_sub_f32 v240,v240,v248\n"
"v_sub_f32 v241,v241,v248\n"
"v_sub_f32 v242,v242,v248\n"
"v_sub_f32 v243,v243,v248\n"
"v_sub_f32 v244,v244,v248\n"
"v_sub_f32 v245,v245,v248\n"
"v_sub_f32 v246,v246,v248\n"
"v_sub_f32 v247,v247,v248\n"
EX"v240,v240\n"
EX"v241,v241\n"
EX"v242,v242\n"
EX"v243,v243\n"
EX"v244,v244\n"
EX"v245,v245\n"
EX"v246,v246\n"
EX"v247,v247\n"
"s_nop 0\n"
PK"v240,v240,v241\n"
PK"v241,v242,v243\n"
PK"v242,v244,v245\n"
PK"v243,v246,v247\n"
"s_nop 1\n"
MF"v[176:179],a[148:151],v[240:243],v[176:179]\n"
MF"v[180:183],a[152:155],v[240:243],v[180:183]\n"
MF"v[184:187],a[156:159],v[240:243],v[184:187]\n"
MF"v[188:191],a[160:163],v[240:243],v[188:191]\n"
MF"v[192:195],a[164:167],v[240:243],v[192:195]\n"
MF"v[196:199],a[168:171],v[240:243],v[196:199]\n"
MF"v[200:203],a[172:175],v[240:243],v[200:203]\n"
MF"v[204:207],a[176:179],v[240:243],v[204:207]\n"
MF"a[144:147],a[128:131],v[240:243],a[144:147]\n"
"s_nop 7\n"
"L1078_%=:\n"
SA"s88,s88,1\n"
"s_cmp_le_u32 s88,s89\n"
"s_cbranch_scc1 L1076_%=\n"
"s_cmp_lg_u32 s90,0\n"
"s_cbranch_scc1 L1079_%=\n"
"v_mov_b32 v254,v248\n"
"s_nop 1\n"
"v_permlane16_swap_b32 v254,v248\n"
"s_nop 1\n"
"v_max_f32 v248,v248,v254\n"
"v_mov_b32 v254,v248\n"
"s_nop 1\n"
"v_permlane32_swap_b32 v254,v248\n"
"s_nop 1\n"
"v_max_f32 v248,v248,v254\n"
"s_mov_b32 s90,1\n"
"s_mov_b32 s88,0\n"
"s_branch L1076_%=\n"
"L1079_%=:\n"
"s_nop 7\n"
"v_accvgpr_read_b32 v252,a144\n"
"s_nop 1\n"
"s_branch L1074_%=\n"
"L1080_%=:\n"
"s_mov_b32 s54,0\n"
"L1082_%=:\n"
"s_sleep 2\n"
"buffer_load_dwordx2 v[240:241],v238,s[8:11],s63 offen sc1\n"
WT"vmcnt(0)\n"
SA"s54,s54,1\n"
"v_cmp_eq_u64 vcc,s[28:29],v[240:241]\n"
"s_cmp_eq_u64 vcc,exec\n"
"s_cbranch_scc1 L1081_%=\n"
"s_cmp_lt_u32 s54,1048576\n"
"s_cbranch_scc1 L1082_%=\n"
"s_branch L1081_%=\n"
"L1083_%=:\n"
"s_mov_b32 s54,0\n"
"L1085_%=:\n"
"s_sleep 2\n"
"buffer_load_dwordx2 v[240:241],v238,s[8:11],s63 offen sc1\n"
WT"vmcnt(0)\n"
SA"s54,s54,1\n"
"v_cmp_eq_u64 vcc,s[28:29],v[240:241]\n"
"s_cmp_eq_u64 vcc,exec\n"
"s_cbranch_scc1 L1084_%=\n"
"s_cmp_lt_u32 s54,1048576\n"
"s_cbranch_scc1 L1085_%=\n"
"s_branch L1084_%=\n"
"L1086_%=:\n"
"s_mov_b32 s54,0\n"
"L1088_%=:\n"
"s_sleep 2\n"
"buffer_load_dwordx2 v[240:241],v244,s[8:11],s92 offen sc1\n"
WT"vmcnt(0)\n"
SA"s54,s54,1\n"
"v_cmp_eq_u64 vcc,s[28:29],v[240:241]\n"
"s_cmp_eq_u64 vcc,exec\n"
"s_cbranch_scc1 L1087_%=\n"
"s_cmp_lt_u32 s54,1048576\n"
"s_cbranch_scc1 L1088_%=\n"
"s_branch L1087_%=\n"
"L1099_%=:\n"
"s_mov_b32 s54,0\n"
"L1101_%=:\n"
"s_sleep 2\n"
BL"v[240:243],v238,s[8:11],0 offen sc1\n"
WT"vmcnt(0)\n"
SA"s54,s54,1\n"
"v_cmp_eq_u64 vcc,s[28:29],v[240:241]\n"
"v_cmp_eq_u64_e64 s[50:51],s[28:29],v[242:243]\n"
"s_and_b64 vcc,vcc,s[50:51]\n"
"s_cmp_eq_u64 vcc,exec\n"
"s_cbranch_scc1 L1100_%=\n"
"s_cmp_lt_u32 s54,1048576\n"
"s_cbranch_scc1 L1101_%=\n"
"s_branch L1100_%=\n"
"L1102_%=:\n"
"s_mov_b32 s54,0\n"
"L1104_%=:\n"
"s_sleep 2\n"
"buffer_load_dwordx2 v[240:241],v238,s[8:11],s50 offen sc1\n"
WT"vmcnt(0)\n"
SA"s54,s54,1\n"
"v_cmp_eq_u64 vcc,s[28:29],v[240:241]\n"
"s_cmp_eq_u64 vcc,exec\n"
"s_cbranch_scc1 L1103_%=\n"
"s_cmp_lt_u32 s54,1048576\n"
"s_cbranch_scc1 L1104_%=\n"
"s_branch L1103_%=\n"
"L1105_%=:\n"
"s_mov_b32 s54,0\n"
"L1107_%=:\n"
"s_sleep 2\n"
"buffer_load_dwordx2 v[244:245],v242,s[8:11],s23 offen sc1\n"
WT"vmcnt(0)\n"
SA"s54,s54,1\n"
"v_cmp_eq_u64 vcc,s[28:29],v[244:245]\n"
"s_cmp_eq_u64 vcc,exec\n"
"s_cbranch_scc1 L1106_%=\n"
"s_cmp_lt_u32 s54,1048576\n"
"s_cbranch_scc1 L1107_%=\n"
"s_branch L1106_%=\n"
".p2align 6\n"
"LFA_%=:\n"
WT"lgkmcnt(6)\n"
MF"v[0:3],a[64:67],a[0:3],v[208:211]\n"
WT"lgkmcnt(3)\n"
PK"v192,v192,v193\n"
PK"v193,v194,v195\n"
MF"v[0:3],a[68:71],a[4:7],v[0:3]\n"
"s_mov_b32 m0,s40\n"
"v_readlane_b32 s43,v228,s42\n"
PK"v194,v196,v197\n"
MF"v[8:11],a[68:71],a[20:23],0\n"
PK"v195,v198,v199\n"
"v_readlane_b32 s44,v229,s42\n"
MF"v[8:11],a[64:67],a[16:19],v[8:11]\n"
SA"s42,s42,1\n"
BL"v225,s[8:11],s43 offen offset:0 sc1 lds\n"
MF"v[16:19],a[64:67],a[32:35],0\n"
"v_accvgpr_write_b32 a44,v192\n"
"v_accvgpr_write_b32 a45,v193\n"
MF"v[16:19],a[68:71],a[36:39],v[16:19]\n"
RD"a[92:95],v226 offset:7168\n"
"v_accvgpr_write_b32 a46,v194\n"
MF"v[24:27],a[68:71],a[52:55],0\n"
"v_accvgpr_write_b32 a47,v195\n"
PK"v200,v200,v201\n"
MF"v[24:27],a[64:67],a[48:51],v[24:27]\n"
PK"v201,v202,v203\n"
PK"v202,v204,v205\n"
MF"v[0:3],a[72:75],a[8:11],v[0:3]\n"
BL"v225,s[8:11],s43 offen offset:1024 sc1 lds\n"
PK"v203,v206,v207\n"
MF"v[0:3],a[76:79],a[12:15],v[0:3]\n"
"v_accvgpr_write_b32 a60,v200\n"
"v_accvgpr_write_b32 a61,v201\n"
MF"v[8:11],a[76:79],a[28:31],v[8:11]\n"
"s_mul_i32 s48,s32,655360\n"
"v_accvgpr_write_b32 a62,v202\n"
"v_accvgpr_write_b32 a63,v203\n"
MF"v[8:11],a[72:75],a[24:27],v[8:11]\n"
SA"s12,s74,s48\n"
"s_addc_u32 s13,s75,0\n"
MF"v[16:19],a[72:75],a[40:43],v[16:19]\n"
"s_and_b32 s13,s13,0xffff\n"
MF"v[16:19],a[76:79],a[44:47],v[16:19]\n"
"s_mov_b32 s14,655360\n"
RD"a[96:99],v226 offset:8192\n"
MF"v[24:27],a[76:79],a[60:63],v[24:27]\n"
"s_lshl_b32 s48,s33,3\n"
BL"v225,s[8:11],s43 offen offset:2048 sc1 lds\n"
MF"v[24:27],a[72:75],a[56:59],v[24:27]\n"
WT"lgkmcnt(1)\n"
EX"v0,v0\n"
MF"v[4:7],a[80:83],a[0:3],v[212:215]\n"
"s_and_b32 s49,s34,2\n"
EX"v1,v1\n"
MF"v[4:7],a[84:87],a[4:7],v[4:7]\n"
"s_lshl_b32 s49,s49,1\n"
EX"v2,v2\n"
MF"v[12:15],a[84:87],a[20:23],0\n"
SA"s48,s48,s49\n"
RD"a[100:103],v226 offset:9216\n"
PK"v0,v0,v1\n"
MF"v[12:15],a[80:83],a[16:19],v[12:15]\n"
SA"s48,s48,s30\n"
EX"v3,v3\n"
MF"v[20:23],a[80:83],a[32:35],0\n"
"s_mul_i32 s48,s48,5120\n"
EX"v8,v8\n"
MF"v[20:23],a[84:87],a[36:39],v[20:23]\n"
"s_or_b32 s49,s34,2\n"
BL"v225,s[8:11],s43 offen offset:3072 sc1 lds\n"
MF"v[28:31],a[84:87],a[52:55],0\n"
"s_cmp_eq_u32 s49,7\n"
EX"v9,v9\n"
PK"v1,v2,v3\n"
MF"v[28:31],a[80:83],a[48:51],v[28:31]\n"
"s_cselect_b32 s55,s48,2130706432\n"
RD"a[104:107],v226 offset:10240\n"
PK"v8,v8,v9\n"
MF"v[4:7],a[88:91],a[8:11],v[4:7]\n"
SA"m0,s40,16384\n"
EX"v10,v10\n"
MF"v[4:7],a[92:95],a[12:15],v[4:7]\n"
EX"v11,v11\n"
MF"v[12:15],a[92:95],a[28:31],v[12:15]\n"
EX"v16,v16\n"
MF"v[12:15],a[88:91],a[24:27],v[12:15]\n"
SA"s40,s40,32768\n"
RD"a[108:111],v226 offset:11264\n"
PK"v9,v10,v11\n"
MF"v[20:23],a[88:91],a[40:43],v[20:23]\n"
"s_cmp_eq_u32 s40,s46\n"
BL"v225,s[8:11],s44 offen offset:0 sc1 lds\n"
EX"v17,v17\n"
MF"v[20:23],a[92:95],a[44:47],v[20:23]\n"
"s_cselect_b32 s40,s47,s40\n"
EX"v18,v18\n"
MF"v[28:31],a[92:95],a[60:63],v[28:31]\n"
EX"v19,v19\n"
PK"v16,v16,v17\n"
MF"v[28:31],a[88:91],a[56:59],v[28:31]\n"
WT"lgkmcnt(1)\n"
RD"a[112:115],v226 offset:12288\n"
PK"v17,v18,v19\n"
MF"v[32:35],a[96:99],a[0:3],v[216:219]\n"
EX"v24,v24\n"
MF"v[32:35],a[100:103],a[4:7],v[32:35]\n"
EX"v25,v25\n"
"v_mov_b32 v227,v226\n"
MF"v[40:43],a[100:103],a[20:23],0\n"
BL"v225,s[8:11],s44 offen offset:1024 sc1 lds\n"
EX"v26,v26\n"
MF"v[40:43],a[96:99],a[16:19],v[40:43]\n"
RD"a[116:119],v226 offset:13312\n"
PK"v24,v24,v25\n"
MF"v[48:51],a[96:99],a[32:35],0\n"
EX"v27,v27\n"
MF"v[48:51],a[100:103],a[36:39],v[48:51]\n"
EX"v4,v4\n"
MF"v[56:59],a[100:103],a[52:55],0\n"
EX"v5,v5\n"
PK"v25,v26,v27\n"
MF"v[56:59],a[96:99],a[48:51],v[56:59]\n"
RD"a[120:123],v226 offset:14336\n"
PK"v2,v4,v5\n"
MF"v[32:35],a[104:107],a[8:11],v[32:35]\n"
WT"lgkmcnt(2)\n"
BL"v225,s[8:11],s44 offen offset:2048 sc1 lds\n"
EX"v6,v6\n"
MF"v[32:35],a[108:111],a[12:15],v[32:35]\n"
EX"v7,v7\n"
MF"v[40:43],a[108:111],a[28:31],v[40:43]\n"
EX"v12,v12\n"
MF"v[40:43],a[104:107],a[24:27],v[40:43]\n"
RD"a[124:127],v226 offset:15360\n"
"v_readlane_b32 s43,v228,s42\n"
PK"v3,v6,v7\n"
MF"v[48:51],a[104:107],a[40:43],v[48:51]\n"
EX"v13,v13\n"
MF"v[48:51],a[108:111],a[44:47],v[48:51]\n"
EX"v14,v14\n"
MF"v[56:59],a[108:111],a[60:63],v[56:59]\n"
BL"v225,s[8:11],s44 offen offset:3072 sc1 lds\n"
EX"v15,v15\n"
MF"v[56:59],a[104:107],a[56:59],v[56:59]\n"
"s_mov_b32 m0,s40\n"
RD"a[96:99],v227 offset:24576\n"
PK"v10,v12,v13\n"
MF"v[36:39],a[112:115],a[0:3],v[220:223]\n"
WT"lgkmcnt(2)\n"
EX"v20,v20\n"
PK"v11,v14,v15\n"
MF"v[36:39],a[116:119],a[4:7],v[36:39]\n"
EX"v21,v21\n"
MF"v[44:47],a[116:119],a[20:23],0\n"
EX"v22,v22\n"
MF"v[44:47],a[112:115],a[16:19],v[44:47]\n"
RD"a[100:103],v227 offset:25600\n"
"v_readlane_b32 s44,v229,s42\n"
PK"v18,v20,v21\n"
MF"v[52:55],a[112:115],a[32:35],0\n"
SA"s42,s42,1\n"
BL"v225,s[8:11],s43 offen offset:0 sc1 lds\n"
MF"v[52:55],a[116:119],a[36:39],v[52:55]\n"
EX"v23,v23\n"
MF"v[60:63],a[116:119],a[52:55],0\n"
EX"v28,v28\n"
MF"v[60:63],a[112:115],a[48:51],v[60:63]\n"
RD"a[104:107],v227 offset:26624\n"
PK"v19,v22,v23\n"
MF"v[36:39],a[120:123],a[8:11],v[36:39]\n"
WT"lgkmcnt(2)\n"
EX"v29,v29\n"
MF"v[36:39],a[124:127],a[12:15],v[36:39]\n"
BL"v225,s[8:11],s43 offen offset:1024 sc1 lds\n"
MF"v[44:47],a[124:127],a[28:31],v[44:47]\n"
EX"v30,v30\n"
PK"v26,v28,v29\n"
MF"v[44:47],a[120:123],a[24:27],v[44:47]\n"
RD"a[108:111],v227 offset:27648\n"
MF"v[52:55],a[120:123],a[40:43],v[52:55]\n"
EX"v31,v31\n"
MF"v[52:55],a[124:127],a[44:47],v[52:55]\n"
EX"v32,v32\n"
MF"v[60:63],a[124:127],a[60:63],v[60:63]\n"
EX"v33,v33\n"
PK"v27,v30,v31\n"
MF"v[60:63],a[120:123],a[56:59],v[60:63]\n"
SA"s41,s41,32768\n"
BL"v225,s[8:11],s43 offen offset:2048 sc1 lds\n"
RD"a[112:115],v227 offset:28672\n"
MF"v[80:83],a[96:99],v[0:3],0\n"
"s_cmp_eq_u32 s41,163840\n"
EX"v34,v34\n"
MF"v[112:115],a[96:99],v[8:11],0\n"
"s_cselect_b32 s41,0,s41\n"
EX"v35,v35\n"
MF"v[144:147],a[96:99],v[16:19],0\n"
EX"v40,v40\n"
"v_add_u32 v226,s41,v224\n"
MF"v[176:179],a[96:99],v[24:27],0\n"
WT"lgkmcnt(1)\n"
RD"a[116:119],v227 offset:29696\n"
MF"v[84:87],a[100:103],v[0:3],0\n"
EX"v41,v41\n"
MF"v[116:119],a[100:103],v[8:11],0\n"
BL"v225,s[8:11],s43 offen offset:3072 sc1 lds\n"
MF"v[148:151],a[100:103],v[16:19],0\n"
SA"m0,s40,16384\n"
EX"v42,v42\n"
MF"v[180:183],a[100:103],v[24:27],0\n"
RD"a[120:123],v227 offset:30720\n"
MF"v[88:91],a[104:107],v[0:3],0\n"
EX"v43,v43\n"
MF"v[120:123],a[104:107],v[8:11],0\n"
EX"v48,v48\n"
MF"v[152:155],a[104:107],v[16:19],0\n"
SA"s40,s40,32768\n"
EX"v49,v49\n"
MF"v[184:187],a[104:107],v[24:27],0\n"
"s_cmp_eq_u32 s40,s46\n"
BL"v225,s[8:11],s44 offen offset:0 sc1 lds\n"
RD"a[124:127],v227 offset:31744\n"
MF"v[92:95],a[108:111],v[0:3],0\n"
"s_cselect_b32 s40,s47,s40\n"
EX"v50,v50\n"
MF"v[124:127],a[108:111],v[8:11],0\n"
EX"v51,v51\n"
MF"v[156:159],a[108:111],v[16:19],0\n"
EX"v56,v56\n"
MF"v[188:191],a[108:111],v[24:27],0\n"
WT"lgkmcnt(1)\n"
EX"v57,v57\n"
MF"v[96:99],a[112:115],v[0:3],0\n"
EX"v58,v58\n"
MF"v[128:131],a[112:115],v[8:11],0\n"
WT"vmcnt(15) lgkmcnt(0)\n"
BL"v225,s[8:11],s44 offen offset:1024 sc1 lds\n"
MF"v[160:163],a[112:115],v[16:19],0\n"
EX"v59,v59\n"
MF"v[192:195],a[112:115],v[24:27],0\n"
EX"v36,v36\n"
MF"v[100:103],a[116:119],v[0:3],0\n"
"s_barrier\n"
MF"v[132:135],a[116:119],v[8:11],0\n"
RD"a[64:67],v226 offset:0\n"
"buffer_store_dwordx2 v[232:233],v230,s[8:11],s98 offen sc1\n"
MF"v[164:167],a[116:119],v[16:19],0\n"
EX"v37,v37\n"
MF"v[196:199],a[116:119],v[24:27],0\n"
BL"v225,s[8:11],s44 offen offset:2048 sc1 lds\n"
RD"a[68:71],v226 offset:1024\n"
MF"v[104:107],a[120:123],v[0:3],0\n"
EX"v38,v38\n"
MF"v[136:139],a[120:123],v[8:11],0\n"
EX"v39,v39\n"
MF"v[168:171],a[120:123],v[16:19],0\n"
EX"v44,v44\n"
MF"v[200:203],a[120:123],v[24:27],0\n"
RD"a[72:75],v226 offset:2048\n"
RD"a[76:79],v226 offset:3072\n"
MF"v[108:111],a[124:127],v[0:3],0\n"
EX"v45,v45\n"
MF"v[140:143],a[124:127],v[8:11],0\n"
BL"v225,s[8:11],s44 offen offset:3072 sc1 lds\n"
MF"v[172:175],a[124:127],v[16:19],0\n"
EX"v46,v46\n"
MF"v[204:207],a[124:127],v[24:27],0\n"
RD"a[80:83],v226 offset:4096\n"
MF"a[132:135],a[128:131],v[0:3],0\n"
EX"v47,v47\n"
MF"a[136:139],a[128:131],v[8:11],0\n"
"buffer_load_dword v255,v253,s[12:15],s55 offen\n"
EX"v52,v52\n"
MF"a[140:143],a[128:131],v[16:19],0\n"
WT"lgkmcnt(3)\n"
EX"v53,v53\n"
MF"a[144:147],a[128:131],v[24:27],0\n"
SA"s55,s55,163840\n"
RD"a[84:87],v226 offset:5120\n"
RD"a[88:91],v226 offset:6144\n"
EX"v54,v54\n"
EX"v55,v55\n"
EX"v60,v60\n"
EX"v61,v61\n"
EX"v62,v62\n"
EX"v63,v63\n"
"s_branch LDB_%=\n"
".p2align 6\n"
"LEV_%=:\n"
"s_mov_b32 s14,0\n"
MF"v[0:3],a[64:67],a[0:3],0\n"
SA"s95,s40,32768\n"
PK"v64,v32,v33\n"
PK"v65,v34,v35\n"
MF"v[0:3],a[68:71],a[4:7],v[0:3]\n"
"s_cmp_eq_u32 s95,s46\n"
PK"v66,v36,v37\n"
PK"v67,v38,v39\n"
MF"v[8:11],a[68:71],a[20:23],0\n"
"s_cselect_b32 s95,s47,s95\n"
PK"v68,v40,v41\n"
PK"v69,v42,v43\n"
MF"v[8:11],a[64:67],a[16:19],v[8:11]\n"
PK"v70,v44,v45\n"
PK"v71,v46,v47\n"
MF"v[16:19],a[64:67],a[32:35],0\n"
SA"m0,s95,0\n"
PK"v72,v48,v49\n"
PK"v73,v50,v51\n"
MF"v[16:19],a[68:71],a[36:39],v[16:19]\n"
RD"a[92:95],v226 offset:7168\n"
PK"v74,v52,v53\n"
MF"v[24:27],a[68:71],a[52:55],0\n"
PK"v75,v54,v55\n"
PK"v76,v56,v57\n"
MF"v[24:27],a[64:67],a[48:51],v[24:27]\n"
WT"lgkmcnt(1)\n"
BL"v235,s[24:27],s52 offen lds\n"
MF"v[0:3],a[72:75],a[8:11],v[0:3]\n"
SA"s52,s52,s62\n"
PK"v77,v58,v59\n"
PK"v78,v60,v61\n"
MF"v[0:3],a[76:79],a[12:15],v[0:3]\n"
PK"v79,v62,v63\n"
MF"v[8:11],a[76:79],a[28:31],v[8:11]\n"
MF"v[8:11],a[72:75],a[24:27],v[8:11]\n"
MF"v[16:19],a[72:75],a[40:43],v[16:19]\n"
SA"m0,s95,1024\n"
MF"v[16:19],a[76:79],a[44:47],v[16:19]\n"
RD"a[96:99],v226 offset:8192\n"
MF"v[24:27],a[76:79],a[60:63],v[24:27]\n"
EX"v0,v0\n"
MF"v[24:27],a[72:75],a[56:59],v[24:27]\n"
BL"v235,s[24:27],s52 offen lds\n"
MF"v[4:7],a[80:83],a[0:3],0\n"
SA"s52,s52,s62\n"
EX"v1,v1\n"
MF"v[4:7],a[84:87],a[4:7],v[4:7]\n"
RD"a[100:103],v226 offset:9216\n"
PK"v0,v0,v1\n"
MF"v[12:15],a[84:87],a[20:23],0\n"
EX"v2,v2\n"
MF"v[12:15],a[80:83],a[16:19],v[12:15]\n"
EX"v3,v3\n"
MF"v[20:23],a[80:83],a[32:35],0\n"
SA"m0,s95,2048\n"
EX"v8,v8\n"
MF"v[20:23],a[84:87],a[36:39],v[20:23]\n"
RD"a[104:107],v226 offset:10240\n"
PK"v1,v2,v3\n"
MF"v[28:31],a[84:87],a[52:55],0\n"
EX"v9,v9\n"
MF"v[28:31],a[80:83],a[48:51],v[28:31]\n"
BL"v235,s[24:27],s52 offen lds\n"
MF"v[4:7],a[88:91],a[8:11],v[4:7]\n"
WT"lgkmcnt(1)\n"
EX"v10,v10\n"
MF"v[4:7],a[92:95],a[12:15],v[4:7]\n"
SA"s52,s52,s62\n"
RD"a[108:111],v226 offset:11264\n"
PK"v8,v8,v9\n"
MF"v[12:15],a[92:95],a[28:31],v[12:15]\n"
EX"v11,v11\n"
MF"v[12:15],a[88:91],a[24:27],v[12:15]\n"
EX"v16,v16\n"
MF"v[20:23],a[88:91],a[40:43],v[20:23]\n"
SA"m0,s95,3072\n"
EX"v17,v17\n"
MF"v[20:23],a[92:95],a[44:47],v[20:23]\n"
RD"a[112:115],v226 offset:12288\n"
PK"v9,v10,v11\n"
MF"v[28:31],a[92:95],a[60:63],v[28:31]\n"
EX"v18,v18\n"
MF"v[28:31],a[88:91],a[56:59],v[28:31]\n"
BL"v235,s[24:27],s52 offen lds\n"
MF"v[32:35],a[96:99],a[0:3],0\n"
SA"s52,s52,s62\n"
EX"v19,v19\n"
MF"v[32:35],a[100:103],a[4:7],v[32:35]\n"
RD"a[116:119],v226 offset:13312\n"
PK"v16,v16,v17\n"
MF"v[40:43],a[100:103],a[20:23],0\n"
EX"v24,v24\n"
MF"v[40:43],a[96:99],a[16:19],v[40:43]\n"
EX"v25,v25\n"
MF"v[48:51],a[96:99],a[32:35],0\n"
SA"m0,s95,16384\n"
EX"v26,v26\n"
MF"v[48:51],a[100:103],a[36:39],v[48:51]\n"
RD"a[120:123],v226 offset:14336\n"
"v_mov_b32 v227,v226\n"
MF"v[56:59],a[100:103],a[52:55],0\n"
EX"v27,v27\n"
MF"v[56:59],a[96:99],a[48:51],v[56:59]\n"
WT"lgkmcnt(2)\n"
BL"v235,s[24:27],s52 offen lds\n"
MF"v[32:35],a[104:107],a[8:11],v[32:35]\n"
SA"s52,s52,s62\n"
EX"v4,v4\n"
MF"v[32:35],a[108:111],a[12:15],v[32:35]\n"
RD"a[124:127],v226 offset:15360\n"
PK"v17,v18,v19\n"
MF"v[40:43],a[108:111],a[28:31],v[40:43]\n"
EX"v5,v5\n"
MF"v[40:43],a[104:107],a[24:27],v[40:43]\n"
EX"v6,v6\n"
MF"v[48:51],a[104:107],a[40:43],v[48:51]\n"
SA"m0,s95,17408\n"
EX"v7,v7\n"
PK"v24,v24,v25\n"
MF"v[48:51],a[108:111],a[44:47],v[48:51]\n"
PK"v25,v26,v27\n"
PK"v2,v4,v5\n"
MF"v[56:59],a[108:111],a[60:63],v[56:59]\n"
EX"v12,v12\n"
MF"v[56:59],a[104:107],a[56:59],v[56:59]\n"
BL"v235,s[24:27],s52 offen lds\n"
MF"v[36:39],a[112:115],a[0:3],0\n"
WT"lgkmcnt(0)\n"
EX"v13,v13\n"
MF"v[36:39],a[116:119],a[4:7],v[36:39]\n"
SA"s52,s52,s62\n"
RD"a[64:67],v227 offset:16384\n"
PK"v3,v6,v7\n"
MF"v[44:47],a[116:119],a[20:23],0\n"
EX"v14,v14\n"
MF"v[44:47],a[112:115],a[16:19],v[44:47]\n"
PK"v10,v12,v13\n"
MF"v[52:55],a[112:115],a[32:35],0\n"
SA"m0,s95,18432\n"
EX"v15,v15\n"
MF"v[52:55],a[116:119],a[36:39],v[52:55]\n"
RD"a[68:71],v227 offset:17408\n"
PK"v11,v14,v15\n"
MF"v[60:63],a[116:119],a[52:55],0\n"
EX"v20,v20\n"
MF"v[60:63],a[112:115],a[48:51],v[60:63]\n"
BL"v235,s[24:27],s52 offen lds\n"
MF"v[36:39],a[120:123],a[8:11],v[36:39]\n"
SA"s52,s52,s62\n"
EX"v21,v21\n"
MF"v[36:39],a[124:127],a[12:15],v[36:39]\n"
RD"a[72:75],v227 offset:18432\n"
"v_readlane_b32 s43,v228,s42\n"
MF"v[44:47],a[124:127],a[28:31],v[44:47]\n"
EX"v22,v22\n"
MF"v[44:47],a[120:123],a[24:27],v[44:47]\n"
"s_mov_b32 m0,s40\n"
PK"v18,v20,v21\n"
MF"v[52:55],a[120:123],a[40:43],v[52:55]\n"
EX"v23,v23\n"
MF"v[52:55],a[124:127],a[44:47],v[52:55]\n"
RD"a[76:79],v227 offset:19456\n"
"v_readlane_b32 s44,v229,s42\n"
MF"v[60:63],a[124:127],a[60:63],v[60:63]\n"
EX"v28,v28\n"
MF"v[60:63],a[120:123],a[56:59],v[60:63]\n"
WT"lgkmcnt(2)\n"
BL"v225,s[8:11],s43 offen offset:0 sc1 lds\n"
MF"v[80:83],a[64:67],v[64:67],v[80:83]\n"
SA"s42,s42,1\n"
EX"v29,v29\n"
MF"v[112:115],a[64:67],v[68:71],v[112:115]\n"
SA"s41,s41,32768\n"
RD"a[80:83],v227 offset:20480\n"
PK"v19,v22,v23\n"
MF"v[144:147],a[64:67],v[72:75],v[144:147]\n"
"s_cmp_eq_u32 s41,163840\n"
EX"v30,v30\n"
MF"v[176:179],a[64:67],v[76:79],v[176:179]\n"
"s_cselect_b32 s41,0,s41\n"
PK"v26,v28,v29\n"
"v_add_u32 v226,s41,v224\n"
MF"v[84:87],a[68:71],v[64:67],v[84:87]\n"
EX"v31,v31\n"
MF"v[116:119],a[68:71],v[68:71],v[116:119]\n"
RD"a[84:87],v227 offset:21504\n"
PK"v27,v30,v31\n"
MF"v[148:151],a[68:71],v[72:75],v[148:151]\n"
EX"v32,v32\n"
MF"v[180:183],a[68:71],v[76:79],v[180:183]\n"
WT"lgkmcnt(2)\n"
BL"v225,s[8:11],s43 offen offset:1024 sc1 lds\n"
MF"v[88:91],a[72:75],v[64:67],v[88:91]\n"
EX"v33,v33\n"
MF"v[120:123],a[72:75],v[68:71],v[120:123]\n"
RD"a[88:91],v227 offset:22528\n"
MF"v[152:155],a[72:75],v[72:75],v[152:155]\n"
EX"v34,v34\n"
MF"v[184:187],a[72:75],v[76:79],v[184:187]\n"
MF"v[92:95],a[76:79],v[64:67],v[92:95]\n"
EX"v35,v35\n"
MF"v[124:127],a[76:79],v[68:71],v[124:127]\n"
RD"a[92:95],v227 offset:23552\n"
MF"v[156:159],a[76:79],v[72:75],v[156:159]\n"
EX"v40,v40\n"
MF"v[188:191],a[76:79],v[76:79],v[188:191]\n"
WT"lgkmcnt(2)\n"
BL"v225,s[8:11],s43 offen offset:2048 sc1 lds\n"
MF"v[96:99],a[80:83],v[64:67],v[96:99]\n"
EX"v41,v41\n"
MF"v[128:131],a[80:83],v[68:71],v[128:131]\n"
RD"a[96:99],v227 offset:24576\n"
MF"v[160:163],a[80:83],v[72:75],v[160:163]\n"
EX"v42,v42\n"
MF"v[192:195],a[80:83],v[76:79],v[192:195]\n"
MF"v[100:103],a[84:87],v[64:67],v[100:103]\n"
EX"v43,v43\n"
MF"v[132:135],a[84:87],v[68:71],v[132:135]\n"
RD"a[100:103],v227 offset:25600\n"
MF"v[164:167],a[84:87],v[72:75],v[164:167]\n"
EX"v48,v48\n"
MF"v[196:199],a[84:87],v[76:79],v[196:199]\n"
WT"lgkmcnt(2)\n"
BL"v225,s[8:11],s43 offen offset:3072 sc1 lds\n"
MF"v[104:107],a[88:91],v[64:67],v[104:107]\n"
EX"v49,v49\n"
MF"v[136:139],a[88:91],v[68:71],v[136:139]\n"
RD"a[104:107],v227 offset:26624\n"
MF"v[168:171],a[88:91],v[72:75],v[168:171]\n"
EX"v50,v50\n"
MF"v[200:203],a[88:91],v[76:79],v[200:203]\n"
SA"m0,s40,16384\n"
MF"v[108:111],a[92:95],v[64:67],v[108:111]\n"
EX"v51,v51\n"
MF"v[140:143],a[92:95],v[68:71],v[140:143]\n"
RD"a[108:111],v227 offset:27648\n"
MF"v[172:175],a[92:95],v[72:75],v[172:175]\n"
EX"v56,v56\n"
MF"v[204:207],a[92:95],v[76:79],v[204:207]\n"
BL"v225,s[8:11],s44 offen offset:0 sc1 lds\n"
MF"a[132:135],a[128:131],v[64:67],a[132:135]\n"
SA"s40,s40,32768\n"
EX"v57,v57\n"
MF"a[136:139],a[128:131],v[68:71],a[136:139]\n"
"s_cmp_eq_u32 s40,s46\n"
RD"a[112:115],v227 offset:28672\n"
"s_cselect_b32 s40,s47,s40\n"
MF"a[140:143],a[128:131],v[72:75],a[140:143]\n"
EX"v58,v58\n"
MF"a[144:147],a[128:131],v[76:79],a[144:147]\n"
WT"lgkmcnt(2)\n"
BL"v[240:243],v238,s[8:11],0 offen sc1\n"
MF"v[80:83],a[96:99],v[0:3],v[80:83]\n"
EX"v59,v59\n"
MF"v[112:115],a[96:99],v[8:11],v[112:115]\n"
RD"a[116:119],v227 offset:29696\n"
MF"v[144:147],a[96:99],v[16:19],v[144:147]\n"
EX"v36,v36\n"
MF"v[176:179],a[96:99],v[24:27],v[176:179]\n"
BL"v225,s[8:11],s44 offen offset:1024 sc1 lds\n"
MF"v[84:87],a[100:103],v[0:3],v[84:87]\n"
EX"v37,v37\n"
MF"v[116:119],a[100:103],v[8:11],v[116:119]\n"
RD"a[120:123],v227 offset:30720\n"
MF"v[148:151],a[100:103],v[16:19],v[148:151]\n"
EX"v38,v38\n"
MF"v[180:183],a[100:103],v[24:27],v[180:183]\n"
MF"v[88:91],a[104:107],v[0:3],v[88:91]\n"
EX"v39,v39\n"
MF"v[120:123],a[104:107],v[8:11],v[120:123]\n"
RD"a[124:127],v227 offset:31744\n"
MF"v[152:155],a[104:107],v[16:19],v[152:155]\n"
EX"v44,v44\n"
MF"v[184:187],a[104:107],v[24:27],v[184:187]\n"
WT"lgkmcnt(2)\n"
BL"v225,s[8:11],s44 offen offset:2048 sc1 lds\n"
MF"v[92:95],a[108:111],v[0:3],v[92:95]\n"
EX"v45,v45\n"
MF"v[124:127],a[108:111],v[8:11],v[124:127]\n"
MF"v[156:159],a[108:111],v[16:19],v[156:159]\n"
EX"v46,v46\n"
MF"v[188:191],a[108:111],v[24:27],v[188:191]\n"
WT"vmcnt(23) lgkmcnt(0)\n"
MF"v[96:99],a[112:115],v[0:3],v[96:99]\n"
"s_barrier\n"
MF"v[128:131],a[112:115],v[8:11],v[128:131]\n"
RD"a[64:67],v226 offset:0\n"
MF"v[160:163],a[112:115],v[16:19],v[160:163]\n"
EX"v47,v47\n"
MF"v[192:195],a[112:115],v[24:27],v[192:195]\n"
BL"v225,s[8:11],s44 offen offset:3072 sc1 lds\n"
MF"v[100:103],a[116:119],v[0:3],v[100:103]\n"
EX"v52,v52\n"
MF"v[132:135],a[116:119],v[8:11],v[132:135]\n"
RD"a[68:71],v226 offset:1024\n"
MF"v[164:167],a[116:119],v[16:19],v[164:167]\n"
EX"v53,v53\n"
MF"v[196:199],a[116:119],v[24:27],v[196:199]\n"
RD"a[72:75],v226 offset:2048\n"
MF"v[104:107],a[120:123],v[0:3],v[104:107]\n"
EX"v54,v54\n"
MF"v[136:139],a[120:123],v[8:11],v[136:139]\n"
RD"a[76:79],v226 offset:3072\n"
MF"v[168:171],a[120:123],v[16:19],v[168:171]\n"
EX"v55,v55\n"
MF"v[200:203],a[120:123],v[24:27],v[200:203]\n"
MF"v[108:111],a[124:127],v[0:3],v[108:111]\n"
EX"v60,v60\n"
MF"v[140:143],a[124:127],v[8:11],v[140:143]\n"
RD"a[80:83],v226 offset:4096\n"
MF"v[172:175],a[124:127],v[16:19],v[172:175]\n"
EX"v61,v61\n"
MF"v[204:207],a[124:127],v[24:27],v[204:207]\n"
RD"a[84:87],v226 offset:5120\n"
MF"a[132:135],a[128:131],v[0:3],a[132:135]\n"
EX"v62,v62\n"
MF"a[136:139],a[128:131],v[8:11],a[136:139]\n"
RD"a[88:91],v226 offset:6144\n"
MF"a[140:143],a[128:131],v[16:19],a[140:143]\n"
WT"lgkmcnt(5)\n"
EX"v63,v63\n"
MF"a[144:147],a[128:131],v[24:27],a[144:147]\n"
MF"v[0:3],a[64:67],a[0:3],0\n"
SA"s95,s40,32768\n"
"v_lshrrev_b32 v237,1,v224\n"
PK"v64,v32,v33\n"
MF"v[0:3],a[68:71],a[4:7],v[0:3]\n"
"s_cmp_eq_u32 s95,s46\n"
"v_and_b32 v237,0x78,v237\n"
PK"v65,v34,v35\n"
MF"v[8:11],a[68:71],a[20:23],0\n"
"s_cselect_b32 s95,s47,s95\n"
"v_and_b32 v239,0x300,v224\n"
PK"v66,v36,v37\n"
MF"v[8:11],a[64:67],a[16:19],v[8:11]\n"
"v_add3_u32 v237,v237,v239,s40\n"
PK"v67,v38,v39\n"
MF"v[16:19],a[64:67],a[32:35],0\n"
SA"m0,s95,0\n"
PK"v68,v40,v41\n"
PK"v69,v42,v43\n"
MF"v[16:19],a[68:71],a[36:39],v[16:19]\n"
RD"a[92:95],v226 offset:7168\n"
PK"v70,v44,v45\n"
MF"v[24:27],a[68:71],a[52:55],0\n"
PK"v71,v46,v47\n"
PK"v72,v48,v49\n"
MF"v[24:27],a[64:67],a[48:51],v[24:27]\n"
WT"lgkmcnt(1)\n"
BL"v235,s[24:27],s52 offen lds\n"
MF"v[0:3],a[72:75],a[8:11],v[0:3]\n"
SA"s52,s52,s62\n"
PK"v73,v50,v51\n"
PK"v74,v52,v53\n"
MF"v[0:3],a[76:79],a[12:15],v[0:3]\n"
PK"v75,v54,v55\n"
PK"v76,v56,v57\n"
MF"v[8:11],a[76:79],a[28:31],v[8:11]\n"
PK"v77,v58,v59\n"
PK"v78,v60,v61\n"
MF"v[8:11],a[72:75],a[24:27],v[8:11]\n"
WT"vmcnt(4)\n"
"v_cmp_eq_u64 vcc,s[28:29],v[240:241]\n"
"v_cmp_eq_u64_e64 s[50:51],s[28:29],v[242:243]\n"
"s_and_b64 vcc,vcc,s[50:51]\n"
"s_cmp_eq_u64 vcc,exec\n"
"s_cbranch_scc0 L1099_%=\n"
"L1100_%=:\n"
MF"v[16:19],a[72:75],a[40:43],v[16:19]\n"
SA"m0,s95,1024\n"
PK"v79,v62,v63\n"
MF"v[16:19],a[76:79],a[44:47],v[16:19]\n"
RD"a[96:99],v226 offset:8192\n"
MF"v[24:27],a[76:79],a[60:63],v[24:27]\n"
EX"v0,v0\n"
MF"v[24:27],a[72:75],a[56:59],v[24:27]\n"
BL"v235,s[24:27],s52 offen lds\n"
MF"v[4:7],a[80:83],a[0:3],0\n"
SA"s52,s52,s62\n"
EX"v1,v1\n"
MF"v[4:7],a[84:87],a[4:7],v[4:7]\n"
RD"a[100:103],v226 offset:9216\n"
PK"v0,v0,v1\n"
MF"v[12:15],a[84:87],a[20:23],0\n"
EX"v2,v2\n"
MF"v[12:15],a[80:83],a[16:19],v[12:15]\n"
EX"v3,v3\n"
MF"v[20:23],a[80:83],a[32:35],0\n"
SA"m0,s95,2048\n"
EX"v8,v8\n"
MF"v[20:23],a[84:87],a[36:39],v[20:23]\n"
RD"a[104:107],v226 offset:10240\n"
PK"v1,v2,v3\n"
MF"v[28:31],a[84:87],a[52:55],0\n"
EX"v9,v9\n"
MF"v[28:31],a[80:83],a[48:51],v[28:31]\n"
BL"v235,s[24:27],s52 offen lds\n"
MF"v[4:7],a[88:91],a[8:11],v[4:7]\n"
WT"lgkmcnt(1)\n"
EX"v10,v10\n"
MF"v[4:7],a[92:95],a[12:15],v[4:7]\n"
SA"s52,s52,s62\n"
RD"a[108:111],v226 offset:11264\n"
PK"v8,v8,v9\n"
MF"v[12:15],a[92:95],a[28:31],v[12:15]\n"
EX"v11,v11\n"
MF"v[12:15],a[88:91],a[24:27],v[12:15]\n"
RT"a[148:149],v237 offset:0\n"
RT"a[150:151],v237 offset:128\n"
MF"v[20:23],a[88:91],a[40:43],v[20:23]\n"
SA"m0,s95,3072\n"
EX"v16,v16\n"
MF"v[20:23],a[92:95],a[44:47],v[20:23]\n"
EX"v17,v17\n"
MF"v[28:31],a[92:95],a[60:63],v[28:31]\n"
RD"a[112:115],v226 offset:12288\n"
PK"v9,v10,v11\n"
MF"v[28:31],a[88:91],a[56:59],v[28:31]\n"
EX"v18,v18\n"
MF"v[32:35],a[96:99],a[0:3],0\n"
RT"a[152:153],v237 offset:1024\n"
RT"a[154:155],v237 offset:1152\n"
MF"v[32:35],a[100:103],a[4:7],v[32:35]\n"
BL"v235,s[24:27],s52 offen lds\n"
MF"v[40:43],a[100:103],a[20:23],0\n"
SA"s52,s52,s62\n"
EX"v19,v19\n"
MF"v[40:43],a[96:99],a[16:19],v[40:43]\n"
RD"a[116:119],v226 offset:13312\n"
RT"a[156:157],v237 offset:2048\n"
MF"v[48:51],a[96:99],a[32:35],0\n"
SA"m0,s95,16384\n"
EX"v24,v24\n"
MF"v[48:51],a[100:103],a[36:39],v[48:51]\n"
RT"a[158:159],v237 offset:2176\n"
RD"a[120:123],v226 offset:14336\n"
EX"v25,v25\n"
MF"v[56:59],a[100:103],a[52:55],0\n"
EX"v26,v26\n"
PK"v16,v16,v17\n"
MF"v[56:59],a[96:99],a[48:51],v[56:59]\n"
WT"lgkmcnt(4)\n"
EX"v27,v27\n"
"v_mov_b32 v227,v226\n"
MF"v[32:35],a[104:107],a[8:11],v[32:35]\n"
RT"a[160:161],v237 offset:3072\n"
RT"a[162:163],v237 offset:3200\n"
MF"v[32:35],a[108:111],a[12:15],v[32:35]\n"
BL"v235,s[24:27],s52 offen lds\n"
PK"v17,v18,v19\n"
MF"v[40:43],a[108:111],a[28:31],v[40:43]\n"
SA"s52,s52,s62\n"
EX"v4,v4\n"
MF"v[40:43],a[104:107],a[24:27],v[40:43]\n"
RD"a[124:127],v226 offset:15360\n"
RT"a[164:165],v237 offset:16384\n"
MF"v[48:51],a[104:107],a[40:43],v[48:51]\n"
SA"m0,s95,17408\n"
EX"v5,v5\n"
PK"v24,v24,v25\n"
MF"v[48:51],a[108:111],a[44:47],v[48:51]\n"
RT"a[166:167],v237 offset:16512\n"
PK"v25,v26,v27\n"
EX"v6,v6\n"
MF"v[56:59],a[108:111],a[60:63],v[56:59]\n"
EX"v7,v7\n"
MF"v[56:59],a[104:107],a[56:59],v[56:59]\n"
EX"v12,v12\n"
MF"v[36:39],a[112:115],a[0:3],0\n"
WT"lgkmcnt(3)\n"
RT"a[168:169],v237 offset:17408\n"
RT"a[170:171],v237 offset:17536\n"
MF"v[36:39],a[116:119],a[4:7],v[36:39]\n"
BL"v235,s[24:27],s52 offen lds\n"
MF"v[44:47],a[116:119],a[20:23],0\n"
SA"s52,s52,s62\n"
EX"v13,v13\n"
MF"v[44:47],a[112:115],a[16:19],v[44:47]\n"
RD"a[64:67],v227 offset:16384\n"
RT"a[172:173],v237 offset:18432\n"
PK"v2,v4,v5\n"
MF"v[52:55],a[112:115],a[32:35],0\n"
SA"m0,s95,18432\n"
EX"v14,v14\n"
MF"v[52:55],a[116:119],a[36:39],v[52:55]\n"
RT"a[174:175],v237 offset:18560\n"
RD"a[68:71],v227 offset:17408\n"
PK"v3,v6,v7\n"
MF"v[60:63],a[116:119],a[52:55],0\n"
EX"v15,v15\n"
MF"v[60:63],a[112:115],a[48:51],v[60:63]\n"
EX"v20,v20\n"
MF"v[36:39],a[120:123],a[8:11],v[36:39]\n"
WT"lgkmcnt(4)\n"
BL"v235,s[24:27],s52 offen lds\n"
MF"v[36:39],a[124:127],a[12:15],v[36:39]\n"
SA"s52,s52,s62\n"
EX"v21,v21\n"
PK"v10,v12,v13\n"
MF"v[44:47],a[124:127],a[28:31],v[44:47]\n"
"s_cmp_gt_u32 s92,0\n"
RD"a[72:75],v227 offset:18432\n"
"v_readlane_b32 s43,v228,s42\n"
MF"v[44:47],a[120:123],a[24:27],v[44:47]\n"
"s_cselect_b32 s63,s63,1065353216\n"
EX"v22,v22\n"
MF"v[52:55],a[120:123],a[40:43],v[52:55]\n"
"s_mov_b32 m0,s40\n"
BL"a[204:207],v236,s[8:11],s63 offen offset:0 sc1\n"
MF"v[52:55],a[124:127],a[44:47],v[52:55]\n"
EX"v23,v23\n"
PK"v11,v14,v15\n"
MF"v[60:63],a[124:127],a[60:63],v[60:63]\n"
RD"a[76:79],v227 offset:19456\n"
"v_readlane_b32 s44,v229,s42\n"
MF"v[60:63],a[120:123],a[56:59],v[60:63]\n"
WT"lgkmcnt(2)\n"
EX"v28,v28\n"
MF"v[80:83],a[64:67],v[64:67],v[80:83]\n"
SA"s42,s42,1\n"
BL"v225,s[8:11],s43 offen offset:0 sc1 lds\n"
MF"v[112:115],a[64:67],v[68:71],v[112:115]\n"
SA"s41,s41,32768\n"
EX"v29,v29\n"
PK"v18,v20,v21\n"
MF"v[144:147],a[64:67],v[72:75],v[144:147]\n"
"s_cmp_eq_u32 s41,163840\n"
RD"a[80:83],v227 offset:20480\n"
PK"v19,v22,v23\n"
MF"v[176:179],a[64:67],v[76:79],v[176:179]\n"
"s_cselect_b32 s41,0,s41\n"
EX"v30,v30\n"
MF"v[84:87],a[68:71],v[64:67],v[84:87]\n"
BL"a[208:211],v236,s[8:11],s63 offen offset:1024 sc1\n"
MF"v[116:119],a[68:71],v[68:71],v[116:119]\n"
EX"v31,v31\n"
MF"v[148:151],a[68:71],v[72:75],v[148:151]\n"
RD"a[84:87],v227 offset:21504\n"
"v_add_u32 v226,s41,v224\n"
MF"v[180:183],a[68:71],v[76:79],v[180:183]\n"
WT"lgkmcnt(2)\n"
EX"v32,v32\n"
MF"v[88:91],a[72:75],v[64:67],v[88:91]\n"
BL"v225,s[8:11],s43 offen offset:1024 sc1 lds\n"
MF"v[120:123],a[72:75],v[68:71],v[120:123]\n"
EX"v33,v33\n"
PK"v26,v28,v29\n"
MF"v[152:155],a[72:75],v[72:75],v[152:155]\n"
RD"a[88:91],v227 offset:22528\n"
PK"v27,v30,v31\n"
MF"v[184:187],a[72:75],v[76:79],v[184:187]\n"
EX"v34,v34\n"
MF"v[92:95],a[76:79],v[64:67],v[92:95]\n"
BL"a[212:215],v236,s[8:11],s63 offen offset:2048 sc1\n"
MF"v[124:127],a[76:79],v[68:71],v[124:127]\n"
EX"v35,v35\n"
MF"v[156:159],a[76:79],v[72:75],v[156:159]\n"
RD"a[92:95],v227 offset:23552\n"
MF"v[188:191],a[76:79],v[76:79],v[188:191]\n"
WT"lgkmcnt(2)\n"
EX"v40,v40\n"
MF"v[96:99],a[80:83],v[64:67],v[96:99]\n"
BL"v225,s[8:11],s43 offen offset:2048 sc1 lds\n"
MF"v[128:131],a[80:83],v[68:71],v[128:131]\n"
EX"v41,v41\n"
MF"v[160:163],a[80:83],v[72:75],v[160:163]\n"
RD"a[96:99],v227 offset:24576\n"
MF"v[192:195],a[80:83],v[76:79],v[192:195]\n"
EX"v42,v42\n"
MF"v[100:103],a[84:87],v[64:67],v[100:103]\n"
BL"a[216:219],v236,s[8:11],s63 offen offset:3072 sc1\n"
MF"v[132:135],a[84:87],v[68:71],v[132:135]\n"
EX"v43,v43\n"
MF"v[164:167],a[84:87],v[72:75],v[164:167]\n"
RD"a[100:103],v227 offset:25600\n"
MF"v[196:199],a[84:87],v[76:79],v[196:199]\n"
WT"lgkmcnt(2)\n"
EX"v48,v48\n"
MF"v[104:107],a[88:91],v[64:67],v[104:107]\n"
SA"s63,s63,4096\n"
BL"v225,s[8:11],s43 offen offset:3072 sc1 lds\n"
MF"v[136:139],a[88:91],v[68:71],v[136:139]\n"
EX"v49,v49\n"
MF"v[168:171],a[88:91],v[72:75],v[168:171]\n"
RD"a[104:107],v227 offset:26624\n"
MF"v[200:203],a[88:91],v[76:79],v[200:203]\n"
SA"m0,s40,16384\n"
EX"v50,v50\n"
MF"v[108:111],a[92:95],v[64:67],v[108:111]\n"
BL"a[220:223],v236,s[8:11],s63 offen offset:0 sc1\n"
MF"v[140:143],a[92:95],v[68:71],v[140:143]\n"
EX"v51,v51\n"
MF"v[172:175],a[92:95],v[72:75],v[172:175]\n"
RD"a[108:111],v227 offset:27648\n"
MF"v[204:207],a[92:95],v[76:79],v[204:207]\n"
EX"v56,v56\n"
MF"a[132:135],a[128:131],v[64:67],a[132:135]\n"
SA"s40,s40,32768\n"
BL"v225,s[8:11],s44 offen offset:0 sc1 lds\n"
MF"a[136:139],a[128:131],v[68:71],a[136:139]\n"
"s_cmp_eq_u32 s40,s46\n"
EX"v57,v57\n"
MF"a[140:143],a[128:131],v[72:75],a[140:143]\n"
"s_cselect_b32 s40,s47,s40\n"
RD"a[112:115],v227 offset:28672\n"
MF"a[144:147],a[128:131],v[76:79],a[144:147]\n"
WT"lgkmcnt(2)\n"
EX"v58,v58\n"
MF"v[80:83],a[96:99],v[0:3],v[80:83]\n"
BL"a[224:227],v236,s[8:11],s63 offen offset:1024 sc1\n"
MF"v[112:115],a[96:99],v[8:11],v[112:115]\n"
EX"v59,v59\n"
MF"v[144:147],a[96:99],v[16:19],v[144:147]\n"
RD"a[116:119],v227 offset:29696\n"
MF"v[176:179],a[96:99],v[24:27],v[176:179]\n"
EX"v36,v36\n"
MF"v[84:87],a[100:103],v[0:3],v[84:87]\n"
BL"v225,s[8:11],s44 offen offset:1024 sc1 lds\n"
MF"v[116:119],a[100:103],v[8:11],v[116:119]\n"
EX"v37,v37\n"
MF"v[148:151],a[100:103],v[16:19],v[148:151]\n"
RD"a[120:123],v227 offset:30720\n"
MF"v[180:183],a[100:103],v[24:27],v[180:183]\n"
EX"v38,v38\n"
MF"v[88:91],a[104:107],v[0:3],v[88:91]\n"
BL"a[228:231],v236,s[8:11],s63 offen offset:2048 sc1\n"
MF"v[120:123],a[104:107],v[8:11],v[120:123]\n"
EX"v39,v39\n"
MF"v[152:155],a[104:107],v[16:19],v[152:155]\n"
RD"a[124:127],v227 offset:31744\n"
MF"v[184:187],a[104:107],v[24:27],v[184:187]\n"
WT"lgkmcnt(2)\n"
EX"v44,v44\n"
MF"v[92:95],a[108:111],v[0:3],v[92:95]\n"
BL"v225,s[8:11],s44 offen offset:2048 sc1 lds\n"
MF"v[124:127],a[108:111],v[8:11],v[124:127]\n"
EX"v45,v45\n"
MF"v[156:159],a[108:111],v[16:19],v[156:159]\n"
EX"v46,v46\n"
MF"v[188:191],a[108:111],v[24:27],v[188:191]\n"
WT"lgkmcnt(0)\n"
BL"a[232:235],v236,s[8:11],s63 offen offset:3072 sc1\n"
MF"v[96:99],a[112:115],v[0:3],v[96:99]\n"
"s_barrier\n"
MF"v[128:131],a[112:115],v[8:11],v[128:131]\n"
RD"a[64:67],v226 offset:0\n"
MF"v[160:163],a[112:115],v[16:19],v[160:163]\n"
EX"v47,v47\n"
MF"v[192:195],a[112:115],v[24:27],v[192:195]\n"
SA"s63,s63,4096\n"
BL"v225,s[8:11],s44 offen offset:3072 sc1 lds\n"
MF"v[100:103],a[116:119],v[0:3],v[100:103]\n"
EX"v52,v52\n"
MF"v[132:135],a[116:119],v[8:11],v[132:135]\n"
RD"a[68:71],v226 offset:1024\n"
MF"v[164:167],a[116:119],v[16:19],v[164:167]\n"
EX"v53,v53\n"
MF"v[196:199],a[116:119],v[24:27],v[196:199]\n"
BL"a[236:239],v236,s[8:11],s63 offen offset:0 sc1\n"
MF"v[104:107],a[120:123],v[0:3],v[104:107]\n"
RD"a[72:75],v226 offset:2048\n"
MF"v[136:139],a[120:123],v[8:11],v[136:139]\n"
EX"v54,v54\n"
MF"v[168:171],a[120:123],v[16:19],v[168:171]\n"
RD"a[76:79],v226 offset:3072\n"
MF"v[200:203],a[120:123],v[24:27],v[200:203]\n"
EX"v55,v55\n"
MF"v[108:111],a[124:127],v[0:3],v[108:111]\n"
EX"v60,v60\n"
MF"v[140:143],a[124:127],v[8:11],v[140:143]\n"
RD"a[80:83],v226 offset:4096\n"
MF"v[172:175],a[124:127],v[16:19],v[172:175]\n"
EX"v61,v61\n"
MF"v[204:207],a[124:127],v[24:27],v[204:207]\n"
RD"a[84:87],v226 offset:5120\n"
MF"a[132:135],a[128:131],v[0:3],a[132:135]\n"
EX"v62,v62\n"
MF"a[136:139],a[128:131],v[8:11],a[136:139]\n"
RD"a[88:91],v226 offset:6144\n"
MF"a[140:143],a[128:131],v[16:19],a[140:143]\n"
WT"lgkmcnt(5)\n"
EX"v63,v63\n"
MF"a[144:147],a[128:131],v[24:27],a[144:147]\n"
MF"v[0:3],a[64:67],a[0:3],0\n"
SA"s95,s40,32768\n"
"v_lshrrev_b32 v237,1,v224\n"
PK"v64,v32,v33\n"
MF"v[0:3],a[68:71],a[4:7],v[0:3]\n"
"s_cmp_eq_u32 s95,s46\n"
"v_and_b32 v237,0x78,v237\n"
PK"v65,v34,v35\n"
MF"v[8:11],a[68:71],a[20:23],0\n"
"s_cselect_b32 s95,s47,s95\n"
"v_and_b32 v239,0x300,v224\n"
PK"v66,v36,v37\n"
MF"v[8:11],a[64:67],a[16:19],v[8:11]\n"
"s_sub_u32 s95,s95,s47\n"
BL"a[240:243],v236,s[8:11],s63 offen offset:1024 sc1\n"
MF"v[16:19],a[64:67],a[32:35],0\n"
"s_lshr_b32 s53,s47,2\n"
"v_add3_u32 v237,v237,v239,s40\n"
PK"v67,v38,v39\n"
MF"v[16:19],a[68:71],a[36:39],v[16:19]\n"
"s_cmp_gt_u32 s92,10\n"
RD"a[92:95],v226 offset:7168\n"
PK"v68,v40,v41\n"
MF"v[24:27],a[68:71],a[52:55],0\n"
"s_cselect_b32 s63,s63,1065353216\n"
PK"v69,v42,v43\n"
PK"v70,v44,v45\n"
MF"v[24:27],a[64:67],a[48:51],v[24:27]\n"
WT"lgkmcnt(1)\n"
BL"a[244:247],v236,s[8:11],s63 offen offset:2048 sc1\n"
MF"v[0:3],a[72:75],a[8:11],v[0:3]\n"
"s_lshl_b32 s50,s35,6\n"
PK"v71,v46,v47\n"
"v_add_u32 v235,s95,v224\n"
MF"v[0:3],a[76:79],a[12:15],v[0:3]\n"
SA"s50,s50,4352\n"
PK"v72,v48,v49\n"
PK"v73,v50,v51\n"
MF"v[8:11],a[76:79],a[28:31],v[8:11]\n"
PK"v74,v52,v53\n"
PK"v75,v54,v55\n"
MF"v[8:11],a[72:75],a[24:27],v[8:11]\n"
BL"a[248:251],v236,s[8:11],s63 offen offset:3072 sc1\n"
MF"v[16:19],a[72:75],a[40:43],v[16:19]\n"
SA"s63,s63,4096\n"
PK"v76,v56,v57\n"
PK"v77,v58,v59\n"
MF"v[16:19],a[76:79],a[44:47],v[16:19]\n"
RD"a[96:99],v226 offset:8192\n"
PK"v78,v60,v61\n"
MF"v[24:27],a[76:79],a[60:63],v[24:27]\n"
EX"v0,v0\n"
MF"v[24:27],a[72:75],a[56:59],v[24:27]\n"
BL"a[252:255],v236,s[8:11],s63 offen offset:0 sc1\n"
PK"v79,v62,v63\n"
MF"v[4:7],a[80:83],a[0:3],0\n"
"s_cmp_gt_u32 s92,13\n"
EX"v1,v1\n"
"v_lshrrev_b32 v238,1,v224\n"
MF"v[4:7],a[84:87],a[4:7],v[4:7]\n"
"s_cselect_b32 s63,s63,1065353216\n"
RD"a[100:103],v226 offset:9216\n"
"v_and_b32 v238,56,v238\n"
MF"v[12:15],a[84:87],a[20:23],0\n"
EX"v2,v2\n"
MF"v[12:15],a[80:83],a[16:19],v[12:15]\n"
BL"v[244:247],v236,s[8:11],s63 offen offset:1024 sc1\n"
"v_add_u32 v236,s53,v235\n"
MF"v[20:23],a[80:83],a[32:35],0\n"
EX"v3,v3\n"
MF"v[20:23],a[84:87],a[36:39],v[20:23]\n"
EX"v8,v8\n"
MF"v[28:31],a[84:87],a[52:55],0\n"
RD"a[104:107],v226 offset:10240\n"
PK"v0,v0,v1\n"
MF"v[28:31],a[80:83],a[48:51],v[28:31]\n"
EX"v9,v9\n"
MF"v[4:7],a[88:91],a[8:11],v[4:7]\n"
WT"lgkmcnt(1)\n"
"buffer_load_dwordx2 v[240:241],v238,s[8:11],s50 offen sc1\n"
MF"v[4:7],a[92:95],a[12:15],v[4:7]\n"
EX"v10,v10\n"
MF"v[12:15],a[92:95],a[28:31],v[12:15]\n"
RD"a[108:111],v226 offset:11264\n"
PK"v1,v2,v3\n"
MF"v[12:15],a[88:91],a[24:27],v[12:15]\n"
EX"v11,v11\n"
MF"v[20:23],a[88:91],a[40:43],v[20:23]\n"
WT"vmcnt(23)\n"
RT"a[176:177],v237 offset:0\n"
RT"a[178:179],v237 offset:128\n"
MF"v[20:23],a[92:95],a[44:47],v[20:23]\n"
EX"v16,v16\n"
MF"v[28:31],a[92:95],a[60:63],v[28:31]\n"
EX"v17,v17\n"
MF"v[28:31],a[88:91],a[56:59],v[28:31]\n"
RD"a[112:115],v226 offset:12288\n"
RT"a[180:181],v237 offset:1024\n"
PK"v8,v8,v9\n"
MF"v[32:35],a[96:99],a[0:3],0\n"
EX"v18,v18\n"
MF"v[32:35],a[100:103],a[4:7],v[32:35]\n"
RT"a[182:183],v237 offset:1152\n"
RD"a[116:119],v226 offset:13312\n"
MF"v[40:43],a[100:103],a[20:23],0\n"
EX"v19,v19\n"
MF"v[40:43],a[96:99],a[16:19],v[40:43]\n"
EX"v24,v24\n"
PK"v9,v10,v11\n"
MF"v[48:51],a[96:99],a[32:35],0\n"
RT"a[184:185],v237 offset:2048\n"
RT"a[186:187],v237 offset:2176\n"
MF"v[48:51],a[100:103],a[36:39],v[48:51]\n"
EX"v25,v25\n"
PK"v16,v16,v17\n"
MF"v[56:59],a[100:103],a[52:55],0\n"
EX"v26,v26\n"
"v_mov_b32 v227,v226\n"
MF"v[56:59],a[96:99],a[48:51],v[56:59]\n"
WT"lgkmcnt(4)\n"
RD"a[120:123],v226 offset:14336\n"
RT"a[188:189],v237 offset:3072\n"
MF"v[32:35],a[104:107],a[8:11],v[32:35]\n"
EX"v27,v27\n"
MF"v[32:35],a[108:111],a[12:15],v[32:35]\n"
RT"a[190:191],v237 offset:3200\n"
RD"a[124:127],v226 offset:15360\n"
PK"v17,v18,v19\n"
MF"v[40:43],a[108:111],a[28:31],v[40:43]\n"
EX"v4,v4\n"
MF"v[40:43],a[104:107],a[24:27],v[40:43]\n"
EX"v5,v5\n"
MF"v[48:51],a[104:107],a[40:43],v[48:51]\n"
RT"a[192:193],v237 offset:16384\n"
RT"a[194:195],v237 offset:16512\n"
PK"v24,v24,v25\n"
MF"v[48:51],a[108:111],a[44:47],v[48:51]\n"
EX"v6,v6\n"
MF"v[56:59],a[108:111],a[60:63],v[56:59]\n"
EX"v7,v7\n"
MF"v[56:59],a[104:107],a[56:59],v[56:59]\n"
EX"v12,v12\n"
PK"v25,v26,v27\n"
MF"v[36:39],a[112:115],a[0:3],0\n"
WT"lgkmcnt(2)\n"
RT"a[196:197],v237 offset:17408\n"
RT"a[198:199],v237 offset:17536\n"
MF"v[36:39],a[116:119],a[4:7],v[36:39]\n"
EX"v13,v13\n"
MF"v[44:47],a[116:119],a[20:23],0\n"
RD"a[64:67],v227 offset:16384\n"
PK"v2,v4,v5\n"
MF"v[44:47],a[112:115],a[16:19],v[44:47]\n"
EX"v14,v14\n"
MF"v[52:55],a[112:115],a[32:35],0\n"
RT"a[200:201],v237 offset:18432\n"
RT"a[202:203],v237 offset:18560\n"
MF"v[52:55],a[116:119],a[36:39],v[52:55]\n"
EX"v15,v15\n"
PK"v3,v6,v7\n"
MF"v[60:63],a[116:119],a[52:55],0\n"
RD"a[68:71],v227 offset:17408\n"
PK"v10,v12,v13\n"
MF"v[60:63],a[112:115],a[48:51],v[60:63]\n"
EX"v20,v20\n"
MF"v[36:39],a[120:123],a[8:11],v[36:39]\n"
EX"v21,v21\n"
MF"v[36:39],a[124:127],a[12:15],v[36:39]\n"
RD"a[72:75],v227 offset:18432\n"
"v_readlane_b32 s43,v228,s42\n"
MF"v[44:47],a[124:127],a[28:31],v[44:47]\n"
EX"v22,v22\n"
MF"v[44:47],a[120:123],a[24:27],v[44:47]\n"
"s_mov_b32 m0,s40\n"
PK"v11,v14,v15\n"
PK"v18,v20,v21\n"
MF"v[52:55],a[120:123],a[40:43],v[52:55]\n"
EX"v23,v23\n"
MF"v[52:55],a[124:127],a[44:47],v[52:55]\n"
RD"a[76:79],v227 offset:19456\n"
"v_readlane_b32 s44,v229,s42\n"
MF"v[60:63],a[124:127],a[60:63],v[60:63]\n"
EX"v28,v28\n"
MF"v[60:63],a[120:123],a[56:59],v[60:63]\n"
WT"lgkmcnt(2)\n"
BL"v225,s[8:11],s43 offen offset:0 sc1 lds\n"
MF"v[80:83],a[64:67],v[64:67],v[80:83]\n"
SA"s42,s42,1\n"
EX"v29,v29\n"
MF"v[112:115],a[64:67],v[68:71],v[112:115]\n"
SA"s41,s41,32768\n"
RD"a[80:83],v227 offset:20480\n"
PK"v19,v22,v23\n"
MF"v[144:147],a[64:67],v[72:75],v[144:147]\n"
"s_cmp_eq_u32 s41,163840\n"
EX"v30,v30\n"
MF"v[176:179],a[64:67],v[76:79],v[176:179]\n"
"s_cselect_b32 s41,0,s41\n"
PK"v26,v28,v29\n"
"v_add_u32 v226,s41,v224\n"
MF"v[84:87],a[68:71],v[64:67],v[84:87]\n"
EX"v31,v31\n"
MF"v[116:119],a[68:71],v[68:71],v[116:119]\n"
RD"a[84:87],v227 offset:21504\n"
PK"v27,v30,v31\n"
MF"v[148:151],a[68:71],v[72:75],v[148:151]\n"
WT"vmcnt(1)\n"
"v_cmp_eq_u64 vcc,s[28:29],v[240:241]\n"
"s_cmp_eq_u64 vcc,exec\n"
"s_cbranch_scc0 L1102_%=\n"
"L1103_%=:\n"
MF"v[180:183],a[68:71],v[76:79],v[180:183]\n"
WT"lgkmcnt(2)\n"
EX"v32,v32\n"
MF"v[88:91],a[72:75],v[64:67],v[88:91]\n"
BL"v225,s[8:11],s43 offen offset:1024 sc1 lds\n"
MF"v[120:123],a[72:75],v[68:71],v[120:123]\n"
EX"v33,v33\n"
MF"v[152:155],a[72:75],v[72:75],v[152:155]\n"
RD"a[88:91],v227 offset:22528\n"
MF"v[184:187],a[72:75],v[76:79],v[184:187]\n"
EX"v34,v34\n"
MF"v[92:95],a[76:79],v[64:67],v[92:95]\n"
EX"v35,v35\n"
MF"v[124:127],a[76:79],v[68:71],v[124:127]\n"
RD"a[92:95],v227 offset:23552\n"
MF"v[156:159],a[76:79],v[72:75],v[156:159]\n"
EX"v40,v40\n"
MF"v[188:191],a[76:79],v[76:79],v[188:191]\n"
WT"lgkmcnt(2)\n"
BL"v225,s[8:11],s43 offen offset:2048 sc1 lds\n"
MF"v[96:99],a[80:83],v[64:67],v[96:99]\n"
EX"v41,v41\n"
MF"v[128:131],a[80:83],v[68:71],v[128:131]\n"
RD"a[96:99],v227 offset:24576\n"
MF"v[160:163],a[80:83],v[72:75],v[160:163]\n"
EX"v42,v42\n"
MF"v[192:195],a[80:83],v[76:79],v[192:195]\n"
MF"v[100:103],a[84:87],v[64:67],v[100:103]\n"
EX"v43,v43\n"
MF"v[132:135],a[84:87],v[68:71],v[132:135]\n"
RD"a[100:103],v227 offset:25600\n"
MF"v[164:167],a[84:87],v[72:75],v[164:167]\n"
EX"v48,v48\n"
MF"v[196:199],a[84:87],v[76:79],v[196:199]\n"
WT"lgkmcnt(2)\n"
BL"v225,s[8:11],s43 offen offset:3072 sc1 lds\n"
MF"v[104:107],a[88:91],v[64:67],v[104:107]\n"
EX"v49,v49\n"
MF"v[136:139],a[88:91],v[68:71],v[136:139]\n"
RD"a[104:107],v227 offset:26624\n"
MF"v[168:171],a[88:91],v[72:75],v[168:171]\n"
EX"v50,v50\n"
MF"v[200:203],a[88:91],v[76:79],v[200:203]\n"
SA"m0,s40,16384\n"
MF"v[108:111],a[92:95],v[64:67],v[108:111]\n"
EX"v51,v51\n"
MF"v[140:143],a[92:95],v[68:71],v[140:143]\n"
RD"a[108:111],v227 offset:27648\n"
MF"v[172:175],a[92:95],v[72:75],v[172:175]\n"
EX"v56,v56\n"
MF"v[204:207],a[92:95],v[76:79],v[204:207]\n"
BL"v225,s[8:11],s44 offen offset:0 sc1 lds\n"
MF"a[132:135],a[128:131],v[64:67],a[132:135]\n"
SA"s40,s40,32768\n"
EX"v57,v57\n"
MF"a[136:139],a[128:131],v[68:71],a[136:139]\n"
"s_cmp_eq_u32 s40,s46\n"
RD"a[112:115],v227 offset:28672\n"
"s_cselect_b32 s40,s47,s40\n"
MF"a[140:143],a[128:131],v[72:75],a[140:143]\n"
EX"v58,v58\n"
MF"a[144:147],a[128:131],v[76:79],a[144:147]\n"
MF"v[240:243],a[148:151],a[204:207],0\n"
EX"v59,v59\n"
MF"v[240:243],a[152:155],a[208:211],v[240:243]\n"
RD"a[116:119],v227 offset:29696\n"
MF"v[240:243],a[156:159],a[212:215],v[240:243]\n"
EX"v36,v36\n"
MF"v[240:243],a[160:163],a[216:219],v[240:243]\n"
BL"v225,s[8:11],s44 offen offset:1024 sc1 lds\n"
MF"v[240:243],a[164:167],a[220:223],v[240:243]\n"
EX"v37,v37\n"
MF"v[240:243],a[168:171],a[224:227],v[240:243]\n"
RD"a[120:123],v227 offset:30720\n"
MF"v[240:243],a[172:175],a[228:231],v[240:243]\n"
EX"v38,v38\n"
MF"v[240:243],a[176:179],a[232:235],v[240:243]\n"
MF"v[240:243],a[180:183],a[236:239],v[240:243]\n"
EX"v39,v39\n"
MF"v[240:243],a[184:187],a[240:243],v[240:243]\n"
RD"a[124:127],v227 offset:31744\n"
MF"v[240:243],a[188:191],a[244:247],v[240:243]\n"
EX"v44,v44\n"
MF"v[240:243],a[192:195],a[248:251],v[240:243]\n"
BL"v225,s[8:11],s44 offen offset:2048 sc1 lds\n"
MF"v[240:243],a[196:199],a[252:255],v[240:243]\n"
EX"v45,v45\n"
MF"v[240:243],a[200:203],v[244:247],v[240:243]\n"
WT"lgkmcnt(1)\n"
MF"v[80:83],a[96:99],v[0:3],v[80:83]\n"
EX"v46,v46\n"
MF"v[112:115],a[96:99],v[8:11],v[112:115]\n"
MF"v[144:147],a[96:99],v[16:19],v[144:147]\n"
MF"v[176:179],a[96:99],v[24:27],v[176:179]\n"
MF"v[84:87],a[100:103],v[0:3],v[84:87]\n"
EX"v47,v47\n"
MF"v[116:119],a[100:103],v[8:11],v[116:119]\n"
"ds_write_b128 v236,v[240:243]\n"
MF"v[148:151],a[100:103],v[16:19],v[148:151]\n"
BL"v225,s[8:11],s44 offen offset:3072 sc1 lds\n"
MF"v[180:183],a[100:103],v[24:27],v[180:183]\n"
EX"v52,v52\n"
MF"v[88:91],a[104:107],v[0:3],v[88:91]\n"
EX"v53,v53\n"
MF"v[120:123],a[104:107],v[8:11],v[120:123]\n"
MF"v[152:155],a[104:107],v[16:19],v[152:155]\n"
EX"v54,v54\n"
MF"v[184:187],a[104:107],v[24:27],v[184:187]\n"
MF"v[92:95],a[108:111],v[0:3],v[92:95]\n"
EX"v55,v55\n"
MF"v[124:127],a[108:111],v[8:11],v[124:127]\n"
MF"v[156:159],a[108:111],v[16:19],v[156:159]\n"
EX"v60,v60\n"
MF"v[188:191],a[108:111],v[24:27],v[188:191]\n"
WT"lgkmcnt(0)\n"
MF"v[96:99],a[112:115],v[0:3],v[96:99]\n"
EX"v61,v61\n"
MF"v[128:131],a[112:115],v[8:11],v[128:131]\n"
"s_barrier\n"
MF"v[160:163],a[112:115],v[16:19],v[160:163]\n"
RD"a[64:67],v226 offset:0\n"
MF"v[192:195],a[112:115],v[24:27],v[192:195]\n"
EX"v62,v62\n"
MF"v[100:103],a[116:119],v[0:3],v[100:103]\n"
RD"v[240:243],v235 offset:0\n"
RD"v[244:247],v235 offset:1024\n"
MF"v[132:135],a[116:119],v[8:11],v[132:135]\n"
EX"v63,v63\n"
MF"v[164:167],a[116:119],v[16:19],v[164:167]\n"
RD"a[68:71],v226 offset:1024\n"
RD"v[248:251],v235 offset:2048\n"
MF"v[196:199],a[116:119],v[24:27],v[196:199]\n"
RD"a[72:75],v226 offset:2048\n"
MF"v[104:107],a[120:123],v[0:3],v[104:107]\n"
RD"v[252:255],v235 offset:3072\n"
MF"v[136:139],a[120:123],v[8:11],v[136:139]\n"
RD"a[76:79],v226 offset:3072\n"
MF"v[168:171],a[120:123],v[16:19],v[168:171]\n"
MF"v[200:203],a[120:123],v[24:27],v[200:203]\n"
MF"v[108:111],a[124:127],v[0:3],v[108:111]\n"
MF"v[140:143],a[124:127],v[8:11],v[140:143]\n"
RD"a[80:83],v226 offset:4096\n"
MF"v[172:175],a[124:127],v[16:19],v[172:175]\n"
MF"v[204:207],a[124:127],v[24:27],v[204:207]\n"
RD"a[84:87],v226 offset:5120\n"
MF"a[132:135],a[128:131],v[0:3],a[132:135]\n"
MF"a[136:139],a[128:131],v[8:11],a[136:139]\n"
RD"a[88:91],v226 offset:6144\n"
MF"a[140:143],a[128:131],v[16:19],a[140:143]\n"
WT"lgkmcnt(7)\n"
MF"a[144:147],a[128:131],v[24:27],a[144:147]\n"
"s_cmp_eq_u32 s57,2\n"
"s_cbranch_scc1 LMD_%=\n"
"s_cmp_eq_u32 s34,2\n"
"s_cbranch_scc1 LMA_%=\n"
MF"v[0:3],a[64:67],a[0:3],0\n"
"v_add_f32 v240,v240,v244\n"
PK"v64,v32,v33\n"
MF"v[0:3],a[68:71],a[4:7],v[0:3]\n"
PK"v65,v34,v35\n"
PK"v66,v36,v37\n"
MF"v[8:11],a[68:71],a[20:23],0\n"
WT"lgkmcnt(4)\n"
"v_add_f32 v248,v248,v252\n"
PK"v67,v38,v39\n"
MF"v[8:11],a[64:67],a[16:19],v[8:11]\n"
"v_add_f32 v241,v241,v245\n"
PK"v68,v40,v41\n"
MF"v[16:19],a[64:67],a[32:35],0\n"
PK"v69,v42,v43\n"
"v_add_f32 v249,v249,v253\n"
MF"v[16:19],a[68:71],a[36:39],v[16:19]\n"
RD"a[92:95],v226 offset:7168\n"
PK"v70,v44,v45\n"
MF"v[24:27],a[68:71],a[52:55],0\n"
PK"v71,v46,v47\n"
"v_add_f32 v242,v242,v246\n"
MF"v[24:27],a[64:67],a[48:51],v[24:27]\n"
PK"v72,v48,v49\n"
PK"v73,v50,v51\n"
MF"v[0:3],a[72:75],a[8:11],v[0:3]\n"
WT"lgkmcnt(1)\n"
"v_add_f32 v250,v250,v254\n"
PK"v74,v52,v53\n"
MF"v[0:3],a[76:79],a[12:15],v[0:3]\n"
PK"v75,v54,v55\n"
"v_add_f32 v243,v243,v247\n"
MF"v[8:11],a[76:79],a[28:31],v[8:11]\n"
PK"v76,v56,v57\n"
PK"v77,v58,v59\n"
MF"v[8:11],a[72:75],a[24:27],v[8:11]\n"
"v_add_f32 v251,v251,v255\n"
PK"v78,v60,v61\n"
MF"v[16:19],a[72:75],a[40:43],v[16:19]\n"
PK"v79,v62,v63\n"
PK"v240,v240,v240\n"
MF"v[16:19],a[76:79],a[44:47],v[16:19]\n"
RD"a[96:99],v226 offset:8192\n"
"v_lshlrev_b32 v240,16,v240\n"
MF"v[24:27],a[76:79],a[60:63],v[24:27]\n"
EX"v0,v0\n"
MF"v[24:27],a[72:75],a[56:59],v[24:27]\n"
PK"v241,v241,v241\n"
"v_lshlrev_b32 v241,16,v241\n"
MF"v[4:7],a[80:83],a[0:3],0\n"
EX"v1,v1\n"
MF"v[4:7],a[84:87],a[4:7],v[4:7]\n"
RD"a[100:103],v226 offset:9216\n"
PK"v242,v242,v242\n"
MF"v[12:15],a[84:87],a[20:23],0\n"
EX"v2,v2\n"
MF"v[12:15],a[80:83],a[16:19],v[12:15]\n"
EX"v3,v3\n"
MF"v[20:23],a[80:83],a[32:35],0\n"
EX"v8,v8\n"
MF"v[20:23],a[84:87],a[36:39],v[20:23]\n"
RD"a[104:107],v226 offset:10240\n"
"v_lshlrev_b32 v242,16,v242\n"
MF"v[28:31],a[84:87],a[52:55],0\n"
EX"v9,v9\n"
PK"v243,v243,v243\n"
MF"v[28:31],a[80:83],a[48:51],v[28:31]\n"
"v_lshlrev_b32 v243,16,v243\n"
MU"v244,0xbfb8aa3b,v240\n"
EX"v244,v244\n"
MF"v[4:7],a[88:91],a[8:11],v[4:7]\n"
WT"lgkmcnt(1)\n"
EX"v10,v10\n"
MF"v[4:7],a[92:95],a[12:15],v[4:7]\n"
RD"a[108:111],v226 offset:11264\n"
MU"v245,0xbfb8aa3b,v241\n"
EX"v245,v245\n"
MF"v[12:15],a[92:95],a[28:31],v[12:15]\n"
EX"v11,v11\n"
PK"v0,v0,v1\n"
MF"v[12:15],a[88:91],a[24:27],v[12:15]\n"
EX"v16,v16\n"
MU"v246,0xbfb8aa3b,v242\n"
MF"v[20:23],a[88:91],a[40:43],v[20:23]\n"
EX"v246,v246\n"
MU"v247,0xbfb8aa3b,v243\n"
MF"v[20:23],a[92:95],a[44:47],v[20:23]\n"
EX"v17,v17\n"
PK"v1,v2,v3\n"
MF"v[28:31],a[92:95],a[60:63],v[28:31]\n"
EX"v247,v247\n"
PK"v248,v248,v248\n"
MF"v[28:31],a[88:91],a[56:59],v[28:31]\n"
RD"a[112:115],v226 offset:12288\n"
"v_lshlrev_b32 v248,16,v248\n"
PK"v8,v8,v9\n"
MF"v[32:35],a[96:99],a[0:3],0\n"
EX"v18,v18\n"
PK"v249,v249,v249\n"
MF"v[32:35],a[100:103],a[4:7],v[32:35]\n"
EX"v19,v19\n"
"v_lshlrev_b32 v249,16,v249\n"
MF"v[40:43],a[100:103],a[20:23],0\n"
RD"a[116:119],v226 offset:13312\n"
PK"v250,v250,v250\n"
MF"v[40:43],a[96:99],a[16:19],v[40:43]\n"
EX"v24,v24\n"
PK"v9,v10,v11\n"
MF"v[48:51],a[96:99],a[32:35],0\n"
EX"v25,v25\n"
"v_lshlrev_b32 v250,16,v250\n"
MF"v[48:51],a[100:103],a[36:39],v[48:51]\n"
EX"v26,v26\n"
PK"v16,v16,v17\n"
MF"v[56:59],a[100:103],a[52:55],0\n"
RD"a[120:123],v226 offset:14336\n"
PK"v251,v251,v251\n"
"v_mov_b32 v227,v226\n"
MF"v[56:59],a[96:99],a[48:51],v[56:59]\n"
WT"lgkmcnt(2)\n"
EX"v27,v27\n"
"v_lshlrev_b32 v251,16,v251\n"
MF"v[32:35],a[104:107],a[8:11],v[32:35]\n"
EX"v4,v4\n"
"v_add_f32 v244,1.0,v244\n"
MF"v[32:35],a[108:111],a[12:15],v[32:35]\n"
"v_rcp_f32 v244,v244\n"
"v_add_f32 v245,1.0,v245\n"
MF"v[40:43],a[108:111],a[28:31],v[40:43]\n"
"v_rcp_f32 v245,v245\n"
PK"v17,v18,v19\n"
MF"v[40:43],a[104:107],a[24:27],v[40:43]\n"
RD"a[124:127],v226 offset:15360\n"
"v_add_f32 v246,1.0,v246\n"
"v_rcp_f32 v246,v246\n"
MF"v[48:51],a[104:107],a[40:43],v[48:51]\n"
EX"v5,v5\n"
"v_add_f32 v247,1.0,v247\n"
MF"v[48:51],a[108:111],a[44:47],v[48:51]\n"
"v_rcp_f32 v247,v247\n"
EX"v6,v6\n"
MF"v[56:59],a[108:111],a[60:63],v[56:59]\n"
EX"v7,v7\n"
PK"v24,v24,v25\n"
MF"v[56:59],a[104:107],a[56:59],v[56:59]\n"
EX"v12,v12\n"
PK"v25,v26,v27\n"
MF"v[36:39],a[112:115],a[0:3],0\n"
WT"lgkmcnt(1)\n"
EX"v13,v13\n"
MU"v240,v240,v244\n"
MF"v[36:39],a[116:119],a[4:7],v[36:39]\n"
RD"a[64:67],v227 offset:16384\n"
MU"v241,v241,v245\n"
MF"v[44:47],a[116:119],a[20:23],0\n"
EX"v14,v14\n"
MU"v242,v242,v246\n"
MF"v[44:47],a[112:115],a[16:19],v[44:47]\n"
MU"v243,v243,v247\n"
PK"v2,v4,v5\n"
MF"v[52:55],a[112:115],a[32:35],0\n"
EX"v15,v15\n"
PK"v240,v240,v240\n"
MF"v[52:55],a[116:119],a[36:39],v[52:55]\n"
RD"a[68:71],v227 offset:17408\n"
"v_lshlrev_b32 v240,16,v240\n"
PK"v3,v6,v7\n"
MF"v[60:63],a[116:119],a[52:55],0\n"
EX"v20,v20\n"
PK"v241,v241,v241\n"
MF"v[60:63],a[112:115],a[48:51],v[60:63]\n"
"v_readlane_b32 s43,v228,s42\n"
"v_lshlrev_b32 v241,16,v241\n"
MF"v[36:39],a[120:123],a[8:11],v[36:39]\n"
WT"lgkmcnt(1)\n"
EX"v21,v21\n"
PK"v242,v242,v242\n"
MF"v[36:39],a[124:127],a[12:15],v[36:39]\n"
RD"a[72:75],v227 offset:18432\n"
"v_lshlrev_b32 v242,16,v242\n"
PK"v10,v12,v13\n"
MF"v[44:47],a[124:127],a[28:31],v[44:47]\n"
EX"v22,v22\n"
PK"v243,v243,v243\n"
MF"v[44:47],a[120:123],a[24:27],v[44:47]\n"
"s_mov_b32 m0,s40\n"
"v_lshlrev_b32 v243,16,v243\n"
MU"v240,v240,v248\n"
MF"v[52:55],a[120:123],a[40:43],v[52:55]\n"
EX"v23,v23\n"
MF"v[52:55],a[124:127],a[44:47],v[52:55]\n"
RD"a[76:79],v227 offset:19456\n"
"v_readlane_b32 s44,v229,s42\n"
MU"v241,v241,v249\n"
MF"v[60:63],a[124:127],a[60:63],v[60:63]\n"
EX"v28,v28\n"
PK"v11,v14,v15\n"
MF"v[60:63],a[120:123],a[56:59],v[60:63]\n"
SA"s42,s42,1\n"
BL"v225,s[8:11],s43 offen offset:0 sc1 lds\n"
MU"v242,v242,v250\n"
MF"v[80:83],a[64:67],v[64:67],v[80:83]\n"
SA"s41,s41,32768\n"
EX"v29,v29\n"
MU"v243,v243,v251\n"
MF"v[112:115],a[64:67],v[68:71],v[112:115]\n"
"s_cmp_eq_u32 s41,163840\n"
RD"a[80:83],v227 offset:20480\n"
PK"v240,v240,v241\n"
PK"v241,v242,v243\n"
MF"v[144:147],a[64:67],v[72:75],v[144:147]\n"
"s_cselect_b32 s41,0,s41\n"
EX"v30,v30\n"
PK"v18,v20,v21\n"
MF"v[176:179],a[64:67],v[76:79],v[176:179]\n"
WT"lgkmcnt(2)\n"
"v_add_u32 v226,s41,v224\n"
"v_lshrrev_b32 v239,9,v224\n"
"v_lshlrev_b32 v239,8,v239\n"
MF"v[84:87],a[68:71],v[64:67],v[84:87]\n"
EX"v31,v31\n"
"v_bfe_u32 v237,v224,8,1\n"
MF"v[116:119],a[68:71],v[68:71],v[116:119]\n"
RD"a[84:87],v227 offset:21504\n"
"v_lshl_add_u32 v239,v237,3,v239\n"
PK"v19,v22,v23\n"
MF"v[148:151],a[68:71],v[72:75],v[148:151]\n"
EX"v32,v32\n"
"v_and_b32 v237,0x80,v224\n"
MF"v[180:183],a[68:71],v[76:79],v[180:183]\n"
BL"v225,s[8:11],s43 offen offset:1024 sc1 lds\n"
"v_lshl_add_u32 v239,v237,23,v239\n"
MF"v[88:91],a[72:75],v[64:67],v[88:91]\n"
EX"v33,v33\n"
"v_and_b32 v237,0x70,v224\n"
MF"v[120:123],a[72:75],v[68:71],v[120:123]\n"
RD"a[88:91],v227 offset:22528\n"
"v_add_u32 v239,v239,v237\n"
PK"v26,v28,v29\n"
MF"v[152:155],a[72:75],v[72:75],v[152:155]\n"
"buffer_store_dwordx2 v[240:241],v239,s[8:11],s93 offen sc1\n"
MF"v[184:187],a[72:75],v[76:79],v[184:187]\n"
WT"lgkmcnt(2)\n"
EX"v34,v34\n"
MF"v[92:95],a[76:79],v[64:67],v[92:95]\n"
EX"v35,v35\n"
MF"v[124:127],a[76:79],v[68:71],v[124:127]\n"
RD"a[92:95],v227 offset:23552\n"
PK"v27,v30,v31\n"
MF"v[156:159],a[76:79],v[72:75],v[156:159]\n"
EX"v40,v40\n"
MF"v[188:191],a[76:79],v[76:79],v[188:191]\n"
BL"v225,s[8:11],s43 offen offset:2048 sc1 lds\n"
MF"v[96:99],a[80:83],v[64:67],v[96:99]\n"
EX"v41,v41\n"
MF"v[128:131],a[80:83],v[68:71],v[128:131]\n"
RD"a[96:99],v227 offset:24576\n"
MF"v[160:163],a[80:83],v[72:75],v[160:163]\n"
EX"v42,v42\n"
MF"v[192:195],a[80:83],v[76:79],v[192:195]\n"
WT"lgkmcnt(2)\n"
MF"v[100:103],a[84:87],v[64:67],v[100:103]\n"
EX"v43,v43\n"
MF"v[132:135],a[84:87],v[68:71],v[132:135]\n"
RD"a[100:103],v227 offset:25600\n"
MF"v[164:167],a[84:87],v[72:75],v[164:167]\n"
EX"v48,v48\n"
MF"v[196:199],a[84:87],v[76:79],v[196:199]\n"
BL"v225,s[8:11],s43 offen offset:3072 sc1 lds\n"
MF"v[104:107],a[88:91],v[64:67],v[104:107]\n"
EX"v49,v49\n"
MF"v[136:139],a[88:91],v[68:71],v[136:139]\n"
RD"a[104:107],v227 offset:26624\n"
MF"v[168:171],a[88:91],v[72:75],v[168:171]\n"
EX"v50,v50\n"
MF"v[200:203],a[88:91],v[76:79],v[200:203]\n"
WT"lgkmcnt(2)\n"
SA"m0,s40,16384\n"
MF"v[108:111],a[92:95],v[64:67],v[108:111]\n"
EX"v51,v51\n"
MF"v[140:143],a[92:95],v[68:71],v[140:143]\n"
RD"a[108:111],v227 offset:27648\n"
MF"v[172:175],a[92:95],v[72:75],v[172:175]\n"
EX"v56,v56\n"
MF"v[204:207],a[92:95],v[76:79],v[204:207]\n"
BL"v225,s[8:11],s44 offen offset:0 sc1 lds\n"
MF"a[132:135],a[128:131],v[64:67],a[132:135]\n"
SA"s40,s40,32768\n"
EX"v57,v57\n"
MF"a[136:139],a[128:131],v[68:71],a[136:139]\n"
"s_cmp_eq_u32 s40,s46\n"
RD"a[112:115],v227 offset:28672\n"
"s_cselect_b32 s40,s47,s40\n"
MF"a[140:143],a[128:131],v[72:75],a[140:143]\n"
EX"v58,v58\n"
MF"a[144:147],a[128:131],v[76:79],a[144:147]\n"
MF"v[80:83],a[96:99],v[0:3],v[80:83]\n"
EX"v59,v59\n"
MF"v[112:115],a[96:99],v[8:11],v[112:115]\n"
RD"a[116:119],v227 offset:29696\n"
MF"v[144:147],a[96:99],v[16:19],v[144:147]\n"
EX"v36,v36\n"
MF"v[176:179],a[96:99],v[24:27],v[176:179]\n"
WT"lgkmcnt(2)\n"
BL"v225,s[8:11],s44 offen offset:1024 sc1 lds\n"
MF"v[84:87],a[100:103],v[0:3],v[84:87]\n"
EX"v37,v37\n"
MF"v[116:119],a[100:103],v[8:11],v[116:119]\n"
RD"a[120:123],v227 offset:30720\n"
MF"v[148:151],a[100:103],v[16:19],v[148:151]\n"
EX"v38,v38\n"
MF"v[180:183],a[100:103],v[24:27],v[180:183]\n"
MF"v[88:91],a[104:107],v[0:3],v[88:91]\n"
EX"v39,v39\n"
MF"v[120:123],a[104:107],v[8:11],v[120:123]\n"
RD"a[124:127],v227 offset:31744\n"
MF"v[152:155],a[104:107],v[16:19],v[152:155]\n"
EX"v44,v44\n"
MF"v[184:187],a[104:107],v[24:27],v[184:187]\n"
BL"v225,s[8:11],s44 offen offset:2048 sc1 lds\n"
MF"v[92:95],a[108:111],v[0:3],v[92:95]\n"
EX"v45,v45\n"
MF"v[124:127],a[108:111],v[8:11],v[124:127]\n"
MF"v[156:159],a[108:111],v[16:19],v[156:159]\n"
EX"v46,v46\n"
MF"v[188:191],a[108:111],v[24:27],v[188:191]\n"
WT"lgkmcnt(1)\n"
WT"lgkmcnt(0)\n"
MF"v[96:99],a[112:115],v[0:3],v[96:99]\n"
"s_barrier\n"
MF"v[128:131],a[112:115],v[8:11],v[128:131]\n"
RD"a[64:67],v226 offset:0\n"
MF"v[160:163],a[112:115],v[16:19],v[160:163]\n"
EX"v47,v47\n"
MF"v[192:195],a[112:115],v[24:27],v[192:195]\n"
BL"v225,s[8:11],s44 offen offset:3072 sc1 lds\n"
MF"v[100:103],a[116:119],v[0:3],v[100:103]\n"
EX"v52,v52\n"
MF"v[132:135],a[116:119],v[8:11],v[132:135]\n"
RD"a[68:71],v226 offset:1024\n"
MF"v[164:167],a[116:119],v[16:19],v[164:167]\n"
EX"v53,v53\n"
MF"v[196:199],a[116:119],v[24:27],v[196:199]\n"
RD"a[72:75],v226 offset:2048\n"
MF"v[104:107],a[120:123],v[0:3],v[104:107]\n"
EX"v54,v54\n"
MF"v[136:139],a[120:123],v[8:11],v[136:139]\n"
RD"a[76:79],v226 offset:3072\n"
MF"v[168:171],a[120:123],v[16:19],v[168:171]\n"
EX"v55,v55\n"
MF"v[200:203],a[120:123],v[24:27],v[200:203]\n"
MF"v[108:111],a[124:127],v[0:3],v[108:111]\n"
WT"vmcnt(6)\n"
"buffer_store_dwordx2 v[232:233],v230,s[8:11],s94 offen sc1\n"
MF"v[140:143],a[124:127],v[8:11],v[140:143]\n"
EX"v60,v60\n"
MF"v[172:175],a[124:127],v[16:19],v[172:175]\n"
RD"a[80:83],v226 offset:4096\n"
MF"v[204:207],a[124:127],v[24:27],v[204:207]\n"
EX"v61,v61\n"
MF"a[132:135],a[128:131],v[0:3],a[132:135]\n"
RD"a[84:87],v226 offset:5120\n"
MF"a[136:139],a[128:131],v[8:11],a[136:139]\n"
EX"v62,v62\n"
MF"a[140:143],a[128:131],v[16:19],a[140:143]\n"
WT"lgkmcnt(4)\n"
RD"a[88:91],v226 offset:6144\n"
MF"a[144:147],a[128:131],v[24:27],a[144:147]\n"
EX"v63,v63\n"
"s_mov_b32 s57,0\n"
"s_lshl_b32 s56,s34,1\n"
"s_sub_u32 s56,s56,6\n"
"s_branch LP1_%=\n"
".p2align 6\n"
"LMA_%=:\n"
MF"v[0:3],a[64:67],a[0:3],v[208:211]\n"
"v_add_f32 v240,v240,v244\n"
PK"v64,v32,v33\n"
MF"v[0:3],a[68:71],a[4:7],v[0:3]\n"
PK"v65,v34,v35\n"
PK"v66,v36,v37\n"
MF"v[8:11],a[68:71],a[20:23],0\n"
WT"lgkmcnt(4)\n"
"v_add_f32 v248,v248,v252\n"
PK"v67,v38,v39\n"
MF"v[8:11],a[64:67],a[16:19],v[8:11]\n"
"v_add_f32 v241,v241,v245\n"
PK"v68,v40,v41\n"
MF"v[16:19],a[64:67],a[32:35],0\n"
PK"v69,v42,v43\n"
"v_add_f32 v249,v249,v253\n"
MF"v[16:19],a[68:71],a[36:39],v[16:19]\n"
RD"a[92:95],v226 offset:7168\n"
PK"v70,v44,v45\n"
MF"v[24:27],a[68:71],a[52:55],0\n"
PK"v71,v46,v47\n"
"v_add_f32 v242,v242,v246\n"
MF"v[24:27],a[64:67],a[48:51],v[24:27]\n"
PK"v72,v48,v49\n"
PK"v73,v50,v51\n"
MF"v[0:3],a[72:75],a[8:11],v[0:3]\n"
WT"lgkmcnt(1)\n"
"v_add_f32 v250,v250,v254\n"
PK"v74,v52,v53\n"
MF"v[0:3],a[76:79],a[12:15],v[0:3]\n"
PK"v75,v54,v55\n"
"v_add_f32 v243,v243,v247\n"
MF"v[8:11],a[76:79],a[28:31],v[8:11]\n"
PK"v76,v56,v57\n"
PK"v77,v58,v59\n"
MF"v[8:11],a[72:75],a[24:27],v[8:11]\n"
"v_add_f32 v251,v251,v255\n"
PK"v78,v60,v61\n"
MF"v[16:19],a[72:75],a[40:43],v[16:19]\n"
PK"v79,v62,v63\n"
PK"v240,v240,v240\n"
MF"v[16:19],a[76:79],a[44:47],v[16:19]\n"
RD"a[96:99],v226 offset:8192\n"
"v_lshlrev_b32 v240,16,v240\n"
MF"v[24:27],a[76:79],a[60:63],v[24:27]\n"
EX"v0,v0\n"
MF"v[24:27],a[72:75],a[56:59],v[24:27]\n"
PK"v241,v241,v241\n"
"v_lshlrev_b32 v241,16,v241\n"
MF"v[4:7],a[80:83],a[0:3],v[212:215]\n"
EX"v1,v1\n"
MF"v[4:7],a[84:87],a[4:7],v[4:7]\n"
RD"a[100:103],v226 offset:9216\n"
PK"v242,v242,v242\n"
MF"v[12:15],a[84:87],a[20:23],0\n"
EX"v2,v2\n"
MF"v[12:15],a[80:83],a[16:19],v[12:15]\n"
EX"v3,v3\n"
MF"v[20:23],a[80:83],a[32:35],0\n"
EX"v8,v8\n"
MF"v[20:23],a[84:87],a[36:39],v[20:23]\n"
RD"a[104:107],v226 offset:10240\n"
"v_lshlrev_b32 v242,16,v242\n"
MF"v[28:31],a[84:87],a[52:55],0\n"
EX"v9,v9\n"
PK"v243,v243,v243\n"
MF"v[28:31],a[80:83],a[48:51],v[28:31]\n"
"v_lshlrev_b32 v243,16,v243\n"
MU"v244,0xbfb8aa3b,v240\n"
EX"v244,v244\n"
MF"v[4:7],a[88:91],a[8:11],v[4:7]\n"
WT"lgkmcnt(1)\n"
EX"v10,v10\n"
MF"v[4:7],a[92:95],a[12:15],v[4:7]\n"
RD"a[108:111],v226 offset:11264\n"
MU"v245,0xbfb8aa3b,v241\n"
EX"v245,v245\n"
MF"v[12:15],a[92:95],a[28:31],v[12:15]\n"
EX"v11,v11\n"
PK"v0,v0,v1\n"
MF"v[12:15],a[88:91],a[24:27],v[12:15]\n"
EX"v16,v16\n"
MU"v246,0xbfb8aa3b,v242\n"
MF"v[20:23],a[88:91],a[40:43],v[20:23]\n"
EX"v246,v246\n"
MU"v247,0xbfb8aa3b,v243\n"
MF"v[20:23],a[92:95],a[44:47],v[20:23]\n"
EX"v17,v17\n"
PK"v1,v2,v3\n"
MF"v[28:31],a[92:95],a[60:63],v[28:31]\n"
EX"v247,v247\n"
PK"v248,v248,v248\n"
MF"v[28:31],a[88:91],a[56:59],v[28:31]\n"
RD"a[112:115],v226 offset:12288\n"
"v_lshlrev_b32 v248,16,v248\n"
PK"v8,v8,v9\n"
MF"v[32:35],a[96:99],a[0:3],v[216:219]\n"
EX"v18,v18\n"
PK"v249,v249,v249\n"
MF"v[32:35],a[100:103],a[4:7],v[32:35]\n"
EX"v19,v19\n"
"v_lshlrev_b32 v249,16,v249\n"
MF"v[40:43],a[100:103],a[20:23],0\n"
RD"a[116:119],v226 offset:13312\n"
PK"v250,v250,v250\n"
MF"v[40:43],a[96:99],a[16:19],v[40:43]\n"
EX"v24,v24\n"
PK"v9,v10,v11\n"
MF"v[48:51],a[96:99],a[32:35],0\n"
EX"v25,v25\n"
"v_lshlrev_b32 v250,16,v250\n"
MF"v[48:51],a[100:103],a[36:39],v[48:51]\n"
EX"v26,v26\n"
PK"v16,v16,v17\n"
MF"v[56:59],a[100:103],a[52:55],0\n"
RD"a[120:123],v226 offset:14336\n"
PK"v251,v251,v251\n"
"v_mov_b32 v227,v226\n"
MF"v[56:59],a[96:99],a[48:51],v[56:59]\n"
WT"lgkmcnt(2)\n"
EX"v27,v27\n"
"v_lshlrev_b32 v251,16,v251\n"
MF"v[32:35],a[104:107],a[8:11],v[32:35]\n"
EX"v4,v4\n"
"v_add_f32 v244,1.0,v244\n"
MF"v[32:35],a[108:111],a[12:15],v[32:35]\n"
"v_rcp_f32 v244,v244\n"
"v_add_f32 v245,1.0,v245\n"
MF"v[40:43],a[108:111],a[28:31],v[40:43]\n"
"v_rcp_f32 v245,v245\n"
PK"v17,v18,v19\n"
MF"v[40:43],a[104:107],a[24:27],v[40:43]\n"
RD"a[124:127],v226 offset:15360\n"
"v_add_f32 v246,1.0,v246\n"
"v_rcp_f32 v246,v246\n"
MF"v[48:51],a[104:107],a[40:43],v[48:51]\n"
EX"v5,v5\n"
"v_add_f32 v247,1.0,v247\n"
MF"v[48:51],a[108:111],a[44:47],v[48:51]\n"
"v_rcp_f32 v247,v247\n"
EX"v6,v6\n"
MF"v[56:59],a[108:111],a[60:63],v[56:59]\n"
EX"v7,v7\n"
PK"v24,v24,v25\n"
MF"v[56:59],a[104:107],a[56:59],v[56:59]\n"
EX"v12,v12\n"
PK"v25,v26,v27\n"
MF"v[36:39],a[112:115],a[0:3],v[220:223]\n"
WT"lgkmcnt(1)\n"
EX"v13,v13\n"
MU"v240,v240,v244\n"
MF"v[36:39],a[116:119],a[4:7],v[36:39]\n"
RD"a[64:67],v227 offset:16384\n"
MU"v241,v241,v245\n"
MF"v[44:47],a[116:119],a[20:23],0\n"
EX"v14,v14\n"
MU"v242,v242,v246\n"
MF"v[44:47],a[112:115],a[16:19],v[44:47]\n"
MU"v243,v243,v247\n"
PK"v2,v4,v5\n"
MF"v[52:55],a[112:115],a[32:35],0\n"
EX"v15,v15\n"
PK"v240,v240,v240\n"
MF"v[52:55],a[116:119],a[36:39],v[52:55]\n"
RD"a[68:71],v227 offset:17408\n"
"v_lshlrev_b32 v240,16,v240\n"
PK"v3,v6,v7\n"
MF"v[60:63],a[116:119],a[52:55],0\n"
EX"v20,v20\n"
PK"v241,v241,v241\n"
MF"v[60:63],a[112:115],a[48:51],v[60:63]\n"
"v_readlane_b32 s43,v228,s42\n"
"v_lshlrev_b32 v241,16,v241\n"
MF"v[36:39],a[120:123],a[8:11],v[36:39]\n"
WT"lgkmcnt(1)\n"
EX"v21,v21\n"
PK"v242,v242,v242\n"
MF"v[36:39],a[124:127],a[12:15],v[36:39]\n"
RD"a[72:75],v227 offset:18432\n"
"v_lshlrev_b32 v242,16,v242\n"
PK"v10,v12,v13\n"
MF"v[44:47],a[124:127],a[28:31],v[44:47]\n"
EX"v22,v22\n"
PK"v243,v243,v243\n"
MF"v[44:47],a[120:123],a[24:27],v[44:47]\n"
"s_mov_b32 m0,s40\n"
"v_lshlrev_b32 v243,16,v243\n"
MU"v240,v240,v248\n"
MF"v[52:55],a[120:123],a[40:43],v[52:55]\n"
EX"v23,v23\n"
MF"v[52:55],a[124:127],a[44:47],v[52:55]\n"
RD"a[76:79],v227 offset:19456\n"
"v_readlane_b32 s44,v229,s42\n"
MU"v241,v241,v249\n"
MF"v[60:63],a[124:127],a[60:63],v[60:63]\n"
EX"v28,v28\n"
PK"v11,v14,v15\n"
MF"v[60:63],a[120:123],a[56:59],v[60:63]\n"
SA"s42,s42,1\n"
BL"v225,s[8:11],s43 offen offset:0 sc1 lds\n"
MU"v242,v242,v250\n"
MF"v[80:83],a[64:67],v[64:67],v[80:83]\n"
SA"s41,s41,32768\n"
EX"v29,v29\n"
MU"v243,v243,v251\n"
MF"v[112:115],a[64:67],v[68:71],v[112:115]\n"
"s_cmp_eq_u32 s41,163840\n"
RD"a[80:83],v227 offset:20480\n"
PK"v240,v240,v241\n"
PK"v241,v242,v243\n"
MF"v[144:147],a[64:67],v[72:75],v[144:147]\n"
"s_cselect_b32 s41,0,s41\n"
EX"v30,v30\n"
PK"v18,v20,v21\n"
MF"v[176:179],a[64:67],v[76:79],v[176:179]\n"
WT"lgkmcnt(2)\n"
"v_add_u32 v226,s41,v224\n"
"v_lshrrev_b32 v239,9,v224\n"
"v_lshlrev_b32 v239,8,v239\n"
MF"v[84:87],a[68:71],v[64:67],v[84:87]\n"
EX"v31,v31\n"
"v_bfe_u32 v237,v224,8,1\n"
MF"v[116:119],a[68:71],v[68:71],v[116:119]\n"
RD"a[84:87],v227 offset:21504\n"
"v_lshl_add_u32 v239,v237,3,v239\n"
PK"v19,v22,v23\n"
MF"v[148:151],a[68:71],v[72:75],v[148:151]\n"
EX"v32,v32\n"
"v_and_b32 v237,0x80,v224\n"
MF"v[180:183],a[68:71],v[76:79],v[180:183]\n"
BL"v225,s[8:11],s43 offen offset:1024 sc1 lds\n"
"v_lshl_add_u32 v239,v237,23,v239\n"
MF"v[88:91],a[72:75],v[64:67],v[88:91]\n"
EX"v33,v33\n"
"v_and_b32 v237,0x70,v224\n"
MF"v[120:123],a[72:75],v[68:71],v[120:123]\n"
RD"a[88:91],v227 offset:22528\n"
"v_add_u32 v239,v239,v237\n"
PK"v26,v28,v29\n"
MF"v[152:155],a[72:75],v[72:75],v[152:155]\n"
"buffer_store_dwordx2 v[240:241],v239,s[8:11],s93 offen sc1\n"
MF"v[184:187],a[72:75],v[76:79],v[184:187]\n"
WT"lgkmcnt(2)\n"
EX"v34,v34\n"
MF"v[92:95],a[76:79],v[64:67],v[92:95]\n"
EX"v35,v35\n"
MF"v[124:127],a[76:79],v[68:71],v[124:127]\n"
RD"a[92:95],v227 offset:23552\n"
PK"v27,v30,v31\n"
MF"v[156:159],a[76:79],v[72:75],v[156:159]\n"
EX"v40,v40\n"
MF"v[188:191],a[76:79],v[76:79],v[188:191]\n"
BL"v225,s[8:11],s43 offen offset:2048 sc1 lds\n"
MF"v[96:99],a[80:83],v[64:67],v[96:99]\n"
EX"v41,v41\n"
MF"v[128:131],a[80:83],v[68:71],v[128:131]\n"
RD"a[96:99],v227 offset:24576\n"
MF"v[160:163],a[80:83],v[72:75],v[160:163]\n"
EX"v42,v42\n"
MF"v[192:195],a[80:83],v[76:79],v[192:195]\n"
WT"lgkmcnt(2)\n"
MF"v[100:103],a[84:87],v[64:67],v[100:103]\n"
EX"v43,v43\n"
MF"v[132:135],a[84:87],v[68:71],v[132:135]\n"
RD"a[100:103],v227 offset:25600\n"
MF"v[164:167],a[84:87],v[72:75],v[164:167]\n"
EX"v48,v48\n"
MF"v[196:199],a[84:87],v[76:79],v[196:199]\n"
BL"v225,s[8:11],s43 offen offset:3072 sc1 lds\n"
MF"v[104:107],a[88:91],v[64:67],v[104:107]\n"
EX"v49,v49\n"
MF"v[136:139],a[88:91],v[68:71],v[136:139]\n"
RD"a[104:107],v227 offset:26624\n"
MF"v[168:171],a[88:91],v[72:75],v[168:171]\n"
EX"v50,v50\n"
MF"v[200:203],a[88:91],v[76:79],v[200:203]\n"
WT"lgkmcnt(2)\n"
SA"m0,s40,16384\n"
MF"v[108:111],a[92:95],v[64:67],v[108:111]\n"
EX"v51,v51\n"
MF"v[140:143],a[92:95],v[68:71],v[140:143]\n"
RD"a[108:111],v227 offset:27648\n"
MF"v[172:175],a[92:95],v[72:75],v[172:175]\n"
EX"v56,v56\n"
MF"v[204:207],a[92:95],v[76:79],v[204:207]\n"
BL"v225,s[8:11],s44 offen offset:0 sc1 lds\n"
MF"a[132:135],a[128:131],v[64:67],a[132:135]\n"
SA"s40,s40,32768\n"
EX"v57,v57\n"
MF"a[136:139],a[128:131],v[68:71],a[136:139]\n"
"s_cmp_eq_u32 s40,s46\n"
RD"a[112:115],v227 offset:28672\n"
"s_cselect_b32 s40,s47,s40\n"
MF"a[140:143],a[128:131],v[72:75],a[140:143]\n"
EX"v58,v58\n"
MF"a[144:147],a[128:131],v[76:79],a[144:147]\n"
MF"v[80:83],a[96:99],v[0:3],v[80:83]\n"
EX"v59,v59\n"
MF"v[112:115],a[96:99],v[8:11],v[112:115]\n"
RD"a[116:119],v227 offset:29696\n"
MF"v[144:147],a[96:99],v[16:19],v[144:147]\n"
EX"v36,v36\n"
MF"v[176:179],a[96:99],v[24:27],v[176:179]\n"
WT"lgkmcnt(2)\n"
BL"v225,s[8:11],s44 offen offset:1024 sc1 lds\n"
MF"v[84:87],a[100:103],v[0:3],v[84:87]\n"
EX"v37,v37\n"
MF"v[116:119],a[100:103],v[8:11],v[116:119]\n"
RD"a[120:123],v227 offset:30720\n"
MF"v[148:151],a[100:103],v[16:19],v[148:151]\n"
EX"v38,v38\n"
MF"v[180:183],a[100:103],v[24:27],v[180:183]\n"
MF"v[88:91],a[104:107],v[0:3],v[88:91]\n"
EX"v39,v39\n"
MF"v[120:123],a[104:107],v[8:11],v[120:123]\n"
RD"a[124:127],v227 offset:31744\n"
MF"v[152:155],a[104:107],v[16:19],v[152:155]\n"
EX"v44,v44\n"
MF"v[184:187],a[104:107],v[24:27],v[184:187]\n"
BL"v225,s[8:11],s44 offen offset:2048 sc1 lds\n"
MF"v[92:95],a[108:111],v[0:3],v[92:95]\n"
EX"v45,v45\n"
MF"v[124:127],a[108:111],v[8:11],v[124:127]\n"
MF"v[156:159],a[108:111],v[16:19],v[156:159]\n"
EX"v46,v46\n"
MF"v[188:191],a[108:111],v[24:27],v[188:191]\n"
WT"lgkmcnt(1)\n"
WT"lgkmcnt(0)\n"
MF"v[96:99],a[112:115],v[0:3],v[96:99]\n"
"s_barrier\n"
MF"v[128:131],a[112:115],v[8:11],v[128:131]\n"
RD"a[64:67],v226 offset:0\n"
MF"v[160:163],a[112:115],v[16:19],v[160:163]\n"
EX"v47,v47\n"
MF"v[192:195],a[112:115],v[24:27],v[192:195]\n"
BL"v225,s[8:11],s44 offen offset:3072 sc1 lds\n"
MF"v[100:103],a[116:119],v[0:3],v[100:103]\n"
EX"v52,v52\n"
MF"v[132:135],a[116:119],v[8:11],v[132:135]\n"
RD"a[68:71],v226 offset:1024\n"
MF"v[164:167],a[116:119],v[16:19],v[164:167]\n"
EX"v53,v53\n"
MF"v[196:199],a[116:119],v[24:27],v[196:199]\n"
RD"a[72:75],v226 offset:2048\n"
MF"v[104:107],a[120:123],v[0:3],v[104:107]\n"
EX"v54,v54\n"
MF"v[136:139],a[120:123],v[8:11],v[136:139]\n"
RD"a[76:79],v226 offset:3072\n"
MF"v[168:171],a[120:123],v[16:19],v[168:171]\n"
EX"v55,v55\n"
MF"v[200:203],a[120:123],v[24:27],v[200:203]\n"
MF"v[108:111],a[124:127],v[0:3],v[108:111]\n"
WT"vmcnt(6)\n"
"buffer_store_dwordx2 v[232:233],v230,s[8:11],s94 offen sc1\n"
MF"v[140:143],a[124:127],v[8:11],v[140:143]\n"
EX"v60,v60\n"
MF"v[172:175],a[124:127],v[16:19],v[172:175]\n"
RD"a[80:83],v226 offset:4096\n"
MF"v[204:207],a[124:127],v[24:27],v[204:207]\n"
EX"v61,v61\n"
MF"a[132:135],a[128:131],v[0:3],a[132:135]\n"
RD"a[84:87],v226 offset:5120\n"
MF"a[136:139],a[128:131],v[8:11],a[136:139]\n"
EX"v62,v62\n"
MF"a[140:143],a[128:131],v[16:19],a[140:143]\n"
WT"lgkmcnt(4)\n"
RD"a[88:91],v226 offset:6144\n"
MF"a[144:147],a[128:131],v[24:27],a[144:147]\n"
EX"v63,v63\n"
"s_branch LDB_%=\n"
".p2align 6\n"
"LMD_%=:\n"
"s_cmp_eq_u32 s34,6\n"
"s_cbranch_scc1 LME_%=\n"
MF"v[0:3],a[64:67],a[0:3],v[208:211]\n"
"v_lshrrev_b32 v239,9,v224\n"
"v_lshlrev_b32 v239,8,v239\n"
MF"v[0:3],a[68:71],a[4:7],v[0:3]\n"
PK"v64,v32,v33\n"
PK"v65,v34,v35\n"
MF"v[8:11],a[68:71],a[20:23],0\n"
"v_bfe_u32 v237,v224,8,1\n"
PK"v66,v36,v37\n"
MF"v[8:11],a[64:67],a[16:19],v[8:11]\n"
PK"v67,v38,v39\n"
"v_lshl_add_u32 v239,v237,3,v239\n"
MF"v[16:19],a[64:67],a[32:35],0\n"
PK"v68,v40,v41\n"
PK"v69,v42,v43\n"
MF"v[16:19],a[68:71],a[36:39],v[16:19]\n"
RD"a[92:95],v226 offset:7168\n"
"v_and_b32 v237,0x80,v224\n"
MF"v[24:27],a[68:71],a[52:55],0\n"
PK"v70,v44,v45\n"
PK"v71,v46,v47\n"
MF"v[24:27],a[64:67],a[48:51],v[24:27]\n"
WT"lgkmcnt(1)\n"
"v_lshl_add_u32 v239,v237,23,v239\n"
"v_not_b32 v236,v234\n"
MF"v[0:3],a[72:75],a[8:11],v[0:3]\n"
PK"v72,v48,v49\n"
PK"v73,v50,v51\n"
MF"v[0:3],a[76:79],a[12:15],v[0:3]\n"
SA"s53,s93,452608\n"
"v_and_b32 v237,15,v236\n"
PK"v74,v52,v53\n"
MF"v[8:11],a[76:79],a[28:31],v[8:11]\n"
PK"v75,v54,v55\n"
"v_bfe_u32 v236,v236,4,7\n"
MF"v[8:11],a[72:75],a[24:27],v[8:11]\n"
PK"v76,v56,v57\n"
PK"v77,v58,v59\n"
MF"v[16:19],a[72:75],a[40:43],v[16:19]\n"
"v_mul_u32_u24 v236,20480,v236\n"
PK"v78,v60,v61\n"
MF"v[16:19],a[76:79],a[44:47],v[16:19]\n"
RD"a[96:99],v226 offset:8192\n"
PK"v79,v62,v63\n"
MF"v[24:27],a[76:79],a[60:63],v[24:27]\n"
EX"v0,v0\n"
MF"v[24:27],a[72:75],a[56:59],v[24:27]\n"
"v_lshl_add_u32 v236,v237,4,v236\n"
"v_add_u32 v236,v236,v239\n"
MF"v[4:7],a[80:83],a[0:3],v[212:215]\n"
EX"v1,v1\n"
MF"v[4:7],a[84:87],a[4:7],v[4:7]\n"
RD"a[100:103],v226 offset:9216\n"
"v_and_b32 v237,0x70,v224\n"
MF"v[12:15],a[84:87],a[20:23],0\n"
EX"v2,v2\n"
MF"v[12:15],a[80:83],a[16:19],v[12:15]\n"
EX"v3,v3\n"
"v_add_u32 v239,v239,v237\n"
MF"v[20:23],a[80:83],a[32:35],0\n"
EX"v8,v8\n"
"v_add_f32 v240,v240,v244\n"
MF"v[20:23],a[84:87],a[36:39],v[20:23]\n"
RD"a[104:107],v226 offset:10240\n"
"v_add_f32 v248,v248,v252\n"
"v_add_f32 v241,v241,v245\n"
MF"v[28:31],a[84:87],a[52:55],0\n"
EX"v9,v9\n"
"v_add_f32 v249,v249,v253\n"
MF"v[28:31],a[80:83],a[48:51],v[28:31]\n"
"v_add_f32 v242,v242,v246\n"
"v_add_f32 v250,v250,v254\n"
MF"v[4:7],a[88:91],a[8:11],v[4:7]\n"
WT"lgkmcnt(1)\n"
EX"v10,v10\n"
"v_add_f32 v243,v243,v247\n"
MF"v[4:7],a[92:95],a[12:15],v[4:7]\n"
RD"a[108:111],v226 offset:11264\n"
"v_add_f32 v251,v251,v255\n"
PK"v0,v0,v1\n"
MF"v[12:15],a[92:95],a[28:31],v[12:15]\n"
"buffer_load_dwordx2 v[254:255],v236,s[8:11],s53 offen sc1\n"
"v_ashrrev_i32 v235,31,v234\n"
MF"v[12:15],a[88:91],a[24:27],v[12:15]\n"
EX"v11,v11\n"
"v_not_b32 v235,v235\n"
MF"v[20:23],a[88:91],a[40:43],v[20:23]\n"
EX"v16,v16\n"
"v_or_b32 v235,0x80000000,v235\n"
MF"v[20:23],a[92:95],a[44:47],v[20:23]\n"
EX"v17,v17\n"
PK"v1,v2,v3\n"
MF"v[28:31],a[92:95],a[60:63],v[28:31]\n"
RD"a[112:115],v226 offset:12288\n"
"v_xor_b32 v235,v235,v234\n"
"v_and_b32 v235,0xffff0000,v235\n"
MF"v[28:31],a[88:91],a[56:59],v[28:31]\n"
EX"v18,v18\n"
MU"v235,0xbfb8aa3b,v235\n"
MF"v[32:35],a[96:99],a[0:3],v[216:219]\n"
EX"v235,v235\n"
PK"v8,v8,v9\n"
MF"v[32:35],a[100:103],a[4:7],v[32:35]\n"
EX"v19,v19\n"
"v_add_f32 v235,1.0,v235\n"
MF"v[40:43],a[100:103],a[20:23],0\n"
"v_rcp_f32 v235,v235\n"
MF"v[40:43],a[96:99],a[16:19],v[40:43]\n"
RD"a[116:119],v226 offset:13312\n"
PK"v235,v235,v235\n"
PK"v9,v10,v11\n"
MF"v[48:51],a[96:99],a[32:35],0\n"
EX"v24,v24\n"
"v_lshlrev_b32 v235,16,v235\n"
MF"v[48:51],a[100:103],a[36:39],v[48:51]\n"
EX"v25,v25\n"
"v_add_f32 v240,v240,v248\n"
MF"v[56:59],a[100:103],a[52:55],0\n"
EX"v26,v26\n"
PK"v16,v16,v17\n"
MF"v[56:59],a[96:99],a[48:51],v[56:59]\n"
WT"lgkmcnt(1)\n"
RD"a[120:123],v226 offset:14336\n"
"v_add_f32 v241,v241,v249\n"
"v_mov_b32 v227,v226\n"
MF"v[32:35],a[104:107],a[8:11],v[32:35]\n"
EX"v27,v27\n"
"v_add_f32 v242,v242,v250\n"
MF"v[32:35],a[108:111],a[12:15],v[32:35]\n"
EX"v4,v4\n"
"v_add_f32 v243,v243,v251\n"
MF"v[40:43],a[108:111],a[28:31],v[40:43]\n"
RD"a[124:127],v226 offset:15360\n"
PK"v240,v240,v240\n"
PK"v17,v18,v19\n"
MF"v[40:43],a[104:107],a[24:27],v[40:43]\n"
EX"v5,v5\n"
"v_lshlrev_b32 v240,16,v240\n"
MF"v[48:51],a[104:107],a[40:43],v[48:51]\n"
EX"v6,v6\n"
MU"v240,v240,v235\n"
MF"v[48:51],a[108:111],a[44:47],v[48:51]\n"
EX"v7,v7\n"
PK"v240,v240,v240\n"
MF"v[56:59],a[108:111],a[60:63],v[56:59]\n"
EX"v12,v12\n"
PK"v24,v24,v25\n"
MF"v[56:59],a[104:107],a[56:59],v[56:59]\n"
"v_lshlrev_b32 v240,16,v240\n"
PK"v241,v241,v241\n"
"v_lshlrev_b32 v241,16,v241\n"
MF"v[36:39],a[112:115],a[0:3],v[220:223]\n"
WT"lgkmcnt(1)\n"
EX"v13,v13\n"
PK"v25,v26,v27\n"
MF"v[36:39],a[116:119],a[4:7],v[36:39]\n"
RD"a[64:67],v227 offset:16384\n"
MU"v241,v241,v235\n"
PK"v241,v241,v241\n"
MF"v[44:47],a[116:119],a[20:23],0\n"
EX"v14,v14\n"
"v_lshlrev_b32 v241,16,v241\n"
MF"v[44:47],a[112:115],a[16:19],v[44:47]\n"
PK"v242,v242,v242\n"
PK"v2,v4,v5\n"
MF"v[52:55],a[112:115],a[32:35],0\n"
EX"v15,v15\n"
"v_lshlrev_b32 v242,16,v242\n"
MF"v[52:55],a[116:119],a[36:39],v[52:55]\n"
RD"a[68:71],v227 offset:17408\n"
MU"v242,v242,v235\n"
PK"v3,v6,v7\n"
MF"v[60:63],a[116:119],a[52:55],0\n"
EX"v20,v20\n"
PK"v242,v242,v242\n"
MF"v[60:63],a[112:115],a[48:51],v[60:63]\n"
"v_readlane_b32 s43,v228,s42\n"
"v_lshlrev_b32 v242,16,v242\n"
MF"v[36:39],a[120:123],a[8:11],v[36:39]\n"
WT"lgkmcnt(1)\n"
EX"v21,v21\n"
PK"v243,v243,v243\n"
MF"v[36:39],a[124:127],a[12:15],v[36:39]\n"
RD"a[72:75],v227 offset:18432\n"
"v_lshlrev_b32 v243,16,v243\n"
PK"v10,v12,v13\n"
MF"v[44:47],a[124:127],a[28:31],v[44:47]\n"
EX"v22,v22\n"
MU"v243,v243,v235\n"
MF"v[44:47],a[120:123],a[24:27],v[44:47]\n"
"s_mov_b32 m0,s40\n"
PK"v243,v243,v243\n"
"v_lshlrev_b32 v243,16,v243\n"
MF"v[52:55],a[120:123],a[40:43],v[52:55]\n"
EX"v23,v23\n"
MF"v[52:55],a[124:127],a[44:47],v[52:55]\n"
RD"a[76:79],v227 offset:19456\n"
"v_readlane_b32 s44,v229,s42\n"
PK"v11,v14,v15\n"
MF"v[60:63],a[124:127],a[60:63],v[60:63]\n"
EX"v28,v28\n"
MF"v[60:63],a[120:123],a[56:59],v[60:63]\n"
SA"s42,s42,1\n"
BL"v225,s[8:11],s43 offen offset:0 sc1 lds\n"
MF"v[80:83],a[64:67],v[64:67],v[80:83]\n"
SA"s41,s41,32768\n"
EX"v29,v29\n"
MF"v[112:115],a[64:67],v[68:71],v[112:115]\n"
"s_cmp_eq_u32 s41,163840\n"
RD"a[80:83],v227 offset:20480\n"
PK"v18,v20,v21\n"
MF"v[144:147],a[64:67],v[72:75],v[144:147]\n"
"s_cselect_b32 s41,0,s41\n"
EX"v30,v30\n"
MF"v[176:179],a[64:67],v[76:79],v[176:179]\n"
WT"lgkmcnt(2)\n"
"v_add_u32 v226,s41,v224\n"
PK"v19,v22,v23\n"
MF"v[84:87],a[68:71],v[64:67],v[84:87]\n"
EX"v31,v31\n"
MF"v[116:119],a[68:71],v[68:71],v[116:119]\n"
RD"a[84:87],v227 offset:21504\n"
PK"v26,v28,v29\n"
MF"v[148:151],a[68:71],v[72:75],v[148:151]\n"
EX"v32,v32\n"
MF"v[180:183],a[68:71],v[76:79],v[180:183]\n"
BL"v225,s[8:11],s43 offen offset:1024 sc1 lds\n"
MF"v[88:91],a[72:75],v[64:67],v[88:91]\n"
EX"v33,v33\n"
MF"v[120:123],a[72:75],v[68:71],v[120:123]\n"
RD"a[88:91],v227 offset:22528\n"
PK"v27,v30,v31\n"
MF"v[152:155],a[72:75],v[72:75],v[152:155]\n"
EX"v34,v34\n"
MF"v[184:187],a[72:75],v[76:79],v[184:187]\n"
WT"lgkmcnt(2)\n"
WT"vmcnt(2)\n"
"v_lshlrev_b32 v244,16,v254\n"
"v_and_b32 v245,0xffff0000,v254\n"
MF"v[92:95],a[76:79],v[64:67],v[92:95]\n"
EX"v35,v35\n"
"v_lshlrev_b32 v246,16,v255\n"
MF"v[124:127],a[76:79],v[68:71],v[124:127]\n"
RD"a[92:95],v227 offset:23552\n"
"v_and_b32 v247,0xffff0000,v255\n"
"v_add_f32 v240,v240,v244\n"
MF"v[156:159],a[76:79],v[72:75],v[156:159]\n"
EX"v40,v40\n"
"v_add_f32 v241,v241,v245\n"
MF"v[188:191],a[76:79],v[76:79],v[188:191]\n"
BL"v225,s[8:11],s43 offen offset:2048 sc1 lds\n"
"v_add_f32 v242,v242,v246\n"
MF"v[96:99],a[80:83],v[64:67],v[96:99]\n"
EX"v41,v41\n"
"v_add_f32 v243,v243,v247\n"
MF"v[128:131],a[80:83],v[68:71],v[128:131]\n"
RD"a[96:99],v227 offset:24576\n"
PK"v240,v240,v241\n"
PK"v241,v242,v243\n"
MF"v[160:163],a[80:83],v[72:75],v[160:163]\n"
"buffer_store_dwordx2 v[240:241],v239,s[8:11],s93 offen sc1\n"
MF"v[192:195],a[80:83],v[76:79],v[192:195]\n"
WT"lgkmcnt(2)\n"
EX"v42,v42\n"
MF"v[100:103],a[84:87],v[64:67],v[100:103]\n"
EX"v43,v43\n"
MF"v[132:135],a[84:87],v[68:71],v[132:135]\n"
RD"a[100:103],v227 offset:25600\n"
MF"v[164:167],a[84:87],v[72:75],v[164:167]\n"
EX"v48,v48\n"
MF"v[196:199],a[84:87],v[76:79],v[196:199]\n"
BL"v225,s[8:11],s43 offen offset:3072 sc1 lds\n"
MF"v[104:107],a[88:91],v[64:67],v[104:107]\n"
EX"v49,v49\n"
MF"v[136:139],a[88:91],v[68:71],v[136:139]\n"
RD"a[104:107],v227 offset:26624\n"
MF"v[168:171],a[88:91],v[72:75],v[168:171]\n"
EX"v50,v50\n"
MF"v[200:203],a[88:91],v[76:79],v[200:203]\n"
WT"lgkmcnt(2)\n"
SA"m0,s40,16384\n"
MF"v[108:111],a[92:95],v[64:67],v[108:111]\n"
EX"v51,v51\n"
MF"v[140:143],a[92:95],v[68:71],v[140:143]\n"
RD"a[108:111],v227 offset:27648\n"
MF"v[172:175],a[92:95],v[72:75],v[172:175]\n"
EX"v56,v56\n"
MF"v[204:207],a[92:95],v[76:79],v[204:207]\n"
BL"v225,s[8:11],s44 offen offset:0 sc1 lds\n"
MF"a[132:135],a[128:131],v[64:67],a[132:135]\n"
SA"s40,s40,32768\n"
EX"v57,v57\n"
MF"a[136:139],a[128:131],v[68:71],a[136:139]\n"
"s_cmp_eq_u32 s40,s46\n"
RD"a[112:115],v227 offset:28672\n"
"s_cselect_b32 s40,s47,s40\n"
MF"a[140:143],a[128:131],v[72:75],a[140:143]\n"
EX"v58,v58\n"
MF"a[144:147],a[128:131],v[76:79],a[144:147]\n"
MF"v[80:83],a[96:99],v[0:3],v[80:83]\n"
EX"v59,v59\n"
MF"v[112:115],a[96:99],v[8:11],v[112:115]\n"
RD"a[116:119],v227 offset:29696\n"
MF"v[144:147],a[96:99],v[16:19],v[144:147]\n"
EX"v36,v36\n"
MF"v[176:179],a[96:99],v[24:27],v[176:179]\n"
WT"lgkmcnt(2)\n"
BL"v225,s[8:11],s44 offen offset:1024 sc1 lds\n"
MF"v[84:87],a[100:103],v[0:3],v[84:87]\n"
EX"v37,v37\n"
MF"v[116:119],a[100:103],v[8:11],v[116:119]\n"
RD"a[120:123],v227 offset:30720\n"
MF"v[148:151],a[100:103],v[16:19],v[148:151]\n"
EX"v38,v38\n"
MF"v[180:183],a[100:103],v[24:27],v[180:183]\n"
MF"v[88:91],a[104:107],v[0:3],v[88:91]\n"
EX"v39,v39\n"
MF"v[120:123],a[104:107],v[8:11],v[120:123]\n"
RD"a[124:127],v227 offset:31744\n"
MF"v[152:155],a[104:107],v[16:19],v[152:155]\n"
EX"v44,v44\n"
MF"v[184:187],a[104:107],v[24:27],v[184:187]\n"
BL"v225,s[8:11],s44 offen offset:2048 sc1 lds\n"
MF"v[92:95],a[108:111],v[0:3],v[92:95]\n"
EX"v45,v45\n"
MF"v[124:127],a[108:111],v[8:11],v[124:127]\n"
MF"v[156:159],a[108:111],v[16:19],v[156:159]\n"
EX"v46,v46\n"
MF"v[188:191],a[108:111],v[24:27],v[188:191]\n"
WT"lgkmcnt(1)\n"
WT"lgkmcnt(0)\n"
MF"v[96:99],a[112:115],v[0:3],v[96:99]\n"
"s_barrier\n"
MF"v[128:131],a[112:115],v[8:11],v[128:131]\n"
RD"a[64:67],v226 offset:0\n"
MF"v[160:163],a[112:115],v[16:19],v[160:163]\n"
EX"v47,v47\n"
MF"v[192:195],a[112:115],v[24:27],v[192:195]\n"
BL"v225,s[8:11],s44 offen offset:3072 sc1 lds\n"
MF"v[100:103],a[116:119],v[0:3],v[100:103]\n"
EX"v52,v52\n"
MF"v[132:135],a[116:119],v[8:11],v[132:135]\n"
RD"a[68:71],v226 offset:1024\n"
MF"v[164:167],a[116:119],v[16:19],v[164:167]\n"
EX"v53,v53\n"
MF"v[196:199],a[116:119],v[24:27],v[196:199]\n"
RD"a[72:75],v226 offset:2048\n"
MF"v[104:107],a[120:123],v[0:3],v[104:107]\n"
EX"v54,v54\n"
MF"v[136:139],a[120:123],v[8:11],v[136:139]\n"
RD"a[76:79],v226 offset:3072\n"
MF"v[168:171],a[120:123],v[16:19],v[168:171]\n"
EX"v55,v55\n"
MF"v[200:203],a[120:123],v[24:27],v[200:203]\n"
MF"v[108:111],a[124:127],v[0:3],v[108:111]\n"
WT"vmcnt(5)\n"
"buffer_store_dwordx2 v[232:233],v230,s[8:11],s94 offen sc1\n"
MF"v[140:143],a[124:127],v[8:11],v[140:143]\n"
EX"v60,v60\n"
MF"v[172:175],a[124:127],v[16:19],v[172:175]\n"
RD"a[80:83],v226 offset:4096\n"
MF"v[204:207],a[124:127],v[24:27],v[204:207]\n"
EX"v61,v61\n"
MF"a[132:135],a[128:131],v[0:3],a[132:135]\n"
RD"a[84:87],v226 offset:5120\n"
MF"a[136:139],a[128:131],v[8:11],a[136:139]\n"
EX"v62,v62\n"
MF"a[140:143],a[128:131],v[16:19],a[140:143]\n"
WT"lgkmcnt(4)\n"
RD"a[88:91],v226 offset:6144\n"
MF"a[144:147],a[128:131],v[24:27],a[144:147]\n"
EX"v63,v63\n"
"s_branch LDB_%=\n"
".p2align 6\n"
"LME_%=:\n"
MF"v[0:3],a[64:67],a[0:3],0\n"
"v_lshrrev_b32 v239,9,v224\n"
"v_lshlrev_b32 v239,8,v239\n"
MF"v[0:3],a[68:71],a[4:7],v[0:3]\n"
PK"v64,v32,v33\n"
PK"v65,v34,v35\n"
MF"v[8:11],a[68:71],a[20:23],0\n"
"v_bfe_u32 v237,v224,8,1\n"
PK"v66,v36,v37\n"
MF"v[8:11],a[64:67],a[16:19],v[8:11]\n"
PK"v67,v38,v39\n"
"v_lshl_add_u32 v239,v237,3,v239\n"
MF"v[16:19],a[64:67],a[32:35],0\n"
PK"v68,v40,v41\n"
PK"v69,v42,v43\n"
MF"v[16:19],a[68:71],a[36:39],v[16:19]\n"
RD"a[92:95],v226 offset:7168\n"
"v_and_b32 v237,0x80,v224\n"
MF"v[24:27],a[68:71],a[52:55],0\n"
PK"v70,v44,v45\n"
PK"v71,v46,v47\n"
MF"v[24:27],a[64:67],a[48:51],v[24:27]\n"
WT"lgkmcnt(1)\n"
"v_lshl_add_u32 v239,v237,23,v239\n"
"v_not_b32 v236,v234\n"
MF"v[0:3],a[72:75],a[8:11],v[0:3]\n"
PK"v72,v48,v49\n"
PK"v73,v50,v51\n"
MF"v[0:3],a[76:79],a[12:15],v[0:3]\n"
SA"s53,s93,452608\n"
"v_and_b32 v237,15,v236\n"
PK"v74,v52,v53\n"
MF"v[8:11],a[76:79],a[28:31],v[8:11]\n"
PK"v75,v54,v55\n"
"v_bfe_u32 v236,v236,4,7\n"
MF"v[8:11],a[72:75],a[24:27],v[8:11]\n"
PK"v76,v56,v57\n"
PK"v77,v58,v59\n"
MF"v[16:19],a[72:75],a[40:43],v[16:19]\n"
"v_mul_u32_u24 v236,20480,v236\n"
PK"v78,v60,v61\n"
MF"v[16:19],a[76:79],a[44:47],v[16:19]\n"
RD"a[96:99],v226 offset:8192\n"
PK"v79,v62,v63\n"
MF"v[24:27],a[76:79],a[60:63],v[24:27]\n"
EX"v0,v0\n"
MF"v[24:27],a[72:75],a[56:59],v[24:27]\n"
"v_lshl_add_u32 v236,v237,4,v236\n"
"v_add_u32 v236,v236,v239\n"
MF"v[4:7],a[80:83],a[0:3],0\n"
EX"v1,v1\n"
MF"v[4:7],a[84:87],a[4:7],v[4:7]\n"
RD"a[100:103],v226 offset:9216\n"
"v_and_b32 v237,0x70,v224\n"
MF"v[12:15],a[84:87],a[20:23],0\n"
EX"v2,v2\n"
MF"v[12:15],a[80:83],a[16:19],v[12:15]\n"
EX"v3,v3\n"
"v_add_u32 v239,v239,v237\n"
MF"v[20:23],a[80:83],a[32:35],0\n"
EX"v8,v8\n"
"v_add_f32 v240,v240,v244\n"
MF"v[20:23],a[84:87],a[36:39],v[20:23]\n"
RD"a[104:107],v226 offset:10240\n"
"v_add_f32 v248,v248,v252\n"
"v_add_f32 v241,v241,v245\n"
MF"v[28:31],a[84:87],a[52:55],0\n"
EX"v9,v9\n"
"v_add_f32 v249,v249,v253\n"
MF"v[28:31],a[80:83],a[48:51],v[28:31]\n"
"v_add_f32 v242,v242,v246\n"
"v_add_f32 v250,v250,v254\n"
MF"v[4:7],a[88:91],a[8:11],v[4:7]\n"
WT"lgkmcnt(1)\n"
EX"v10,v10\n"
"v_add_f32 v243,v243,v247\n"
MF"v[4:7],a[92:95],a[12:15],v[4:7]\n"
RD"a[108:111],v226 offset:11264\n"
"v_add_f32 v251,v251,v255\n"
PK"v0,v0,v1\n"
MF"v[12:15],a[92:95],a[28:31],v[12:15]\n"
"buffer_load_dwordx2 v[254:255],v236,s[8:11],s53 offen sc1\n"
"v_ashrrev_i32 v235,31,v234\n"
MF"v[12:15],a[88:91],a[24:27],v[12:15]\n"
EX"v11,v11\n"
"v_not_b32 v235,v235\n"
MF"v[20:23],a[88:91],a[40:43],v[20:23]\n"
EX"v16,v16\n"
"v_or_b32 v235,0x80000000,v235\n"
MF"v[20:23],a[92:95],a[44:47],v[20:23]\n"
EX"v17,v17\n"
PK"v1,v2,v3\n"
MF"v[28:31],a[92:95],a[60:63],v[28:31]\n"
RD"a[112:115],v226 offset:12288\n"
"v_xor_b32 v235,v235,v234\n"
"v_and_b32 v235,0xffff0000,v235\n"
MF"v[28:31],a[88:91],a[56:59],v[28:31]\n"
EX"v18,v18\n"
MU"v235,0xbfb8aa3b,v235\n"
MF"v[32:35],a[96:99],a[0:3],0\n"
EX"v235,v235\n"
PK"v8,v8,v9\n"
MF"v[32:35],a[100:103],a[4:7],v[32:35]\n"
EX"v19,v19\n"
"v_add_f32 v235,1.0,v235\n"
MF"v[40:43],a[100:103],a[20:23],0\n"
"v_rcp_f32 v235,v235\n"
MF"v[40:43],a[96:99],a[16:19],v[40:43]\n"
RD"a[116:119],v226 offset:13312\n"
PK"v235,v235,v235\n"
PK"v9,v10,v11\n"
MF"v[48:51],a[96:99],a[32:35],0\n"
EX"v24,v24\n"
"v_lshlrev_b32 v235,16,v235\n"
MF"v[48:51],a[100:103],a[36:39],v[48:51]\n"
EX"v25,v25\n"
"v_add_f32 v240,v240,v248\n"
MF"v[56:59],a[100:103],a[52:55],0\n"
EX"v26,v26\n"
PK"v16,v16,v17\n"
MF"v[56:59],a[96:99],a[48:51],v[56:59]\n"
WT"lgkmcnt(1)\n"
RD"a[120:123],v226 offset:14336\n"
"v_add_f32 v241,v241,v249\n"
"v_mov_b32 v227,v226\n"
MF"v[32:35],a[104:107],a[8:11],v[32:35]\n"
EX"v27,v27\n"
"v_add_f32 v242,v242,v250\n"
MF"v[32:35],a[108:111],a[12:15],v[32:35]\n"
EX"v4,v4\n"
"v_add_f32 v243,v243,v251\n"
MF"v[40:43],a[108:111],a[28:31],v[40:43]\n"
RD"a[124:127],v226 offset:15360\n"
PK"v240,v240,v240\n"
PK"v17,v18,v19\n"
MF"v[40:43],a[104:107],a[24:27],v[40:43]\n"
EX"v5,v5\n"
"v_lshlrev_b32 v240,16,v240\n"
MF"v[48:51],a[104:107],a[40:43],v[48:51]\n"
EX"v6,v6\n"
MU"v240,v240,v235\n"
MF"v[48:51],a[108:111],a[44:47],v[48:51]\n"
EX"v7,v7\n"
PK"v240,v240,v240\n"
MF"v[56:59],a[108:111],a[60:63],v[56:59]\n"
EX"v12,v12\n"
PK"v24,v24,v25\n"
MF"v[56:59],a[104:107],a[56:59],v[56:59]\n"
"v_lshlrev_b32 v240,16,v240\n"
PK"v241,v241,v241\n"
"v_lshlrev_b32 v241,16,v241\n"
MF"v[36:39],a[112:115],a[0:3],0\n"
WT"lgkmcnt(1)\n"
EX"v13,v13\n"
PK"v25,v26,v27\n"
MF"v[36:39],a[116:119],a[4:7],v[36:39]\n"
RD"a[64:67],v227 offset:16384\n"
MU"v241,v241,v235\n"
PK"v241,v241,v241\n"
MF"v[44:47],a[116:119],a[20:23],0\n"
EX"v14,v14\n"
"v_lshlrev_b32 v241,16,v241\n"
MF"v[44:47],a[112:115],a[16:19],v[44:47]\n"
PK"v242,v242,v242\n"
PK"v2,v4,v5\n"
MF"v[52:55],a[112:115],a[32:35],0\n"
EX"v15,v15\n"
"v_lshlrev_b32 v242,16,v242\n"
MF"v[52:55],a[116:119],a[36:39],v[52:55]\n"
RD"a[68:71],v227 offset:17408\n"
MU"v242,v242,v235\n"
PK"v3,v6,v7\n"
MF"v[60:63],a[116:119],a[52:55],0\n"
EX"v20,v20\n"
PK"v242,v242,v242\n"
MF"v[60:63],a[112:115],a[48:51],v[60:63]\n"
"v_readlane_b32 s43,v228,s42\n"
"v_lshlrev_b32 v242,16,v242\n"
MF"v[36:39],a[120:123],a[8:11],v[36:39]\n"
WT"lgkmcnt(1)\n"
EX"v21,v21\n"
PK"v243,v243,v243\n"
MF"v[36:39],a[124:127],a[12:15],v[36:39]\n"
RD"a[72:75],v227 offset:18432\n"
"v_lshlrev_b32 v243,16,v243\n"
PK"v10,v12,v13\n"
MF"v[44:47],a[124:127],a[28:31],v[44:47]\n"
EX"v22,v22\n"
MU"v243,v243,v235\n"
MF"v[44:47],a[120:123],a[24:27],v[44:47]\n"
"s_mov_b32 m0,s40\n"
PK"v243,v243,v243\n"
"v_lshlrev_b32 v243,16,v243\n"
MF"v[52:55],a[120:123],a[40:43],v[52:55]\n"
EX"v23,v23\n"
MF"v[52:55],a[124:127],a[44:47],v[52:55]\n"
RD"a[76:79],v227 offset:19456\n"
"v_readlane_b32 s44,v229,s42\n"
PK"v11,v14,v15\n"
MF"v[60:63],a[124:127],a[60:63],v[60:63]\n"
EX"v28,v28\n"
MF"v[60:63],a[120:123],a[56:59],v[60:63]\n"
SA"s42,s42,1\n"
BL"v225,s[8:11],s43 offen offset:0 sc1 lds\n"
MF"v[80:83],a[64:67],v[64:67],v[80:83]\n"
SA"s41,s41,32768\n"
EX"v29,v29\n"
MF"v[112:115],a[64:67],v[68:71],v[112:115]\n"
"s_cmp_eq_u32 s41,163840\n"
RD"a[80:83],v227 offset:20480\n"
PK"v18,v20,v21\n"
MF"v[144:147],a[64:67],v[72:75],v[144:147]\n"
"s_cselect_b32 s41,0,s41\n"
EX"v30,v30\n"
MF"v[176:179],a[64:67],v[76:79],v[176:179]\n"
WT"lgkmcnt(2)\n"
"v_add_u32 v226,s41,v224\n"
PK"v19,v22,v23\n"
MF"v[84:87],a[68:71],v[64:67],v[84:87]\n"
EX"v31,v31\n"
MF"v[116:119],a[68:71],v[68:71],v[116:119]\n"
RD"a[84:87],v227 offset:21504\n"
PK"v26,v28,v29\n"
MF"v[148:151],a[68:71],v[72:75],v[148:151]\n"
EX"v32,v32\n"
MF"v[180:183],a[68:71],v[76:79],v[180:183]\n"
BL"v225,s[8:11],s43 offen offset:1024 sc1 lds\n"
MF"v[88:91],a[72:75],v[64:67],v[88:91]\n"
EX"v33,v33\n"
MF"v[120:123],a[72:75],v[68:71],v[120:123]\n"
RD"a[88:91],v227 offset:22528\n"
PK"v27,v30,v31\n"
MF"v[152:155],a[72:75],v[72:75],v[152:155]\n"
EX"v34,v34\n"
MF"v[184:187],a[72:75],v[76:79],v[184:187]\n"
WT"lgkmcnt(2)\n"
WT"vmcnt(2)\n"
"v_lshlrev_b32 v244,16,v254\n"
"v_and_b32 v245,0xffff0000,v254\n"
MF"v[92:95],a[76:79],v[64:67],v[92:95]\n"
EX"v35,v35\n"
"v_lshlrev_b32 v246,16,v255\n"
MF"v[124:127],a[76:79],v[68:71],v[124:127]\n"
RD"a[92:95],v227 offset:23552\n"
"v_and_b32 v247,0xffff0000,v255\n"
"v_add_f32 v240,v240,v244\n"
MF"v[156:159],a[76:79],v[72:75],v[156:159]\n"
EX"v40,v40\n"
"v_add_f32 v241,v241,v245\n"
MF"v[188:191],a[76:79],v[76:79],v[188:191]\n"
BL"v225,s[8:11],s43 offen offset:2048 sc1 lds\n"
"v_add_f32 v242,v242,v246\n"
MF"v[96:99],a[80:83],v[64:67],v[96:99]\n"
EX"v41,v41\n"
"v_add_f32 v243,v243,v247\n"
MF"v[128:131],a[80:83],v[68:71],v[128:131]\n"
RD"a[96:99],v227 offset:24576\n"
PK"v240,v240,v241\n"
PK"v241,v242,v243\n"
MF"v[160:163],a[80:83],v[72:75],v[160:163]\n"
"buffer_store_dwordx2 v[240:241],v239,s[8:11],s93 offen sc1\n"
MF"v[192:195],a[80:83],v[76:79],v[192:195]\n"
WT"lgkmcnt(2)\n"
EX"v42,v42\n"
MF"v[100:103],a[84:87],v[64:67],v[100:103]\n"
EX"v43,v43\n"
MF"v[132:135],a[84:87],v[68:71],v[132:135]\n"
RD"a[100:103],v227 offset:25600\n"
MF"v[164:167],a[84:87],v[72:75],v[164:167]\n"
EX"v48,v48\n"
MF"v[196:199],a[84:87],v[76:79],v[196:199]\n"
BL"v225,s[8:11],s43 offen offset:3072 sc1 lds\n"
MF"v[104:107],a[88:91],v[64:67],v[104:107]\n"
EX"v49,v49\n"
MF"v[136:139],a[88:91],v[68:71],v[136:139]\n"
RD"a[104:107],v227 offset:26624\n"
MF"v[168:171],a[88:91],v[72:75],v[168:171]\n"
EX"v50,v50\n"
MF"v[200:203],a[88:91],v[76:79],v[200:203]\n"
WT"lgkmcnt(2)\n"
SA"m0,s40,16384\n"
MF"v[108:111],a[92:95],v[64:67],v[108:111]\n"
EX"v51,v51\n"
MF"v[140:143],a[92:95],v[68:71],v[140:143]\n"
RD"a[108:111],v227 offset:27648\n"
MF"v[172:175],a[92:95],v[72:75],v[172:175]\n"
EX"v56,v56\n"
MF"v[204:207],a[92:95],v[76:79],v[204:207]\n"
BL"v225,s[8:11],s44 offen offset:0 sc1 lds\n"
MF"a[132:135],a[128:131],v[64:67],a[132:135]\n"
SA"s40,s40,32768\n"
EX"v57,v57\n"
MF"a[136:139],a[128:131],v[68:71],a[136:139]\n"
"s_cmp_eq_u32 s40,s46\n"
RD"a[112:115],v227 offset:28672\n"
"s_cselect_b32 s40,s47,s40\n"
MF"a[140:143],a[128:131],v[72:75],a[140:143]\n"
EX"v58,v58\n"
MF"a[144:147],a[128:131],v[76:79],a[144:147]\n"
MF"v[80:83],a[96:99],v[0:3],v[80:83]\n"
EX"v59,v59\n"
MF"v[112:115],a[96:99],v[8:11],v[112:115]\n"
RD"a[116:119],v227 offset:29696\n"
MF"v[144:147],a[96:99],v[16:19],v[144:147]\n"
EX"v36,v36\n"
MF"v[176:179],a[96:99],v[24:27],v[176:179]\n"
WT"lgkmcnt(2)\n"
BL"v225,s[8:11],s44 offen offset:1024 sc1 lds\n"
MF"v[84:87],a[100:103],v[0:3],v[84:87]\n"
EX"v37,v37\n"
MF"v[116:119],a[100:103],v[8:11],v[116:119]\n"
RD"a[120:123],v227 offset:30720\n"
MF"v[148:151],a[100:103],v[16:19],v[148:151]\n"
EX"v38,v38\n"
MF"v[180:183],a[100:103],v[24:27],v[180:183]\n"
MF"v[88:91],a[104:107],v[0:3],v[88:91]\n"
EX"v39,v39\n"
MF"v[120:123],a[104:107],v[8:11],v[120:123]\n"
RD"a[124:127],v227 offset:31744\n"
MF"v[152:155],a[104:107],v[16:19],v[152:155]\n"
EX"v44,v44\n"
MF"v[184:187],a[104:107],v[24:27],v[184:187]\n"
BL"v225,s[8:11],s44 offen offset:2048 sc1 lds\n"
MF"v[92:95],a[108:111],v[0:3],v[92:95]\n"
EX"v45,v45\n"
MF"v[124:127],a[108:111],v[8:11],v[124:127]\n"
MF"v[156:159],a[108:111],v[16:19],v[156:159]\n"
EX"v46,v46\n"
MF"v[188:191],a[108:111],v[24:27],v[188:191]\n"
WT"lgkmcnt(1)\n"
WT"lgkmcnt(0)\n"
MF"v[96:99],a[112:115],v[0:3],v[96:99]\n"
"s_barrier\n"
MF"v[128:131],a[112:115],v[8:11],v[128:131]\n"
RD"a[64:67],v226 offset:0\n"
MF"v[160:163],a[112:115],v[16:19],v[160:163]\n"
EX"v47,v47\n"
MF"v[192:195],a[112:115],v[24:27],v[192:195]\n"
BL"v225,s[8:11],s44 offen offset:3072 sc1 lds\n"
MF"v[100:103],a[116:119],v[0:3],v[100:103]\n"
EX"v52,v52\n"
MF"v[132:135],a[116:119],v[8:11],v[132:135]\n"
RD"a[68:71],v226 offset:1024\n"
MF"v[164:167],a[116:119],v[16:19],v[164:167]\n"
EX"v53,v53\n"
MF"v[196:199],a[116:119],v[24:27],v[196:199]\n"
RD"a[72:75],v226 offset:2048\n"
MF"v[104:107],a[120:123],v[0:3],v[104:107]\n"
EX"v54,v54\n"
MF"v[136:139],a[120:123],v[8:11],v[136:139]\n"
RD"a[76:79],v226 offset:3072\n"
MF"v[168:171],a[120:123],v[16:19],v[168:171]\n"
EX"v55,v55\n"
MF"v[200:203],a[120:123],v[24:27],v[200:203]\n"
MF"v[108:111],a[124:127],v[0:3],v[108:111]\n"
WT"vmcnt(5)\n"
"buffer_store_dwordx2 v[232:233],v230,s[8:11],s94 offen sc1\n"
MF"v[140:143],a[124:127],v[8:11],v[140:143]\n"
EX"v60,v60\n"
MF"v[172:175],a[124:127],v[16:19],v[172:175]\n"
RD"a[80:83],v226 offset:4096\n"
MF"v[204:207],a[124:127],v[24:27],v[204:207]\n"
EX"v61,v61\n"
MF"a[132:135],a[128:131],v[0:3],a[132:135]\n"
RD"a[84:87],v226 offset:5120\n"
MF"a[136:139],a[128:131],v[8:11],a[136:139]\n"
EX"v62,v62\n"
MF"a[140:143],a[128:131],v[16:19],a[140:143]\n"
WT"lgkmcnt(4)\n"
RD"a[88:91],v226 offset:6144\n"
MF"a[144:147],a[128:131],v[24:27],a[144:147]\n"
EX"v63,v63\n"
"s_mov_b32 s57,0\n"
"s_lshl_b32 s56,s34,1\n"
"s_sub_u32 s56,s56,12\n"
"s_branch LP1_%=\n"
".p2align 6\n"
"LRT_%=:\n"
MF"v[0:3],a[64:67],a[0:3],0\n"
"s_mov_b32 s12,s74\n"
"v_lshrrev_b32 v242,1,v224\n"
PK"v64,v32,v33\n"
MF"v[0:3],a[68:71],a[4:7],v[0:3]\n"
"s_and_b32 s13,s75,0xffff\n"
PK"v65,v34,v35\n"
"v_and_b32 v242,56,v242\n"
MF"v[8:11],a[68:71],a[20:23],0\n"
"s_mov_b32 s14,5242880\n"
PK"v66,v36,v37\n"
PK"v67,v38,v39\n"
MF"v[8:11],a[64:67],a[16:19],v[8:11]\n"
"s_lshl_b32 s23,s35,6\n"
PK"v68,v40,v41\n"
PK"v69,v42,v43\n"
MF"v[16:19],a[64:67],a[32:35],0\n"
"s_mov_b32 s15,0x20000\n"
PK"v70,v44,v45\n"
PK"v71,v46,v47\n"
MF"v[16:19],a[68:71],a[36:39],v[16:19]\n"
SA"s23,s23,4352\n"
RD"a[92:95],v226 offset:7168\n"
"v_add_u32 v243,s40,v252\n"
MF"v[24:27],a[68:71],a[52:55],0\n"
SA"s19,s40,32768\n"
PK"v72,v48,v49\n"
PK"v73,v50,v51\n"
MF"v[24:27],a[64:67],a[48:51],v[24:27]\n"
WT"lgkmcnt(1)\n"
"buffer_load_dwordx2 v[244:245],v242,s[8:11],s23 offen sc1\n"
MF"v[0:3],a[72:75],a[8:11],v[0:3]\n"
"s_cmp_eq_u32 s19,s46\n"
PK"v74,v52,v53\n"
PK"v75,v54,v55\n"
MF"v[0:3],a[76:79],a[12:15],v[0:3]\n"
"s_cselect_b32 s19,s47,s19\n"
PK"v76,v56,v57\n"
PK"v77,v58,v59\n"
MF"v[8:11],a[76:79],a[28:31],v[8:11]\n"
"s_cmp_eq_u32 s20,0\n"
PK"v78,v60,v61\n"
PK"v79,v62,v63\n"
MF"v[8:11],a[72:75],a[24:27],v[8:11]\n"
"s_cselect_b32 s17,2130706432,s17\n"
SA"m0,s19,0\n"
MF"v[16:19],a[72:75],a[40:43],v[16:19]\n"
BL"v240,s[12:15],s17 offen offset:0 lds\n"
MF"v[16:19],a[76:79],a[44:47],v[16:19]\n"
RD"a[96:99],v226 offset:8192\n"
MF"v[24:27],a[76:79],a[60:63],v[24:27]\n"
EX"v0,v0\n"
MF"v[24:27],a[72:75],a[56:59],v[24:27]\n"
BL"v241,s[12:15],s17 offen offset:1024 lds\n"
MF"v[4:7],a[80:83],a[0:3],0\n"
EX"v1,v1\n"
MF"v[4:7],a[84:87],a[4:7],v[4:7]\n"
RD"a[100:103],v226 offset:9216\n"
PK"v0,v0,v1\n"
MF"v[12:15],a[84:87],a[20:23],0\n"
EX"v2,v2\n"
MF"v[12:15],a[80:83],a[16:19],v[12:15]\n"
BL"v251,s[12:15],s17 offen offset:2048 lds\n"
MF"v[20:23],a[80:83],a[32:35],0\n"
SA"m0,s19,16384\n"
EX"v3,v3\n"
MF"v[20:23],a[84:87],a[36:39],v[20:23]\n"
SA"s17,s17,20480\n"
EX"v8,v8\n"
MF"v[28:31],a[84:87],a[52:55],0\n"
RD"a[104:107],v226 offset:10240\n"
PK"v1,v2,v3\n"
MF"v[28:31],a[80:83],a[48:51],v[28:31]\n"
EX"v9,v9\n"
MF"v[4:7],a[88:91],a[8:11],v[4:7]\n"
WT"lgkmcnt(1)\n"
BL"v240,s[12:15],s17 offen offset:0 lds\n"
MF"v[4:7],a[92:95],a[12:15],v[4:7]\n"
EX"v10,v10\n"
MF"v[12:15],a[92:95],a[28:31],v[12:15]\n"
RD"a[108:111],v226 offset:11264\n"
PK"v8,v8,v9\n"
MF"v[12:15],a[88:91],a[24:27],v[12:15]\n"
EX"v11,v11\n"
MF"v[20:23],a[88:91],a[40:43],v[20:23]\n"
WT"vmcnt(13)\n"
RT"a[148:149],v243 offset:0\n"
RT"a[150:151],v243 offset:16384\n"
MF"v[20:23],a[92:95],a[44:47],v[20:23]\n"
EX"v16,v16\n"
MF"v[28:31],a[92:95],a[60:63],v[28:31]\n"
EX"v17,v17\n"
MF"v[28:31],a[88:91],a[56:59],v[28:31]\n"
RD"a[112:115],v226 offset:12288\n"
PK"v9,v10,v11\n"
MF"v[32:35],a[96:99],a[0:3],0\n"
EX"v18,v18\n"
MF"v[32:35],a[100:103],a[4:7],v[32:35]\n"
BL"v241,s[12:15],s17 offen offset:1024 lds\n"
MF"v[40:43],a[100:103],a[20:23],0\n"
EX"v19,v19\n"
MF"v[40:43],a[96:99],a[16:19],v[40:43]\n"
RD"a[116:119],v226 offset:13312\n"
RT"a[152:153],v243 offset:32\n"
MF"v[48:51],a[96:99],a[32:35],0\n"
EX"v24,v24\n"
MF"v[48:51],a[100:103],a[36:39],v[48:51]\n"
RT"a[154:155],v243 offset:16416\n"
RD"a[120:123],v226 offset:14336\n"
EX"v25,v25\n"
MF"v[56:59],a[100:103],a[52:55],0\n"
EX"v26,v26\n"
PK"v16,v16,v17\n"
MF"v[56:59],a[96:99],a[48:51],v[56:59]\n"
WT"lgkmcnt(4)\n"
EX"v27,v27\n"
"v_mov_b32 v227,v226\n"
MF"v[32:35],a[104:107],a[8:11],v[32:35]\n"
BL"v251,s[12:15],s17 offen offset:2048 lds\n"
MF"v[32:35],a[108:111],a[12:15],v[32:35]\n"
EX"v4,v4\n"
PK"v17,v18,v19\n"
MF"v[40:43],a[108:111],a[28:31],v[40:43]\n"
RD"a[124:127],v226 offset:15360\n"
PK"v24,v24,v25\n"
MF"v[40:43],a[104:107],a[24:27],v[40:43]\n"
EX"v5,v5\n"
MF"v[48:51],a[104:107],a[40:43],v[48:51]\n"
RT"a[156:157],v243 offset:64\n"
RT"a[158:159],v243 offset:16448\n"
MF"v[48:51],a[108:111],a[44:47],v[48:51]\n"
EX"v6,v6\n"
MF"v[56:59],a[108:111],a[60:63],v[56:59]\n"
EX"v7,v7\n"
MF"v[56:59],a[104:107],a[56:59],v[56:59]\n"
EX"v12,v12\n"
PK"v25,v26,v27\n"
MF"v[36:39],a[112:115],a[0:3],0\n"
WT"lgkmcnt(3)\n"
EX"v13,v13\n"
MF"v[36:39],a[116:119],a[4:7],v[36:39]\n"
RD"a[64:67],v227 offset:16384\n"
PK"v2,v4,v5\n"
MF"v[44:47],a[116:119],a[20:23],0\n"
EX"v14,v14\n"
MF"v[44:47],a[112:115],a[16:19],v[44:47]\n"
RT"a[160:161],v243 offset:96\n"
RT"a[162:163],v243 offset:16480\n"
MF"v[52:55],a[112:115],a[32:35],0\n"
EX"v15,v15\n"
MF"v[52:55],a[116:119],a[36:39],v[52:55]\n"
RD"a[68:71],v227 offset:17408\n"
PK"v3,v6,v7\n"
MF"v[60:63],a[116:119],a[52:55],0\n"
EX"v20,v20\n"
MF"v[60:63],a[112:115],a[48:51],v[60:63]\n"
"v_readlane_b32 s43,v228,s42\n"
PK"v10,v12,v13\n"
MF"v[36:39],a[120:123],a[8:11],v[36:39]\n"
WT"lgkmcnt(3)\n"
EX"v21,v21\n"
MF"v[36:39],a[124:127],a[12:15],v[36:39]\n"
RD"a[72:75],v227 offset:18432\n"
PK"v11,v14,v15\n"
MF"v[44:47],a[124:127],a[28:31],v[44:47]\n"
EX"v22,v22\n"
MF"v[44:47],a[120:123],a[24:27],v[44:47]\n"
"s_mov_b32 m0,s40\n"
RT"a[164:165],v243 offset:128\n"
RT"a[166:167],v243 offset:16512\n"
MF"v[52:55],a[120:123],a[40:43],v[52:55]\n"
SA"s17,s17,61440\n"
EX"v23,v23\n"
MF"v[52:55],a[124:127],a[44:47],v[52:55]\n"
RD"a[76:79],v227 offset:19456\n"
"v_readlane_b32 s44,v229,s42\n"
MF"v[60:63],a[124:127],a[60:63],v[60:63]\n"
EX"v28,v28\n"
MF"v[60:63],a[120:123],a[56:59],v[60:63]\n"
WT"lgkmcnt(1)\n"
BL"v225,s[8:11],s43 offen offset:0 sc1 lds\n"
MF"v[80:83],a[64:67],v[64:67],v[80:83]\n"
SA"s42,s42,1\n"
EX"v29,v29\n"
MF"v[112:115],a[64:67],v[68:71],v[112:115]\n"
SA"s41,s41,32768\n"
RD"a[80:83],v227 offset:20480\n"
PK"v18,v20,v21\n"
MF"v[144:147],a[64:67],v[72:75],v[144:147]\n"
"s_cmp_eq_u32 s41,163840\n"
EX"v30,v30\n"
MF"v[176:179],a[64:67],v[76:79],v[176:179]\n"
"s_cselect_b32 s41,0,s41\n"
BS"a[148:151],v224,s[8:11],s18 offen offset:0 sc1\n"
MF"v[84:87],a[68:71],v[64:67],v[84:87]\n"
EX"v31,v31\n"
MF"v[116:119],a[68:71],v[68:71],v[116:119]\n"
RD"a[84:87],v227 offset:21504\n"
"v_add_u32 v226,s41,v224\n"
PK"v19,v22,v23\n"
MF"v[148:151],a[68:71],v[72:75],v[148:151]\n"
EX"v32,v32\n"
MF"v[180:183],a[68:71],v[76:79],v[180:183]\n"
BL"v225,s[8:11],s43 offen offset:1024 sc1 lds\n"
MF"v[88:91],a[72:75],v[64:67],v[88:91]\n"
EX"v33,v33\n"
MF"v[120:123],a[72:75],v[68:71],v[120:123]\n"
RD"a[88:91],v227 offset:22528\n"
PK"v26,v28,v29\n"
MF"v[152:155],a[72:75],v[72:75],v[152:155]\n"
EX"v34,v34\n"
MF"v[184:187],a[72:75],v[76:79],v[184:187]\n"
WT"lgkmcnt(2)\n"
BS"a[152:155],v224,s[8:11],s18 offen offset:2048 sc1\n"
MF"v[92:95],a[76:79],v[64:67],v[92:95]\n"
EX"v35,v35\n"
MF"v[124:127],a[76:79],v[68:71],v[124:127]\n"
RD"a[92:95],v227 offset:23552\n"
PK"v27,v30,v31\n"
MF"v[156:159],a[76:79],v[72:75],v[156:159]\n"
SA"s18,s18,4096\n"
EX"v40,v40\n"
MF"v[188:191],a[76:79],v[76:79],v[188:191]\n"
BL"v225,s[8:11],s43 offen offset:2048 sc1 lds\n"
MF"v[96:99],a[80:83],v[64:67],v[96:99]\n"
EX"v41,v41\n"
MF"v[128:131],a[80:83],v[68:71],v[128:131]\n"
RD"a[96:99],v227 offset:24576\n"
MF"v[160:163],a[80:83],v[72:75],v[160:163]\n"
EX"v42,v42\n"
MF"v[192:195],a[80:83],v[76:79],v[192:195]\n"
WT"lgkmcnt(2)\n"
BS"a[156:159],v224,s[8:11],s18 offen offset:0 sc1\n"
MF"v[100:103],a[84:87],v[64:67],v[100:103]\n"
EX"v43,v43\n"
MF"v[132:135],a[84:87],v[68:71],v[132:135]\n"
RD"a[100:103],v227 offset:25600\n"
MF"v[164:167],a[84:87],v[72:75],v[164:167]\n"
EX"v48,v48\n"
MF"v[196:199],a[84:87],v[76:79],v[196:199]\n"
BL"v225,s[8:11],s43 offen offset:3072 sc1 lds\n"
MF"v[104:107],a[88:91],v[64:67],v[104:107]\n"
EX"v49,v49\n"
MF"v[136:139],a[88:91],v[68:71],v[136:139]\n"
RD"a[104:107],v227 offset:26624\n"
MF"v[168:171],a[88:91],v[72:75],v[168:171]\n"
EX"v50,v50\n"
MF"v[200:203],a[88:91],v[76:79],v[200:203]\n"
WT"lgkmcnt(2)\n"
BS"a[160:163],v224,s[8:11],s18 offen offset:2048 sc1\n"
MF"v[108:111],a[92:95],v[64:67],v[108:111]\n"
SA"m0,s40,16384\n"
EX"v51,v51\n"
MF"v[140:143],a[92:95],v[68:71],v[140:143]\n"
RD"a[108:111],v227 offset:27648\n"
MF"v[172:175],a[92:95],v[72:75],v[172:175]\n"
SA"s18,s18,4096\n"
EX"v56,v56\n"
MF"v[204:207],a[92:95],v[76:79],v[204:207]\n"
BL"v225,s[8:11],s44 offen offset:0 sc1 lds\n"
MF"a[132:135],a[128:131],v[64:67],a[132:135]\n"
SA"s40,s40,32768\n"
EX"v57,v57\n"
MF"a[136:139],a[128:131],v[68:71],a[136:139]\n"
"s_cmp_eq_u32 s40,s46\n"
RD"a[112:115],v227 offset:28672\n"
"s_cselect_b32 s40,s47,s40\n"
MF"a[140:143],a[128:131],v[72:75],a[140:143]\n"
EX"v58,v58\n"
MF"a[144:147],a[128:131],v[76:79],a[144:147]\n"
BS"a[164:167],v224,s[8:11],s18 offen offset:0 sc1\n"
MF"v[80:83],a[96:99],v[0:3],v[80:83]\n"
"s_lshr_b32 s21,s16,1\n"
EX"v59,v59\n"
MF"v[112:115],a[96:99],v[8:11],v[112:115]\n"
"s_sub_u32 s21,s21,s33\n"
RD"a[116:119],v227 offset:29696\n"
MF"v[144:147],a[96:99],v[16:19],v[144:147]\n"
"s_and_b32 s21,s21,3\n"
EX"v36,v36\n"
MF"v[176:179],a[96:99],v[24:27],v[176:179]\n"
WT"lgkmcnt(2)\n"
BL"v225,s[8:11],s44 offen offset:1024 sc1 lds\n"
MF"v[84:87],a[100:103],v[0:3],v[84:87]\n"
"s_lshl_b32 s21,s21,1\n"
EX"v37,v37\n"
MF"v[116:119],a[100:103],v[8:11],v[116:119]\n"
"s_and_b32 s22,s16,1\n"
RD"a[120:123],v227 offset:30720\n"
SA"s21,s21,s22\n"
MF"v[148:151],a[100:103],v[16:19],v[148:151]\n"
"s_lshl_b32 s21,s21,1\n"
EX"v38,v38\n"
MF"v[180:183],a[100:103],v[24:27],v[180:183]\n"
"s_and_b32 s22,s30,1\n"
MF"v[88:91],a[104:107],v[0:3],v[88:91]\n"
SA"s21,s21,s22\n"
EX"v39,v39\n"
MF"v[120:123],a[104:107],v[8:11],v[120:123]\n"
"s_mul_i32 s21,s21,10240\n"
RD"a[124:127],v227 offset:31744\n"
MF"v[152:155],a[104:107],v[16:19],v[152:155]\n"
"s_lshr_b32 s22,s30,1\n"
EX"v44,v44\n"
MF"v[184:187],a[104:107],v[24:27],v[184:187]\n"
"s_lshl_b32 s22,s22,10\n"
BL"v225,s[8:11],s44 offen offset:2048 sc1 lds\n"
MF"v[92:95],a[108:111],v[0:3],v[92:95]\n"
SA"s21,s21,s22\n"
EX"v45,v45\n"
MF"v[124:127],a[108:111],v[8:11],v[124:127]\n"
"s_mul_i32 s22,s35,163840\n"
MF"v[156:159],a[108:111],v[16:19],v[156:159]\n"
SA"s21,s21,s22\n"
EX"v46,v46\n"
MF"v[188:191],a[108:111],v[24:27],v[188:191]\n"
WT"lgkmcnt(1)\n"
WT"lgkmcnt(0)\n"
SA"s18,s21,3801088\n"
MF"v[96:99],a[112:115],v[0:3],v[96:99]\n"
SA"s16,s16,1\n"
"s_barrier\n"
MF"v[128:131],a[112:115],v[8:11],v[128:131]\n"
RD"a[64:67],v226 offset:0\n"
MF"v[160:163],a[112:115],v[16:19],v[160:163]\n"
EX"v47,v47\n"
MF"v[192:195],a[112:115],v[24:27],v[192:195]\n"
BL"v225,s[8:11],s44 offen offset:3072 sc1 lds\n"
MF"v[100:103],a[116:119],v[0:3],v[100:103]\n"
EX"v52,v52\n"
MF"v[132:135],a[116:119],v[8:11],v[132:135]\n"
RD"a[68:71],v226 offset:1024\n"
MF"v[164:167],a[116:119],v[16:19],v[164:167]\n"
EX"v53,v53\n"
MF"v[196:199],a[116:119],v[24:27],v[196:199]\n"
RD"a[72:75],v226 offset:2048\n"
MF"v[104:107],a[120:123],v[0:3],v[104:107]\n"
EX"v54,v54\n"
MF"v[136:139],a[120:123],v[8:11],v[136:139]\n"
RD"a[76:79],v226 offset:3072\n"
MF"v[168:171],a[120:123],v[16:19],v[168:171]\n"
EX"v55,v55\n"
MF"v[200:203],a[120:123],v[24:27],v[200:203]\n"
MF"v[108:111],a[124:127],v[0:3],v[108:111]\n"
EX"v60,v60\n"
MF"v[140:143],a[124:127],v[8:11],v[140:143]\n"
RD"a[80:83],v226 offset:4096\n"
MF"v[172:175],a[124:127],v[16:19],v[172:175]\n"
EX"v61,v61\n"
MF"v[204:207],a[124:127],v[24:27],v[204:207]\n"
RD"a[84:87],v226 offset:5120\n"
MF"a[132:135],a[128:131],v[0:3],a[132:135]\n"
EX"v62,v62\n"
MF"a[136:139],a[128:131],v[8:11],a[136:139]\n"
RD"a[88:91],v226 offset:6144\n"
MF"a[140:143],a[128:131],v[16:19],a[140:143]\n"
WT"lgkmcnt(5)\n"
"s_sub_u32 s20,s20,1\n"
EX"v63,v63\n"
MF"a[144:147],a[128:131],v[24:27],a[144:147]\n"
"s_cbranch_scc0 LRT_%=\n"
"s_lshl_b32 s21,s35,8\n"
SA"s21,s21,92160\n"
SA"s21,s21,s45\n"
WT"vmcnt(3)\n"
"s_barrier\n"
"buffer_store_dwordx2 v[232:233],v230,s[8:11],s21 offen sc1\n"
"v_cmp_eq_u64 vcc,s[28:29],v[244:245]\n"
"s_cmp_eq_u64 vcc,exec\n"
"s_cbranch_scc0 L1105_%=\n"
"L1106_%=:\n"
"s_mov_b32 s57,0\n"
"s_mov_b32 s56,0\n"
"s_mov_b32 s14,0\n"
"s_branch LP1_%=\n"
"LEND_%=:\n"
      : : [ka] "s"(__builtin_amdgcn_kernarg_segment_ptr()), [b] "s"(blockIdx.x), [t] "v"(threadIdx.x)
      : "v1", "v2", "v3", "v4", "v5", "v6", "v7", "v8", "v9", "v10", "v11", "v12", "v13", "v14", "v15", "v16", "v17", "v18", "v19", "v20", "v21", "v22", "v23", "v24", "v25", "v26", "v27", "v28", "v29", "v30", "v31", "v32", "v33", "v34", "v35", "v36", "v37", "v38", "v39", "v40", "v41", "v42", "v43", "v44", "v45", "v46", "v47", "v48", "v49", "v50", "v51", "v52", "v53", "v54", "v55", "v56", "v57", "v58", "v59", "v60", "v61", "v62", "v63", "v64", "v65", "v66", "v67", "v68", "v69", "v70", "v71", "v72", "v73", "v74", "v75", "v76", "v77", "v78", "v79", "v80", "v81", "v82", "v83", "v84", "v85", "v86", "v87", "v88", "v89", "v90", "v91", "v92", "v93", "v94", "v95", "v96", "v97", "v98", "v99", "v100", "v101", "v102", "v103", "v104", "v105", "v106", "v107", "v108", "v109", "v110", "v111", "v112", "v113", "v114", "v115", "v116", "v117", "v118", "v119", "v120", "v121", "v122", "v123", "v124", "v125", "v126", "v127", "v128", "v129", "v130", "v131", "v132", "v133", "v134", "v135", "v136", "v137", "v138", "v139", "v140", "v141", "v142", "v143", "v144", "v145", "v146", "v147", "v148", "v149", "v150", "v151", "v152", "v153", "v154", "v155", "v156", "v157", "v158", "v159", "v160", "v161", "v162", "v163", "v164", "v165", "v166", "v167", "v168", "v169", "v170", "v171", "v172", "v173", "v174", "v175", "v176", "v177", "v178", "v179", "v180", "v181", "v182", "v183", "v184", "v185", "v186", "v187", "v188", "v189", "v190", "v191", "v192", "v193", "v194", "v195", "v196", "v197", "v198", "v199", "v200", "v201", "v202", "v203", "v204", "v205", "v206", "v207", "v208", "v209", "v210", "v211", "v212", "v213", "v214", "v215", "v216", "v217", "v218", "v219", "v220", "v221", "v222", "v223", "v224", "v225", "v226", "v227", "v228", "v229", "v230", "v231", "v232", "v233", "v234", "v235", "v236", "v237", "v238", "v239", "v240", "v241", "v242", "v243", "v244", "v245", "v246", "v247", "v248", "v249", "v250", "v251", "v252", "v253", "v254", "v255", "a0", "a1", "a2", "a3", "a4", "a5", "a6", "a7", "a8", "a9", "a10", "a11", "a12", "a13", "a14", "a15", "a16", "a17", "a18", "a19", "a20", "a21", "a22", "a23", "a24", "a25", "a26", "a27", "a28", "a29", "a30", "a31", "a32", "a33", "a34", "a35", "a36", "a37", "a38", "a39", "a40", "a41", "a42", "a43", "a44", "a45", "a46", "a47", "a48", "a49", "a50", "a51", "a52", "a53", "a54", "a55", "a56", "a57", "a58", "a59", "a60", "a61", "a62", "a63", "a64", "a65", "a66", "a67", "a68", "a69", "a70", "a71", "a72", "a73", "a74", "a75", "a76", "a77", "a78", "a79", "a80", "a81", "a82", "a83", "a84", "a85", "a86", "a87", "a88", "a89", "a90", "a91", "a92", "a93", "a94", "a95", "a96", "a97", "a98", "a99", "a100", "a101", "a102", "a103", "a104", "a105", "a106", "a107", "a108", "a109", "a110", "a111", "a112", "a113", "a114", "a115", "a116", "a117", "a118", "a119", "a120", "a121", "a122", "a123", "a124", "a125", "a126", "a127", "a128", "a129", "a130", "a131", "a132", "a133", "a134", "a135", "a136", "a137", "a138", "a139", "a140", "a141", "a142", "a143", "a144", "a145", "a146", "a147", "a148", "a149", "a150", "a151", "a152", "a153", "a154", "a155", "a156", "a157", "a158", "a159", "a160", "a161", "a162", "a163", "a164", "a165", "a166", "a167", "a168", "a169", "a170", "a171", "a172", "a173", "a174", "a175", "a176", "a177", "a178", "a179", "a180", "a181", "a182", "a183", "a184", "a185", "a186", "a187", "a188", "a189", "a190", "a191", "a192", "a193", "a194", "a195", "a196", "a197", "a198", "a199", "a200", "a201", "a202", "a203", "a204", "a205", "a206", "a207", "a208", "a209", "a210", "a211", "a212", "a213", "a214", "a215", "a216", "a217", "a218", "a219", "a220", "a221", "a222", "a223", "a224", "a225", "a226", "a227", "a228", "a229", "a230", "a231", "a232", "a233", "a234", "a235", "a236", "a237", "a238", "a239", "a240", "a241", "a242", "a243", "a244", "a245", "a246", "a247", "a248", "a249", "a250", "a251", "a252", "a253", "a254", "a255", "s8", "s9", "s10", "s11", "s12", "s13", "s14", "s15", "s16", "s17", "s18", "s19", "s20", "s21", "s22", "s23", "s24", "s25", "s26", "s27", "s28", "s29", "s30", "s31", "s32", "s33", "s34", "s35", "s36", "s37", "s38", "s39", "s40", "s41", "s42", "s43", "s44", "s45", "s46", "s47", "s48", "s49", "s50", "s51", "s52", "s53", "s54", "s55", "s56", "s57", "s58", "s59", "s60", "s61", "s62", "s63", "s64", "s65", "s66", "s67", "s68", "s69", "s70", "s71", "s72", "s73", "s74", "s75", "s76", "s77", "s78", "s79", "s80", "s81", "s82", "s83", "s84", "s85", "s86", "s87", "s88", "s89", "s90", "s91", "s92", "s93", "s94", "s95", "s96", "s97", "s98", "s99", "scc", "vcc", "memory");
}

extern "C" void kernel_launch(void* const* d_in, const int* in_sizes, int n_in, void* d_out, int out_size, void* d_ws, size_t ws_size, hipStream_t stream) {
  (void)hipFuncSetAttribute((const void*)mega_kernel, hipFuncAttributeMaxDynamicSharedMemorySize, 163840);
  mega_kernel<<<256, 256, 163840, stream>>>((const bf16*)d_in[0], (const bf16*)d_in[1], (const bf16*)d_in[2], (const bf16*)d_in[3], (const bf16*)d_in[4], (const bf16*)d_in[5],
      (const bf16*)d_in[7], (const bf16*)d_in[8], (const bf16*)d_in[9], (const bf16*)d_in[10], (char*)d_ws + (size_t)0 * 1048576, (bf16*)d_out);
}
```

```cpp
#include <hip/hip_runtime.h>
#include <hip/hip_bf16.h>
typedef __hip_bfloat16 bf16;
#define MF "v_mfma_f32_16x16x32_bf16 "
#define RD "ds_read_b128 "
#define RT "ds_read_b64_tr_b16 "
#define BL "buffer_load_dwordx4 "
#define BS "buffer_store_dwordx4 "
#define PK "v_cvt_pk_bf16_f32 "
#define EX "v_exp_f32 "
#define WT "s_waitcnt "
#define MU "v_mul_f32 "
#define SA "s_add_u32 "

__global__ __launch_bounds__(256, 1) void mega_kernel(const bf16* x, const bf16* ckv, const bf16* wq, const bf16* wuk, const bf16* wuv, const bf16* wo, const bf16* w_r,
                                                      const bf16* wg, const bf16* wu, const bf16* wd, char* ws, bf16* out) {
  asm volatile(
"s_load_dwordx16 s[64:79],%[ka],0\n"
"s_load_dwordx8 s[80:87],%[ka],64\n"
"s_mov_b32 s31,%[b]\n"
"s_lshr_b32 s92,s31,3\n"
"s_min_u32 s93,s92,12\n"
"s_cmp_eq_u32 s93,12\n"
"s_cselect_b32 s93,10,s93\n"
"s_lshl_b32 s93,s93,3\n"
"s_load_dwordx2 s[94:95],%[ka],s93\n"
"v_lshrrev_b32 v1,6,%[t]\n"
"v_and_b32 v2,63,%[t]\n"
"v_readfirstlane_b32 s30,v1\n"
"v_lshlrev_b32 v224,4,v2\n"
"s_getpc_b64 s[24:25]\n"
"LPC_%=:\n"
"s_and_b32 s25,s25,0xffff\n"
"s_mov_b32 s26,LEND_%=-LPC_%=\n"
"s_mov_b32 s27,0x20000\n"
"s_lshr_b32 s49,s31,3\n"
"s_lshl_b32 s49,s49,2\n"
SA"s49,s49,s30\n"
"s_lshl_b32 s49,s49,7\n"
"v_lshlrev_b32 v10,14,v2\n"
"buffer_load_dword v255,v10,s[24:27],s49 offen\n"
"s_and_b32 s32,s31,7\n"
"s_lshr_b32 s33,s31,6\n"
"s_bfe_u32 s34,s31,0x30003\n"
"s_lshl_b32 s35,s32,2\n"
SA"s35,s35,s33\n"
"s_mov_b32 s46,0x65327410\n"
"s_mov_b32 s47,0x54217630\n"
"s_bitcmp1_b32 s34,0\n"
"s_cselect_b32 s46,0x64317520,s46\n"
"s_cselect_b32 s47,0x64327510,s47\n"
"s_bitcmp1_b32 s34,1\n"
"s_cselect_b32 s46,s47,s46\n"
"s_mul_i32 s47,s32,0x11111111\n"
SA"s46,s46,s47\n"
"s_lshl_b32 s36,s30,12\n"
"s_mul_i32 s37,s30,24576\n"
SA"s37,s37,65536\n"
"v_lshrrev_b32 v3,3,v2\n"
"v_lshrrev_b32 v4,4,v2\n"
"v_and_b32 v5,7,v2\n"
"v_xor_b32 v6,v5,v4\n"
"v_lshlrev_b32 v6,4,v6\n"
"v_lshl_add_u32 v6,v3,10,v6\n"
"s_lshl_b32 s50,s30,15\n"
"v_add_u32 v234,s50,v6\n"
"v_xor_b32 v235,64,v234\n"
"v_add_u32 v235,7168,v235\n"
"v_add_u32 v236,14336,v234\n"
"v_add_u32 v237,14336,v235\n"
"v_and_b32 v7,1,v4\n"
"v_lshlrev_b32 v7,1,v7\n"
"v_xor_b32 v7,v7,v5\n"
"v_lshlrev_b32 v7,4,v7\n"
"v_lshl_add_u32 v238,v3,13,v7\n"
"v_xor_b32 v239,64,v238\n"
"v_add_u32 v239,64512,v239\n"
"v_add_u32 v240,129024,v238\n"
"v_add_u32 v241,129024,v239\n"
"v_and_b32 v8,31,v2\n"
"v_lshlrev_b32 v8,3,v8\n"
"v_lshrrev_b32 v9,5,v2\n"
"s_lshl_b32 s50,s31,2\n"
SA"s50,s50,s30\n"
"s_mul_i32 s50,s50,2560\n"
"v_mul_u32_u24 v9,1280,v9\n"
"v_add3_u32 v9,v9,v8,s50\n"
"s_mov_b32 s52,0xaad5556a\n"
"s_mov_b32 s53,0x5900d5aa\n"
"s_cmp_ge_u32 s93,64\n"
"s_cselect_b32 s52,0x6a005959,s52\n"
"s_lshr_b64 s[52:53],s[52:53],s93\n"
"s_and_b32 s52,s52,255\n"
"s_lshl_b32 s52,s52,13\n"
"s_mul_i32 s52,s52,s30\n"
"s_lshl_b32 s53,s92,2\n"
SA"s53,s53,s30\n"
"s_sub_u32 s53,s53,47\n"
"s_lshl_b32 s53,s53,20\n"
"s_min_u32 s53,s53,82575356\n"
"s_cmp_ge_u32 s92,12\n"
"s_cselect_b32 s93,s53,s52\n"
"s_lshl_b32 s50,s34,17\n"
"s_lshl_b32 s51,s35,8\n"
"s_and_b32 s52,s30,1\n"
"s_lshl_b32 s52,s52,7\n"
SA"s52,s52,s51\n"
"s_mov_b32 s14,0x120000\n"
"s_mov_b32 s15,0x20000\n"
"s_sub_u32 s18,4194304,s52\n"
"s_mov_b32 s19,0x20000\n"
WT"lgkmcnt(0)\n"
SA"s94,s94,s93\n"
"s_addc_u32 s95,s95,0\n"
"s_load_dword s92,s[94:95],0\n"
"s_load_dwordx2 s[28:29],s[84:85],0\n"
SA"s12,s66,s50\n"
"s_addc_u32 s13,s67,0\n"
"s_and_b32 s13,s13,0xffff\n"
"s_cmp_lt_u32 s30,2\n"
"s_cselect_b64 s[16:17],s[70:71],s[72:73]\n"
SA"s16,s16,s52\n"
"s_addc_u32 s17,s17,0\n"
"s_and_b32 s17,s17,0xffff\n"
"global_load_dwordx2 v[128:129],v9,s[64:65] offset:0\n"
"global_load_dwordx2 v[130:131],v9,s[64:65] offset:256\n"
"global_load_dwordx2 v[132:133],v9,s[64:65] offset:512\n"
"global_load_dwordx2 v[134:135],v9,s[64:65] offset:768\n"
"global_load_dwordx2 v[136:137],v9,s[64:65] offset:1024\n"
"global_load_dwordx2 v[138:139],v8,s[76:77] offset:0\n"
"global_load_dwordx2 v[140:141],v8,s[76:77] offset:256\n"
"global_load_dwordx2 v[142:143],v8,s[76:77] offset:512\n"
"global_load_dwordx2 v[144:145],v8,s[76:77] offset:768\n"
"global_load_dwordx2 v[146:147],v8,s[76:77] offset:1024\n"
SA"m0,s37,0\n"
"s_bfe_u32 s48,s46,0x30000\n"
"s_lshl_b32 s48,s48,19\n"
BL"v238,s[16:19],s48 offen offset:0 lds\n"
BL"v239,s[16:19],s48 offen offset:1024 lds\n"
BL"v240,s[16:19],s48 offen offset:2048 lds\n"
BL"v241,s[16:19],s48 offen offset:3072 lds\n"
SA"m0,s37,4096\n"
"s_bfe_u32 s48,s46,0x30000\n"
"s_lshl_b32 s48,s48,19\n"
"s_bitset1_b32 s48,18\n"
BL"v238,s[16:19],s48 offen offset:0 lds\n"
BL"v239,s[16:19],s48 offen offset:1024 lds\n"
BL"v240,s[16:19],s48 offen offset:2048 lds\n"
BL"v241,s[16:19],s48 offen offset:3072 lds\n"
"v_lshlrev_b32 v253,7,%[t]\n"
"s_mov_b32 s8,s84\n"
"s_and_b32 s9,s85,0xffff\n"
"s_mov_b32 s10,82575360\n"
"s_mov_b32 s11,0x20000\n"
SA"s20,s68,s51\n"
"s_addc_u32 s21,s69,0\n"
"s_and_b32 s21,s21,0xffff\n"
"s_sub_u32 s22,5242880,s51\n"
"s_mov_b32 s23,0x20000\n"
"v_and_b32 v11,15,v2\n"
"v_lshrrev_b32 v12,1,v11\n"
"v_xor_b32 v12,v12,v4\n"
"v_lshlrev_b32 v12,4,v12\n"
"v_lshl_add_u32 v242,v11,7,v12\n"
"v_xor_b32 v243,64,v242\n"
"v_lshrrev_b32 v12,2,v11\n"
"v_lshl_add_u32 v12,v4,3,v12\n"
"v_lshlrev_b32 v12,7,v12\n"
"v_and_b32 v13,3,v2\n"
"v_lshl_add_u32 v12,v13,3,v12\n"
"v_and_b32 v13,3,v3\n"
"v_lshl_add_u32 v12,v13,5,v12\n"
"v_add_u32 v246,s37,v12\n"
"v_xor_b32 v247,32,v246\n"
"v_xor_b32 v248,64,v246\n"
"v_xor_b32 v249,96,v246\n"
"v_mov_b32 v13,2130706432\n"
"v_cmp_eq_u32 vcc,0,v2\n"
"s_cmp_eq_u32 s30,0\n"
"s_cselect_b32 s45,0,2130706432\n"
"v_cndmask_b32_e64 v230,v13,0,vcc\n"
"s_lshl_b32 s39,s30,3\n"
"s_lshr_b32 s98,s46,s39\n"
"s_and_b32 s39,s98,7\n"
"s_lshl_b32 s39,s39,7\n"
"s_and_b32 s98,s98,0x70\n"
"s_lshl_b32 s98,s98,3\n"
"s_lshl_b32 s50,s30,15\n"
"s_sub_u32 s50,1048576,s50\n"
SA"s39,s39,s50\n"
SA"s98,s98,s50\n"
"s_mov_b32 s55,0x3e0293ee\n"
"s_cmp_lt_u32 s30,2\n"
"s_cbranch_scc0 LV_%=\n"
WT"vmcnt(17)\n"
"v_lshlrev_b32 v148,16,v128\n"
"v_and_b32 v149,0xffff0000,v128\n"
"v_lshlrev_b32 v150,16,v129\n"
"v_and_b32 v151,0xffff0000,v129\n"
WT"vmcnt(16)\n"
"v_lshlrev_b32 v152,16,v130\n"
"v_and_b32 v153,0xffff0000,v130\n"
"v_lshlrev_b32 v154,16,v131\n"
"v_and_b32 v155,0xffff0000,v131\n"
WT"vmcnt(15)\n"
"v_lshlrev_b32 v156,16,v132\n"
"v_and_b32 v157,0xffff0000,v132\n"
"v_lshlrev_b32 v158,16,v133\n"
"v_and_b32 v159,0xffff0000,v133\n"
WT"vmcnt(14)\n"
"v_lshlrev_b32 v160,16,v134\n"
"v_and_b32 v161,0xffff0000,v134\n"
"v_lshlrev_b32 v162,16,v135\n"
"v_and_b32 v163,0xffff0000,v135\n"
WT"vmcnt(13)\n"
"v_lshlrev_b32 v164,16,v136\n"
"v_and_b32 v165,0xffff0000,v136\n"
"v_lshlrev_b32 v166,16,v137\n"
"v_and_b32 v167,0xffff0000,v137\n"
"v_add_f32 v188,v148,v149\n"
"v_add_f32 v189,v150,v151\n"
"v_add_f32 v188,v188,v152\n"
"v_add_f32 v189,v189,v153\n"
"v_add_f32 v188,v188,v154\n"
"v_add_f32 v189,v189,v155\n"
"v_add_f32 v188,v188,v156\n"
"v_add_f32 v189,v189,v157\n"
"v_add_f32 v188,v188,v158\n"
"v_add_f32 v189,v189,v159\n"
"v_add_f32 v188,v188,v160\n"
"v_add_f32 v189,v189,v161\n"
"v_add_f32 v188,v188,v162\n"
"v_add_f32 v189,v189,v163\n"
"v_add_f32 v188,v188,v164\n"
"v_add_f32 v189,v189,v165\n"
"v_add_f32 v188,v188,v166\n"
"v_add_f32 v189,v189,v167\n"
"v_add_f32 v188,v188,v189\n"
"s_nop 1\n"
"v_add_f32_dpp v188,v188,v188 quad_perm:[1,0,3,2] row_mask:0xf bank_mask:0xf\n"
WT"vmcnt(12)\n"
"v_lshlrev_b32 v168,16,v138\n"
"v_and_b32 v169,0xffff0000,v138\n"
"v_add_f32_dpp v188,v188,v188 quad_perm:[2,3,0,1] row_mask:0xf bank_mask:0xf\n"
"v_lshlrev_b32 v170,16,v139\n"
"v_and_b32 v171,0xffff0000,v139\n"
"v_add_f32_dpp v188,v188,v188 row_half_mirror row_mask:0xf bank_mask:0xf\n"
WT"vmcnt(11)\n"
"v_lshlrev_b32 v172,16,v140\n"
"v_and_b32 v173,0xffff0000,v140\n"
"v_add_f32_dpp v188,v188,v188 row_mirror row_mask:0xf bank_mask:0xf\n"
"v_lshlrev_b32 v174,16,v141\n"
"v_and_b32 v175,0xffff0000,v141\n"
"v_mov_b32 v189,v188\n"
WT"vmcnt(10)\n"
"v_lshlrev_b32 v176,16,v142\n"
"v_and_b32 v177,0xffff0000,v142\n"
"v_permlane16_swap_b32 v189,v188\n"
"v_lshlrev_b32 v178,16,v143\n"
"v_and_b32 v179,0xffff0000,v143\n"
"v_add_f32 v188,v188,v189\n"
WT"vmcnt(9)\n"
"v_lshlrev_b32 v180,16,v144\n"
SA"m0,s36,0\n"
"s_bfe_u32 s48,s46,0x30000\n"
"s_lshl_b32 s48,s48,7\n"
BL"v234,s[12:15],s48 offen offset:0 lds\n"
BL"v235,s[12:15],s48 offen offset:1024 lds\n"
BL"v236,s[12:15],s48 offen offset:2048 lds\n"
BL"v237,s[12:15],s48 offen offset:3072 lds\n"
"v_and_b32 v181,0xffff0000,v144\n"
MU"v191,0x3acccccd,v188\n"
"v_lshlrev_b32 v182,16,v145\n"
"v_and_b32 v183,0xffff0000,v145\n"
PK"v128,v191,v191\n"
WT"vmcnt(12)\n"
"v_lshlrev_b32 v184,16,v146\n"
"v_and_b32 v185,0xffff0000,v146\n"
"v_lshlrev_b32 v128,16,v128\n"
"v_lshlrev_b32 v186,16,v147\n"
"v_and_b32 v187,0xffff0000,v147\n"
"v_sub_f32 v148,v148,v128\n"
"v_sub_f32 v149,v149,v128\n"
"v_sub_f32 v150,v150,v128\n"
"v_sub_f32 v151,v151,v128\n"
"v_sub_f32 v152,v152,v128\n"
"v_sub_f32 v153,v153,v128\n"
"v_sub_f32 v154,v154,v128\n"
"v_sub_f32 v155,v155,v128\n"
"v_sub_f32 v156,v156,v128\n"
"v_sub_f32 v157,v157,v128\n"
"v_sub_f32 v158,v158,v128\n"
"v_sub_f32 v159,v159,v128\n"
"v_sub_f32 v160,v160,v128\n"
"v_sub_f32 v161,v161,v128\n"
"v_sub_f32 v162,v162,v128\n"
"v_sub_f32 v163,v163,v128\n"
"v_sub_f32 v164,v164,v128\n"
"v_sub_f32 v165,v165,v128\n"
"v_sub_f32 v166,v166,v128\n"
"v_sub_f32 v167,v167,v128\n"
MU"v188,v148,v148\n"
MU"v189,v149,v149\n"
"v_fmac_f32 v188,v150,v150\n"
"v_fmac_f32 v189,v151,v151\n"
"v_fmac_f32 v188,v152,v152\n"
"v_fmac_f32 v189,v153,v153\n"
"v_fmac_f32 v188,v154,v154\n"
"v_fmac_f32 v189,v155,v155\n"
"v_fmac_f32 v188,v156,v156\n"
"v_fmac_f32 v189,v157,v157\n"
"v_fmac_f32 v188,v158,v158\n"
"v_fmac_f32 v189,v159,v159\n"
"v_fmac_f32 v188,v160,v160\n"
"v_fmac_f32 v189,v161,v161\n"
"v_fmac_f32 v188,v162,v162\n"
"v_fmac_f32 v189,v163,v163\n"
"v_fmac_f32 v188,v164,v164\n"
"v_fmac_f32 v189,v165,v165\n"
"v_fmac_f32 v188,v166,v166\n"
"v_fmac_f32 v189,v167,v167\n"
"v_add_f32 v188,v188,v189\n"
PK"v149,v148,v149\n"
"v_lshlrev_b32 v148,16,v149\n"
"v_add_f32_dpp v188,v188,v188 quad_perm:[1,0,3,2] row_mask:0xf bank_mask:0xf\n"
"v_and_b32 v149,0xffff0000,v149\n"
PK"v151,v150,v151\n"
"v_add_f32_dpp v188,v188,v188 quad_perm:[2,3,0,1] row_mask:0xf bank_mask:0xf\n"
"v_lshlrev_b32 v150,16,v151\n"
"v_and_b32 v151,0xffff0000,v151\n"
SA"m0,s37,8192\n"
"s_bfe_u32 s48,s46,0x30004\n"
"s_lshl_b32 s48,s48,19\n"
BL"v238,s[16:19],s48 offen offset:0 lds\n"
BL"v239,s[16:19],s48 offen offset:1024 lds\n"
BL"v240,s[16:19],s48 offen offset:2048 lds\n"
BL"v241,s[16:19],s48 offen offset:3072 lds\n"
"v_add_f32_dpp v188,v188,v188 row_half_mirror row_mask:0xf bank_mask:0xf\n"
PK"v153,v152,v153\n"
"v_lshlrev_b32 v152,16,v153\n"
"v_add_f32_dpp v188,v188,v188 row_mirror row_mask:0xf bank_mask:0xf\n"
"v_and_b32 v153,0xffff0000,v153\n"
PK"v155,v154,v155\n"
"v_mov_b32 v189,v188\n"
"v_lshlrev_b32 v154,16,v155\n"
"v_and_b32 v155,0xffff0000,v155\n"
"v_permlane16_swap_b32 v189,v188\n"
PK"v157,v156,v157\n"
"v_lshlrev_b32 v156,16,v157\n"
"v_add_f32 v188,v188,v189\n"
"v_and_b32 v157,0xffff0000,v157\n"
PK"v159,v158,v159\n"
"v_sub_f32 v191,v191,v128\n"
"v_lshlrev_b32 v158,16,v159\n"
"v_and_b32 v159,0xffff0000,v159\n"
MU"v188,0x3acccccd,v188\n"
PK"v161,v160,v161\n"
"v_lshlrev_b32 v160,16,v161\n"
"v_fma_f32 v188,-v191,v191,v188\n"
"v_and_b32 v161,0xffff0000,v161\n"
PK"v163,v162,v163\n"
"v_max_f32 v188,0,v188\n"
"v_lshlrev_b32 v162,16,v163\n"
"v_and_b32 v163,0xffff0000,v163\n"
PK"v188,v188,v188\n"
PK"v165,v164,v165\n"
"v_lshlrev_b32 v164,16,v165\n"
"v_lshlrev_b32 v188,16,v188\n"
"v_and_b32 v165,0xffff0000,v165\n"
PK"v167,v166,v167\n"
"v_add_f32 v188,0x3727c5ac,v188\n"
"v_lshlrev_b32 v166,16,v167\n"
"v_and_b32 v167,0xffff0000,v167\n"
PK"v188,v188,v188\n"
"v_bfe_u32 v130,v224,5,4\n"
"v_mul_u32_u24 v130,144,v130\n"
"v_lshlrev_b32 v188,16,v188\n"
"v_bfe_u32 v131,v224,4,1\n"
"v_lshl_add_u32 v130,v131,3,v130\n"
"v_sqrt_f32 v189,v188\n"
"v_lshrrev_b32 v131,9,v224\n"
"v_lshl_add_u32 v130,v131,4,v130\n"
"v_rcp_f32 v190,v189\n"
"s_lshl_b32 s52,s30,5\n"
"v_add_u32 v130,s52,v130\n"
"v_fma_f32 v188,-v189,v189,v188\n"
MU"v190,0.5,v190\n"
"v_fma_f32 v189,v188,v190,v189\n"
PK"v189,v189,v189\n"
"v_lshlrev_b32 v189,16,v189\n"
"v_rcp_f32 v129,v189\n"
"s_nop 0\n"
MU"v148,v148,v129\n"
MU"v149,v149,v129\n"
PK"v138,v148,v149\n"
MU"v150,v150,v129\n"
MU"v151,v151,v129\n"
SA"m0,s37,12288\n"
"s_bfe_u32 s48,s46,0x30004\n"
"s_lshl_b32 s48,s48,19\n"
"s_bitset1_b32 s48,18\n"
BL"v238,s[16:19],s48 offen offset:0 lds\n"
BL"v239,s[16:19],s48 offen offset:1024 lds\n"
BL"v240,s[16:19],s48 offen offset:2048 lds\n"
BL"v241,s[16:19],s48 offen offset:3072 lds\n"
PK"v139,v150,v151\n"
"ds_write_b64 v130,v[138:139] offset:49152\n"
MU"v152,v152,v129\n"
MU"v153,v153,v129\n"
PK"v140,v152,v153\n"
MU"v154,v154,v129\n"
MU"v155,v155,v129\n"
PK"v141,v154,v155\n"
"ds_write_b64 v130,v[140:141] offset:51456\n"
MU"v156,v156,v129\n"
MU"v157,v157,v129\n"
PK"v142,v156,v157\n"
MU"v158,v158,v129\n"
MU"v159,v159,v129\n"
PK"v143,v158,v159\n"
"ds_write_b64 v130,v[142:143] offset:53760\n"
MU"v160,v160,v129\n"
MU"v161,v161,v129\n"
PK"v144,v160,v161\n"
MU"v162,v162,v129\n"
MU"v163,v163,v129\n"
PK"v145,v162,v163\n"
"ds_write_b64 v130,v[144:145] offset:56064\n"
MU"v164,v164,v129\n"
MU"v165,v165,v129\n"
PK"v146,v164,v165\n"
MU"v166,v166,v129\n"
MU"v167,v167,v129\n"
PK"v147,v166,v167\n"
"ds_write_b64 v130,v[146:147] offset:58368\n"
"v_lshlrev_b32 v148,16,v138\n"
"v_and_b32 v149,0xffff0000,v138\n"
MU"v188,v148,v168\n"
MU"v189,v149,v169\n"
"v_lshlrev_b32 v150,16,v139\n"
"v_and_b32 v151,0xffff0000,v139\n"
"v_fmac_f32 v188,v150,v170\n"
"v_fmac_f32 v189,v151,v171\n"
"v_lshlrev_b32 v152,16,v140\n"
"v_and_b32 v153,0xffff0000,v140\n"
"v_fmac_f32 v188,v152,v172\n"
"v_fmac_f32 v189,v153,v173\n"
"v_lshlrev_b32 v154,16,v141\n"
"v_and_b32 v155,0xffff0000,v141\n"
"v_fmac_f32 v188,v154,v174\n"
"v_fmac_f32 v189,v155,v175\n"
"v_lshlrev_b32 v156,16,v142\n"
"v_and_b32 v157,0xffff0000,v142\n"
"v_fmac_f32 v188,v156,v176\n"
"v_fmac_f32 v189,v157,v177\n"
"v_lshlrev_b32 v158,16,v143\n"
"v_and_b32 v159,0xffff0000,v143\n"
"v_fmac_f32 v188,v158,v178\n"
"v_fmac_f32 v189,v159,v179\n"
"v_lshlrev_b32 v160,16,v144\n"
"v_and_b32 v161,0xffff0000,v144\n"
"v_fmac_f32 v188,v160,v180\n"
"v_fmac_f32 v189,v161,v181\n"
"v_lshlrev_b32 v162,16,v145\n"
"v_and_b32 v163,0xffff0000,v145\n"
WT"lgkmcnt(0)\n"
SA"s28,s28,0x7f4a7c15\n"
"s_addc_u32 s29,s29,0x9e3779b9\n"
"v_mov_b32 v232,s28\n"
"v_mov_b32 v233,s29\n"
WT"vmcnt(16)\n"
RT"a[0:1],v246 offset:0\n"
RT"a[2:3],v246 offset:512\n"
RT"a[4:5],v247 offset:0\n"
RT"a[6:7],v247 offset:512\n"
RT"a[8:9],v248 offset:0\n"
RT"a[10:11],v248 offset:512\n"
RT"a[12:13],v249 offset:0\n"
RT"a[14:15],v249 offset:512\n"
WT"vmcnt(12)\n"
RT"a[16:17],v246 offset:4096\n"
RT"a[18:19],v246 offset:4608\n"
RT"a[20:21],v247 offset:4096\n"
RT"a[22:23],v247 offset:4608\n"
RT"a[24:25],v248 offset:4096\n"
RT"a[26:27],v248 offset:4608\n"
RT"a[28:29],v249 offset:4096\n"
RT"a[30:31],v249 offset:4608\n"
WT"vmcnt(8) lgkmcnt(15)\n"
"s_barrier\n"
RD"v[192:195],v242 offset:0\n"
RD"v[196:199],v243 offset:0\n"
RD"v[200:203],v242 offset:2048\n"
RD"v[204:207],v243 offset:2048\n"
RD"v[208:211],v242 offset:4096\n"
"v_lshrrev_b32 v131,7,v224\n"
WT"lgkmcnt(4)\n"
MF"v[0:3],a[0:3],v[192:195],0\n"
WT"lgkmcnt(3)\n"
RD"v[212:215],v243 offset:4096\n"
"v_lshl_add_u32 v131,v131,4,v224\n"
MF"v[0:3],a[16:19],v[196:199],v[0:3]\n"
SA"m0,s36,16384\n"
"s_bfe_u32 s48,s46,0x30004\n"
"s_lshl_b32 s48,s48,7\n"
MF"v[4:7],a[20:23],v[196:199],0\n"
"s_mul_i32 s52,s30,1152\n"
BL"v234,s[12:15],s48 offen offset:0 lds\n"
MF"v[4:7],a[4:7],v[192:195],v[4:7]\n"
"v_add_u32 v128,s52,v131\n"
"s_and_b32 s52,s30,1\n"
MF"v[8:11],a[8:11],v[192:195],0\n"
"s_mul_i32 s51,s52,1152\n"
RD"v[216:219],v242 offset:6144\n"
"v_add_u32 v131,s51,v131\n"
MF"v[8:11],a[24:27],v[196:199],v[8:11]\n"
"s_lshl_b32 s52,s52,10\n"
"v_fmac_f32 v188,v162,v182\n"
"v_fmac_f32 v189,v163,v183\n"
MF"v[12:15],a[28:31],v[196:199],0\n"
BL"v235,s[12:15],s48 offen offset:1024 lds\n"
MF"v[12:15],a[12:15],v[192:195],v[12:15]\n"
WT"lgkmcnt(1)\n"
RD"v[220:223],v243 offset:6144\n"
"v_lshlrev_b32 v164,16,v146\n"
MF"v[16:19],a[0:3],v[200:203],0\n"
RD"v[132:135],v128 offset:49152\n"
"v_and_b32 v129,0x70,v224\n"
MF"v[16:19],a[16:19],v[204:207],v[16:19]\n"
"s_lshr_b32 s50,s31,1\n"
WT"vmcnt(6)\n"
RT"a[32:33],v246 offset:8192\n"
MF"v[20:23],a[20:23],v[204:207],0\n"
"s_mul_i32 s50,s50,20480\n"
RD"a[240:243],v128 offset:53760\n"
"v_and_b32 v165,0xffff0000,v146\n"
MF"v[20:23],a[4:7],v[200:203],v[20:23]\n"
"s_and_b32 s51,s31,1\n"
RD"a[244:247],v131 offset:58368\n"
"v_and_b32 v130,0x380,v224\n"
MF"v[24:27],a[8:11],v[200:203],0\n"
"s_lshl_b32 s51,s51,7\n"
RD"v[192:195],v242 offset:8192\n"
"v_fmac_f32 v188,v164,v184\n"
MF"v[24:27],a[24:27],v[204:207],v[24:27]\n"
SA"s50,s50,s51\n"
RT"a[34:35],v246 offset:8704\n"
"v_lshl_add_u32 v129,v130,1,v129\n"
MF"v[28:31],a[28:31],v[204:207],0\n"
SA"s50,s50,524288\n"
BL"v236,s[12:15],s48 offen offset:2048 lds\n"
MF"v[28:31],a[12:15],v[200:203],v[28:31]\n"
"s_lshl_b32 s52,s52,1\n"
RT"a[36:37],v247 offset:8192\n"
"v_fmac_f32 v189,v165,v185\n"
MF"v[32:35],a[0:3],v[208:211],0\n"
SA"s52,s52,s50\n"
RD"v[196:199],v243 offset:8192\n"
"v_lshlrev_b32 v166,16,v147\n"
MF"v[32:35],a[16:19],v[212:215],v[32:35]\n"
SA"s52,s52,16384\n"
BL"v237,s[12:15],s48 offen offset:3072 lds\n"
MF"v[36:39],a[20:23],v[212:215],0\n"
SA"m0,s37,16384\n"
RT"a[38:39],v247 offset:8704\n"
"v_and_b32 v167,0xffff0000,v147\n"
MF"v[36:39],a[4:7],v[208:211],v[36:39]\n"
"s_bfe_u32 s48,s46,0x30008\n"
RD"v[200:203],v242 offset:10240\n"
"v_fmac_f32 v188,v166,v186\n"
MF"v[40:43],a[8:11],v[208:211],0\n"
"s_lshl_b32 s48,s48,19\n"
RT"a[40:41],v248 offset:8192\n"
"v_fmac_f32 v189,v167,v187\n"
MF"v[40:43],a[24:27],v[212:215],v[40:43]\n"
"s_lshl_b32 s51,s30,11\n"
BL"v238,s[16:19],s48 offen offset:0 lds\n"
MF"v[44:47],a[28:31],v[212:215],0\n"
SA"s50,s50,s51\n"
RD"v[204:207],v243 offset:10240\n"
"v_add_f32 v188,v188,v189\n"
MF"v[44:47],a[12:15],v[208:211],v[44:47]\n"
WT"lgkmcnt(4)\n"
BS"v[132:135],v129,s[8:11],s50 offen sc1\n"
MF"v[48:51],a[0:3],v[216:219],0\n"
SA"s51,s50,8192\n"
RT"a[42:43],v248 offset:8704\n"
"v_add_f32_dpp v188,v188,v188 quad_perm:[1,0,3,2] row_mask:0xf bank_mask:0xf\n"
MF"v[48:51],a[16:19],v[220:223],v[48:51]\n"
"s_lshl_b32 s90,s31,2\n"
BL"v239,s[16:19],s48 offen offset:1024 lds\n"
MF"v[52:55],a[20:23],v[220:223],0\n"
SA"s90,s90,s30\n"
BS"a[240:243],v129,s[8:11],s51 offen sc1\n"
MF"v[52:55],a[4:7],v[216:219],v[52:55]\n"
"s_lshl_b32 s90,s90,1\n"
RD"v[208:211],v242 offset:12288\n"
"v_add_f32_dpp v188,v188,v188 quad_perm:[2,3,0,1] row_mask:0xf bank_mask:0xf\n"
MF"v[56:59],a[8:11],v[216:219],0\n"
"s_sub_u32 s91,2047,s90\n"
RT"a[44:45],v249 offset:8192\n"
"v_add_f32_dpp v188,v188,v188 row_half_mirror row_mask:0xf bank_mask:0xf\n"
MF"v[56:59],a[24:27],v[220:223],v[56:59]\n"
BS"a[244:247],v129,s[8:11],s52 offen sc1\n"
MF"v[60:63],a[28:31],v[220:223],0\n"
RD"v[212:215],v243 offset:12288\n"
"v_add_f32_dpp v188,v188,v188 row_mirror row_mask:0xf bank_mask:0xf\n"
MF"v[60:63],a[12:15],v[216:219],v[60:63]\n"
RT"a[46:47],v249 offset:8704\n"
"v_mov_b32 v189,v188\n"
MF"v[64:67],a[0:3],v[192:195],0\n"
BL"v240,s[16:19],s48 offen offset:2048 lds\n"
MF"v[64:67],a[16:19],v[196:199],v[64:67]\n"
RD"v[216:219],v242 offset:14336\n"
"v_permlane16_swap_b32 v189,v188\n"
MF"v[68:71],a[20:23],v[196:199],0\n"
"s_lshl_b32 s90,s90,2\n"
WT"vmcnt(10)\n"
RT"a[48:49],v246 offset:12288\n"
MF"v[68:71],a[4:7],v[192:195],v[68:71]\n"
SA"s90,s90,8192\n"
BL"v241,s[16:19],s48 offen offset:3072 lds\n"
MF"v[72:75],a[8:11],v[192:195],0\n"
SA"m0,s37,20480\n"
RT"a[50:51],v246 offset:12800\n"
"v_add_f32 v188,v188,v189\n"
MF"v[72:75],a[24:27],v[196:199],v[72:75]\n"
"s_bfe_u32 s48,s46,0x30008\n"
RD"v[220:223],v243 offset:14336\n"
PK"v188,v188,v188\n"
MF"v[76:79],a[28:31],v[196:199],0\n"
"s_lshl_b32 s48,s48,19\n"
RT"a[52:53],v247 offset:12288\n"
"v_lshlrev_b32 v188,16,v188\n"
MF"v[76:79],a[12:15],v[192:195],v[76:79]\n"
WT"lgkmcnt(5)\n"
"v_ashrrev_i32 v189,31,v188\n"
"v_or_b32 v189,0x80000000,v189\n"
MF"v[80:83],a[0:3],v[200:203],0\n"
"s_bitset1_b32 s48,18\n"
"v_xor_b32 v188,v188,v189\n"
"v_and_b32 v188,0xffff0000,v188\n"
MF"v[80:83],a[16:19],v[204:207],v[80:83]\n"
BL"v238,s[16:19],s48 offen offset:0 lds\n"
MF"v[84:87],a[20:23],v[204:207],0\n"
RT"a[54:55],v247 offset:12800\n"
"v_lshrrev_b32 v189,9,v224\n"
MF"v[84:87],a[4:7],v[200:203],v[84:87]\n"
BL"v239,s[16:19],s48 offen offset:1024 lds\n"
MF"v[88:91],a[8:11],v[200:203],0\n"
WT"vmcnt(9) lgkmcnt(2)\n"
RT"a[56:57],v248 offset:12288\n"
"v_sub_u32 v190,s91,v189\n"
MF"v[88:91],a[24:27],v[204:207],v[88:91]\n"
"s_barrier\n"
MF"v[92:95],a[28:31],v[204:207],0\n"
RD"v[192:195],v242 offset:16384\n"
"v_or_b32 v188,v188,v190\n"
MF"v[92:95],a[12:15],v[200:203],v[92:95]\n"
RT"a[58:59],v248 offset:12800\n"
"v_and_b32 v190,0x1f0,v224\n"
MF"v[96:99],a[0:3],v[208:211],0\n"
BL"v240,s[16:19],s48 offen offset:2048 lds\n"
MF"v[96:99],a[16:19],v[212:215],v[96:99]\n"
"v_lshlrev_b32 v190,23,v190\n"
"v_lshl_add_u32 v190,v189,2,v190\n"
MF"v[100:103],a[20:23],v[212:215],0\n"
"buffer_store_dword v188,v190,s[8:11],s90 offen sc1\n"
MF"v[100:103],a[4:7],v[208:211],v[100:103]\n"
RD"v[196:199],v243 offset:16384\n"
MF"v[104:107],a[8:11],v[208:211],0\n"
RT"a[60:61],v249 offset:12288\n"
MF"v[104:107],a[24:27],v[212:215],v[104:107]\n"
BL"v241,s[16:19],s48 offen offset:3072 lds\n"
MF"v[108:111],a[28:31],v[212:215],0\n"
SA"m0,s36,32768\n"
RT"a[62:63],v249 offset:12800\n"
"s_bfe_u32 s48,s46,0x30008\n"
MF"v[108:111],a[12:15],v[208:211],v[108:111]\n"
"s_lshl_b32 s48,s48,7\n"
RD"v[200:203],v242 offset:18432\n"
MF"v[112:115],a[0:3],v[216:219],0\n"
BL"v234,s[12:15],s48 offen offset:0 lds\n"
MF"v[112:115],a[16:19],v[220:223],v[112:115]\n"
RD"v[204:207],v243 offset:18432\n"
MF"v[116:119],a[20:23],v[220:223],0\n"
MF"v[116:119],a[4:7],v[216:219],v[116:119]\n"
MF"v[120:123],a[8:11],v[216:219],0\n"
BL"v235,s[12:15],s48 offen offset:1024 lds\n"
MF"v[120:123],a[24:27],v[220:223],v[120:123]\n"
RD"v[208:211],v242 offset:20480\n"
MF"v[124:127],a[28:31],v[220:223],0\n"
MF"v[124:127],a[12:15],v[216:219],v[124:127]\n"
WT"lgkmcnt(2)\n"
MF"v[0:3],a[32:35],v[192:195],v[0:3]\n"
RD"v[212:215],v243 offset:20480\n"
MF"v[0:3],a[48:51],v[196:199],v[0:3]\n"
BL"v236,s[12:15],s48 offen offset:2048 lds\n"
MF"v[4:7],a[52:55],v[196:199],v[4:7]\n"
MF"v[4:7],a[36:39],v[192:195],v[4:7]\n"
MF"v[8:11],a[40:43],v[192:195],v[8:11]\n"
RD"v[216:219],v242 offset:22528\n"
MF"v[8:11],a[56:59],v[196:199],v[8:11]\n"
BL"v237,s[12:15],s48 offen offset:3072 lds\n"
MF"v[12:15],a[60:63],v[196:199],v[12:15]\n"
MF"v[12:15],a[44:47],v[192:195],v[12:15]\n"
RD"v[220:223],v243 offset:22528\n"
MF"v[16:19],a[32:35],v[200:203],v[16:19]\n"
WT"lgkmcnt(2)\n"
WT"vmcnt(9)\n"
RT"a[64:65],v246 offset:16384\n"
MF"v[16:19],a[48:51],v[204:207],v[16:19]\n"
SA"m0,s37,0\n"
"s_bfe_u32 s48,s46,0x3000c\n"
"s_lshl_b32 s48,s48,19\n"
MF"v[20:23],a[52:55],v[204:207],v[20:23]\n"
BL"v238,s[16:19],s48 offen offset:0 lds\n"
MF"v[20:23],a[36:39],v[200:203],v[20:23]\n"
RD"v[192:195],v242 offset:24576\n"
MF"v[24:27],a[40:43],v[200:203],v[24:27]\n"
RT"a[66:67],v246 offset:16896\n"
MF"v[24:27],a[56:59],v[204:207],v[24:27]\n"
MF"v[28:31],a[60:63],v[204:207],v[28:31]\n"
RT"a[68:69],v247 offset:16384\n"
MF"v[28:31],a[44:47],v[200:203],v[28:31]\n"
BL"v239,s[16:19],s48 offen offset:1024 lds\n"
MF"v[32:35],a[32:35],v[208:211],v[32:35]\n"
RD"v[196:199],v243 offset:24576\n"
MF"v[32:35],a[48:51],v[212:215],v[32:35]\n"
RT"a[70:71],v247 offset:16896\n"
MF"v[36:39],a[52:55],v[212:215],v[36:39]\n"
RD"v[200:203],v242 offset:26624\n"
MF"v[36:39],a[36:39],v[208:211],v[36:39]\n"
BL"v240,s[16:19],s48 offen offset:2048 lds\n"
MF"v[40:43],a[40:43],v[208:211],v[40:43]\n"
RT"a[72:73],v248 offset:16384\n"
MF"v[40:43],a[56:59],v[212:215],v[40:43]\n"
MF"v[44:47],a[60:63],v[212:215],v[44:47]\n"
RD"v[204:207],v243 offset:26624\n"
MF"v[44:47],a[44:47],v[208:211],v[44:47]\n"
WT"lgkmcnt(4)\n"
RT"a[74:75],v248 offset:16896\n"
MF"v[48:51],a[32:35],v[216:219],v[48:51]\n"
BL"v241,s[16:19],s48 offen offset:3072 lds\n"
MF"v[48:51],a[48:51],v[220:223],v[48:51]\n"
RD"v[208:211],v242 offset:28672\n"
MF"v[52:55],a[52:55],v[220:223],v[52:55]\n"
SA"m0,s37,4096\n"
RT"a[76:77],v249 offset:16384\n"
"s_bfe_u32 s48,s46,0x3000c\n"
MF"v[52:55],a[36:39],v[216:219],v[52:55]\n"
"s_lshl_b32 s48,s48,19\n"
"s_bitset1_b32 s48,18\n"
MF"v[56:59],a[40:43],v[216:219],v[56:59]\n"
BL"v238,s[16:19],s48 offen offset:0 lds\n"
MF"v[56:59],a[56:59],v[220:223],v[56:59]\n"
RD"v[212:215],v243 offset:28672\n"
MF"v[60:63],a[60:63],v[220:223],v[60:63]\n"
RT"a[78:79],v249 offset:16896\n"
MF"v[60:63],a[44:47],v[216:219],v[60:63]\n"
MF"v[64:67],a[32:35],v[192:195],v[64:67]\n"
RD"v[216:219],v242 offset:30720\n"
MF"v[64:67],a[48:51],v[196:199],v[64:67]\n"
WT"vmcnt(9)\n"
RT"a[80:81],v246 offset:20480\n"
MF"v[68:71],a[52:55],v[196:199],v[68:71]\n"
BL"v239,s[16:19],s48 offen offset:1024 lds\n"
MF"v[68:71],a[36:39],v[192:195],v[68:71]\n"
RT"a[82:83],v246 offset:20992\n"
MF"v[72:75],a[40:43],v[192:195],v[72:75]\n"
RD"v[220:223],v243 offset:30720\n"
MF"v[72:75],a[56:59],v[196:199],v[72:75]\n"
BL"v240,s[16:19],s48 offen offset:2048 lds\n"
MF"v[76:79],a[60:63],v[196:199],v[76:79]\n"
RT"a[84:85],v247 offset:20480\n"
MF"v[76:79],a[44:47],v[192:195],v[76:79]\n"
WT"lgkmcnt(4)\n"
MF"v[80:83],a[32:35],v[200:203],v[80:83]\n"
"s_lshl_b32 s51,s32,8\n"
"s_andn2_b32 s50,s31,7\n"
SA"s50,s50,s51\n"
MF"v[80:83],a[48:51],v[204:207],v[80:83]\n"
SA"s50,s50,256\n"
RT"a[86:87],v247 offset:20992\n"
SA"s50,s50,s45\n"
MF"v[84:87],a[52:55],v[204:207],v[84:87]\n"
BL"v241,s[16:19],s48 offen offset:3072 lds\n"
MF"v[84:87],a[36:39],v[200:203],v[84:87]\n"
MF"v[88:91],a[40:43],v[200:203],v[88:91]\n"
WT"vmcnt(8) lgkmcnt(2)\n"
RT"a[88:89],v248 offset:20480\n"
SA"m0,s36,0\n"
MF"v[88:91],a[56:59],v[204:207],v[88:91]\n"
"s_bfe_u32 s48,s46,0x3000c\n"
"s_barrier\n"
MF"v[92:95],a[60:63],v[204:207],v[92:95]\n"
"s_lshl_b32 s48,s48,7\n"
RD"v[192:195],v242 offset:32768\n"
MF"v[92:95],a[44:47],v[200:203],v[92:95]\n"
BL"v234,s[12:15],s48 offen offset:0 lds\n"
MF"v[96:99],a[32:35],v[208:211],v[96:99]\n"
"buffer_store_dwordx2 v[232:233],v230,s[8:11],s50 offen sc1\n"
MF"v[96:99],a[48:51],v[212:215],v[96:99]\n"
RT"a[90:91],v248 offset:20992\n"
MF"v[100:103],a[52:55],v[212:215],v[100:103]\n"
RD"v[196:199],v243 offset:32768\n"
MF"v[100:103],a[36:39],v[208:211],v[100:103]\n"
RT"a[92:93],v249 offset:20480\n"
MF"v[104:107],a[40:43],v[208:211],v[104:107]\n"
BL"v235,s[12:15],s48 offen offset:1024 lds\n"
MF"v[104:107],a[56:59],v[212:215],v[104:107]\n"
RT"a[94:95],v249 offset:20992\n"
MF"v[108:111],a[60:63],v[212:215],v[108:111]\n"
RD"v[200:203],v242 offset:34816\n"
MF"v[108:111],a[44:47],v[208:211],v[108:111]\n"
BL"v236,s[12:15],s48 offen offset:2048 lds\n"
MF"v[112:115],a[32:35],v[216:219],v[112:115]\n"
MF"v[112:115],a[48:51],v[220:223],v[112:115]\n"
RD"v[204:207],v243 offset:34816\n"
MF"v[116:119],a[52:55],v[220:223],v[116:119]\n"
MF"v[116:119],a[36:39],v[216:219],v[116:119]\n"
BL"v237,s[12:15],s48 offen offset:3072 lds\n"
MF"v[120:123],a[40:43],v[216:219],v[120:123]\n"
MF"v[120:123],a[56:59],v[220:223],v[120:123]\n"
RD"v[208:211],v242 offset:36864\n"
MF"v[124:127],a[60:63],v[220:223],v[124:127]\n"
SA"m0,s37,8192\n"
"s_bfe_u32 s48,s46,0x30010\n"
"s_lshl_b32 s48,s48,19\n"
MF"v[124:127],a[44:47],v[216:219],v[124:127]\n"
WT"lgkmcnt(2)\n"
MF"v[0:3],a[64:67],v[192:195],v[0:3]\n"
RD"v[212:215],v243 offset:36864\n"
MF"v[0:3],a[80:83],v[196:199],v[0:3]\n"
BL"v238,s[16:19],s48 offen offset:0 lds\n"
MF"v[4:7],a[84:87],v[196:199],v[4:7]\n"
MF"v[4:7],a[68:71],v[192:195],v[4:7]\n"
MF"v[8:11],a[72:75],v[192:195],v[8:11]\n"
RD"v[216:219],v242 offset:38912\n"
MF"v[8:11],a[88:91],v[196:199],v[8:11]\n"
BL"v239,s[16:19],s48 offen offset:1024 lds\n"
MF"v[12:15],a[92:95],v[196:199],v[12:15]\n"
MF"v[12:15],a[76:79],v[192:195],v[12:15]\n"
RD"v[220:223],v243 offset:38912\n"
MF"v[16:19],a[64:67],v[200:203],v[16:19]\n"
WT"lgkmcnt(2)\n"
WT"vmcnt(11)\n"
RT"a[96:97],v246 offset:0\n"
MF"v[16:19],a[80:83],v[204:207],v[16:19]\n"
BL"v240,s[16:19],s48 offen offset:2048 lds\n"
MF"v[20:23],a[84:87],v[204:207],v[20:23]\n"
MF"v[20:23],a[68:71],v[200:203],v[20:23]\n"
RD"v[192:195],v242 offset:40960\n"
MF"v[24:27],a[72:75],v[200:203],v[24:27]\n"
RT"a[98:99],v246 offset:512\n"
MF"v[24:27],a[88:91],v[204:207],v[24:27]\n"
BL"v241,s[16:19],s48 offen offset:3072 lds\n"
MF"v[28:31],a[92:95],v[204:207],v[28:31]\n"
RT"a[100:101],v247 offset:0\n"
MF"v[28:31],a[76:79],v[200:203],v[28:31]\n"
RD"v[196:199],v243 offset:40960\n"
MF"v[32:35],a[64:67],v[208:211],v[32:35]\n"
SA"m0,s37,12288\n"
"s_bfe_u32 s48,s46,0x30010\n"
"s_lshl_b32 s48,s48,19\n"
MF"v[32:35],a[80:83],v[212:215],v[32:35]\n"
"s_bitset1_b32 s48,18\n"
RT"a[102:103],v247 offset:512\n"
MF"v[36:39],a[84:87],v[212:215],v[36:39]\n"
RD"v[200:203],v242 offset:43008\n"
MF"v[36:39],a[68:71],v[208:211],v[36:39]\n"
BL"v238,s[16:19],s48 offen offset:0 lds\n"
MF"v[40:43],a[72:75],v[208:211],v[40:43]\n"
RT"a[104:105],v248 offset:0\n"
MF"v[40:43],a[88:91],v[212:215],v[40:43]\n"
MF"v[44:47],a[92:95],v[212:215],v[44:47]\n"
RD"v[204:207],v243 offset:43008\n"
MF"v[44:47],a[76:79],v[208:211],v[44:47]\n"
WT"lgkmcnt(4)\n"
BL"v239,s[16:19],s48 offen offset:1024 lds\n"
MF"v[48:51],a[64:67],v[216:219],v[48:51]\n"
RT"a[106:107],v248 offset:512\n"
MF"v[48:51],a[80:83],v[220:223],v[48:51]\n"
RD"v[208:211],v242 offset:45056\n"
MF"v[52:55],a[84:87],v[220:223],v[52:55]\n"
RT"a[108:109],v249 offset:0\n"
MF"v[52:55],a[68:71],v[216:219],v[52:55]\n"
BL"v240,s[16:19],s48 offen offset:2048 lds\n"
MF"v[56:59],a[72:75],v[216:219],v[56:59]\n"
MF"v[56:59],a[88:91],v[220:223],v[56:59]\n"
RD"v[212:215],v243 offset:45056\n"
MF"v[60:63],a[92:95],v[220:223],v[60:63]\n"
RT"a[110:111],v249 offset:512\n"
MF"v[60:63],a[76:79],v[216:219],v[60:63]\n"
BL"v241,s[16:19],s48 offen offset:3072 lds\n"
MF"v[64:67],a[64:67],v[192:195],v[64:67]\n"
RD"v[216:219],v242 offset:47104\n"
MF"v[64:67],a[80:83],v[196:199],v[64:67]\n"
WT"vmcnt(13)\n"
RT"a[112:113],v246 offset:4096\n"
MF"v[68:71],a[84:87],v[196:199],v[68:71]\n"
SA"m0,s36,16384\n"
"s_bfe_u32 s48,s46,0x30010\n"
"s_lshl_b32 s48,s48,7\n"
MF"v[68:71],a[68:71],v[192:195],v[68:71]\n"
RT"a[114:115],v246 offset:4608\n"
MF"v[72:75],a[72:75],v[192:195],v[72:75]\n"
RD"v[220:223],v243 offset:47104\n"
MF"v[72:75],a[88:91],v[196:199],v[72:75]\n"
BL"v234,s[12:15],s48 offen offset:0 lds\n"
MF"v[76:79],a[92:95],v[196:199],v[76:79]\n"
RT"a[116:117],v247 offset:4096\n"
MF"v[76:79],a[76:79],v[192:195],v[76:79]\n"
WT"lgkmcnt(4)\n"
MF"v[80:83],a[64:67],v[200:203],v[80:83]\n"
BL"v235,s[12:15],s48 offen offset:1024 lds\n"
MF"v[80:83],a[80:83],v[204:207],v[80:83]\n"
RT"a[118:119],v247 offset:4608\n"
MF"v[84:87],a[84:87],v[204:207],v[84:87]\n"
MF"v[84:87],a[68:71],v[200:203],v[84:87]\n"
MF"v[88:91],a[72:75],v[200:203],v[88:91]\n"
WT"vmcnt(10) lgkmcnt(2)\n"
RT"a[120:121],v248 offset:4096\n"
MF"v[88:91],a[88:91],v[204:207],v[88:91]\n"
"s_barrier\n"
MF"v[92:95],a[92:95],v[204:207],v[92:95]\n"
BL"v236,s[12:15],s48 offen offset:2048 lds\n"
MF"v[92:95],a[76:79],v[200:203],v[92:95]\n"
RD"v[192:195],v242 offset:0\n"
MF"v[96:99],a[64:67],v[208:211],v[96:99]\n"
RT"a[122:123],v248 offset:4608\n"
MF"v[96:99],a[80:83],v[212:215],v[96:99]\n"
BL"v237,s[12:15],s48 offen offset:3072 lds\n"
MF"v[100:103],a[84:87],v[212:215],v[100:103]\n"
RD"v[196:199],v243 offset:0\n"
MF"v[100:103],a[68:71],v[208:211],v[100:103]\n"
RT"a[124:125],v249 offset:4096\n"
MF"v[104:107],a[72:75],v[208:211],v[104:107]\n"
SA"m0,s37,16384\n"
"s_bfe_u32 s48,s46,0x30014\n"
"s_lshl_b32 s48,s48,19\n"
MF"v[104:107],a[88:91],v[212:215],v[104:107]\n"
RT"a[126:127],v249 offset:4608\n"
MF"v[108:111],a[92:95],v[212:215],v[108:111]\n"
RD"v[200:203],v242 offset:2048\n"
MF"v[108:111],a[76:79],v[208:211],v[108:111]\n"
BL"v238,s[16:19],s48 offen offset:0 lds\n"
MF"v[112:115],a[64:67],v[216:219],v[112:115]\n"
MF"v[112:115],a[80:83],v[220:223],v[112:115]\n"
RD"v[204:207],v243 offset:2048\n"
MF"v[116:119],a[84:87],v[220:223],v[116:119]\n"
BL"v239,s[16:19],s48 offen offset:1024 lds\n"
MF"v[116:119],a[68:71],v[216:219],v[116:119]\n"
MF"v[120:123],a[72:75],v[216:219],v[120:123]\n"
MF"v[120:123],a[88:91],v[220:223],v[120:123]\n"
RD"v[208:211],v242 offset:4096\n"
MF"v[124:127],a[92:95],v[220:223],v[124:127]\n"
MF"v[124:127],a[76:79],v[216:219],v[124:127]\n"
WT"lgkmcnt(2)\n"
BL"v240,s[16:19],s48 offen offset:2048 lds\n"
MF"v[0:3],a[96:99],v[192:195],v[0:3]\n"
RD"v[212:215],v243 offset:4096\n"
MF"v[0:3],a[112:115],v[196:199],v[0:3]\n"
MF"v[4:7],a[116:119],v[196:199],v[4:7]\n"
MF"v[4:7],a[100:103],v[192:195],v[4:7]\n"
BL"v241,s[16:19],s48 offen offset:3072 lds\n"
MF"v[8:11],a[104:107],v[192:195],v[8:11]\n"
RD"v[216:219],v242 offset:6144\n"
MF"v[8:11],a[120:123],v[196:199],v[8:11]\n"
MF"v[12:15],a[124:127],v[196:199],v[12:15]\n"
SA"m0,s37,20480\n"
"s_bfe_u32 s48,s46,0x30014\n"
"s_lshl_b32 s48,s48,19\n"
MF"v[12:15],a[108:111],v[192:195],v[12:15]\n"
"s_bitset1_b32 s48,18\n"
RD"v[220:223],v243 offset:6144\n"
MF"v[16:19],a[96:99],v[200:203],v[16:19]\n"
WT"lgkmcnt(2)\n"
WT"vmcnt(12)\n"
RT"a[128:129],v246 offset:8192\n"
MF"v[16:19],a[112:115],v[204:207],v[16:19]\n"
BL"v238,s[16:19],s48 offen offset:0 lds\n"
MF"v[20:23],a[116:119],v[204:207],v[20:23]\n"
MF"v[20:23],a[100:103],v[200:203],v[20:23]\n"
RD"v[192:195],v242 offset:8192\n"
MF"v[24:27],a[104:107],v[200:203],v[24:27]\n"
RT"a[130:131],v246 offset:8704\n"
MF"v[24:27],a[120:123],v[204:207],v[24:27]\n"
BL"v239,s[16:19],s48 offen offset:1024 lds\n"
MF"v[28:31],a[124:127],v[204:207],v[28:31]\n"
RT"a[132:133],v247 offset:8192\n"
MF"v[28:31],a[108:111],v[200:203],v[28:31]\n"
RD"v[196:199],v243 offset:8192\n"
MF"v[32:35],a[96:99],v[208:211],v[32:35]\n"
MF"v[32:35],a[112:115],v[212:215],v[32:35]\n"
RT"a[134:135],v247 offset:8704\n"
MF"v[36:39],a[116:119],v[212:215],v[36:39]\n"
BL"v240,s[16:19],s48 offen offset:2048 lds\n"
MF"v[36:39],a[100:103],v[208:211],v[36:39]\n"
RD"v[200:203],v242 offset:10240\n"
MF"v[40:43],a[104:107],v[208:211],v[40:43]\n"
RT"a[136:137],v248 offset:8192\n"
MF"v[40:43],a[120:123],v[212:215],v[40:43]\n"
BL"v241,s[16:19],s48 offen offset:3072 lds\n"
MF"v[44:47],a[124:127],v[212:215],v[44:47]\n"
RD"v[204:207],v243 offset:10240\n"
MF"v[44:47],a[108:111],v[208:211],v[44:47]\n"
WT"lgkmcnt(4)\n"
RT"a[138:139],v248 offset:8704\n"
MF"v[48:51],a[96:99],v[216:219],v[48:51]\n"
SA"m0,s36,32768\n"
"s_bfe_u32 s48,s46,0x30014\n"
"s_lshl_b32 s48,s48,7\n"
MF"v[48:51],a[112:115],v[220:223],v[48:51]\n"
RD"v[208:211],v242 offset:12288\n"
MF"v[52:55],a[116:119],v[220:223],v[52:55]\n"
RT"a[140:141],v249 offset:8192\n"
MF"v[52:55],a[100:103],v[216:219],v[52:55]\n"
BL"v234,s[12:15],s48 offen offset:0 lds\n"
MF"v[56:59],a[104:107],v[216:219],v[56:59]\n"
MF"v[56:59],a[120:123],v[220:223],v[56:59]\n"
RD"v[212:215],v243 offset:12288\n"
MF"v[60:63],a[124:127],v[220:223],v[60:63]\n"
RT"a[142:143],v249 offset:8704\n"
MF"v[60:63],a[108:111],v[216:219],v[60:63]\n"
BL"v235,s[12:15],s48 offen offset:1024 lds\n"
MF"v[64:67],a[96:99],v[192:195],v[64:67]\n"
RD"v[216:219],v242 offset:14336\n"
MF"v[64:67],a[112:115],v[196:199],v[64:67]\n"
WT"vmcnt(14)\n"
RT"a[144:145],v246 offset:12288\n"
MF"v[68:71],a[116:119],v[196:199],v[68:71]\n"
MF"v[68:71],a[100:103],v[192:195],v[68:71]\n"
RT"a[146:147],v246 offset:12800\n"
MF"v[72:75],a[104:107],v[192:195],v[72:75]\n"
BL"v236,s[12:15],s48 offen offset:2048 lds\n"
MF"v[72:75],a[120:123],v[196:199],v[72:75]\n"
RD"v[220:223],v243 offset:14336\n"
MF"v[76:79],a[124:127],v[196:199],v[76:79]\n"
RT"a[148:149],v247 offset:12288\n"
MF"v[76:79],a[108:111],v[192:195],v[76:79]\n"
WT"lgkmcnt(4)\n"
BL"v237,s[12:15],s48 offen offset:3072 lds\n"
MF"v[80:83],a[96:99],v[200:203],v[80:83]\n"
MF"v[80:83],a[112:115],v[204:207],v[80:83]\n"
RT"a[150:151],v247 offset:12800\n"
MF"v[84:87],a[116:119],v[204:207],v[84:87]\n"
SA"m0,s37,0\n"
"s_bfe_u32 s48,s46,0x30018\n"
"s_lshl_b32 s48,s48,19\n"
MF"v[84:87],a[100:103],v[200:203],v[84:87]\n"
MF"v[88:91],a[104:107],v[200:203],v[88:91]\n"
WT"vmcnt(12) lgkmcnt(2)\n"
RT"a[152:153],v248 offset:12288\n"
MF"v[88:91],a[120:123],v[204:207],v[88:91]\n"
BL"v238,s[16:19],s48 offen offset:0 lds\n"
MF"v[92:95],a[124:127],v[204:207],v[92:95]\n"
"s_barrier\n"
MF"v[92:95],a[108:111],v[200:203],v[92:95]\n"
RD"v[192:195],v242 offset:16384\n"
MF"v[96:99],a[96:99],v[208:211],v[96:99]\n"
RT"a[154:155],v248 offset:12800\n"
MF"v[96:99],a[112:115],v[212:215],v[96:99]\n"
BL"v239,s[16:19],s48 offen offset:1024 lds\n"
MF"v[100:103],a[116:119],v[212:215],v[100:103]\n"
RD"v[196:199],v243 offset:16384\n"
MF"v[100:103],a[100:103],v[208:211],v[100:103]\n"
RT"a[156:157],v249 offset:12288\n"
MF"v[104:107],a[104:107],v[208:211],v[104:107]\n"
MF"v[104:107],a[120:123],v[212:215],v[104:107]\n"
RT"a[158:159],v249 offset:12800\n"
MF"v[108:111],a[124:127],v[212:215],v[108:111]\n"
BL"v240,s[16:19],s48 offen offset:2048 lds\n"
MF"v[108:111],a[108:111],v[208:211],v[108:111]\n"
RD"v[200:203],v242 offset:18432\n"
MF"v[112:115],a[96:99],v[216:219],v[112:115]\n"
MF"v[112:115],a[112:115],v[220:223],v[112:115]\n"
RD"v[204:207],v243 offset:18432\n"
MF"v[116:119],a[116:119],v[220:223],v[116:119]\n"
BL"v241,s[16:19],s48 offen offset:3072 lds\n"
MF"v[116:119],a[100:103],v[216:219],v[116:119]\n"
MF"v[120:123],a[104:107],v[216:219],v[120:123]\n"
SA"m0,s37,4096\n"
"s_bfe_u32 s48,s46,0x30018\n"
"s_lshl_b32 s48,s48,19\n"
MF"v[120:123],a[120:123],v[220:223],v[120:123]\n"
"s_bitset1_b32 s48,18\n"
RD"v[208:211],v242 offset:20480\n"
MF"v[124:127],a[124:127],v[220:223],v[124:127]\n"
BL"v238,s[16:19],s48 offen offset:0 lds\n"
MF"v[124:127],a[108:111],v[216:219],v[124:127]\n"
WT"lgkmcnt(2)\n"
MF"v[0:3],a[128:131],v[192:195],v[0:3]\n"
RD"v[212:215],v243 offset:20480\n"
MF"v[0:3],a[144:147],v[196:199],v[0:3]\n"
MF"v[4:7],a[148:151],v[196:199],v[4:7]\n"
BL"v239,s[16:19],s48 offen offset:1024 lds\n"
MF"v[4:7],a[132:135],v[192:195],v[4:7]\n"
MF"v[8:11],a[136:139],v[192:195],v[8:11]\n"
RD"v[216:219],v242 offset:22528\n"
MF"v[8:11],a[152:155],v[196:199],v[8:11]\n"
MF"v[12:15],a[156:159],v[196:199],v[12:15]\n"
MF"v[12:15],a[140:143],v[192:195],v[12:15]\n"
RD"v[220:223],v243 offset:22528\n"
MF"v[16:19],a[128:131],v[200:203],v[16:19]\n"
WT"lgkmcnt(2)\n"
BL"v240,s[16:19],s48 offen offset:2048 lds\n"
MF"v[16:19],a[144:147],v[204:207],v[16:19]\n"
WT"vmcnt(15)\n"
RT"a[160:161],v246 offset:16384\n"
MF"v[20:23],a[148:151],v[204:207],v[20:23]\n"
MF"v[20:23],a[132:135],v[200:203],v[20:23]\n"
RD"v[192:195],v242 offset:24576\n"
MF"v[24:27],a[136:139],v[200:203],v[24:27]\n"
RT"a[162:163],v246 offset:16896\n"
MF"v[24:27],a[152:155],v[204:207],v[24:27]\n"
BL"v241,s[16:19],s48 offen offset:3072 lds\n"
MF"v[28:31],a[156:159],v[204:207],v[28:31]\n"
SA"m0,s36,0\n"
RT"a[164:165],v247 offset:16384\n"
"s_bfe_u32 s48,s46,0x30018\n"
MF"v[28:31],a[140:143],v[200:203],v[28:31]\n"
"s_lshl_b32 s48,s48,7\n"
RD"v[196:199],v243 offset:24576\n"
MF"v[32:35],a[128:131],v[208:211],v[32:35]\n"
BL"v234,s[12:15],s48 offen offset:0 lds\n"
MF"v[32:35],a[144:147],v[212:215],v[32:35]\n"
RT"a[166:167],v247 offset:16896\n"
MF"v[36:39],a[148:151],v[212:215],v[36:39]\n"
RD"v[200:203],v242 offset:26624\n"
MF"v[36:39],a[132:135],v[208:211],v[36:39]\n"
MF"v[40:43],a[136:139],v[208:211],v[40:43]\n"
RT"a[168:169],v248 offset:16384\n"
MF"v[40:43],a[152:155],v[212:215],v[40:43]\n"
BL"v235,s[12:15],s48 offen offset:1024 lds\n"
MF"v[44:47],a[156:159],v[212:215],v[44:47]\n"
RD"v[204:207],v243 offset:26624\n"
MF"v[44:47],a[140:143],v[208:211],v[44:47]\n"
WT"lgkmcnt(4)\n"
RT"a[170:171],v248 offset:16896\n"
MF"v[48:51],a[128:131],v[216:219],v[48:51]\n"
MF"v[48:51],a[144:147],v[220:223],v[48:51]\n"
RD"v[208:211],v242 offset:28672\n"
MF"v[52:55],a[148:151],v[220:223],v[52:55]\n"
BL"v236,s[12:15],s48 offen offset:2048 lds\n"
MF"v[52:55],a[132:135],v[216:219],v[52:55]\n"
RT"a[172:173],v249 offset:16384\n"
MF"v[56:59],a[136:139],v[216:219],v[56:59]\n"
MF"v[56:59],a[152:155],v[220:223],v[56:59]\n"
RD"v[212:215],v243 offset:28672\n"
MF"v[60:63],a[156:159],v[220:223],v[60:63]\n"
RT"a[174:175],v249 offset:16896\n"
MF"v[60:63],a[140:143],v[216:219],v[60:63]\n"
BL"v237,s[12:15],s48 offen offset:3072 lds\n"
MF"v[64:67],a[128:131],v[192:195],v[64:67]\n"
SA"m0,s37,8192\n"
RD"v[216:219],v242 offset:30720\n"
"s_bfe_u32 s48,s46,0x3001c\n"
MF"v[64:67],a[144:147],v[196:199],v[64:67]\n"
"s_lshl_b32 s48,s48,19\n"
WT"vmcnt(16)\n"
RT"a[176:177],v246 offset:20480\n"
MF"v[68:71],a[148:151],v[196:199],v[68:71]\n"
BL"v238,s[16:19],s48 offen offset:0 lds\n"
MF"v[68:71],a[132:135],v[192:195],v[68:71]\n"
RT"a[178:179],v246 offset:20992\n"
MF"v[72:75],a[136:139],v[192:195],v[72:75]\n"
RD"v[220:223],v243 offset:30720\n"
MF"v[72:75],a[152:155],v[196:199],v[72:75]\n"
MF"v[76:79],a[156:159],v[196:199],v[76:79]\n"
RT"a[180:181],v247 offset:20480\n"
MF"v[76:79],a[140:143],v[192:195],v[76:79]\n"
WT"lgkmcnt(4)\n"
BL"v239,s[16:19],s48 offen offset:1024 lds\n"
MF"v[80:83],a[128:131],v[200:203],v[80:83]\n"
MF"v[80:83],a[144:147],v[204:207],v[80:83]\n"
RT"a[182:183],v247 offset:20992\n"
MF"v[84:87],a[148:151],v[204:207],v[84:87]\n"
MF"v[84:87],a[132:135],v[200:203],v[84:87]\n"
BL"v240,s[16:19],s48 offen offset:2048 lds\n"
MF"v[88:91],a[136:139],v[200:203],v[88:91]\n"
WT"vmcnt(15) lgkmcnt(2)\n"
RT"a[184:185],v248 offset:20480\n"
MF"v[88:91],a[152:155],v[204:207],v[88:91]\n"
"s_barrier\n"
MF"v[92:95],a[156:159],v[204:207],v[92:95]\n"
RD"v[192:195],v242 offset:32768\n"
MF"v[92:95],a[140:143],v[200:203],v[92:95]\n"
RT"a[186:187],v248 offset:20992\n"
MF"v[96:99],a[128:131],v[208:211],v[96:99]\n"
BL"v241,s[16:19],s48 offen offset:3072 lds\n"
MF"v[96:99],a[144:147],v[212:215],v[96:99]\n"
MF"v[100:103],a[148:151],v[212:215],v[100:103]\n"
SA"m0,s37,12288\n"
RD"v[196:199],v243 offset:32768\n"
"s_bfe_u32 s48,s46,0x3001c\n"
MF"v[100:103],a[132:135],v[208:211],v[100:103]\n"
"s_lshl_b32 s48,s48,19\n"
RT"a[188:189],v249 offset:20480\n"
"s_bitset1_b32 s48,18\n"
MF"v[104:107],a[136:139],v[208:211],v[104:107]\n"
BL"v238,s[16:19],s48 offen offset:0 lds\n"
MF"v[104:107],a[152:155],v[212:215],v[104:107]\n"
RT"a[190:191],v249 offset:20992\n"
MF"v[108:111],a[156:159],v[212:215],v[108:111]\n"
RD"v[200:203],v242 offset:34816\n"
MF"v[108:111],a[140:143],v[208:211],v[108:111]\n"
MF"v[112:115],a[128:131],v[216:219],v[112:115]\n"
BL"v239,s[16:19],s48 offen offset:1024 lds\n"
MF"v[112:115],a[144:147],v[220:223],v[112:115]\n"
RD"v[204:207],v243 offset:34816\n"
MF"v[116:119],a[148:151],v[220:223],v[116:119]\n"
MF"v[116:119],a[132:135],v[216:219],v[116:119]\n"
MF"v[120:123],a[136:139],v[216:219],v[120:123]\n"
MF"v[120:123],a[152:155],v[220:223],v[120:123]\n"
RD"v[208:211],v242 offset:36864\n"
MF"v[124:127],a[156:159],v[220:223],v[124:127]\n"
BL"v240,s[16:19],s48 offen offset:2048 lds\n"
MF"v[124:127],a[140:143],v[216:219],v[124:127]\n"
WT"lgkmcnt(2)\n"
MF"v[0:3],a[160:163],v[192:195],v[0:3]\n"
RD"v[212:215],v243 offset:36864\n"
MF"v[0:3],a[176:179],v[196:199],v[0:3]\n"
BL"v241,s[16:19],s48 offen offset:3072 lds\n"
MF"v[4:7],a[180:183],v[196:199],v[4:7]\n"
MF"v[4:7],a[164:167],v[192:195],v[4:7]\n"
MF"v[8:11],a[168:171],v[192:195],v[8:11]\n"
SA"m0,s36,16384\n"
RD"v[216:219],v242 offset:38912\n"
"s_bfe_u32 s48,s46,0x3001c\n"
MF"v[8:11],a[184:187],v[196:199],v[8:11]\n"
"s_lshl_b32 s48,s48,7\n"
MF"v[12:15],a[188:191],v[196:199],v[12:15]\n"
BL"v234,s[12:15],s48 offen offset:0 lds\n"
MF"v[12:15],a[172:175],v[192:195],v[12:15]\n"
RD"v[220:223],v243 offset:38912\n"
MF"v[16:19],a[160:163],v[200:203],v[16:19]\n"
WT"lgkmcnt(2)\n"
WT"vmcnt(17)\n"
RT"a[192:193],v246 offset:0\n"
MF"v[16:19],a[176:179],v[204:207],v[16:19]\n"
MF"v[20:23],a[180:183],v[204:207],v[20:23]\n"
BL"v235,s[12:15],s48 offen offset:1024 lds\n"
MF"v[20:23],a[164:167],v[200:203],v[20:23]\n"
RD"v[192:195],v242 offset:40960\n"
MF"v[24:27],a[168:171],v[200:203],v[24:27]\n"
RT"a[194:195],v246 offset:512\n"
MF"v[24:27],a[184:187],v[204:207],v[24:27]\n"
MF"v[28:31],a[188:191],v[204:207],v[28:31]\n"
RT"a[196:197],v247 offset:0\n"
MF"v[28:31],a[172:175],v[200:203],v[28:31]\n"
RD"v[196:199],v243 offset:40960\n"
MF"v[32:35],a[160:163],v[208:211],v[32:35]\n"
BL"v236,s[12:15],s48 offen offset:2048 lds\n"
MF"v[32:35],a[176:179],v[212:215],v[32:35]\n"
RT"a[198:199],v247 offset:512\n"
MF"v[36:39],a[180:183],v[212:215],v[36:39]\n"
RD"v[200:203],v242 offset:43008\n"
MF"v[36:39],a[164:167],v[208:211],v[36:39]\n"
BL"v237,s[12:15],s48 offen offset:3072 lds\n"
MF"v[40:43],a[168:171],v[208:211],v[40:43]\n"
RT"a[200:201],v248 offset:0\n"
MF"v[40:43],a[184:187],v[212:215],v[40:43]\n"
MF"v[44:47],a[188:191],v[212:215],v[44:47]\n"
SA"m0,s37,16384\n"
RD"v[204:207],v243 offset:43008\n"
SA"s48,s39,0\n"
MF"v[44:47],a[172:175],v[208:211],v[44:47]\n"
WT"lgkmcnt(4)\n"
RT"a[202:203],v248 offset:512\n"
"s_lshr_b32 s50,s30,1\n"
MF"v[48:51],a[160:163],v[216:219],v[48:51]\n"
"s_and_b32 s51,s30,1\n"
BL"v234,s[12:15],s48 offen offset:0 lds\n"
MF"v[48:51],a[176:179],v[220:223],v[48:51]\n"
"s_mul_i32 s38,s50,24576\n"
RD"v[208:211],v242 offset:45056\n"
MF"v[52:55],a[180:183],v[220:223],v[52:55]\n"
"s_lshl_b32 s60,s50,18\n"
RT"a[204:205],v249 offset:0\n"
MF"v[52:55],a[164:167],v[216:219],v[52:55]\n"
"s_lshl_b32 s50,s51,12\n"
MF"v[56:59],a[168:171],v[216:219],v[56:59]\n"
SA"s38,s38,s50\n"
BL"v235,s[12:15],s48 offen offset:1024 lds\n"
MF"v[56:59],a[184:187],v[220:223],v[56:59]\n"
"s_lshl_b32 s50,s51,7\n"
RD"v[212:215],v243 offset:45056\n"
MF"v[60:63],a[188:191],v[220:223],v[60:63]\n"
SA"s60,s60,s50\n"
RT"a[206:207],v249 offset:512\n"
MF"v[60:63],a[172:175],v[216:219],v[60:63]\n"
MF"v[64:67],a[160:163],v[192:195],v[64:67]\n"
RD"v[216:219],v242 offset:47104\n"
MF"v[64:67],a[176:179],v[196:199],v[64:67]\n"
"s_sub_u32 s99,s34,5\n"
WT"vmcnt(18)\n"
RT"a[208:209],v246 offset:4096\n"
MF"v[68:71],a[180:183],v[196:199],v[68:71]\n"
"s_cmp_lt_u32 s34,5\n"
BL"v236,s[12:15],s48 offen offset:2048 lds\n"
MF"v[68:71],a[164:167],v[192:195],v[68:71]\n"
"s_cselect_b32 s99,s34,s99\n"
RT"a[210:211],v246 offset:4608\n"
"s_mul_i32 s99,s99,1\n"
MF"v[72:75],a[168:171],v[192:195],v[72:75]\n"
"s_lshl_b32 s61,s99,20\n"
RD"v[220:223],v243 offset:47104\n"
MF"v[72:75],a[184:187],v[196:199],v[72:75]\n"
BL"v237,s[12:15],s48 offen offset:3072 lds\n"
MF"v[76:79],a[188:191],v[196:199],v[76:79]\n"
RT"a[212:213],v247 offset:4096\n"
MF"v[76:79],a[172:175],v[192:195],v[76:79]\n"
WT"lgkmcnt(4)\n"
MF"v[80:83],a[160:163],v[200:203],v[80:83]\n"
SA"m0,s37,20480\n"
SA"s48,s98,0\n"
MF"v[80:83],a[176:179],v[204:207],v[80:83]\n"
RT"a[214:215],v247 offset:4608\n"
MF"v[84:87],a[180:183],v[204:207],v[84:87]\n"
BL"v234,s[12:15],s48 offen offset:0 lds\n"
MF"v[84:87],a[164:167],v[200:203],v[84:87]\n"
MF"v[88:91],a[168:171],v[200:203],v[88:91]\n"
WT"vmcnt(17) lgkmcnt(2)\n"
RT"a[216:217],v248 offset:4096\n"
MF"v[88:91],a[184:187],v[204:207],v[88:91]\n"
"s_barrier\n"
MF"v[92:95],a[188:191],v[204:207],v[92:95]\n"
RD"v[192:195],v242 offset:0\n"
MF"v[92:95],a[172:175],v[200:203],v[92:95]\n"
BL"v235,s[12:15],s48 offen offset:1024 lds\n"
MF"v[96:99],a[160:163],v[208:211],v[96:99]\n"
RT"a[218:219],v248 offset:4608\n"
MF"v[96:99],a[176:179],v[212:215],v[96:99]\n"
MF"v[100:103],a[180:183],v[212:215],v[100:103]\n"
RD"v[196:199],v243 offset:0\n"
MF"v[100:103],a[164:167],v[208:211],v[100:103]\n"
RT"a[220:221],v249 offset:4096\n"
MF"v[104:107],a[168:171],v[208:211],v[104:107]\n"
BL"v236,s[12:15],s48 offen offset:2048 lds\n"
MF"v[104:107],a[184:187],v[212:215],v[104:107]\n"
RT"a[222:223],v249 offset:4608\n"
MF"v[108:111],a[188:191],v[212:215],v[108:111]\n"
RD"v[200:203],v242 offset:2048\n"
MF"v[108:111],a[172:175],v[208:211],v[108:111]\n"
BL"v237,s[12:15],s48 offen offset:3072 lds\n"
MF"v[112:115],a[160:163],v[216:219],v[112:115]\n"
MF"v[112:115],a[176:179],v[220:223],v[112:115]\n"
RD"v[204:207],v243 offset:2048\n"
MF"v[116:119],a[180:183],v[220:223],v[116:119]\n"
SA"m0,s37,0\n"
SA"s48,s39,32768\n"
MF"v[116:119],a[164:167],v[216:219],v[116:119]\n"
MF"v[120:123],a[168:171],v[216:219],v[120:123]\n"
BL"v234,s[12:15],s48 offen offset:0 lds\n"
MF"v[120:123],a[184:187],v[220:223],v[120:123]\n"
RD"v[208:211],v242 offset:4096\n"
MF"v[124:127],a[188:191],v[220:223],v[124:127]\n"
MF"v[124:127],a[172:175],v[216:219],v[124:127]\n"
WT"lgkmcnt(2)\n"
MF"v[0:3],a[192:195],v[192:195],v[0:3]\n"
RD"v[212:215],v243 offset:4096\n"
MF"v[0:3],a[208:211],v[196:199],v[0:3]\n"
BL"v235,s[12:15],s48 offen offset:1024 lds\n"
MF"v[4:7],a[212:215],v[196:199],v[4:7]\n"
MF"v[4:7],a[196:199],v[192:195],v[4:7]\n"
MF"v[8:11],a[200:203],v[192:195],v[8:11]\n"
RD"v[216:219],v242 offset:6144\n"
MF"v[8:11],a[216:219],v[196:199],v[8:11]\n"
BL"v236,s[12:15],s48 offen offset:2048 lds\n"
MF"v[12:15],a[220:223],v[196:199],v[12:15]\n"
MF"v[12:15],a[204:207],v[192:195],v[12:15]\n"
RD"v[220:223],v243 offset:6144\n"
MF"v[16:19],a[192:195],v[200:203],v[16:19]\n"
WT"lgkmcnt(2)\n"
WT"vmcnt(19)\n"
RT"a[224:225],v246 offset:8192\n"
MF"v[16:19],a[208:211],v[204:207],v[16:19]\n"
BL"v237,s[12:15],s48 offen offset:3072 lds\n"
MF"v[20:23],a[212:215],v[204:207],v[20:23]\n"
MF"v[20:23],a[196:199],v[200:203],v[20:23]\n"
RD"v[192:195],v242 offset:8192\n"
MF"v[24:27],a[200:203],v[200:203],v[24:27]\n"
SA"m0,s37,4096\n"
RT"a[226:227],v246 offset:8704\n"
SA"s48,s98,32768\n"
MF"v[24:27],a[216:219],v[204:207],v[24:27]\n"
MF"v[28:31],a[220:223],v[204:207],v[28:31]\n"
RT"a[228:229],v247 offset:8192\n"
MF"v[28:31],a[204:207],v[200:203],v[28:31]\n"
BL"v234,s[12:15],s48 offen offset:0 lds\n"
MF"v[32:35],a[192:195],v[208:211],v[32:35]\n"
RD"v[196:199],v243 offset:8192\n"
MF"v[32:35],a[208:211],v[212:215],v[32:35]\n"
RT"a[230:231],v247 offset:8704\n"
MF"v[36:39],a[212:215],v[212:215],v[36:39]\n"
RD"v[200:203],v242 offset:10240\n"
MF"v[36:39],a[196:199],v[208:211],v[36:39]\n"
BL"v235,s[12:15],s48 offen offset:1024 lds\n"
MF"v[40:43],a[200:203],v[208:211],v[40:43]\n"
RT"a[232:233],v248 offset:8192\n"
MF"v[40:43],a[216:219],v[212:215],v[40:43]\n"
MF"v[44:47],a[220:223],v[212:215],v[44:47]\n"
RD"v[204:207],v243 offset:10240\n"
MF"v[44:47],a[204:207],v[208:211],v[44:47]\n"
WT"lgkmcnt(4)\n"
RT"a[234:235],v248 offset:8704\n"
MF"v[48:51],a[192:195],v[216:219],v[48:51]\n"
BL"v236,s[12:15],s48 offen offset:2048 lds\n"
MF"v[48:51],a[208:211],v[220:223],v[48:51]\n"
RD"v[208:211],v242 offset:12288\n"
MF"v[52:55],a[212:215],v[220:223],v[52:55]\n"
RT"a[236:237],v249 offset:8192\n"
MF"v[52:55],a[196:199],v[216:219],v[52:55]\n"
BL"v237,s[12:15],s48 offen offset:3072 lds\n"
MF"v[56:59],a[200:203],v[216:219],v[56:59]\n"
MF"v[56:59],a[216:219],v[220:223],v[56:59]\n"
RD"v[212:215],v243 offset:12288\n"
MF"v[60:63],a[220:223],v[220:223],v[60:63]\n"
RT"a[238:239],v249 offset:8704\n"
MF"v[60:63],a[204:207],v[216:219],v[60:63]\n"
MF"v[64:67],a[192:195],v[192:195],v[64:67]\n"
RD"v[216:219],v242 offset:14336\n"
MF"v[64:67],a[208:211],v[196:199],v[64:67]\n"
SA"m0,s37,8192\n"
WT"vmcnt(20)\n"
RT"a[240:241],v246 offset:12288\n"
MF"v[68:71],a[212:215],v[196:199],v[68:71]\n"
SA"s48,s39,65536\n"
MF"v[68:71],a[196:199],v[192:195],v[68:71]\n"
RT"a[242:243],v246 offset:12800\n"
MF"v[72:75],a[200:203],v[192:195],v[72:75]\n"
WT"lgkmcnt(3)\n"
BL"v234,s[12:15],s48 offen offset:0 lds\n"
MF"v[72:75],a[216:219],v[196:199],v[72:75]\n"
RD"v[220:223],v243 offset:14336\n"
MF"v[76:79],a[220:223],v[196:199],v[76:79]\n"
RT"a[244:245],v247 offset:12288\n"
MF"v[76:79],a[204:207],v[192:195],v[76:79]\n"
BL"v235,s[12:15],s48 offen offset:1024 lds\n"
MF"v[80:83],a[192:195],v[200:203],v[80:83]\n"
MF"v[80:83],a[208:211],v[204:207],v[80:83]\n"
RT"a[246:247],v247 offset:12800\n"
MF"v[84:87],a[212:215],v[204:207],v[84:87]\n"
MF"v[84:87],a[196:199],v[200:203],v[84:87]\n"
MF"v[88:91],a[200:203],v[200:203],v[88:91]\n"
WT"vmcnt(18) lgkmcnt(2)\n"
RT"a[248:249],v248 offset:12288\n"
MF"v[88:91],a[216:219],v[204:207],v[88:91]\n"
BL"v236,s[12:15],s48 offen offset:2048 lds\n"
MF"v[92:95],a[220:223],v[204:207],v[92:95]\n"
"s_barrier\n"
MF"v[92:95],a[204:207],v[200:203],v[92:95]\n"
RD"v[192:195],v242 offset:16384\n"
MF"v[96:99],a[192:195],v[208:211],v[96:99]\n"
RT"a[250:251],v248 offset:12800\n"
MF"v[96:99],a[208:211],v[212:215],v[96:99]\n"
BL"v237,s[12:15],s48 offen offset:3072 lds\n"
MF"v[100:103],a[212:215],v[212:215],v[100:103]\n"
RD"v[196:199],v243 offset:16384\n"
MF"v[100:103],a[196:199],v[208:211],v[100:103]\n"
RT"a[252:253],v249 offset:12288\n"
MF"v[104:107],a[200:203],v[208:211],v[104:107]\n"
MF"v[104:107],a[216:219],v[212:215],v[104:107]\n"
RT"a[254:255],v249 offset:12800\n"
MF"v[108:111],a[220:223],v[212:215],v[108:111]\n"
RD"v[200:203],v242 offset:18432\n"
MF"v[108:111],a[204:207],v[208:211],v[108:111]\n"
MF"v[112:115],a[192:195],v[216:219],v[112:115]\n"
"s_mul_i32 s50,s35,1048576\n"
MF"v[112:115],a[208:211],v[220:223],v[112:115]\n"
"s_lshl_b32 s51,s34,15\n"
RD"v[204:207],v243 offset:18432\n"
SA"m0,s37,12288\n"
MF"v[116:119],a[212:215],v[220:223],v[116:119]\n"
SA"s48,s98,65536\n"
SA"s50,s50,s51\n"
MF"v[116:119],a[196:199],v[216:219],v[116:119]\n"
WT"lgkmcnt(2)\n"
BL"v234,s[12:15],s48 offen offset:0 lds\n"
MF"v[120:123],a[200:203],v[216:219],v[120:123]\n"
"s_and_b32 s51,s30,1\n"
"s_lshl_b32 s51,s51,11\n"
MF"v[120:123],a[216:219],v[220:223],v[120:123]\n"
SA"s50,s50,s51\n"
RD"v[208:211],v242 offset:20480\n"
MF"v[124:127],a[220:223],v[220:223],v[124:127]\n"
SA"s50,s50,9175040\n"
MF"v[124:127],a[204:207],v[216:219],v[124:127]\n"
BL"v235,s[12:15],s48 offen offset:1024 lds\n"
MF"v[0:3],a[224:227],v[192:195],v[0:3]\n"
RD"v[212:215],v243 offset:20480\n"
"v_add_u32 v250,s50,v224\n"
MF"v[0:3],a[240:243],v[196:199],v[0:3]\n"
MF"v[4:7],a[244:247],v[196:199],v[4:7]\n"
MF"v[4:7],a[228:231],v[192:195],v[4:7]\n"
MF"v[8:11],a[232:235],v[192:195],v[8:11]\n"
RD"v[216:219],v242 offset:22528\n"
MF"v[8:11],a[248:251],v[196:199],v[8:11]\n"
BL"v236,s[12:15],s48 offen offset:2048 lds\n"
MF"v[12:15],a[252:255],v[196:199],v[12:15]\n"
MF"v[12:15],a[236:239],v[192:195],v[12:15]\n"
WT"lgkmcnt(1)\n"
RD"v[220:223],v243 offset:22528\n"
MF"v[16:19],a[224:227],v[200:203],v[16:19]\n"
BL"v237,s[12:15],s48 offen offset:3072 lds\n"
MF"v[16:19],a[240:243],v[204:207],v[16:19]\n"
MF"v[20:23],a[244:247],v[204:207],v[20:23]\n"
MF"v[20:23],a[228:231],v[200:203],v[20:23]\n"
RD"v[192:195],v242 offset:24576\n"
MF"v[24:27],a[232:235],v[200:203],v[24:27]\n"
MF"v[24:27],a[248:251],v[204:207],v[24:27]\n"
MF"v[28:31],a[252:255],v[204:207],v[28:31]\n"
MF"v[28:31],a[236:239],v[200:203],v[28:31]\n"
RD"v[196:199],v243 offset:24576\n"
MF"v[32:35],a[224:227],v[208:211],v[32:35]\n"
MF"v[32:35],a[240:243],v[212:215],v[32:35]\n"
MF"v[36:39],a[244:247],v[212:215],v[36:39]\n"
RD"v[200:203],v242 offset:26624\n"
MF"v[36:39],a[228:231],v[208:211],v[36:39]\n"
MF"v[40:43],a[232:235],v[208:211],v[40:43]\n"
MF"v[40:43],a[248:251],v[212:215],v[40:43]\n"
MF"v[44:47],a[252:255],v[212:215],v[44:47]\n"
RD"v[204:207],v243 offset:26624\n"
MF"v[44:47],a[236:239],v[208:211],v[44:47]\n"
WT"lgkmcnt(2)\n"
MF"v[48:51],a[224:227],v[216:219],v[48:51]\n"
MF"v[48:51],a[240:243],v[220:223],v[48:51]\n"
RD"v[208:211],v242 offset:28672\n"
MF"v[52:55],a[244:247],v[220:223],v[52:55]\n"
MF"v[52:55],a[228:231],v[216:219],v[52:55]\n"
MF"v[56:59],a[232:235],v[216:219],v[56:59]\n"
MF"v[56:59],a[248:251],v[220:223],v[56:59]\n"
RD"v[212:215],v243 offset:28672\n"
MF"v[60:63],a[252:255],v[220:223],v[60:63]\n"
MF"v[60:63],a[236:239],v[216:219],v[60:63]\n"
MF"v[64:67],a[224:227],v[192:195],v[64:67]\n"
RD"v[216:219],v242 offset:30720\n"
MF"v[64:67],a[240:243],v[196:199],v[64:67]\n"
MF"v[68:71],a[244:247],v[196:199],v[68:71]\n"
MF"v[68:71],a[228:231],v[192:195],v[68:71]\n"
MF"v[72:75],a[232:235],v[192:195],v[72:75]\n"
RD"v[220:223],v243 offset:30720\n"
MF"v[72:75],a[248:251],v[196:199],v[72:75]\n"
MF"v[76:79],a[252:255],v[196:199],v[76:79]\n"
MF"v[76:79],a[236:239],v[192:195],v[76:79]\n"
WT"lgkmcnt(1)\n"
MF"v[80:83],a[224:227],v[200:203],v[80:83]\n"
MF"v[80:83],a[240:243],v[204:207],v[80:83]\n"
MF"v[84:87],a[244:247],v[204:207],v[84:87]\n"
"v_add_u32 v244,114688,v242\n"
"v_add_u32 v245,114688,v243\n"
MF"v[84:87],a[228:231],v[200:203],v[84:87]\n"
"v_add_u32 v242,65536,v242\n"
"v_add_u32 v243,65536,v243\n"
MF"v[88:91],a[232:235],v[200:203],v[88:91]\n"
WT"vmcnt(16) lgkmcnt(0)\n"
MF"v[88:91],a[248:251],v[204:207],v[88:91]\n"
"s_barrier\n"
MF"v[92:95],a[252:255],v[204:207],v[92:95]\n"
RD"v[192:195],v242 offset:16384\n"
MF"v[92:95],a[236:239],v[200:203],v[92:95]\n"
MF"v[96:99],a[224:227],v[208:211],v[96:99]\n"
MF"v[96:99],a[240:243],v[212:215],v[96:99]\n"
MF"v[100:103],a[244:247],v[212:215],v[100:103]\n"
SA"m0,s36,49152\n"
RD"v[196:199],v243 offset:16384\n"
SA"s48,s60,s61\n"
MF"v[100:103],a[228:231],v[208:211],v[100:103]\n"
MF"v[104:107],a[232:235],v[208:211],v[104:107]\n"
BL"v238,s[20:23],s48 offen offset:0 lds\n"
MF"v[104:107],a[248:251],v[212:215],v[104:107]\n"
MF"v[108:111],a[252:255],v[212:215],v[108:111]\n"
RD"v[200:203],v242 offset:18432\n"
MF"v[108:111],a[236:239],v[208:211],v[108:111]\n"
MF"v[112:115],a[224:227],v[216:219],v[112:115]\n"
BL"v239,s[20:23],s48 offen offset:1024 lds\n"
MF"v[112:115],a[240:243],v[220:223],v[112:115]\n"
MF"v[116:119],a[244:247],v[220:223],v[116:119]\n"
RD"v[204:207],v243 offset:18432\n"
MF"v[116:119],a[228:231],v[216:219],v[116:119]\n"
MF"v[120:123],a[232:235],v[216:219],v[120:123]\n"
MF"v[120:123],a[248:251],v[220:223],v[120:123]\n"
BL"v240,s[20:23],s48 offen offset:2048 lds\n"
MF"v[124:127],a[252:255],v[220:223],v[124:127]\n"
SA"s61,s61,524288\n"
RD"v[208:211],v242 offset:20480\n"
MF"v[124:127],a[236:239],v[216:219],v[124:127]\n"
WT"lgkmcnt(2)\n"
MF"v[128:131],a[0:3],v[192:195],0\n"
MF"v[128:131],a[16:19],v[196:199],v[128:131]\n"
RD"v[212:215],v243 offset:20480\n"
MF"v[132:135],a[20:23],v[196:199],0\n"
BL"v241,s[20:23],s48 offen offset:3072 lds\n"
MF"v[132:135],a[4:7],v[192:195],v[132:135]\n"
MU"v0,s55,v0\n"
MU"v1,s55,v1\n"
MF"v[136:139],a[8:11],v[192:195],0\n"
MU"v2,s55,v2\n"
MU"v3,s55,v3\n"
MF"v[136:139],a[24:27],v[196:199],v[136:139]\n"
RD"v[216:219],v242 offset:22528\n"
MU"v4,s55,v4\n"
MF"v[140:143],a[28:31],v[196:199],0\n"
SA"m0,s36,32768\n"
MU"v5,s55,v5\n"
MU"v6,s55,v6\n"
MF"v[140:143],a[12:15],v[192:195],v[140:143]\n"
SA"s48,s60,s61\n"
MU"v7,s55,v7\n"
PK"v0,v0,v1\n"
MF"v[144:147],a[0:3],v[200:203],0\n"
WT"lgkmcnt(1)\n"
BL"v238,s[20:23],s48 offen offset:0 lds\n"
MF"v[144:147],a[16:19],v[204:207],v[144:147]\n"
RD"v[220:223],v243 offset:22528\n"
PK"v1,v2,v3\n"
MF"v[148:151],a[20:23],v[204:207],0\n"
PK"v2,v4,v5\n"
PK"v3,v6,v7\n"
MF"v[148:151],a[4:7],v[200:203],v[148:151]\n"
MF"v[152:155],a[8:11],v[200:203],0\n"
BL"v239,s[20:23],s48 offen offset:1024 lds\n"
MF"v[152:155],a[24:27],v[204:207],v[152:155]\n"
RD"v[192:195],v242 offset:40960\n"
MU"v8,s55,v8\n"
MF"v[156:159],a[28:31],v[204:207],0\n"
MU"v9,s55,v9\n"
MU"v10,s55,v10\n"
MF"v[156:159],a[12:15],v[200:203],v[156:159]\n"
MU"v11,s55,v11\n"
MU"v12,s55,v12\n"
MF"v[128:131],a[32:35],v[208:211],v[128:131]\n"
MU"v13,s55,v13\n"
MU"v14,s55,v14\n"
MF"v[128:131],a[48:51],v[212:215],v[128:131]\n"
RD"v[196:199],v243 offset:40960\n"
MU"v15,s55,v15\n"
MF"v[132:135],a[52:55],v[212:215],v[132:135]\n"
SA"s61,s61,524288\n"
BL"v240,s[20:23],s48 offen offset:2048 lds\n"
MF"v[132:135],a[36:39],v[208:211],v[132:135]\n"
"s_cmp_eq_u32 s61,5242880\n"
PK"v8,v8,v9\n"
PK"v9,v10,v11\n"
MF"v[136:139],a[40:43],v[208:211],v[136:139]\n"
"s_cselect_b32 s61,0,s61\n"
RD"v[200:203],v242 offset:43008\n"
PK"v10,v12,v13\n"
MF"v[136:139],a[56:59],v[212:215],v[136:139]\n"
BL"v241,s[20:23],s48 offen offset:3072 lds\n"
MF"v[140:143],a[60:63],v[212:215],v[140:143]\n"
PK"v11,v14,v15\n"
MU"v16,s55,v16\n"
MF"v[140:143],a[44:47],v[208:211],v[140:143]\n"
WT"lgkmcnt(2)\n"
MU"v17,s55,v17\n"
MU"v18,s55,v18\n"
MF"v[144:147],a[32:35],v[216:219],v[144:147]\n"
RD"v[204:207],v243 offset:43008\n"
MU"v19,s55,v19\n"
MF"v[144:147],a[48:51],v[220:223],v[144:147]\n"
MU"v20,s55,v20\n"
MU"v21,s55,v21\n"
MF"v[148:151],a[52:55],v[220:223],v[148:151]\n"
MU"v22,s55,v22\n"
MU"v23,s55,v23\n"
MF"v[148:151],a[36:39],v[216:219],v[148:151]\n"
PK"v16,v16,v17\n"
PK"v17,v18,v19\n"
MF"v[152:155],a[40:43],v[216:219],v[152:155]\n"
RD"v[208:211],v242 offset:45056\n"
PK"v18,v20,v21\n"
MF"v[152:155],a[56:59],v[220:223],v[152:155]\n"
"s_lshl_b32 s90,s31,2\n"
PK"v19,v22,v23\n"
"v_min_u32 v254,48,v224\n"
MF"v[156:159],a[60:63],v[220:223],v[156:159]\n"
SA"s90,s90,s30\n"
"v_lshlrev_b32 v254,3,v254\n"
MF"v[156:159],a[44:47],v[216:219],v[156:159]\n"
"s_mov_b32 s49,0\n"
"s_lshl_b32 s90,s90,9\n"
MF"v[128:131],a[64:67],v[192:195],v[128:131]\n"
WT"lgkmcnt(1)\n"
RD"v[212:215],v243 offset:45056\n"
MU"v24,s55,v24\n"
MF"v[128:131],a[80:83],v[196:199],v[128:131]\n"
BS"v[0:3],v250,s[8:11],s49 offen offset:0 sc1\n"
MF"v[132:135],a[84:87],v[196:199],v[132:135]\n"
"buffer_load_dword v255,v254,s[8:11],s90 offen sc1\n"
MF"v[132:135],a[68:71],v[192:195],v[132:135]\n"
RD"v[216:219],v242 offset:47104\n"
MU"v25,s55,v25\n"
MF"v[136:139],a[72:75],v[192:195],v[136:139]\n"
MU"v26,s55,v26\n"
MU"v27,s55,v27\n"
MF"v[136:139],a[88:91],v[196:199],v[136:139]\n"
MU"v28,s55,v28\n"
MU"v29,s55,v29\n"
MF"v[140:143],a[92:95],v[196:199],v[140:143]\n"
MU"v30,s55,v30\n"
MU"v31,s55,v31\n"
MF"v[140:143],a[76:79],v[192:195],v[140:143]\n"
RD"v[220:223],v243 offset:47104\n"
PK"v24,v24,v25\n"
MF"v[144:147],a[64:67],v[200:203],v[144:147]\n"
PK"v25,v26,v27\n"
PK"v26,v28,v29\n"
MF"v[144:147],a[80:83],v[204:207],v[144:147]\n"
PK"v27,v30,v31\n"
MF"v[148:151],a[84:87],v[204:207],v[148:151]\n"
MU"v32,s55,v32\n"
MU"v33,s55,v33\n"
MF"v[148:151],a[68:71],v[200:203],v[148:151]\n"
RD"v[192:195],v244 offset:16384\n"
MU"v34,s55,v34\n"
MF"v[152:155],a[72:75],v[200:203],v[152:155]\n"
MU"v35,s55,v35\n"
MU"v36,s55,v36\n"
MF"v[152:155],a[88:91],v[204:207],v[152:155]\n"
MU"v37,s55,v37\n"
MU"v38,s55,v38\n"
MF"v[156:159],a[92:95],v[204:207],v[156:159]\n"
MU"v39,s55,v39\n"
PK"v32,v32,v33\n"
MF"v[156:159],a[76:79],v[200:203],v[156:159]\n"
WT"lgkmcnt(1)\n"
RD"v[196:199],v245 offset:16384\n"
PK"v33,v34,v35\n"
MF"v[128:131],a[96:99],v[208:211],v[128:131]\n"
PK"v34,v36,v37\n"
PK"v35,v38,v39\n"
MF"v[128:131],a[112:115],v[212:215],v[128:131]\n"
MF"v[132:135],a[116:119],v[212:215],v[132:135]\n"
MF"v[132:135],a[100:103],v[208:211],v[132:135]\n"
RD"v[200:203],v244 offset:18432\n"
MF"v[136:139],a[104:107],v[208:211],v[136:139]\n"
MU"v40,s55,v40\n"
MU"v41,s55,v41\n"
MF"v[136:139],a[120:123],v[212:215],v[136:139]\n"
MU"v42,s55,v42\n"
MU"v43,s55,v43\n"
MF"v[140:143],a[124:127],v[212:215],v[140:143]\n"
RD"v[204:207],v245 offset:18432\n"
MU"v44,s55,v44\n"
MF"v[140:143],a[108:111],v[208:211],v[140:143]\n"
MU"v45,s55,v45\n"
MU"v46,s55,v46\n"
MF"v[144:147],a[96:99],v[216:219],v[144:147]\n"
MU"v47,s55,v47\n"
PK"v40,v40,v41\n"
MF"v[144:147],a[112:115],v[220:223],v[144:147]\n"
PK"v41,v42,v43\n"
PK"v42,v44,v45\n"
MF"v[148:151],a[116:119],v[220:223],v[148:151]\n"
RD"v[208:211],v244 offset:20480\n"
PK"v43,v46,v47\n"
MF"v[148:151],a[100:103],v[216:219],v[148:151]\n"
MF"v[152:155],a[104:107],v[216:219],v[152:155]\n"
MF"v[152:155],a[120:123],v[220:223],v[152:155]\n"
MF"v[156:159],a[124:127],v[220:223],v[156:159]\n"
RD"v[212:215],v245 offset:20480\n"
MU"v48,s55,v48\n"
MF"v[156:159],a[108:111],v[216:219],v[156:159]\n"
WT"lgkmcnt(2)\n"
MU"v49,s55,v49\n"
MU"v50,s55,v50\n"
MF"v[128:131],a[128:131],v[192:195],v[128:131]\n"
BS"v[8:11],v250,s[8:11],s49 offen offset:1024 sc1\n"
MF"v[128:131],a[144:147],v[196:199],v[128:131]\n"
MU"v51,s55,v51\n"
MU"v52,s55,v52\n"
MF"v[132:135],a[148:151],v[196:199],v[132:135]\n"
RD"v[216:219],v244 offset:22528\n"
MU"v53,s55,v53\n"
MF"v[132:135],a[132:135],v[192:195],v[132:135]\n"
MU"v54,s55,v54\n"
MU"v55,s55,v55\n"
MF"v[136:139],a[136:139],v[192:195],v[136:139]\n"
PK"v48,v48,v49\n"
PK"v49,v50,v51\n"
MF"v[136:139],a[152:155],v[196:199],v[136:139]\n"
RD"v[220:223],v245 offset:22528\n"
PK"v50,v52,v53\n"
MF"v[140:143],a[156:159],v[196:199],v[140:143]\n"
PK"v51,v54,v55\n"
MF"v[140:143],a[140:143],v[192:195],v[140:143]\n"
MF"v[144:147],a[128:131],v[200:203],v[144:147]\n"
MU"v56,s55,v56\n"
MU"v57,s55,v57\n"
MF"v[144:147],a[144:147],v[204:207],v[144:147]\n"
RD"v[192:195],v244 offset:40960\n"
MU"v58,s55,v58\n"
MF"v[148:151],a[148:151],v[204:207],v[148:151]\n"
MU"v59,s55,v59\n"
MU"v60,s55,v60\n"
MF"v[148:151],a[132:135],v[200:203],v[148:151]\n"
MU"v61,s55,v61\n"
MU"v62,s55,v62\n"
MF"v[152:155],a[136:139],v[200:203],v[152:155]\n"
MU"v63,s55,v63\n"
PK"v56,v56,v57\n"
MF"v[152:155],a[152:155],v[204:207],v[152:155]\n"
RD"v[196:199],v245 offset:40960\n"
PK"v57,v58,v59\n"
MF"v[156:159],a[156:159],v[204:207],v[156:159]\n"
PK"v58,v60,v61\n"
PK"v59,v62,v63\n"
MF"v[156:159],a[140:143],v[200:203],v[156:159]\n"
WT"lgkmcnt(2)\n"
MF"v[128:131],a[160:163],v[208:211],v[128:131]\n"
MF"v[128:131],a[176:179],v[212:215],v[128:131]\n"
RD"v[200:203],v244 offset:43008\n"
MF"v[132:135],a[180:183],v[212:215],v[132:135]\n"
MU"v64,s55,v64\n"
MU"v65,s55,v65\n"
MF"v[132:135],a[164:167],v[208:211],v[132:135]\n"
MU"v66,s55,v66\n"
MU"v67,s55,v67\n"
MF"v[136:139],a[168:171],v[208:211],v[136:139]\n"
MU"v68,s55,v68\n"
MU"v69,s55,v69\n"
MF"v[136:139],a[184:187],v[212:215],v[136:139]\n"
RD"v[204:207],v245 offset:43008\n"
MU"v70,s55,v70\n"
MF"v[140:143],a[188:191],v[212:215],v[140:143]\n"
MU"v71,s55,v71\n"
PK"v64,v64,v65\n"
MF"v[140:143],a[172:175],v[208:211],v[140:143]\n"
PK"v65,v66,v67\n"
PK"v66,v68,v69\n"
MF"v[144:147],a[160:163],v[216:219],v[144:147]\n"
RD"v[208:211],v244 offset:45056\n"
PK"v67,v70,v71\n"
MF"v[144:147],a[176:179],v[220:223],v[144:147]\n"
MF"v[148:151],a[180:183],v[220:223],v[148:151]\n"
MF"v[148:151],a[164:167],v[216:219],v[148:151]\n"
MF"v[152:155],a[168:171],v[216:219],v[152:155]\n"
RD"v[212:215],v245 offset:45056\n"
MU"v72,s55,v72\n"
MF"v[152:155],a[184:187],v[220:223],v[152:155]\n"
"s_mov_b32 s49,4096\n"
MU"v73,s55,v73\n"
MU"v74,s55,v74\n"
MF"v[156:159],a[188:191],v[220:223],v[156:159]\n"
MU"v75,s55,v75\n"
MU"v76,s55,v76\n"
MF"v[156:159],a[172:175],v[216:219],v[156:159]\n"
WT"lgkmcnt(1)\n"
BS"v[16:19],v250,s[8:11],s49 offen offset:0 sc1\n"
MF"v[128:131],a[192:195],v[192:195],v[128:131]\n"
RD"v[216:219],v244 offset:47104\n"
MU"v77,s55,v77\n"
MF"v[128:131],a[208:211],v[196:199],v[128:131]\n"
MU"v78,s55,v78\n"
MU"v79,s55,v79\n"
MF"v[132:135],a[212:215],v[196:199],v[132:135]\n"
PK"v72,v72,v73\n"
PK"v73,v74,v75\n"
MF"v[132:135],a[196:199],v[192:195],v[132:135]\n"
PK"v74,v76,v77\n"
PK"v75,v78,v79\n"
MF"v[136:139],a[200:203],v[192:195],v[136:139]\n"
RD"v[220:223],v245 offset:47104\n"
MF"v[136:139],a[216:219],v[196:199],v[136:139]\n"
MF"v[140:143],a[220:223],v[196:199],v[140:143]\n"
MU"v80,s55,v80\n"
MU"v81,s55,v81\n"
MF"v[140:143],a[204:207],v[192:195],v[140:143]\n"
MU"v82,s55,v82\n"
MU"v83,s55,v83\n"
MF"v[144:147],a[192:195],v[200:203],v[144:147]\n"
MU"v84,s55,v84\n"
MU"v85,s55,v85\n"
MF"v[144:147],a[208:211],v[204:207],v[144:147]\n"
MU"v86,s55,v86\n"
MU"v87,s55,v87\n"
MF"v[148:151],a[212:215],v[204:207],v[148:151]\n"
PK"v80,v80,v81\n"
PK"v81,v82,v83\n"
MF"v[148:151],a[196:199],v[200:203],v[148:151]\n"
PK"v82,v84,v85\n"
PK"v83,v86,v87\n"
MF"v[152:155],a[200:203],v[200:203],v[152:155]\n"
WT"vmcnt(20) lgkmcnt(0)\n"
MF"v[152:155],a[216:219],v[204:207],v[152:155]\n"
"s_barrier\n"
MF"v[156:159],a[220:223],v[204:207],v[156:159]\n"
RD"v[192:195],v242 offset:0\n"
MF"v[156:159],a[204:207],v[200:203],v[156:159]\n"
MF"v[128:131],a[224:227],v[208:211],v[128:131]\n"
MU"v88,s55,v88\n"
MU"v89,s55,v89\n"
MF"v[128:131],a[240:243],v[212:215],v[128:131]\n"
MU"v90,s55,v90\n"
MU"v91,s55,v91\n"
MF"v[132:135],a[244:247],v[212:215],v[132:135]\n"
RD"v[196:199],v243 offset:0\n"
MU"v92,s55,v92\n"
MF"v[132:135],a[228:231],v[208:211],v[132:135]\n"
MU"v93,s55,v93\n"
MU"v94,s55,v94\n"
MF"v[136:139],a[232:235],v[208:211],v[136:139]\n"
MU"v95,s55,v95\n"
PK"v88,v88,v89\n"
MF"v[136:139],a[248:251],v[212:215],v[136:139]\n"
PK"v89,v90,v91\n"
PK"v90,v92,v93\n"
MF"v[140:143],a[252:255],v[212:215],v[140:143]\n"
RD"v[200:203],v242 offset:2048\n"
PK"v91,v94,v95\n"
MF"v[140:143],a[236:239],v[208:211],v[140:143]\n"
MF"v[144:147],a[224:227],v[216:219],v[144:147]\n"
MF"v[144:147],a[240:243],v[220:223],v[144:147]\n"
MF"v[148:151],a[244:247],v[220:223],v[148:151]\n"
RD"v[204:207],v243 offset:2048\n"
MF"v[148:151],a[228:231],v[216:219],v[148:151]\n"
MF"v[152:155],a[232:235],v[216:219],v[152:155]\n"
MF"v[152:155],a[248:251],v[220:223],v[152:155]\n"
MF"v[156:159],a[252:255],v[220:223],v[156:159]\n"
RD"v[208:211],v242 offset:4096\n"
MF"v[156:159],a[236:239],v[216:219],v[156:159]\n"
WT"lgkmcnt(2)\n"
BS"v[24:27],v250,s[8:11],s49 offen offset:1024 sc1\n"
MF"v[36:39],a[0:3],v[192:195],0\n"
MF"v[36:39],a[16:19],v[196:199],v[36:39]\n"
RD"v[212:215],v243 offset:4096\n"
MF"v[68:71],a[20:23],v[196:199],0\n"
MF"v[68:71],a[4:7],v[192:195],v[68:71]\n"
MU"v128,s55,v128\n"
MU"v129,s55,v129\n"
MF"v[44:47],a[8:11],v[192:195],0\n"
MU"v130,s55,v130\n"
MU"v131,s55,v131\n"
MF"v[44:47],a[24:27],v[196:199],v[44:47]\n"
RD"v[216:219],v242 offset:6144\n"
MU"v132,s55,v132\n"
MF"v[76:79],a[28:31],v[196:199],0\n"
MU"v133,s55,v133\n"
MU"v134,s55,v134\n"
MF"v[76:79],a[12:15],v[192:195],v[76:79]\n"
MU"v135,s55,v135\n"
PK"v4,v128,v129\n"
MF"v[52:55],a[0:3],v[200:203],0\n"
WT"lgkmcnt(1)\n"
PK"v5,v130,v131\n"
PK"v6,v132,v133\n"
MF"v[52:55],a[16:19],v[204:207],v[52:55]\n"
RD"v[220:223],v243 offset:6144\n"
PK"v7,v134,v135\n"
MF"v[84:87],a[20:23],v[204:207],0\n"
MF"v[84:87],a[4:7],v[200:203],v[84:87]\n"
MF"v[60:63],a[8:11],v[200:203],0\n"
MF"v[60:63],a[24:27],v[204:207],v[60:63]\n"
RD"v[192:195],v242 offset:24576\n"
MU"v136,s55,v136\n"
MF"v[92:95],a[28:31],v[204:207],0\n"
MU"v137,s55,v137\n"
MU"v138,s55,v138\n"
MF"v[92:95],a[12:15],v[200:203],v[92:95]\n"
MU"v139,s55,v139\n"
MU"v140,s55,v140\n"
MF"v[36:39],a[32:35],v[208:211],v[36:39]\n"
MU"v141,s55,v141\n"
MU"v142,s55,v142\n"
MF"v[36:39],a[48:51],v[212:215],v[36:39]\n"
RD"v[196:199],v243 offset:24576\n"
MU"v143,s55,v143\n"
MF"v[68:71],a[52:55],v[212:215],v[68:71]\n"
PK"v12,v136,v137\n"
PK"v13,v138,v139\n"
MF"v[68:71],a[36:39],v[208:211],v[68:71]\n"
PK"v14,v140,v141\n"
PK"v15,v142,v143\n"
MF"v[44:47],a[40:43],v[208:211],v[44:47]\n"
RD"v[200:203],v242 offset:26624\n"
MF"v[44:47],a[56:59],v[212:215],v[44:47]\n"
MF"v[76:79],a[60:63],v[212:215],v[76:79]\n"
MF"v[76:79],a[44:47],v[208:211],v[76:79]\n"
WT"lgkmcnt(2)\n"
MU"v144,s55,v144\n"
MU"v145,s55,v145\n"
MF"v[52:55],a[32:35],v[216:219],v[52:55]\n"
RD"v[204:207],v243 offset:26624\n"
MU"v146,s55,v146\n"
MF"v[52:55],a[48:51],v[220:223],v[52:55]\n"
MU"v147,s55,v147\n"
MU"v148,s55,v148\n"
MF"v[84:87],a[52:55],v[220:223],v[84:87]\n"
MU"v149,s55,v149\n"
MU"v150,s55,v150\n"
MF"v[84:87],a[36:39],v[216:219],v[84:87]\n"
"s_mov_b32 s49,8192\n"
MU"v151,s55,v151\n"
PK"v20,v144,v145\n"
MF"v[60:63],a[40:43],v[216:219],v[60:63]\n"
RD"v[208:211],v242 offset:28672\n"
PK"v21,v146,v147\n"
MF"v[60:63],a[56:59],v[220:223],v[60:63]\n"
BS"v[32:35],v250,s[8:11],s49 offen offset:0 sc1\n"
MF"v[92:95],a[60:63],v[220:223],v[92:95]\n"
PK"v22,v148,v149\n"
PK"v23,v150,v151\n"
MF"v[92:95],a[44:47],v[216:219],v[92:95]\n"
MF"v[36:39],a[64:67],v[192:195],v[36:39]\n"
WT"lgkmcnt(1)\n"
RD"v[212:215],v243 offset:28672\n"
MF"v[36:39],a[80:83],v[196:199],v[36:39]\n"
MU"v152,s55,v152\n"
MU"v153,s55,v153\n"
MF"v[68:71],a[84:87],v[196:199],v[68:71]\n"
MU"v154,s55,v154\n"
MU"v155,s55,v155\n"
MF"v[68:71],a[68:71],v[192:195],v[68:71]\n"
RD"v[216:219],v242 offset:30720\n"
MU"v156,s55,v156\n"
MF"v[44:47],a[72:75],v[192:195],v[44:47]\n"
MU"v157,s55,v157\n"
MU"v158,s55,v158\n"
MF"v[44:47],a[88:91],v[196:199],v[44:47]\n"
MU"v159,s55,v159\n"
PK"v28,v152,v153\n"
MF"v[76:79],a[92:95],v[196:199],v[76:79]\n"
PK"v29,v154,v155\n"
PK"v30,v156,v157\n"
MF"v[76:79],a[76:79],v[192:195],v[76:79]\n"
RD"v[220:223],v243 offset:30720\n"
PK"v31,v158,v159\n"
MF"v[52:55],a[64:67],v[200:203],v[52:55]\n"
MF"v[52:55],a[80:83],v[204:207],v[52:55]\n"
MF"v[84:87],a[84:87],v[204:207],v[84:87]\n"
MF"v[84:87],a[68:71],v[200:203],v[84:87]\n"
RD"v[192:195],v244 offset:0\n"
MF"v[60:63],a[72:75],v[200:203],v[60:63]\n"
MF"v[60:63],a[88:91],v[204:207],v[60:63]\n"
MF"v[92:95],a[92:95],v[204:207],v[92:95]\n"
MF"v[92:95],a[76:79],v[200:203],v[92:95]\n"
WT"lgkmcnt(1)\n"
RD"v[196:199],v245 offset:0\n"
MF"v[36:39],a[96:99],v[208:211],v[36:39]\n"
MF"v[36:39],a[112:115],v[212:215],v[36:39]\n"
MF"v[68:71],a[116:119],v[212:215],v[68:71]\n"
MF"v[68:71],a[100:103],v[208:211],v[68:71]\n"
RD"v[200:203],v244 offset:2048\n"
MF"v[44:47],a[104:107],v[208:211],v[44:47]\n"
MF"v[44:47],a[120:123],v[212:215],v[44:47]\n"
MF"v[76:79],a[124:127],v[212:215],v[76:79]\n"
RD"v[204:207],v245 offset:2048\n"
MF"v[76:79],a[108:111],v[208:211],v[76:79]\n"
MF"v[52:55],a[96:99],v[216:219],v[52:55]\n"
MF"v[52:55],a[112:115],v[220:223],v[52:55]\n"
MF"v[84:87],a[116:119],v[220:223],v[84:87]\n"
SA"m0,s37,16384\n"
RD"v[208:211],v244 offset:4096\n"
SA"s48,s39,98304\n"
MF"v[84:87],a[100:103],v[216:219],v[84:87]\n"
MF"v[60:63],a[104:107],v[216:219],v[60:63]\n"
BS"v[40:43],v250,s[8:11],s49 offen offset:1024 sc1\n"
MF"v[60:63],a[120:123],v[220:223],v[60:63]\n"
BL"v234,s[12:15],s48 offen offset:0 lds\n"
MF"v[92:95],a[124:127],v[220:223],v[92:95]\n"
RD"v[212:215],v245 offset:4096\n"
MU"v96,s55,v96\n"
MF"v[92:95],a[108:111],v[216:219],v[92:95]\n"
WT"lgkmcnt(2)\n"
MU"v97,s55,v97\n"
MU"v98,s55,v98\n"
MF"v[36:39],a[128:131],v[192:195],v[36:39]\n"
MU"v99,s55,v99\n"
MU"v100,s55,v100\n"
MF"v[36:39],a[144:147],v[196:199],v[36:39]\n"
MU"v101,s55,v101\n"
MU"v102,s55,v102\n"
MF"v[68:71],a[148:151],v[196:199],v[68:71]\n"
RD"v[216:219],v244 offset:6144\n"
MU"v103,s55,v103\n"
MF"v[68:71],a[132:135],v[192:195],v[68:71]\n"
BL"v235,s[12:15],s48 offen offset:1024 lds\n"
MF"v[44:47],a[136:139],v[192:195],v[44:47]\n"
PK"v96,v96,v97\n"
PK"v97,v98,v99\n"
MF"v[44:47],a[152:155],v[196:199],v[44:47]\n"
RD"v[220:223],v245 offset:6144\n"
PK"v98,v100,v101\n"
MF"v[76:79],a[156:159],v[196:199],v[76:79]\n"
PK"v99,v102,v103\n"
MU"v104,s55,v104\n"
MF"v[76:79],a[140:143],v[192:195],v[76:79]\n"
MU"v105,s55,v105\n"
MU"v106,s55,v106\n"
MF"v[52:55],a[128:131],v[200:203],v[52:55]\n"
BL"v236,s[12:15],s48 offen offset:2048 lds\n"
MF"v[52:55],a[144:147],v[204:207],v[52:55]\n"
RD"v[192:195],v244 offset:24576\n"
MU"v107,s55,v107\n"
MF"v[84:87],a[148:151],v[204:207],v[84:87]\n"
MU"v108,s55,v108\n"
MU"v109,s55,v109\n"
MF"v[84:87],a[132:135],v[200:203],v[84:87]\n"
MU"v110,s55,v110\n"
MU"v111,s55,v111\n"
MF"v[60:63],a[136:139],v[200:203],v[60:63]\n"
PK"v104,v104,v105\n"
PK"v105,v106,v107\n"
MF"v[60:63],a[152:155],v[204:207],v[60:63]\n"
RD"v[196:199],v245 offset:24576\n"
PK"v106,v108,v109\n"
MF"v[92:95],a[156:159],v[204:207],v[92:95]\n"
BL"v237,s[12:15],s48 offen offset:3072 lds\n"
MF"v[92:95],a[140:143],v[200:203],v[92:95]\n"
WT"lgkmcnt(2)\n"
PK"v107,v110,v111\n"
MF"v[36:39],a[160:163],v[208:211],v[36:39]\n"
MU"v112,s55,v112\n"
MU"v113,s55,v113\n"
MF"v[36:39],a[176:179],v[212:215],v[36:39]\n"
RD"v[200:203],v244 offset:26624\n"
MU"v114,s55,v114\n"
MF"v[68:71],a[180:183],v[212:215],v[68:71]\n"
SA"m0,s37,20480\n"
MU"v115,s55,v115\n"
MU"v116,s55,v116\n"
MF"v[68:71],a[164:167],v[208:211],v[68:71]\n"
SA"s48,s98,98304\n"
MU"v117,s55,v117\n"
MU"v118,s55,v118\n"
MF"v[44:47],a[168:171],v[208:211],v[44:47]\n"
BL"v234,s[12:15],s48 offen offset:0 lds\n"
MF"v[44:47],a[184:187],v[212:215],v[44:47]\n"
RD"v[204:207],v245 offset:26624\n"
MU"v119,s55,v119\n"
MF"v[76:79],a[188:191],v[212:215],v[76:79]\n"
PK"v112,v112,v113\n"
PK"v113,v114,v115\n"
MF"v[76:79],a[172:175],v[208:211],v[76:79]\n"
PK"v114,v116,v117\n"
PK"v115,v118,v119\n"
MF"v[52:55],a[160:163],v[216:219],v[52:55]\n"
RD"v[208:211],v244 offset:28672\n"
MF"v[52:55],a[176:179],v[220:223],v[52:55]\n"
"s_mov_b32 s49,12288\n"
MF"v[84:87],a[180:183],v[220:223],v[84:87]\n"
BL"v235,s[12:15],s48 offen offset:1024 lds\n"
MF"v[84:87],a[164:167],v[216:219],v[84:87]\n"
BS"v[48:51],v250,s[8:11],s49 offen offset:0 sc1\n"
MF"v[60:63],a[168:171],v[216:219],v[60:63]\n"
RD"v[212:215],v245 offset:28672\n"
MU"v120,s55,v120\n"
MF"v[60:63],a[184:187],v[220:223],v[60:63]\n"
MU"v121,s55,v121\n"
MU"v122,s55,v122\n"
MF"v[92:95],a[188:191],v[220:223],v[92:95]\n"
MU"v123,s55,v123\n"
MU"v124,s55,v124\n"
MF"v[92:95],a[172:175],v[216:219],v[92:95]\n"
WT"lgkmcnt(1)\n"
MU"v125,s55,v125\n"
MU"v126,s55,v126\n"
MF"v[36:39],a[192:195],v[192:195],v[36:39]\n"
RD"v[216:219],v244 offset:30720\n"
MU"v127,s55,v127\n"
MF"v[36:39],a[208:211],v[196:199],v[36:39]\n"
BL"v236,s[12:15],s48 offen offset:2048 lds\n"
MF"v[68:71],a[212:215],v[196:199],v[68:71]\n"
PK"v120,v120,v121\n"
PK"v121,v122,v123\n"
MF"v[68:71],a[196:199],v[192:195],v[68:71]\n"
PK"v122,v124,v125\n"
PK"v123,v126,v127\n"
MF"v[44:47],a[200:203],v[192:195],v[44:47]\n"
RD"v[220:223],v245 offset:30720\n"
MF"v[44:47],a[216:219],v[196:199],v[44:47]\n"
MF"v[76:79],a[220:223],v[196:199],v[76:79]\n"
BL"v237,s[12:15],s48 offen offset:3072 lds\n"
MF"v[76:79],a[204:207],v[192:195],v[76:79]\n"
MF"v[52:55],a[192:195],v[200:203],v[52:55]\n"
MF"v[52:55],a[208:211],v[204:207],v[52:55]\n"
MF"v[84:87],a[212:215],v[204:207],v[84:87]\n"
MF"v[84:87],a[196:199],v[200:203],v[84:87]\n"
"v_lshrrev_b32 v251,2,v224\n"
MF"v[60:63],a[200:203],v[200:203],v[60:63]\n"
WT"vmcnt(24) lgkmcnt(0)\n"
"v_and_b32 v251,28,v251\n"
MF"v[60:63],a[216:219],v[204:207],v[60:63]\n"
"s_barrier\n"
MF"v[92:95],a[220:223],v[204:207],v[92:95]\n"
RD"v[192:195],v242 offset:8192\n"
MF"v[92:95],a[204:207],v[200:203],v[92:95]\n"
"ds_read_b32 v234,v251 offset:16384\n"
MF"v[36:39],a[224:227],v[208:211],v[36:39]\n"
MF"v[36:39],a[240:243],v[212:215],v[36:39]\n"
MF"v[68:71],a[244:247],v[212:215],v[68:71]\n"
RD"v[196:199],v243 offset:8192\n"
MF"v[68:71],a[228:231],v[208:211],v[68:71]\n"
MF"v[44:47],a[232:235],v[208:211],v[44:47]\n"
MF"v[44:47],a[248:251],v[212:215],v[44:47]\n"
MF"v[76:79],a[252:255],v[212:215],v[76:79]\n"
RD"v[200:203],v242 offset:10240\n"
MF"v[76:79],a[236:239],v[208:211],v[76:79]\n"
WT"lgkmcnt(2)\n"
"v_not_b32 v251,v234\n"
"v_bfe_u32 v252,v251,4,7\n"
MF"v[52:55],a[224:227],v[216:219],v[52:55]\n"
"v_mul_u32_u24 v252,20480,v252\n"
MF"v[52:55],a[240:243],v[220:223],v[52:55]\n"
"v_and_b32 v251,15,v251\n"
MF"v[84:87],a[244:247],v[220:223],v[84:87]\n"
RD"v[204:207],v243 offset:10240\n"
"v_lshl_add_u32 v251,v251,4,v252\n"
MF"v[84:87],a[228:231],v[216:219],v[84:87]\n"
BS"v[56:59],v250,s[8:11],s49 offen offset:1024 sc1\n"
MF"v[60:63],a[232:235],v[216:219],v[60:63]\n"
"v_and_b32 v252,0x380,v224\n"
"v_lshl_add_u32 v251,v252,1,v251\n"
MF"v[60:63],a[248:251],v[220:223],v[60:63]\n"
"v_add_u32 v252,v252,v224\n"
MF"v[92:95],a[252:255],v[220:223],v[92:95]\n"
"s_xor_b32 s92,s34,0\n"
RD"v[208:211],v242 offset:12288\n"
MF"v[92:95],a[236:239],v[216:219],v[92:95]\n"
"s_lshl_b32 s92,s92,2\n"
MF"v[128:131],a[0:3],v[192:195],0\n"
WT"lgkmcnt(2)\n"
SA"s92,s92,s30\n"
MF"v[128:131],a[16:19],v[196:199],v[128:131]\n"
"s_lshl_b32 s93,s92,11\n"
RD"v[212:215],v243 offset:12288\n"
MF"v[132:135],a[20:23],v[196:199],0\n"
"s_cmp_lt_u32 s92,10\n"
"s_cselect_b32 s93,s93,2130706432\n"
MF"v[132:135],a[4:7],v[192:195],v[132:135]\n"
SA"s92,s93,524288\n"
MU"v36,s55,v36\n"
MU"v37,s55,v37\n"
MF"v[136:139],a[8:11],v[192:195],0\n"
BL"v[100:103],v251,s[8:11],s92 offen sc1\n"
MF"v[136:139],a[24:27],v[196:199],v[136:139]\n"
"s_mul_i32 s92,s35,20480\n"
RD"v[216:219],v242 offset:14336\n"
MU"v38,s55,v38\n"
MF"v[140:143],a[28:31],v[196:199],0\n"
SA"s92,s92,s93\n"
MU"v39,s55,v39\n"
MU"v68,s55,v68\n"
MF"v[140:143],a[12:15],v[192:195],v[140:143]\n"
SA"s92,s92,3145728\n"
MU"v69,s55,v69\n"
MU"v70,s55,v70\n"
MF"v[144:147],a[0:3],v[200:203],0\n"
WT"lgkmcnt(1)\n"
MU"v71,s55,v71\n"
PK"v36,v36,v37\n"
MF"v[144:147],a[16:19],v[204:207],v[144:147]\n"
RD"v[220:223],v243 offset:14336\n"
PK"v37,v38,v39\n"
MF"v[148:151],a[20:23],v[204:207],0\n"
PK"v38,v68,v69\n"
PK"v39,v70,v71\n"
MF"v[148:151],a[4:7],v[200:203],v[148:151]\n"
MF"v[152:155],a[8:11],v[200:203],0\n"
MF"v[152:155],a[24:27],v[204:207],v[152:155]\n"
RD"v[192:195],v242 offset:32768\n"
MU"v44,s55,v44\n"
MF"v[156:159],a[28:31],v[204:207],0\n"
MU"v45,s55,v45\n"
MU"v46,s55,v46\n"
MF"v[156:159],a[12:15],v[200:203],v[156:159]\n"
MU"v47,s55,v47\n"
MU"v76,s55,v76\n"
MF"v[128:131],a[32:35],v[208:211],v[128:131]\n"
MU"v77,s55,v77\n"
MU"v78,s55,v78\n"
MF"v[128:131],a[48:51],v[212:215],v[128:131]\n"
RD"v[196:199],v243 offset:32768\n"
MU"v79,s55,v79\n"
MF"v[132:135],a[52:55],v[212:215],v[132:135]\n"
PK"v44,v44,v45\n"
PK"v45,v46,v47\n"
MF"v[132:135],a[36:39],v[208:211],v[132:135]\n"
PK"v46,v76,v77\n"
PK"v47,v78,v79\n"
MF"v[136:139],a[40:43],v[208:211],v[136:139]\n"
SA"m0,s36,0\n"
RD"v[200:203],v242 offset:34816\n"
SA"s48,s60,s61\n"
MF"v[136:139],a[56:59],v[212:215],v[136:139]\n"
MF"v[140:143],a[60:63],v[212:215],v[140:143]\n"
BL"v238,s[20:23],s48 offen offset:0 lds\n"
MF"v[140:143],a[44:47],v[208:211],v[140:143]\n"
WT"lgkmcnt(2)\n"
MU"v52,s55,v52\n"
MU"v53,s55,v53\n"
MF"v[144:147],a[32:35],v[216:219],v[144:147]\n"
"s_mov_b32 s49,16384\n"
RD"v[204:207],v243 offset:34816\n"
MU"v54,s55,v54\n"
MF"v[144:147],a[48:51],v[220:223],v[144:147]\n"
BS"v[64:67],v250,s[8:11],s49 offen offset:0 sc1\n"
MF"v[148:151],a[52:55],v[220:223],v[148:151]\n"
MU"v55,s55,v55\n"
MU"v84,s55,v84\n"
MF"v[148:151],a[36:39],v[216:219],v[148:151]\n"
MU"v85,s55,v85\n"
MU"v86,s55,v86\n"
MF"v[152:155],a[40:43],v[216:219],v[152:155]\n"
RD"v[208:211],v242 offset:36864\n"
MU"v87,s55,v87\n"
MF"v[152:155],a[56:59],v[220:223],v[152:155]\n"
PK"v52,v52,v53\n"
PK"v53,v54,v55\n"
MF"v[156:159],a[60:63],v[220:223],v[156:159]\n"
BL"v239,s[20:23],s48 offen offset:1024 lds\n"
MF"v[156:159],a[44:47],v[216:219],v[156:159]\n"
PK"v54,v84,v85\n"
PK"v55,v86,v87\n"
MF"v[128:131],a[64:67],v[192:195],v[128:131]\n"
WT"lgkmcnt(1)\n"
RD"v[212:215],v243 offset:36864\n"
MF"v[128:131],a[80:83],v[196:199],v[128:131]\n"
MU"v60,s55,v60\n"
MU"v61,s55,v61\n"
MF"v[132:135],a[84:87],v[196:199],v[132:135]\n"
MU"v62,s55,v62\n"
MU"v63,s55,v63\n"
MF"v[132:135],a[68:71],v[192:195],v[132:135]\n"
RD"v[216:219],v242 offset:38912\n"
MU"v92,s55,v92\n"
MF"v[136:139],a[72:75],v[192:195],v[136:139]\n"
MU"v93,s55,v93\n"
MU"v94,s55,v94\n"
MF"v[136:139],a[88:91],v[196:199],v[136:139]\n"
MU"v95,s55,v95\n"
PK"v60,v60,v61\n"
MF"v[140:143],a[92:95],v[196:199],v[140:143]\n"
BL"v240,s[20:23],s48 offen offset:2048 lds\n"
MF"v[140:143],a[76:79],v[192:195],v[140:143]\n"
RD"v[220:223],v243 offset:38912\n"
PK"v61,v62,v63\n"
MF"v[144:147],a[64:67],v[200:203],v[144:147]\n"
PK"v62,v92,v93\n"
PK"v63,v94,v95\n"
MF"v[144:147],a[80:83],v[204:207],v[144:147]\n"
MF"v[148:151],a[84:87],v[204:207],v[148:151]\n"
MF"v[148:151],a[68:71],v[200:203],v[148:151]\n"
RD"v[192:195],v244 offset:8192\n"
MF"v[152:155],a[72:75],v[200:203],v[152:155]\n"
MF"v[152:155],a[88:91],v[204:207],v[152:155]\n"
MF"v[156:159],a[92:95],v[204:207],v[156:159]\n"
SA"s61,s61,524288\n"
BL"v241,s[20:23],s48 offen offset:3072 lds\n"
MF"v[156:159],a[76:79],v[200:203],v[156:159]\n"
WT"lgkmcnt(1)\n"
RD"v[196:199],v245 offset:8192\n"
MF"v[128:131],a[96:99],v[208:211],v[128:131]\n"
MF"v[128:131],a[112:115],v[212:215],v[128:131]\n"
MF"v[132:135],a[116:119],v[212:215],v[132:135]\n"
MF"v[132:135],a[100:103],v[208:211],v[132:135]\n"
RD"v[200:203],v244 offset:10240\n"
MF"v[136:139],a[104:107],v[208:211],v[136:139]\n"
MF"v[136:139],a[120:123],v[212:215],v[136:139]\n"
MF"v[140:143],a[124:127],v[212:215],v[140:143]\n"
RD"v[204:207],v245 offset:10240\n"
MF"v[140:143],a[108:111],v[208:211],v[140:143]\n"
MF"v[144:147],a[96:99],v[216:219],v[144:147]\n"
BS"v[72:75],v250,s[8:11],s49 offen offset:1024 sc1\n"
MF"v[144:147],a[112:115],v[220:223],v[144:147]\n"
MF"v[148:151],a[116:119],v[220:223],v[148:151]\n"
RD"v[208:211],v244 offset:12288\n"
MF"v[148:151],a[100:103],v[216:219],v[148:151]\n"
MF"v[152:155],a[104:107],v[216:219],v[152:155]\n"
SA"m0,s38,65536\n"
SA"s48,s60,s61\n"
MF"v[152:155],a[120:123],v[220:223],v[152:155]\n"
MF"v[156:159],a[124:127],v[220:223],v[156:159]\n"
RD"v[212:215],v245 offset:12288\n"
MF"v[156:159],a[108:111],v[216:219],v[156:159]\n"
WT"lgkmcnt(2)\n"
BL"v238,s[20:23],s48 offen offset:0 lds\n"
MF"v[128:131],a[128:131],v[192:195],v[128:131]\n"
MF"v[128:131],a[144:147],v[196:199],v[128:131]\n"
MF"v[132:135],a[148:151],v[196:199],v[132:135]\n"
RD"v[216:219],v244 offset:14336\n"
MF"v[132:135],a[132:135],v[192:195],v[132:135]\n"
MF"v[136:139],a[136:139],v[192:195],v[136:139]\n"
MF"v[136:139],a[152:155],v[196:199],v[136:139]\n"
RD"v[220:223],v245 offset:14336\n"
MF"v[140:143],a[156:159],v[196:199],v[140:143]\n"
BL"v239,s[20:23],s48 offen offset:1024 lds\n"
MF"v[140:143],a[140:143],v[192:195],v[140:143]\n"
MF"v[144:147],a[128:131],v[200:203],v[144:147]\n"
MF"v[144:147],a[144:147],v[204:207],v[144:147]\n"
RD"v[192:195],v244 offset:32768\n"
MF"v[148:151],a[148:151],v[204:207],v[148:151]\n"
MF"v[148:151],a[132:135],v[200:203],v[148:151]\n"
MF"v[152:155],a[136:139],v[200:203],v[152:155]\n"
MF"v[152:155],a[152:155],v[204:207],v[152:155]\n"
RD"v[196:199],v245 offset:32768\n"
MF"v[156:159],a[156:159],v[204:207],v[156:159]\n"
BL"v240,s[20:23],s48 offen offset:2048 lds\n"
MF"v[156:159],a[140:143],v[200:203],v[156:159]\n"
WT"lgkmcnt(2)\n"
MF"v[128:131],a[160:163],v[208:211],v[128:131]\n"
MF"v[128:131],a[176:179],v[212:215],v[128:131]\n"
RD"v[200:203],v244 offset:34816\n"
MF"v[132:135],a[180:183],v[212:215],v[132:135]\n"
MF"v[132:135],a[164:167],v[208:211],v[132:135]\n"
MF"v[136:139],a[168:171],v[208:211],v[136:139]\n"
MF"v[136:139],a[184:187],v[212:215],v[136:139]\n"
"s_mov_b32 s49,20480\n"
RD"v[204:207],v245 offset:34816\n"
MF"v[140:143],a[188:191],v[212:215],v[140:143]\n"
SA"s61,s61,524288\n"
BL"v241,s[20:23],s48 offen offset:3072 lds\n"
MF"v[140:143],a[172:175],v[208:211],v[140:143]\n"
"s_cmp_eq_u32 s61,5242880\n"
BS"v[80:83],v250,s[8:11],s49 offen offset:0 sc1\n"
MF"v[144:147],a[160:163],v[216:219],v[144:147]\n"
"s_cselect_b32 s61,0,s61\n"
RD"v[208:211],v244 offset:36864\n"
MF"v[144:147],a[176:179],v[220:223],v[144:147]\n"
MF"v[148:151],a[180:183],v[220:223],v[148:151]\n"
MF"v[148:151],a[164:167],v[216:219],v[148:151]\n"
MF"v[152:155],a[168:171],v[216:219],v[152:155]\n"
SA"m0,s38,114688\n"
RD"v[212:215],v245 offset:36864\n"
SA"s48,s60,s61\n"
MF"v[152:155],a[184:187],v[220:223],v[152:155]\n"
MF"v[156:159],a[188:191],v[220:223],v[156:159]\n"
BL"v238,s[20:23],s48 offen offset:0 lds\n"
MF"v[156:159],a[172:175],v[216:219],v[156:159]\n"
WT"lgkmcnt(1)\n"
MF"v[128:131],a[192:195],v[192:195],v[128:131]\n"
RD"v[216:219],v244 offset:38912\n"
MF"v[128:131],a[208:211],v[196:199],v[128:131]\n"
MF"v[132:135],a[212:215],v[196:199],v[132:135]\n"
MF"v[132:135],a[196:199],v[192:195],v[132:135]\n"
MF"v[136:139],a[200:203],v[192:195],v[136:139]\n"
RD"v[220:223],v245 offset:38912\n"
MF"v[136:139],a[216:219],v[196:199],v[136:139]\n"
MF"v[140:143],a[220:223],v[196:199],v[140:143]\n"
BL"v239,s[20:23],s48 offen offset:1024 lds\n"
MF"v[140:143],a[204:207],v[192:195],v[140:143]\n"
MF"v[144:147],a[192:195],v[200:203],v[144:147]\n"
WT"vmcnt(13)\n"
BS"v[100:103],v252,s[8:11],s92 offen sc1\n"
MF"v[144:147],a[208:211],v[204:207],v[144:147]\n"
MF"v[148:151],a[212:215],v[204:207],v[148:151]\n"
MF"v[148:151],a[196:199],v[200:203],v[148:151]\n"
MF"v[152:155],a[200:203],v[200:203],v[152:155]\n"
WT"lgkmcnt(0)\n"
MF"v[152:155],a[216:219],v[204:207],v[152:155]\n"
"s_barrier\n"
MF"v[156:159],a[220:223],v[204:207],v[156:159]\n"
RD"v[192:195],v242 offset:16384\n"
MF"v[156:159],a[204:207],v[200:203],v[156:159]\n"
BL"v240,s[20:23],s48 offen offset:2048 lds\n"
MF"v[128:131],a[224:227],v[208:211],v[128:131]\n"
MF"v[128:131],a[240:243],v[212:215],v[128:131]\n"
MF"v[132:135],a[244:247],v[212:215],v[132:135]\n"
RD"v[196:199],v243 offset:16384\n"
MF"v[132:135],a[228:231],v[208:211],v[132:135]\n"
MF"v[136:139],a[232:235],v[208:211],v[136:139]\n"
MF"v[136:139],a[248:251],v[212:215],v[136:139]\n"
MF"v[140:143],a[252:255],v[212:215],v[140:143]\n"
SA"s61,s61,524288\n"
RD"v[200:203],v242 offset:18432\n"
MF"v[140:143],a[236:239],v[208:211],v[140:143]\n"
BS"v[88:91],v250,s[8:11],s49 offen offset:1024 sc1\n"
MF"v[144:147],a[224:227],v[216:219],v[144:147]\n"
"s_lshl_b32 s50,s34,3\n"
BL"v241,s[20:23],s48 offen offset:3072 lds\n"
MF"v[144:147],a[240:243],v[220:223],v[144:147]\n"
SA"s51,s50,s30\n"
MF"v[148:151],a[244:247],v[220:223],v[148:151]\n"
"s_mul_i32 s51,s51,20480\n"
RD"v[204:207],v243 offset:18432\n"
MF"v[148:151],a[228:231],v[216:219],v[148:151]\n"
SA"s56,s51,524288\n"
MF"v[152:155],a[232:235],v[216:219],v[152:155]\n"
SA"m0,s36,16384\n"
SA"s48,s60,s61\n"
SA"s57,s56,81920\n"
MF"v[152:155],a[248:251],v[220:223],v[152:155]\n"
"s_sub_u32 s51,s30,s50\n"
MF"v[156:159],a[252:255],v[220:223],v[156:159]\n"
SA"s51,s51,120\n"
RD"v[208:211],v242 offset:20480\n"
MF"v[156:159],a[236:239],v[216:219],v[156:159]\n"
WT"lgkmcnt(2)\n"
BL"v238,s[20:23],s48 offen offset:0 lds\n"
MF"v[0:3],a[0:3],v[192:195],0\n"
"s_mul_i32 s51,s51,20480\n"
SA"s58,s51,524288\n"
"s_lshl_b32 s99,s99,12\n"
MF"v[0:3],a[16:19],v[196:199],v[0:3]\n"
SA"s59,s58,81920\n"
RD"v[212:215],v243 offset:20480\n"
MF"v[8:11],a[20:23],v[196:199],0\n"
"v_add_u32 v235,s99,v224\n"
MF"v[8:11],a[4:7],v[192:195],v[8:11]\n"
MU"v128,s55,v128\n"
MU"v129,s55,v129\n"
MF"v[16:19],a[8:11],v[192:195],0\n"
MU"v130,s55,v130\n"
MU"v131,s55,v131\n"
MF"v[16:19],a[24:27],v[196:199],v[16:19]\n"
RD"v[216:219],v242 offset:22528\n"
MU"v132,s55,v132\n"
MF"v[24:27],a[28:31],v[196:199],0\n"
BL"v239,s[20:23],s48 offen offset:1024 lds\n"
MF"v[24:27],a[12:15],v[192:195],v[24:27]\n"
MU"v133,s55,v133\n"
MU"v134,s55,v134\n"
MF"v[32:35],a[0:3],v[200:203],0\n"
WT"lgkmcnt(1)\n"
MU"v135,s55,v135\n"
PK"v64,v128,v129\n"
MF"v[32:35],a[16:19],v[204:207],v[32:35]\n"
RD"v[220:223],v243 offset:22528\n"
PK"v65,v130,v131\n"
MF"v[40:43],a[20:23],v[204:207],0\n"
PK"v66,v132,v133\n"
PK"v67,v134,v135\n"
MF"v[40:43],a[4:7],v[200:203],v[40:43]\n"
MF"v[48:51],a[8:11],v[200:203],0\n"
MF"v[48:51],a[24:27],v[204:207],v[48:51]\n"
RD"v[192:195],v242 offset:40960\n"
MU"v136,s55,v136\n"
MF"v[56:59],a[28:31],v[204:207],0\n"
BL"v240,s[20:23],s48 offen offset:2048 lds\n"
MF"v[56:59],a[12:15],v[200:203],v[56:59]\n"
MU"v137,s55,v137\n"
MU"v138,s55,v138\n"
MF"v[0:3],a[32:35],v[208:211],v[0:3]\n"
BL"a[0:3],v235,s[8:11],s56 offen offset:0 sc1\n"
MF"v[0:3],a[48:51],v[212:215],v[0:3]\n"
RD"v[196:199],v243 offset:40960\n"
MU"v139,s55,v139\n"
MF"v[8:11],a[52:55],v[212:215],v[8:11]\n"
MU"v140,s55,v140\n"
MU"v141,s55,v141\n"
MF"v[8:11],a[36:39],v[208:211],v[8:11]\n"
"s_mov_b32 s49,24576\n"
MU"v142,s55,v142\n"
MU"v143,s55,v143\n"
MF"v[16:19],a[40:43],v[208:211],v[16:19]\n"
RD"v[200:203],v242 offset:43008\n"
PK"v68,v136,v137\n"
MF"v[16:19],a[56:59],v[212:215],v[16:19]\n"
BS"v[96:99],v250,s[8:11],s49 offen offset:0 sc1\n"
MF"v[24:27],a[60:63],v[212:215],v[24:27]\n"
SA"s61,s61,524288\n"
BL"v241,s[20:23],s48 offen offset:3072 lds\n"
MF"v[24:27],a[44:47],v[208:211],v[24:27]\n"
WT"lgkmcnt(2)\n"
BL"a[4:7],v235,s[8:11],s57 offen offset:0 sc1\n"
MF"v[32:35],a[32:35],v[216:219],v[32:35]\n"
"s_cmp_eq_u32 s61,5242880\n"
RD"v[204:207],v243 offset:43008\n"
PK"v69,v138,v139\n"
MF"v[32:35],a[48:51],v[220:223],v[32:35]\n"
"s_cselect_b32 s61,0,s61\n"
PK"v70,v140,v141\n"
PK"v71,v142,v143\n"
MF"v[40:43],a[52:55],v[220:223],v[40:43]\n"
MU"v144,s55,v144\n"
MU"v145,s55,v145\n"
MF"v[40:43],a[36:39],v[216:219],v[40:43]\n"
MU"v146,s55,v146\n"
MU"v147,s55,v147\n"
MF"v[48:51],a[40:43],v[216:219],v[48:51]\n"
RD"v[208:211],v242 offset:45056\n"
MU"v148,s55,v148\n"
MF"v[48:51],a[56:59],v[220:223],v[48:51]\n"
BL"a[8:11],v235,s[8:11],s58 offen offset:0 sc1\n"
MF"v[56:59],a[60:63],v[220:223],v[56:59]\n"
MU"v149,s55,v149\n"
MU"v150,s55,v150\n"
MF"v[56:59],a[44:47],v[216:219],v[56:59]\n"
MU"v151,s55,v151\n"
PK"v72,v144,v145\n"
MF"v[0:3],a[64:67],v[192:195],v[0:3]\n"
WT"lgkmcnt(1)\n"
RD"v[212:215],v243 offset:45056\n"
PK"v73,v146,v147\n"
MF"v[0:3],a[80:83],v[196:199],v[0:3]\n"
PK"v74,v148,v149\n"
PK"v75,v150,v151\n"
MF"v[8:11],a[84:87],v[196:199],v[8:11]\n"
BL"a[12:15],v235,s[8:11],s59 offen offset:0 sc1\n"
MF"v[8:11],a[68:71],v[192:195],v[8:11]\n"
RD"v[216:219],v242 offset:47104\n"
MU"v152,s55,v152\n"
MF"v[16:19],a[72:75],v[192:195],v[16:19]\n"
MU"v153,s55,v153\n"
MU"v154,s55,v154\n"
MF"v[16:19],a[88:91],v[196:199],v[16:19]\n"
MU"v155,s55,v155\n"
MU"v156,s55,v156\n"
MF"v[24:27],a[92:95],v[196:199],v[24:27]\n"
MU"v157,s55,v157\n"
MU"v158,s55,v158\n"
MF"v[24:27],a[76:79],v[192:195],v[24:27]\n"
RD"v[220:223],v243 offset:47104\n"
MU"v159,s55,v159\n"
MF"v[32:35],a[64:67],v[200:203],v[32:35]\n"
BL"a[16:19],v235,s[8:11],s56 offen offset:1024 sc1\n"
MF"v[32:35],a[80:83],v[204:207],v[32:35]\n"
PK"v76,v152,v153\n"
PK"v77,v154,v155\n"
MF"v[40:43],a[84:87],v[204:207],v[40:43]\n"
PK"v78,v156,v157\n"
PK"v79,v158,v159\n"
MF"v[40:43],a[68:71],v[200:203],v[40:43]\n"
RD"v[192:195],v244 offset:16384\n"
MF"v[48:51],a[72:75],v[200:203],v[48:51]\n"
MF"v[48:51],a[88:91],v[204:207],v[48:51]\n"
MF"v[56:59],a[92:95],v[204:207],v[56:59]\n"
BL"a[20:23],v235,s[8:11],s57 offen offset:1024 sc1\n"
MF"v[56:59],a[76:79],v[200:203],v[56:59]\n"
WT"lgkmcnt(1)\n"
RD"v[196:199],v245 offset:16384\n"
MF"v[0:3],a[96:99],v[208:211],v[0:3]\n"
MF"v[0:3],a[112:115],v[212:215],v[0:3]\n"
MF"v[8:11],a[116:119],v[212:215],v[8:11]\n"
MF"v[8:11],a[100:103],v[208:211],v[8:11]\n"
RD"v[200:203],v244 offset:18432\n"
MF"v[16:19],a[104:107],v[208:211],v[16:19]\n"
BS"v[104:107],v250,s[8:11],s49 offen offset:1024 sc1\n"
MF"v[16:19],a[120:123],v[212:215],v[16:19]\n"
BL"a[24:27],v235,s[8:11],s58 offen offset:1024 sc1\n"
MF"v[24:27],a[124:127],v[212:215],v[24:27]\n"
RD"v[204:207],v245 offset:18432\n"
MF"v[24:27],a[108:111],v[208:211],v[24:27]\n"
MF"v[32:35],a[96:99],v[216:219],v[32:35]\n"
MF"v[32:35],a[112:115],v[220:223],v[32:35]\n"
MF"v[40:43],a[116:119],v[220:223],v[40:43]\n"
RD"v[208:211],v244 offset:20480\n"
MF"v[40:43],a[100:103],v[216:219],v[40:43]\n"
BL"a[28:31],v235,s[8:11],s59 offen offset:1024 sc1\n"
MF"v[48:51],a[104:107],v[216:219],v[48:51]\n"
MF"v[48:51],a[120:123],v[220:223],v[48:51]\n"
MF"v[56:59],a[124:127],v[220:223],v[56:59]\n"
RD"v[212:215],v245 offset:20480\n"
MF"v[56:59],a[108:111],v[216:219],v[56:59]\n"
WT"lgkmcnt(2)\n"
MF"v[0:3],a[128:131],v[192:195],v[0:3]\n"
BL"a[32:35],v235,s[8:11],s56 offen offset:2048 sc1\n"
MF"v[0:3],a[144:147],v[196:199],v[0:3]\n"
MF"v[8:11],a[148:151],v[196:199],v[8:11]\n"
RD"v[216:219],v244 offset:22528\n"
MF"v[8:11],a[132:135],v[192:195],v[8:11]\n"
MF"v[16:19],a[136:139],v[192:195],v[16:19]\n"
MF"v[16:19],a[152:155],v[196:199],v[16:19]\n"
RD"v[220:223],v245 offset:22528\n"
MF"v[24:27],a[156:159],v[196:199],v[24:27]\n"
BL"a[36:39],v235,s[8:11],s57 offen offset:2048 sc1\n"
MF"v[24:27],a[140:143],v[192:195],v[24:27]\n"
MF"v[32:35],a[128:131],v[200:203],v[32:35]\n"
MF"v[32:35],a[144:147],v[204:207],v[32:35]\n"
RD"v[192:195],v244 offset:40960\n"
MF"v[40:43],a[148:151],v[204:207],v[40:43]\n"
MF"v[40:43],a[132:135],v[200:203],v[40:43]\n"
MF"v[48:51],a[136:139],v[200:203],v[48:51]\n"
BL"a[40:43],v235,s[8:11],s58 offen offset:2048 sc1\n"
MF"v[48:51],a[152:155],v[204:207],v[48:51]\n"
RD"v[196:199],v245 offset:40960\n"
MF"v[56:59],a[156:159],v[204:207],v[56:59]\n"
MF"v[56:59],a[140:143],v[200:203],v[56:59]\n"
WT"lgkmcnt(2)\n"
MF"v[0:3],a[160:163],v[208:211],v[0:3]\n"
"s_sub_u32 s53,s37,65536\n"
MF"v[0:3],a[176:179],v[212:215],v[0:3]\n"
"s_mov_b32 s49,28672\n"
RD"v[200:203],v244 offset:43008\n"
MF"v[8:11],a[180:183],v[212:215],v[8:11]\n"
BL"a[44:47],v235,s[8:11],s59 offen offset:2048 sc1\n"
MF"v[8:11],a[164:167],v[208:211],v[8:11]\n"
BS"v[112:115],v250,s[8:11],s49 offen offset:0 sc1\n"
MF"v[16:19],a[168:171],v[208:211],v[16:19]\n"
"v_subrev_u32 v246,s53,v246\n"
"v_subrev_u32 v247,s53,v247\n"
MF"v[16:19],a[184:187],v[212:215],v[16:19]\n"
RD"v[204:207],v245 offset:43008\n"
"v_subrev_u32 v248,s53,v248\n"
MF"v[24:27],a[188:191],v[212:215],v[24:27]\n"
"v_subrev_u32 v249,s53,v249\n"
"v_subrev_u32 v236,65536,v246\n"
MF"v[24:27],a[172:175],v[208:211],v[24:27]\n"
"v_subrev_u32 v237,65536,v247\n"
"v_subrev_u32 v228,65536,v248\n"
MF"v[32:35],a[160:163],v[216:219],v[32:35]\n"
RD"v[208:211],v244 offset:45056\n"
"v_subrev_u32 v229,65536,v249\n"
MF"v[32:35],a[176:179],v[220:223],v[32:35]\n"
BL"a[48:51],v235,s[8:11],s56 offen offset:3072 sc1\n"
MF"v[40:43],a[180:183],v[220:223],v[40:43]\n"
MF"v[40:43],a[164:167],v[216:219],v[40:43]\n"
MF"v[48:51],a[168:171],v[216:219],v[48:51]\n"
RD"v[212:215],v245 offset:45056\n"
MF"v[48:51],a[184:187],v[220:223],v[48:51]\n"
MF"v[56:59],a[188:191],v[220:223],v[56:59]\n"
BL"a[52:55],v235,s[8:11],s57 offen offset:3072 sc1\n"
MF"v[56:59],a[172:175],v[216:219],v[56:59]\n"
WT"lgkmcnt(1)\n"
MF"v[0:3],a[192:195],v[192:195],v[0:3]\n"
RD"v[216:219],v244 offset:47104\n"
MF"v[0:3],a[208:211],v[196:199],v[0:3]\n"
MF"v[8:11],a[212:215],v[196:199],v[8:11]\n"
MF"v[8:11],a[196:199],v[192:195],v[8:11]\n"
MF"v[16:19],a[200:203],v[192:195],v[16:19]\n"
RD"v[220:223],v245 offset:47104\n"
MF"v[16:19],a[216:219],v[196:199],v[16:19]\n"
BL"a[56:59],v235,s[8:11],s58 offen offset:3072 sc1\n"
MF"v[24:27],a[220:223],v[196:199],v[24:27]\n"
MF"v[24:27],a[204:207],v[192:195],v[24:27]\n"
MF"v[32:35],a[192:195],v[200:203],v[32:35]\n"
SA"s99,s99,4096\n"
MF"v[32:35],a[208:211],v[204:207],v[32:35]\n"
"s_cmp_eq_u32 s99,20480\n"
"s_cselect_b32 s99,0,s99\n"
MF"v[40:43],a[212:215],v[204:207],v[40:43]\n"
BL"a[60:63],v235,s[8:11],s59 offen offset:3072 sc1\n"
MF"v[40:43],a[196:199],v[200:203],v[40:43]\n"
MF"v[48:51],a[200:203],v[200:203],v[48:51]\n"
WT"lgkmcnt(0)\n"
MF"v[48:51],a[216:219],v[204:207],v[48:51]\n"
"s_barrier\n"
MF"v[56:59],a[220:223],v[204:207],v[56:59]\n"
RT"a[128:129],v236 offset:49152\n"
MF"v[56:59],a[204:207],v[200:203],v[56:59]\n"
"v_add_u32 v235,s99,v224\n"
MF"v[0:3],a[224:227],v[208:211],v[0:3]\n"
SA"m0,s38,73728\n"
RT"a[130:131],v236 offset:49664\n"
SA"s48,s60,s61\n"
MF"v[0:3],a[240:243],v[212:215],v[0:3]\n"
BL"a[64:67],v235,s[8:11],s56 offen offset:0 sc1\n"
MF"v[8:11],a[244:247],v[212:215],v[8:11]\n"
RT"a[132:133],v236 offset:57344\n"
"v_add_u32 v242,49152,v246\n"
MF"v[8:11],a[228:231],v[208:211],v[8:11]\n"
BS"v[120:123],v250,s[8:11],s49 offen offset:1024 sc1\n"
MF"v[16:19],a[232:235],v[208:211],v[16:19]\n"
BL"v238,s[20:23],s48 offen offset:0 lds\n"
MF"v[16:19],a[248:251],v[212:215],v[16:19]\n"
RT"a[134:135],v236 offset:57856\n"
"v_add_u32 v243,49152,v247\n"
MF"v[24:27],a[252:255],v[212:215],v[24:27]\n"
RT"a[136:137],v237 offset:49152\n"
"v_add_u32 v244,49152,v248\n"
MF"v[24:27],a[236:239],v[208:211],v[24:27]\n"
BL"a[68:71],v235,s[8:11],s57 offen offset:0 sc1\n"
MF"v[32:35],a[224:227],v[216:219],v[32:35]\n"
RT"a[138:139],v237 offset:49664\n"
"v_add_u32 v245,49152,v249\n"
MF"v[32:35],a[240:243],v[220:223],v[32:35]\n"
MF"v[40:43],a[244:247],v[220:223],v[40:43]\n"
RT"a[140:141],v237 offset:57344\n"
MF"v[40:43],a[228:231],v[216:219],v[40:43]\n"
BL"v239,s[20:23],s48 offen offset:1024 lds\n"
MF"v[48:51],a[232:235],v[216:219],v[48:51]\n"
RT"a[142:143],v237 offset:57856\n"
MF"v[48:51],a[248:251],v[220:223],v[48:51]\n"
BL"a[72:75],v235,s[8:11],s58 offen offset:0 sc1\n"
MF"v[56:59],a[252:255],v[220:223],v[56:59]\n"
RT"a[144:145],v228 offset:49152\n"
MF"v[56:59],a[236:239],v[216:219],v[56:59]\n"
WT"vmcnt(25) lgkmcnt(3)\n"
MF"v[80:83],a[128:131],a[0:3],0\n"
WT"vmcnt(19)\n"
RT"a[146:147],v228 offset:49664\n"
MF"v[80:83],a[132:135],a[16:19],v[80:83]\n"
WT"vmcnt(18)\n"
MF"v[88:91],a[132:135],a[20:23],0\n"
RT"a[148:149],v228 offset:57344\n"
MF"v[88:91],a[128:131],a[4:7],v[88:91]\n"
BL"v240,s[20:23],s48 offen offset:2048 lds\n"
MF"v[96:99],a[128:131],a[8:11],0\n"
WT"vmcnt(17)\n"
BL"a[76:79],v235,s[8:11],s59 offen offset:0 sc1\n"
MF"v[96:99],a[132:135],a[24:27],v[96:99]\n"
WT"vmcnt(17)\n"
RT"a[150:151],v228 offset:57856\n"
MU"v0,s55,v0\n"
MF"v[104:107],a[132:135],a[28:31],0\n"
RT"a[152:153],v229 offset:49152\n"
MU"v1,s55,v1\n"
MF"v[104:107],a[128:131],a[12:15],v[104:107]\n"
RT"a[154:155],v229 offset:49664\n"
MU"v2,s55,v2\n"
MF"v[84:87],a[136:139],a[0:3],0\n"
WT"lgkmcnt(4)\n"
MU"v3,s55,v3\n"
MU"v8,s55,v8\n"
MF"v[84:87],a[140:143],a[16:19],v[84:87]\n"
RT"a[156:157],v229 offset:57344\n"
MU"v9,s55,v9\n"
MF"v[92:95],a[140:143],a[20:23],0\n"
SA"s61,s61,524288\n"
BL"a[80:83],v235,s[8:11],s56 offen offset:1024 sc1\n"
MF"v[92:95],a[136:139],a[4:7],v[92:95]\n"
BL"v241,s[20:23],s48 offen offset:3072 lds\n"
MF"v[100:103],a[136:139],a[8:11],0\n"
RT"a[158:159],v229 offset:57856\n"
MU"v10,s55,v10\n"
MF"v[100:103],a[140:143],a[24:27],v[100:103]\n"
RT"a[160:161],v236 offset:53248\n"
MU"v11,s55,v11\n"
MF"v[108:111],a[140:143],a[28:31],0\n"
PK"v0,v0,v1\n"
PK"v1,v2,v3\n"
MF"v[108:111],a[136:139],a[12:15],v[108:111]\n"
RT"a[162:163],v236 offset:53760\n"
PK"v2,v8,v9\n"
MF"v[112:115],a[144:147],a[0:3],0\n"
WT"lgkmcnt(3)\n"
BL"a[84:87],v235,s[8:11],s57 offen offset:1024 sc1\n"
MF"v[112:115],a[148:151],a[16:19],v[112:115]\n"
RT"a[164:165],v236 offset:61440\n"
PK"v3,v10,v11\n"
MF"v[120:123],a[148:151],a[20:23],0\n"
MU"v16,s55,v16\n"
MU"v17,s55,v17\n"
MF"v[120:123],a[144:147],a[4:7],v[120:123]\n"
RT"a[166:167],v236 offset:61952\n"
MU"v18,s55,v18\n"
MF"v[128:131],a[144:147],a[8:11],0\n"
MU"v19,s55,v19\n"
MU"v24,s55,v24\n"
MF"v[128:131],a[148:151],a[24:27],v[128:131]\n"
RT"a[168:169],v237 offset:53248\n"
MU"v25,s55,v25\n"
MF"v[136:139],a[148:151],a[28:31],0\n"
MU"v26,s55,v26\n"
MU"v27,s55,v27\n"
MF"v[136:139],a[144:147],a[12:15],v[136:139]\n"
RT"a[170:171],v237 offset:53760\n"
PK"v16,v16,v17\n"
MF"v[116:119],a[152:155],a[0:3],0\n"
WT"lgkmcnt(3)\n"
BL"a[88:91],v235,s[8:11],s58 offen offset:1024 sc1\n"
MF"v[116:119],a[156:159],a[16:19],v[116:119]\n"
"s_mov_b32 s49,262144\n"
RT"a[172:173],v237 offset:61440\n"
PK"v17,v18,v19\n"
MF"v[124:127],a[156:159],a[20:23],0\n"
RT"a[174:175],v237 offset:61952\n"
PK"v18,v24,v25\n"
MF"v[124:127],a[152:155],a[4:7],v[124:127]\n"
BS"v[4:7],v250,s[8:11],s49 offen offset:0 sc1\n"
MF"v[132:135],a[152:155],a[8:11],0\n"
RT"a[176:177],v228 offset:53248\n"
PK"v19,v26,v27\n"
MF"v[132:135],a[156:159],a[24:27],v[132:135]\n"
MU"v32,s55,v32\n"
MU"v33,s55,v33\n"
MF"v[140:143],a[156:159],a[28:31],0\n"
RT"a[178:179],v228 offset:53760\n"
MU"v34,s55,v34\n"
MF"v[140:143],a[152:155],a[12:15],v[140:143]\n"
BL"a[92:95],v235,s[8:11],s59 offen offset:1024 sc1\n"
MF"v[144:147],a[160:163],a[0:3],0\n"
WT"lgkmcnt(3)\n"
RT"a[180:181],v228 offset:61440\n"
MU"v35,s55,v35\n"
MF"v[144:147],a[164:167],a[16:19],v[144:147]\n"
MU"v40,s55,v40\n"
MU"v41,s55,v41\n"
MF"v[152:155],a[164:167],a[20:23],0\n"
SA"m0,s38,122880\n"
RT"a[182:183],v228 offset:61952\n"
MU"v42,s55,v42\n"
MF"v[152:155],a[160:163],a[4:7],v[152:155]\n"
SA"s48,s60,s61\n"
MU"v43,s55,v43\n"
PK"v32,v32,v33\n"
MF"v[160:163],a[160:163],a[8:11],0\n"
"s_mov_b32 s88,4096\n"
RT"a[184:185],v229 offset:53248\n"
PK"v33,v34,v35\n"
MF"v[160:163],a[164:167],a[24:27],v[160:163]\n"
BL"v238,s[20:23],s48 offen offset:0 lds\n"
MF"v[168:171],a[164:167],a[28:31],0\n"
BL"a[96:99],v235,s[8:11],s56 offen offset:2048 sc1\n"
MF"v[168:171],a[160:163],a[12:15],v[168:171]\n"
RT"a[186:187],v229 offset:53760\n"
PK"v34,v40,v41\n"
MF"v[148:151],a[168:171],a[0:3],0\n"
WT"lgkmcnt(3)\n"
RT"a[188:189],v229 offset:61440\n"
PK"v35,v42,v43\n"
MF"v[148:151],a[172:175],a[16:19],v[148:151]\n"
MU"v48,s55,v48\n"
MU"v49,s55,v49\n"
MF"v[156:159],a[172:175],a[20:23],0\n"
RT"a[190:191],v229 offset:61952\n"
MU"v50,s55,v50\n"
MF"v[156:159],a[168:171],a[4:7],v[156:159]\n"
RT"a[128:129],v236 offset:32768\n"
MU"v51,s55,v51\n"
MF"v[164:167],a[168:171],a[8:11],0\n"
BL"v239,s[20:23],s48 offen offset:1024 lds\n"
MF"v[164:167],a[172:175],a[24:27],v[164:167]\n"
BL"a[100:103],v235,s[8:11],s57 offen offset:2048 sc1\n"
MF"v[172:175],a[172:175],a[28:31],0\n"
RT"a[130:131],v236 offset:33280\n"
MU"v56,s55,v56\n"
MF"v[172:175],a[168:171],a[12:15],v[172:175]\n"
RT"a[132:133],v236 offset:40960\n"
MU"v57,s55,v57\n"
MF"v[176:179],a[176:179],a[0:3],0\n"
WT"lgkmcnt(4)\n"
MU"v58,s55,v58\n"
MU"v59,s55,v59\n"
MF"v[176:179],a[180:183],a[16:19],v[176:179]\n"
RT"a[134:135],v236 offset:41472\n"
PK"v48,v48,v49\n"
MF"v[184:187],a[180:183],a[20:23],0\n"
PK"v49,v50,v51\n"
PK"v50,v56,v57\n"
MF"v[184:187],a[176:179],a[4:7],v[184:187]\n"
RT"a[136:137],v237 offset:32768\n"
PK"v51,v58,v59\n"
MF"v[192:195],a[176:179],a[8:11],0\n"
BL"a[104:107],v235,s[8:11],s58 offen offset:2048 sc1\n"
MF"v[192:195],a[180:183],a[24:27],v[192:195]\n"
BL"v240,s[20:23],s48 offen offset:2048 lds\n"
MF"v[200:203],a[180:183],a[28:31],0\n"
RT"a[138:139],v237 offset:33280\n"
MF"v[200:203],a[176:179],a[12:15],v[200:203]\n"
RT"a[140:141],v237 offset:40960\n"
MF"v[180:183],a[184:187],a[0:3],0\n"
WT"lgkmcnt(3)\n"
MF"v[180:183],a[188:191],a[16:19],v[180:183]\n"
RT"a[142:143],v237 offset:41472\n"
MF"v[188:191],a[188:191],a[20:23],0\n"
BL"a[108:111],v235,s[8:11],s59 offen offset:2048 sc1\n"
MF"v[188:191],a[184:187],a[4:7],v[188:191]\n"
RT"a[144:145],v228 offset:32768\n"
MF"v[196:199],a[184:187],a[8:11],0\n"
SA"s61,s61,524288\n"
BS"v[12:15],v250,s[8:11],s49 offen offset:1024 sc1\n"
MF"v[196:199],a[188:191],a[24:27],v[196:199]\n"
"s_cmp_eq_u32 s61,5242880\n"
BL"v241,s[20:23],s48 offen offset:3072 lds\n"
MF"v[204:207],a[188:191],a[28:31],0\n"
"s_cselect_b32 s61,0,s61\n"
RT"a[146:147],v228 offset:33280\n"
MF"v[204:207],a[184:187],a[12:15],v[204:207]\n"
WT"vmcnt(31)\n"
RT"a[148:149],v228 offset:40960\n"
MF"v[80:83],a[128:131],a[32:35],v[80:83]\n"
WT"vmcnt(26)\n"
MF"v[80:83],a[132:135],a[48:51],v[80:83]\n"
WT"vmcnt(25)\n"
RT"a[150:151],v228 offset:41472\n"
MF"v[88:91],a[132:135],a[52:55],v[88:91]\n"
BL"a[112:115],v235,s[8:11],s56 offen offset:3072 sc1\n"
MF"v[88:91],a[128:131],a[36:39],v[88:91]\n"
RT"a[152:153],v229 offset:32768\n"
MF"v[96:99],a[128:131],a[40:43],v[96:99]\n"
WT"vmcnt(25)\n"
RT"a[154:155],v229 offset:33280\n"
MF"v[96:99],a[132:135],a[56:59],v[96:99]\n"
WT"vmcnt(24)\n"
MF"v[104:107],a[132:135],a[60:63],v[104:107]\n"
RT"a[156:157],v229 offset:40960\n"
MF"v[104:107],a[128:131],a[44:47],v[104:107]\n"
WT"lgkmcnt(4)\n"
MF"v[84:87],a[136:139],a[32:35],v[84:87]\n"
RT"a[158:159],v229 offset:41472\n"
MF"v[84:87],a[140:143],a[48:51],v[84:87]\n"
BL"a[116:119],v235,s[8:11],s57 offen offset:3072 sc1\n"
MF"v[92:95],a[140:143],a[52:55],v[92:95]\n"
RT"a[160:161],v236 offset:36864\n"
MF"v[92:95],a[136:139],a[36:39],v[92:95]\n"
MF"v[100:103],a[136:139],a[40:43],v[100:103]\n"
RT"a[162:163],v236 offset:37376\n"
MF"v[100:103],a[140:143],a[56:59],v[100:103]\n"
MF"v[108:111],a[140:143],a[60:63],v[108:111]\n"
RT"a[164:165],v236 offset:45056\n"
MF"v[108:111],a[136:139],a[44:47],v[108:111]\n"
BL"a[120:123],v235,s[8:11],s58 offen offset:3072 sc1\n"
MF"v[112:115],a[144:147],a[32:35],v[112:115]\n"
WT"lgkmcnt(3)\n"
RT"a[166:167],v236 offset:45568\n"
MF"v[112:115],a[148:151],a[48:51],v[112:115]\n"
MF"v[120:123],a[148:151],a[52:55],v[120:123]\n"
RT"a[168:169],v237 offset:36864\n"
MF"v[120:123],a[144:147],a[36:39],v[120:123]\n"
MF"v[128:131],a[144:147],a[40:43],v[128:131]\n"
RT"a[170:171],v237 offset:37376\n"
MF"v[128:131],a[148:151],a[56:59],v[128:131]\n"
SA"s99,s99,4096\n"
RT"a[172:173],v237 offset:45056\n"
MF"v[136:139],a[148:151],a[60:63],v[136:139]\n"
"s_cmp_eq_u32 s99,20480\n"
BL"a[124:127],v235,s[8:11],s59 offen offset:3072 sc1\n"
MF"v[136:139],a[144:147],a[44:47],v[136:139]\n"
"s_cselect_b32 s99,0,s99\n"
RT"a[174:175],v237 offset:45568\n"
MF"v[116:119],a[152:155],a[32:35],v[116:119]\n"
MF"v[116:119],a[156:159],a[48:51],v[116:119]\n"
RT"a[176:177],v228 offset:36864\n"
MF"v[124:127],a[156:159],a[52:55],v[124:127]\n"
MF"v[124:127],a[152:155],a[36:39],v[124:127]\n"
RT"a[178:179],v228 offset:37376\n"
MF"v[132:135],a[152:155],a[40:43],v[132:135]\n"
"s_mov_b32 s49,266240\n"
"v_add_u32 v235,s99,v224\n"
MF"v[132:135],a[156:159],a[56:59],v[132:135]\n"
RT"a[180:181],v228 offset:45056\n"
MF"v[140:143],a[156:159],a[60:63],v[140:143]\n"
BL"a[0:3],v235,s[8:11],s56 offen offset:0 sc1\n"
MF"v[140:143],a[152:155],a[44:47],v[140:143]\n"
WT"lgkmcnt(3)\n"
BS"v[20:23],v250,s[8:11],s49 offen offset:0 sc1\n"
MF"v[144:147],a[160:163],a[32:35],v[144:147]\n"
RT"a[182:183],v228 offset:45568\n"
MF"v[144:147],a[164:167],a[48:51],v[144:147]\n"
RT"a[184:185],v229 offset:36864\n"
MF"v[152:155],a[164:167],a[52:55],v[152:155]\n"
MF"v[152:155],a[160:163],a[36:39],v[152:155]\n"
RT"a[186:187],v229 offset:37376\n"
MF"v[160:163],a[160:163],a[40:43],v[160:163]\n"
BL"a[4:7],v235,s[8:11],s57 offen offset:0 sc1\n"
MF"v[160:163],a[164:167],a[56:59],v[160:163]\n"
RT"a[188:189],v229 offset:45056\n"
MF"v[168:171],a[164:167],a[60:63],v[168:171]\n"
MF"v[168:171],a[160:163],a[44:47],v[168:171]\n"
RT"a[190:191],v229 offset:45568\n"
MF"v[148:151],a[168:171],a[32:35],v[148:151]\n"
"s_lshl_b32 s50,s35,3\n"
SA"s50,s50,s34\n"
"s_lshl_b32 s50,s50,3\n"
MF"v[148:151],a[172:175],a[48:51],v[148:151]\n"
SA"s50,s50,2304\n"
SA"s51,s50,s45\n"
MF"v[156:159],a[172:175],a[52:55],v[156:159]\n"
MF"v[156:159],a[168:171],a[36:39],v[156:159]\n"
BL"a[8:11],v235,s[8:11],s58 offen offset:0 sc1\n"
MF"v[164:167],a[168:171],a[40:43],v[164:167]\n"
WT"vmcnt(29) lgkmcnt(0)\n"
MF"v[164:167],a[172:175],a[56:59],v[164:167]\n"
"s_barrier\n"
MF"v[172:175],a[172:175],a[60:63],v[172:175]\n"
RT"a[128:129],v236 offset:0\n"
MF"v[172:175],a[168:171],a[44:47],v[172:175]\n"
"buffer_store_dwordx2 v[232:233],v230,s[8:11],s51 offen sc1\n"
MF"v[176:179],a[176:179],a[32:35],v[176:179]\n"
RT"a[130:131],v236 offset:512\n"
MF"v[176:179],a[180:183],a[48:51],v[176:179]\n"
MF"v[184:187],a[180:183],a[52:55],v[184:187]\n"
SA"m0,s38,81920\n"
RT"a[132:133],v236 offset:8192\n"
SA"s48,s60,s61\n"
MF"v[184:187],a[176:179],a[36:39],v[184:187]\n"
BL"a[12:15],v235,s[8:11],s59 offen offset:0 sc1\n"
MF"v[192:195],a[176:179],a[40:43],v[192:195]\n"
RT"a[134:135],v236 offset:8704\n"
MF"v[192:195],a[180:183],a[56:59],v[192:195]\n"
BL"v238,s[20:23],s48 offen offset:0 lds\n"
MF"v[200:203],a[180:183],a[60:63],v[200:203]\n"
RT"a[136:137],v237 offset:0\n"
MF"v[200:203],a[176:179],a[44:47],v[200:203]\n"
MF"v[180:183],a[184:187],a[32:35],v[180:183]\n"
RT"a[138:139],v237 offset:512\n"
MF"v[180:183],a[188:191],a[48:51],v[180:183]\n"
BL"a[16:19],v235,s[8:11],s56 offen offset:1024 sc1\n"
MF"v[188:191],a[188:191],a[52:55],v[188:191]\n"
RT"a[140:141],v237 offset:8192\n"
MF"v[188:191],a[184:187],a[36:39],v[188:191]\n"
MF"v[196:199],a[184:187],a[40:43],v[196:199]\n"
RT"a[142:143],v237 offset:8704\n"
MF"v[196:199],a[188:191],a[56:59],v[196:199]\n"
BL"v239,s[20:23],s48 offen offset:1024 lds\n"
MF"v[204:207],a[188:191],a[60:63],v[204:207]\n"
SA"s49,s88,262144\n"
RT"a[144:145],v228 offset:0\n"
MF"v[204:207],a[184:187],a[44:47],v[204:207]\n"
"s_branch LQ_%=\n"
".p2align 6\n"
"LV_%=:\n"
WT"vmcnt(17)\n"
"v_lshlrev_b32 v148,16,v128\n"
"v_and_b32 v149,0xffff0000,v128\n"
"v_lshlrev_b32 v150,16,v129\n"
"v_and_b32 v151,0xffff0000,v129\n"
WT"vmcnt(16)\n"
"v_lshlrev_b32 v152,16,v130\n"
"v_and_b32 v153,0xffff0000,v130\n"
"v_lshlrev_b32 v154,16,v131\n"
"v_and_b32 v155,0xffff0000,v131\n"
WT"vmcnt(15)\n"
"v_lshlrev_b32 v156,16,v132\n"
"v_and_b32 v157,0xffff0000,v132\n"
"v_lshlrev_b32 v158,16,v133\n"
"v_and_b32 v159,0xffff0000,v133\n"
WT"vmcnt(14)\n"
"v_lshlrev_b32 v160,16,v134\n"
"v_and_b32 v161,0xffff0000,v134\n"
"v_lshlrev_b32 v162,16,v135\n"
"v_and_b32 v163,0xffff0000,v135\n"
WT"vmcnt(13)\n"
"v_lshlrev_b32 v164,16,v136\n"
"v_and_b32 v165,0xffff0000,v136\n"
"v_lshlrev_b32 v166,16,v137\n"
"v_and_b32 v167,0xffff0000,v137\n"
"v_add_f32 v188,v148,v149\n"
"v_add_f32 v189,v150,v151\n"
"v_add_f32 v188,v188,v152\n"
"v_add_f32 v189,v189,v153\n"
"v_add_f32 v188,v188,v154\n"
"v_add_f32 v189,v189,v155\n"
"v_add_f32 v188,v188,v156\n"
"v_add_f32 v189,v189,v157\n"
"v_add_f32 v188,v188,v158\n"
"v_add_f32 v189,v189,v159\n"
"v_add_f32 v188,v188,v160\n"
"v_add_f32 v189,v189,v161\n"
"v_add_f32 v188,v188,v162\n"
"v_add_f32 v189,v189,v163\n"
"v_add_f32 v188,v188,v164\n"
"v_add_f32 v189,v189,v165\n"
"v_add_f32 v188,v188,v166\n"
"v_add_f32 v189,v189,v167\n"
"v_add_f32 v188,v188,v189\n"
"s_nop 1\n"
"v_add_f32_dpp v188,v188,v188 quad_perm:[1,0,3,2] row_mask:0xf bank_mask:0xf\n"
WT"vmcnt(12)\n"
"v_lshlrev_b32 v168,16,v138\n"
"v_and_b32 v169,0xffff0000,v138\n"
"v_add_f32_dpp v188,v188,v188 quad_perm:[2,3,0,1] row_mask:0xf bank_mask:0xf\n"
"v_lshlrev_b32 v170,16,v139\n"
"v_and_b32 v171,0xffff0000,v139\n"
"v_add_f32_dpp v188,v188,v188 row_half_mirror row_mask:0xf bank_mask:0xf\n"
WT"vmcnt(11)\n"
"v_lshlrev_b32 v172,16,v140\n"
"v_and_b32 v173,0xffff0000,v140\n"
"v_add_f32_dpp v188,v188,v188 row_mirror row_mask:0xf bank_mask:0xf\n"
"v_lshlrev_b32 v174,16,v141\n"
"v_and_b32 v175,0xffff0000,v141\n"
"v_mov_b32 v189,v188\n"
WT"vmcnt(10)\n"
"v_lshlrev_b32 v176,16,v142\n"
"v_and_b32 v177,0xffff0000,v142\n"
"v_permlane16_swap_b32 v189,v188\n"
"v_lshlrev_b32 v178,16,v143\n"
"v_and_b32 v179,0xffff0000,v143\n"
"v_add_f32 v188,v188,v189\n"
WT"vmcnt(9)\n"
"v_lshlrev_b32 v180,16,v144\n"
SA"m0,s36,0\n"
"s_bfe_u32 s48,s46,0x30000\n"
"s_lshl_b32 s48,s48,7\n"
BL"v234,s[12:15],s48 offen offset:0 lds\n"
BL"v235,s[12:15],s48 offen offset:1024 lds\n"
BL"v236,s[12:15],s48 offen offset:2048 lds\n"
BL"v237,s[12:15],s48 offen offset:3072 lds\n"
"v_and_b32 v181,0xffff0000,v144\n"
MU"v191,0x3acccccd,v188\n"
"v_lshlrev_b32 v182,16,v145\n"
"v_and_b32 v183,0xffff0000,v145\n"
PK"v128,v191,v191\n"
WT"vmcnt(12)\n"
"v_lshlrev_b32 v184,16,v146\n"
"v_and_b32 v185,0xffff0000,v146\n"
"v_lshlrev_b32 v128,16,v128\n"
"v_lshlrev_b32 v186,16,v147\n"
"v_and_b32 v187,0xffff0000,v147\n"
"v_sub_f32 v148,v148,v128\n"
"v_sub_f32 v149,v149,v128\n"
"v_sub_f32 v150,v150,v128\n"
"v_sub_f32 v151,v151,v128\n"
"v_sub_f32 v152,v152,v128\n"
"v_sub_f32 v153,v153,v128\n"
"v_sub_f32 v154,v154,v128\n"
"v_sub_f32 v155,v155,v128\n"
"v_sub_f32 v156,v156,v128\n"
"v_sub_f32 v157,v157,v128\n"
"v_sub_f32 v158,v158,v128\n"
"v_sub_f32 v159,v159,v128\n"
"v_sub_f32 v160,v160,v128\n"
"v_sub_f32 v161,v161,v128\n"
"v_sub_f32 v162,v162,v128\n"
"v_sub_f32 v163,v163,v128\n"
"v_sub_f32 v164,v164,v128\n"
"v_sub_f32 v165,v165,v128\n"
"v_sub_f32 v166,v166,v128\n"
"v_sub_f32 v167,v167,v128\n"
MU"v188,v148,v148\n"
MU"v189,v149,v149\n"
"v_fmac_f32 v188,v150,v150\n"
"v_fmac_f32 v189,v151,v151\n"
"v_fmac_f32 v188,v152,v152\n"
"v_fmac_f32 v189,v153,v153\n"
"v_fmac_f32 v188,v154,v154\n"
"v_fmac_f32 v189,v155,v155\n"
"v_fmac_f32 v188,v156,v156\n"
"v_fmac_f32 v189,v157,v157\n"
"v_fmac_f32 v188,v158,v158\n"
"v_fmac_f32 v189,v159,v159\n"
"v_fmac_f32 v188,v160,v160\n"
"v_fmac_f32 v189,v161,v161\n"
"v_fmac_f32 v188,v162,v162\n"
"v_fmac_f32 v189,v163,v163\n"
"v_fmac_f32 v188,v164,v164\n"
"v_fmac_f32 v189,v165,v165\n"
"v_fmac_f32 v188,v166,v166\n"
"v_fmac_f32 v189,v167,v167\n"
"v_add_f32 v188,v188,v189\n"
PK"v149,v148,v149\n"
"v_lshlrev_b32 v148,16,v149\n"
"v_add_f32_dpp v188,v188,v188 quad_perm:[1,0,3,2] row_mask:0xf bank_mask:0xf\n"
"v_and_b32 v149,0xffff0000,v149\n"
PK"v151,v150,v151\n"
"v_add_f32_dpp v188,v188,v188 quad_perm:[2,3,0,1] row_mask:0xf bank_mask:0xf\n"
"v_lshlrev_b32 v150,16,v151\n"
"v_and_b32 v151,0xffff0000,v151\n"
SA"m0,s37,8192\n"
"s_bfe_u32 s48,s46,0x30004\n"
"s_lshl_b32 s48,s48,19\n"
BL"v238,s[16:19],s48 offen offset:0 lds\n"
BL"v239,s[16:19],s48 offen offset:1024 lds\n"
BL"v240,s[16:19],s48 offen offset:2048 lds\n"
BL"v241,s[16:19],s48 offen offset:3072 lds\n"
"v_add_f32_dpp v188,v188,v188 row_half_mirror row_mask:0xf bank_mask:0xf\n"
PK"v153,v152,v153\n"
"v_lshlrev_b32 v152,16,v153\n"
"v_add_f32_dpp v188,v188,v188 row_mirror row_mask:0xf bank_mask:0xf\n"
"v_and_b32 v153,0xffff0000,v153\n"
PK"v155,v154,v155\n"
"v_mov_b32 v189,v188\n"
"v_lshlrev_b32 v154,16,v155\n"
"v_and_b32 v155,0xffff0000,v155\n"
"v_permlane16_swap_b32 v189,v188\n"
PK"v157,v156,v157\n"
"v_lshlrev_b32 v156,16,v157\n"
"v_add_f32 v188,v188,v189\n"
"v_and_b32 v157,0xffff0000,v157\n"
PK"v159,v158,v159\n"
"v_sub_f32 v191,v191,v128\n"
"v_lshlrev_b32 v158,16,v159\n"
"v_and_b32 v159,0xffff0000,v159\n"
MU"v188,0x3acccccd,v188\n"
PK"v161,v160,v161\n"
"v_lshlrev_b32 v160,16,v161\n"
"v_fma_f32 v188,-v191,v191,v188\n"
"v_and_b32 v161,0xffff0000,v161\n"
PK"v163,v162,v163\n"
"v_max_f32 v188,0,v188\n"
"v_lshlrev_b32 v162,16,v163\n"
"v_and_b32 v163,0xffff0000,v163\n"
PK"v188,v188,v188\n"
PK"v165,v164,v165\n"
"v_lshlrev_b32 v164,16,v165\n"
"v_lshlrev_b32 v188,16,v188\n"
"v_and_b32 v165,0xffff0000,v165\n"
PK"v167,v166,v167\n"
"v_add_f32 v188,0x3727c5ac,v188\n"
"v_lshlrev_b32 v166,16,v167\n"
"v_and_b32 v167,0xffff0000,v167\n"
PK"v188,v188,v188\n"
"v_bfe_u32 v130,v224,5,4\n"
"v_mul_u32_u24 v130,144,v130\n"
"v_lshlrev_b32 v188,16,v188\n"
"v_bfe_u32 v131,v224,4,1\n"
"v_lshl_add_u32 v130,v131,3,v130\n"
"v_sqrt_f32 v189,v188\n"
"v_lshrrev_b32 v131,9,v224\n"
"v_lshl_add_u32 v130,v131,4,v130\n"
"v_rcp_f32 v190,v189\n"
"s_lshl_b32 s52,s30,5\n"
"v_add_u32 v130,s52,v130\n"
"v_fma_f32 v188,-v189,v189,v188\n"
MU"v190,0.5,v190\n"
"v_fma_f32 v189,v188,v190,v189\n"
PK"v189,v189,v189\n"
"v_lshlrev_b32 v189,16,v189\n"
"v_rcp_f32 v129,v189\n"
"s_nop 0\n"
MU"v148,v148,v129\n"
MU"v149,v149,v129\n"
PK"v138,v148,v149\n"
MU"v150,v150,v129\n"
MU"v151,v151,v129\n"
SA"m0,s37,12288\n"
"s_bfe_u32 s48,s46,0x30004\n"
"s_lshl_b32 s48,s48,19\n"
"s_bitset1_b32 s48,18\n"
BL"v238,s[16:19],s48 offen offset:0 lds\n"
BL"v239,s[16:19],s48 offen offset:1024 lds\n"
BL"v240,s[16:19],s48 offen offset:2048 lds\n"
BL"v241,s[16:19],s48 offen offset:3072 lds\n"
PK"v139,v150,v151\n"
"ds_write_b64 v130,v[138:139] offset:49152\n"
MU"v152,v152,v129\n"
MU"v153,v153,v129\n"
PK"v140,v152,v153\n"
MU"v154,v154,v129\n"
MU"v155,v155,v129\n"
PK"v141,v154,v155\n"
"ds_write_b64 v130,v[140:141] offset:51456\n"
MU"v156,v156,v129\n"
MU"v157,v157,v129\n"
PK"v142,v156,v157\n"
MU"v158,v158,v129\n"
MU"v159,v159,v129\n"
PK"v143,v158,v159\n"
"ds_write_b64 v130,v[142:143] offset:53760\n"
MU"v160,v160,v129\n"
MU"v161,v161,v129\n"
PK"v144,v160,v161\n"
MU"v162,v162,v129\n"
MU"v163,v163,v129\n"
PK"v145,v162,v163\n"
"ds_write_b64 v130,v[144:145] offset:56064\n"
MU"v164,v164,v129\n"
MU"v165,v165,v129\n"
PK"v146,v164,v165\n"
MU"v166,v166,v129\n"
MU"v167,v167,v129\n"
PK"v147,v166,v167\n"
"ds_write_b64 v130,v[146:147] offset:58368\n"
"v_lshlrev_b32 v148,16,v138\n"
"v_and_b32 v149,0xffff0000,v138\n"
MU"v188,v148,v168\n"
MU"v189,v149,v169\n"
"v_lshlrev_b32 v150,16,v139\n"
"v_and_b32 v151,0xffff0000,v139\n"
"v_fmac_f32 v188,v150,v170\n"
"v_fmac_f32 v189,v151,v171\n"
"v_lshlrev_b32 v152,16,v140\n"
"v_and_b32 v153,0xffff0000,v140\n"
"v_fmac_f32 v188,v152,v172\n"
"v_fmac_f32 v189,v153,v173\n"
"v_lshlrev_b32 v154,16,v141\n"
"v_and_b32 v155,0xffff0000,v141\n"
"v_fmac_f32 v188,v154,v174\n"
"v_fmac_f32 v189,v155,v175\n"
"v_lshlrev_b32 v156,16,v142\n"
"v_and_b32 v157,0xffff0000,v142\n"
"v_fmac_f32 v188,v156,v176\n"
"v_fmac_f32 v189,v157,v177\n"
"v_lshlrev_b32 v158,16,v143\n"
"v_and_b32 v159,0xffff0000,v143\n"
"v_fmac_f32 v188,v158,v178\n"
"v_fmac_f32 v189,v159,v179\n"
"v_lshlrev_b32 v160,16,v144\n"
"v_and_b32 v161,0xffff0000,v144\n"
"v_fmac_f32 v188,v160,v180\n"
"v_fmac_f32 v189,v161,v181\n"
"v_lshlrev_b32 v162,16,v145\n"
"v_and_b32 v163,0xffff0000,v145\n"
WT"lgkmcnt(0)\n"
SA"s28,s28,0x7f4a7c15\n"
"s_addc_u32 s29,s29,0x9e3779b9\n"
"v_mov_b32 v232,s28\n"
"v_mov_b32 v233,s29\n"
WT"vmcnt(16)\n"
RT"a[0:1],v246 offset:0\n"
RT"a[2:3],v246 offset:512\n"
RT"a[4:5],v247 offset:0\n"
RT"a[6:7],v247 offset:512\n"
RT"a[8:9],v248 offset:0\n"
RT"a[10:11],v248 offset:512\n"
RT"a[12:13],v249 offset:0\n"
RT"a[14:15],v249 offset:512\n"
WT"vmcnt(12)\n"
RT"a[16:17],v246 offset:4096\n"
RT"a[18:19],v246 offset:4608\n"
RT"a[20:21],v247 offset:4096\n"
RT"a[22:23],v247 offset:4608\n"
RT"a[24:25],v248 offset:4096\n"
RT"a[26:27],v248 offset:4608\n"
RT"a[28:29],v249 offset:4096\n"
RT"a[30:31],v249 offset:4608\n"
WT"vmcnt(8) lgkmcnt(15)\n"
"s_barrier\n"
RD"v[192:195],v242 offset:0\n"
RD"v[196:199],v243 offset:0\n"
RD"v[200:203],v242 offset:2048\n"
RD"v[204:207],v243 offset:2048\n"
RD"v[208:211],v242 offset:4096\n"
"v_lshrrev_b32 v131,7,v224\n"
WT"lgkmcnt(4)\n"
MF"v[0:3],v[192:195],a[0:3],0\n"
WT"lgkmcnt(3)\n"
RD"v[212:215],v243 offset:4096\n"
"v_lshl_add_u32 v131,v131,4,v224\n"
MF"v[0:3],v[196:199],a[16:19],v[0:3]\n"
SA"m0,s36,16384\n"
"s_bfe_u32 s48,s46,0x30004\n"
"s_lshl_b32 s48,s48,7\n"
MF"v[8:11],v[196:199],a[20:23],0\n"
"s_mul_i32 s52,s30,1152\n"
BL"v234,s[12:15],s48 offen offset:0 lds\n"
MF"v[8:11],v[192:195],a[4:7],v[8:11]\n"
"v_add_u32 v128,s52,v131\n"
"s_and_b32 s52,s30,1\n"
MF"v[16:19],v[192:195],a[8:11],0\n"
"s_mul_i32 s51,s52,1152\n"
RD"v[216:219],v242 offset:6144\n"
"v_add_u32 v131,s51,v131\n"
MF"v[16:19],v[196:199],a[24:27],v[16:19]\n"
"s_lshl_b32 s52,s52,10\n"
"v_fmac_f32 v188,v162,v182\n"
"v_fmac_f32 v189,v163,v183\n"
MF"v[24:27],v[196:199],a[28:31],0\n"
BL"v235,s[12:15],s48 offen offset:1024 lds\n"
MF"v[24:27],v[192:195],a[12:15],v[24:27]\n"
WT"lgkmcnt(1)\n"
RD"v[220:223],v243 offset:6144\n"
"v_lshlrev_b32 v164,16,v146\n"
MF"v[4:7],v[200:203],a[0:3],0\n"
RD"v[132:135],v128 offset:49152\n"
"v_and_b32 v129,0x70,v224\n"
MF"v[4:7],v[204:207],a[16:19],v[4:7]\n"
"s_lshr_b32 s50,s31,1\n"
WT"vmcnt(6)\n"
RT"a[32:33],v246 offset:8192\n"
MF"v[12:15],v[204:207],a[20:23],0\n"
"s_mul_i32 s50,s50,20480\n"
RD"a[240:243],v128 offset:53760\n"
"v_and_b32 v165,0xffff0000,v146\n"
MF"v[12:15],v[200:203],a[4:7],v[12:15]\n"
"s_and_b32 s51,s31,1\n"
RD"a[244:247],v131 offset:58368\n"
"v_and_b32 v130,0x380,v224\n"
MF"v[20:23],v[200:203],a[8:11],0\n"
"s_lshl_b32 s51,s51,7\n"
RD"v[192:195],v242 offset:8192\n"
"v_fmac_f32 v188,v164,v184\n"
MF"v[20:23],v[204:207],a[24:27],v[20:23]\n"
SA"s50,s50,s51\n"
RT"a[34:35],v246 offset:8704\n"
"v_lshl_add_u32 v129,v130,1,v129\n"
MF"v[28:31],v[204:207],a[28:31],0\n"
SA"s50,s50,524288\n"
BL"v236,s[12:15],s48 offen offset:2048 lds\n"
MF"v[28:31],v[200:203],a[12:15],v[28:31]\n"
"s_lshl_b32 s52,s52,1\n"
RT"a[36:37],v247 offset:8192\n"
"v_fmac_f32 v189,v165,v185\n"
MF"v[32:35],v[208:211],a[0:3],0\n"
SA"s52,s52,s50\n"
RD"v[196:199],v243 offset:8192\n"
"v_lshlrev_b32 v166,16,v147\n"
MF"v[32:35],v[212:215],a[16:19],v[32:35]\n"
SA"s52,s52,16384\n"
BL"v237,s[12:15],s48 offen offset:3072 lds\n"
MF"v[40:43],v[212:215],a[20:23],0\n"
SA"m0,s37,16384\n"
RT"a[38:39],v247 offset:8704\n"
"v_and_b32 v167,0xffff0000,v147\n"
MF"v[40:43],v[208:211],a[4:7],v[40:43]\n"
"s_bfe_u32 s48,s46,0x30008\n"
RD"v[200:203],v242 offset:10240\n"
"v_fmac_f32 v188,v166,v186\n"
MF"v[48:51],v[208:211],a[8:11],0\n"
"s_lshl_b32 s48,s48,19\n"
RT"a[40:41],v248 offset:8192\n"
"v_fmac_f32 v189,v167,v187\n"
MF"v[48:51],v[212:215],a[24:27],v[48:51]\n"
"s_lshl_b32 s51,s30,11\n"
BL"v238,s[16:19],s48 offen offset:0 lds\n"
MF"v[56:59],v[212:215],a[28:31],0\n"
SA"s50,s50,s51\n"
RD"v[204:207],v243 offset:10240\n"
"v_add_f32 v188,v188,v189\n"
MF"v[56:59],v[208:211],a[12:15],v[56:59]\n"
WT"lgkmcnt(4)\n"
BS"v[132:135],v129,s[8:11],s50 offen sc1\n"
MF"v[36:39],v[216:219],a[0:3],0\n"
SA"s51,s50,8192\n"
RT"a[42:43],v248 offset:8704\n"
"v_add_f32_dpp v188,v188,v188 quad_perm:[1,0,3,2] row_mask:0xf bank_mask:0xf\n"
MF"v[36:39],v[220:223],a[16:19],v[36:39]\n"
"s_lshl_b32 s90,s31,2\n"
BL"v239,s[16:19],s48 offen offset:1024 lds\n"
MF"v[44:47],v[220:223],a[20:23],0\n"
SA"s90,s90,s30\n"
BS"a[240:243],v129,s[8:11],s51 offen sc1\n"
MF"v[44:47],v[216:219],a[4:7],v[44:47]\n"
"s_lshl_b32 s90,s90,1\n"
RD"v[208:211],v242 offset:12288\n"
"v_add_f32_dpp v188,v188,v188 quad_perm:[2,3,0,1] row_mask:0xf bank_mask:0xf\n"
MF"v[52:55],v[216:219],a[8:11],0\n"
"s_sub_u32 s91,2047,s90\n"
RT"a[44:45],v249 offset:8192\n"
"v_add_f32_dpp v188,v188,v188 row_half_mirror row_mask:0xf bank_mask:0xf\n"
MF"v[52:55],v[220:223],a[24:27],v[52:55]\n"
BS"a[244:247],v129,s[8:11],s52 offen sc1\n"
MF"v[60:63],v[220:223],a[28:31],0\n"
RD"v[212:215],v243 offset:12288\n"
"v_add_f32_dpp v188,v188,v188 row_mirror row_mask:0xf bank_mask:0xf\n"
MF"v[60:63],v[216:219],a[12:15],v[60:63]\n"
RT"a[46:47],v249 offset:8704\n"
"v_mov_b32 v189,v188\n"
MF"v[64:67],v[192:195],a[0:3],0\n"
BL"v240,s[16:19],s48 offen offset:2048 lds\n"
MF"v[64:67],v[196:199],a[16:19],v[64:67]\n"
RD"v[216:219],v242 offset:14336\n"
"v_permlane16_swap_b32 v189,v188\n"
MF"v[72:75],v[196:199],a[20:23],0\n"
"s_lshl_b32 s90,s90,2\n"
WT"vmcnt(10)\n"
RT"a[48:49],v246 offset:12288\n"
MF"v[72:75],v[192:195],a[4:7],v[72:75]\n"
SA"s90,s90,8192\n"
BL"v241,s[16:19],s48 offen offset:3072 lds\n"
MF"v[80:83],v[192:195],a[8:11],0\n"
SA"m0,s37,20480\n"
RT"a[50:51],v246 offset:12800\n"
"v_add_f32 v188,v188,v189\n"
MF"v[80:83],v[196:199],a[24:27],v[80:83]\n"
"s_bfe_u32 s48,s46,0x30008\n"
RD"v[220:223],v243 offset:14336\n"
PK"v188,v188,v188\n"
MF"v[88:91],v[196:199],a[28:31],0\n"
"s_lshl_b32 s48,s48,19\n"
RT"a[52:53],v247 offset:12288\n"
"v_lshlrev_b32 v188,16,v188\n"
MF"v[88:91],v[192:195],a[12:15],v[88:91]\n"
WT"lgkmcnt(5)\n"
"v_ashrrev_i32 v189,31,v188\n"
"v_or_b32 v189,0x80000000,v189\n"
MF"v[68:71],v[200:203],a[0:3],0\n"
"s_bitset1_b32 s48,18\n"
"v_xor_b32 v188,v188,v189\n"
"v_and_b32 v188,0xffff0000,v188\n"
MF"v[68:71],v[204:207],a[16:19],v[68:71]\n"
BL"v238,s[16:19],s48 offen offset:0 lds\n"
MF"v[76:79],v[204:207],a[20:23],0\n"
RT"a[54:55],v247 offset:12800\n"
"v_lshrrev_b32 v189,9,v224\n"
MF"v[76:79],v[200:203],a[4:7],v[76:79]\n"
BL"v239,s[16:19],s48 offen offset:1024 lds\n"
MF"v[84:87],v[200:203],a[8:11],0\n"
WT"vmcnt(9) lgkmcnt(2)\n"
RT"a[56:57],v248 offset:12288\n"
"v_sub_u32 v190,s91,v189\n"
MF"v[84:87],v[204:207],a[24:27],v[84:87]\n"
"s_barrier\n"
MF"v[92:95],v[204:207],a[28:31],0\n"
RD"v[192:195],v242 offset:16384\n"
"v_or_b32 v188,v188,v190\n"
MF"v[92:95],v[200:203],a[12:15],v[92:95]\n"
RT"a[58:59],v248 offset:12800\n"
"v_and_b32 v190,0x1f0,v224\n"
MF"v[96:99],v[208:211],a[0:3],0\n"
BL"v240,s[16:19],s48 offen offset:2048 lds\n"
MF"v[96:99],v[212:215],a[16:19],v[96:99]\n"
"v_lshlrev_b32 v190,23,v190\n"
"v_lshl_add_u32 v190,v189,2,v190\n"
MF"v[104:107],v[212:215],a[20:23],0\n"
"buffer_store_dword v188,v190,s[8:11],s90 offen sc1\n"
MF"v[104:107],v[208:211],a[4:7],v[104:107]\n"
RD"v[196:199],v243 offset:16384\n"
MF"v[112:115],v[208:211],a[8:11],0\n"
RT"a[60:61],v249 offset:12288\n"
MF"v[112:115],v[212:215],a[24:27],v[112:115]\n"
BL"v241,s[16:19],s48 offen offset:3072 lds\n"
MF"v[120:123],v[212:215],a[28:31],0\n"
SA"m0,s36,32768\n"
RT"a[62:63],v249 offset:12800\n"
"s_bfe_u32 s48,s46,0x30008\n"
MF"v[120:123],v[208:211],a[12:15],v[120:123]\n"
"s_lshl_b32 s48,s48,7\n"
RD"v[200:203],v242 offset:18432\n"
MF"v[100:103],v[216:219],a[0:3],0\n"
BL"v234,s[12:15],s48 offen offset:0 lds\n"
MF"v[100:103],v[220:223],a[16:19],v[100:103]\n"
RD"v[204:207],v243 offset:18432\n"
MF"v[108:111],v[220:223],a[20:23],0\n"
MF"v[108:111],v[216:219],a[4:7],v[108:111]\n"
MF"v[116:119],v[216:219],a[8:11],0\n"
BL"v235,s[12:15],s48 offen offset:1024 lds\n"
MF"v[116:119],v[220:223],a[24:27],v[116:119]\n"
RD"v[208:211],v242 offset:20480\n"
MF"v[124:127],v[220:223],a[28:31],0\n"
MF"v[124:127],v[216:219],a[12:15],v[124:127]\n"
WT"lgkmcnt(2)\n"
MF"v[0:3],v[192:195],a[32:35],v[0:3]\n"
RD"v[212:215],v243 offset:20480\n"
MF"v[0:3],v[196:199],a[48:51],v[0:3]\n"
BL"v236,s[12:15],s48 offen offset:2048 lds\n"
MF"v[8:11],v[196:199],a[52:55],v[8:11]\n"
MF"v[8:11],v[192:195],a[36:39],v[8:11]\n"
MF"v[16:19],v[192:195],a[40:43],v[16:19]\n"
RD"v[216:219],v242 offset:22528\n"
MF"v[16:19],v[196:199],a[56:59],v[16:19]\n"
BL"v237,s[12:15],s48 offen offset:3072 lds\n"
MF"v[24:27],v[196:199],a[60:63],v[24:27]\n"
MF"v[24:27],v[192:195],a[44:47],v[24:27]\n"
RD"v[220:223],v243 offset:22528\n"
MF"v[4:7],v[200:203],a[32:35],v[4:7]\n"
WT"lgkmcnt(2)\n"
WT"vmcnt(9)\n"
RT"a[64:65],v246 offset:16384\n"
MF"v[4:7],v[204:207],a[48:51],v[4:7]\n"
SA"m0,s37,0\n"
"s_bfe_u32 s48,s46,0x3000c\n"
"s_lshl_b32 s48,s48,19\n"
MF"v[12:15],v[204:207],a[52:55],v[12:15]\n"
BL"v238,s[16:19],s48 offen offset:0 lds\n"
MF"v[12:15],v[200:203],a[36:39],v[12:15]\n"
RD"v[192:195],v242 offset:24576\n"
MF"v[20:23],v[200:203],a[40:43],v[20:23]\n"
RT"a[66:67],v246 offset:16896\n"
MF"v[20:23],v[204:207],a[56:59],v[20:23]\n"
MF"v[28:31],v[204:207],a[60:63],v[28:31]\n"
RT"a[68:69],v247 offset:16384\n"
MF"v[28:31],v[200:203],a[44:47],v[28:31]\n"
BL"v239,s[16:19],s48 offen offset:1024 lds\n"
MF"v[32:35],v[208:211],a[32:35],v[32:35]\n"
RD"v[196:199],v243 offset:24576\n"
MF"v[32:35],v[212:215],a[48:51],v[32:35]\n"
RT"a[70:71],v247 offset:16896\n"
MF"v[40:43],v[212:215],a[52:55],v[40:43]\n"
RD"v[200:203],v242 offset:26624\n"
MF"v[40:43],v[208:211],a[36:39],v[40:43]\n"
BL"v240,s[16:19],s48 offen offset:2048 lds\n"
MF"v[48:51],v[208:211],a[40:43],v[48:51]\n"
RT"a[72:73],v248 offset:16384\n"
MF"v[48:51],v[212:215],a[56:59],v[48:51]\n"
MF"v[56:59],v[212:215],a[60:63],v[56:59]\n"
RD"v[204:207],v243 offset:26624\n"
MF"v[56:59],v[208:211],a[44:47],v[56:59]\n"
WT"lgkmcnt(4)\n"
RT"a[74:75],v248 offset:16896\n"
MF"v[36:39],v[216:219],a[32:35],v[36:39]\n"
BL"v241,s[16:19],s48 offen offset:3072 lds\n"
MF"v[36:39],v[220:223],a[48:51],v[36:39]\n"
RD"v[208:211],v242 offset:28672\n"
MF"v[44:47],v[220:223],a[52:55],v[44:47]\n"
SA"m0,s37,4096\n"
RT"a[76:77],v249 offset:16384\n"
"s_bfe_u32 s48,s46,0x3000c\n"
MF"v[44:47],v[216:219],a[36:39],v[44:47]\n"
"s_lshl_b32 s48,s48,19\n"
"s_bitset1_b32 s48,18\n"
MF"v[52:55],v[216:219],a[40:43],v[52:55]\n"
BL"v238,s[16:19],s48 offen offset:0 lds\n"
MF"v[52:55],v[220:223],a[56:59],v[52:55]\n"
RD"v[212:215],v243 offset:28672\n"
MF"v[60:63],v[220:223],a[60:63],v[60:63]\n"
RT"a[78:79],v249 offset:16896\n"
MF"v[60:63],v[216:219],a[44:47],v[60:63]\n"
MF"v[64:67],v[192:195],a[32:35],v[64:67]\n"
RD"v[216:219],v242 offset:30720\n"
MF"v[64:67],v[196:199],a[48:51],v[64:67]\n"
WT"vmcnt(9)\n"
RT"a[80:81],v246 offset:20480\n"
MF"v[72:75],v[196:199],a[52:55],v[72:75]\n"
BL"v239,s[16:19],s48 offen offset:1024 lds\n"
MF"v[72:75],v[192:195],a[36:39],v[72:75]\n"
RT"a[82:83],v246 offset:20992\n"
MF"v[80:83],v[192:195],a[40:43],v[80:83]\n"
RD"v[220:223],v243 offset:30720\n"
MF"v[80:83],v[196:199],a[56:59],v[80:83]\n"
BL"v240,s[16:19],s48 offen offset:2048 lds\n"
MF"v[88:91],v[196:199],a[60:63],v[88:91]\n"
RT"a[84:85],v247 offset:20480\n"
MF"v[88:91],v[192:195],a[44:47],v[88:91]\n"
WT"lgkmcnt(4)\n"
MF"v[68:71],v[200:203],a[32:35],v[68:71]\n"
"s_lshl_b32 s51,s32,8\n"
"s_andn2_b32 s50,s31,7\n"
SA"s50,s50,s51\n"
MF"v[68:71],v[204:207],a[48:51],v[68:71]\n"
SA"s50,s50,256\n"
RT"a[86:87],v247 offset:20992\n"
SA"s50,s50,s45\n"
MF"v[76:79],v[204:207],a[52:55],v[76:79]\n"
BL"v241,s[16:19],s48 offen offset:3072 lds\n"
MF"v[76:79],v[200:203],a[36:39],v[76:79]\n"
MF"v[84:87],v[200:203],a[40:43],v[84:87]\n"
WT"vmcnt(8) lgkmcnt(2)\n"
RT"a[88:89],v248 offset:20480\n"
SA"m0,s36,0\n"
MF"v[84:87],v[204:207],a[56:59],v[84:87]\n"
"s_bfe_u32 s48,s46,0x3000c\n"
"s_barrier\n"
MF"v[92:95],v[204:207],a[60:63],v[92:95]\n"
"s_lshl_b32 s48,s48,7\n"
RD"v[192:195],v242 offset:32768\n"
MF"v[92:95],v[200:203],a[44:47],v[92:95]\n"
BL"v234,s[12:15],s48 offen offset:0 lds\n"
MF"v[96:99],v[208:211],a[32:35],v[96:99]\n"
"buffer_store_dwordx2 v[232:233],v230,s[8:11],s50 offen sc1\n"
MF"v[96:99],v[212:215],a[48:51],v[96:99]\n"
RT"a[90:91],v248 offset:20992\n"
MF"v[104:107],v[212:215],a[52:55],v[104:107]\n"
RD"v[196:199],v243 offset:32768\n"
MF"v[104:107],v[208:211],a[36:39],v[104:107]\n"
RT"a[92:93],v249 offset:20480\n"
MF"v[112:115],v[208:211],a[40:43],v[112:115]\n"
BL"v235,s[12:15],s48 offen offset:1024 lds\n"
MF"v[112:115],v[212:215],a[56:59],v[112:115]\n"
RT"a[94:95],v249 offset:20992\n"
MF"v[120:123],v[212:215],a[60:63],v[120:123]\n"
RD"v[200:203],v242 offset:34816\n"
MF"v[120:123],v[208:211],a[44:47],v[120:123]\n"
BL"v236,s[12:15],s48 offen offset:2048 lds\n"
MF"v[100:103],v[216:219],a[32:35],v[100:103]\n"
MF"v[100:103],v[220:223],a[48:51],v[100:103]\n"
RD"v[204:207],v243 offset:34816\n"
MF"v[108:111],v[220:223],a[52:55],v[108:111]\n"
MF"v[108:111],v[216:219],a[36:39],v[108:111]\n"
BL"v237,s[12:15],s48 offen offset:3072 lds\n"
MF"v[116:119],v[216:219],a[40:43],v[116:119]\n"
MF"v[116:119],v[220:223],a[56:59],v[116:119]\n"
RD"v[208:211],v242 offset:36864\n"
MF"v[124:127],v[220:223],a[60:63],v[124:127]\n"
SA"m0,s37,8192\n"
"s_bfe_u32 s48,s46,0x30010\n"
"s_lshl_b32 s48,s48,19\n"
MF"v[124:127],v[216:219],a[44:47],v[124:127]\n"
WT"lgkmcnt(2)\n"
MF"v[0:3],v[192:195],a[64:67],v[0:3]\n"
RD"v[212:215],v243 offset:36864\n"
MF"v[0:3],v[196:199],a[80:83],v[0:3]\n"
BL"v238,s[16:19],s48 offen offset:0 lds\n"
MF"v[8:11],v[196:199],a[84:87],v[8:11]\n"
MF"v[8:11],v[192:195],a[68:71],v[8:11]\n"
MF"v[16:19],v[192:195],a[72:75],v[16:19]\n"
RD"v[216:219],v242 offset:38912\n"
MF"v[16:19],v[196:199],a[88:91],v[16:19]\n"
BL"v239,s[16:19],s48 offen offset:1024 lds\n"
MF"v[24:27],v[196:199],a[92:95],v[24:27]\n"
MF"v[24:27],v[192:195],a[76:79],v[24:27]\n"
RD"v[220:223],v243 offset:38912\n"
MF"v[4:7],v[200:203],a[64:67],v[4:7]\n"
WT"lgkmcnt(2)\n"
WT"vmcnt(11)\n"
RT"a[96:97],v246 offset:0\n"
MF"v[4:7],v[204:207],a[80:83],v[4:7]\n"
BL"v240,s[16:19],s48 offen offset:2048 lds\n"
MF"v[12:15],v[204:207],a[84:87],v[12:15]\n"
MF"v[12:15],v[200:203],a[68:71],v[12:15]\n"
RD"v[192:195],v242 offset:40960\n"
MF"v[20:23],v[200:203],a[72:75],v[20:23]\n"
RT"a[98:99],v246 offset:512\n"
MF"v[20:23],v[204:207],a[88:91],v[20:23]\n"
BL"v241,s[16:19],s48 offen offset:3072 lds\n"
MF"v[28:31],v[204:207],a[92:95],v[28:31]\n"
RT"a[100:101],v247 offset:0\n"
MF"v[28:31],v[200:203],a[76:79],v[28:31]\n"
RD"v[196:199],v243 offset:40960\n"
MF"v[32:35],v[208:211],a[64:67],v[32:35]\n"
SA"m0,s37,12288\n"
"s_bfe_u32 s48,s46,0x30010\n"
"s_lshl_b32 s48,s48,19\n"
MF"v[32:35],v[212:215],a[80:83],v[32:35]\n"
"s_bitset1_b32 s48,18\n"
RT"a[102:103],v247 offset:512\n"
MF"v[40:43],v[212:215],a[84:87],v[40:43]\n"
RD"v[200:203],v242 offset:43008\n"
MF"v[40:43],v[208:211],a[68:71],v[40:43]\n"
BL"v238,s[16:19],s48 offen offset:0 lds\n"
MF"v[48:51],v[208:211],a[72:75],v[48:51]\n"
RT"a[104:105],v248 offset:0\n"
MF"v[48:51],v[212:215],a[88:91],v[48:51]\n"
MF"v[56:59],v[212:215],a[92:95],v[56:59]\n"
RD"v[204:207],v243 offset:43008\n"
MF"v[56:59],v[208:211],a[76:79],v[56:59]\n"
WT"lgkmcnt(4)\n"
BL"v239,s[16:19],s48 offen offset:1024 lds\n"
MF"v[36:39],v[216:219],a[64:67],v[36:39]\n"
RT"a[106:107],v248 offset:512\n"
MF"v[36:39],v[220:223],a[80:83],v[36:39]\n"
RD"v[208:211],v242 offset:45056\n"
MF"v[44:47],v[220:223],a[84:87],v[44:47]\n"
RT"a[108:109],v249 offset:0\n"
MF"v[44:47],v[216:219],a[68:71],v[44:47]\n"
BL"v240,s[16:19],s48 offen offset:2048 lds\n"
MF"v[52:55],v[216:219],a[72:75],v[52:55]\n"
MF"v[52:55],v[220:223],a[88:91],v[52:55]\n"
RD"v[212:215],v243 offset:45056\n"
MF"v[60:63],v[220:223],a[92:95],v[60:63]\n"
RT"a[110:111],v249 offset:512\n"
MF"v[60:63],v[216:219],a[76:79],v[60:63]\n"
BL"v241,s[16:19],s48 offen offset:3072 lds\n"
MF"v[64:67],v[192:195],a[64:67],v[64:67]\n"
RD"v[216:219],v242 offset:47104\n"
MF"v[64:67],v[196:199],a[80:83],v[64:67]\n"
WT"vmcnt(13)\n"
RT"a[112:113],v246 offset:4096\n"
MF"v[72:75],v[196:199],a[84:87],v[72:75]\n"
SA"m0,s36,16384\n"
"s_bfe_u32 s48,s46,0x30010\n"
"s_lshl_b32 s48,s48,7\n"
MF"v[72:75],v[192:195],a[68:71],v[72:75]\n"
RT"a[114:115],v246 offset:4608\n"
MF"v[80:83],v[192:195],a[72:75],v[80:83]\n"
RD"v[220:223],v243 offset:47104\n"
MF"v[80:83],v[196:199],a[88:91],v[80:83]\n"
BL"v234,s[12:15],s48 offen offset:0 lds\n"
MF"v[88:91],v[196:199],a[92:95],v[88:91]\n"
RT"a[116:117],v247 offset:4096\n"
MF"v[88:91],v[192:195],a[76:79],v[88:91]\n"
WT"lgkmcnt(4)\n"
MF"v[68:71],v[200:203],a[64:67],v[68:71]\n"
BL"v235,s[12:15],s48 offen offset:1024 lds\n"
MF"v[68:71],v[204:207],a[80:83],v[68:71]\n"
RT"a[118:119],v247 offset:4608\n"
MF"v[76:79],v[204:207],a[84:87],v[76:79]\n"
MF"v[76:79],v[200:203],a[68:71],v[76:79]\n"
MF"v[84:87],v[200:203],a[72:75],v[84:87]\n"
WT"vmcnt(10) lgkmcnt(2)\n"
RT"a[120:121],v248 offset:4096\n"
MF"v[84:87],v[204:207],a[88:91],v[84:87]\n"
"s_barrier\n"
MF"v[92:95],v[204:207],a[92:95],v[92:95]\n"
BL"v236,s[12:15],s48 offen offset:2048 lds\n"
MF"v[92:95],v[200:203],a[76:79],v[92:95]\n"
RD"v[192:195],v242 offset:0\n"
MF"v[96:99],v[208:211],a[64:67],v[96:99]\n"
RT"a[122:123],v248 offset:4608\n"
MF"v[96:99],v[212:215],a[80:83],v[96:99]\n"
BL"v237,s[12:15],s48 offen offset:3072 lds\n"
MF"v[104:107],v[212:215],a[84:87],v[104:107]\n"
RD"v[196:199],v243 offset:0\n"
MF"v[104:107],v[208:211],a[68:71],v[104:107]\n"
RT"a[124:125],v249 offset:4096\n"
MF"v[112:115],v[208:211],a[72:75],v[112:115]\n"
SA"m0,s37,16384\n"
"s_bfe_u32 s48,s46,0x30014\n"
"s_lshl_b32 s48,s48,19\n"
MF"v[112:115],v[212:215],a[88:91],v[112:115]\n"
RT"a[126:127],v249 offset:4608\n"
MF"v[120:123],v[212:215],a[92:95],v[120:123]\n"
RD"v[200:203],v242 offset:2048\n"
MF"v[120:123],v[208:211],a[76:79],v[120:123]\n"
BL"v238,s[16:19],s48 offen offset:0 lds\n"
MF"v[100:103],v[216:219],a[64:67],v[100:103]\n"
MF"v[100:103],v[220:223],a[80:83],v[100:103]\n"
RD"v[204:207],v243 offset:2048\n"
MF"v[108:111],v[220:223],a[84:87],v[108:111]\n"
BL"v239,s[16:19],s48 offen offset:1024 lds\n"
MF"v[108:111],v[216:219],a[68:71],v[108:111]\n"
MF"v[116:119],v[216:219],a[72:75],v[116:119]\n"
MF"v[116:119],v[220:223],a[88:91],v[116:119]\n"
RD"v[208:211],v242 offset:4096\n"
MF"v[124:127],v[220:223],a[92:95],v[124:127]\n"
MF"v[124:127],v[216:219],a[76:79],v[124:127]\n"
WT"lgkmcnt(2)\n"
BL"v240,s[16:19],s48 offen offset:2048 lds\n"
MF"v[0:3],v[192:195],a[96:99],v[0:3]\n"
RD"v[212:215],v243 offset:4096\n"
MF"v[0:3],v[196:199],a[112:115],v[0:3]\n"
MF"v[8:11],v[196:199],a[116:119],v[8:11]\n"
MF"v[8:11],v[192:195],a[100:103],v[8:11]\n"
BL"v241,s[16:19],s48 offen offset:3072 lds\n"
MF"v[16:19],v[192:195],a[104:107],v[16:19]\n"
RD"v[216:219],v242 offset:6144\n"
MF"v[16:19],v[196:199],a[120:123],v[16:19]\n"
MF"v[24:27],v[196:199],a[124:127],v[24:27]\n"
SA"m0,s37,20480\n"
"s_bfe_u32 s48,s46,0x30014\n"
"s_lshl_b32 s48,s48,19\n"
MF"v[24:27],v[192:195],a[108:111],v[24:27]\n"
"s_bitset1_b32 s48,18\n"
RD"v[220:223],v243 offset:6144\n"
MF"v[4:7],v[200:203],a[96:99],v[4:7]\n"
WT"lgkmcnt(2)\n"
WT"vmcnt(12)\n"
RT"a[128:129],v246 offset:8192\n"
MF"v[4:7],v[204:207],a[112:115],v[4:7]\n"
BL"v238,s[16:19],s48 offen offset:0 lds\n"
MF"v[12:15],v[204:207],a[116:119],v[12:15]\n"
MF"v[12:15],v[200:203],a[100:103],v[12:15]\n"
RD"v[192:195],v242 offset:8192\n"
MF"v[20:23],v[200:203],a[104:107],v[20:23]\n"
RT"a[130:131],v246 offset:8704\n"
MF"v[20:23],v[204:207],a[120:123],v[20:23]\n"
BL"v239,s[16:19],s48 offen offset:1024 lds\n"
MF"v[28:31],v[204:207],a[124:127],v[28:31]\n"
RT"a[132:133],v247 offset:8192\n"
MF"v[28:31],v[200:203],a[108:111],v[28:31]\n"
RD"v[196:199],v243 offset:8192\n"
MF"v[32:35],v[208:211],a[96:99],v[32:35]\n"
MF"v[32:35],v[212:215],a[112:115],v[32:35]\n"
RT"a[134:135],v247 offset:8704\n"
MF"v[40:43],v[212:215],a[116:119],v[40:43]\n"
BL"v240,s[16:19],s48 offen offset:2048 lds\n"
MF"v[40:43],v[208:211],a[100:103],v[40:43]\n"
RD"v[200:203],v242 offset:10240\n"
MF"v[48:51],v[208:211],a[104:107],v[48:51]\n"
RT"a[136:137],v248 offset:8192\n"
MF"v[48:51],v[212:215],a[120:123],v[48:51]\n"
BL"v241,s[16:19],s48 offen offset:3072 lds\n"
MF"v[56:59],v[212:215],a[124:127],v[56:59]\n"
RD"v[204:207],v243 offset:10240\n"
MF"v[56:59],v[208:211],a[108:111],v[56:59]\n"
WT"lgkmcnt(4)\n"
RT"a[138:139],v248 offset:8704\n"
MF"v[36:39],v[216:219],a[96:99],v[36:39]\n"
SA"m0,s36,32768\n"
"s_bfe_u32 s48,s46,0x30014\n"
"s_lshl_b32 s48,s48,7\n"
MF"v[36:39],v[220:223],a[112:115],v[36:39]\n"
RD"v[208:211],v242 offset:12288\n"
MF"v[44:47],v[220:223],a[116:119],v[44:47]\n"
RT"a[140:141],v249 offset:8192\n"
MF"v[44:47],v[216:219],a[100:103],v[44:47]\n"
BL"v234,s[12:15],s48 offen offset:0 lds\n"
MF"v[52:55],v[216:219],a[104:107],v[52:55]\n"
MF"v[52:55],v[220:223],a[120:123],v[52:55]\n"
RD"v[212:215],v243 offset:12288\n"
MF"v[60:63],v[220:223],a[124:127],v[60:63]\n"
RT"a[142:143],v249 offset:8704\n"
MF"v[60:63],v[216:219],a[108:111],v[60:63]\n"
BL"v235,s[12:15],s48 offen offset:1024 lds\n"
MF"v[64:67],v[192:195],a[96:99],v[64:67]\n"
RD"v[216:219],v242 offset:14336\n"
MF"v[64:67],v[196:199],a[112:115],v[64:67]\n"
WT"vmcnt(14)\n"
RT"a[144:145],v246 offset:12288\n"
MF"v[72:75],v[196:199],a[116:119],v[72:75]\n"
MF"v[72:75],v[192:195],a[100:103],v[72:75]\n"
RT"a[146:147],v246 offset:12800\n"
MF"v[80:83],v[192:195],a[104:107],v[80:83]\n"
BL"v236,s[12:15],s48 offen offset:2048 lds\n"
MF"v[80:83],v[196:199],a[120:123],v[80:83]\n"
RD"v[220:223],v243 offset:14336\n"
MF"v[88:91],v[196:199],a[124:127],v[88:91]\n"
RT"a[148:149],v247 offset:12288\n"
MF"v[88:91],v[192:195],a[108:111],v[88:91]\n"
WT"lgkmcnt(4)\n"
BL"v237,s[12:15],s48 offen offset:3072 lds\n"
MF"v[68:71],v[200:203],a[96:99],v[68:71]\n"
MF"v[68:71],v[204:207],a[112:115],v[68:71]\n"
RT"a[150:151],v247 offset:12800\n"
MF"v[76:79],v[204:207],a[116:119],v[76:79]\n"
SA"m0,s37,0\n"
"s_bfe_u32 s48,s46,0x30018\n"
"s_lshl_b32 s48,s48,19\n"
MF"v[76:79],v[200:203],a[100:103],v[76:79]\n"
MF"v[84:87],v[200:203],a[104:107],v[84:87]\n"
WT"vmcnt(12) lgkmcnt(2)\n"
RT"a[152:153],v248 offset:12288\n"
MF"v[84:87],v[204:207],a[120:123],v[84:87]\n"
BL"v238,s[16:19],s48 offen offset:0 lds\n"
MF"v[92:95],v[204:207],a[124:127],v[92:95]\n"
"s_barrier\n"
MF"v[92:95],v[200:203],a[108:111],v[92:95]\n"
RD"v[192:195],v242 offset:16384\n"
MF"v[96:99],v[208:211],a[96:99],v[96:99]\n"
RT"a[154:155],v248 offset:12800\n"
MF"v[96:99],v[212:215],a[112:115],v[96:99]\n"
BL"v239,s[16:19],s48 offen offset:1024 lds\n"
MF"v[104:107],v[212:215],a[116:119],v[104:107]\n"
RD"v[196:199],v243 offset:16384\n"
MF"v[104:107],v[208:211],a[100:103],v[104:107]\n"
RT"a[156:157],v249 offset:12288\n"
MF"v[112:115],v[208:211],a[104:107],v[112:115]\n"
MF"v[112:115],v[212:215],a[120:123],v[112:115]\n"
RT"a[158:159],v249 offset:12800\n"
MF"v[120:123],v[212:215],a[124:127],v[120:123]\n"
BL"v240,s[16:19],s48 offen offset:2048 lds\n"
MF"v[120:123],v[208:211],a[108:111],v[120:123]\n"
RD"v[200:203],v242 offset:18432\n"
MF"v[100:103],v[216:219],a[96:99],v[100:103]\n"
MF"v[100:103],v[220:223],a[112:115],v[100:103]\n"
RD"v[204:207],v243 offset:18432\n"
MF"v[108:111],v[220:223],a[116:119],v[108:111]\n"
BL"v241,s[16:19],s48 offen offset:3072 lds\n"
MF"v[108:111],v[216:219],a[100:103],v[108:111]\n"
MF"v[116:119],v[216:219],a[104:107],v[116:119]\n"
SA"m0,s37,4096\n"
"s_bfe_u32 s48,s46,0x30018\n"
"s_lshl_b32 s48,s48,19\n"
MF"v[116:119],v[220:223],a[120:123],v[116:119]\n"
"s_bitset1_b32 s48,18\n"
RD"v[208:211],v242 offset:20480\n"
MF"v[124:127],v[220:223],a[124:127],v[124:127]\n"
BL"v238,s[16:19],s48 offen offset:0 lds\n"
MF"v[124:127],v[216:219],a[108:111],v[124:127]\n"
WT"lgkmcnt(2)\n"
MF"v[0:3],v[192:195],a[128:131],v[0:3]\n"
RD"v[212:215],v243 offset:20480\n"
MF"v[0:3],v[196:199],a[144:147],v[0:3]\n"
MF"v[8:11],v[196:199],a[148:151],v[8:11]\n"
BL"v239,s[16:19],s48 offen offset:1024 lds\n"
MF"v[8:11],v[192:195],a[132:135],v[8:11]\n"
MF"v[16:19],v[192:195],a[136:139],v[16:19]\n"
RD"v[216:219],v242 offset:22528\n"
MF"v[16:19],v[196:199],a[152:155],v[16:19]\n"
MF"v[24:27],v[196:199],a[156:159],v[24:27]\n"
MF"v[24:27],v[192:195],a[140:143],v[24:27]\n"
RD"v[220:223],v243 offset:22528\n"
MF"v[4:7],v[200:203],a[128:131],v[4:7]\n"
WT"lgkmcnt(2)\n"
BL"v240,s[16:19],s48 offen offset:2048 lds\n"
MF"v[4:7],v[204:207],a[144:147],v[4:7]\n"
WT"vmcnt(15)\n"
RT"a[160:161],v246 offset:16384\n"
MF"v[12:15],v[204:207],a[148:151],v[12:15]\n"
MF"v[12:15],v[200:203],a[132:135],v[12:15]\n"
RD"v[192:195],v242 offset:24576\n"
MF"v[20:23],v[200:203],a[136:139],v[20:23]\n"
RT"a[162:163],v246 offset:16896\n"
MF"v[20:23],v[204:207],a[152:155],v[20:23]\n"
BL"v241,s[16:19],s48 offen offset:3072 lds\n"
MF"v[28:31],v[204:207],a[156:159],v[28:31]\n"
SA"m0,s36,0\n"
RT"a[164:165],v247 offset:16384\n"
"s_bfe_u32 s48,s46,0x30018\n"
MF"v[28:31],v[200:203],a[140:143],v[28:31]\n"
"s_lshl_b32 s48,s48,7\n"
RD"v[196:199],v243 offset:24576\n"
MF"v[32:35],v[208:211],a[128:131],v[32:35]\n"
BL"v234,s[12:15],s48 offen offset:0 lds\n"
MF"v[32:35],v[212:215],a[144:147],v[32:35]\n"
RT"a[166:167],v247 offset:16896\n"
MF"v[40:43],v[212:215],a[148:151],v[40:43]\n"
RD"v[200:203],v242 offset:26624\n"
MF"v[40:43],v[208:211],a[132:135],v[40:43]\n"
MF"v[48:51],v[208:211],a[136:139],v[48:51]\n"
RT"a[168:169],v248 offset:16384\n"
MF"v[48:51],v[212:215],a[152:155],v[48:51]\n"
BL"v235,s[12:15],s48 offen offset:1024 lds\n"
MF"v[56:59],v[212:215],a[156:159],v[56:59]\n"
RD"v[204:207],v243 offset:26624\n"
MF"v[56:59],v[208:211],a[140:143],v[56:59]\n"
WT"lgkmcnt(4)\n"
RT"a[170:171],v248 offset:16896\n"
MF"v[36:39],v[216:219],a[128:131],v[36:39]\n"
MF"v[36:39],v[220:223],a[144:147],v[36:39]\n"
RD"v[208:211],v242 offset:28672\n"
MF"v[44:47],v[220:223],a[148:151],v[44:47]\n"
BL"v236,s[12:15],s48 offen offset:2048 lds\n"
MF"v[44:47],v[216:219],a[132:135],v[44:47]\n"
RT"a[172:173],v249 offset:16384\n"
MF"v[52:55],v[216:219],a[136:139],v[52:55]\n"
MF"v[52:55],v[220:223],a[152:155],v[52:55]\n"
RD"v[212:215],v243 offset:28672\n"
MF"v[60:63],v[220:223],a[156:159],v[60:63]\n"
RT"a[174:175],v249 offset:16896\n"
MF"v[60:63],v[216:219],a[140:143],v[60:63]\n"
BL"v237,s[12:15],s48 offen offset:3072 lds\n"
MF"v[64:67],v[192:195],a[128:131],v[64:67]\n"
SA"m0,s37,8192\n"
RD"v[216:219],v242 offset:30720\n"
"s_bfe_u32 s48,s46,0x3001c\n"
MF"v[64:67],v[196:199],a[144:147],v[64:67]\n"
"s_lshl_b32 s48,s48,19\n"
WT"vmcnt(16)\n"
RT"a[176:177],v246 offset:20480\n"
MF"v[72:75],v[196:199],a[148:151],v[72:75]\n"
BL"v238,s[16:19],s48 offen offset:0 lds\n"
MF"v[72:75],v[192:195],a[132:135],v[72:75]\n"
RT"a[178:179],v246 offset:20992\n"
MF"v[80:83],v[192:195],a[136:139],v[80:83]\n"
RD"v[220:223],v243 offset:30720\n"
MF"v[80:83],v[196:199],a[152:155],v[80:83]\n"
MF"v[88:91],v[196:199],a[156:159],v[88:91]\n"
RT"a[180:181],v247 offset:20480\n"
MF"v[88:91],v[192:195],a[140:143],v[88:91]\n"
WT"lgkmcnt(4)\n"
BL"v239,s[16:19],s48 offen offset:1024 lds\n"
MF"v[68:71],v[200:203],a[128:131],v[68:71]\n"
MF"v[68:71],v[204:207],a[144:147],v[68:71]\n"
RT"a[182:183],v247 offset:20992\n"
MF"v[76:79],v[204:207],a[148:151],v[76:79]\n"
MF"v[76:79],v[200:203],a[132:135],v[76:79]\n"
BL"v240,s[16:19],s48 offen offset:2048 lds\n"
MF"v[84:87],v[200:203],a[136:139],v[84:87]\n"
WT"vmcnt(15) lgkmcnt(2)\n"
RT"a[184:185],v248 offset:20480\n"
MF"v[84:87],v[204:207],a[152:155],v[84:87]\n"
"s_barrier\n"
MF"v[92:95],v[204:207],a[156:159],v[92:95]\n"
RD"v[192:195],v242 offset:32768\n"
MF"v[92:95],v[200:203],a[140:143],v[92:95]\n"
RT"a[186:187],v248 offset:20992\n"
MF"v[96:99],v[208:211],a[128:131],v[96:99]\n"
BL"v241,s[16:19],s48 offen offset:3072 lds\n"
MF"v[96:99],v[212:215],a[144:147],v[96:99]\n"
MF"v[104:107],v[212:215],a[148:151],v[104:107]\n"
SA"m0,s37,12288\n"
RD"v[196:199],v243 offset:32768\n"
"s_bfe_u32 s48,s46,0x3001c\n"
MF"v[104:107],v[208:211],a[132:135],v[104:107]\n"
"s_lshl_b32 s48,s48,19\n"
RT"a[188:189],v249 offset:20480\n"
"s_bitset1_b32 s48,18\n"
MF"v[112:115],v[208:211],a[136:139],v[112:115]\n"
BL"v238,s[16:19],s48 offen offset:0 lds\n"
MF"v[112:115],v[212:215],a[152:155],v[112:115]\n"
RT"a[190:191],v249 offset:20992\n"
MF"v[120:123],v[212:215],a[156:159],v[120:123]\n"
RD"v[200:203],v242 offset:34816\n"
MF"v[120:123],v[208:211],a[140:143],v[120:123]\n"
MF"v[100:103],v[216:219],a[128:131],v[100:103]\n"
BL"v239,s[16:19],s48 offen offset:1024 lds\n"
MF"v[100:103],v[220:223],a[144:147],v[100:103]\n"
RD"v[204:207],v243 offset:34816\n"
MF"v[108:111],v[220:223],a[148:151],v[108:111]\n"
MF"v[108:111],v[216:219],a[132:135],v[108:111]\n"
MF"v[116:119],v[216:219],a[136:139],v[116:119]\n"
MF"v[116:119],v[220:223],a[152:155],v[116:119]\n"
RD"v[208:211],v242 offset:36864\n"
MF"v[124:127],v[220:223],a[156:159],v[124:127]\n"
BL"v240,s[16:19],s48 offen offset:2048 lds\n"
MF"v[124:127],v[216:219],a[140:143],v[124:127]\n"
WT"lgkmcnt(2)\n"
MF"v[0:3],v[192:195],a[160:163],v[0:3]\n"
RD"v[212:215],v243 offset:36864\n"
MF"v[0:3],v[196:199],a[176:179],v[0:3]\n"
BL"v241,s[16:19],s48 offen offset:3072 lds\n"
MF"v[8:11],v[196:199],a[180:183],v[8:11]\n"
MF"v[8:11],v[192:195],a[164:167],v[8:11]\n"
MF"v[16:19],v[192:195],a[168:171],v[16:19]\n"
SA"m0,s36,16384\n"
RD"v[216:219],v242 offset:38912\n"
"s_bfe_u32 s48,s46,0x3001c\n"
MF"v[16:19],v[196:199],a[184:187],v[16:19]\n"
"s_lshl_b32 s48,s48,7\n"
MF"v[24:27],v[196:199],a[188:191],v[24:27]\n"
BL"v234,s[12:15],s48 offen offset:0 lds\n"
MF"v[24:27],v[192:195],a[172:175],v[24:27]\n"
RD"v[220:223],v243 offset:38912\n"
MF"v[4:7],v[200:203],a[160:163],v[4:7]\n"
WT"lgkmcnt(2)\n"
WT"vmcnt(17)\n"
RT"a[192:193],v246 offset:0\n"
MF"v[4:7],v[204:207],a[176:179],v[4:7]\n"
MF"v[12:15],v[204:207],a[180:183],v[12:15]\n"
BL"v235,s[12:15],s48 offen offset:1024 lds\n"
MF"v[12:15],v[200:203],a[164:167],v[12:15]\n"
RD"v[192:195],v242 offset:40960\n"
MF"v[20:23],v[200:203],a[168:171],v[20:23]\n"
RT"a[194:195],v246 offset:512\n"
MF"v[20:23],v[204:207],a[184:187],v[20:23]\n"
MF"v[28:31],v[204:207],a[188:191],v[28:31]\n"
RT"a[196:197],v247 offset:0\n"
MF"v[28:31],v[200:203],a[172:175],v[28:31]\n"
RD"v[196:199],v243 offset:40960\n"
MF"v[32:35],v[208:211],a[160:163],v[32:35]\n"
BL"v236,s[12:15],s48 offen offset:2048 lds\n"
MF"v[32:35],v[212:215],a[176:179],v[32:35]\n"
RT"a[198:199],v247 offset:512\n"
MF"v[40:43],v[212:215],a[180:183],v[40:43]\n"
RD"v[200:203],v242 offset:43008\n"
MF"v[40:43],v[208:211],a[164:167],v[40:43]\n"
BL"v237,s[12:15],s48 offen offset:3072 lds\n"
MF"v[48:51],v[208:211],a[168:171],v[48:51]\n"
RT"a[200:201],v248 offset:0\n"
MF"v[48:51],v[212:215],a[184:187],v[48:51]\n"
MF"v[56:59],v[212:215],a[188:191],v[56:59]\n"
SA"m0,s37,16384\n"
RD"v[204:207],v243 offset:43008\n"
SA"s48,s39,0\n"
MF"v[56:59],v[208:211],a[172:175],v[56:59]\n"
WT"lgkmcnt(4)\n"
RT"a[202:203],v248 offset:512\n"
"s_lshr_b32 s50,s30,1\n"
MF"v[36:39],v[216:219],a[160:163],v[36:39]\n"
"s_and_b32 s51,s30,1\n"
BL"v234,s[12:15],s48 offen offset:0 lds\n"
MF"v[36:39],v[220:223],a[176:179],v[36:39]\n"
"s_mul_i32 s38,s50,24576\n"
RD"v[208:211],v242 offset:45056\n"
MF"v[44:47],v[220:223],a[180:183],v[44:47]\n"
"s_lshl_b32 s60,s50,18\n"
RT"a[204:205],v249 offset:0\n"
MF"v[44:47],v[216:219],a[164:167],v[44:47]\n"
"s_lshl_b32 s50,s51,12\n"
MF"v[52:55],v[216:219],a[168:171],v[52:55]\n"
SA"s38,s38,s50\n"
BL"v235,s[12:15],s48 offen offset:1024 lds\n"
MF"v[52:55],v[220:223],a[184:187],v[52:55]\n"
"s_lshl_b32 s50,s51,7\n"
RD"v[212:215],v243 offset:45056\n"
MF"v[60:63],v[220:223],a[188:191],v[60:63]\n"
SA"s60,s60,s50\n"
RT"a[206:207],v249 offset:512\n"
MF"v[60:63],v[216:219],a[172:175],v[60:63]\n"
MF"v[64:67],v[192:195],a[160:163],v[64:67]\n"
RD"v[216:219],v242 offset:47104\n"
MF"v[64:67],v[196:199],a[176:179],v[64:67]\n"
"s_sub_u32 s99,s34,5\n"
WT"vmcnt(18)\n"
RT"a[208:209],v246 offset:4096\n"
MF"v[72:75],v[196:199],a[180:183],v[72:75]\n"
"s_cmp_lt_u32 s34,5\n"
BL"v236,s[12:15],s48 offen offset:2048 lds\n"
MF"v[72:75],v[192:195],a[164:167],v[72:75]\n"
"s_cselect_b32 s99,s34,s99\n"
RT"a[210:211],v246 offset:4608\n"
"s_mul_i32 s99,s99,1\n"
MF"v[80:83],v[192:195],a[168:171],v[80:83]\n"
"s_lshl_b32 s61,s99,20\n"
RD"v[220:223],v243 offset:47104\n"
MF"v[80:83],v[196:199],a[184:187],v[80:83]\n"
BL"v237,s[12:15],s48 offen offset:3072 lds\n"
MF"v[88:91],v[196:199],a[188:191],v[88:91]\n"
RT"a[212:213],v247 offset:4096\n"
MF"v[88:91],v[192:195],a[172:175],v[88:91]\n"
WT"lgkmcnt(4)\n"
MF"v[68:71],v[200:203],a[160:163],v[68:71]\n"
SA"m0,s37,20480\n"
SA"s48,s98,0\n"
MF"v[68:71],v[204:207],a[176:179],v[68:71]\n"
RT"a[214:215],v247 offset:4608\n"
MF"v[76:79],v[204:207],a[180:183],v[76:79]\n"
BL"v234,s[12:15],s48 offen offset:0 lds\n"
MF"v[76:79],v[200:203],a[164:167],v[76:79]\n"
MF"v[84:87],v[200:203],a[168:171],v[84:87]\n"
WT"vmcnt(17) lgkmcnt(2)\n"
RT"a[216:217],v248 offset:4096\n"
MF"v[84:87],v[204:207],a[184:187],v[84:87]\n"
"s_barrier\n"
MF"v[92:95],v[204:207],a[188:191],v[92:95]\n"
RD"v[192:195],v242 offset:0\n"
MF"v[92:95],v[200:203],a[172:175],v[92:95]\n"
BL"v235,s[12:15],s48 offen offset:1024 lds\n"
MF"v[96:99],v[208:211],a[160:163],v[96:99]\n"
RT"a[218:219],v248 offset:4608\n"
MF"v[96:99],v[212:215],a[176:179],v[96:99]\n"
MF"v[104:107],v[212:215],a[180:183],v[104:107]\n"
RD"v[196:199],v243 offset:0\n"
MF"v[104:107],v[208:211],a[164:167],v[104:107]\n"
RT"a[220:221],v249 offset:4096\n"
MF"v[112:115],v[208:211],a[168:171],v[112:115]\n"
BL"v236,s[12:15],s48 offen offset:2048 lds\n"
MF"v[112:115],v[212:215],a[184:187],v[112:115]\n"
RT"a[222:223],v249 offset:4608\n"
MF"v[120:123],v[212:215],a[188:191],v[120:123]\n"
RD"v[200:203],v242 offset:2048\n"
MF"v[120:123],v[208:211],a[172:175],v[120:123]\n"
BL"v237,s[12:15],s48 offen offset:3072 lds\n"
MF"v[100:103],v[216:219],a[160:163],v[100:103]\n"
MF"v[100:103],v[220:223],a[176:179],v[100:103]\n"
RD"v[204:207],v243 offset:2048\n"
MF"v[108:111],v[220:223],a[180:183],v[108:111]\n"
SA"m0,s37,0\n"
SA"s48,s39,32768\n"
MF"v[108:111],v[216:219],a[164:167],v[108:111]\n"
MF"v[116:119],v[216:219],a[168:171],v[116:119]\n"
BL"v234,s[12:15],s48 offen offset:0 lds\n"
MF"v[116:119],v[220:223],a[184:187],v[116:119]\n"
RD"v[208:211],v242 offset:4096\n"
MF"v[124:127],v[220:223],a[188:191],v[124:127]\n"
MF"v[124:127],v[216:219],a[172:175],v[124:127]\n"
WT"lgkmcnt(2)\n"
MF"v[0:3],v[192:195],a[192:195],v[0:3]\n"
RD"v[212:215],v243 offset:4096\n"
MF"v[0:3],v[196:199],a[208:211],v[0:3]\n"
BL"v235,s[12:15],s48 offen offset:1024 lds\n"
MF"v[8:11],v[196:199],a[212:215],v[8:11]\n"
MF"v[8:11],v[192:195],a[196:199],v[8:11]\n"
MF"v[16:19],v[192:195],a[200:203],v[16:19]\n"
RD"v[216:219],v242 offset:6144\n"
MF"v[16:19],v[196:199],a[216:219],v[16:19]\n"
BL"v236,s[12:15],s48 offen offset:2048 lds\n"
MF"v[24:27],v[196:199],a[220:223],v[24:27]\n"
BL"v[160:163],v224,s[8:11],0 offen offset:256 sc1\n"
MF"v[24:27],v[192:195],a[204:207],v[24:27]\n"
RD"v[220:223],v243 offset:6144\n"
MF"v[4:7],v[200:203],a[192:195],v[4:7]\n"
WT"lgkmcnt(2)\n"
WT"vmcnt(20)\n"
RT"a[224:225],v246 offset:8192\n"
MF"v[4:7],v[204:207],a[208:211],v[4:7]\n"
BL"v[164:167],v224,s[8:11],0 offen offset:1280 sc1\n"
MF"v[12:15],v[204:207],a[212:215],v[12:15]\n"
BL"v237,s[12:15],s48 offen offset:3072 lds\n"
MF"v[12:15],v[200:203],a[196:199],v[12:15]\n"
RD"v[192:195],v242 offset:8192\n"
MF"v[20:23],v[200:203],a[200:203],v[20:23]\n"
SA"m0,s37,4096\n"
RT"a[226:227],v246 offset:8704\n"
SA"s48,s98,32768\n"
MF"v[20:23],v[204:207],a[216:219],v[20:23]\n"
MF"v[28:31],v[204:207],a[220:223],v[28:31]\n"
RT"a[228:229],v247 offset:8192\n"
MF"v[28:31],v[200:203],a[204:207],v[28:31]\n"
BL"v234,s[12:15],s48 offen offset:0 lds\n"
MF"v[32:35],v[208:211],a[192:195],v[32:35]\n"
RD"v[196:199],v243 offset:8192\n"
MF"v[32:35],v[212:215],a[208:211],v[32:35]\n"
RT"a[230:231],v247 offset:8704\n"
MF"v[40:43],v[212:215],a[212:215],v[40:43]\n"
RD"v[200:203],v242 offset:10240\n"
MF"v[40:43],v[208:211],a[196:199],v[40:43]\n"
BL"v235,s[12:15],s48 offen offset:1024 lds\n"
MF"v[48:51],v[208:211],a[200:203],v[48:51]\n"
RT"a[232:233],v248 offset:8192\n"
MF"v[48:51],v[212:215],a[216:219],v[48:51]\n"
MF"v[56:59],v[212:215],a[220:223],v[56:59]\n"
RD"v[204:207],v243 offset:10240\n"
MF"v[56:59],v[208:211],a[204:207],v[56:59]\n"
WT"lgkmcnt(4)\n"
RT"a[234:235],v248 offset:8704\n"
MF"v[36:39],v[216:219],a[192:195],v[36:39]\n"
BL"v236,s[12:15],s48 offen offset:2048 lds\n"
MF"v[36:39],v[220:223],a[208:211],v[36:39]\n"
RD"v[208:211],v242 offset:12288\n"
MF"v[44:47],v[220:223],a[212:215],v[44:47]\n"
RT"a[236:237],v249 offset:8192\n"
MF"v[44:47],v[216:219],a[196:199],v[44:47]\n"
BL"v237,s[12:15],s48 offen offset:3072 lds\n"
MF"v[52:55],v[216:219],a[200:203],v[52:55]\n"
MF"v[52:55],v[220:223],a[216:219],v[52:55]\n"
RD"v[212:215],v243 offset:12288\n"
MF"v[60:63],v[220:223],a[220:223],v[60:63]\n"
RT"a[238:239],v249 offset:8704\n"
MF"v[60:63],v[216:219],a[204:207],v[60:63]\n"
MF"v[64:67],v[192:195],a[192:195],v[64:67]\n"
RD"v[216:219],v242 offset:14336\n"
MF"v[64:67],v[196:199],a[208:211],v[64:67]\n"
SA"m0,s37,8192\n"
WT"vmcnt(22)\n"
RT"a[240:241],v246 offset:12288\n"
MF"v[72:75],v[196:199],a[212:215],v[72:75]\n"
SA"s48,s39,65536\n"
MF"v[72:75],v[192:195],a[196:199],v[72:75]\n"
RT"a[242:243],v246 offset:12800\n"
MF"v[80:83],v[192:195],a[200:203],v[80:83]\n"
WT"lgkmcnt(3)\n"
BL"v234,s[12:15],s48 offen offset:0 lds\n"
MF"v[80:83],v[196:199],a[216:219],v[80:83]\n"
RD"v[220:223],v243 offset:14336\n"
MF"v[88:91],v[196:199],a[220:223],v[88:91]\n"
RT"a[244:245],v247 offset:12288\n"
MF"v[88:91],v[192:195],a[204:207],v[88:91]\n"
BL"v235,s[12:15],s48 offen offset:1024 lds\n"
MF"v[68:71],v[200:203],a[192:195],v[68:71]\n"
MF"v[68:71],v[204:207],a[208:211],v[68:71]\n"
RT"a[246:247],v247 offset:12800\n"
MF"v[76:79],v[204:207],a[212:215],v[76:79]\n"
MF"v[76:79],v[200:203],a[196:199],v[76:79]\n"
MF"v[84:87],v[200:203],a[200:203],v[84:87]\n"
WT"vmcnt(20) lgkmcnt(2)\n"
RT"a[248:249],v248 offset:12288\n"
MF"v[84:87],v[204:207],a[216:219],v[84:87]\n"
BL"v236,s[12:15],s48 offen offset:2048 lds\n"
MF"v[92:95],v[204:207],a[220:223],v[92:95]\n"
"s_barrier\n"
MF"v[92:95],v[200:203],a[204:207],v[92:95]\n"
RD"v[192:195],v242 offset:16384\n"
MF"v[96:99],v[208:211],a[192:195],v[96:99]\n"
RT"a[250:251],v248 offset:12800\n"
MF"v[96:99],v[212:215],a[208:211],v[96:99]\n"
BL"v237,s[12:15],s48 offen offset:3072 lds\n"
MF"v[104:107],v[212:215],a[212:215],v[104:107]\n"
RD"v[196:199],v243 offset:16384\n"
MF"v[104:107],v[208:211],a[196:199],v[104:107]\n"
RT"a[252:253],v249 offset:12288\n"
MF"v[112:115],v[208:211],a[200:203],v[112:115]\n"
MF"v[112:115],v[212:215],a[216:219],v[112:115]\n"
RT"a[254:255],v249 offset:12800\n"
MF"v[120:123],v[212:215],a[220:223],v[120:123]\n"
RD"v[200:203],v242 offset:18432\n"
MF"v[120:123],v[208:211],a[204:207],v[120:123]\n"
MF"v[100:103],v[216:219],a[192:195],v[100:103]\n"
"s_mul_i32 s50,s35,1048576\n"
MF"v[100:103],v[220:223],a[208:211],v[100:103]\n"
"s_lshl_b32 s51,s34,15\n"
RD"v[204:207],v243 offset:18432\n"
SA"m0,s37,12288\n"
MF"v[108:111],v[220:223],a[212:215],v[108:111]\n"
SA"s48,s98,65536\n"
SA"s50,s50,s51\n"
MF"v[108:111],v[216:219],a[196:199],v[108:111]\n"
WT"lgkmcnt(2)\n"
BL"v234,s[12:15],s48 offen offset:0 lds\n"
MF"v[116:119],v[216:219],a[200:203],v[116:119]\n"
"s_and_b32 s51,s30,1\n"
"s_lshl_b32 s51,s51,12\n"
MF"v[116:119],v[220:223],a[216:219],v[116:119]\n"
SA"s50,s50,s51\n"
RD"v[208:211],v242 offset:20480\n"
MF"v[124:127],v[220:223],a[220:223],v[124:127]\n"
SA"s50,s50,9699328\n"
WT"vmcnt(10)\n"
"v_cmp_eq_u64 vcc,s[28:29],v[160:161]\n"
"v_cmp_eq_u64_e64 s[62:63],s[28:29],v[162:163]\n"
"s_and_b64 vcc,vcc,s[62:63]\n"
"v_cmp_eq_u64_e64 s[62:63],s[28:29],v[164:165]\n"
"s_and_b64 vcc,vcc,s[62:63]\n"
"v_cmp_eq_u64_e64 s[62:63],s[28:29],v[166:167]\n"
"s_and_b64 vcc,vcc,s[62:63]\n"
"s_cmp_eq_u64 vcc,exec\n"
"s_cbranch_scc0 L1004_%=\n"
"L1005_%=:\n"
MF"v[124:127],v[216:219],a[204:207],v[124:127]\n"
"s_mov_b32 s94,8192\n"
BL"v235,s[12:15],s48 offen offset:1024 lds\n"
MF"v[0:3],v[192:195],a[224:227],v[0:3]\n"
"s_mov_b32 s95,12288\n"
RD"v[212:215],v243 offset:20480\n"
"v_add_u32 v250,s50,v224\n"
MF"v[0:3],v[196:199],a[240:243],v[0:3]\n"
BL"v[160:163],v224,s[8:11],s94 offen offset:0 sc1\n"
MF"v[8:11],v[196:199],a[244:247],v[8:11]\n"
BL"v[164:167],v224,s[8:11],s94 offen offset:1024 sc1\n"
MF"v[8:11],v[192:195],a[228:231],v[8:11]\n"
MF"v[16:19],v[192:195],a[232:235],v[16:19]\n"
RD"v[216:219],v242 offset:22528\n"
MF"v[16:19],v[196:199],a[248:251],v[16:19]\n"
BL"v236,s[12:15],s48 offen offset:2048 lds\n"
MF"v[24:27],v[196:199],a[252:255],v[24:27]\n"
BL"v[168:171],v224,s[8:11],s94 offen offset:2048 sc1\n"
MF"v[24:27],v[192:195],a[236:239],v[24:27]\n"
WT"lgkmcnt(1)\n"
BL"v[172:175],v224,s[8:11],s94 offen offset:3072 sc1\n"
MF"v[4:7],v[200:203],a[224:227],v[4:7]\n"
RD"v[220:223],v243 offset:22528\n"
MF"v[4:7],v[204:207],a[240:243],v[4:7]\n"
BL"v237,s[12:15],s48 offen offset:3072 lds\n"
MF"v[12:15],v[204:207],a[244:247],v[12:15]\n"
BL"v[176:179],v224,s[8:11],s95 offen offset:0 sc1\n"
MF"v[12:15],v[200:203],a[228:231],v[12:15]\n"
BL"v[180:183],v224,s[8:11],s95 offen offset:1024 sc1\n"
MF"v[20:23],v[200:203],a[232:235],v[20:23]\n"
RD"v[192:195],v242 offset:24576\n"
MF"v[20:23],v[204:207],a[248:251],v[20:23]\n"
BL"v[184:187],v224,s[8:11],s95 offen offset:2048 sc1\n"
MF"v[28:31],v[204:207],a[252:255],v[28:31]\n"
BL"v[188:191],v224,s[8:11],s95 offen offset:3072 sc1\n"
MF"v[28:31],v[200:203],a[236:239],v[28:31]\n"
RD"v[196:199],v243 offset:24576\n"
MF"v[32:35],v[208:211],a[224:227],v[32:35]\n"
MF"v[32:35],v[212:215],a[240:243],v[32:35]\n"
MF"v[40:43],v[212:215],a[244:247],v[40:43]\n"
RD"v[200:203],v242 offset:26624\n"
MF"v[40:43],v[208:211],a[228:231],v[40:43]\n"
MF"v[48:51],v[208:211],a[232:235],v[48:51]\n"
MF"v[48:51],v[212:215],a[248:251],v[48:51]\n"
MF"v[56:59],v[212:215],a[252:255],v[56:59]\n"
RD"v[204:207],v243 offset:26624\n"
MF"v[56:59],v[208:211],a[236:239],v[56:59]\n"
WT"lgkmcnt(2)\n"
MF"v[36:39],v[216:219],a[224:227],v[36:39]\n"
MF"v[36:39],v[220:223],a[240:243],v[36:39]\n"
RD"v[208:211],v242 offset:28672\n"
MF"v[44:47],v[220:223],a[244:247],v[44:47]\n"
MF"v[44:47],v[216:219],a[228:231],v[44:47]\n"
MF"v[52:55],v[216:219],a[232:235],v[52:55]\n"
MF"v[52:55],v[220:223],a[248:251],v[52:55]\n"
RD"v[212:215],v243 offset:28672\n"
MF"v[60:63],v[220:223],a[252:255],v[60:63]\n"
MF"v[60:63],v[216:219],a[236:239],v[60:63]\n"
MF"v[64:67],v[192:195],a[224:227],v[64:67]\n"
RD"v[216:219],v242 offset:30720\n"
MF"v[64:67],v[196:199],a[240:243],v[64:67]\n"
MF"v[72:75],v[196:199],a[244:247],v[72:75]\n"
MF"v[72:75],v[192:195],a[228:231],v[72:75]\n"
MF"v[80:83],v[192:195],a[232:235],v[80:83]\n"
RD"v[220:223],v243 offset:30720\n"
MF"v[80:83],v[196:199],a[248:251],v[80:83]\n"
MF"v[88:91],v[196:199],a[252:255],v[88:91]\n"
MF"v[88:91],v[192:195],a[236:239],v[88:91]\n"
WT"lgkmcnt(1)\n"
MF"v[68:71],v[200:203],a[224:227],v[68:71]\n"
MF"v[68:71],v[204:207],a[240:243],v[68:71]\n"
MF"v[76:79],v[204:207],a[244:247],v[76:79]\n"
"v_add_u32 v244,114688,v242\n"
"v_add_u32 v245,114688,v243\n"
MF"v[76:79],v[200:203],a[228:231],v[76:79]\n"
"v_add_u32 v242,65536,v242\n"
"v_add_u32 v243,65536,v243\n"
MF"v[84:87],v[200:203],a[232:235],v[84:87]\n"
WT"lgkmcnt(0)\n"
MF"v[84:87],v[204:207],a[248:251],v[84:87]\n"
"s_barrier\n"
MF"v[92:95],v[204:207],a[252:255],v[92:95]\n"
RD"v[192:195],v242 offset:16384\n"
MF"v[92:95],v[200:203],a[236:239],v[92:95]\n"
MF"v[96:99],v[208:211],a[224:227],v[96:99]\n"
MF"v[96:99],v[212:215],a[240:243],v[96:99]\n"
MF"v[104:107],v[212:215],a[244:247],v[104:107]\n"
SA"m0,s36,49152\n"
RD"v[196:199],v243 offset:16384\n"
SA"s48,s60,s61\n"
MF"v[104:107],v[208:211],a[228:231],v[104:107]\n"
MF"v[112:115],v[208:211],a[232:235],v[112:115]\n"
BL"v238,s[20:23],s48 offen offset:0 lds\n"
MF"v[112:115],v[212:215],a[248:251],v[112:115]\n"
MF"v[120:123],v[212:215],a[252:255],v[120:123]\n"
RD"v[200:203],v242 offset:18432\n"
MF"v[120:123],v[208:211],a[236:239],v[120:123]\n"
MF"v[100:103],v[216:219],a[224:227],v[100:103]\n"
BL"v239,s[20:23],s48 offen offset:1024 lds\n"
MF"v[100:103],v[220:223],a[240:243],v[100:103]\n"
"s_cmp_eq_u32 s31,1\n"
"s_cselect_b32 s94,0,2130706432\n"
MF"v[108:111],v[220:223],a[244:247],v[108:111]\n"
"buffer_store_dwordx2 v[232:233],v230,s[8:11],s94 offen sc1\n"
MF"v[108:111],v[216:219],a[228:231],v[108:111]\n"
RD"v[204:207],v243 offset:18432\n"
"v_mov_b32 v227,0\n"
MF"v[116:119],v[216:219],a[232:235],v[116:119]\n"
WT"vmcnt(12)\n"
"v_mov_b32 v226,v160\n"
"v_mov_b32 v228,0\n"
MF"v[116:119],v[220:223],a[248:251],v[116:119]\n"
BL"v240,s[20:23],s48 offen offset:2048 lds\n"
MF"v[124:127],v[220:223],a[252:255],v[124:127]\n"
SA"s61,s61,524288\n"
RD"v[208:211],v242 offset:20480\n"
"v_med3_u32 v228,v227,v228,v161\n"
MF"v[124:127],v[216:219],a[236:239],v[124:127]\n"
WT"lgkmcnt(2)\n"
"v_med3_u32 v227,v226,v227,v161\n"
"v_max_u32 v226,v226,v161\n"
MF"v[128:131],v[192:195],a[0:3],0\n"
"v_med3_u32 v228,v227,v228,v162\n"
"v_med3_u32 v227,v226,v227,v162\n"
MF"v[128:131],v[196:199],a[16:19],v[128:131]\n"
RD"v[212:215],v243 offset:20480\n"
"v_max_u32 v226,v226,v162\n"
MF"v[136:139],v[196:199],a[20:23],0\n"
BL"v241,s[20:23],s48 offen offset:3072 lds\n"
MF"v[136:139],v[192:195],a[4:7],v[136:139]\n"
"v_med3_u32 v228,v227,v228,v163\n"
"v_med3_u32 v227,v226,v227,v163\n"
MF"v[144:147],v[192:195],a[8:11],0\n"
"v_max_u32 v226,v226,v163\n"
PK"v0,v0,v1\n"
MF"v[144:147],v[196:199],a[24:27],v[144:147]\n"
RD"v[216:219],v242 offset:22528\n"
PK"v1,v2,v3\n"
MF"v[152:155],v[196:199],a[28:31],0\n"
SA"m0,s36,32768\n"
WT"vmcnt(13)\n"
"v_med3_u32 v228,v227,v228,v164\n"
MF"v[152:155],v[192:195],a[12:15],v[152:155]\n"
SA"s48,s60,s61\n"
PK"v2,v4,v5\n"
PK"v3,v6,v7\n"
MF"v[132:135],v[200:203],a[0:3],0\n"
WT"lgkmcnt(1)\n"
BL"v238,s[20:23],s48 offen offset:0 lds\n"
MF"v[132:135],v[204:207],a[16:19],v[132:135]\n"
RD"v[220:223],v243 offset:22528\n"
"v_med3_u32 v227,v226,v227,v164\n"
MF"v[140:143],v[204:207],a[20:23],0\n"
"v_max_u32 v226,v226,v164\n"
"v_med3_u32 v228,v227,v228,v165\n"
MF"v[140:143],v[200:203],a[4:7],v[140:143]\n"
"v_med3_u32 v227,v226,v227,v165\n"
"v_max_u32 v226,v226,v165\n"
MF"v[148:151],v[200:203],a[8:11],0\n"
BL"v239,s[20:23],s48 offen offset:1024 lds\n"
MF"v[148:151],v[204:207],a[24:27],v[148:151]\n"
RD"v[192:195],v242 offset:40960\n"
"v_med3_u32 v228,v227,v228,v166\n"
MF"v[156:159],v[204:207],a[28:31],0\n"
PK"v8,v8,v9\n"
PK"v9,v10,v11\n"
MF"v[156:159],v[200:203],a[12:15],v[156:159]\n"
"v_med3_u32 v227,v226,v227,v166\n"
PK"v10,v12,v13\n"
MF"v[128:131],v[208:211],a[32:35],v[128:131]\n"
"s_mov_b32 s49,0\n"
PK"v11,v14,v15\n"
"v_max_u32 v226,v226,v166\n"
MF"v[128:131],v[212:215],a[48:51],v[128:131]\n"
RD"v[196:199],v243 offset:40960\n"
"v_med3_u32 v228,v227,v228,v167\n"
MF"v[136:139],v[212:215],a[52:55],v[136:139]\n"
SA"s61,s61,524288\n"
BL"v240,s[20:23],s48 offen offset:2048 lds\n"
MF"v[136:139],v[208:211],a[36:39],v[136:139]\n"
"s_cmp_eq_u32 s61,5242880\n"
BS"v[0:3],v250,s[8:11],s49 offen offset:0 sc1\n"
MF"v[144:147],v[208:211],a[40:43],v[144:147]\n"
"s_cselect_b32 s61,0,s61\n"
RD"v[200:203],v242 offset:43008\n"
"v_med3_u32 v227,v226,v227,v167\n"
MF"v[144:147],v[212:215],a[56:59],v[144:147]\n"
BL"v241,s[20:23],s48 offen offset:3072 lds\n"
MF"v[152:155],v[212:215],a[60:63],v[152:155]\n"
"v_max_u32 v226,v226,v167\n"
PK"v16,v16,v17\n"
MF"v[152:155],v[208:211],a[44:47],v[152:155]\n"
WT"lgkmcnt(2)\n"
WT"vmcnt(16)\n"
"v_med3_u32 v228,v227,v228,v168\n"
MF"v[132:135],v[216:219],a[32:35],v[132:135]\n"
RD"v[204:207],v243 offset:43008\n"
"v_med3_u32 v227,v226,v227,v168\n"
MF"v[132:135],v[220:223],a[48:51],v[132:135]\n"
PK"v17,v18,v19\n"
"v_max_u32 v226,v226,v168\n"
MF"v[140:143],v[220:223],a[52:55],v[140:143]\n"
"v_med3_u32 v228,v227,v228,v169\n"
PK"v18,v20,v21\n"
MF"v[140:143],v[216:219],a[36:39],v[140:143]\n"
PK"v19,v22,v23\n"
"v_med3_u32 v227,v226,v227,v169\n"
MF"v[148:151],v[216:219],a[40:43],v[148:151]\n"
RD"v[208:211],v242 offset:45056\n"
"v_max_u32 v226,v226,v169\n"
MF"v[148:151],v[220:223],a[56:59],v[148:151]\n"
"s_lshl_b32 s90,s31,2\n"
"v_med3_u32 v228,v227,v228,v170\n"
"v_med3_u32 v227,v226,v227,v170\n"
MF"v[156:159],v[220:223],a[60:63],v[156:159]\n"
SA"s90,s90,s30\n"
"v_max_u32 v226,v226,v170\n"
PK"v24,v24,v25\n"
MF"v[156:159],v[216:219],a[44:47],v[156:159]\n"
"s_lshl_b32 s90,s90,9\n"
PK"v25,v26,v27\n"
"v_med3_u32 v228,v227,v228,v171\n"
MF"v[128:131],v[192:195],a[64:67],v[128:131]\n"
WT"lgkmcnt(1)\n"
RD"v[212:215],v243 offset:45056\n"
"v_med3_u32 v227,v226,v227,v171\n"
MF"v[128:131],v[196:199],a[80:83],v[128:131]\n"
PK"v26,v28,v29\n"
PK"v27,v30,v31\n"
MF"v[136:139],v[196:199],a[84:87],v[136:139]\n"
"v_max_u32 v226,v226,v171\n"
PK"v32,v32,v33\n"
MF"v[136:139],v[192:195],a[68:71],v[136:139]\n"
RD"v[216:219],v242 offset:47104\n"
PK"v33,v34,v35\n"
MF"v[144:147],v[192:195],a[72:75],v[144:147]\n"
WT"vmcnt(15)\n"
"v_med3_u32 v228,v227,v228,v172\n"
"v_med3_u32 v227,v226,v227,v172\n"
MF"v[144:147],v[196:199],a[88:91],v[144:147]\n"
"v_max_u32 v226,v226,v172\n"
"v_med3_u32 v228,v227,v228,v173\n"
MF"v[152:155],v[196:199],a[92:95],v[152:155]\n"
"v_med3_u32 v227,v226,v227,v173\n"
"v_max_u32 v226,v226,v173\n"
MF"v[152:155],v[192:195],a[76:79],v[152:155]\n"
RD"v[220:223],v243 offset:47104\n"
PK"v34,v36,v37\n"
MF"v[132:135],v[200:203],a[64:67],v[132:135]\n"
PK"v35,v38,v39\n"
"v_med3_u32 v228,v227,v228,v174\n"
MF"v[132:135],v[204:207],a[80:83],v[132:135]\n"
"v_med3_u32 v227,v226,v227,v174\n"
"v_max_u32 v226,v226,v174\n"
MF"v[140:143],v[204:207],a[84:87],v[140:143]\n"
"v_med3_u32 v228,v227,v228,v175\n"
"v_med3_u32 v227,v226,v227,v175\n"
MF"v[140:143],v[200:203],a[68:71],v[140:143]\n"
RD"v[192:195],v244 offset:16384\n"
"v_max_u32 v226,v226,v175\n"
MF"v[148:151],v[200:203],a[72:75],v[148:151]\n"
PK"v40,v40,v41\n"
PK"v41,v42,v43\n"
MF"v[148:151],v[204:207],a[88:91],v[148:151]\n"
"v_min_u32 v254,48,v224\n"
PK"v42,v44,v45\n"
MF"v[156:159],v[204:207],a[92:95],v[156:159]\n"
WT"vmcnt(13)\n"
"v_med3_u32 v228,v227,v228,v176\n"
PK"v43,v46,v47\n"
MF"v[156:159],v[200:203],a[76:79],v[156:159]\n"
WT"lgkmcnt(1)\n"
RD"v[196:199],v245 offset:16384\n"
"v_lshlrev_b32 v254,3,v254\n"
MF"v[128:131],v[208:211],a[96:99],v[128:131]\n"
"buffer_load_dword v255,v254,s[8:11],s90 offen sc1\n"
MF"v[128:131],v[212:215],a[112:115],v[128:131]\n"
BS"v[8:11],v250,s[8:11],s49 offen offset:1024 sc1\n"
MF"v[136:139],v[212:215],a[116:119],v[136:139]\n"
"v_med3_u32 v227,v226,v227,v176\n"
"v_max_u32 v226,v226,v176\n"
MF"v[136:139],v[208:211],a[100:103],v[136:139]\n"
RD"v[200:203],v244 offset:18432\n"
"v_med3_u32 v228,v227,v228,v177\n"
MF"v[144:147],v[208:211],a[104:107],v[144:147]\n"
"v_med3_u32 v227,v226,v227,v177\n"
"v_max_u32 v226,v226,v177\n"
MF"v[144:147],v[212:215],a[120:123],v[144:147]\n"
"v_med3_u32 v228,v227,v228,v178\n"
PK"v48,v48,v49\n"
MF"v[152:155],v[212:215],a[124:127],v[152:155]\n"
RD"v[204:207],v245 offset:18432\n"
PK"v49,v50,v51\n"
MF"v[152:155],v[208:211],a[108:111],v[152:155]\n"
"v_med3_u32 v227,v226,v227,v178\n"
PK"v50,v52,v53\n"
MF"v[132:135],v[216:219],a[96:99],v[132:135]\n"
PK"v51,v54,v55\n"
"v_max_u32 v226,v226,v178\n"
MF"v[132:135],v[220:223],a[112:115],v[132:135]\n"
"v_med3_u32 v228,v227,v228,v179\n"
"v_med3_u32 v227,v226,v227,v179\n"
MF"v[140:143],v[220:223],a[116:119],v[140:143]\n"
RD"v[208:211],v244 offset:20480\n"
"v_max_u32 v226,v226,v179\n"
MF"v[140:143],v[216:219],a[100:103],v[140:143]\n"
WT"vmcnt(14)\n"
"v_med3_u32 v228,v227,v228,v180\n"
"v_med3_u32 v227,v226,v227,v180\n"
MF"v[148:151],v[216:219],a[104:107],v[148:151]\n"
PK"v56,v56,v57\n"
PK"v57,v58,v59\n"
MF"v[148:151],v[220:223],a[120:123],v[148:151]\n"
"v_max_u32 v226,v226,v180\n"
PK"v58,v60,v61\n"
MF"v[156:159],v[220:223],a[124:127],v[156:159]\n"
RD"v[212:215],v245 offset:20480\n"
PK"v59,v62,v63\n"
MF"v[156:159],v[216:219],a[108:111],v[156:159]\n"
WT"lgkmcnt(2)\n"
"v_med3_u32 v228,v227,v228,v181\n"
"v_med3_u32 v227,v226,v227,v181\n"
MF"v[128:131],v[192:195],a[128:131],v[128:131]\n"
"v_max_u32 v226,v226,v181\n"
"v_med3_u32 v228,v227,v228,v182\n"
MF"v[128:131],v[196:199],a[144:147],v[128:131]\n"
"v_med3_u32 v227,v226,v227,v182\n"
"v_max_u32 v226,v226,v182\n"
MF"v[136:139],v[196:199],a[148:151],v[136:139]\n"
RD"v[216:219],v244 offset:22528\n"
PK"v64,v64,v65\n"
MF"v[136:139],v[192:195],a[132:135],v[136:139]\n"
PK"v65,v66,v67\n"
"v_med3_u32 v228,v227,v228,v183\n"
MF"v[144:147],v[192:195],a[136:139],v[144:147]\n"
PK"v66,v68,v69\n"
PK"v67,v70,v71\n"
MF"v[144:147],v[196:199],a[152:155],v[144:147]\n"
RD"v[220:223],v245 offset:22528\n"
"v_med3_u32 v227,v226,v227,v183\n"
MF"v[152:155],v[196:199],a[156:159],v[152:155]\n"
"v_max_u32 v226,v226,v183\n"
PK"v72,v72,v73\n"
MF"v[152:155],v[192:195],a[140:143],v[152:155]\n"
WT"vmcnt(13)\n"
"v_med3_u32 v228,v227,v228,v184\n"
"v_med3_u32 v227,v226,v227,v184\n"
MF"v[132:135],v[200:203],a[128:131],v[132:135]\n"
"v_max_u32 v226,v226,v184\n"
"v_med3_u32 v228,v227,v228,v185\n"
MF"v[132:135],v[204:207],a[144:147],v[132:135]\n"
RD"v[192:195],v244 offset:40960\n"
PK"v73,v74,v75\n"
MF"v[140:143],v[204:207],a[148:151],v[140:143]\n"
"v_med3_u32 v227,v226,v227,v185\n"
"v_max_u32 v226,v226,v185\n"
MF"v[140:143],v[200:203],a[132:135],v[140:143]\n"
PK"v74,v76,v77\n"
PK"v75,v78,v79\n"
MF"v[148:151],v[200:203],a[136:139],v[148:151]\n"
"v_med3_u32 v228,v227,v228,v186\n"
"v_med3_u32 v227,v226,v227,v186\n"
MF"v[148:151],v[204:207],a[152:155],v[148:151]\n"
RD"v[196:199],v245 offset:40960\n"
"v_max_u32 v226,v226,v186\n"
MF"v[156:159],v[204:207],a[156:159],v[156:159]\n"
"v_med3_u32 v228,v227,v228,v187\n"
"v_med3_u32 v227,v226,v227,v187\n"
MF"v[156:159],v[200:203],a[140:143],v[156:159]\n"
WT"lgkmcnt(2)\n"
PK"v80,v80,v81\n"
PK"v81,v82,v83\n"
MF"v[128:131],v[208:211],a[160:163],v[128:131]\n"
BS"v[16:19],v250,s[8:11],s49 offen offset:2048 sc1\n"
MF"v[128:131],v[212:215],a[176:179],v[128:131]\n"
RD"v[200:203],v244 offset:43008\n"
"v_max_u32 v226,v226,v187\n"
MF"v[136:139],v[212:215],a[180:183],v[136:139]\n"
WT"vmcnt(13)\n"
"v_med3_u32 v228,v227,v228,v188\n"
PK"v82,v84,v85\n"
MF"v[136:139],v[208:211],a[164:167],v[136:139]\n"
PK"v83,v86,v87\n"
"v_med3_u32 v227,v226,v227,v188\n"
MF"v[144:147],v[208:211],a[168:171],v[144:147]\n"
"v_max_u32 v226,v226,v188\n"
"v_med3_u32 v228,v227,v228,v189\n"
MF"v[144:147],v[212:215],a[184:187],v[144:147]\n"
RD"v[204:207],v245 offset:43008\n"
"v_med3_u32 v227,v226,v227,v189\n"
MF"v[152:155],v[212:215],a[188:191],v[152:155]\n"
"v_max_u32 v226,v226,v189\n"
PK"v88,v88,v89\n"
MF"v[152:155],v[208:211],a[172:175],v[152:155]\n"
PK"v89,v90,v91\n"
"v_med3_u32 v228,v227,v228,v190\n"
MF"v[132:135],v[216:219],a[160:163],v[132:135]\n"
RD"v[208:211],v244 offset:45056\n"
"v_med3_u32 v227,v226,v227,v190\n"
MF"v[132:135],v[220:223],a[176:179],v[132:135]\n"
PK"v90,v92,v93\n"
PK"v91,v94,v95\n"
MF"v[140:143],v[220:223],a[180:183],v[140:143]\n"
"v_max_u32 v226,v226,v190\n"
"v_med3_u32 v228,v227,v228,v191\n"
MF"v[140:143],v[216:219],a[164:167],v[140:143]\n"
"v_med3_u32 v227,v226,v227,v191\n"
"v_max_u32 v226,v226,v191\n"
MF"v[148:151],v[216:219],a[168:171],v[148:151]\n"
RD"v[212:215],v245 offset:45056\n"
"v_mov_b32 v229,v226\n"
MF"v[148:151],v[220:223],a[184:187],v[148:151]\n"
MF"v[156:159],v[220:223],a[188:191],v[156:159]\n"
"v_max_u32_dpp v229,v229,v229 row_shr:1 row_mask:0xf bank_mask:0xf\n"
MF"v[156:159],v[216:219],a[172:175],v[156:159]\n"
WT"lgkmcnt(1)\n"
"v_max_u32_dpp v229,v229,v229 row_shr:2 row_mask:0xf bank_mask:0xf\n"
MF"v[128:131],v[192:195],a[192:195],v[128:131]\n"
RD"v[216:219],v244 offset:47104\n"
"v_max_u32_dpp v229,v229,v229 row_shr:4 row_mask:0xf bank_mask:0xf\n"
MF"v[128:131],v[196:199],a[208:211],v[128:131]\n"
MF"v[136:139],v[196:199],a[212:215],v[136:139]\n"
"v_max_u32_dpp v229,v229,v229 row_shr:8 row_mask:0xf bank_mask:0xf\n"
MF"v[136:139],v[192:195],a[196:199],v[136:139]\n"
MF"v[144:147],v[192:195],a[200:203],v[144:147]\n"
RD"v[220:223],v245 offset:47104\n"
"v_max_u32_dpp v229,v229,v229 row_bcast:15 row_mask:0xa bank_mask:0xf\n"
MF"v[144:147],v[196:199],a[216:219],v[144:147]\n"
MF"v[152:155],v[196:199],a[220:223],v[152:155]\n"
"v_max_u32_dpp v229,v229,v229 row_bcast:31 row_mask:0xc bank_mask:0xf\n"
MF"v[152:155],v[192:195],a[204:207],v[152:155]\n"
"v_readlane_b32 s62,v229,63\n"
MF"v[132:135],v[200:203],a[192:195],v[132:135]\n"
MF"v[132:135],v[204:207],a[208:211],v[132:135]\n"
"v_cmp_eq_u32 vcc,s62,v226\n"
"v_writelane_b32 v225,s62,0\n"
MF"v[140:143],v[204:207],a[212:215],v[140:143]\n"
"v_cndmask_b32 v226,v226,v227,vcc\n"
"v_cndmask_b32_e64 v227,v227,0,vcc\n"
MF"v[140:143],v[200:203],a[196:199],v[140:143]\n"
"v_mov_b32 v229,v226\n"
MF"v[148:151],v[200:203],a[200:203],v[148:151]\n"
WT"lgkmcnt(0)\n"
"v_max_u32_dpp v229,v229,v229 row_shr:1 row_mask:0xf bank_mask:0xf\n"
MF"v[148:151],v[204:207],a[216:219],v[148:151]\n"
"s_barrier\n"
MF"v[156:159],v[204:207],a[220:223],v[156:159]\n"
RD"v[192:195],v242 offset:0\n"
"v_max_u32_dpp v229,v229,v229 row_shr:2 row_mask:0xf bank_mask:0xf\n"
MF"v[156:159],v[200:203],a[204:207],v[156:159]\n"
BS"v[24:27],v250,s[8:11],s49 offen offset:3072 sc1\n"
MF"v[128:131],v[208:211],a[224:227],v[128:131]\n"
"v_max_u32_dpp v229,v229,v229 row_shr:4 row_mask:0xf bank_mask:0xf\n"
MF"v[128:131],v[212:215],a[240:243],v[128:131]\n"
MF"v[136:139],v[212:215],a[244:247],v[136:139]\n"
RD"v[196:199],v243 offset:0\n"
"v_max_u32_dpp v229,v229,v229 row_shr:8 row_mask:0xf bank_mask:0xf\n"
MF"v[136:139],v[208:211],a[228:231],v[136:139]\n"
MF"v[144:147],v[208:211],a[232:235],v[144:147]\n"
"v_max_u32_dpp v229,v229,v229 row_bcast:15 row_mask:0xa bank_mask:0xf\n"
MF"v[144:147],v[212:215],a[248:251],v[144:147]\n"
MF"v[152:155],v[212:215],a[252:255],v[152:155]\n"
RD"v[200:203],v242 offset:2048\n"
"v_max_u32_dpp v229,v229,v229 row_bcast:31 row_mask:0xc bank_mask:0xf\n"
MF"v[152:155],v[208:211],a[236:239],v[152:155]\n"
"v_readlane_b32 s62,v229,63\n"
MF"v[132:135],v[216:219],a[224:227],v[132:135]\n"
MF"v[132:135],v[220:223],a[240:243],v[132:135]\n"
"v_cmp_eq_u32 vcc,s62,v226\n"
"v_writelane_b32 v225,s62,1\n"
MF"v[140:143],v[220:223],a[244:247],v[140:143]\n"
RD"v[204:207],v243 offset:2048\n"
"v_cndmask_b32 v226,v226,v227,vcc\n"
MF"v[140:143],v[216:219],a[228:231],v[140:143]\n"
"v_cndmask_b32_e64 v227,v227,0,vcc\n"
"v_mov_b32 v229,v226\n"
MF"v[148:151],v[216:219],a[232:235],v[148:151]\n"
MF"v[148:151],v[220:223],a[248:251],v[148:151]\n"
"v_max_u32_dpp v229,v229,v229 row_shr:1 row_mask:0xf bank_mask:0xf\n"
MF"v[156:159],v[220:223],a[252:255],v[156:159]\n"
RD"v[208:211],v242 offset:4096\n"
"v_max_u32_dpp v229,v229,v229 row_shr:2 row_mask:0xf bank_mask:0xf\n"
MF"v[156:159],v[216:219],a[236:239],v[156:159]\n"
WT"lgkmcnt(2)\n"
"v_max_u32_dpp v229,v229,v229 row_shr:4 row_mask:0xf bank_mask:0xf\n"
MF"v[36:39],v[192:195],a[0:3],0\n"
MF"v[36:39],v[196:199],a[16:19],v[36:39]\n"
RD"v[212:215],v243 offset:4096\n"
"v_max_u32_dpp v229,v229,v229 row_shr:8 row_mask:0xf bank_mask:0xf\n"
MF"v[44:47],v[196:199],a[20:23],0\n"
MF"v[44:47],v[192:195],a[4:7],v[44:47]\n"
"v_max_u32_dpp v229,v229,v229 row_bcast:15 row_mask:0xa bank_mask:0xf\n"
PK"v4,v128,v129\n"
MF"v[52:55],v[192:195],a[8:11],0\n"
"v_max_u32_dpp v229,v229,v229 row_bcast:31 row_mask:0xc bank_mask:0xf\n"
PK"v5,v130,v131\n"
MF"v[52:55],v[196:199],a[24:27],v[52:55]\n"
RD"v[216:219],v242 offset:6144\n"
"v_readlane_b32 s62,v229,63\n"
MF"v[60:63],v[196:199],a[28:31],0\n"
PK"v6,v132,v133\n"
"v_cmp_eq_u32 vcc,s62,v226\n"
MF"v[60:63],v[192:195],a[12:15],v[60:63]\n"
"v_writelane_b32 v225,s62,2\n"
"v_cndmask_b32 v226,v226,v227,vcc\n"
MF"v[68:71],v[200:203],a[0:3],0\n"
WT"lgkmcnt(1)\n"
"v_cndmask_b32_e64 v227,v227,0,vcc\n"
"v_mov_b32 v229,v226\n"
MF"v[68:71],v[204:207],a[16:19],v[68:71]\n"
RD"v[220:223],v243 offset:6144\n"
"v_max_u32_dpp v229,v229,v229 row_shr:1 row_mask:0xf bank_mask:0xf\n"
MF"v[76:79],v[204:207],a[20:23],0\n"
PK"v7,v134,v135\n"
"v_max_u32_dpp v229,v229,v229 row_shr:2 row_mask:0xf bank_mask:0xf\n"
MF"v[76:79],v[200:203],a[4:7],v[76:79]\n"
PK"v12,v136,v137\n"
"v_max_u32_dpp v229,v229,v229 row_shr:4 row_mask:0xf bank_mask:0xf\n"
MF"v[84:87],v[200:203],a[8:11],0\n"
"s_mov_b32 s49,8192\n"
"v_max_u32_dpp v229,v229,v229 row_shr:8 row_mask:0xf bank_mask:0xf\n"
PK"v13,v138,v139\n"
MF"v[84:87],v[204:207],a[24:27],v[84:87]\n"
RD"v[192:195],v242 offset:24576\n"
"v_max_u32_dpp v229,v229,v229 row_bcast:15 row_mask:0xa bank_mask:0xf\n"
MF"v[92:95],v[204:207],a[28:31],0\n"
BS"v[32:35],v250,s[8:11],s49 offen offset:0 sc1\n"
MF"v[92:95],v[200:203],a[12:15],v[92:95]\n"
"v_max_u32_dpp v229,v229,v229 row_bcast:31 row_mask:0xc bank_mask:0xf\n"
PK"v14,v140,v141\n"
MF"v[36:39],v[208:211],a[32:35],v[36:39]\n"
"v_readlane_b32 s62,v229,63\n"
PK"v15,v142,v143\n"
MF"v[36:39],v[212:215],a[48:51],v[36:39]\n"
RD"v[196:199],v243 offset:24576\n"
"v_cmp_eq_u32 vcc,s62,v226\n"
MF"v[44:47],v[212:215],a[52:55],v[44:47]\n"
"v_writelane_b32 v225,s62,3\n"
"v_cndmask_b32 v226,v226,v227,vcc\n"
MF"v[44:47],v[208:211],a[36:39],v[44:47]\n"
"v_cndmask_b32_e64 v227,v227,0,vcc\n"
"v_mov_b32 v229,v226\n"
MF"v[52:55],v[208:211],a[40:43],v[52:55]\n"
RD"v[200:203],v242 offset:26624\n"
"v_max_u32_dpp v229,v229,v229 row_shr:1 row_mask:0xf bank_mask:0xf\n"
MF"v[52:55],v[212:215],a[56:59],v[52:55]\n"
PK"v20,v144,v145\n"
"v_max_u32_dpp v229,v229,v229 row_shr:2 row_mask:0xf bank_mask:0xf\n"
MF"v[60:63],v[212:215],a[60:63],v[60:63]\n"
PK"v21,v146,v147\n"
"v_max_u32_dpp v229,v229,v229 row_shr:4 row_mask:0xf bank_mask:0xf\n"
MF"v[60:63],v[208:211],a[44:47],v[60:63]\n"
WT"lgkmcnt(2)\n"
"v_max_u32_dpp v229,v229,v229 row_shr:8 row_mask:0xf bank_mask:0xf\n"
PK"v22,v148,v149\n"
MF"v[68:71],v[216:219],a[32:35],v[68:71]\n"
RD"v[204:207],v243 offset:26624\n"
"v_max_u32_dpp v229,v229,v229 row_bcast:15 row_mask:0xa bank_mask:0xf\n"
MF"v[68:71],v[220:223],a[48:51],v[68:71]\n"
PK"v23,v150,v151\n"
"v_max_u32_dpp v229,v229,v229 row_bcast:31 row_mask:0xc bank_mask:0xf\n"
MF"v[76:79],v[220:223],a[52:55],v[76:79]\n"
"v_readlane_b32 s62,v229,63\n"
PK"v28,v152,v153\n"
MF"v[76:79],v[216:219],a[36:39],v[76:79]\n"
"v_cmp_eq_u32 vcc,s62,v226\n"
"v_writelane_b32 v225,s62,4\n"
MF"v[84:87],v[216:219],a[40:43],v[84:87]\n"
RD"v[208:211],v242 offset:28672\n"
"v_cndmask_b32 v226,v226,v227,vcc\n"
MF"v[84:87],v[220:223],a[56:59],v[84:87]\n"
"v_cndmask_b32_e64 v227,v227,0,vcc\n"
"v_mov_b32 v229,v226\n"
MF"v[92:95],v[220:223],a[60:63],v[92:95]\n"
PK"v29,v154,v155\n"
"v_max_u32_dpp v229,v229,v229 row_shr:1 row_mask:0xf bank_mask:0xf\n"
MF"v[92:95],v[216:219],a[44:47],v[92:95]\n"
PK"v30,v156,v157\n"
"v_max_u32_dpp v229,v229,v229 row_shr:2 row_mask:0xf bank_mask:0xf\n"
MF"v[36:39],v[192:195],a[64:67],v[36:39]\n"
WT"lgkmcnt(1)\n"
RD"v[212:215],v243 offset:28672\n"
"v_max_u32_dpp v229,v229,v229 row_shr:4 row_mask:0xf bank_mask:0xf\n"
MF"v[36:39],v[196:199],a[80:83],v[36:39]\n"
PK"v31,v158,v159\n"
"v_max_u32_dpp v229,v229,v229 row_shr:8 row_mask:0xf bank_mask:0xf\n"
MF"v[44:47],v[196:199],a[84:87],v[44:47]\n"
MF"v[44:47],v[192:195],a[68:71],v[44:47]\n"
RD"v[216:219],v242 offset:30720\n"
"v_max_u32_dpp v229,v229,v229 row_bcast:15 row_mask:0xa bank_mask:0xf\n"
MF"v[52:55],v[192:195],a[72:75],v[52:55]\n"
MF"v[52:55],v[196:199],a[88:91],v[52:55]\n"
"v_max_u32_dpp v229,v229,v229 row_bcast:31 row_mask:0xc bank_mask:0xf\n"
MF"v[60:63],v[196:199],a[92:95],v[60:63]\n"
"v_readlane_b32 s62,v229,63\n"
MF"v[60:63],v[192:195],a[76:79],v[60:63]\n"
RD"v[220:223],v243 offset:30720\n"
"v_cmp_eq_u32 vcc,s62,v226\n"
MF"v[68:71],v[200:203],a[64:67],v[68:71]\n"
"v_writelane_b32 v225,s62,5\n"
"v_cndmask_b32 v226,v226,v227,vcc\n"
MF"v[68:71],v[204:207],a[80:83],v[68:71]\n"
"v_cndmask_b32_e64 v227,v227,0,vcc\n"
"v_mov_b32 v229,v226\n"
MF"v[76:79],v[204:207],a[84:87],v[76:79]\n"
MF"v[76:79],v[200:203],a[68:71],v[76:79]\n"
RD"v[192:195],v244 offset:0\n"
"v_max_u32_dpp v229,v229,v229 row_shr:1 row_mask:0xf bank_mask:0xf\n"
MF"v[84:87],v[200:203],a[72:75],v[84:87]\n"
MF"v[84:87],v[204:207],a[88:91],v[84:87]\n"
BS"v[40:43],v250,s[8:11],s49 offen offset:1024 sc1\n"
MF"v[92:95],v[204:207],a[92:95],v[92:95]\n"
"v_max_u32_dpp v229,v229,v229 row_shr:2 row_mask:0xf bank_mask:0xf\n"
MF"v[92:95],v[200:203],a[76:79],v[92:95]\n"
WT"lgkmcnt(1)\n"
RD"v[196:199],v245 offset:0\n"
"v_max_u32_dpp v229,v229,v229 row_shr:4 row_mask:0xf bank_mask:0xf\n"
MF"v[36:39],v[208:211],a[96:99],v[36:39]\n"
MF"v[36:39],v[212:215],a[112:115],v[36:39]\n"
"v_max_u32_dpp v229,v229,v229 row_shr:8 row_mask:0xf bank_mask:0xf\n"
MF"v[44:47],v[212:215],a[116:119],v[44:47]\n"
MF"v[44:47],v[208:211],a[100:103],v[44:47]\n"
RD"v[200:203],v244 offset:2048\n"
"v_max_u32_dpp v229,v229,v229 row_bcast:15 row_mask:0xa bank_mask:0xf\n"
MF"v[52:55],v[208:211],a[104:107],v[52:55]\n"
MF"v[52:55],v[212:215],a[120:123],v[52:55]\n"
"v_max_u32_dpp v229,v229,v229 row_bcast:31 row_mask:0xc bank_mask:0xf\n"
MF"v[60:63],v[212:215],a[124:127],v[60:63]\n"
RD"v[204:207],v245 offset:2048\n"
"v_readlane_b32 s62,v229,63\n"
MF"v[60:63],v[208:211],a[108:111],v[60:63]\n"
MF"v[68:71],v[216:219],a[96:99],v[68:71]\n"
"v_cmp_eq_u32 vcc,s62,v226\n"
"v_writelane_b32 v225,s62,6\n"
MF"v[68:71],v[220:223],a[112:115],v[68:71]\n"
"v_cndmask_b32 v226,v226,v227,vcc\n"
"v_cndmask_b32_e64 v227,v227,0,vcc\n"
MF"v[76:79],v[220:223],a[116:119],v[76:79]\n"
SA"m0,s37,16384\n"
RD"v[208:211],v244 offset:4096\n"
"v_mov_b32 v229,v226\n"
MF"v[76:79],v[216:219],a[100:103],v[76:79]\n"
SA"s48,s39,98304\n"
"v_max_u32_dpp v229,v229,v229 row_shr:1 row_mask:0xf bank_mask:0xf\n"
MF"v[84:87],v[216:219],a[104:107],v[84:87]\n"
BL"v234,s[12:15],s48 offen offset:0 lds\n"
MF"v[84:87],v[220:223],a[120:123],v[84:87]\n"
"v_max_u32_dpp v229,v229,v229 row_shr:2 row_mask:0xf bank_mask:0xf\n"
PK"v96,v96,v97\n"
MF"v[92:95],v[220:223],a[124:127],v[92:95]\n"
RD"v[212:215],v245 offset:4096\n"
"v_max_u32_dpp v229,v229,v229 row_shr:4 row_mask:0xf bank_mask:0xf\n"
MF"v[92:95],v[216:219],a[108:111],v[92:95]\n"
WT"lgkmcnt(2)\n"
"v_max_u32_dpp v229,v229,v229 row_shr:8 row_mask:0xf bank_mask:0xf\n"
PK"v97,v98,v99\n"
MF"v[36:39],v[192:195],a[128:131],v[36:39]\n"
"v_max_u32_dpp v229,v229,v229 row_bcast:15 row_mask:0xa bank_mask:0xf\n"
PK"v98,v100,v101\n"
MF"v[36:39],v[196:199],a[144:147],v[36:39]\n"
"v_max_u32_dpp v229,v229,v229 row_bcast:31 row_mask:0xc bank_mask:0xf\n"
PK"v99,v102,v103\n"
MF"v[44:47],v[196:199],a[148:151],v[44:47]\n"
RD"v[216:219],v244 offset:6144\n"
"v_readlane_b32 s62,v229,63\n"
MF"v[44:47],v[192:195],a[132:135],v[44:47]\n"
BL"v235,s[12:15],s48 offen offset:1024 lds\n"
MF"v[52:55],v[192:195],a[136:139],v[52:55]\n"
"v_cmp_eq_u32 vcc,s62,v226\n"
"v_writelane_b32 v225,s62,7\n"
MF"v[52:55],v[196:199],a[152:155],v[52:55]\n"
RD"v[220:223],v245 offset:6144\n"
"v_cndmask_b32 v226,v226,v227,vcc\n"
MF"v[60:63],v[196:199],a[156:159],v[60:63]\n"
"v_cndmask_b32_e64 v227,v227,0,vcc\n"
"v_mov_b32 v229,v228\n"
MF"v[60:63],v[192:195],a[140:143],v[60:63]\n"
PK"v104,v104,v105\n"
"v_max_u32_dpp v229,v229,v229 row_shr:1 row_mask:0xf bank_mask:0xf\n"
MF"v[68:71],v[200:203],a[128:131],v[68:71]\n"
BL"v236,s[12:15],s48 offen offset:2048 lds\n"
MF"v[68:71],v[204:207],a[144:147],v[68:71]\n"
RD"v[192:195],v244 offset:24576\n"
"v_max_u32_dpp v229,v229,v229 row_shr:2 row_mask:0xf bank_mask:0xf\n"
MF"v[76:79],v[204:207],a[148:151],v[76:79]\n"
PK"v105,v106,v107\n"
"v_max_u32_dpp v229,v229,v229 row_shr:4 row_mask:0xf bank_mask:0xf\n"
MF"v[76:79],v[200:203],a[132:135],v[76:79]\n"
PK"v106,v108,v109\n"
"v_max_u32_dpp v229,v229,v229 row_shr:8 row_mask:0xf bank_mask:0xf\n"
MF"v[84:87],v[200:203],a[136:139],v[84:87]\n"
BS"v[48:51],v250,s[8:11],s49 offen offset:2048 sc1\n"
MF"v[84:87],v[204:207],a[152:155],v[84:87]\n"
RD"v[196:199],v245 offset:24576\n"
"v_max_u32_dpp v229,v229,v229 row_bcast:15 row_mask:0xa bank_mask:0xf\n"
MF"v[92:95],v[204:207],a[156:159],v[92:95]\n"
BL"v237,s[12:15],s48 offen offset:3072 lds\n"
MF"v[92:95],v[200:203],a[140:143],v[92:95]\n"
WT"lgkmcnt(2)\n"
"v_max_u32_dpp v229,v229,v229 row_bcast:31 row_mask:0xc bank_mask:0xf\n"
PK"v107,v110,v111\n"
MF"v[36:39],v[208:211],a[160:163],v[36:39]\n"
"v_readlane_b32 s63,v229,63\n"
PK"v112,v112,v113\n"
MF"v[36:39],v[212:215],a[176:179],v[36:39]\n"
"s_cmp_gt_u32 s63,s62\n"
"s_cbranch_scc1 L1007_%=\n"
"L1008_%=:\n"
MF"v[44:47],v[212:215],a[180:183],v[44:47]\n"
SA"m0,s37,20480\n"
RD"v[200:203],v244 offset:26624\n"
"v_lshrrev_b32 v229,2,v224\n"
MF"v[44:47],v[208:211],a[164:167],v[44:47]\n"
SA"s48,s98,98304\n"
"ds_write_b32 v229,v225 offset:16384\n"
MF"v[52:55],v[208:211],a[168:171],v[52:55]\n"
BL"v234,s[12:15],s48 offen offset:0 lds\n"
MF"v[52:55],v[212:215],a[184:187],v[52:55]\n"
RD"v[204:207],v245 offset:26624\n"
PK"v113,v114,v115\n"
MF"v[60:63],v[212:215],a[188:191],v[60:63]\n"
PK"v114,v116,v117\n"
PK"v115,v118,v119\n"
MF"v[60:63],v[208:211],a[172:175],v[60:63]\n"
PK"v120,v120,v121\n"
PK"v121,v122,v123\n"
MF"v[68:71],v[216:219],a[160:163],v[68:71]\n"
RD"v[208:211],v244 offset:28672\n"
PK"v122,v124,v125\n"
MF"v[68:71],v[220:223],a[176:179],v[68:71]\n"
PK"v123,v126,v127\n"
MF"v[76:79],v[220:223],a[180:183],v[76:79]\n"
BL"v235,s[12:15],s48 offen offset:1024 lds\n"
MF"v[76:79],v[216:219],a[164:167],v[76:79]\n"
MF"v[84:87],v[216:219],a[168:171],v[84:87]\n"
RD"v[212:215],v245 offset:28672\n"
MF"v[84:87],v[220:223],a[184:187],v[84:87]\n"
MF"v[92:95],v[220:223],a[188:191],v[92:95]\n"
MF"v[92:95],v[216:219],a[172:175],v[92:95]\n"
WT"lgkmcnt(1)\n"
MF"v[36:39],v[192:195],a[192:195],v[36:39]\n"
RD"v[216:219],v244 offset:30720\n"
MF"v[36:39],v[196:199],a[208:211],v[36:39]\n"
BL"v236,s[12:15],s48 offen offset:2048 lds\n"
MF"v[44:47],v[196:199],a[212:215],v[44:47]\n"
MF"v[44:47],v[192:195],a[196:199],v[44:47]\n"
MF"v[52:55],v[192:195],a[200:203],v[52:55]\n"
RD"v[220:223],v245 offset:30720\n"
MF"v[52:55],v[196:199],a[216:219],v[52:55]\n"
MF"v[60:63],v[196:199],a[220:223],v[60:63]\n"
BL"v237,s[12:15],s48 offen offset:3072 lds\n"
MF"v[60:63],v[192:195],a[204:207],v[60:63]\n"
MF"v[68:71],v[200:203],a[192:195],v[68:71]\n"
MF"v[68:71],v[204:207],a[208:211],v[68:71]\n"
MF"v[76:79],v[204:207],a[212:215],v[76:79]\n"
MF"v[76:79],v[200:203],a[196:199],v[76:79]\n"
BS"v[56:59],v250,s[8:11],s49 offen offset:3072 sc1\n"
MF"v[84:87],v[200:203],a[200:203],v[84:87]\n"
WT"lgkmcnt(0)\n"
"v_lshrrev_b32 v251,2,v224\n"
"v_and_b32 v251,28,v251\n"
MF"v[84:87],v[204:207],a[216:219],v[84:87]\n"
"s_barrier\n"
MF"v[92:95],v[204:207],a[220:223],v[92:95]\n"
RD"v[192:195],v242 offset:8192\n"
MF"v[92:95],v[200:203],a[204:207],v[92:95]\n"
"ds_read_b32 v234,v251 offset:16384\n"
MF"v[36:39],v[208:211],a[224:227],v[36:39]\n"
MF"v[36:39],v[212:215],a[240:243],v[36:39]\n"
MF"v[44:47],v[212:215],a[244:247],v[44:47]\n"
RD"v[196:199],v243 offset:8192\n"
MF"v[44:47],v[208:211],a[228:231],v[44:47]\n"
MF"v[52:55],v[208:211],a[232:235],v[52:55]\n"
MF"v[52:55],v[212:215],a[248:251],v[52:55]\n"
MF"v[60:63],v[212:215],a[252:255],v[60:63]\n"
RD"v[200:203],v242 offset:10240\n"
MF"v[60:63],v[208:211],a[236:239],v[60:63]\n"
WT"lgkmcnt(2)\n"
"v_not_b32 v251,v234\n"
"v_bfe_u32 v252,v251,4,7\n"
MF"v[68:71],v[216:219],a[224:227],v[68:71]\n"
"v_mul_u32_u24 v252,20480,v252\n"
MF"v[68:71],v[220:223],a[240:243],v[68:71]\n"
"v_and_b32 v251,15,v251\n"
MF"v[76:79],v[220:223],a[244:247],v[76:79]\n"
RD"v[204:207],v243 offset:10240\n"
"v_lshl_add_u32 v251,v251,4,v252\n"
MF"v[76:79],v[216:219],a[228:231],v[76:79]\n"
"v_and_b32 v252,0x380,v224\n"
MF"v[84:87],v[216:219],a[232:235],v[84:87]\n"
"v_lshl_add_u32 v251,v252,1,v251\n"
MF"v[84:87],v[220:223],a[248:251],v[84:87]\n"
"v_add_u32 v252,v252,v224\n"
MF"v[92:95],v[220:223],a[252:255],v[92:95]\n"
"s_xor_b32 s92,s34,0\n"
RD"v[208:211],v242 offset:12288\n"
MF"v[92:95],v[216:219],a[236:239],v[92:95]\n"
"s_lshl_b32 s92,s92,2\n"
MF"v[128:131],v[192:195],a[0:3],0\n"
WT"lgkmcnt(2)\n"
SA"s92,s92,s30\n"
MF"v[128:131],v[196:199],a[16:19],v[128:131]\n"
"s_lshl_b32 s93,s92,11\n"
RD"v[212:215],v243 offset:12288\n"
MF"v[136:139],v[196:199],a[20:23],0\n"
"s_cmp_lt_u32 s92,10\n"
"s_cselect_b32 s93,s93,2130706432\n"
MF"v[136:139],v[192:195],a[4:7],v[136:139]\n"
SA"s92,s93,524288\n"
PK"v36,v36,v37\n"
PK"v37,v38,v39\n"
MF"v[144:147],v[192:195],a[8:11],0\n"
BL"v[100:103],v251,s[8:11],s92 offen sc1\n"
MF"v[144:147],v[196:199],a[24:27],v[144:147]\n"
"s_mul_i32 s92,s35,20480\n"
RD"v[216:219],v242 offset:14336\n"
PK"v38,v68,v69\n"
MF"v[152:155],v[196:199],a[28:31],0\n"
SA"s92,s92,s93\n"
PK"v39,v70,v71\n"
MF"v[152:155],v[192:195],a[12:15],v[152:155]\n"
SA"s92,s92,3145728\n"
MF"v[132:135],v[200:203],a[0:3],0\n"
WT"lgkmcnt(1)\n"
PK"v44,v44,v45\n"
PK"v45,v46,v47\n"
MF"v[132:135],v[204:207],a[16:19],v[132:135]\n"
"s_mov_b32 s49,16384\n"
RD"v[220:223],v243 offset:14336\n"
PK"v46,v76,v77\n"
MF"v[140:143],v[204:207],a[20:23],0\n"
BS"v[64:67],v250,s[8:11],s49 offen offset:0 sc1\n"
MF"v[140:143],v[200:203],a[4:7],v[140:143]\n"
PK"v47,v78,v79\n"
MF"v[148:151],v[200:203],a[8:11],0\n"
MF"v[148:151],v[204:207],a[24:27],v[148:151]\n"
RD"v[192:195],v242 offset:32768\n"
PK"v52,v52,v53\n"
MF"v[156:159],v[204:207],a[28:31],0\n"
PK"v53,v54,v55\n"
PK"v54,v84,v85\n"
MF"v[156:159],v[200:203],a[12:15],v[156:159]\n"
PK"v55,v86,v87\n"
MF"v[128:131],v[208:211],a[32:35],v[128:131]\n"
MF"v[128:131],v[212:215],a[48:51],v[128:131]\n"
RD"v[196:199],v243 offset:32768\n"
MF"v[136:139],v[212:215],a[52:55],v[136:139]\n"
PK"v60,v60,v61\n"
PK"v61,v62,v63\n"
MF"v[136:139],v[208:211],a[36:39],v[136:139]\n"
PK"v62,v92,v93\n"
PK"v63,v94,v95\n"
MF"v[144:147],v[208:211],a[40:43],v[144:147]\n"
SA"m0,s36,0\n"
RD"v[200:203],v242 offset:34816\n"
SA"s48,s60,s61\n"
MF"v[144:147],v[212:215],a[56:59],v[144:147]\n"
MF"v[152:155],v[212:215],a[60:63],v[152:155]\n"
BL"v238,s[20:23],s48 offen offset:0 lds\n"
MF"v[152:155],v[208:211],a[44:47],v[152:155]\n"
WT"lgkmcnt(2)\n"
MF"v[132:135],v[216:219],a[32:35],v[132:135]\n"
RD"v[204:207],v243 offset:34816\n"
MF"v[132:135],v[220:223],a[48:51],v[132:135]\n"
MF"v[140:143],v[220:223],a[52:55],v[140:143]\n"
MF"v[140:143],v[216:219],a[36:39],v[140:143]\n"
MF"v[148:151],v[216:219],a[40:43],v[148:151]\n"
RD"v[208:211],v242 offset:36864\n"
MF"v[148:151],v[220:223],a[56:59],v[148:151]\n"
MF"v[156:159],v[220:223],a[60:63],v[156:159]\n"
BL"v239,s[20:23],s48 offen offset:1024 lds\n"
MF"v[156:159],v[216:219],a[44:47],v[156:159]\n"
MF"v[128:131],v[192:195],a[64:67],v[128:131]\n"
WT"lgkmcnt(1)\n"
RD"v[212:215],v243 offset:36864\n"
MF"v[128:131],v[196:199],a[80:83],v[128:131]\n"
MF"v[136:139],v[196:199],a[84:87],v[136:139]\n"
MF"v[136:139],v[192:195],a[68:71],v[136:139]\n"
RD"v[216:219],v242 offset:38912\n"
MF"v[144:147],v[192:195],a[72:75],v[144:147]\n"
MF"v[144:147],v[196:199],a[88:91],v[144:147]\n"
MF"v[152:155],v[196:199],a[92:95],v[152:155]\n"
BL"v240,s[20:23],s48 offen offset:2048 lds\n"
MF"v[152:155],v[192:195],a[76:79],v[152:155]\n"
RD"v[220:223],v243 offset:38912\n"
MF"v[132:135],v[200:203],a[64:67],v[132:135]\n"
MF"v[132:135],v[204:207],a[80:83],v[132:135]\n"
BS"v[72:75],v250,s[8:11],s49 offen offset:1024 sc1\n"
MF"v[140:143],v[204:207],a[84:87],v[140:143]\n"
MF"v[140:143],v[200:203],a[68:71],v[140:143]\n"
RD"v[192:195],v244 offset:8192\n"
MF"v[148:151],v[200:203],a[72:75],v[148:151]\n"
MF"v[148:151],v[204:207],a[88:91],v[148:151]\n"
MF"v[156:159],v[204:207],a[92:95],v[156:159]\n"
SA"s61,s61,524288\n"
BL"v241,s[20:23],s48 offen offset:3072 lds\n"
MF"v[156:159],v[200:203],a[76:79],v[156:159]\n"
WT"lgkmcnt(1)\n"
RD"v[196:199],v245 offset:8192\n"
MF"v[128:131],v[208:211],a[96:99],v[128:131]\n"
MF"v[128:131],v[212:215],a[112:115],v[128:131]\n"
MF"v[136:139],v[212:215],a[116:119],v[136:139]\n"
MF"v[136:139],v[208:211],a[100:103],v[136:139]\n"
RD"v[200:203],v244 offset:10240\n"
MF"v[144:147],v[208:211],a[104:107],v[144:147]\n"
MF"v[144:147],v[212:215],a[120:123],v[144:147]\n"
MF"v[152:155],v[212:215],a[124:127],v[152:155]\n"
RD"v[204:207],v245 offset:10240\n"
MF"v[152:155],v[208:211],a[108:111],v[152:155]\n"
MF"v[132:135],v[216:219],a[96:99],v[132:135]\n"
MF"v[132:135],v[220:223],a[112:115],v[132:135]\n"
MF"v[140:143],v[220:223],a[116:119],v[140:143]\n"
RD"v[208:211],v244 offset:12288\n"
MF"v[140:143],v[216:219],a[100:103],v[140:143]\n"
MF"v[148:151],v[216:219],a[104:107],v[148:151]\n"
SA"m0,s38,65536\n"
SA"s48,s60,s61\n"
MF"v[148:151],v[220:223],a[120:123],v[148:151]\n"
MF"v[156:159],v[220:223],a[124:127],v[156:159]\n"
RD"v[212:215],v245 offset:12288\n"
MF"v[156:159],v[216:219],a[108:111],v[156:159]\n"
WT"lgkmcnt(2)\n"
BL"v238,s[20:23],s48 offen offset:0 lds\n"
MF"v[128:131],v[192:195],a[128:131],v[128:131]\n"
MF"v[128:131],v[196:199],a[144:147],v[128:131]\n"
MF"v[136:139],v[196:199],a[148:151],v[136:139]\n"
RD"v[216:219],v244 offset:14336\n"
MF"v[136:139],v[192:195],a[132:135],v[136:139]\n"
MF"v[144:147],v[192:195],a[136:139],v[144:147]\n"
MF"v[144:147],v[196:199],a[152:155],v[144:147]\n"
RD"v[220:223],v245 offset:14336\n"
MF"v[152:155],v[196:199],a[156:159],v[152:155]\n"
BL"v239,s[20:23],s48 offen offset:1024 lds\n"
MF"v[152:155],v[192:195],a[140:143],v[152:155]\n"
MF"v[132:135],v[200:203],a[128:131],v[132:135]\n"
BS"v[80:83],v250,s[8:11],s49 offen offset:2048 sc1\n"
MF"v[132:135],v[204:207],a[144:147],v[132:135]\n"
RD"v[192:195],v244 offset:32768\n"
MF"v[140:143],v[204:207],a[148:151],v[140:143]\n"
MF"v[140:143],v[200:203],a[132:135],v[140:143]\n"
MF"v[148:151],v[200:203],a[136:139],v[148:151]\n"
MF"v[148:151],v[204:207],a[152:155],v[148:151]\n"
RD"v[196:199],v245 offset:32768\n"
MF"v[156:159],v[204:207],a[156:159],v[156:159]\n"
BL"v240,s[20:23],s48 offen offset:2048 lds\n"
MF"v[156:159],v[200:203],a[140:143],v[156:159]\n"
WT"lgkmcnt(2)\n"
MF"v[128:131],v[208:211],a[160:163],v[128:131]\n"
MF"v[128:131],v[212:215],a[176:179],v[128:131]\n"
RD"v[200:203],v244 offset:34816\n"
MF"v[136:139],v[212:215],a[180:183],v[136:139]\n"
MF"v[136:139],v[208:211],a[164:167],v[136:139]\n"
MF"v[144:147],v[208:211],a[168:171],v[144:147]\n"
MF"v[144:147],v[212:215],a[184:187],v[144:147]\n"
RD"v[204:207],v245 offset:34816\n"
MF"v[152:155],v[212:215],a[188:191],v[152:155]\n"
SA"s61,s61,524288\n"
BL"v241,s[20:23],s48 offen offset:3072 lds\n"
MF"v[152:155],v[208:211],a[172:175],v[152:155]\n"
"s_cmp_eq_u32 s61,5242880\n"
"s_cselect_b32 s61,0,s61\n"
MF"v[132:135],v[216:219],a[160:163],v[132:135]\n"
RD"v[208:211],v244 offset:36864\n"
MF"v[132:135],v[220:223],a[176:179],v[132:135]\n"
MF"v[140:143],v[220:223],a[180:183],v[140:143]\n"
MF"v[140:143],v[216:219],a[164:167],v[140:143]\n"
MF"v[148:151],v[216:219],a[168:171],v[148:151]\n"
SA"m0,s38,114688\n"
RD"v[212:215],v245 offset:36864\n"
SA"s48,s60,s61\n"
MF"v[148:151],v[220:223],a[184:187],v[148:151]\n"
MF"v[156:159],v[220:223],a[188:191],v[156:159]\n"
BL"v238,s[20:23],s48 offen offset:0 lds\n"
MF"v[156:159],v[216:219],a[172:175],v[156:159]\n"
WT"lgkmcnt(1)\n"
MF"v[128:131],v[192:195],a[192:195],v[128:131]\n"
RD"v[216:219],v244 offset:38912\n"
MF"v[128:131],v[196:199],a[208:211],v[128:131]\n"
MF"v[136:139],v[196:199],a[212:215],v[136:139]\n"
MF"v[136:139],v[192:195],a[196:199],v[136:139]\n"
MF"v[144:147],v[192:195],a[200:203],v[144:147]\n"
RD"v[220:223],v245 offset:38912\n"
MF"v[144:147],v[196:199],a[216:219],v[144:147]\n"
MF"v[152:155],v[196:199],a[220:223],v[152:155]\n"
BL"v239,s[20:23],s48 offen offset:1024 lds\n"
MF"v[152:155],v[192:195],a[204:207],v[152:155]\n"
BS"v[88:91],v250,s[8:11],s49 offen offset:3072 sc1\n"
MF"v[132:135],v[200:203],a[192:195],v[132:135]\n"
WT"vmcnt(14)\n"
BS"v[100:103],v252,s[8:11],s92 offen sc1\n"
MF"v[132:135],v[204:207],a[208:211],v[132:135]\n"
MF"v[140:143],v[204:207],a[212:215],v[140:143]\n"
MF"v[140:143],v[200:203],a[196:199],v[140:143]\n"
MF"v[148:151],v[200:203],a[200:203],v[148:151]\n"
WT"lgkmcnt(0)\n"
MF"v[148:151],v[204:207],a[216:219],v[148:151]\n"
"s_barrier\n"
MF"v[156:159],v[204:207],a[220:223],v[156:159]\n"
RD"v[192:195],v242 offset:16384\n"
MF"v[156:159],v[200:203],a[204:207],v[156:159]\n"
BL"v240,s[20:23],s48 offen offset:2048 lds\n"
MF"v[128:131],v[208:211],a[224:227],v[128:131]\n"
MF"v[128:131],v[212:215],a[240:243],v[128:131]\n"
MF"v[136:139],v[212:215],a[244:247],v[136:139]\n"
RD"v[196:199],v243 offset:16384\n"
MF"v[136:139],v[208:211],a[228:231],v[136:139]\n"
MF"v[144:147],v[208:211],a[232:235],v[144:147]\n"
MF"v[144:147],v[212:215],a[248:251],v[144:147]\n"
MF"v[152:155],v[212:215],a[252:255],v[152:155]\n"
SA"s61,s61,524288\n"
RD"v[200:203],v242 offset:18432\n"
MF"v[152:155],v[208:211],a[236:239],v[152:155]\n"
BL"v241,s[20:23],s48 offen offset:3072 lds\n"
MF"v[132:135],v[216:219],a[224:227],v[132:135]\n"
"s_lshl_b32 s50,s34,3\n"
MF"v[132:135],v[220:223],a[240:243],v[132:135]\n"
SA"s51,s50,s30\n"
MF"v[140:143],v[220:223],a[244:247],v[140:143]\n"
"s_mul_i32 s51,s51,20480\n"
RD"v[204:207],v243 offset:18432\n"
MF"v[140:143],v[216:219],a[228:231],v[140:143]\n"
SA"s56,s51,524288\n"
MF"v[148:151],v[216:219],a[232:235],v[148:151]\n"
SA"m0,s36,16384\n"
SA"s48,s60,s61\n"
SA"s57,s56,81920\n"
MF"v[148:151],v[220:223],a[248:251],v[148:151]\n"
"s_sub_u32 s51,s30,s50\n"
MF"v[156:159],v[220:223],a[252:255],v[156:159]\n"
SA"s51,s51,120\n"
RD"v[208:211],v242 offset:20480\n"
MF"v[156:159],v[216:219],a[236:239],v[156:159]\n"
WT"lgkmcnt(2)\n"
BL"v238,s[20:23],s48 offen offset:0 lds\n"
MF"v[0:3],v[192:195],a[0:3],0\n"
"s_mul_i32 s51,s51,20480\n"
SA"s58,s51,524288\n"
"s_lshl_b32 s99,s99,12\n"
MF"v[0:3],v[196:199],a[16:19],v[0:3]\n"
SA"s59,s58,81920\n"
RD"v[212:215],v243 offset:20480\n"
MF"v[16:19],v[196:199],a[20:23],0\n"
"v_add_u32 v235,s99,v224\n"
MF"v[16:19],v[192:195],a[4:7],v[16:19]\n"
PK"v64,v128,v129\n"
PK"v65,v130,v131\n"
MF"v[32:35],v[192:195],a[8:11],0\n"
"s_mov_b32 s49,24576\n"
PK"v66,v132,v133\n"
PK"v67,v134,v135\n"
MF"v[32:35],v[196:199],a[24:27],v[32:35]\n"
RD"v[216:219],v242 offset:22528\n"
MF"v[48:51],v[196:199],a[28:31],0\n"
BS"v[96:99],v250,s[8:11],s49 offen offset:0 sc1\n"
MF"v[48:51],v[192:195],a[12:15],v[48:51]\n"
BL"v239,s[20:23],s48 offen offset:1024 lds\n"
MF"v[8:11],v[200:203],a[0:3],0\n"
WT"lgkmcnt(1)\n"
PK"v68,v136,v137\n"
PK"v69,v138,v139\n"
MF"v[8:11],v[204:207],a[16:19],v[8:11]\n"
RD"v[220:223],v243 offset:22528\n"
PK"v70,v140,v141\n"
MF"v[24:27],v[204:207],a[20:23],0\n"
PK"v71,v142,v143\n"
MF"v[24:27],v[200:203],a[4:7],v[24:27]\n"
MF"v[40:43],v[200:203],a[8:11],0\n"
MF"v[40:43],v[204:207],a[24:27],v[40:43]\n"
RD"v[192:195],v242 offset:40960\n"
PK"v72,v144,v145\n"
MF"v[56:59],v[204:207],a[28:31],0\n"
BL"v240,s[20:23],s48 offen offset:2048 lds\n"
MF"v[56:59],v[200:203],a[12:15],v[56:59]\n"
PK"v73,v146,v147\n"
PK"v74,v148,v149\n"
MF"v[0:3],v[208:211],a[32:35],v[0:3]\n"
BL"a[0:3],v235,s[8:11],s56 offen offset:0 sc1\n"
MF"v[0:3],v[212:215],a[48:51],v[0:3]\n"
RD"v[196:199],v243 offset:40960\n"
PK"v75,v150,v151\n"
MF"v[16:19],v[212:215],a[52:55],v[16:19]\n"
PK"v76,v152,v153\n"
PK"v77,v154,v155\n"
MF"v[16:19],v[208:211],a[36:39],v[16:19]\n"
PK"v78,v156,v157\n"
PK"v79,v158,v159\n"
MF"v[32:35],v[208:211],a[40:43],v[32:35]\n"
RD"v[200:203],v242 offset:43008\n"
MF"v[32:35],v[212:215],a[56:59],v[32:35]\n"
MF"v[48:51],v[212:215],a[60:63],v[48:51]\n"
SA"s61,s61,524288\n"
BL"v241,s[20:23],s48 offen offset:3072 lds\n"
MF"v[48:51],v[208:211],a[44:47],v[48:51]\n"
WT"lgkmcnt(2)\n"
BL"a[4:7],v235,s[8:11],s57 offen offset:0 sc1\n"
MF"v[8:11],v[216:219],a[32:35],v[8:11]\n"
"s_cmp_eq_u32 s61,5242880\n"
RD"v[204:207],v243 offset:43008\n"
"s_cselect_b32 s61,0,s61\n"
MF"v[8:11],v[220:223],a[48:51],v[8:11]\n"
MF"v[24:27],v[220:223],a[52:55],v[24:27]\n"
MF"v[24:27],v[216:219],a[36:39],v[24:27]\n"
MF"v[40:43],v[216:219],a[40:43],v[40:43]\n"
RD"v[208:211],v242 offset:45056\n"
MF"v[40:43],v[220:223],a[56:59],v[40:43]\n"
BL"a[8:11],v235,s[8:11],s58 offen offset:0 sc1\n"
MF"v[56:59],v[220:223],a[60:63],v[56:59]\n"
MF"v[56:59],v[216:219],a[44:47],v[56:59]\n"
MF"v[0:3],v[192:195],a[64:67],v[0:3]\n"
WT"lgkmcnt(1)\n"
RD"v[212:215],v243 offset:45056\n"
MF"v[0:3],v[196:199],a[80:83],v[0:3]\n"
MF"v[16:19],v[196:199],a[84:87],v[16:19]\n"
BL"a[12:15],v235,s[8:11],s59 offen offset:0 sc1\n"
MF"v[16:19],v[192:195],a[68:71],v[16:19]\n"
RD"v[216:219],v242 offset:47104\n"
MF"v[32:35],v[192:195],a[72:75],v[32:35]\n"
MF"v[32:35],v[196:199],a[88:91],v[32:35]\n"
BS"v[104:107],v250,s[8:11],s49 offen offset:1024 sc1\n"
MF"v[48:51],v[196:199],a[92:95],v[48:51]\n"
MF"v[48:51],v[192:195],a[76:79],v[48:51]\n"
RD"v[220:223],v243 offset:47104\n"
MF"v[8:11],v[200:203],a[64:67],v[8:11]\n"
BL"a[16:19],v235,s[8:11],s56 offen offset:1024 sc1\n"
MF"v[8:11],v[204:207],a[80:83],v[8:11]\n"
MF"v[24:27],v[204:207],a[84:87],v[24:27]\n"
MF"v[24:27],v[200:203],a[68:71],v[24:27]\n"
RD"v[192:195],v244 offset:16384\n"
MF"v[40:43],v[200:203],a[72:75],v[40:43]\n"
MF"v[40:43],v[204:207],a[88:91],v[40:43]\n"
MF"v[56:59],v[204:207],a[92:95],v[56:59]\n"
BL"a[20:23],v235,s[8:11],s57 offen offset:1024 sc1\n"
MF"v[56:59],v[200:203],a[76:79],v[56:59]\n"
WT"lgkmcnt(1)\n"
RD"v[196:199],v245 offset:16384\n"
MF"v[0:3],v[208:211],a[96:99],v[0:3]\n"
MF"v[0:3],v[212:215],a[112:115],v[0:3]\n"
MF"v[16:19],v[212:215],a[116:119],v[16:19]\n"
MF"v[16:19],v[208:211],a[100:103],v[16:19]\n"
RD"v[200:203],v244 offset:18432\n"
MF"v[32:35],v[208:211],a[104:107],v[32:35]\n"
BL"a[24:27],v235,s[8:11],s58 offen offset:1024 sc1\n"
MF"v[32:35],v[212:215],a[120:123],v[32:35]\n"
MF"v[48:51],v[212:215],a[124:127],v[48:51]\n"
RD"v[204:207],v245 offset:18432\n"
MF"v[48:51],v[208:211],a[108:111],v[48:51]\n"
MF"v[8:11],v[216:219],a[96:99],v[8:11]\n"
MF"v[8:11],v[220:223],a[112:115],v[8:11]\n"
MF"v[24:27],v[220:223],a[116:119],v[24:27]\n"
RD"v[208:211],v244 offset:20480\n"
MF"v[24:27],v[216:219],a[100:103],v[24:27]\n"
BL"a[28:31],v235,s[8:11],s59 offen offset:1024 sc1\n"
MF"v[40:43],v[216:219],a[104:107],v[40:43]\n"
MF"v[40:43],v[220:223],a[120:123],v[40:43]\n"
MF"v[56:59],v[220:223],a[124:127],v[56:59]\n"
RD"v[212:215],v245 offset:20480\n"
MF"v[56:59],v[216:219],a[108:111],v[56:59]\n"
WT"lgkmcnt(2)\n"
MF"v[0:3],v[192:195],a[128:131],v[0:3]\n"
BL"a[32:35],v235,s[8:11],s56 offen offset:2048 sc1\n"
MF"v[0:3],v[196:199],a[144:147],v[0:3]\n"
MF"v[16:19],v[196:199],a[148:151],v[16:19]\n"
RD"v[216:219],v244 offset:22528\n"
MF"v[16:19],v[192:195],a[132:135],v[16:19]\n"
MF"v[32:35],v[192:195],a[136:139],v[32:35]\n"
BS"v[112:115],v250,s[8:11],s49 offen offset:2048 sc1\n"
MF"v[32:35],v[196:199],a[152:155],v[32:35]\n"
RD"v[220:223],v245 offset:22528\n"
MF"v[48:51],v[196:199],a[156:159],v[48:51]\n"
BL"a[36:39],v235,s[8:11],s57 offen offset:2048 sc1\n"
MF"v[48:51],v[192:195],a[140:143],v[48:51]\n"
MF"v[8:11],v[200:203],a[128:131],v[8:11]\n"
MF"v[8:11],v[204:207],a[144:147],v[8:11]\n"
RD"v[192:195],v244 offset:40960\n"
MF"v[24:27],v[204:207],a[148:151],v[24:27]\n"
MF"v[24:27],v[200:203],a[132:135],v[24:27]\n"
MF"v[40:43],v[200:203],a[136:139],v[40:43]\n"
BL"a[40:43],v235,s[8:11],s58 offen offset:2048 sc1\n"
MF"v[40:43],v[204:207],a[152:155],v[40:43]\n"
RD"v[196:199],v245 offset:40960\n"
MF"v[56:59],v[204:207],a[156:159],v[56:59]\n"
MF"v[56:59],v[200:203],a[140:143],v[56:59]\n"
WT"lgkmcnt(2)\n"
MF"v[0:3],v[208:211],a[160:163],v[0:3]\n"
"s_sub_u32 s53,s37,65536\n"
MF"v[0:3],v[212:215],a[176:179],v[0:3]\n"
RD"v[200:203],v244 offset:43008\n"
MF"v[16:19],v[212:215],a[180:183],v[16:19]\n"
BL"a[44:47],v235,s[8:11],s59 offen offset:2048 sc1\n"
MF"v[16:19],v[208:211],a[164:167],v[16:19]\n"
"v_subrev_u32 v246,s53,v246\n"
"v_subrev_u32 v247,s53,v247\n"
MF"v[32:35],v[208:211],a[168:171],v[32:35]\n"
"v_subrev_u32 v248,s53,v248\n"
"v_subrev_u32 v249,s53,v249\n"
MF"v[32:35],v[212:215],a[184:187],v[32:35]\n"
RD"v[204:207],v245 offset:43008\n"
"v_subrev_u32 v236,65536,v246\n"
MF"v[48:51],v[212:215],a[188:191],v[48:51]\n"
"v_subrev_u32 v237,65536,v247\n"
"v_subrev_u32 v228,65536,v248\n"
MF"v[48:51],v[208:211],a[172:175],v[48:51]\n"
"v_subrev_u32 v229,65536,v249\n"
MF"v[8:11],v[216:219],a[160:163],v[8:11]\n"
RD"v[208:211],v244 offset:45056\n"
MF"v[8:11],v[220:223],a[176:179],v[8:11]\n"
BL"a[48:51],v235,s[8:11],s56 offen offset:3072 sc1\n"
MF"v[24:27],v[220:223],a[180:183],v[24:27]\n"
MF"v[24:27],v[216:219],a[164:167],v[24:27]\n"
MF"v[40:43],v[216:219],a[168:171],v[40:43]\n"
RD"v[212:215],v245 offset:45056\n"
MF"v[40:43],v[220:223],a[184:187],v[40:43]\n"
MF"v[56:59],v[220:223],a[188:191],v[56:59]\n"
BL"a[52:55],v235,s[8:11],s57 offen offset:3072 sc1\n"
MF"v[56:59],v[216:219],a[172:175],v[56:59]\n"
WT"lgkmcnt(1)\n"
MF"v[0:3],v[192:195],a[192:195],v[0:3]\n"
RD"v[216:219],v244 offset:47104\n"
MF"v[0:3],v[196:199],a[208:211],v[0:3]\n"
MF"v[16:19],v[196:199],a[212:215],v[16:19]\n"
MF"v[16:19],v[192:195],a[196:199],v[16:19]\n"
BS"v[120:123],v250,s[8:11],s49 offen offset:3072 sc1\n"
MF"v[32:35],v[192:195],a[200:203],v[32:35]\n"
RD"v[220:223],v245 offset:47104\n"
MF"v[32:35],v[196:199],a[216:219],v[32:35]\n"
BL"a[56:59],v235,s[8:11],s58 offen offset:3072 sc1\n"
MF"v[48:51],v[196:199],a[220:223],v[48:51]\n"
MF"v[48:51],v[192:195],a[204:207],v[48:51]\n"
MF"v[8:11],v[200:203],a[192:195],v[8:11]\n"
SA"s99,s99,4096\n"
MF"v[8:11],v[204:207],a[208:211],v[8:11]\n"
"s_cmp_eq_u32 s99,20480\n"
"s_cselect_b32 s99,0,s99\n"
MF"v[24:27],v[204:207],a[212:215],v[24:27]\n"
BL"a[60:63],v235,s[8:11],s59 offen offset:3072 sc1\n"
MF"v[24:27],v[200:203],a[196:199],v[24:27]\n"
MF"v[40:43],v[200:203],a[200:203],v[40:43]\n"
WT"lgkmcnt(0)\n"
MF"v[40:43],v[204:207],a[216:219],v[40:43]\n"
"s_barrier\n"
MF"v[56:59],v[204:207],a[220:223],v[56:59]\n"
RT"a[128:129],v236 offset:49152\n"
MF"v[56:59],v[200:203],a[204:207],v[56:59]\n"
"v_add_u32 v235,s99,v224\n"
MF"v[0:3],v[208:211],a[224:227],v[0:3]\n"
SA"m0,s38,73728\n"
RT"a[130:131],v236 offset:49664\n"
SA"s48,s60,s61\n"
MF"v[0:3],v[212:215],a[240:243],v[0:3]\n"
BL"a[64:67],v235,s[8:11],s56 offen offset:0 sc1\n"
MF"v[16:19],v[212:215],a[244:247],v[16:19]\n"
RT"a[132:133],v236 offset:57344\n"
"v_add_u32 v242,49152,v246\n"
MF"v[16:19],v[208:211],a[228:231],v[16:19]\n"
BL"v238,s[20:23],s48 offen offset:0 lds\n"
MF"v[32:35],v[208:211],a[232:235],v[32:35]\n"
RT"a[134:135],v236 offset:57856\n"
"v_add_u32 v243,49152,v247\n"
MF"v[32:35],v[212:215],a[248:251],v[32:35]\n"
"v_add_u32 v244,49152,v248\n"
"v_add_u32 v245,49152,v249\n"
MF"v[48:51],v[212:215],a[252:255],v[48:51]\n"
RT"a[136:137],v237 offset:49152\n"
MF"v[48:51],v[208:211],a[236:239],v[48:51]\n"
BL"a[68:71],v235,s[8:11],s57 offen offset:0 sc1\n"
MF"v[8:11],v[216:219],a[224:227],v[8:11]\n"
RT"a[138:139],v237 offset:49664\n"
MF"v[8:11],v[220:223],a[240:243],v[8:11]\n"
MF"v[24:27],v[220:223],a[244:247],v[24:27]\n"
RT"a[140:141],v237 offset:57344\n"
MF"v[24:27],v[216:219],a[228:231],v[24:27]\n"
BL"v239,s[20:23],s48 offen offset:1024 lds\n"
MF"v[40:43],v[216:219],a[232:235],v[40:43]\n"
RT"a[142:143],v237 offset:57856\n"
MF"v[40:43],v[220:223],a[248:251],v[40:43]\n"
BL"a[72:75],v235,s[8:11],s58 offen offset:0 sc1\n"
MF"v[56:59],v[220:223],a[252:255],v[56:59]\n"
RT"a[144:145],v228 offset:49152\n"
MF"v[56:59],v[216:219],a[236:239],v[56:59]\n"
WT"vmcnt(24) lgkmcnt(3)\n"
MF"v[80:83],a[128:131],a[0:3],0\n"
WT"vmcnt(18)\n"
RT"a[146:147],v228 offset:49664\n"
MF"v[80:83],a[132:135],a[16:19],v[80:83]\n"
WT"vmcnt(17)\n"
MF"v[88:91],a[132:135],a[20:23],0\n"
RT"a[148:149],v228 offset:57344\n"
MF"v[88:91],a[128:131],a[4:7],v[88:91]\n"
BL"v240,s[20:23],s48 offen offset:2048 lds\n"
MF"v[96:99],a[128:131],a[8:11],0\n"
WT"vmcnt(17)\n"
BL"a[76:79],v235,s[8:11],s59 offen offset:0 sc1\n"
MF"v[96:99],a[132:135],a[24:27],v[96:99]\n"
WT"vmcnt(17)\n"
RT"a[150:151],v228 offset:57856\n"
PK"v0,v0,v1\n"
MF"v[104:107],a[132:135],a[28:31],0\n"
RT"a[152:153],v229 offset:49152\n"
PK"v1,v2,v3\n"
MF"v[104:107],a[128:131],a[12:15],v[104:107]\n"
RT"a[154:155],v229 offset:49664\n"
PK"v2,v8,v9\n"
MF"v[84:87],a[136:139],a[0:3],0\n"
WT"lgkmcnt(4)\n"
PK"v3,v10,v11\n"
PK"v16,v16,v17\n"
MF"v[84:87],a[140:143],a[16:19],v[84:87]\n"
RT"a[156:157],v229 offset:57344\n"
PK"v17,v18,v19\n"
MF"v[92:95],a[140:143],a[20:23],0\n"
SA"s61,s61,524288\n"
BL"a[80:83],v235,s[8:11],s56 offen offset:1024 sc1\n"
MF"v[92:95],a[136:139],a[4:7],v[92:95]\n"
BL"v241,s[20:23],s48 offen offset:3072 lds\n"
MF"v[100:103],a[136:139],a[8:11],0\n"
RT"a[158:159],v229 offset:57856\n"
PK"v18,v24,v25\n"
MF"v[100:103],a[140:143],a[24:27],v[100:103]\n"
RT"a[160:161],v236 offset:53248\n"
PK"v19,v26,v27\n"
MF"v[108:111],a[140:143],a[28:31],0\n"
PK"v32,v32,v33\n"
PK"v33,v34,v35\n"
MF"v[108:111],a[136:139],a[12:15],v[108:111]\n"
RT"a[162:163],v236 offset:53760\n"
PK"v34,v40,v41\n"
MF"v[112:115],a[144:147],a[0:3],0\n"
WT"lgkmcnt(3)\n"
BL"a[84:87],v235,s[8:11],s57 offen offset:1024 sc1\n"
MF"v[112:115],a[148:151],a[16:19],v[112:115]\n"
RT"a[164:165],v236 offset:61440\n"
PK"v35,v42,v43\n"
MF"v[120:123],a[148:151],a[20:23],0\n"
PK"v48,v48,v49\n"
PK"v49,v50,v51\n"
MF"v[120:123],a[144:147],a[4:7],v[120:123]\n"
RT"a[166:167],v236 offset:61952\n"
PK"v50,v56,v57\n"
MF"v[128:131],a[144:147],a[8:11],0\n"
PK"v51,v58,v59\n"
MF"v[128:131],a[148:151],a[24:27],v[128:131]\n"
RT"a[168:169],v237 offset:53248\n"
MF"v[136:139],a[148:151],a[28:31],0\n"
MF"v[136:139],a[144:147],a[12:15],v[136:139]\n"
RT"a[170:171],v237 offset:53760\n"
MF"v[116:119],a[152:155],a[0:3],0\n"
WT"lgkmcnt(3)\n"
BL"a[88:91],v235,s[8:11],s58 offen offset:1024 sc1\n"
MF"v[116:119],a[156:159],a[16:19],v[116:119]\n"
"s_mov_b32 s49,262144\n"
RT"a[172:173],v237 offset:61440\n"
MF"v[124:127],a[156:159],a[20:23],0\n"
RT"a[174:175],v237 offset:61952\n"
MF"v[124:127],a[152:155],a[4:7],v[124:127]\n"
BS"v[4:7],v250,s[8:11],s49 offen offset:0 sc1\n"
MF"v[132:135],a[152:155],a[8:11],0\n"
RT"a[176:177],v228 offset:53248\n"
MF"v[132:135],a[156:159],a[24:27],v[132:135]\n"
MF"v[140:143],a[156:159],a[28:31],0\n"
RT"a[178:179],v228 offset:53760\n"
MF"v[140:143],a[152:155],a[12:15],v[140:143]\n"
BL"a[92:95],v235,s[8:11],s59 offen offset:1024 sc1\n"
MF"v[144:147],a[160:163],a[0:3],0\n"
WT"lgkmcnt(3)\n"
RT"a[180:181],v228 offset:61440\n"
MF"v[144:147],a[164:167],a[16:19],v[144:147]\n"
MF"v[152:155],a[164:167],a[20:23],0\n"
SA"m0,s38,122880\n"
RT"a[182:183],v228 offset:61952\n"
SA"s48,s60,s61\n"
MF"v[152:155],a[160:163],a[4:7],v[152:155]\n"
MF"v[160:163],a[160:163],a[8:11],0\n"
"s_mov_b32 s88,2048\n"
RT"a[184:185],v229 offset:53248\n"
MF"v[160:163],a[164:167],a[24:27],v[160:163]\n"
BL"v238,s[20:23],s48 offen offset:0 lds\n"
MF"v[168:171],a[164:167],a[28:31],0\n"
BL"a[96:99],v235,s[8:11],s56 offen offset:2048 sc1\n"
MF"v[168:171],a[160:163],a[12:15],v[168:171]\n"
RT"a[186:187],v229 offset:53760\n"
MF"v[148:151],a[168:171],a[0:3],0\n"
WT"lgkmcnt(3)\n"
RT"a[188:189],v229 offset:61440\n"
MF"v[148:151],a[172:175],a[16:19],v[148:151]\n"
MF"v[156:159],a[172:175],a[20:23],0\n"
RT"a[190:191],v229 offset:61952\n"
MF"v[156:159],a[168:171],a[4:7],v[156:159]\n"
RT"a[128:129],v236 offset:32768\n"
MF"v[164:167],a[168:171],a[8:11],0\n"
BL"v239,s[20:23],s48 offen offset:1024 lds\n"
MF"v[164:167],a[172:175],a[24:27],v[164:167]\n"
BL"a[100:103],v235,s[8:11],s57 offen offset:2048 sc1\n"
MF"v[172:175],a[172:175],a[28:31],0\n"
RT"a[130:131],v236 offset:33280\n"
MF"v[172:175],a[168:171],a[12:15],v[172:175]\n"
RT"a[132:133],v236 offset:40960\n"
MF"v[176:179],a[176:179],a[0:3],0\n"
WT"lgkmcnt(4)\n"
MF"v[176:179],a[180:183],a[16:19],v[176:179]\n"
RT"a[134:135],v236 offset:41472\n"
MF"v[184:187],a[180:183],a[20:23],0\n"
MF"v[184:187],a[176:179],a[4:7],v[184:187]\n"
RT"a[136:137],v237 offset:32768\n"
MF"v[192:195],a[176:179],a[8:11],0\n"
BL"a[104:107],v235,s[8:11],s58 offen offset:2048 sc1\n"
MF"v[192:195],a[180:183],a[24:27],v[192:195]\n"
BL"v240,s[20:23],s48 offen offset:2048 lds\n"
MF"v[200:203],a[180:183],a[28:31],0\n"
RT"a[138:139],v237 offset:33280\n"
MF"v[200:203],a[176:179],a[12:15],v[200:203]\n"
RT"a[140:141],v237 offset:40960\n"
MF"v[180:183],a[184:187],a[0:3],0\n"
WT"lgkmcnt(3)\n"
MF"v[180:183],a[188:191],a[16:19],v[180:183]\n"
RT"a[142:143],v237 offset:41472\n"
MF"v[188:191],a[188:191],a[20:23],0\n"
BL"a[108:111],v235,s[8:11],s59 offen offset:2048 sc1\n"
MF"v[188:191],a[184:187],a[4:7],v[188:191]\n"
RT"a[144:145],v228 offset:32768\n"
MF"v[196:199],a[184:187],a[8:11],0\n"
SA"s61,s61,524288\n"
BS"v[12:15],v250,s[8:11],s49 offen offset:1024 sc1\n"
MF"v[196:199],a[188:191],a[24:27],v[196:199]\n"
"s_cmp_eq_u32 s61,5242880\n"
BL"v241,s[20:23],s48 offen offset:3072 lds\n"
MF"v[204:207],a[188:191],a[28:31],0\n"
"s_cselect_b32 s61,0,s61\n"
RT"a[146:147],v228 offset:33280\n"
MF"v[204:207],a[184:187],a[12:15],v[204:207]\n"
WT"vmcnt(31)\n"
RT"a[148:149],v228 offset:40960\n"
MF"v[80:83],a[128:131],a[32:35],v[80:83]\n"
WT"vmcnt(26)\n"
MF"v[80:83],a[132:135],a[48:51],v[80:83]\n"
WT"vmcnt(25)\n"
RT"a[150:151],v228 offset:41472\n"
MF"v[88:91],a[132:135],a[52:55],v[88:91]\n"
BL"a[112:115],v235,s[8:11],s56 offen offset:3072 sc1\n"
MF"v[88:91],a[128:131],a[36:39],v[88:91]\n"
RT"a[152:153],v229 offset:32768\n"
MF"v[96:99],a[128:131],a[40:43],v[96:99]\n"
WT"vmcnt(24)\n"
RT"a[154:155],v229 offset:33280\n"
MF"v[96:99],a[132:135],a[56:59],v[96:99]\n"
WT"vmcnt(23)\n"
MF"v[104:107],a[132:135],a[60:63],v[104:107]\n"
RT"a[156:157],v229 offset:40960\n"
MF"v[104:107],a[128:131],a[44:47],v[104:107]\n"
WT"lgkmcnt(4)\n"
MF"v[84:87],a[136:139],a[32:35],v[84:87]\n"
RT"a[158:159],v229 offset:41472\n"
MF"v[84:87],a[140:143],a[48:51],v[84:87]\n"
BL"a[116:119],v235,s[8:11],s57 offen offset:3072 sc1\n"
MF"v[92:95],a[140:143],a[52:55],v[92:95]\n"
RT"a[160:161],v236 offset:36864\n"
MF"v[92:95],a[136:139],a[36:39],v[92:95]\n"
MF"v[100:103],a[136:139],a[40:43],v[100:103]\n"
RT"a[162:163],v236 offset:37376\n"
MF"v[100:103],a[140:143],a[56:59],v[100:103]\n"
MF"v[108:111],a[140:143],a[60:63],v[108:111]\n"
RT"a[164:165],v236 offset:45056\n"
MF"v[108:111],a[136:139],a[44:47],v[108:111]\n"
BL"a[120:123],v235,s[8:11],s58 offen offset:3072 sc1\n"
MF"v[112:115],a[144:147],a[32:35],v[112:115]\n"
WT"lgkmcnt(3)\n"
RT"a[166:167],v236 offset:45568\n"
MF"v[112:115],a[148:151],a[48:51],v[112:115]\n"
MF"v[120:123],a[148:151],a[52:55],v[120:123]\n"
RT"a[168:169],v237 offset:36864\n"
MF"v[120:123],a[144:147],a[36:39],v[120:123]\n"
MF"v[128:131],a[144:147],a[40:43],v[128:131]\n"
RT"a[170:171],v237 offset:37376\n"
MF"v[128:131],a[148:151],a[56:59],v[128:131]\n"
SA"s99,s99,4096\n"
RT"a[172:173],v237 offset:45056\n"
MF"v[136:139],a[148:151],a[60:63],v[136:139]\n"
"s_cmp_eq_u32 s99,20480\n"
BL"a[124:127],v235,s[8:11],s59 offen offset:3072 sc1\n"
MF"v[136:139],a[144:147],a[44:47],v[136:139]\n"
"s_cselect_b32 s99,0,s99\n"
RT"a[174:175],v237 offset:45568\n"
MF"v[116:119],a[152:155],a[32:35],v[116:119]\n"
MF"v[116:119],a[156:159],a[48:51],v[116:119]\n"
RT"a[176:177],v228 offset:36864\n"
MF"v[124:127],a[156:159],a[52:55],v[124:127]\n"
MF"v[124:127],a[152:155],a[36:39],v[124:127]\n"
RT"a[178:179],v228 offset:37376\n"
MF"v[132:135],a[152:155],a[40:43],v[132:135]\n"
"v_add_u32 v235,s99,v224\n"
MF"v[132:135],a[156:159],a[56:59],v[132:135]\n"
RT"a[180:181],v228 offset:45056\n"
MF"v[140:143],a[156:159],a[60:63],v[140:143]\n"
BL"a[0:3],v235,s[8:11],s56 offen offset:0 sc1\n"
MF"v[140:143],a[152:155],a[44:47],v[140:143]\n"
WT"lgkmcnt(3)\n"
BS"v[20:23],v250,s[8:11],s49 offen offset:2048 sc1\n"
MF"v[144:147],a[160:163],a[32:35],v[144:147]\n"
RT"a[182:183],v228 offset:45568\n"
MF"v[144:147],a[164:167],a[48:51],v[144:147]\n"
RT"a[184:185],v229 offset:36864\n"
MF"v[152:155],a[164:167],a[52:55],v[152:155]\n"
MF"v[152:155],a[160:163],a[36:39],v[152:155]\n"
RT"a[186:187],v229 offset:37376\n"
MF"v[160:163],a[160:163],a[40:43],v[160:163]\n"
BL"a[4:7],v235,s[8:11],s57 offen offset:0 sc1\n"
MF"v[160:163],a[164:167],a[56:59],v[160:163]\n"
RT"a[188:189],v229 offset:45056\n"
MF"v[168:171],a[164:167],a[60:63],v[168:171]\n"
MF"v[168:171],a[160:163],a[44:47],v[168:171]\n"
RT"a[190:191],v229 offset:45568\n"
MF"v[148:151],a[168:171],a[32:35],v[148:151]\n"
"s_lshl_b32 s50,s35,3\n"
SA"s50,s50,s34\n"
"s_lshl_b32 s50,s50,3\n"
MF"v[148:151],a[172:175],a[48:51],v[148:151]\n"
SA"s50,s50,2304\n"
SA"s51,s50,s45\n"
MF"v[156:159],a[172:175],a[52:55],v[156:159]\n"
MF"v[156:159],a[168:171],a[36:39],v[156:159]\n"
BL"a[8:11],v235,s[8:11],s58 offen offset:0 sc1\n"
MF"v[164:167],a[168:171],a[40:43],v[164:167]\n"
WT"lgkmcnt(0)\n"
MF"v[164:167],a[172:175],a[56:59],v[164:167]\n"
"s_barrier\n"
MF"v[172:175],a[172:175],a[60:63],v[172:175]\n"
RT"a[128:129],v236 offset:0\n"
MF"v[172:175],a[168:171],a[44:47],v[172:175]\n"
"buffer_store_dwordx2 v[232:233],v230,s[8:11],s51 offen sc1\n"
MF"v[176:179],a[176:179],a[32:35],v[176:179]\n"
RT"a[130:131],v236 offset:512\n"
MF"v[176:179],a[180:183],a[48:51],v[176:179]\n"
MF"v[184:187],a[180:183],a[52:55],v[184:187]\n"
SA"m0,s38,81920\n"
RT"a[132:133],v236 offset:8192\n"
SA"s48,s60,s61\n"
MF"v[184:187],a[176:179],a[36:39],v[184:187]\n"
BL"a[12:15],v235,s[8:11],s59 offen offset:0 sc1\n"
MF"v[192:195],a[176:179],a[40:43],v[192:195]\n"
RT"a[134:135],v236 offset:8704\n"
MF"v[192:195],a[180:183],a[56:59],v[192:195]\n"
BL"v238,s[20:23],s48 offen offset:0 lds\n"
MF"v[200:203],a[180:183],a[60:63],v[200:203]\n"
RT"a[136:137],v237 offset:0\n"
MF"v[200:203],a[176:179],a[44:47],v[200:203]\n"
MF"v[180:183],a[184:187],a[32:35],v[180:183]\n"
RT"a[138:139],v237 offset:512\n"
MF"v[180:183],a[188:191],a[48:51],v[180:183]\n"
BL"a[16:19],v235,s[8:11],s56 offen offset:1024 sc1\n"
MF"v[188:191],a[188:191],a[52:55],v[188:191]\n"
RT"a[140:141],v237 offset:8192\n"
MF"v[188:191],a[184:187],a[36:39],v[188:191]\n"
MF"v[196:199],a[184:187],a[40:43],v[196:199]\n"
RT"a[142:143],v237 offset:8704\n"
MF"v[196:199],a[188:191],a[56:59],v[196:199]\n"
BL"v239,s[20:23],s48 offen offset:1024 lds\n"
MF"v[204:207],a[188:191],a[60:63],v[204:207]\n"
SA"s49,s88,262144\n"
RT"a[144:145],v228 offset:0\n"
MF"v[204:207],a[184:187],a[44:47],v[204:207]\n"
"LQ_%=:\n"
WT"vmcnt(34) lgkmcnt(7)\n"
MF"v[80:83],a[128:131],a[64:67],v[80:83]\n"
WT"vmcnt(27) lgkmcnt(3)\n"
RT"a[146:147],v228 offset:512\n"
MF"v[80:83],a[132:135],a[80:83],v[80:83]\n"
WT"vmcnt(25)\n"
BS"v[28:31],v250,s[8:11],s49 offen offset:1024 sc1\n"
MF"v[88:91],a[132:135],a[84:87],v[88:91]\n"
BL"a[20:23],v235,s[8:11],s57 offen offset:1024 sc1\n"
MF"v[88:91],a[128:131],a[68:71],v[88:91]\n"
RT"a[148:149],v228 offset:8192\n"
MF"v[96:99],a[128:131],a[72:75],v[96:99]\n"
WT"vmcnt(26)\n"
RT"a[150:151],v228 offset:8704\n"
MF"v[96:99],a[132:135],a[88:91],v[96:99]\n"
WT"vmcnt(24)\n"
BL"v240,s[20:23],s48 offen offset:2048 lds\n"
MF"v[104:107],a[132:135],a[92:95],v[104:107]\n"
RT"a[152:153],v229 offset:0\n"
MF"v[104:107],a[128:131],a[76:79],v[104:107]\n"
RT"a[154:155],v229 offset:512\n"
MF"v[84:87],a[136:139],a[64:67],v[84:87]\n"
WT"lgkmcnt(4)\n"
BL"a[24:27],v235,s[8:11],s58 offen offset:1024 sc1\n"
MF"v[84:87],a[140:143],a[80:83],v[84:87]\n"
RT"a[156:157],v229 offset:8192\n"
MF"v[92:95],a[140:143],a[84:87],v[92:95]\n"
MF"v[92:95],a[136:139],a[68:71],v[92:95]\n"
RT"a[158:159],v229 offset:8704\n"
MF"v[100:103],a[136:139],a[72:75],v[100:103]\n"
SA"s61,s61,524288\n"
BL"v241,s[20:23],s48 offen offset:3072 lds\n"
MF"v[100:103],a[140:143],a[88:91],v[100:103]\n"
RT"a[160:161],v236 offset:4096\n"
MF"v[108:111],a[140:143],a[92:95],v[108:111]\n"
BL"a[28:31],v235,s[8:11],s59 offen offset:1024 sc1\n"
MF"v[108:111],a[136:139],a[76:79],v[108:111]\n"
RT"a[162:163],v236 offset:4608\n"
MF"v[112:115],a[144:147],a[64:67],v[112:115]\n"
WT"lgkmcnt(3)\n"
MF"v[112:115],a[148:151],a[80:83],v[112:115]\n"
RT"a[164:165],v236 offset:12288\n"
MF"v[120:123],a[148:151],a[84:87],v[120:123]\n"
MF"v[120:123],a[144:147],a[68:71],v[120:123]\n"
RT"a[166:167],v236 offset:12800\n"
MF"v[128:131],a[144:147],a[72:75],v[128:131]\n"
MF"v[128:131],a[148:151],a[88:91],v[128:131]\n"
RT"a[168:169],v237 offset:4096\n"
MF"v[136:139],a[148:151],a[92:95],v[136:139]\n"
BL"a[32:35],v235,s[8:11],s56 offen offset:2048 sc1\n"
MF"v[136:139],a[144:147],a[76:79],v[136:139]\n"
RT"a[170:171],v237 offset:4608\n"
MF"v[116:119],a[152:155],a[64:67],v[116:119]\n"
WT"lgkmcnt(3)\n"
RT"a[172:173],v237 offset:12288\n"
MF"v[116:119],a[156:159],a[80:83],v[116:119]\n"
MF"v[124:127],a[156:159],a[84:87],v[124:127]\n"
RT"a[174:175],v237 offset:12800\n"
MF"v[124:127],a[152:155],a[68:71],v[124:127]\n"
MF"v[132:135],a[152:155],a[72:75],v[132:135]\n"
RT"a[176:177],v228 offset:4096\n"
MF"v[132:135],a[156:159],a[88:91],v[132:135]\n"
BL"a[36:39],v235,s[8:11],s57 offen offset:2048 sc1\n"
MF"v[140:143],a[156:159],a[92:95],v[140:143]\n"
RT"a[178:179],v228 offset:4608\n"
MF"v[140:143],a[152:155],a[76:79],v[140:143]\n"
MF"v[144:147],a[160:163],a[64:67],v[144:147]\n"
WT"lgkmcnt(3)\n"
RT"a[180:181],v228 offset:12288\n"
"s_mov_b32 s49,286720\n"
MF"v[144:147],a[164:167],a[80:83],v[144:147]\n"
MF"v[152:155],a[164:167],a[84:87],v[152:155]\n"
RT"a[182:183],v228 offset:12800\n"
MF"v[152:155],a[160:163],a[68:71],v[152:155]\n"
BS"v[0:3],v250,s[8:11],s49 offen offset:0 sc1\n"
MF"v[160:163],a[160:163],a[72:75],v[160:163]\n"
BL"a[40:43],v235,s[8:11],s58 offen offset:2048 sc1\n"
MF"v[160:163],a[164:167],a[88:91],v[160:163]\n"
RT"a[184:185],v229 offset:4096\n"
MF"v[168:171],a[164:167],a[92:95],v[168:171]\n"
RT"a[186:187],v229 offset:4608\n"
MF"v[168:171],a[160:163],a[76:79],v[168:171]\n"
MF"v[148:151],a[168:171],a[64:67],v[148:151]\n"
WT"lgkmcnt(3)\n"
RT"a[188:189],v229 offset:12288\n"
MF"v[148:151],a[172:175],a[80:83],v[148:151]\n"
MF"v[156:159],a[172:175],a[84:87],v[156:159]\n"
RT"a[190:191],v229 offset:12800\n"
MF"v[156:159],a[168:171],a[68:71],v[156:159]\n"
BL"a[44:47],v235,s[8:11],s59 offen offset:2048 sc1\n"
MF"v[164:167],a[168:171],a[72:75],v[164:167]\n"
RT"a[128:129],v246 offset:0\n"
MF"v[164:167],a[172:175],a[88:91],v[164:167]\n"
RT"a[130:131],v246 offset:512\n"
MF"v[172:175],a[172:175],a[92:95],v[172:175]\n"
SA"m0,s38,131072\n"
SA"s48,s60,s61\n"
MF"v[172:175],a[168:171],a[76:79],v[172:175]\n"
RT"a[132:133],v246 offset:24576\n"
MF"v[176:179],a[176:179],a[64:67],v[176:179]\n"
WT"lgkmcnt(4)\n"
BL"v238,s[20:23],s48 offen offset:0 lds\n"
MF"v[176:179],a[180:183],a[80:83],v[176:179]\n"
RT"a[134:135],v246 offset:25088\n"
MF"v[184:187],a[180:183],a[84:87],v[184:187]\n"
BL"a[48:51],v235,s[8:11],s56 offen offset:3072 sc1\n"
MF"v[184:187],a[176:179],a[68:71],v[184:187]\n"
RT"a[136:137],v247 offset:0\n"
MF"v[192:195],a[176:179],a[72:75],v[192:195]\n"
MF"v[192:195],a[180:183],a[88:91],v[192:195]\n"
RT"a[138:139],v247 offset:512\n"
MF"v[200:203],a[180:183],a[92:95],v[200:203]\n"
MF"v[200:203],a[176:179],a[76:79],v[200:203]\n"
RT"a[140:141],v247 offset:24576\n"
MF"v[180:183],a[184:187],a[64:67],v[180:183]\n"
WT"lgkmcnt(3)\n"
BL"v239,s[20:23],s48 offen offset:1024 lds\n"
MF"v[180:183],a[188:191],a[80:83],v[180:183]\n"
BL"a[52:55],v235,s[8:11],s57 offen offset:3072 sc1\n"
MF"v[188:191],a[188:191],a[84:87],v[188:191]\n"
RT"a[142:143],v247 offset:25088\n"
MF"v[188:191],a[184:187],a[68:71],v[188:191]\n"
RT"a[144:145],v248 offset:0\n"
MF"v[196:199],a[184:187],a[72:75],v[196:199]\n"
MF"v[196:199],a[188:191],a[88:91],v[196:199]\n"
RT"a[146:147],v248 offset:512\n"
MF"v[204:207],a[188:191],a[92:95],v[204:207]\n"
MF"v[204:207],a[184:187],a[76:79],v[204:207]\n"
WT"vmcnt(35)\n"
RT"a[148:149],v248 offset:24576\n"
MF"v[80:83],a[128:131],a[96:99],v[80:83]\n"
WT"vmcnt(27)\n"
BL"a[56:59],v235,s[8:11],s58 offen offset:3072 sc1\n"
MF"v[80:83],a[132:135],a[112:115],v[80:83]\n"
WT"vmcnt(27)\n"
BL"v240,s[20:23],s48 offen offset:2048 lds\n"
MF"v[88:91],a[132:135],a[116:119],v[88:91]\n"
RT"a[150:151],v248 offset:25088\n"
MF"v[88:91],a[128:131],a[100:103],v[88:91]\n"
RT"a[152:153],v249 offset:0\n"
MF"v[96:99],a[128:131],a[104:107],v[96:99]\n"
WT"vmcnt(27)\n"
RT"a[154:155],v249 offset:512\n"
MF"v[96:99],a[132:135],a[120:123],v[96:99]\n"
WT"vmcnt(26)\n"
BS"v[16:19],v250,s[8:11],s49 offen offset:1024 sc1\n"
MF"v[104:107],a[132:135],a[124:127],v[104:107]\n"
SA"s99,s99,4096\n"
RT"a[156:157],v249 offset:24576\n"
"s_cmp_eq_u32 s99,20480\n"
MF"v[104:107],a[128:131],a[108:111],v[104:107]\n"
WT"lgkmcnt(4)\n"
BL"a[60:63],v235,s[8:11],s59 offen offset:3072 sc1\n"
MF"v[84:87],a[136:139],a[96:99],v[84:87]\n"
"s_cselect_b32 s99,0,s99\n"
RT"a[158:159],v249 offset:25088\n"
SA"s61,s61,524288\n"
MF"v[84:87],a[140:143],a[112:115],v[84:87]\n"
"s_cmp_eq_u32 s61,5242880\n"
BL"v241,s[20:23],s48 offen offset:3072 lds\n"
MF"v[92:95],a[140:143],a[116:119],v[92:95]\n"
"s_cselect_b32 s61,0,s61\n"
RT"a[160:161],v246 offset:4096\n"
MF"v[92:95],a[136:139],a[100:103],v[92:95]\n"
MF"v[100:103],a[136:139],a[104:107],v[100:103]\n"
RT"a[162:163],v246 offset:4608\n"
"v_add_u32 v235,s99,v224\n"
MF"v[100:103],a[140:143],a[120:123],v[100:103]\n"
BL"a[64:67],v235,s[8:11],s56 offen offset:0 sc1\n"
MF"v[108:111],a[140:143],a[124:127],v[108:111]\n"
RT"a[164:165],v246 offset:28672\n"
MF"v[108:111],a[136:139],a[108:111],v[108:111]\n"
MF"v[112:115],a[144:147],a[96:99],v[112:115]\n"
WT"lgkmcnt(3)\n"
RT"a[166:167],v246 offset:29184\n"
"v_add_u32 v225,s36,v224\n"
MF"v[112:115],a[148:151],a[112:115],v[112:115]\n"
"s_mul_i32 s39,s35,1048576\n"
SA"s39,s39,9175040\n"
MF"v[120:123],a[148:151],a[116:119],v[120:123]\n"
RT"a[168:169],v247 offset:4096\n"
MF"v[120:123],a[144:147],a[100:103],v[120:123]\n"
MF"v[128:131],a[144:147],a[104:107],v[128:131]\n"
RT"a[170:171],v247 offset:4608\n"
MF"v[128:131],a[148:151],a[120:123],v[128:131]\n"
BL"a[68:71],v235,s[8:11],s57 offen offset:0 sc1\n"
MF"v[136:139],a[148:151],a[124:127],v[136:139]\n"
RT"a[172:173],v247 offset:28672\n"
MF"v[136:139],a[144:147],a[108:111],v[136:139]\n"
RT"a[174:175],v247 offset:29184\n"
MF"v[116:119],a[152:155],a[96:99],v[116:119]\n"
MF"v[116:119],a[156:159],a[112:115],v[116:119]\n"
RT"a[176:177],v248 offset:4096\n"
MF"v[124:127],a[156:159],a[116:119],v[124:127]\n"
MF"v[124:127],a[152:155],a[100:103],v[124:127]\n"
RT"a[178:179],v248 offset:4608\n"
MF"v[132:135],a[152:155],a[104:107],v[132:135]\n"
BL"a[72:75],v235,s[8:11],s58 offen offset:0 sc1\n"
MF"v[132:135],a[156:159],a[120:123],v[132:135]\n"
RT"a[180:181],v248 offset:28672\n"
MF"v[140:143],a[156:159],a[124:127],v[140:143]\n"
MF"v[140:143],a[152:155],a[108:111],v[140:143]\n"
WT"lgkmcnt(3)\n"
RT"a[182:183],v248 offset:29184\n"
MF"v[144:147],a[160:163],a[96:99],v[144:147]\n"
MF"v[144:147],a[164:167],a[112:115],v[144:147]\n"
RT"a[184:185],v249 offset:4096\n"
MF"v[152:155],a[164:167],a[116:119],v[152:155]\n"
BL"a[76:79],v235,s[8:11],s59 offen offset:0 sc1\n"
MF"v[152:155],a[160:163],a[100:103],v[152:155]\n"
RT"a[186:187],v249 offset:4608\n"
MF"v[160:163],a[160:163],a[104:107],v[160:163]\n"
SA"s49,s88,286720\n"
MF"v[160:163],a[164:167],a[120:123],v[160:163]\n"
RT"a[188:189],v249 offset:28672\n"
MF"v[168:171],a[164:167],a[124:127],v[168:171]\n"
BS"v[32:35],v250,s[8:11],s49 offen offset:0 sc1\n"
MF"v[168:171],a[160:163],a[108:111],v[168:171]\n"
RT"a[190:191],v249 offset:29184\n"
MF"v[148:151],a[168:171],a[96:99],v[148:151]\n"
MF"v[148:151],a[172:175],a[112:115],v[148:151]\n"
BL"a[80:83],v235,s[8:11],s56 offen offset:1024 sc1\n"
MF"v[156:159],a[172:175],a[116:119],v[156:159]\n"
MF"v[156:159],a[168:171],a[100:103],v[156:159]\n"
MF"v[164:167],a[168:171],a[104:107],v[164:167]\n"
MF"v[164:167],a[172:175],a[120:123],v[164:167]\n"
"s_barrier\n"
MF"v[172:175],a[172:175],a[124:127],v[172:175]\n"
RT"a[128:129],v242 offset:0\n"
MF"v[172:175],a[168:171],a[108:111],v[172:175]\n"
WT"lgkmcnt(1)\n"
MF"v[176:179],a[176:179],a[96:99],v[176:179]\n"
RT"a[130:131],v242 offset:512\n"
MF"v[176:179],a[180:183],a[112:115],v[176:179]\n"
BL"a[84:87],v235,s[8:11],s57 offen offset:1024 sc1\n"
MF"v[184:187],a[180:183],a[116:119],v[184:187]\n"
RT"a[132:133],v242 offset:24576\n"
MF"v[184:187],a[176:179],a[100:103],v[184:187]\n"
MF"v[192:195],a[176:179],a[104:107],v[192:195]\n"
RT"a[134:135],v242 offset:25088\n"
MF"v[192:195],a[180:183],a[120:123],v[192:195]\n"
MF"v[200:203],a[180:183],a[124:127],v[200:203]\n"
RT"a[136:137],v243 offset:0\n"
MF"v[200:203],a[176:179],a[108:111],v[200:203]\n"
"s_mov_b32 s14,LEND_%=-LP2C_%=\n"
"s_getpc_b64 s[12:13]\n"
"LP2C_%=:\n"
"s_and_b32 s13,s13,0xffff\n"
MF"v[180:183],a[184:187],a[96:99],v[180:183]\n"
"s_lshr_b32 s90,s31,3\n"
BL"a[88:91],v235,s[8:11],s58 offen offset:1024 sc1\n"
MF"v[180:183],a[188:191],a[112:115],v[180:183]\n"
"s_lshl_b32 s90,s90,2\n"
RT"a[138:139],v243 offset:512\n"
MF"v[188:191],a[188:191],a[116:119],v[188:191]\n"
SA"s90,s90,s30\n"
RT"a[140:141],v243 offset:24576\n"
MF"v[188:191],a[184:187],a[100:103],v[188:191]\n"
"s_lshl_b32 s90,s90,7\n"
MF"v[196:199],a[184:187],a[104:107],v[196:199]\n"
RT"a[142:143],v243 offset:25088\n"
"v_lshlrev_b32 v253,10,v224\n"
MF"v[196:199],a[188:191],a[120:123],v[196:199]\n"
MF"v[204:207],a[188:191],a[124:127],v[204:207]\n"
RT"a[144:145],v244 offset:0\n"
MF"v[204:207],a[184:187],a[108:111],v[204:207]\n"
WT"vmcnt(36) lgkmcnt(4)\n"
"buffer_load_dword v253,v253,s[12:15],s90 offen\n"
MF"v[80:83],a[128:131],a[0:3],v[80:83]\n"
WT"vmcnt(30)\n"
BL"a[92:95],v235,s[8:11],s59 offen offset:1024 sc1\n"
MF"v[80:83],a[132:135],a[16:19],v[80:83]\n"
WT"vmcnt(28)\n"
RT"a[146:147],v244 offset:512\n"
MF"v[88:91],a[132:135],a[20:23],v[88:91]\n"
RT"a[148:149],v244 offset:24576\n"
MF"v[88:91],a[128:131],a[4:7],v[88:91]\n"
MF"v[96:99],a[128:131],a[8:11],v[96:99]\n"
WT"vmcnt(26)\n"
RT"a[150:151],v244 offset:25088\n"
MF"v[96:99],a[132:135],a[24:27],v[96:99]\n"
WT"vmcnt(24)\n"
RT"a[152:153],v245 offset:0\n"
MF"v[104:107],a[132:135],a[28:31],v[104:107]\n"
BL"a[96:99],v235,s[8:11],s56 offen offset:2048 sc1\n"
MF"v[104:107],a[128:131],a[12:15],v[104:107]\n"
WT"lgkmcnt(4)\n"
RT"a[154:155],v245 offset:512\n"
MF"v[84:87],a[136:139],a[0:3],v[84:87]\n"
BS"v[48:51],v250,s[8:11],s49 offen offset:1024 sc1\n"
MF"v[84:87],a[140:143],a[16:19],v[84:87]\n"
RT"a[156:157],v245 offset:24576\n"
MF"v[92:95],a[140:143],a[20:23],v[92:95]\n"
MF"v[92:95],a[136:139],a[4:7],v[92:95]\n"
RT"a[158:159],v245 offset:25088\n"
MF"v[100:103],a[136:139],a[8:11],v[100:103]\n"
BL"a[100:103],v235,s[8:11],s57 offen offset:2048 sc1\n"
MF"v[100:103],a[140:143],a[24:27],v[100:103]\n"
RT"a[160:161],v242 offset:4096\n"
MF"v[108:111],a[140:143],a[28:31],v[108:111]\n"
MF"v[108:111],a[136:139],a[12:15],v[108:111]\n"
WT"lgkmcnt(3)\n"
RT"a[162:163],v242 offset:4608\n"
MF"v[112:115],a[144:147],a[0:3],v[112:115]\n"
MF"v[112:115],a[148:151],a[16:19],v[112:115]\n"
RT"a[164:165],v242 offset:28672\n"
MF"v[120:123],a[148:151],a[20:23],v[120:123]\n"
MF"v[120:123],a[144:147],a[4:7],v[120:123]\n"
RT"a[166:167],v242 offset:29184\n"
MF"v[128:131],a[144:147],a[8:11],v[128:131]\n"
BL"a[104:107],v235,s[8:11],s58 offen offset:2048 sc1\n"
MF"v[128:131],a[148:151],a[24:27],v[128:131]\n"
RT"a[168:169],v243 offset:4096\n"
MF"v[136:139],a[148:151],a[28:31],v[136:139]\n"
MF"v[136:139],a[144:147],a[12:15],v[136:139]\n"
RT"a[170:171],v243 offset:4608\n"
MF"v[116:119],a[152:155],a[0:3],v[116:119]\n"
WT"lgkmcnt(3)\n"
RT"a[172:173],v243 offset:28672\n"
MF"v[116:119],a[156:159],a[16:19],v[116:119]\n"
MF"v[124:127],a[156:159],a[20:23],v[124:127]\n"
RT"a[174:175],v243 offset:29184\n"
MF"v[124:127],a[152:155],a[4:7],v[124:127]\n"
BL"a[108:111],v235,s[8:11],s59 offen offset:2048 sc1\n"
MF"v[132:135],a[152:155],a[8:11],v[132:135]\n"
RT"a[176:177],v244 offset:4096\n"
MF"v[132:135],a[156:159],a[24:27],v[132:135]\n"
MF"v[140:143],a[156:159],a[28:31],v[140:143]\n"
RT"a[178:179],v244 offset:4608\n"
MF"v[140:143],a[152:155],a[12:15],v[140:143]\n"
MF"v[144:147],a[160:163],a[0:3],v[144:147]\n"
WT"lgkmcnt(3)\n"
RT"a[180:181],v244 offset:28672\n"
MF"v[144:147],a[164:167],a[16:19],v[144:147]\n"
BL"a[112:115],v235,s[8:11],s56 offen offset:3072 sc1\n"
MF"v[152:155],a[164:167],a[20:23],v[152:155]\n"
RT"a[182:183],v244 offset:29184\n"
MF"v[152:155],a[160:163],a[4:7],v[152:155]\n"
MF"v[160:163],a[160:163],a[8:11],v[160:163]\n"
RT"a[184:185],v245 offset:4096\n"
MF"v[160:163],a[164:167],a[24:27],v[160:163]\n"
MF"v[168:171],a[164:167],a[28:31],v[168:171]\n"
RT"a[186:187],v245 offset:4608\n"
MF"v[168:171],a[160:163],a[12:15],v[168:171]\n"
MF"v[148:151],a[168:171],a[0:3],v[148:151]\n"
WT"lgkmcnt(3)\n"
RT"a[188:189],v245 offset:28672\n"
"s_mov_b32 s49,270336\n"
MF"v[148:151],a[172:175],a[16:19],v[148:151]\n"
BL"a[116:119],v235,s[8:11],s57 offen offset:3072 sc1\n"
MF"v[156:159],a[172:175],a[20:23],v[156:159]\n"
RT"a[190:191],v245 offset:29184\n"
MF"v[156:159],a[168:171],a[4:7],v[156:159]\n"
BS"v[36:39],v250,s[8:11],s49 offen offset:0 sc1\n"
MF"v[164:167],a[168:171],a[8:11],v[164:167]\n"
RT"a[128:129],v236 offset:16384\n"
MF"v[164:167],a[172:175],a[24:27],v[164:167]\n"
RT"a[130:131],v236 offset:16896\n"
MF"v[172:175],a[172:175],a[28:31],v[172:175]\n"
MF"v[172:175],a[168:171],a[12:15],v[172:175]\n"
RT"a[132:133],v236 offset:24576\n"
MF"v[176:179],a[176:179],a[0:3],v[176:179]\n"
WT"lgkmcnt(4)\n"
BL"a[120:123],v235,s[8:11],s58 offen offset:3072 sc1\n"
MF"v[176:179],a[180:183],a[16:19],v[176:179]\n"
RT"a[134:135],v236 offset:25088\n"
MF"v[184:187],a[180:183],a[20:23],v[184:187]\n"
MF"v[184:187],a[176:179],a[4:7],v[184:187]\n"
RT"a[136:137],v237 offset:16384\n"
MF"v[192:195],a[176:179],a[8:11],v[192:195]\n"
MF"v[192:195],a[180:183],a[24:27],v[192:195]\n"
SA"s99,s99,4096\n"
RT"a[138:139],v237 offset:16896\n"
MF"v[200:203],a[180:183],a[28:31],v[200:203]\n"
"s_cmp_eq_u32 s99,20480\n"
BL"a[124:127],v235,s[8:11],s59 offen offset:3072 sc1\n"
MF"v[200:203],a[176:179],a[12:15],v[200:203]\n"
"s_cselect_b32 s99,0,s99\n"
RT"a[140:141],v237 offset:24576\n"
MF"v[180:183],a[184:187],a[0:3],v[180:183]\n"
WT"lgkmcnt(3)\n"
"v_lshrrev_b32 v252,1,v224\n"
MF"v[180:183],a[188:191],a[16:19],v[180:183]\n"
RT"a[142:143],v237 offset:25088\n"
"v_and_b32 v252,56,v252\n"
MF"v[188:191],a[188:191],a[20:23],v[188:191]\n"
"s_lshl_b32 s50,s35,6\n"
MF"v[188:191],a[184:187],a[4:7],v[188:191]\n"
SA"s50,s50,2304\n"
RT"a[144:145],v228 offset:16384\n"
MF"v[196:199],a[184:187],a[8:11],v[196:199]\n"
"buffer_load_dwordx2 v[208:209],v252,s[8:11],s50 offen sc1\n"
MF"v[196:199],a[188:191],a[24:27],v[196:199]\n"
RT"a[146:147],v228 offset:16896\n"
"v_add_u32 v235,s99,v224\n"
MF"v[204:207],a[188:191],a[28:31],v[204:207]\n"
BL"a[192:195],v235,s[8:11],s56 offen offset:0 sc1\n"
MF"v[204:207],a[184:187],a[12:15],v[204:207]\n"
WT"vmcnt(35)\n"
RT"a[148:149],v228 offset:24576\n"
MF"v[80:83],a[128:131],a[32:35],v[80:83]\n"
WT"vmcnt(29)\n"
MF"v[80:83],a[132:135],a[48:51],v[80:83]\n"
WT"vmcnt(27)\n"
RT"a[150:151],v228 offset:25088\n"
MF"v[88:91],a[132:135],a[52:55],v[88:91]\n"
RT"a[152:153],v229 offset:16384\n"
MF"v[88:91],a[128:131],a[36:39],v[88:91]\n"
MF"v[96:99],a[128:131],a[40:43],v[96:99]\n"
WT"vmcnt(26)\n"
RT"a[154:155],v229 offset:16896\n"
MF"v[96:99],a[132:135],a[56:59],v[96:99]\n"
WT"vmcnt(23)\n"
BL"a[196:199],v235,s[8:11],s57 offen offset:0 sc1\n"
MF"v[104:107],a[132:135],a[60:63],v[104:107]\n"
RT"a[156:157],v229 offset:24576\n"
MF"v[104:107],a[128:131],a[44:47],v[104:107]\n"
WT"lgkmcnt(4)\n"
MF"v[84:87],a[136:139],a[32:35],v[84:87]\n"
RT"a[158:159],v229 offset:25088\n"
MF"v[84:87],a[140:143],a[48:51],v[84:87]\n"
MF"v[92:95],a[140:143],a[52:55],v[92:95]\n"
RT"a[160:161],v236 offset:20480\n"
MF"v[92:95],a[136:139],a[36:39],v[92:95]\n"
BL"a[200:203],v235,s[8:11],s58 offen offset:0 sc1\n"
MF"v[100:103],a[136:139],a[40:43],v[100:103]\n"
RT"a[162:163],v236 offset:20992\n"
MF"v[100:103],a[140:143],a[56:59],v[100:103]\n"
BS"v[44:47],v250,s[8:11],s49 offen offset:1024 sc1\n"
MF"v[108:111],a[140:143],a[60:63],v[108:111]\n"
RT"a[164:165],v236 offset:28672\n"
MF"v[108:111],a[136:139],a[44:47],v[108:111]\n"
MF"v[112:115],a[144:147],a[32:35],v[112:115]\n"
WT"lgkmcnt(3)\n"
RT"a[166:167],v236 offset:29184\n"
MF"v[112:115],a[148:151],a[48:51],v[112:115]\n"
MF"v[120:123],a[148:151],a[52:55],v[120:123]\n"
RT"a[168:169],v237 offset:20480\n"
MF"v[120:123],a[144:147],a[36:39],v[120:123]\n"
BL"a[204:207],v235,s[8:11],s59 offen offset:0 sc1\n"
MF"v[128:131],a[144:147],a[40:43],v[128:131]\n"
RT"a[170:171],v237 offset:20992\n"
MF"v[128:131],a[148:151],a[56:59],v[128:131]\n"
RT"a[172:173],v237 offset:28672\n"
MF"v[136:139],a[148:151],a[60:63],v[136:139]\n"
MF"v[136:139],a[144:147],a[44:47],v[136:139]\n"
RT"a[174:175],v237 offset:29184\n"
MF"v[116:119],a[152:155],a[32:35],v[116:119]\n"
MF"v[116:119],a[156:159],a[48:51],v[116:119]\n"
RT"a[176:177],v228 offset:20480\n"
MF"v[124:127],a[156:159],a[52:55],v[124:127]\n"
BL"a[208:211],v235,s[8:11],s56 offen offset:1024 sc1\n"
MF"v[124:127],a[152:155],a[36:39],v[124:127]\n"
RT"a[178:179],v228 offset:20992\n"
MF"v[132:135],a[152:155],a[40:43],v[132:135]\n"
MF"v[132:135],a[156:159],a[56:59],v[132:135]\n"
RT"a[180:181],v228 offset:28672\n"
MF"v[140:143],a[156:159],a[60:63],v[140:143]\n"
MF"v[140:143],a[152:155],a[44:47],v[140:143]\n"
WT"lgkmcnt(3)\n"
RT"a[182:183],v228 offset:29184\n"
MF"v[144:147],a[160:163],a[32:35],v[144:147]\n"
BL"a[212:215],v235,s[8:11],s57 offen offset:1024 sc1\n"
MF"v[144:147],a[164:167],a[48:51],v[144:147]\n"
RT"a[184:185],v229 offset:20480\n"
MF"v[152:155],a[164:167],a[52:55],v[152:155]\n"
MF"v[152:155],a[160:163],a[36:39],v[152:155]\n"
RT"a[186:187],v229 offset:20992\n"
MF"v[160:163],a[160:163],a[40:43],v[160:163]\n"
MF"v[160:163],a[164:167],a[56:59],v[160:163]\n"
RT"a[188:189],v229 offset:28672\n"
MF"v[168:171],a[164:167],a[60:63],v[168:171]\n"
MF"v[168:171],a[160:163],a[44:47],v[168:171]\n"
RT"a[190:191],v229 offset:29184\n"
MF"v[148:151],a[168:171],a[32:35],v[148:151]\n"
BL"a[216:219],v235,s[8:11],s58 offen offset:1024 sc1\n"
MF"v[148:151],a[172:175],a[48:51],v[148:151]\n"
WT"vmcnt(8)\n"
"v_cmp_eq_u64 vcc,s[28:29],v[208:209]\n"
"s_cmp_eq_u64 vcc,exec\n"
"s_cbranch_scc0 L1010_%=\n"
"L1011_%=:\n"
MF"v[156:159],a[172:175],a[52:55],v[156:159]\n"
MF"v[156:159],a[168:171],a[36:39],v[156:159]\n"
MF"v[164:167],a[168:171],a[40:43],v[164:167]\n"
WT"lgkmcnt(0)\n"
SA"s49,s88,270336\n"
MF"v[164:167],a[172:175],a[56:59],v[164:167]\n"
"s_barrier\n"
MF"v[172:175],a[172:175],a[60:63],v[172:175]\n"
SA"m0,s36,0\n"
RT"a[128:129],v246 offset:8192\n"
SA"s48,s39,0\n"
MF"v[172:175],a[168:171],a[44:47],v[172:175]\n"
BS"v[52:55],v250,s[8:11],s49 offen offset:0 sc1\n"
MF"v[176:179],a[176:179],a[32:35],v[176:179]\n"
BL"a[220:223],v235,s[8:11],s59 offen offset:1024 sc1\n"
MF"v[176:179],a[180:183],a[48:51],v[176:179]\n"
RT"a[130:131],v246 offset:8704\n"
MF"v[184:187],a[180:183],a[52:55],v[184:187]\n"
BL"v225,s[8:11],s48 offen offset:0 sc1 lds\n"
MF"v[184:187],a[176:179],a[36:39],v[184:187]\n"
RT"a[132:133],v246 offset:32768\n"
"v_lshlrev_b32 v254,10,v224\n"
MF"v[192:195],a[176:179],a[40:43],v[192:195]\n"
"s_lshl_b32 s94,s34,1\n"
RT"a[134:135],v246 offset:33280\n"
"v_lshrrev_b32 v255,4,v224\n"
MF"v[192:195],a[180:183],a[56:59],v[192:195]\n"
SA"s95,s94,2\n"
BL"a[224:227],v235,s[8:11],s56 offen offset:2048 sc1\n"
MF"v[200:203],a[180:183],a[60:63],v[200:203]\n"
"s_sub_u32 s94,32,s94\n"
RT"a[136:137],v247 offset:8192\n"
MF"v[200:203],a[176:179],a[44:47],v[200:203]\n"
"v_add_u32 v228,s39,v254\n"
MF"v[180:183],a[184:187],a[32:35],v[180:183]\n"
RT"a[138:139],v247 offset:8704\n"
"v_and_b32 v254,1,v255\n"
MF"v[180:183],a[188:191],a[48:51],v[180:183]\n"
BL"v225,s[8:11],s48 offen offset:1024 sc1 lds\n"
MF"v[188:191],a[188:191],a[52:55],v[188:191]\n"
RT"a[140:141],v247 offset:32768\n"
"v_mul_u32_u24 v254,491520,v254\n"
MF"v[188:191],a[184:187],a[36:39],v[188:191]\n"
"v_cmp_gt_u32 vcc,s95,v255\n"
"v_mov_b32 v229,16384\n"
MF"v[196:199],a[184:187],a[40:43],v[196:199]\n"
RT"a[142:143],v247 offset:33280\n"
"v_mov_b32 v253,516096\n"
MF"v[196:199],a[188:191],a[56:59],v[196:199]\n"
BL"a[228:231],v235,s[8:11],s57 offen offset:2048 sc1\n"
MF"v[204:207],a[188:191],a[60:63],v[204:207]\n"
RT"a[144:145],v248 offset:8192\n"
"v_cndmask_b32_e64 v254,v254,0,vcc\n"
MF"v[204:207],a[184:187],a[44:47],v[204:207]\n"
WT"lgkmcnt(3)\n"
"v_cndmask_b32 v229,v229,v253,vcc\n"
"v_cmp_eq_u32 vcc,s94,v255\n"
MF"v[80:83],a[128:131],a[64:67],v[80:83]\n"
RT"a[146:147],v248 offset:8704\n"
"v_mov_b32 v253,507904\n"
MF"v[80:83],a[132:135],a[80:83],v[80:83]\n"
BL"v225,s[8:11],s48 offen offset:2048 sc1 lds\n"
MF"v[88:91],a[132:135],a[84:87],v[88:91]\n"
RT"a[148:149],v248 offset:32768\n"
"v_cndmask_b32 v254,v254,v253,vcc\n"
MF"v[88:91],a[128:131],a[68:71],v[88:91]\n"
BL"a[232:235],v235,s[8:11],s58 offen offset:2048 sc1\n"
MF"v[96:99],a[128:131],a[72:75],v[96:99]\n"
RT"a[150:151],v248 offset:33280\n"
"v_mov_b32 v253,1862270976\n"
MF"v[96:99],a[132:135],a[88:91],v[96:99]\n"
"s_mul_i32 s95,s35,163840\n"
RT"a[152:153],v249 offset:8192\n"
"v_cndmask_b32 v229,v229,v253,vcc\n"
MF"v[104:107],a[132:135],a[92:95],v[104:107]\n"
SA"s95,s95,3768320\n"
"v_add_u32 v228,v228,v254\n"
MF"v[104:107],a[128:131],a[76:79],v[104:107]\n"
RT"a[154:155],v249 offset:8704\n"
"v_subrev_u32 v254,s94,v255\n"
MF"v[84:87],a[136:139],a[64:67],v[84:87]\n"
WT"lgkmcnt(4)\n"
BL"v225,s[8:11],s48 offen offset:3072 sc1 lds\n"
MF"v[84:87],a[140:143],a[80:83],v[84:87]\n"
RT"a[156:157],v249 offset:32768\n"
"v_lshlrev_b32 v254,15,v254\n"
MF"v[92:95],a[140:143],a[84:87],v[92:95]\n"
BL"a[236:239],v235,s[8:11],s59 offen offset:2048 sc1\n"
MF"v[92:95],a[136:139],a[68:71],v[92:95]\n"
RT"a[158:159],v249 offset:33280\n"
"v_add_u32 v254,s95,v254\n"
MF"v[100:103],a[136:139],a[72:75],v[100:103]\n"
"v_cmp_lt_u32 vcc,s94,v255\n"
MF"v[100:103],a[140:143],a[88:91],v[100:103]\n"
"s_mov_b32 s47,s36\n"
RT"a[160:161],v246 offset:12288\n"
"v_cndmask_b32 v228,v228,v254,vcc\n"
MF"v[108:111],a[140:143],a[92:95],v[108:111]\n"
SA"m0,s36,16384\n"
"v_add_u32 v229,v229,v228\n"
SA"s48,s39,516096\n"
MF"v[108:111],a[136:139],a[76:79],v[108:111]\n"
"s_cmp_lt_u32 s30,2\n"
RT"a[162:163],v246 offset:12800\n"
"s_cselect_b32 s48,2130706432,s48\n"
MF"v[112:115],a[144:147],a[64:67],v[112:115]\n"
WT"lgkmcnt(3)\n"
BS"v[60:63],v250,s[8:11],s49 offen offset:1024 sc1\n"
MF"v[112:115],a[148:151],a[80:83],v[112:115]\n"
SA"s46,s36,163840\n"
BL"v225,s[8:11],s48 offen offset:0 sc1 lds\n"
MF"v[120:123],a[148:151],a[84:87],v[120:123]\n"
SA"s40,s36,65536\n"
RT"a[164:165],v246 offset:36864\n"
"v_mov_b32 v226,v224\n"
MF"v[120:123],a[144:147],a[68:71],v[120:123]\n"
"s_mov_b32 s41,0\n"
BL"a[240:243],v235,s[8:11],s56 offen offset:3072 sc1\n"
MF"v[128:131],a[144:147],a[72:75],v[128:131]\n"
"s_mov_b32 s42,2\n"
RT"a[166:167],v246 offset:37376\n"
"v_bfe_u32 v254,v224,4,4\n"
MF"v[128:131],a[148:151],a[88:91],v[128:131]\n"
RT"a[168:169],v247 offset:12288\n"
"v_lshrrev_b32 v255,8,v224\n"
MF"v[136:139],a[148:151],a[92:95],v[136:139]\n"
"s_lshl_b32 s94,s30,4\n"
"v_lshlrev_b32 v255,2,v255\n"
"v_sub_u32 v254,v254,v255\n"
MF"v[136:139],a[144:147],a[76:79],v[136:139]\n"
RT"a[170:171],v247 offset:12800\n"
"v_add_u32 v254,s94,v254\n"
MF"v[116:119],a[152:155],a[64:67],v[116:119]\n"
WT"lgkmcnt(4)\n"
RT"a[172:173],v247 offset:36864\n"
"v_mov_b32 v255,0xff800000\n"
MF"v[116:119],a[156:159],a[80:83],v[116:119]\n"
BL"a[244:247],v235,s[8:11],s57 offen offset:3072 sc1\n"
MF"v[124:127],a[156:159],a[84:87],v[124:127]\n"
BL"v225,s[8:11],s48 offen offset:1024 sc1 lds\n"
MF"v[124:127],a[152:155],a[68:71],v[124:127]\n"
RT"a[174:175],v247 offset:37376\n"
"v_cmp_gt_i32_e64 vcc,0,v254\n"
MF"v[132:135],a[152:155],a[72:75],v[132:135]\n"
RT"a[176:177],v248 offset:12288\n"
"v_cndmask_b32_e64 v208,0,v255,vcc\n"
MF"v[132:135],a[156:159],a[88:91],v[132:135]\n"
"v_cmp_gt_i32_e64 s[52:53],1,v254\n"
"v_cmp_gt_i32_e64 s[62:63],2,v254\n"
MF"v[140:143],a[156:159],a[92:95],v[140:143]\n"
RT"a[178:179],v248 offset:12800\n"
"v_cndmask_b32_e64 v209,0,v255,s[52:53]\n"
MF"v[140:143],a[152:155],a[76:79],v[140:143]\n"
BL"a[248:251],v235,s[8:11],s58 offen offset:3072 sc1\n"
MF"v[144:147],a[160:163],a[64:67],v[144:147]\n"
WT"lgkmcnt(3)\n"
RT"a[180:181],v248 offset:36864\n"
"v_cndmask_b32_e64 v210,0,v255,s[62:63]\n"
MF"v[144:147],a[164:167],a[80:83],v[144:147]\n"
BL"v225,s[8:11],s48 offen offset:2048 sc1 lds\n"
MF"v[152:155],a[164:167],a[84:87],v[152:155]\n"
RT"a[182:183],v248 offset:37376\n"
"v_cmp_gt_i32_e64 vcc,3,v254\n"
MF"v[152:155],a[160:163],a[68:71],v[152:155]\n"
"v_cmp_gt_i32_e64 s[52:53],16,v254\n"
"v_cndmask_b32_e64 v211,0,v255,vcc\n"
MF"v[160:163],a[160:163],a[72:75],v[160:163]\n"
RT"a[184:185],v249 offset:12288\n"
"v_cndmask_b32_e64 v212,0,v255,s[52:53]\n"
MF"v[160:163],a[164:167],a[88:91],v[160:163]\n"
"v_cmp_gt_i32_e64 s[62:63],17,v254\n"
"v_cmp_gt_i32_e64 vcc,18,v254\n"
MF"v[168:171],a[164:167],a[92:95],v[168:171]\n"
RT"a[186:187],v249 offset:12800\n"
"v_cndmask_b32_e64 v213,0,v255,s[62:63]\n"
MF"v[168:171],a[160:163],a[76:79],v[168:171]\n"
BL"a[252:255],v235,s[8:11],s59 offen offset:3072 sc1\n"
MF"v[148:151],a[168:171],a[64:67],v[148:151]\n"
WT"lgkmcnt(3)\n"
RT"a[188:189],v249 offset:36864\n"
"v_cndmask_b32_e64 v214,0,v255,vcc\n"
MF"v[148:151],a[172:175],a[80:83],v[148:151]\n"
BL"v225,s[8:11],s48 offen offset:3072 sc1 lds\n"
MF"v[156:159],a[172:175],a[84:87],v[156:159]\n"
RT"a[190:191],v249 offset:37376\n"
"v_cmp_gt_i32_e64 s[52:53],19,v254\n"
MF"v[156:159],a[168:171],a[68:71],v[156:159]\n"
RT"a[128:129],v242 offset:8192\n"
"v_cndmask_b32_e64 v215,0,v255,s[52:53]\n"
MF"v[164:167],a[168:171],a[72:75],v[164:167]\n"
"v_cmp_gt_i32_e64 s[62:63],32,v254\n"
"v_cmp_gt_i32_e64 vcc,33,v254\n"
MF"v[164:167],a[172:175],a[88:91],v[164:167]\n"
RT"a[130:131],v242 offset:8704\n"
"v_cndmask_b32_e64 v216,0,v255,s[62:63]\n"
MF"v[172:175],a[172:175],a[92:95],v[172:175]\n"
SA"m0,s36,32768\n"
"v_cndmask_b32_e64 v217,0,v255,vcc\n"
"v_cmp_gt_i32_e64 s[52:53],34,v254\n"
MF"v[172:175],a[168:171],a[76:79],v[172:175]\n"
SA"s48,s39,16384\n"
RT"a[132:133],v242 offset:32768\n"
"v_cndmask_b32_e64 v218,0,v255,s[52:53]\n"
MF"v[176:179],a[176:179],a[64:67],v[176:179]\n"
WT"lgkmcnt(4)\n"
BL"v225,s[8:11],s48 offen offset:0 sc1 lds\n"
MF"v[176:179],a[180:183],a[80:83],v[176:179]\n"
"s_mov_b32 s49,278528\n"
RT"a[134:135],v242 offset:33280\n"
"v_cmp_gt_i32_e64 s[62:63],35,v254\n"
MF"v[184:187],a[180:183],a[84:87],v[184:187]\n"
BS"v[64:67],v250,s[8:11],s49 offen offset:0 sc1\n"
MF"v[184:187],a[176:179],a[68:71],v[184:187]\n"
RT"a[136:137],v243 offset:8192\n"
"v_cndmask_b32_e64 v219,0,v255,s[62:63]\n"
MF"v[192:195],a[176:179],a[72:75],v[192:195]\n"
"s_lshl_b32 s59,s34,5\n"
"v_cmp_gt_i32_e64 vcc,48,v254\n"
"v_cmp_gt_i32_e64 s[52:53],49,v254\n"
MF"v[192:195],a[180:183],a[88:91],v[192:195]\n"
"s_sub_u32 s60,480,s59\n"
RT"a[138:139],v243 offset:8704\n"
"v_cndmask_b32_e64 v220,0,v255,vcc\n"
MF"v[200:203],a[180:183],a[92:95],v[200:203]\n"
SA"s59,s59,s35\n"
"v_cndmask_b32_e64 v221,0,v255,s[52:53]\n"
"v_cmp_gt_i32_e64 s[62:63],50,v254\n"
MF"v[200:203],a[176:179],a[76:79],v[200:203]\n"
SA"s60,s60,s35\n"
RT"a[140:141],v243 offset:32768\n"
"v_cndmask_b32_e64 v222,0,v255,s[62:63]\n"
MF"v[180:183],a[184:187],a[64:67],v[180:183]\n"
WT"lgkmcnt(3)\n"
BL"v225,s[8:11],s48 offen offset:1024 sc1 lds\n"
MF"v[180:183],a[188:191],a[80:83],v[180:183]\n"
"s_lshl_b32 s59,s59,15\n"
RT"a[142:143],v243 offset:33280\n"
"v_cmp_gt_i32_e64 vcc,51,v254\n"
MF"v[188:191],a[188:191],a[84:87],v[188:191]\n"
"s_lshl_b32 s60,s60,15\n"
"v_cndmask_b32_e64 v223,0,v255,vcc\n"
SA"s50,s36,44826624\n"
MF"v[188:191],a[184:187],a[68:71],v[188:191]\n"
SA"s59,s59,s50\n"
RT"a[144:145],v244 offset:8192\n"
SA"s60,s60,s50\n"
MF"v[196:199],a[184:187],a[72:75],v[196:199]\n"
"s_lshl_b32 s61,s34,3\n"
SA"s61,s61,s32\n"
"s_lshl_b32 s61,s61,2\n"
MF"v[196:199],a[188:191],a[88:91],v[196:199]\n"
SA"s61,s61,s30\n"
RT"a[146:147],v244 offset:8704\n"
"s_lshl_b32 s61,s61,2\n"
MF"v[204:207],a[188:191],a[92:95],v[204:207]\n"
SA"s61,s61,s33\n"
"s_lshl_b32 s61,s61,6\n"
MF"v[204:207],a[184:187],a[76:79],v[204:207]\n"
SA"s61,s61,393216\n"
RT"a[148:149],v244 offset:32768\n"
"s_lshl_b32 s98,s35,3\n"
MF"v[80:83],a[128:131],a[96:99],v[80:83]\n"
SA"s98,s98,s34\n"
BL"v225,s[8:11],s48 offen offset:2048 sc1 lds\n"
MF"v[80:83],a[132:135],a[112:115],v[80:83]\n"
"s_lshl_b32 s98,s98,3\n"
RT"a[150:151],v244 offset:33280\n"
MF"v[88:91],a[132:135],a[116:119],v[88:91]\n"
SA"s98,s98,4352\n"
RT"a[152:153],v245 offset:8192\n"
SA"s98,s98,s45\n"
MF"v[88:91],a[128:131],a[100:103],v[88:91]\n"
"s_lshl_b32 s50,s34,2\n"
MF"v[96:99],a[128:131],a[104:107],v[96:99]\n"
"s_mul_i32 s51,s33,0x1011100\n"
RT"a[154:155],v245 offset:8704\n"
SA"s51,s51,0xf48c00\n"
MF"v[96:99],a[132:135],a[120:123],v[96:99]\n"
"s_lshr_b32 s51,s51,s50\n"
MF"v[104:107],a[132:135],a[124:127],v[104:107]\n"
"s_and_b32 s51,s51,15\n"
RT"a[156:157],v245 offset:32768\n"
"s_lshr_b32 s50,s31,3\n"
MF"v[104:107],a[128:131],a[108:111],v[104:107]\n"
WT"lgkmcnt(4)\n"
"s_cmp_eq_u32 s50,6\n"
"s_cselect_b32 s51,15,s51\n"
MF"v[84:87],a[136:139],a[96:99],v[84:87]\n"
"s_cmp_eq_u32 s50,26\n"
RT"a[158:159],v245 offset:33280\n"
"s_cselect_b32 s51,0,s51\n"
MF"v[84:87],a[140:143],a[112:115],v[84:87]\n"
"s_mul_i32 s58,s32,14\n"
BL"v225,s[8:11],s48 offen offset:3072 sc1 lds\n"
MF"v[92:95],a[140:143],a[116:119],v[92:95]\n"
SA"s58,s58,s51\n"
RT"a[160:161],v242 offset:12288\n"
"s_sub_u32 s58,s58,1\n"
MF"v[92:95],a[136:139],a[100:103],v[92:95]\n"
"s_cmp_lt_u32 s58,108\n"
"s_cselect_b32 s90,s51,0\n"
"s_cmp_lg_u32 s90,0\n"
MF"v[100:103],a[136:139],a[104:107],v[100:103]\n"
"s_cselect_b32 s57,1,0\n"
RT"a[162:163],v242 offset:12800\n"
"s_cmp_eq_u32 s51,15\n"
MF"v[100:103],a[140:143],a[120:123],v[100:103]\n"
"s_cselect_b32 s57,2,s57\n"
"s_cmp_eq_u32 s50,6\n"
"s_cselect_b32 s90,4,s33\n"
MF"v[108:111],a[140:143],a[124:127],v[108:111]\n"
"s_mul_i32 s50,s32,5\n"
RT"a[164:165],v242 offset:36864\n"
SA"m0,s36,49152\n"
MF"v[108:111],a[136:139],a[108:111],v[108:111]\n"
SA"s48,s39,532480\n"
SA"s90,s90,s50\n"
"s_cmp_eq_u32 s57,2\n"
MF"v[112:115],a[144:147],a[96:99],v[112:115]\n"
WT"lgkmcnt(3)\n"
RT"a[166:167],v242 offset:37376\n"
"s_cselect_b32 s58,s90,s58\n"
MF"v[112:115],a[148:151],a[112:115],v[112:115]\n"
"s_lshl_b32 s56,s34,1\n"
BL"v225,s[8:11],s48 offen offset:0 sc1 lds\n"
MF"v[120:123],a[148:151],a[116:119],v[120:123]\n"
"s_sub_u32 s56,s56,2\n"
RT"a[168:169],v243 offset:12288\n"
"s_cmp_eq_u32 s34,7\n"
MF"v[120:123],a[144:147],a[100:103],v[120:123]\n"
"s_cselect_b32 s56,2,s56\n"
"s_cmp_eq_u32 s57,2\n"
"s_cselect_b32 s56,5,s56\n"
MF"v[128:131],a[144:147],a[104:107],v[128:131]\n"
"s_cmp_lt_u32 s30,2\n"
RT"a[170:171],v243 offset:12800\n"
"s_cselect_b64 s[24:25],s[78:79],s[80:81]\n"
MF"v[128:131],a[148:151],a[120:123],v[128:131]\n"
"s_cmp_eq_u32 s57,2\n"
BS"v[68:71],v250,s[8:11],s49 offen offset:1024 sc1\n"
MF"v[136:139],a[148:151],a[124:127],v[136:139]\n"
"s_cselect_b64 s[24:25],s[82:83],s[24:25]\n"
RT"a[172:173],v243 offset:36864\n"
"s_and_b32 s50,s30,1\n"
MF"v[136:139],a[144:147],a[108:111],v[136:139]\n"
"s_mul_i32 s50,s50,10\n"
RT"a[174:175],v243 offset:37376\n"
"s_mul_i32 s51,s30,14\n"
MF"v[116:119],a[152:155],a[96:99],v[116:119]\n"
"s_min_u32 s51,s51,41\n"
BL"v225,s[8:11],s48 offen offset:1024 sc1 lds\n"
MF"v[116:119],a[156:159],a[112:115],v[116:119]\n"
"s_cmp_eq_u32 s57,2\n"
RT"a[176:177],v244 offset:12288\n"
"s_cselect_b32 s50,s51,s50\n"
MF"v[124:127],a[156:159],a[116:119],v[124:127]\n"
"s_cmp_lt_u32 s30,2\n"
"s_cselect_b32 s51,14,13\n"
"s_cmp_lt_u32 s58,108\n"
MF"v[124:127],a[152:155],a[100:103],v[124:127]\n"
"s_cselect_b32 s92,10,0\n"
RT"a[178:179],v244 offset:12800\n"
"s_cmp_eq_u32 s57,2\n"
MF"v[132:135],a[152:155],a[104:107],v[132:135]\n"
"s_cselect_b32 s92,s51,s92\n"
"s_mov_b32 s51,3456\n"
"s_cmp_eq_u32 s57,2\n"
MF"v[132:135],a[156:159],a[120:123],v[132:135]\n"
"s_cselect_b32 s51,1280,s51\n"
RT"a[180:181],v244 offset:36864\n"
"s_lshl_b32 s62,s51,5\n"
MF"v[140:143],a[156:159],a[124:127],v[140:143]\n"
"s_mul_i32 s26,s62,s92\n"
"s_mul_i32 s90,s62,s50\n"
"s_lshl_b32 s91,s58,5\n"
MF"v[140:143],a[152:155],a[108:111],v[140:143]\n"
WT"lgkmcnt(3)\n"
RT"a[182:183],v244 offset:37376\n"
SA"s90,s90,s91\n"
MF"v[144:147],a[160:163],a[96:99],v[144:147]\n"
SA"s24,s24,s90\n"
BL"v225,s[8:11],s48 offen offset:2048 sc1 lds\n"
MF"v[144:147],a[164:167],a[112:115],v[144:147]\n"
"s_addc_u32 s25,s25,0\n"
RT"a[184:185],v245 offset:12288\n"
"s_and_b32 s25,s25,0xffff\n"
MF"v[152:155],a[164:167],a[116:119],v[152:155]\n"
"s_sub_u32 s90,s51,32\n"
"v_lshrrev_b32 v235,5,v224\n"
"v_mul_u32_u24 v235,s51,v235\n"
MF"v[152:155],a[160:163],a[100:103],v[152:155]\n"
"s_sub_u32 s26,s26,s90\n"
RT"a[186:187],v245 offset:12800\n"
"v_and_b32 v239,16,v224\n"
MF"v[160:163],a[160:163],a[104:107],v[160:163]\n"
"s_cselect_b32 s26,0,s26\n"
"v_add_u32 v235,v235,v239\n"
"s_mov_b32 s27,0x20000\n"
MF"v[160:163],a[164:167],a[120:123],v[160:163]\n"
"s_mul_i32 s63,s35,20480\n"
RT"a[188:189],v245 offset:36864\n"
SA"s63,s63,3145728\n"
MF"v[168:171],a[164:167],a[124:127],v[168:171]\n"
"s_cmp_eq_u32 s57,2\n"
"s_cselect_b32 s63,16384,s63\n"
MF"v[168:171],a[160:163],a[108:111],v[168:171]\n"
"s_lshl_b32 s50,s50,10\n"
RT"a[190:191],v245 offset:37376\n"
SA"s63,s63,s50\n"
MF"v[148:151],a[168:171],a[96:99],v[148:151]\n"
"s_mov_b32 s50,1065353216\n"
BL"v225,s[8:11],s48 offen offset:3072 sc1 lds\n"
MF"v[148:151],a[172:175],a[112:115],v[148:151]\n"
"s_cmp_lg_u32 s92,0\n"
"s_cselect_b32 s50,s45,s50\n"
"s_mov_b32 s93,16384\n"
MF"v[156:159],a[172:175],a[116:119],v[156:159]\n"
"s_cmp_eq_u32 s57,2\n"
"s_cselect_b32 s93,71680,s93\n"
MF"v[156:159],a[168:171],a[100:103],v[156:159]\n"
"s_lshl_b32 s51,s58,9\n"
MF"v[164:167],a[168:171],a[104:107],v[164:167]\n"
SA"s93,s93,s51\n"
SA"s93,s93,s50\n"
MF"v[164:167],a[172:175],a[120:123],v[164:167]\n"
"s_mov_b32 s94,6912\n"
"s_barrier\n"
MF"v[172:175],a[172:175],a[124:127],v[172:175]\n"
"s_cmp_eq_u32 s57,2\n"
RT"a[128:129],v246 offset:16384\n"
"s_cselect_b32 s94,6400,s94\n"
MF"v[172:175],a[168:171],a[108:111],v[172:175]\n"
WT"lgkmcnt(1)\n"
"s_lshl_b32 s51,s58,3\n"
SA"s94,s94,s51\n"
MF"v[176:179],a[176:179],a[96:99],v[176:179]\n"
SA"s94,s94,s50\n"
RT"a[130:131],v246 offset:16896\n"
"s_mov_b32 s52,0\n"
MF"v[176:179],a[180:183],a[112:115],v[176:179]\n"
"s_lshl_b32 s50,s35,6\n"
RT"a[132:133],v246 offset:40960\n"
MF"v[184:187],a[180:183],a[116:119],v[184:187]\n"
SA"s50,s50,2304\n"
MF"v[184:187],a[176:179],a[100:103],v[184:187]\n"
"s_cmp_eq_u32 s57,2\n"
RT"a[134:135],v246 offset:41472\n"
"s_cselect_b32 s50,6912,s50\n"
MF"v[192:195],a[176:179],a[104:107],v[192:195]\n"
"s_mov_b32 s51,48\n"
RT"a[136:137],v242 offset:16384\n"
SA"s49,s88,278528\n"
MF"v[192:195],a[180:183],a[120:123],v[192:195]\n"
"s_cmp_eq_u32 s57,2\n"
"s_cselect_b32 s51,848,s51\n"
"v_min_u32 v238,s51,v224\n"
MF"v[200:203],a[180:183],a[124:127],v[200:203]\n"
RT"a[138:139],v242 offset:16896\n"
"v_add_u32 v238,s50,v238\n"
MF"v[200:203],a[176:179],a[108:111],v[200:203]\n"
"s_mov_b32 s16,0\n"
BS"v[72:75],v250,s[8:11],s49 offen offset:0 sc1\n"
MF"v[180:183],a[184:187],a[96:99],v[180:183]\n"
"s_mov_b32 s20,8\n"
RT"a[140:141],v242 offset:40960\n"
"v_and_b32 v239,0x80,v224\n"
MF"v[180:183],a[188:191],a[112:115],v[180:183]\n"
"s_mov_b32 s18,2130706432\n"
RT"a[142:143],v242 offset:41472\n"
"v_lshl_add_u32 v236,v239,23,v224\n"
MF"v[188:191],a[188:191],a[116:119],v[188:191]\n"
"s_lshl_b32 s21,s32,4\n"
"s_lshr_b32 s22,s30,1\n"
MF"v[188:191],a[184:187],a[100:103],v[188:191]\n"
SA"s21,s21,s22\n"
RT"a[144:145],v247 offset:16384\n"
MF"v[196:199],a[184:187],a[104:107],v[196:199]\n"
"s_mul_i32 s21,s21,40960\n"
"s_lshl_b32 s22,s33,1\n"
MF"v[196:199],a[188:191],a[120:123],v[196:199]\n"
"s_and_b32 s23,s30,1\n"
RT"a[146:147],v247 offset:16896\n"
MF"v[204:207],a[188:191],a[124:127],v[204:207]\n"
SA"s22,s22,s23\n"
RT"a[148:149],v247 offset:40960\n"
"s_mul_i32 s22,s22,160\n"
MF"v[204:207],a[184:187],a[108:111],v[204:207]\n"
WT"vmcnt(37) lgkmcnt(4)\n"
SA"s17,s21,s22\n"
MF"v[80:83],a[128:131],a[192:195],v[80:83]\n"
WT"vmcnt(32)\n"
RT"a[150:151],v247 offset:41472\n"
"v_lshrrev_b32 v240,4,v224\n"
MF"v[80:83],a[132:135],a[208:211],v[80:83]\n"
WT"vmcnt(31)\n"
RT"a[152:153],v243 offset:16384\n"
"v_add_u32 v240,0,v240\n"
MF"v[88:91],a[132:135],a[212:215],v[88:91]\n"
"v_mul_u32_u24 v240,205,v240\n"
"v_lshrrev_b32 v240,11,v240\n"
MF"v[88:91],a[128:131],a[196:199],v[88:91]\n"
RT"a[154:155],v243 offset:16896\n"
"v_mul_u32_u24 v240,1120,v240\n"
MF"v[96:99],a[128:131],a[200:203],v[96:99]\n"
WT"vmcnt(30)\n"
RT"a[156:157],v243 offset:40960\n"
"v_add_u32 v240,v240,v224\n"
MF"v[96:99],a[132:135],a[216:219],v[96:99]\n"
WT"vmcnt(28)\n"
"v_lshrrev_b32 v241,4,v224\n"
"v_add_u32 v241,64,v241\n"
MF"v[104:107],a[132:135],a[220:223],v[104:107]\n"
RT"a[158:159],v243 offset:41472\n"
"v_mul_u32_u24 v241,205,v241\n"
MF"v[104:107],a[128:131],a[204:207],v[104:107]\n"
WT"vmcnt(26)\n"
"v_lshrrev_b32 v241,11,v241\n"
"v_mul_u32_u24 v241,1120,v241\n"
MF"v[80:83],a[136:139],a[224:227],v[80:83]\n"
WT"vmcnt(17) lgkmcnt(3)\n"
RT"a[160:161],v248 offset:16384\n"
"v_add_u32 v241,v241,v224\n"
MF"v[80:83],a[140:143],a[240:243],v[80:83]\n"
WT"vmcnt(16)\n"
"v_lshrrev_b32 v251,4,v224\n"
"v_add_u32 v251,128,v251\n"
MF"v[88:91],a[140:143],a[244:247],v[88:91]\n"
RT"a[162:163],v248 offset:16896\n"
"v_mul_u32_u24 v251,205,v251\n"
MF"v[88:91],a[136:139],a[228:231],v[88:91]\n"
RT"a[164:165],v248 offset:40960\n"
"v_lshrrev_b32 v251,11,v251\n"
MF"v[96:99],a[136:139],a[232:235],v[96:99]\n"
WT"vmcnt(14)\n"
"v_mul_u32_u24 v251,1120,v251\n"
"v_add_u32 v251,v251,v224\n"
MF"v[96:99],a[140:143],a[248:251],v[96:99]\n"
WT"vmcnt(12)\n"
RT"a[166:167],v248 offset:41472\n"
"v_and_b32 v237,512,v224\n"
MF"v[104:107],a[140:143],a[252:255],v[104:107]\n"
RT"a[168:169],v244 offset:16384\n"
"v_lshl_add_u32 v251,v237,20,v251\n"
MF"v[104:107],a[136:139],a[236:239],v[104:107]\n"
"v_lshrrev_b32 v252,6,v224\n"
"v_mul_u32_u24 v252,160,v252\n"
MF"v[84:87],a[144:147],a[192:195],v[84:87]\n"
RT"a[170:171],v244 offset:16896\n"
"v_bfe_u32 v237,v224,4,2\n"
MF"v[84:87],a[148:151],a[208:211],v[84:87]\n"
"s_cmp_eq_u32 s34,7\n"
RT"a[172:173],v244 offset:40960\n"
"v_lshl_add_u32 v252,v237,3,v252\n"
MF"v[92:95],a[148:151],a[212:215],v[92:95]\n"
"s_cselect_b32 s57,3,s57\n"
"v_lshrrev_b32 v254,4,v224\n"
"v_min_u32 v254,49,v254\n"
MF"v[92:95],a[144:147],a[196:199],v[92:95]\n"
RT"a[174:175],v244 offset:41472\n"
"v_mul_u32_u24 v253,52,v254\n"
MF"v[100:103],a[144:147],a[200:203],v[100:103]\n"
"v_lshrrev_b32 v253,8,v253\n"
MF"v[100:103],a[148:151],a[216:219],v[100:103]\n"
RT"a[176:177],v249 offset:16384\n"
"v_mul_u32_u24 v253,2901,v253\n"
MF"v[108:111],a[148:151],a[220:223],v[108:111]\n"
"v_mul_u32_u24 v254,111,v254\n"
"v_add_u32 v254,v254,v253\n"
MF"v[108:111],a[144:147],a[204:207],v[108:111]\n"
WT"lgkmcnt(3)\n"
RT"a[178:179],v249 offset:16896\n"
"v_and_b32 v254,-4,v254\n"
MF"v[84:87],a[152:155],a[224:227],v[84:87]\n"
RT"a[180:181],v249 offset:40960\n"
"v_lshrrev_b32 v253,5,v224\n"
MF"v[84:87],a[156:159],a[240:243],v[84:87]\n"
BS"v[76:79],v250,s[8:11],s49 offen offset:1024 sc1\n"
MF"v[92:95],a[156:159],a[244:247],v[92:95]\n"
RT"a[182:183],v249 offset:41472\n"
"v_min_u32 v253,13,v253\n"
MF"v[92:95],a[152:155],a[228:231],v[92:95]\n"
RT"a[184:185],v245 offset:16384\n"
"v_mul_u32_u24 v253,1280,v253\n"
MF"v[100:103],a[152:155],a[232:235],v[100:103]\n"
"v_bfe_u32 v237,v224,4,1\n"
"v_mul_u32_u24 v237,156,v237\n"
MF"v[100:103],a[156:159],a[248:251],v[100:103]\n"
"s_cmp_lt_u32 s30,2\n"
RT"a[186:187],v245 offset:16896\n"
"v_add_u32 v253,v253,v237\n"
MF"v[108:111],a[156:159],a[252:255],v[108:111]\n"
"s_cselect_b64 s[12:13],s[78:79],s[80:81]\n"
RT"a[188:189],v245 offset:40960\n"
"s_and_b32 s13,s13,0xffff\n"
MF"v[108:111],a[152:155],a[236:239],v[108:111]\n"
"s_mov_b32 s14,2211840\n"
"s_mov_b32 s15,0x20000\n"
MF"v[112:115],a[160:163],a[192:195],v[112:115]\n"
"s_lshr_b32 s90,s31,3\n"
RT"a[190:191],v245 offset:41472\n"
MF"v[112:115],a[164:167],a[208:211],v[112:115]\n"
"s_lshl_b32 s91,s90,1\n"
MF"v[120:123],a[164:167],a[212:215],v[120:123]\n"
"s_and_b32 s50,s30,1\n"
RT"a[128:129],v246 offset:20480\n"
PK"v80,v80,v81\n"
MF"v[120:123],a[160:163],a[196:199],v[120:123]\n"
SA"s91,s91,s50\n"
PK"v81,v82,v83\n"
PK"v82,v84,v85\n"
MF"v[128:131],a[160:163],a[200:203],v[128:131]\n"
"s_mul_i32 s91,s91,34560\n"
RT"a[130:131],v246 offset:20992\n"
PK"v83,v86,v87\n"
MF"v[128:131],a[164:167],a[216:219],v[128:131]\n"
"s_mul_i32 s50,s32,448\n"
RT"a[132:133],v246 offset:45056\n"
"v_accvgpr_write_b32 a0,v80\n"
MF"v[136:139],a[164:167],a[220:223],v[136:139]\n"
SA"s91,s91,s50\n"
"v_accvgpr_write_b32 a1,v81\n"
"v_accvgpr_write_b32 a2,v82\n"
MF"v[136:139],a[160:163],a[204:207],v[136:139]\n"
"buffer_load_dword v255,v254,s[12:15],s91 offen\n"
MF"v[112:115],a[168:171],a[224:227],v[112:115]\n"
WT"lgkmcnt(3)\n"
RT"a[134:135],v246 offset:45568\n"
"v_accvgpr_write_b32 a3,v83\n"
MF"v[112:115],a[172:175],a[240:243],v[112:115]\n"
"s_mov_b32 s12,s82\n"
RT"a[136:137],v242 offset:20480\n"
PK"v88,v88,v89\n"
MF"v[120:123],a[172:175],a[244:247],v[120:123]\n"
"s_and_b32 s13,s83,0xffff\n"
RT"a[138:139],v242 offset:20992\n"
PK"v89,v90,v91\n"
MF"v[120:123],a[168:171],a[228:231],v[120:123]\n"
"s_lshl_b32 s90,s90,2\n"
RT"a[140:141],v242 offset:45056\n"
PK"v90,v92,v93\n"
MF"v[128:131],a[168:171],a[232:235],v[128:131]\n"
SA"s90,s90,s30\n"
PK"v91,v94,v95\n"
"v_accvgpr_write_b32 a16,v88\n"
MF"v[128:131],a[172:175],a[248:251],v[128:131]\n"
"s_mul_i32 s90,s90,17920\n"
RT"a[142:143],v242 offset:45568\n"
"v_accvgpr_write_b32 a17,v89\n"
MF"v[136:139],a[172:175],a[252:255],v[136:139]\n"
"s_mul_i32 s50,s32,160\n"
"v_accvgpr_write_b32 a18,v90\n"
"v_accvgpr_write_b32 a19,v91\n"
MF"v[136:139],a[168:171],a[236:239],v[136:139]\n"
SA"s90,s90,s50\n"
RT"a[144:145],v247 offset:20480\n"
PK"v96,v96,v97\n"
MF"v[116:119],a[176:179],a[192:195],v[116:119]\n"
"buffer_load_dword v255,v253,s[12:15],s90 offen\n"
MF"v[116:119],a[180:183],a[208:211],v[116:119]\n"
RT"a[146:147],v247 offset:20992\n"
"v_min_u32 v253,624,v224\n"
MF"v[124:127],a[180:183],a[212:215],v[124:127]\n"
RT"a[148:149],v247 offset:45056\n"
"v_lshlrev_b32 v253,3,v253\n"
MF"v[124:127],a[176:179],a[196:199],v[124:127]\n"
PK"v97,v98,v99\n"
PK"v98,v100,v101\n"
MF"v[132:135],a[176:179],a[200:203],v[132:135]\n"
RT"a[150:151],v247 offset:45568\n"
PK"v99,v102,v103\n"
MF"v[132:135],a[180:183],a[216:219],v[132:135]\n"
RT"a[152:153],v243 offset:20480\n"
"v_accvgpr_write_b32 a32,v96\n"
MF"v[140:143],a[180:183],a[220:223],v[140:143]\n"
"v_accvgpr_write_b32 a33,v97\n"
"v_accvgpr_write_b32 a34,v98\n"
MF"v[140:143],a[176:179],a[204:207],v[140:143]\n"
RT"a[154:155],v243 offset:20992\n"
"v_accvgpr_write_b32 a35,v99\n"
MF"v[116:119],a[184:187],a[224:227],v[116:119]\n"
RT"a[156:157],v243 offset:45056\n"
PK"v104,v104,v105\n"
MF"v[116:119],a[188:191],a[240:243],v[116:119]\n"
PK"v105,v106,v107\n"
PK"v106,v108,v109\n"
MF"v[124:127],a[188:191],a[244:247],v[124:127]\n"
RT"a[158:159],v243 offset:45568\n"
PK"v107,v110,v111\n"
MF"v[124:127],a[184:187],a[228:231],v[124:127]\n"
"v_accvgpr_write_b32 a48,v104\n"
"v_accvgpr_write_b32 a49,v105\n"
MF"v[132:135],a[184:187],a[232:235],v[132:135]\n"
RT"a[160:161],v248 offset:20480\n"
"v_accvgpr_write_b32 a50,v106\n"
MF"v[132:135],a[188:191],a[248:251],v[132:135]\n"
"v_accvgpr_write_b32 a51,v107\n"
MF"v[140:143],a[188:191],a[252:255],v[140:143]\n"
RT"a[162:163],v248 offset:20992\n"
MF"v[140:143],a[184:187],a[236:239],v[140:143]\n"
WT"lgkmcnt(3)\n"
RT"a[164:165],v248 offset:45056\n"
MF"v[144:147],a[128:131],a[192:195],v[144:147]\n"
MF"v[144:147],a[132:135],a[208:211],v[144:147]\n"
RT"a[166:167],v248 offset:45568\n"
MF"v[152:155],a[132:135],a[212:215],v[152:155]\n"
RT"a[168:169],v244 offset:20480\n"
PK"v112,v112,v113\n"
MF"v[152:155],a[128:131],a[196:199],v[152:155]\n"
PK"v113,v114,v115\n"
PK"v114,v116,v117\n"
MF"v[160:163],a[128:131],a[200:203],v[160:163]\n"
RT"a[170:171],v244 offset:20992\n"
PK"v115,v118,v119\n"
MF"v[160:163],a[132:135],a[216:219],v[160:163]\n"
RT"a[172:173],v244 offset:45056\n"
"v_accvgpr_write_b32 a4,v112\n"
MF"v[168:171],a[132:135],a[220:223],v[168:171]\n"
"v_accvgpr_write_b32 a5,v113\n"
"v_accvgpr_write_b32 a6,v114\n"
MF"v[168:171],a[128:131],a[204:207],v[168:171]\n"
RT"a[174:175],v244 offset:45568\n"
"v_accvgpr_write_b32 a7,v115\n"
MF"v[144:147],a[136:139],a[224:227],v[144:147]\n"
PK"v120,v120,v121\n"
PK"v121,v122,v123\n"
MF"v[144:147],a[140:143],a[240:243],v[144:147]\n"
RT"a[176:177],v249 offset:20480\n"
PK"v122,v124,v125\n"
MF"v[152:155],a[140:143],a[244:247],v[152:155]\n"
PK"v123,v126,v127\n"
"v_accvgpr_write_b32 a20,v120\n"
MF"v[152:155],a[136:139],a[228:231],v[152:155]\n"
RT"a[178:179],v249 offset:20992\n"
"v_accvgpr_write_b32 a21,v121\n"
MF"v[160:163],a[136:139],a[232:235],v[160:163]\n"
RT"a[180:181],v249 offset:45056\n"
"v_accvgpr_write_b32 a22,v122\n"
MF"v[160:163],a[140:143],a[248:251],v[160:163]\n"
"v_accvgpr_write_b32 a23,v123\n"
MF"v[168:171],a[140:143],a[252:255],v[168:171]\n"
RT"a[182:183],v249 offset:45568\n"
PK"v128,v128,v129\n"
MF"v[168:171],a[136:139],a[236:239],v[168:171]\n"
RT"a[184:185],v245 offset:20480\n"
PK"v129,v130,v131\n"
MF"v[148:151],a[144:147],a[192:195],v[148:151]\n"
PK"v130,v132,v133\n"
PK"v131,v134,v135\n"
MF"v[148:151],a[148:151],a[208:211],v[148:151]\n"
RT"a[186:187],v245 offset:20992\n"
"v_accvgpr_write_b32 a36,v128\n"
MF"v[156:159],a[148:151],a[212:215],v[156:159]\n"
RT"a[188:189],v245 offset:45056\n"
"v_accvgpr_write_b32 a37,v129\n"
MF"v[156:159],a[144:147],a[196:199],v[156:159]\n"
"v_accvgpr_write_b32 a38,v130\n"
"v_accvgpr_write_b32 a39,v131\n"
MF"v[164:167],a[144:147],a[200:203],v[164:167]\n"
RT"a[190:191],v245 offset:45568\n"
PK"v136,v136,v137\n"
MF"v[164:167],a[148:151],a[216:219],v[164:167]\n"
PK"v137,v138,v139\n"
PK"v138,v140,v141\n"
MF"v[172:175],a[148:151],a[220:223],v[172:175]\n"
PK"v139,v142,v143\n"
"v_accvgpr_write_b32 a52,v136\n"
MF"v[172:175],a[144:147],a[204:207],v[172:175]\n"
"v_accvgpr_write_b32 a53,v137\n"
"v_accvgpr_write_b32 a54,v138\n"
MF"v[148:151],a[152:155],a[224:227],v[148:151]\n"
WT"lgkmcnt(2)\n"
"v_accvgpr_write_b32 a55,v139\n"
MF"v[148:151],a[156:159],a[240:243],v[148:151]\n"
MF"v[156:159],a[156:159],a[244:247],v[156:159]\n"
MF"v[156:159],a[152:155],a[228:231],v[156:159]\n"
MF"v[164:167],a[152:155],a[232:235],v[164:167]\n"
MF"v[164:167],a[156:159],a[248:251],v[164:167]\n"
MF"v[172:175],a[156:159],a[252:255],v[172:175]\n"
MF"v[172:175],a[152:155],a[236:239],v[172:175]\n"
MF"v[176:179],a[160:163],a[192:195],v[176:179]\n"
MF"v[176:179],a[164:167],a[208:211],v[176:179]\n"
MF"v[176:179],a[168:171],a[224:227],v[176:179]\n"
PK"v144,v144,v145\n"
PK"v145,v146,v147\n"
MF"v[176:179],a[172:175],a[240:243],v[176:179]\n"
PK"v146,v148,v149\n"
PK"v147,v150,v151\n"
MF"v[180:183],a[176:179],a[192:195],v[180:183]\n"
"v_mov_b32 v254,0x3f803f80\n"
"v_accvgpr_write_b32 a8,v144\n"
MF"v[180:183],a[180:183],a[208:211],v[180:183]\n"
"v_accvgpr_write_b32 a9,v145\n"
"v_accvgpr_write_b32 a128,v254\n"
MF"v[180:183],a[184:187],a[224:227],v[180:183]\n"
WT"lgkmcnt(0)\n"
"v_accvgpr_write_b32 a129,v254\n"
"v_accvgpr_write_b32 a130,v254\n"
MF"v[180:183],a[188:191],a[240:243],v[180:183]\n"
"v_accvgpr_write_b32 a131,v254\n"
"v_accvgpr_write_b32 a10,v146\n"
MF"v[184:187],a[160:163],a[196:199],v[184:187]\n"
"v_accvgpr_write_b32 a11,v147\n"
PK"v152,v152,v153\n"
MF"v[184:187],a[164:167],a[212:215],v[184:187]\n"
PK"v153,v154,v155\n"
PK"v154,v156,v157\n"
MF"v[184:187],a[168:171],a[228:231],v[184:187]\n"
PK"v155,v158,v159\n"
"v_accvgpr_write_b32 a24,v152\n"
MF"v[184:187],a[172:175],a[244:247],v[184:187]\n"
"v_accvgpr_write_b32 a25,v153\n"
PK"v176,v176,v177\n"
MF"v[188:191],a[176:179],a[196:199],v[188:191]\n"
PK"v177,v178,v179\n"
"v_accvgpr_write_b32 a26,v154\n"
MF"v[188:191],a[180:183],a[212:215],v[188:191]\n"
"v_accvgpr_write_b32 a27,v155\n"
PK"v178,v180,v181\n"
MF"v[188:191],a[184:187],a[228:231],v[188:191]\n"
PK"v179,v182,v183\n"
"v_accvgpr_write_b32 a12,v176\n"
MF"v[188:191],a[188:191],a[244:247],v[188:191]\n"
"v_accvgpr_write_b32 a13,v177\n"
"v_accvgpr_write_b32 a14,v178\n"
MF"v[192:195],a[160:163],a[200:203],v[192:195]\n"
WT"vmcnt(14)\n"
"v_accvgpr_write_b32 a15,v179\n"
PK"v160,v160,v161\n"
MF"v[192:195],a[164:167],a[216:219],v[192:195]\n"
"s_barrier\n"
MF"v[192:195],a[168:171],a[232:235],v[192:195]\n"
RD"a[64:67],v226 offset:0\n"
PK"v161,v162,v163\n"
MF"v[192:195],a[172:175],a[248:251],v[192:195]\n"
PK"v162,v164,v165\n"
PK"v163,v166,v167\n"
MF"v[196:199],a[176:179],a[200:203],v[196:199]\n"
RD"a[68:71],v226 offset:1024\n"
"v_accvgpr_write_b32 a40,v160\n"
MF"v[196:199],a[180:183],a[216:219],v[196:199]\n"
"v_accvgpr_write_b32 a41,v161\n"
"v_accvgpr_write_b32 a42,v162\n"
MF"v[196:199],a[184:187],a[232:235],v[196:199]\n"
RD"a[72:75],v226 offset:2048\n"
"v_accvgpr_write_b32 a43,v163\n"
MF"v[196:199],a[188:191],a[248:251],v[196:199]\n"
PK"v184,v184,v185\n"
PK"v185,v186,v187\n"
MF"v[200:203],a[160:163],a[204:207],v[200:203]\n"
RD"a[76:79],v226 offset:3072\n"
PK"v186,v188,v189\n"
MF"v[200:203],a[164:167],a[220:223],v[200:203]\n"
PK"v187,v190,v191\n"
PK"v168,v168,v169\n"
MF"v[200:203],a[168:171],a[236:239],v[200:203]\n"
RD"a[80:83],v226 offset:4096\n"
PK"v169,v170,v171\n"
MF"v[200:203],a[172:175],a[252:255],v[200:203]\n"
"v_accvgpr_write_b32 a28,v184\n"
"v_accvgpr_write_b32 a29,v185\n"
MF"v[204:207],a[176:179],a[204:207],v[204:207]\n"
RD"a[84:87],v226 offset:5120\n"
PK"v170,v172,v173\n"
MF"v[204:207],a[180:183],a[220:223],v[204:207]\n"
PK"v171,v174,v175\n"
"v_accvgpr_write_b32 a30,v186\n"
MF"v[204:207],a[184:187],a[236:239],v[204:207]\n"
"v_accvgpr_write_b32 a31,v187\n"
"v_accvgpr_write_b32 a56,v168\n"
MF"v[204:207],a[188:191],a[252:255],v[204:207]\n"
"v_accvgpr_write_b32 a57,v169\n"
"v_accvgpr_write_b32 a58,v170\n"
"v_accvgpr_write_b32 a59,v171\n"
RD"a[88:91],v226 offset:6144\n"
"s_cmp_eq_u32 s34,0\n"
"s_cbranch_scc1 LFA_%=\n"
WT"lgkmcnt(6)\n"
MF"v[0:3],a[64:67],a[0:3],0\n"
WT"lgkmcnt(3)\n"
PK"v192,v192,v193\n"
PK"v193,v194,v195\n"
MF"v[0:3],a[68:71],a[4:7],v[0:3]\n"
"s_mov_b32 m0,s40\n"
"v_readlane_b32 s43,v228,s42\n"
PK"v194,v196,v197\n"
MF"v[8:11],a[68:71],a[20:23],0\n"
PK"v195,v198,v199\n"
"v_readlane_b32 s44,v229,s42\n"
MF"v[8:11],a[64:67],a[16:19],v[8:11]\n"
SA"s42,s42,1\n"
BL"v225,s[8:11],s43 offen offset:0 sc1 lds\n"
MF"v[16:19],a[64:67],a[32:35],0\n"
"v_accvgpr_write_b32 a44,v192\n"
"v_accvgpr_write_b32 a45,v193\n"
MF"v[16:19],a[68:71],a[36:39],v[16:19]\n"
RD"a[92:95],v226 offset:7168\n"
"v_accvgpr_write_b32 a46,v194\n"
MF"v[24:27],a[68:71],a[52:55],0\n"
"v_accvgpr_write_b32 a47,v195\n"
PK"v200,v200,v201\n"
MF"v[24:27],a[64:67],a[48:51],v[24:27]\n"
PK"v201,v202,v203\n"
PK"v202,v204,v205\n"
MF"v[0:3],a[72:75],a[8:11],v[0:3]\n"
BL"v225,s[8:11],s43 offen offset:1024 sc1 lds\n"
PK"v203,v206,v207\n"
MF"v[0:3],a[76:79],a[12:15],v[0:3]\n"
"v_accvgpr_write_b32 a60,v200\n"
"v_accvgpr_write_b32 a61,v201\n"
MF"v[8:11],a[76:79],a[28:31],v[8:11]\n"
"s_mul_i32 s48,s32,655360\n"
"v_accvgpr_write_b32 a62,v202\n"
"v_accvgpr_write_b32 a63,v203\n"
MF"v[8:11],a[72:75],a[24:27],v[8:11]\n"
SA"s12,s74,s48\n"
"s_addc_u32 s13,s75,0\n"
MF"v[16:19],a[72:75],a[40:43],v[16:19]\n"
"s_and_b32 s13,s13,0xffff\n"
MF"v[16:19],a[76:79],a[44:47],v[16:19]\n"
"s_mov_b32 s14,655360\n"
RD"a[96:99],v226 offset:8192\n"
MF"v[24:27],a[76:79],a[60:63],v[24:27]\n"
"s_lshl_b32 s48,s33,3\n"
BL"v225,s[8:11],s43 offen offset:2048 sc1 lds\n"
MF"v[24:27],a[72:75],a[56:59],v[24:27]\n"
WT"lgkmcnt(1)\n"
EX"v0,v0\n"
MF"v[4:7],a[80:83],a[0:3],0\n"
"s_and_b32 s49,s34,2\n"
EX"v1,v1\n"
MF"v[4:7],a[84:87],a[4:7],v[4:7]\n"
"s_lshl_b32 s49,s49,1\n"
EX"v2,v2\n"
MF"v[12:15],a[84:87],a[20:23],0\n"
SA"s48,s48,s49\n"
RD"a[100:103],v226 offset:9216\n"
PK"v0,v0,v1\n"
MF"v[12:15],a[80:83],a[16:19],v[12:15]\n"
SA"s48,s48,s30\n"
EX"v3,v3\n"
MF"v[20:23],a[80:83],a[32:35],0\n"
"s_mul_i32 s48,s48,5120\n"
EX"v8,v8\n"
MF"v[20:23],a[84:87],a[36:39],v[20:23]\n"
"s_or_b32 s49,s34,2\n"
BL"v225,s[8:11],s43 offen offset:3072 sc1 lds\n"
MF"v[28:31],a[84:87],a[52:55],0\n"
"s_cmp_eq_u32 s49,7\n"
EX"v9,v9\n"
PK"v1,v2,v3\n"
MF"v[28:31],a[80:83],a[48:51],v[28:31]\n"
"s_cselect_b32 s55,s48,2130706432\n"
RD"a[104:107],v226 offset:10240\n"
PK"v8,v8,v9\n"
MF"v[4:7],a[88:91],a[8:11],v[4:7]\n"
SA"m0,s40,16384\n"
EX"v10,v10\n"
MF"v[4:7],a[92:95],a[12:15],v[4:7]\n"
EX"v11,v11\n"
MF"v[12:15],a[92:95],a[28:31],v[12:15]\n"
EX"v16,v16\n"
MF"v[12:15],a[88:91],a[24:27],v[12:15]\n"
SA"s40,s40,32768\n"
RD"a[108:111],v226 offset:11264\n"
PK"v9,v10,v11\n"
MF"v[20:23],a[88:91],a[40:43],v[20:23]\n"
"s_cmp_eq_u32 s40,s46\n"
BL"v225,s[8:11],s44 offen offset:0 sc1 lds\n"
EX"v17,v17\n"
MF"v[20:23],a[92:95],a[44:47],v[20:23]\n"
"s_cselect_b32 s40,s47,s40\n"
EX"v18,v18\n"
MF"v[28:31],a[92:95],a[60:63],v[28:31]\n"
EX"v19,v19\n"
PK"v16,v16,v17\n"
MF"v[28:31],a[88:91],a[56:59],v[28:31]\n"
WT"lgkmcnt(1)\n"
RD"a[112:115],v226 offset:12288\n"
PK"v17,v18,v19\n"
MF"v[32:35],a[96:99],a[0:3],0\n"
EX"v24,v24\n"
MF"v[32:35],a[100:103],a[4:7],v[32:35]\n"
EX"v25,v25\n"
"v_mov_b32 v227,v226\n"
MF"v[40:43],a[100:103],a[20:23],0\n"
BL"v225,s[8:11],s44 offen offset:1024 sc1 lds\n"
EX"v26,v26\n"
MF"v[40:43],a[96:99],a[16:19],v[40:43]\n"
RD"a[116:119],v226 offset:13312\n"
PK"v24,v24,v25\n"
MF"v[48:51],a[96:99],a[32:35],0\n"
EX"v27,v27\n"
MF"v[48:51],a[100:103],a[36:39],v[48:51]\n"
EX"v4,v4\n"
MF"v[56:59],a[100:103],a[52:55],0\n"
EX"v5,v5\n"
PK"v25,v26,v27\n"
MF"v[56:59],a[96:99],a[48:51],v[56:59]\n"
RD"a[120:123],v226 offset:14336\n"
PK"v2,v4,v5\n"
MF"v[32:35],a[104:107],a[8:11],v[32:35]\n"
WT"lgkmcnt(2)\n"
BL"v225,s[8:11],s44 offen offset:2048 sc1 lds\n"
EX"v6,v6\n"
MF"v[32:35],a[108:111],a[12:15],v[32:35]\n"
EX"v7,v7\n"
MF"v[40:43],a[108:111],a[28:31],v[40:43]\n"
EX"v12,v12\n"
MF"v[40:43],a[104:107],a[24:27],v[40:43]\n"
RD"a[124:127],v226 offset:15360\n"
"v_readlane_b32 s43,v228,s42\n"
PK"v3,v6,v7\n"
MF"v[48:51],a[104:107],a[40:43],v[48:51]\n"
EX"v13,v13\n"
MF"v[48:51],a[108:111],a[44:47],v[48:51]\n"
EX"v14,v14\n"
MF"v[56:59],a[108:111],a[60:63],v[56:59]\n"
BL"v225,s[8:11],s44 offen offset:3072 sc1 lds\n"
EX"v15,v15\n"
MF"v[56:59],a[104:107],a[56:59],v[56:59]\n"
"s_mov_b32 m0,s40\n"
RD"a[96:99],v227 offset:24576\n"
PK"v10,v12,v13\n"
MF"v[36:39],a[112:115],a[0:3],0\n"
WT"lgkmcnt(2)\n"
EX"v20,v20\n"
PK"v11,v14,v15\n"
MF"v[36:39],a[116:119],a[4:7],v[36:39]\n"
EX"v21,v21\n"
MF"v[44:47],a[116:119],a[20:23],0\n"
EX"v22,v22\n"
MF"v[44:47],a[112:115],a[16:19],v[44:47]\n"
RD"a[100:103],v227 offset:25600\n"
"v_readlane_b32 s44,v229,s42\n"
PK"v18,v20,v21\n"
MF"v[52:55],a[112:115],a[32:35],0\n"
SA"s42,s42,1\n"
BL"v225,s[8:11],s43 offen offset:0 sc1 lds\n"
MF"v[52:55],a[116:119],a[36:39],v[52:55]\n"
EX"v23,v23\n"
MF"v[60:63],a[116:119],a[52:55],0\n"
EX"v28,v28\n"
MF"v[60:63],a[112:115],a[48:51],v[60:63]\n"
RD"a[104:107],v227 offset:26624\n"
PK"v19,v22,v23\n"
MF"v[36:39],a[120:123],a[8:11],v[36:39]\n"
WT"lgkmcnt(2)\n"
EX"v29,v29\n"
MF"v[36:39],a[124:127],a[12:15],v[36:39]\n"
BL"v225,s[8:11],s43 offen offset:1024 sc1 lds\n"
MF"v[44:47],a[124:127],a[28:31],v[44:47]\n"
EX"v30,v30\n"
PK"v26,v28,v29\n"
MF"v[44:47],a[120:123],a[24:27],v[44:47]\n"
RD"a[108:111],v227 offset:27648\n"
MF"v[52:55],a[120:123],a[40:43],v[52:55]\n"
EX"v31,v31\n"
MF"v[52:55],a[124:127],a[44:47],v[52:55]\n"
EX"v32,v32\n"
MF"v[60:63],a[124:127],a[60:63],v[60:63]\n"
EX"v33,v33\n"
PK"v27,v30,v31\n"
MF"v[60:63],a[120:123],a[56:59],v[60:63]\n"
SA"s41,s41,32768\n"
BL"v225,s[8:11],s43 offen offset:2048 sc1 lds\n"
RD"a[112:115],v227 offset:28672\n"
MF"v[80:83],a[96:99],v[0:3],0\n"
"s_cmp_eq_u32 s41,163840\n"
EX"v34,v34\n"
MF"v[112:115],a[96:99],v[8:11],0\n"
"s_cselect_b32 s41,0,s41\n"
EX"v35,v35\n"
MF"v[144:147],a[96:99],v[16:19],0\n"
EX"v40,v40\n"
"v_add_u32 v226,s41,v224\n"
MF"v[176:179],a[96:99],v[24:27],0\n"
WT"lgkmcnt(1)\n"
RD"a[116:119],v227 offset:29696\n"
MF"v[84:87],a[100:103],v[0:3],0\n"
EX"v41,v41\n"
MF"v[116:119],a[100:103],v[8:11],0\n"
BL"v225,s[8:11],s43 offen offset:3072 sc1 lds\n"
MF"v[148:151],a[100:103],v[16:19],0\n"
SA"m0,s40,16384\n"
EX"v42,v42\n"
MF"v[180:183],a[100:103],v[24:27],0\n"
RD"a[120:123],v227 offset:30720\n"
MF"v[88:91],a[104:107],v[0:3],0\n"
EX"v43,v43\n"
MF"v[120:123],a[104:107],v[8:11],0\n"
EX"v48,v48\n"
MF"v[152:155],a[104:107],v[16:19],0\n"
SA"s40,s40,32768\n"
EX"v49,v49\n"
MF"v[184:187],a[104:107],v[24:27],0\n"
"s_cmp_eq_u32 s40,s46\n"
BL"v225,s[8:11],s44 offen offset:0 sc1 lds\n"
RD"a[124:127],v227 offset:31744\n"
MF"v[92:95],a[108:111],v[0:3],0\n"
"s_cselect_b32 s40,s47,s40\n"
EX"v50,v50\n"
MF"v[124:127],a[108:111],v[8:11],0\n"
EX"v51,v51\n"
MF"v[156:159],a[108:111],v[16:19],0\n"
EX"v56,v56\n"
MF"v[188:191],a[108:111],v[24:27],0\n"
WT"lgkmcnt(1)\n"
EX"v57,v57\n"
MF"v[96:99],a[112:115],v[0:3],0\n"
EX"v58,v58\n"
MF"v[128:131],a[112:115],v[8:11],0\n"
WT"vmcnt(15) lgkmcnt(0)\n"
BL"v225,s[8:11],s44 offen offset:1024 sc1 lds\n"
MF"v[160:163],a[112:115],v[16:19],0\n"
EX"v59,v59\n"
MF"v[192:195],a[112:115],v[24:27],0\n"
EX"v36,v36\n"
MF"v[100:103],a[116:119],v[0:3],0\n"
"s_barrier\n"
MF"v[132:135],a[116:119],v[8:11],0\n"
RD"a[64:67],v226 offset:0\n"
"buffer_store_dwordx2 v[232:233],v230,s[8:11],s98 offen sc1\n"
MF"v[164:167],a[116:119],v[16:19],0\n"
EX"v37,v37\n"
MF"v[196:199],a[116:119],v[24:27],0\n"
BL"v225,s[8:11],s44 offen offset:2048 sc1 lds\n"
RD"a[68:71],v226 offset:1024\n"
MF"v[104:107],a[120:123],v[0:3],0\n"
EX"v38,v38\n"
MF"v[136:139],a[120:123],v[8:11],0\n"
EX"v39,v39\n"
MF"v[168:171],a[120:123],v[16:19],0\n"
EX"v44,v44\n"
MF"v[200:203],a[120:123],v[24:27],0\n"
RD"a[72:75],v226 offset:2048\n"
RD"a[76:79],v226 offset:3072\n"
MF"v[108:111],a[124:127],v[0:3],0\n"
EX"v45,v45\n"
MF"v[140:143],a[124:127],v[8:11],0\n"
BL"v225,s[8:11],s44 offen offset:3072 sc1 lds\n"
MF"v[172:175],a[124:127],v[16:19],0\n"
EX"v46,v46\n"
MF"v[204:207],a[124:127],v[24:27],0\n"
RD"a[80:83],v226 offset:4096\n"
MF"a[132:135],a[128:131],v[0:3],0\n"
EX"v47,v47\n"
MF"a[136:139],a[128:131],v[8:11],0\n"
"buffer_load_dword v255,v253,s[12:15],s55 offen\n"
EX"v52,v52\n"
MF"a[140:143],a[128:131],v[16:19],0\n"
WT"lgkmcnt(3)\n"
EX"v53,v53\n"
MF"a[144:147],a[128:131],v[24:27],0\n"
SA"s55,s55,163840\n"
RD"a[84:87],v226 offset:5120\n"
RD"a[88:91],v226 offset:6144\n"
EX"v54,v54\n"
EX"v55,v55\n"
EX"v60,v60\n"
EX"v61,v61\n"
EX"v62,v62\n"
EX"v63,v63\n"
"s_cmp_eq_u32 s57,1\n"
"s_cbranch_scc1 LEV_%=\n"
".p2align 6\n"
"LP1_%=:\n"
MF"v[0:3],a[64:67],a[0:3],0\n"
MF"v[0:3],a[68:71],a[4:7],v[0:3]\n"
MF"v[8:11],a[68:71],a[20:23],0\n"
MF"v[8:11],a[64:67],a[16:19],v[8:11]\n"
MF"v[16:19],a[64:67],a[32:35],0\n"
MF"v[16:19],a[68:71],a[36:39],v[16:19]\n"
RD"a[92:95],v226 offset:7168\n"
PK"v64,v32,v33\n"
MF"v[24:27],a[68:71],a[52:55],0\n"
PK"v65,v34,v35\n"
PK"v66,v36,v37\n"
MF"v[24:27],a[64:67],a[48:51],v[24:27]\n"
WT"lgkmcnt(1)\n"
PK"v67,v38,v39\n"
PK"v68,v40,v41\n"
MF"v[0:3],a[72:75],a[8:11],v[0:3]\n"
PK"v69,v42,v43\n"
PK"v70,v44,v45\n"
MF"v[0:3],a[76:79],a[12:15],v[0:3]\n"
PK"v71,v46,v47\n"
PK"v72,v48,v49\n"
MF"v[8:11],a[76:79],a[28:31],v[8:11]\n"
PK"v73,v50,v51\n"
PK"v74,v52,v53\n"
MF"v[8:11],a[72:75],a[24:27],v[8:11]\n"
PK"v75,v54,v55\n"
PK"v76,v56,v57\n"
MF"v[16:19],a[72:75],a[40:43],v[16:19]\n"
PK"v77,v58,v59\n"
PK"v78,v60,v61\n"
MF"v[16:19],a[76:79],a[44:47],v[16:19]\n"
RD"a[96:99],v226 offset:8192\n"
PK"v79,v62,v63\n"
MF"v[24:27],a[76:79],a[60:63],v[24:27]\n"
EX"v0,v0\n"
MF"v[24:27],a[72:75],a[56:59],v[24:27]\n"
EX"v1,v1\n"
MF"v[4:7],a[80:83],a[0:3],0\n"
EX"v2,v2\n"
MF"v[4:7],a[84:87],a[4:7],v[4:7]\n"
RD"a[100:103],v226 offset:9216\n"
PK"v0,v0,v1\n"
MF"v[12:15],a[84:87],a[20:23],0\n"
EX"v3,v3\n"
MF"v[12:15],a[80:83],a[16:19],v[12:15]\n"
EX"v8,v8\n"
MF"v[20:23],a[80:83],a[32:35],0\n"
EX"v9,v9\n"
MF"v[20:23],a[84:87],a[36:39],v[20:23]\n"
RD"a[104:107],v226 offset:10240\n"
PK"v1,v2,v3\n"
MF"v[28:31],a[84:87],a[52:55],0\n"
EX"v10,v10\n"
MF"v[28:31],a[80:83],a[48:51],v[28:31]\n"
EX"v11,v11\n"
MF"v[4:7],a[88:91],a[8:11],v[4:7]\n"
WT"lgkmcnt(1)\n"
EX"v16,v16\n"
MF"v[4:7],a[92:95],a[12:15],v[4:7]\n"
RD"a[108:111],v226 offset:11264\n"
PK"v8,v8,v9\n"
MF"v[12:15],a[92:95],a[28:31],v[12:15]\n"
EX"v17,v17\n"
MF"v[12:15],a[88:91],a[24:27],v[12:15]\n"
EX"v18,v18\n"
MF"v[20:23],a[88:91],a[40:43],v[20:23]\n"
EX"v19,v19\n"
MF"v[20:23],a[92:95],a[44:47],v[20:23]\n"
RD"a[112:115],v226 offset:12288\n"
PK"v9,v10,v11\n"
MF"v[28:31],a[92:95],a[60:63],v[28:31]\n"
EX"v24,v24\n"
MF"v[28:31],a[88:91],a[56:59],v[28:31]\n"
EX"v25,v25\n"
MF"v[32:35],a[96:99],a[0:3],0\n"
EX"v26,v26\n"
MF"v[32:35],a[100:103],a[4:7],v[32:35]\n"
RD"a[116:119],v226 offset:13312\n"
PK"v16,v16,v17\n"
MF"v[40:43],a[100:103],a[20:23],0\n"
EX"v27,v27\n"
MF"v[40:43],a[96:99],a[16:19],v[40:43]\n"
EX"v4,v4\n"
MF"v[48:51],a[96:99],a[32:35],0\n"
EX"v5,v5\n"
MF"v[48:51],a[100:103],a[36:39],v[48:51]\n"
RD"a[120:123],v226 offset:14336\n"
PK"v17,v18,v19\n"
MF"v[56:59],a[100:103],a[52:55],0\n"
EX"v6,v6\n"
MF"v[56:59],a[96:99],a[48:51],v[56:59]\n"
WT"lgkmcnt(2)\n"
EX"v7,v7\n"
MF"v[32:35],a[104:107],a[8:11],v[32:35]\n"
EX"v12,v12\n"
MF"v[32:35],a[108:111],a[12:15],v[32:35]\n"
RD"a[124:127],v226 offset:15360\n"
"v_mov_b32 v227,v226\n"
MF"v[40:43],a[108:111],a[28:31],v[40:43]\n"
EX"v13,v13\n"
MF"v[40:43],a[104:107],a[24:27],v[40:43]\n"
EX"v14,v14\n"
MF"v[48:51],a[104:107],a[40:43],v[48:51]\n"
EX"v15,v15\n"
MF"v[48:51],a[108:111],a[44:47],v[48:51]\n"
RD"a[64:67],v227 offset:16384\n"
PK"v24,v24,v25\n"
MF"v[56:59],a[108:111],a[60:63],v[56:59]\n"
EX"v20,v20\n"
MF"v[56:59],a[104:107],a[56:59],v[56:59]\n"
EX"v21,v21\n"
MF"v[36:39],a[112:115],a[0:3],0\n"
WT"lgkmcnt(1)\n"
EX"v22,v22\n"
MF"v[36:39],a[116:119],a[4:7],v[36:39]\n"
RD"a[68:71],v227 offset:24576\n"
PK"v25,v26,v27\n"
MF"v[44:47],a[116:119],a[20:23],0\n"
EX"v23,v23\n"
MF"v[44:47],a[112:115],a[16:19],v[44:47]\n"
EX"v28,v28\n"
MF"v[52:55],a[112:115],a[32:35],0\n"
EX"v29,v29\n"
MF"v[52:55],a[116:119],a[36:39],v[52:55]\n"
RD"a[72:75],v227 offset:17408\n"
"v_readlane_b32 s43,v228,s42\n"
MF"v[60:63],a[116:119],a[52:55],0\n"
EX"v30,v30\n"
MF"v[60:63],a[112:115],a[48:51],v[60:63]\n"
"s_mov_b32 m0,s40\n"
EX"v31,v31\n"
MF"v[36:39],a[120:123],a[8:11],v[36:39]\n"
PK"v2,v4,v5\n"
PK"v3,v6,v7\n"
MF"v[36:39],a[124:127],a[12:15],v[36:39]\n"
RD"a[76:79],v227 offset:25600\n"
"v_readlane_b32 s44,v229,s42\n"
MF"v[44:47],a[124:127],a[28:31],v[44:47]\n"
PK"v10,v12,v13\n"
PK"v11,v14,v15\n"
MF"v[44:47],a[120:123],a[24:27],v[44:47]\n"
SA"s42,s42,1\n"
BL"v225,s[8:11],s43 offen offset:0 sc1 lds\n"
MF"v[52:55],a[120:123],a[40:43],v[52:55]\n"
PK"v18,v20,v21\n"
PK"v19,v22,v23\n"
MF"v[52:55],a[124:127],a[44:47],v[52:55]\n"
RD"a[80:83],v227 offset:18432\n"
PK"v26,v28,v29\n"
MF"v[60:63],a[124:127],a[60:63],v[60:63]\n"
EX"v32,v32\n"
MF"v[60:63],a[120:123],a[56:59],v[60:63]\n"
WT"lgkmcnt(2)\n"
PK"v27,v30,v31\n"
SA"s41,s41,32768\n"
MF"v[80:83],a[64:67],v[64:67],v[80:83]\n"
"s_cmp_eq_u32 s41,163840\n"
EX"v33,v33\n"
MF"v[80:83],a[68:71],v[0:3],v[80:83]\n"
"s_cselect_b32 s41,0,s41\n"
RD"a[84:87],v227 offset:26624\n"
"v_add_u32 v226,s41,v224\n"
MF"v[112:115],a[68:71],v[8:11],v[112:115]\n"
EX"v34,v34\n"
MF"v[112:115],a[64:67],v[68:71],v[112:115]\n"
BL"v225,s[8:11],s43 offen offset:1024 sc1 lds\n"
MF"v[144:147],a[64:67],v[72:75],v[144:147]\n"
EX"v35,v35\n"
MF"v[144:147],a[68:71],v[16:19],v[144:147]\n"
RD"a[88:91],v227 offset:19456\n"
MF"v[176:179],a[68:71],v[24:27],v[176:179]\n"
EX"v40,v40\n"
MF"v[176:179],a[64:67],v[76:79],v[176:179]\n"
MF"v[84:87],a[72:75],v[64:67],v[84:87]\n"
WT"lgkmcnt(1)\n"
EX"v41,v41\n"
MF"v[84:87],a[76:79],v[0:3],v[84:87]\n"
RD"a[92:95],v227 offset:27648\n"
MF"v[116:119],a[76:79],v[8:11],v[116:119]\n"
EX"v42,v42\n"
MF"v[116:119],a[72:75],v[68:71],v[116:119]\n"
BL"v225,s[8:11],s43 offen offset:2048 sc1 lds\n"
MF"v[148:151],a[72:75],v[72:75],v[148:151]\n"
EX"v43,v43\n"
MF"v[148:151],a[76:79],v[16:19],v[148:151]\n"
RD"a[96:99],v227 offset:20480\n"
MF"v[180:183],a[76:79],v[24:27],v[180:183]\n"
EX"v48,v48\n"
MF"v[180:183],a[72:75],v[76:79],v[180:183]\n"
MF"v[88:91],a[80:83],v[64:67],v[88:91]\n"
EX"v49,v49\n"
MF"v[88:91],a[84:87],v[0:3],v[88:91]\n"
RD"a[100:103],v227 offset:28672\n"
MF"v[120:123],a[84:87],v[8:11],v[120:123]\n"
EX"v50,v50\n"
MF"v[120:123],a[80:83],v[68:71],v[120:123]\n"
BL"v225,s[8:11],s43 offen offset:3072 sc1 lds\n"
MF"v[152:155],a[80:83],v[72:75],v[152:155]\n"
EX"v51,v51\n"
MF"v[152:155],a[84:87],v[16:19],v[152:155]\n"
RD"a[104:107],v227 offset:21504\n"
MF"v[184:187],a[84:87],v[24:27],v[184:187]\n"
EX"v56,v56\n"
MF"v[184:187],a[80:83],v[76:79],v[184:187]\n"
WT"lgkmcnt(2)\n"
SA"m0,s40,16384\n"
MF"v[92:95],a[88:91],v[64:67],v[92:95]\n"
EX"v57,v57\n"
MF"v[92:95],a[92:95],v[0:3],v[92:95]\n"
RD"a[108:111],v227 offset:29696\n"
MF"v[124:127],a[92:95],v[8:11],v[124:127]\n"
EX"v58,v58\n"
MF"v[124:127],a[88:91],v[68:71],v[124:127]\n"
BL"v225,s[8:11],s44 offen offset:0 sc1 lds\n"
MF"v[156:159],a[88:91],v[72:75],v[156:159]\n"
SA"s40,s40,32768\n"
EX"v59,v59\n"
MF"v[156:159],a[92:95],v[16:19],v[156:159]\n"
"s_cmp_eq_u32 s40,s46\n"
RD"a[112:115],v227 offset:22528\n"
"s_cselect_b32 s40,s47,s40\n"
MF"v[188:191],a[92:95],v[24:27],v[188:191]\n"
EX"v36,v36\n"
MF"v[188:191],a[88:91],v[76:79],v[188:191]\n"
MF"v[96:99],a[96:99],v[64:67],v[96:99]\n"
WT"lgkmcnt(1)\n"
EX"v37,v37\n"
MF"v[96:99],a[100:103],v[0:3],v[96:99]\n"
RD"a[116:119],v227 offset:30720\n"
MF"v[128:131],a[100:103],v[8:11],v[128:131]\n"
EX"v38,v38\n"
MF"v[128:131],a[96:99],v[68:71],v[128:131]\n"
BL"v225,s[8:11],s44 offen offset:1024 sc1 lds\n"
MF"v[160:163],a[96:99],v[72:75],v[160:163]\n"
EX"v39,v39\n"
MF"v[160:163],a[100:103],v[16:19],v[160:163]\n"
RD"a[120:123],v227 offset:23552\n"
MF"v[192:195],a[100:103],v[24:27],v[192:195]\n"
EX"v44,v44\n"
MF"v[192:195],a[96:99],v[76:79],v[192:195]\n"
MF"v[100:103],a[104:107],v[64:67],v[100:103]\n"
EX"v45,v45\n"
MF"v[100:103],a[108:111],v[0:3],v[100:103]\n"
RD"a[124:127],v227 offset:31744\n"
MF"v[132:135],a[108:111],v[8:11],v[132:135]\n"
EX"v46,v46\n"
MF"v[132:135],a[104:107],v[68:71],v[132:135]\n"
BL"v225,s[8:11],s44 offen offset:2048 sc1 lds\n"
MF"v[164:167],a[104:107],v[72:75],v[164:167]\n"
EX"v47,v47\n"
MF"v[164:167],a[108:111],v[16:19],v[164:167]\n"
MF"v[196:199],a[108:111],v[24:27],v[196:199]\n"
EX"v52,v52\n"
MF"v[196:199],a[104:107],v[76:79],v[196:199]\n"
WT"lgkmcnt(1)\n"
MF"v[104:107],a[112:115],v[64:67],v[104:107]\n"
EX"v53,v53\n"
MF"v[104:107],a[116:119],v[0:3],v[104:107]\n"
MF"v[136:139],a[116:119],v[8:11],v[136:139]\n"
EX"v54,v54\n"
MF"v[136:139],a[112:115],v[68:71],v[136:139]\n"
WT"vmcnt(15) lgkmcnt(0)\n"
BL"v225,s[8:11],s44 offen offset:3072 sc1 lds\n"
MF"v[168:171],a[112:115],v[72:75],v[168:171]\n"
"s_barrier\n"
MF"v[168:171],a[116:119],v[16:19],v[168:171]\n"
RD"a[64:67],v226 offset:0\n"
MF"v[200:203],a[116:119],v[24:27],v[200:203]\n"
EX"v55,v55\n"
MF"v[200:203],a[112:115],v[76:79],v[200:203]\n"
"buffer_load_dword v255,v253,s[12:15],s55 offen\n"
MF"v[108:111],a[120:123],v[64:67],v[108:111]\n"
EX"v60,v60\n"
MF"v[108:111],a[124:127],v[0:3],v[108:111]\n"
SA"s55,s55,163840\n"
RD"a[68:71],v226 offset:1024\n"
MF"v[140:143],a[124:127],v[8:11],v[140:143]\n"
EX"v61,v61\n"
MF"v[140:143],a[120:123],v[68:71],v[140:143]\n"
RD"a[72:75],v226 offset:2048\n"
MF"v[172:175],a[120:123],v[72:75],v[172:175]\n"
EX"v62,v62\n"
MF"v[172:175],a[124:127],v[16:19],v[172:175]\n"
RD"a[76:79],v226 offset:3072\n"
MF"v[204:207],a[124:127],v[24:27],v[204:207]\n"
EX"v63,v63\n"
MF"v[204:207],a[120:123],v[76:79],v[204:207]\n"
MF"a[132:135],a[128:131],v[64:67],a[132:135]\n"
MF"a[132:135],a[128:131],v[0:3],a[132:135]\n"
RD"a[80:83],v226 offset:4096\n"
MF"a[136:139],a[128:131],v[8:11],a[136:139]\n"
MF"a[136:139],a[128:131],v[68:71],a[136:139]\n"
RD"a[84:87],v226 offset:5120\n"
MF"a[140:143],a[128:131],v[72:75],a[140:143]\n"
MF"a[140:143],a[128:131],v[16:19],a[140:143]\n"
RD"a[88:91],v226 offset:6144\n"
MF"a[144:147],a[128:131],v[24:27],a[144:147]\n"
WT"lgkmcnt(5)\n"
"s_sub_u32 s56,s56,1\n"
MF"a[144:147],a[128:131],v[76:79],a[144:147]\n"
"s_cbranch_scc0 LP1_%=\n"
"s_cmp_eq_u32 s57,2\n"
"s_cbranch_scc1 LEV_%=\n"
"s_cmp_eq_u32 s57,3\n"
"s_cbranch_scc1 LRT_%=\n"
"LDA_%=:\n"
MF"v[0:3],a[64:67],a[0:3],v[208:211]\n"
MF"v[0:3],a[68:71],a[4:7],v[0:3]\n"
MF"v[8:11],a[68:71],a[20:23],0\n"
MF"v[8:11],a[64:67],a[16:19],v[8:11]\n"
MF"v[16:19],a[64:67],a[32:35],0\n"
MF"v[16:19],a[68:71],a[36:39],v[16:19]\n"
RD"a[92:95],v226 offset:7168\n"
PK"v64,v32,v33\n"
MF"v[24:27],a[68:71],a[52:55],0\n"
PK"v65,v34,v35\n"
PK"v66,v36,v37\n"
MF"v[24:27],a[64:67],a[48:51],v[24:27]\n"
WT"lgkmcnt(1)\n"
PK"v67,v38,v39\n"
PK"v68,v40,v41\n"
MF"v[0:3],a[72:75],a[8:11],v[0:3]\n"
PK"v69,v42,v43\n"
PK"v70,v44,v45\n"
MF"v[0:3],a[76:79],a[12:15],v[0:3]\n"
PK"v71,v46,v47\n"
PK"v72,v48,v49\n"
MF"v[8:11],a[76:79],a[28:31],v[8:11]\n"
PK"v73,v50,v51\n"
PK"v74,v52,v53\n"
MF"v[8:11],a[72:75],a[24:27],v[8:11]\n"
PK"v75,v54,v55\n"
PK"v76,v56,v57\n"
MF"v[16:19],a[72:75],a[40:43],v[16:19]\n"
PK"v77,v58,v59\n"
PK"v78,v60,v61\n"
MF"v[16:19],a[76:79],a[44:47],v[16:19]\n"
RD"a[96:99],v226 offset:8192\n"
PK"v79,v62,v63\n"
MF"v[24:27],a[76:79],a[60:63],v[24:27]\n"
EX"v0,v0\n"
MF"v[24:27],a[72:75],a[56:59],v[24:27]\n"
EX"v1,v1\n"
MF"v[4:7],a[80:83],a[0:3],v[212:215]\n"
EX"v2,v2\n"
MF"v[4:7],a[84:87],a[4:7],v[4:7]\n"
RD"a[100:103],v226 offset:9216\n"
PK"v0,v0,v1\n"
MF"v[12:15],a[84:87],a[20:23],0\n"
EX"v3,v3\n"
MF"v[12:15],a[80:83],a[16:19],v[12:15]\n"
EX"v8,v8\n"
MF"v[20:23],a[80:83],a[32:35],0\n"
EX"v9,v9\n"
MF"v[20:23],a[84:87],a[36:39],v[20:23]\n"
RD"a[104:107],v226 offset:10240\n"
PK"v1,v2,v3\n"
MF"v[28:31],a[84:87],a[52:55],0\n"
EX"v10,v10\n"
MF"v[28:31],a[80:83],a[48:51],v[28:31]\n"
EX"v11,v11\n"
MF"v[4:7],a[88:91],a[8:11],v[4:7]\n"
WT"lgkmcnt(1)\n"
EX"v16,v16\n"
MF"v[4:7],a[92:95],a[12:15],v[4:7]\n"
RD"a[108:111],v226 offset:11264\n"
PK"v8,v8,v9\n"
MF"v[12:15],a[92:95],a[28:31],v[12:15]\n"
EX"v17,v17\n"
MF"v[12:15],a[88:91],a[24:27],v[12:15]\n"
EX"v18,v18\n"
MF"v[20:23],a[88:91],a[40:43],v[20:23]\n"
EX"v19,v19\n"
MF"v[20:23],a[92:95],a[44:47],v[20:23]\n"
RD"a[112:115],v226 offset:12288\n"
PK"v9,v10,v11\n"
MF"v[28:31],a[92:95],a[60:63],v[28:31]\n"
EX"v24,v24\n"
MF"v[28:31],a[88:91],a[56:59],v[28:31]\n"
EX"v25,v25\n"
MF"v[32:35],a[96:99],a[0:3],v[216:219]\n"
EX"v26,v26\n"
MF"v[32:35],a[100:103],a[4:7],v[32:35]\n"
RD"a[116:119],v226 offset:13312\n"
PK"v16,v16,v17\n"
MF"v[40:43],a[100:103],a[20:23],0\n"
EX"v27,v27\n"
MF"v[40:43],a[96:99],a[16:19],v[40:43]\n"
EX"v4,v4\n"
MF"v[48:51],a[96:99],a[32:35],0\n"
EX"v5,v5\n"
MF"v[48:51],a[100:103],a[36:39],v[48:51]\n"
RD"a[120:123],v226 offset:14336\n"
PK"v17,v18,v19\n"
MF"v[56:59],a[100:103],a[52:55],0\n"
EX"v6,v6\n"
MF"v[56:59],a[96:99],a[48:51],v[56:59]\n"
WT"lgkmcnt(2)\n"
EX"v7,v7\n"
MF"v[32:35],a[104:107],a[8:11],v[32:35]\n"
EX"v12,v12\n"
MF"v[32:35],a[108:111],a[12:15],v[32:35]\n"
RD"a[124:127],v226 offset:15360\n"
"v_mov_b32 v227,v226\n"
MF"v[40:43],a[108:111],a[28:31],v[40:43]\n"
EX"v13,v13\n"
MF"v[40:43],a[104:107],a[24:27],v[40:43]\n"
EX"v14,v14\n"
MF"v[48:51],a[104:107],a[40:43],v[48:51]\n"
EX"v15,v15\n"
MF"v[48:51],a[108:111],a[44:47],v[48:51]\n"
RD"a[64:67],v227 offset:16384\n"
PK"v24,v24,v25\n"
MF"v[56:59],a[108:111],a[60:63],v[56:59]\n"
EX"v20,v20\n"
MF"v[56:59],a[104:107],a[56:59],v[56:59]\n"
EX"v21,v21\n"
MF"v[36:39],a[112:115],a[0:3],v[220:223]\n"
WT"lgkmcnt(1)\n"
EX"v22,v22\n"
MF"v[36:39],a[116:119],a[4:7],v[36:39]\n"
RD"a[68:71],v227 offset:24576\n"
PK"v25,v26,v27\n"
MF"v[44:47],a[116:119],a[20:23],0\n"
EX"v23,v23\n"
MF"v[44:47],a[112:115],a[16:19],v[44:47]\n"
EX"v28,v28\n"
MF"v[52:55],a[112:115],a[32:35],0\n"
EX"v29,v29\n"
MF"v[52:55],a[116:119],a[36:39],v[52:55]\n"
RD"a[72:75],v227 offset:17408\n"
"v_readlane_b32 s43,v228,s42\n"
MF"v[60:63],a[116:119],a[52:55],0\n"
EX"v30,v30\n"
MF"v[60:63],a[112:115],a[48:51],v[60:63]\n"
"s_mov_b32 m0,s40\n"
EX"v31,v31\n"
MF"v[36:39],a[120:123],a[8:11],v[36:39]\n"
PK"v2,v4,v5\n"
PK"v3,v6,v7\n"
MF"v[36:39],a[124:127],a[12:15],v[36:39]\n"
RD"a[76:79],v227 offset:25600\n"
"v_readlane_b32 s44,v229,s42\n"
MF"v[44:47],a[124:127],a[28:31],v[44:47]\n"
PK"v10,v12,v13\n"
PK"v11,v14,v15\n"
MF"v[44:47],a[120:123],a[24:27],v[44:47]\n"
SA"s42,s42,1\n"
BL"v225,s[8:11],s43 offen offset:0 sc1 lds\n"
MF"v[52:55],a[120:123],a[40:43],v[52:55]\n"
PK"v18,v20,v21\n"
PK"v19,v22,v23\n"
MF"v[52:55],a[124:127],a[44:47],v[52:55]\n"
RD"a[80:83],v227 offset:18432\n"
PK"v26,v28,v29\n"
MF"v[60:63],a[124:127],a[60:63],v[60:63]\n"
EX"v32,v32\n"
MF"v[60:63],a[120:123],a[56:59],v[60:63]\n"
WT"lgkmcnt(2)\n"
PK"v27,v30,v31\n"
SA"s41,s41,32768\n"
MF"v[80:83],a[64:67],v[64:67],v[80:83]\n"
"s_cmp_eq_u32 s41,163840\n"
EX"v33,v33\n"
MF"v[80:83],a[68:71],v[0:3],v[80:83]\n"
"s_cselect_b32 s41,0,s41\n"
RD"a[84:87],v227 offset:26624\n"
"v_add_u32 v226,s41,v224\n"
MF"v[112:115],a[68:71],v[8:11],v[112:115]\n"
EX"v34,v34\n"
MF"v[112:115],a[64:67],v[68:71],v[112:115]\n"
BL"v225,s[8:11],s43 offen offset:1024 sc1 lds\n"
MF"v[144:147],a[64:67],v[72:75],v[144:147]\n"
EX"v35,v35\n"
MF"v[144:147],a[68:71],v[16:19],v[144:147]\n"
RD"a[88:91],v227 offset:19456\n"
MF"v[176:179],a[68:71],v[24:27],v[176:179]\n"
EX"v40,v40\n"
MF"v[176:179],a[64:67],v[76:79],v[176:179]\n"
MF"v[84:87],a[72:75],v[64:67],v[84:87]\n"
WT"lgkmcnt(1)\n"
EX"v41,v41\n"
MF"v[84:87],a[76:79],v[0:3],v[84:87]\n"
RD"a[92:95],v227 offset:27648\n"
MF"v[116:119],a[76:79],v[8:11],v[116:119]\n"
EX"v42,v42\n"
MF"v[116:119],a[72:75],v[68:71],v[116:119]\n"
BL"v225,s[8:11],s43 offen offset:2048 sc1 lds\n"
MF"v[148:151],a[72:75],v[72:75],v[148:151]\n"
EX"v43,v43\n"
MF"v[148:151],a[76:79],v[16:19],v[148:151]\n"
RD"a[96:99],v227 offset:20480\n"
MF"v[180:183],a[76:79],v[24:27],v[180:183]\n"
EX"v48,v48\n"
MF"v[180:183],a[72:75],v[76:79],v[180:183]\n"
MF"v[88:91],a[80:83],v[64:67],v[88:91]\n"
EX"v49,v49\n"
MF"v[88:91],a[84:87],v[0:3],v[88:91]\n"
RD"a[100:103],v227 offset:28672\n"
MF"v[120:123],a[84:87],v[8:11],v[120:123]\n"
EX"v50,v50\n"
MF"v[120:123],a[80:83],v[68:71],v[120:123]\n"
BL"v225,s[8:11],s43 offen offset:3072 sc1 lds\n"
MF"v[152:155],a[80:83],v[72:75],v[152:155]\n"
EX"v51,v51\n"
MF"v[152:155],a[84:87],v[16:19],v[152:155]\n"
RD"a[104:107],v227 offset:21504\n"
MF"v[184:187],a[84:87],v[24:27],v[184:187]\n"
EX"v56,v56\n"
MF"v[184:187],a[80:83],v[76:79],v[184:187]\n"
WT"lgkmcnt(2)\n"
SA"m0,s40,16384\n"
MF"v[92:95],a[88:91],v[64:67],v[92:95]\n"
EX"v57,v57\n"
MF"v[92:95],a[92:95],v[0:3],v[92:95]\n"
RD"a[108:111],v227 offset:29696\n"
MF"v[124:127],a[92:95],v[8:11],v[124:127]\n"
EX"v58,v58\n"
MF"v[124:127],a[88:91],v[68:71],v[124:127]\n"
BL"v225,s[8:11],s44 offen offset:0 sc1 lds\n"
MF"v[156:159],a[88:91],v[72:75],v[156:159]\n"
SA"s40,s40,32768\n"
EX"v59,v59\n"
MF"v[156:159],a[92:95],v[16:19],v[156:159]\n"
"s_cmp_eq_u32 s40,s46\n"
RD"a[112:115],v227 offset:22528\n"
"s_cselect_b32 s40,s47,s40\n"
MF"v[188:191],a[92:95],v[24:27],v[188:191]\n"
EX"v36,v36\n"
MF"v[188:191],a[88:91],v[76:79],v[188:191]\n"
MF"v[96:99],a[96:99],v[64:67],v[96:99]\n"
WT"lgkmcnt(1)\n"
EX"v37,v37\n"
MF"v[96:99],a[100:103],v[0:3],v[96:99]\n"
RD"a[116:119],v227 offset:30720\n"
MF"v[128:131],a[100:103],v[8:11],v[128:131]\n"
EX"v38,v38\n"
MF"v[128:131],a[96:99],v[68:71],v[128:131]\n"
BL"v225,s[8:11],s44 offen offset:1024 sc1 lds\n"
MF"v[160:163],a[96:99],v[72:75],v[160:163]\n"
EX"v39,v39\n"
MF"v[160:163],a[100:103],v[16:19],v[160:163]\n"
RD"a[120:123],v227 offset:23552\n"
MF"v[192:195],a[100:103],v[24:27],v[192:195]\n"
EX"v44,v44\n"
MF"v[192:195],a[96:99],v[76:79],v[192:195]\n"
MF"v[100:103],a[104:107],v[64:67],v[100:103]\n"
EX"v45,v45\n"
MF"v[100:103],a[108:111],v[0:3],v[100:103]\n"
RD"a[124:127],v227 offset:31744\n"
MF"v[132:135],a[108:111],v[8:11],v[132:135]\n"
EX"v46,v46\n"
MF"v[132:135],a[104:107],v[68:71],v[132:135]\n"
BL"v225,s[8:11],s44 offen offset:2048 sc1 lds\n"
MF"v[164:167],a[104:107],v[72:75],v[164:167]\n"
EX"v47,v47\n"
MF"v[164:167],a[108:111],v[16:19],v[164:167]\n"
MF"v[196:199],a[108:111],v[24:27],v[196:199]\n"
EX"v52,v52\n"
MF"v[196:199],a[104:107],v[76:79],v[196:199]\n"
WT"lgkmcnt(1)\n"
MF"v[104:107],a[112:115],v[64:67],v[104:107]\n"
EX"v53,v53\n"
MF"v[104:107],a[116:119],v[0:3],v[104:107]\n"
MF"v[136:139],a[116:119],v[8:11],v[136:139]\n"
EX"v54,v54\n"
MF"v[136:139],a[112:115],v[68:71],v[136:139]\n"
WT"vmcnt(15) lgkmcnt(0)\n"
BL"v225,s[8:11],s44 offen offset:3072 sc1 lds\n"
MF"v[168:171],a[112:115],v[72:75],v[168:171]\n"
"s_barrier\n"
MF"v[168:171],a[116:119],v[16:19],v[168:171]\n"
RD"a[64:67],v226 offset:0\n"
MF"v[200:203],a[116:119],v[24:27],v[200:203]\n"
EX"v55,v55\n"
MF"v[200:203],a[112:115],v[76:79],v[200:203]\n"
MF"v[108:111],a[120:123],v[64:67],v[108:111]\n"
EX"v60,v60\n"
MF"v[108:111],a[124:127],v[0:3],v[108:111]\n"
RD"a[68:71],v226 offset:1024\n"
MF"v[140:143],a[124:127],v[8:11],v[140:143]\n"
EX"v61,v61\n"
MF"v[140:143],a[120:123],v[68:71],v[140:143]\n"
RD"a[72:75],v226 offset:2048\n"
MF"v[172:175],a[120:123],v[72:75],v[172:175]\n"
EX"v62,v62\n"
MF"v[172:175],a[124:127],v[16:19],v[172:175]\n"
RD"a[76:79],v226 offset:3072\n"
MF"v[204:207],a[124:127],v[24:27],v[204:207]\n"
EX"v63,v63\n"
MF"v[204:207],a[120:123],v[76:79],v[204:207]\n"
MF"a[132:135],a[128:131],v[64:67],a[132:135]\n"
MF"a[132:135],a[128:131],v[0:3],a[132:135]\n"
RD"a[80:83],v226 offset:4096\n"
MF"a[136:139],a[128:131],v[8:11],a[136:139]\n"
MF"a[136:139],a[128:131],v[68:71],a[136:139]\n"
RD"a[84:87],v226 offset:5120\n"
MF"a[140:143],a[128:131],v[72:75],a[140:143]\n"
MF"a[140:143],a[128:131],v[16:19],a[140:143]\n"
RD"a[88:91],v226 offset:6144\n"
MF"a[144:147],a[128:131],v[24:27],a[144:147]\n"
WT"lgkmcnt(5)\n"
MF"a[144:147],a[128:131],v[76:79],a[144:147]\n"
"LDB_%=:\n"
MF"v[8:11],a[64:67],a[16:19],v[208:211]\n"
"v_readlane_b32 s43,v228,s42\n"
PK"v64,v32,v33\n"
MF"v[8:11],a[68:71],a[20:23],v[8:11]\n"
RD"a[92:95],v226 offset:7168\n"
PK"v65,v34,v35\n"
MF"v[16:19],a[68:71],a[36:39],0\n"
PK"v66,v36,v37\n"
PK"v67,v38,v39\n"
MF"v[16:19],a[64:67],a[32:35],v[16:19]\n"
"s_mov_b32 m0,s40\n"
PK"v68,v40,v41\n"
PK"v69,v42,v43\n"
MF"v[24:27],a[64:67],a[48:51],0\n"
PK"v70,v44,v45\n"
PK"v71,v46,v47\n"
MF"v[24:27],a[68:71],a[52:55],v[24:27]\n"
WT"lgkmcnt(1)\n"
RD"a[96:99],v226 offset:8192\n"
"v_readlane_b32 s44,v229,s42\n"
MF"v[8:11],a[72:75],a[24:27],v[8:11]\n"
PK"v72,v48,v49\n"
PK"v73,v50,v51\n"
MF"v[8:11],a[76:79],a[28:31],v[8:11]\n"
SA"s42,s42,1\n"
BL"v225,s[8:11],s43 offen offset:0 sc1 lds\n"
MF"v[16:19],a[76:79],a[44:47],v[16:19]\n"
PK"v74,v52,v53\n"
PK"v75,v54,v55\n"
MF"v[16:19],a[72:75],a[40:43],v[16:19]\n"
RD"a[100:103],v226 offset:9216\n"
PK"v76,v56,v57\n"
PK"v77,v58,v59\n"
MF"v[24:27],a[72:75],a[56:59],v[24:27]\n"
PK"v78,v60,v61\n"
PK"v79,v62,v63\n"
MF"v[24:27],a[76:79],a[60:63],v[24:27]\n"
RD"a[104:107],v226 offset:10240\n"
MF"v[12:15],a[80:83],a[16:19],v[212:215]\n"
EX"v8,v8\n"
MF"v[12:15],a[84:87],a[20:23],v[12:15]\n"
RD"a[108:111],v226 offset:11264\n"
"v_lshrrev_b32 v235,7,v224\n"
MF"v[20:23],a[84:87],a[36:39],0\n"
EX"v9,v9\n"
MF"v[20:23],a[80:83],a[32:35],v[20:23]\n"
BL"v225,s[8:11],s43 offen offset:1024 sc1 lds\n"
MF"v[28:31],a[80:83],a[48:51],0\n"
EX"v10,v10\n"
"v_lshrrev_b32 v230,1,v224\n"
MF"v[28:31],a[84:87],a[52:55],v[28:31]\n"
RD"a[112:115],v226 offset:12288\n"
"v_lshl_add_u32 v230,v235,27,v230\n"
MF"v[12:15],a[88:91],a[24:27],v[12:15]\n"
WT"lgkmcnt(2)\n"
EX"v11,v11\n"
PK"v8,v8,v9\n"
MF"v[12:15],a[92:95],a[28:31],v[12:15]\n"
RD"a[116:119],v226 offset:13312\n"
PK"v9,v10,v11\n"
MF"v[20:23],a[92:95],a[44:47],v[20:23]\n"
"s_lshl_b32 s48,s35,8\n"
EX"v16,v16\n"
MF"v[20:23],a[88:91],a[40:43],v[20:23]\n"
MF"v[28:31],a[88:91],a[56:59],v[28:31]\n"
SA"s48,s48,92160\n"
EX"v17,v17\n"
MF"v[28:31],a[92:95],a[60:63],v[28:31]\n"
BL"v225,s[8:11],s43 offen offset:2048 sc1 lds\n"
MF"v[40:43],a[96:99],a[16:19],v[216:219]\n"
"s_andn2_b32 s49,s61,255\n"
EX"v18,v18\n"
MF"v[40:43],a[100:103],a[20:23],v[40:43]\n"
RD"a[120:123],v226 offset:14336\n"
PK"v16,v16,v17\n"
MF"v[48:51],a[100:103],a[36:39],0\n"
EX"v19,v19\n"
MF"v[48:51],a[96:99],a[32:35],v[48:51]\n"
RD"a[124:127],v226 offset:15360\n"
"v_bfe_u32 v236,v224,4,2\n"
MF"v[56:59],a[96:99],a[48:51],0\n"
EX"v24,v24\n"
MF"v[56:59],a[100:103],a[52:55],v[56:59]\n"
PK"v17,v18,v19\n"
"v_lshl_add_u32 v236,v236,6,s49\n"
MF"v[40:43],a[104:107],a[24:27],v[40:43]\n"
WT"lgkmcnt(2)\n"
EX"v25,v25\n"
MF"v[40:43],a[108:111],a[28:31],v[40:43]\n"
BL"v225,s[8:11],s43 offen offset:3072 sc1 lds\n"
MF"v[48:51],a[108:111],a[44:47],v[48:51]\n"
EX"v26,v26\n"
MF"v[48:51],a[104:107],a[40:43],v[48:51]\n"
"v_mov_b32 v237,s48\n"
"v_mov_b32 v227,v226\n"
MF"v[56:59],a[104:107],a[56:59],v[56:59]\n"
RD"a[64:67],v227 offset:16384\n"
PK"v24,v24,v25\n"
MF"v[56:59],a[108:111],a[60:63],v[56:59]\n"
SA"m0,s40,16384\n"
EX"v27,v27\n"
"v_cmp_lt_u32 vcc,511,v224\n"
MF"v[44:47],a[112:115],a[16:19],v[220:223]\n"
EX"v12,v12\n"
MF"v[44:47],a[116:119],a[20:23],v[44:47]\n"
RD"a[68:71],v227 offset:17408\n"
"v_cndmask_b32 v236,v236,v237,vcc\n"
MF"v[52:55],a[116:119],a[36:39],0\n"
EX"v13,v13\n"
PK"v25,v26,v27\n"
MF"v[52:55],a[112:115],a[32:35],v[52:55]\n"
BL"v225,s[8:11],s44 offen offset:0 sc1 lds\n"
MF"v[60:63],a[112:115],a[48:51],0\n"
SA"s40,s40,32768\n"
EX"v14,v14\n"
MF"v[60:63],a[116:119],a[52:55],v[60:63]\n"
WT"lgkmcnt(1)\n"
RD"a[72:75],v227 offset:18432\n"
PK"v10,v12,v13\n"
MF"v[44:47],a[120:123],a[24:27],v[44:47]\n"
"s_cmp_eq_u32 s40,s46\n"
EX"v15,v15\n"
MF"v[44:47],a[124:127],a[28:31],v[44:47]\n"
"s_cselect_b32 s40,s47,s40\n"
PK"v11,v14,v15\n"
MF"v[52:55],a[124:127],a[44:47],v[52:55]\n"
EX"v20,v20\n"
MF"v[52:55],a[120:123],a[40:43],v[52:55]\n"
RD"a[76:79],v227 offset:19456\n"
MF"v[60:63],a[120:123],a[56:59],v[60:63]\n"
EX"v21,v21\n"
MF"v[60:63],a[124:127],a[60:63],v[60:63]\n"
SA"s41,s41,32768\n"
BL"v225,s[8:11],s44 offen offset:1024 sc1 lds\n"
MF"v[80:83],a[64:67],v[64:67],v[80:83]\n"
"s_cmp_eq_u32 s41,163840\n"
EX"v22,v22\n"
MF"v[112:115],a[64:67],v[68:71],v[112:115]\n"
"s_cselect_b32 s41,0,s41\n"
RD"a[80:83],v227 offset:20480\n"
"v_add_u32 v226,s41,v224\n"
MF"v[144:147],a[64:67],v[72:75],v[144:147]\n"
EX"v23,v23\n"
PK"v18,v20,v21\n"
MF"v[176:179],a[64:67],v[76:79],v[176:179]\n"
WT"lgkmcnt(2)\n"
PK"v19,v22,v23\n"
MF"v[84:87],a[68:71],v[64:67],v[84:87]\n"
EX"v28,v28\n"
MF"v[116:119],a[68:71],v[68:71],v[116:119]\n"
RD"a[84:87],v227 offset:21504\n"
MF"v[148:151],a[68:71],v[72:75],v[148:151]\n"
EX"v29,v29\n"
MF"v[180:183],a[68:71],v[76:79],v[180:183]\n"
BL"v225,s[8:11],s44 offen offset:2048 sc1 lds\n"
MF"v[88:91],a[72:75],v[64:67],v[88:91]\n"
EX"v30,v30\n"
MF"v[120:123],a[72:75],v[68:71],v[120:123]\n"
RD"a[88:91],v227 offset:22528\n"
PK"v26,v28,v29\n"
MF"v[152:155],a[72:75],v[72:75],v[152:155]\n"
EX"v31,v31\n"
MF"v[184:187],a[72:75],v[76:79],v[184:187]\n"
WT"lgkmcnt(2)\n"
PK"v27,v30,v31\n"
MF"v[92:95],a[76:79],v[64:67],v[92:95]\n"
EX"v40,v40\n"
MF"v[124:127],a[76:79],v[68:71],v[124:127]\n"
RD"a[92:95],v227 offset:23552\n"
MF"v[156:159],a[76:79],v[72:75],v[156:159]\n"
EX"v41,v41\n"
MF"v[188:191],a[76:79],v[76:79],v[188:191]\n"
BL"v225,s[8:11],s44 offen offset:3072 sc1 lds\n"
MF"v[96:99],a[80:83],v[64:67],v[96:99]\n"
EX"v42,v42\n"
MF"v[128:131],a[80:83],v[68:71],v[128:131]\n"
RD"a[96:99],v227 offset:24576\n"
MF"v[160:163],a[80:83],v[72:75],v[160:163]\n"
EX"v43,v43\n"
MF"v[192:195],a[80:83],v[76:79],v[192:195]\n"
WT"lgkmcnt(2)\n"
RD"a[100:103],v227 offset:25600\n"
MF"v[100:103],a[84:87],v[64:67],v[100:103]\n"
EX"v48,v48\n"
MF"v[132:135],a[84:87],v[68:71],v[132:135]\n"
MF"v[164:167],a[84:87],v[72:75],v[164:167]\n"
EX"v49,v49\n"
MF"v[196:199],a[84:87],v[76:79],v[196:199]\n"
MF"v[104:107],a[88:91],v[64:67],v[104:107]\n"
EX"v50,v50\n"
MF"v[136:139],a[88:91],v[68:71],v[136:139]\n"
RD"a[104:107],v227 offset:26624\n"
MF"v[168:171],a[88:91],v[72:75],v[168:171]\n"
EX"v51,v51\n"
MF"v[200:203],a[88:91],v[76:79],v[200:203]\n"
WT"lgkmcnt(1)\n"
RD"a[108:111],v227 offset:27648\n"
MF"v[108:111],a[92:95],v[64:67],v[108:111]\n"
EX"v44,v44\n"
MF"v[140:143],a[92:95],v[68:71],v[140:143]\n"
RD"a[112:115],v227 offset:28672\n"
MF"v[172:175],a[92:95],v[72:75],v[172:175]\n"
EX"v45,v45\n"
MF"v[204:207],a[92:95],v[76:79],v[204:207]\n"
MF"a[132:135],a[128:131],v[64:67],a[132:135]\n"
EX"v46,v46\n"
MF"a[136:139],a[128:131],v[68:71],a[136:139]\n"
RD"a[116:119],v227 offset:29696\n"
PK"v68,v40,v41\n"
PK"v69,v42,v43\n"
MF"a[140:143],a[128:131],v[72:75],a[140:143]\n"
EX"v47,v47\n"
PK"v72,v48,v49\n"
MF"a[144:147],a[128:131],v[76:79],a[144:147]\n"
RD"a[120:123],v227 offset:30720\n"
PK"v73,v50,v51\n"
PK"v70,v44,v45\n"
MF"v[112:115],a[96:99],v[8:11],v[112:115]\n"
EX"v52,v52\n"
MF"v[144:147],a[96:99],v[16:19],v[144:147]\n"
RD"a[124:127],v227 offset:31744\n"
PK"v71,v46,v47\n"
MF"v[176:179],a[96:99],v[24:27],v[176:179]\n"
EX"v53,v53\n"
MF"v[116:119],a[100:103],v[8:11],v[116:119]\n"
"v_accvgpr_read_b32 v251,a132\n"
"v_subrev_u32 v253,0x30800000,v251\n"
MF"v[148:151],a[100:103],v[16:19],v[148:151]\n"
"v_cmp_le_u32 vcc,0x1f000000,v253\n"
"s_cmp_lg_u64 vcc,0\n"
"s_cbranch_scc1 L1014_%=\n"
"L1013_%=:\n"
MF"v[180:183],a[100:103],v[24:27],v[180:183]\n"
WT"lgkmcnt(2)\n"
EX"v54,v54\n"
MF"v[120:123],a[104:107],v[8:11],v[120:123]\n"
"v_rcp_f32 v251,v251\n"
PK"v74,v52,v53\n"
MF"v[152:155],a[104:107],v[16:19],v[152:155]\n"
WT"vmcnt(16) lgkmcnt(0)\n"
EX"v55,v55\n"
MF"v[184:187],a[104:107],v[24:27],v[184:187]\n"
"s_barrier\n"
MF"v[124:127],a[108:111],v[8:11],v[124:127]\n"
RD"a[64:67],v226 offset:0\n"
MU"v80,v251,v80\n"
MU"v81,v251,v81\n"
MF"v[156:159],a[108:111],v[16:19],v[156:159]\n"
MU"v82,v251,v82\n"
MU"v83,v251,v83\n"
PK"v75,v54,v55\n"
MF"v[188:191],a[108:111],v[24:27],v[188:191]\n"
MU"v84,v251,v84\n"
MU"v85,v251,v85\n"
MU"v86,v251,v86\n"
MF"v[128:131],a[112:115],v[8:11],v[128:131]\n"
MU"v87,v251,v87\n"
PK"v80,v80,v81\n"
PK"v81,v82,v83\n"
MF"v[160:163],a[112:115],v[16:19],v[160:163]\n"
RD"a[68:71],v226 offset:1024\n"
PK"v82,v84,v85\n"
PK"v83,v86,v87\n"
MF"v[192:195],a[112:115],v[24:27],v[192:195]\n"
BS"v[80:83],v224,s[8:11],s59 offen offset:0 sc1\n"
MU"v88,v251,v88\n"
MF"v[132:135],a[116:119],v[8:11],v[132:135]\n"
RD"a[72:75],v226 offset:2048\n"
MU"v89,v251,v89\n"
MU"v90,v251,v90\n"
MF"v[164:167],a[116:119],v[16:19],v[164:167]\n"
MU"v91,v251,v91\n"
MU"v92,v251,v92\n"
MU"v93,v251,v93\n"
MF"v[196:199],a[116:119],v[24:27],v[196:199]\n"
RD"a[76:79],v226 offset:3072\n"
MU"v94,v251,v94\n"
MU"v95,v251,v95\n"
MF"v[136:139],a[120:123],v[8:11],v[136:139]\n"
PK"v88,v88,v89\n"
PK"v89,v90,v91\n"
MF"v[168:171],a[120:123],v[16:19],v[168:171]\n"
PK"v90,v92,v93\n"
PK"v91,v94,v95\n"
MF"v[200:203],a[120:123],v[24:27],v[200:203]\n"
BS"v[88:91],v224,s[8:11],s59 offen offset:1024 sc1\n"
MU"v96,v251,v96\n"
MF"v[140:143],a[124:127],v[8:11],v[140:143]\n"
RD"a[80:83],v226 offset:4096\n"
MU"v97,v251,v97\n"
MU"v98,v251,v98\n"
MF"v[172:175],a[124:127],v[16:19],v[172:175]\n"
MU"v99,v251,v99\n"
MU"v100,v251,v100\n"
MU"v101,v251,v101\n"
MF"v[204:207],a[124:127],v[24:27],v[204:207]\n"
MU"v104,v251,v104\n"
MU"v105,v251,v105\n"
MU"v102,v251,v102\n"
MF"a[136:139],a[128:131],v[8:11],a[136:139]\n"
PK"v96,v96,v97\n"
MU"v106,v251,v106\n"
MU"v103,v251,v103\n"
MF"a[140:143],a[128:131],v[16:19],a[140:143]\n"
WT"lgkmcnt(3)\n"
MU"v107,v251,v107\n"
PK"v97,v98,v99\n"
PK"v98,v100,v101\n"
MF"a[144:147],a[128:131],v[24:27],a[144:147]\n"
MU"v108,v251,v108\n"
MU"v109,v251,v109\n"
PK"v99,v102,v103\n"
BS"v[96:99],v224,s[8:11],s59 offen offset:2048 sc1\n"
MU"v110,v251,v110\n"
MU"v111,v251,v111\n"
PK"v104,v104,v105\n"
PK"v105,v106,v107\n"
PK"v106,v108,v109\n"
PK"v107,v110,v111\n"
BS"v[104:107],v224,s[8:11],s59 offen offset:3072 sc1\n"
RD"a[84:87],v226 offset:5120\n"
RD"a[88:91],v226 offset:6144\n"
"s_cmp_eq_u32 s34,7\n"
"s_cbranch_scc1 LD2_%=\n"
MF"v[0:3],a[64:67],a[32:35],0\n"
"s_mov_b32 m0,s40\n"
EX"v56,v56\n"
MF"v[0:3],a[68:71],a[36:39],v[0:3]\n"
EX"v57,v57\n"
MF"v[8:11],a[68:71],a[52:55],0\n"
"s_lshl_b32 s50,s35,6\n"
RD"a[92:95],v226 offset:7168\n"
"v_readlane_b32 s43,v228,s42\n"
MF"v[8:11],a[64:67],a[48:51],v[8:11]\n"
WT"lgkmcnt(5)\n"
EX"v58,v58\n"
MF"v[0:3],a[72:75],a[40:43],v[0:3]\n"
WT"lgkmcnt(1)\n"
BL"v225,s[8:11],s43 offen offset:0 sc1 lds\n"
"v_readlane_b32 s44,v229,s42\n"
MF"v[0:3],a[76:79],a[44:47],v[0:3]\n"
SA"s50,s50,4352\n"
RD"a[96:99],v226 offset:8192\n"
PK"v76,v56,v57\n"
MF"v[8:11],a[76:79],a[60:63],v[8:11]\n"
SA"s42,s42,1\n"
RD"a[100:103],v226 offset:9216\n"
"v_lshrrev_b32 v237,1,v224\n"
MF"v[8:11],a[72:75],a[56:59],v[8:11]\n"
EX"v59,v59\n"
MF"v[4:7],a[80:83],a[32:35],0\n"
RD"a[104:107],v226 offset:10240\n"
"v_and_b32 v237,56,v237\n"
MF"v[4:7],a[84:87],a[36:39],v[4:7]\n"
EX"v60,v60\n"
PK"v77,v58,v59\n"
MF"v[12:15],a[84:87],a[52:55],0\n"
EX"v61,v61\n"
MF"v[12:15],a[80:83],a[48:51],v[12:15]\n"
RD"a[108:111],v226 offset:11264\n"
RD"a[112:115],v226 offset:12288\n"
MF"v[4:7],a[88:91],a[40:43],v[4:7]\n"
WT"lgkmcnt(4)\n"
BL"v225,s[8:11],s43 offen offset:1024 sc1 lds\n"
MF"v[4:7],a[92:95],a[44:47],v[4:7]\n"
EX"v62,v62\n"
PK"v78,v60,v61\n"
MF"v[12:15],a[92:95],a[60:63],v[12:15]\n"
RD"a[116:119],v226 offset:13312\n"
RD"a[120:123],v226 offset:14336\n"
MF"v[12:15],a[88:91],a[56:59],v[12:15]\n"
EX"v63,v63\n"
MF"v[16:19],a[96:99],a[32:35],0\n"
WT"lgkmcnt(4)\n"
EX"v0,v0\n"
MF"v[16:19],a[100:103],a[36:39],v[16:19]\n"
EX"v1,v1\n"
PK"v79,v62,v63\n"
MF"v[24:27],a[100:103],a[52:55],0\n"
RD"a[124:127],v226 offset:15360\n"
PK"v0,v0,v1\n"
MF"v[24:27],a[96:99],a[48:51],v[24:27]\n"
BL"v225,s[8:11],s43 offen offset:2048 sc1 lds\n"
MF"v[16:19],a[104:107],a[40:43],v[16:19]\n"
WT"lgkmcnt(3)\n"
EX"v2,v2\n"
MF"v[16:19],a[108:111],a[44:47],v[16:19]\n"
"s_lshl_b32 s56,s34,1\n"
RD"a[64:67],v226 offset:16384\n"
RD"a[68:71],v226 offset:17408\n"
MF"v[24:27],a[108:111],a[60:63],v[24:27]\n"
SA"s41,s41,32768\n"
EX"v3,v3\n"
MF"v[24:27],a[104:107],a[56:59],v[24:27]\n"
"s_cmp_eq_u32 s41,163840\n"
RD"a[72:75],v226 offset:18432\n"
PK"v1,v2,v3\n"
MF"v[20:23],a[112:115],a[32:35],0\n"
WT"lgkmcnt(4)\n"
EX"v8,v8\n"
MF"v[20:23],a[116:119],a[36:39],v[20:23]\n"
"s_cselect_b32 s41,0,s41\n"
EX"v9,v9\n"
"v_add_u32 v227,s41,v224\n"
MF"v[28:31],a[116:119],a[52:55],0\n"
"s_sub_u32 s56,12,s56\n"
BL"v225,s[8:11],s43 offen offset:3072 sc1 lds\n"
MF"v[28:31],a[112:115],a[48:51],v[28:31]\n"
"s_sub_u32 s22,s56,2\n"
RD"a[76:79],v226 offset:19456\n"
PK"v8,v8,v9\n"
MF"v[20:23],a[120:123],a[40:43],v[20:23]\n"
WT"lgkmcnt(2)\n"
EX"v10,v10\n"
MF"v[20:23],a[124:127],a[44:47],v[20:23]\n"
"s_min_u32 s56,s56,1\n"
RD"a[80:83],v226 offset:20480\n"
RD"a[84:87],v226 offset:21504\n"
EX"v11,v11\n"
MF"v[28:31],a[124:127],a[60:63],v[28:31]\n"
"s_mov_b32 s57,s61\n"
RD"a[88:91],v226 offset:22528\n"
PK"v9,v10,v11\n"
MF"v[28:31],a[120:123],a[56:59],v[28:31]\n"
SA"m0,s40,16384\n"
EX"v4,v4\n"
MF"v[32:35],a[64:67],a[32:35],0\n"
"buffer_load_dwordx2 v[238:239],v237,s[8:11],s50 offen sc1\n"
MF"v[32:35],a[68:71],a[36:39],v[32:35]\n"
EX"v5,v5\n"
MF"v[40:43],a[68:71],a[52:55],0\n"
SA"s40,s40,32768\n"
BL"v225,s[8:11],s44 offen offset:0 sc1 lds\n"
MF"v[40:43],a[64:67],a[48:51],v[40:43]\n"
WT"lgkmcnt(3)\n"
RD"a[92:95],v226 offset:23552\n"
PK"v2,v4,v5\n"
EX"v6,v6\n"
MF"v[32:35],a[72:75],a[40:43],v[32:35]\n"
"s_cmp_eq_u32 s40,s46\n"
RD"a[96:99],v226 offset:24576\n"
RD"a[100:103],v226 offset:25600\n"
MF"v[32:35],a[76:79],a[44:47],v[32:35]\n"
"s_cselect_b32 s40,s47,s40\n"
EX"v7,v7\n"
MF"v[40:43],a[76:79],a[60:63],v[40:43]\n"
RD"a[104:107],v226 offset:26624\n"
PK"v3,v6,v7\n"
MF"v[40:43],a[72:75],a[56:59],v[40:43]\n"
WT"lgkmcnt(4)\n"
EX"v12,v12\n"
MF"v[36:39],a[80:83],a[32:35],0\n"
EX"v13,v13\n"
MF"v[36:39],a[84:87],a[36:39],v[36:39]\n"
BL"v225,s[8:11],s44 offen offset:1024 sc1 lds\n"
MF"v[44:47],a[84:87],a[52:55],0\n"
EX"v14,v14\n"
MF"v[44:47],a[80:83],a[48:51],v[44:47]\n"
RD"a[108:111],v226 offset:27648\n"
RD"a[112:115],v226 offset:28672\n"
PK"v10,v12,v13\n"
MF"v[36:39],a[88:91],a[40:43],v[36:39]\n"
WT"lgkmcnt(3)\n"
EX"v15,v15\n"
MF"v[36:39],a[92:95],a[44:47],v[36:39]\n"
RD"a[116:119],v226 offset:29696\n"
RD"a[120:123],v226 offset:30720\n"
MF"v[44:47],a[92:95],a[60:63],v[44:47]\n"
EX"v16,v16\n"
PK"v11,v14,v15\n"
MF"v[44:47],a[88:91],a[56:59],v[44:47]\n"
EX"v17,v17\n"
MF"v[48:51],a[96:99],a[32:35],0\n"
BL"v225,s[8:11],s44 offen offset:2048 sc1 lds\n"
MF"v[48:51],a[100:103],a[36:39],v[48:51]\n"
WT"vmcnt(16)\n"
EX"v18,v18\n"
MF"v[56:59],a[100:103],a[52:55],0\n"
RD"a[124:127],v226 offset:31744\n"
PK"v16,v16,v17\n"
MF"v[56:59],a[96:99],a[48:51],v[56:59]\n"
WT"lgkmcnt(3)\n"
EX"v19,v19\n"
MF"v[48:51],a[104:107],a[40:43],v[48:51]\n"
"s_barrier\n"
MF"v[48:51],a[108:111],a[44:47],v[48:51]\n"
RD"a[96:99],v227 offset:8192\n"
PK"v17,v18,v19\n"
MF"v[56:59],a[108:111],a[60:63],v[56:59]\n"
EX"v24,v24\n"
MF"v[56:59],a[104:107],a[56:59],v[56:59]\n"
EX"v25,v25\n"
MF"v[52:55],a[112:115],a[32:35],0\n"
WT"lgkmcnt(2)\n"
RD"a[100:103],v227 offset:9216\n"
"v_readlane_b32 s43,v228,s42\n"
MF"v[52:55],a[116:119],a[36:39],v[52:55]\n"
BL"v225,s[8:11],s44 offen offset:3072 sc1 lds\n"
EX"v26,v26\n"
MF"v[60:63],a[116:119],a[52:55],0\n"
EX"v27,v27\n"
PK"v24,v24,v25\n"
MF"v[60:63],a[112:115],a[48:51],v[60:63]\n"
"s_mov_b32 m0,s40\n"
RD"a[104:107],v227 offset:10240\n"
RD"a[108:111],v227 offset:11264\n"
MF"v[52:55],a[120:123],a[40:43],v[52:55]\n"
WT"lgkmcnt(3)\n"
EX"v20,v20\n"
MF"v[52:55],a[124:127],a[44:47],v[52:55]\n"
EX"v21,v21\n"
PK"v25,v26,v27\n"
MF"v[60:63],a[124:127],a[60:63],v[60:63]\n"
RD"a[112:115],v227 offset:12288\n"
"v_readlane_b32 s44,v229,s42\n"
MF"v[60:63],a[120:123],a[56:59],v[60:63]\n"
SA"s41,s41,32768\n"
EX"v22,v22\n"
MF"v[112:115],a[96:99],v[68:71],v[112:115]\n"
"s_cmp_eq_u32 s41,163840\n"
BL"v225,s[8:11],s43 offen offset:0 sc1 lds\n"
MF"v[144:147],a[96:99],v[72:75],v[144:147]\n"
"s_cselect_b32 s41,0,s41\n"
EX"v23,v23\n"
PK"v18,v20,v21\n"
MF"v[176:179],a[96:99],v[76:79],v[176:179]\n"
WT"lgkmcnt(1)\n"
RD"a[116:119],v227 offset:13312\n"
"v_add_u32 v226,s41,v224\n"
MF"v[116:119],a[100:103],v[68:71],v[116:119]\n"
SA"s42,s42,1\n"
EX"v28,v28\n"
MF"v[148:151],a[100:103],v[72:75],v[148:151]\n"
RD"a[120:123],v227 offset:14336\n"
PK"v19,v22,v23\n"
MF"v[180:183],a[100:103],v[76:79],v[180:183]\n"
EX"v29,v29\n"
MF"v[120:123],a[104:107],v[68:71],v[120:123]\n"
EX"v30,v30\n"
MF"v[152:155],a[104:107],v[72:75],v[152:155]\n"
BL"v225,s[8:11],s43 offen offset:1024 sc1 lds\n"
MF"v[184:187],a[104:107],v[76:79],v[184:187]\n"
EX"v31,v31\n"
PK"v26,v28,v29\n"
MF"v[124:127],a[108:111],v[68:71],v[124:127]\n"
RD"a[124:127],v227 offset:15360\n"
PK"v27,v30,v31\n"
MF"v[156:159],a[108:111],v[72:75],v[156:159]\n"
EX"v32,v32\n"
MF"v[188:191],a[108:111],v[76:79],v[188:191]\n"
WT"lgkmcnt(1)\n"
MF"v[128:131],a[112:115],v[68:71],v[128:131]\n"
EX"v33,v33\n"
MF"v[160:163],a[112:115],v[72:75],v[160:163]\n"
RD"a[64:67],v227 offset:16384\n"
PK"v64,v32,v33\n"
MF"v[192:195],a[112:115],v[76:79],v[192:195]\n"
BL"v225,s[8:11],s43 offen offset:2048 sc1 lds\n"
MF"v[132:135],a[116:119],v[68:71],v[132:135]\n"
EX"v34,v34\n"
MF"v[164:167],a[116:119],v[72:75],v[164:167]\n"
EX"v35,v35\n"
MF"v[196:199],a[116:119],v[76:79],v[196:199]\n"
EX"v40,v40\n"
MF"v[136:139],a[120:123],v[68:71],v[136:139]\n"
RD"a[68:71],v227 offset:24576\n"
PK"v65,v34,v35\n"
MF"v[168:171],a[120:123],v[72:75],v[168:171]\n"
EX"v41,v41\n"
MF"v[200:203],a[120:123],v[76:79],v[200:203]\n"
WT"lgkmcnt(1)\n"
RD"a[72:75],v227 offset:17408\n"
RD"a[76:79],v227 offset:25600\n"
MF"v[140:143],a[124:127],v[68:71],v[140:143]\n"
WT"vmcnt(7)\n"
"v_cmp_eq_u64 vcc,s[28:29],v[238:239]\n"
"s_cmp_eq_u64 vcc,exec\n"
"s_cbranch_scc0 L1019_%=\n"
"L1020_%=:\n"
MF"v[172:175],a[124:127],v[72:75],v[172:175]\n"
EX"v42,v42\n"
MF"v[204:207],a[124:127],v[76:79],v[204:207]\n"
RD"a[80:83],v227 offset:18432\n"
MF"a[136:139],a[128:131],v[68:71],a[136:139]\n"
BL"v225,s[8:11],s43 offen offset:3072 sc1 lds\n"
PK"v68,v40,v41\n"
MF"a[140:143],a[128:131],v[72:75],a[140:143]\n"
SA"m0,s40,16384\n"
EX"v43,v43\n"
MF"a[144:147],a[128:131],v[76:79],a[144:147]\n"
EX"v36,v36\n"
MF"v[144:147],a[64:67],v[0:3],v[144:147]\n"
WT"lgkmcnt(1)\n"
RD"a[84:87],v227 offset:26624\n"
RD"a[88:91],v227 offset:19456\n"
PK"v69,v42,v43\n"
MF"v[144:147],a[68:71],v[16:19],v[144:147]\n"
EX"v37,v37\n"
MF"v[176:179],a[68:71],v[24:27],v[176:179]\n"
SA"s51,s59,16384\n"
RD"a[92:95],v227 offset:27648\n"
RD"a[96:99],v227 offset:20480\n"
MF"v[176:179],a[64:67],v[8:11],v[176:179]\n"
SA"s40,s40,32768\n"
EX"v38,v38\n"
MF"v[148:151],a[72:75],v[0:3],v[148:151]\n"
"s_cmp_eq_u32 s40,s46\n"
BL"v225,s[8:11],s44 offen offset:0 sc1 lds\n"
PK"v66,v36,v37\n"
MF"v[148:151],a[76:79],v[16:19],v[148:151]\n"
"s_cselect_b32 s40,s47,s40\n"
EX"v39,v39\n"
"v_accvgpr_read_b32 v252,a136\n"
MF"v[180:183],a[76:79],v[24:27],v[180:183]\n"
EX"v44,v44\n"
"v_subrev_u32 v253,0x30800000,v252\n"
MF"v[180:183],a[72:75],v[8:11],v[180:183]\n"
WT"lgkmcnt(2)\n"
"v_cmp_le_u32 vcc,0x1f000000,v253\n"
"s_cmp_lg_u64 vcc,0\n"
"s_cbranch_scc1 L1023_%=\n"
"L1022_%=:\n"
MF"v[152:155],a[80:83],v[0:3],v[152:155]\n"
"v_rcp_f32 v252,v252\n"
PK"v67,v38,v39\n"
MF"v[152:155],a[84:87],v[16:19],v[152:155]\n"
RD"a[100:103],v227 offset:28672\n"
RD"a[104:107],v227 offset:21504\n"
EX"v45,v45\n"
MF"v[184:187],a[84:87],v[24:27],v[184:187]\n"
RD"a[108:111],v227 offset:29696\n"
RD"a[112:115],v227 offset:22528\n"
MU"v112,v252,v112\n"
MF"v[184:187],a[80:83],v[8:11],v[184:187]\n"
EX"v46,v46\n"
MU"v113,v252,v113\n"
MF"v[156:159],a[88:91],v[0:3],v[156:159]\n"
WT"lgkmcnt(4)\n"
BL"v225,s[8:11],s44 offen offset:1024 sc1 lds\n"
MU"v114,v252,v114\n"
MF"v[156:159],a[92:95],v[16:19],v[156:159]\n"
EX"v47,v47\n"
MU"v115,v252,v115\n"
MF"v[188:191],a[92:95],v[24:27],v[188:191]\n"
EX"v48,v48\n"
MU"v116,v252,v116\n"
MF"v[188:191],a[88:91],v[8:11],v[188:191]\n"
EX"v49,v49\n"
PK"v70,v44,v45\n"
MF"v[160:163],a[96:99],v[0:3],v[160:163]\n"
WT"lgkmcnt(2)\n"
RD"a[116:119],v227 offset:30720\n"
RD"a[120:123],v227 offset:23552\n"
MU"v117,v252,v117\n"
MF"v[160:163],a[100:103],v[16:19],v[160:163]\n"
EX"v50,v50\n"
MU"v118,v252,v118\n"
MF"v[192:195],a[100:103],v[24:27],v[192:195]\n"
RD"a[124:127],v227 offset:31744\n"
MU"v119,v252,v119\n"
PK"v112,v112,v113\n"
MF"v[192:195],a[96:99],v[8:11],v[192:195]\n"
BL"v225,s[8:11],s44 offen offset:2048 sc1 lds\n"
PK"v113,v114,v115\n"
MF"v[164:167],a[104:107],v[0:3],v[164:167]\n"
WT"lgkmcnt(3)\n"
EX"v51,v51\n"
PK"v114,v116,v117\n"
MF"v[164:167],a[108:111],v[16:19],v[164:167]\n"
EX"v52,v52\n"
PK"v115,v118,v119\n"
MF"v[196:199],a[108:111],v[24:27],v[196:199]\n"
BS"v[112:115],v224,s[8:11],s51 offen offset:0 sc1\n"
PK"v71,v46,v47\n"
MF"v[196:199],a[104:107],v[8:11],v[196:199]\n"
"s_barrier\n"
MF"v[168:171],a[112:115],v[0:3],v[168:171]\n"
WT"lgkmcnt(1)\n"
EX"v53,v53\n"
MU"v120,v252,v120\n"
MF"v[168:171],a[116:119],v[16:19],v[168:171]\n"
RD"a[64:67],v226 offset:0\n"
RD"a[68:71],v226 offset:1024\n"
MU"v121,v252,v121\n"
MF"v[200:203],a[116:119],v[24:27],v[200:203]\n"
EX"v54,v54\n"
MU"v122,v252,v122\n"
MF"v[200:203],a[112:115],v[8:11],v[200:203]\n"
BL"v225,s[8:11],s44 offen offset:3072 sc1 lds\n"
MU"v123,v252,v123\n"
MF"v[172:175],a[120:123],v[0:3],v[172:175]\n"
WT"lgkmcnt(2)\n"
EX"v55,v55\n"
MU"v124,v252,v124\n"
MF"v[172:175],a[124:127],v[16:19],v[172:175]\n"
RD"a[72:75],v226 offset:2048\n"
PK"v72,v48,v49\n"
MU"v125,v252,v125\n"
MF"v[204:207],a[124:127],v[24:27],v[204:207]\n"
RD"a[76:79],v226 offset:3072\n"
MU"v126,v252,v126\n"
MU"v127,v252,v127\n"
MF"v[204:207],a[120:123],v[8:11],v[204:207]\n"
PK"v120,v120,v121\n"
PK"v121,v122,v123\n"
PK"v122,v124,v125\n"
MF"a[140:143],a[128:131],v[0:3],a[140:143]\n"
MU"v128,v252,v128\n"
MU"v129,v252,v129\n"
MU"v136,v252,v136\n"
MF"a[140:143],a[128:131],v[16:19],a[140:143]\n"
MU"v137,v252,v137\n"
MU"v130,v252,v130\n"
MU"v131,v252,v131\n"
MF"a[144:147],a[128:131],v[24:27],a[144:147]\n"
WT"lgkmcnt(2)\n"
PK"v128,v128,v129\n"
MU"v138,v252,v138\n"
MU"v139,v252,v139\n"
MF"a[144:147],a[128:131],v[8:11],a[144:147]\n"
PK"v136,v136,v137\n"
MU"v132,v252,v132\n"
MU"v133,v252,v133\n"
PK"v73,v50,v51\n"
PK"v74,v52,v53\n"
PK"v75,v54,v55\n"
PK"v123,v126,v127\n"
BS"v[120:123],v224,s[8:11],s51 offen offset:1024 sc1\n"
MU"v134,v252,v134\n"
MU"v135,v252,v135\n"
PK"v129,v130,v131\n"
PK"v130,v132,v133\n"
PK"v131,v134,v135\n"
BS"v[128:131],v224,s[8:11],s51 offen offset:2048 sc1\n"
MU"v140,v252,v140\n"
MU"v141,v252,v141\n"
MU"v142,v252,v142\n"
MU"v143,v252,v143\n"
PK"v137,v138,v139\n"
PK"v138,v140,v141\n"
PK"v139,v142,v143\n"
BS"v[136:139],v224,s[8:11],s51 offen offset:3072 sc1\n"
RD"a[80:83],v226 offset:4096\n"
RD"a[84:87],v226 offset:5120\n"
RD"a[88:91],v226 offset:6144\n"
".p2align 6\n"
"LP2_%=:\n"
MF"v[0:3],a[64:67],a[32:35],0\n"
EX"v56,v56\n"
MF"v[0:3],a[68:71],a[36:39],v[0:3]\n"
RD"a[92:95],v226 offset:7168\n"
"v_readlane_b32 s43,v228,s42\n"
MF"v[8:11],a[68:71],a[52:55],0\n"
EX"v57,v57\n"
MF"v[8:11],a[64:67],a[48:51],v[8:11]\n"
WT"lgkmcnt(5)\n"
RD"a[96:99],v226 offset:8192\n"
RD"a[100:103],v226 offset:9216\n"
MF"v[0:3],a[72:75],a[40:43],v[0:3]\n"
WT"lgkmcnt(3)\n"
EX"v58,v58\n"
MF"v[0:3],a[76:79],a[44:47],v[0:3]\n"
"s_mov_b32 m0,s40\n"
RD"a[104:107],v226 offset:10240\n"
"v_readlane_b32 s44,v229,s42\n"
MF"v[8:11],a[76:79],a[60:63],v[8:11]\n"
EX"v59,v59\n"
MF"v[8:11],a[72:75],a[56:59],v[8:11]\n"
SA"s42,s42,1\n"
BL"v225,s[8:11],s43 offen offset:0 sc1 lds\n"
MF"v[4:7],a[80:83],a[32:35],0\n"
EX"v60,v60\n"
MF"v[4:7],a[84:87],a[36:39],v[4:7]\n"
RD"a[108:111],v226 offset:11264\n"
PK"v76,v56,v57\n"
MF"v[12:15],a[84:87],a[52:55],0\n"
EX"v61,v61\n"
MF"v[12:15],a[80:83],a[48:51],v[12:15]\n"
RD"a[112:115],v226 offset:12288\n"
RD"a[116:119],v226 offset:13312\n"
MF"v[4:7],a[88:91],a[40:43],v[4:7]\n"
WT"lgkmcnt(3)\n"
EX"v62,v62\n"
MF"v[4:7],a[92:95],a[44:47],v[4:7]\n"
RD"a[120:123],v226 offset:14336\n"
PK"v77,v58,v59\n"
MF"v[12:15],a[92:95],a[60:63],v[12:15]\n"
EX"v63,v63\n"
MF"v[12:15],a[88:91],a[56:59],v[12:15]\n"
BL"v225,s[8:11],s43 offen offset:1024 sc1 lds\n"
MF"v[16:19],a[96:99],a[32:35],0\n"
EX"v0,v0\n"
MF"v[16:19],a[100:103],a[36:39],v[16:19]\n"
RD"a[124:127],v226 offset:15360\n"
PK"v78,v60,v61\n"
MF"v[24:27],a[100:103],a[52:55],0\n"
EX"v1,v1\n"
MF"v[24:27],a[96:99],a[48:51],v[24:27]\n"
RD"a[64:67],v226 offset:16384\n"
RD"a[68:71],v226 offset:17408\n"
MF"v[16:19],a[104:107],a[40:43],v[16:19]\n"
WT"lgkmcnt(3)\n"
EX"v2,v2\n"
MF"v[16:19],a[108:111],a[44:47],v[16:19]\n"
SA"s41,s41,32768\n"
RD"a[72:75],v226 offset:18432\n"
PK"v79,v62,v63\n"
MF"v[24:27],a[108:111],a[60:63],v[24:27]\n"
"s_cmp_eq_u32 s41,163840\n"
EX"v3,v3\n"
MF"v[24:27],a[104:107],a[56:59],v[24:27]\n"
"s_cselect_b32 s41,0,s41\n"
BL"v225,s[8:11],s43 offen offset:2048 sc1 lds\n"
MF"v[20:23],a[112:115],a[32:35],0\n"
EX"v8,v8\n"
MF"v[20:23],a[116:119],a[36:39],v[20:23]\n"
RD"a[76:79],v226 offset:19456\n"
"v_add_u32 v227,s41,v224\n"
MF"v[28:31],a[116:119],a[52:55],0\n"
EX"v9,v9\n"
MF"v[28:31],a[112:115],a[48:51],v[28:31]\n"
RD"a[80:83],v226 offset:20480\n"
RD"a[84:87],v226 offset:21504\n"
MF"v[20:23],a[120:123],a[40:43],v[20:23]\n"
WT"lgkmcnt(3)\n"
EX"v10,v10\n"
MF"v[20:23],a[124:127],a[44:47],v[20:23]\n"
RD"a[88:91],v226 offset:22528\n"
PK"v0,v0,v1\n"
MF"v[28:31],a[124:127],a[60:63],v[28:31]\n"
EX"v11,v11\n"
MF"v[28:31],a[120:123],a[56:59],v[28:31]\n"
BL"v225,s[8:11],s43 offen offset:3072 sc1 lds\n"
MF"v[32:35],a[64:67],a[32:35],0\n"
EX"v4,v4\n"
MF"v[32:35],a[68:71],a[36:39],v[32:35]\n"
RD"a[92:95],v226 offset:23552\n"
PK"v1,v2,v3\n"
MF"v[40:43],a[68:71],a[52:55],0\n"
EX"v5,v5\n"
MF"v[40:43],a[64:67],a[48:51],v[40:43]\n"
SA"m0,s40,16384\n"
RD"a[96:99],v226 offset:24576\n"
RD"a[100:103],v226 offset:25600\n"
MF"v[32:35],a[72:75],a[40:43],v[32:35]\n"
WT"lgkmcnt(3)\n"
EX"v6,v6\n"
MF"v[32:35],a[76:79],a[44:47],v[32:35]\n"
RD"a[104:107],v226 offset:26624\n"
PK"v8,v8,v9\n"
MF"v[40:43],a[76:79],a[60:63],v[40:43]\n"
EX"v7,v7\n"
MF"v[40:43],a[72:75],a[56:59],v[40:43]\n"
BL"v225,s[8:11],s44 offen offset:0 sc1 lds\n"
MF"v[36:39],a[80:83],a[32:35],0\n"
SA"s40,s40,32768\n"
EX"v12,v12\n"
MF"v[36:39],a[84:87],a[36:39],v[36:39]\n"
"s_cmp_eq_u32 s40,s46\n"
RD"a[108:111],v226 offset:27648\n"
PK"v9,v10,v11\n"
MF"v[44:47],a[84:87],a[52:55],0\n"
"s_cselect_b32 s40,s47,s40\n"
EX"v13,v13\n"
MF"v[44:47],a[80:83],a[48:51],v[44:47]\n"
RD"a[112:115],v226 offset:28672\n"
RD"a[116:119],v226 offset:29696\n"
MF"v[36:39],a[88:91],a[40:43],v[36:39]\n"
WT"lgkmcnt(3)\n"
EX"v14,v14\n"
MF"v[36:39],a[92:95],a[44:47],v[36:39]\n"
RD"a[120:123],v226 offset:30720\n"
PK"v2,v4,v5\n"
MF"v[44:47],a[92:95],a[60:63],v[44:47]\n"
EX"v15,v15\n"
MF"v[44:47],a[88:91],a[56:59],v[44:47]\n"
BL"v225,s[8:11],s44 offen offset:1024 sc1 lds\n"
MF"v[48:51],a[96:99],a[32:35],0\n"
EX"v16,v16\n"
MF"v[48:51],a[100:103],a[36:39],v[48:51]\n"
WT"vmcnt(14)\n"
RD"a[124:127],v226 offset:31744\n"
PK"v3,v6,v7\n"
MF"v[56:59],a[100:103],a[52:55],0\n"
"s_barrier\n"
MF"v[56:59],a[96:99],a[48:51],v[56:59]\n"
RD"a[64:67],v227 offset:0\n"
PK"v10,v12,v13\n"
MF"v[48:51],a[104:107],a[40:43],v[48:51]\n"
WT"lgkmcnt(2)\n"
RD"a[68:71],v227 offset:8192\n"
PK"v11,v14,v15\n"
MF"v[48:51],a[108:111],a[44:47],v[48:51]\n"
EX"v17,v17\n"
MF"v[56:59],a[108:111],a[60:63],v[56:59]\n"
RD"a[72:75],v227 offset:1024\n"
PK"v16,v16,v17\n"
MF"v[56:59],a[104:107],a[56:59],v[56:59]\n"
EX"v18,v18\n"
MF"v[52:55],a[112:115],a[32:35],0\n"
BL"v225,s[8:11],s44 offen offset:2048 sc1 lds\n"
MF"v[52:55],a[116:119],a[36:39],v[52:55]\n"
EX"v19,v19\n"
MF"v[60:63],a[116:119],a[52:55],0\n"
RD"a[76:79],v227 offset:9216\n"
PK"v17,v18,v19\n"
MF"v[60:63],a[112:115],a[48:51],v[60:63]\n"
EX"v24,v24\n"
MF"v[52:55],a[120:123],a[40:43],v[52:55]\n"
WT"lgkmcnt(2)\n"
RD"a[80:83],v227 offset:2048\n"
RD"a[84:87],v227 offset:10240\n"
MF"v[52:55],a[124:127],a[44:47],v[52:55]\n"
EX"v25,v25\n"
MF"v[60:63],a[124:127],a[60:63],v[60:63]\n"
RD"a[88:91],v227 offset:3072\n"
PK"v24,v24,v25\n"
MF"v[60:63],a[120:123],a[56:59],v[60:63]\n"
SA"s41,s41,32768\n"
EX"v26,v26\n"
MF"v[144:147],a[64:67],v[64:67],v[144:147]\n"
"s_cmp_eq_u32 s41,163840\n"
BL"v225,s[8:11],s44 offen offset:3072 sc1 lds\n"
MF"v[144:147],a[68:71],v[72:75],v[144:147]\n"
"s_cselect_b32 s41,0,s41\n"
EX"v27,v27\n"
MF"v[176:179],a[68:71],v[76:79],v[176:179]\n"
RD"a[92:95],v227 offset:11264\n"
"v_add_u32 v226,s41,v224\n"
MF"v[176:179],a[64:67],v[68:71],v[176:179]\n"
WT"lgkmcnt(2)\n"
EX"v20,v20\n"
MF"v[148:151],a[72:75],v[64:67],v[148:151]\n"
"s_mov_b32 m0,s40\n"
RD"a[96:99],v227 offset:4096\n"
RD"a[100:103],v227 offset:12288\n"
MF"v[148:151],a[76:79],v[72:75],v[148:151]\n"
EX"v21,v21\n"
MF"v[180:183],a[76:79],v[76:79],v[180:183]\n"
RD"a[104:107],v227 offset:5120\n"
"v_readlane_b32 s43,v228,s42\n"
MF"v[180:183],a[72:75],v[68:71],v[180:183]\n"
EX"v22,v22\n"
MF"v[152:155],a[80:83],v[64:67],v[152:155]\n"
EX"v23,v23\n"
MF"v[152:155],a[84:87],v[72:75],v[152:155]\n"
BL"v225,s[8:11],s43 offen offset:0 sc1 lds\n"
MF"v[184:187],a[84:87],v[76:79],v[184:187]\n"
RD"a[108:111],v227 offset:13312\n"
"v_readlane_b32 s44,v229,s42\n"
MF"v[184:187],a[80:83],v[68:71],v[184:187]\n"
WT"lgkmcnt(2)\n"
EX"v28,v28\n"
MF"v[156:159],a[88:91],v[64:67],v[156:159]\n"
SA"s42,s42,1\n"
RD"a[112:115],v227 offset:6144\n"
RD"a[116:119],v227 offset:14336\n"
MF"v[156:159],a[92:95],v[72:75],v[156:159]\n"
EX"v29,v29\n"
MF"v[188:191],a[92:95],v[76:79],v[188:191]\n"
RD"a[120:123],v227 offset:7168\n"
PK"v25,v26,v27\n"
MF"v[188:191],a[88:91],v[68:71],v[188:191]\n"
EX"v30,v30\n"
MF"v[160:163],a[96:99],v[64:67],v[160:163]\n"
BL"v225,s[8:11],s43 offen offset:1024 sc1 lds\n"
MF"v[160:163],a[100:103],v[72:75],v[160:163]\n"
EX"v31,v31\n"
MF"v[192:195],a[100:103],v[76:79],v[192:195]\n"
RD"a[124:127],v227 offset:15360\n"
PK"v18,v20,v21\n"
MF"v[192:195],a[96:99],v[68:71],v[192:195]\n"
WT"lgkmcnt(2)\n"
EX"v32,v32\n"
MF"v[164:167],a[104:107],v[64:67],v[164:167]\n"
EX"v33,v33\n"
MF"v[164:167],a[108:111],v[72:75],v[164:167]\n"
RD"a[64:67],v227 offset:16384\n"
PK"v19,v22,v23\n"
MF"v[196:199],a[108:111],v[76:79],v[196:199]\n"
EX"v34,v34\n"
MF"v[196:199],a[104:107],v[68:71],v[196:199]\n"
BL"v225,s[8:11],s43 offen offset:2048 sc1 lds\n"
MF"v[168:171],a[112:115],v[64:67],v[168:171]\n"
EX"v35,v35\n"
MF"v[168:171],a[116:119],v[72:75],v[168:171]\n"
RD"a[68:71],v227 offset:24576\n"
PK"v26,v28,v29\n"
MF"v[200:203],a[116:119],v[76:79],v[200:203]\n"
EX"v40,v40\n"
MF"v[200:203],a[112:115],v[68:71],v[200:203]\n"
WT"lgkmcnt(2)\n"
RD"a[72:75],v227 offset:17408\n"
RD"a[76:79],v227 offset:25600\n"
MF"v[172:175],a[120:123],v[64:67],v[172:175]\n"
EX"v41,v41\n"
MF"v[172:175],a[124:127],v[72:75],v[172:175]\n"
RD"a[80:83],v227 offset:18432\n"
PK"v27,v30,v31\n"
MF"v[204:207],a[124:127],v[76:79],v[204:207]\n"
EX"v42,v42\n"
MF"v[204:207],a[120:123],v[68:71],v[204:207]\n"
BL"v225,s[8:11],s43 offen offset:3072 sc1 lds\n"
MF"a[140:143],a[128:131],v[64:67],a[140:143]\n"
EX"v43,v43\n"
MF"a[140:143],a[128:131],v[72:75],a[140:143]\n"
RD"a[84:87],v227 offset:26624\n"
PK"v64,v32,v33\n"
MF"a[144:147],a[128:131],v[76:79],a[144:147]\n"
EX"v36,v36\n"
MF"a[144:147],a[128:131],v[68:71],a[144:147]\n"
WT"lgkmcnt(2)\n"
RD"a[88:91],v227 offset:19456\n"
RD"a[92:95],v227 offset:27648\n"
MF"v[144:147],a[64:67],v[0:3],v[144:147]\n"
SA"m0,s40,16384\n"
EX"v37,v37\n"
MF"v[144:147],a[68:71],v[16:19],v[144:147]\n"
RD"a[96:99],v227 offset:20480\n"
PK"v65,v34,v35\n"
MF"v[176:179],a[68:71],v[24:27],v[176:179]\n"
EX"v38,v38\n"
MF"v[176:179],a[64:67],v[8:11],v[176:179]\n"
BL"v225,s[8:11],s44 offen offset:0 sc1 lds\n"
MF"v[148:151],a[72:75],v[0:3],v[148:151]\n"
SA"s40,s40,32768\n"
EX"v39,v39\n"
MF"v[148:151],a[76:79],v[16:19],v[148:151]\n"
"s_cmp_eq_u32 s40,s46\n"
RD"a[100:103],v227 offset:28672\n"
PK"v68,v40,v41\n"
MF"v[180:183],a[76:79],v[24:27],v[180:183]\n"
"s_cselect_b32 s40,s47,s40\n"
EX"v44,v44\n"
MF"v[180:183],a[72:75],v[8:11],v[180:183]\n"
WT"lgkmcnt(2)\n"
RD"a[104:107],v227 offset:21504\n"
RD"a[108:111],v227 offset:29696\n"
MF"v[152:155],a[80:83],v[0:3],v[152:155]\n"
EX"v45,v45\n"
MF"v[152:155],a[84:87],v[16:19],v[152:155]\n"
RD"a[112:115],v227 offset:22528\n"
PK"v69,v42,v43\n"
MF"v[184:187],a[84:87],v[24:27],v[184:187]\n"
EX"v46,v46\n"
MF"v[184:187],a[80:83],v[8:11],v[184:187]\n"
BL"v225,s[8:11],s44 offen offset:1024 sc1 lds\n"
MF"v[156:159],a[88:91],v[0:3],v[156:159]\n"
EX"v47,v47\n"
MF"v[156:159],a[92:95],v[16:19],v[156:159]\n"
RD"a[116:119],v227 offset:30720\n"
PK"v66,v36,v37\n"
MF"v[188:191],a[92:95],v[24:27],v[188:191]\n"
EX"v48,v48\n"
MF"v[188:191],a[88:91],v[8:11],v[188:191]\n"
WT"lgkmcnt(2)\n"
RD"a[120:123],v227 offset:23552\n"
RD"a[124:127],v227 offset:31744\n"
MF"v[160:163],a[96:99],v[0:3],v[160:163]\n"
EX"v49,v49\n"
MF"v[160:163],a[100:103],v[16:19],v[160:163]\n"
"buffer_load_dwordx2 v[242:243],v236,s[8:11],0 offen sc1\n"
MF"v[192:195],a[100:103],v[24:27],v[192:195]\n"
EX"v50,v50\n"
MF"v[192:195],a[96:99],v[8:11],v[192:195]\n"
WT"vmcnt(15)\n"
BL"v225,s[8:11],s44 offen offset:2048 sc1 lds\n"
MF"v[164:167],a[104:107],v[0:3],v[164:167]\n"
"s_barrier\n"
MF"v[164:167],a[108:111],v[16:19],v[164:167]\n"
RD"a[64:67],v226 offset:0\n"
PK"v67,v38,v39\n"
MF"v[196:199],a[108:111],v[24:27],v[196:199]\n"
EX"v51,v51\n"
MF"v[196:199],a[104:107],v[8:11],v[196:199]\n"
WT"lgkmcnt(1)\n"
RD"a[68:71],v226 offset:1024\n"
PK"v70,v44,v45\n"
MF"v[168:171],a[112:115],v[0:3],v[168:171]\n"
EX"v52,v52\n"
MF"v[168:171],a[116:119],v[16:19],v[168:171]\n"
RD"a[72:75],v226 offset:2048\n"
PK"v71,v46,v47\n"
MF"v[200:203],a[116:119],v[24:27],v[200:203]\n"
EX"v53,v53\n"
MF"v[200:203],a[112:115],v[8:11],v[200:203]\n"
BL"v225,s[8:11],s44 offen offset:3072 sc1 lds\n"
MF"v[172:175],a[120:123],v[0:3],v[172:175]\n"
EX"v54,v54\n"
MF"v[172:175],a[124:127],v[16:19],v[172:175]\n"
"buffer_store_dwordx2 v[232:233],v230,s[8:11],s57 offen sc1\n"
MF"v[204:207],a[124:127],v[24:27],v[204:207]\n"
"s_mov_b32 s57,2130706432\n"
RD"a[76:79],v226 offset:3072\n"
PK"v72,v48,v49\n"
MF"v[204:207],a[120:123],v[8:11],v[204:207]\n"
EX"v55,v55\n"
MF"a[140:143],a[128:131],v[0:3],a[140:143]\n"
RD"a[80:83],v226 offset:4096\n"
RD"a[84:87],v226 offset:5120\n"
MF"a[140:143],a[128:131],v[16:19],a[140:143]\n"
RD"a[88:91],v226 offset:6144\n"
PK"v73,v50,v51\n"
MF"a[144:147],a[128:131],v[24:27],a[144:147]\n"
WT"lgkmcnt(5)\n"
"s_sub_u32 s56,s56,1\n"
PK"v74,v52,v53\n"
PK"v75,v54,v55\n"
MF"a[144:147],a[128:131],v[8:11],a[144:147]\n"
"s_cbranch_scc0 LP2_%=\n"
"s_cmp_lt_i32 s22,0\n"
"s_cbranch_scc1 LE2_%=\n"
MF"v[0:3],a[64:67],a[32:35],0\n"
SA"s48,s59,32768\n"
WT"vmcnt(3)\n"
"v_cmp_eq_u64 vcc,s[28:29],v[242:243]\n"
"s_or_b32 vcc_hi,vcc_hi,-1\n"
"s_cmp_eq_u64 vcc,exec\n"
"s_cbranch_scc0 L1028_%=\n"
"L1029_%=:\n"
MF"v[0:3],a[68:71],a[36:39],v[0:3]\n"
"s_and_b32 s48,s48,0x18000\n"
EX"v56,v56\n"
MF"v[8:11],a[68:71],a[52:55],0\n"
"s_andn2_b32 s49,s59,0x18000\n"
RD"a[92:95],v226 offset:7168\n"
"v_readlane_b32 s43,v228,s42\n"
MF"v[8:11],a[64:67],a[48:51],v[8:11]\n"
WT"lgkmcnt(5)\n"
EX"v57,v57\n"
MF"v[0:3],a[72:75],a[40:43],v[0:3]\n"
WT"lgkmcnt(1)\n"
RD"a[96:99],v226 offset:8192\n"
RD"a[100:103],v226 offset:9216\n"
MF"v[0:3],a[76:79],a[44:47],v[0:3]\n"
"s_or_b32 s52,s48,s49\n"
EX"v58,v58\n"
MF"v[8:11],a[76:79],a[60:63],v[8:11]\n"
SA"s53,s52,16384\n"
BL"a[0:3],v224,s[8:11],s52 offen offset:0 sc1\n"
MF"v[8:11],a[72:75],a[56:59],v[8:11]\n"
"s_mov_b32 m0,s40\n"
RD"a[104:107],v226 offset:10240\n"
"v_readlane_b32 s44,v229,s42\n"
MF"v[4:7],a[80:83],a[32:35],0\n"
SA"s42,s42,1\n"
EX"v59,v59\n"
MF"v[4:7],a[84:87],a[36:39],v[4:7]\n"
BL"v225,s[8:11],s43 offen offset:0 sc1 lds\n"
MF"v[12:15],a[84:87],a[52:55],0\n"
EX"v60,v60\n"
MF"v[12:15],a[80:83],a[48:51],v[12:15]\n"
RD"a[108:111],v226 offset:11264\n"
RD"a[112:115],v226 offset:12288\n"
PK"v76,v56,v57\n"
MF"v[4:7],a[88:91],a[40:43],v[4:7]\n"
WT"lgkmcnt(3)\n"
BL"a[4:7],v224,s[8:11],s53 offen offset:0 sc1\n"
EX"v61,v61\n"
MF"v[4:7],a[92:95],a[44:47],v[4:7]\n"
RD"a[116:119],v226 offset:13312\n"
RD"a[120:123],v226 offset:14336\n"
MF"v[12:15],a[92:95],a[60:63],v[12:15]\n"
EX"v62,v62\n"
MF"v[12:15],a[88:91],a[56:59],v[12:15]\n"
EX"v63,v63\n"
PK"v77,v58,v59\n"
MF"v[16:19],a[96:99],a[32:35],0\n"
BL"a[8:11],v224,s[8:11],s52 offen offset:1024 sc1\n"
MF"v[16:19],a[100:103],a[36:39],v[16:19]\n"
BL"v225,s[8:11],s43 offen offset:1024 sc1 lds\n"
MF"v[24:27],a[100:103],a[52:55],0\n"
EX"v0,v0\n"
MF"v[24:27],a[96:99],a[48:51],v[24:27]\n"
WT"lgkmcnt(2)\n"
RD"a[124:127],v226 offset:15360\n"
RD"a[64:67],v226 offset:16384\n"
PK"v78,v60,v61\n"
MF"v[16:19],a[104:107],a[40:43],v[16:19]\n"
SA"s41,s41,32768\n"
EX"v1,v1\n"
MF"v[16:19],a[108:111],a[44:47],v[16:19]\n"
"s_cmp_eq_u32 s41,163840\n"
RD"a[68:71],v226 offset:17408\n"
RD"a[72:75],v226 offset:18432\n"
MF"v[24:27],a[108:111],a[60:63],v[24:27]\n"
"s_cselect_b32 s41,0,s41\n"
BL"a[12:15],v224,s[8:11],s53 offen offset:1024 sc1\n"
EX"v2,v2\n"
MF"v[24:27],a[104:107],a[56:59],v[24:27]\n"
EX"v3,v3\n"
PK"v79,v62,v63\n"
MF"v[20:23],a[112:115],a[32:35],0\n"
WT"lgkmcnt(4)\n"
BL"v225,s[8:11],s43 offen offset:2048 sc1 lds\n"
MF"v[20:23],a[116:119],a[36:39],v[20:23]\n"
EX"v8,v8\n"
MF"v[28:31],a[116:119],a[52:55],0\n"
RD"a[76:79],v226 offset:19456\n"
"v_add_u32 v227,s41,v224\n"
MF"v[28:31],a[112:115],a[48:51],v[28:31]\n"
BL"a[16:19],v224,s[8:11],s52 offen offset:2048 sc1\n"
PK"v0,v0,v1\n"
MF"v[20:23],a[120:123],a[40:43],v[20:23]\n"
WT"lgkmcnt(1)\n"
EX"v9,v9\n"
MF"v[20:23],a[124:127],a[44:47],v[20:23]\n"
RD"a[80:83],v226 offset:20480\n"
RD"a[84:87],v226 offset:21504\n"
MF"v[28:31],a[124:127],a[60:63],v[28:31]\n"
EX"v10,v10\n"
MF"v[28:31],a[120:123],a[56:59],v[28:31]\n"
RD"a[88:91],v226 offset:22528\n"
PK"v1,v2,v3\n"
MF"v[32:35],a[64:67],a[32:35],0\n"
EX"v11,v11\n"
MF"v[32:35],a[68:71],a[36:39],v[32:35]\n"
BL"a[20:23],v224,s[8:11],s53 offen offset:2048 sc1\n"
BL"v225,s[8:11],s43 offen offset:3072 sc1 lds\n"
MF"v[40:43],a[68:71],a[52:55],0\n"
EX"v4,v4\n"
MF"v[40:43],a[64:67],a[48:51],v[40:43]\n"
SA"m0,s40,16384\n"
RD"a[92:95],v226 offset:23552\n"
RD"a[96:99],v226 offset:24576\n"
PK"v8,v8,v9\n"
MF"v[32:35],a[72:75],a[40:43],v[32:35]\n"
WT"lgkmcnt(3)\n"
EX"v5,v5\n"
MF"v[32:35],a[76:79],a[44:47],v[32:35]\n"
RD"a[100:103],v226 offset:25600\n"
RD"a[104:107],v226 offset:26624\n"
MF"v[40:43],a[76:79],a[60:63],v[40:43]\n"
BL"a[24:27],v224,s[8:11],s52 offen offset:3072 sc1\n"
EX"v6,v6\n"
MF"v[40:43],a[72:75],a[56:59],v[40:43]\n"
EX"v7,v7\n"
PK"v9,v10,v11\n"
MF"v[36:39],a[80:83],a[32:35],0\n"
SA"s40,s40,32768\n"
BL"v225,s[8:11],s44 offen offset:0 sc1 lds\n"
MF"v[36:39],a[84:87],a[36:39],v[36:39]\n"
"s_cmp_eq_u32 s40,s46\n"
EX"v12,v12\n"
MF"v[44:47],a[84:87],a[52:55],0\n"
"s_cselect_b32 s40,s47,s40\n"
BL"a[28:31],v224,s[8:11],s53 offen offset:3072 sc1\n"
MF"v[44:47],a[80:83],a[48:51],v[44:47]\n"
WT"lgkmcnt(2)\n"
RD"a[108:111],v226 offset:27648\n"
PK"v2,v4,v5\n"
MF"v[36:39],a[88:91],a[40:43],v[36:39]\n"
SA"s48,s59,65536\n"
EX"v13,v13\n"
MF"v[36:39],a[92:95],a[44:47],v[36:39]\n"
"s_and_b32 s48,s48,0x18000\n"
RD"a[112:115],v226 offset:28672\n"
RD"a[116:119],v226 offset:29696\n"
MF"v[44:47],a[92:95],a[60:63],v[44:47]\n"
"s_andn2_b32 s49,s59,0x18000\n"
EX"v14,v14\n"
MF"v[44:47],a[88:91],a[56:59],v[44:47]\n"
"s_or_b32 s52,s48,s49\n"
RD"a[120:123],v226 offset:30720\n"
PK"v3,v6,v7\n"
MF"v[48:51],a[96:99],a[32:35],0\n"
WT"lgkmcnt(4)\n"
EX"v15,v15\n"
MF"v[48:51],a[100:103],a[36:39],v[48:51]\n"
BL"a[180:183],v224,s[8:11],s52 offen offset:0 sc1\n"
BL"v225,s[8:11],s44 offen offset:1024 sc1 lds\n"
MF"v[56:59],a[100:103],a[52:55],0\n"
SA"s53,s52,16384\n"
EX"v16,v16\n"
MF"v[56:59],a[96:99],a[48:51],v[56:59]\n"
RD"a[124:127],v226 offset:31744\n"
PK"v10,v12,v13\n"
MF"v[48:51],a[104:107],a[40:43],v[48:51]\n"
WT"lgkmcnt(2)\n"
"s_barrier\n"
MF"v[48:51],a[108:111],a[44:47],v[48:51]\n"
RD"a[64:67],v227 offset:0\n"
PK"v11,v14,v15\n"
MF"v[56:59],a[108:111],a[60:63],v[56:59]\n"
RD"a[68:71],v227 offset:8192\n"
BL"a[184:187],v224,s[8:11],s53 offen offset:0 sc1\n"
MF"v[56:59],a[104:107],a[56:59],v[56:59]\n"
EX"v17,v17\n"
MF"v[52:55],a[112:115],a[32:35],0\n"
RD"a[72:75],v227 offset:1024\n"
PK"v16,v16,v17\n"
EX"v18,v18\n"
MF"v[52:55],a[116:119],a[36:39],v[52:55]\n"
BL"v225,s[8:11],s44 offen offset:2048 sc1 lds\n"
MF"v[60:63],a[116:119],a[52:55],0\n"
EX"v19,v19\n"
MF"v[60:63],a[112:115],a[48:51],v[60:63]\n"
WT"lgkmcnt(3)\n"
BL"a[188:191],v224,s[8:11],s52 offen offset:1024 sc1\n"
RD"a[76:79],v227 offset:9216\n"
MF"v[52:55],a[120:123],a[40:43],v[52:55]\n"
EX"v24,v24\n"
MF"v[52:55],a[124:127],a[44:47],v[52:55]\n"
RD"a[80:83],v227 offset:2048\n"
RD"a[84:87],v227 offset:10240\n"
MF"v[60:63],a[124:127],a[60:63],v[60:63]\n"
EX"v25,v25\n"
MF"v[60:63],a[120:123],a[56:59],v[60:63]\n"
WT"lgkmcnt(3)\n"
RD"a[88:91],v227 offset:3072\n"
"v_readlane_b32 s43,v228,s42\n"
MF"v[144:147],a[64:67],v[64:67],v[144:147]\n"
SA"s41,s41,32768\n"
BL"a[192:195],v224,s[8:11],s53 offen offset:1024 sc1\n"
EX"v26,v26\n"
MF"v[144:147],a[68:71],v[72:75],v[144:147]\n"
"s_cmp_eq_u32 s41,163840\n"
BL"v225,s[8:11],s44 offen offset:3072 sc1 lds\n"
PK"v17,v18,v19\n"
MF"v[176:179],a[68:71],v[76:79],v[176:179]\n"
"s_cselect_b32 s41,0,s41\n"
EX"v27,v27\n"
MF"v[176:179],a[64:67],v[68:71],v[176:179]\n"
"s_mov_b32 m0,s40\n"
RD"a[92:95],v227 offset:11264\n"
RD"a[96:99],v227 offset:4096\n"
MF"v[148:151],a[72:75],v[64:67],v[148:151]\n"
WT"lgkmcnt(3)\n"
EX"v20,v20\n"
MF"v[148:151],a[76:79],v[72:75],v[148:151]\n"
RD"a[100:103],v227 offset:12288\n"
RD"a[104:107],v227 offset:5120\n"
PK"v24,v24,v25\n"
MF"v[180:183],a[76:79],v[76:79],v[180:183]\n"
BL"a[196:199],v224,s[8:11],s52 offen offset:2048 sc1\n"
EX"v21,v21\n"
MF"v[180:183],a[72:75],v[68:71],v[180:183]\n"
EX"v22,v22\n"
"v_add_u32 v226,s41,v224\n"
MF"v[152:155],a[80:83],v[64:67],v[152:155]\n"
BL"v225,s[8:11],s43 offen offset:0 sc1 lds\n"
MF"v[152:155],a[84:87],v[72:75],v[152:155]\n"
EX"v23,v23\n"
PK"v25,v26,v27\n"
MF"v[184:187],a[84:87],v[76:79],v[184:187]\n"
BL"a[200:203],v224,s[8:11],s53 offen offset:2048 sc1\n"
MF"v[184:187],a[80:83],v[68:71],v[184:187]\n"
WT"lgkmcnt(2)\n"
RD"a[108:111],v227 offset:13312\n"
"v_readlane_b32 s44,v229,s42\n"
MF"v[156:159],a[88:91],v[64:67],v[156:159]\n"
SA"s42,s42,1\n"
EX"v28,v28\n"
MF"v[156:159],a[92:95],v[72:75],v[156:159]\n"
RD"a[112:115],v227 offset:6144\n"
RD"a[116:119],v227 offset:14336\n"
PK"v18,v20,v21\n"
MF"v[188:191],a[92:95],v[76:79],v[188:191]\n"
EX"v29,v29\n"
MF"v[188:191],a[88:91],v[68:71],v[188:191]\n"
RD"a[120:123],v227 offset:7168\n"
PK"v19,v22,v23\n"
MF"v[160:163],a[96:99],v[64:67],v[160:163]\n"
WT"lgkmcnt(4)\n"
BL"a[204:207],v224,s[8:11],s52 offen offset:3072 sc1\n"
EX"v30,v30\n"
MF"v[160:163],a[100:103],v[72:75],v[160:163]\n"
BL"v225,s[8:11],s43 offen offset:1024 sc1 lds\n"
MF"v[192:195],a[100:103],v[76:79],v[192:195]\n"
EX"v31,v31\n"
MF"v[192:195],a[96:99],v[68:71],v[192:195]\n"
RD"a[124:127],v227 offset:15360\n"
PK"v26,v28,v29\n"
MF"v[164:167],a[104:107],v[64:67],v[164:167]\n"
WT"lgkmcnt(2)\n"
EX"v32,v32\n"
MF"v[164:167],a[108:111],v[72:75],v[164:167]\n"
SA"s48,s59,98304\n"
BL"a[208:211],v224,s[8:11],s53 offen offset:3072 sc1\n"
MF"v[196:199],a[108:111],v[76:79],v[196:199]\n"
"s_and_b32 s48,s48,0x18000\n"
EX"v33,v33\n"
MF"v[196:199],a[104:107],v[68:71],v[196:199]\n"
"s_andn2_b32 s49,s59,0x18000\n"
RD"a[64:67],v227 offset:16384\n"
PK"v27,v30,v31\n"
MF"v[168:171],a[112:115],v[64:67],v[168:171]\n"
"s_or_b32 s52,s48,s49\n"
EX"v34,v34\n"
MF"v[168:171],a[116:119],v[72:75],v[168:171]\n"
SA"s53,s52,16384\n"
BL"v225,s[8:11],s43 offen offset:2048 sc1 lds\n"
MF"v[200:203],a[116:119],v[76:79],v[200:203]\n"
EX"v35,v35\n"
MF"v[200:203],a[112:115],v[68:71],v[200:203]\n"
WT"lgkmcnt(1)\n"
BL"a[212:215],v224,s[8:11],s52 offen offset:0 sc1\n"
RD"a[68:71],v227 offset:24576\n"
MF"v[172:175],a[120:123],v[64:67],v[172:175]\n"
EX"v40,v40\n"
MF"v[172:175],a[124:127],v[72:75],v[172:175]\n"
RD"a[72:75],v227 offset:17408\n"
RD"a[76:79],v227 offset:25600\n"
MF"v[204:207],a[124:127],v[76:79],v[204:207]\n"
EX"v41,v41\n"
MF"v[204:207],a[120:123],v[68:71],v[204:207]\n"
RD"a[80:83],v227 offset:18432\n"
MF"a[140:143],a[128:131],v[64:67],a[140:143]\n"
BL"a[216:219],v224,s[8:11],s53 offen offset:0 sc1\n"
EX"v42,v42\n"
MF"a[140:143],a[128:131],v[72:75],a[140:143]\n"
BL"v225,s[8:11],s43 offen offset:3072 sc1 lds\n"
PK"v64,v32,v33\n"
MF"a[144:147],a[128:131],v[76:79],a[144:147]\n"
EX"v43,v43\n"
PK"v65,v34,v35\n"
MF"a[144:147],a[128:131],v[68:71],a[144:147]\n"
WT"lgkmcnt(3)\n"
RD"a[84:87],v227 offset:26624\n"
RD"a[88:91],v227 offset:19456\n"
MF"v[144:147],a[64:67],v[0:3],v[144:147]\n"
SA"m0,s40,16384\n"
EX"v36,v36\n"
MF"v[144:147],a[68:71],v[16:19],v[144:147]\n"
BL"a[220:223],v224,s[8:11],s52 offen offset:1024 sc1\n"
RD"a[92:95],v227 offset:27648\n"
MF"v[176:179],a[68:71],v[24:27],v[176:179]\n"
EX"v37,v37\n"
PK"v68,v40,v41\n"
MF"v[176:179],a[64:67],v[8:11],v[176:179]\n"
WT"lgkmcnt(3)\n"
RD"a[96:99],v227 offset:20480\n"
PK"v69,v42,v43\n"
MF"v[148:151],a[72:75],v[0:3],v[148:151]\n"
SA"s40,s40,32768\n"
EX"v38,v38\n"
MF"v[148:151],a[76:79],v[16:19],v[148:151]\n"
"s_cmp_eq_u32 s40,s46\n"
BL"v225,s[8:11],s44 offen offset:0 sc1 lds\n"
MF"v[180:183],a[76:79],v[24:27],v[180:183]\n"
"s_cselect_b32 s40,s47,s40\n"
BL"a[224:227],v224,s[8:11],s53 offen offset:1024 sc1\n"
EX"v39,v39\n"
MF"v[180:183],a[72:75],v[8:11],v[180:183]\n"
RD"a[100:103],v227 offset:28672\n"
RD"a[104:107],v227 offset:21504\n"
MF"v[152:155],a[80:83],v[0:3],v[152:155]\n"
WT"lgkmcnt(3)\n"
EX"v44,v44\n"
MF"v[152:155],a[84:87],v[16:19],v[152:155]\n"
RD"a[108:111],v227 offset:29696\n"
RD"a[112:115],v227 offset:22528\n"
PK"v66,v36,v37\n"
MF"v[184:187],a[84:87],v[24:27],v[184:187]\n"
EX"v45,v45\n"
MF"v[184:187],a[80:83],v[8:11],v[184:187]\n"
BL"a[228:231],v224,s[8:11],s52 offen offset:2048 sc1\n"
MF"v[156:159],a[88:91],v[0:3],v[156:159]\n"
EX"v46,v46\n"
MF"v[156:159],a[92:95],v[16:19],v[156:159]\n"
BL"v225,s[8:11],s44 offen offset:1024 sc1 lds\n"
PK"v67,v38,v39\n"
MF"v[188:191],a[92:95],v[24:27],v[188:191]\n"
EX"v47,v47\n"
MF"v[188:191],a[88:91],v[8:11],v[188:191]\n"
WT"lgkmcnt(2)\n"
RD"a[116:119],v227 offset:30720\n"
RD"a[120:123],v227 offset:23552\n"
MF"v[160:163],a[96:99],v[0:3],v[160:163]\n"
EX"v48,v48\n"
MF"v[160:163],a[100:103],v[16:19],v[160:163]\n"
BL"a[232:235],v224,s[8:11],s53 offen offset:2048 sc1\n"
RD"a[124:127],v227 offset:31744\n"
MF"v[192:195],a[100:103],v[24:27],v[192:195]\n"
"buffer_load_dwordx2 v[242:243],v236,s[8:11],0 offen sc1\n"
EX"v49,v49\n"
MF"v[192:195],a[96:99],v[8:11],v[192:195]\n"
WT"vmcnt(38)\n"
EX"v50,v50\n"
PK"v70,v44,v45\n"
MF"v[164:167],a[104:107],v[0:3],v[164:167]\n"
WT"lgkmcnt(3)\n"
BL"v225,s[8:11],s44 offen offset:2048 sc1 lds\n"
MF"v[164:167],a[108:111],v[16:19],v[164:167]\n"
BL"a[236:239],v224,s[8:11],s52 offen offset:3072 sc1\n"
PK"v71,v46,v47\n"
MF"v[196:199],a[108:111],v[24:27],v[196:199]\n"
"s_barrier\n"
MF"v[196:199],a[104:107],v[8:11],v[196:199]\n"
RD"a[64:67],v226 offset:0\n"
PK"v72,v48,v49\n"
MF"v[168:171],a[112:115],v[0:3],v[168:171]\n"
WT"lgkmcnt(1)\n"
EX"v51,v51\n"
MF"v[168:171],a[116:119],v[16:19],v[168:171]\n"
RD"a[68:71],v226 offset:1024\n"
RD"a[72:75],v226 offset:2048\n"
MF"v[200:203],a[116:119],v[24:27],v[200:203]\n"
EX"v52,v52\n"
MF"v[200:203],a[112:115],v[8:11],v[200:203]\n"
BL"a[240:243],v224,s[8:11],s53 offen offset:3072 sc1\n"
MF"v[172:175],a[120:123],v[0:3],v[172:175]\n"
EX"v53,v53\n"
MF"v[172:175],a[124:127],v[16:19],v[172:175]\n"
BL"v225,s[8:11],s44 offen offset:3072 sc1 lds\n"
MF"v[204:207],a[124:127],v[24:27],v[204:207]\n"
EX"v54,v54\n"
MF"v[204:207],a[120:123],v[8:11],v[204:207]\n"
RD"a[76:79],v226 offset:3072\n"
RD"a[80:83],v226 offset:4096\n"
PK"v73,v50,v51\n"
MF"a[140:143],a[128:131],v[0:3],a[140:143]\n"
EX"v55,v55\n"
MF"a[140:143],a[128:131],v[16:19],a[140:143]\n"
RD"a[84:87],v226 offset:5120\n"
RD"a[88:91],v226 offset:6144\n"
MF"a[144:147],a[128:131],v[24:27],a[144:147]\n"
WT"lgkmcnt(5)\n"
PK"v74,v52,v53\n"
PK"v75,v54,v55\n"
MF"a[144:147],a[128:131],v[8:11],a[144:147]\n"
"s_sub_u32 s56,s22,1\n"
"s_mov_b32 s22,-1\n"
"s_cbranch_scc0 LP2_%=\n"
"LE2_%=:\n"
MF"v[0:3],a[64:67],a[32:35],v[208:211]\n"
WT"vmcnt(3)\n"
"v_cmp_eq_u64 vcc,s[28:29],v[242:243]\n"
"s_or_b32 vcc_lo,vcc_lo,-1\n"
"s_cmp_eq_u64 vcc,exec\n"
"s_cbranch_scc0 L1031_%=\n"
"L1032_%=:\n"
MF"v[0:3],a[68:71],a[36:39],v[0:3]\n"
"s_mov_b32 m0,s40\n"
EX"v56,v56\n"
MF"v[8:11],a[68:71],a[52:55],0\n"
EX"v57,v57\n"
MF"v[8:11],a[64:67],a[48:51],v[8:11]\n"
WT"lgkmcnt(4)\n"
RD"a[92:95],v226 offset:7168\n"
RD"a[96:99],v226 offset:8192\n"
"v_readlane_b32 s43,v228,s42\n"
MF"v[0:3],a[72:75],a[40:43],v[0:3]\n"
WT"lgkmcnt(2)\n"
EX"v58,v58\n"
"v_readlane_b32 s44,v229,s42\n"
MF"v[0:3],a[76:79],a[44:47],v[0:3]\n"
SA"s42,s42,1\n"
BL"v225,s[8:11],s43 offen offset:0 sc1 lds\n"
RD"a[100:103],v226 offset:9216\n"
MF"v[8:11],a[76:79],a[60:63],v[8:11]\n"
EX"v59,v59\n"
PK"v76,v56,v57\n"
MF"v[8:11],a[72:75],a[56:59],v[8:11]\n"
RD"a[104:107],v226 offset:10240\n"
PK"v77,v58,v59\n"
MF"v[4:7],a[80:83],a[32:35],v[212:215]\n"
EX"v60,v60\n"
MF"v[4:7],a[84:87],a[36:39],v[4:7]\n"
BL"v225,s[8:11],s43 offen offset:1024 sc1 lds\n"
MF"v[12:15],a[84:87],a[52:55],0\n"
EX"v61,v61\n"
MF"v[12:15],a[80:83],a[48:51],v[12:15]\n"
RD"a[108:111],v226 offset:11264\n"
RD"a[112:115],v226 offset:12288\n"
MF"v[4:7],a[88:91],a[40:43],v[4:7]\n"
WT"lgkmcnt(3)\n"
EX"v62,v62\n"
MF"v[4:7],a[92:95],a[44:47],v[4:7]\n"
RD"a[116:119],v226 offset:13312\n"
RD"a[120:123],v226 offset:14336\n"
PK"v78,v60,v61\n"
MF"v[12:15],a[92:95],a[60:63],v[12:15]\n"
EX"v63,v63\n"
MF"v[12:15],a[88:91],a[56:59],v[12:15]\n"
BL"v225,s[8:11],s43 offen offset:2048 sc1 lds\n"
MF"v[16:19],a[96:99],a[32:35],v[216:219]\n"
EX"v0,v0\n"
MF"v[16:19],a[100:103],a[36:39],v[16:19]\n"
RD"a[124:127],v226 offset:15360\n"
RD"a[64:67],v226 offset:16384\n"
PK"v79,v62,v63\n"
MF"v[24:27],a[100:103],a[52:55],0\n"
EX"v1,v1\n"
MF"v[24:27],a[96:99],a[48:51],v[24:27]\n"
WT"lgkmcnt(4)\n"
RD"a[68:71],v226 offset:17408\n"
RD"a[72:75],v226 offset:18432\n"
MF"v[16:19],a[104:107],a[40:43],v[16:19]\n"
SA"s41,s41,32768\n"
BL"v225,s[8:11],s43 offen offset:3072 sc1 lds\n"
MF"v[16:19],a[108:111],a[44:47],v[16:19]\n"
"s_cmp_eq_u32 s41,163840\n"
EX"v2,v2\n"
PK"v0,v0,v1\n"
MF"v[24:27],a[108:111],a[60:63],v[24:27]\n"
"s_cselect_b32 s41,0,s41\n"
RD"a[76:79],v226 offset:19456\n"
RD"a[80:83],v226 offset:20480\n"
MF"v[24:27],a[104:107],a[56:59],v[24:27]\n"
SA"m0,s40,16384\n"
EX"v3,v3\n"
"v_add_u32 v227,s41,v224\n"
MF"v[20:23],a[112:115],a[32:35],v[220:223]\n"
WT"lgkmcnt(4)\n"
RD"a[84:87],v226 offset:21504\n"
RD"a[88:91],v226 offset:22528\n"
MF"v[20:23],a[116:119],a[36:39],v[20:23]\n"
EX"v8,v8\n"
PK"v1,v2,v3\n"
MF"v[28:31],a[116:119],a[52:55],0\n"
SA"s40,s40,32768\n"
BL"v225,s[8:11],s44 offen offset:0 sc1 lds\n"
MF"v[28:31],a[112:115],a[48:51],v[28:31]\n"
"s_cmp_eq_u32 s40,s46\n"
RD"a[92:95],v226 offset:23552\n"
EX"v9,v9\n"
MF"v[20:23],a[120:123],a[40:43],v[20:23]\n"
"s_cselect_b32 s40,s47,s40\n"
RD"a[96:99],v226 offset:24576\n"
RD"a[100:103],v226 offset:25600\n"
EX"v10,v10\n"
MF"v[20:23],a[124:127],a[44:47],v[20:23]\n"
RD"a[104:107],v226 offset:26624\n"
RD"a[108:111],v226 offset:27648\n"
PK"v8,v8,v9\n"
MF"v[28:31],a[124:127],a[60:63],v[28:31]\n"
EX"v11,v11\n"
MF"v[28:31],a[120:123],a[56:59],v[28:31]\n"
RD"a[112:115],v226 offset:28672\n"
RD"a[116:119],v226 offset:29696\n"
MF"v[40:43],a[64:67],a[48:51],v[208:211]\n"
WT"lgkmcnt(7)\n"
BL"v225,s[8:11],s44 offen offset:1024 sc1 lds\n"
EX"v4,v4\n"
MF"v[40:43],a[68:71],a[52:55],v[40:43]\n"
RD"a[120:123],v226 offset:30720\n"
RD"a[124:127],v226 offset:31744\n"
PK"v9,v10,v11\n"
MF"v[40:43],a[72:75],a[56:59],v[40:43]\n"
EX"v5,v5\n"
MF"v[40:43],a[76:79],a[60:63],v[40:43]\n"
EX"v6,v6\n"
MF"v[44:47],a[80:83],a[48:51],v[212:215]\n"
"s_barrier\n"
MF"v[44:47],a[84:87],a[52:55],v[44:47]\n"
RD"a[64:67],v227 offset:0\n"
PK"v2,v4,v5\n"
MF"v[44:47],a[88:91],a[56:59],v[44:47]\n"
WT"lgkmcnt(3)\n"
RD"a[68:71],v227 offset:8192\n"
BL"v225,s[8:11],s44 offen offset:2048 sc1 lds\n"
MF"v[44:47],a[92:95],a[60:63],v[44:47]\n"
EX"v7,v7\n"
MF"v[56:59],a[96:99],a[48:51],v[216:219]\n"
RD"a[72:75],v227 offset:1024\n"
"v_readlane_b32 s43,v228,s42\n"
EX"v12,v12\n"
MF"v[56:59],a[100:103],a[52:55],v[56:59]\n"
EX"v13,v13\n"
PK"v3,v6,v7\n"
MF"v[56:59],a[104:107],a[56:59],v[56:59]\n"
BL"v225,s[8:11],s44 offen offset:3072 sc1 lds\n"
MF"v[56:59],a[108:111],a[60:63],v[56:59]\n"
"s_mov_b32 m0,s40\n"
RD"a[76:79],v227 offset:9216\n"
PK"v10,v12,v13\n"
MF"v[60:63],a[112:115],a[48:51],v[220:223]\n"
EX"v14,v14\n"
MF"v[60:63],a[116:119],a[52:55],v[60:63]\n"
WT"lgkmcnt(2)\n"
RD"a[80:83],v227 offset:2048\n"
RD"a[84:87],v227 offset:10240\n"
MF"v[60:63],a[120:123],a[56:59],v[60:63]\n"
EX"v15,v15\n"
MF"v[60:63],a[124:127],a[60:63],v[60:63]\n"
SA"s41,s41,32768\n"
RD"a[88:91],v227 offset:3072\n"
"v_readlane_b32 s44,v229,s42\n"
MF"v[144:147],a[64:67],v[64:67],v[144:147]\n"
"s_cmp_eq_u32 s41,163840\n"
EX"v16,v16\n"
PK"v11,v14,v15\n"
MF"v[144:147],a[68:71],v[72:75],v[144:147]\n"
"s_cselect_b32 s41,0,s41\n"
BL"v225,s[8:11],s43 offen offset:0 sc1 lds\n"
MF"v[176:179],a[68:71],v[76:79],v[176:179]\n"
SA"s42,s42,1\n"
EX"v17,v17\n"
"v_add_u32 v226,s41,v224\n"
MF"v[176:179],a[64:67],v[68:71],v[176:179]\n"
WT"lgkmcnt(3)\n"
RD"a[92:95],v227 offset:11264\n"
RD"a[96:99],v227 offset:4096\n"
MF"v[148:151],a[72:75],v[64:67],v[148:151]\n"
EX"v18,v18\n"
PK"v16,v16,v17\n"
MF"v[148:151],a[76:79],v[72:75],v[148:151]\n"
RD"a[100:103],v227 offset:12288\n"
RD"a[104:107],v227 offset:5120\n"
MF"v[180:183],a[76:79],v[76:79],v[180:183]\n"
EX"v19,v19\n"
MF"v[180:183],a[72:75],v[68:71],v[180:183]\n"
WT"lgkmcnt(4)\n"
BL"v225,s[8:11],s43 offen offset:1024 sc1 lds\n"
MF"v[152:155],a[80:83],v[64:67],v[152:155]\n"
EX"v24,v24\n"
PK"v17,v18,v19\n"
MF"v[152:155],a[84:87],v[72:75],v[152:155]\n"
EX"v25,v25\n"
MF"v[184:187],a[84:87],v[76:79],v[184:187]\n"
EX"v26,v26\n"
MF"v[184:187],a[80:83],v[68:71],v[184:187]\n"
RD"a[108:111],v227 offset:13312\n"
RD"a[112:115],v227 offset:6144\n"
PK"v24,v24,v25\n"
MF"v[156:159],a[88:91],v[64:67],v[156:159]\n"
WT"lgkmcnt(2)\n"
BL"v225,s[8:11],s43 offen offset:2048 sc1 lds\n"
MF"v[156:159],a[92:95],v[72:75],v[156:159]\n"
EX"v27,v27\n"
MF"v[188:191],a[92:95],v[76:79],v[188:191]\n"
RD"a[116:119],v227 offset:14336\n"
RD"a[120:123],v227 offset:7168\n"
MF"v[188:191],a[88:91],v[68:71],v[188:191]\n"
EX"v20,v20\n"
PK"v25,v26,v27\n"
MF"v[160:163],a[96:99],v[64:67],v[160:163]\n"
EX"v21,v21\n"
MF"v[160:163],a[100:103],v[72:75],v[160:163]\n"
BL"v225,s[8:11],s43 offen offset:3072 sc1 lds\n"
MF"v[192:195],a[100:103],v[76:79],v[192:195]\n"
SA"m0,s40,16384\n"
EX"v22,v22\n"
MF"v[192:195],a[96:99],v[68:71],v[192:195]\n"
RD"a[124:127],v227 offset:15360\n"
PK"v18,v20,v21\n"
MF"v[164:167],a[104:107],v[64:67],v[164:167]\n"
WT"lgkmcnt(3)\n"
EX"v23,v23\n"
MF"v[164:167],a[108:111],v[72:75],v[164:167]\n"
EX"v28,v28\n"
MF"v[196:199],a[108:111],v[76:79],v[196:199]\n"
SA"s40,s40,32768\n"
BL"v225,s[8:11],s44 offen offset:0 sc1 lds\n"
MF"v[196:199],a[104:107],v[68:71],v[196:199]\n"
"s_cmp_eq_u32 s40,s46\n"
RD"a[64:67],v227 offset:16384\n"
PK"v19,v22,v23\n"
MF"v[168:171],a[112:115],v[64:67],v[168:171]\n"
WT"lgkmcnt(2)\n"
EX"v29,v29\n"
MF"v[168:171],a[116:119],v[72:75],v[168:171]\n"
"s_cselect_b32 s40,s47,s40\n"
EX"v30,v30\n"
MF"v[200:203],a[116:119],v[76:79],v[200:203]\n"
RD"a[68:71],v227 offset:24576\n"
PK"v26,v28,v29\n"
MF"v[200:203],a[112:115],v[68:71],v[200:203]\n"
BL"v225,s[8:11],s44 offen offset:1024 sc1 lds\n"
MF"v[172:175],a[120:123],v[64:67],v[172:175]\n"
WT"lgkmcnt(2)\n"
EX"v31,v31\n"
MF"v[172:175],a[124:127],v[72:75],v[172:175]\n"
RD"a[72:75],v227 offset:17408\n"
RD"a[76:79],v227 offset:25600\n"
MF"v[204:207],a[124:127],v[76:79],v[204:207]\n"
EX"v40,v40\n"
MF"v[204:207],a[120:123],v[68:71],v[204:207]\n"
RD"a[80:83],v227 offset:18432\n"
PK"v27,v30,v31\n"
MF"a[140:143],a[128:131],v[64:67],a[140:143]\n"
EX"v41,v41\n"
MF"a[140:143],a[128:131],v[72:75],a[140:143]\n"
BL"v225,s[8:11],s44 offen offset:2048 sc1 lds\n"
MF"a[144:147],a[128:131],v[76:79],a[144:147]\n"
EX"v42,v42\n"
MF"a[144:147],a[128:131],v[68:71],a[144:147]\n"
WT"lgkmcnt(3)\n"
RD"a[84:87],v227 offset:26624\n"
RD"a[88:91],v227 offset:19456\n"
PK"v68,v40,v41\n"
MF"v[144:147],a[64:67],v[0:3],v[144:147]\n"
EX"v43,v43\n"
MF"v[144:147],a[68:71],v[16:19],v[144:147]\n"
RD"a[92:95],v227 offset:27648\n"
RD"a[96:99],v227 offset:20480\n"
MF"v[176:179],a[68:71],v[24:27],v[176:179]\n"
EX"v44,v44\n"
"v_readlane_b32 s43,v228,s42\n"
MF"v[176:179],a[64:67],v[8:11],v[176:179]\n"
WT"lgkmcnt(4)\n"
BL"v225,s[8:11],s44 offen offset:3072 sc1 lds\n"
PK"v69,v42,v43\n"
MF"v[148:151],a[72:75],v[0:3],v[148:151]\n"
"s_mov_b32 m0,s40\n"
EX"v45,v45\n"
MF"v[148:151],a[76:79],v[16:19],v[148:151]\n"
EX"v46,v46\n"
MF"v[180:183],a[76:79],v[24:27],v[180:183]\n"
RD"a[100:103],v227 offset:28672\n"
"v_readlane_b32 s44,v229,s42\n"
PK"v70,v44,v45\n"
MF"v[180:183],a[72:75],v[8:11],v[180:183]\n"
SA"s42,s42,1\n"
EX"v47,v47\n"
MF"v[152:155],a[80:83],v[0:3],v[152:155]\n"
WT"lgkmcnt(1)\n"
RD"a[104:107],v227 offset:21504\n"
RD"a[108:111],v227 offset:29696\n"
MF"v[152:155],a[84:87],v[16:19],v[152:155]\n"
EX"v56,v56\n"
MF"v[184:187],a[84:87],v[24:27],v[184:187]\n"
RD"a[112:115],v227 offset:22528\n"
PK"v71,v46,v47\n"
MF"v[184:187],a[80:83],v[8:11],v[184:187]\n"
EX"v57,v57\n"
MF"v[156:159],a[88:91],v[0:3],v[156:159]\n"
EX"v58,v58\n"
MF"v[156:159],a[92:95],v[16:19],v[156:159]\n"
RD"a[116:119],v227 offset:30720\n"
PK"v76,v56,v57\n"
MF"v[188:191],a[92:95],v[24:27],v[188:191]\n"
EX"v59,v59\n"
MF"v[188:191],a[88:91],v[8:11],v[188:191]\n"
RD"a[120:123],v227 offset:23552\n"
RD"a[124:127],v227 offset:31744\n"
MF"v[160:163],a[96:99],v[0:3],v[160:163]\n"
WT"lgkmcnt(4)\n"
EX"v60,v60\n"
MF"v[160:163],a[100:103],v[16:19],v[160:163]\n"
PK"v77,v58,v59\n"
MF"v[192:195],a[100:103],v[24:27],v[192:195]\n"
EX"v61,v61\n"
MF"v[192:195],a[96:99],v[8:11],v[192:195]\n"
WT"vmcnt(16)\n"
PK"v78,v60,v61\n"
MF"v[164:167],a[104:107],v[0:3],v[164:167]\n"
EX"v62,v62\n"
MF"v[164:167],a[108:111],v[16:19],v[164:167]\n"
SA"s41,s41,32768\n"
"s_barrier\n"
MF"v[196:199],a[108:111],v[24:27],v[196:199]\n"
"s_cmp_eq_u32 s41,163840\n"
BL"v225,s[8:11],s43 offen offset:0 sc1 lds\n"
BL"v225,s[8:11],s43 offen offset:1024 sc1 lds\n"
MF"v[196:199],a[104:107],v[8:11],v[196:199]\n"
WT"lgkmcnt(0)\n"
BL"v225,s[8:11],s43 offen offset:2048 sc1 lds\n"
RD"a[64:67],v226 offset:0\n"
MF"v[168:171],a[112:115],v[0:3],v[168:171]\n"
"s_cselect_b32 s41,0,s41\n"
EX"v63,v63\n"
"v_add_u32 v227,s41,v224\n"
MF"v[168:171],a[116:119],v[16:19],v[168:171]\n"
RD"a[68:71],v226 offset:8192\n"
RD"a[72:75],v226 offset:1024\n"
BL"v225,s[8:11],s43 offen offset:3072 sc1 lds\n"
MF"v[200:203],a[116:119],v[24:27],v[200:203]\n"
SA"m0,s40,16384\n"
RD"a[76:79],v226 offset:9216\n"
PK"v79,v62,v63\n"
MF"v[200:203],a[112:115],v[8:11],v[200:203]\n"
RD"a[80:83],v226 offset:2048\n"
MF"v[172:175],a[120:123],v[0:3],v[172:175]\n"
RD"a[84:87],v226 offset:10240\n"
MF"v[172:175],a[124:127],v[16:19],v[172:175]\n"
RD"a[88:91],v226 offset:3072\n"
MF"v[204:207],a[124:127],v[24:27],v[204:207]\n"
SA"s40,s40,32768\n"
BL"v225,s[8:11],s44 offen offset:0 sc1 lds\n"
MF"v[204:207],a[120:123],v[8:11],v[204:207]\n"
"s_cmp_eq_u32 s40,s46\n"
RD"a[92:95],v226 offset:11264\n"
"s_cselect_b32 s40,s47,s40\n"
MF"a[140:143],a[128:131],v[0:3],a[140:143]\n"
RD"a[96:99],v226 offset:4096\n"
MF"a[140:143],a[128:131],v[16:19],a[140:143]\n"
RD"a[100:103],v226 offset:12288\n"
RD"a[104:107],v226 offset:5120\n"
MF"a[144:147],a[128:131],v[24:27],a[144:147]\n"
RD"a[108:111],v226 offset:13312\n"
MF"a[144:147],a[128:131],v[8:11],a[144:147]\n"
WT"lgkmcnt(6)\n"
RD"a[112:115],v226 offset:6144\n"
MF"v[176:179],a[64:67],v[68:71],v[176:179]\n"
BL"v225,s[8:11],s44 offen offset:1024 sc1 lds\n"
RD"a[116:119],v226 offset:14336\n"
MF"v[176:179],a[68:71],v[76:79],v[176:179]\n"
WT"vmcnt(14)\n"
RD"a[120:123],v226 offset:7168\n"
MF"v[180:183],a[72:75],v[68:71],v[180:183]\n"
RD"a[124:127],v226 offset:15360\n"
MF"v[180:183],a[76:79],v[76:79],v[180:183]\n"
"s_barrier\n"
MF"v[184:187],a[80:83],v[68:71],v[184:187]\n"
RD"a[64:67],v227 offset:0\n"
MF"v[184:187],a[84:87],v[76:79],v[184:187]\n"
WT"lgkmcnt(5)\n"
BL"v225,s[8:11],s44 offen offset:2048 sc1 lds\n"
MF"v[188:191],a[88:91],v[68:71],v[188:191]\n"
RD"a[68:71],v227 offset:1024\n"
MF"v[188:191],a[92:95],v[76:79],v[188:191]\n"
MF"v[192:195],a[96:99],v[68:71],v[192:195]\n"
MF"v[192:195],a[100:103],v[76:79],v[192:195]\n"
BL"v225,s[8:11],s44 offen offset:3072 sc1 lds\n"
MF"v[196:199],a[104:107],v[68:71],v[196:199]\n"
RD"a[72:75],v227 offset:2048\n"
MF"v[196:199],a[108:111],v[76:79],v[196:199]\n"
WT"lgkmcnt(2)\n"
RD"a[76:79],v227 offset:3072\n"
MF"v[200:203],a[112:115],v[68:71],v[200:203]\n"
RD"a[80:83],v227 offset:4096\n"
MF"v[200:203],a[116:119],v[76:79],v[200:203]\n"
RD"a[84:87],v227 offset:5120\n"
MF"v[204:207],a[120:123],v[68:71],v[204:207]\n"
MF"v[204:207],a[124:127],v[76:79],v[204:207]\n"
MF"a[144:147],a[128:131],v[68:71],a[144:147]\n"
WT"lgkmcnt(4)\n"
MF"a[144:147],a[128:131],v[76:79],a[144:147]\n"
RD"a[88:91],v227 offset:6144\n"
"LTL_%=:\n"
"s_cmp_ge_u32 s34,6\n"
"s_cbranch_scc1 LTH_%=\n"
RD"a[92:95],v227 offset:7168\n"
MF"v[0:3],a[64:67],v[80:83],0\n"
"v_accvgpr_read_b32 v251,a140\n"
"v_subrev_u32 v253,0x30800000,v251\n"
MF"v[0:3],a[68:71],v[88:91],v[0:3]\n"
RD"a[96:99],v227 offset:8192\n"
MF"v[40:43],a[68:71],v[120:123],0\n"
"v_cmp_le_u32 vcc,0x1f000000,v253\n"
"s_cmp_lg_u64 vcc,0\n"
"s_cbranch_scc1 L1041_%=\n"
"L1040_%=:\n"
MF"v[40:43],a[64:67],v[112:115],v[40:43]\n"
WT"lgkmcnt(3)\n"
"v_rcp_f32 v251,v251\n"
MF"v[4:7],a[72:75],v[80:83],0\n"
SA"s41,s41,32768\n"
RD"a[100:103],v227 offset:9216\n"
MU"v144,v251,v144\n"
MF"v[4:7],a[76:79],v[88:91],v[4:7]\n"
"s_cmp_eq_u32 s41,163840\n"
RD"a[104:107],v227 offset:10240\n"
MU"v145,v251,v145\n"
MF"v[44:47],a[76:79],v[120:123],0\n"
"s_cselect_b32 s41,0,s41\n"
MU"v146,v251,v146\n"
"v_add_u32 v226,s41,v224\n"
MF"v[44:47],a[72:75],v[112:115],v[44:47]\n"
RD"a[108:111],v227 offset:11264\n"
MU"v147,v251,v147\n"
MF"v[8:11],a[80:83],v[80:83],0\n"
SA"s41,s41,32768\n"
MU"v148,v251,v148\n"
MU"v149,v251,v149\n"
MF"v[8:11],a[84:87],v[88:91],v[8:11]\n"
"s_cmp_eq_u32 s41,163840\n"
RD"a[112:115],v227 offset:12288\n"
MU"v150,v251,v150\n"
MU"v151,v251,v151\n"
MF"v[48:51],a[84:87],v[120:123],0\n"
"s_cselect_b32 s41,0,s41\n"
PK"v144,v144,v145\n"
"v_add_u32 v235,s41,v224\n"
PK"v145,v146,v147\n"
MF"v[48:51],a[80:83],v[112:115],v[48:51]\n"
WT"lgkmcnt(3)\n"
RD"a[116:119],v227 offset:13312\n"
PK"v146,v148,v149\n"
MF"v[12:15],a[88:91],v[80:83],0\n"
SA"s41,s41,32768\n"
PK"v147,v150,v151\n"
MU"v152,v251,v152\n"
MF"v[12:15],a[92:95],v[88:91],v[12:15]\n"
"s_cmp_eq_u32 s41,163840\n"
BS"v[144:147],v224,s[8:11],s60 offen offset:0 sc1\n"
MU"v153,v251,v153\n"
MF"v[52:55],a[92:95],v[120:123],0\n"
"s_cselect_b32 s41,0,s41\n"
RD"a[120:123],v227 offset:14336\n"
"v_add_u32 v236,s41,v224\n"
MU"v154,v251,v154\n"
MF"v[52:55],a[88:91],v[112:115],v[52:55]\n"
RD"a[124:127],v227 offset:15360\n"
MU"v155,v251,v155\n"
MU"v156,v251,v156\n"
MF"v[16:19],a[96:99],v[80:83],0\n"
SA"s41,s41,32768\n"
MU"v157,v251,v157\n"
MU"v158,v251,v158\n"
MU"v159,v251,v159\n"
MF"v[16:19],a[100:103],v[88:91],v[16:19]\n"
"s_cmp_eq_u32 s41,163840\n"
RD"a[64:67],v227 offset:16384\n"
PK"v152,v152,v153\n"
PK"v153,v154,v155\n"
MF"v[56:59],a[100:103],v[120:123],0\n"
"s_cselect_b32 s41,0,s41\n"
PK"v154,v156,v157\n"
"v_add_u32 v237,s41,v224\n"
PK"v155,v158,v159\n"
MF"v[56:59],a[96:99],v[112:115],v[56:59]\n"
WT"lgkmcnt(3)\n"
BS"v[152:155],v224,s[8:11],s60 offen offset:1024 sc1\n"
MU"v160,v251,v160\n"
MF"v[20:23],a[104:107],v[80:83],0\n"
RD"a[68:71],v227 offset:17408\n"
MU"v161,v251,v161\n"
MU"v162,v251,v162\n"
MF"v[20:23],a[108:111],v[88:91],v[20:23]\n"
RD"a[72:75],v227 offset:18432\n"
MU"v163,v251,v163\n"
MU"v164,v251,v164\n"
MF"v[60:63],a[108:111],v[120:123],0\n"
MU"v165,v251,v165\n"
MU"v166,v251,v166\n"
MF"v[60:63],a[104:107],v[112:115],v[60:63]\n"
RD"a[76:79],v227 offset:19456\n"
MU"v167,v251,v167\n"
PK"v160,v160,v161\n"
MF"v[24:27],a[112:115],v[80:83],0\n"
SA"s51,s60,16384\n"
"v_accvgpr_read_b32 v252,a144\n"
"v_subrev_u32 v253,0x30800000,v252\n"
PK"v161,v162,v163\n"
MF"v[24:27],a[116:119],v[88:91],v[24:27]\n"
"s_lshl_b32 s62,s34,14\n"
RD"a[80:83],v227 offset:20480\n"
PK"v162,v164,v165\n"
PK"v163,v166,v167\n"
MF"v[64:67],a[116:119],v[120:123],0\n"
"s_sub_u32 s62,s61,s62\n"
BS"v[160:163],v224,s[8:11],s60 offen offset:2048 sc1\n"
MU"v168,v251,v168\n"
MF"v[64:67],a[112:115],v[112:115],v[64:67]\n"
WT"lgkmcnt(3)\n"
"v_cmp_le_u32 vcc,0x1f000000,v253\n"
"s_cmp_lg_u64 vcc,0\n"
"s_cbranch_scc1 L1047_%=\n"
"L1046_%=:\n"
MF"v[28:31],a[120:123],v[80:83],0\n"
SA"s62,s62,122880\n"
"v_rcp_f32 v252,v252\n"
MU"v169,v251,v169\n"
MF"v[28:31],a[124:127],v[88:91],v[28:31]\n"
RD"a[84:87],v227 offset:21504\n"
RD"a[88:91],v227 offset:22528\n"
MU"v170,v251,v170\n"
MF"v[68:71],a[124:127],v[120:123],0\n"
MU"v171,v251,v171\n"
MU"v172,v251,v172\n"
MU"v173,v251,v173\n"
MF"v[68:71],a[120:123],v[112:115],v[68:71]\n"
RD"a[92:95],v227 offset:23552\n"
MU"v174,v251,v174\n"
MU"v175,v251,v175\n"
MF"v[32:35],a[64:67],v[80:83],0\n"
PK"v168,v168,v169\n"
PK"v169,v170,v171\n"
MU"v176,v252,v176\n"
MF"v[32:35],a[68:71],v[88:91],v[32:35]\n"
RD"a[96:99],v227 offset:24576\n"
MU"v177,v252,v177\n"
PK"v170,v172,v173\n"
MF"v[72:75],a[68:71],v[120:123],0\n"
PK"v171,v174,v175\n"
MU"v178,v252,v178\n"
MU"v179,v252,v179\n"
MF"v[72:75],a[64:67],v[112:115],v[72:75]\n"
WT"lgkmcnt(4)\n"
BS"v[168:171],v224,s[8:11],s60 offen offset:3072 sc1\n"
MU"v180,v252,v180\n"
MF"v[36:39],a[72:75],v[80:83],0\n"
RD"a[100:103],v227 offset:25600\n"
MU"v181,v252,v181\n"
MU"v182,v252,v182\n"
MF"v[36:39],a[76:79],v[88:91],v[36:39]\n"
RD"a[104:107],v227 offset:26624\n"
MU"v183,v252,v183\n"
PK"v176,v176,v177\n"
MF"v[76:79],a[76:79],v[120:123],0\n"
PK"v177,v178,v179\n"
PK"v178,v180,v181\n"
PK"v179,v182,v183\n"
MF"v[76:79],a[72:75],v[112:115],v[76:79]\n"
BS"v[176:179],v224,s[8:11],s51 offen offset:0 sc1\n"
MU"v184,v252,v184\n"
MF"v[0:3],a[80:83],v[96:99],v[0:3]\n"
WT"lgkmcnt(2)\n"
RD"a[108:111],v227 offset:27648\n"
MU"v185,v252,v185\n"
MU"v186,v252,v186\n"
MF"v[0:3],a[84:87],v[104:107],v[0:3]\n"
RD"a[112:115],v227 offset:28672\n"
MU"v187,v252,v187\n"
MU"v188,v252,v188\n"
MF"v[40:43],a[84:87],v[136:139],v[40:43]\n"
MU"v189,v252,v189\n"
MU"v190,v252,v190\n"
MU"v191,v252,v191\n"
MF"v[40:43],a[80:83],v[128:131],v[40:43]\n"
RD"a[116:119],v227 offset:29696\n"
PK"v184,v184,v185\n"
PK"v185,v186,v187\n"
MF"v[4:7],a[88:91],v[96:99],v[4:7]\n"
PK"v186,v188,v189\n"
PK"v187,v190,v191\n"
MU"v192,v252,v192\n"
MF"v[4:7],a[92:95],v[104:107],v[4:7]\n"
BS"v[184:187],v224,s[8:11],s51 offen offset:1024 sc1\n"
MU"v193,v252,v193\n"
MF"v[44:47],a[92:95],v[136:139],v[44:47]\n"
WT"vmcnt(14)\n"
RD"a[120:123],v227 offset:30720\n"
MU"v194,v252,v194\n"
MU"v195,v252,v195\n"
MF"v[44:47],a[88:91],v[128:131],v[44:47]\n"
RD"a[124:127],v227 offset:31744\n"
MU"v196,v252,v196\n"
MU"v197,v252,v197\n"
MF"v[8:11],a[96:99],v[96:99],v[8:11]\n"
WT"lgkmcnt(3)\n"
"s_barrier\n"
MF"v[8:11],a[100:103],v[104:107],v[8:11]\n"
RD"a[64:67],v226 offset:0\n"
MU"v198,v252,v198\n"
MU"v199,v252,v199\n"
MF"v[48:51],a[100:103],v[136:139],v[48:51]\n"
PK"v192,v192,v193\n"
PK"v193,v194,v195\n"
PK"v194,v196,v197\n"
MF"v[48:51],a[96:99],v[128:131],v[48:51]\n"
RD"a[68:71],v226 offset:1024\n"
PK"v195,v198,v199\n"
MU"v200,v252,v200\n"
MF"v[12:15],a[104:107],v[96:99],v[12:15]\n"
BS"v[192:195],v224,s[8:11],s51 offen offset:2048 sc1\n"
MU"v201,v252,v201\n"
MF"v[12:15],a[108:111],v[104:107],v[12:15]\n"
RD"a[72:75],v226 offset:2048\n"
MU"v202,v252,v202\n"
MU"v203,v252,v203\n"
MF"v[52:55],a[108:111],v[136:139],v[52:55]\n"
MU"v204,v252,v204\n"
MU"v205,v252,v205\n"
MU"v206,v252,v206\n"
MF"v[52:55],a[104:107],v[128:131],v[52:55]\n"
RD"a[76:79],v226 offset:3072\n"
MU"v207,v252,v207\n"
PK"v200,v200,v201\n"
MF"v[16:19],a[112:115],v[96:99],v[16:19]\n"
WT"lgkmcnt(3)\n"
PK"v201,v202,v203\n"
PK"v202,v204,v205\n"
PK"v203,v206,v207\n"
MF"v[16:19],a[116:119],v[104:107],v[16:19]\n"
BS"v[200:203],v224,s[8:11],s51 offen offset:3072 sc1\n"
MF"v[56:59],a[116:119],v[136:139],v[56:59]\n"
RD"a[80:83],v226 offset:4096\n"
"v_readlane_b32 s43,v228,s42\n"
MF"v[56:59],a[112:115],v[128:131],v[56:59]\n"
RD"a[84:87],v226 offset:5120\n"
MF"v[20:23],a[120:123],v[96:99],v[20:23]\n"
MF"v[20:23],a[124:127],v[104:107],v[20:23]\n"
RD"a[88:91],v226 offset:6144\n"
MF"v[60:63],a[124:127],v[136:139],v[60:63]\n"
"s_mov_b32 m0,s40\n"
MF"v[60:63],a[120:123],v[128:131],v[60:63]\n"
RD"a[92:95],v226 offset:7168\n"
MF"v[24:27],a[64:67],v[96:99],v[24:27]\n"
WT"lgkmcnt(4)\n"
"v_readlane_b32 s44,v229,s42\n"
MF"v[24:27],a[68:71],v[104:107],v[24:27]\n"
RD"a[96:99],v227 offset:0\n"
MF"v[64:67],a[68:71],v[136:139],v[64:67]\n"
SA"s42,s42,1\n"
BL"v225,s[8:11],s43 offen offset:0 sc1 lds\n"
MF"v[64:67],a[64:67],v[128:131],v[64:67]\n"
RD"a[100:103],v227 offset:1024\n"
MF"v[28:31],a[72:75],v[96:99],v[28:31]\n"
MF"v[28:31],a[76:79],v[104:107],v[28:31]\n"
RD"a[104:107],v227 offset:2048\n"
MF"v[68:71],a[76:79],v[136:139],v[68:71]\n"
MF"v[68:71],a[72:75],v[128:131],v[68:71]\n"
WT"lgkmcnt(3)\n"
RD"a[108:111],v227 offset:3072\n"
MF"v[32:35],a[80:83],v[96:99],v[32:35]\n"
MF"v[32:35],a[84:87],v[104:107],v[32:35]\n"
RD"a[112:115],v227 offset:4096\n"
MF"v[72:75],a[84:87],v[136:139],v[72:75]\n"
BL"v225,s[8:11],s43 offen offset:1024 sc1 lds\n"
MF"v[72:75],a[80:83],v[128:131],v[72:75]\n"
RD"a[116:119],v227 offset:5120\n"
MF"v[36:39],a[88:91],v[96:99],v[36:39]\n"
MF"v[36:39],a[92:95],v[104:107],v[36:39]\n"
RD"a[120:123],v227 offset:6144\n"
MF"v[76:79],a[92:95],v[136:139],v[76:79]\n"
MF"v[76:79],a[88:91],v[128:131],v[76:79]\n"
WT"lgkmcnt(3)\n"
RD"a[124:127],v227 offset:7168\n"
MF"v[84:87],a[96:99],v[144:147],0\n"
BL"v225,s[8:11],s43 offen offset:2048 sc1 lds\n"
MF"v[84:87],a[100:103],v[152:155],v[84:87]\n"
RD"a[64:67],v227 offset:8192\n"
MF"v[164:167],a[100:103],v[184:187],0\n"
MF"v[164:167],a[96:99],v[176:179],v[164:167]\n"
RD"a[68:71],v227 offset:9216\n"
MF"v[92:95],a[104:107],v[144:147],0\n"
MF"v[92:95],a[108:111],v[152:155],v[92:95]\n"
RD"a[72:75],v227 offset:10240\n"
MF"v[172:175],a[108:111],v[184:187],0\n"
MF"v[172:175],a[104:107],v[176:179],v[172:175]\n"
WT"lgkmcnt(3)\n"
RD"a[76:79],v227 offset:11264\n"
MF"v[100:103],a[112:115],v[144:147],0\n"
BL"v225,s[8:11],s43 offen offset:3072 sc1 lds\n"
MF"v[100:103],a[116:119],v[152:155],v[100:103]\n"
RD"a[80:83],v227 offset:12288\n"
MF"v[180:183],a[116:119],v[184:187],0\n"
SA"m0,s40,16384\n"
MF"v[180:183],a[112:115],v[176:179],v[180:183]\n"
RD"a[84:87],v227 offset:13312\n"
MF"v[108:111],a[120:123],v[144:147],0\n"
MF"v[108:111],a[124:127],v[152:155],v[108:111]\n"
RD"a[88:91],v227 offset:14336\n"
MF"v[188:191],a[124:127],v[184:187],0\n"
BL"v225,s[8:11],s44 offen offset:0 sc1 lds\n"
MF"v[188:191],a[120:123],v[176:179],v[188:191]\n"
WT"lgkmcnt(3)\n"
RD"a[92:95],v227 offset:15360\n"
SA"s40,s40,32768\n"
MF"v[116:119],a[64:67],v[144:147],0\n"
"s_cmp_eq_u32 s40,s46\n"
"s_cselect_b32 s40,s47,s40\n"
MF"v[116:119],a[68:71],v[152:155],v[116:119]\n"
RD"a[96:99],v227 offset:16384\n"
MF"v[196:199],a[68:71],v[184:187],0\n"
MF"v[196:199],a[64:67],v[176:179],v[196:199]\n"
RD"a[100:103],v227 offset:17408\n"
MF"v[124:127],a[72:75],v[144:147],0\n"
MF"v[124:127],a[76:79],v[152:155],v[124:127]\n"
RD"a[104:107],v227 offset:18432\n"
MF"v[204:207],a[76:79],v[184:187],0\n"
BL"v225,s[8:11],s44 offen offset:1024 sc1 lds\n"
MF"v[204:207],a[72:75],v[176:179],v[204:207]\n"
WT"lgkmcnt(3)\n"
RD"a[108:111],v227 offset:19456\n"
MF"v[132:135],a[80:83],v[144:147],0\n"
MF"v[132:135],a[84:87],v[152:155],v[132:135]\n"
RD"a[112:115],v227 offset:20480\n"
MF"v[208:211],a[84:87],v[184:187],0\n"
MF"v[208:211],a[80:83],v[176:179],v[208:211]\n"
RD"a[116:119],v227 offset:21504\n"
MF"v[140:143],a[88:91],v[144:147],0\n"
BL"v225,s[8:11],s44 offen offset:2048 sc1 lds\n"
MF"v[140:143],a[92:95],v[152:155],v[140:143]\n"
RD"a[120:123],v227 offset:22528\n"
MF"v[212:215],a[92:95],v[184:187],0\n"
WT"vmcnt(7)\n"
"buffer_store_dwordx2 v[232:233],v230,s[8:11],s62 offen sc1\n"
MF"v[212:215],a[88:91],v[176:179],v[212:215]\n"
WT"lgkmcnt(3)\n"
RD"a[124:127],v227 offset:23552\n"
MF"v[148:151],a[96:99],v[144:147],0\n"
MF"v[148:151],a[100:103],v[152:155],v[148:151]\n"
RD"a[64:67],v227 offset:24576\n"
MF"v[216:219],a[100:103],v[184:187],0\n"
"v_readlane_b32 s43,v228,s42\n"
MF"v[216:219],a[96:99],v[176:179],v[216:219]\n"
RD"a[68:71],v227 offset:25600\n"
MF"v[156:159],a[104:107],v[144:147],0\n"
BL"v225,s[8:11],s44 offen offset:3072 sc1 lds\n"
MF"v[156:159],a[108:111],v[152:155],v[156:159]\n"
RD"a[72:75],v227 offset:26624\n"
MF"v[220:223],a[108:111],v[184:187],0\n"
"s_mov_b32 m0,s40\n"
MF"v[220:223],a[104:107],v[176:179],v[220:223]\n"
WT"lgkmcnt(3)\n"
RD"a[76:79],v227 offset:27648\n"
MF"v[84:87],a[112:115],v[160:163],v[84:87]\n"
"s_lshr_b32 s89,s31,3\n"
"v_readlane_b32 s44,v229,s42\n"
MF"v[84:87],a[116:119],v[168:171],v[84:87]\n"
"s_lshl_b32 s89,s89,4\n"
RD"a[80:83],v227 offset:28672\n"
MF"v[164:167],a[116:119],v[200:203],v[164:167]\n"
SA"s42,s42,1\n"
BL"v225,s[8:11],s43 offen offset:0 sc1 lds\n"
MF"v[164:167],a[112:115],v[192:195],v[164:167]\n"
SA"s89,s89,s30\n"
RD"a[84:87],v227 offset:29696\n"
"s_lshl_b32 s89,s89,3\n"
MF"v[92:95],a[120:123],v[160:163],v[92:95]\n"
"s_xor_b32 s23,s32,s30\n"
MF"v[92:95],a[124:127],v[168:171],v[92:95]\n"
SA"s89,s89,s23\n"
RD"a[88:91],v227 offset:30720\n"
MF"v[172:175],a[124:127],v[200:203],v[172:175]\n"
"s_xor_b32 s90,s89,36\n"
MF"v[172:175],a[120:123],v[192:195],v[172:175]\n"
WT"lgkmcnt(3)\n"
RD"a[92:95],v227 offset:31744\n"
"s_mul_i32 s88,s89,5120\n"
MF"v[100:103],a[64:67],v[160:163],v[100:103]\n"
"s_mul_i32 s22,s90,5120\n"
MF"v[100:103],a[68:71],v[168:171],v[100:103]\n"
"s_sub_u32 s90,s90,s89\n"
RD"a[96:99],v226 offset:0\n"
MF"v[180:183],a[68:71],v[200:203],v[180:183]\n"
"s_lshl_b32 s90,s90,6\n"
BL"v225,s[8:11],s43 offen offset:1024 sc1 lds\n"
MF"v[180:183],a[64:67],v[192:195],v[180:183]\n"
SA"s88,s88,61603840\n"
RD"a[100:103],v226 offset:1024\n"
MF"v[108:111],a[72:75],v[160:163],v[108:111]\n"
SA"s22,s22,61603840\n"
MF"v[108:111],a[76:79],v[168:171],v[108:111]\n"
"s_cmp_eq_u32 s23,0\n"
RD"a[104:107],v226 offset:2048\n"
"s_cselect_b32 s88,2130706432,s88\n"
MF"v[188:191],a[76:79],v[200:203],v[188:191]\n"
"s_cmp_eq_u32 s23,4\n"
"s_cselect_b32 s22,2130706432,s22\n"
MF"v[188:191],a[72:75],v[192:195],v[188:191]\n"
WT"lgkmcnt(3)\n"
RD"a[108:111],v226 offset:3072\n"
"s_lshl_b32 s89,s89,6\n"
MF"v[116:119],a[80:83],v[160:163],v[116:119]\n"
SA"s89,s89,131072\n"
BL"v225,s[8:11],s43 offen offset:2048 sc1 lds\n"
MF"v[116:119],a[84:87],v[168:171],v[116:119]\n"
RD"a[112:115],v226 offset:4096\n"
"v_bfe_u32 v239,v224,1,6\n"
MF"v[196:199],a[84:87],v[200:203],v[196:199]\n"
"v_bfe_u32 v242,v224,7,1\n"
MF"v[196:199],a[80:83],v[192:195],v[196:199]\n"
RD"a[116:119],v226 offset:5120\n"
"v_mad_u32_u24 v239,v242,s90,v239\n"
MF"v[124:127],a[88:91],v[160:163],v[124:127]\n"
"v_lshrrev_b32 v242,8,v224\n"
MF"v[124:127],a[92:95],v[168:171],v[124:127]\n"
RD"a[120:123],v226 offset:6144\n"
"v_lshl_add_u32 v239,v242,27,v239\n"
MF"v[204:207],a[92:95],v[200:203],v[204:207]\n"
MF"v[204:207],a[88:91],v[192:195],v[204:207]\n"
WT"lgkmcnt(3)\n"
RD"a[124:127],v226 offset:7168\n"
MF"v[132:135],a[96:99],v[160:163],v[132:135]\n"
BL"v225,s[8:11],s43 offen offset:3072 sc1 lds\n"
MF"v[132:135],a[100:103],v[168:171],v[132:135]\n"
RD"a[64:67],v226 offset:8192\n"
MF"v[208:211],a[100:103],v[200:203],v[208:211]\n"
SA"m0,s40,16384\n"
MF"v[208:211],a[96:99],v[192:195],v[208:211]\n"
RD"a[68:71],v226 offset:9216\n"
MF"v[140:143],a[104:107],v[160:163],v[140:143]\n"
MF"v[140:143],a[108:111],v[168:171],v[140:143]\n"
RD"a[72:75],v226 offset:10240\n"
MF"v[212:215],a[108:111],v[200:203],v[212:215]\n"
BL"v225,s[8:11],s44 offen offset:0 sc1 lds\n"
MF"v[212:215],a[104:107],v[192:195],v[212:215]\n"
WT"lgkmcnt(3)\n"
RD"a[76:79],v226 offset:11264\n"
SA"s40,s40,32768\n"
MF"v[148:151],a[112:115],v[160:163],v[148:151]\n"
"s_cmp_eq_u32 s40,s46\n"
"s_cselect_b32 s40,s47,s40\n"
MF"v[148:151],a[116:119],v[168:171],v[148:151]\n"
RD"a[80:83],v226 offset:12288\n"
MF"v[216:219],a[116:119],v[200:203],v[216:219]\n"
MF"v[216:219],a[112:115],v[192:195],v[216:219]\n"
RD"a[84:87],v226 offset:13312\n"
MF"v[156:159],a[120:123],v[160:163],v[156:159]\n"
MF"v[156:159],a[124:127],v[168:171],v[156:159]\n"
RD"a[88:91],v226 offset:14336\n"
MF"v[220:223],a[124:127],v[200:203],v[220:223]\n"
BL"v225,s[8:11],s44 offen offset:1024 sc1 lds\n"
MF"v[220:223],a[120:123],v[192:195],v[220:223]\n"
WT"lgkmcnt(3)\n"
RD"a[92:95],v226 offset:15360\n"
MF"v[0:3],a[64:67],a[0:3],v[0:3]\n"
MF"v[0:3],a[68:71],a[8:11],v[0:3]\n"
RD"a[96:99],v226 offset:16384\n"
MF"v[40:43],a[68:71],a[12:15],v[40:43]\n"
MF"v[40:43],a[64:67],a[4:7],v[40:43]\n"
RD"a[100:103],v226 offset:17408\n"
MF"v[4:7],a[72:75],a[0:3],v[4:7]\n"
BL"v225,s[8:11],s44 offen offset:2048 sc1 lds\n"
MF"v[4:7],a[76:79],a[8:11],v[4:7]\n"
RD"a[104:107],v226 offset:18432\n"
MF"v[44:47],a[76:79],a[12:15],v[44:47]\n"
MF"v[44:47],a[72:75],a[4:7],v[44:47]\n"
WT"lgkmcnt(3)\n"
RD"a[108:111],v226 offset:19456\n"
MF"v[8:11],a[80:83],a[0:3],v[8:11]\n"
MF"v[8:11],a[84:87],a[8:11],v[8:11]\n"
RD"a[112:115],v226 offset:20480\n"
MF"v[48:51],a[84:87],a[12:15],v[48:51]\n"
MF"v[48:51],a[80:83],a[4:7],v[48:51]\n"
RD"a[116:119],v226 offset:21504\n"
MF"v[12:15],a[88:91],a[0:3],v[12:15]\n"
BL"v225,s[8:11],s44 offen offset:3072 sc1 lds\n"
MF"v[12:15],a[92:95],a[8:11],v[12:15]\n"
RD"a[120:123],v226 offset:22528\n"
MF"v[52:55],a[92:95],a[12:15],v[52:55]\n"
MF"v[52:55],a[88:91],a[4:7],v[52:55]\n"
WT"lgkmcnt(3)\n"
RD"a[124:127],v226 offset:23552\n"
MF"v[16:19],a[96:99],a[0:3],v[16:19]\n"
MF"v[16:19],a[100:103],a[8:11],v[16:19]\n"
RD"a[64:67],v226 offset:24576\n"
MF"v[56:59],a[100:103],a[12:15],v[56:59]\n"
MF"v[56:59],a[96:99],a[4:7],v[56:59]\n"
RD"a[68:71],v226 offset:25600\n"
MF"v[20:23],a[104:107],a[0:3],v[20:23]\n"
MF"v[20:23],a[108:111],a[8:11],v[20:23]\n"
RD"a[72:75],v226 offset:26624\n"
MF"v[60:63],a[108:111],a[12:15],v[60:63]\n"
MF"v[60:63],a[104:107],a[4:7],v[60:63]\n"
WT"lgkmcnt(3)\n"
RD"a[76:79],v226 offset:27648\n"
MF"v[24:27],a[112:115],a[0:3],v[24:27]\n"
MF"v[24:27],a[116:119],a[8:11],v[24:27]\n"
RD"a[80:83],v226 offset:28672\n"
MF"v[64:67],a[116:119],a[12:15],v[64:67]\n"
MF"v[64:67],a[112:115],a[4:7],v[64:67]\n"
RD"a[84:87],v226 offset:29696\n"
MF"v[28:31],a[120:123],a[0:3],v[28:31]\n"
MF"v[28:31],a[124:127],a[8:11],v[28:31]\n"
RD"a[88:91],v226 offset:30720\n"
MF"v[68:71],a[124:127],a[12:15],v[68:71]\n"
MF"v[68:71],a[120:123],a[4:7],v[68:71]\n"
WT"lgkmcnt(3)\n"
RD"a[92:95],v226 offset:31744\n"
MF"v[32:35],a[64:67],a[0:3],v[32:35]\n"
"s_barrier\n"
MF"v[32:35],a[68:71],a[8:11],v[32:35]\n"
RD"a[96:99],v235 offset:0\n"
MF"v[72:75],a[68:71],a[12:15],v[72:75]\n"
MF"v[72:75],a[64:67],a[4:7],v[72:75]\n"
RD"a[100:103],v235 offset:1024\n"
MF"v[36:39],a[72:75],a[0:3],v[36:39]\n"
MF"v[36:39],a[76:79],a[8:11],v[36:39]\n"
RD"a[104:107],v235 offset:2048\n"
MF"v[76:79],a[76:79],a[12:15],v[76:79]\n"
MF"v[76:79],a[72:75],a[4:7],v[76:79]\n"
WT"lgkmcnt(3)\n"
RD"a[108:111],v235 offset:3072\n"
MF"v[0:3],a[80:83],a[16:19],v[0:3]\n"
MF"v[0:3],a[84:87],a[24:27],v[0:3]\n"
RD"a[112:115],v235 offset:4096\n"
MF"v[40:43],a[84:87],a[28:31],v[40:43]\n"
MF"v[40:43],a[80:83],a[20:23],v[40:43]\n"
RD"a[116:119],v235 offset:5120\n"
MF"v[4:7],a[88:91],a[16:19],v[4:7]\n"
MF"v[4:7],a[92:95],a[24:27],v[4:7]\n"
RD"a[120:123],v235 offset:6144\n"
MF"v[44:47],a[92:95],a[28:31],v[44:47]\n"
MF"v[44:47],a[88:91],a[20:23],v[44:47]\n"
WT"lgkmcnt(3)\n"
RD"a[124:127],v235 offset:7168\n"
MF"v[8:11],a[96:99],a[16:19],v[8:11]\n"
MF"v[8:11],a[100:103],a[24:27],v[8:11]\n"
RD"a[64:67],v235 offset:8192\n"
MF"v[48:51],a[100:103],a[28:31],v[48:51]\n"
MF"v[48:51],a[96:99],a[20:23],v[48:51]\n"
RD"a[68:71],v235 offset:9216\n"
MF"v[12:15],a[104:107],a[16:19],v[12:15]\n"
MF"v[12:15],a[108:111],a[24:27],v[12:15]\n"
RD"a[72:75],v235 offset:10240\n"
"v_bfe_u32 v238,v224,4,2\n"
MF"v[52:55],a[108:111],a[28:31],v[52:55]\n"
"s_andn2_b32 s63,s62,255\n"
"v_lshlrev_b32 v238,6,v238\n"
MF"v[52:55],a[104:107],a[20:23],v[52:55]\n"
WT"lgkmcnt(3)\n"
RD"a[76:79],v235 offset:11264\n"
MF"v[16:19],a[112:115],a[16:19],v[16:19]\n"
"buffer_load_dwordx2 v[240:241],v238,s[8:11],s63 offen sc1\n"
MF"v[16:19],a[116:119],a[24:27],v[16:19]\n"
RD"a[80:83],v235 offset:12288\n"
MF"v[56:59],a[116:119],a[28:31],v[56:59]\n"
MF"v[56:59],a[112:115],a[20:23],v[56:59]\n"
RD"a[84:87],v235 offset:13312\n"
MF"v[20:23],a[120:123],a[16:19],v[20:23]\n"
MF"v[20:23],a[124:127],a[24:27],v[20:23]\n"
RD"a[88:91],v235 offset:14336\n"
MF"v[60:63],a[124:127],a[28:31],v[60:63]\n"
MF"v[60:63],a[120:123],a[20:23],v[60:63]\n"
RD"a[92:95],v235 offset:15360\n"
"LTJ_%=:\n"
WT"lgkmcnt(7)\n"
MF"v[24:27],a[64:67],a[16:19],v[24:27]\n"
WT"lgkmcnt(3)\n"
MF"v[24:27],a[68:71],a[24:27],v[24:27]\n"
RD"a[96:99],v235 offset:16384\n"
MF"v[64:67],a[68:71],a[28:31],v[64:67]\n"
MF"v[64:67],a[64:67],a[20:23],v[64:67]\n"
RD"a[100:103],v235 offset:17408\n"
MF"v[28:31],a[72:75],a[16:19],v[28:31]\n"
MF"v[28:31],a[76:79],a[24:27],v[28:31]\n"
RD"a[104:107],v235 offset:18432\n"
MF"v[68:71],a[76:79],a[28:31],v[68:71]\n"
WT"vmcnt(1)\n"
MF"v[68:71],a[72:75],a[20:23],v[68:71]\n"
RD"a[108:111],v235 offset:19456\n"
MF"v[32:35],a[80:83],a[16:19],v[32:35]\n"
WT"lgkmcnt(3)\n"
"s_barrier\n"
MF"v[32:35],a[84:87],a[24:27],v[32:35]\n"
RD"a[112:115],v236 offset:4096\n"
MF"v[72:75],a[84:87],a[28:31],v[72:75]\n"
MF"v[72:75],a[80:83],a[20:23],v[72:75]\n"
RD"a[116:119],v236 offset:5120\n"
MF"v[36:39],a[88:91],a[16:19],v[36:39]\n"
MF"v[36:39],a[92:95],a[24:27],v[36:39]\n"
RD"a[120:123],v236 offset:6144\n"
MF"v[76:79],a[92:95],a[28:31],v[76:79]\n"
MF"v[76:79],a[88:91],a[20:23],v[76:79]\n"
RD"a[124:127],v236 offset:7168\n"
MF"v[0:3],a[96:99],a[180:183],v[0:3]\n"
WT"lgkmcnt(3)\n"
MF"v[0:3],a[100:103],a[188:191],v[0:3]\n"
RD"a[64:67],v236 offset:24576\n"
MF"v[40:43],a[100:103],a[192:195],v[40:43]\n"
MF"v[40:43],a[96:99],a[184:187],v[40:43]\n"
RD"a[68:71],v236 offset:25600\n"
MF"v[4:7],a[104:107],a[180:183],v[4:7]\n"
MF"v[4:7],a[108:111],a[188:191],v[4:7]\n"
RD"a[72:75],v236 offset:26624\n"
MF"v[44:47],a[108:111],a[192:195],v[44:47]\n"
MF"v[44:47],a[104:107],a[184:187],v[44:47]\n"
RD"a[76:79],v236 offset:27648\n"
MF"v[0:3],a[112:115],a[196:199],v[0:3]\n"
WT"lgkmcnt(3)\n"
MF"v[0:3],a[116:119],a[204:207],v[0:3]\n"
RD"a[80:83],v237 offset:12288\n"
MF"v[40:43],a[116:119],a[208:211],v[40:43]\n"
MF"v[40:43],a[112:115],a[200:203],v[40:43]\n"
RD"a[84:87],v237 offset:13312\n"
MF"v[4:7],a[120:123],a[196:199],v[4:7]\n"
MF"v[4:7],a[124:127],a[204:207],v[4:7]\n"
SA"s48,s60,32768\n"
RD"a[88:91],v237 offset:14336\n"
MF"v[44:47],a[124:127],a[208:211],v[44:47]\n"
"s_and_b32 s48,s48,0x18000\n"
MF"v[44:47],a[120:123],a[200:203],v[44:47]\n"
"s_andn2_b32 s49,s60,0x18000\n"
RD"a[92:95],v237 offset:15360\n"
MF"v[0:3],a[64:67],a[212:215],v[0:3]\n"
WT"lgkmcnt(3)\n"
WT"vmcnt(0)\n"
"v_cmp_eq_u64 vcc,s[28:29],v[240:241]\n"
"s_cmp_eq_u64 vcc,exec\n"
"s_cbranch_scc0 L1083_%=\n"
"L1084_%=:\n"
MF"v[0:3],a[68:71],a[220:223],v[0:3]\n"
"s_or_b32 s52,s48,s49\n"
RD"a[96:99],v235 offset:20480\n"
SA"s53,s52,16384\n"
MF"v[40:43],a[68:71],a[224:227],v[40:43]\n"
BL"a[244:247],v224,s[8:11],s52 offen offset:0 sc1\n"
MF"v[40:43],a[64:67],a[216:219],v[40:43]\n"
RD"a[100:103],v235 offset:21504\n"
MF"v[4:7],a[72:75],a[212:215],v[4:7]\n"
MF"v[4:7],a[76:79],a[220:223],v[4:7]\n"
RD"a[104:107],v235 offset:22528\n"
MF"v[44:47],a[76:79],a[224:227],v[44:47]\n"
"v_bfe_u32 v243,v224,6,2\n"
MF"v[44:47],a[72:75],a[216:219],v[44:47]\n"
RD"a[108:111],v235 offset:23552\n"
"v_bfe_u32 v244,v224,4,2\n"
MF"v[0:3],a[80:83],a[228:231],v[0:3]\n"
WT"lgkmcnt(3)\n"
BL"a[248:251],v224,s[8:11],s53 offen offset:0 sc1\n"
MF"v[0:3],a[84:87],a[236:239],v[0:3]\n"
RD"a[112:115],v236 offset:8192\n"
"v_lshrrev_b32 v245,8,v224\n"
MF"v[40:43],a[84:87],a[240:243],v[40:43]\n"
"v_lshlrev_b32 v245,3,v245\n"
"v_sub_u32 v244,v244,v245\n"
MF"v[40:43],a[80:83],a[232:235],v[40:43]\n"
RD"a[116:119],v236 offset:9216\n"
"v_and_b32 v245,16,v224\n"
MF"v[4:7],a[88:91],a[228:231],v[4:7]\n"
"v_mov_b32 v246,0x3f80\n"
MF"v[4:7],a[92:95],a[236:239],v[4:7]\n"
RD"a[120:123],v236 offset:10240\n"
"v_lshlrev_b32 v245,v245,v246\n"
MF"v[44:47],a[92:95],a[240:243],v[44:47]\n"
BL"a[252:255],v224,s[8:11],s52 offen offset:1024 sc1\n"
MF"v[44:47],a[88:91],a[232:235],v[44:47]\n"
RD"a[124:127],v236 offset:11264\n"
"v_lshlrev_b32 v246,4,v243\n"
MF"v[8:11],a[96:99],a[180:183],v[8:11]\n"
WT"lgkmcnt(3)\n"
"v_and_b32 v246,16,v246\n"
"v_and_b32 v247,2,v243\n"
MF"v[8:11],a[100:103],a[188:191],v[8:11]\n"
SA"s90,s88,0\n"
RD"a[64:67],v236 offset:28672\n"
PK"v0,v0,v1\n"
MF"v[48:51],a[100:103],a[192:195],v[48:51]\n"
BL"a[128:131],v224,s[8:11],s53 offen offset:1024 sc1\n"
MF"v[48:51],a[96:99],a[184:187],v[48:51]\n"
RD"a[68:71],v236 offset:29696\n"
PK"v1,v2,v3\n"
MF"v[12:15],a[104:107],a[180:183],v[12:15]\n"
"v_lshl_add_u32 v246,v247,1,v246\n"
PK"v2,v4,v5\n"
MF"v[12:15],a[108:111],a[188:191],v[12:15]\n"
RD"a[72:75],v236 offset:30720\n"
PK"v3,v6,v7\n"
"v_add3_u32 v246,v246,v244,0\n"
MF"v[52:55],a[108:111],a[192:195],v[52:55]\n"
BS"v[0:3],v224,s[8:11],s90 offen sc1\n"
"v_ashrrev_i32 v246,1,v246\n"
MF"v[52:55],a[104:107],a[184:187],v[52:55]\n"
RD"a[76:79],v236 offset:31744\n"
"v_cmp_eq_u32_e64 vcc,0,v246\n"
MF"v[8:11],a[112:115],a[196:199],v[8:11]\n"
WT"lgkmcnt(3)\n"
BL"a[132:135],v224,s[8:11],s52 offen offset:2048 sc1\n"
"v_cndmask_b32_e64 v80,0,v245,vcc\n"
MF"v[8:11],a[116:119],a[204:207],v[8:11]\n"
SA"s91,s22,0\n"
RD"a[80:83],v237 offset:16384\n"
"v_cmp_eq_u32_e64 s[92:93],1,v246\n"
MF"v[48:51],a[116:119],a[208:211],v[48:51]\n"
"v_cmp_eq_u32_e64 s[94:95],2,v246\n"
"v_cndmask_b32_e64 v81,0,v245,s[92:93]\n"
MF"v[48:51],a[112:115],a[200:203],v[48:51]\n"
RD"a[84:87],v237 offset:17408\n"
PK"v40,v40,v41\n"
MF"v[12:15],a[120:123],a[196:199],v[12:15]\n"
PK"v41,v42,v43\n"
"v_cndmask_b32_e64 v82,0,v245,s[94:95]\n"
MF"v[12:15],a[124:127],a[204:207],v[12:15]\n"
RD"a[88:91],v237 offset:18432\n"
PK"v42,v44,v45\n"
PK"v43,v46,v47\n"
MF"v[52:55],a[124:127],a[208:211],v[52:55]\n"
BS"v[40:43],v224,s[8:11],s91 offen sc1\n"
"v_cmp_eq_u32_e64 vcc,3,v246\n"
MF"v[52:55],a[120:123],a[200:203],v[52:55]\n"
BL"a[136:139],v224,s[8:11],s53 offen offset:2048 sc1\n"
"v_cndmask_b32_e64 v83,0,v245,vcc\n"
MF"v[8:11],a[64:67],a[212:215],v[8:11]\n"
WT"lgkmcnt(2)\n"
RD"a[92:95],v237 offset:19456\n"
"v_lshlrev_b32 v246,4,v243\n"
"v_and_b32 v246,16,v246\n"
MF"v[8:11],a[68:71],a[220:223],v[8:11]\n"
RD"a[96:99],v235 offset:24576\n"
"v_and_b32 v247,2,v243\n"
MF"v[48:51],a[68:71],a[224:227],v[48:51]\n"
"v_lshl_add_u32 v246,v247,1,v246\n"
"v_add3_u32 v246,v246,v244,8\n"
MF"v[48:51],a[64:67],a[216:219],v[48:51]\n"
RD"a[100:103],v235 offset:25600\n"
"v_ashrrev_i32 v246,1,v246\n"
MF"v[12:15],a[72:75],a[212:215],v[12:15]\n"
BL"a[140:143],v224,s[8:11],s52 offen offset:3072 sc1\n"
MF"v[12:15],a[76:79],a[220:223],v[12:15]\n"
RD"a[104:107],v235 offset:26624\n"
"v_cmp_eq_u32_e64 vcc,0,v246\n"
MF"v[52:55],a[76:79],a[224:227],v[52:55]\n"
"v_cmp_eq_u32_e64 s[92:93],1,v246\n"
"v_cndmask_b32_e64 v88,0,v245,vcc\n"
MF"v[52:55],a[72:75],a[216:219],v[52:55]\n"
RD"a[108:111],v235 offset:27648\n"
"v_cndmask_b32_e64 v89,0,v245,s[92:93]\n"
MF"v[8:11],a[80:83],a[228:231],v[8:11]\n"
WT"lgkmcnt(3)\n"
BL"a[144:147],v224,s[8:11],s53 offen offset:3072 sc1\n"
MF"v[8:11],a[84:87],a[236:239],v[8:11]\n"
SA"s48,s60,65536\n"
RD"a[112:115],v236 offset:12288\n"
"v_cmp_eq_u32_e64 s[94:95],2,v246\n"
MF"v[48:51],a[84:87],a[240:243],v[48:51]\n"
"s_and_b32 s48,s48,0x18000\n"
"v_cndmask_b32_e64 v90,0,v245,s[94:95]\n"
"v_cmp_eq_u32_e64 vcc,3,v246\n"
MF"v[48:51],a[80:83],a[232:235],v[48:51]\n"
"s_andn2_b32 s49,s60,0x18000\n"
RD"a[116:119],v236 offset:13312\n"
"v_cndmask_b32_e64 v91,0,v245,vcc\n"
MF"v[12:15],a[88:91],a[228:231],v[12:15]\n"
"s_or_b32 s52,s48,s49\n"
"v_lshl_add_u32 v246,v243,3,v244\n"
"v_add_u32 v246,0,v246\n"
MF"v[12:15],a[92:95],a[236:239],v[12:15]\n"
SA"s53,s52,16384\n"
RD"a[120:123],v236 offset:14336\n"
"v_ashrrev_i32 v246,1,v246\n"
MF"v[52:55],a[92:95],a[240:243],v[52:55]\n"
BL"a[148:151],v224,s[8:11],s52 offen offset:0 sc1\n"
MF"v[52:55],a[88:91],a[232:235],v[52:55]\n"
RD"a[124:127],v236 offset:15360\n"
"v_cmp_eq_u32_e64 vcc,0,v246\n"
MF"v[16:19],a[96:99],a[180:183],v[16:19]\n"
WT"lgkmcnt(3)\n"
"v_cndmask_b32_e64 v96,0,v245,vcc\n"
"v_cmp_eq_u32_e64 s[92:93],1,v246\n"
MF"v[16:19],a[100:103],a[188:191],v[16:19]\n"
SA"s90,s88,1024\n"
RD"a[64:67],v237 offset:0\n"
"v_cndmask_b32_e64 v97,0,v245,s[92:93]\n"
MF"v[56:59],a[100:103],a[192:195],v[56:59]\n"
PK"v8,v8,v9\n"
PK"v9,v10,v11\n"
MF"v[56:59],a[96:99],a[184:187],v[56:59]\n"
RD"a[68:71],v237 offset:1024\n"
"v_cmp_eq_u32_e64 s[94:95],2,v246\n"
MF"v[20:23],a[104:107],a[180:183],v[20:23]\n"
BL"a[152:155],v224,s[8:11],s53 offen offset:0 sc1\n"
MF"v[20:23],a[108:111],a[188:191],v[20:23]\n"
RD"a[72:75],v237 offset:2048\n"
PK"v10,v12,v13\n"
PK"v11,v14,v15\n"
MF"v[60:63],a[108:111],a[192:195],v[60:63]\n"
BS"v[8:11],v224,s[8:11],s90 offen sc1\n"
"v_cndmask_b32_e64 v98,0,v245,s[94:95]\n"
MF"v[60:63],a[104:107],a[184:187],v[60:63]\n"
RD"a[76:79],v237 offset:3072\n"
"v_cmp_eq_u32_e64 vcc,3,v246\n"
MF"v[16:19],a[112:115],a[196:199],v[16:19]\n"
WT"lgkmcnt(3)\n"
"v_cndmask_b32_e64 v99,0,v245,vcc\n"
"v_lshl_add_u32 v246,v243,3,v244\n"
MF"v[16:19],a[116:119],a[204:207],v[16:19]\n"
SA"s91,s22,1024\n"
RD"a[80:83],v237 offset:20480\n"
"v_add_u32 v246,4,v246\n"
MF"v[56:59],a[116:119],a[208:211],v[56:59]\n"
BL"a[156:159],v224,s[8:11],s52 offen offset:1024 sc1\n"
"v_ashrrev_i32 v246,1,v246\n"
MF"v[56:59],a[112:115],a[200:203],v[56:59]\n"
RD"a[84:87],v237 offset:21504\n"
"v_cmp_eq_u32_e64 vcc,0,v246\n"
MF"v[20:23],a[120:123],a[196:199],v[20:23]\n"
PK"v48,v48,v49\n"
PK"v49,v50,v51\n"
"v_cndmask_b32_e64 v104,0,v245,vcc\n"
MF"v[20:23],a[124:127],a[204:207],v[20:23]\n"
RD"a[88:91],v237 offset:22528\n"
PK"v50,v52,v53\n"
PK"v51,v54,v55\n"
MF"v[60:63],a[124:127],a[208:211],v[60:63]\n"
BS"v[48:51],v224,s[8:11],s91 offen sc1\n"
"v_cmp_eq_u32_e64 s[92:93],1,v246\n"
MF"v[60:63],a[120:123],a[200:203],v[60:63]\n"
BL"a[160:163],v224,s[8:11],s53 offen offset:1024 sc1\n"
"v_cndmask_b32_e64 v105,0,v245,s[92:93]\n"
MF"v[16:19],a[64:67],a[212:215],v[16:19]\n"
WT"lgkmcnt(2)\n"
RD"a[92:95],v237 offset:23552\n"
"v_cmp_eq_u32_e64 s[94:95],2,v246\n"
MF"v[16:19],a[68:71],a[220:223],v[16:19]\n"
"s_mov_b32 s12,s64\n"
RD"a[96:99],v235 offset:28672\n"
"v_cndmask_b32_e64 v106,0,v245,s[94:95]\n"
"v_cmp_eq_u32_e64 vcc,3,v246\n"
MF"v[56:59],a[68:71],a[224:227],v[56:59]\n"
"s_and_b32 s13,s65,0xffff\n"
"v_cndmask_b32_e64 v107,0,v245,vcc\n"
"s_mov_b32 s14,2621440\n"
MF"v[56:59],a[64:67],a[216:219],v[56:59]\n"
"s_mov_b32 s15,0x20000\n"
RD"a[100:103],v235 offset:29696\n"
MF"v[20:23],a[72:75],a[212:215],v[20:23]\n"
"s_mov_b32 s16,s86\n"
BL"a[164:167],v224,s[8:11],s52 offen offset:2048 sc1\n"
MF"v[20:23],a[76:79],a[220:223],v[20:23]\n"
"s_and_b32 s17,s87,0xffff\n"
RD"a[104:107],v235 offset:30720\n"
MF"v[60:63],a[76:79],a[224:227],v[60:63]\n"
"s_mov_b32 s18,2621440\n"
MF"v[60:63],a[72:75],a[216:219],v[60:63]\n"
"s_mov_b32 s19,0x20000\n"
RD"a[108:111],v235 offset:31744\n"
MF"v[16:19],a[80:83],a[228:231],v[16:19]\n"
WT"lgkmcnt(3)\n"
"v_bfe_u32 v243,v224,4,4\n"
MF"v[16:19],a[84:87],a[236:239],v[16:19]\n"
"s_lshl_b32 s92,s34,3\n"
RD"a[112:115],v236 offset:16384\n"
MF"v[56:59],a[84:87],a[240:243],v[56:59]\n"
SA"s93,s92,s32\n"
BL"a[168:171],v224,s[8:11],s53 offen offset:2048 sc1\n"
MF"v[56:59],a[80:83],a[232:235],v[56:59]\n"
"s_sub_u32 s92,s32,s92\n"
RD"a[116:119],v236 offset:17408\n"
MF"v[20:23],a[88:91],a[228:231],v[20:23]\n"
SA"s92,s92,120\n"
MF"v[20:23],a[92:95],a[236:239],v[20:23]\n"
RD"a[120:123],v236 offset:18432\n"
"v_lshl_add_u32 v244,s93,4,v243\n"
MF"v[60:63],a[92:95],a[240:243],v[60:63]\n"
"v_lshl_add_u32 v245,s92,4,v243\n"
MF"v[60:63],a[88:91],a[232:235],v[60:63]\n"
"s_mul_i32 s93,s93,20480\n"
RD"a[124:127],v236 offset:19456\n"
MF"v[24:27],a[96:99],a[180:183],v[24:27]\n"
WT"lgkmcnt(3)\n"
BL"a[172:175],v224,s[8:11],s52 offen offset:3072 sc1\n"
MF"v[24:27],a[100:103],a[188:191],v[24:27]\n"
"s_mul_i32 s92,s92,20480\n"
RD"a[64:67],v237 offset:4096\n"
PK"v16,v16,v17\n"
MF"v[64:67],a[100:103],a[192:195],v[64:67]\n"
SA"s90,s88,2048\n"
PK"v17,v18,v19\n"
PK"v18,v20,v21\n"
MF"v[64:67],a[96:99],a[184:187],v[64:67]\n"
SA"s93,s93,452608\n"
RD"a[68:71],v237 offset:5120\n"
PK"v19,v22,v23\n"
MF"v[28:31],a[104:107],a[180:183],v[28:31]\n"
SA"s92,s92,452608\n"
BL"a[176:179],v224,s[8:11],s53 offen offset:3072 sc1\n"
MF"v[28:31],a[108:111],a[188:191],v[28:31]\n"
SA"s48,s60,98304\n"
BS"v[16:19],v224,s[8:11],s90 offen sc1\n"
MF"v[68:71],a[108:111],a[192:195],v[68:71]\n"
"s_and_b32 s48,s48,0x18000\n"
RD"a[72:75],v237 offset:6144\n"
"v_add_u32 v178,s93,v224\n"
MF"v[68:71],a[104:107],a[184:187],v[68:71]\n"
"s_andn2_b32 s49,s60,0x18000\n"
RD"a[76:79],v237 offset:7168\n"
"v_add_u32 v179,s92,v224\n"
MF"v[24:27],a[112:115],a[196:199],v[24:27]\n"
WT"lgkmcnt(3)\n"
"v_lshrrev_b32 v243,4,v224\n"
"v_and_b32 v243,48,v243\n"
MF"v[24:27],a[116:119],a[204:207],v[24:27]\n"
"s_or_b32 s52,s48,s49\n"
RD"a[80:83],v237 offset:24576\n"
"v_mul_u32_u24 v176,1280,v244\n"
MF"v[64:67],a[116:119],a[208:211],v[64:67]\n"
SA"s53,s52,16384\n"
BL"a[32:35],v224,s[8:11],s52 offen offset:0 sc1\n"
MF"v[64:67],a[112:115],a[200:203],v[64:67]\n"
SA"s91,s22,2048\n"
RD"a[84:87],v237 offset:25600\n"
"v_mul_u32_u24 v177,1280,v245\n"
MF"v[28:31],a[120:123],a[196:199],v[28:31]\n"
PK"v56,v56,v57\n"
PK"v57,v58,v59\n"
"v_add_u32 v176,v176,v243\n"
MF"v[28:31],a[124:127],a[204:207],v[28:31]\n"
RD"a[88:91],v237 offset:26624\n"
PK"v58,v60,v61\n"
PK"v59,v62,v63\n"
MF"v[68:71],a[124:127],a[208:211],v[68:71]\n"
BS"v[56:59],v224,s[8:11],s91 offen sc1\n"
"v_add_u32 v177,v177,v243\n"
MF"v[68:71],a[120:123],a[200:203],v[68:71]\n"
RD"a[92:95],v237 offset:27648\n"
"v_and_b32 v243,0x300,v224\n"
"v_sub_u32 v244,2047,v244\n"
MF"v[24:27],a[64:67],a[212:215],v[24:27]\n"
WT"lgkmcnt(3)\n"
BL"a[36:39],v224,s[8:11],s53 offen offset:0 sc1\n"
"v_sub_u32 v245,2047,v245\n"
MF"v[24:27],a[68:71],a[220:223],v[24:27]\n"
RD"a[96:99],v236 offset:0\n"
"v_readlane_b32 s92,v234,0\n"
MF"v[64:67],a[68:71],a[224:227],v[64:67]\n"
"s_and_b32 s92,s92,0xffff\n"
"v_cmp_eq_u32 vcc,s92,v244\n"
"v_cmp_eq_u32_e64 s[94:95],s92,v245\n"
MF"v[64:67],a[64:67],a[216:219],v[64:67]\n"
RD"a[100:103],v236 offset:1024\n"
"v_cndmask_b32 v178,v178,v243,vcc\n"
MF"v[28:31],a[72:75],a[212:215],v[28:31]\n"
"v_cndmask_b32_e64 v179,v179,v243,s[94:95]\n"
"v_add_u32 v243,16,v243\n"
MF"v[28:31],a[76:79],a[220:223],v[28:31]\n"
RD"a[104:107],v236 offset:2048\n"
"v_readlane_b32 s92,v234,1\n"
MF"v[68:71],a[76:79],a[224:227],v[68:71]\n"
"s_and_b32 s92,s92,0xffff\n"
BL"a[40:43],v224,s[8:11],s52 offen offset:1024 sc1\n"
MF"v[68:71],a[72:75],a[216:219],v[68:71]\n"
RD"a[108:111],v236 offset:3072\n"
"v_cmp_eq_u32 vcc,s92,v244\n"
MF"v[24:27],a[80:83],a[228:231],v[24:27]\n"
WT"lgkmcnt(3)\n"
"v_cmp_eq_u32_e64 s[94:95],s92,v245\n"
MF"v[24:27],a[84:87],a[236:239],v[24:27]\n"
RD"a[112:115],v236 offset:20480\n"
"v_cndmask_b32 v178,v178,v243,vcc\n"
MF"v[64:67],a[84:87],a[240:243],v[64:67]\n"
BL"a[44:47],v224,s[8:11],s53 offen offset:1024 sc1\n"
MF"v[64:67],a[80:83],a[232:235],v[64:67]\n"
RD"a[116:119],v236 offset:21504\n"
"v_cndmask_b32_e64 v179,v179,v243,s[94:95]\n"
MF"v[28:31],a[88:91],a[228:231],v[28:31]\n"
"v_add_u32 v243,16,v243\n"
"v_readlane_b32 s92,v234,2\n"
MF"v[28:31],a[92:95],a[236:239],v[28:31]\n"
"s_and_b32 s92,s92,0xffff\n"
RD"a[120:123],v236 offset:22528\n"
MF"v[68:71],a[92:95],a[240:243],v[68:71]\n"
"v_cmp_eq_u32 vcc,s92,v244\n"
MF"v[68:71],a[88:91],a[232:235],v[68:71]\n"
RD"a[124:127],v236 offset:23552\n"
"v_cmp_eq_u32_e64 s[94:95],s92,v245\n"
MF"v[32:35],a[96:99],a[180:183],v[32:35]\n"
WT"lgkmcnt(3)\n"
BL"a[48:51],v224,s[8:11],s52 offen offset:2048 sc1\n"
MF"v[32:35],a[100:103],a[188:191],v[32:35]\n"
SA"s90,s88,3072\n"
RD"a[64:67],v237 offset:8192\n"
"v_cndmask_b32 v178,v178,v243,vcc\n"
MF"v[72:75],a[100:103],a[192:195],v[72:75]\n"
PK"v24,v24,v25\n"
PK"v25,v26,v27\n"
MF"v[72:75],a[96:99],a[184:187],v[72:75]\n"
RD"a[68:71],v237 offset:9216\n"
"v_cndmask_b32_e64 v179,v179,v243,s[94:95]\n"
MF"v[36:39],a[104:107],a[180:183],v[36:39]\n"
PK"v26,v28,v29\n"
PK"v27,v30,v31\n"
MF"v[36:39],a[108:111],a[188:191],v[36:39]\n"
BS"v[24:27],v224,s[8:11],s90 offen sc1\n"
"v_add_u32 v243,16,v243\n"
MF"v[76:79],a[108:111],a[192:195],v[76:79]\n"
RD"a[72:75],v237 offset:10240\n"
"v_readlane_b32 s92,v234,3\n"
MF"v[76:79],a[104:107],a[184:187],v[76:79]\n"
"s_and_b32 s92,s92,0xffff\n"
BL"a[52:55],v224,s[8:11],s53 offen offset:2048 sc1\n"
MF"v[32:35],a[112:115],a[196:199],v[32:35]\n"
WT"lgkmcnt(2)\n"
RD"a[76:79],v237 offset:11264\n"
"v_cmp_eq_u32 vcc,s92,v244\n"
MF"v[32:35],a[116:119],a[204:207],v[32:35]\n"
SA"s91,s22,3072\n"
RD"a[80:83],v237 offset:28672\n"
"v_cmp_eq_u32_e64 s[94:95],s92,v245\n"
MF"v[72:75],a[116:119],a[208:211],v[72:75]\n"
"v_cndmask_b32 v178,v178,v243,vcc\n"
"v_cndmask_b32_e64 v179,v179,v243,s[94:95]\n"
MF"v[72:75],a[112:115],a[200:203],v[72:75]\n"
RD"a[84:87],v237 offset:29696\n"
PK"v64,v64,v65\n"
MF"v[36:39],a[120:123],a[196:199],v[36:39]\n"
BL"a[56:59],v224,s[8:11],s52 offen offset:3072 sc1\n"
PK"v65,v66,v67\n"
MF"v[36:39],a[124:127],a[204:207],v[36:39]\n"
RD"a[88:91],v237 offset:30720\n"
"v_add_u32 v243,16,v243\n"
PK"v66,v68,v69\n"
MF"v[76:79],a[124:127],a[208:211],v[76:79]\n"
PK"v67,v70,v71\n"
"v_readlane_b32 s92,v234,4\n"
MF"v[76:79],a[120:123],a[200:203],v[76:79]\n"
"s_and_b32 s92,s92,0xffff\n"
BS"v[64:67],v224,s[8:11],s91 offen sc1\n"
"v_cmp_eq_u32 vcc,s92,v244\n"
MF"v[32:35],a[64:67],a[212:215],v[32:35]\n"
WT"lgkmcnt(2)\n"
RD"a[92:95],v237 offset:31744\n"
"v_cmp_eq_u32_e64 s[94:95],s92,v245\n"
MF"v[32:35],a[68:71],a[220:223],v[32:35]\n"
BL"a[60:63],v224,s[8:11],s53 offen offset:3072 sc1\n"
"v_cndmask_b32 v178,v178,v243,vcc\n"
MF"v[72:75],a[68:71],a[224:227],v[72:75]\n"
RD"a[96:99],v226 offset:8192\n"
"v_cndmask_b32_e64 v179,v179,v243,s[94:95]\n"
MF"v[72:75],a[64:67],a[216:219],v[72:75]\n"
RD"a[100:103],v226 offset:9216\n"
"v_add_u32 v243,16,v243\n"
MF"v[36:39],a[72:75],a[212:215],v[36:39]\n"
"v_readlane_b32 s92,v234,5\n"
"s_and_b32 s92,s92,0xffff\n"
MF"v[36:39],a[76:79],a[220:223],v[36:39]\n"
RD"a[104:107],v226 offset:10240\n"
"v_cmp_eq_u32 vcc,s92,v244\n"
MF"v[76:79],a[76:79],a[224:227],v[76:79]\n"
"v_cmp_eq_u32_e64 s[94:95],s92,v245\n"
"v_cndmask_b32 v178,v178,v243,vcc\n"
MF"v[76:79],a[72:75],a[216:219],v[76:79]\n"
RD"a[108:111],v226 offset:11264\n"
"v_cndmask_b32_e64 v179,v179,v243,s[94:95]\n"
MF"v[32:35],a[80:83],a[228:231],v[32:35]\n"
WT"lgkmcnt(4)\n"
"v_add_u32 v243,16,v243\n"
"v_readlane_b32 s92,v234,6\n"
MF"v[32:35],a[84:87],a[236:239],v[32:35]\n"
"s_and_b32 s92,s92,0xffff\n"
RD"a[112:115],v226 offset:28672\n"
MF"v[72:75],a[84:87],a[240:243],v[72:75]\n"
"v_cmp_eq_u32 vcc,s92,v244\n"
MF"v[72:75],a[80:83],a[232:235],v[72:75]\n"
RD"a[116:119],v226 offset:29696\n"
"v_cmp_eq_u32_e64 s[94:95],s92,v245\n"
MF"v[36:39],a[88:91],a[228:231],v[36:39]\n"
"v_cndmask_b32 v178,v178,v243,vcc\n"
MF"v[36:39],a[92:95],a[236:239],v[36:39]\n"
RD"a[120:123],v226 offset:30720\n"
"v_cndmask_b32_e64 v179,v179,v243,s[94:95]\n"
MF"v[76:79],a[92:95],a[240:243],v[76:79]\n"
"v_add_u32 v243,16,v243\n"
MF"v[76:79],a[88:91],a[232:235],v[76:79]\n"
WT"vmcnt(31) lgkmcnt(3)\n"
RD"a[124:127],v226 offset:31744\n"
"v_readlane_b32 s92,v234,7\n"
MF"v[84:87],a[96:99],a[244:247],v[84:87]\n"
WT"vmcnt(29)\n"
"s_and_b32 s92,s92,0xffff\n"
MF"v[84:87],a[100:103],a[252:255],v[84:87]\n"
WT"vmcnt(28)\n"
RD"a[64:67],v235 offset:16384\n"
PK"v32,v32,v33\n"
MF"v[164:167],a[100:103],a[128:131],v[164:167]\n"
SA"s90,s88,4096\n"
PK"v33,v34,v35\n"
"v_cmp_eq_u32 vcc,s92,v244\n"
MF"v[164:167],a[96:99],a[248:251],v[164:167]\n"
RD"a[68:71],v235 offset:17408\n"
PK"v34,v36,v37\n"
MF"v[92:95],a[104:107],a[244:247],v[92:95]\n"
PK"v35,v38,v39\n"
"v_cmp_eq_u32_e64 s[94:95],s92,v245\n"
MF"v[92:95],a[108:111],a[252:255],v[92:95]\n"
"s_mul_i32 s92,s33,5\n"
BS"v[32:35],v224,s[8:11],s90 offen sc1\n"
MF"v[172:175],a[108:111],a[128:131],v[172:175]\n"
SA"s93,s92,s30\n"
RD"a[72:75],v235 offset:18432\n"
"v_cndmask_b32 v178,v178,v243,vcc\n"
MF"v[172:175],a[104:107],a[248:251],v[172:175]\n"
WT"vmcnt(27) lgkmcnt(3)\n"
RD"a[76:79],v235 offset:19456\n"
"v_cndmask_b32_e64 v179,v179,v243,s[94:95]\n"
MF"v[84:87],a[112:115],a[132:135],v[84:87]\n"
WT"vmcnt(24)\n"
SA"s92,s92,4\n"
"s_lshl_b32 s38,s93,6\n"
MF"v[84:87],a[116:119],a[140:143],v[84:87]\n"
WT"vmcnt(23)\n"
RD"a[80:83],v236 offset:4096\n"
PK"v72,v72,v73\n"
MF"v[164:167],a[116:119],a[144:147],v[164:167]\n"
SA"s91,s22,4096\n"
PK"v73,v74,v75\n"
PK"v74,v76,v77\n"
MF"v[164:167],a[112:115],a[136:139],v[164:167]\n"
"s_lshl_b32 s55,s92,6\n"
RD"a[84:87],v236 offset:5120\n"
PK"v75,v78,v79\n"
MF"v[92:95],a[120:123],a[132:135],v[92:95]\n"
SA"s55,s55,s45\n"
BS"v[72:75],v224,s[8:11],s91 offen sc1\n"
MF"v[92:95],a[124:127],a[140:143],v[92:95]\n"
"s_lshl_b32 s36,s93,10\n"
RD"a[88:91],v236 offset:6144\n"
"s_lshl_b32 s37,s92,10\n"
MF"v[172:175],a[124:127],a[144:147],v[172:175]\n"
SA"s36,s36,71680\n"
SA"s37,s37,71680\n"
MF"v[172:175],a[120:123],a[136:139],v[172:175]\n"
WT"vmcnt(23) lgkmcnt(3)\n"
RD"a[92:95],v236 offset:7168\n"
SA"s37,s37,s45\n"
MF"v[84:87],a[64:67],a[148:151],v[84:87]\n"
WT"vmcnt(20)\n"
"v_lshrrev_b32 v243,1,v224\n"
MF"v[84:87],a[68:71],a[156:159],v[84:87]\n"
WT"vmcnt(18)\n"
RD"a[96:99],v236 offset:24576\n"
"v_min_u32 v244,312,v243\n"
MF"v[164:167],a[68:71],a[160:163],v[164:167]\n"
"v_and_b32 v243,56,v243\n"
MF"v[164:167],a[64:67],a[152:155],v[164:167]\n"
RD"a[100:103],v236 offset:25600\n"
"v_lshlrev_b32 v243,3,v243\n"
MF"v[92:95],a[72:75],a[148:151],v[92:95]\n"
"s_mov_b32 s92,6400\n"
MF"v[92:95],a[76:79],a[156:159],v[92:95]\n"
RD"a[104:107],v236 offset:26624\n"
MF"v[172:175],a[76:79],a[160:163],v[172:175]\n"
MF"v[172:175],a[72:75],a[152:155],v[172:175]\n"
WT"vmcnt(17) lgkmcnt(3)\n"
RD"a[108:111],v236 offset:27648\n"
MF"v[84:87],a[80:83],a[164:167],v[84:87]\n"
WT"vmcnt(15)\n"
MF"v[84:87],a[84:87],a[172:175],v[84:87]\n"
WT"vmcnt(14)\n"
RD"a[112:115],v237 offset:12288\n"
MF"v[164:167],a[84:87],a[176:179],v[164:167]\n"
MF"v[164:167],a[80:83],a[168:171],v[164:167]\n"
RD"a[116:119],v237 offset:13312\n"
MF"v[92:95],a[88:91],a[164:167],v[92:95]\n"
MF"v[92:95],a[92:95],a[172:175],v[92:95]\n"
RD"a[120:123],v237 offset:14336\n"
MF"v[172:175],a[92:95],a[176:179],v[172:175]\n"
MF"v[172:175],a[88:91],a[168:171],v[172:175]\n"
WT"vmcnt(12) lgkmcnt(3)\n"
RD"a[124:127],v237 offset:15360\n"
MF"v[84:87],a[96:99],a[32:35],v[84:87]\n"
WT"vmcnt(9)\n"
MF"v[84:87],a[100:103],a[40:43],v[84:87]\n"
WT"vmcnt(8)\n"
RD"a[64:67],v226 offset:12288\n"
MF"v[164:167],a[100:103],a[44:47],v[164:167]\n"
MF"v[164:167],a[96:99],a[36:39],v[164:167]\n"
RD"a[68:71],v226 offset:13312\n"
MF"v[92:95],a[104:107],a[32:35],v[92:95]\n"
MF"v[92:95],a[108:111],a[40:43],v[92:95]\n"
RD"a[72:75],v226 offset:14336\n"
MF"v[172:175],a[108:111],a[44:47],v[172:175]\n"
MF"v[172:175],a[104:107],a[36:39],v[172:175]\n"
WT"vmcnt(7) lgkmcnt(3)\n"
RD"a[76:79],v226 offset:15360\n"
MF"v[84:87],a[112:115],a[48:51],v[84:87]\n"
WT"vmcnt(4)\n"
"buffer_load_dwordx2 v[240:241],v244,s[8:11],s92 offen sc1\n"
MF"v[84:87],a[116:119],a[56:59],v[84:87]\n"
WT"vmcnt(3)\n"
RD"a[80:83],v235 offset:0\n"
MF"v[164:167],a[116:119],a[60:63],v[164:167]\n"
MF"v[164:167],a[112:115],a[52:55],v[164:167]\n"
RD"a[84:87],v235 offset:1024\n"
MF"v[92:95],a[120:123],a[48:51],v[92:95]\n"
MF"v[92:95],a[124:127],a[56:59],v[92:95]\n"
RD"a[88:91],v235 offset:2048\n"
MF"v[172:175],a[124:127],a[60:63],v[172:175]\n"
MF"v[172:175],a[120:123],a[52:55],v[172:175]\n"
WT"lgkmcnt(3)\n"
RD"a[92:95],v235 offset:3072\n"
MF"v[100:103],a[64:67],a[244:247],v[100:103]\n"
MF"v[100:103],a[68:71],a[252:255],v[100:103]\n"
SA"s90,s88,327680\n"
RD"a[96:99],v235 offset:20480\n"
PK"v84,v84,v85\n"
MF"v[180:183],a[68:71],a[128:131],v[180:183]\n"
PK"v85,v86,v87\n"
PK"v86,v92,v93\n"
MF"v[180:183],a[64:67],a[248:251],v[180:183]\n"
RD"a[100:103],v235 offset:21504\n"
PK"v87,v94,v95\n"
MF"v[108:111],a[72:75],a[244:247],v[108:111]\n"
BS"v[84:87],v224,s[8:11],s90 offen sc1\n"
MF"v[108:111],a[76:79],a[252:255],v[108:111]\n"
RD"a[104:107],v235 offset:22528\n"
MF"v[188:191],a[76:79],a[128:131],v[188:191]\n"
MF"v[188:191],a[72:75],a[248:251],v[188:191]\n"
WT"lgkmcnt(3)\n"
RD"a[108:111],v235 offset:23552\n"
MF"v[100:103],a[80:83],a[132:135],v[100:103]\n"
MF"v[100:103],a[84:87],a[140:143],v[100:103]\n"
SA"s91,s22,327680\n"
RD"a[112:115],v236 offset:8192\n"
PK"v164,v164,v165\n"
MF"v[180:183],a[84:87],a[144:147],v[180:183]\n"
PK"v165,v166,v167\n"
PK"v166,v172,v173\n"
MF"v[180:183],a[80:83],a[136:139],v[180:183]\n"
RD"a[116:119],v236 offset:9216\n"
PK"v167,v174,v175\n"
MF"v[108:111],a[88:91],a[132:135],v[108:111]\n"
BS"v[164:167],v224,s[8:11],s91 offen sc1\n"
MF"v[108:111],a[92:95],a[140:143],v[108:111]\n"
RD"a[120:123],v236 offset:10240\n"
MF"v[188:191],a[92:95],a[144:147],v[188:191]\n"
MF"v[188:191],a[88:91],a[136:139],v[188:191]\n"
WT"lgkmcnt(3)\n"
RD"a[124:127],v236 offset:11264\n"
MF"v[100:103],a[96:99],a[148:151],v[100:103]\n"
MF"v[100:103],a[100:103],a[156:159],v[100:103]\n"
RD"a[64:67],v236 offset:28672\n"
MF"v[180:183],a[100:103],a[160:163],v[180:183]\n"
WT"vmcnt(3)\n"
"buffer_store_dwordx2 v[232:233],v239,s[8:11],s89 offen sc1\n"
MF"v[180:183],a[96:99],a[152:155],v[180:183]\n"
RD"a[68:71],v236 offset:29696\n"
MF"v[108:111],a[104:107],a[148:151],v[108:111]\n"
MF"v[108:111],a[108:111],a[156:159],v[108:111]\n"
RD"a[72:75],v236 offset:30720\n"
MF"v[188:191],a[108:111],a[160:163],v[188:191]\n"
MF"v[188:191],a[104:107],a[152:155],v[188:191]\n"
WT"lgkmcnt(3)\n"
RD"a[76:79],v236 offset:31744\n"
MF"v[100:103],a[112:115],a[164:167],v[100:103]\n"
MF"v[100:103],a[116:119],a[172:175],v[100:103]\n"
RD"a[80:83],v237 offset:16384\n"
MF"v[180:183],a[116:119],a[176:179],v[180:183]\n"
MF"v[180:183],a[112:115],a[168:171],v[180:183]\n"
RD"a[84:87],v237 offset:17408\n"
MF"v[108:111],a[120:123],a[164:167],v[108:111]\n"
MF"v[108:111],a[124:127],a[172:175],v[108:111]\n"
RD"a[88:91],v237 offset:18432\n"
MF"v[188:191],a[124:127],a[176:179],v[188:191]\n"
MF"v[188:191],a[120:123],a[168:171],v[188:191]\n"
WT"lgkmcnt(3)\n"
RD"a[92:95],v237 offset:19456\n"
MF"v[100:103],a[64:67],a[32:35],v[100:103]\n"
MF"v[100:103],a[68:71],a[40:43],v[100:103]\n"
RD"a[96:99],v226 offset:16384\n"
MF"v[180:183],a[68:71],a[44:47],v[180:183]\n"
MF"v[180:183],a[64:67],a[36:39],v[180:183]\n"
RD"a[100:103],v226 offset:17408\n"
MF"v[108:111],a[72:75],a[32:35],v[108:111]\n"
MF"v[108:111],a[76:79],a[40:43],v[108:111]\n"
RD"a[104:107],v226 offset:18432\n"
MF"v[188:191],a[76:79],a[44:47],v[188:191]\n"
MF"v[188:191],a[72:75],a[36:39],v[188:191]\n"
WT"lgkmcnt(3)\n"
RD"a[108:111],v226 offset:19456\n"
MF"v[100:103],a[80:83],a[48:51],v[100:103]\n"
MF"v[100:103],a[84:87],a[56:59],v[100:103]\n"
RD"a[112:115],v235 offset:4096\n"
MF"v[180:183],a[84:87],a[60:63],v[180:183]\n"
MF"v[180:183],a[80:83],a[52:55],v[180:183]\n"
RD"a[116:119],v235 offset:5120\n"
MF"v[108:111],a[88:91],a[48:51],v[108:111]\n"
MF"v[108:111],a[92:95],a[56:59],v[108:111]\n"
RD"a[120:123],v235 offset:6144\n"
MF"v[188:191],a[92:95],a[60:63],v[188:191]\n"
MF"v[188:191],a[88:91],a[52:55],v[188:191]\n"
WT"lgkmcnt(3)\n"
RD"a[124:127],v235 offset:7168\n"
MF"v[116:119],a[96:99],a[244:247],v[116:119]\n"
MF"v[116:119],a[100:103],a[252:255],v[116:119]\n"
SA"s90,s88,328704\n"
RD"a[64:67],v235 offset:24576\n"
PK"v100,v100,v101\n"
MF"v[196:199],a[100:103],a[128:131],v[196:199]\n"
PK"v101,v102,v103\n"
PK"v102,v108,v109\n"
MF"v[196:199],a[96:99],a[248:251],v[196:199]\n"
RD"a[68:71],v235 offset:25600\n"
PK"v103,v110,v111\n"
MF"v[124:127],a[104:107],a[244:247],v[124:127]\n"
BS"v[100:103],v224,s[8:11],s90 offen sc1\n"
MF"v[124:127],a[108:111],a[252:255],v[124:127]\n"
WT"vmcnt(4)\n"
"v_cmp_eq_u64 vcc,s[28:29],v[240:241]\n"
"s_cmp_eq_u64 vcc,exec\n"
"s_cbranch_scc0 L1086_%=\n"
"L1087_%=:\n"
MF"v[204:207],a[108:111],a[128:131],v[204:207]\n"
RD"a[72:75],v235 offset:26624\n"
MF"v[204:207],a[104:107],a[248:251],v[204:207]\n"
WT"lgkmcnt(3)\n"
RD"a[76:79],v235 offset:27648\n"
MF"v[116:119],a[112:115],a[132:135],v[116:119]\n"
BL"v[112:115],v176,s[12:15],s38 offen\n"
MF"v[116:119],a[116:119],a[140:143],v[116:119]\n"
SA"s91,s22,328704\n"
RD"a[80:83],v236 offset:12288\n"
PK"v180,v180,v181\n"
MF"v[196:199],a[116:119],a[144:147],v[196:199]\n"
BL"v[144:147],v178,s[8:11],s36 offen sc1\n"
MF"v[196:199],a[112:115],a[136:139],v[196:199]\n"
RD"a[84:87],v236 offset:13312\n"
PK"v181,v182,v183\n"
MF"v[124:127],a[120:123],a[132:135],v[124:127]\n"
PK"v182,v188,v189\n"
PK"v183,v190,v191\n"
MF"v[124:127],a[124:127],a[140:143],v[124:127]\n"
BS"v[180:183],v224,s[8:11],s91 offen sc1\n"
MF"v[204:207],a[124:127],a[144:147],v[204:207]\n"
RD"a[88:91],v236 offset:14336\n"
MF"v[204:207],a[120:123],a[136:139],v[204:207]\n"
WT"lgkmcnt(3)\n"
BL"v[120:123],v176,s[12:15],s55 offen\n"
MF"v[116:119],a[64:67],a[148:151],v[116:119]\n"
RD"a[92:95],v236 offset:15360\n"
MF"v[116:119],a[68:71],a[156:159],v[116:119]\n"
BL"v[152:155],v178,s[8:11],s37 offen sc1\n"
MF"v[196:199],a[68:71],a[160:163],v[196:199]\n"
RD"a[96:99],v237 offset:0\n"
MF"v[196:199],a[64:67],a[152:155],v[196:199]\n"
RD"a[100:103],v237 offset:1024\n"
MF"v[124:127],a[72:75],a[148:151],v[124:127]\n"
BL"v[128:131],v177,s[12:15],s38 offen\n"
MF"v[124:127],a[76:79],a[156:159],v[124:127]\n"
RD"a[104:107],v237 offset:2048\n"
MF"v[204:207],a[76:79],a[160:163],v[204:207]\n"
BL"v[160:163],v179,s[8:11],s36 offen sc1\n"
MF"v[204:207],a[72:75],a[152:155],v[204:207]\n"
WT"lgkmcnt(3)\n"
RD"a[108:111],v237 offset:3072\n"
MF"v[116:119],a[80:83],a[164:167],v[116:119]\n"
MF"v[116:119],a[84:87],a[172:175],v[116:119]\n"
RD"a[112:115],v237 offset:20480\n"
MF"v[196:199],a[84:87],a[176:179],v[196:199]\n"
BL"v[136:139],v177,s[12:15],s55 offen\n"
MF"v[196:199],a[80:83],a[168:171],v[196:199]\n"
RD"a[116:119],v237 offset:21504\n"
MF"v[124:127],a[88:91],a[164:167],v[124:127]\n"
BL"v[168:171],v179,s[8:11],s37 offen sc1\n"
MF"v[124:127],a[92:95],a[172:175],v[124:127]\n"
RD"a[120:123],v237 offset:22528\n"
MF"v[204:207],a[92:95],a[176:179],v[204:207]\n"
MF"v[204:207],a[88:91],a[168:171],v[204:207]\n"
WT"lgkmcnt(3)\n"
RD"a[124:127],v237 offset:23552\n"
MF"v[116:119],a[96:99],a[32:35],v[116:119]\n"
MF"v[116:119],a[100:103],a[40:43],v[116:119]\n"
RD"a[64:67],v226 offset:20480\n"
MF"v[196:199],a[100:103],a[44:47],v[196:199]\n"
"s_lshr_b32 s93,s31,3\n"
MF"v[196:199],a[96:99],a[36:39],v[196:199]\n"
"s_lshl_b32 s93,s93,4\n"
RD"a[68:71],v226 offset:21504\n"
MF"v[124:127],a[104:107],a[32:35],v[124:127]\n"
SA"s93,s93,s32\n"
MF"v[124:127],a[108:111],a[40:43],v[124:127]\n"
"s_mul_i32 s94,s93,40960\n"
RD"a[72:75],v226 offset:22528\n"
MF"v[204:207],a[108:111],a[44:47],v[204:207]\n"
"s_lshl_b32 s93,s93,9\n"
MF"v[204:207],a[104:107],a[36:39],v[204:207]\n"
WT"lgkmcnt(3)\n"
RD"a[76:79],v226 offset:23552\n"
SA"s93,s93,131072\n"
MF"v[116:119],a[112:115],a[48:51],v[116:119]\n"
SA"s94,s94,61603840\n"
MF"v[116:119],a[116:119],a[56:59],v[116:119]\n"
"s_lshl_b32 s92,s30,10\n"
RD"a[80:83],v235 offset:8192\n"
MF"v[196:199],a[116:119],a[60:63],v[196:199]\n"
"v_add_u32 v244,s92,v224\n"
MF"v[196:199],a[112:115],a[52:55],v[196:199]\n"
SA"s92,s45,4096\n"
RD"a[84:87],v235 offset:9216\n"
MF"v[124:127],a[120:123],a[48:51],v[124:127]\n"
"v_add_u32 v245,s92,v224\n"
MF"v[124:127],a[124:127],a[56:59],v[124:127]\n"
RD"a[88:91],v235 offset:10240\n"
MF"v[204:207],a[124:127],a[60:63],v[204:207]\n"
"v_and_b32 v246,128,v224\n"
MF"v[204:207],a[120:123],a[52:55],v[204:207]\n"
WT"lgkmcnt(3)\n"
RD"a[92:95],v235 offset:11264\n"
"v_lshl_add_u32 v243,v246,5,v243\n"
MF"v[132:135],a[64:67],a[244:247],v[132:135]\n"
"v_lshrrev_b32 v246,8,v224\n"
MF"v[132:135],a[68:71],a[252:255],v[132:135]\n"
SA"s90,s88,329728\n"
RD"a[96:99],v235 offset:28672\n"
PK"v116,v116,v117\n"
MF"v[208:211],a[68:71],a[128:131],v[208:211]\n"
"s_mov_b32 s95,0xffff0101\n"
PK"v117,v118,v119\n"
"v_lshl_add_u32 v243,v246,27,v243\n"
MF"v[208:211],a[64:67],a[248:251],v[208:211]\n"
"s_lshl_b32 s92,s30,10\n"
RD"a[100:103],v235 offset:29696\n"
PK"v118,v124,v125\n"
MF"v[140:143],a[72:75],a[244:247],v[140:143]\n"
PK"v119,v126,v127\n"
"v_add_u32 v247,s92,v227\n"
MF"v[140:143],a[76:79],a[252:255],v[140:143]\n"
BS"v[116:119],v224,s[8:11],s90 offen sc1\n"
MF"v[212:215],a[76:79],a[128:131],v[212:215]\n"
RD"a[104:107],v235 offset:30720\n"
MF"v[212:215],a[72:75],a[248:251],v[212:215]\n"
WT"lgkmcnt(3)\n"
"buffer_load_dwordx2 v[240:241],v243,s[8:11],s93 offen sc1\n"
MF"v[132:135],a[80:83],a[132:135],v[132:135]\n"
RD"a[108:111],v235 offset:31744\n"
MF"v[132:135],a[84:87],a[140:143],v[132:135]\n"
SA"s91,s22,329728\n"
RD"a[112:115],v236 offset:16384\n"
PK"v196,v196,v197\n"
MF"v[208:211],a[84:87],a[144:147],v[208:211]\n"
PK"v197,v198,v199\n"
PK"v198,v204,v205\n"
MF"v[208:211],a[80:83],a[136:139],v[208:211]\n"
RD"a[116:119],v236 offset:17408\n"
PK"v199,v206,v207\n"
MF"v[140:143],a[88:91],a[132:135],v[140:143]\n"
BS"v[196:199],v224,s[8:11],s91 offen sc1\n"
MF"v[140:143],a[92:95],a[140:143],v[140:143]\n"
RD"a[120:123],v236 offset:18432\n"
MF"v[212:215],a[92:95],a[144:147],v[212:215]\n"
MF"v[212:215],a[88:91],a[136:139],v[212:215]\n"
WT"lgkmcnt(3)\n"
RD"a[124:127],v236 offset:19456\n"
MF"v[132:135],a[96:99],a[148:151],v[132:135]\n"
MF"v[132:135],a[100:103],a[156:159],v[132:135]\n"
RD"a[64:67],v237 offset:4096\n"
MF"v[208:211],a[100:103],a[160:163],v[208:211]\n"
MF"v[208:211],a[96:99],a[152:155],v[208:211]\n"
RD"a[68:71],v237 offset:5120\n"
MF"v[140:143],a[104:107],a[148:151],v[140:143]\n"
MF"v[140:143],a[108:111],a[156:159],v[140:143]\n"
RD"a[72:75],v237 offset:6144\n"
MF"v[212:215],a[108:111],a[160:163],v[212:215]\n"
MF"v[212:215],a[104:107],a[152:155],v[212:215]\n"
WT"lgkmcnt(3)\n"
RD"a[76:79],v237 offset:7168\n"
MF"v[132:135],a[112:115],a[164:167],v[132:135]\n"
MF"v[132:135],a[116:119],a[172:175],v[132:135]\n"
RD"a[80:83],v237 offset:24576\n"
MF"v[208:211],a[116:119],a[176:179],v[208:211]\n"
MF"v[208:211],a[112:115],a[168:171],v[208:211]\n"
RD"a[84:87],v237 offset:25600\n"
MF"v[140:143],a[120:123],a[164:167],v[140:143]\n"
MF"v[140:143],a[124:127],a[172:175],v[140:143]\n"
RD"a[88:91],v237 offset:26624\n"
MF"v[212:215],a[124:127],a[176:179],v[212:215]\n"
MF"v[212:215],a[120:123],a[168:171],v[212:215]\n"
WT"lgkmcnt(3)\n"
RD"a[92:95],v237 offset:27648\n"
MF"v[132:135],a[64:67],a[32:35],v[132:135]\n"
MF"v[132:135],a[68:71],a[40:43],v[132:135]\n"
RD"a[96:99],v226 offset:24576\n"
MF"v[208:211],a[68:71],a[44:47],v[208:211]\n"
MF"v[208:211],a[64:67],a[36:39],v[208:211]\n"
RD"a[100:103],v226 offset:25600\n"
MF"v[140:143],a[72:75],a[32:35],v[140:143]\n"
MF"v[140:143],a[76:79],a[40:43],v[140:143]\n"
RD"a[104:107],v226 offset:26624\n"
MF"v[212:215],a[76:79],a[44:47],v[212:215]\n"
MF"v[212:215],a[72:75],a[36:39],v[212:215]\n"
WT"lgkmcnt(3)\n"
RD"a[108:111],v226 offset:27648\n"
MF"v[132:135],a[80:83],a[48:51],v[132:135]\n"
MF"v[132:135],a[84:87],a[56:59],v[132:135]\n"
RD"a[112:115],v235 offset:12288\n"
MF"v[208:211],a[84:87],a[60:63],v[208:211]\n"
WT"vmcnt(1)\n"
"v_cmp_eq_u64 vcc,s[28:29],v[240:241]\n"
MF"v[208:211],a[80:83],a[52:55],v[208:211]\n"
"s_and_b32 s50,vcc_lo,0xff\n"
RD"a[116:119],v235 offset:13312\n"
"s_or_b32 s95,s95,s50\n"
MF"v[140:143],a[88:91],a[48:51],v[140:143]\n"
"s_bitcmp1_b32 s95,1\n"
"s_cselect_b32 s51,s94,1065353216\n"
MF"v[140:143],a[92:95],a[56:59],v[140:143]\n"
SA"s92,s51,5120\n"
RD"a[120:123],v235 offset:14336\n"
MF"v[212:215],a[92:95],a[60:63],v[212:215]\n"
BL"a[184:187],v244,s[8:11],s92 offen sc1\n"
MF"v[212:215],a[88:91],a[52:55],v[212:215]\n"
WT"lgkmcnt(3)\n"
RD"a[124:127],v235 offset:15360\n"
MF"v[148:151],a[96:99],a[244:247],v[148:151]\n"
BL"a[216:219],v245,s[8:11],s92 offen sc1\n"
MF"v[148:151],a[100:103],a[252:255],v[148:151]\n"
SA"s90,s88,330752\n"
RD"a[64:67],v236 offset:0\n"
PK"v132,v132,v133\n"
MF"v[216:219],a[100:103],a[128:131],v[216:219]\n"
"s_bitcmp1_b32 s95,2\n"
PK"v133,v134,v135\n"
PK"v134,v140,v141\n"
MF"v[216:219],a[96:99],a[248:251],v[216:219]\n"
"s_cselect_b32 s51,s94,1065353216\n"
RD"a[68:71],v236 offset:1024\n"
PK"v135,v142,v143\n"
MF"v[156:159],a[104:107],a[244:247],v[156:159]\n"
SA"s92,s51,10240\n"
BS"v[132:135],v224,s[8:11],s90 offen sc1\n"
MF"v[156:159],a[108:111],a[252:255],v[156:159]\n"
BL"a[188:191],v244,s[8:11],s92 offen sc1\n"
MF"v[220:223],a[108:111],a[128:131],v[220:223]\n"
RD"a[72:75],v236 offset:2048\n"
MF"v[220:223],a[104:107],a[248:251],v[220:223]\n"
WT"lgkmcnt(3)\n"
BL"a[220:223],v245,s[8:11],s92 offen sc1\n"
MF"v[148:151],a[112:115],a[132:135],v[148:151]\n"
"s_bitcmp1_b32 s95,3\n"
RD"a[76:79],v236 offset:3072\n"
"s_cselect_b32 s51,s94,1065353216\n"
MF"v[148:151],a[116:119],a[140:143],v[148:151]\n"
SA"s91,s22,330752\n"
RD"a[80:83],v236 offset:20480\n"
PK"v208,v208,v209\n"
MF"v[216:219],a[116:119],a[144:147],v[216:219]\n"
SA"s92,s51,15360\n"
PK"v209,v210,v211\n"
PK"v210,v212,v213\n"
MF"v[216:219],a[112:115],a[136:139],v[216:219]\n"
BL"a[192:195],v244,s[8:11],s92 offen sc1\n"
MF"v[156:159],a[120:123],a[132:135],v[156:159]\n"
RD"a[84:87],v236 offset:21504\n"
PK"v211,v214,v215\n"
MF"v[156:159],a[124:127],a[140:143],v[156:159]\n"
BS"v[208:211],v224,s[8:11],s91 offen sc1\n"
MF"v[220:223],a[124:127],a[144:147],v[220:223]\n"
"s_bitcmp1_b32 s95,4\n"
BL"a[224:227],v245,s[8:11],s92 offen sc1\n"
MF"v[220:223],a[120:123],a[136:139],v[220:223]\n"
WT"lgkmcnt(2)\n"
RD"a[88:91],v236 offset:22528\n"
"s_cselect_b32 s51,s94,1065353216\n"
MF"v[148:151],a[64:67],a[148:151],v[148:151]\n"
SA"s92,s51,20480\n"
RD"a[92:95],v236 offset:23552\n"
MF"v[148:151],a[68:71],a[156:159],v[148:151]\n"
BL"a[196:199],v244,s[8:11],s92 offen sc1\n"
MF"v[216:219],a[68:71],a[160:163],v[216:219]\n"
RD"a[96:99],v237 offset:8192\n"
MF"v[216:219],a[64:67],a[152:155],v[216:219]\n"
BL"a[228:231],v245,s[8:11],s92 offen sc1\n"
MF"v[156:159],a[72:75],a[148:151],v[156:159]\n"
"s_bitcmp1_b32 s95,5\n"
RD"a[100:103],v237 offset:9216\n"
"s_cselect_b32 s51,s94,1065353216\n"
MF"v[156:159],a[76:79],a[156:159],v[156:159]\n"
SA"s92,s51,25600\n"
RD"a[104:107],v237 offset:10240\n"
MF"v[220:223],a[76:79],a[160:163],v[220:223]\n"
BL"a[200:203],v244,s[8:11],s92 offen sc1\n"
MF"v[220:223],a[72:75],a[152:155],v[220:223]\n"
WT"lgkmcnt(3)\n"
RD"a[108:111],v237 offset:11264\n"
MF"v[148:151],a[80:83],a[164:167],v[148:151]\n"
BL"a[232:235],v245,s[8:11],s92 offen sc1\n"
MF"v[148:151],a[84:87],a[172:175],v[148:151]\n"
RD"a[112:115],v237 offset:28672\n"
MF"v[216:219],a[84:87],a[176:179],v[216:219]\n"
"s_bitcmp1_b32 s95,6\n"
"s_cselect_b32 s51,s94,1065353216\n"
MF"v[216:219],a[80:83],a[168:171],v[216:219]\n"
SA"s92,s51,30720\n"
RD"a[116:119],v237 offset:29696\n"
MF"v[156:159],a[88:91],a[164:167],v[156:159]\n"
BL"a[204:207],v244,s[8:11],s92 offen sc1\n"
MF"v[156:159],a[92:95],a[172:175],v[156:159]\n"
RD"a[120:123],v237 offset:30720\n"
MF"v[220:223],a[92:95],a[176:179],v[220:223]\n"
BL"a[236:239],v245,s[8:11],s92 offen sc1\n"
MF"v[220:223],a[88:91],a[168:171],v[220:223]\n"
WT"lgkmcnt(3)\n"
RD"a[124:127],v237 offset:31744\n"
MF"v[148:151],a[96:99],a[32:35],v[148:151]\n"
"s_bitcmp1_b32 s95,7\n"
"s_cselect_b32 s51,s94,1065353216\n"
MF"v[148:151],a[100:103],a[40:43],v[148:151]\n"
SA"s92,s51,35840\n"
MF"v[216:219],a[100:103],a[44:47],v[216:219]\n"
BL"a[208:211],v244,s[8:11],s92 offen sc1\n"
MF"v[216:219],a[96:99],a[36:39],v[216:219]\n"
MF"v[156:159],a[104:107],a[32:35],v[156:159]\n"
BL"a[240:243],v245,s[8:11],s92 offen sc1\n"
MF"v[156:159],a[108:111],a[40:43],v[156:159]\n"
MF"v[220:223],a[108:111],a[44:47],v[220:223]\n"
MF"v[220:223],a[104:107],a[36:39],v[220:223]\n"
WT"lgkmcnt(0)\n"
MF"v[148:151],a[112:115],a[48:51],v[148:151]\n"
MF"v[148:151],a[116:119],a[56:59],v[148:151]\n"
MF"v[216:219],a[116:119],a[60:63],v[216:219]\n"
MF"v[216:219],a[112:115],a[52:55],v[216:219]\n"
MF"v[156:159],a[120:123],a[48:51],v[156:159]\n"
MF"v[156:159],a[124:127],a[56:59],v[156:159]\n"
MF"v[220:223],a[124:127],a[60:63],v[220:223]\n"
MF"v[220:223],a[120:123],a[52:55],v[220:223]\n"
"s_nop 5\n"
PK"v148,v148,v149\n"
PK"v149,v150,v151\n"
PK"v150,v156,v157\n"
PK"v151,v158,v159\n"
SA"s90,s88,331776\n"
BS"v[148:151],v224,s[8:11],s90 offen sc1\n"
PK"v216,v216,v217\n"
PK"v217,v218,v219\n"
PK"v218,v220,v221\n"
PK"v219,v222,v223\n"
SA"s91,s22,331776\n"
BS"v[216:219],v224,s[8:11],s91 offen sc1\n"
"s_cmp_eq_u32 s23,0\n"
"s_cbranch_scc0 L1089_%=\n"
"ds_write_b128 v227,v[0:3] offset:0\n"
"ds_write_b128 v227,v[8:11] offset:1024\n"
"ds_write_b128 v227,v[16:19] offset:2048\n"
"ds_write_b128 v227,v[24:27] offset:3072\n"
"ds_write_b128 v227,v[32:35] offset:4096\n"
"ds_write_b128 v227,v[84:87] offset:5120\n"
"ds_write_b128 v227,v[100:103] offset:6144\n"
"ds_write_b128 v227,v[116:119] offset:7168\n"
"ds_write_b128 v227,v[132:135] offset:8192\n"
"ds_write_b128 v227,v[148:151] offset:9216\n"
"L1089_%=:\n"
"s_cmp_eq_u32 s23,4\n"
"s_cbranch_scc0 L1090_%=\n"
"ds_write_b128 v227,v[40:43] offset:0\n"
"ds_write_b128 v227,v[48:51] offset:1024\n"
"ds_write_b128 v227,v[56:59] offset:2048\n"
"ds_write_b128 v227,v[64:67] offset:3072\n"
"ds_write_b128 v227,v[72:75] offset:4096\n"
"ds_write_b128 v227,v[164:167] offset:5120\n"
"ds_write_b128 v227,v[180:183] offset:6144\n"
"ds_write_b128 v227,v[196:199] offset:7168\n"
"ds_write_b128 v227,v[208:211] offset:8192\n"
"ds_write_b128 v227,v[216:219] offset:9216\n"
"L1090_%=:\n"
MF"v[4:7],v[96:99],v[112:115],0\n"
MF"v[12:15],v[104:107],v[112:115],0\n"
MF"v[20:23],v[96:99],v[120:123],0\n"
MF"v[28:31],v[104:107],v[120:123],0\n"
MF"v[4:7],v[96:99],v[144:147],v[4:7]\n"
MF"v[12:15],v[104:107],v[144:147],v[12:15]\n"
MF"v[20:23],v[96:99],v[152:155],v[20:23]\n"
MF"v[28:31],v[104:107],v[152:155],v[28:31]\n"
MF"v[36:39],v[96:99],v[128:131],0\n"
MF"v[44:47],v[104:107],v[128:131],0\n"
MF"v[52:55],v[96:99],v[136:139],0\n"
MF"v[60:63],v[104:107],v[136:139],0\n"
MF"v[36:39],v[96:99],v[160:163],v[36:39]\n"
MF"v[44:47],v[104:107],v[160:163],v[44:47]\n"
MF"v[52:55],v[96:99],v[168:171],v[52:55]\n"
MF"v[60:63],v[104:107],v[168:171],v[60:63]\n"
SA"s89,s89,4096\n"
"s_mov_b32 s54,1048576\n"
WT"vmcnt(0)\n"
"buffer_store_dwordx2 v[232:233],v239,s[8:11],s89 offen sc1\n"
"buffer_load_dwordx2 v[240:241],v243,s[8:11],s93 offen sc1\n"
WT"lgkmcnt(0)\n"
"s_barrier\n"
RD"a[180:183],v247 offset:0\n"
RD"a[212:215],v227 offset:4096\n"
RD"a[64:67],v247 offset:5120\n"
RD"a[96:99],v227 offset:9216\n"
"s_getpc_b64 s[52:53]\n"
"L1097_%=:\n"
SA"s52,s52,L1096_%=-L1097_%=-32\n"
"s_addc_u32 s53,s53,0\n"
"s_mov_b32 s89,0\n"
WT"lgkmcnt(0)\n"
MF"v[4:7],v[80:83],a[180:183],v[4:7]\n"
MF"v[12:15],v[88:91],a[180:183],v[12:15]\n"
MF"v[20:23],v[80:83],a[212:215],v[20:23]\n"
MF"v[28:31],v[88:91],a[212:215],v[28:31]\n"
MF"v[36:39],v[80:83],a[64:67],v[36:39]\n"
MF"v[44:47],v[88:91],a[64:67],v[44:47]\n"
MF"v[52:55],v[80:83],a[96:99],v[52:55]\n"
MF"v[60:63],v[88:91],a[96:99],v[60:63]\n"
"s_branch L1092_%=\n"
"L1094_%=:\n"
"s_sleep 3\n"
"buffer_load_dwordx2 v[240:241],v243,s[8:11],s93 offen sc1\n"
"L1091_%=:\n"
WT"vmcnt(0)\n"
"v_cmp_eq_u64 vcc,s[28:29],v[240:241]\n"
"s_sub_u32 s54,s54,1\n"
"s_cselect_b32 s90,-1,vcc_lo\n"
"s_andn2_b32 s90,s90,s95\n"
"s_cbranch_scc0 L1094_%=\n"
"s_or_b32 s95,s95,s90\n"
"L1098_%=:\n"
"s_ff1_i32_b32 s50,s90\n"
"s_bitset0_b32 s90,s50\n"
"s_lshl_b32 s50,s50,5\n"
SA"s50,s50,s52\n"
"s_addc_u32 s51,s53,0\n"
"s_setpc_b64 s[50:51]\n"
"L1096_%=:\n"
SA"s92,s94,5120\n"
BL"a[184:187],v244,s[8:11],s92 offen sc1\n"
BL"a[216:219],v245,s[8:11],s92 offen sc1\n"
"s_branch L1093_%=\n"
"s_nop 0\n"
SA"s92,s94,10240\n"
BL"a[188:191],v244,s[8:11],s92 offen sc1\n"
BL"a[220:223],v245,s[8:11],s92 offen sc1\n"
"s_branch L1093_%=\n"
"s_nop 0\n"
SA"s92,s94,15360\n"
BL"a[192:195],v244,s[8:11],s92 offen sc1\n"
BL"a[224:227],v245,s[8:11],s92 offen sc1\n"
"s_branch L1093_%=\n"
"s_nop 0\n"
SA"s92,s94,20480\n"
BL"a[196:199],v244,s[8:11],s92 offen sc1\n"
BL"a[228:231],v245,s[8:11],s92 offen sc1\n"
"s_branch L1093_%=\n"
"s_nop 0\n"
SA"s92,s94,25600\n"
BL"a[200:203],v244,s[8:11],s92 offen sc1\n"
BL"a[232:235],v245,s[8:11],s92 offen sc1\n"
"s_branch L1093_%=\n"
"s_nop 0\n"
SA"s92,s94,30720\n"
BL"a[204:207],v244,s[8:11],s92 offen sc1\n"
BL"a[236:239],v245,s[8:11],s92 offen sc1\n"
"s_branch L1093_%=\n"
"s_nop 0\n"
SA"s92,s94,35840\n"
BL"a[208:211],v244,s[8:11],s92 offen sc1\n"
BL"a[240:243],v245,s[8:11],s92 offen sc1\n"
"s_branch L1093_%=\n"
"s_nop 0\n"
"s_nop 0\n"
"s_nop 0\n"
"s_nop 0\n"
"s_nop 0\n"
"s_nop 0\n"
"s_nop 0\n"
"s_nop 0\n"
"s_nop 0\n"
SA"s92,s94,332800\n"
BL"a[68:71],v244,s[8:11],s92 offen sc1\n"
BL"a[100:103],v245,s[8:11],s92 offen sc1\n"
"s_branch L1093_%=\n"
"s_nop 0\n"
SA"s92,s94,337920\n"
BL"a[72:75],v244,s[8:11],s92 offen sc1\n"
BL"a[104:107],v245,s[8:11],s92 offen sc1\n"
"s_branch L1093_%=\n"
"s_nop 0\n"
SA"s92,s94,343040\n"
BL"a[76:79],v244,s[8:11],s92 offen sc1\n"
BL"a[108:111],v245,s[8:11],s92 offen sc1\n"
"s_branch L1093_%=\n"
"s_nop 0\n"
SA"s92,s94,348160\n"
BL"a[80:83],v244,s[8:11],s92 offen sc1\n"
BL"a[112:115],v245,s[8:11],s92 offen sc1\n"
"s_branch L1093_%=\n"
"s_nop 0\n"
SA"s92,s94,353280\n"
BL"a[84:87],v244,s[8:11],s92 offen sc1\n"
BL"a[116:119],v245,s[8:11],s92 offen sc1\n"
"s_branch L1093_%=\n"
"s_nop 0\n"
SA"s92,s94,358400\n"
BL"a[88:91],v244,s[8:11],s92 offen sc1\n"
BL"a[120:123],v245,s[8:11],s92 offen sc1\n"
"s_branch L1093_%=\n"
"s_nop 0\n"
SA"s92,s94,363520\n"
BL"a[92:95],v244,s[8:11],s92 offen sc1\n"
BL"a[124:127],v245,s[8:11],s92 offen sc1\n"
"s_branch L1093_%=\n"
"s_nop 0\n"
"L1093_%=:\n"
"s_cmp_lg_u32 s90,0\n"
"s_cbranch_scc1 L1098_%=\n"
"s_and_b32 s92,s95,s89\n"
"s_cmp_eq_u32 s92,-1\n"
"s_cbranch_scc1 L1095_%=\n"
"buffer_load_dwordx2 v[240:241],v243,s[8:11],s93 offen sc1\n"
WT"vmcnt(1)\n"
"L1092_%=:\n"
"s_or_b32 s92,s95,0xff00\n"
"s_andn2_b32 s92,s92,s89\n"
"s_cmp_eq_u32 s92,-1\n"
"s_cbranch_scc0 L1091_%=\n"
MF"v[4:7],v[80:83],a[184:187],v[4:7]\n"
MF"v[12:15],v[88:91],a[184:187],v[12:15]\n"
MF"v[4:7],v[80:83],a[188:191],v[4:7]\n"
MF"v[12:15],v[88:91],a[188:191],v[12:15]\n"
MF"v[4:7],v[80:83],a[192:195],v[4:7]\n"
MF"v[12:15],v[88:91],a[192:195],v[12:15]\n"
MF"v[4:7],v[80:83],a[196:199],v[4:7]\n"
MF"v[12:15],v[88:91],a[196:199],v[12:15]\n"
MF"v[4:7],v[80:83],a[200:203],v[4:7]\n"
MF"v[12:15],v[88:91],a[200:203],v[12:15]\n"
MF"v[4:7],v[80:83],a[204:207],v[4:7]\n"
MF"v[12:15],v[88:91],a[204:207],v[12:15]\n"
MF"v[4:7],v[80:83],a[208:211],v[4:7]\n"
MF"v[12:15],v[88:91],a[208:211],v[12:15]\n"
MF"v[20:23],v[80:83],a[216:219],v[20:23]\n"
MF"v[28:31],v[88:91],a[216:219],v[28:31]\n"
MF"v[20:23],v[80:83],a[220:223],v[20:23]\n"
MF"v[28:31],v[88:91],a[220:223],v[28:31]\n"
MF"v[20:23],v[80:83],a[224:227],v[20:23]\n"
MF"v[28:31],v[88:91],a[224:227],v[28:31]\n"
MF"v[20:23],v[80:83],a[228:231],v[20:23]\n"
MF"v[28:31],v[88:91],a[228:231],v[28:31]\n"
PK"v4,v4,v5\n"
PK"v5,v6,v7\n"
PK"v6,v12,v13\n"
PK"v7,v14,v15\n"
BS"v[4:7],v176,s[16:19],s38 offen\n"
MF"v[20:23],v[80:83],a[232:235],v[20:23]\n"
MF"v[28:31],v[88:91],a[232:235],v[28:31]\n"
MF"v[20:23],v[80:83],a[236:239],v[20:23]\n"
MF"v[28:31],v[88:91],a[236:239],v[28:31]\n"
MF"v[20:23],v[80:83],a[240:243],v[20:23]\n"
MF"v[28:31],v[88:91],a[240:243],v[28:31]\n"
"s_nop 7\n"
PK"v20,v20,v21\n"
PK"v21,v22,v23\n"
PK"v22,v28,v29\n"
PK"v23,v30,v31\n"
BS"v[20:23],v176,s[16:19],s55 offen\n"
"s_mov_b32 s89,-1\n"
"s_cmp_eq_u32 s95,-1\n"
"s_cbranch_scc0 L1091_%=\n"
"L1095_%=:\n"
WT"vmcnt(0)\n"
MF"v[36:39],v[80:83],a[68:71],v[36:39]\n"
MF"v[44:47],v[88:91],a[68:71],v[44:47]\n"
MF"v[36:39],v[80:83],a[72:75],v[36:39]\n"
MF"v[44:47],v[88:91],a[72:75],v[44:47]\n"
MF"v[36:39],v[80:83],a[76:79],v[36:39]\n"
MF"v[44:47],v[88:91],a[76:79],v[44:47]\n"
MF"v[36:39],v[80:83],a[80:83],v[36:39]\n"
MF"v[44:47],v[88:91],a[80:83],v[44:47]\n"
MF"v[36:39],v[80:83],a[84:87],v[36:39]\n"
MF"v[44:47],v[88:91],a[84:87],v[44:47]\n"
MF"v[36:39],v[80:83],a[88:91],v[36:39]\n"
MF"v[44:47],v[88:91],a[88:91],v[44:47]\n"
MF"v[36:39],v[80:83],a[92:95],v[36:39]\n"
MF"v[44:47],v[88:91],a[92:95],v[44:47]\n"
MF"v[52:55],v[80:83],a[100:103],v[52:55]\n"
MF"v[60:63],v[88:91],a[100:103],v[60:63]\n"
MF"v[52:55],v[80:83],a[104:107],v[52:55]\n"
MF"v[60:63],v[88:91],a[104:107],v[60:63]\n"
MF"v[52:55],v[80:83],a[108:111],v[52:55]\n"
MF"v[60:63],v[88:91],a[108:111],v[60:63]\n"
MF"v[52:55],v[80:83],a[112:115],v[52:55]\n"
MF"v[60:63],v[88:91],a[112:115],v[60:63]\n"
PK"v36,v36,v37\n"
PK"v37,v38,v39\n"
PK"v38,v44,v45\n"
PK"v39,v46,v47\n"
BS"v[36:39],v177,s[16:19],s38 offen\n"
MF"v[52:55],v[80:83],a[116:119],v[52:55]\n"
MF"v[60:63],v[88:91],a[116:119],v[60:63]\n"
MF"v[52:55],v[80:83],a[120:123],v[52:55]\n"
MF"v[60:63],v[88:91],a[120:123],v[60:63]\n"
MF"v[52:55],v[80:83],a[124:127],v[52:55]\n"
MF"v[60:63],v[88:91],a[124:127],v[60:63]\n"
"s_nop 7\n"
PK"v52,v52,v53\n"
PK"v53,v54,v55\n"
PK"v54,v60,v61\n"
PK"v55,v62,v63\n"
BS"v[52:55],v177,s[16:19],s55 offen\n"
WT"vmcnt(0) lgkmcnt(0)\n"
"s_endpgm\n"
".p2align 6\n"
"LD2_%=:\n"
MF"v[0:3],a[64:67],a[32:35],v[208:211]\n"
"s_mov_b32 m0,s40\n"
EX"v56,v56\n"
MF"v[0:3],a[68:71],a[36:39],v[0:3]\n"
EX"v57,v57\n"
MF"v[8:11],a[68:71],a[52:55],0\n"
RD"a[92:95],v226 offset:7168\n"
"v_readlane_b32 s43,v228,s42\n"
MF"v[8:11],a[64:67],a[48:51],v[8:11]\n"
WT"lgkmcnt(3)\n"
EX"v58,v58\n"
MF"v[0:3],a[72:75],a[40:43],v[0:3]\n"
RD"a[96:99],v226 offset:8192\n"
RD"a[100:103],v226 offset:9216\n"
"v_readlane_b32 s44,v229,s42\n"
MF"v[0:3],a[76:79],a[44:47],v[0:3]\n"
SA"s42,s42,1\n"
BL"v225,s[8:11],s43 offen offset:0 sc1 lds\n"
PK"v76,v56,v57\n"
MF"v[8:11],a[76:79],a[60:63],v[8:11]\n"
EX"v59,v59\n"
MF"v[8:11],a[72:75],a[56:59],v[8:11]\n"
RD"a[104:107],v226 offset:10240\n"
PK"v77,v58,v59\n"
MF"v[4:7],a[80:83],a[32:35],v[212:215]\n"
WT"lgkmcnt(4)\n"
EX"v60,v60\n"
MF"v[4:7],a[84:87],a[36:39],v[4:7]\n"
EX"v61,v61\n"
MF"v[12:15],a[84:87],a[52:55],0\n"
BL"v225,s[8:11],s43 offen offset:1024 sc1 lds\n"
MF"v[12:15],a[80:83],a[48:51],v[12:15]\n"
RD"a[108:111],v226 offset:11264\n"
PK"v78,v60,v61\n"
MF"v[4:7],a[88:91],a[40:43],v[4:7]\n"
WT"lgkmcnt(2)\n"
EX"v62,v62\n"
MF"v[4:7],a[92:95],a[44:47],v[4:7]\n"
RD"a[112:115],v226 offset:12288\n"
RD"a[116:119],v226 offset:13312\n"
MF"v[12:15],a[92:95],a[60:63],v[12:15]\n"
EX"v63,v63\n"
MF"v[12:15],a[88:91],a[56:59],v[12:15]\n"
BL"v225,s[8:11],s43 offen offset:2048 sc1 lds\n"
RD"a[120:123],v226 offset:14336\n"
MF"v[16:19],a[96:99],a[32:35],v[216:219]\n"
EX"v0,v0\n"
PK"v79,v62,v63\n"
MF"v[16:19],a[100:103],a[36:39],v[16:19]\n"
EX"v1,v1\n"
MF"v[24:27],a[100:103],a[52:55],0\n"
RD"a[124:127],v226 offset:15360\n"
RD"a[64:67],v226 offset:16384\n"
MF"v[24:27],a[96:99],a[48:51],v[24:27]\n"
WT"lgkmcnt(5)\n"
EX"v2,v2\n"
PK"v0,v0,v1\n"
MF"v[16:19],a[104:107],a[40:43],v[16:19]\n"
SA"s41,s41,32768\n"
BL"v225,s[8:11],s43 offen offset:3072 sc1 lds\n"
RD"a[68:71],v226 offset:17408\n"
MF"v[16:19],a[108:111],a[44:47],v[16:19]\n"
SA"m0,s40,16384\n"
RD"a[72:75],v226 offset:18432\n"
EX"v3,v3\n"
MF"v[24:27],a[108:111],a[60:63],v[24:27]\n"
"s_cmp_eq_u32 s41,163840\n"
RD"a[76:79],v226 offset:19456\n"
RD"a[80:83],v226 offset:20480\n"
MF"v[24:27],a[104:107],a[56:59],v[24:27]\n"
WT"lgkmcnt(6)\n"
EX"v8,v8\n"
PK"v1,v2,v3\n"
MF"v[20:23],a[112:115],a[32:35],v[220:223]\n"
"s_cselect_b32 s41,0,s41\n"
RD"a[84:87],v226 offset:21504\n"
RD"a[88:91],v226 offset:22528\n"
EX"v9,v9\n"
MF"v[20:23],a[116:119],a[36:39],v[20:23]\n"
SA"s40,s40,32768\n"
BL"v225,s[8:11],s44 offen offset:0 sc1 lds\n"
"v_add_u32 v227,s41,v224\n"
MF"v[28:31],a[116:119],a[52:55],0\n"
"s_cmp_eq_u32 s40,s46\n"
EX"v10,v10\n"
PK"v8,v8,v9\n"
MF"v[28:31],a[112:115],a[48:51],v[28:31]\n"
"s_cselect_b32 s40,s47,s40\n"
RD"a[92:95],v226 offset:23552\n"
RD"a[96:99],v226 offset:24576\n"
MF"v[20:23],a[120:123],a[40:43],v[20:23]\n"
WT"lgkmcnt(6)\n"
EX"v11,v11\n"
MF"v[20:23],a[124:127],a[44:47],v[20:23]\n"
RD"a[100:103],v226 offset:25600\n"
RD"a[104:107],v226 offset:26624\n"
MF"v[28:31],a[124:127],a[60:63],v[28:31]\n"
BL"v225,s[8:11],s44 offen offset:1024 sc1 lds\n"
RD"a[108:111],v226 offset:27648\n"
MF"v[28:31],a[120:123],a[56:59],v[28:31]\n"
EX"v4,v4\n"
PK"v9,v10,v11\n"
MF"v[40:43],a[64:67],a[48:51],v[208:211]\n"
RD"a[112:115],v226 offset:28672\n"
RD"a[116:119],v226 offset:29696\n"
EX"v5,v5\n"
MF"v[40:43],a[68:71],a[52:55],v[40:43]\n"
WT"vmcnt(18)\n"
EX"v6,v6\n"
MF"v[40:43],a[72:75],a[56:59],v[40:43]\n"
WT"lgkmcnt(5)\n"
RD"a[120:123],v226 offset:30720\n"
RD"a[124:127],v226 offset:31744\n"
PK"v2,v4,v5\n"
MF"v[40:43],a[76:79],a[60:63],v[40:43]\n"
BL"v225,s[8:11],s44 offen offset:2048 sc1 lds\n"
EX"v7,v7\n"
MF"v[44:47],a[80:83],a[48:51],v[212:215]\n"
"s_barrier\n"
MF"v[44:47],a[84:87],a[52:55],v[44:47]\n"
EX"v12,v12\n"
PK"v3,v6,v7\n"
MF"v[44:47],a[88:91],a[56:59],v[44:47]\n"
EX"v13,v13\n"
MF"v[44:47],a[92:95],a[60:63],v[44:47]\n"
BL"v225,s[8:11],s44 offen offset:3072 sc1 lds\n"
"v_readlane_b32 s43,v228,s42\n"
MF"v[56:59],a[96:99],a[48:51],v[216:219]\n"
WT"lgkmcnt(2)\n"
RD"a[96:99],v227 offset:8192\n"
PK"v10,v12,v13\n"
EX"v14,v14\n"
MF"v[56:59],a[100:103],a[52:55],v[56:59]\n"
"s_mov_b32 m0,s40\n"
RD"a[100:103],v227 offset:9216\n"
MF"v[56:59],a[104:107],a[56:59],v[56:59]\n"
EX"v15,v15\n"
MF"v[56:59],a[108:111],a[60:63],v[56:59]\n"
RD"a[104:107],v227 offset:10240\n"
RD"a[108:111],v227 offset:11264\n"
MF"v[60:63],a[112:115],a[48:51],v[220:223]\n"
EX"v16,v16\n"
PK"v11,v14,v15\n"
MF"v[60:63],a[116:119],a[52:55],v[60:63]\n"
WT"lgkmcnt(4)\n"
EX"v17,v17\n"
"v_readlane_b32 s44,v229,s42\n"
MF"v[60:63],a[120:123],a[56:59],v[60:63]\n"
SA"s42,s42,1\n"
BL"v225,s[8:11],s43 offen offset:0 sc1 lds\n"
MF"v[60:63],a[124:127],a[60:63],v[60:63]\n"
WT"lgkmcnt(3)\n"
EX"v18,v18\n"
MF"v[112:115],a[96:99],v[68:71],v[112:115]\n"
SA"s41,s41,32768\n"
RD"a[112:115],v227 offset:12288\n"
PK"v16,v16,v17\n"
MF"v[144:147],a[96:99],v[72:75],v[144:147]\n"
"s_cmp_eq_u32 s41,163840\n"
EX"v19,v19\n"
MF"v[176:179],a[96:99],v[76:79],v[176:179]\n"
WT"lgkmcnt(1)\n"
EX"v24,v24\n"
MF"v[116:119],a[100:103],v[68:71],v[116:119]\n"
"s_cselect_b32 s41,0,s41\n"
BL"v225,s[8:11],s43 offen offset:1024 sc1 lds\n"
RD"a[116:119],v227 offset:13312\n"
MF"v[148:151],a[100:103],v[72:75],v[148:151]\n"
EX"v25,v25\n"
"v_add_u32 v226,s41,v224\n"
MF"v[180:183],a[100:103],v[76:79],v[180:183]\n"
RD"a[120:123],v227 offset:14336\n"
PK"v17,v18,v19\n"
MF"v[120:123],a[104:107],v[68:71],v[120:123]\n"
EX"v26,v26\n"
MF"v[152:155],a[104:107],v[72:75],v[152:155]\n"
EX"v27,v27\n"
PK"v24,v24,v25\n"
MF"v[184:187],a[104:107],v[76:79],v[184:187]\n"
BL"v225,s[8:11],s43 offen offset:2048 sc1 lds\n"
MF"v[124:127],a[108:111],v[68:71],v[124:127]\n"
EX"v20,v20\n"
MF"v[156:159],a[108:111],v[72:75],v[156:159]\n"
RD"a[124:127],v227 offset:15360\n"
PK"v25,v26,v27\n"
MF"v[188:191],a[108:111],v[76:79],v[188:191]\n"
WT"lgkmcnt(2)\n"
EX"v21,v21\n"
MF"v[128:131],a[112:115],v[68:71],v[128:131]\n"
BL"v225,s[8:11],s43 offen offset:3072 sc1 lds\n"
MF"v[160:163],a[112:115],v[72:75],v[160:163]\n"
SA"m0,s40,16384\n"
EX"v22,v22\n"
MF"v[192:195],a[112:115],v[76:79],v[192:195]\n"
EX"v23,v23\n"
PK"v18,v20,v21\n"
MF"v[132:135],a[116:119],v[68:71],v[132:135]\n"
RD"a[64:67],v227 offset:16384\n"
PK"v19,v22,v23\n"
MF"v[164:167],a[116:119],v[72:75],v[164:167]\n"
EX"v28,v28\n"
MF"v[196:199],a[116:119],v[76:79],v[196:199]\n"
WT"lgkmcnt(1)\n"
BL"v225,s[8:11],s44 offen offset:0 sc1 lds\n"
MF"v[136:139],a[120:123],v[68:71],v[136:139]\n"
SA"s40,s40,32768\n"
EX"v29,v29\n"
MF"v[168:171],a[120:123],v[72:75],v[168:171]\n"
"s_cmp_eq_u32 s40,s46\n"
RD"a[68:71],v227 offset:24576\n"
RD"a[72:75],v227 offset:17408\n"
MF"v[200:203],a[120:123],v[76:79],v[200:203]\n"
"s_cselect_b32 s40,s47,s40\n"
EX"v30,v30\n"
MF"v[140:143],a[124:127],v[68:71],v[140:143]\n"
RD"a[76:79],v227 offset:25600\n"
RD"a[80:83],v227 offset:18432\n"
PK"v26,v28,v29\n"
MF"v[172:175],a[124:127],v[72:75],v[172:175]\n"
EX"v31,v31\n"
MF"v[204:207],a[124:127],v[76:79],v[204:207]\n"
BL"v225,s[8:11],s44 offen offset:1024 sc1 lds\n"
MF"a[136:139],a[128:131],v[68:71],a[136:139]\n"
EX"v40,v40\n"
PK"v27,v30,v31\n"
MF"a[140:143],a[128:131],v[72:75],a[140:143]\n"
EX"v41,v41\n"
MF"a[144:147],a[128:131],v[76:79],a[144:147]\n"
WT"lgkmcnt(2)\n"
RD"a[84:87],v227 offset:26624\n"
PK"v68,v40,v41\n"
MF"v[144:147],a[64:67],v[0:3],v[144:147]\n"
BL"v225,s[8:11],s44 offen offset:2048 sc1 lds\n"
MF"v[144:147],a[68:71],v[16:19],v[144:147]\n"
EX"v42,v42\n"
MF"v[176:179],a[68:71],v[24:27],v[176:179]\n"
SA"s51,s59,16384\n"
RD"a[88:91],v227 offset:19456\n"
RD"a[92:95],v227 offset:27648\n"
MF"v[176:179],a[64:67],v[8:11],v[176:179]\n"
EX"v43,v43\n"
MF"v[148:151],a[72:75],v[0:3],v[148:151]\n"
WT"lgkmcnt(3)\n"
EX"v44,v44\n"
MF"v[148:151],a[76:79],v[16:19],v[148:151]\n"
RD"a[96:99],v227 offset:20480\n"
"v_accvgpr_read_b32 v252,a136\n"
"v_subrev_u32 v253,0x30800000,v252\n"
MF"v[180:183],a[76:79],v[24:27],v[180:183]\n"
"v_cmp_le_u32 vcc,0x1f000000,v253\n"
"s_cmp_lg_u64 vcc,0\n"
"s_cbranch_scc1 L1035_%=\n"
"L1034_%=:\n"
MF"v[180:183],a[72:75],v[8:11],v[180:183]\n"
BL"v225,s[8:11],s44 offen offset:3072 sc1 lds\n"
EX"v45,v45\n"
MF"v[152:155],a[80:83],v[0:3],v[152:155]\n"
WT"lgkmcnt(3)\n"
"v_rcp_f32 v252,v252\n"
EX"v46,v46\n"
MF"v[152:155],a[84:87],v[16:19],v[152:155]\n"
"s_mov_b32 m0,s40\n"
RD"a[100:103],v227 offset:28672\n"
RD"a[104:107],v227 offset:21504\n"
"v_readlane_b32 s43,v228,s42\n"
MF"v[184:187],a[84:87],v[24:27],v[184:187]\n"
EX"v47,v47\n"
PK"v69,v42,v43\n"
MF"v[184:187],a[80:83],v[8:11],v[184:187]\n"
WT"lgkmcnt(3)\n"
RD"a[108:111],v227 offset:29696\n"
RD"a[112:115],v227 offset:22528\n"
EX"v56,v56\n"
MF"v[156:159],a[88:91],v[0:3],v[156:159]\n"
EX"v57,v57\n"
"v_readlane_b32 s44,v229,s42\n"
MF"v[156:159],a[92:95],v[16:19],v[156:159]\n"
SA"s42,s42,1\n"
EX"v58,v58\n"
PK"v70,v44,v45\n"
MF"v[188:191],a[92:95],v[24:27],v[188:191]\n"
RD"a[116:119],v227 offset:30720\n"
MU"v112,v252,v112\n"
MU"v113,v252,v113\n"
MF"v[188:191],a[88:91],v[8:11],v[188:191]\n"
WT"lgkmcnt(5)\n"
EX"v59,v59\n"
MU"v114,v252,v114\n"
MF"v[160:163],a[96:99],v[0:3],v[160:163]\n"
WT"lgkmcnt(3)\n"
EX"v60,v60\n"
MU"v115,v252,v115\n"
MF"v[160:163],a[100:103],v[16:19],v[160:163]\n"
RD"a[120:123],v227 offset:23552\n"
RD"a[124:127],v227 offset:31744\n"
MU"v116,v252,v116\n"
MF"v[192:195],a[100:103],v[24:27],v[192:195]\n"
EX"v61,v61\n"
MU"v117,v252,v117\n"
MF"v[192:195],a[96:99],v[8:11],v[192:195]\n"
WT"vmcnt(20)\n"
MU"v118,v252,v118\n"
MU"v119,v252,v119\n"
PK"v71,v46,v47\n"
MF"v[164:167],a[104:107],v[0:3],v[164:167]\n"
WT"lgkmcnt(3)\n"
EX"v62,v62\n"
PK"v112,v112,v113\n"
MF"v[164:167],a[108:111],v[16:19],v[164:167]\n"
SA"s41,s41,32768\n"
"s_barrier\n"
MF"v[196:199],a[108:111],v[24:27],v[196:199]\n"
"s_cmp_eq_u32 s41,163840\n"
BL"v225,s[8:11],s43 offen offset:0 sc1 lds\n"
BL"v225,s[8:11],s43 offen offset:1024 sc1 lds\n"
MF"v[196:199],a[104:107],v[8:11],v[196:199]\n"
"s_cselect_b32 s41,0,s41\n"
BL"v225,s[8:11],s43 offen offset:2048 sc1 lds\n"
RD"a[64:67],v226 offset:0\n"
MF"v[168:171],a[112:115],v[0:3],v[168:171]\n"
WT"lgkmcnt(1)\n"
EX"v63,v63\n"
"v_add_u32 v227,s41,v224\n"
MF"v[168:171],a[116:119],v[16:19],v[168:171]\n"
RD"a[68:71],v226 offset:8192\n"
RD"a[72:75],v226 offset:1024\n"
PK"v113,v114,v115\n"
MF"v[200:203],a[116:119],v[24:27],v[200:203]\n"
RD"a[76:79],v226 offset:9216\n"
PK"v114,v116,v117\n"
PK"v115,v118,v119\n"
MF"v[200:203],a[112:115],v[8:11],v[200:203]\n"
BS"v[112:115],v224,s[8:11],s51 offen offset:0 sc1\n"
BL"v225,s[8:11],s43 offen offset:3072 sc1 lds\n"
MF"v[172:175],a[120:123],v[0:3],v[172:175]\n"
SA"m0,s40,16384\n"
RD"a[80:83],v226 offset:2048\n"
MU"v120,v252,v120\n"
MU"v121,v252,v121\n"
MF"v[172:175],a[124:127],v[16:19],v[172:175]\n"
RD"a[84:87],v226 offset:10240\n"
PK"v76,v56,v57\n"
MU"v122,v252,v122\n"
MF"v[204:207],a[124:127],v[24:27],v[204:207]\n"
RD"a[88:91],v226 offset:3072\n"
MU"v123,v252,v123\n"
MU"v124,v252,v124\n"
MF"v[204:207],a[120:123],v[8:11],v[204:207]\n"
SA"s40,s40,32768\n"
RD"a[92:95],v226 offset:11264\n"
MU"v125,v252,v125\n"
MU"v126,v252,v126\n"
MF"a[140:143],a[128:131],v[0:3],a[140:143]\n"
"s_cmp_eq_u32 s40,s46\n"
BL"v225,s[8:11],s44 offen offset:0 sc1 lds\n"
RD"a[96:99],v226 offset:4096\n"
MF"a[140:143],a[128:131],v[16:19],a[140:143]\n"
"s_cselect_b32 s40,s47,s40\n"
RD"a[100:103],v226 offset:12288\n"
MU"v127,v252,v127\n"
PK"v120,v120,v121\n"
MF"a[144:147],a[128:131],v[24:27],a[144:147]\n"
PK"v77,v58,v59\n"
PK"v78,v60,v61\n"
PK"v79,v62,v63\n"
MF"a[144:147],a[128:131],v[8:11],a[144:147]\n"
WT"lgkmcnt(5)\n"
RD"a[104:107],v226 offset:5120\n"
PK"v121,v122,v123\n"
PK"v122,v124,v125\n"
MF"v[176:179],a[64:67],v[68:71],v[176:179]\n"
RD"a[108:111],v226 offset:13312\n"
PK"v123,v126,v127\n"
MU"v128,v252,v128\n"
MF"v[176:179],a[68:71],v[76:79],v[176:179]\n"
WT"vmcnt(14)\n"
BS"v[120:123],v224,s[8:11],s51 offen offset:1024 sc1\n"
RD"a[112:115],v226 offset:6144\n"
MF"v[180:183],a[72:75],v[68:71],v[180:183]\n"
RD"a[116:119],v226 offset:14336\n"
MU"v129,v252,v129\n"
MU"v130,v252,v130\n"
MF"v[180:183],a[76:79],v[76:79],v[180:183]\n"
BL"v225,s[8:11],s44 offen offset:1024 sc1 lds\n"
RD"a[120:123],v226 offset:7168\n"
MF"v[184:187],a[80:83],v[68:71],v[184:187]\n"
WT"lgkmcnt(5)\n"
RD"a[124:127],v226 offset:15360\n"
MU"v131,v252,v131\n"
MU"v132,v252,v132\n"
MF"v[184:187],a[84:87],v[76:79],v[184:187]\n"
"s_barrier\n"
MF"v[188:191],a[88:91],v[68:71],v[188:191]\n"
RD"a[64:67],v227 offset:0\n"
MU"v133,v252,v133\n"
MU"v134,v252,v134\n"
MF"v[188:191],a[92:95],v[76:79],v[188:191]\n"
BL"v225,s[8:11],s44 offen offset:2048 sc1 lds\n"
RD"a[68:71],v227 offset:1024\n"
MF"v[192:195],a[96:99],v[68:71],v[192:195]\n"
RD"a[72:75],v227 offset:2048\n"
MU"v135,v252,v135\n"
PK"v128,v128,v129\n"
MF"v[192:195],a[100:103],v[76:79],v[192:195]\n"
WT"lgkmcnt(5)\n"
BL"v225,s[8:11],s44 offen offset:3072 sc1 lds\n"
PK"v129,v130,v131\n"
MF"v[196:199],a[104:107],v[68:71],v[196:199]\n"
RD"a[76:79],v227 offset:3072\n"
PK"v130,v132,v133\n"
PK"v131,v134,v135\n"
MF"v[196:199],a[108:111],v[76:79],v[196:199]\n"
BS"v[128:131],v224,s[8:11],s51 offen offset:2048 sc1\n"
MU"v136,v252,v136\n"
MF"v[200:203],a[112:115],v[68:71],v[200:203]\n"
RD"a[80:83],v227 offset:4096\n"
MU"v137,v252,v137\n"
MU"v138,v252,v138\n"
MF"v[200:203],a[116:119],v[76:79],v[200:203]\n"
WT"lgkmcnt(4)\n"
RD"a[84:87],v227 offset:5120\n"
MU"v139,v252,v139\n"
MU"v140,v252,v140\n"
MF"v[204:207],a[120:123],v[68:71],v[204:207]\n"
RD"a[88:91],v227 offset:6144\n"
MU"v141,v252,v141\n"
MU"v142,v252,v142\n"
MF"v[204:207],a[124:127],v[76:79],v[204:207]\n"
PK"v136,v136,v137\n"
MU"v143,v252,v143\n"
PK"v137,v138,v139\n"
MF"a[144:147],a[128:131],v[68:71],a[144:147]\n"
WT"lgkmcnt(5)\n"
PK"v138,v140,v141\n"
PK"v139,v142,v143\n"
MF"a[144:147],a[128:131],v[76:79],a[144:147]\n"
BS"v[136:139],v224,s[8:11],s51 offen offset:3072 sc1\n"
"s_branch LTL_%=\n"
".p2align 6\n"
"LTH_%=:\n"
RD"a[92:95],v227 offset:7168\n"
MF"v[0:3],a[64:67],v[80:83],0\n"
"v_accvgpr_read_b32 v251,a140\n"
"v_subrev_u32 v253,0x30800000,v251\n"
MF"v[0:3],a[68:71],v[88:91],v[0:3]\n"
RD"a[96:99],v227 offset:8192\n"
MF"v[40:43],a[68:71],v[120:123],0\n"
"v_cmp_le_u32 vcc,0x1f000000,v253\n"
"s_cmp_lg_u64 vcc,0\n"
"s_cbranch_scc1 L1069_%=\n"
"L1068_%=:\n"
MF"v[40:43],a[64:67],v[112:115],v[40:43]\n"
WT"lgkmcnt(3)\n"
"v_rcp_f32 v251,v251\n"
MF"v[4:7],a[72:75],v[80:83],0\n"
SA"s41,s41,32768\n"
RD"a[100:103],v227 offset:9216\n"
MU"v144,v251,v144\n"
MF"v[4:7],a[76:79],v[88:91],v[4:7]\n"
"s_cmp_eq_u32 s41,163840\n"
RD"a[104:107],v227 offset:10240\n"
MU"v145,v251,v145\n"
MF"v[44:47],a[76:79],v[120:123],0\n"
"s_cselect_b32 s41,0,s41\n"
MU"v146,v251,v146\n"
"v_add_u32 v226,s41,v224\n"
MF"v[44:47],a[72:75],v[112:115],v[44:47]\n"
RD"a[108:111],v227 offset:11264\n"
MU"v147,v251,v147\n"
MF"v[8:11],a[80:83],v[80:83],0\n"
SA"s41,s41,32768\n"
MU"v148,v251,v148\n"
MU"v149,v251,v149\n"
MF"v[8:11],a[84:87],v[88:91],v[8:11]\n"
"s_cmp_eq_u32 s41,163840\n"
RD"a[112:115],v227 offset:12288\n"
MU"v150,v251,v150\n"
MU"v151,v251,v151\n"
MF"v[48:51],a[84:87],v[120:123],0\n"
"s_cselect_b32 s41,0,s41\n"
PK"v144,v144,v145\n"
"v_add_u32 v235,s41,v224\n"
PK"v145,v146,v147\n"
MF"v[48:51],a[80:83],v[112:115],v[48:51]\n"
WT"lgkmcnt(3)\n"
RD"a[116:119],v227 offset:13312\n"
PK"v146,v148,v149\n"
MF"v[12:15],a[88:91],v[80:83],0\n"
SA"s41,s41,32768\n"
PK"v147,v150,v151\n"
MU"v152,v251,v152\n"
MF"v[12:15],a[92:95],v[88:91],v[12:15]\n"
"s_cmp_eq_u32 s41,163840\n"
BS"v[144:147],v224,s[8:11],s60 offen offset:0 sc1\n"
MU"v153,v251,v153\n"
MF"v[52:55],a[92:95],v[120:123],0\n"
"s_cselect_b32 s41,0,s41\n"
RD"a[120:123],v227 offset:14336\n"
"v_add_u32 v236,s41,v224\n"
MU"v154,v251,v154\n"
MF"v[52:55],a[88:91],v[112:115],v[52:55]\n"
RD"a[124:127],v227 offset:15360\n"
MU"v155,v251,v155\n"
MU"v156,v251,v156\n"
MF"v[16:19],a[96:99],v[80:83],0\n"
SA"s41,s41,32768\n"
MU"v157,v251,v157\n"
MU"v158,v251,v158\n"
MU"v159,v251,v159\n"
MF"v[16:19],a[100:103],v[88:91],v[16:19]\n"
"s_cmp_eq_u32 s41,163840\n"
RD"a[64:67],v227 offset:16384\n"
PK"v152,v152,v153\n"
PK"v153,v154,v155\n"
MF"v[56:59],a[100:103],v[120:123],0\n"
"s_cselect_b32 s41,0,s41\n"
PK"v154,v156,v157\n"
"v_add_u32 v237,s41,v224\n"
PK"v155,v158,v159\n"
MF"v[56:59],a[96:99],v[112:115],v[56:59]\n"
WT"lgkmcnt(3)\n"
BS"v[152:155],v224,s[8:11],s60 offen offset:1024 sc1\n"
MU"v160,v251,v160\n"
MF"v[20:23],a[104:107],v[80:83],0\n"
RD"a[68:71],v227 offset:17408\n"
MU"v161,v251,v161\n"
MU"v162,v251,v162\n"
MF"v[20:23],a[108:111],v[88:91],v[20:23]\n"
RD"a[72:75],v227 offset:18432\n"
MU"v163,v251,v163\n"
MU"v164,v251,v164\n"
MF"v[60:63],a[108:111],v[120:123],0\n"
MU"v165,v251,v165\n"
MU"v166,v251,v166\n"
MF"v[60:63],a[104:107],v[112:115],v[60:63]\n"
RD"a[76:79],v227 offset:19456\n"
MU"v167,v251,v167\n"
PK"v160,v160,v161\n"
MF"v[24:27],a[112:115],v[80:83],0\n"
SA"s51,s60,16384\n"
"v_accvgpr_read_b32 v252,a144\n"
"v_subrev_u32 v253,0x30800000,v252\n"
PK"v161,v162,v163\n"
MF"v[24:27],a[116:119],v[88:91],v[24:27]\n"
"s_lshl_b32 s62,s34,14\n"
RD"a[80:83],v227 offset:20480\n"
PK"v162,v164,v165\n"
PK"v163,v166,v167\n"
MF"v[64:67],a[116:119],v[120:123],0\n"
"s_sub_u32 s62,s61,s62\n"
BS"v[160:163],v224,s[8:11],s60 offen offset:2048 sc1\n"
MU"v168,v251,v168\n"
MF"v[64:67],a[112:115],v[112:115],v[64:67]\n"
WT"lgkmcnt(3)\n"
"v_cmp_le_u32 vcc,0x1f000000,v253\n"
"s_cmp_lg_u64 vcc,0\n"
"s_cbranch_scc1 L1075_%=\n"
"L1074_%=:\n"
MF"v[28:31],a[120:123],v[80:83],0\n"
SA"s62,s62,122880\n"
"v_rcp_f32 v252,v252\n"
MU"v169,v251,v169\n"
MF"v[28:31],a[124:127],v[88:91],v[28:31]\n"
RD"a[84:87],v227 offset:21504\n"
RD"a[88:91],v227 offset:22528\n"
MU"v170,v251,v170\n"
MF"v[68:71],a[124:127],v[120:123],0\n"
WT"vmcnt(3)\n"
"buffer_store_dwordx2 v[232:233],v230,s[8:11],s61 offen sc1\n"
MU"v171,v251,v171\n"
MF"v[68:71],a[120:123],v[112:115],v[68:71]\n"
RD"a[92:95],v227 offset:23552\n"
MU"v172,v251,v172\n"
MU"v173,v251,v173\n"
MF"v[32:35],a[64:67],v[80:83],0\n"
MU"v174,v251,v174\n"
MU"v175,v251,v175\n"
PK"v168,v168,v169\n"
MF"v[32:35],a[68:71],v[88:91],v[32:35]\n"
RD"a[96:99],v227 offset:24576\n"
PK"v169,v170,v171\n"
MU"v176,v252,v176\n"
MF"v[72:75],a[68:71],v[120:123],0\n"
MU"v177,v252,v177\n"
PK"v170,v172,v173\n"
PK"v171,v174,v175\n"
MF"v[72:75],a[64:67],v[112:115],v[72:75]\n"
WT"lgkmcnt(4)\n"
BS"v[168:171],v224,s[8:11],s60 offen offset:3072 sc1\n"
MU"v178,v252,v178\n"
MF"v[36:39],a[72:75],v[80:83],0\n"
RD"a[100:103],v227 offset:25600\n"
MU"v179,v252,v179\n"
MU"v180,v252,v180\n"
MF"v[36:39],a[76:79],v[88:91],v[36:39]\n"
RD"a[104:107],v227 offset:26624\n"
MU"v181,v252,v181\n"
MU"v182,v252,v182\n"
MF"v[76:79],a[76:79],v[120:123],0\n"
MU"v183,v252,v183\n"
PK"v176,v176,v177\n"
PK"v177,v178,v179\n"
MF"v[76:79],a[72:75],v[112:115],v[76:79]\n"
RD"a[108:111],v227 offset:27648\n"
PK"v178,v180,v181\n"
PK"v179,v182,v183\n"
MF"v[0:3],a[80:83],v[96:99],v[0:3]\n"
WT"lgkmcnt(3)\n"
BS"v[176:179],v224,s[8:11],s51 offen offset:0 sc1\n"
MU"v184,v252,v184\n"
MF"v[0:3],a[84:87],v[104:107],v[0:3]\n"
RD"a[112:115],v227 offset:28672\n"
MU"v185,v252,v185\n"
MU"v186,v252,v186\n"
MF"v[40:43],a[84:87],v[136:139],v[40:43]\n"
MU"v187,v252,v187\n"
MU"v188,v252,v188\n"
MU"v189,v252,v189\n"
MF"v[40:43],a[80:83],v[128:131],v[40:43]\n"
RD"a[116:119],v227 offset:29696\n"
MU"v190,v252,v190\n"
MU"v191,v252,v191\n"
MF"v[4:7],a[88:91],v[96:99],v[4:7]\n"
PK"v184,v184,v185\n"
PK"v185,v186,v187\n"
PK"v186,v188,v189\n"
MF"v[4:7],a[92:95],v[104:107],v[4:7]\n"
RD"a[120:123],v227 offset:30720\n"
PK"v187,v190,v191\n"
MU"v192,v252,v192\n"
MF"v[44:47],a[92:95],v[136:139],v[44:47]\n"
BS"v[184:187],v224,s[8:11],s51 offen offset:1024 sc1\n"
MU"v193,v252,v193\n"
MF"v[44:47],a[88:91],v[128:131],v[44:47]\n"
RD"a[124:127],v227 offset:31744\n"
MU"v194,v252,v194\n"
MU"v195,v252,v195\n"
MF"v[8:11],a[96:99],v[96:99],v[8:11]\n"
WT"lgkmcnt(3)\n"
"s_barrier\n"
MF"v[8:11],a[100:103],v[104:107],v[8:11]\n"
RD"a[64:67],v226 offset:0\n"
"v_readlane_b32 s43,v228,s42\n"
MU"v196,v252,v196\n"
MF"v[48:51],a[100:103],v[136:139],v[48:51]\n"
MU"v197,v252,v197\n"
MU"v198,v252,v198\n"
MU"v199,v252,v199\n"
MF"v[48:51],a[96:99],v[128:131],v[48:51]\n"
RD"a[68:71],v226 offset:1024\n"
PK"v192,v192,v193\n"
PK"v193,v194,v195\n"
MF"v[12:15],a[104:107],v[96:99],v[12:15]\n"
"s_mov_b32 m0,s40\n"
PK"v194,v196,v197\n"
PK"v195,v198,v199\n"
MU"v200,v252,v200\n"
MF"v[12:15],a[108:111],v[104:107],v[12:15]\n"
BS"v[192:195],v224,s[8:11],s51 offen offset:2048 sc1\n"
MU"v201,v252,v201\n"
MF"v[52:55],a[108:111],v[136:139],v[52:55]\n"
RD"a[72:75],v226 offset:2048\n"
"v_readlane_b32 s44,v229,s42\n"
MU"v202,v252,v202\n"
MF"v[52:55],a[104:107],v[128:131],v[52:55]\n"
RD"a[76:79],v226 offset:3072\n"
MU"v203,v252,v203\n"
MU"v204,v252,v204\n"
MF"v[16:19],a[112:115],v[96:99],v[16:19]\n"
WT"lgkmcnt(3)\n"
BL"v225,s[8:11],s43 offen offset:0 sc1 lds\n"
MU"v205,v252,v205\n"
MF"v[16:19],a[116:119],v[104:107],v[16:19]\n"
SA"s42,s42,1\n"
RD"a[80:83],v226 offset:4096\n"
MU"v206,v252,v206\n"
MU"v207,v252,v207\n"
MF"v[56:59],a[116:119],v[136:139],v[56:59]\n"
PK"v200,v200,v201\n"
PK"v201,v202,v203\n"
PK"v202,v204,v205\n"
MF"v[56:59],a[112:115],v[128:131],v[56:59]\n"
RD"a[84:87],v226 offset:5120\n"
PK"v203,v206,v207\n"
MF"v[20:23],a[120:123],v[96:99],v[20:23]\n"
BS"v[200:203],v224,s[8:11],s51 offen offset:3072 sc1\n"
MF"v[20:23],a[124:127],v[104:107],v[20:23]\n"
BL"v225,s[8:11],s43 offen offset:1024 sc1 lds\n"
MF"v[60:63],a[124:127],v[136:139],v[60:63]\n"
RD"a[88:91],v226 offset:6144\n"
MF"v[60:63],a[120:123],v[128:131],v[60:63]\n"
RD"a[92:95],v226 offset:7168\n"
MF"v[24:27],a[64:67],v[96:99],v[24:27]\n"
WT"lgkmcnt(3)\n"
BL"v225,s[8:11],s43 offen offset:2048 sc1 lds\n"
MF"v[24:27],a[68:71],v[104:107],v[24:27]\n"
RD"a[96:99],v227 offset:0\n"
MF"v[64:67],a[68:71],v[136:139],v[64:67]\n"
MF"v[64:67],a[64:67],v[128:131],v[64:67]\n"
RD"a[100:103],v227 offset:1024\n"
"v_bfe_u32 v238,v224,4,2\n"
MF"v[28:31],a[72:75],v[96:99],v[28:31]\n"
"s_andn2_b32 s63,s61,255\n"
BL"v225,s[8:11],s43 offen offset:3072 sc1 lds\n"
MF"v[28:31],a[76:79],v[104:107],v[28:31]\n"
SA"m0,s40,16384\n"
RD"a[104:107],v227 offset:2048\n"
"v_lshlrev_b32 v238,6,v238\n"
MF"v[68:71],a[76:79],v[136:139],v[68:71]\n"
"buffer_load_dwordx2 v[240:241],v238,s[8:11],s63 offen sc1\n"
MF"v[68:71],a[72:75],v[128:131],v[68:71]\n"
RD"a[108:111],v227 offset:3072\n"
MF"v[32:35],a[80:83],v[96:99],v[32:35]\n"
WT"lgkmcnt(3)\n"
BL"v225,s[8:11],s44 offen offset:0 sc1 lds\n"
MF"v[32:35],a[84:87],v[104:107],v[32:35]\n"
SA"s40,s40,32768\n"
RD"a[112:115],v227 offset:4096\n"
MF"v[72:75],a[84:87],v[136:139],v[72:75]\n"
"s_cmp_eq_u32 s40,s46\n"
"s_cselect_b32 s40,s47,s40\n"
MF"v[72:75],a[80:83],v[128:131],v[72:75]\n"
RD"a[116:119],v227 offset:5120\n"
MF"v[36:39],a[88:91],v[96:99],v[36:39]\n"
BL"v225,s[8:11],s44 offen offset:1024 sc1 lds\n"
MF"v[36:39],a[92:95],v[104:107],v[36:39]\n"
RD"a[120:123],v227 offset:6144\n"
MF"v[76:79],a[92:95],v[136:139],v[76:79]\n"
MF"v[76:79],a[88:91],v[128:131],v[76:79]\n"
RD"a[124:127],v227 offset:7168\n"
MF"v[84:87],a[96:99],v[144:147],0\n"
WT"lgkmcnt(3)\n"
BL"v225,s[8:11],s44 offen offset:2048 sc1 lds\n"
MF"v[84:87],a[100:103],v[152:155],v[84:87]\n"
RD"a[64:67],v227 offset:8192\n"
"v_readlane_b32 s43,v228,s42\n"
MF"v[164:167],a[100:103],v[184:187],0\n"
MF"v[164:167],a[96:99],v[176:179],v[164:167]\n"
RD"a[68:71],v227 offset:9216\n"
MF"v[92:95],a[104:107],v[144:147],0\n"
BL"v225,s[8:11],s44 offen offset:3072 sc1 lds\n"
MF"v[92:95],a[108:111],v[152:155],v[92:95]\n"
"s_mov_b32 m0,s40\n"
RD"a[72:75],v227 offset:10240\n"
MF"v[172:175],a[108:111],v[184:187],0\n"
"v_readlane_b32 s44,v229,s42\n"
MF"v[172:175],a[104:107],v[176:179],v[172:175]\n"
RD"a[76:79],v227 offset:11264\n"
MF"v[100:103],a[112:115],v[144:147],0\n"
WT"lgkmcnt(3)\n"
BL"v225,s[8:11],s43 offen offset:0 sc1 lds\n"
MF"v[100:103],a[116:119],v[152:155],v[100:103]\n"
SA"s42,s42,1\n"
RD"a[80:83],v227 offset:12288\n"
MF"v[180:183],a[116:119],v[184:187],0\n"
MF"v[180:183],a[112:115],v[176:179],v[180:183]\n"
RD"a[84:87],v227 offset:13312\n"
MF"v[108:111],a[120:123],v[144:147],0\n"
BL"v225,s[8:11],s43 offen offset:1024 sc1 lds\n"
MF"v[108:111],a[124:127],v[152:155],v[108:111]\n"
RD"a[88:91],v227 offset:14336\n"
MF"v[188:191],a[124:127],v[184:187],0\n"
MF"v[188:191],a[120:123],v[176:179],v[188:191]\n"
RD"a[92:95],v227 offset:15360\n"
MF"v[116:119],a[64:67],v[144:147],0\n"
WT"lgkmcnt(3)\n"
BL"v225,s[8:11],s43 offen offset:2048 sc1 lds\n"
MF"v[116:119],a[68:71],v[152:155],v[116:119]\n"
RD"a[96:99],v227 offset:16384\n"
MF"v[196:199],a[68:71],v[184:187],0\n"
MF"v[196:199],a[64:67],v[176:179],v[196:199]\n"
RD"a[100:103],v227 offset:17408\n"
MF"v[124:127],a[72:75],v[144:147],0\n"
BL"v225,s[8:11],s43 offen offset:3072 sc1 lds\n"
MF"v[124:127],a[76:79],v[152:155],v[124:127]\n"
SA"m0,s40,16384\n"
RD"a[104:107],v227 offset:18432\n"
MF"v[204:207],a[76:79],v[184:187],0\n"
MF"v[204:207],a[72:75],v[176:179],v[204:207]\n"
RD"a[108:111],v227 offset:19456\n"
MF"v[132:135],a[80:83],v[144:147],0\n"
WT"lgkmcnt(3)\n"
BL"v225,s[8:11],s44 offen offset:0 sc1 lds\n"
MF"v[132:135],a[84:87],v[152:155],v[132:135]\n"
SA"s40,s40,32768\n"
RD"a[112:115],v227 offset:20480\n"
MF"v[208:211],a[84:87],v[184:187],0\n"
"s_cmp_eq_u32 s40,s46\n"
"s_cselect_b32 s40,s47,s40\n"
MF"v[208:211],a[80:83],v[176:179],v[208:211]\n"
RD"a[116:119],v227 offset:21504\n"
MF"v[140:143],a[88:91],v[144:147],0\n"
BL"v225,s[8:11],s44 offen offset:1024 sc1 lds\n"
MF"v[140:143],a[92:95],v[152:155],v[140:143]\n"
SA"s48,s59,32768\n"
RD"a[120:123],v227 offset:22528\n"
MF"v[212:215],a[92:95],v[184:187],0\n"
WT"vmcnt(14)\n"
"buffer_store_dwordx2 v[232:233],v230,s[8:11],s62 offen sc1\n"
MF"v[212:215],a[88:91],v[176:179],v[212:215]\n"
"s_and_b32 s48,s48,0x18000\n"
RD"a[124:127],v227 offset:23552\n"
"s_andn2_b32 s49,s59,0x18000\n"
MF"v[148:151],a[96:99],v[144:147],0\n"
WT"lgkmcnt(3)\n"
BL"v225,s[8:11],s44 offen offset:2048 sc1 lds\n"
MF"v[148:151],a[100:103],v[152:155],v[148:151]\n"
"s_or_b32 s52,s48,s49\n"
WT"vmcnt(12)\n"
"v_cmp_eq_u64 vcc,s[28:29],v[240:241]\n"
"s_cmp_eq_u64 vcc,exec\n"
"s_cbranch_scc0 L1080_%=\n"
"L1081_%=:\n"
MF"v[216:219],a[100:103],v[184:187],0\n"
SA"s53,s52,16384\n"
RD"a[64:67],v227 offset:24576\n"
MF"v[216:219],a[96:99],v[176:179],v[216:219]\n"
BL"a[0:3],v224,s[8:11],s52 offen offset:0 sc1\n"
MF"v[156:159],a[104:107],v[144:147],0\n"
RD"a[68:71],v227 offset:25600\n"
MF"v[156:159],a[108:111],v[152:155],v[156:159]\n"
BL"v225,s[8:11],s44 offen offset:3072 sc1 lds\n"
MF"v[220:223],a[108:111],v[184:187],0\n"
RD"a[72:75],v227 offset:26624\n"
MF"v[220:223],a[104:107],v[176:179],v[220:223]\n"
BL"a[4:7],v224,s[8:11],s53 offen offset:0 sc1\n"
MF"v[84:87],a[112:115],v[160:163],v[84:87]\n"
WT"lgkmcnt(3)\n"
RD"a[76:79],v227 offset:27648\n"
"s_lshr_b32 s89,s31,3\n"
MF"v[84:87],a[116:119],v[168:171],v[84:87]\n"
"s_lshl_b32 s89,s89,4\n"
RD"a[80:83],v227 offset:28672\n"
MF"v[164:167],a[116:119],v[200:203],v[164:167]\n"
SA"s89,s89,s30\n"
BL"a[8:11],v224,s[8:11],s52 offen offset:1024 sc1\n"
MF"v[164:167],a[112:115],v[192:195],v[164:167]\n"
"s_lshl_b32 s89,s89,3\n"
RD"a[84:87],v227 offset:29696\n"
MF"v[92:95],a[120:123],v[160:163],v[92:95]\n"
"s_xor_b32 s23,s32,s30\n"
MF"v[92:95],a[124:127],v[168:171],v[92:95]\n"
SA"s89,s89,s23\n"
RD"a[88:91],v227 offset:30720\n"
MF"v[172:175],a[124:127],v[200:203],v[172:175]\n"
"s_xor_b32 s90,s89,36\n"
BL"a[12:15],v224,s[8:11],s53 offen offset:1024 sc1\n"
MF"v[172:175],a[120:123],v[192:195],v[172:175]\n"
WT"lgkmcnt(3)\n"
RD"a[92:95],v227 offset:31744\n"
"s_mul_i32 s88,s89,5120\n"
MF"v[100:103],a[64:67],v[160:163],v[100:103]\n"
"s_mul_i32 s22,s90,5120\n"
MF"v[100:103],a[68:71],v[168:171],v[100:103]\n"
"s_sub_u32 s90,s90,s89\n"
RD"a[96:99],v226 offset:0\n"
MF"v[180:183],a[68:71],v[200:203],v[180:183]\n"
"s_lshl_b32 s90,s90,6\n"
BL"a[16:19],v224,s[8:11],s52 offen offset:2048 sc1\n"
MF"v[180:183],a[64:67],v[192:195],v[180:183]\n"
SA"s88,s88,61603840\n"
RD"a[100:103],v226 offset:1024\n"
MF"v[108:111],a[72:75],v[160:163],v[108:111]\n"
SA"s22,s22,61603840\n"
MF"v[108:111],a[76:79],v[168:171],v[108:111]\n"
"s_cmp_eq_u32 s23,0\n"
RD"a[104:107],v226 offset:2048\n"
"s_cselect_b32 s88,2130706432,s88\n"
MF"v[188:191],a[76:79],v[200:203],v[188:191]\n"
"s_cmp_eq_u32 s23,4\n"
BL"a[20:23],v224,s[8:11],s53 offen offset:2048 sc1\n"
MF"v[188:191],a[72:75],v[192:195],v[188:191]\n"
WT"lgkmcnt(3)\n"
RD"a[108:111],v226 offset:3072\n"
"s_cselect_b32 s22,2130706432,s22\n"
MF"v[116:119],a[80:83],v[160:163],v[116:119]\n"
"s_lshl_b32 s89,s89,6\n"
SA"s89,s89,131072\n"
MF"v[116:119],a[84:87],v[168:171],v[116:119]\n"
RD"a[112:115],v226 offset:4096\n"
"v_bfe_u32 v239,v224,1,6\n"
MF"v[196:199],a[84:87],v[200:203],v[196:199]\n"
BL"a[24:27],v224,s[8:11],s52 offen offset:3072 sc1\n"
MF"v[196:199],a[80:83],v[192:195],v[196:199]\n"
RD"a[116:119],v226 offset:5120\n"
"v_bfe_u32 v242,v224,7,1\n"
MF"v[124:127],a[88:91],v[160:163],v[124:127]\n"
"v_mad_u32_u24 v239,v242,s90,v239\n"
"v_lshrrev_b32 v242,8,v224\n"
MF"v[124:127],a[92:95],v[168:171],v[124:127]\n"
SA"s48,s59,65536\n"
RD"a[120:123],v226 offset:6144\n"
"v_lshl_add_u32 v239,v242,27,v239\n"
MF"v[204:207],a[92:95],v[200:203],v[204:207]\n"
"s_and_b32 s48,s48,0x18000\n"
BL"a[28:31],v224,s[8:11],s53 offen offset:3072 sc1\n"
MF"v[204:207],a[88:91],v[192:195],v[204:207]\n"
WT"lgkmcnt(3)\n"
RD"a[124:127],v226 offset:7168\n"
"s_andn2_b32 s49,s59,0x18000\n"
MF"v[132:135],a[96:99],v[160:163],v[132:135]\n"
"s_or_b32 s52,s48,s49\n"
SA"s53,s52,16384\n"
MF"v[132:135],a[100:103],v[168:171],v[132:135]\n"
RD"a[64:67],v226 offset:8192\n"
MF"v[208:211],a[100:103],v[200:203],v[208:211]\n"
BL"a[180:183],v224,s[8:11],s52 offen offset:0 sc1\n"
MF"v[208:211],a[96:99],v[192:195],v[208:211]\n"
RD"a[68:71],v226 offset:9216\n"
MF"v[140:143],a[104:107],v[160:163],v[140:143]\n"
MF"v[140:143],a[108:111],v[168:171],v[140:143]\n"
RD"a[72:75],v226 offset:10240\n"
MF"v[212:215],a[108:111],v[200:203],v[212:215]\n"
BL"a[184:187],v224,s[8:11],s53 offen offset:0 sc1\n"
MF"v[212:215],a[104:107],v[192:195],v[212:215]\n"
WT"lgkmcnt(3)\n"
RD"a[76:79],v226 offset:11264\n"
MF"v[148:151],a[112:115],v[160:163],v[148:151]\n"
MF"v[148:151],a[116:119],v[168:171],v[148:151]\n"
RD"a[80:83],v226 offset:12288\n"
MF"v[216:219],a[116:119],v[200:203],v[216:219]\n"
BL"a[188:191],v224,s[8:11],s52 offen offset:1024 sc1\n"
MF"v[216:219],a[112:115],v[192:195],v[216:219]\n"
RD"a[84:87],v226 offset:13312\n"
MF"v[156:159],a[120:123],v[160:163],v[156:159]\n"
MF"v[156:159],a[124:127],v[168:171],v[156:159]\n"
RD"a[88:91],v226 offset:14336\n"
MF"v[220:223],a[124:127],v[200:203],v[220:223]\n"
BL"a[192:195],v224,s[8:11],s53 offen offset:1024 sc1\n"
MF"v[220:223],a[120:123],v[192:195],v[220:223]\n"
WT"vmcnt(12) lgkmcnt(3)\n"
RD"a[92:95],v226 offset:15360\n"
MF"v[0:3],a[64:67],a[0:3],v[0:3]\n"
WT"vmcnt(9)\n"
MF"v[0:3],a[68:71],a[8:11],v[0:3]\n"
WT"vmcnt(8)\n"
RD"a[96:99],v226 offset:16384\n"
MF"v[40:43],a[68:71],a[12:15],v[40:43]\n"
BL"a[196:199],v224,s[8:11],s52 offen offset:2048 sc1\n"
MF"v[40:43],a[64:67],a[4:7],v[40:43]\n"
RD"a[100:103],v226 offset:17408\n"
MF"v[4:7],a[72:75],a[0:3],v[4:7]\n"
MF"v[4:7],a[76:79],a[8:11],v[4:7]\n"
RD"a[104:107],v226 offset:18432\n"
MF"v[44:47],a[76:79],a[12:15],v[44:47]\n"
BL"a[200:203],v224,s[8:11],s53 offen offset:2048 sc1\n"
MF"v[44:47],a[72:75],a[4:7],v[44:47]\n"
WT"lgkmcnt(3)\n"
RD"a[108:111],v226 offset:19456\n"
MF"v[8:11],a[80:83],a[0:3],v[8:11]\n"
MF"v[8:11],a[84:87],a[8:11],v[8:11]\n"
RD"a[112:115],v226 offset:20480\n"
MF"v[48:51],a[84:87],a[12:15],v[48:51]\n"
BL"a[204:207],v224,s[8:11],s52 offen offset:3072 sc1\n"
MF"v[48:51],a[80:83],a[4:7],v[48:51]\n"
RD"a[116:119],v226 offset:21504\n"
MF"v[12:15],a[88:91],a[0:3],v[12:15]\n"
MF"v[12:15],a[92:95],a[8:11],v[12:15]\n"
SA"s48,s59,98304\n"
RD"a[120:123],v226 offset:22528\n"
MF"v[52:55],a[92:95],a[12:15],v[52:55]\n"
"s_and_b32 s48,s48,0x18000\n"
BL"a[208:211],v224,s[8:11],s53 offen offset:3072 sc1\n"
MF"v[52:55],a[88:91],a[4:7],v[52:55]\n"
WT"lgkmcnt(3)\n"
RD"a[124:127],v226 offset:23552\n"
"s_andn2_b32 s49,s59,0x18000\n"
MF"v[16:19],a[96:99],a[0:3],v[16:19]\n"
"s_or_b32 s52,s48,s49\n"
SA"s53,s52,16384\n"
MF"v[16:19],a[100:103],a[8:11],v[16:19]\n"
RD"a[64:67],v226 offset:24576\n"
MF"v[56:59],a[100:103],a[12:15],v[56:59]\n"
BL"a[212:215],v224,s[8:11],s52 offen offset:0 sc1\n"
MF"v[56:59],a[96:99],a[4:7],v[56:59]\n"
RD"a[68:71],v226 offset:25600\n"
MF"v[20:23],a[104:107],a[0:3],v[20:23]\n"
MF"v[20:23],a[108:111],a[8:11],v[20:23]\n"
RD"a[72:75],v226 offset:26624\n"
MF"v[60:63],a[108:111],a[12:15],v[60:63]\n"
BL"a[216:219],v224,s[8:11],s53 offen offset:0 sc1\n"
MF"v[60:63],a[104:107],a[4:7],v[60:63]\n"
WT"lgkmcnt(3)\n"
RD"a[76:79],v226 offset:27648\n"
MF"v[24:27],a[112:115],a[0:3],v[24:27]\n"
MF"v[24:27],a[116:119],a[8:11],v[24:27]\n"
RD"a[80:83],v226 offset:28672\n"
MF"v[64:67],a[116:119],a[12:15],v[64:67]\n"
BL"a[220:223],v224,s[8:11],s52 offen offset:1024 sc1\n"
MF"v[64:67],a[112:115],a[4:7],v[64:67]\n"
RD"a[84:87],v226 offset:29696\n"
MF"v[28:31],a[120:123],a[0:3],v[28:31]\n"
MF"v[28:31],a[124:127],a[8:11],v[28:31]\n"
RD"a[88:91],v226 offset:30720\n"
MF"v[68:71],a[124:127],a[12:15],v[68:71]\n"
BL"a[224:227],v224,s[8:11],s53 offen offset:1024 sc1\n"
MF"v[68:71],a[120:123],a[4:7],v[68:71]\n"
WT"lgkmcnt(3)\n"
RD"a[92:95],v226 offset:31744\n"
MF"v[32:35],a[64:67],a[0:3],v[32:35]\n"
"s_barrier\n"
MF"v[32:35],a[68:71],a[8:11],v[32:35]\n"
RD"a[96:99],v235 offset:0\n"
MF"v[72:75],a[68:71],a[12:15],v[72:75]\n"
BL"a[228:231],v224,s[8:11],s52 offen offset:2048 sc1\n"
MF"v[72:75],a[64:67],a[4:7],v[72:75]\n"
RD"a[100:103],v235 offset:1024\n"
MF"v[36:39],a[72:75],a[0:3],v[36:39]\n"
MF"v[36:39],a[76:79],a[8:11],v[36:39]\n"
RD"a[104:107],v235 offset:2048\n"
MF"v[76:79],a[76:79],a[12:15],v[76:79]\n"
BL"a[232:235],v224,s[8:11],s53 offen offset:2048 sc1\n"
MF"v[76:79],a[72:75],a[4:7],v[76:79]\n"
WT"vmcnt(17) lgkmcnt(3)\n"
RD"a[108:111],v235 offset:3072\n"
MF"v[0:3],a[80:83],a[16:19],v[0:3]\n"
WT"vmcnt(15)\n"
MF"v[0:3],a[84:87],a[24:27],v[0:3]\n"
WT"vmcnt(14)\n"
RD"a[112:115],v235 offset:4096\n"
MF"v[40:43],a[84:87],a[28:31],v[40:43]\n"
BL"a[236:239],v224,s[8:11],s52 offen offset:3072 sc1\n"
MF"v[40:43],a[80:83],a[20:23],v[40:43]\n"
RD"a[116:119],v235 offset:5120\n"
MF"v[4:7],a[88:91],a[16:19],v[4:7]\n"
MF"v[4:7],a[92:95],a[24:27],v[4:7]\n"
RD"a[120:123],v235 offset:6144\n"
MF"v[44:47],a[92:95],a[28:31],v[44:47]\n"
BL"a[240:243],v224,s[8:11],s53 offen offset:3072 sc1\n"
MF"v[44:47],a[88:91],a[20:23],v[44:47]\n"
WT"lgkmcnt(3)\n"
RD"a[124:127],v235 offset:7168\n"
MF"v[8:11],a[96:99],a[16:19],v[8:11]\n"
MF"v[8:11],a[100:103],a[24:27],v[8:11]\n"
RD"a[64:67],v235 offset:8192\n"
MF"v[48:51],a[100:103],a[28:31],v[48:51]\n"
MF"v[48:51],a[96:99],a[20:23],v[48:51]\n"
RD"a[68:71],v235 offset:9216\n"
MF"v[12:15],a[104:107],a[16:19],v[12:15]\n"
MF"v[12:15],a[108:111],a[24:27],v[12:15]\n"
RD"a[72:75],v235 offset:10240\n"
"v_bfe_u32 v238,v224,4,2\n"
MF"v[52:55],a[108:111],a[28:31],v[52:55]\n"
"s_andn2_b32 s63,s62,255\n"
"v_lshlrev_b32 v238,6,v238\n"
MF"v[52:55],a[104:107],a[20:23],v[52:55]\n"
WT"lgkmcnt(3)\n"
RD"a[76:79],v235 offset:11264\n"
MF"v[16:19],a[112:115],a[16:19],v[16:19]\n"
"buffer_load_dwordx2 v[240:241],v238,s[8:11],s63 offen sc1\n"
MF"v[16:19],a[116:119],a[24:27],v[16:19]\n"
RD"a[80:83],v235 offset:12288\n"
MF"v[56:59],a[116:119],a[28:31],v[56:59]\n"
MF"v[56:59],a[112:115],a[20:23],v[56:59]\n"
RD"a[84:87],v235 offset:13312\n"
MF"v[20:23],a[120:123],a[16:19],v[20:23]\n"
MF"v[20:23],a[124:127],a[24:27],v[20:23]\n"
RD"a[88:91],v235 offset:14336\n"
MF"v[60:63],a[124:127],a[28:31],v[60:63]\n"
MF"v[60:63],a[120:123],a[20:23],v[60:63]\n"
RD"a[92:95],v235 offset:15360\n"
"LTE_%=:\n"
"s_branch LTJ_%=\n"
"L1004_%=:\n"
"s_mov_b32 s54,0\n"
"L1006_%=:\n"
"s_sleep 2\n"
BL"v[160:163],v224,s[8:11],0 offen offset:256 sc1\n"
BL"v[164:167],v224,s[8:11],0 offen offset:1280 sc1\n"
WT"vmcnt(0)\n"
SA"s54,s54,1\n"
"v_cmp_eq_u64 vcc,s[28:29],v[160:161]\n"
"v_cmp_eq_u64_e64 s[62:63],s[28:29],v[162:163]\n"
"s_and_b64 vcc,vcc,s[62:63]\n"
"v_cmp_eq_u64_e64 s[62:63],s[28:29],v[164:165]\n"
"s_and_b64 vcc,vcc,s[62:63]\n"
"v_cmp_eq_u64_e64 s[62:63],s[28:29],v[166:167]\n"
"s_and_b64 vcc,vcc,s[62:63]\n"
"s_cmp_eq_u64 vcc,exec\n"
"s_cbranch_scc1 L1005_%=\n"
"s_cmp_lt_u32 s54,1048576\n"
"s_cbranch_scc1 L1006_%=\n"
"s_branch L1005_%=\n"
"L1007_%=:\n"
"s_mov_b32 s54,0\n"
"L1009_%=:\n"
"v_max3_u32 v226,v160,v161,v162\n"
"v_max3_u32 v226,v226,v163,v164\n"
"v_max3_u32 v226,v226,v165,v166\n"
"v_max3_u32 v226,v226,v167,v168\n"
"v_max3_u32 v226,v226,v169,v170\n"
"v_max3_u32 v226,v226,v171,v172\n"
"v_max3_u32 v226,v226,v173,v174\n"
"v_max3_u32 v226,v226,v175,v176\n"
"v_max3_u32 v226,v226,v177,v178\n"
"v_max3_u32 v226,v226,v179,v180\n"
"v_max3_u32 v226,v226,v181,v182\n"
"v_max3_u32 v226,v226,v183,v184\n"
"v_max3_u32 v226,v226,v185,v186\n"
"v_max3_u32 v226,v226,v187,v188\n"
"v_max3_u32 v226,v226,v189,v190\n"
"v_max_u32 v226,v226,v191\n"
"v_mov_b32 v229,v226\n"
"s_nop 1\n"
"v_max_u32_dpp v229,v229,v229 row_shr:1 row_mask:0xf bank_mask:0xf\n"
"s_nop 1\n"
"v_max_u32_dpp v229,v229,v229 row_shr:2 row_mask:0xf bank_mask:0xf\n"
"s_nop 1\n"
"v_max_u32_dpp v229,v229,v229 row_shr:4 row_mask:0xf bank_mask:0xf\n"
"s_nop 1\n"
"v_max_u32_dpp v229,v229,v229 row_shr:8 row_mask:0xf bank_mask:0xf\n"
"s_nop 1\n"
"v_max_u32_dpp v229,v229,v229 row_bcast:15 row_mask:0xa bank_mask:0xf\n"
"s_nop 1\n"
"v_max_u32_dpp v229,v229,v229 row_bcast:31 row_mask:0xc bank_mask:0xf\n"
"s_nop 1\n"
"v_readlane_b32 s62,v229,63\n"
"s_nop 1\n"
"v_cmp_eq_u32 vcc,s62,v160\n"
"s_nop 1\n"
"v_cndmask_b32_e64 v160,v160,0,vcc\n"
"v_cmp_eq_u32 vcc,s62,v161\n"
"s_nop 1\n"
"v_cndmask_b32_e64 v161,v161,0,vcc\n"
"v_cmp_eq_u32 vcc,s62,v162\n"
"s_nop 1\n"
"v_cndmask_b32_e64 v162,v162,0,vcc\n"
"v_cmp_eq_u32 vcc,s62,v163\n"
"s_nop 1\n"
"v_cndmask_b32_e64 v163,v163,0,vcc\n"
"v_cmp_eq_u32 vcc,s62,v164\n"
"s_nop 1\n"
"v_cndmask_b32_e64 v164,v164,0,vcc\n"
"v_cmp_eq_u32 vcc,s62,v165\n"
"s_nop 1\n"
"v_cndmask_b32_e64 v165,v165,0,vcc\n"
"v_cmp_eq_u32 vcc,s62,v166\n"
"s_nop 1\n"
"v_cndmask_b32_e64 v166,v166,0,vcc\n"
"v_cmp_eq_u32 vcc,s62,v167\n"
"s_nop 1\n"
"v_cndmask_b32_e64 v167,v167,0,vcc\n"
"v_cmp_eq_u32 vcc,s62,v168\n"
"s_nop 1\n"
"v_cndmask_b32_e64 v168,v168,0,vcc\n"
"v_cmp_eq_u32 vcc,s62,v169\n"
"s_nop 1\n"
"v_cndmask_b32_e64 v169,v169,0,vcc\n"
"v_cmp_eq_u32 vcc,s62,v170\n"
"s_nop 1\n"
"v_cndmask_b32_e64 v170,v170,0,vcc\n"
"v_cmp_eq_u32 vcc,s62,v171\n"
"s_nop 1\n"
"v_cndmask_b32_e64 v171,v171,0,vcc\n"
"v_cmp_eq_u32 vcc,s62,v172\n"
"s_nop 1\n"
"v_cndmask_b32_e64 v172,v172,0,vcc\n"
"v_cmp_eq_u32 vcc,s62,v173\n"
"s_nop 1\n"
"v_cndmask_b32_e64 v173,v173,0,vcc\n"
"v_cmp_eq_u32 vcc,s62,v174\n"
"s_nop 1\n"
"v_cndmask_b32_e64 v174,v174,0,vcc\n"
"v_cmp_eq_u32 vcc,s62,v175\n"
"s_nop 1\n"
"v_cndmask_b32_e64 v175,v175,0,vcc\n"
"v_cmp_eq_u32 vcc,s62,v176\n"
"s_nop 1\n"
"v_cndmask_b32_e64 v176,v176,0,vcc\n"
"v_cmp_eq_u32 vcc,s62,v177\n"
"s_nop 1\n"
"v_cndmask_b32_e64 v177,v177,0,vcc\n"
"v_cmp_eq_u32 vcc,s62,v178\n"
"s_nop 1\n"
"v_cndmask_b32_e64 v178,v178,0,vcc\n"
"v_cmp_eq_u32 vcc,s62,v179\n"
"s_nop 1\n"
"v_cndmask_b32_e64 v179,v179,0,vcc\n"
"v_cmp_eq_u32 vcc,s62,v180\n"
"s_nop 1\n"
"v_cndmask_b32_e64 v180,v180,0,vcc\n"
"v_cmp_eq_u32 vcc,s62,v181\n"
"s_nop 1\n"
"v_cndmask_b32_e64 v181,v181,0,vcc\n"
"v_cmp_eq_u32 vcc,s62,v182\n"
"s_nop 1\n"
"v_cndmask_b32_e64 v182,v182,0,vcc\n"
"v_cmp_eq_u32 vcc,s62,v183\n"
"s_nop 1\n"
"v_cndmask_b32_e64 v183,v183,0,vcc\n"
"v_cmp_eq_u32 vcc,s62,v184\n"
"s_nop 1\n"
"v_cndmask_b32_e64 v184,v184,0,vcc\n"
"v_cmp_eq_u32 vcc,s62,v185\n"
"s_nop 1\n"
"v_cndmask_b32_e64 v185,v185,0,vcc\n"
"v_cmp_eq_u32 vcc,s62,v186\n"
"s_nop 1\n"
"v_cndmask_b32_e64 v186,v186,0,vcc\n"
"v_cmp_eq_u32 vcc,s62,v187\n"
"s_nop 1\n"
"v_cndmask_b32_e64 v187,v187,0,vcc\n"
"v_cmp_eq_u32 vcc,s62,v188\n"
"s_nop 1\n"
"v_cndmask_b32_e64 v188,v188,0,vcc\n"
"v_cmp_eq_u32 vcc,s62,v189\n"
"s_nop 1\n"
"v_cndmask_b32_e64 v189,v189,0,vcc\n"
"v_cmp_eq_u32 vcc,s62,v190\n"
"s_nop 1\n"
"v_cndmask_b32_e64 v190,v190,0,vcc\n"
"v_cmp_eq_u32 vcc,s62,v191\n"
"s_nop 1\n"
"v_cndmask_b32_e64 v191,v191,0,vcc\n"
"v_mov_b32 v229,s62\n"
"s_lshl_b64 exec,1,s54\n"
"v_mov_b32 v225,v229\n"
"s_mov_b64 exec,-1\n"
SA"s54,s54,1\n"
"s_cmp_lt_u32 s54,8\n"
"s_cbranch_scc1 L1009_%=\n"
"s_nop 4\n"
"s_branch L1008_%=\n"
"L1010_%=:\n"
"s_mov_b32 s54,0\n"
"L1012_%=:\n"
"s_sleep 2\n"
"buffer_load_dwordx2 v[208:209],v252,s[8:11],s50 offen sc1\n"
WT"vmcnt(0)\n"
SA"s54,s54,1\n"
"v_cmp_eq_u64 vcc,s[28:29],v[208:209]\n"
"s_cmp_eq_u64 vcc,exec\n"
"s_cbranch_scc1 L1011_%=\n"
"s_cmp_lt_u32 s54,1048576\n"
"s_cbranch_scc1 L1012_%=\n"
"s_branch L1011_%=\n"
"L1014_%=:\n"
"s_nop 15\n"
WT"vmcnt(0)\n"
"s_lshl_b32 s89,s34,3\n"
SA"s89,s89,s30\n"
SA"s89,s89,0\n"
"s_lshl_b32 s91,s89,4\n"
"s_lshr_b32 s89,s89,1\n"
"v_bfe_u32 v249,v224,4,4\n"
"v_lshrrev_b32 v254,8,v224\n"
"v_lshlrev_b32 v254,2,v254\n"
"v_sub_u32 v249,v249,v254\n"
"v_add_u32 v249,s91,v249\n"
"v_mov_b32 v255,0xff800000\n"
"v_mov_b32 v248,v255\n"
"s_mov_b32 s90,0\n"
"s_mov_b32 s88,0\n"
"v_mov_b32 v80,0\n"
"v_mov_b32 v81,0\n"
"v_mov_b32 v82,0\n"
"v_mov_b32 v83,0\n"
"v_mov_b32 v84,0\n"
"v_mov_b32 v85,0\n"
"v_mov_b32 v86,0\n"
"v_mov_b32 v87,0\n"
"v_mov_b32 v88,0\n"
"v_mov_b32 v89,0\n"
"v_mov_b32 v90,0\n"
"v_mov_b32 v91,0\n"
"v_mov_b32 v92,0\n"
"v_mov_b32 v93,0\n"
"v_mov_b32 v94,0\n"
"v_mov_b32 v95,0\n"
"v_mov_b32 v96,0\n"
"v_mov_b32 v97,0\n"
"v_mov_b32 v98,0\n"
"v_mov_b32 v99,0\n"
"v_mov_b32 v100,0\n"
"v_mov_b32 v101,0\n"
"v_mov_b32 v102,0\n"
"v_mov_b32 v103,0\n"
"v_mov_b32 v104,0\n"
"v_mov_b32 v105,0\n"
"v_mov_b32 v106,0\n"
"v_mov_b32 v107,0\n"
"v_mov_b32 v108,0\n"
"v_mov_b32 v109,0\n"
"v_mov_b32 v110,0\n"
"v_mov_b32 v111,0\n"
"v_accvgpr_write_b32 a132,0\n"
"v_accvgpr_write_b32 a133,0\n"
"v_accvgpr_write_b32 a134,0\n"
"v_accvgpr_write_b32 a135,0\n"
"L1015_%=:\n"
"s_lshl_b32 s91,s88,13\n"
SA"s91,s91,s39\n"
SA"s91,s91,0\n"
BL"a[148:151],v224,s[8:11],s91 offen offset:0 sc1\n"
BL"a[152:155],v224,s[8:11],s91 offen offset:1024 sc1\n"
BL"a[156:159],v224,s[8:11],s91 offen offset:2048 sc1\n"
BL"a[160:163],v224,s[8:11],s91 offen offset:3072 sc1\n"
SA"s91,s91,4096\n"
BL"a[164:167],v224,s[8:11],s91 offen offset:0 sc1\n"
BL"a[168:171],v224,s[8:11],s91 offen offset:1024 sc1\n"
BL"a[172:175],v224,s[8:11],s91 offen offset:2048 sc1\n"
BL"a[176:179],v224,s[8:11],s91 offen offset:3072 sc1\n"
WT"vmcnt(0)\n"
MF"v[240:243],a[148:151],a[0:3],0\n"
MF"v[240:243],a[152:155],a[4:7],v[240:243]\n"
MF"v[240:243],a[156:159],a[8:11],v[240:243]\n"
MF"v[240:243],a[160:163],a[12:15],v[240:243]\n"
MF"v[244:247],a[164:167],a[0:3],0\n"
MF"v[244:247],a[168:171],a[4:7],v[244:247]\n"
MF"v[244:247],a[172:175],a[8:11],v[244:247]\n"
MF"v[244:247],a[176:179],a[12:15],v[244:247]\n"
"s_lshl_b32 s91,s88,5\n"
"v_subrev_u32 v250,s91,v249\n"
"s_nop 7\n"
"v_cmp_gt_i32 vcc,0,v250\n"
"s_nop 1\n"
"v_cndmask_b32 v240,v240,v255,vcc\n"
"v_cmp_gt_i32 vcc,1,v250\n"
"s_nop 1\n"
"v_cndmask_b32 v241,v241,v255,vcc\n"
"v_cmp_gt_i32 vcc,2,v250\n"
"s_nop 1\n"
"v_cndmask_b32 v242,v242,v255,vcc\n"
"v_cmp_gt_i32 vcc,3,v250\n"
"s_nop 1\n"
"v_cndmask_b32 v243,v243,v255,vcc\n"
"v_cmp_gt_i32 vcc,16,v250\n"
"s_nop 1\n"
"v_cndmask_b32 v244,v244,v255,vcc\n"
"v_cmp_gt_i32 vcc,17,v250\n"
"s_nop 1\n"
"v_cndmask_b32 v245,v245,v255,vcc\n"
"v_cmp_gt_i32 vcc,18,v250\n"
"s_nop 1\n"
"v_cndmask_b32 v246,v246,v255,vcc\n"
"v_cmp_gt_i32 vcc,19,v250\n"
"s_nop 1\n"
"v_cndmask_b32 v247,v247,v255,vcc\n"
"s_cmp_lg_u32 s90,0\n"
"s_cbranch_scc1 L1016_%=\n"
"v_max3_f32 v248,v248,v240,v241\n"
"v_max3_f32 v248,v248,v242,v243\n"
"v_max3_f32 v248,v248,v244,v245\n"
"v_max3_f32 v248,v248,v246,v247\n"
"s_branch L1017_%=\n"
"L1016_%=:\n"
"s_lshl_b32 s91,s88,13\n"
SA"s91,s91,s39\n"
SA"s91,s91,524288\n"
BL"a[148:151],v224,s[8:11],s91 offen offset:0 sc1\n"
BL"a[152:155],v224,s[8:11],s91 offen offset:1024 sc1\n"
BL"a[156:159],v224,s[8:11],s91 offen offset:2048 sc1\n"
BL"a[160:163],v224,s[8:11],s91 offen offset:3072 sc1\n"
SA"s91,s91,4096\n"
BL"a[164:167],v224,s[8:11],s91 offen offset:0 sc1\n"
BL"a[168:171],v224,s[8:11],s91 offen offset:1024 sc1\n"
BL"a[172:175],v224,s[8:11],s91 offen offset:2048 sc1\n"
BL"a[176:179],v224,s[8:11],s91 offen offset:3072 sc1\n"
WT"vmcnt(0)\n"
"v_sub_f32 v240,v240,v248\n"
"v_sub_f32 v241,v241,v248\n"
"v_sub_f32 v242,v242,v248\n"
"v_sub_f32 v243,v243,v248\n"
"v_sub_f32 v244,v244,v248\n"
"v_sub_f32 v245,v245,v248\n"
"v_sub_f32 v246,v246,v248\n"
"v_sub_f32 v247,v247,v248\n"
EX"v240,v240\n"
EX"v241,v241\n"
EX"v242,v242\n"
EX"v243,v243\n"
EX"v244,v244\n"
EX"v245,v245\n"
EX"v246,v246\n"
EX"v247,v247\n"
"s_nop 0\n"
PK"v240,v240,v241\n"
PK"v241,v242,v243\n"
PK"v242,v244,v245\n"
PK"v243,v246,v247\n"
"s_nop 1\n"
MF"v[80:83],a[148:151],v[240:243],v[80:83]\n"
MF"v[84:87],a[152:155],v[240:243],v[84:87]\n"
MF"v[88:91],a[156:159],v[240:243],v[88:91]\n"
MF"v[92:95],a[160:163],v[240:243],v[92:95]\n"
MF"v[96:99],a[164:167],v[240:243],v[96:99]\n"
MF"v[100:103],a[168:171],v[240:243],v[100:103]\n"
MF"v[104:107],a[172:175],v[240:243],v[104:107]\n"
MF"v[108:111],a[176:179],v[240:243],v[108:111]\n"
MF"a[132:135],a[128:131],v[240:243],a[132:135]\n"
"s_nop 7\n"
"L1017_%=:\n"
SA"s88,s88,1\n"
"s_cmp_le_u32 s88,s89\n"
"s_cbranch_scc1 L1015_%=\n"
"s_cmp_lg_u32 s90,0\n"
"s_cbranch_scc1 L1018_%=\n"
"v_mov_b32 v254,v248\n"
"s_nop 1\n"
"v_permlane16_swap_b32 v254,v248\n"
"s_nop 1\n"
"v_max_f32 v248,v248,v254\n"
"v_mov_b32 v254,v248\n"
"s_nop 1\n"
"v_permlane32_swap_b32 v254,v248\n"
"s_nop 1\n"
"v_max_f32 v248,v248,v254\n"
"s_mov_b32 s90,1\n"
"s_mov_b32 s88,0\n"
"s_branch L1015_%=\n"
"L1018_%=:\n"
"s_nop 7\n"
"v_accvgpr_read_b32 v251,a132\n"
"s_nop 1\n"
"s_branch L1013_%=\n"
"L1019_%=:\n"
"s_mov_b32 s54,0\n"
"L1021_%=:\n"
"s_sleep 2\n"
"buffer_load_dwordx2 v[238:239],v237,s[8:11],s50 offen sc1\n"
WT"vmcnt(0)\n"
SA"s54,s54,1\n"
"v_cmp_eq_u64 vcc,s[28:29],v[238:239]\n"
"s_cmp_eq_u64 vcc,exec\n"
"s_cbranch_scc1 L1020_%=\n"
"s_cmp_lt_u32 s54,1048576\n"
"s_cbranch_scc1 L1021_%=\n"
"s_branch L1020_%=\n"
"L1023_%=:\n"
"s_nop 15\n"
WT"vmcnt(0)\n"
"s_lshl_b32 s89,s34,3\n"
SA"s89,s89,s30\n"
SA"s89,s89,4\n"
"s_lshl_b32 s91,s89,4\n"
"s_lshr_b32 s89,s89,1\n"
"v_bfe_u32 v249,v224,4,4\n"
"v_lshrrev_b32 v254,8,v224\n"
"v_lshlrev_b32 v254,2,v254\n"
"v_sub_u32 v249,v249,v254\n"
"v_add_u32 v249,s91,v249\n"
"v_mov_b32 v255,0xff800000\n"
"v_mov_b32 v248,v255\n"
"s_mov_b32 s90,0\n"
"s_mov_b32 s88,0\n"
"v_mov_b32 v112,0\n"
"v_mov_b32 v113,0\n"
"v_mov_b32 v114,0\n"
"v_mov_b32 v115,0\n"
"v_mov_b32 v116,0\n"
"v_mov_b32 v117,0\n"
"v_mov_b32 v118,0\n"
"v_mov_b32 v119,0\n"
"v_mov_b32 v120,0\n"
"v_mov_b32 v121,0\n"
"v_mov_b32 v122,0\n"
"v_mov_b32 v123,0\n"
"v_mov_b32 v124,0\n"
"v_mov_b32 v125,0\n"
"v_mov_b32 v126,0\n"
"v_mov_b32 v127,0\n"
"v_mov_b32 v128,0\n"
"v_mov_b32 v129,0\n"
"v_mov_b32 v130,0\n"
"v_mov_b32 v131,0\n"
"v_mov_b32 v132,0\n"
"v_mov_b32 v133,0\n"
"v_mov_b32 v134,0\n"
"v_mov_b32 v135,0\n"
"v_mov_b32 v136,0\n"
"v_mov_b32 v137,0\n"
"v_mov_b32 v138,0\n"
"v_mov_b32 v139,0\n"
"v_mov_b32 v140,0\n"
"v_mov_b32 v141,0\n"
"v_mov_b32 v142,0\n"
"v_mov_b32 v143,0\n"
"v_accvgpr_write_b32 a136,0\n"
"v_accvgpr_write_b32 a137,0\n"
"v_accvgpr_write_b32 a138,0\n"
"v_accvgpr_write_b32 a139,0\n"
"L1024_%=:\n"
"s_lshl_b32 s91,s88,13\n"
SA"s91,s91,s39\n"
SA"s91,s91,0\n"
BL"a[148:151],v224,s[8:11],s91 offen offset:0 sc1\n"
BL"a[152:155],v224,s[8:11],s91 offen offset:1024 sc1\n"
BL"a[156:159],v224,s[8:11],s91 offen offset:2048 sc1\n"
BL"a[160:163],v224,s[8:11],s91 offen offset:3072 sc1\n"
SA"s91,s91,4096\n"
BL"a[164:167],v224,s[8:11],s91 offen offset:0 sc1\n"
BL"a[168:171],v224,s[8:11],s91 offen offset:1024 sc1\n"
BL"a[172:175],v224,s[8:11],s91 offen offset:2048 sc1\n"
BL"a[176:179],v224,s[8:11],s91 offen offset:3072 sc1\n"
WT"vmcnt(0)\n"
MF"v[240:243],a[148:151],a[16:19],0\n"
MF"v[240:243],a[152:155],a[20:23],v[240:243]\n"
MF"v[240:243],a[156:159],a[24:27],v[240:243]\n"
MF"v[240:243],a[160:163],a[28:31],v[240:243]\n"
MF"v[244:247],a[164:167],a[16:19],0\n"
MF"v[244:247],a[168:171],a[20:23],v[244:247]\n"
MF"v[244:247],a[172:175],a[24:27],v[244:247]\n"
MF"v[244:247],a[176:179],a[28:31],v[244:247]\n"
"s_lshl_b32 s91,s88,5\n"
"v_subrev_u32 v250,s91,v249\n"
"s_nop 7\n"
"v_cmp_gt_i32 vcc,0,v250\n"
"s_nop 1\n"
"v_cndmask_b32 v240,v240,v255,vcc\n"
"v_cmp_gt_i32 vcc,1,v250\n"
"s_nop 1\n"
"v_cndmask_b32 v241,v241,v255,vcc\n"
"v_cmp_gt_i32 vcc,2,v250\n"
"s_nop 1\n"
"v_cndmask_b32 v242,v242,v255,vcc\n"
"v_cmp_gt_i32 vcc,3,v250\n"
"s_nop 1\n"
"v_cndmask_b32 v243,v243,v255,vcc\n"
"v_cmp_gt_i32 vcc,16,v250\n"
"s_nop 1\n"
"v_cndmask_b32 v244,v244,v255,vcc\n"
"v_cmp_gt_i32 vcc,17,v250\n"
"s_nop 1\n"
"v_cndmask_b32 v245,v245,v255,vcc\n"
"v_cmp_gt_i32 vcc,18,v250\n"
"s_nop 1\n"
"v_cndmask_b32 v246,v246,v255,vcc\n"
"v_cmp_gt_i32 vcc,19,v250\n"
"s_nop 1\n"
"v_cndmask_b32 v247,v247,v255,vcc\n"
"s_cmp_lg_u32 s90,0\n"
"s_cbranch_scc1 L1025_%=\n"
"v_max3_f32 v248,v248,v240,v241\n"
"v_max3_f32 v248,v248,v242,v243\n"
"v_max3_f32 v248,v248,v244,v245\n"
"v_max3_f32 v248,v248,v246,v247\n"
"s_branch L1026_%=\n"
"L1025_%=:\n"
"s_lshl_b32 s91,s88,13\n"
SA"s91,s91,s39\n"
SA"s91,s91,524288\n"
BL"a[148:151],v224,s[8:11],s91 offen offset:0 sc1\n"
BL"a[152:155],v224,s[8:11],s91 offen offset:1024 sc1\n"
BL"a[156:159],v224,s[8:11],s91 offen offset:2048 sc1\n"
BL"a[160:163],v224,s[8:11],s91 offen offset:3072 sc1\n"
SA"s91,s91,4096\n"
BL"a[164:167],v224,s[8:11],s91 offen offset:0 sc1\n"
BL"a[168:171],v224,s[8:11],s91 offen offset:1024 sc1\n"
BL"a[172:175],v224,s[8:11],s91 offen offset:2048 sc1\n"
BL"a[176:179],v224,s[8:11],s91 offen offset:3072 sc1\n"
WT"vmcnt(0)\n"
"v_sub_f32 v240,v240,v248\n"
"v_sub_f32 v241,v241,v248\n"
"v_sub_f32 v242,v242,v248\n"
"v_sub_f32 v243,v243,v248\n"
"v_sub_f32 v244,v244,v248\n"
"v_sub_f32 v245,v245,v248\n"
"v_sub_f32 v246,v246,v248\n"
"v_sub_f32 v247,v247,v248\n"
EX"v240,v240\n"
EX"v241,v241\n"
EX"v242,v242\n"
EX"v243,v243\n"
EX"v244,v244\n"
EX"v245,v245\n"
EX"v246,v246\n"
EX"v247,v247\n"
"s_nop 0\n"
PK"v240,v240,v241\n"
PK"v241,v242,v243\n"
PK"v242,v244,v245\n"
PK"v243,v246,v247\n"
"s_nop 1\n"
MF"v[112:115],a[148:151],v[240:243],v[112:115]\n"
MF"v[116:119],a[152:155],v[240:243],v[116:119]\n"
MF"v[120:123],a[156:159],v[240:243],v[120:123]\n"
MF"v[124:127],a[160:163],v[240:243],v[124:127]\n"
MF"v[128:131],a[164:167],v[240:243],v[128:131]\n"
MF"v[132:135],a[168:171],v[240:243],v[132:135]\n"
MF"v[136:139],a[172:175],v[240:243],v[136:139]\n"
MF"v[140:143],a[176:179],v[240:243],v[140:143]\n"
MF"a[136:139],a[128:131],v[240:243],a[136:139]\n"
"s_nop 7\n"
"L1026_%=:\n"
SA"s88,s88,1\n"
"s_cmp_le_u32 s88,s89\n"
"s_cbranch_scc1 L1024_%=\n"
"s_cmp_lg_u32 s90,0\n"
"s_cbranch_scc1 L1027_%=\n"
"v_mov_b32 v254,v248\n"
"s_nop 1\n"
"v_permlane16_swap_b32 v254,v248\n"
"s_nop 1\n"
"v_max_f32 v248,v248,v254\n"
"v_mov_b32 v254,v248\n"
"s_nop 1\n"
"v_permlane32_swap_b32 v254,v248\n"
"s_nop 1\n"
"v_max_f32 v248,v248,v254\n"
"s_mov_b32 s90,1\n"
"s_mov_b32 s88,0\n"
"s_branch L1024_%=\n"
"L1027_%=:\n"
"s_nop 7\n"
"v_accvgpr_read_b32 v252,a136\n"
"s_nop 1\n"
"s_branch L1022_%=\n"
"L1028_%=:\n"
"s_mov_b32 s54,0\n"
"L1030_%=:\n"
"s_sleep 2\n"
"buffer_load_dwordx2 v[242:243],v236,s[8:11],0 offen sc1\n"
WT"vmcnt(0)\n"
SA"s54,s54,1\n"
"v_cmp_eq_u64 vcc,s[28:29],v[242:243]\n"
"s_or_b32 vcc_hi,vcc_hi,-1\n"
"s_cmp_eq_u64 vcc,exec\n"
"s_cbranch_scc1 L1029_%=\n"
"s_cmp_lt_u32 s54,1048576\n"
"s_cbranch_scc1 L1030_%=\n"
"s_branch L1029_%=\n"
"L1031_%=:\n"
"s_mov_b32 s54,0\n"
"L1033_%=:\n"
"s_sleep 2\n"
"buffer_load_dwordx2 v[242:243],v236,s[8:11],0 offen sc1\n"
WT"vmcnt(0)\n"
SA"s54,s54,1\n"
"v_cmp_eq_u64 vcc,s[28:29],v[242:243]\n"
"s_or_b32 vcc_lo,vcc_lo,-1\n"
"s_cmp_eq_u64 vcc,exec\n"
"s_cbranch_scc1 L1032_%=\n"
"s_cmp_lt_u32 s54,1048576\n"
"s_cbranch_scc1 L1033_%=\n"
"s_branch L1032_%=\n"
"L1035_%=:\n"
"s_nop 15\n"
WT"vmcnt(0)\n"
"s_lshl_b32 s89,s34,3\n"
SA"s89,s89,s30\n"
SA"s89,s89,4\n"
"s_lshl_b32 s91,s89,4\n"
"s_lshr_b32 s89,s89,1\n"
"v_bfe_u32 v249,v224,4,4\n"
"v_lshrrev_b32 v254,8,v224\n"
"v_lshlrev_b32 v254,2,v254\n"
"v_sub_u32 v249,v249,v254\n"
"v_add_u32 v249,s91,v249\n"
"v_mov_b32 v255,0xff800000\n"
"v_mov_b32 v248,v255\n"
"s_mov_b32 s90,0\n"
"s_mov_b32 s88,0\n"
"v_mov_b32 v112,0\n"
"v_mov_b32 v113,0\n"
"v_mov_b32 v114,0\n"
"v_mov_b32 v115,0\n"
"v_mov_b32 v116,0\n"
"v_mov_b32 v117,0\n"
"v_mov_b32 v118,0\n"
"v_mov_b32 v119,0\n"
"v_mov_b32 v120,0\n"
"v_mov_b32 v121,0\n"
"v_mov_b32 v122,0\n"
"v_mov_b32 v123,0\n"
"v_mov_b32 v124,0\n"
"v_mov_b32 v125,0\n"
"v_mov_b32 v126,0\n"
"v_mov_b32 v127,0\n"
"v_mov_b32 v128,0\n"
"v_mov_b32 v129,0\n"
"v_mov_b32 v130,0\n"
"v_mov_b32 v131,0\n"
"v_mov_b32 v132,0\n"
"v_mov_b32 v133,0\n"
"v_mov_b32 v134,0\n"
"v_mov_b32 v135,0\n"
"v_mov_b32 v136,0\n"
"v_mov_b32 v137,0\n"
"v_mov_b32 v138,0\n"
"v_mov_b32 v139,0\n"
"v_mov_b32 v140,0\n"
"v_mov_b32 v141,0\n"
"v_mov_b32 v142,0\n"
"v_mov_b32 v143,0\n"
"v_accvgpr_write_b32 a136,0\n"
"v_accvgpr_write_b32 a137,0\n"
"v_accvgpr_write_b32 a138,0\n"
"v_accvgpr_write_b32 a139,0\n"
"L1036_%=:\n"
"s_lshl_b32 s91,s88,13\n"
SA"s91,s91,s39\n"
SA"s91,s91,0\n"
BL"a[148:151],v224,s[8:11],s91 offen offset:0 sc1\n"
BL"a[152:155],v224,s[8:11],s91 offen offset:1024 sc1\n"
BL"a[156:159],v224,s[8:11],s91 offen offset:2048 sc1\n"
BL"a[160:163],v224,s[8:11],s91 offen offset:3072 sc1\n"
SA"s91,s91,4096\n"
BL"a[164:167],v224,s[8:11],s91 offen offset:0 sc1\n"
BL"a[168:171],v224,s[8:11],s91 offen offset:1024 sc1\n"
BL"a[172:175],v224,s[8:11],s91 offen offset:2048 sc1\n"
BL"a[176:179],v224,s[8:11],s91 offen offset:3072 sc1\n"
WT"vmcnt(0)\n"
MF"v[240:243],a[148:151],a[16:19],0\n"
MF"v[240:243],a[152:155],a[20:23],v[240:243]\n"
MF"v[240:243],a[156:159],a[24:27],v[240:243]\n"
MF"v[240:243],a[160:163],a[28:31],v[240:243]\n"
MF"v[244:247],a[164:167],a[16:19],0\n"
MF"v[244:247],a[168:171],a[20:23],v[244:247]\n"
MF"v[244:247],a[172:175],a[24:27],v[244:247]\n"
MF"v[244:247],a[176:179],a[28:31],v[244:247]\n"
"s_lshl_b32 s91,s88,5\n"
"v_subrev_u32 v250,s91,v249\n"
"s_nop 7\n"
"v_cmp_gt_i32 vcc,0,v250\n"
"s_nop 1\n"
"v_cndmask_b32 v240,v240,v255,vcc\n"
"v_cmp_gt_i32 vcc,1,v250\n"
"s_nop 1\n"
"v_cndmask_b32 v241,v241,v255,vcc\n"
"v_cmp_gt_i32 vcc,2,v250\n"
"s_nop 1\n"
"v_cndmask_b32 v242,v242,v255,vcc\n"
"v_cmp_gt_i32 vcc,3,v250\n"
"s_nop 1\n"
"v_cndmask_b32 v243,v243,v255,vcc\n"
"v_cmp_gt_i32 vcc,16,v250\n"
"s_nop 1\n"
"v_cndmask_b32 v244,v244,v255,vcc\n"
"v_cmp_gt_i32 vcc,17,v250\n"
"s_nop 1\n"
"v_cndmask_b32 v245,v245,v255,vcc\n"
"v_cmp_gt_i32 vcc,18,v250\n"
"s_nop 1\n"
"v_cndmask_b32 v246,v246,v255,vcc\n"
"v_cmp_gt_i32 vcc,19,v250\n"
"s_nop 1\n"
"v_cndmask_b32 v247,v247,v255,vcc\n"
"s_cmp_lg_u32 s90,0\n"
"s_cbranch_scc1 L1037_%=\n"
"v_max3_f32 v248,v248,v240,v241\n"
"v_max3_f32 v248,v248,v242,v243\n"
"v_max3_f32 v248,v248,v244,v245\n"
"v_max3_f32 v248,v248,v246,v247\n"
"s_branch L1038_%=\n"
"L1037_%=:\n"
"s_lshl_b32 s91,s88,13\n"
SA"s91,s91,s39\n"
SA"s91,s91,524288\n"
BL"a[148:151],v224,s[8:11],s91 offen offset:0 sc1\n"
BL"a[152:155],v224,s[8:11],s91 offen offset:1024 sc1\n"
BL"a[156:159],v224,s[8:11],s91 offen offset:2048 sc1\n"
BL"a[160:163],v224,s[8:11],s91 offen offset:3072 sc1\n"
SA"s91,s91,4096\n"
BL"a[164:167],v224,s[8:11],s91 offen offset:0 sc1\n"
BL"a[168:171],v224,s[8:11],s91 offen offset:1024 sc1\n"
BL"a[172:175],v224,s[8:11],s91 offen offset:2048 sc1\n"
BL"a[176:179],v224,s[8:11],s91 offen offset:3072 sc1\n"
WT"vmcnt(0)\n"
"v_sub_f32 v240,v240,v248\n"
"v_sub_f32 v241,v241,v248\n"
"v_sub_f32 v242,v242,v248\n"
"v_sub_f32 v243,v243,v248\n"
"v_sub_f32 v244,v244,v248\n"
"v_sub_f32 v245,v245,v248\n"
"v_sub_f32 v246,v246,v248\n"
"v_sub_f32 v247,v247,v248\n"
EX"v240,v240\n"
EX"v241,v241\n"
EX"v242,v242\n"
EX"v243,v243\n"
EX"v244,v244\n"
EX"v245,v245\n"
EX"v246,v246\n"
EX"v247,v247\n"
"s_nop 0\n"
PK"v240,v240,v241\n"
PK"v241,v242,v243\n"
PK"v242,v244,v245\n"
PK"v243,v246,v247\n"
"s_nop 1\n"
MF"v[112:115],a[148:151],v[240:243],v[112:115]\n"
MF"v[116:119],a[152:155],v[240:243],v[116:119]\n"
MF"v[120:123],a[156:159],v[240:243],v[120:123]\n"
MF"v[124:127],a[160:163],v[240:243],v[124:127]\n"
MF"v[128:131],a[164:167],v[240:243],v[128:131]\n"
MF"v[132:135],a[168:171],v[240:243],v[132:135]\n"
MF"v[136:139],a[172:175],v[240:243],v[136:139]\n"
MF"v[140:143],a[176:179],v[240:243],v[140:143]\n"
MF"a[136:139],a[128:131],v[240:243],a[136:139]\n"
"s_nop 7\n"
"L1038_%=:\n"
SA"s88,s88,1\n"
"s_cmp_le_u32 s88,s89\n"
"s_cbranch_scc1 L1036_%=\n"
"s_cmp_lg_u32 s90,0\n"
"s_cbranch_scc1 L1039_%=\n"
"v_mov_b32 v254,v248\n"
"s_nop 1\n"
"v_permlane16_swap_b32 v254,v248\n"
"s_nop 1\n"
"v_max_f32 v248,v248,v254\n"
"v_mov_b32 v254,v248\n"
"s_nop 1\n"
"v_permlane32_swap_b32 v254,v248\n"
"s_nop 1\n"
"v_max_f32 v248,v248,v254\n"
"s_mov_b32 s90,1\n"
"s_mov_b32 s88,0\n"
"s_branch L1036_%=\n"
"L1039_%=:\n"
"s_nop 7\n"
"v_accvgpr_read_b32 v252,a136\n"
"s_nop 1\n"
"s_branch L1034_%=\n"
"L1041_%=:\n"
"s_nop 15\n"
WT"vmcnt(0)\n"
"s_lshl_b32 s89,s34,3\n"
"s_sub_u32 s89,120,s89\n"
SA"s89,s89,s30\n"
SA"s89,s89,0\n"
"s_lshl_b32 s91,s89,4\n"
"s_lshr_b32 s89,s89,1\n"
"v_bfe_u32 v249,v224,4,4\n"
"v_lshrrev_b32 v254,8,v224\n"
"v_lshlrev_b32 v254,2,v254\n"
"v_sub_u32 v249,v249,v254\n"
"v_add_u32 v249,s91,v249\n"
"v_mov_b32 v255,0xff800000\n"
"v_mov_b32 v248,v255\n"
"s_mov_b32 s90,0\n"
"s_mov_b32 s88,0\n"
"v_mov_b32 v144,0\n"
"v_mov_b32 v145,0\n"
"v_mov_b32 v146,0\n"
"v_mov_b32 v147,0\n"
"v_mov_b32 v148,0\n"
"v_mov_b32 v149,0\n"
"v_mov_b32 v150,0\n"
"v_mov_b32 v151,0\n"
"v_mov_b32 v152,0\n"
"v_mov_b32 v153,0\n"
"v_mov_b32 v154,0\n"
"v_mov_b32 v155,0\n"
"v_mov_b32 v156,0\n"
"v_mov_b32 v157,0\n"
"v_mov_b32 v158,0\n"
"v_mov_b32 v159,0\n"
"v_mov_b32 v160,0\n"
"v_mov_b32 v161,0\n"
"v_mov_b32 v162,0\n"
"v_mov_b32 v163,0\n"
"v_mov_b32 v164,0\n"
"v_mov_b32 v165,0\n"
"v_mov_b32 v166,0\n"
"v_mov_b32 v167,0\n"
"v_mov_b32 v168,0\n"
"v_mov_b32 v169,0\n"
"v_mov_b32 v170,0\n"
"v_mov_b32 v171,0\n"
"v_mov_b32 v172,0\n"
"v_mov_b32 v173,0\n"
"v_mov_b32 v174,0\n"
"v_mov_b32 v175,0\n"
"v_accvgpr_write_b32 a140,0\n"
"v_accvgpr_write_b32 a141,0\n"
"v_accvgpr_write_b32 a142,0\n"
"v_accvgpr_write_b32 a143,0\n"
"L1042_%=:\n"
"s_lshl_b32 s91,s88,13\n"
SA"s91,s91,s39\n"
SA"s91,s91,0\n"
BL"a[148:151],v224,s[8:11],s91 offen offset:0 sc1\n"
BL"a[152:155],v224,s[8:11],s91 offen offset:1024 sc1\n"
BL"a[156:159],v224,s[8:11],s91 offen offset:2048 sc1\n"
BL"a[160:163],v224,s[8:11],s91 offen offset:3072 sc1\n"
SA"s91,s91,4096\n"
BL"a[164:167],v224,s[8:11],s91 offen offset:0 sc1\n"
BL"a[168:171],v224,s[8:11],s91 offen offset:1024 sc1\n"
BL"a[172:175],v224,s[8:11],s91 offen offset:2048 sc1\n"
BL"a[176:179],v224,s[8:11],s91 offen offset:3072 sc1\n"
WT"vmcnt(0)\n"
MF"v[240:243],a[148:151],a[32:35],0\n"
MF"v[240:243],a[152:155],a[36:39],v[240:243]\n"
MF"v[240:243],a[156:159],a[40:43],v[240:243]\n"
MF"v[240:243],a[160:163],a[44:47],v[240:243]\n"
MF"v[244:247],a[164:167],a[32:35],0\n"
MF"v[244:247],a[168:171],a[36:39],v[244:247]\n"
MF"v[244:247],a[172:175],a[40:43],v[244:247]\n"
MF"v[244:247],a[176:179],a[44:47],v[244:247]\n"
"s_lshl_b32 s91,s88,5\n"
"v_subrev_u32 v250,s91,v249\n"
"s_nop 7\n"
"v_cmp_gt_i32 vcc,0,v250\n"
"s_nop 1\n"
"v_cndmask_b32 v240,v240,v255,vcc\n"
"v_cmp_gt_i32 vcc,1,v250\n"
"s_nop 1\n"
"v_cndmask_b32 v241,v241,v255,vcc\n"
"v_cmp_gt_i32 vcc,2,v250\n"
"s_nop 1\n"
"v_cndmask_b32 v242,v242,v255,vcc\n"
"v_cmp_gt_i32 vcc,3,v250\n"
"s_nop 1\n"
"v_cndmask_b32 v243,v243,v255,vcc\n"
"v_cmp_gt_i32 vcc,16,v250\n"
"s_nop 1\n"
"v_cndmask_b32 v244,v244,v255,vcc\n"
"v_cmp_gt_i32 vcc,17,v250\n"
"s_nop 1\n"
"v_cndmask_b32 v245,v245,v255,vcc\n"
"v_cmp_gt_i32 vcc,18,v250\n"
"s_nop 1\n"
"v_cndmask_b32 v246,v246,v255,vcc\n"
"v_cmp_gt_i32 vcc,19,v250\n"
"s_nop 1\n"
"v_cndmask_b32 v247,v247,v255,vcc\n"
"s_cmp_lg_u32 s90,0\n"
"s_cbranch_scc1 L1043_%=\n"
"v_max3_f32 v248,v248,v240,v241\n"
"v_max3_f32 v248,v248,v242,v243\n"
"v_max3_f32 v248,v248,v244,v245\n"
"v_max3_f32 v248,v248,v246,v247\n"
"s_branch L1044_%=\n"
"L1043_%=:\n"
"s_lshl_b32 s91,s88,13\n"
SA"s91,s91,s39\n"
SA"s91,s91,524288\n"
BL"a[148:151],v224,s[8:11],s91 offen offset:0 sc1\n"
BL"a[152:155],v224,s[8:11],s91 offen offset:1024 sc1\n"
BL"a[156:159],v224,s[8:11],s91 offen offset:2048 sc1\n"
BL"a[160:163],v224,s[8:11],s91 offen offset:3072 sc1\n"
SA"s91,s91,4096\n"
BL"a[164:167],v224,s[8:11],s91 offen offset:0 sc1\n"
BL"a[168:171],v224,s[8:11],s91 offen offset:1024 sc1\n"
BL"a[172:175],v224,s[8:11],s91 offen offset:2048 sc1\n"
BL"a[176:179],v224,s[8:11],s91 offen offset:3072 sc1\n"
WT"vmcnt(0)\n"
"v_sub_f32 v240,v240,v248\n"
"v_sub_f32 v241,v241,v248\n"
"v_sub_f32 v242,v242,v248\n"
"v_sub_f32 v243,v243,v248\n"
"v_sub_f32 v244,v244,v248\n"
"v_sub_f32 v245,v245,v248\n"
"v_sub_f32 v246,v246,v248\n"
"v_sub_f32 v247,v247,v248\n"
EX"v240,v240\n"
EX"v241,v241\n"
EX"v242,v242\n"
EX"v243,v243\n"
EX"v244,v244\n"
EX"v245,v245\n"
EX"v246,v246\n"
EX"v247,v247\n"
"s_nop 0\n"
PK"v240,v240,v241\n"
PK"v241,v242,v243\n"
PK"v242,v244,v245\n"
PK"v243,v246,v247\n"
"s_nop 1\n"
MF"v[144:147],a[148:151],v[240:243],v[144:147]\n"
MF"v[148:151],a[152:155],v[240:243],v[148:151]\n"
MF"v[152:155],a[156:159],v[240:243],v[152:155]\n"
MF"v[156:159],a[160:163],v[240:243],v[156:159]\n"
MF"v[160:163],a[164:167],v[240:243],v[160:163]\n"
MF"v[164:167],a[168:171],v[240:243],v[164:167]\n"
MF"v[168:171],a[172:175],v[240:243],v[168:171]\n"
MF"v[172:175],a[176:179],v[240:243],v[172:175]\n"
MF"a[140:143],a[128:131],v[240:243],a[140:143]\n"
"s_nop 7\n"
"L1044_%=:\n"
SA"s88,s88,1\n"
"s_cmp_le_u32 s88,s89\n"
"s_cbranch_scc1 L1042_%=\n"
"s_cmp_lg_u32 s90,0\n"
"s_cbranch_scc1 L1045_%=\n"
"v_mov_b32 v254,v248\n"
"s_nop 1\n"
"v_permlane16_swap_b32 v254,v248\n"
"s_nop 1\n"
"v_max_f32 v248,v248,v254\n"
"v_mov_b32 v254,v248\n"
"s_nop 1\n"
"v_permlane32_swap_b32 v254,v248\n"
"s_nop 1\n"
"v_max_f32 v248,v248,v254\n"
"s_mov_b32 s90,1\n"
"s_mov_b32 s88,0\n"
"s_branch L1042_%=\n"
"L1045_%=:\n"
"s_nop 7\n"
"v_accvgpr_read_b32 v251,a140\n"
"s_nop 1\n"
"s_branch L1040_%=\n"
"L1047_%=:\n"
"s_nop 15\n"
WT"vmcnt(0)\n"
"s_lshl_b32 s89,s34,3\n"
"s_sub_u32 s89,120,s89\n"
SA"s89,s89,s30\n"
SA"s89,s89,4\n"
"s_lshl_b32 s91,s89,4\n"
"s_lshr_b32 s89,s89,1\n"
"v_bfe_u32 v249,v224,4,4\n"
"v_lshrrev_b32 v254,8,v224\n"
"v_lshlrev_b32 v254,2,v254\n"
"v_sub_u32 v249,v249,v254\n"
"v_add_u32 v249,s91,v249\n"
"v_mov_b32 v255,0xff800000\n"
"v_mov_b32 v248,v255\n"
"s_mov_b32 s90,0\n"
"s_mov_b32 s88,0\n"
"v_mov_b32 v176,0\n"
"v_mov_b32 v177,0\n"
"v_mov_b32 v178,0\n"
"v_mov_b32 v179,0\n"
"v_mov_b32 v180,0\n"
"v_mov_b32 v181,0\n"
"v_mov_b32 v182,0\n"
"v_mov_b32 v183,0\n"
"v_mov_b32 v184,0\n"
"v_mov_b32 v185,0\n"
"v_mov_b32 v186,0\n"
"v_mov_b32 v187,0\n"
"v_mov_b32 v188,0\n"
"v_mov_b32 v189,0\n"
"v_mov_b32 v190,0\n"
"v_mov_b32 v191,0\n"
"v_mov_b32 v192,0\n"
"v_mov_b32 v193,0\n"
"v_mov_b32 v194,0\n"
"v_mov_b32 v195,0\n"
"v_mov_b32 v196,0\n"
"v_mov_b32 v197,0\n"
"v_mov_b32 v198,0\n"
"v_mov_b32 v199,0\n"
"v_mov_b32 v200,0\n"
"v_mov_b32 v201,0\n"
"v_mov_b32 v202,0\n"
"v_mov_b32 v203,0\n"
"v_mov_b32 v204,0\n"
"v_mov_b32 v205,0\n"
"v_mov_b32 v206,0\n"
"v_mov_b32 v207,0\n"
"v_accvgpr_write_b32 a144,0\n"
"v_accvgpr_write_b32 a145,0\n"
"v_accvgpr_write_b32 a146,0\n"
"v_accvgpr_write_b32 a147,0\n"
"L1048_%=:\n"
"s_lshl_b32 s91,s88,13\n"
SA"s91,s91,s39\n"
SA"s91,s91,0\n"
BL"a[148:151],v224,s[8:11],s91 offen offset:0 sc1\n"
BL"a[152:155],v224,s[8:11],s91 offen offset:1024 sc1\n"
BL"a[156:159],v224,s[8:11],s91 offen offset:2048 sc1\n"
BL"a[160:163],v224,s[8:11],s91 offen offset:3072 sc1\n"
SA"s91,s91,4096\n"
BL"a[164:167],v224,s[8:11],s91 offen offset:0 sc1\n"
BL"a[168:171],v224,s[8:11],s91 offen offset:1024 sc1\n"
BL"a[172:175],v224,s[8:11],s91 offen offset:2048 sc1\n"
BL"a[176:179],v224,s[8:11],s91 offen offset:3072 sc1\n"
WT"vmcnt(0)\n"
MF"v[240:243],a[148:151],a[48:51],0\n"
MF"v[240:243],a[152:155],a[52:55],v[240:243]\n"
MF"v[240:243],a[156:159],a[56:59],v[240:243]\n"
MF"v[240:243],a[160:163],a[60:63],v[240:243]\n"
MF"v[244:247],a[164:167],a[48:51],0\n"
MF"v[244:247],a[168:171],a[52:55],v[244:247]\n"
MF"v[244:247],a[172:175],a[56:59],v[244:247]\n"
MF"v[244:247],a[176:179],a[60:63],v[244:247]\n"
"s_lshl_b32 s91,s88,5\n"
"v_subrev_u32 v250,s91,v249\n"
"s_nop 7\n"
"v_cmp_gt_i32 vcc,0,v250\n"
"s_nop 1\n"
"v_cndmask_b32 v240,v240,v255,vcc\n"
"v_cmp_gt_i32 vcc,1,v250\n"
"s_nop 1\n"
"v_cndmask_b32 v241,v241,v255,vcc\n"
"v_cmp_gt_i32 vcc,2,v250\n"
"s_nop 1\n"
"v_cndmask_b32 v242,v242,v255,vcc\n"
"v_cmp_gt_i32 vcc,3,v250\n"
"s_nop 1\n"
"v_cndmask_b32 v243,v243,v255,vcc\n"
"v_cmp_gt_i32 vcc,16,v250\n"
"s_nop 1\n"
"v_cndmask_b32 v244,v244,v255,vcc\n"
"v_cmp_gt_i32 vcc,17,v250\n"
"s_nop 1\n"
"v_cndmask_b32 v245,v245,v255,vcc\n"
"v_cmp_gt_i32 vcc,18,v250\n"
"s_nop 1\n"
"v_cndmask_b32 v246,v246,v255,vcc\n"
"v_cmp_gt_i32 vcc,19,v250\n"
"s_nop 1\n"
"v_cndmask_b32 v247,v247,v255,vcc\n"
"s_cmp_lg_u32 s90,0\n"
"s_cbranch_scc1 L1049_%=\n"
"v_max3_f32 v248,v248,v240,v241\n"
"v_max3_f32 v248,v248,v242,v243\n"
"v_max3_f32 v248,v248,v244,v245\n"
"v_max3_f32 v248,v248,v246,v247\n"
"s_branch L1050_%=\n"
"L1049_%=:\n"
"s_lshl_b32 s91,s88,13\n"
SA"s91,s91,s39\n"
SA"s91,s91,524288\n"
BL"a[148:151],v224,s[8:11],s91 offen offset:0 sc1\n"
BL"a[152:155],v224,s[8:11],s91 offen offset:1024 sc1\n"
BL"a[156:159],v224,s[8:11],s91 offen offset:2048 sc1\n"
BL"a[160:163],v224,s[8:11],s91 offen offset:3072 sc1\n"
SA"s91,s91,4096\n"
BL"a[164:167],v224,s[8:11],s91 offen offset:0 sc1\n"
BL"a[168:171],v224,s[8:11],s91 offen offset:1024 sc1\n"
BL"a[172:175],v224,s[8:11],s91 offen offset:2048 sc1\n"
BL"a[176:179],v224,s[8:11],s91 offen offset:3072 sc1\n"
WT"vmcnt(0)\n"
"v_sub_f32 v240,v240,v248\n"
"v_sub_f32 v241,v241,v248\n"
"v_sub_f32 v242,v242,v248\n"
"v_sub_f32 v243,v243,v248\n"
"v_sub_f32 v244,v244,v248\n"
"v_sub_f32 v245,v245,v248\n"
"v_sub_f32 v246,v246,v248\n"
"v_sub_f32 v247,v247,v248\n"
EX"v240,v240\n"
EX"v241,v241\n"
EX"v242,v242\n"
EX"v243,v243\n"
EX"v244,v244\n"
EX"v245,v245\n"
EX"v246,v246\n"
EX"v247,v247\n"
"s_nop 0\n"
PK"v240,v240,v241\n"
PK"v241,v242,v243\n"
PK"v242,v244,v245\n"
PK"v243,v246,v247\n"
"s_nop 1\n"
MF"v[176:179],a[148:151],v[240:243],v[176:179]\n"
MF"v[180:183],a[152:155],v[240:243],v[180:183]\n"
MF"v[184:187],a[156:159],v[240:243],v[184:187]\n"
MF"v[188:191],a[160:163],v[240:243],v[188:191]\n"
MF"v[192:195],a[164:167],v[240:243],v[192:195]\n"
MF"v[196:199],a[168:171],v[240:243],v[196:199]\n"
MF"v[200:203],a[172:175],v[240:243],v[200:203]\n"
MF"v[204:207],a[176:179],v[240:243],v[204:207]\n"
MF"a[144:147],a[128:131],v[240:243],a[144:147]\n"
"s_nop 7\n"
"L1050_%=:\n"
SA"s88,s88,1\n"
"s_cmp_le_u32 s88,s89\n"
"s_cbranch_scc1 L1048_%=\n"
"s_cmp_lg_u32 s90,0\n"
"s_cbranch_scc1 L1051_%=\n"
"v_mov_b32 v254,v248\n"
"s_nop 1\n"
"v_permlane16_swap_b32 v254,v248\n"
"s_nop 1\n"
"v_max_f32 v248,v248,v254\n"
"v_mov_b32 v254,v248\n"
"s_nop 1\n"
"v_permlane32_swap_b32 v254,v248\n"
"s_nop 1\n"
"v_max_f32 v248,v248,v254\n"
"s_mov_b32 s90,1\n"
"s_mov_b32 s88,0\n"
"s_branch L1048_%=\n"
"L1051_%=:\n"
"s_nop 7\n"
"v_accvgpr_read_b32 v252,a144\n"
"s_nop 1\n"
"s_branch L1046_%=\n"
"L1069_%=:\n"
"s_nop 15\n"
WT"vmcnt(0)\n"
"s_lshl_b32 s89,s34,3\n"
"s_sub_u32 s89,120,s89\n"
SA"s89,s89,s30\n"
SA"s89,s89,0\n"
"s_lshl_b32 s91,s89,4\n"
"s_lshr_b32 s89,s89,1\n"
"v_bfe_u32 v249,v224,4,4\n"
"v_lshrrev_b32 v254,8,v224\n"
"v_lshlrev_b32 v254,2,v254\n"
"v_sub_u32 v249,v249,v254\n"
"v_add_u32 v249,s91,v249\n"
"v_mov_b32 v255,0xff800000\n"
"v_mov_b32 v248,v255\n"
"s_mov_b32 s90,0\n"
"s_mov_b32 s88,0\n"
"v_mov_b32 v144,0\n"
"v_mov_b32 v145,0\n"
"v_mov_b32 v146,0\n"
"v_mov_b32 v147,0\n"
"v_mov_b32 v148,0\n"
"v_mov_b32 v149,0\n"
"v_mov_b32 v150,0\n"
"v_mov_b32 v151,0\n"
"v_mov_b32 v152,0\n"
"v_mov_b32 v153,0\n"
"v_mov_b32 v154,0\n"
"v_mov_b32 v155,0\n"
"v_mov_b32 v156,0\n"
"v_mov_b32 v157,0\n"
"v_mov_b32 v158,0\n"
"v_mov_b32 v159,0\n"
"v_mov_b32 v160,0\n"
"v_mov_b32 v161,0\n"
"v_mov_b32 v162,0\n"
"v_mov_b32 v163,0\n"
"v_mov_b32 v164,0\n"
"v_mov_b32 v165,0\n"
"v_mov_b32 v166,0\n"
"v_mov_b32 v167,0\n"
"v_mov_b32 v168,0\n"
"v_mov_b32 v169,0\n"
"v_mov_b32 v170,0\n"
"v_mov_b32 v171,0\n"
"v_mov_b32 v172,0\n"
"v_mov_b32 v173,0\n"
"v_mov_b32 v174,0\n"
"v_mov_b32 v175,0\n"
"v_accvgpr_write_b32 a140,0\n"
"v_accvgpr_write_b32 a141,0\n"
"v_accvgpr_write_b32 a142,0\n"
"v_accvgpr_write_b32 a143,0\n"
"L1070_%=:\n"
"s_lshl_b32 s91,s88,13\n"
SA"s91,s91,s39\n"
SA"s91,s91,0\n"
BL"a[148:151],v224,s[8:11],s91 offen offset:0 sc1\n"
BL"a[152:155],v224,s[8:11],s91 offen offset:1024 sc1\n"
BL"a[156:159],v224,s[8:11],s91 offen offset:2048 sc1\n"
BL"a[160:163],v224,s[8:11],s91 offen offset:3072 sc1\n"
SA"s91,s91,4096\n"
BL"a[164:167],v224,s[8:11],s91 offen offset:0 sc1\n"
BL"a[168:171],v224,s[8:11],s91 offen offset:1024 sc1\n"
BL"a[172:175],v224,s[8:11],s91 offen offset:2048 sc1\n"
BL"a[176:179],v224,s[8:11],s91 offen offset:3072 sc1\n"
WT"vmcnt(0)\n"
MF"v[240:243],a[148:151],a[32:35],0\n"
MF"v[240:243],a[152:155],a[36:39],v[240:243]\n"
MF"v[240:243],a[156:159],a[40:43],v[240:243]\n"
MF"v[240:243],a[160:163],a[44:47],v[240:243]\n"
MF"v[244:247],a[164:167],a[32:35],0\n"
MF"v[244:247],a[168:171],a[36:39],v[244:247]\n"
MF"v[244:247],a[172:175],a[40:43],v[244:247]\n"
MF"v[244:247],a[176:179],a[44:47],v[244:247]\n"
"s_lshl_b32 s91,s88,5\n"
"v_subrev_u32 v250,s91,v249\n"
"s_nop 7\n"
"v_cmp_gt_i32 vcc,0,v250\n"
"s_nop 1\n"
"v_cndmask_b32 v240,v240,v255,vcc\n"
"v_cmp_gt_i32 vcc,1,v250\n"
"s_nop 1\n"
"v_cndmask_b32 v241,v241,v255,vcc\n"
"v_cmp_gt_i32 vcc,2,v250\n"
"s_nop 1\n"
"v_cndmask_b32 v242,v242,v255,vcc\n"
"v_cmp_gt_i32 vcc,3,v250\n"
"s_nop 1\n"
"v_cndmask_b32 v243,v243,v255,vcc\n"
"v_cmp_gt_i32 vcc,16,v250\n"
"s_nop 1\n"
"v_cndmask_b32 v244,v244,v255,vcc\n"
"v_cmp_gt_i32 vcc,17,v250\n"
"s_nop 1\n"
"v_cndmask_b32 v245,v245,v255,vcc\n"
"v_cmp_gt_i32 vcc,18,v250\n"
"s_nop 1\n"
"v_cndmask_b32 v246,v246,v255,vcc\n"
"v_cmp_gt_i32 vcc,19,v250\n"
"s_nop 1\n"
"v_cndmask_b32 v247,v247,v255,vcc\n"
"s_cmp_lg_u32 s90,0\n"
"s_cbranch_scc1 L1071_%=\n"
"v_max3_f32 v248,v248,v240,v241\n"
"v_max3_f32 v248,v248,v242,v243\n"
"v_max3_f32 v248,v248,v244,v245\n"
"v_max3_f32 v248,v248,v246,v247\n"
"s_branch L1072_%=\n"
"L1071_%=:\n"
"s_lshl_b32 s91,s88,13\n"
SA"s91,s91,s39\n"
SA"s91,s91,524288\n"
BL"a[148:151],v224,s[8:11],s91 offen offset:0 sc1\n"
BL"a[152:155],v224,s[8:11],s91 offen offset:1024 sc1\n"
BL"a[156:159],v224,s[8:11],s91 offen offset:2048 sc1\n"
BL"a[160:163],v224,s[8:11],s91 offen offset:3072 sc1\n"
SA"s91,s91,4096\n"
BL"a[164:167],v224,s[8:11],s91 offen offset:0 sc1\n"
BL"a[168:171],v224,s[8:11],s91 offen offset:1024 sc1\n"
BL"a[172:175],v224,s[8:11],s91 offen offset:2048 sc1\n"
BL"a[176:179],v224,s[8:11],s91 offen offset:3072 sc1\n"
WT"vmcnt(0)\n"
"v_sub_f32 v240,v240,v248\n"
"v_sub_f32 v241,v241,v248\n"
"v_sub_f32 v242,v242,v248\n"
"v_sub_f32 v243,v243,v248\n"
"v_sub_f32 v244,v244,v248\n"
"v_sub_f32 v245,v245,v248\n"
"v_sub_f32 v246,v246,v248\n"
"v_sub_f32 v247,v247,v248\n"
EX"v240,v240\n"
EX"v241,v241\n"
EX"v242,v242\n"
EX"v243,v243\n"
EX"v244,v244\n"
EX"v245,v245\n"
EX"v246,v246\n"
EX"v247,v247\n"
"s_nop 0\n"
PK"v240,v240,v241\n"
PK"v241,v242,v243\n"
PK"v242,v244,v245\n"
PK"v243,v246,v247\n"
"s_nop 1\n"
MF"v[144:147],a[148:151],v[240:243],v[144:147]\n"
MF"v[148:151],a[152:155],v[240:243],v[148:151]\n"
MF"v[152:155],a[156:159],v[240:243],v[152:155]\n"
MF"v[156:159],a[160:163],v[240:243],v[156:159]\n"
MF"v[160:163],a[164:167],v[240:243],v[160:163]\n"
MF"v[164:167],a[168:171],v[240:243],v[164:167]\n"
MF"v[168:171],a[172:175],v[240:243],v[168:171]\n"
MF"v[172:175],a[176:179],v[240:243],v[172:175]\n"
MF"a[140:143],a[128:131],v[240:243],a[140:143]\n"
"s_nop 7\n"
"L1072_%=:\n"
SA"s88,s88,1\n"
"s_cmp_le_u32 s88,s89\n"
"s_cbranch_scc1 L1070_%=\n"
"s_cmp_lg_u32 s90,0\n"
"s_cbranch_scc1 L1073_%=\n"
"v_mov_b32 v254,v248\n"
"s_nop 1\n"
"v_permlane16_swap_b32 v254,v248\n"
"s_nop 1\n"
"v_max_f32 v248,v248,v254\n"
"v_mov_b32 v254,v248\n"
"s_nop 1\n"
"v_permlane32_swap_b32 v254,v248\n"
"s_nop 1\n"
"v_max_f32 v248,v248,v254\n"
"s_mov_b32 s90,1\n"
"s_mov_b32 s88,0\n"
"s_branch L1070_%=\n"
"L1073_%=:\n"
"s_nop 7\n"
"v_accvgpr_read_b32 v251,a140\n"
"s_nop 1\n"
"s_branch L1068_%=\n"
"L1075_%=:\n"
"s_nop 15\n"
WT"vmcnt(0)\n"
"s_lshl_b32 s89,s34,3\n"
"s_sub_u32 s89,120,s89\n"
SA"s89,s89,s30\n"
SA"s89,s89,4\n"
"s_lshl_b32 s91,s89,4\n"
"s_lshr_b32 s89,s89,1\n"
"v_bfe_u32 v249,v224,4,4\n"
"v_lshrrev_b32 v254,8,v224\n"
"v_lshlrev_b32 v254,2,v254\n"
"v_sub_u32 v249,v249,v254\n"
"v_add_u32 v249,s91,v249\n"
"v_mov_b32 v255,0xff800000\n"
"v_mov_b32 v248,v255\n"
"s_mov_b32 s90,0\n"
"s_mov_b32 s88,0\n"
"v_mov_b32 v176,0\n"
"v_mov_b32 v177,0\n"
"v_mov_b32 v178,0\n"
"v_mov_b32 v179,0\n"
"v_mov_b32 v180,0\n"
"v_mov_b32 v181,0\n"
"v_mov_b32 v182,0\n"
"v_mov_b32 v183,0\n"
"v_mov_b32 v184,0\n"
"v_mov_b32 v185,0\n"
"v_mov_b32 v186,0\n"
"v_mov_b32 v187,0\n"
"v_mov_b32 v188,0\n"
"v_mov_b32 v189,0\n"
"v_mov_b32 v190,0\n"
"v_mov_b32 v191,0\n"
"v_mov_b32 v192,0\n"
"v_mov_b32 v193,0\n"
"v_mov_b32 v194,0\n"
"v_mov_b32 v195,0\n"
"v_mov_b32 v196,0\n"
"v_mov_b32 v197,0\n"
"v_mov_b32 v198,0\n"
"v_mov_b32 v199,0\n"
"v_mov_b32 v200,0\n"
"v_mov_b32 v201,0\n"
"v_mov_b32 v202,0\n"
"v_mov_b32 v203,0\n"
"v_mov_b32 v204,0\n"
"v_mov_b32 v205,0\n"
"v_mov_b32 v206,0\n"
"v_mov_b32 v207,0\n"
"v_accvgpr_write_b32 a144,0\n"
"v_accvgpr_write_b32 a145,0\n"
"v_accvgpr_write_b32 a146,0\n"
"v_accvgpr_write_b32 a147,0\n"
"L1076_%=:\n"
"s_lshl_b32 s91,s88,13\n"
SA"s91,s91,s39\n"
SA"s91,s91,0\n"
BL"a[148:151],v224,s[8:11],s91 offen offset:0 sc1\n"
BL"a[152:155],v224,s[8:11],s91 offen offset:1024 sc1\n"
BL"a[156:159],v224,s[8:11],s91 offen offset:2048 sc1\n"
BL"a[160:163],v224,s[8:11],s91 offen offset:3072 sc1\n"
SA"s91,s91,4096\n"
BL"a[164:167],v224,s[8:11],s91 offen offset:0 sc1\n"
BL"a[168:171],v224,s[8:11],s91 offen offset:1024 sc1\n"
BL"a[172:175],v224,s[8:11],s91 offen offset:2048 sc1\n"
BL"a[176:179],v224,s[8:11],s91 offen offset:3072 sc1\n"
WT"vmcnt(0)\n"
MF"v[240:243],a[148:151],a[48:51],0\n"
MF"v[240:243],a[152:155],a[52:55],v[240:243]\n"
MF"v[240:243],a[156:159],a[56:59],v[240:243]\n"
MF"v[240:243],a[160:163],a[60:63],v[240:243]\n"
MF"v[244:247],a[164:167],a[48:51],0\n"
MF"v[244:247],a[168:171],a[52:55],v[244:247]\n"
MF"v[244:247],a[172:175],a[56:59],v[244:247]\n"
MF"v[244:247],a[176:179],a[60:63],v[244:247]\n"
"s_lshl_b32 s91,s88,5\n"
"v_subrev_u32 v250,s91,v249\n"
"s_nop 7\n"
"v_cmp_gt_i32 vcc,0,v250\n"
"s_nop 1\n"
"v_cndmask_b32 v240,v240,v255,vcc\n"
"v_cmp_gt_i32 vcc,1,v250\n"
"s_nop 1\n"
"v_cndmask_b32 v241,v241,v255,vcc\n"
"v_cmp_gt_i32 vcc,2,v250\n"
"s_nop 1\n"
"v_cndmask_b32 v242,v242,v255,vcc\n"
"v_cmp_gt_i32 vcc,3,v250\n"
"s_nop 1\n"
"v_cndmask_b32 v243,v243,v255,vcc\n"
"v_cmp_gt_i32 vcc,16,v250\n"
"s_nop 1\n"
"v_cndmask_b32 v244,v244,v255,vcc\n"
"v_cmp_gt_i32 vcc,17,v250\n"
"s_nop 1\n"
"v_cndmask_b32 v245,v245,v255,vcc\n"
"v_cmp_gt_i32 vcc,18,v250\n"
"s_nop 1\n"
"v_cndmask_b32 v246,v246,v255,vcc\n"
"v_cmp_gt_i32 vcc,19,v250\n"
"s_nop 1\n"
"v_cndmask_b32 v247,v247,v255,vcc\n"
"s_cmp_lg_u32 s90,0\n"
"s_cbranch_scc1 L1077_%=\n"
"v_max3_f32 v248,v248,v240,v241\n"
"v_max3_f32 v248,v248,v242,v243\n"
"v_max3_f32 v248,v248,v244,v245\n"
"v_max3_f32 v248,v248,v246,v247\n"
"s_branch L1078_%=\n"
"L1077_%=:\n"
"s_lshl_b32 s91,s88,13\n"
SA"s91,s91,s39\n"
SA"s91,s91,524288\n"
BL"a[148:151],v224,s[8:11],s91 offen offset:0 sc1\n"
BL"a[152:155],v224,s[8:11],s91 offen offset:1024 sc1\n"
BL"a[156:159],v224,s[8:11],s91 offen offset:2048 sc1\n"
BL"a[160:163],v224,s[8:11],s91 offen offset:3072 sc1\n"
SA"s91,s91,4096\n"
BL"a[164:167],v224,s[8:11],s91 offen offset:0 sc1\n"
BL"a[168:171],v224,s[8:11],s91 offen offset:1024 sc1\n"
BL"a[172:175],v224,s[8:11],s91 offen offset:2048 sc1\n"
BL"a[176:179],v224,s[8:11],s91 offen offset:3072 sc1\n"
WT"vmcnt(0)\n"
"v_sub_f32 v240,v240,v248\n"
"v_sub_f32 v241,v241,v248\n"
"v_sub_f32 v242,v242,v248\n"
"v_sub_f32 v243,v243,v248\n"
"v_sub_f32 v244,v244,v248\n"
"v_sub_f32 v245,v245,v248\n"
"v_sub_f32 v246,v246,v248\n"
"v_sub_f32 v247,v247,v248\n"
EX"v240,v240\n"
EX"v241,v241\n"
EX"v242,v242\n"
EX"v243,v243\n"
EX"v244,v244\n"
EX"v245,v245\n"
EX"v246,v246\n"
EX"v247,v247\n"
"s_nop 0\n"
PK"v240,v240,v241\n"
PK"v241,v242,v243\n"
PK"v242,v244,v245\n"
PK"v243,v246,v247\n"
"s_nop 1\n"
MF"v[176:179],a[148:151],v[240:243],v[176:179]\n"
MF"v[180:183],a[152:155],v[240:243],v[180:183]\n"
MF"v[184:187],a[156:159],v[240:243],v[184:187]\n"
MF"v[188:191],a[160:163],v[240:243],v[188:191]\n"
MF"v[192:195],a[164:167],v[240:243],v[192:195]\n"
MF"v[196:199],a[168:171],v[240:243],v[196:199]\n"
MF"v[200:203],a[172:175],v[240:243],v[200:203]\n"
MF"v[204:207],a[176:179],v[240:243],v[204:207]\n"
MF"a[144:147],a[128:131],v[240:243],a[144:147]\n"
"s_nop 7\n"
"L1078_%=:\n"
SA"s88,s88,1\n"
"s_cmp_le_u32 s88,s89\n"
"s_cbranch_scc1 L1076_%=\n"
"s_cmp_lg_u32 s90,0\n"
"s_cbranch_scc1 L1079_%=\n"
"v_mov_b32 v254,v248\n"
"s_nop 1\n"
"v_permlane16_swap_b32 v254,v248\n"
"s_nop 1\n"
"v_max_f32 v248,v248,v254\n"
"v_mov_b32 v254,v248\n"
"s_nop 1\n"
"v_permlane32_swap_b32 v254,v248\n"
"s_nop 1\n"
"v_max_f32 v248,v248,v254\n"
"s_mov_b32 s90,1\n"
"s_mov_b32 s88,0\n"
"s_branch L1076_%=\n"
"L1079_%=:\n"
"s_nop 7\n"
"v_accvgpr_read_b32 v252,a144\n"
"s_nop 1\n"
"s_branch L1074_%=\n"
"L1080_%=:\n"
"s_mov_b32 s54,0\n"
"L1082_%=:\n"
"s_sleep 2\n"
"buffer_load_dwordx2 v[240:241],v238,s[8:11],s63 offen sc1\n"
WT"vmcnt(0)\n"
SA"s54,s54,1\n"
"v_cmp_eq_u64 vcc,s[28:29],v[240:241]\n"
"s_cmp_eq_u64 vcc,exec\n"
"s_cbranch_scc1 L1081_%=\n"
"s_cmp_lt_u32 s54,1048576\n"
"s_cbranch_scc1 L1082_%=\n"
"s_branch L1081_%=\n"
"L1083_%=:\n"
"s_mov_b32 s54,0\n"
"L1085_%=:\n"
"s_sleep 2\n"
"buffer_load_dwordx2 v[240:241],v238,s[8:11],s63 offen sc1\n"
WT"vmcnt(0)\n"
SA"s54,s54,1\n"
"v_cmp_eq_u64 vcc,s[28:29],v[240:241]\n"
"s_cmp_eq_u64 vcc,exec\n"
"s_cbranch_scc1 L1084_%=\n"
"s_cmp_lt_u32 s54,1048576\n"
"s_cbranch_scc1 L1085_%=\n"
"s_branch L1084_%=\n"
"L1086_%=:\n"
"s_mov_b32 s54,0\n"
"L1088_%=:\n"
"s_sleep 2\n"
"buffer_load_dwordx2 v[240:241],v244,s[8:11],s92 offen sc1\n"
WT"vmcnt(0)\n"
SA"s54,s54,1\n"
"v_cmp_eq_u64 vcc,s[28:29],v[240:241]\n"
"s_cmp_eq_u64 vcc,exec\n"
"s_cbranch_scc1 L1087_%=\n"
"s_cmp_lt_u32 s54,1048576\n"
"s_cbranch_scc1 L1088_%=\n"
"s_branch L1087_%=\n"
"L1099_%=:\n"
"s_mov_b32 s54,0\n"
"L1101_%=:\n"
"s_sleep 2\n"
BL"v[240:243],v238,s[8:11],0 offen sc1\n"
WT"vmcnt(0)\n"
SA"s54,s54,1\n"
"v_cmp_eq_u64 vcc,s[28:29],v[240:241]\n"
"v_cmp_eq_u64_e64 s[50:51],s[28:29],v[242:243]\n"
"s_and_b64 vcc,vcc,s[50:51]\n"
"s_cmp_eq_u64 vcc,exec\n"
"s_cbranch_scc1 L1100_%=\n"
"s_cmp_lt_u32 s54,1048576\n"
"s_cbranch_scc1 L1101_%=\n"
"s_branch L1100_%=\n"
"L1102_%=:\n"
"s_mov_b32 s54,0\n"
"L1104_%=:\n"
"s_sleep 2\n"
"buffer_load_dwordx2 v[240:241],v238,s[8:11],s50 offen sc1\n"
WT"vmcnt(0)\n"
SA"s54,s54,1\n"
"v_cmp_eq_u64 vcc,s[28:29],v[240:241]\n"
"s_cmp_eq_u64 vcc,exec\n"
"s_cbranch_scc1 L1103_%=\n"
"s_cmp_lt_u32 s54,1048576\n"
"s_cbranch_scc1 L1104_%=\n"
"s_branch L1103_%=\n"
"L1105_%=:\n"
"s_mov_b32 s54,0\n"
"L1107_%=:\n"
"s_sleep 2\n"
"buffer_load_dwordx2 v[244:245],v242,s[8:11],s23 offen sc1\n"
WT"vmcnt(0)\n"
SA"s54,s54,1\n"
"v_cmp_eq_u64 vcc,s[28:29],v[244:245]\n"
"s_cmp_eq_u64 vcc,exec\n"
"s_cbranch_scc1 L1106_%=\n"
"s_cmp_lt_u32 s54,1048576\n"
"s_cbranch_scc1 L1107_%=\n"
"s_branch L1106_%=\n"
".p2align 6\n"
"LFA_%=:\n"
WT"lgkmcnt(6)\n"
MF"v[0:3],a[64:67],a[0:3],v[208:211]\n"
WT"lgkmcnt(3)\n"
PK"v192,v192,v193\n"
PK"v193,v194,v195\n"
MF"v[0:3],a[68:71],a[4:7],v[0:3]\n"
"s_mov_b32 m0,s40\n"
"v_readlane_b32 s43,v228,s42\n"
PK"v194,v196,v197\n"
MF"v[8:11],a[68:71],a[20:23],0\n"
PK"v195,v198,v199\n"
"v_readlane_b32 s44,v229,s42\n"
MF"v[8:11],a[64:67],a[16:19],v[8:11]\n"
SA"s42,s42,1\n"
BL"v225,s[8:11],s43 offen offset:0 sc1 lds\n"
MF"v[16:19],a[64:67],a[32:35],0\n"
"v_accvgpr_write_b32 a44,v192\n"
"v_accvgpr_write_b32 a45,v193\n"
MF"v[16:19],a[68:71],a[36:39],v[16:19]\n"
RD"a[92:95],v226 offset:7168\n"
"v_accvgpr_write_b32 a46,v194\n"
MF"v[24:27],a[68:71],a[52:55],0\n"
"v_accvgpr_write_b32 a47,v195\n"
PK"v200,v200,v201\n"
MF"v[24:27],a[64:67],a[48:51],v[24:27]\n"
PK"v201,v202,v203\n"
PK"v202,v204,v205\n"
MF"v[0:3],a[72:75],a[8:11],v[0:3]\n"
BL"v225,s[8:11],s43 offen offset:1024 sc1 lds\n"
PK"v203,v206,v207\n"
MF"v[0:3],a[76:79],a[12:15],v[0:3]\n"
"v_accvgpr_write_b32 a60,v200\n"
"v_accvgpr_write_b32 a61,v201\n"
MF"v[8:11],a[76:79],a[28:31],v[8:11]\n"
"s_mul_i32 s48,s32,655360\n"
"v_accvgpr_write_b32 a62,v202\n"
"v_accvgpr_write_b32 a63,v203\n"
MF"v[8:11],a[72:75],a[24:27],v[8:11]\n"
SA"s12,s74,s48\n"
"s_addc_u32 s13,s75,0\n"
MF"v[16:19],a[72:75],a[40:43],v[16:19]\n"
"s_and_b32 s13,s13,0xffff\n"
MF"v[16:19],a[76:79],a[44:47],v[16:19]\n"
"s_mov_b32 s14,655360\n"
RD"a[96:99],v226 offset:8192\n"
MF"v[24:27],a[76:79],a[60:63],v[24:27]\n"
"s_lshl_b32 s48,s33,3\n"
BL"v225,s[8:11],s43 offen offset:2048 sc1 lds\n"
MF"v[24:27],a[72:75],a[56:59],v[24:27]\n"
WT"lgkmcnt(1)\n"
EX"v0,v0\n"
MF"v[4:7],a[80:83],a[0:3],v[212:215]\n"
"s_and_b32 s49,s34,2\n"
EX"v1,v1\n"
MF"v[4:7],a[84:87],a[4:7],v[4:7]\n"
"s_lshl_b32 s49,s49,1\n"
EX"v2,v2\n"
MF"v[12:15],a[84:87],a[20:23],0\n"
SA"s48,s48,s49\n"
RD"a[100:103],v226 offset:9216\n"
PK"v0,v0,v1\n"
MF"v[12:15],a[80:83],a[16:19],v[12:15]\n"
SA"s48,s48,s30\n"
EX"v3,v3\n"
MF"v[20:23],a[80:83],a[32:35],0\n"
"s_mul_i32 s48,s48,5120\n"
EX"v8,v8\n"
MF"v[20:23],a[84:87],a[36:39],v[20:23]\n"
"s_or_b32 s49,s34,2\n"
BL"v225,s[8:11],s43 offen offset:3072 sc1 lds\n"
MF"v[28:31],a[84:87],a[52:55],0\n"
"s_cmp_eq_u32 s49,7\n"
EX"v9,v9\n"
PK"v1,v2,v3\n"
MF"v[28:31],a[80:83],a[48:51],v[28:31]\n"
"s_cselect_b32 s55,s48,2130706432\n"
RD"a[104:107],v226 offset:10240\n"
PK"v8,v8,v9\n"
MF"v[4:7],a[88:91],a[8:11],v[4:7]\n"
SA"m0,s40,16384\n"
EX"v10,v10\n"
MF"v[4:7],a[92:95],a[12:15],v[4:7]\n"
EX"v11,v11\n"
MF"v[12:15],a[92:95],a[28:31],v[12:15]\n"
EX"v16,v16\n"
MF"v[12:15],a[88:91],a[24:27],v[12:15]\n"
SA"s40,s40,32768\n"
RD"a[108:111],v226 offset:11264\n"
PK"v9,v10,v11\n"
MF"v[20:23],a[88:91],a[40:43],v[20:23]\n"
"s_cmp_eq_u32 s40,s46\n"
BL"v225,s[8:11],s44 offen offset:0 sc1 lds\n"
EX"v17,v17\n"
MF"v[20:23],a[92:95],a[44:47],v[20:23]\n"
"s_cselect_b32 s40,s47,s40\n"
EX"v18,v18\n"
MF"v[28:31],a[92:95],a[60:63],v[28:31]\n"
EX"v19,v19\n"
PK"v16,v16,v17\n"
MF"v[28:31],a[88:91],a[56:59],v[28:31]\n"
WT"lgkmcnt(1)\n"
RD"a[112:115],v226 offset:12288\n"
PK"v17,v18,v19\n"
MF"v[32:35],a[96:99],a[0:3],v[216:219]\n"
EX"v24,v24\n"
MF"v[32:35],a[100:103],a[4:7],v[32:35]\n"
EX"v25,v25\n"
"v_mov_b32 v227,v226\n"
MF"v[40:43],a[100:103],a[20:23],0\n"
BL"v225,s[8:11],s44 offen offset:1024 sc1 lds\n"
EX"v26,v26\n"
MF"v[40:43],a[96:99],a[16:19],v[40:43]\n"
RD"a[116:119],v226 offset:13312\n"
PK"v24,v24,v25\n"
MF"v[48:51],a[96:99],a[32:35],0\n"
EX"v27,v27\n"
MF"v[48:51],a[100:103],a[36:39],v[48:51]\n"
EX"v4,v4\n"
MF"v[56:59],a[100:103],a[52:55],0\n"
EX"v5,v5\n"
PK"v25,v26,v27\n"
MF"v[56:59],a[96:99],a[48:51],v[56:59]\n"
RD"a[120:123],v226 offset:14336\n"
PK"v2,v4,v5\n"
MF"v[32:35],a[104:107],a[8:11],v[32:35]\n"
WT"lgkmcnt(2)\n"
BL"v225,s[8:11],s44 offen offset:2048 sc1 lds\n"
EX"v6,v6\n"
MF"v[32:35],a[108:111],a[12:15],v[32:35]\n"
EX"v7,v7\n"
MF"v[40:43],a[108:111],a[28:31],v[40:43]\n"
EX"v12,v12\n"
MF"v[40:43],a[104:107],a[24:27],v[40:43]\n"
RD"a[124:127],v226 offset:15360\n"
"v_readlane_b32 s43,v228,s42\n"
PK"v3,v6,v7\n"
MF"v[48:51],a[104:107],a[40:43],v[48:51]\n"
EX"v13,v13\n"
MF"v[48:51],a[108:111],a[44:47],v[48:51]\n"
EX"v14,v14\n"
MF"v[56:59],a[108:111],a[60:63],v[56:59]\n"
BL"v225,s[8:11],s44 offen offset:3072 sc1 lds\n"
EX"v15,v15\n"
MF"v[56:59],a[104:107],a[56:59],v[56:59]\n"
"s_mov_b32 m0,s40\n"
RD"a[96:99],v227 offset:24576\n"
PK"v10,v12,v13\n"
MF"v[36:39],a[112:115],a[0:3],v[220:223]\n"
WT"lgkmcnt(2)\n"
EX"v20,v20\n"
PK"v11,v14,v15\n"
MF"v[36:39],a[116:119],a[4:7],v[36:39]\n"
EX"v21,v21\n"
MF"v[44:47],a[116:119],a[20:23],0\n"
EX"v22,v22\n"
MF"v[44:47],a[112:115],a[16:19],v[44:47]\n"
RD"a[100:103],v227 offset:25600\n"
"v_readlane_b32 s44,v229,s42\n"
PK"v18,v20,v21\n"
MF"v[52:55],a[112:115],a[32:35],0\n"
SA"s42,s42,1\n"
BL"v225,s[8:11],s43 offen offset:0 sc1 lds\n"
MF"v[52:55],a[116:119],a[36:39],v[52:55]\n"
EX"v23,v23\n"
MF"v[60:63],a[116:119],a[52:55],0\n"
EX"v28,v28\n"
MF"v[60:63],a[112:115],a[48:51],v[60:63]\n"
RD"a[104:107],v227 offset:26624\n"
PK"v19,v22,v23\n"
MF"v[36:39],a[120:123],a[8:11],v[36:39]\n"
WT"lgkmcnt(2)\n"
EX"v29,v29\n"
MF"v[36:39],a[124:127],a[12:15],v[36:39]\n"
BL"v225,s[8:11],s43 offen offset:1024 sc1 lds\n"
MF"v[44:47],a[124:127],a[28:31],v[44:47]\n"
EX"v30,v30\n"
PK"v26,v28,v29\n"
MF"v[44:47],a[120:123],a[24:27],v[44:47]\n"
RD"a[108:111],v227 offset:27648\n"
MF"v[52:55],a[120:123],a[40:43],v[52:55]\n"
EX"v31,v31\n"
MF"v[52:55],a[124:127],a[44:47],v[52:55]\n"
EX"v32,v32\n"
MF"v[60:63],a[124:127],a[60:63],v[60:63]\n"
EX"v33,v33\n"
PK"v27,v30,v31\n"
MF"v[60:63],a[120:123],a[56:59],v[60:63]\n"
SA"s41,s41,32768\n"
BL"v225,s[8:11],s43 offen offset:2048 sc1 lds\n"
RD"a[112:115],v227 offset:28672\n"
MF"v[80:83],a[96:99],v[0:3],0\n"
"s_cmp_eq_u32 s41,163840\n"
EX"v34,v34\n"
MF"v[112:115],a[96:99],v[8:11],0\n"
"s_cselect_b32 s41,0,s41\n"
EX"v35,v35\n"
MF"v[144:147],a[96:99],v[16:19],0\n"
EX"v40,v40\n"
"v_add_u32 v226,s41,v224\n"
MF"v[176:179],a[96:99],v[24:27],0\n"
WT"lgkmcnt(1)\n"
RD"a[116:119],v227 offset:29696\n"
MF"v[84:87],a[100:103],v[0:3],0\n"
EX"v41,v41\n"
MF"v[116:119],a[100:103],v[8:11],0\n"
BL"v225,s[8:11],s43 offen offset:3072 sc1 lds\n"
MF"v[148:151],a[100:103],v[16:19],0\n"
SA"m0,s40,16384\n"
EX"v42,v42\n"
MF"v[180:183],a[100:103],v[24:27],0\n"
RD"a[120:123],v227 offset:30720\n"
MF"v[88:91],a[104:107],v[0:3],0\n"
EX"v43,v43\n"
MF"v[120:123],a[104:107],v[8:11],0\n"
EX"v48,v48\n"
MF"v[152:155],a[104:107],v[16:19],0\n"
SA"s40,s40,32768\n"
EX"v49,v49\n"
MF"v[184:187],a[104:107],v[24:27],0\n"
"s_cmp_eq_u32 s40,s46\n"
BL"v225,s[8:11],s44 offen offset:0 sc1 lds\n"
RD"a[124:127],v227 offset:31744\n"
MF"v[92:95],a[108:111],v[0:3],0\n"
"s_cselect_b32 s40,s47,s40\n"
EX"v50,v50\n"
MF"v[124:127],a[108:111],v[8:11],0\n"
EX"v51,v51\n"
MF"v[156:159],a[108:111],v[16:19],0\n"
EX"v56,v56\n"
MF"v[188:191],a[108:111],v[24:27],0\n"
WT"lgkmcnt(1)\n"
EX"v57,v57\n"
MF"v[96:99],a[112:115],v[0:3],0\n"
EX"v58,v58\n"
MF"v[128:131],a[112:115],v[8:11],0\n"
WT"vmcnt(15) lgkmcnt(0)\n"
BL"v225,s[8:11],s44 offen offset:1024 sc1 lds\n"
MF"v[160:163],a[112:115],v[16:19],0\n"
EX"v59,v59\n"
MF"v[192:195],a[112:115],v[24:27],0\n"
EX"v36,v36\n"
MF"v[100:103],a[116:119],v[0:3],0\n"
"s_barrier\n"
MF"v[132:135],a[116:119],v[8:11],0\n"
RD"a[64:67],v226 offset:0\n"
"buffer_store_dwordx2 v[232:233],v230,s[8:11],s98 offen sc1\n"
MF"v[164:167],a[116:119],v[16:19],0\n"
EX"v37,v37\n"
MF"v[196:199],a[116:119],v[24:27],0\n"
BL"v225,s[8:11],s44 offen offset:2048 sc1 lds\n"
RD"a[68:71],v226 offset:1024\n"
MF"v[104:107],a[120:123],v[0:3],0\n"
EX"v38,v38\n"
MF"v[136:139],a[120:123],v[8:11],0\n"
EX"v39,v39\n"
MF"v[168:171],a[120:123],v[16:19],0\n"
EX"v44,v44\n"
MF"v[200:203],a[120:123],v[24:27],0\n"
RD"a[72:75],v226 offset:2048\n"
RD"a[76:79],v226 offset:3072\n"
MF"v[108:111],a[124:127],v[0:3],0\n"
EX"v45,v45\n"
MF"v[140:143],a[124:127],v[8:11],0\n"
BL"v225,s[8:11],s44 offen offset:3072 sc1 lds\n"
MF"v[172:175],a[124:127],v[16:19],0\n"
EX"v46,v46\n"
MF"v[204:207],a[124:127],v[24:27],0\n"
RD"a[80:83],v226 offset:4096\n"
MF"a[132:135],a[128:131],v[0:3],0\n"
EX"v47,v47\n"
MF"a[136:139],a[128:131],v[8:11],0\n"
"buffer_load_dword v255,v253,s[12:15],s55 offen\n"
EX"v52,v52\n"
MF"a[140:143],a[128:131],v[16:19],0\n"
WT"lgkmcnt(3)\n"
EX"v53,v53\n"
MF"a[144:147],a[128:131],v[24:27],0\n"
SA"s55,s55,163840\n"
RD"a[84:87],v226 offset:5120\n"
RD"a[88:91],v226 offset:6144\n"
EX"v54,v54\n"
EX"v55,v55\n"
EX"v60,v60\n"
EX"v61,v61\n"
EX"v62,v62\n"
EX"v63,v63\n"
"s_branch LDB_%=\n"
".p2align 6\n"
"LEV_%=:\n"
"s_mov_b32 s14,0\n"
MF"v[0:3],a[64:67],a[0:3],0\n"
SA"s95,s40,32768\n"
PK"v64,v32,v33\n"
PK"v65,v34,v35\n"
MF"v[0:3],a[68:71],a[4:7],v[0:3]\n"
"s_cmp_eq_u32 s95,s46\n"
PK"v66,v36,v37\n"
PK"v67,v38,v39\n"
MF"v[8:11],a[68:71],a[20:23],0\n"
"s_cselect_b32 s95,s47,s95\n"
PK"v68,v40,v41\n"
PK"v69,v42,v43\n"
MF"v[8:11],a[64:67],a[16:19],v[8:11]\n"
PK"v70,v44,v45\n"
PK"v71,v46,v47\n"
MF"v[16:19],a[64:67],a[32:35],0\n"
SA"m0,s95,0\n"
PK"v72,v48,v49\n"
PK"v73,v50,v51\n"
MF"v[16:19],a[68:71],a[36:39],v[16:19]\n"
RD"a[92:95],v226 offset:7168\n"
PK"v74,v52,v53\n"
MF"v[24:27],a[68:71],a[52:55],0\n"
PK"v75,v54,v55\n"
PK"v76,v56,v57\n"
MF"v[24:27],a[64:67],a[48:51],v[24:27]\n"
WT"lgkmcnt(1)\n"
BL"v235,s[24:27],s52 offen lds\n"
MF"v[0:3],a[72:75],a[8:11],v[0:3]\n"
SA"s52,s52,s62\n"
PK"v77,v58,v59\n"
PK"v78,v60,v61\n"
MF"v[0:3],a[76:79],a[12:15],v[0:3]\n"
PK"v79,v62,v63\n"
MF"v[8:11],a[76:79],a[28:31],v[8:11]\n"
MF"v[8:11],a[72:75],a[24:27],v[8:11]\n"
MF"v[16:19],a[72:75],a[40:43],v[16:19]\n"
SA"m0,s95,1024\n"
MF"v[16:19],a[76:79],a[44:47],v[16:19]\n"
RD"a[96:99],v226 offset:8192\n"
MF"v[24:27],a[76:79],a[60:63],v[24:27]\n"
EX"v0,v0\n"
MF"v[24:27],a[72:75],a[56:59],v[24:27]\n"
BL"v235,s[24:27],s52 offen lds\n"
MF"v[4:7],a[80:83],a[0:3],0\n"
SA"s52,s52,s62\n"
EX"v1,v1\n"
MF"v[4:7],a[84:87],a[4:7],v[4:7]\n"
RD"a[100:103],v226 offset:9216\n"
PK"v0,v0,v1\n"
MF"v[12:15],a[84:87],a[20:23],0\n"
EX"v2,v2\n"
MF"v[12:15],a[80:83],a[16:19],v[12:15]\n"
EX"v3,v3\n"
MF"v[20:23],a[80:83],a[32:35],0\n"
SA"m0,s95,2048\n"
EX"v8,v8\n"
MF"v[20:23],a[84:87],a[36:39],v[20:23]\n"
RD"a[104:107],v226 offset:10240\n"
PK"v1,v2,v3\n"
MF"v[28:31],a[84:87],a[52:55],0\n"
EX"v9,v9\n"
MF"v[28:31],a[80:83],a[48:51],v[28:31]\n"
BL"v235,s[24:27],s52 offen lds\n"
MF"v[4:7],a[88:91],a[8:11],v[4:7]\n"
WT"lgkmcnt(1)\n"
EX"v10,v10\n"
MF"v[4:7],a[92:95],a[12:15],v[4:7]\n"
SA"s52,s52,s62\n"
RD"a[108:111],v226 offset:11264\n"
PK"v8,v8,v9\n"
MF"v[12:15],a[92:95],a[28:31],v[12:15]\n"
EX"v11,v11\n"
MF"v[12:15],a[88:91],a[24:27],v[12:15]\n"
EX"v16,v16\n"
MF"v[20:23],a[88:91],a[40:43],v[20:23]\n"
SA"m0,s95,3072\n"
EX"v17,v17\n"
MF"v[20:23],a[92:95],a[44:47],v[20:23]\n"
RD"a[112:115],v226 offset:12288\n"
PK"v9,v10,v11\n"
MF"v[28:31],a[92:95],a[60:63],v[28:31]\n"
EX"v18,v18\n"
MF"v[28:31],a[88:91],a[56:59],v[28:31]\n"
BL"v235,s[24:27],s52 offen lds\n"
MF"v[32:35],a[96:99],a[0:3],0\n"
SA"s52,s52,s62\n"
EX"v19,v19\n"
MF"v[32:35],a[100:103],a[4:7],v[32:35]\n"
RD"a[116:119],v226 offset:13312\n"
PK"v16,v16,v17\n"
MF"v[40:43],a[100:103],a[20:23],0\n"
EX"v24,v24\n"
MF"v[40:43],a[96:99],a[16:19],v[40:43]\n"
EX"v25,v25\n"
MF"v[48:51],a[96:99],a[32:35],0\n"
SA"m0,s95,16384\n"
EX"v26,v26\n"
MF"v[48:51],a[100:103],a[36:39],v[48:51]\n"
RD"a[120:123],v226 offset:14336\n"
"v_mov_b32 v227,v226\n"
MF"v[56:59],a[100:103],a[52:55],0\n"
EX"v27,v27\n"
MF"v[56:59],a[96:99],a[48:51],v[56:59]\n"
WT"lgkmcnt(2)\n"
BL"v235,s[24:27],s52 offen lds\n"
MF"v[32:35],a[104:107],a[8:11],v[32:35]\n"
SA"s52,s52,s62\n"
EX"v4,v4\n"
MF"v[32:35],a[108:111],a[12:15],v[32:35]\n"
RD"a[124:127],v226 offset:15360\n"
PK"v17,v18,v19\n"
MF"v[40:43],a[108:111],a[28:31],v[40:43]\n"
EX"v5,v5\n"
MF"v[40:43],a[104:107],a[24:27],v[40:43]\n"
EX"v6,v6\n"
MF"v[48:51],a[104:107],a[40:43],v[48:51]\n"
SA"m0,s95,17408\n"
EX"v7,v7\n"
PK"v24,v24,v25\n"
MF"v[48:51],a[108:111],a[44:47],v[48:51]\n"
PK"v25,v26,v27\n"
PK"v2,v4,v5\n"
MF"v[56:59],a[108:111],a[60:63],v[56:59]\n"
EX"v12,v12\n"
MF"v[56:59],a[104:107],a[56:59],v[56:59]\n"
BL"v235,s[24:27],s52 offen lds\n"
MF"v[36:39],a[112:115],a[0:3],0\n"
WT"lgkmcnt(0)\n"
EX"v13,v13\n"
MF"v[36:39],a[116:119],a[4:7],v[36:39]\n"
SA"s52,s52,s62\n"
RD"a[64:67],v227 offset:16384\n"
PK"v3,v6,v7\n"
MF"v[44:47],a[116:119],a[20:23],0\n"
EX"v14,v14\n"
MF"v[44:47],a[112:115],a[16:19],v[44:47]\n"
PK"v10,v12,v13\n"
MF"v[52:55],a[112:115],a[32:35],0\n"
SA"m0,s95,18432\n"
EX"v15,v15\n"
MF"v[52:55],a[116:119],a[36:39],v[52:55]\n"
RD"a[68:71],v227 offset:17408\n"
PK"v11,v14,v15\n"
MF"v[60:63],a[116:119],a[52:55],0\n"
EX"v20,v20\n"
MF"v[60:63],a[112:115],a[48:51],v[60:63]\n"
BL"v235,s[24:27],s52 offen lds\n"
MF"v[36:39],a[120:123],a[8:11],v[36:39]\n"
SA"s52,s52,s62\n"
EX"v21,v21\n"
MF"v[36:39],a[124:127],a[12:15],v[36:39]\n"
RD"a[72:75],v227 offset:18432\n"
"v_readlane_b32 s43,v228,s42\n"
MF"v[44:47],a[124:127],a[28:31],v[44:47]\n"
EX"v22,v22\n"
MF"v[44:47],a[120:123],a[24:27],v[44:47]\n"
"s_mov_b32 m0,s40\n"
PK"v18,v20,v21\n"
MF"v[52:55],a[120:123],a[40:43],v[52:55]\n"
EX"v23,v23\n"
MF"v[52:55],a[124:127],a[44:47],v[52:55]\n"
RD"a[76:79],v227 offset:19456\n"
"v_readlane_b32 s44,v229,s42\n"
MF"v[60:63],a[124:127],a[60:63],v[60:63]\n"
EX"v28,v28\n"
MF"v[60:63],a[120:123],a[56:59],v[60:63]\n"
WT"lgkmcnt(2)\n"
BL"v225,s[8:11],s43 offen offset:0 sc1 lds\n"
MF"v[80:83],a[64:67],v[64:67],v[80:83]\n"
SA"s42,s42,1\n"
EX"v29,v29\n"
MF"v[112:115],a[64:67],v[68:71],v[112:115]\n"
SA"s41,s41,32768\n"
RD"a[80:83],v227 offset:20480\n"
PK"v19,v22,v23\n"
MF"v[144:147],a[64:67],v[72:75],v[144:147]\n"
"s_cmp_eq_u32 s41,163840\n"
EX"v30,v30\n"
MF"v[176:179],a[64:67],v[76:79],v[176:179]\n"
"s_cselect_b32 s41,0,s41\n"
PK"v26,v28,v29\n"
"v_add_u32 v226,s41,v224\n"
MF"v[84:87],a[68:71],v[64:67],v[84:87]\n"
EX"v31,v31\n"
MF"v[116:119],a[68:71],v[68:71],v[116:119]\n"
RD"a[84:87],v227 offset:21504\n"
PK"v27,v30,v31\n"
MF"v[148:151],a[68:71],v[72:75],v[148:151]\n"
EX"v32,v32\n"
MF"v[180:183],a[68:71],v[76:79],v[180:183]\n"
WT"lgkmcnt(2)\n"
BL"v225,s[8:11],s43 offen offset:1024 sc1 lds\n"
MF"v[88:91],a[72:75],v[64:67],v[88:91]\n"
EX"v33,v33\n"
MF"v[120:123],a[72:75],v[68:71],v[120:123]\n"
RD"a[88:91],v227 offset:22528\n"
MF"v[152:155],a[72:75],v[72:75],v[152:155]\n"
EX"v34,v34\n"
MF"v[184:187],a[72:75],v[76:79],v[184:187]\n"
MF"v[92:95],a[76:79],v[64:67],v[92:95]\n"
EX"v35,v35\n"
MF"v[124:127],a[76:79],v[68:71],v[124:127]\n"
RD"a[92:95],v227 offset:23552\n"
MF"v[156:159],a[76:79],v[72:75],v[156:159]\n"
EX"v40,v40\n"
MF"v[188:191],a[76:79],v[76:79],v[188:191]\n"
WT"lgkmcnt(2)\n"
BL"v225,s[8:11],s43 offen offset:2048 sc1 lds\n"
MF"v[96:99],a[80:83],v[64:67],v[96:99]\n"
EX"v41,v41\n"
MF"v[128:131],a[80:83],v[68:71],v[128:131]\n"
RD"a[96:99],v227 offset:24576\n"
MF"v[160:163],a[80:83],v[72:75],v[160:163]\n"
EX"v42,v42\n"
MF"v[192:195],a[80:83],v[76:79],v[192:195]\n"
"s_getpc_b64 s[12:13]\n"
"LMC_%=:\n"
SA"s12,s12,LMA_%=-LMC_%=\n"
"s_addc_u32 s13,s13,0\n"
MF"v[100:103],a[84:87],v[64:67],v[100:103]\n"
EX"v43,v43\n"
MF"v[132:135],a[84:87],v[68:71],v[132:135]\n"
"s_and_b32 s13,s13,0xffff\n"
RD"a[100:103],v227 offset:25600\n"
MF"v[164:167],a[84:87],v[72:75],v[164:167]\n"
EX"v48,v48\n"
MF"v[196:199],a[84:87],v[76:79],v[196:199]\n"
WT"lgkmcnt(2)\n"
BL"v225,s[8:11],s43 offen offset:3072 sc1 lds\n"
MF"v[104:107],a[88:91],v[64:67],v[104:107]\n"
"s_mov_b32 s14,LRT_%=-LMA_%=\n"
EX"v49,v49\n"
MF"v[136:139],a[88:91],v[68:71],v[136:139]\n"
"s_lshl_b32 s50,s30,11\n"
RD"a[104:107],v227 offset:26624\n"
MF"v[168:171],a[88:91],v[72:75],v[168:171]\n"
EX"v50,v50\n"
MF"v[200:203],a[88:91],v[76:79],v[200:203]\n"
SA"m0,s40,16384\n"
"v_min_u32 v244,240,v224\n"
MF"v[108:111],a[92:95],v[64:67],v[108:111]\n"
EX"v51,v51\n"
MF"v[140:143],a[92:95],v[68:71],v[140:143]\n"
RD"a[108:111],v227 offset:27648\n"
"v_lshlrev_b32 v244,3,v244\n"
MF"v[172:175],a[92:95],v[72:75],v[172:175]\n"
EX"v56,v56\n"
MF"v[204:207],a[92:95],v[76:79],v[204:207]\n"
"buffer_load_dword v255,v244,s[12:15],s50 offen\n"
MF"a[132:135],a[128:131],v[64:67],a[132:135]\n"
SA"s40,s40,32768\n"
BL"v225,s[8:11],s44 offen offset:0 sc1 lds\n"
MF"a[136:139],a[128:131],v[68:71],a[136:139]\n"
"s_mov_b32 s14,0\n"
EX"v57,v57\n"
MF"a[140:143],a[128:131],v[72:75],a[140:143]\n"
"s_cmp_eq_u32 s40,s46\n"
RD"a[112:115],v227 offset:28672\n"
"s_cselect_b32 s40,s47,s40\n"
MF"a[144:147],a[128:131],v[76:79],a[144:147]\n"
WT"lgkmcnt(2)\n"
EX"v58,v58\n"
MF"v[80:83],a[96:99],v[0:3],v[80:83]\n"
EX"v59,v59\n"
MF"v[112:115],a[96:99],v[8:11],v[112:115]\n"
RD"a[116:119],v227 offset:29696\n"
MF"v[144:147],a[96:99],v[16:19],v[144:147]\n"
EX"v36,v36\n"
MF"v[176:179],a[96:99],v[24:27],v[176:179]\n"
BL"v225,s[8:11],s44 offen offset:1024 sc1 lds\n"
MF"v[84:87],a[100:103],v[0:3],v[84:87]\n"
EX"v37,v37\n"
MF"v[116:119],a[100:103],v[8:11],v[116:119]\n"
RD"a[120:123],v227 offset:30720\n"
MF"v[148:151],a[100:103],v[16:19],v[148:151]\n"
EX"v38,v38\n"
MF"v[180:183],a[100:103],v[24:27],v[180:183]\n"
MF"v[88:91],a[104:107],v[0:3],v[88:91]\n"
EX"v39,v39\n"
MF"v[120:123],a[104:107],v[8:11],v[120:123]\n"
RD"a[124:127],v227 offset:31744\n"
MF"v[152:155],a[104:107],v[16:19],v[152:155]\n"
EX"v44,v44\n"
MF"v[184:187],a[104:107],v[24:27],v[184:187]\n"
WT"lgkmcnt(2)\n"
BL"v225,s[8:11],s44 offen offset:2048 sc1 lds\n"
MF"v[92:95],a[108:111],v[0:3],v[92:95]\n"
EX"v45,v45\n"
MF"v[124:127],a[108:111],v[8:11],v[124:127]\n"
MF"v[156:159],a[108:111],v[16:19],v[156:159]\n"
EX"v46,v46\n"
MF"v[188:191],a[108:111],v[24:27],v[188:191]\n"
WT"vmcnt(23) lgkmcnt(0)\n"
MF"v[96:99],a[112:115],v[0:3],v[96:99]\n"
"s_barrier\n"
MF"v[128:131],a[112:115],v[8:11],v[128:131]\n"
RD"a[64:67],v226 offset:0\n"
MF"v[160:163],a[112:115],v[16:19],v[160:163]\n"
EX"v47,v47\n"
MF"v[192:195],a[112:115],v[24:27],v[192:195]\n"
BL"v225,s[8:11],s44 offen offset:3072 sc1 lds\n"
MF"v[100:103],a[116:119],v[0:3],v[100:103]\n"
EX"v52,v52\n"
MF"v[132:135],a[116:119],v[8:11],v[132:135]\n"
RD"a[68:71],v226 offset:1024\n"
MF"v[164:167],a[116:119],v[16:19],v[164:167]\n"
EX"v53,v53\n"
MF"v[196:199],a[116:119],v[24:27],v[196:199]\n"
RD"a[72:75],v226 offset:2048\n"
MF"v[104:107],a[120:123],v[0:3],v[104:107]\n"
EX"v54,v54\n"
MF"v[136:139],a[120:123],v[8:11],v[136:139]\n"
RD"a[76:79],v226 offset:3072\n"
MF"v[168:171],a[120:123],v[16:19],v[168:171]\n"
EX"v55,v55\n"
MF"v[200:203],a[120:123],v[24:27],v[200:203]\n"
MF"v[108:111],a[124:127],v[0:3],v[108:111]\n"
EX"v60,v60\n"
MF"v[140:143],a[124:127],v[8:11],v[140:143]\n"
RD"a[80:83],v226 offset:4096\n"
MF"v[172:175],a[124:127],v[16:19],v[172:175]\n"
EX"v61,v61\n"
MF"v[204:207],a[124:127],v[24:27],v[204:207]\n"
RD"a[84:87],v226 offset:5120\n"
MF"a[132:135],a[128:131],v[0:3],a[132:135]\n"
EX"v62,v62\n"
MF"a[136:139],a[128:131],v[8:11],a[136:139]\n"
RD"a[88:91],v226 offset:6144\n"
MF"a[140:143],a[128:131],v[16:19],a[140:143]\n"
WT"lgkmcnt(5)\n"
EX"v63,v63\n"
MF"a[144:147],a[128:131],v[24:27],a[144:147]\n"
MF"v[0:3],a[64:67],a[0:3],0\n"
SA"s95,s40,32768\n"
"v_lshrrev_b32 v237,1,v224\n"
PK"v64,v32,v33\n"
MF"v[0:3],a[68:71],a[4:7],v[0:3]\n"
"s_cmp_eq_u32 s95,s46\n"
"v_and_b32 v237,0x78,v237\n"
PK"v65,v34,v35\n"
MF"v[8:11],a[68:71],a[20:23],0\n"
"s_cselect_b32 s95,s47,s95\n"
"v_and_b32 v239,0x300,v224\n"
PK"v66,v36,v37\n"
MF"v[8:11],a[64:67],a[16:19],v[8:11]\n"
BL"v[240:243],v238,s[8:11],0 offen sc1\n"
MF"v[16:19],a[64:67],a[32:35],0\n"
SA"m0,s95,0\n"
"v_add3_u32 v237,v237,v239,s40\n"
PK"v67,v38,v39\n"
MF"v[16:19],a[68:71],a[36:39],v[16:19]\n"
RD"a[92:95],v226 offset:7168\n"
PK"v68,v40,v41\n"
MF"v[24:27],a[68:71],a[52:55],0\n"
PK"v69,v42,v43\n"
PK"v70,v44,v45\n"
MF"v[24:27],a[64:67],a[48:51],v[24:27]\n"
WT"lgkmcnt(1)\n"
BL"v235,s[24:27],s52 offen lds\n"
MF"v[0:3],a[72:75],a[8:11],v[0:3]\n"
SA"s52,s52,s62\n"
PK"v71,v46,v47\n"
PK"v72,v48,v49\n"
MF"v[0:3],a[76:79],a[12:15],v[0:3]\n"
PK"v73,v50,v51\n"
PK"v74,v52,v53\n"
MF"v[8:11],a[76:79],a[28:31],v[8:11]\n"
PK"v75,v54,v55\n"
PK"v76,v56,v57\n"
MF"v[8:11],a[72:75],a[24:27],v[8:11]\n"
PK"v77,v58,v59\n"
PK"v78,v60,v61\n"
MF"v[16:19],a[72:75],a[40:43],v[16:19]\n"
SA"m0,s95,1024\n"
PK"v79,v62,v63\n"
MF"v[16:19],a[76:79],a[44:47],v[16:19]\n"
RD"a[96:99],v226 offset:8192\n"
MF"v[24:27],a[76:79],a[60:63],v[24:27]\n"
EX"v0,v0\n"
MF"v[24:27],a[72:75],a[56:59],v[24:27]\n"
BL"v235,s[24:27],s52 offen lds\n"
MF"v[4:7],a[80:83],a[0:3],0\n"
SA"s52,s52,s62\n"
EX"v1,v1\n"
MF"v[4:7],a[84:87],a[4:7],v[4:7]\n"
RD"a[100:103],v226 offset:9216\n"
PK"v0,v0,v1\n"
MF"v[12:15],a[84:87],a[20:23],0\n"
EX"v2,v2\n"
MF"v[12:15],a[80:83],a[16:19],v[12:15]\n"
EX"v3,v3\n"
MF"v[20:23],a[80:83],a[32:35],0\n"
SA"m0,s95,2048\n"
EX"v8,v8\n"
MF"v[20:23],a[84:87],a[36:39],v[20:23]\n"
RD"a[104:107],v226 offset:10240\n"
PK"v1,v2,v3\n"
MF"v[28:31],a[84:87],a[52:55],0\n"
EX"v9,v9\n"
MF"v[28:31],a[80:83],a[48:51],v[28:31]\n"
BL"v235,s[24:27],s52 offen lds\n"
MF"v[4:7],a[88:91],a[8:11],v[4:7]\n"
WT"lgkmcnt(1)\n"
EX"v10,v10\n"
MF"v[4:7],a[92:95],a[12:15],v[4:7]\n"
SA"s52,s52,s62\n"
RD"a[108:111],v226 offset:11264\n"
PK"v8,v8,v9\n"
MF"v[12:15],a[92:95],a[28:31],v[12:15]\n"
EX"v11,v11\n"
MF"v[12:15],a[88:91],a[24:27],v[12:15]\n"
WT"vmcnt(13)\n"
RT"a[148:149],v237 offset:0\n"
RT"a[150:151],v237 offset:128\n"
MF"v[20:23],a[88:91],a[40:43],v[20:23]\n"
SA"m0,s95,3072\n"
EX"v16,v16\n"
MF"v[20:23],a[92:95],a[44:47],v[20:23]\n"
EX"v17,v17\n"
MF"v[28:31],a[92:95],a[60:63],v[28:31]\n"
RD"a[112:115],v226 offset:12288\n"
PK"v9,v10,v11\n"
MF"v[28:31],a[88:91],a[56:59],v[28:31]\n"
EX"v18,v18\n"
MF"v[32:35],a[96:99],a[0:3],0\n"
RT"a[152:153],v237 offset:1024\n"
RT"a[154:155],v237 offset:1152\n"
MF"v[32:35],a[100:103],a[4:7],v[32:35]\n"
BL"v235,s[24:27],s52 offen lds\n"
MF"v[40:43],a[100:103],a[20:23],0\n"
SA"s52,s52,s62\n"
EX"v19,v19\n"
MF"v[40:43],a[96:99],a[16:19],v[40:43]\n"
RD"a[116:119],v226 offset:13312\n"
RT"a[156:157],v237 offset:2048\n"
MF"v[48:51],a[96:99],a[32:35],0\n"
SA"m0,s95,16384\n"
EX"v24,v24\n"
MF"v[48:51],a[100:103],a[36:39],v[48:51]\n"
RT"a[158:159],v237 offset:2176\n"
RD"a[120:123],v226 offset:14336\n"
EX"v25,v25\n"
MF"v[56:59],a[100:103],a[52:55],0\n"
EX"v26,v26\n"
PK"v16,v16,v17\n"
MF"v[56:59],a[96:99],a[48:51],v[56:59]\n"
WT"lgkmcnt(4)\n"
EX"v27,v27\n"
"v_mov_b32 v227,v226\n"
MF"v[32:35],a[104:107],a[8:11],v[32:35]\n"
RT"a[160:161],v237 offset:3072\n"
RT"a[162:163],v237 offset:3200\n"
MF"v[32:35],a[108:111],a[12:15],v[32:35]\n"
BL"v235,s[24:27],s52 offen lds\n"
PK"v17,v18,v19\n"
MF"v[40:43],a[108:111],a[28:31],v[40:43]\n"
SA"s52,s52,s62\n"
EX"v4,v4\n"
MF"v[40:43],a[104:107],a[24:27],v[40:43]\n"
RD"a[124:127],v226 offset:15360\n"
RT"a[164:165],v237 offset:16384\n"
MF"v[48:51],a[104:107],a[40:43],v[48:51]\n"
SA"m0,s95,17408\n"
EX"v5,v5\n"
PK"v24,v24,v25\n"
MF"v[48:51],a[108:111],a[44:47],v[48:51]\n"
RT"a[166:167],v237 offset:16512\n"
PK"v25,v26,v27\n"
EX"v6,v6\n"
MF"v[56:59],a[108:111],a[60:63],v[56:59]\n"
EX"v7,v7\n"
MF"v[56:59],a[104:107],a[56:59],v[56:59]\n"
EX"v12,v12\n"
MF"v[36:39],a[112:115],a[0:3],0\n"
WT"lgkmcnt(3)\n"
RT"a[168:169],v237 offset:17408\n"
RT"a[170:171],v237 offset:17536\n"
MF"v[36:39],a[116:119],a[4:7],v[36:39]\n"
BL"v235,s[24:27],s52 offen lds\n"
MF"v[44:47],a[116:119],a[20:23],0\n"
SA"s52,s52,s62\n"
EX"v13,v13\n"
MF"v[44:47],a[112:115],a[16:19],v[44:47]\n"
RD"a[64:67],v227 offset:16384\n"
RT"a[172:173],v237 offset:18432\n"
PK"v2,v4,v5\n"
MF"v[52:55],a[112:115],a[32:35],0\n"
SA"m0,s95,18432\n"
EX"v14,v14\n"
MF"v[52:55],a[116:119],a[36:39],v[52:55]\n"
RT"a[174:175],v237 offset:18560\n"
RD"a[68:71],v227 offset:17408\n"
PK"v3,v6,v7\n"
MF"v[60:63],a[116:119],a[52:55],0\n"
EX"v15,v15\n"
MF"v[60:63],a[112:115],a[48:51],v[60:63]\n"
EX"v20,v20\n"
MF"v[36:39],a[120:123],a[8:11],v[36:39]\n"
WT"lgkmcnt(4)\n"
BL"v235,s[24:27],s52 offen lds\n"
MF"v[36:39],a[124:127],a[12:15],v[36:39]\n"
SA"s52,s52,s62\n"
EX"v21,v21\n"
PK"v10,v12,v13\n"
MF"v[44:47],a[124:127],a[28:31],v[44:47]\n"
"s_cmp_gt_u32 s92,0\n"
"s_cselect_b32 s63,s63,1065353216\n"
WT"vmcnt(7)\n"
"v_cmp_eq_u64 vcc,s[28:29],v[240:241]\n"
"v_cmp_eq_u64_e64 s[50:51],s[28:29],v[242:243]\n"
"s_and_b64 vcc,vcc,s[50:51]\n"
"s_cmp_eq_u64 vcc,exec\n"
"s_cbranch_scc0 L1099_%=\n"
"L1100_%=:\n"
MF"v[44:47],a[120:123],a[24:27],v[44:47]\n"
"s_mov_b32 m0,s40\n"
RD"a[72:75],v227 offset:18432\n"
"v_readlane_b32 s43,v228,s42\n"
MF"v[52:55],a[120:123],a[40:43],v[52:55]\n"
EX"v22,v22\n"
MF"v[52:55],a[124:127],a[44:47],v[52:55]\n"
BL"a[204:207],v236,s[8:11],s63 offen offset:0 sc1\n"
PK"v11,v14,v15\n"
MF"v[60:63],a[124:127],a[60:63],v[60:63]\n"
EX"v23,v23\n"
MF"v[60:63],a[120:123],a[56:59],v[60:63]\n"
WT"lgkmcnt(1)\n"
RD"a[76:79],v227 offset:19456\n"
"v_readlane_b32 s44,v229,s42\n"
MF"v[80:83],a[64:67],v[64:67],v[80:83]\n"
SA"s42,s42,1\n"
EX"v28,v28\n"
MF"v[112:115],a[64:67],v[68:71],v[112:115]\n"
SA"s41,s41,32768\n"
BL"v225,s[8:11],s43 offen offset:0 sc1 lds\n"
PK"v18,v20,v21\n"
MF"v[144:147],a[64:67],v[72:75],v[144:147]\n"
"s_cmp_eq_u32 s41,163840\n"
EX"v29,v29\n"
MF"v[176:179],a[64:67],v[76:79],v[176:179]\n"
"s_cselect_b32 s41,0,s41\n"
RD"a[80:83],v227 offset:20480\n"
"v_add_u32 v226,s41,v224\n"
MF"v[84:87],a[68:71],v[64:67],v[84:87]\n"
EX"v30,v30\n"
MF"v[116:119],a[68:71],v[68:71],v[116:119]\n"
BL"a[208:211],v236,s[8:11],s63 offen offset:1024 sc1\n"
PK"v19,v22,v23\n"
MF"v[148:151],a[68:71],v[72:75],v[148:151]\n"
EX"v31,v31\n"
MF"v[180:183],a[68:71],v[76:79],v[180:183]\n"
WT"lgkmcnt(1)\n"
RD"a[84:87],v227 offset:21504\n"
PK"v26,v28,v29\n"
MF"v[88:91],a[72:75],v[64:67],v[88:91]\n"
EX"v32,v32\n"
MF"v[120:123],a[72:75],v[68:71],v[120:123]\n"
BL"v225,s[8:11],s43 offen offset:1024 sc1 lds\n"
MF"v[152:155],a[72:75],v[72:75],v[152:155]\n"
EX"v33,v33\n"
MF"v[184:187],a[72:75],v[76:79],v[184:187]\n"
RD"a[88:91],v227 offset:22528\n"
PK"v27,v30,v31\n"
MF"v[92:95],a[76:79],v[64:67],v[92:95]\n"
EX"v34,v34\n"
MF"v[124:127],a[76:79],v[68:71],v[124:127]\n"
BL"a[212:215],v236,s[8:11],s63 offen offset:2048 sc1\n"
MF"v[156:159],a[76:79],v[72:75],v[156:159]\n"
EX"v35,v35\n"
MF"v[188:191],a[76:79],v[76:79],v[188:191]\n"
WT"lgkmcnt(1)\n"
RD"a[92:95],v227 offset:23552\n"
MF"v[96:99],a[80:83],v[64:67],v[96:99]\n"
EX"v40,v40\n"
MF"v[128:131],a[80:83],v[68:71],v[128:131]\n"
BL"v225,s[8:11],s43 offen offset:2048 sc1 lds\n"
MF"v[160:163],a[80:83],v[72:75],v[160:163]\n"
EX"v41,v41\n"
MF"v[192:195],a[80:83],v[76:79],v[192:195]\n"
RD"a[96:99],v227 offset:24576\n"
MF"v[100:103],a[84:87],v[64:67],v[100:103]\n"
EX"v42,v42\n"
MF"v[132:135],a[84:87],v[68:71],v[132:135]\n"
BL"a[216:219],v236,s[8:11],s63 offen offset:3072 sc1\n"
MF"v[164:167],a[84:87],v[72:75],v[164:167]\n"
EX"v43,v43\n"
MF"v[196:199],a[84:87],v[76:79],v[196:199]\n"
WT"lgkmcnt(1)\n"
RD"a[100:103],v227 offset:25600\n"
SA"s63,s63,4096\n"
MF"v[104:107],a[88:91],v[64:67],v[104:107]\n"
EX"v48,v48\n"
MF"v[136:139],a[88:91],v[68:71],v[136:139]\n"
BL"v225,s[8:11],s43 offen offset:3072 sc1 lds\n"
MF"v[168:171],a[88:91],v[72:75],v[168:171]\n"
EX"v49,v49\n"
MF"v[200:203],a[88:91],v[76:79],v[200:203]\n"
SA"m0,s40,16384\n"
RD"a[104:107],v227 offset:26624\n"
MF"v[108:111],a[92:95],v[64:67],v[108:111]\n"
EX"v50,v50\n"
MF"v[140:143],a[92:95],v[68:71],v[140:143]\n"
BL"a[220:223],v236,s[8:11],s63 offen offset:0 sc1\n"
MF"v[172:175],a[92:95],v[72:75],v[172:175]\n"
EX"v51,v51\n"
MF"v[204:207],a[92:95],v[76:79],v[204:207]\n"
RD"a[108:111],v227 offset:27648\n"
MF"a[132:135],a[128:131],v[64:67],a[132:135]\n"
SA"s40,s40,32768\n"
EX"v56,v56\n"
MF"a[136:139],a[128:131],v[68:71],a[136:139]\n"
"s_cmp_eq_u32 s40,s46\n"
BL"v225,s[8:11],s44 offen offset:0 sc1 lds\n"
MF"a[140:143],a[128:131],v[72:75],a[140:143]\n"
"s_cselect_b32 s40,s47,s40\n"
EX"v57,v57\n"
MF"a[144:147],a[128:131],v[76:79],a[144:147]\n"
WT"lgkmcnt(1)\n"
RD"a[112:115],v227 offset:28672\n"
MF"v[80:83],a[96:99],v[0:3],v[80:83]\n"
EX"v58,v58\n"
MF"v[112:115],a[96:99],v[8:11],v[112:115]\n"
BL"a[224:227],v236,s[8:11],s63 offen offset:1024 sc1\n"
MF"v[144:147],a[96:99],v[16:19],v[144:147]\n"
EX"v59,v59\n"
MF"v[176:179],a[96:99],v[24:27],v[176:179]\n"
RD"a[116:119],v227 offset:29696\n"
MF"v[84:87],a[100:103],v[0:3],v[84:87]\n"
EX"v36,v36\n"
MF"v[116:119],a[100:103],v[8:11],v[116:119]\n"
BL"v225,s[8:11],s44 offen offset:1024 sc1 lds\n"
MF"v[148:151],a[100:103],v[16:19],v[148:151]\n"
EX"v37,v37\n"
MF"v[180:183],a[100:103],v[24:27],v[180:183]\n"
RD"a[120:123],v227 offset:30720\n"
MF"v[88:91],a[104:107],v[0:3],v[88:91]\n"
EX"v38,v38\n"
MF"v[120:123],a[104:107],v[8:11],v[120:123]\n"
BL"a[228:231],v236,s[8:11],s63 offen offset:2048 sc1\n"
MF"v[152:155],a[104:107],v[16:19],v[152:155]\n"
EX"v39,v39\n"
MF"v[184:187],a[104:107],v[24:27],v[184:187]\n"
WT"lgkmcnt(1)\n"
RD"a[124:127],v227 offset:31744\n"
MF"v[92:95],a[108:111],v[0:3],v[92:95]\n"
EX"v44,v44\n"
MF"v[124:127],a[108:111],v[8:11],v[124:127]\n"
BL"v225,s[8:11],s44 offen offset:2048 sc1 lds\n"
MF"v[156:159],a[108:111],v[16:19],v[156:159]\n"
EX"v45,v45\n"
MF"v[188:191],a[108:111],v[24:27],v[188:191]\n"
WT"lgkmcnt(0)\n"
EX"v46,v46\n"
MF"v[96:99],a[112:115],v[0:3],v[96:99]\n"
BL"a[232:235],v236,s[8:11],s63 offen offset:3072 sc1\n"
MF"v[128:131],a[112:115],v[8:11],v[128:131]\n"
"s_barrier\n"
MF"v[160:163],a[112:115],v[16:19],v[160:163]\n"
RD"a[64:67],v226 offset:0\n"
MF"v[192:195],a[112:115],v[24:27],v[192:195]\n"
SA"s63,s63,4096\n"
EX"v47,v47\n"
MF"v[100:103],a[116:119],v[0:3],v[100:103]\n"
BL"v225,s[8:11],s44 offen offset:3072 sc1 lds\n"
MF"v[132:135],a[116:119],v[8:11],v[132:135]\n"
EX"v52,v52\n"
MF"v[164:167],a[116:119],v[16:19],v[164:167]\n"
RD"a[68:71],v226 offset:1024\n"
MF"v[196:199],a[116:119],v[24:27],v[196:199]\n"
EX"v53,v53\n"
MF"v[104:107],a[120:123],v[0:3],v[104:107]\n"
BL"a[236:239],v236,s[8:11],s63 offen offset:0 sc1\n"
MF"v[136:139],a[120:123],v[8:11],v[136:139]\n"
RD"a[72:75],v226 offset:2048\n"
MF"v[168:171],a[120:123],v[16:19],v[168:171]\n"
EX"v54,v54\n"
MF"v[200:203],a[120:123],v[24:27],v[200:203]\n"
RD"a[76:79],v226 offset:3072\n"
MF"v[108:111],a[124:127],v[0:3],v[108:111]\n"
EX"v55,v55\n"
MF"v[140:143],a[124:127],v[8:11],v[140:143]\n"
EX"v60,v60\n"
MF"v[172:175],a[124:127],v[16:19],v[172:175]\n"
RD"a[80:83],v226 offset:4096\n"
MF"v[204:207],a[124:127],v[24:27],v[204:207]\n"
EX"v61,v61\n"
MF"a[132:135],a[128:131],v[0:3],a[132:135]\n"
RD"a[84:87],v226 offset:5120\n"
MF"a[136:139],a[128:131],v[8:11],a[136:139]\n"
EX"v62,v62\n"
MF"a[140:143],a[128:131],v[16:19],a[140:143]\n"
WT"lgkmcnt(4)\n"
RD"a[88:91],v226 offset:6144\n"
MF"a[144:147],a[128:131],v[24:27],a[144:147]\n"
EX"v63,v63\n"
MF"v[0:3],a[64:67],a[0:3],0\n"
SA"s95,s40,32768\n"
"v_lshrrev_b32 v237,1,v224\n"
PK"v64,v32,v33\n"
MF"v[0:3],a[68:71],a[4:7],v[0:3]\n"
"s_cmp_eq_u32 s95,s46\n"
"v_and_b32 v237,0x78,v237\n"
PK"v65,v34,v35\n"
MF"v[8:11],a[68:71],a[20:23],0\n"
"s_cselect_b32 s95,s47,s95\n"
"v_and_b32 v239,0x300,v224\n"
PK"v66,v36,v37\n"
MF"v[8:11],a[64:67],a[16:19],v[8:11]\n"
"s_sub_u32 s95,s95,s47\n"
BL"a[240:243],v236,s[8:11],s63 offen offset:1024 sc1\n"
MF"v[16:19],a[64:67],a[32:35],0\n"
"s_lshr_b32 s53,s47,2\n"
"v_add3_u32 v237,v237,v239,s40\n"
PK"v67,v38,v39\n"
MF"v[16:19],a[68:71],a[36:39],v[16:19]\n"
"s_cmp_gt_u32 s92,10\n"
RD"a[92:95],v226 offset:7168\n"
PK"v68,v40,v41\n"
MF"v[24:27],a[68:71],a[52:55],0\n"
"s_cselect_b32 s63,s63,1065353216\n"
PK"v69,v42,v43\n"
PK"v70,v44,v45\n"
MF"v[24:27],a[64:67],a[48:51],v[24:27]\n"
WT"lgkmcnt(1)\n"
BL"a[244:247],v236,s[8:11],s63 offen offset:2048 sc1\n"
MF"v[0:3],a[72:75],a[8:11],v[0:3]\n"
"s_lshl_b32 s50,s35,6\n"
PK"v71,v46,v47\n"
"v_add_u32 v235,s95,v224\n"
MF"v[0:3],a[76:79],a[12:15],v[0:3]\n"
SA"s50,s50,4352\n"
PK"v72,v48,v49\n"
PK"v73,v50,v51\n"
MF"v[8:11],a[76:79],a[28:31],v[8:11]\n"
PK"v74,v52,v53\n"
PK"v75,v54,v55\n"
MF"v[8:11],a[72:75],a[24:27],v[8:11]\n"
BL"a[248:251],v236,s[8:11],s63 offen offset:3072 sc1\n"
MF"v[16:19],a[72:75],a[40:43],v[16:19]\n"
SA"s63,s63,4096\n"
PK"v76,v56,v57\n"
PK"v77,v58,v59\n"
MF"v[16:19],a[76:79],a[44:47],v[16:19]\n"
RD"a[96:99],v226 offset:8192\n"
PK"v78,v60,v61\n"
MF"v[24:27],a[76:79],a[60:63],v[24:27]\n"
EX"v0,v0\n"
MF"v[24:27],a[72:75],a[56:59],v[24:27]\n"
BL"a[252:255],v236,s[8:11],s63 offen offset:0 sc1\n"
PK"v79,v62,v63\n"
MF"v[4:7],a[80:83],a[0:3],0\n"
"s_cmp_gt_u32 s92,13\n"
EX"v1,v1\n"
"v_lshrrev_b32 v238,1,v224\n"
MF"v[4:7],a[84:87],a[4:7],v[4:7]\n"
"s_cselect_b32 s63,s63,1065353216\n"
RD"a[100:103],v226 offset:9216\n"
"v_and_b32 v238,56,v238\n"
MF"v[12:15],a[84:87],a[20:23],0\n"
EX"v2,v2\n"
MF"v[12:15],a[80:83],a[16:19],v[12:15]\n"
BL"v[244:247],v236,s[8:11],s63 offen offset:1024 sc1\n"
"v_add_u32 v236,s53,v235\n"
MF"v[20:23],a[80:83],a[32:35],0\n"
EX"v3,v3\n"
MF"v[20:23],a[84:87],a[36:39],v[20:23]\n"
EX"v8,v8\n"
MF"v[28:31],a[84:87],a[52:55],0\n"
RD"a[104:107],v226 offset:10240\n"
PK"v0,v0,v1\n"
MF"v[28:31],a[80:83],a[48:51],v[28:31]\n"
EX"v9,v9\n"
MF"v[4:7],a[88:91],a[8:11],v[4:7]\n"
WT"lgkmcnt(1)\n"
"buffer_load_dwordx2 v[240:241],v238,s[8:11],s50 offen sc1\n"
MF"v[4:7],a[92:95],a[12:15],v[4:7]\n"
EX"v10,v10\n"
MF"v[12:15],a[92:95],a[28:31],v[12:15]\n"
RD"a[108:111],v226 offset:11264\n"
PK"v1,v2,v3\n"
MF"v[12:15],a[88:91],a[24:27],v[12:15]\n"
EX"v11,v11\n"
MF"v[20:23],a[88:91],a[40:43],v[20:23]\n"
WT"vmcnt(23)\n"
RT"a[176:177],v237 offset:0\n"
RT"a[178:179],v237 offset:128\n"
MF"v[20:23],a[92:95],a[44:47],v[20:23]\n"
EX"v16,v16\n"
MF"v[28:31],a[92:95],a[60:63],v[28:31]\n"
EX"v17,v17\n"
MF"v[28:31],a[88:91],a[56:59],v[28:31]\n"
RD"a[112:115],v226 offset:12288\n"
RT"a[180:181],v237 offset:1024\n"
PK"v8,v8,v9\n"
MF"v[32:35],a[96:99],a[0:3],0\n"
EX"v18,v18\n"
MF"v[32:35],a[100:103],a[4:7],v[32:35]\n"
RT"a[182:183],v237 offset:1152\n"
RD"a[116:119],v226 offset:13312\n"
MF"v[40:43],a[100:103],a[20:23],0\n"
EX"v19,v19\n"
MF"v[40:43],a[96:99],a[16:19],v[40:43]\n"
EX"v24,v24\n"
PK"v9,v10,v11\n"
MF"v[48:51],a[96:99],a[32:35],0\n"
RT"a[184:185],v237 offset:2048\n"
RT"a[186:187],v237 offset:2176\n"
MF"v[48:51],a[100:103],a[36:39],v[48:51]\n"
EX"v25,v25\n"
PK"v16,v16,v17\n"
MF"v[56:59],a[100:103],a[52:55],0\n"
EX"v26,v26\n"
"v_mov_b32 v227,v226\n"
MF"v[56:59],a[96:99],a[48:51],v[56:59]\n"
WT"lgkmcnt(4)\n"
RD"a[120:123],v226 offset:14336\n"
RT"a[188:189],v237 offset:3072\n"
MF"v[32:35],a[104:107],a[8:11],v[32:35]\n"
EX"v27,v27\n"
MF"v[32:35],a[108:111],a[12:15],v[32:35]\n"
RT"a[190:191],v237 offset:3200\n"
RD"a[124:127],v226 offset:15360\n"
PK"v17,v18,v19\n"
MF"v[40:43],a[108:111],a[28:31],v[40:43]\n"
EX"v4,v4\n"
MF"v[40:43],a[104:107],a[24:27],v[40:43]\n"
EX"v5,v5\n"
MF"v[48:51],a[104:107],a[40:43],v[48:51]\n"
RT"a[192:193],v237 offset:16384\n"
RT"a[194:195],v237 offset:16512\n"
PK"v24,v24,v25\n"
MF"v[48:51],a[108:111],a[44:47],v[48:51]\n"
EX"v6,v6\n"
MF"v[56:59],a[108:111],a[60:63],v[56:59]\n"
EX"v7,v7\n"
MF"v[56:59],a[104:107],a[56:59],v[56:59]\n"
EX"v12,v12\n"
PK"v25,v26,v27\n"
MF"v[36:39],a[112:115],a[0:3],0\n"
WT"lgkmcnt(2)\n"
RT"a[196:197],v237 offset:17408\n"
RT"a[198:199],v237 offset:17536\n"
MF"v[36:39],a[116:119],a[4:7],v[36:39]\n"
EX"v13,v13\n"
MF"v[44:47],a[116:119],a[20:23],0\n"
RD"a[64:67],v227 offset:16384\n"
PK"v2,v4,v5\n"
MF"v[44:47],a[112:115],a[16:19],v[44:47]\n"
EX"v14,v14\n"
MF"v[52:55],a[112:115],a[32:35],0\n"
RT"a[200:201],v237 offset:18432\n"
RT"a[202:203],v237 offset:18560\n"
MF"v[52:55],a[116:119],a[36:39],v[52:55]\n"
EX"v15,v15\n"
PK"v3,v6,v7\n"
MF"v[60:63],a[116:119],a[52:55],0\n"
RD"a[68:71],v227 offset:17408\n"
PK"v10,v12,v13\n"
MF"v[60:63],a[112:115],a[48:51],v[60:63]\n"
EX"v20,v20\n"
MF"v[36:39],a[120:123],a[8:11],v[36:39]\n"
EX"v21,v21\n"
MF"v[36:39],a[124:127],a[12:15],v[36:39]\n"
RD"a[72:75],v227 offset:18432\n"
"v_readlane_b32 s43,v228,s42\n"
MF"v[44:47],a[124:127],a[28:31],v[44:47]\n"
EX"v22,v22\n"
MF"v[44:47],a[120:123],a[24:27],v[44:47]\n"
"s_mov_b32 m0,s40\n"
PK"v11,v14,v15\n"
PK"v18,v20,v21\n"
MF"v[52:55],a[120:123],a[40:43],v[52:55]\n"
EX"v23,v23\n"
MF"v[52:55],a[124:127],a[44:47],v[52:55]\n"
RD"a[76:79],v227 offset:19456\n"
"v_readlane_b32 s44,v229,s42\n"
MF"v[60:63],a[124:127],a[60:63],v[60:63]\n"
EX"v28,v28\n"
MF"v[60:63],a[120:123],a[56:59],v[60:63]\n"
WT"lgkmcnt(2)\n"
BL"v225,s[8:11],s43 offen offset:0 sc1 lds\n"
MF"v[80:83],a[64:67],v[64:67],v[80:83]\n"
SA"s42,s42,1\n"
EX"v29,v29\n"
MF"v[112:115],a[64:67],v[68:71],v[112:115]\n"
SA"s41,s41,32768\n"
RD"a[80:83],v227 offset:20480\n"
PK"v19,v22,v23\n"
MF"v[144:147],a[64:67],v[72:75],v[144:147]\n"
"s_cmp_eq_u32 s41,163840\n"
EX"v30,v30\n"
MF"v[176:179],a[64:67],v[76:79],v[176:179]\n"
"s_cselect_b32 s41,0,s41\n"
PK"v26,v28,v29\n"
"v_add_u32 v226,s41,v224\n"
MF"v[84:87],a[68:71],v[64:67],v[84:87]\n"
EX"v31,v31\n"
MF"v[116:119],a[68:71],v[68:71],v[116:119]\n"
RD"a[84:87],v227 offset:21504\n"
PK"v27,v30,v31\n"
MF"v[148:151],a[68:71],v[72:75],v[148:151]\n"
WT"vmcnt(1)\n"
"v_cmp_eq_u64 vcc,s[28:29],v[240:241]\n"
"s_cmp_eq_u64 vcc,exec\n"
"s_cbranch_scc0 L1102_%=\n"
"L1103_%=:\n"
MF"v[180:183],a[68:71],v[76:79],v[180:183]\n"
WT"lgkmcnt(2)\n"
EX"v32,v32\n"
MF"v[88:91],a[72:75],v[64:67],v[88:91]\n"
BL"v225,s[8:11],s43 offen offset:1024 sc1 lds\n"
MF"v[120:123],a[72:75],v[68:71],v[120:123]\n"
EX"v33,v33\n"
MF"v[152:155],a[72:75],v[72:75],v[152:155]\n"
RD"a[88:91],v227 offset:22528\n"
MF"v[184:187],a[72:75],v[76:79],v[184:187]\n"
EX"v34,v34\n"
MF"v[92:95],a[76:79],v[64:67],v[92:95]\n"
EX"v35,v35\n"
MF"v[124:127],a[76:79],v[68:71],v[124:127]\n"
RD"a[92:95],v227 offset:23552\n"
MF"v[156:159],a[76:79],v[72:75],v[156:159]\n"
EX"v40,v40\n"
MF"v[188:191],a[76:79],v[76:79],v[188:191]\n"
WT"lgkmcnt(2)\n"
BL"v225,s[8:11],s43 offen offset:2048 sc1 lds\n"
MF"v[96:99],a[80:83],v[64:67],v[96:99]\n"
EX"v41,v41\n"
MF"v[128:131],a[80:83],v[68:71],v[128:131]\n"
RD"a[96:99],v227 offset:24576\n"
MF"v[160:163],a[80:83],v[72:75],v[160:163]\n"
EX"v42,v42\n"
MF"v[192:195],a[80:83],v[76:79],v[192:195]\n"
MF"v[100:103],a[84:87],v[64:67],v[100:103]\n"
EX"v43,v43\n"
MF"v[132:135],a[84:87],v[68:71],v[132:135]\n"
RD"a[100:103],v227 offset:25600\n"
MF"v[164:167],a[84:87],v[72:75],v[164:167]\n"
EX"v48,v48\n"
MF"v[196:199],a[84:87],v[76:79],v[196:199]\n"
WT"lgkmcnt(2)\n"
BL"v225,s[8:11],s43 offen offset:3072 sc1 lds\n"
MF"v[104:107],a[88:91],v[64:67],v[104:107]\n"
EX"v49,v49\n"
MF"v[136:139],a[88:91],v[68:71],v[136:139]\n"
RD"a[104:107],v227 offset:26624\n"
MF"v[168:171],a[88:91],v[72:75],v[168:171]\n"
EX"v50,v50\n"
MF"v[200:203],a[88:91],v[76:79],v[200:203]\n"
SA"m0,s40,16384\n"
MF"v[108:111],a[92:95],v[64:67],v[108:111]\n"
EX"v51,v51\n"
MF"v[140:143],a[92:95],v[68:71],v[140:143]\n"
RD"a[108:111],v227 offset:27648\n"
MF"v[172:175],a[92:95],v[72:75],v[172:175]\n"
EX"v56,v56\n"
MF"v[204:207],a[92:95],v[76:79],v[204:207]\n"
BL"v225,s[8:11],s44 offen offset:0 sc1 lds\n"
MF"a[132:135],a[128:131],v[64:67],a[132:135]\n"
SA"s40,s40,32768\n"
EX"v57,v57\n"
MF"a[136:139],a[128:131],v[68:71],a[136:139]\n"
"s_cmp_eq_u32 s40,s46\n"
RD"a[112:115],v227 offset:28672\n"
"s_cselect_b32 s40,s47,s40\n"
MF"a[140:143],a[128:131],v[72:75],a[140:143]\n"
EX"v58,v58\n"
MF"a[144:147],a[128:131],v[76:79],a[144:147]\n"
MF"v[240:243],a[148:151],a[204:207],0\n"
EX"v59,v59\n"
MF"v[240:243],a[152:155],a[208:211],v[240:243]\n"
RD"a[116:119],v227 offset:29696\n"
MF"v[240:243],a[156:159],a[212:215],v[240:243]\n"
EX"v36,v36\n"
MF"v[240:243],a[160:163],a[216:219],v[240:243]\n"
BL"v225,s[8:11],s44 offen offset:1024 sc1 lds\n"
MF"v[240:243],a[164:167],a[220:223],v[240:243]\n"
EX"v37,v37\n"
MF"v[240:243],a[168:171],a[224:227],v[240:243]\n"
RD"a[120:123],v227 offset:30720\n"
MF"v[240:243],a[172:175],a[228:231],v[240:243]\n"
EX"v38,v38\n"
MF"v[240:243],a[176:179],a[232:235],v[240:243]\n"
MF"v[240:243],a[180:183],a[236:239],v[240:243]\n"
EX"v39,v39\n"
MF"v[240:243],a[184:187],a[240:243],v[240:243]\n"
RD"a[124:127],v227 offset:31744\n"
MF"v[240:243],a[188:191],a[244:247],v[240:243]\n"
EX"v44,v44\n"
MF"v[240:243],a[192:195],a[248:251],v[240:243]\n"
BL"v225,s[8:11],s44 offen offset:2048 sc1 lds\n"
MF"v[240:243],a[196:199],a[252:255],v[240:243]\n"
EX"v45,v45\n"
MF"v[240:243],a[200:203],v[244:247],v[240:243]\n"
WT"lgkmcnt(1)\n"
MF"v[80:83],a[96:99],v[0:3],v[80:83]\n"
EX"v46,v46\n"
MF"v[112:115],a[96:99],v[8:11],v[112:115]\n"
MF"v[144:147],a[96:99],v[16:19],v[144:147]\n"
MF"v[176:179],a[96:99],v[24:27],v[176:179]\n"
MF"v[84:87],a[100:103],v[0:3],v[84:87]\n"
EX"v47,v47\n"
MF"v[116:119],a[100:103],v[8:11],v[116:119]\n"
"ds_write_b128 v236,v[240:243]\n"
MF"v[148:151],a[100:103],v[16:19],v[148:151]\n"
BL"v225,s[8:11],s44 offen offset:3072 sc1 lds\n"
MF"v[180:183],a[100:103],v[24:27],v[180:183]\n"
EX"v52,v52\n"
MF"v[88:91],a[104:107],v[0:3],v[88:91]\n"
EX"v53,v53\n"
MF"v[120:123],a[104:107],v[8:11],v[120:123]\n"
MF"v[152:155],a[104:107],v[16:19],v[152:155]\n"
EX"v54,v54\n"
MF"v[184:187],a[104:107],v[24:27],v[184:187]\n"
MF"v[92:95],a[108:111],v[0:3],v[92:95]\n"
EX"v55,v55\n"
MF"v[124:127],a[108:111],v[8:11],v[124:127]\n"
MF"v[156:159],a[108:111],v[16:19],v[156:159]\n"
EX"v60,v60\n"
MF"v[188:191],a[108:111],v[24:27],v[188:191]\n"
WT"lgkmcnt(0)\n"
MF"v[96:99],a[112:115],v[0:3],v[96:99]\n"
EX"v61,v61\n"
MF"v[128:131],a[112:115],v[8:11],v[128:131]\n"
"s_barrier\n"
MF"v[160:163],a[112:115],v[16:19],v[160:163]\n"
RD"a[64:67],v226 offset:0\n"
MF"v[192:195],a[112:115],v[24:27],v[192:195]\n"
EX"v62,v62\n"
MF"v[100:103],a[116:119],v[0:3],v[100:103]\n"
RD"v[240:243],v235 offset:0\n"
RD"v[244:247],v235 offset:1024\n"
MF"v[132:135],a[116:119],v[8:11],v[132:135]\n"
EX"v63,v63\n"
MF"v[164:167],a[116:119],v[16:19],v[164:167]\n"
RD"a[68:71],v226 offset:1024\n"
RD"v[248:251],v235 offset:2048\n"
MF"v[196:199],a[116:119],v[24:27],v[196:199]\n"
RD"a[72:75],v226 offset:2048\n"
MF"v[104:107],a[120:123],v[0:3],v[104:107]\n"
RD"v[252:255],v235 offset:3072\n"
MF"v[136:139],a[120:123],v[8:11],v[136:139]\n"
RD"a[76:79],v226 offset:3072\n"
MF"v[168:171],a[120:123],v[16:19],v[168:171]\n"
MF"v[200:203],a[120:123],v[24:27],v[200:203]\n"
MF"v[108:111],a[124:127],v[0:3],v[108:111]\n"
MF"v[140:143],a[124:127],v[8:11],v[140:143]\n"
RD"a[80:83],v226 offset:4096\n"
MF"v[172:175],a[124:127],v[16:19],v[172:175]\n"
MF"v[204:207],a[124:127],v[24:27],v[204:207]\n"
RD"a[84:87],v226 offset:5120\n"
MF"a[132:135],a[128:131],v[0:3],a[132:135]\n"
MF"a[136:139],a[128:131],v[8:11],a[136:139]\n"
RD"a[88:91],v226 offset:6144\n"
MF"a[140:143],a[128:131],v[16:19],a[140:143]\n"
WT"lgkmcnt(7)\n"
MF"a[144:147],a[128:131],v[24:27],a[144:147]\n"
"s_cmp_eq_u32 s57,2\n"
"s_cbranch_scc1 LMD_%=\n"
"s_cmp_eq_u32 s34,2\n"
"s_cbranch_scc1 LMA_%=\n"
MF"v[0:3],a[64:67],a[0:3],0\n"
"v_add_f32 v240,v240,v244\n"
PK"v64,v32,v33\n"
MF"v[0:3],a[68:71],a[4:7],v[0:3]\n"
PK"v65,v34,v35\n"
PK"v66,v36,v37\n"
MF"v[8:11],a[68:71],a[20:23],0\n"
WT"lgkmcnt(4)\n"
"v_add_f32 v248,v248,v252\n"
PK"v67,v38,v39\n"
MF"v[8:11],a[64:67],a[16:19],v[8:11]\n"
"v_add_f32 v241,v241,v245\n"
PK"v68,v40,v41\n"
MF"v[16:19],a[64:67],a[32:35],0\n"
PK"v69,v42,v43\n"
"v_add_f32 v249,v249,v253\n"
MF"v[16:19],a[68:71],a[36:39],v[16:19]\n"
RD"a[92:95],v226 offset:7168\n"
PK"v70,v44,v45\n"
MF"v[24:27],a[68:71],a[52:55],0\n"
PK"v71,v46,v47\n"
"v_add_f32 v242,v242,v246\n"
MF"v[24:27],a[64:67],a[48:51],v[24:27]\n"
PK"v72,v48,v49\n"
PK"v73,v50,v51\n"
MF"v[0:3],a[72:75],a[8:11],v[0:3]\n"
WT"lgkmcnt(1)\n"
PK"v74,v52,v53\n"
PK"v75,v54,v55\n"
MF"v[0:3],a[76:79],a[12:15],v[0:3]\n"
"v_add_f32 v250,v250,v254\n"
PK"v76,v56,v57\n"
MF"v[8:11],a[76:79],a[28:31],v[8:11]\n"
PK"v77,v58,v59\n"
"v_add_f32 v243,v243,v247\n"
MF"v[8:11],a[72:75],a[24:27],v[8:11]\n"
PK"v78,v60,v61\n"
PK"v79,v62,v63\n"
MF"v[16:19],a[72:75],a[40:43],v[16:19]\n"
"v_add_f32 v251,v251,v255\n"
PK"v240,v240,v240\n"
MF"v[16:19],a[76:79],a[44:47],v[16:19]\n"
RD"a[96:99],v226 offset:8192\n"
"v_lshlrev_b32 v240,16,v240\n"
MF"v[24:27],a[76:79],a[60:63],v[24:27]\n"
EX"v0,v0\n"
MF"v[24:27],a[72:75],a[56:59],v[24:27]\n"
PK"v241,v241,v241\n"
"v_lshlrev_b32 v241,16,v241\n"
MF"v[4:7],a[80:83],a[0:3],0\n"
EX"v1,v1\n"
MF"v[4:7],a[84:87],a[4:7],v[4:7]\n"
RD"a[100:103],v226 offset:9216\n"
PK"v242,v242,v242\n"
MF"v[12:15],a[84:87],a[20:23],0\n"
EX"v2,v2\n"
MF"v[12:15],a[80:83],a[16:19],v[12:15]\n"
EX"v3,v3\n"
MF"v[20:23],a[80:83],a[32:35],0\n"
EX"v8,v8\n"
MF"v[20:23],a[84:87],a[36:39],v[20:23]\n"
RD"a[104:107],v226 offset:10240\n"
"v_lshlrev_b32 v242,16,v242\n"
MF"v[28:31],a[84:87],a[52:55],0\n"
EX"v9,v9\n"
MF"v[28:31],a[80:83],a[48:51],v[28:31]\n"
PK"v243,v243,v243\n"
PK"v0,v0,v1\n"
MF"v[4:7],a[88:91],a[8:11],v[4:7]\n"
WT"lgkmcnt(1)\n"
EX"v10,v10\n"
MF"v[4:7],a[92:95],a[12:15],v[4:7]\n"
RD"a[108:111],v226 offset:11264\n"
"v_lshlrev_b32 v243,16,v243\n"
MF"v[12:15],a[92:95],a[28:31],v[12:15]\n"
EX"v11,v11\n"
MF"v[12:15],a[88:91],a[24:27],v[12:15]\n"
EX"v16,v16\n"
MU"v244,0xbfb8aa3b,v240\n"
MF"v[20:23],a[88:91],a[40:43],v[20:23]\n"
EX"v244,v244\n"
MU"v245,0xbfb8aa3b,v241\n"
MF"v[20:23],a[92:95],a[44:47],v[20:23]\n"
EX"v245,v245\n"
PK"v1,v2,v3\n"
MF"v[28:31],a[92:95],a[60:63],v[28:31]\n"
EX"v17,v17\n"
MU"v246,0xbfb8aa3b,v242\n"
MF"v[28:31],a[88:91],a[56:59],v[28:31]\n"
RD"a[112:115],v226 offset:12288\n"
MU"v247,0xbfb8aa3b,v243\n"
PK"v8,v8,v9\n"
MF"v[32:35],a[96:99],a[0:3],0\n"
EX"v18,v18\n"
MF"v[32:35],a[100:103],a[4:7],v[32:35]\n"
EX"v246,v246\n"
PK"v248,v248,v248\n"
MF"v[40:43],a[100:103],a[20:23],0\n"
EX"v19,v19\n"
"v_lshlrev_b32 v248,16,v248\n"
MF"v[40:43],a[96:99],a[16:19],v[40:43]\n"
RD"a[116:119],v226 offset:13312\n"
PK"v9,v10,v11\n"
MF"v[48:51],a[96:99],a[32:35],0\n"
EX"v247,v247\n"
EX"v24,v24\n"
MF"v[48:51],a[100:103],a[36:39],v[48:51]\n"
EX"v25,v25\n"
PK"v249,v249,v249\n"
MF"v[56:59],a[100:103],a[52:55],0\n"
EX"v26,v26\n"
PK"v16,v16,v17\n"
MF"v[56:59],a[96:99],a[48:51],v[56:59]\n"
WT"lgkmcnt(1)\n"
RD"a[120:123],v226 offset:14336\n"
"v_lshlrev_b32 v249,16,v249\n"
"v_mov_b32 v227,v226\n"
MF"v[32:35],a[104:107],a[8:11],v[32:35]\n"
EX"v27,v27\n"
PK"v250,v250,v250\n"
MF"v[32:35],a[108:111],a[12:15],v[32:35]\n"
EX"v4,v4\n"
"v_lshlrev_b32 v250,16,v250\n"
MF"v[40:43],a[108:111],a[28:31],v[40:43]\n"
RD"a[124:127],v226 offset:15360\n"
PK"v17,v18,v19\n"
MF"v[40:43],a[104:107],a[24:27],v[40:43]\n"
EX"v5,v5\n"
MF"v[48:51],a[104:107],a[40:43],v[48:51]\n"
EX"v6,v6\n"
PK"v251,v251,v251\n"
MF"v[48:51],a[108:111],a[44:47],v[48:51]\n"
EX"v7,v7\n"
PK"v24,v24,v25\n"
MF"v[56:59],a[108:111],a[60:63],v[56:59]\n"
EX"v12,v12\n"
"v_lshlrev_b32 v251,16,v251\n"
MF"v[56:59],a[104:107],a[56:59],v[56:59]\n"
"v_add_f32 v244,1.0,v244\n"
"v_add_f32 v245,1.0,v245\n"
"v_rcp_f32 v244,v244\n"
MF"v[36:39],a[112:115],a[0:3],0\n"
WT"lgkmcnt(1)\n"
"v_rcp_f32 v245,v245\n"
PK"v25,v26,v27\n"
MF"v[36:39],a[116:119],a[4:7],v[36:39]\n"
EX"v13,v13\n"
"v_add_f32 v246,1.0,v246\n"
MF"v[44:47],a[116:119],a[20:23],0\n"
"v_rcp_f32 v246,v246\n"
"v_add_f32 v247,1.0,v247\n"
MF"v[44:47],a[112:115],a[16:19],v[44:47]\n"
"v_rcp_f32 v247,v247\n"
PK"v2,v4,v5\n"
MF"v[52:55],a[112:115],a[32:35],0\n"
RD"a[64:67],v227 offset:16384\n"
PK"v3,v6,v7\n"
EX"v14,v14\n"
MF"v[52:55],a[116:119],a[36:39],v[52:55]\n"
EX"v15,v15\n"
MF"v[60:63],a[116:119],a[52:55],0\n"
RD"a[68:71],v227 offset:17408\n"
MU"v240,v240,v244\n"
MF"v[60:63],a[112:115],a[48:51],v[60:63]\n"
EX"v20,v20\n"
MF"v[36:39],a[120:123],a[8:11],v[36:39]\n"
WT"lgkmcnt(2)\n"
EX"v21,v21\n"
MF"v[36:39],a[124:127],a[12:15],v[36:39]\n"
RD"a[72:75],v227 offset:18432\n"
"v_readlane_b32 s43,v228,s42\n"
MU"v241,v241,v245\n"
MF"v[44:47],a[124:127],a[28:31],v[44:47]\n"
EX"v22,v22\n"
PK"v10,v12,v13\n"
MF"v[44:47],a[120:123],a[24:27],v[44:47]\n"
"s_mov_b32 m0,s40\n"
MU"v242,v242,v246\n"
MU"v243,v243,v247\n"
MF"v[52:55],a[120:123],a[40:43],v[52:55]\n"
EX"v23,v23\n"
PK"v240,v240,v240\n"
MF"v[52:55],a[124:127],a[44:47],v[52:55]\n"
RD"a[76:79],v227 offset:19456\n"
"v_readlane_b32 s44,v229,s42\n"
PK"v11,v14,v15\n"
MF"v[60:63],a[124:127],a[60:63],v[60:63]\n"
EX"v28,v28\n"
"v_lshlrev_b32 v240,16,v240\n"
MF"v[60:63],a[120:123],a[56:59],v[60:63]\n"
WT"lgkmcnt(2)\n"
BL"v225,s[8:11],s43 offen offset:0 sc1 lds\n"
PK"v241,v241,v241\n"
MF"v[80:83],a[64:67],v[64:67],v[80:83]\n"
SA"s42,s42,1\n"
EX"v29,v29\n"
"v_lshlrev_b32 v241,16,v241\n"
MF"v[112:115],a[64:67],v[68:71],v[112:115]\n"
SA"s41,s41,32768\n"
RD"a[80:83],v227 offset:20480\n"
PK"v242,v242,v242\n"
PK"v18,v20,v21\n"
MF"v[144:147],a[64:67],v[72:75],v[144:147]\n"
"s_cmp_eq_u32 s41,163840\n"
EX"v30,v30\n"
"v_lshlrev_b32 v242,16,v242\n"
MF"v[176:179],a[64:67],v[76:79],v[176:179]\n"
"s_cselect_b32 s41,0,s41\n"
PK"v243,v243,v243\n"
"v_add_u32 v226,s41,v224\n"
MF"v[84:87],a[68:71],v[64:67],v[84:87]\n"
EX"v31,v31\n"
MF"v[116:119],a[68:71],v[68:71],v[116:119]\n"
RD"a[84:87],v227 offset:21504\n"
"v_lshlrev_b32 v243,16,v243\n"
PK"v19,v22,v23\n"
MF"v[148:151],a[68:71],v[72:75],v[148:151]\n"
EX"v32,v32\n"
MU"v240,v240,v248\n"
MF"v[180:183],a[68:71],v[76:79],v[180:183]\n"
WT"lgkmcnt(2)\n"
BL"v225,s[8:11],s43 offen offset:1024 sc1 lds\n"
MU"v241,v241,v249\n"
MF"v[88:91],a[72:75],v[64:67],v[88:91]\n"
EX"v33,v33\n"
MU"v242,v242,v250\n"
MF"v[120:123],a[72:75],v[68:71],v[120:123]\n"
RD"a[88:91],v227 offset:22528\n"
PK"v26,v28,v29\n"
MF"v[152:155],a[72:75],v[72:75],v[152:155]\n"
EX"v34,v34\n"
MU"v243,v243,v251\n"
MF"v[184:187],a[72:75],v[76:79],v[184:187]\n"
PK"v240,v240,v241\n"
PK"v241,v242,v243\n"
MF"v[92:95],a[76:79],v[64:67],v[92:95]\n"
EX"v35,v35\n"
MF"v[124:127],a[76:79],v[68:71],v[124:127]\n"
RD"a[92:95],v227 offset:23552\n"
"v_lshrrev_b32 v239,9,v224\n"
PK"v27,v30,v31\n"
MF"v[156:159],a[76:79],v[72:75],v[156:159]\n"
EX"v40,v40\n"
"v_lshlrev_b32 v239,8,v239\n"
MF"v[188:191],a[76:79],v[76:79],v[188:191]\n"
WT"lgkmcnt(2)\n"
BL"v225,s[8:11],s43 offen offset:2048 sc1 lds\n"
MF"v[96:99],a[80:83],v[64:67],v[96:99]\n"
EX"v41,v41\n"
"v_bfe_u32 v237,v224,8,1\n"
MF"v[128:131],a[80:83],v[68:71],v[128:131]\n"
RD"a[96:99],v227 offset:24576\n"
"v_lshl_add_u32 v239,v237,3,v239\n"
MF"v[160:163],a[80:83],v[72:75],v[160:163]\n"
EX"v42,v42\n"
"v_and_b32 v237,0x80,v224\n"
MF"v[192:195],a[80:83],v[76:79],v[192:195]\n"
"v_lshl_add_u32 v239,v237,23,v239\n"
"v_and_b32 v237,0x70,v224\n"
MF"v[100:103],a[84:87],v[64:67],v[100:103]\n"
EX"v43,v43\n"
MF"v[132:135],a[84:87],v[68:71],v[132:135]\n"
RD"a[100:103],v227 offset:25600\n"
"v_add_u32 v239,v239,v237\n"
MF"v[164:167],a[84:87],v[72:75],v[164:167]\n"
"buffer_store_dwordx2 v[240:241],v239,s[8:11],s93 offen sc1\n"
MF"v[196:199],a[84:87],v[76:79],v[196:199]\n"
WT"lgkmcnt(2)\n"
EX"v48,v48\n"
MF"v[104:107],a[88:91],v[64:67],v[104:107]\n"
BL"v225,s[8:11],s43 offen offset:3072 sc1 lds\n"
MF"v[136:139],a[88:91],v[68:71],v[136:139]\n"
EX"v49,v49\n"
MF"v[168:171],a[88:91],v[72:75],v[168:171]\n"
RD"a[104:107],v227 offset:26624\n"
MF"v[200:203],a[88:91],v[76:79],v[200:203]\n"
SA"m0,s40,16384\n"
EX"v50,v50\n"
MF"v[108:111],a[92:95],v[64:67],v[108:111]\n"
EX"v51,v51\n"
MF"v[140:143],a[92:95],v[68:71],v[140:143]\n"
RD"a[108:111],v227 offset:27648\n"
MF"v[172:175],a[92:95],v[72:75],v[172:175]\n"
EX"v56,v56\n"
MF"v[204:207],a[92:95],v[76:79],v[204:207]\n"
BL"v225,s[8:11],s44 offen offset:0 sc1 lds\n"
MF"a[132:135],a[128:131],v[64:67],a[132:135]\n"
SA"s40,s40,32768\n"
EX"v57,v57\n"
MF"a[136:139],a[128:131],v[68:71],a[136:139]\n"
"s_cmp_eq_u32 s40,s46\n"
RD"a[112:115],v227 offset:28672\n"
"s_cselect_b32 s40,s47,s40\n"
MF"a[140:143],a[128:131],v[72:75],a[140:143]\n"
EX"v58,v58\n"
MF"a[144:147],a[128:131],v[76:79],a[144:147]\n"
WT"lgkmcnt(2)\n"
MF"v[80:83],a[96:99],v[0:3],v[80:83]\n"
EX"v59,v59\n"
MF"v[112:115],a[96:99],v[8:11],v[112:115]\n"
RD"a[116:119],v227 offset:29696\n"
MF"v[144:147],a[96:99],v[16:19],v[144:147]\n"
EX"v36,v36\n"
MF"v[176:179],a[96:99],v[24:27],v[176:179]\n"
BL"v225,s[8:11],s44 offen offset:1024 sc1 lds\n"
MF"v[84:87],a[100:103],v[0:3],v[84:87]\n"
EX"v37,v37\n"
MF"v[116:119],a[100:103],v[8:11],v[116:119]\n"
RD"a[120:123],v227 offset:30720\n"
MF"v[148:151],a[100:103],v[16:19],v[148:151]\n"
EX"v38,v38\n"
MF"v[180:183],a[100:103],v[24:27],v[180:183]\n"
MF"v[88:91],a[104:107],v[0:3],v[88:91]\n"
EX"v39,v39\n"
MF"v[120:123],a[104:107],v[8:11],v[120:123]\n"
RD"a[124:127],v227 offset:31744\n"
MF"v[152:155],a[104:107],v[16:19],v[152:155]\n"
EX"v44,v44\n"
MF"v[184:187],a[104:107],v[24:27],v[184:187]\n"
WT"lgkmcnt(2)\n"
BL"v225,s[8:11],s44 offen offset:2048 sc1 lds\n"
MF"v[92:95],a[108:111],v[0:3],v[92:95]\n"
EX"v45,v45\n"
MF"v[124:127],a[108:111],v[8:11],v[124:127]\n"
MF"v[156:159],a[108:111],v[16:19],v[156:159]\n"
EX"v46,v46\n"
MF"v[188:191],a[108:111],v[24:27],v[188:191]\n"
WT"lgkmcnt(0)\n"
MF"v[96:99],a[112:115],v[0:3],v[96:99]\n"
"s_barrier\n"
MF"v[128:131],a[112:115],v[8:11],v[128:131]\n"
RD"a[64:67],v226 offset:0\n"
MF"v[160:163],a[112:115],v[16:19],v[160:163]\n"
EX"v47,v47\n"
MF"v[192:195],a[112:115],v[24:27],v[192:195]\n"
BL"v225,s[8:11],s44 offen offset:3072 sc1 lds\n"
MF"v[100:103],a[116:119],v[0:3],v[100:103]\n"
EX"v52,v52\n"
MF"v[132:135],a[116:119],v[8:11],v[132:135]\n"
RD"a[68:71],v226 offset:1024\n"
MF"v[164:167],a[116:119],v[16:19],v[164:167]\n"
EX"v53,v53\n"
MF"v[196:199],a[116:119],v[24:27],v[196:199]\n"
RD"a[72:75],v226 offset:2048\n"
MF"v[104:107],a[120:123],v[0:3],v[104:107]\n"
EX"v54,v54\n"
MF"v[136:139],a[120:123],v[8:11],v[136:139]\n"
RD"a[76:79],v226 offset:3072\n"
MF"v[168:171],a[120:123],v[16:19],v[168:171]\n"
EX"v55,v55\n"
MF"v[200:203],a[120:123],v[24:27],v[200:203]\n"
MF"v[108:111],a[124:127],v[0:3],v[108:111]\n"
EX"v60,v60\n"
MF"v[140:143],a[124:127],v[8:11],v[140:143]\n"
RD"a[80:83],v226 offset:4096\n"
MF"v[172:175],a[124:127],v[16:19],v[172:175]\n"
EX"v61,v61\n"
MF"v[204:207],a[124:127],v[24:27],v[204:207]\n"
WT"vmcnt(5)\n"
"buffer_store_dwordx2 v[232:233],v230,s[8:11],s94 offen sc1\n"
MF"a[132:135],a[128:131],v[0:3],a[132:135]\n"
RD"a[84:87],v226 offset:5120\n"
MF"a[136:139],a[128:131],v[8:11],a[136:139]\n"
EX"v62,v62\n"
MF"a[140:143],a[128:131],v[16:19],a[140:143]\n"
WT"lgkmcnt(4)\n"
RD"a[88:91],v226 offset:6144\n"
MF"a[144:147],a[128:131],v[24:27],a[144:147]\n"
EX"v63,v63\n"
"s_mov_b32 s57,0\n"
"s_lshl_b32 s56,s34,1\n"
"s_sub_u32 s56,s56,6\n"
"s_branch LP1_%=\n"
".p2align 6\n"
"LMA_%=:\n"
MF"v[0:3],a[64:67],a[0:3],v[208:211]\n"
"v_add_f32 v240,v240,v244\n"
PK"v64,v32,v33\n"
MF"v[0:3],a[68:71],a[4:7],v[0:3]\n"
PK"v65,v34,v35\n"
PK"v66,v36,v37\n"
MF"v[8:11],a[68:71],a[20:23],0\n"
WT"lgkmcnt(4)\n"
"v_add_f32 v248,v248,v252\n"
PK"v67,v38,v39\n"
MF"v[8:11],a[64:67],a[16:19],v[8:11]\n"
"v_add_f32 v241,v241,v245\n"
PK"v68,v40,v41\n"
MF"v[16:19],a[64:67],a[32:35],0\n"
PK"v69,v42,v43\n"
"v_add_f32 v249,v249,v253\n"
MF"v[16:19],a[68:71],a[36:39],v[16:19]\n"
RD"a[92:95],v226 offset:7168\n"
PK"v70,v44,v45\n"
MF"v[24:27],a[68:71],a[52:55],0\n"
PK"v71,v46,v47\n"
"v_add_f32 v242,v242,v246\n"
MF"v[24:27],a[64:67],a[48:51],v[24:27]\n"
PK"v72,v48,v49\n"
PK"v73,v50,v51\n"
MF"v[0:3],a[72:75],a[8:11],v[0:3]\n"
WT"lgkmcnt(1)\n"
PK"v74,v52,v53\n"
PK"v75,v54,v55\n"
MF"v[0:3],a[76:79],a[12:15],v[0:3]\n"
"v_add_f32 v250,v250,v254\n"
PK"v76,v56,v57\n"
MF"v[8:11],a[76:79],a[28:31],v[8:11]\n"
PK"v77,v58,v59\n"
"v_add_f32 v243,v243,v247\n"
MF"v[8:11],a[72:75],a[24:27],v[8:11]\n"
PK"v78,v60,v61\n"
PK"v79,v62,v63\n"
MF"v[16:19],a[72:75],a[40:43],v[16:19]\n"
"v_add_f32 v251,v251,v255\n"
PK"v240,v240,v240\n"
MF"v[16:19],a[76:79],a[44:47],v[16:19]\n"
RD"a[96:99],v226 offset:8192\n"
"v_lshlrev_b32 v240,16,v240\n"
MF"v[24:27],a[76:79],a[60:63],v[24:27]\n"
EX"v0,v0\n"
MF"v[24:27],a[72:75],a[56:59],v[24:27]\n"
PK"v241,v241,v241\n"
"v_lshlrev_b32 v241,16,v241\n"
MF"v[4:7],a[80:83],a[0:3],v[212:215]\n"
EX"v1,v1\n"
MF"v[4:7],a[84:87],a[4:7],v[4:7]\n"
RD"a[100:103],v226 offset:9216\n"
PK"v242,v242,v242\n"
MF"v[12:15],a[84:87],a[20:23],0\n"
EX"v2,v2\n"
MF"v[12:15],a[80:83],a[16:19],v[12:15]\n"
EX"v3,v3\n"
MF"v[20:23],a[80:83],a[32:35],0\n"
EX"v8,v8\n"
MF"v[20:23],a[84:87],a[36:39],v[20:23]\n"
RD"a[104:107],v226 offset:10240\n"
"v_lshlrev_b32 v242,16,v242\n"
MF"v[28:31],a[84:87],a[52:55],0\n"
EX"v9,v9\n"
MF"v[28:31],a[80:83],a[48:51],v[28:31]\n"
PK"v243,v243,v243\n"
PK"v0,v0,v1\n"
MF"v[4:7],a[88:91],a[8:11],v[4:7]\n"
WT"lgkmcnt(1)\n"
EX"v10,v10\n"
MF"v[4:7],a[92:95],a[12:15],v[4:7]\n"
RD"a[108:111],v226 offset:11264\n"
"v_lshlrev_b32 v243,16,v243\n"
MF"v[12:15],a[92:95],a[28:31],v[12:15]\n"
EX"v11,v11\n"
MF"v[12:15],a[88:91],a[24:27],v[12:15]\n"
EX"v16,v16\n"
MU"v244,0xbfb8aa3b,v240\n"
MF"v[20:23],a[88:91],a[40:43],v[20:23]\n"
EX"v244,v244\n"
MU"v245,0xbfb8aa3b,v241\n"
MF"v[20:23],a[92:95],a[44:47],v[20:23]\n"
EX"v245,v245\n"
PK"v1,v2,v3\n"
MF"v[28:31],a[92:95],a[60:63],v[28:31]\n"
EX"v17,v17\n"
MU"v246,0xbfb8aa3b,v242\n"
MF"v[28:31],a[88:91],a[56:59],v[28:31]\n"
RD"a[112:115],v226 offset:12288\n"
MU"v247,0xbfb8aa3b,v243\n"
PK"v8,v8,v9\n"
MF"v[32:35],a[96:99],a[0:3],v[216:219]\n"
EX"v18,v18\n"
MF"v[32:35],a[100:103],a[4:7],v[32:35]\n"
EX"v246,v246\n"
PK"v248,v248,v248\n"
MF"v[40:43],a[100:103],a[20:23],0\n"
EX"v19,v19\n"
"v_lshlrev_b32 v248,16,v248\n"
MF"v[40:43],a[96:99],a[16:19],v[40:43]\n"
RD"a[116:119],v226 offset:13312\n"
PK"v9,v10,v11\n"
MF"v[48:51],a[96:99],a[32:35],0\n"
EX"v247,v247\n"
EX"v24,v24\n"
MF"v[48:51],a[100:103],a[36:39],v[48:51]\n"
EX"v25,v25\n"
PK"v249,v249,v249\n"
MF"v[56:59],a[100:103],a[52:55],0\n"
EX"v26,v26\n"
PK"v16,v16,v17\n"
MF"v[56:59],a[96:99],a[48:51],v[56:59]\n"
WT"lgkmcnt(1)\n"
RD"a[120:123],v226 offset:14336\n"
"v_lshlrev_b32 v249,16,v249\n"
"v_mov_b32 v227,v226\n"
MF"v[32:35],a[104:107],a[8:11],v[32:35]\n"
EX"v27,v27\n"
PK"v250,v250,v250\n"
MF"v[32:35],a[108:111],a[12:15],v[32:35]\n"
EX"v4,v4\n"
"v_lshlrev_b32 v250,16,v250\n"
MF"v[40:43],a[108:111],a[28:31],v[40:43]\n"
RD"a[124:127],v226 offset:15360\n"
PK"v17,v18,v19\n"
MF"v[40:43],a[104:107],a[24:27],v[40:43]\n"
EX"v5,v5\n"
MF"v[48:51],a[104:107],a[40:43],v[48:51]\n"
EX"v6,v6\n"
PK"v251,v251,v251\n"
MF"v[48:51],a[108:111],a[44:47],v[48:51]\n"
EX"v7,v7\n"
PK"v24,v24,v25\n"
MF"v[56:59],a[108:111],a[60:63],v[56:59]\n"
EX"v12,v12\n"
"v_lshlrev_b32 v251,16,v251\n"
MF"v[56:59],a[104:107],a[56:59],v[56:59]\n"
"v_add_f32 v244,1.0,v244\n"
"v_add_f32 v245,1.0,v245\n"
"v_rcp_f32 v244,v244\n"
MF"v[36:39],a[112:115],a[0:3],v[220:223]\n"
WT"lgkmcnt(1)\n"
"v_rcp_f32 v245,v245\n"
PK"v25,v26,v27\n"
MF"v[36:39],a[116:119],a[4:7],v[36:39]\n"
EX"v13,v13\n"
"v_add_f32 v246,1.0,v246\n"
MF"v[44:47],a[116:119],a[20:23],0\n"
"v_rcp_f32 v246,v246\n"
"v_add_f32 v247,1.0,v247\n"
MF"v[44:47],a[112:115],a[16:19],v[44:47]\n"
"v_rcp_f32 v247,v247\n"
PK"v2,v4,v5\n"
MF"v[52:55],a[112:115],a[32:35],0\n"
RD"a[64:67],v227 offset:16384\n"
PK"v3,v6,v7\n"
EX"v14,v14\n"
MF"v[52:55],a[116:119],a[36:39],v[52:55]\n"
EX"v15,v15\n"
MF"v[60:63],a[116:119],a[52:55],0\n"
RD"a[68:71],v227 offset:17408\n"
MU"v240,v240,v244\n"
MF"v[60:63],a[112:115],a[48:51],v[60:63]\n"
EX"v20,v20\n"
MF"v[36:39],a[120:123],a[8:11],v[36:39]\n"
WT"lgkmcnt(2)\n"
EX"v21,v21\n"
MF"v[36:39],a[124:127],a[12:15],v[36:39]\n"
RD"a[72:75],v227 offset:18432\n"
"v_readlane_b32 s43,v228,s42\n"
MU"v241,v241,v245\n"
MF"v[44:47],a[124:127],a[28:31],v[44:47]\n"
EX"v22,v22\n"
PK"v10,v12,v13\n"
MF"v[44:47],a[120:123],a[24:27],v[44:47]\n"
"s_mov_b32 m0,s40\n"
MU"v242,v242,v246\n"
MU"v243,v243,v247\n"
MF"v[52:55],a[120:123],a[40:43],v[52:55]\n"
EX"v23,v23\n"
PK"v240,v240,v240\n"
MF"v[52:55],a[124:127],a[44:47],v[52:55]\n"
RD"a[76:79],v227 offset:19456\n"
"v_readlane_b32 s44,v229,s42\n"
PK"v11,v14,v15\n"
MF"v[60:63],a[124:127],a[60:63],v[60:63]\n"
EX"v28,v28\n"
"v_lshlrev_b32 v240,16,v240\n"
MF"v[60:63],a[120:123],a[56:59],v[60:63]\n"
WT"lgkmcnt(2)\n"
BL"v225,s[8:11],s43 offen offset:0 sc1 lds\n"
PK"v241,v241,v241\n"
MF"v[80:83],a[64:67],v[64:67],v[80:83]\n"
SA"s42,s42,1\n"
EX"v29,v29\n"
"v_lshlrev_b32 v241,16,v241\n"
MF"v[112:115],a[64:67],v[68:71],v[112:115]\n"
SA"s41,s41,32768\n"
RD"a[80:83],v227 offset:20480\n"
PK"v242,v242,v242\n"
PK"v18,v20,v21\n"
MF"v[144:147],a[64:67],v[72:75],v[144:147]\n"
"s_cmp_eq_u32 s41,163840\n"
EX"v30,v30\n"
"v_lshlrev_b32 v242,16,v242\n"
MF"v[176:179],a[64:67],v[76:79],v[176:179]\n"
"s_cselect_b32 s41,0,s41\n"
PK"v243,v243,v243\n"
"v_add_u32 v226,s41,v224\n"
MF"v[84:87],a[68:71],v[64:67],v[84:87]\n"
EX"v31,v31\n"
MF"v[116:119],a[68:71],v[68:71],v[116:119]\n"
RD"a[84:87],v227 offset:21504\n"
"v_lshlrev_b32 v243,16,v243\n"
PK"v19,v22,v23\n"
MF"v[148:151],a[68:71],v[72:75],v[148:151]\n"
EX"v32,v32\n"
MU"v240,v240,v248\n"
MF"v[180:183],a[68:71],v[76:79],v[180:183]\n"
WT"lgkmcnt(2)\n"
BL"v225,s[8:11],s43 offen offset:1024 sc1 lds\n"
MU"v241,v241,v249\n"
MF"v[88:91],a[72:75],v[64:67],v[88:91]\n"
EX"v33,v33\n"
MU"v242,v242,v250\n"
MF"v[120:123],a[72:75],v[68:71],v[120:123]\n"
RD"a[88:91],v227 offset:22528\n"
PK"v26,v28,v29\n"
MF"v[152:155],a[72:75],v[72:75],v[152:155]\n"
EX"v34,v34\n"
MU"v243,v243,v251\n"
MF"v[184:187],a[72:75],v[76:79],v[184:187]\n"
PK"v240,v240,v241\n"
PK"v241,v242,v243\n"
MF"v[92:95],a[76:79],v[64:67],v[92:95]\n"
EX"v35,v35\n"
MF"v[124:127],a[76:79],v[68:71],v[124:127]\n"
RD"a[92:95],v227 offset:23552\n"
"v_lshrrev_b32 v239,9,v224\n"
PK"v27,v30,v31\n"
MF"v[156:159],a[76:79],v[72:75],v[156:159]\n"
EX"v40,v40\n"
"v_lshlrev_b32 v239,8,v239\n"
MF"v[188:191],a[76:79],v[76:79],v[188:191]\n"
WT"lgkmcnt(2)\n"
BL"v225,s[8:11],s43 offen offset:2048 sc1 lds\n"
MF"v[96:99],a[80:83],v[64:67],v[96:99]\n"
EX"v41,v41\n"
"v_bfe_u32 v237,v224,8,1\n"
MF"v[128:131],a[80:83],v[68:71],v[128:131]\n"
RD"a[96:99],v227 offset:24576\n"
"v_lshl_add_u32 v239,v237,3,v239\n"
MF"v[160:163],a[80:83],v[72:75],v[160:163]\n"
EX"v42,v42\n"
"v_and_b32 v237,0x80,v224\n"
MF"v[192:195],a[80:83],v[76:79],v[192:195]\n"
"v_lshl_add_u32 v239,v237,23,v239\n"
"v_and_b32 v237,0x70,v224\n"
MF"v[100:103],a[84:87],v[64:67],v[100:103]\n"
EX"v43,v43\n"
MF"v[132:135],a[84:87],v[68:71],v[132:135]\n"
RD"a[100:103],v227 offset:25600\n"
"v_add_u32 v239,v239,v237\n"
MF"v[164:167],a[84:87],v[72:75],v[164:167]\n"
"buffer_store_dwordx2 v[240:241],v239,s[8:11],s93 offen sc1\n"
MF"v[196:199],a[84:87],v[76:79],v[196:199]\n"
WT"lgkmcnt(2)\n"
EX"v48,v48\n"
MF"v[104:107],a[88:91],v[64:67],v[104:107]\n"
BL"v225,s[8:11],s43 offen offset:3072 sc1 lds\n"
MF"v[136:139],a[88:91],v[68:71],v[136:139]\n"
EX"v49,v49\n"
MF"v[168:171],a[88:91],v[72:75],v[168:171]\n"
RD"a[104:107],v227 offset:26624\n"
MF"v[200:203],a[88:91],v[76:79],v[200:203]\n"
SA"m0,s40,16384\n"
EX"v50,v50\n"
MF"v[108:111],a[92:95],v[64:67],v[108:111]\n"
EX"v51,v51\n"
MF"v[140:143],a[92:95],v[68:71],v[140:143]\n"
RD"a[108:111],v227 offset:27648\n"
MF"v[172:175],a[92:95],v[72:75],v[172:175]\n"
EX"v56,v56\n"
MF"v[204:207],a[92:95],v[76:79],v[204:207]\n"
BL"v225,s[8:11],s44 offen offset:0 sc1 lds\n"
MF"a[132:135],a[128:131],v[64:67],a[132:135]\n"
SA"s40,s40,32768\n"
EX"v57,v57\n"
MF"a[136:139],a[128:131],v[68:71],a[136:139]\n"
"s_cmp_eq_u32 s40,s46\n"
RD"a[112:115],v227 offset:28672\n"
"s_cselect_b32 s40,s47,s40\n"
MF"a[140:143],a[128:131],v[72:75],a[140:143]\n"
EX"v58,v58\n"
MF"a[144:147],a[128:131],v[76:79],a[144:147]\n"
WT"lgkmcnt(2)\n"
MF"v[80:83],a[96:99],v[0:3],v[80:83]\n"
EX"v59,v59\n"
MF"v[112:115],a[96:99],v[8:11],v[112:115]\n"
RD"a[116:119],v227 offset:29696\n"
MF"v[144:147],a[96:99],v[16:19],v[144:147]\n"
EX"v36,v36\n"
MF"v[176:179],a[96:99],v[24:27],v[176:179]\n"
BL"v225,s[8:11],s44 offen offset:1024 sc1 lds\n"
MF"v[84:87],a[100:103],v[0:3],v[84:87]\n"
EX"v37,v37\n"
MF"v[116:119],a[100:103],v[8:11],v[116:119]\n"
RD"a[120:123],v227 offset:30720\n"
MF"v[148:151],a[100:103],v[16:19],v[148:151]\n"
EX"v38,v38\n"
MF"v[180:183],a[100:103],v[24:27],v[180:183]\n"
MF"v[88:91],a[104:107],v[0:3],v[88:91]\n"
EX"v39,v39\n"
MF"v[120:123],a[104:107],v[8:11],v[120:123]\n"
RD"a[124:127],v227 offset:31744\n"
MF"v[152:155],a[104:107],v[16:19],v[152:155]\n"
EX"v44,v44\n"
MF"v[184:187],a[104:107],v[24:27],v[184:187]\n"
WT"lgkmcnt(2)\n"
BL"v225,s[8:11],s44 offen offset:2048 sc1 lds\n"
MF"v[92:95],a[108:111],v[0:3],v[92:95]\n"
EX"v45,v45\n"
MF"v[124:127],a[108:111],v[8:11],v[124:127]\n"
MF"v[156:159],a[108:111],v[16:19],v[156:159]\n"
EX"v46,v46\n"
MF"v[188:191],a[108:111],v[24:27],v[188:191]\n"
WT"lgkmcnt(0)\n"
MF"v[96:99],a[112:115],v[0:3],v[96:99]\n"
"s_barrier\n"
MF"v[128:131],a[112:115],v[8:11],v[128:131]\n"
RD"a[64:67],v226 offset:0\n"
MF"v[160:163],a[112:115],v[16:19],v[160:163]\n"
EX"v47,v47\n"
MF"v[192:195],a[112:115],v[24:27],v[192:195]\n"
BL"v225,s[8:11],s44 offen offset:3072 sc1 lds\n"
MF"v[100:103],a[116:119],v[0:3],v[100:103]\n"
EX"v52,v52\n"
MF"v[132:135],a[116:119],v[8:11],v[132:135]\n"
RD"a[68:71],v226 offset:1024\n"
MF"v[164:167],a[116:119],v[16:19],v[164:167]\n"
EX"v53,v53\n"
MF"v[196:199],a[116:119],v[24:27],v[196:199]\n"
RD"a[72:75],v226 offset:2048\n"
MF"v[104:107],a[120:123],v[0:3],v[104:107]\n"
EX"v54,v54\n"
MF"v[136:139],a[120:123],v[8:11],v[136:139]\n"
RD"a[76:79],v226 offset:3072\n"
MF"v[168:171],a[120:123],v[16:19],v[168:171]\n"
EX"v55,v55\n"
MF"v[200:203],a[120:123],v[24:27],v[200:203]\n"
MF"v[108:111],a[124:127],v[0:3],v[108:111]\n"
EX"v60,v60\n"
MF"v[140:143],a[124:127],v[8:11],v[140:143]\n"
RD"a[80:83],v226 offset:4096\n"
MF"v[172:175],a[124:127],v[16:19],v[172:175]\n"
EX"v61,v61\n"
MF"v[204:207],a[124:127],v[24:27],v[204:207]\n"
WT"vmcnt(5)\n"
"buffer_store_dwordx2 v[232:233],v230,s[8:11],s94 offen sc1\n"
MF"a[132:135],a[128:131],v[0:3],a[132:135]\n"
RD"a[84:87],v226 offset:5120\n"
MF"a[136:139],a[128:131],v[8:11],a[136:139]\n"
EX"v62,v62\n"
MF"a[140:143],a[128:131],v[16:19],a[140:143]\n"
WT"lgkmcnt(4)\n"
RD"a[88:91],v226 offset:6144\n"
MF"a[144:147],a[128:131],v[24:27],a[144:147]\n"
EX"v63,v63\n"
"s_branch LDB_%=\n"
".p2align 6\n"
"LMD_%=:\n"
"s_cmp_eq_u32 s34,6\n"
"s_cbranch_scc1 LME_%=\n"
MF"v[0:3],a[64:67],a[0:3],v[208:211]\n"
"v_lshrrev_b32 v239,9,v224\n"
"v_lshlrev_b32 v239,8,v239\n"
MF"v[0:3],a[68:71],a[4:7],v[0:3]\n"
"v_bfe_u32 v237,v224,8,1\n"
"v_lshl_add_u32 v239,v237,3,v239\n"
MF"v[8:11],a[68:71],a[20:23],0\n"
"v_and_b32 v237,0x80,v224\n"
PK"v64,v32,v33\n"
MF"v[8:11],a[64:67],a[16:19],v[8:11]\n"
"v_lshl_add_u32 v239,v237,23,v239\n"
"v_and_b32 v237,0x70,v224\n"
MF"v[16:19],a[64:67],a[32:35],0\n"
"s_mul_i32 s53,s35,20480\n"
"v_add_u32 v239,v239,v237\n"
PK"v65,v34,v35\n"
MF"v[16:19],a[68:71],a[36:39],v[16:19]\n"
SA"s53,s53,s93\n"
RD"a[92:95],v226 offset:7168\n"
PK"v66,v36,v37\n"
MF"v[24:27],a[68:71],a[52:55],0\n"
SA"s53,s53,3074048\n"
PK"v67,v38,v39\n"
PK"v68,v40,v41\n"
MF"v[24:27],a[64:67],a[48:51],v[24:27]\n"
WT"lgkmcnt(1)\n"
"buffer_load_dwordx2 v[236:237],v239,s[8:11],s53 offen sc1\n"
MF"v[0:3],a[72:75],a[8:11],v[0:3]\n"
PK"v69,v42,v43\n"
PK"v70,v44,v45\n"
MF"v[0:3],a[76:79],a[12:15],v[0:3]\n"
PK"v71,v46,v47\n"
PK"v72,v48,v49\n"
MF"v[8:11],a[76:79],a[28:31],v[8:11]\n"
PK"v73,v50,v51\n"
PK"v74,v52,v53\n"
MF"v[8:11],a[72:75],a[24:27],v[8:11]\n"
PK"v75,v54,v55\n"
"v_add_f32 v240,v240,v244\n"
MF"v[16:19],a[72:75],a[40:43],v[16:19]\n"
"v_add_f32 v248,v248,v252\n"
PK"v76,v56,v57\n"
MF"v[16:19],a[76:79],a[44:47],v[16:19]\n"
RD"a[96:99],v226 offset:8192\n"
PK"v77,v58,v59\n"
MF"v[24:27],a[76:79],a[60:63],v[24:27]\n"
EX"v0,v0\n"
MF"v[24:27],a[72:75],a[56:59],v[24:27]\n"
"v_add_f32 v241,v241,v245\n"
PK"v78,v60,v61\n"
PK"v79,v62,v63\n"
MF"v[4:7],a[80:83],a[0:3],v[212:215]\n"
EX"v1,v1\n"
"v_add_f32 v249,v249,v253\n"
MF"v[4:7],a[84:87],a[4:7],v[4:7]\n"
RD"a[100:103],v226 offset:9216\n"
"v_add_f32 v242,v242,v246\n"
MF"v[12:15],a[84:87],a[20:23],0\n"
EX"v2,v2\n"
"v_add_f32 v250,v250,v254\n"
MF"v[12:15],a[80:83],a[16:19],v[12:15]\n"
EX"v3,v3\n"
"v_add_f32 v243,v243,v247\n"
MF"v[20:23],a[80:83],a[32:35],0\n"
EX"v8,v8\n"
"v_add_f32 v251,v251,v255\n"
MF"v[20:23],a[84:87],a[36:39],v[20:23]\n"
RD"a[104:107],v226 offset:10240\n"
"v_ashrrev_i32 v235,31,v234\n"
"v_not_b32 v235,v235\n"
MF"v[28:31],a[84:87],a[52:55],0\n"
EX"v9,v9\n"
"v_or_b32 v235,0x80000000,v235\n"
MF"v[28:31],a[80:83],a[48:51],v[28:31]\n"
"v_xor_b32 v235,v235,v234\n"
"v_and_b32 v235,0xffff0000,v235\n"
MF"v[4:7],a[88:91],a[8:11],v[4:7]\n"
WT"lgkmcnt(1)\n"
EX"v10,v10\n"
MF"v[4:7],a[92:95],a[12:15],v[4:7]\n"
RD"a[108:111],v226 offset:11264\n"
PK"v0,v0,v1\n"
MF"v[12:15],a[92:95],a[28:31],v[12:15]\n"
EX"v11,v11\n"
MU"v235,0xbfb8aa3b,v235\n"
MF"v[12:15],a[88:91],a[24:27],v[12:15]\n"
EX"v235,v235\n"
MF"v[20:23],a[88:91],a[40:43],v[20:23]\n"
EX"v16,v16\n"
"v_add_f32 v235,1.0,v235\n"
MF"v[20:23],a[92:95],a[44:47],v[20:23]\n"
"v_rcp_f32 v235,v235\n"
PK"v1,v2,v3\n"
MF"v[28:31],a[92:95],a[60:63],v[28:31]\n"
EX"v17,v17\n"
PK"v235,v235,v235\n"
MF"v[28:31],a[88:91],a[56:59],v[28:31]\n"
RD"a[112:115],v226 offset:12288\n"
PK"v8,v8,v9\n"
MF"v[32:35],a[96:99],a[0:3],v[216:219]\n"
EX"v18,v18\n"
"v_lshlrev_b32 v235,16,v235\n"
MF"v[32:35],a[100:103],a[4:7],v[32:35]\n"
EX"v19,v19\n"
"v_add_f32 v240,v240,v248\n"
MF"v[40:43],a[100:103],a[20:23],0\n"
RD"a[116:119],v226 offset:13312\n"
"v_add_f32 v241,v241,v249\n"
MF"v[40:43],a[96:99],a[16:19],v[40:43]\n"
EX"v24,v24\n"
"v_add_f32 v242,v242,v250\n"
MF"v[48:51],a[96:99],a[32:35],0\n"
EX"v25,v25\n"
PK"v9,v10,v11\n"
MF"v[48:51],a[100:103],a[36:39],v[48:51]\n"
EX"v26,v26\n"
"v_add_f32 v243,v243,v251\n"
MF"v[56:59],a[100:103],a[52:55],0\n"
RD"a[120:123],v226 offset:14336\n"
PK"v16,v16,v17\n"
PK"v240,v240,v240\n"
MF"v[56:59],a[96:99],a[48:51],v[56:59]\n"
WT"lgkmcnt(2)\n"
EX"v27,v27\n"
"v_mov_b32 v227,v226\n"
MF"v[32:35],a[104:107],a[8:11],v[32:35]\n"
EX"v4,v4\n"
"v_lshlrev_b32 v240,16,v240\n"
MF"v[32:35],a[108:111],a[12:15],v[32:35]\n"
RD"a[124:127],v226 offset:15360\n"
MU"v240,v240,v235\n"
PK"v240,v240,v240\n"
MF"v[40:43],a[108:111],a[28:31],v[40:43]\n"
EX"v5,v5\n"
PK"v17,v18,v19\n"
MF"v[40:43],a[104:107],a[24:27],v[40:43]\n"
EX"v6,v6\n"
"v_lshlrev_b32 v240,16,v240\n"
MF"v[48:51],a[104:107],a[40:43],v[48:51]\n"
EX"v7,v7\n"
PK"v241,v241,v241\n"
MF"v[48:51],a[108:111],a[44:47],v[48:51]\n"
PK"v24,v24,v25\n"
"v_lshlrev_b32 v241,16,v241\n"
MF"v[56:59],a[108:111],a[60:63],v[56:59]\n"
EX"v12,v12\n"
MU"v241,v241,v235\n"
MF"v[56:59],a[104:107],a[56:59],v[56:59]\n"
PK"v241,v241,v241\n"
PK"v25,v26,v27\n"
MF"v[36:39],a[112:115],a[0:3],v[220:223]\n"
WT"lgkmcnt(0)\n"
EX"v13,v13\n"
"v_lshlrev_b32 v241,16,v241\n"
MF"v[36:39],a[116:119],a[4:7],v[36:39]\n"
RD"a[64:67],v227 offset:16384\n"
PK"v242,v242,v242\n"
MF"v[44:47],a[116:119],a[20:23],0\n"
EX"v14,v14\n"
MF"v[44:47],a[112:115],a[16:19],v[44:47]\n"
"v_lshlrev_b32 v242,16,v242\n"
PK"v2,v4,v5\n"
MF"v[52:55],a[112:115],a[32:35],0\n"
EX"v15,v15\n"
MU"v242,v242,v235\n"
MF"v[52:55],a[116:119],a[36:39],v[52:55]\n"
RD"a[68:71],v227 offset:17408\n"
PK"v242,v242,v242\n"
PK"v3,v6,v7\n"
MF"v[60:63],a[116:119],a[52:55],0\n"
EX"v20,v20\n"
"v_lshlrev_b32 v242,16,v242\n"
MF"v[60:63],a[112:115],a[48:51],v[60:63]\n"
"v_readlane_b32 s43,v228,s42\n"
PK"v243,v243,v243\n"
MF"v[36:39],a[120:123],a[8:11],v[36:39]\n"
EX"v21,v21\n"
MF"v[36:39],a[124:127],a[12:15],v[36:39]\n"
RD"a[72:75],v227 offset:18432\n"
"v_lshlrev_b32 v243,16,v243\n"
PK"v10,v12,v13\n"
MF"v[44:47],a[124:127],a[28:31],v[44:47]\n"
EX"v22,v22\n"
MU"v243,v243,v235\n"
MF"v[44:47],a[120:123],a[24:27],v[44:47]\n"
"s_mov_b32 m0,s40\n"
PK"v243,v243,v243\n"
"v_lshlrev_b32 v243,16,v243\n"
MF"v[52:55],a[120:123],a[40:43],v[52:55]\n"
EX"v23,v23\n"
MF"v[52:55],a[124:127],a[44:47],v[52:55]\n"
RD"a[76:79],v227 offset:19456\n"
"v_readlane_b32 s44,v229,s42\n"
PK"v11,v14,v15\n"
MF"v[60:63],a[124:127],a[60:63],v[60:63]\n"
EX"v28,v28\n"
MF"v[60:63],a[120:123],a[56:59],v[60:63]\n"
WT"lgkmcnt(2)\n"
BL"v225,s[8:11],s43 offen offset:0 sc1 lds\n"
MF"v[80:83],a[64:67],v[64:67],v[80:83]\n"
SA"s42,s42,1\n"
EX"v29,v29\n"
MF"v[112:115],a[64:67],v[68:71],v[112:115]\n"
SA"s41,s41,32768\n"
RD"a[80:83],v227 offset:20480\n"
PK"v18,v20,v21\n"
MF"v[144:147],a[64:67],v[72:75],v[144:147]\n"
"s_cmp_eq_u32 s41,163840\n"
EX"v30,v30\n"
MF"v[176:179],a[64:67],v[76:79],v[176:179]\n"
"s_cselect_b32 s41,0,s41\n"
WT"vmcnt(1)\n"
"v_lshlrev_b32 v244,16,v236\n"
"v_and_b32 v245,0xffff0000,v236\n"
MF"v[84:87],a[68:71],v[64:67],v[84:87]\n"
EX"v31,v31\n"
"v_lshlrev_b32 v246,16,v237\n"
MF"v[116:119],a[68:71],v[68:71],v[116:119]\n"
RD"a[84:87],v227 offset:21504\n"
"v_add_u32 v226,s41,v224\n"
"v_and_b32 v247,0xffff0000,v237\n"
MF"v[148:151],a[68:71],v[72:75],v[148:151]\n"
EX"v32,v32\n"
"v_add_f32 v240,v240,v244\n"
MF"v[180:183],a[68:71],v[76:79],v[180:183]\n"
WT"lgkmcnt(2)\n"
BL"v225,s[8:11],s43 offen offset:1024 sc1 lds\n"
"v_add_f32 v241,v241,v245\n"
MF"v[88:91],a[72:75],v[64:67],v[88:91]\n"
EX"v33,v33\n"
"v_add_f32 v242,v242,v246\n"
MF"v[120:123],a[72:75],v[68:71],v[120:123]\n"
RD"a[88:91],v227 offset:22528\n"
PK"v19,v22,v23\n"
"v_add_f32 v243,v243,v247\n"
MF"v[152:155],a[72:75],v[72:75],v[152:155]\n"
EX"v34,v34\n"
PK"v240,v240,v241\n"
MF"v[184:187],a[72:75],v[76:79],v[184:187]\n"
PK"v241,v242,v243\n"
PK"v26,v28,v29\n"
MF"v[92:95],a[76:79],v[64:67],v[92:95]\n"
"buffer_store_dwordx2 v[240:241],v239,s[8:11],s93 offen sc1\n"
MF"v[124:127],a[76:79],v[68:71],v[124:127]\n"
EX"v35,v35\n"
PK"v27,v30,v31\n"
MF"v[156:159],a[76:79],v[72:75],v[156:159]\n"
RD"a[92:95],v227 offset:23552\n"
MF"v[188:191],a[76:79],v[76:79],v[188:191]\n"
WT"lgkmcnt(2)\n"
EX"v40,v40\n"
MF"v[96:99],a[80:83],v[64:67],v[96:99]\n"
BL"v225,s[8:11],s43 offen offset:2048 sc1 lds\n"
MF"v[128:131],a[80:83],v[68:71],v[128:131]\n"
EX"v41,v41\n"
MF"v[160:163],a[80:83],v[72:75],v[160:163]\n"
RD"a[96:99],v227 offset:24576\n"
MF"v[192:195],a[80:83],v[76:79],v[192:195]\n"
EX"v42,v42\n"
MF"v[100:103],a[84:87],v[64:67],v[100:103]\n"
EX"v43,v43\n"
MF"v[132:135],a[84:87],v[68:71],v[132:135]\n"
RD"a[100:103],v227 offset:25600\n"
MF"v[164:167],a[84:87],v[72:75],v[164:167]\n"
EX"v48,v48\n"
MF"v[196:199],a[84:87],v[76:79],v[196:199]\n"
WT"lgkmcnt(2)\n"
BL"v225,s[8:11],s43 offen offset:3072 sc1 lds\n"
MF"v[104:107],a[88:91],v[64:67],v[104:107]\n"
EX"v49,v49\n"
MF"v[136:139],a[88:91],v[68:71],v[136:139]\n"
RD"a[104:107],v227 offset:26624\n"
MF"v[168:171],a[88:91],v[72:75],v[168:171]\n"
EX"v50,v50\n"
MF"v[200:203],a[88:91],v[76:79],v[200:203]\n"
SA"m0,s40,16384\n"
MF"v[108:111],a[92:95],v[64:67],v[108:111]\n"
EX"v51,v51\n"
MF"v[140:143],a[92:95],v[68:71],v[140:143]\n"
RD"a[108:111],v227 offset:27648\n"
MF"v[172:175],a[92:95],v[72:75],v[172:175]\n"
EX"v56,v56\n"
MF"v[204:207],a[92:95],v[76:79],v[204:207]\n"
BL"v225,s[8:11],s44 offen offset:0 sc1 lds\n"
MF"a[132:135],a[128:131],v[64:67],a[132:135]\n"
SA"s40,s40,32768\n"
EX"v57,v57\n"
MF"a[136:139],a[128:131],v[68:71],a[136:139]\n"
"s_cmp_eq_u32 s40,s46\n"
RD"a[112:115],v227 offset:28672\n"
"s_cselect_b32 s40,s47,s40\n"
MF"a[140:143],a[128:131],v[72:75],a[140:143]\n"
EX"v58,v58\n"
MF"a[144:147],a[128:131],v[76:79],a[144:147]\n"
WT"lgkmcnt(2)\n"
MF"v[80:83],a[96:99],v[0:3],v[80:83]\n"
EX"v59,v59\n"
MF"v[112:115],a[96:99],v[8:11],v[112:115]\n"
RD"a[116:119],v227 offset:29696\n"
MF"v[144:147],a[96:99],v[16:19],v[144:147]\n"
EX"v36,v36\n"
MF"v[176:179],a[96:99],v[24:27],v[176:179]\n"
BL"v225,s[8:11],s44 offen offset:1024 sc1 lds\n"
MF"v[84:87],a[100:103],v[0:3],v[84:87]\n"
EX"v37,v37\n"
MF"v[116:119],a[100:103],v[8:11],v[116:119]\n"
RD"a[120:123],v227 offset:30720\n"
MF"v[148:151],a[100:103],v[16:19],v[148:151]\n"
EX"v38,v38\n"
MF"v[180:183],a[100:103],v[24:27],v[180:183]\n"
MF"v[88:91],a[104:107],v[0:3],v[88:91]\n"
EX"v39,v39\n"
MF"v[120:123],a[104:107],v[8:11],v[120:123]\n"
RD"a[124:127],v227 offset:31744\n"
MF"v[152:155],a[104:107],v[16:19],v[152:155]\n"
EX"v44,v44\n"
MF"v[184:187],a[104:107],v[24:27],v[184:187]\n"
WT"lgkmcnt(2)\n"
BL"v225,s[8:11],s44 offen offset:2048 sc1 lds\n"
MF"v[92:95],a[108:111],v[0:3],v[92:95]\n"
EX"v45,v45\n"
MF"v[124:127],a[108:111],v[8:11],v[124:127]\n"
MF"v[156:159],a[108:111],v[16:19],v[156:159]\n"
EX"v46,v46\n"
MF"v[188:191],a[108:111],v[24:27],v[188:191]\n"
WT"lgkmcnt(0)\n"
MF"v[96:99],a[112:115],v[0:3],v[96:99]\n"
"s_barrier\n"
MF"v[128:131],a[112:115],v[8:11],v[128:131]\n"
RD"a[64:67],v226 offset:0\n"
MF"v[160:163],a[112:115],v[16:19],v[160:163]\n"
EX"v47,v47\n"
MF"v[192:195],a[112:115],v[24:27],v[192:195]\n"
BL"v225,s[8:11],s44 offen offset:3072 sc1 lds\n"
MF"v[100:103],a[116:119],v[0:3],v[100:103]\n"
EX"v52,v52\n"
MF"v[132:135],a[116:119],v[8:11],v[132:135]\n"
RD"a[68:71],v226 offset:1024\n"
MF"v[164:167],a[116:119],v[16:19],v[164:167]\n"
EX"v53,v53\n"
MF"v[196:199],a[116:119],v[24:27],v[196:199]\n"
RD"a[72:75],v226 offset:2048\n"
MF"v[104:107],a[120:123],v[0:3],v[104:107]\n"
EX"v54,v54\n"
MF"v[136:139],a[120:123],v[8:11],v[136:139]\n"
RD"a[76:79],v226 offset:3072\n"
MF"v[168:171],a[120:123],v[16:19],v[168:171]\n"
EX"v55,v55\n"
MF"v[200:203],a[120:123],v[24:27],v[200:203]\n"
MF"v[108:111],a[124:127],v[0:3],v[108:111]\n"
EX"v60,v60\n"
MF"v[140:143],a[124:127],v[8:11],v[140:143]\n"
RD"a[80:83],v226 offset:4096\n"
MF"v[172:175],a[124:127],v[16:19],v[172:175]\n"
EX"v61,v61\n"
MF"v[204:207],a[124:127],v[24:27],v[204:207]\n"
WT"vmcnt(6)\n"
"buffer_store_dwordx2 v[232:233],v230,s[8:11],s94 offen sc1\n"
MF"a[132:135],a[128:131],v[0:3],a[132:135]\n"
RD"a[84:87],v226 offset:5120\n"
MF"a[136:139],a[128:131],v[8:11],a[136:139]\n"
EX"v62,v62\n"
MF"a[140:143],a[128:131],v[16:19],a[140:143]\n"
WT"lgkmcnt(4)\n"
RD"a[88:91],v226 offset:6144\n"
MF"a[144:147],a[128:131],v[24:27],a[144:147]\n"
EX"v63,v63\n"
"s_branch LDB_%=\n"
".p2align 6\n"
"LME_%=:\n"
MF"v[0:3],a[64:67],a[0:3],0\n"
"v_lshrrev_b32 v239,9,v224\n"
"v_lshlrev_b32 v239,8,v239\n"
MF"v[0:3],a[68:71],a[4:7],v[0:3]\n"
"v_bfe_u32 v237,v224,8,1\n"
"v_lshl_add_u32 v239,v237,3,v239\n"
MF"v[8:11],a[68:71],a[20:23],0\n"
"v_and_b32 v237,0x80,v224\n"
PK"v64,v32,v33\n"
MF"v[8:11],a[64:67],a[16:19],v[8:11]\n"
"v_lshl_add_u32 v239,v237,23,v239\n"
"v_and_b32 v237,0x70,v224\n"
MF"v[16:19],a[64:67],a[32:35],0\n"
"s_mul_i32 s53,s35,20480\n"
"v_add_u32 v239,v239,v237\n"
PK"v65,v34,v35\n"
MF"v[16:19],a[68:71],a[36:39],v[16:19]\n"
SA"s53,s53,s93\n"
RD"a[92:95],v226 offset:7168\n"
PK"v66,v36,v37\n"
MF"v[24:27],a[68:71],a[52:55],0\n"
SA"s53,s53,3074048\n"
PK"v67,v38,v39\n"
PK"v68,v40,v41\n"
MF"v[24:27],a[64:67],a[48:51],v[24:27]\n"
WT"lgkmcnt(1)\n"
"buffer_load_dwordx2 v[236:237],v239,s[8:11],s53 offen sc1\n"
MF"v[0:3],a[72:75],a[8:11],v[0:3]\n"
PK"v69,v42,v43\n"
PK"v70,v44,v45\n"
MF"v[0:3],a[76:79],a[12:15],v[0:3]\n"
PK"v71,v46,v47\n"
PK"v72,v48,v49\n"
MF"v[8:11],a[76:79],a[28:31],v[8:11]\n"
PK"v73,v50,v51\n"
PK"v74,v52,v53\n"
MF"v[8:11],a[72:75],a[24:27],v[8:11]\n"
PK"v75,v54,v55\n"
"v_add_f32 v240,v240,v244\n"
MF"v[16:19],a[72:75],a[40:43],v[16:19]\n"
"v_add_f32 v248,v248,v252\n"
PK"v76,v56,v57\n"
MF"v[16:19],a[76:79],a[44:47],v[16:19]\n"
RD"a[96:99],v226 offset:8192\n"
PK"v77,v58,v59\n"
MF"v[24:27],a[76:79],a[60:63],v[24:27]\n"
EX"v0,v0\n"
MF"v[24:27],a[72:75],a[56:59],v[24:27]\n"
"v_add_f32 v241,v241,v245\n"
PK"v78,v60,v61\n"
PK"v79,v62,v63\n"
MF"v[4:7],a[80:83],a[0:3],0\n"
EX"v1,v1\n"
"v_add_f32 v249,v249,v253\n"
MF"v[4:7],a[84:87],a[4:7],v[4:7]\n"
RD"a[100:103],v226 offset:9216\n"
"v_add_f32 v242,v242,v246\n"
MF"v[12:15],a[84:87],a[20:23],0\n"
EX"v2,v2\n"
"v_add_f32 v250,v250,v254\n"
MF"v[12:15],a[80:83],a[16:19],v[12:15]\n"
EX"v3,v3\n"
"v_add_f32 v243,v243,v247\n"
MF"v[20:23],a[80:83],a[32:35],0\n"
EX"v8,v8\n"
"v_add_f32 v251,v251,v255\n"
MF"v[20:23],a[84:87],a[36:39],v[20:23]\n"
RD"a[104:107],v226 offset:10240\n"
"v_ashrrev_i32 v235,31,v234\n"
"v_not_b32 v235,v235\n"
MF"v[28:31],a[84:87],a[52:55],0\n"
EX"v9,v9\n"
"v_or_b32 v235,0x80000000,v235\n"
MF"v[28:31],a[80:83],a[48:51],v[28:31]\n"
"v_xor_b32 v235,v235,v234\n"
"v_and_b32 v235,0xffff0000,v235\n"
MF"v[4:7],a[88:91],a[8:11],v[4:7]\n"
WT"lgkmcnt(1)\n"
EX"v10,v10\n"
MF"v[4:7],a[92:95],a[12:15],v[4:7]\n"
RD"a[108:111],v226 offset:11264\n"
PK"v0,v0,v1\n"
MF"v[12:15],a[92:95],a[28:31],v[12:15]\n"
EX"v11,v11\n"
MU"v235,0xbfb8aa3b,v235\n"
MF"v[12:15],a[88:91],a[24:27],v[12:15]\n"
EX"v235,v235\n"
MF"v[20:23],a[88:91],a[40:43],v[20:23]\n"
EX"v16,v16\n"
"v_add_f32 v235,1.0,v235\n"
MF"v[20:23],a[92:95],a[44:47],v[20:23]\n"
"v_rcp_f32 v235,v235\n"
PK"v1,v2,v3\n"
MF"v[28:31],a[92:95],a[60:63],v[28:31]\n"
EX"v17,v17\n"
PK"v235,v235,v235\n"
MF"v[28:31],a[88:91],a[56:59],v[28:31]\n"
RD"a[112:115],v226 offset:12288\n"
PK"v8,v8,v9\n"
MF"v[32:35],a[96:99],a[0:3],0\n"
EX"v18,v18\n"
"v_lshlrev_b32 v235,16,v235\n"
MF"v[32:35],a[100:103],a[4:7],v[32:35]\n"
EX"v19,v19\n"
"v_add_f32 v240,v240,v248\n"
MF"v[40:43],a[100:103],a[20:23],0\n"
RD"a[116:119],v226 offset:13312\n"
"v_add_f32 v241,v241,v249\n"
MF"v[40:43],a[96:99],a[16:19],v[40:43]\n"
EX"v24,v24\n"
"v_add_f32 v242,v242,v250\n"
MF"v[48:51],a[96:99],a[32:35],0\n"
EX"v25,v25\n"
PK"v9,v10,v11\n"
MF"v[48:51],a[100:103],a[36:39],v[48:51]\n"
EX"v26,v26\n"
"v_add_f32 v243,v243,v251\n"
MF"v[56:59],a[100:103],a[52:55],0\n"
RD"a[120:123],v226 offset:14336\n"
PK"v16,v16,v17\n"
PK"v240,v240,v240\n"
MF"v[56:59],a[96:99],a[48:51],v[56:59]\n"
WT"lgkmcnt(2)\n"
EX"v27,v27\n"
"v_mov_b32 v227,v226\n"
MF"v[32:35],a[104:107],a[8:11],v[32:35]\n"
EX"v4,v4\n"
"v_lshlrev_b32 v240,16,v240\n"
MF"v[32:35],a[108:111],a[12:15],v[32:35]\n"
RD"a[124:127],v226 offset:15360\n"
MU"v240,v240,v235\n"
PK"v240,v240,v240\n"
MF"v[40:43],a[108:111],a[28:31],v[40:43]\n"
EX"v5,v5\n"
PK"v17,v18,v19\n"
MF"v[40:43],a[104:107],a[24:27],v[40:43]\n"
EX"v6,v6\n"
"v_lshlrev_b32 v240,16,v240\n"
MF"v[48:51],a[104:107],a[40:43],v[48:51]\n"
EX"v7,v7\n"
PK"v241,v241,v241\n"
MF"v[48:51],a[108:111],a[44:47],v[48:51]\n"
PK"v24,v24,v25\n"
"v_lshlrev_b32 v241,16,v241\n"
MF"v[56:59],a[108:111],a[60:63],v[56:59]\n"
EX"v12,v12\n"
MU"v241,v241,v235\n"
MF"v[56:59],a[104:107],a[56:59],v[56:59]\n"
PK"v241,v241,v241\n"
PK"v25,v26,v27\n"
MF"v[36:39],a[112:115],a[0:3],0\n"
WT"lgkmcnt(0)\n"
EX"v13,v13\n"
"v_lshlrev_b32 v241,16,v241\n"
MF"v[36:39],a[116:119],a[4:7],v[36:39]\n"
RD"a[64:67],v227 offset:16384\n"
PK"v242,v242,v242\n"
MF"v[44:47],a[116:119],a[20:23],0\n"
EX"v14,v14\n"
MF"v[44:47],a[112:115],a[16:19],v[44:47]\n"
"v_lshlrev_b32 v242,16,v242\n"
PK"v2,v4,v5\n"
MF"v[52:55],a[112:115],a[32:35],0\n"
EX"v15,v15\n"
MU"v242,v242,v235\n"
MF"v[52:55],a[116:119],a[36:39],v[52:55]\n"
RD"a[68:71],v227 offset:17408\n"
PK"v242,v242,v242\n"
PK"v3,v6,v7\n"
MF"v[60:63],a[116:119],a[52:55],0\n"
EX"v20,v20\n"
"v_lshlrev_b32 v242,16,v242\n"
MF"v[60:63],a[112:115],a[48:51],v[60:63]\n"
"v_readlane_b32 s43,v228,s42\n"
PK"v243,v243,v243\n"
MF"v[36:39],a[120:123],a[8:11],v[36:39]\n"
EX"v21,v21\n"
MF"v[36:39],a[124:127],a[12:15],v[36:39]\n"
RD"a[72:75],v227 offset:18432\n"
"v_lshlrev_b32 v243,16,v243\n"
PK"v10,v12,v13\n"
MF"v[44:47],a[124:127],a[28:31],v[44:47]\n"
EX"v22,v22\n"
MU"v243,v243,v235\n"
MF"v[44:47],a[120:123],a[24:27],v[44:47]\n"
"s_mov_b32 m0,s40\n"
PK"v243,v243,v243\n"
"v_lshlrev_b32 v243,16,v243\n"
MF"v[52:55],a[120:123],a[40:43],v[52:55]\n"
EX"v23,v23\n"
MF"v[52:55],a[124:127],a[44:47],v[52:55]\n"
RD"a[76:79],v227 offset:19456\n"
"v_readlane_b32 s44,v229,s42\n"
PK"v11,v14,v15\n"
MF"v[60:63],a[124:127],a[60:63],v[60:63]\n"
EX"v28,v28\n"
MF"v[60:63],a[120:123],a[56:59],v[60:63]\n"
WT"lgkmcnt(2)\n"
BL"v225,s[8:11],s43 offen offset:0 sc1 lds\n"
MF"v[80:83],a[64:67],v[64:67],v[80:83]\n"
SA"s42,s42,1\n"
EX"v29,v29\n"
MF"v[112:115],a[64:67],v[68:71],v[112:115]\n"
SA"s41,s41,32768\n"
RD"a[80:83],v227 offset:20480\n"
PK"v18,v20,v21\n"
MF"v[144:147],a[64:67],v[72:75],v[144:147]\n"
"s_cmp_eq_u32 s41,163840\n"
EX"v30,v30\n"
MF"v[176:179],a[64:67],v[76:79],v[176:179]\n"
"s_cselect_b32 s41,0,s41\n"
WT"vmcnt(1)\n"
"v_lshlrev_b32 v244,16,v236\n"
"v_and_b32 v245,0xffff0000,v236\n"
MF"v[84:87],a[68:71],v[64:67],v[84:87]\n"
EX"v31,v31\n"
"v_lshlrev_b32 v246,16,v237\n"
MF"v[116:119],a[68:71],v[68:71],v[116:119]\n"
RD"a[84:87],v227 offset:21504\n"
"v_add_u32 v226,s41,v224\n"
"v_and_b32 v247,0xffff0000,v237\n"
MF"v[148:151],a[68:71],v[72:75],v[148:151]\n"
EX"v32,v32\n"
"v_add_f32 v240,v240,v244\n"
MF"v[180:183],a[68:71],v[76:79],v[180:183]\n"
WT"lgkmcnt(2)\n"
BL"v225,s[8:11],s43 offen offset:1024 sc1 lds\n"
"v_add_f32 v241,v241,v245\n"
MF"v[88:91],a[72:75],v[64:67],v[88:91]\n"
EX"v33,v33\n"
"v_add_f32 v242,v242,v246\n"
MF"v[120:123],a[72:75],v[68:71],v[120:123]\n"
RD"a[88:91],v227 offset:22528\n"
PK"v19,v22,v23\n"
"v_add_f32 v243,v243,v247\n"
MF"v[152:155],a[72:75],v[72:75],v[152:155]\n"
EX"v34,v34\n"
PK"v240,v240,v241\n"
MF"v[184:187],a[72:75],v[76:79],v[184:187]\n"
PK"v241,v242,v243\n"
PK"v26,v28,v29\n"
MF"v[92:95],a[76:79],v[64:67],v[92:95]\n"
"buffer_store_dwordx2 v[240:241],v239,s[8:11],s93 offen sc1\n"
MF"v[124:127],a[76:79],v[68:71],v[124:127]\n"
EX"v35,v35\n"
PK"v27,v30,v31\n"
MF"v[156:159],a[76:79],v[72:75],v[156:159]\n"
RD"a[92:95],v227 offset:23552\n"
MF"v[188:191],a[76:79],v[76:79],v[188:191]\n"
WT"lgkmcnt(2)\n"
EX"v40,v40\n"
MF"v[96:99],a[80:83],v[64:67],v[96:99]\n"
BL"v225,s[8:11],s43 offen offset:2048 sc1 lds\n"
MF"v[128:131],a[80:83],v[68:71],v[128:131]\n"
EX"v41,v41\n"
MF"v[160:163],a[80:83],v[72:75],v[160:163]\n"
RD"a[96:99],v227 offset:24576\n"
MF"v[192:195],a[80:83],v[76:79],v[192:195]\n"
EX"v42,v42\n"
MF"v[100:103],a[84:87],v[64:67],v[100:103]\n"
EX"v43,v43\n"
MF"v[132:135],a[84:87],v[68:71],v[132:135]\n"
RD"a[100:103],v227 offset:25600\n"
MF"v[164:167],a[84:87],v[72:75],v[164:167]\n"
EX"v48,v48\n"
MF"v[196:199],a[84:87],v[76:79],v[196:199]\n"
WT"lgkmcnt(2)\n"
BL"v225,s[8:11],s43 offen offset:3072 sc1 lds\n"
MF"v[104:107],a[88:91],v[64:67],v[104:107]\n"
EX"v49,v49\n"
MF"v[136:139],a[88:91],v[68:71],v[136:139]\n"
RD"a[104:107],v227 offset:26624\n"
MF"v[168:171],a[88:91],v[72:75],v[168:171]\n"
EX"v50,v50\n"
MF"v[200:203],a[88:91],v[76:79],v[200:203]\n"
SA"m0,s40,16384\n"
MF"v[108:111],a[92:95],v[64:67],v[108:111]\n"
EX"v51,v51\n"
MF"v[140:143],a[92:95],v[68:71],v[140:143]\n"
RD"a[108:111],v227 offset:27648\n"
MF"v[172:175],a[92:95],v[72:75],v[172:175]\n"
EX"v56,v56\n"
MF"v[204:207],a[92:95],v[76:79],v[204:207]\n"
BL"v225,s[8:11],s44 offen offset:0 sc1 lds\n"
MF"a[132:135],a[128:131],v[64:67],a[132:135]\n"
SA"s40,s40,32768\n"
EX"v57,v57\n"
MF"a[136:139],a[128:131],v[68:71],a[136:139]\n"
"s_cmp_eq_u32 s40,s46\n"
RD"a[112:115],v227 offset:28672\n"
"s_cselect_b32 s40,s47,s40\n"
MF"a[140:143],a[128:131],v[72:75],a[140:143]\n"
EX"v58,v58\n"
MF"a[144:147],a[128:131],v[76:79],a[144:147]\n"
WT"lgkmcnt(2)\n"
MF"v[80:83],a[96:99],v[0:3],v[80:83]\n"
EX"v59,v59\n"
MF"v[112:115],a[96:99],v[8:11],v[112:115]\n"
RD"a[116:119],v227 offset:29696\n"
MF"v[144:147],a[96:99],v[16:19],v[144:147]\n"
EX"v36,v36\n"
MF"v[176:179],a[96:99],v[24:27],v[176:179]\n"
BL"v225,s[8:11],s44 offen offset:1024 sc1 lds\n"
MF"v[84:87],a[100:103],v[0:3],v[84:87]\n"
EX"v37,v37\n"
MF"v[116:119],a[100:103],v[8:11],v[116:119]\n"
RD"a[120:123],v227 offset:30720\n"
MF"v[148:151],a[100:103],v[16:19],v[148:151]\n"
EX"v38,v38\n"
MF"v[180:183],a[100:103],v[24:27],v[180:183]\n"
MF"v[88:91],a[104:107],v[0:3],v[88:91]\n"
EX"v39,v39\n"
MF"v[120:123],a[104:107],v[8:11],v[120:123]\n"
RD"a[124:127],v227 offset:31744\n"
MF"v[152:155],a[104:107],v[16:19],v[152:155]\n"
EX"v44,v44\n"
MF"v[184:187],a[104:107],v[24:27],v[184:187]\n"
WT"lgkmcnt(2)\n"
BL"v225,s[8:11],s44 offen offset:2048 sc1 lds\n"
MF"v[92:95],a[108:111],v[0:3],v[92:95]\n"
EX"v45,v45\n"
MF"v[124:127],a[108:111],v[8:11],v[124:127]\n"
MF"v[156:159],a[108:111],v[16:19],v[156:159]\n"
EX"v46,v46\n"
MF"v[188:191],a[108:111],v[24:27],v[188:191]\n"
WT"lgkmcnt(0)\n"
MF"v[96:99],a[112:115],v[0:3],v[96:99]\n"
"s_barrier\n"
MF"v[128:131],a[112:115],v[8:11],v[128:131]\n"
RD"a[64:67],v226 offset:0\n"
MF"v[160:163],a[112:115],v[16:19],v[160:163]\n"
EX"v47,v47\n"
MF"v[192:195],a[112:115],v[24:27],v[192:195]\n"
BL"v225,s[8:11],s44 offen offset:3072 sc1 lds\n"
MF"v[100:103],a[116:119],v[0:3],v[100:103]\n"
EX"v52,v52\n"
MF"v[132:135],a[116:119],v[8:11],v[132:135]\n"
RD"a[68:71],v226 offset:1024\n"
MF"v[164:167],a[116:119],v[16:19],v[164:167]\n"
EX"v53,v53\n"
MF"v[196:199],a[116:119],v[24:27],v[196:199]\n"
RD"a[72:75],v226 offset:2048\n"
MF"v[104:107],a[120:123],v[0:3],v[104:107]\n"
EX"v54,v54\n"
MF"v[136:139],a[120:123],v[8:11],v[136:139]\n"
RD"a[76:79],v226 offset:3072\n"
MF"v[168:171],a[120:123],v[16:19],v[168:171]\n"
EX"v55,v55\n"
MF"v[200:203],a[120:123],v[24:27],v[200:203]\n"
MF"v[108:111],a[124:127],v[0:3],v[108:111]\n"
EX"v60,v60\n"
MF"v[140:143],a[124:127],v[8:11],v[140:143]\n"
RD"a[80:83],v226 offset:4096\n"
MF"v[172:175],a[124:127],v[16:19],v[172:175]\n"
EX"v61,v61\n"
MF"v[204:207],a[124:127],v[24:27],v[204:207]\n"
WT"vmcnt(6)\n"
"buffer_store_dwordx2 v[232:233],v230,s[8:11],s94 offen sc1\n"
MF"a[132:135],a[128:131],v[0:3],a[132:135]\n"
RD"a[84:87],v226 offset:5120\n"
MF"a[136:139],a[128:131],v[8:11],a[136:139]\n"
EX"v62,v62\n"
MF"a[140:143],a[128:131],v[16:19],a[140:143]\n"
WT"lgkmcnt(4)\n"
RD"a[88:91],v226 offset:6144\n"
MF"a[144:147],a[128:131],v[24:27],a[144:147]\n"
EX"v63,v63\n"
"s_mov_b32 s57,0\n"
"s_lshl_b32 s56,s34,1\n"
"s_sub_u32 s56,s56,12\n"
"s_branch LP1_%=\n"
".p2align 6\n"
"LRT_%=:\n"
MF"v[0:3],a[64:67],a[0:3],0\n"
"s_mov_b32 s12,s74\n"
"v_lshrrev_b32 v242,1,v224\n"
PK"v64,v32,v33\n"
MF"v[0:3],a[68:71],a[4:7],v[0:3]\n"
"s_and_b32 s13,s75,0xffff\n"
PK"v65,v34,v35\n"
"v_and_b32 v242,56,v242\n"
MF"v[8:11],a[68:71],a[20:23],0\n"
"s_mov_b32 s14,5242880\n"
PK"v66,v36,v37\n"
PK"v67,v38,v39\n"
MF"v[8:11],a[64:67],a[16:19],v[8:11]\n"
"s_lshl_b32 s23,s35,6\n"
PK"v68,v40,v41\n"
PK"v69,v42,v43\n"
MF"v[16:19],a[64:67],a[32:35],0\n"
"s_mov_b32 s15,0x20000\n"
PK"v70,v44,v45\n"
PK"v71,v46,v47\n"
MF"v[16:19],a[68:71],a[36:39],v[16:19]\n"
SA"s23,s23,4352\n"
RD"a[92:95],v226 offset:7168\n"
"v_add_u32 v243,s40,v252\n"
MF"v[24:27],a[68:71],a[52:55],0\n"
SA"s19,s40,32768\n"
PK"v72,v48,v49\n"
PK"v73,v50,v51\n"
MF"v[24:27],a[64:67],a[48:51],v[24:27]\n"
WT"lgkmcnt(1)\n"
"buffer_load_dwordx2 v[244:245],v242,s[8:11],s23 offen sc1\n"
MF"v[0:3],a[72:75],a[8:11],v[0:3]\n"
"s_cmp_eq_u32 s19,s46\n"
PK"v74,v52,v53\n"
PK"v75,v54,v55\n"
MF"v[0:3],a[76:79],a[12:15],v[0:3]\n"
"s_cselect_b32 s19,s47,s19\n"
PK"v76,v56,v57\n"
PK"v77,v58,v59\n"
MF"v[8:11],a[76:79],a[28:31],v[8:11]\n"
"s_cmp_eq_u32 s20,0\n"
PK"v78,v60,v61\n"
PK"v79,v62,v63\n"
MF"v[8:11],a[72:75],a[24:27],v[8:11]\n"
"s_cselect_b32 s17,2130706432,s17\n"
SA"m0,s19,0\n"
MF"v[16:19],a[72:75],a[40:43],v[16:19]\n"
BL"v240,s[12:15],s17 offen offset:0 lds\n"
MF"v[16:19],a[76:79],a[44:47],v[16:19]\n"
RD"a[96:99],v226 offset:8192\n"
MF"v[24:27],a[76:79],a[60:63],v[24:27]\n"
EX"v0,v0\n"
MF"v[24:27],a[72:75],a[56:59],v[24:27]\n"
BL"v241,s[12:15],s17 offen offset:1024 lds\n"
MF"v[4:7],a[80:83],a[0:3],0\n"
EX"v1,v1\n"
MF"v[4:7],a[84:87],a[4:7],v[4:7]\n"
RD"a[100:103],v226 offset:9216\n"
PK"v0,v0,v1\n"
MF"v[12:15],a[84:87],a[20:23],0\n"
EX"v2,v2\n"
MF"v[12:15],a[80:83],a[16:19],v[12:15]\n"
BL"v251,s[12:15],s17 offen offset:2048 lds\n"
MF"v[20:23],a[80:83],a[32:35],0\n"
SA"m0,s19,16384\n"
EX"v3,v3\n"
MF"v[20:23],a[84:87],a[36:39],v[20:23]\n"
SA"s17,s17,20480\n"
EX"v8,v8\n"
MF"v[28:31],a[84:87],a[52:55],0\n"
RD"a[104:107],v226 offset:10240\n"
PK"v1,v2,v3\n"
MF"v[28:31],a[80:83],a[48:51],v[28:31]\n"
EX"v9,v9\n"
MF"v[4:7],a[88:91],a[8:11],v[4:7]\n"
WT"lgkmcnt(1)\n"
BL"v240,s[12:15],s17 offen offset:0 lds\n"
MF"v[4:7],a[92:95],a[12:15],v[4:7]\n"
EX"v10,v10\n"
MF"v[12:15],a[92:95],a[28:31],v[12:15]\n"
RD"a[108:111],v226 offset:11264\n"
PK"v8,v8,v9\n"
MF"v[12:15],a[88:91],a[24:27],v[12:15]\n"
EX"v11,v11\n"
MF"v[20:23],a[88:91],a[40:43],v[20:23]\n"
WT"vmcnt(13)\n"
RT"a[148:149],v243 offset:0\n"
RT"a[150:151],v243 offset:16384\n"
MF"v[20:23],a[92:95],a[44:47],v[20:23]\n"
EX"v16,v16\n"
MF"v[28:31],a[92:95],a[60:63],v[28:31]\n"
EX"v17,v17\n"
MF"v[28:31],a[88:91],a[56:59],v[28:31]\n"
RD"a[112:115],v226 offset:12288\n"
PK"v9,v10,v11\n"
MF"v[32:35],a[96:99],a[0:3],0\n"
EX"v18,v18\n"
MF"v[32:35],a[100:103],a[4:7],v[32:35]\n"
BL"v241,s[12:15],s17 offen offset:1024 lds\n"
MF"v[40:43],a[100:103],a[20:23],0\n"
EX"v19,v19\n"
MF"v[40:43],a[96:99],a[16:19],v[40:43]\n"
RD"a[116:119],v226 offset:13312\n"
RT"a[152:153],v243 offset:32\n"
MF"v[48:51],a[96:99],a[32:35],0\n"
EX"v24,v24\n"
MF"v[48:51],a[100:103],a[36:39],v[48:51]\n"
RT"a[154:155],v243 offset:16416\n"
RD"a[120:123],v226 offset:14336\n"
EX"v25,v25\n"
MF"v[56:59],a[100:103],a[52:55],0\n"
EX"v26,v26\n"
PK"v16,v16,v17\n"
MF"v[56:59],a[96:99],a[48:51],v[56:59]\n"
WT"lgkmcnt(4)\n"
EX"v27,v27\n"
"v_mov_b32 v227,v226\n"
MF"v[32:35],a[104:107],a[8:11],v[32:35]\n"
BL"v251,s[12:15],s17 offen offset:2048 lds\n"
MF"v[32:35],a[108:111],a[12:15],v[32:35]\n"
EX"v4,v4\n"
PK"v17,v18,v19\n"
MF"v[40:43],a[108:111],a[28:31],v[40:43]\n"
RD"a[124:127],v226 offset:15360\n"
PK"v24,v24,v25\n"
MF"v[40:43],a[104:107],a[24:27],v[40:43]\n"
EX"v5,v5\n"
MF"v[48:51],a[104:107],a[40:43],v[48:51]\n"
RT"a[156:157],v243 offset:64\n"
RT"a[158:159],v243 offset:16448\n"
MF"v[48:51],a[108:111],a[44:47],v[48:51]\n"
EX"v6,v6\n"
MF"v[56:59],a[108:111],a[60:63],v[56:59]\n"
EX"v7,v7\n"
MF"v[56:59],a[104:107],a[56:59],v[56:59]\n"
EX"v12,v12\n"
PK"v25,v26,v27\n"
MF"v[36:39],a[112:115],a[0:3],0\n"
WT"lgkmcnt(3)\n"
EX"v13,v13\n"
MF"v[36:39],a[116:119],a[4:7],v[36:39]\n"
RD"a[64:67],v227 offset:16384\n"
PK"v2,v4,v5\n"
MF"v[44:47],a[116:119],a[20:23],0\n"
EX"v14,v14\n"
MF"v[44:47],a[112:115],a[16:19],v[44:47]\n"
RT"a[160:161],v243 offset:96\n"
RT"a[162:163],v243 offset:16480\n"
MF"v[52:55],a[112:115],a[32:35],0\n"
EX"v15,v15\n"
MF"v[52:55],a[116:119],a[36:39],v[52:55]\n"
RD"a[68:71],v227 offset:17408\n"
PK"v3,v6,v7\n"
MF"v[60:63],a[116:119],a[52:55],0\n"
EX"v20,v20\n"
MF"v[60:63],a[112:115],a[48:51],v[60:63]\n"
"v_readlane_b32 s43,v228,s42\n"
PK"v10,v12,v13\n"
MF"v[36:39],a[120:123],a[8:11],v[36:39]\n"
WT"lgkmcnt(3)\n"
EX"v21,v21\n"
MF"v[36:39],a[124:127],a[12:15],v[36:39]\n"
RD"a[72:75],v227 offset:18432\n"
PK"v11,v14,v15\n"
MF"v[44:47],a[124:127],a[28:31],v[44:47]\n"
EX"v22,v22\n"
MF"v[44:47],a[120:123],a[24:27],v[44:47]\n"
"s_mov_b32 m0,s40\n"
RT"a[164:165],v243 offset:128\n"
RT"a[166:167],v243 offset:16512\n"
MF"v[52:55],a[120:123],a[40:43],v[52:55]\n"
SA"s17,s17,61440\n"
EX"v23,v23\n"
MF"v[52:55],a[124:127],a[44:47],v[52:55]\n"
RD"a[76:79],v227 offset:19456\n"
"v_readlane_b32 s44,v229,s42\n"
MF"v[60:63],a[124:127],a[60:63],v[60:63]\n"
EX"v28,v28\n"
MF"v[60:63],a[120:123],a[56:59],v[60:63]\n"
WT"lgkmcnt(1)\n"
BL"v225,s[8:11],s43 offen offset:0 sc1 lds\n"
MF"v[80:83],a[64:67],v[64:67],v[80:83]\n"
SA"s42,s42,1\n"
EX"v29,v29\n"
MF"v[112:115],a[64:67],v[68:71],v[112:115]\n"
SA"s41,s41,32768\n"
RD"a[80:83],v227 offset:20480\n"
PK"v18,v20,v21\n"
MF"v[144:147],a[64:67],v[72:75],v[144:147]\n"
"s_cmp_eq_u32 s41,163840\n"
EX"v30,v30\n"
MF"v[176:179],a[64:67],v[76:79],v[176:179]\n"
"s_cselect_b32 s41,0,s41\n"
BS"a[148:151],v224,s[8:11],s18 offen offset:0 sc1\n"
MF"v[84:87],a[68:71],v[64:67],v[84:87]\n"
EX"v31,v31\n"
MF"v[116:119],a[68:71],v[68:71],v[116:119]\n"
RD"a[84:87],v227 offset:21504\n"
"v_add_u32 v226,s41,v224\n"
PK"v19,v22,v23\n"
MF"v[148:151],a[68:71],v[72:75],v[148:151]\n"
EX"v32,v32\n"
MF"v[180:183],a[68:71],v[76:79],v[180:183]\n"
BL"v225,s[8:11],s43 offen offset:1024 sc1 lds\n"
MF"v[88:91],a[72:75],v[64:67],v[88:91]\n"
EX"v33,v33\n"
MF"v[120:123],a[72:75],v[68:71],v[120:123]\n"
RD"a[88:91],v227 offset:22528\n"
PK"v26,v28,v29\n"
MF"v[152:155],a[72:75],v[72:75],v[152:155]\n"
EX"v34,v34\n"
MF"v[184:187],a[72:75],v[76:79],v[184:187]\n"
WT"lgkmcnt(2)\n"
BS"a[152:155],v224,s[8:11],s18 offen offset:2048 sc1\n"
MF"v[92:95],a[76:79],v[64:67],v[92:95]\n"
EX"v35,v35\n"
MF"v[124:127],a[76:79],v[68:71],v[124:127]\n"
RD"a[92:95],v227 offset:23552\n"
PK"v27,v30,v31\n"
MF"v[156:159],a[76:79],v[72:75],v[156:159]\n"
SA"s18,s18,4096\n"
EX"v40,v40\n"
MF"v[188:191],a[76:79],v[76:79],v[188:191]\n"
BL"v225,s[8:11],s43 offen offset:2048 sc1 lds\n"
MF"v[96:99],a[80:83],v[64:67],v[96:99]\n"
EX"v41,v41\n"
MF"v[128:131],a[80:83],v[68:71],v[128:131]\n"
RD"a[96:99],v227 offset:24576\n"
MF"v[160:163],a[80:83],v[72:75],v[160:163]\n"
EX"v42,v42\n"
MF"v[192:195],a[80:83],v[76:79],v[192:195]\n"
WT"lgkmcnt(2)\n"
BS"a[156:159],v224,s[8:11],s18 offen offset:0 sc1\n"
MF"v[100:103],a[84:87],v[64:67],v[100:103]\n"
EX"v43,v43\n"
MF"v[132:135],a[84:87],v[68:71],v[132:135]\n"
RD"a[100:103],v227 offset:25600\n"
MF"v[164:167],a[84:87],v[72:75],v[164:167]\n"
EX"v48,v48\n"
MF"v[196:199],a[84:87],v[76:79],v[196:199]\n"
BL"v225,s[8:11],s43 offen offset:3072 sc1 lds\n"
MF"v[104:107],a[88:91],v[64:67],v[104:107]\n"
EX"v49,v49\n"
MF"v[136:139],a[88:91],v[68:71],v[136:139]\n"
RD"a[104:107],v227 offset:26624\n"
MF"v[168:171],a[88:91],v[72:75],v[168:171]\n"
EX"v50,v50\n"
MF"v[200:203],a[88:91],v[76:79],v[200:203]\n"
WT"lgkmcnt(2)\n"
BS"a[160:163],v224,s[8:11],s18 offen offset:2048 sc1\n"
MF"v[108:111],a[92:95],v[64:67],v[108:111]\n"
SA"m0,s40,16384\n"
EX"v51,v51\n"
MF"v[140:143],a[92:95],v[68:71],v[140:143]\n"
RD"a[108:111],v227 offset:27648\n"
MF"v[172:175],a[92:95],v[72:75],v[172:175]\n"
SA"s18,s18,4096\n"
EX"v56,v56\n"
MF"v[204:207],a[92:95],v[76:79],v[204:207]\n"
BL"v225,s[8:11],s44 offen offset:0 sc1 lds\n"
MF"a[132:135],a[128:131],v[64:67],a[132:135]\n"
SA"s40,s40,32768\n"
EX"v57,v57\n"
MF"a[136:139],a[128:131],v[68:71],a[136:139]\n"
"s_cmp_eq_u32 s40,s46\n"
RD"a[112:115],v227 offset:28672\n"
"s_cselect_b32 s40,s47,s40\n"
MF"a[140:143],a[128:131],v[72:75],a[140:143]\n"
EX"v58,v58\n"
MF"a[144:147],a[128:131],v[76:79],a[144:147]\n"
BS"a[164:167],v224,s[8:11],s18 offen offset:0 sc1\n"
MF"v[80:83],a[96:99],v[0:3],v[80:83]\n"
"s_lshr_b32 s21,s16,1\n"
EX"v59,v59\n"
MF"v[112:115],a[96:99],v[8:11],v[112:115]\n"
"s_sub_u32 s21,s21,s33\n"
RD"a[116:119],v227 offset:29696\n"
MF"v[144:147],a[96:99],v[16:19],v[144:147]\n"
"s_and_b32 s21,s21,3\n"
EX"v36,v36\n"
MF"v[176:179],a[96:99],v[24:27],v[176:179]\n"
WT"lgkmcnt(2)\n"
BL"v225,s[8:11],s44 offen offset:1024 sc1 lds\n"
MF"v[84:87],a[100:103],v[0:3],v[84:87]\n"
"s_lshl_b32 s21,s21,1\n"
EX"v37,v37\n"
MF"v[116:119],a[100:103],v[8:11],v[116:119]\n"
"s_and_b32 s22,s16,1\n"
RD"a[120:123],v227 offset:30720\n"
SA"s21,s21,s22\n"
MF"v[148:151],a[100:103],v[16:19],v[148:151]\n"
"s_lshl_b32 s21,s21,1\n"
EX"v38,v38\n"
MF"v[180:183],a[100:103],v[24:27],v[180:183]\n"
"s_and_b32 s22,s30,1\n"
MF"v[88:91],a[104:107],v[0:3],v[88:91]\n"
SA"s21,s21,s22\n"
EX"v39,v39\n"
MF"v[120:123],a[104:107],v[8:11],v[120:123]\n"
"s_mul_i32 s21,s21,10240\n"
RD"a[124:127],v227 offset:31744\n"
MF"v[152:155],a[104:107],v[16:19],v[152:155]\n"
"s_lshr_b32 s22,s30,1\n"
EX"v44,v44\n"
MF"v[184:187],a[104:107],v[24:27],v[184:187]\n"
"s_lshl_b32 s22,s22,10\n"
BL"v225,s[8:11],s44 offen offset:2048 sc1 lds\n"
MF"v[92:95],a[108:111],v[0:3],v[92:95]\n"
SA"s21,s21,s22\n"
EX"v45,v45\n"
MF"v[124:127],a[108:111],v[8:11],v[124:127]\n"
"s_mul_i32 s22,s35,163840\n"
MF"v[156:159],a[108:111],v[16:19],v[156:159]\n"
SA"s21,s21,s22\n"
EX"v46,v46\n"
MF"v[188:191],a[108:111],v[24:27],v[188:191]\n"
WT"lgkmcnt(1)\n"
WT"lgkmcnt(0)\n"
SA"s18,s21,3801088\n"
MF"v[96:99],a[112:115],v[0:3],v[96:99]\n"
SA"s16,s16,1\n"
"s_barrier\n"
MF"v[128:131],a[112:115],v[8:11],v[128:131]\n"
RD"a[64:67],v226 offset:0\n"
MF"v[160:163],a[112:115],v[16:19],v[160:163]\n"
EX"v47,v47\n"
MF"v[192:195],a[112:115],v[24:27],v[192:195]\n"
BL"v225,s[8:11],s44 offen offset:3072 sc1 lds\n"
MF"v[100:103],a[116:119],v[0:3],v[100:103]\n"
EX"v52,v52\n"
MF"v[132:135],a[116:119],v[8:11],v[132:135]\n"
RD"a[68:71],v226 offset:1024\n"
MF"v[164:167],a[116:119],v[16:19],v[164:167]\n"
EX"v53,v53\n"
MF"v[196:199],a[116:119],v[24:27],v[196:199]\n"
RD"a[72:75],v226 offset:2048\n"
MF"v[104:107],a[120:123],v[0:3],v[104:107]\n"
EX"v54,v54\n"
MF"v[136:139],a[120:123],v[8:11],v[136:139]\n"
RD"a[76:79],v226 offset:3072\n"
MF"v[168:171],a[120:123],v[16:19],v[168:171]\n"
EX"v55,v55\n"
MF"v[200:203],a[120:123],v[24:27],v[200:203]\n"
MF"v[108:111],a[124:127],v[0:3],v[108:111]\n"
EX"v60,v60\n"
MF"v[140:143],a[124:127],v[8:11],v[140:143]\n"
RD"a[80:83],v226 offset:4096\n"
MF"v[172:175],a[124:127],v[16:19],v[172:175]\n"
EX"v61,v61\n"
MF"v[204:207],a[124:127],v[24:27],v[204:207]\n"
RD"a[84:87],v226 offset:5120\n"
MF"a[132:135],a[128:131],v[0:3],a[132:135]\n"
EX"v62,v62\n"
MF"a[136:139],a[128:131],v[8:11],a[136:139]\n"
RD"a[88:91],v226 offset:6144\n"
MF"a[140:143],a[128:131],v[16:19],a[140:143]\n"
WT"lgkmcnt(5)\n"
"s_sub_u32 s20,s20,1\n"
EX"v63,v63\n"
MF"a[144:147],a[128:131],v[24:27],a[144:147]\n"
"s_cbranch_scc0 LRT_%=\n"
"s_lshl_b32 s21,s35,8\n"
SA"s21,s21,92160\n"
SA"s21,s21,s45\n"
WT"vmcnt(3)\n"
"s_barrier\n"
"buffer_store_dwordx2 v[232:233],v230,s[8:11],s21 offen sc1\n"
"v_cmp_eq_u64 vcc,s[28:29],v[244:245]\n"
"s_cmp_eq_u64 vcc,exec\n"
"s_cbranch_scc0 L1105_%=\n"
"L1106_%=:\n"
"s_getpc_b64 s[12:13]\n"
"LRC_%=:\n"
"s_sub_u32 s12,s12,LRC_%=-LD2_%=\n"
"s_subb_u32 s13,s13,0\n"
"s_and_b32 s13,s13,0xffff\n"
"s_mov_b32 s14,LTE_%=-LD2_%=\n"
"s_lshl_b32 s50,s30,11\n"
"v_min_u32 v244,240,v224\n"
"v_lshlrev_b32 v244,3,v244\n"
"buffer_load_dword v255,v244,s[12:15],s50 offen\n"
"s_mov_b32 s14,0\n"
"s_mov_b32 s57,0\n"
"s_mov_b32 s56,0\n"
"s_mov_b32 s14,0\n"
"s_branch LP1_%=\n"
"LEND_%=:\n"
      : : [ka] "s"(__builtin_amdgcn_kernarg_segment_ptr()), [b] "s"(blockIdx.x), [t] "v"(threadIdx.x)
      : "v1", "v2", "v3", "v4", "v5", "v6", "v7", "v8", "v9", "v10", "v11", "v12", "v13", "v14", "v15", "v16", "v17", "v18", "v19", "v20", "v21", "v22", "v23", "v24", "v25", "v26", "v27", "v28", "v29", "v30", "v31", "v32", "v33", "v34", "v35", "v36", "v37", "v38", "v39", "v40", "v41", "v42", "v43", "v44", "v45", "v46", "v47", "v48", "v49", "v50", "v51", "v52", "v53", "v54", "v55", "v56", "v57", "v58", "v59", "v60", "v61", "v62", "v63", "v64", "v65", "v66", "v67", "v68", "v69", "v70", "v71", "v72", "v73", "v74", "v75", "v76", "v77", "v78", "v79", "v80", "v81", "v82", "v83", "v84", "v85", "v86", "v87", "v88", "v89", "v90", "v91", "v92", "v93", "v94", "v95", "v96", "v97", "v98", "v99", "v100", "v101", "v102", "v103", "v104", "v105", "v106", "v107", "v108", "v109", "v110", "v111", "v112", "v113", "v114", "v115", "v116", "v117", "v118", "v119", "v120", "v121", "v122", "v123", "v124", "v125", "v126", "v127", "v128", "v129", "v130", "v131", "v132", "v133", "v134", "v135", "v136", "v137", "v138", "v139", "v140", "v141", "v142", "v143", "v144", "v145", "v146", "v147", "v148", "v149", "v150", "v151", "v152", "v153", "v154", "v155", "v156", "v157", "v158", "v159", "v160", "v161", "v162", "v163", "v164", "v165", "v166", "v167", "v168", "v169", "v170", "v171", "v172", "v173", "v174", "v175", "v176", "v177", "v178", "v179", "v180", "v181", "v182", "v183", "v184", "v185", "v186", "v187", "v188", "v189", "v190", "v191", "v192", "v193", "v194", "v195", "v196", "v197", "v198", "v199", "v200", "v201", "v202", "v203", "v204", "v205", "v206", "v207", "v208", "v209", "v210", "v211", "v212", "v213", "v214", "v215", "v216", "v217", "v218", "v219", "v220", "v221", "v222", "v223", "v224", "v225", "v226", "v227", "v228", "v229", "v230", "v231", "v232", "v233", "v234", "v235", "v236", "v237", "v238", "v239", "v240", "v241", "v242", "v243", "v244", "v245", "v246", "v247", "v248", "v249", "v250", "v251", "v252", "v253", "v254", "v255", "a0", "a1", "a2", "a3", "a4", "a5", "a6", "a7", "a8", "a9", "a10", "a11", "a12", "a13", "a14", "a15", "a16", "a17", "a18", "a19", "a20", "a21", "a22", "a23", "a24", "a25", "a26", "a27", "a28", "a29", "a30", "a31", "a32", "a33", "a34", "a35", "a36", "a37", "a38", "a39", "a40", "a41", "a42", "a43", "a44", "a45", "a46", "a47", "a48", "a49", "a50", "a51", "a52", "a53", "a54", "a55", "a56", "a57", "a58", "a59", "a60", "a61", "a62", "a63", "a64", "a65", "a66", "a67", "a68", "a69", "a70", "a71", "a72", "a73", "a74", "a75", "a76", "a77", "a78", "a79", "a80", "a81", "a82", "a83", "a84", "a85", "a86", "a87", "a88", "a89", "a90", "a91", "a92", "a93", "a94", "a95", "a96", "a97", "a98", "a99", "a100", "a101", "a102", "a103", "a104", "a105", "a106", "a107", "a108", "a109", "a110", "a111", "a112", "a113", "a114", "a115", "a116", "a117", "a118", "a119", "a120", "a121", "a122", "a123", "a124", "a125", "a126", "a127", "a128", "a129", "a130", "a131", "a132", "a133", "a134", "a135", "a136", "a137", "a138", "a139", "a140", "a141", "a142", "a143", "a144", "a145", "a146", "a147", "a148", "a149", "a150", "a151", "a152", "a153", "a154", "a155", "a156", "a157", "a158", "a159", "a160", "a161", "a162", "a163", "a164", "a165", "a166", "a167", "a168", "a169", "a170", "a171", "a172", "a173", "a174", "a175", "a176", "a177", "a178", "a179", "a180", "a181", "a182", "a183", "a184", "a185", "a186", "a187", "a188", "a189", "a190", "a191", "a192", "a193", "a194", "a195", "a196", "a197", "a198", "a199", "a200", "a201", "a202", "a203", "a204", "a205", "a206", "a207", "a208", "a209", "a210", "a211", "a212", "a213", "a214", "a215", "a216", "a217", "a218", "a219", "a220", "a221", "a222", "a223", "a224", "a225", "a226", "a227", "a228", "a229", "a230", "a231", "a232", "a233", "a234", "a235", "a236", "a237", "a238", "a239", "a240", "a241", "a242", "a243", "a244", "a245", "a246", "a247", "a248", "a249", "a250", "a251", "a252", "a253", "a254", "a255", "s8", "s9", "s10", "s11", "s12", "s13", "s14", "s15", "s16", "s17", "s18", "s19", "s20", "s21", "s22", "s23", "s24", "s25", "s26", "s27", "s28", "s29", "s30", "s31", "s32", "s33", "s34", "s35", "s36", "s37", "s38", "s39", "s40", "s41", "s42", "s43", "s44", "s45", "s46", "s47", "s48", "s49", "s50", "s51", "s52", "s53", "s54", "s55", "s56", "s57", "s58", "s59", "s60", "s61", "s62", "s63", "s64", "s65", "s66", "s67", "s68", "s69", "s70", "s71", "s72", "s73", "s74", "s75", "s76", "s77", "s78", "s79", "s80", "s81", "s82", "s83", "s84", "s85", "s86", "s87", "s88", "s89", "s90", "s91", "s92", "s93", "s94", "s95", "s96", "s97", "s98", "s99", "scc", "vcc", "memory");
}

extern "C" void kernel_launch(void* const* d_in, const int* in_sizes, int n_in, void* d_out, int out_size, void* d_ws, size_t ws_size, hipStream_t stream) {
  (void)hipFuncSetAttribute((const void*)mega_kernel, hipFuncAttributeMaxDynamicSharedMemorySize, 163840);
  mega_kernel<<<256, 256, 163840, stream>>>((const bf16*)d_in[0], (const bf16*)d_in[1], (const bf16*)d_in[2], (const bf16*)d_in[3], (const bf16*)d_in[4], (const bf16*)d_in[5],
      (const bf16*)d_in[7], (const bf16*)d_in[8], (const bf16*)d_in[9], (const bf16*)d_in[10], (char*)d_ws + (size_t)0 * 1048576, (bf16*)d_out);
}
```
